```python
import torch

def _softmax(x, axis=-1):
    return torch.softmax(x, dim=axis)
def _gelu(x):
    return torch.nn.functional.gelu(x, approximate="tanh")
def _silu(x):
    return torch.nn.functional.silu(x)
def _sigmoid(x):
    return torch.sigmoid(x)
def _layernorm(x, axis=-1, eps=1e-5):
    m = x.mean(dim=axis, keepdim=True)
    v = x.var(dim=axis, keepdim=True, unbiased=False)
    return (x - m) / torch.sqrt(v + eps)
def _scatter_add(tgt, idx, src, axis=0):
    return tgt.clone().index_add_(axis, idx, src)
def _segment_sum(src, seg, n):
    out = torch.zeros((n,) + src.shape[1:], dtype=src.dtype)
    return out.index_add_(0, seg, src)
def _pad(x, pw):
    flat = tuple(v for pair in reversed(pw) for v in pair)
    return torch.nn.functional.pad(x, flat)

kernel_name = 'standalone__modern_conv_block_convnext__vision'

def setup_inputs():

    _w = (lambda a: (lambda t: t.to(torch.bfloat16) if t.is_floating_point() else t)(torch.as_tensor(a)))
    H = W = 512
    C = 96
    K = 3
    return {
        "x": _w((lambda *s: torch.randn(*s, dtype=torch.bfloat16) * 0.05)(1, H, W, C)),
        "dw": _w((lambda *s: torch.randn(*s, dtype=torch.bfloat16) * 0.05)(K, K, C)),
        "w1": _w((lambda *s: torch.randn(*s, dtype=torch.bfloat16) * 0.05)(C, 4 * C)),
        "b1": _w((lambda *s: torch.randn(*s, dtype=torch.bfloat16) * 0.2)(4 * C)),
        "w2": _w((lambda *s: torch.randn(*s, dtype=torch.bfloat16) * 0.05)(4 * C, C)),
        "b2": _w((lambda *s: torch.randn(*s, dtype=torch.bfloat16) * 0.2)(C)),
        "gamma": _w((lambda *s: torch.randn(*s, dtype=torch.bfloat16) * 0.05)(C)),
    }

def reference(x, dw, w1, b1, w2, b2, gamma):
    B, H, W, C = x.shape
    K = dw.shape[0]
    pad = K // 2
    xp = _pad(x, ((0,0),(pad,pad),(pad,pad),(0,0)))
    out = torch.zeros_like(x)
    for i in range(K):
        for j in range(K):
            out = out + xp[:, i:i+H, j:j+W, :] * dw[i, j][None, None, None, :]
    h = _layernorm(out, axis=-1)
    h = torch.einsum('bhwc,ce->bhwe', h, w1) + b1
    h = _gelu(h)
    h = torch.einsum('bhwe,ec->bhwc', h, w2) + b2
    return x + gamma[None, None, None, :] * h
```

```cpp
#include <hip/hip_runtime.h>
#include <stdint.h>

constexpr int LDS_BYTES = 163584;

__global__ __launch_bounds__(512, 2) void convnext_kernel(
    const unsigned short* __restrict__ x, const unsigned short* __restrict__ dw, const unsigned short* __restrict__ w1,
    const unsigned short* __restrict__ b1, const unsigned short* __restrict__ w2, const unsigned short* __restrict__ b2,
    const unsigned short* __restrict__ gamma, unsigned short* __restrict__ out) {
    const unsigned tid = threadIdx.x;
    asm volatile(
R"ASM(
N_main_%=:
s_load_dwordx16 s[20:35],%2,0
v_lshrrev_b32 v236,6,%1
v_and_b32 v232,63,%1
v_readfirstlane_b32 s52,v236
s_getpc_b64 s[14:15]
N_ep_%=:
v_lshlrev_b32 v252,7,v232
v_lshrrev_b32 v253,8,%1
v_mul_u32_u24 v253,N_B_%=-N_ep_%=,v253
v_add_u32 v252,v252,v253
global_load_dword v254,v252,s[14:15]
v_mov_b32 v228,0
v_mov_b32 v229,0
v_mov_b32 v230,0
v_mov_b32 v231,0
s_and_b32 s64,%0,7
s_lshr_b32 s70,%0,3
s_lshl_b32 s71,s64,6
s_mul_i32 s68,s70,3072
s_and_b32 s64,s52,3
s_mul_i32 s67,s64,54
s_cmp_eq_u32 s70,0
s_cselect_b32 s66,0,99
s_cmp_eq_u32 s70,31
s_cselect_b32 s66,17,s66
v_add_u32 v236,s67,v232
s_mul_i32 s57,s64,864
s_add_u32 s57,s57,73728
v_mul_u32_u24 v237,0xaaab,v236
s_sub_u32 s65,s71,1
v_lshrrev_b32 v237,19,v237
s_mul_i32 s56,s65,98304
v_mul_u32_u24 v238,12,v237
s_add_u32 s56,s56,s68
v_sub_u32 v238,v236,v238
s_cmp_lt_i32 s65,0
s_cselect_b32 s101,0x70000000,s56
v_add_u32 v236,1,v237
s_mov_b32 s62,-1
s_mov_b32 s63,0x3fffff
v_bfe_u32 v236,v236,2,2
s_mov_b32 s38,50331840
s_mov_b32 s39,0x00020000
v_xor_b32 v238,v238,v236
s_mov_b32 s96,0x8befe000
s_mov_b32 s99,0x72800000
v_mul_u32_u24 v236,192,v237
s_mov_b32 s98,0xffff0000
s_bitcmp1_b32 s52,1
s_cselect_b64 vcc,-1,0
v_lshl_add_u32 v241,v238,4,v236
v_cmp_eq_u32_e64 s[92:93],s66,v237
v_mov_b32 v236,0x7fff0000
s_nop 0
v_cndmask_b32_e64 v241,v241,v236,s[92:93]
s_cmp_ge_u32 s52,4
s_cbranch_scc1 N_B_%=
s_setprio 3
s_and_b32 s66,s52,3
s_cmp_eq_u32 s66,3
s_cselect_b32 s14,1,s66
v_min_u32 v236,47,v232
s_mul_i32 s64,s14,192
s_lshl_b32 s65,s14,7
v_lshl_add_u32 v200,v236,2,s64
s_add_u32 s65,s65,144256
v_and_b32 v199,255,%1
v_min_u32 v237,191,v199
v_lshlrev_b32 v201,2,v237
v_min_u32 v249,95,v199
v_lshlrev_b32 v202,1,v249
s_lshl_b32 s64,s66,3
s_lshr_b32 s67,0x30011100,s64
s_and_b32 s67,s67,0xff
s_lshl_b32 s67,s67,3
s_add_u32 s67,s67,143488
v_lshrrev_b32 v238,2,v236
v_and_b32 v239,3,v236
v_mul_u32_u24 v238,1152,v238
v_lshl_add_u32 v238,v239,5,v238
s_and_b32 s66,s66,1
s_lshl_b32 s64,s66,3
s_cmp_eq_u32 s66,0
s_movk_i32 s66,128
s_cselect_b32 s66,640,s66
v_add_u32 v203,s65,v238
v_add_u32 v209,s67,v238
v_lshl_add_u32 v204,v239,2,v203
v_add_u32 v215,s66,v209
v_xor_b32 v237,s64,v209
v_lshrrev_b32 v239,1,v239
v_xor_b32 v238,s64,v215
ds_write_b128 v203,v[228:231] offset:0
ds_write_b128 v203,v[228:231] offset:16
ds_write2_b64 v209,v[228:229],v[228:229] offset0:0 offset1:2
ds_write2_b64 v215,v[228:229],v[228:229] offset0:0 offset1:2
ds_write2_b64 v237,v[228:229],v[228:229] offset0:0 offset1:2
ds_write2_b64 v238,v[228:229],v[228:229] offset0:0 offset1:2
v_lshl_add_u32 v209,v239,2,v209
v_lshl_add_u32 v215,v239,2,v215
ds_write_b32 v228,v228 offset:11648
s_waitcnt lgkmcnt(0)
s_sub_u32 s36,s20,192
s_subb_u32 s37,s21,0
s_and_b32 s37,s37,0xffff
s_bitcmp0_b32 s52,0
s_cselect_b64 s[44:45],s[24:25],s[28:29]
s_and_b32 s45,s45,0xffff
s_mov_b64 exec,s[62:63]
s_add_u32 m0,s57,0
s_nop 0
buffer_load_dwordx4 v241,s[36:39],s101 offen lds
s_add_u32 m0,s57,3488
s_add_u32 s101,s56,98304
buffer_load_dwordx4 v241,s[36:39],s101 offen lds
s_mov_b64 exec,-1
global_load_dword v242,v200,s[22:23] offset:0
global_load_dword v243,v200,s[22:23] offset:576
global_load_dword v244,v200,s[22:23] offset:1152
global_load_ushort v247,v202,s[32:33]
global_load_dword v245,v201,s[26:27]
global_load_ushort v246,v202,s[30:31]
s_mov_b64 exec,s[62:63]
s_add_u32 m0,s57,6976
s_add_u32 s101,s56,196608
buffer_load_dwordx4 v241,s[36:39],s101 offen lds
s_add_u32 m0,s57,10464
s_add_u32 s101,s56,294912
buffer_load_dwordx4 v241,s[36:39],s101 offen lds
s_add_u32 m0,s57,13952
s_add_u32 s101,s56,393216
buffer_load_dwordx4 v241,s[36:39],s101 offen lds
s_add_u32 m0,s57,17440
s_add_u32 s101,s56,491520
buffer_load_dwordx4 v241,s[36:39],s101 offen lds
s_add_u32 m0,s57,20928
s_add_u32 s101,s56,589824
buffer_load_dwordx4 v241,s[36:39],s101 offen lds
s_add_u32 m0,s57,24416
s_add_u32 s101,s56,688128
buffer_load_dwordx4 v241,s[36:39],s101 offen lds
s_add_u32 m0,s57,27904
s_add_u32 s101,s56,786432
buffer_load_dwordx4 v241,s[36:39],s101 offen lds
s_add_u32 m0,s57,31392
s_add_u32 s101,s56,884736
buffer_load_dwordx4 v241,s[36:39],s101 offen lds
s_add_u32 m0,s57,34880
s_add_u32 s101,s56,983040
buffer_load_dwordx4 v241,s[36:39],s101 offen lds
s_add_u32 m0,s57,38368
s_add_u32 s101,s56,1081344
buffer_load_dwordx4 v241,s[36:39],s101 offen lds
s_add_u32 m0,s57,41856
s_add_u32 s101,s56,1179648
buffer_load_dwordx4 v241,s[36:39],s101 offen lds
s_add_u32 m0,s57,45344
s_add_u32 s101,s56,1277952
buffer_load_dwordx4 v241,s[36:39],s101 offen lds
s_add_u32 m0,s57,48832
s_add_u32 s101,s56,1376256
buffer_load_dwordx4 v241,s[36:39],s101 offen lds
s_add_u32 m0,s57,52320
s_add_u32 s101,s56,1474560
buffer_load_dwordx4 v241,s[36:39],s101 offen lds
s_add_u32 m0,s57,55808
s_add_u32 s101,s56,1572864
buffer_load_dwordx4 v241,s[36:39],s101 offen lds
s_add_u32 m0,s57,59296
s_add_u32 s101,s56,1671168
buffer_load_dwordx4 v241,s[36:39],s101 offen lds
s_mov_b64 exec,-1
s_and_b32 s64,s52,3
s_mul_i32 s67,s64,54
s_lshl_b32 s10,s64,2
s_cmp_eq_u32 s70,31
s_cselect_b32 s66,17,99
v_add_u32 v236,s67,v232
v_add_u32 v236,108,v236
v_mul_u32_u24 v237,0xaaab,v236
v_lshrrev_b32 v237,19,v237
v_mul_u32_u24 v238,12,v237
v_sub_u32 v238,v236,v238
v_add_u32 v236,1,v237
v_bfe_u32 v236,v236,2,2
v_xor_b32 v238,v238,v236
v_mul_u32_u24 v236,192,v237
v_lshl_add_u32 v239,v238,4,v236
v_cmp_eq_u32_e64 s[92:93],s66,v237
v_mov_b32 v236,0x7fff0000
s_nop 0
v_cndmask_b32_e64 v239,v239,v236,s[92:93]
v_cndmask_b32_e64 v6,v239,v6,vcc
s_add_u32 s64,s10,s71
v_and_b32 v233,15,v232
v_lshrrev_b32 v234,4,v232
s_mul_i32 s61,s64,98304
s_add_u32 s61,s61,s68
v_and_b32 v236,1,v234
v_lshlrev_b32 v236,1,v236
v_lshrrev_b32 v237,5,v232
v_or_b32 v235,v236,v237
s_mov_b32 s54,0x44ea44ea
s_mov_b32 s55,0x877f877f
s_mov_b32 s8,0x0ff00ff0
s_mov_b32 s9,0x0ff00ff0
v_mov_b32 v7,0
v_mov_b32 v239,0x70000
v_bfe_u32 v236,v233,1,1
v_sub_u32 v236,v235,v236
v_cmp_gt_u32_e64 s[92:93],3,v236
v_lshrrev_b32 v237,2,v233
v_lshlrev_b32 v237,1,v237
v_and_b32 v238,1,v233
v_or_b32 v237,v237,v238
v_lshl_add_u32 v236,v236,3,v237
v_lshlrev_b32 v236,4,v236
v_add_u32 v236,143488,v236
v_cndmask_b32_e64 v11,v239,v236,s[92:93]
v_and_b32 v13,3,v237
v_lshlrev_b32 v236,1,v233
v_and_b32 v236,4,v236
v_lshl_add_u32 v236,v234,3,v236
v_mov_b32 v238,0x54250310
v_mul_u32_u24 v13,0x5555,v13
v_bfe_u32 v236,v238,v236,3
v_lshl_add_u32 v236,v236,3,v237
v_lshlrev_b32 v236,4,v236
v_add_u32 v12,143488,v236
v_lshrrev_b32 v237,2,v233
v_cmp_eq_u32_e64 s[92:93],v234,v237
v_lshlrev_b32 v236,4,v233
v_add_u32 v236,10112,v236
v_cndmask_b32_e64 v14,v239,v236,s[92:93]
v_and_b32 v239,7,v233
v_lshl_add_u32 v236,v239,1,v235
v_add_u32 v237,1,v236
v_bfe_u32 v237,v237,2,2
v_mul_u32_u24 v236,192,v236
v_lshl_add_u32 v236,v237,4,v236
v_add_u32 v237,1,v239
v_bfe_u32 v237,v237,1,2
v_xor_b32 v237,v237,v234
v_mul_u32_u24 v239,384,v239
v_lshl_add_u32 v237,v237,4,v239
v_sub_u32 v237,v237,v236
v_add_u32 v8,192,v237
v_add_u32 v15,73728,v236
v_and_b32 v238,1,v233
v_mul_u32_u24 v238,320,v238
v_sub_u32 v239,v239,v238
s_mov_b32 s12,0xaaaaaaaa
s_mov_b32 s13,0xaaaaaaaa
v_lshl_add_u32 v10,v234,4,v239
s_mov_b64 exec,s[8:9]
v_add_u32 v10,196608,v10
s_mov_b64 exec,-1
v_lshrrev_b32 v236,3,v233
v_bfe_u32 v237,v233,2,1
v_lshl_add_u32 v237,v236,4,v237
v_lshl_add_u32 v237,v234,1,v237
v_lshlrev_b32 v237,6,v237
v_lshl_add_u32 v237,v236,4,v237
v_and_b32 v238,1,v233
v_lshl_add_u32 v237,v238,5,v237
v_bfe_u32 v238,v233,1,1
v_lshl_add_u32 v237,v238,3,v237
v_xor_b32 v238,16,v237
v_add_u32 v1,16256,v237
v_add_u32 v2,16256,v238
v_lshrrev_b32 v236,3,v233
v_bfe_u32 v237,v233,2,1
v_lshl_add_u32 v236,v237,1,v236
v_lshrrev_b32 v237,1,v234
v_lshl_add_u32 v236,v237,2,v236
v_mul_u32_u24 v236,192,v236
v_and_b32 v238,1,v234
v_mul_u32_u24 v239,3080,v238
v_add_u32 v236,v236,v239
v_and_b32 v239,3,v233
v_lshl_add_u32 v236,v239,4,v236
v_add_u32 v3,40896,v236
v_lshlrev_b32 v238,6,v238
v_lshl_add_u32 v238,v237,4,v238
v_add_u32 v4,8192,v238
s_mov_b64 s[40:41],s[34:35]
s_and_b32 s41,s41,0xffff
s_mov_b32 s42,50331648
s_mov_b32 s43,0x00020000
v_lshrrev_b32 v236,5,v249
v_bfe_u32 v237,v249,2,1
v_and_b32 v238,3,v249
v_lshlrev_b32 v206,7,v236
v_lshl_add_u32 v206,v237,5,v206
v_lshl_add_u32 v206,v238,2,v206
v_bfe_u32 v239,v249,3,1
v_lshl_add_u32 v206,v239,6,v206
v_bfe_u32 v239,v249,4,1
v_lshl_add_u32 v206,v239,4,v206
v_add_u32 v206,9728,v206
v_lshl_add_u32 v207,v236,1,v237
v_lshlrev_b32 v207,4,v207
v_bfe_u32 v239,v249,3,2
v_lshl_add_u32 v207,v239,2,v207
v_add_u32 v207,v207,v238
v_lshlrev_b32 v207,4,v207
v_add_u32 v207,10112,v207
v_lshlrev_b32 v208,4,v238
v_cmp_eq_u32_e64 s[48:49],1,v237
v_lshlrev_b32 v205,1,v201
v_add_u32 v205,8192,v205
v_mov_b32 v221,0
v_mov_b32 v222,0x49800000
s_waitcnt vmcnt(16)
v_and_b32 v212,0xffff,v242
v_and_b32 v242,s98,v242
ds_write2_b32 v209,v212,v242 offset0:0 offset1:4
v_and_b32 v213,0xffff,v243
v_and_b32 v243,s98,v243
ds_write2_b32 v215,v213,v243 offset0:0 offset1:4
v_and_b32 v214,0xffff,v244
v_and_b32 v244,s98,v244
ds_write2_b32 v204,v214,v244 offset0:0 offset1:4
v_lshlrev_b32 v247,16,v247
v_lshlrev_b32 v246,16,v246
v_mul_f32 v247,0x6c800000,v247
v_mul_f32 v246,0x12800000,v246
v_rcp_f32 v224,v247
v_lshlrev_b32 v210,16,v245
v_and_b32 v211,s98,v245
v_med3_f32 v224,v224,v222,-v222
v_pk_mul_f32 v[210:211],v[210:211],s[96:97] op_sel_hi:[1,0]
v_cvt_pk_bf16_f32 v220,v224,v228
ds_write_b64 v205,v[210:211]
ds_write_b32 v206,v246 offset:2048
v_lshlrev_b32 v222,16,v220
v_rcp_f32 v222,v222
v_lshlrev_b64 v[220:221],v208,v[220:221]
v_cndmask_b32_e64 v224,v220,0,s[48:49]
v_cndmask_b32_e64 v226,0,v220,s[48:49]
v_cndmask_b32_e64 v225,v221,0,s[48:49]
v_cndmask_b32_e64 v227,0,v221,s[48:49]
ds_write_b128 v207,v[224:227]
ds_write_b32 v206,v222
v_bfe_u32 v236,v232,2,3
v_mul_u32_u24 v236,768,v236
v_and_b32 v237,3,v232
v_lshrrev_b32 v238,5,v232
v_lshl_add_u32 v237,v238,2,v237
v_lshl_add_u32 v236,v237,4,v236
v_lshlrev_b32 v237,4,v232
s_bitcmp0_b32 s52,0
s_mov_b32 s58,40896
s_cselect_b32 s58,16256,s58
s_cselect_b64 s[92:93],-1,0
s_cselect_b32 s59,64,6144
s_movk_i32 s53,1024
s_cselect_b32 s53,6144,s53
s_cselect_b32 s60,0,67584
s_cselect_b32 s97,64,0
s_cselect_b32 s69,0,0x70000000
s_mov_b32 s100,73728
s_cselect_b32 s100,110688,s100
s_cselect_b32 s64,16,0
s_movk_i32 s50,1032
s_cselect_b32 s50,1024,s50
s_cselect_b32 s51,s53,-72704
v_cndmask_b32_e64 v236,v237,v236,s[92:93]
v_cndmask_b32_e64 v5,v241,v236,vcc
v_xor_b32 v236,s64,v236
v_cndmask_b32_e64 v6,v6,v236,vcc
s_mov_b32 s46,73728
s_mov_b32 s47,0x00020000
s_waitcnt lgkmcnt(0)
s_waitcnt vmcnt(0)
s_barrier
s_setprio 3
s_add_u32 s4,s10,0
s_cmp_ge_u32 s4,20
s_cselect_b32 s7,20,0
s_sub_u32 s4,s4,s7
s_mul_i32 s4,s4,3488
s_add_u32 s5,s10,2
s_cmp_ge_u32 s5,20
s_cselect_b32 s7,20,0
s_sub_u32 s5,s5,s7
s_mul_i32 s5,s5,3488
s_add_u32 s6,s10,4
s_cmp_ge_u32 s6,20
s_cselect_b32 s7,20,0
s_sub_u32 s6,s6,s7
s_mul_i32 s6,s6,3488
v_add_u32 v112,s4,v15
v_add_u32 v116,s5,v15
s_mov_b64 exec,s[8:9]
v_add_u32 v112,s5,v15
v_add_u32 v116,s6,v15
s_mov_b64 exec,-1
v_xor_b32 v113,16,v112
v_xor_b32 v117,16,v116
v_xor_b32 v114,32,v112
v_xor_b32 v118,32,v116
v_xor_b32 v115,48,v112
v_xor_b32 v119,48,v116
ds_read_b128 v[136:139],v116 offset:0
ds_read_b128 v[88:91],v11 offset:768
ds_read_b128 v[140:143],v116 offset:3488
ds_read_b128 v[128:131],v112 offset:0
ds_read_b128 v[132:135],v112 offset:3488
ds_read_b128 v[92:95],v12 offset:0
ds_read_b128 v[152:155],v117 offset:0
ds_read_b128 v[96:99],v11 offset:1920
ds_read_b128 v[156:159],v117 offset:3488
ds_read_b128 v[144:147],v113 offset:0
ds_read_b128 v[148:151],v113 offset:3488
ds_read_b128 v[100:103],v12 offset:1152
s_waitcnt lgkmcnt(10)
v_mfma_f32_16x16x32_bf16 v[160:163],v[88:91],v[136:139],0
s_waitcnt lgkmcnt(9)
v_mfma_f32_16x16x32_bf16 v[184:187],v[88:91],v[140:143],0
s_waitcnt lgkmcnt(6)
ds_read_b128 v[140:143],v116 offset:3552
ds_read_b128 v[88:91],v11 offset:5376
v_smfmac_f32_16x16x64_bf16 v[160:163],v[92:95],v[128:135],v13
ds_read_b128 v[128:131],v112 offset:64
v_smfmac_f32_16x16x64_bf16 v[184:187],v[92:95],v[132:139],v13
s_waitcnt lgkmcnt(7)
ds_read_b128 v[136:139],v116 offset:64
ds_read_b128 v[132:135],v112 offset:3552
v_mfma_f32_16x16x32_bf16 v[164:167],v[96:99],v[152:155],0
s_waitcnt lgkmcnt(8)
ds_read_b128 v[92:95],v12 offset:4608
v_add_f32 v120,v160,v161
v_mul_f32 v124,v160,v160
v_fmac_f32 v124,v161,v161
v_mfma_f32_16x16x32_bf16 v[188:191],v[96:99],v[156:159],0
s_waitcnt lgkmcnt(6)
ds_read_b128 v[156:159],v117 offset:3552
ds_read_b128 v[96:99],v11 offset:6528
v_add_f32 v121,v162,v163
v_mul_f32 v125,v162,v162
v_fmac_f32 v125,v163,v163
v_smfmac_f32_16x16x64_bf16 v[164:167],v[100:103],v[144:151],v13
ds_read_b128 v[144:147],v113 offset:64
v_add_f32 v122,v184,v185
v_mul_f32 v126,v184,v184
v_fmac_f32 v126,v185,v185
v_smfmac_f32_16x16x64_bf16 v[188:191],v[100:103],v[148:155],v13
s_waitcnt lgkmcnt(5)
ds_read_b128 v[152:155],v117 offset:64
ds_read_b128 v[148:151],v113 offset:3552
v_add_f32 v123,v186,v187
v_mul_f32 v127,v186,v186
v_fmac_f32 v127,v187,v187
v_mfma_f32_16x16x32_bf16 v[168:171],v[88:91],v[136:139],0
ds_read_b128 v[100:103],v12 offset:5760
v_add_f32 v120,v120,v164
v_fmac_f32 v124,v164,v164
v_add_f32 v120,v120,v165
v_fmac_f32 v124,v165,v165
v_mfma_f32_16x16x32_bf16 v[192:195],v[88:91],v[140:143],0
s_waitcnt lgkmcnt(6)
ds_read_b128 v[140:143],v116 offset:3616
ds_read_b128 v[88:91],v11 offset:9984
v_add_f32 v121,v121,v166
v_fmac_f32 v125,v166,v166
v_add_f32 v121,v121,v167
v_fmac_f32 v125,v167,v167
v_smfmac_f32_16x16x64_bf16 v[168:171],v[92:95],v[128:135],v13
ds_read_b128 v[128:131],v112 offset:128
v_add_f32 v122,v122,v188
v_fmac_f32 v126,v188,v188
v_add_f32 v122,v122,v189
v_fmac_f32 v126,v189,v189
v_smfmac_f32_16x16x64_bf16 v[192:195],v[92:95],v[132:139],v13
s_waitcnt lgkmcnt(5)
ds_read_b128 v[136:139],v116 offset:128
ds_read_b128 v[132:135],v112 offset:3616
v_add_f32 v123,v123,v190
v_fmac_f32 v127,v190,v190
v_add_f32 v123,v123,v191
v_fmac_f32 v127,v191,v191
v_mfma_f32_16x16x32_bf16 v[172:175],v[96:99],v[152:155],0
ds_read_b128 v[92:95],v12 offset:9216
v_add_f32 v120,v120,v168
v_fmac_f32 v124,v168,v168
v_add_f32 v120,v120,v169
v_fmac_f32 v124,v169,v169
v_mfma_f32_16x16x32_bf16 v[196:199],v[96:99],v[156:159],0
s_waitcnt lgkmcnt(6)
ds_read_b128 v[156:159],v117 offset:3616
ds_read_b128 v[96:99],v11 offset:11136
v_add_f32 v121,v121,v170
v_fmac_f32 v125,v170,v170
v_add_f32 v121,v121,v171
v_fmac_f32 v125,v171,v171
v_smfmac_f32_16x16x64_bf16 v[172:175],v[100:103],v[144:151],v13
ds_read_b128 v[144:147],v113 offset:128
v_add_f32 v122,v122,v192
v_fmac_f32 v126,v192,v192
v_add_f32 v122,v122,v193
v_fmac_f32 v126,v193,v193
v_smfmac_f32_16x16x64_bf16 v[196:199],v[100:103],v[148:155],v13
s_waitcnt lgkmcnt(5)
ds_read_b128 v[152:155],v117 offset:128
ds_read_b128 v[148:151],v113 offset:3616
v_add_f32 v123,v123,v194
v_fmac_f32 v127,v194,v194
v_add_f32 v123,v123,v195
v_fmac_f32 v127,v195,v195
v_mfma_f32_16x16x32_bf16 v[176:179],v[88:91],v[136:139],0
ds_read_b128 v[100:103],v12 offset:10368
v_add_f32 v120,v120,v172
v_fmac_f32 v124,v172,v172
v_add_f32 v120,v120,v173
v_fmac_f32 v124,v173,v173
v_mfma_f32_16x16x32_bf16 v[200:203],v[88:91],v[140:143],0
s_waitcnt lgkmcnt(6)
ds_read_b128 v[140:143],v118 offset:3488
ds_read_b128 v[88:91],v11 offset:3072
v_add_f32 v121,v121,v174
v_fmac_f32 v125,v174,v174
v_add_f32 v121,v121,v175
v_fmac_f32 v125,v175,v175
v_smfmac_f32_16x16x64_bf16 v[176:179],v[92:95],v[128:135],v13
ds_read_b128 v[128:131],v114 offset:0
v_add_f32 v122,v122,v196
v_fmac_f32 v126,v196,v196
v_add_f32 v122,v122,v197
v_fmac_f32 v126,v197,v197
v_smfmac_f32_16x16x64_bf16 v[200:203],v[92:95],v[132:139],v13
s_waitcnt lgkmcnt(5)
ds_read_b128 v[136:139],v118 offset:0
ds_read_b128 v[132:135],v114 offset:3488
v_add_f32 v123,v123,v198
v_fmac_f32 v127,v198,v198
v_add_f32 v123,v123,v199
v_fmac_f32 v127,v199,v199
v_mfma_f32_16x16x32_bf16 v[180:183],v[96:99],v[152:155],0
ds_read_b128 v[92:95],v12 offset:2304
v_add_f32 v120,v120,v176
v_fmac_f32 v124,v176,v176
v_add_f32 v120,v120,v177
v_fmac_f32 v124,v177,v177
v_mfma_f32_16x16x32_bf16 v[204:207],v[96:99],v[156:159],0
s_waitcnt lgkmcnt(6)
ds_read_b128 v[156:159],v119 offset:3488
ds_read_b128 v[96:99],v11 offset:4224
v_add_f32 v121,v121,v178
v_fmac_f32 v125,v178,v178
v_add_f32 v121,v121,v179
v_fmac_f32 v125,v179,v179
v_smfmac_f32_16x16x64_bf16 v[180:183],v[100:103],v[144:151],v13
ds_read_b128 v[144:147],v115 offset:0
v_add_f32 v122,v122,v200
v_fmac_f32 v126,v200,v200
v_add_f32 v122,v122,v201
v_fmac_f32 v126,v201,v201
v_smfmac_f32_16x16x64_bf16 v[204:207],v[100:103],v[148:155],v13
s_waitcnt lgkmcnt(5)
ds_read_b128 v[152:155],v119 offset:0
ds_read_b128 v[148:151],v115 offset:3488
v_add_f32 v123,v123,v202
v_fmac_f32 v127,v202,v202
v_add_f32 v123,v123,v203
v_fmac_f32 v127,v203,v203
v_mfma_f32_16x16x32_bf16 v[208:211],v[88:91],v[136:139],0
ds_read_b128 v[100:103],v12 offset:3456
v_add_f32 v120,v120,v180
v_fmac_f32 v124,v180,v180
v_add_f32 v120,v120,v181
v_fmac_f32 v124,v181,v181
v_mfma_f32_16x16x32_bf16 v[232:235],v[88:91],v[140:143],0
s_waitcnt lgkmcnt(6)
ds_read_b128 v[140:143],v118 offset:3552
ds_read_b128 v[88:91],v11 offset:7680
v_add_f32 v121,v121,v182
v_fmac_f32 v125,v182,v182
v_add_f32 v121,v121,v183
v_fmac_f32 v125,v183,v183
v_smfmac_f32_16x16x64_bf16 v[208:211],v[92:95],v[128:135],v13
ds_read_b128 v[128:131],v114 offset:64
v_add_f32 v122,v122,v204
v_fmac_f32 v126,v204,v204
v_add_f32 v122,v122,v205
v_fmac_f32 v126,v205,v205
v_smfmac_f32_16x16x64_bf16 v[232:235],v[92:95],v[132:139],v13
s_waitcnt lgkmcnt(5)
ds_read_b128 v[136:139],v118 offset:64
ds_read_b128 v[132:135],v114 offset:3552
v_add_f32 v123,v123,v206
v_fmac_f32 v127,v206,v206
v_add_f32 v123,v123,v207
v_fmac_f32 v127,v207,v207
v_mfma_f32_16x16x32_bf16 v[212:215],v[96:99],v[152:155],0
ds_read_b128 v[92:95],v12 offset:6912
v_add_f32 v120,v120,v208
v_fmac_f32 v124,v208,v208
v_add_f32 v120,v120,v209
v_fmac_f32 v124,v209,v209
v_mfma_f32_16x16x32_bf16 v[236:239],v[96:99],v[156:159],0
s_waitcnt lgkmcnt(6)
ds_read_b128 v[156:159],v119 offset:3552
ds_read_b128 v[96:99],v11 offset:8832
v_add_f32 v121,v121,v210
v_fmac_f32 v125,v210,v210
v_add_f32 v121,v121,v211
v_fmac_f32 v125,v211,v211
v_smfmac_f32_16x16x64_bf16 v[212:215],v[100:103],v[144:151],v13
ds_read_b128 v[144:147],v115 offset:64
v_add_f32 v122,v122,v232
v_fmac_f32 v126,v232,v232
v_add_f32 v122,v122,v233
v_fmac_f32 v126,v233,v233
v_smfmac_f32_16x16x64_bf16 v[236:239],v[100:103],v[148:155],v13
s_waitcnt lgkmcnt(5)
ds_read_b128 v[152:155],v119 offset:64
ds_read_b128 v[148:151],v115 offset:3552
v_add_f32 v123,v123,v234
v_fmac_f32 v127,v234,v234
v_add_f32 v123,v123,v235
v_fmac_f32 v127,v235,v235
v_mfma_f32_16x16x32_bf16 v[216:219],v[88:91],v[136:139],0
ds_read_b128 v[100:103],v12 offset:8064
v_add_f32 v120,v120,v212
v_fmac_f32 v124,v212,v212
v_add_f32 v120,v120,v213
v_fmac_f32 v124,v213,v213
v_add_f32 v121,v121,v214
v_mfma_f32_16x16x32_bf16 v[240:243],v[88:91],v[140:143],0
s_waitcnt lgkmcnt(6)
ds_read_b128 v[140:143],v118 offset:3616
ds_read_b128 v[88:91],v11 offset:12288
v_fmac_f32 v125,v214,v214
v_add_f32 v121,v121,v215
v_fmac_f32 v125,v215,v215
v_add_f32 v122,v122,v236
v_smfmac_f32_16x16x64_bf16 v[216:219],v[92:95],v[128:135],v13
ds_read_b128 v[128:131],v114 offset:128
v_fmac_f32 v126,v236,v236
v_add_f32 v122,v122,v237
v_fmac_f32 v126,v237,v237
v_add_f32 v123,v123,v238
v_fmac_f32 v127,v238,v238
v_smfmac_f32_16x16x64_bf16 v[240:243],v[92:95],v[132:139],v13
s_waitcnt lgkmcnt(5)
ds_read_b128 v[136:139],v118 offset:128
ds_read_b128 v[132:135],v114 offset:3616
v_add_f32 v123,v123,v239
v_fmac_f32 v127,v239,v239
v_add_u32 v112,v112,v8
v_add_u32 v116,v116,v8
v_mfma_f32_16x16x32_bf16 v[220:223],v[96:99],v[152:155],0
ds_read_b128 v[92:95],v12 offset:11520
ds_read_b128 v[104:107],v14 offset:0
v_add_f32 v120,v120,v216
v_fmac_f32 v124,v216,v216
v_add_f32 v120,v120,v217
v_fmac_f32 v124,v217,v217
v_mfma_f32_16x16x32_bf16 v[244:247],v[96:99],v[156:159],0
s_waitcnt lgkmcnt(7)
ds_read_b128 v[108:111],v4 offset:3584
ds_read_b128 v[156:159],v119 offset:3616
v_add_f32 v121,v121,v218
v_fmac_f32 v125,v218,v218
v_add_f32 v121,v121,v219
v_fmac_f32 v125,v219,v219
v_smfmac_f32_16x16x64_bf16 v[220:223],v[100:103],v[144:151],v13
ds_read_b128 v[96:99],v11 offset:13440
ds_read_b128 v[144:147],v115 offset:128
v_add_f32 v122,v122,v240
v_fmac_f32 v126,v240,v240
v_add_f32 v122,v122,v241
v_fmac_f32 v126,v241,v241
v_smfmac_f32_16x16x64_bf16 v[244:247],v[100:103],v[148:155],v13
s_waitcnt lgkmcnt(7)
ds_read_b128 v[152:155],v119 offset:128
ds_read_b128 v[148:151],v115 offset:3616
v_add_f32 v123,v123,v242
v_fmac_f32 v127,v242,v242
v_add_f32 v123,v123,v243
v_fmac_f32 v127,v243,v243
v_mfma_f32_16x16x32_bf16 v[224:227],v[88:91],v[136:139],0
ds_read_b128 v[100:103],v12 offset:12672
v_add_f32 v120,v120,v220
v_fmac_f32 v124,v220,v220
v_add_f32 v120,v120,v221
v_fmac_f32 v124,v221,v221
v_mfma_f32_16x16x32_bf16 v[248:251],v[88:91],v[140:143],0
s_waitcnt lgkmcnt(8)
ds_read_b128 v[140:143],v116 offset:192
ds_read_b128 v[88:91],v112 offset:3616
v_add_f32 v121,v121,v222
v_fmac_f32 v125,v222,v222
v_add_f32 v121,v121,v223
v_fmac_f32 v125,v223,v223
v_smfmac_f32_16x16x64_bf16 v[224:227],v[92:95],v[128:135],v13
ds_read_b128 v[128:131],v112 offset:3488
v_add_f32 v122,v122,v244
v_fmac_f32 v126,v244,v244
v_add_f32 v122,v122,v245
v_fmac_f32 v126,v245,v245
v_smfmac_f32_16x16x64_bf16 v[248:251],v[92:95],v[132:139],v13
s_waitcnt lgkmcnt(5)
ds_read_b128 v[136:139],v116 offset:0
ds_read_b128 v[132:135],v112 offset:3680
v_add_f32 v123,v123,v246
v_fmac_f32 v127,v246,v246
v_add_f32 v123,v123,v247
v_fmac_f32 v127,v247,v247
v_mfma_f32_16x16x32_bf16 v[228:231],v[96:99],v[152:155],0
ds_read_b128 v[92:95],v112 offset:3808
v_add_f32 v120,v120,v224
v_fmac_f32 v124,v224,v224
v_add_f32 v120,v120,v225
v_fmac_f32 v124,v225,v225
v_mfma_f32_16x16x32_bf16 v[252:255],v[96:99],v[156:159],0
s_waitcnt lgkmcnt(6)
ds_read_b128 v[156:159],v116 offset:256
ds_read_b128 v[96:99],v116 offset:128
v_add_f32 v121,v121,v226
v_fmac_f32 v125,v226,v226
v_add_f32 v121,v121,v227
v_fmac_f32 v125,v227,v227
v_smfmac_f32_16x16x64_bf16 v[228:231],v[100:103],v[144:151],v13
ds_read_b128 v[144:147],v112 offset:3552
v_add_f32 v122,v122,v248
v_fmac_f32 v126,v248,v248
v_add_f32 v122,v122,v249
v_fmac_f32 v126,v249,v249
v_smfmac_f32_16x16x64_bf16 v[252:255],v[100:103],v[148:155],v13
ds_read_b128 v[152:155],v116 offset:64
ds_read_b128 v[148:151],v112 offset:3744
v_add_f32 v123,v123,v250
v_fmac_f32 v127,v250,v250
v_add_f32 v123,v123,v251
v_fmac_f32 v127,v251,v251
ds_read_b128 v[100:103],v116 offset:320
v_add_f32 v120,v120,v228
v_fmac_f32 v124,v228,v228
v_add_f32 v120,v120,v229
v_fmac_f32 v124,v229,v229
v_add_f32 v121,v121,v230
v_fmac_f32 v125,v230,v230
v_add_f32 v121,v121,v231
v_fmac_f32 v125,v231,v231
s_waitcnt lgkmcnt(0)
s_barrier
v_add_f32 v122,v122,v252
v_fmac_f32 v126,v252,v252
v_add_f32 v122,v122,v253
v_fmac_f32 v126,v253,v253
v_add_f32 v123,v123,v254
v_fmac_f32 v127,v254,v254
v_add_f32 v123,v123,v255
v_fmac_f32 v127,v255,v255
ds_read_b128 v[56:59],v14 offset:256
ds_read_b128 v[60:63],v4 offset:3616
v_permlane32_swap_b32 v120,v122
v_permlane32_swap_b32 v121,v123
v_permlane32_swap_b32 v124,v126
v_permlane32_swap_b32 v125,v127
v_add_f32 v120,v120,v122
v_add_f32 v121,v121,v123
v_add_f32 v124,v124,v126
v_add_f32 v125,v125,v127
v_permlane16_swap_b32 v120,v121
s_nop 0
v_permlane16_swap_b32 v124,v125
v_add_f32 v113,v120,v121
v_add_f32 v114,v124,v125
v_mul_f32 v113,0x3c2aaaab,v113
v_mul_f32 v114,0x3c2aaaab,v114
v_mul_f32 v115,0x5e000000,v114
v_fma_f32 v114,-v113,v113,v114
v_pk_maximum3_f16 v7,v7,v115,v115 op_sel:[0,1,1] op_sel_hi:[1,1,1]
v_max_f32 v114,0,v114
v_add_f32 v114,0x3727c5ac,v114
v_rsq_f32 v114,v114
s_nop 0
v_mul_f32 v120,0x8befe000,v114
v_mul_f32 v121,0x8befe000,v114
v_mul_f32 v124,-v113,v120
v_mul_f32 v125,-v113,v120
v_permlane16_swap_b32 v120,v121
s_nop 0
v_permlane16_swap_b32 v124,v125
v_mov_b32 v122,v120
v_mov_b32 v123,v121
v_mov_b32 v126,v124
v_mov_b32 v127,v125
v_permlane32_swap_b32 v120,v122
v_permlane32_swap_b32 v124,v126
v_permlane32_swap_b32 v121,v123
v_permlane32_swap_b32 v125,v127
v_fma_f32 v160,v160,v120,v124
ds_read_b64_tr_b16 v[64:65],v1 offset:12832
v_fma_f32 v161,v161,v120,v124
ds_read_b64_tr_b16 v[66:67],v1 offset:14880
v_fma_f32 v162,v162,v121,v125
ds_read_b128 v[112:115],v4 offset:128
v_fma_f32 v163,v163,v121,v125
ds_read_b64_tr_b16 v[68:69],v2 offset:12832
v_fma_f32 v164,v164,v120,v124
ds_read_b64_tr_b16 v[70:71],v2 offset:14880
v_fma_f32 v165,v165,v120,v124
ds_read_b128 v[116:119],v4 offset:160
v_fma_f32 v166,v166,v121,v125
ds_read_b64_tr_b16 v[72:73],v1 offset:16928
v_fma_f32 v167,v167,v121,v125
ds_read_b64_tr_b16 v[74:75],v1 offset:18976
v_fma_f32 v208,v208,v120,v124
ds_read_b64_tr_b16 v[76:77],v2 offset:16928
v_fma_f32 v209,v209,v120,v124
ds_read_b64_tr_b16 v[78:79],v2 offset:18976
v_fma_f32 v210,v210,v121,v125
ds_read_b64_tr_b16 v[80:81],v1 offset:21024
v_fma_f32 v211,v211,v121,v125
ds_read_b64_tr_b16 v[82:83],v1 offset:23072
v_fma_f32 v212,v212,v120,v124
ds_read_b64_tr_b16 v[84:85],v2 offset:21024
v_fma_f32 v213,v213,v120,v124
ds_read_b64_tr_b16 v[86:87],v2 offset:23072
v_fma_f32 v214,v214,v121,v125
v_fma_f32 v215,v215,v121,v125
v_cvt_pk_bf16_f32 v16,v160,v161
v_cvt_pk_bf16_f32 v20,v162,v163
v_cvt_pk_bf16_f32 v17,v164,v165
v_cvt_pk_bf16_f32 v21,v166,v167
v_cvt_pk_bf16_f32 v18,v208,v209
v_cvt_pk_bf16_f32 v22,v210,v211
v_cvt_pk_bf16_f32 v19,v212,v213
v_cvt_pk_bf16_f32 v23,v214,v215
v_fma_f32 v168,v168,v120,v124
v_fma_f32 v169,v169,v120,v124
v_mfma_f32_16x16x32_bf16 v[160:163],v[104:107],v[128:131],v[108:111]
v_fma_f32 v170,v170,v121,v125
v_fma_f32 v171,v171,v121,v125
v_fma_f32 v172,v172,v120,v124
v_fma_f32 v173,v173,v120,v124
v_fma_f32 v174,v174,v121,v125
v_fma_f32 v175,v175,v121,v125
v_mfma_f32_16x16x32_bf16 v[164:167],v[104:107],v[132:135],v[108:111]
v_fma_f32 v216,v216,v120,v124
v_fma_f32 v217,v217,v120,v124
v_fma_f32 v218,v218,v121,v125
v_fma_f32 v219,v219,v121,v125
v_fma_f32 v220,v220,v120,v124
v_mfma_f32_16x16x32_bf16 v[208:211],v[104:107],v[136:139],v[108:111]
v_fma_f32 v221,v221,v120,v124
v_fma_f32 v222,v222,v121,v125
v_fma_f32 v223,v223,v121,v125
v_cvt_pk_bf16_f32 v24,v168,v169
v_cvt_pk_bf16_f32 v28,v170,v171
v_mfma_f32_16x16x32_bf16 v[212:215],v[104:107],v[140:143],v[108:111]
ds_read_b128 v[104:107],v14 offset:512
ds_read_b128 v[108:111],v4 offset:3712
v_cvt_pk_bf16_f32 v25,v172,v173
v_cvt_pk_bf16_f32 v29,v174,v175
v_cvt_pk_bf16_f32 v26,v216,v217
v_cvt_pk_bf16_f32 v30,v218,v219
v_cvt_pk_bf16_f32 v27,v220,v221
v_cvt_pk_bf16_f32 v31,v222,v223
v_fma_f32 v176,v176,v120,v124
v_fma_f32 v177,v177,v120,v124
s_waitcnt lgkmcnt(15)
v_mfma_f32_16x16x32_bf16 v[168:171],v[56:59],v[128:131],v[60:63]
v_fma_f32 v178,v178,v121,v125
v_fma_f32 v179,v179,v121,v125
v_fma_f32 v180,v180,v120,v124
v_fma_f32 v181,v181,v120,v124
v_fma_f32 v182,v182,v121,v125
v_fma_f32 v183,v183,v121,v125
v_mfma_f32_16x16x32_bf16 v[172:175],v[56:59],v[132:135],v[60:63]
v_fma_f32 v224,v224,v120,v124
v_fma_f32 v225,v225,v120,v124
v_fma_f32 v226,v226,v121,v125
v_fma_f32 v227,v227,v121,v125
v_fma_f32 v228,v228,v120,v124
v_mfma_f32_16x16x32_bf16 v[216:219],v[56:59],v[136:139],v[60:63]
v_fma_f32 v229,v229,v120,v124
v_fma_f32 v230,v230,v121,v125
v_fma_f32 v231,v231,v121,v125
v_cvt_pk_bf16_f32 v32,v176,v177
v_cvt_pk_bf16_f32 v36,v178,v179
v_mfma_f32_16x16x32_bf16 v[220:223],v[56:59],v[140:143],v[60:63]
ds_read_b128 v[56:59],v14 offset:768
ds_read_b128 v[60:63],v4 offset:3744
v_cvt_pk_bf16_f32 v33,v180,v181
v_cvt_pk_bf16_f32 v37,v182,v183
v_cvt_pk_bf16_f32 v34,v224,v225
v_cvt_pk_bf16_f32 v38,v226,v227
v_cvt_pk_bf16_f32 v35,v228,v229
v_cvt_pk_bf16_f32 v39,v230,v231
v_fma_f32 v184,v184,v122,v126
s_waitcnt lgkmcnt(2)
v_mfma_f32_16x16x32_bf16 v[176:179],v[104:107],v[144:147],v[108:111]
v_fma_f32 v185,v185,v122,v126
v_fma_f32 v186,v186,v123,v127
v_fma_f32 v187,v187,v123,v127
v_mfma_f32_16x16x32_bf16 v[128:131],v[64:67],v[16:19],v[112:115]
v_fma_f32 v188,v188,v122,v126
v_fma_f32 v189,v189,v122,v126
v_fma_f32 v190,v190,v123,v127
v_mfma_f32_16x16x32_bf16 v[180:183],v[104:107],v[148:151],v[108:111]
v_fma_f32 v191,v191,v123,v127
v_fma_f32 v232,v232,v122,v126
v_fma_f32 v233,v233,v122,v126
v_mfma_f32_16x16x32_bf16 v[136:139],v[64:67],v[20:23],v[112:115]
v_fma_f32 v234,v234,v123,v127
v_fma_f32 v235,v235,v123,v127
v_fma_f32 v236,v236,v122,v126
v_mfma_f32_16x16x32_bf16 v[224:227],v[104:107],v[152:155],v[108:111]
v_fma_f32 v237,v237,v122,v126
v_fma_f32 v238,v238,v123,v127
v_fma_f32 v239,v239,v123,v127
v_mfma_f32_16x16x32_bf16 v[140:143],v[68:71],v[20:23],v[116:119]
v_cvt_pk_bf16_f32 v40,v184,v185
v_cvt_pk_bf16_f32 v44,v186,v187
v_cvt_pk_bf16_f32 v41,v188,v189
v_mfma_f32_16x16x32_bf16 v[228:231],v[104:107],v[156:159],v[108:111]
ds_read_b128 v[104:107],v14 offset:1024
ds_read_b128 v[108:111],v4 offset:3840
v_cvt_pk_bf16_f32 v45,v190,v191
v_cvt_pk_bf16_f32 v42,v232,v233
v_cvt_pk_bf16_f32 v46,v234,v235
v_mfma_f32_16x16x32_bf16 v[132:135],v[68:71],v[16:19],v[116:119]
v_cvt_pk_bf16_f32 v43,v236,v237
v_cvt_pk_bf16_f32 v47,v238,v239
v_fma_f32 v192,v192,v122,v126
s_waitcnt lgkmcnt(2)
v_mfma_f32_16x16x32_bf16 v[184:187],v[56:59],v[144:147],v[60:63]
v_fma_f32 v193,v193,v122,v126
v_fma_f32 v194,v194,v123,v127
v_mfma_f32_16x16x32_bf16 v[128:131],v[72:75],v[24:27],v[128:131]
v_fma_f32 v195,v195,v123,v127
v_fma_f32 v196,v196,v122,v126
v_mfma_f32_16x16x32_bf16 v[188:191],v[56:59],v[148:151],v[60:63]
v_fma_f32 v197,v197,v122,v126
v_fma_f32 v198,v198,v123,v127
v_mfma_f32_16x16x32_bf16 v[136:139],v[72:75],v[28:31],v[136:139]
v_fma_f32 v199,v199,v123,v127
v_fma_f32 v240,v240,v122,v126
v_mfma_f32_16x16x32_bf16 v[232:235],v[56:59],v[152:155],v[60:63]
v_fma_f32 v241,v241,v122,v126
v_fma_f32 v242,v242,v123,v127
v_mfma_f32_16x16x32_bf16 v[140:143],v[76:79],v[28:31],v[140:143]
v_fma_f32 v243,v243,v123,v127
v_fma_f32 v244,v244,v122,v126
v_mfma_f32_16x16x32_bf16 v[236:239],v[56:59],v[156:159],v[60:63]
ds_read_b128 v[144:147],v14 offset:1280
ds_read_b128 v[148:151],v4 offset:3872
v_fma_f32 v245,v245,v122,v126
v_fma_f32 v246,v246,v123,v127
v_mfma_f32_16x16x32_bf16 v[132:135],v[76:79],v[24:27],v[132:135]
v_fma_f32 v247,v247,v123,v127
v_cvt_pk_bf16_f32 v48,v192,v193
v_cvt_pk_bf16_f32 v52,v194,v195
v_cvt_pk_bf16_f32 v49,v196,v197
v_cvt_pk_bf16_f32 v53,v198,v199
v_cvt_pk_bf16_f32 v50,v240,v241
v_cvt_pk_bf16_f32 v54,v242,v243
v_cvt_pk_bf16_f32 v51,v244,v245
v_cvt_pk_bf16_f32 v55,v246,v247
v_fma_f32 v200,v200,v122,v126
s_waitcnt lgkmcnt(2)
v_mfma_f32_16x16x32_bf16 v[192:195],v[104:107],v[88:91],v[108:111]
v_fma_f32 v201,v201,v122,v126
v_fma_f32 v202,v202,v123,v127
v_fma_f32 v203,v203,v123,v127
v_mfma_f32_16x16x32_bf16 v[128:131],v[80:83],v[32:35],v[128:131]
v_fma_f32 v204,v204,v122,v126
v_fma_f32 v205,v205,v122,v126
v_fma_f32 v206,v206,v123,v127
v_mfma_f32_16x16x32_bf16 v[196:199],v[104:107],v[92:95],v[108:111]
v_fma_f32 v207,v207,v123,v127
v_fma_f32 v248,v248,v122,v126
v_fma_f32 v249,v249,v122,v126
v_mfma_f32_16x16x32_bf16 v[136:139],v[80:83],v[36:39],v[136:139]
v_fma_f32 v250,v250,v123,v127
v_fma_f32 v251,v251,v123,v127
v_fma_f32 v252,v252,v122,v126
v_mfma_f32_16x16x32_bf16 v[240:243],v[104:107],v[96:99],v[108:111]
v_fma_f32 v253,v253,v122,v126
v_fma_f32 v254,v254,v123,v127
v_fma_f32 v255,v255,v123,v127
v_mfma_f32_16x16x32_bf16 v[140:143],v[84:87],v[36:39],v[140:143]
v_cvt_pk_bf16_f32 v56,v200,v201
v_cvt_pk_bf16_f32 v60,v202,v203
v_cvt_pk_bf16_f32 v57,v204,v205
v_mfma_f32_16x16x32_bf16 v[244:247],v[104:107],v[100:103],v[108:111]
v_cvt_pk_bf16_f32 v61,v206,v207
v_cvt_pk_bf16_f32 v58,v248,v249
v_cvt_pk_bf16_f32 v62,v250,v251
v_mfma_f32_16x16x32_bf16 v[132:135],v[84:87],v[32:35],v[132:135]
v_cvt_pk_bf16_f32 v59,v252,v253
v_cvt_pk_bf16_f32 v63,v254,v255
s_waitcnt lgkmcnt(0)
v_mfma_f32_16x16x32_bf16 v[200:203],v[144:147],v[88:91],v[148:151]
s_barrier
s_setprio 2
ds_read_b64_tr_b16 v[104:105],v3 offset:128
v_lshlrev_b32_sdwa v128,1,v128 dst_sel:DWORD dst_unused:UNUSED_PAD src0_sel:DWORD src1_sel:WORD_1
v_lshlrev_b32_sdwa v130,1,v130 dst_sel:DWORD dst_unused:UNUSED_PAD src0_sel:DWORD src1_sel:WORD_1
v_mfma_f32_16x16x32_bf16 v[204:207],v[144:147],v[92:95],v[148:151]
ds_read_u16 v128,v128
ds_read_b64_tr_b16 v[106:107],v3 offset:1664
v_lshlrev_b32_sdwa v129,1,v129 dst_sel:DWORD dst_unused:UNUSED_PAD src0_sel:DWORD src1_sel:WORD_1
v_mfma_f32_16x16x32_bf16 v[248:251],v[144:147],v[96:99],v[148:151]
ds_read_u16_d16_hi v130,v130
ds_read_b64_tr_b16 v[108:109],v3 offset:136
v_lshlrev_b32_sdwa v131,1,v131 dst_sel:DWORD dst_unused:UNUSED_PAD src0_sel:DWORD src1_sel:WORD_1
v_mfma_f32_16x16x32_bf16 v[252:255],v[144:147],v[100:103],v[148:151]
ds_read_u16 v129,v129
ds_read_b64_tr_b16 v[110:111],v3 offset:1672
v_lshlrev_b32_sdwa v132,1,v132 dst_sel:DWORD dst_unused:UNUSED_PAD src0_sel:DWORD src1_sel:WORD_1
v_mfma_f32_16x16x32_bf16 v[144:147],v[64:67],v[40:43],v[112:115]
ds_read_u16_d16_hi v131,v131
ds_read_b64_tr_b16 v[88:89],v3 offset:0
v_lshlrev_b32_sdwa v134,1,v134 dst_sel:DWORD dst_unused:UNUSED_PAD src0_sel:DWORD src1_sel:WORD_1
v_lshlrev_b32_sdwa v133,1,v133 dst_sel:DWORD dst_unused:UNUSED_PAD src0_sel:DWORD src1_sel:WORD_1
v_mfma_f32_16x16x32_bf16 v[152:155],v[64:67],v[44:47],v[112:115]
ds_read_u16 v132,v132
ds_read_b64_tr_b16 v[90:91],v3 offset:1536
v_lshlrev_b32_sdwa v135,1,v135 dst_sel:DWORD dst_unused:UNUSED_PAD src0_sel:DWORD src1_sel:WORD_1
v_mfma_f32_16x16x32_bf16 v[156:159],v[68:71],v[44:47],v[116:119]
ds_read_u16_d16_hi v134,v134
ds_read_b64_tr_b16 v[92:93],v3 offset:8
v_lshlrev_b32_sdwa v136,1,v136 dst_sel:DWORD dst_unused:UNUSED_PAD src0_sel:DWORD src1_sel:WORD_1
v_mfma_f32_16x16x32_bf16 v[148:151],v[68:71],v[40:43],v[116:119]
ds_read_u16 v133,v133
ds_read_b64_tr_b16 v[94:95],v3 offset:1544
v_lshlrev_b32_sdwa v138,1,v138 dst_sel:DWORD dst_unused:UNUSED_PAD src0_sel:DWORD src1_sel:WORD_1
v_mfma_f32_16x16x32_bf16 v[144:147],v[72:75],v[48:51],v[144:147]
ds_read_u16_d16_hi v135,v135
ds_read_b64_tr_b16 v[96:97],v3 offset:64
v_lshlrev_b32_sdwa v137,1,v137 dst_sel:DWORD dst_unused:UNUSED_PAD src0_sel:DWORD src1_sel:WORD_1
v_mfma_f32_16x16x32_bf16 v[152:155],v[72:75],v[52:55],v[152:155]
ds_read_u16 v136,v136
ds_read_b64_tr_b16 v[98:99],v3 offset:1600
v_lshlrev_b32_sdwa v139,1,v139 dst_sel:DWORD dst_unused:UNUSED_PAD src0_sel:DWORD src1_sel:WORD_1
v_lshlrev_b32_sdwa v140,1,v140 dst_sel:DWORD dst_unused:UNUSED_PAD src0_sel:DWORD src1_sel:WORD_1
v_mfma_f32_16x16x32_bf16 v[156:159],v[76:79],v[52:55],v[156:159]
ds_read_u16_d16_hi v138,v138
ds_read_b64_tr_b16 v[100:101],v3 offset:72
v_lshlrev_b32_sdwa v142,1,v142 dst_sel:DWORD dst_unused:UNUSED_PAD src0_sel:DWORD src1_sel:WORD_1
v_mfma_f32_16x16x32_bf16 v[148:151],v[76:79],v[48:51],v[148:151]
ds_read_u16 v137,v137
ds_read_b64_tr_b16 v[102:103],v3 offset:1608
v_lshlrev_b32_sdwa v141,1,v141 dst_sel:DWORD dst_unused:UNUSED_PAD src0_sel:DWORD src1_sel:WORD_1
v_mfma_f32_16x16x32_bf16 v[144:147],v[80:83],v[56:59],v[144:147]
ds_read_u16_d16_hi v139,v139
ds_read_b64_tr_b16 v[64:65],v1 offset:0
v_lshlrev_b32_sdwa v143,1,v143 dst_sel:DWORD dst_unused:UNUSED_PAD src0_sel:DWORD src1_sel:WORD_1
v_mfma_f32_16x16x32_bf16 v[152:155],v[80:83],v[60:63],v[152:155]
ds_read_u16 v140,v140
ds_read_b64_tr_b16 v[66:67],v1 offset:2048
s_waitcnt lgkmcnt(15)
v_lshl_add_u64 v[128:129],v[128:129],0,v[130:131]
v_mfma_f32_16x16x32_bf16 v[156:159],v[84:87],v[60:63],v[156:159]
ds_read_u16_d16_hi v142,v142
ds_read_b128 v[112:115],v4 offset:256
s_waitcnt lgkmcnt(13)
v_lshl_add_u64 v[130:131],v[132:133],0,v[134:135]
v_mfma_f32_16x16x32_bf16 v[148:151],v[84:87],v[56:59],v[148:151]
ds_read_u16 v141,v141
ds_read_b64_tr_b16 v[68:69],v2 offset:0
v_lshlrev_b32_sdwa v144,1,v144 dst_sel:DWORD dst_unused:UNUSED_PAD src0_sel:DWORD src1_sel:WORD_1
v_lshlrev_b32_sdwa v146,1,v146 dst_sel:DWORD dst_unused:UNUSED_PAD src0_sel:DWORD src1_sel:WORD_1
v_mfma_f32_16x16x32_bf16 v[160:163],v[88:91],v[128:131],v[160:163]
ds_read_u16_d16_hi v143,v143
v_lshlrev_b32_sdwa v145,1,v145 dst_sel:DWORD dst_unused:UNUSED_PAD src0_sel:DWORD src1_sel:WORD_1
v_mfma_f32_16x16x32_bf16 v[168:171],v[92:95],v[128:131],v[168:171]
s_waitcnt lgkmcnt(13)
ds_read_u16 v144,v144
v_lshlrev_b32_sdwa v147,1,v147 dst_sel:DWORD dst_unused:UNUSED_PAD src0_sel:DWORD src1_sel:WORD_1
v_mfma_f32_16x16x32_bf16 v[176:179],v[96:99],v[128:131],v[176:179]
s_waitcnt lgkmcnt(10)
ds_read_b64_tr_b16 v[70:71],v2 offset:2048
ds_read_b128 v[116:119],v4 offset:288
v_lshlrev_b32_sdwa v148,1,v148 dst_sel:DWORD dst_unused:UNUSED_PAD src0_sel:DWORD src1_sel:WORD_1
v_mfma_f32_16x16x32_bf16 v[184:187],v[100:103],v[128:131],v[184:187]
s_waitcnt lgkmcnt(11)
ds_read_u16_d16_hi v146,v146
v_lshl_add_u64 v[136:137],v[136:137],0,v[138:139]
v_lshlrev_b32_sdwa v150,1,v150 dst_sel:DWORD dst_unused:UNUSED_PAD src0_sel:DWORD src1_sel:WORD_1
v_mfma_f32_16x16x32_bf16 v[192:195],v[104:107],v[128:131],v[192:195]
s_waitcnt lgkmcnt(4)
ds_read_u16 v145,v145
v_lshl_add_u64 v[138:139],v[140:141],0,v[142:143]
v_mfma_f32_16x16x32_bf16 v[200:203],v[108:111],v[128:131],v[200:203]
ds_read_b64_tr_b16 v[72:73],v1 offset:4096
ds_read_b64_tr_b16 v[74:75],v1 offset:6144
v_lshlrev_b32_sdwa v149,1,v149 dst_sel:DWORD dst_unused:UNUSED_PAD src0_sel:DWORD src1_sel:WORD_1
v_mfma_f32_16x16x32_bf16 v[164:167],v[88:91],v[136:139],v[164:167]
ds_read_u16_d16_hi v147,v147
v_lshlrev_b32_sdwa v151,1,v151 dst_sel:DWORD dst_unused:UNUSED_PAD src0_sel:DWORD src1_sel:WORD_1
v_mfma_f32_16x16x32_bf16 v[172:175],v[92:95],v[136:139],v[172:175]
ds_read_u16 v148,v148
ds_read_b64_tr_b16 v[76:77],v2 offset:4096
v_lshlrev_b32_sdwa v152,1,v152 dst_sel:DWORD dst_unused:UNUSED_PAD src0_sel:DWORD src1_sel:WORD_1
v_mfma_f32_16x16x32_bf16 v[180:183],v[96:99],v[136:139],v[180:183]
ds_read_b64_tr_b16 v[78:79],v2 offset:6144
ds_read_b64_tr_b16 v[80:81],v1 offset:8192
v_lshlrev_b32_sdwa v154,1,v154 dst_sel:DWORD dst_unused:UNUSED_PAD src0_sel:DWORD src1_sel:WORD_1
v_mfma_f32_16x16x32_bf16 v[188:191],v[100:103],v[136:139],v[188:191]
ds_read_u16_d16_hi v150,v150
v_lshlrev_b32_sdwa v153,1,v153 dst_sel:DWORD dst_unused:UNUSED_PAD src0_sel:DWORD src1_sel:WORD_1
v_mfma_f32_16x16x32_bf16 v[196:199],v[104:107],v[136:139],v[196:199]
ds_read_u16 v149,v149
v_lshlrev_b32_sdwa v155,1,v155 dst_sel:DWORD dst_unused:UNUSED_PAD src0_sel:DWORD src1_sel:WORD_1
v_mfma_f32_16x16x32_bf16 v[204:207],v[108:111],v[136:139],v[204:207]
ds_read_b64_tr_b16 v[82:83],v1 offset:10240
ds_read_b64_tr_b16 v[84:85],v2 offset:8192
v_lshlrev_b32_sdwa v156,1,v156 dst_sel:DWORD dst_unused:UNUSED_PAD src0_sel:DWORD src1_sel:WORD_1
v_mfma_f32_16x16x32_bf16 v[128:131],v[64:67],v[16:19],v[112:115]
ds_read_u16_d16_hi v151,v151
v_lshlrev_b32_sdwa v158,1,v158 dst_sel:DWORD dst_unused:UNUSED_PAD src0_sel:DWORD src1_sel:WORD_1
v_mfma_f32_16x16x32_bf16 v[136:139],v[64:67],v[20:23],v[112:115]
s_waitcnt lgkmcnt(14)
ds_read_u16 v152,v152
ds_read_b64_tr_b16 v[86:87],v2 offset:10240
v_lshlrev_b32_sdwa v157,1,v157 dst_sel:DWORD dst_unused:UNUSED_PAD src0_sel:DWORD src1_sel:WORD_1
v_mfma_f32_16x16x32_bf16 v[140:143],v[68:71],v[20:23],v[116:119]
v_lshlrev_b32_sdwa v159,1,v159 dst_sel:DWORD dst_unused:UNUSED_PAD src0_sel:DWORD src1_sel:WORD_1
v_mfma_f32_16x16x32_bf16 v[132:135],v[68:71],v[16:19],v[116:119]
s_waitcnt lgkmcnt(12)
ds_read_u16_d16_hi v154,v154
v_mfma_f32_16x16x32_bf16 v[128:131],v[72:75],v[24:27],v[128:131]
ds_read_u16 v153,v153
v_mfma_f32_16x16x32_bf16 v[136:139],v[72:75],v[28:31],v[136:139]
s_waitcnt lgkmcnt(10)
v_mfma_f32_16x16x32_bf16 v[140:143],v[76:79],v[28:31],v[140:143]
ds_read_u16_d16_hi v155,v155
v_mfma_f32_16x16x32_bf16 v[132:135],v[76:79],v[24:27],v[132:135]
s_waitcnt lgkmcnt(7)
ds_read_u16 v156,v156
v_lshl_add_u64 v[144:145],v[144:145],0,v[146:147]
v_mfma_f32_16x16x32_bf16 v[128:131],v[80:83],v[32:35],v[128:131]
s_waitcnt lgkmcnt(6)
v_lshl_add_u64 v[146:147],v[148:149],0,v[150:151]
v_mfma_f32_16x16x32_bf16 v[136:139],v[80:83],v[36:39],v[136:139]
s_waitcnt lgkmcnt(4)
ds_read_u16_d16_hi v158,v158
v_mfma_f32_16x16x32_bf16 v[140:143],v[84:87],v[36:39],v[140:143]
ds_read_u16 v157,v157
v_mfma_f32_16x16x32_bf16 v[132:135],v[84:87],v[32:35],v[132:135]
v_lshlrev_b32_sdwa v128,1,v128 dst_sel:DWORD dst_unused:UNUSED_PAD src0_sel:DWORD src1_sel:WORD_1
v_lshlrev_b32_sdwa v130,1,v130 dst_sel:DWORD dst_unused:UNUSED_PAD src0_sel:DWORD src1_sel:WORD_1
v_mfma_f32_16x16x32_bf16 v[208:211],v[88:91],v[144:147],v[208:211]
ds_read_u16_d16_hi v159,v159
v_lshlrev_b32_sdwa v129,1,v129 dst_sel:DWORD dst_unused:UNUSED_PAD src0_sel:DWORD src1_sel:WORD_1
v_mfma_f32_16x16x32_bf16 v[216:219],v[92:95],v[144:147],v[216:219]
ds_read_u16 v128,v128
v_lshlrev_b32_sdwa v131,1,v131 dst_sel:DWORD dst_unused:UNUSED_PAD src0_sel:DWORD src1_sel:WORD_1
v_mfma_f32_16x16x32_bf16 v[224:227],v[96:99],v[144:147],v[224:227]
v_lshlrev_b32_sdwa v132,1,v132 dst_sel:DWORD dst_unused:UNUSED_PAD src0_sel:DWORD src1_sel:WORD_1
v_mfma_f32_16x16x32_bf16 v[232:235],v[100:103],v[144:147],v[232:235]
s_waitcnt lgkmcnt(5)
ds_read_u16_d16_hi v130,v130
v_lshl_add_u64 v[152:153],v[152:153],0,v[154:155]
v_lshlrev_b32_sdwa v134,1,v134 dst_sel:DWORD dst_unused:UNUSED_PAD src0_sel:DWORD src1_sel:WORD_1
v_mfma_f32_16x16x32_bf16 v[240:243],v[104:107],v[144:147],v[240:243]
s_waitcnt lgkmcnt(2)
ds_read_u16 v129,v129
v_lshl_add_u64 v[154:155],v[156:157],0,v[158:159]
v_mfma_f32_16x16x32_bf16 v[248:251],v[108:111],v[144:147],v[248:251]
v_lshlrev_b32_sdwa v133,1,v133 dst_sel:DWORD dst_unused:UNUSED_PAD src0_sel:DWORD src1_sel:WORD_1
v_mfma_f32_16x16x32_bf16 v[212:215],v[88:91],v[152:155],v[212:215]
ds_read_u16_d16_hi v131,v131
v_lshlrev_b32_sdwa v135,1,v135 dst_sel:DWORD dst_unused:UNUSED_PAD src0_sel:DWORD src1_sel:WORD_1
v_mfma_f32_16x16x32_bf16 v[220:223],v[92:95],v[152:155],v[220:223]
ds_read_u16 v132,v132
v_lshlrev_b32_sdwa v136,1,v136 dst_sel:DWORD dst_unused:UNUSED_PAD src0_sel:DWORD src1_sel:WORD_1
v_mfma_f32_16x16x32_bf16 v[228:231],v[96:99],v[152:155],v[228:231]
ds_read_b64_tr_b16 v[88:89],v3 offset:6160
ds_read_b64_tr_b16 v[90:91],v3 offset:7696
v_lshlrev_b32_sdwa v138,1,v138 dst_sel:DWORD dst_unused:UNUSED_PAD src0_sel:DWORD src1_sel:WORD_1
v_mfma_f32_16x16x32_bf16 v[236:239],v[100:103],v[152:155],v[236:239]
ds_read_u16_d16_hi v134,v134
v_lshlrev_b32_sdwa v137,1,v137 dst_sel:DWORD dst_unused:UNUSED_PAD src0_sel:DWORD src1_sel:WORD_1
v_mfma_f32_16x16x32_bf16 v[244:247],v[104:107],v[152:155],v[244:247]
ds_read_u16 v133,v133
v_lshlrev_b32_sdwa v139,1,v139 dst_sel:DWORD dst_unused:UNUSED_PAD src0_sel:DWORD src1_sel:WORD_1
v_mfma_f32_16x16x32_bf16 v[252:255],v[108:111],v[152:155],v[252:255]
ds_read_b64_tr_b16 v[92:93],v3 offset:6168
ds_read_b64_tr_b16 v[94:95],v3 offset:7704
v_lshlrev_b32_sdwa v140,1,v140 dst_sel:DWORD dst_unused:UNUSED_PAD src0_sel:DWORD src1_sel:WORD_1
v_mfma_f32_16x16x32_bf16 v[144:147],v[64:67],v[40:43],v[112:115]
ds_read_u16_d16_hi v135,v135
v_lshlrev_b32_sdwa v142,1,v142 dst_sel:DWORD dst_unused:UNUSED_PAD src0_sel:DWORD src1_sel:WORD_1
v_mfma_f32_16x16x32_bf16 v[152:155],v[64:67],v[44:47],v[112:115]
ds_read_u16 v136,v136
v_lshlrev_b32_sdwa v141,1,v141 dst_sel:DWORD dst_unused:UNUSED_PAD src0_sel:DWORD src1_sel:WORD_1
v_mfma_f32_16x16x32_bf16 v[156:159],v[68:71],v[44:47],v[116:119]
ds_read_b64_tr_b16 v[96:97],v3 offset:6224
ds_read_b64_tr_b16 v[98:99],v3 offset:7760
v_lshlrev_b32_sdwa v143,1,v143 dst_sel:DWORD dst_unused:UNUSED_PAD src0_sel:DWORD src1_sel:WORD_1
v_mfma_f32_16x16x32_bf16 v[148:151],v[68:71],v[40:43],v[116:119]
ds_read_u16_d16_hi v138,v138
v_mfma_f32_16x16x32_bf16 v[144:147],v[72:75],v[48:51],v[144:147]
ds_read_u16 v137,v137
v_mfma_f32_16x16x32_bf16 v[152:155],v[72:75],v[52:55],v[152:155]
ds_read_b64_tr_b16 v[100:101],v3 offset:6232
ds_read_b64_tr_b16 v[102:103],v3 offset:7768
v_mfma_f32_16x16x32_bf16 v[156:159],v[76:79],v[52:55],v[156:159]
ds_read_u16_d16_hi v139,v139
v_mfma_f32_16x16x32_bf16 v[148:151],v[76:79],v[48:51],v[148:151]
s_waitcnt lgkmcnt(15)
ds_read_u16 v140,v140
v_lshl_add_u64 v[128:129],v[128:129],0,v[130:131]
v_mfma_f32_16x16x32_bf16 v[144:147],v[80:83],v[56:59],v[144:147]
s_waitcnt lgkmcnt(9)
ds_read_b64_tr_b16 v[104:105],v3 offset:6288
ds_read_b64_tr_b16 v[106:107],v3 offset:7824
v_lshl_add_u64 v[130:131],v[132:133],0,v[134:135]
v_mfma_f32_16x16x32_bf16 v[152:155],v[80:83],v[60:63],v[152:155]
ds_read_u16_d16_hi v142,v142
v_mfma_f32_16x16x32_bf16 v[156:159],v[84:87],v[60:63],v[156:159]
ds_read_u16 v141,v141
v_mfma_f32_16x16x32_bf16 v[148:151],v[84:87],v[56:59],v[148:151]
ds_read_b64_tr_b16 v[108:109],v3 offset:6296
ds_read_b64_tr_b16 v[110:111],v3 offset:7832
v_lshlrev_b32_sdwa v144,1,v144 dst_sel:DWORD dst_unused:UNUSED_PAD src0_sel:DWORD src1_sel:WORD_1
v_lshlrev_b32_sdwa v146,1,v146 dst_sel:DWORD dst_unused:UNUSED_PAD src0_sel:DWORD src1_sel:WORD_1
v_mfma_f32_16x16x32_bf16 v[160:163],v[88:91],v[128:131],v[160:163]
ds_read_u16_d16_hi v143,v143
v_lshlrev_b32_sdwa v145,1,v145 dst_sel:DWORD dst_unused:UNUSED_PAD src0_sel:DWORD src1_sel:WORD_1
v_mfma_f32_16x16x32_bf16 v[168:171],v[92:95],v[128:131],v[168:171]
s_waitcnt lgkmcnt(13)
ds_read_u16 v144,v144
v_lshlrev_b32_sdwa v147,1,v147 dst_sel:DWORD dst_unused:UNUSED_PAD src0_sel:DWORD src1_sel:WORD_1
v_mfma_f32_16x16x32_bf16 v[176:179],v[96:99],v[128:131],v[176:179]
s_waitcnt lgkmcnt(10)
ds_read_b64_tr_b16 v[64:65],v1 offset:512
ds_read_b64_tr_b16 v[66:67],v1 offset:2560
v_lshlrev_b32_sdwa v148,1,v148 dst_sel:DWORD dst_unused:UNUSED_PAD src0_sel:DWORD src1_sel:WORD_1
v_mfma_f32_16x16x32_bf16 v[184:187],v[100:103],v[128:131],v[184:187]
s_waitcnt lgkmcnt(8)
ds_read_u16_d16_hi v146,v146
v_lshl_add_u64 v[136:137],v[136:137],0,v[138:139]
v_lshlrev_b32_sdwa v150,1,v150 dst_sel:DWORD dst_unused:UNUSED_PAD src0_sel:DWORD src1_sel:WORD_1
v_mfma_f32_16x16x32_bf16 v[192:195],v[104:107],v[128:131],v[192:195]
s_waitcnt lgkmcnt(4)
ds_read_u16 v145,v145
v_lshl_add_u64 v[138:139],v[140:141],0,v[142:143]
v_mfma_f32_16x16x32_bf16 v[200:203],v[108:111],v[128:131],v[200:203]
ds_read_b128 v[112:115],v4 offset:384
ds_read_b64_tr_b16 v[68:69],v2 offset:512
v_lshlrev_b32_sdwa v149,1,v149 dst_sel:DWORD dst_unused:UNUSED_PAD src0_sel:DWORD src1_sel:WORD_1
v_mfma_f32_16x16x32_bf16 v[164:167],v[88:91],v[136:139],v[164:167]
ds_read_u16_d16_hi v147,v147
v_lshlrev_b32_sdwa v151,1,v151 dst_sel:DWORD dst_unused:UNUSED_PAD src0_sel:DWORD src1_sel:WORD_1
v_mfma_f32_16x16x32_bf16 v[172:175],v[92:95],v[136:139],v[172:175]
ds_read_u16 v148,v148
ds_read_b64_tr_b16 v[70:71],v2 offset:2560
v_lshlrev_b32_sdwa v152,1,v152 dst_sel:DWORD dst_unused:UNUSED_PAD src0_sel:DWORD src1_sel:WORD_1
v_mfma_f32_16x16x32_bf16 v[180:183],v[96:99],v[136:139],v[180:183]
ds_read_b128 v[116:119],v4 offset:416
ds_read_b64_tr_b16 v[72:73],v1 offset:4608
v_lshlrev_b32_sdwa v154,1,v154 dst_sel:DWORD dst_unused:UNUSED_PAD src0_sel:DWORD src1_sel:WORD_1
v_mfma_f32_16x16x32_bf16 v[188:191],v[100:103],v[136:139],v[188:191]
ds_read_u16_d16_hi v150,v150
v_lshlrev_b32_sdwa v153,1,v153 dst_sel:DWORD dst_unused:UNUSED_PAD src0_sel:DWORD src1_sel:WORD_1
v_mfma_f32_16x16x32_bf16 v[196:199],v[104:107],v[136:139],v[196:199]
ds_read_u16 v149,v149
v_lshlrev_b32_sdwa v155,1,v155 dst_sel:DWORD dst_unused:UNUSED_PAD src0_sel:DWORD src1_sel:WORD_1
v_mfma_f32_16x16x32_bf16 v[204:207],v[108:111],v[136:139],v[204:207]
s_waitcnt lgkmcnt(8)
ds_read_b64_tr_b16 v[74:75],v1 offset:6656
ds_read_b64_tr_b16 v[76:77],v2 offset:4608
v_lshlrev_b32_sdwa v156,1,v156 dst_sel:DWORD dst_unused:UNUSED_PAD src0_sel:DWORD src1_sel:WORD_1
v_mfma_f32_16x16x32_bf16 v[128:131],v[64:67],v[16:19],v[112:115]
ds_read_u16_d16_hi v151,v151
v_lshlrev_b32_sdwa v158,1,v158 dst_sel:DWORD dst_unused:UNUSED_PAD src0_sel:DWORD src1_sel:WORD_1
v_mfma_f32_16x16x32_bf16 v[136:139],v[64:67],v[20:23],v[112:115]
s_waitcnt lgkmcnt(6)
ds_read_u16 v152,v152
ds_read_b64_tr_b16 v[78:79],v2 offset:6656
v_lshlrev_b32_sdwa v157,1,v157 dst_sel:DWORD dst_unused:UNUSED_PAD src0_sel:DWORD src1_sel:WORD_1
v_mfma_f32_16x16x32_bf16 v[140:143],v[68:71],v[20:23],v[116:119]
ds_read_b64_tr_b16 v[80:81],v1 offset:8704
ds_read_b64_tr_b16 v[82:83],v1 offset:10752
v_lshlrev_b32_sdwa v159,1,v159 dst_sel:DWORD dst_unused:UNUSED_PAD src0_sel:DWORD src1_sel:WORD_1
v_mfma_f32_16x16x32_bf16 v[132:135],v[68:71],v[16:19],v[116:119]
s_waitcnt lgkmcnt(6)
ds_read_u16_d16_hi v154,v154
v_mfma_f32_16x16x32_bf16 v[128:131],v[72:75],v[24:27],v[128:131]
ds_read_u16 v153,v153
v_mfma_f32_16x16x32_bf16 v[136:139],v[72:75],v[28:31],v[136:139]
s_waitcnt lgkmcnt(4)
ds_read_b64_tr_b16 v[84:85],v2 offset:8704
ds_read_b64_tr_b16 v[86:87],v2 offset:10752
v_mfma_f32_16x16x32_bf16 v[140:143],v[76:79],v[28:31],v[140:143]
ds_read_u16_d16_hi v155,v155
v_mfma_f32_16x16x32_bf16 v[132:135],v[76:79],v[24:27],v[132:135]
s_waitcnt lgkmcnt(5)
ds_read_u16 v156,v156
v_lshl_add_u64 v[144:145],v[144:145],0,v[146:147]
v_mfma_f32_16x16x32_bf16 v[128:131],v[80:83],v[32:35],v[128:131]
v_lshl_add_u64 v[146:147],v[148:149],0,v[150:151]
v_mfma_f32_16x16x32_bf16 v[136:139],v[80:83],v[36:39],v[136:139]
s_waitcnt lgkmcnt(2)
ds_read_u16_d16_hi v158,v158
v_mfma_f32_16x16x32_bf16 v[140:143],v[84:87],v[36:39],v[140:143]
ds_read_u16 v157,v157
v_mfma_f32_16x16x32_bf16 v[132:135],v[84:87],v[32:35],v[132:135]
s_nop 0
v_lshlrev_b32_sdwa v128,1,v128 dst_sel:DWORD dst_unused:UNUSED_PAD src0_sel:DWORD src1_sel:WORD_1
v_lshlrev_b32_sdwa v130,1,v130 dst_sel:DWORD dst_unused:UNUSED_PAD src0_sel:DWORD src1_sel:WORD_1
v_mfma_f32_16x16x32_bf16 v[208:211],v[88:91],v[144:147],v[208:211]
s_barrier
ds_read_u16_d16_hi v159,v159
v_lshlrev_b32_sdwa v129,1,v129 dst_sel:DWORD dst_unused:UNUSED_PAD src0_sel:DWORD src1_sel:WORD_1
v_mfma_f32_16x16x32_bf16 v[216:219],v[92:95],v[144:147],v[216:219]
ds_read_u16 v128,v128
v_lshlrev_b32_sdwa v131,1,v131 dst_sel:DWORD dst_unused:UNUSED_PAD src0_sel:DWORD src1_sel:WORD_1
v_mfma_f32_16x16x32_bf16 v[224:227],v[96:99],v[144:147],v[224:227]
v_lshlrev_b32_sdwa v132,1,v132 dst_sel:DWORD dst_unused:UNUSED_PAD src0_sel:DWORD src1_sel:WORD_1
v_mfma_f32_16x16x32_bf16 v[232:235],v[100:103],v[144:147],v[232:235]
s_waitcnt lgkmcnt(5)
ds_read_u16_d16_hi v130,v130
v_lshl_add_u64 v[152:153],v[152:153],0,v[154:155]
v_lshlrev_b32_sdwa v134,1,v134 dst_sel:DWORD dst_unused:UNUSED_PAD src0_sel:DWORD src1_sel:WORD_1
v_mfma_f32_16x16x32_bf16 v[240:243],v[104:107],v[144:147],v[240:243]
s_waitcnt lgkmcnt(2)
ds_read_u16 v129,v129
v_lshl_add_u64 v[154:155],v[156:157],0,v[158:159]
v_mfma_f32_16x16x32_bf16 v[248:251],v[108:111],v[144:147],v[248:251]
v_lshlrev_b32_sdwa v133,1,v133 dst_sel:DWORD dst_unused:UNUSED_PAD src0_sel:DWORD src1_sel:WORD_1
v_mfma_f32_16x16x32_bf16 v[212:215],v[88:91],v[152:155],v[212:215]
ds_read_u16_d16_hi v131,v131
v_lshlrev_b32_sdwa v135,1,v135 dst_sel:DWORD dst_unused:UNUSED_PAD src0_sel:DWORD src1_sel:WORD_1
v_mfma_f32_16x16x32_bf16 v[220:223],v[92:95],v[152:155],v[220:223]
ds_read_u16 v132,v132
v_lshlrev_b32_sdwa v136,1,v136 dst_sel:DWORD dst_unused:UNUSED_PAD src0_sel:DWORD src1_sel:WORD_1
v_mfma_f32_16x16x32_bf16 v[228:231],v[96:99],v[152:155],v[228:231]
ds_read_b64_tr_b16 v[88:89],v3 offset:12320
ds_read_b64_tr_b16 v[90:91],v3 offset:13856
v_lshlrev_b32_sdwa v138,1,v138 dst_sel:DWORD dst_unused:UNUSED_PAD src0_sel:DWORD src1_sel:WORD_1
v_mfma_f32_16x16x32_bf16 v[236:239],v[100:103],v[152:155],v[236:239]
ds_read_u16_d16_hi v134,v134
v_lshlrev_b32_sdwa v137,1,v137 dst_sel:DWORD dst_unused:UNUSED_PAD src0_sel:DWORD src1_sel:WORD_1
v_mfma_f32_16x16x32_bf16 v[244:247],v[104:107],v[152:155],v[244:247]
ds_read_u16 v133,v133
v_lshlrev_b32_sdwa v139,1,v139 dst_sel:DWORD dst_unused:UNUSED_PAD src0_sel:DWORD src1_sel:WORD_1
v_mfma_f32_16x16x32_bf16 v[252:255],v[108:111],v[152:155],v[252:255]
ds_read_b64_tr_b16 v[92:93],v3 offset:12328
ds_read_b64_tr_b16 v[94:95],v3 offset:13864
v_lshlrev_b32_sdwa v140,1,v140 dst_sel:DWORD dst_unused:UNUSED_PAD src0_sel:DWORD src1_sel:WORD_1
v_mfma_f32_16x16x32_bf16 v[144:147],v[64:67],v[40:43],v[112:115]
ds_read_u16_d16_hi v135,v135
v_lshlrev_b32_sdwa v142,1,v142 dst_sel:DWORD dst_unused:UNUSED_PAD src0_sel:DWORD src1_sel:WORD_1
v_mfma_f32_16x16x32_bf16 v[152:155],v[64:67],v[44:47],v[112:115]
ds_read_u16 v136,v136
v_lshlrev_b32_sdwa v141,1,v141 dst_sel:DWORD dst_unused:UNUSED_PAD src0_sel:DWORD src1_sel:WORD_1
v_mfma_f32_16x16x32_bf16 v[156:159],v[68:71],v[44:47],v[116:119]
ds_read_b64_tr_b16 v[96:97],v3 offset:12384
ds_read_b64_tr_b16 v[98:99],v3 offset:13920
v_lshlrev_b32_sdwa v143,1,v143 dst_sel:DWORD dst_unused:UNUSED_PAD src0_sel:DWORD src1_sel:WORD_1
v_mfma_f32_16x16x32_bf16 v[148:151],v[68:71],v[40:43],v[116:119]
ds_read_u16_d16_hi v138,v138
v_mfma_f32_16x16x32_bf16 v[144:147],v[72:75],v[48:51],v[144:147]
ds_read_u16 v137,v137
v_mfma_f32_16x16x32_bf16 v[152:155],v[72:75],v[52:55],v[152:155]
ds_read_b64_tr_b16 v[100:101],v3 offset:12392
ds_read_b64_tr_b16 v[102:103],v3 offset:13928
v_mfma_f32_16x16x32_bf16 v[156:159],v[76:79],v[52:55],v[156:159]
ds_read_u16_d16_hi v139,v139
v_mfma_f32_16x16x32_bf16 v[148:151],v[76:79],v[48:51],v[148:151]
s_waitcnt lgkmcnt(15)
ds_read_u16 v140,v140
v_lshl_add_u64 v[128:129],v[128:129],0,v[130:131]
v_mfma_f32_16x16x32_bf16 v[144:147],v[80:83],v[56:59],v[144:147]
s_waitcnt lgkmcnt(9)
ds_read_b64_tr_b16 v[104:105],v3 offset:12448
ds_read_b64_tr_b16 v[106:107],v3 offset:13984
v_lshl_add_u64 v[130:131],v[132:133],0,v[134:135]
v_mfma_f32_16x16x32_bf16 v[152:155],v[80:83],v[60:63],v[152:155]
ds_read_u16_d16_hi v142,v142
v_mfma_f32_16x16x32_bf16 v[156:159],v[84:87],v[60:63],v[156:159]
ds_read_u16 v141,v141
v_mfma_f32_16x16x32_bf16 v[148:151],v[84:87],v[56:59],v[148:151]
ds_read_b64_tr_b16 v[108:109],v3 offset:12456
ds_read_b64_tr_b16 v[110:111],v3 offset:13992
v_lshlrev_b32_sdwa v144,1,v144 dst_sel:DWORD dst_unused:UNUSED_PAD src0_sel:DWORD src1_sel:WORD_1
v_lshlrev_b32_sdwa v146,1,v146 dst_sel:DWORD dst_unused:UNUSED_PAD src0_sel:DWORD src1_sel:WORD_1
v_mfma_f32_16x16x32_bf16 v[160:163],v[88:91],v[128:131],v[160:163]
ds_read_u16_d16_hi v143,v143
v_lshlrev_b32_sdwa v145,1,v145 dst_sel:DWORD dst_unused:UNUSED_PAD src0_sel:DWORD src1_sel:WORD_1
v_mfma_f32_16x16x32_bf16 v[168:171],v[92:95],v[128:131],v[168:171]
s_waitcnt lgkmcnt(13)
ds_read_u16 v144,v144
v_lshlrev_b32_sdwa v147,1,v147 dst_sel:DWORD dst_unused:UNUSED_PAD src0_sel:DWORD src1_sel:WORD_1
v_mfma_f32_16x16x32_bf16 v[176:179],v[96:99],v[128:131],v[176:179]
s_waitcnt lgkmcnt(10)
ds_read_b64_tr_b16 v[64:65],v1 offset:12320
ds_read_b64_tr_b16 v[66:67],v1 offset:14368
v_lshlrev_b32_sdwa v148,1,v148 dst_sel:DWORD dst_unused:UNUSED_PAD src0_sel:DWORD src1_sel:WORD_1
v_mfma_f32_16x16x32_bf16 v[184:187],v[100:103],v[128:131],v[184:187]
s_waitcnt lgkmcnt(8)
ds_read_u16_d16_hi v146,v146
v_lshl_add_u64 v[136:137],v[136:137],0,v[138:139]
v_lshlrev_b32_sdwa v150,1,v150 dst_sel:DWORD dst_unused:UNUSED_PAD src0_sel:DWORD src1_sel:WORD_1
v_mfma_f32_16x16x32_bf16 v[192:195],v[104:107],v[128:131],v[192:195]
s_waitcnt lgkmcnt(4)
ds_read_u16 v145,v145
v_lshl_add_u64 v[138:139],v[140:141],0,v[142:143]
v_mfma_f32_16x16x32_bf16 v[200:203],v[108:111],v[128:131],v[200:203]
ds_read_b128 v[112:115],v4 offset:512
ds_read_b64_tr_b16 v[68:69],v2 offset:12320
v_lshlrev_b32_sdwa v149,1,v149 dst_sel:DWORD dst_unused:UNUSED_PAD src0_sel:DWORD src1_sel:WORD_1
v_mfma_f32_16x16x32_bf16 v[164:167],v[88:91],v[136:139],v[164:167]
ds_read_u16_d16_hi v147,v147
v_lshlrev_b32_sdwa v151,1,v151 dst_sel:DWORD dst_unused:UNUSED_PAD src0_sel:DWORD src1_sel:WORD_1
v_mfma_f32_16x16x32_bf16 v[172:175],v[92:95],v[136:139],v[172:175]
ds_read_u16 v148,v148
ds_read_b64_tr_b16 v[70:71],v2 offset:14368
v_lshlrev_b32_sdwa v152,1,v152 dst_sel:DWORD dst_unused:UNUSED_PAD src0_sel:DWORD src1_sel:WORD_1
v_mfma_f32_16x16x32_bf16 v[180:183],v[96:99],v[136:139],v[180:183]
ds_read_b128 v[116:119],v4 offset:544
ds_read_b64_tr_b16 v[72:73],v1 offset:16416
v_lshlrev_b32_sdwa v154,1,v154 dst_sel:DWORD dst_unused:UNUSED_PAD src0_sel:DWORD src1_sel:WORD_1
v_mfma_f32_16x16x32_bf16 v[188:191],v[100:103],v[136:139],v[188:191]
ds_read_u16_d16_hi v150,v150
v_lshlrev_b32_sdwa v153,1,v153 dst_sel:DWORD dst_unused:UNUSED_PAD src0_sel:DWORD src1_sel:WORD_1
v_mfma_f32_16x16x32_bf16 v[196:199],v[104:107],v[136:139],v[196:199]
ds_read_u16 v149,v149
v_lshlrev_b32_sdwa v155,1,v155 dst_sel:DWORD dst_unused:UNUSED_PAD src0_sel:DWORD src1_sel:WORD_1
v_mfma_f32_16x16x32_bf16 v[204:207],v[108:111],v[136:139],v[204:207]
s_waitcnt lgkmcnt(8)
ds_read_b64_tr_b16 v[74:75],v1 offset:18464
ds_read_b64_tr_b16 v[76:77],v2 offset:16416
v_lshlrev_b32_sdwa v156,1,v156 dst_sel:DWORD dst_unused:UNUSED_PAD src0_sel:DWORD src1_sel:WORD_1
v_mfma_f32_16x16x32_bf16 v[128:131],v[64:67],v[16:19],v[112:115]
ds_read_u16_d16_hi v151,v151
v_lshlrev_b32_sdwa v158,1,v158 dst_sel:DWORD dst_unused:UNUSED_PAD src0_sel:DWORD src1_sel:WORD_1
v_mfma_f32_16x16x32_bf16 v[136:139],v[64:67],v[20:23],v[112:115]
s_waitcnt lgkmcnt(6)
ds_read_u16 v152,v152
ds_read_b64_tr_b16 v[78:79],v2 offset:18464
v_lshlrev_b32_sdwa v157,1,v157 dst_sel:DWORD dst_unused:UNUSED_PAD src0_sel:DWORD src1_sel:WORD_1
v_mfma_f32_16x16x32_bf16 v[140:143],v[68:71],v[20:23],v[116:119]
ds_read_b64_tr_b16 v[80:81],v1 offset:20512
ds_read_b64_tr_b16 v[82:83],v1 offset:22560
v_lshlrev_b32_sdwa v159,1,v159 dst_sel:DWORD dst_unused:UNUSED_PAD src0_sel:DWORD src1_sel:WORD_1
v_mfma_f32_16x16x32_bf16 v[132:135],v[68:71],v[16:19],v[116:119]
s_waitcnt lgkmcnt(6)
ds_read_u16_d16_hi v154,v154
v_mfma_f32_16x16x32_bf16 v[128:131],v[72:75],v[24:27],v[128:131]
ds_read_u16 v153,v153
v_mfma_f32_16x16x32_bf16 v[136:139],v[72:75],v[28:31],v[136:139]
s_waitcnt lgkmcnt(4)
ds_read_b64_tr_b16 v[84:85],v2 offset:20512
ds_read_b64_tr_b16 v[86:87],v2 offset:22560
v_mfma_f32_16x16x32_bf16 v[140:143],v[76:79],v[28:31],v[140:143]
ds_read_u16_d16_hi v155,v155
v_mfma_f32_16x16x32_bf16 v[132:135],v[76:79],v[24:27],v[132:135]
s_waitcnt lgkmcnt(5)
ds_read_u16 v156,v156
v_lshl_add_u64 v[144:145],v[144:145],0,v[146:147]
v_mfma_f32_16x16x32_bf16 v[128:131],v[80:83],v[32:35],v[128:131]
v_lshl_add_u64 v[146:147],v[148:149],0,v[150:151]
v_mfma_f32_16x16x32_bf16 v[136:139],v[80:83],v[36:39],v[136:139]
s_waitcnt lgkmcnt(2)
ds_read_u16_d16_hi v158,v158
v_mfma_f32_16x16x32_bf16 v[140:143],v[84:87],v[36:39],v[140:143]
ds_read_u16 v157,v157
v_mfma_f32_16x16x32_bf16 v[132:135],v[84:87],v[32:35],v[132:135]
s_nop 0
v_lshlrev_b32_sdwa v128,1,v128 dst_sel:DWORD dst_unused:UNUSED_PAD src0_sel:DWORD src1_sel:WORD_1
v_lshlrev_b32_sdwa v130,1,v130 dst_sel:DWORD dst_unused:UNUSED_PAD src0_sel:DWORD src1_sel:WORD_1
v_mfma_f32_16x16x32_bf16 v[208:211],v[88:91],v[144:147],v[208:211]
ds_read_u16_d16_hi v159,v159
v_lshlrev_b32_sdwa v129,1,v129 dst_sel:DWORD dst_unused:UNUSED_PAD src0_sel:DWORD src1_sel:WORD_1
v_mfma_f32_16x16x32_bf16 v[216:219],v[92:95],v[144:147],v[216:219]
ds_read_u16 v128,v128
v_lshlrev_b32_sdwa v131,1,v131 dst_sel:DWORD dst_unused:UNUSED_PAD src0_sel:DWORD src1_sel:WORD_1
v_mfma_f32_16x16x32_bf16 v[224:227],v[96:99],v[144:147],v[224:227]
v_lshlrev_b32_sdwa v132,1,v132 dst_sel:DWORD dst_unused:UNUSED_PAD src0_sel:DWORD src1_sel:WORD_1
v_mfma_f32_16x16x32_bf16 v[232:235],v[100:103],v[144:147],v[232:235]
s_waitcnt lgkmcnt(5)
ds_read_u16_d16_hi v130,v130
v_lshl_add_u64 v[152:153],v[152:153],0,v[154:155]
v_lshlrev_b32_sdwa v134,1,v134 dst_sel:DWORD dst_unused:UNUSED_PAD src0_sel:DWORD src1_sel:WORD_1
v_mfma_f32_16x16x32_bf16 v[240:243],v[104:107],v[144:147],v[240:243]
s_waitcnt lgkmcnt(2)
ds_read_u16 v129,v129
v_lshl_add_u64 v[154:155],v[156:157],0,v[158:159]
v_mfma_f32_16x16x32_bf16 v[248:251],v[108:111],v[144:147],v[248:251]
v_lshlrev_b32_sdwa v133,1,v133 dst_sel:DWORD dst_unused:UNUSED_PAD src0_sel:DWORD src1_sel:WORD_1
v_mfma_f32_16x16x32_bf16 v[212:215],v[88:91],v[152:155],v[212:215]
ds_read_u16_d16_hi v131,v131
v_lshlrev_b32_sdwa v135,1,v135 dst_sel:DWORD dst_unused:UNUSED_PAD src0_sel:DWORD src1_sel:WORD_1
v_mfma_f32_16x16x32_bf16 v[220:223],v[92:95],v[152:155],v[220:223]
ds_read_u16 v132,v132
v_lshlrev_b32_sdwa v136,1,v136 dst_sel:DWORD dst_unused:UNUSED_PAD src0_sel:DWORD src1_sel:WORD_1
v_mfma_f32_16x16x32_bf16 v[228:231],v[96:99],v[152:155],v[228:231]
ds_read_b64_tr_b16 v[88:89],v3 offset:18480
ds_read_b64_tr_b16 v[90:91],v3 offset:20016
v_lshlrev_b32_sdwa v138,1,v138 dst_sel:DWORD dst_unused:UNUSED_PAD src0_sel:DWORD src1_sel:WORD_1
v_mfma_f32_16x16x32_bf16 v[236:239],v[100:103],v[152:155],v[236:239]
ds_read_u16_d16_hi v134,v134
v_lshlrev_b32_sdwa v137,1,v137 dst_sel:DWORD dst_unused:UNUSED_PAD src0_sel:DWORD src1_sel:WORD_1
v_mfma_f32_16x16x32_bf16 v[244:247],v[104:107],v[152:155],v[244:247]
ds_read_u16 v133,v133
v_lshlrev_b32_sdwa v139,1,v139 dst_sel:DWORD dst_unused:UNUSED_PAD src0_sel:DWORD src1_sel:WORD_1
v_mfma_f32_16x16x32_bf16 v[252:255],v[108:111],v[152:155],v[252:255]
ds_read_b64_tr_b16 v[92:93],v3 offset:18488
ds_read_b64_tr_b16 v[94:95],v3 offset:20024
v_lshlrev_b32_sdwa v140,1,v140 dst_sel:DWORD dst_unused:UNUSED_PAD src0_sel:DWORD src1_sel:WORD_1
v_mfma_f32_16x16x32_bf16 v[144:147],v[64:67],v[40:43],v[112:115]
ds_read_u16_d16_hi v135,v135
v_lshlrev_b32_sdwa v142,1,v142 dst_sel:DWORD dst_unused:UNUSED_PAD src0_sel:DWORD src1_sel:WORD_1
v_mfma_f32_16x16x32_bf16 v[152:155],v[64:67],v[44:47],v[112:115]
ds_read_u16 v136,v136
v_lshlrev_b32_sdwa v141,1,v141 dst_sel:DWORD dst_unused:UNUSED_PAD src0_sel:DWORD src1_sel:WORD_1
v_mfma_f32_16x16x32_bf16 v[156:159],v[68:71],v[44:47],v[116:119]
ds_read_b64_tr_b16 v[96:97],v3 offset:18544
ds_read_b64_tr_b16 v[98:99],v3 offset:20080
v_lshlrev_b32_sdwa v143,1,v143 dst_sel:DWORD dst_unused:UNUSED_PAD src0_sel:DWORD src1_sel:WORD_1
v_mfma_f32_16x16x32_bf16 v[148:151],v[68:71],v[40:43],v[116:119]
ds_read_u16_d16_hi v138,v138
v_mfma_f32_16x16x32_bf16 v[144:147],v[72:75],v[48:51],v[144:147]
ds_read_u16 v137,v137
v_mfma_f32_16x16x32_bf16 v[152:155],v[72:75],v[52:55],v[152:155]
ds_read_b64_tr_b16 v[100:101],v3 offset:18552
ds_read_b64_tr_b16 v[102:103],v3 offset:20088
v_mfma_f32_16x16x32_bf16 v[156:159],v[76:79],v[52:55],v[156:159]
ds_read_u16_d16_hi v139,v139
v_mfma_f32_16x16x32_bf16 v[148:151],v[76:79],v[48:51],v[148:151]
s_waitcnt lgkmcnt(15)
ds_read_u16 v140,v140
v_lshl_add_u64 v[128:129],v[128:129],0,v[130:131]
v_mfma_f32_16x16x32_bf16 v[144:147],v[80:83],v[56:59],v[144:147]
s_waitcnt lgkmcnt(9)
ds_read_b64_tr_b16 v[104:105],v3 offset:18608
ds_read_b64_tr_b16 v[106:107],v3 offset:20144
v_lshl_add_u64 v[130:131],v[132:133],0,v[134:135]
v_mfma_f32_16x16x32_bf16 v[152:155],v[80:83],v[60:63],v[152:155]
ds_read_u16_d16_hi v142,v142
v_mfma_f32_16x16x32_bf16 v[156:159],v[84:87],v[60:63],v[156:159]
ds_read_u16 v141,v141
v_mfma_f32_16x16x32_bf16 v[148:151],v[84:87],v[56:59],v[148:151]
ds_read_b64_tr_b16 v[108:109],v3 offset:18616
ds_read_b64_tr_b16 v[110:111],v3 offset:20152
v_lshlrev_b32_sdwa v144,1,v144 dst_sel:DWORD dst_unused:UNUSED_PAD src0_sel:DWORD src1_sel:WORD_1
v_lshlrev_b32_sdwa v146,1,v146 dst_sel:DWORD dst_unused:UNUSED_PAD src0_sel:DWORD src1_sel:WORD_1
v_mfma_f32_16x16x32_bf16 v[160:163],v[88:91],v[128:131],v[160:163]
ds_read_u16_d16_hi v143,v143
v_lshlrev_b32_sdwa v145,1,v145 dst_sel:DWORD dst_unused:UNUSED_PAD src0_sel:DWORD src1_sel:WORD_1
v_mfma_f32_16x16x32_bf16 v[168:171],v[92:95],v[128:131],v[168:171]
s_waitcnt lgkmcnt(13)
ds_read_u16 v144,v144
v_lshlrev_b32_sdwa v147,1,v147 dst_sel:DWORD dst_unused:UNUSED_PAD src0_sel:DWORD src1_sel:WORD_1
v_mfma_f32_16x16x32_bf16 v[176:179],v[96:99],v[128:131],v[176:179]
s_waitcnt lgkmcnt(10)
ds_read_b64_tr_b16 v[64:65],v1 offset:12832
ds_read_b64_tr_b16 v[66:67],v1 offset:14880
v_lshlrev_b32_sdwa v148,1,v148 dst_sel:DWORD dst_unused:UNUSED_PAD src0_sel:DWORD src1_sel:WORD_1
v_mfma_f32_16x16x32_bf16 v[184:187],v[100:103],v[128:131],v[184:187]
s_waitcnt lgkmcnt(8)
ds_read_u16_d16_hi v146,v146
v_lshl_add_u64 v[136:137],v[136:137],0,v[138:139]
v_lshlrev_b32_sdwa v150,1,v150 dst_sel:DWORD dst_unused:UNUSED_PAD src0_sel:DWORD src1_sel:WORD_1
v_mfma_f32_16x16x32_bf16 v[192:195],v[104:107],v[128:131],v[192:195]
s_waitcnt lgkmcnt(4)
ds_read_u16 v145,v145
v_lshl_add_u64 v[138:139],v[140:141],0,v[142:143]
v_mfma_f32_16x16x32_bf16 v[200:203],v[108:111],v[128:131],v[200:203]
ds_read_b128 v[112:115],v4 offset:640
ds_read_b64_tr_b16 v[68:69],v2 offset:12832
v_lshlrev_b32_sdwa v149,1,v149 dst_sel:DWORD dst_unused:UNUSED_PAD src0_sel:DWORD src1_sel:WORD_1
v_mfma_f32_16x16x32_bf16 v[164:167],v[88:91],v[136:139],v[164:167]
ds_read_u16_d16_hi v147,v147
v_lshlrev_b32_sdwa v151,1,v151 dst_sel:DWORD dst_unused:UNUSED_PAD src0_sel:DWORD src1_sel:WORD_1
v_mfma_f32_16x16x32_bf16 v[172:175],v[92:95],v[136:139],v[172:175]
ds_read_u16 v148,v148
ds_read_b64_tr_b16 v[70:71],v2 offset:14880
v_lshlrev_b32_sdwa v152,1,v152 dst_sel:DWORD dst_unused:UNUSED_PAD src0_sel:DWORD src1_sel:WORD_1
v_mfma_f32_16x16x32_bf16 v[180:183],v[96:99],v[136:139],v[180:183]
ds_read_b128 v[116:119],v4 offset:672
ds_read_b64_tr_b16 v[72:73],v1 offset:16928
v_lshlrev_b32_sdwa v154,1,v154 dst_sel:DWORD dst_unused:UNUSED_PAD src0_sel:DWORD src1_sel:WORD_1
v_mfma_f32_16x16x32_bf16 v[188:191],v[100:103],v[136:139],v[188:191]
ds_read_u16_d16_hi v150,v150
v_lshlrev_b32_sdwa v153,1,v153 dst_sel:DWORD dst_unused:UNUSED_PAD src0_sel:DWORD src1_sel:WORD_1
v_mfma_f32_16x16x32_bf16 v[196:199],v[104:107],v[136:139],v[196:199]
ds_read_u16 v149,v149
v_lshlrev_b32_sdwa v155,1,v155 dst_sel:DWORD dst_unused:UNUSED_PAD src0_sel:DWORD src1_sel:WORD_1
v_mfma_f32_16x16x32_bf16 v[204:207],v[108:111],v[136:139],v[204:207]
s_waitcnt lgkmcnt(8)
ds_read_b64_tr_b16 v[74:75],v1 offset:18976
ds_read_b64_tr_b16 v[76:77],v2 offset:16928
v_lshlrev_b32_sdwa v156,1,v156 dst_sel:DWORD dst_unused:UNUSED_PAD src0_sel:DWORD src1_sel:WORD_1
v_mfma_f32_16x16x32_bf16 v[128:131],v[64:67],v[16:19],v[112:115]
ds_read_u16_d16_hi v151,v151
v_lshlrev_b32_sdwa v158,1,v158 dst_sel:DWORD dst_unused:UNUSED_PAD src0_sel:DWORD src1_sel:WORD_1
v_mfma_f32_16x16x32_bf16 v[136:139],v[64:67],v[20:23],v[112:115]
s_waitcnt lgkmcnt(6)
ds_read_u16 v152,v152
ds_read_b64_tr_b16 v[78:79],v2 offset:18976
v_lshlrev_b32_sdwa v157,1,v157 dst_sel:DWORD dst_unused:UNUSED_PAD src0_sel:DWORD src1_sel:WORD_1
v_mfma_f32_16x16x32_bf16 v[140:143],v[68:71],v[20:23],v[116:119]
ds_read_b64_tr_b16 v[80:81],v1 offset:21024
ds_read_b64_tr_b16 v[82:83],v1 offset:23072
v_lshlrev_b32_sdwa v159,1,v159 dst_sel:DWORD dst_unused:UNUSED_PAD src0_sel:DWORD src1_sel:WORD_1
v_mfma_f32_16x16x32_bf16 v[132:135],v[68:71],v[16:19],v[116:119]
s_waitcnt lgkmcnt(6)
ds_read_u16_d16_hi v154,v154
v_mfma_f32_16x16x32_bf16 v[128:131],v[72:75],v[24:27],v[128:131]
ds_read_u16 v153,v153
v_mfma_f32_16x16x32_bf16 v[136:139],v[72:75],v[28:31],v[136:139]
s_waitcnt lgkmcnt(4)
ds_read_b64_tr_b16 v[84:85],v2 offset:21024
ds_read_b64_tr_b16 v[86:87],v2 offset:23072
v_mfma_f32_16x16x32_bf16 v[140:143],v[76:79],v[28:31],v[140:143]
ds_read_u16_d16_hi v155,v155
v_mfma_f32_16x16x32_bf16 v[132:135],v[76:79],v[24:27],v[132:135]
s_waitcnt lgkmcnt(5)
ds_read_u16 v156,v156
v_lshl_add_u64 v[144:145],v[144:145],0,v[146:147]
v_mfma_f32_16x16x32_bf16 v[128:131],v[80:83],v[32:35],v[128:131]
v_lshl_add_u64 v[146:147],v[148:149],0,v[150:151]
v_mfma_f32_16x16x32_bf16 v[136:139],v[80:83],v[36:39],v[136:139]
s_waitcnt lgkmcnt(2)
ds_read_u16_d16_hi v158,v158
v_mfma_f32_16x16x32_bf16 v[140:143],v[84:87],v[36:39],v[140:143]
ds_read_u16 v157,v157
v_mfma_f32_16x16x32_bf16 v[132:135],v[84:87],v[32:35],v[132:135]
s_nop 0
v_lshlrev_b32_sdwa v128,1,v128 dst_sel:DWORD dst_unused:UNUSED_PAD src0_sel:DWORD src1_sel:WORD_1
v_lshlrev_b32_sdwa v130,1,v130 dst_sel:DWORD dst_unused:UNUSED_PAD src0_sel:DWORD src1_sel:WORD_1
v_mfma_f32_16x16x32_bf16 v[208:211],v[88:91],v[144:147],v[208:211]
s_barrier
s_setprio 3
ds_read_u16_d16_hi v159,v159
v_lshlrev_b32_sdwa v129,1,v129 dst_sel:DWORD dst_unused:UNUSED_PAD src0_sel:DWORD src1_sel:WORD_1
v_lshlrev_b32_sdwa v131,1,v131 dst_sel:DWORD dst_unused:UNUSED_PAD src0_sel:DWORD src1_sel:WORD_1
v_mfma_f32_16x16x32_bf16 v[216:219],v[92:95],v[144:147],v[216:219]
v_fma_mixlo_f16 v120,v140,s99,0
v_fma_mixlo_f16 v122,v141,s99,0
v_mfma_f32_16x16x32_bf16 v[224:227],v[96:99],v[144:147],v[224:227]
ds_read_u16 v128,v128
v_lshlrev_b32_sdwa v132,1,v132 dst_sel:DWORD dst_unused:UNUSED_PAD src0_sel:DWORD src1_sel:WORD_1
v_lshlrev_b32_sdwa v134,1,v134 dst_sel:DWORD dst_unused:UNUSED_PAD src0_sel:DWORD src1_sel:WORD_1
v_mfma_f32_16x16x32_bf16 v[232:235],v[100:103],v[144:147],v[232:235]
s_waitcnt lgkmcnt(5)
v_lshl_add_u64 v[152:153],v[152:153],0,v[154:155]
v_mfma_f32_16x16x32_bf16 v[240:243],v[104:107],v[144:147],v[240:243]
s_waitcnt lgkmcnt(1)
ds_read_u16_d16_hi v130,v130
v_lshl_add_u64 v[154:155],v[156:157],0,v[158:159]
v_mfma_f32_16x16x32_bf16 v[248:251],v[108:111],v[144:147],v[248:251]
v_fma_mixhi_f16 v120,v142,s99,0
v_fma_mixhi_f16 v122,v143,s99,0
v_mfma_f32_16x16x32_bf16 v[212:215],v[88:91],v[152:155],v[212:215]
ds_read_u16 v129,v129
v_lshlrev_b32_sdwa v133,1,v133 dst_sel:DWORD dst_unused:UNUSED_PAD src0_sel:DWORD src1_sel:WORD_1
v_lshlrev_b32_sdwa v135,1,v135 dst_sel:DWORD dst_unused:UNUSED_PAD src0_sel:DWORD src1_sel:WORD_1
v_mfma_f32_16x16x32_bf16 v[220:223],v[92:95],v[152:155],v[220:223]
ds_read_b64_tr_b16 v[88:89],v3 offset:0
v_pk_fma_f16 v121,v120,v120,s54
v_pk_fma_f16 v123,v122,v122,s54
v_mfma_f32_16x16x32_bf16 v[228:231],v[96:99],v[152:155],v[228:231]
ds_read_u16_d16_hi v131,v131
v_pk_mul_f16 v121,v120,v121
v_pk_mul_f16 v123,v122,v123
v_mfma_f32_16x16x32_bf16 v[236:239],v[100:103],v[152:155],v[236:239]
ds_read_b64_tr_b16 v[90:91],v3 offset:1536
v_lshlrev_b32_sdwa v136,1,v136 dst_sel:DWORD dst_unused:UNUSED_PAD src0_sel:DWORD src1_sel:WORD_1
v_lshlrev_b32_sdwa v138,1,v138 dst_sel:DWORD dst_unused:UNUSED_PAD src0_sel:DWORD src1_sel:WORD_1
v_mfma_f32_16x16x32_bf16 v[244:247],v[104:107],v[152:155],v[244:247]
ds_read_u16 v132,v132
v_exp_f16 v120,v121
v_mfma_f32_16x16x32_bf16 v[252:255],v[108:111],v[152:155],v[252:255]
ds_read_b64_tr_b16 v[92:93],v3 offset:8
v_exp_f16 v122,v123
v_mfma_f32_16x16x32_bf16 v[144:147],v[64:67],v[40:43],v[112:115]
ds_read_u16_d16_hi v134,v134
v_lshlrev_b32_sdwa v137,1,v137 dst_sel:DWORD dst_unused:UNUSED_PAD src0_sel:DWORD src1_sel:WORD_1
v_lshlrev_b32_sdwa v139,1,v139 dst_sel:DWORD dst_unused:UNUSED_PAD src0_sel:DWORD src1_sel:WORD_1
v_mfma_f32_16x16x32_bf16 v[152:155],v[64:67],v[44:47],v[112:115]
ds_read_b64_tr_b16 v[94:95],v3 offset:1544
v_exp_f16_sdwa v120,v121 dst_sel:WORD_1 dst_unused:UNUSED_PRESERVE src0_sel:WORD_1
v_mfma_f32_16x16x32_bf16 v[156:159],v[68:71],v[44:47],v[116:119]
ds_read_u16 v133,v133
v_exp_f16_sdwa v122,v123 dst_sel:WORD_1 dst_unused:UNUSED_PRESERVE src0_sel:WORD_1
v_mfma_f32_16x16x32_bf16 v[148:151],v[68:71],v[40:43],v[116:119]
ds_read_b64_tr_b16 v[96:97],v3 offset:64
v_pk_fma_f16 v120,v120,s55,s55
v_pk_fma_f16 v122,v122,s55,s55
v_mfma_f32_16x16x32_bf16 v[144:147],v[72:75],v[48:51],v[144:147]
ds_read_u16_d16_hi v135,v135
v_rcp_f16 v121,v120
v_mfma_f32_16x16x32_bf16 v[152:155],v[72:75],v[52:55],v[152:155]
ds_read_b64_tr_b16 v[98:99],v3 offset:1600
ds_read_b64_tr_b16 v[100:101],v3 offset:72
v_rcp_f16 v123,v122
v_mfma_f32_16x16x32_bf16 v[156:159],v[76:79],v[52:55],v[156:159]
ds_read_u16 v136,v136
v_rcp_f16_sdwa v121,v120 dst_sel:WORD_1 dst_unused:UNUSED_PRESERVE src0_sel:WORD_1
v_mfma_f32_16x16x32_bf16 v[148:151],v[76:79],v[48:51],v[148:151]
ds_read_b64_tr_b16 v[102:103],v3 offset:1608
ds_read_b64_tr_b16 v[104:105],v3 offset:128
v_rcp_f16_sdwa v123,v122 dst_sel:WORD_1 dst_unused:UNUSED_PRESERVE src0_sel:WORD_1
v_mfma_f32_16x16x32_bf16 v[144:147],v[80:83],v[56:59],v[144:147]
ds_read_u16_d16_hi v138,v138
v_fma_mix_f32 v140,v140,v121,0 op_sel_hi:[0,1,0]
v_fma_mix_f32 v142,v142,v121,0 op_sel:[0,1,0] op_sel_hi:[0,1,0]
v_mfma_f32_16x16x32_bf16 v[152:155],v[80:83],v[60:63],v[152:155]
s_waitcnt lgkmcnt(6)
ds_read_b64_tr_b16 v[106:107],v3 offset:1664
ds_read_b64_tr_b16 v[108:109],v3 offset:136
v_lshl_add_u64 v[128:129],v[128:129],0,v[130:131]
v_lshl_add_u64 v[130:131],v[132:133],0,v[134:135]
v_mfma_f32_16x16x32_bf16 v[156:159],v[84:87],v[60:63],v[156:159]
ds_read_u16 v137,v137
v_fma_mix_f32 v141,v141,v123,0 op_sel_hi:[0,1,0]
v_fma_mix_f32 v143,v143,v123,0 op_sel:[0,1,0] op_sel_hi:[0,1,0]
v_mfma_f32_16x16x32_bf16 v[148:151],v[84:87],v[56:59],v[148:151]
ds_read_b64_tr_b16 v[110:111],v3 offset:1672
ds_read_b64_tr_b16 v[64:65],v1 offset:0
v_lshlrev_b32_sdwa v144,1,v144 dst_sel:DWORD dst_unused:UNUSED_PAD src0_sel:DWORD src1_sel:WORD_1
v_lshlrev_b32_sdwa v146,1,v146 dst_sel:DWORD dst_unused:UNUSED_PAD src0_sel:DWORD src1_sel:WORD_1
v_mfma_f32_16x16x32_bf16 v[160:163],v[88:91],v[128:131],v[160:163]
ds_read_u16_d16_hi v139,v139
v_lshlrev_b32_sdwa v145,1,v145 dst_sel:DWORD dst_unused:UNUSED_PAD src0_sel:DWORD src1_sel:WORD_1
v_lshlrev_b32_sdwa v147,1,v147 dst_sel:DWORD dst_unused:UNUSED_PAD src0_sel:DWORD src1_sel:WORD_1
v_mfma_f32_16x16x32_bf16 v[168:171],v[92:95],v[128:131],v[168:171]
s_waitcnt lgkmcnt(11)
ds_read_b64_tr_b16 v[66:67],v1 offset:2048
ds_read_b128 v[112:115],v4 offset:768
v_fma_mixlo_f16 v120,v156,s99,0
v_fma_mixlo_f16 v122,v157,s99,0
v_mfma_f32_16x16x32_bf16 v[176:179],v[96:99],v[128:131],v[176:179]
s_waitcnt lgkmcnt(10)
ds_read_u16 v144,v144
v_lshlrev_b32_sdwa v148,1,v148 dst_sel:DWORD dst_unused:UNUSED_PAD src0_sel:DWORD src1_sel:WORD_1
v_lshlrev_b32_sdwa v150,1,v150 dst_sel:DWORD dst_unused:UNUSED_PAD src0_sel:DWORD src1_sel:WORD_1
v_mfma_f32_16x16x32_bf16 v[184:187],v[100:103],v[128:131],v[184:187]
s_waitcnt lgkmcnt(3)
ds_read_b64_tr_b16 v[68:69],v2 offset:0
ds_read_b64_tr_b16 v[70:71],v2 offset:2048
v_lshl_add_u64 v[136:137],v[136:137],0,v[138:139]
v_mfma_f32_16x16x32_bf16 v[192:195],v[104:107],v[128:131],v[192:195]
ds_read_u16_d16_hi v146,v146
v_cvt_pk_bf16_f32 v138,v140,v142
v_cvt_pk_bf16_f32 v139,v141,v143
v_mfma_f32_16x16x32_bf16 v[200:203],v[108:111],v[128:131],v[200:203]
ds_read_b128 v[116:119],v4 offset:800
ds_read_b64_tr_b16 v[72:73],v1 offset:4096
v_fma_mixhi_f16 v120,v158,s99,0
v_fma_mixhi_f16 v122,v159,s99,0
v_mfma_f32_16x16x32_bf16 v[164:167],v[88:91],v[136:139],v[164:167]
ds_read_u16 v145,v145
v_lshlrev_b32_sdwa v149,1,v149 dst_sel:DWORD dst_unused:UNUSED_PAD src0_sel:DWORD src1_sel:WORD_1
v_lshlrev_b32_sdwa v151,1,v151 dst_sel:DWORD dst_unused:UNUSED_PAD src0_sel:DWORD src1_sel:WORD_1
v_mfma_f32_16x16x32_bf16 v[172:175],v[92:95],v[136:139],v[172:175]
ds_read_b64_tr_b16 v[74:75],v1 offset:6144
ds_read_b64_tr_b16 v[76:77],v2 offset:4096
v_pk_fma_f16 v121,v120,v120,s54
v_pk_fma_f16 v123,v122,v122,s54
v_mfma_f32_16x16x32_bf16 v[180:183],v[96:99],v[136:139],v[180:183]
ds_read_u16_d16_hi v147,v147
v_pk_mul_f16 v121,v120,v121
v_pk_mul_f16 v123,v122,v123
v_mfma_f32_16x16x32_bf16 v[188:191],v[100:103],v[136:139],v[188:191]
ds_read_b64_tr_b16 v[78:79],v2 offset:6144
v_lshlrev_b32_sdwa v152,1,v152 dst_sel:DWORD dst_unused:UNUSED_PAD src0_sel:DWORD src1_sel:WORD_1
v_lshlrev_b32_sdwa v154,1,v154 dst_sel:DWORD dst_unused:UNUSED_PAD src0_sel:DWORD src1_sel:WORD_1
v_mfma_f32_16x16x32_bf16 v[196:199],v[104:107],v[136:139],v[196:199]
ds_read_u16 v148,v148
v_exp_f16 v120,v121
v_mfma_f32_16x16x32_bf16 v[204:207],v[108:111],v[136:139],v[204:207]
s_waitcnt lgkmcnt(12)
ds_read_b64_tr_b16 v[80:81],v1 offset:8192
v_exp_f16 v122,v123
v_mfma_f32_16x16x32_bf16 v[128:131],v[64:67],v[16:19],v[112:115]
ds_read_u16_d16_hi v150,v150
v_lshlrev_b32_sdwa v153,1,v153 dst_sel:DWORD dst_unused:UNUSED_PAD src0_sel:DWORD src1_sel:WORD_1
v_lshlrev_b32_sdwa v155,1,v155 dst_sel:DWORD dst_unused:UNUSED_PAD src0_sel:DWORD src1_sel:WORD_1
v_mfma_f32_16x16x32_bf16 v[136:139],v[64:67],v[20:23],v[112:115]
s_waitcnt lgkmcnt(9)
ds_read_b64_tr_b16 v[82:83],v1 offset:10240
v_exp_f16_sdwa v120,v121 dst_sel:WORD_1 dst_unused:UNUSED_PRESERVE src0_sel:WORD_1
v_mfma_f32_16x16x32_bf16 v[140:143],v[68:71],v[20:23],v[116:119]
ds_read_u16 v149,v149
v_exp_f16_sdwa v122,v123 dst_sel:WORD_1 dst_unused:UNUSED_PRESERVE src0_sel:WORD_1
v_mfma_f32_16x16x32_bf16 v[132:135],v[68:71],v[16:19],v[116:119]
s_waitcnt lgkmcnt(8)
ds_read_b64_tr_b16 v[84:85],v2 offset:8192
v_pk_fma_f16 v120,v120,s55,s55
v_pk_fma_f16 v122,v122,s55,s55
v_mfma_f32_16x16x32_bf16 v[128:131],v[72:75],v[24:27],v[128:131]
ds_read_u16_d16_hi v151,v151
v_rcp_f16 v121,v120
v_mfma_f32_16x16x32_bf16 v[136:139],v[72:75],v[28:31],v[136:139]
s_waitcnt lgkmcnt(7)
ds_read_b64_tr_b16 v[86:87],v2 offset:10240
v_rcp_f16 v123,v122
v_mfma_f32_16x16x32_bf16 v[140:143],v[76:79],v[28:31],v[140:143]
ds_read_u16 v152,v152
v_rcp_f16_sdwa v121,v120 dst_sel:WORD_1 dst_unused:UNUSED_PRESERVE src0_sel:WORD_1
v_mfma_f32_16x16x32_bf16 v[132:135],v[76:79],v[24:27],v[132:135]
s_waitcnt lgkmcnt(5)
v_rcp_f16_sdwa v123,v122 dst_sel:WORD_1 dst_unused:UNUSED_PRESERVE src0_sel:WORD_1
v_mfma_f32_16x16x32_bf16 v[128:131],v[80:83],v[32:35],v[128:131]
ds_read_u16_d16_hi v154,v154
v_fma_mix_f32 v156,v156,v121,0 op_sel_hi:[0,1,0]
v_fma_mix_f32 v158,v158,v121,0 op_sel:[0,1,0] op_sel_hi:[0,1,0]
v_mfma_f32_16x16x32_bf16 v[136:139],v[80:83],v[36:39],v[136:139]
s_waitcnt lgkmcnt(2)
v_lshl_add_u64 v[144:145],v[144:145],0,v[146:147]
v_lshl_add_u64 v[146:147],v[148:149],0,v[150:151]
v_mfma_f32_16x16x32_bf16 v[140:143],v[84:87],v[36:39],v[140:143]
ds_read_u16 v153,v153
v_fma_mix_f32 v157,v157,v123,0 op_sel_hi:[0,1,0]
v_fma_mix_f32 v159,v159,v123,0 op_sel:[0,1,0] op_sel_hi:[0,1,0]
v_mfma_f32_16x16x32_bf16 v[132:135],v[84:87],v[32:35],v[132:135]
v_lshlrev_b32_sdwa v128,1,v128 dst_sel:DWORD dst_unused:UNUSED_PAD src0_sel:DWORD src1_sel:WORD_1
v_lshlrev_b32_sdwa v130,1,v130 dst_sel:DWORD dst_unused:UNUSED_PAD src0_sel:DWORD src1_sel:WORD_1
v_mfma_f32_16x16x32_bf16 v[208:211],v[88:91],v[144:147],v[208:211]
ds_read_u16_d16_hi v155,v155
v_lshlrev_b32_sdwa v129,1,v129 dst_sel:DWORD dst_unused:UNUSED_PAD src0_sel:DWORD src1_sel:WORD_1
v_lshlrev_b32_sdwa v131,1,v131 dst_sel:DWORD dst_unused:UNUSED_PAD src0_sel:DWORD src1_sel:WORD_1
v_mfma_f32_16x16x32_bf16 v[216:219],v[92:95],v[144:147],v[216:219]
v_fma_mixlo_f16 v120,v140,s99,0
v_fma_mixlo_f16 v122,v141,s99,0
v_mfma_f32_16x16x32_bf16 v[224:227],v[96:99],v[144:147],v[224:227]
ds_read_u16 v128,v128
v_lshlrev_b32_sdwa v132,1,v132 dst_sel:DWORD dst_unused:UNUSED_PAD src0_sel:DWORD src1_sel:WORD_1
v_lshlrev_b32_sdwa v134,1,v134 dst_sel:DWORD dst_unused:UNUSED_PAD src0_sel:DWORD src1_sel:WORD_1
v_mfma_f32_16x16x32_bf16 v[232:235],v[100:103],v[144:147],v[232:235]
s_waitcnt lgkmcnt(1)
v_lshl_add_u64 v[152:153],v[152:153],0,v[154:155]
v_mfma_f32_16x16x32_bf16 v[240:243],v[104:107],v[144:147],v[240:243]
ds_read_u16_d16_hi v130,v130
v_cvt_pk_bf16_f32 v154,v156,v158
v_cvt_pk_bf16_f32 v155,v157,v159
v_mfma_f32_16x16x32_bf16 v[248:251],v[108:111],v[144:147],v[248:251]
v_fma_mixhi_f16 v120,v142,s99,0
v_fma_mixhi_f16 v122,v143,s99,0
v_mfma_f32_16x16x32_bf16 v[212:215],v[88:91],v[152:155],v[212:215]
s_setprio 2
ds_read_u16 v129,v129
v_lshlrev_b32_sdwa v133,1,v133 dst_sel:DWORD dst_unused:UNUSED_PAD src0_sel:DWORD src1_sel:WORD_1
v_lshlrev_b32_sdwa v135,1,v135 dst_sel:DWORD dst_unused:UNUSED_PAD src0_sel:DWORD src1_sel:WORD_1
v_mfma_f32_16x16x32_bf16 v[220:223],v[92:95],v[152:155],v[220:223]
ds_read_b64_tr_b16 v[88:89],v3 offset:6160
v_pk_fma_f16 v121,v120,v120,s54
v_pk_fma_f16 v123,v122,v122,s54
v_mfma_f32_16x16x32_bf16 v[228:231],v[96:99],v[152:155],v[228:231]
ds_read_u16_d16_hi v131,v131
v_pk_mul_f16 v121,v120,v121
v_pk_mul_f16 v123,v122,v123
v_mfma_f32_16x16x32_bf16 v[236:239],v[100:103],v[152:155],v[236:239]
ds_read_b64_tr_b16 v[90:91],v3 offset:7696
v_lshlrev_b32_sdwa v136,1,v136 dst_sel:DWORD dst_unused:UNUSED_PAD src0_sel:DWORD src1_sel:WORD_1
v_lshlrev_b32_sdwa v138,1,v138 dst_sel:DWORD dst_unused:UNUSED_PAD src0_sel:DWORD src1_sel:WORD_1
v_mfma_f32_16x16x32_bf16 v[244:247],v[104:107],v[152:155],v[244:247]
ds_read_u16 v132,v132
v_exp_f16 v120,v121
v_mfma_f32_16x16x32_bf16 v[252:255],v[108:111],v[152:155],v[252:255]
ds_read_b64_tr_b16 v[92:93],v3 offset:6168
v_exp_f16 v122,v123
v_mfma_f32_16x16x32_bf16 v[144:147],v[64:67],v[40:43],v[112:115]
ds_read_u16_d16_hi v134,v134
v_lshlrev_b32_sdwa v137,1,v137 dst_sel:DWORD dst_unused:UNUSED_PAD src0_sel:DWORD src1_sel:WORD_1
v_lshlrev_b32_sdwa v139,1,v139 dst_sel:DWORD dst_unused:UNUSED_PAD src0_sel:DWORD src1_sel:WORD_1
v_mfma_f32_16x16x32_bf16 v[152:155],v[64:67],v[44:47],v[112:115]
ds_read_b64_tr_b16 v[94:95],v3 offset:7704
v_exp_f16_sdwa v120,v121 dst_sel:WORD_1 dst_unused:UNUSED_PRESERVE src0_sel:WORD_1
v_mfma_f32_16x16x32_bf16 v[156:159],v[68:71],v[44:47],v[116:119]
ds_read_u16 v133,v133
v_exp_f16_sdwa v122,v123 dst_sel:WORD_1 dst_unused:UNUSED_PRESERVE src0_sel:WORD_1
v_mfma_f32_16x16x32_bf16 v[148:151],v[68:71],v[40:43],v[116:119]
ds_read_b64_tr_b16 v[96:97],v3 offset:6224
v_pk_fma_f16 v120,v120,s55,s55
v_pk_fma_f16 v122,v122,s55,s55
v_mfma_f32_16x16x32_bf16 v[144:147],v[72:75],v[48:51],v[144:147]
ds_read_u16_d16_hi v135,v135
v_rcp_f16 v121,v120
v_mfma_f32_16x16x32_bf16 v[152:155],v[72:75],v[52:55],v[152:155]
ds_read_b64_tr_b16 v[98:99],v3 offset:7760
ds_read_b64_tr_b16 v[100:101],v3 offset:6232
v_rcp_f16 v123,v122
v_mfma_f32_16x16x32_bf16 v[156:159],v[76:79],v[52:55],v[156:159]
ds_read_u16 v136,v136
v_rcp_f16_sdwa v121,v120 dst_sel:WORD_1 dst_unused:UNUSED_PRESERVE src0_sel:WORD_1
v_mfma_f32_16x16x32_bf16 v[148:151],v[76:79],v[48:51],v[148:151]
ds_read_b64_tr_b16 v[102:103],v3 offset:7768
ds_read_b64_tr_b16 v[104:105],v3 offset:6288
v_rcp_f16_sdwa v123,v122 dst_sel:WORD_1 dst_unused:UNUSED_PRESERVE src0_sel:WORD_1
v_mfma_f32_16x16x32_bf16 v[144:147],v[80:83],v[56:59],v[144:147]
ds_read_u16_d16_hi v138,v138
v_fma_mix_f32 v140,v140,v121,0 op_sel_hi:[0,1,0]
v_fma_mix_f32 v142,v142,v121,0 op_sel:[0,1,0] op_sel_hi:[0,1,0]
v_mfma_f32_16x16x32_bf16 v[152:155],v[80:83],v[60:63],v[152:155]
s_waitcnt lgkmcnt(6)
ds_read_b64_tr_b16 v[106:107],v3 offset:7824
ds_read_b64_tr_b16 v[108:109],v3 offset:6296
v_lshl_add_u64 v[128:129],v[128:129],0,v[130:131]
v_lshl_add_u64 v[130:131],v[132:133],0,v[134:135]
v_mfma_f32_16x16x32_bf16 v[156:159],v[84:87],v[60:63],v[156:159]
ds_read_u16 v137,v137
v_fma_mix_f32 v141,v141,v123,0 op_sel_hi:[0,1,0]
v_fma_mix_f32 v143,v143,v123,0 op_sel:[0,1,0] op_sel_hi:[0,1,0]
v_mfma_f32_16x16x32_bf16 v[148:151],v[84:87],v[56:59],v[148:151]
ds_read_b64_tr_b16 v[110:111],v3 offset:7832
ds_read_b64_tr_b16 v[64:65],v1 offset:512
v_lshlrev_b32_sdwa v144,1,v144 dst_sel:DWORD dst_unused:UNUSED_PAD src0_sel:DWORD src1_sel:WORD_1
v_lshlrev_b32_sdwa v146,1,v146 dst_sel:DWORD dst_unused:UNUSED_PAD src0_sel:DWORD src1_sel:WORD_1
v_mfma_f32_16x16x32_bf16 v[160:163],v[88:91],v[128:131],v[160:163]
ds_read_u16_d16_hi v139,v139
v_lshlrev_b32_sdwa v145,1,v145 dst_sel:DWORD dst_unused:UNUSED_PAD src0_sel:DWORD src1_sel:WORD_1
v_lshlrev_b32_sdwa v147,1,v147 dst_sel:DWORD dst_unused:UNUSED_PAD src0_sel:DWORD src1_sel:WORD_1
v_mfma_f32_16x16x32_bf16 v[168:171],v[92:95],v[128:131],v[168:171]
s_waitcnt lgkmcnt(11)
ds_read_b64_tr_b16 v[66:67],v1 offset:2560
ds_read_b128 v[112:115],v4 offset:896
v_fma_mixlo_f16 v120,v156,s99,0
v_fma_mixlo_f16 v122,v157,s99,0
v_mfma_f32_16x16x32_bf16 v[176:179],v[96:99],v[128:131],v[176:179]
s_waitcnt lgkmcnt(10)
ds_read_u16 v144,v144
v_lshlrev_b32_sdwa v148,1,v148 dst_sel:DWORD dst_unused:UNUSED_PAD src0_sel:DWORD src1_sel:WORD_1
v_lshlrev_b32_sdwa v150,1,v150 dst_sel:DWORD dst_unused:UNUSED_PAD src0_sel:DWORD src1_sel:WORD_1
v_mfma_f32_16x16x32_bf16 v[184:187],v[100:103],v[128:131],v[184:187]
s_waitcnt lgkmcnt(3)
ds_read_b64_tr_b16 v[68:69],v2 offset:512
ds_read_b64_tr_b16 v[70:71],v2 offset:2560
v_lshl_add_u64 v[136:137],v[136:137],0,v[138:139]
v_mfma_f32_16x16x32_bf16 v[192:195],v[104:107],v[128:131],v[192:195]
ds_read_u16_d16_hi v146,v146
v_cvt_pk_bf16_f32 v138,v140,v142
v_cvt_pk_bf16_f32 v139,v141,v143
v_mfma_f32_16x16x32_bf16 v[200:203],v[108:111],v[128:131],v[200:203]
ds_read_b128 v[116:119],v4 offset:928
ds_read_b64_tr_b16 v[72:73],v1 offset:4608
v_fma_mixhi_f16 v120,v158,s99,0
v_fma_mixhi_f16 v122,v159,s99,0
v_mfma_f32_16x16x32_bf16 v[164:167],v[88:91],v[136:139],v[164:167]
ds_read_u16 v145,v145
v_lshlrev_b32_sdwa v149,1,v149 dst_sel:DWORD dst_unused:UNUSED_PAD src0_sel:DWORD src1_sel:WORD_1
v_lshlrev_b32_sdwa v151,1,v151 dst_sel:DWORD dst_unused:UNUSED_PAD src0_sel:DWORD src1_sel:WORD_1
v_mfma_f32_16x16x32_bf16 v[172:175],v[92:95],v[136:139],v[172:175]
s_setprio 1
ds_read_b64_tr_b16 v[74:75],v1 offset:6656
ds_read_b64_tr_b16 v[76:77],v2 offset:4608
v_pk_fma_f16 v121,v120,v120,s54
v_pk_fma_f16 v123,v122,v122,s54
v_mfma_f32_16x16x32_bf16 v[180:183],v[96:99],v[136:139],v[180:183]
ds_read_u16_d16_hi v147,v147
v_pk_mul_f16 v121,v120,v121
v_pk_mul_f16 v123,v122,v123
v_mfma_f32_16x16x32_bf16 v[188:191],v[100:103],v[136:139],v[188:191]
ds_read_b64_tr_b16 v[78:79],v2 offset:6656
v_lshlrev_b32_sdwa v152,1,v152 dst_sel:DWORD dst_unused:UNUSED_PAD src0_sel:DWORD src1_sel:WORD_1
v_lshlrev_b32_sdwa v154,1,v154 dst_sel:DWORD dst_unused:UNUSED_PAD src0_sel:DWORD src1_sel:WORD_1
v_mfma_f32_16x16x32_bf16 v[196:199],v[104:107],v[136:139],v[196:199]
ds_read_u16 v148,v148
v_exp_f16 v120,v121
v_mfma_f32_16x16x32_bf16 v[204:207],v[108:111],v[136:139],v[204:207]
s_waitcnt lgkmcnt(12)
ds_read_b64_tr_b16 v[80:81],v1 offset:8704
v_exp_f16 v122,v123
v_mfma_f32_16x16x32_bf16 v[128:131],v[64:67],v[16:19],v[112:115]
ds_read_u16_d16_hi v150,v150
v_lshlrev_b32_sdwa v153,1,v153 dst_sel:DWORD dst_unused:UNUSED_PAD src0_sel:DWORD src1_sel:WORD_1
v_lshlrev_b32_sdwa v155,1,v155 dst_sel:DWORD dst_unused:UNUSED_PAD src0_sel:DWORD src1_sel:WORD_1
v_mfma_f32_16x16x32_bf16 v[136:139],v[64:67],v[20:23],v[112:115]
s_waitcnt lgkmcnt(9)
ds_read_b64_tr_b16 v[82:83],v1 offset:10752
v_exp_f16_sdwa v120,v121 dst_sel:WORD_1 dst_unused:UNUSED_PRESERVE src0_sel:WORD_1
v_mfma_f32_16x16x32_bf16 v[140:143],v[68:71],v[20:23],v[116:119]
ds_read_u16 v149,v149
v_exp_f16_sdwa v122,v123 dst_sel:WORD_1 dst_unused:UNUSED_PRESERVE src0_sel:WORD_1
v_mfma_f32_16x16x32_bf16 v[132:135],v[68:71],v[16:19],v[116:119]
s_waitcnt lgkmcnt(8)
ds_read_b64_tr_b16 v[84:85],v2 offset:8704
v_pk_fma_f16 v120,v120,s55,s55
v_pk_fma_f16 v122,v122,s55,s55
v_mfma_f32_16x16x32_bf16 v[128:131],v[72:75],v[24:27],v[128:131]
ds_read_u16_d16_hi v151,v151
v_rcp_f16 v121,v120
v_mfma_f32_16x16x32_bf16 v[136:139],v[72:75],v[28:31],v[136:139]
s_waitcnt lgkmcnt(7)
ds_read_b64_tr_b16 v[86:87],v2 offset:10752
v_rcp_f16 v123,v122
v_mfma_f32_16x16x32_bf16 v[140:143],v[76:79],v[28:31],v[140:143]
ds_read_u16 v152,v152
v_rcp_f16_sdwa v121,v120 dst_sel:WORD_1 dst_unused:UNUSED_PRESERVE src0_sel:WORD_1
v_mfma_f32_16x16x32_bf16 v[132:135],v[76:79],v[24:27],v[132:135]
s_waitcnt lgkmcnt(5)
v_rcp_f16_sdwa v123,v122 dst_sel:WORD_1 dst_unused:UNUSED_PRESERVE src0_sel:WORD_1
v_mfma_f32_16x16x32_bf16 v[128:131],v[80:83],v[32:35],v[128:131]
ds_read_u16_d16_hi v154,v154
v_fma_mix_f32 v156,v156,v121,0 op_sel_hi:[0,1,0]
v_fma_mix_f32 v158,v158,v121,0 op_sel:[0,1,0] op_sel_hi:[0,1,0]
v_mfma_f32_16x16x32_bf16 v[136:139],v[80:83],v[36:39],v[136:139]
s_waitcnt lgkmcnt(2)
s_setprio 0
v_lshl_add_u64 v[144:145],v[144:145],0,v[146:147]
v_lshl_add_u64 v[146:147],v[148:149],0,v[150:151]
v_mfma_f32_16x16x32_bf16 v[140:143],v[84:87],v[36:39],v[140:143]
ds_read_u16 v153,v153
v_fma_mix_f32 v157,v157,v123,0 op_sel_hi:[0,1,0]
v_fma_mix_f32 v159,v159,v123,0 op_sel:[0,1,0] op_sel_hi:[0,1,0]
v_mfma_f32_16x16x32_bf16 v[132:135],v[84:87],v[32:35],v[132:135]
v_lshlrev_b32_sdwa v128,1,v128 dst_sel:DWORD dst_unused:UNUSED_PAD src0_sel:DWORD src1_sel:WORD_1
v_lshlrev_b32_sdwa v130,1,v130 dst_sel:DWORD dst_unused:UNUSED_PAD src0_sel:DWORD src1_sel:WORD_1
v_mfma_f32_16x16x32_bf16 v[208:211],v[88:91],v[144:147],v[208:211]
s_barrier
s_setprio 2
ds_read_u16_d16_hi v155,v155
v_lshlrev_b32_sdwa v129,1,v129 dst_sel:DWORD dst_unused:UNUSED_PAD src0_sel:DWORD src1_sel:WORD_1
v_mfma_f32_16x16x32_bf16 v[216:219],v[92:95],v[144:147],v[216:219]
ds_read_u16 v128,v128
v_lshlrev_b32_sdwa v131,1,v131 dst_sel:DWORD dst_unused:UNUSED_PAD src0_sel:DWORD src1_sel:WORD_1
v_mfma_f32_16x16x32_bf16 v[224:227],v[96:99],v[144:147],v[224:227]
v_lshlrev_b32_sdwa v132,1,v132 dst_sel:DWORD dst_unused:UNUSED_PAD src0_sel:DWORD src1_sel:WORD_1
v_mfma_f32_16x16x32_bf16 v[232:235],v[100:103],v[144:147],v[232:235]
s_waitcnt lgkmcnt(1)
ds_read_u16_d16_hi v130,v130
v_lshl_add_u64 v[152:153],v[152:153],0,v[154:155]
v_lshlrev_b32_sdwa v134,1,v134 dst_sel:DWORD dst_unused:UNUSED_PAD src0_sel:DWORD src1_sel:WORD_1
v_mfma_f32_16x16x32_bf16 v[240:243],v[104:107],v[144:147],v[240:243]
ds_read_u16 v129,v129
v_cvt_pk_bf16_f32 v154,v156,v158
v_cvt_pk_bf16_f32 v155,v157,v159
v_mfma_f32_16x16x32_bf16 v[248:251],v[108:111],v[144:147],v[248:251]
v_lshlrev_b32_sdwa v133,1,v133 dst_sel:DWORD dst_unused:UNUSED_PAD src0_sel:DWORD src1_sel:WORD_1
v_mfma_f32_16x16x32_bf16 v[212:215],v[88:91],v[152:155],v[212:215]
ds_read_u16_d16_hi v131,v131
v_lshlrev_b32_sdwa v135,1,v135 dst_sel:DWORD dst_unused:UNUSED_PAD src0_sel:DWORD src1_sel:WORD_1
v_mfma_f32_16x16x32_bf16 v[220:223],v[92:95],v[152:155],v[220:223]
ds_read_u16 v132,v132
v_lshlrev_b32_sdwa v136,1,v136 dst_sel:DWORD dst_unused:UNUSED_PAD src0_sel:DWORD src1_sel:WORD_1
v_mfma_f32_16x16x32_bf16 v[228:231],v[96:99],v[152:155],v[228:231]
ds_read_b64_tr_b16 v[88:89],v3 offset:12320
ds_read_b64_tr_b16 v[90:91],v3 offset:13856
v_lshlrev_b32_sdwa v138,1,v138 dst_sel:DWORD dst_unused:UNUSED_PAD src0_sel:DWORD src1_sel:WORD_1
v_mfma_f32_16x16x32_bf16 v[236:239],v[100:103],v[152:155],v[236:239]
ds_read_u16_d16_hi v134,v134
v_lshlrev_b32_sdwa v137,1,v137 dst_sel:DWORD dst_unused:UNUSED_PAD src0_sel:DWORD src1_sel:WORD_1
v_mfma_f32_16x16x32_bf16 v[244:247],v[104:107],v[152:155],v[244:247]
ds_read_u16 v133,v133
v_lshlrev_b32_sdwa v139,1,v139 dst_sel:DWORD dst_unused:UNUSED_PAD src0_sel:DWORD src1_sel:WORD_1
v_mfma_f32_16x16x32_bf16 v[252:255],v[108:111],v[152:155],v[252:255]
ds_read_b64_tr_b16 v[92:93],v3 offset:12328
ds_read_b64_tr_b16 v[94:95],v3 offset:13864
v_lshlrev_b32_sdwa v140,1,v140 dst_sel:DWORD dst_unused:UNUSED_PAD src0_sel:DWORD src1_sel:WORD_1
v_mfma_f32_16x16x32_bf16 v[144:147],v[64:67],v[40:43],v[112:115]
ds_read_u16_d16_hi v135,v135
v_lshlrev_b32_sdwa v142,1,v142 dst_sel:DWORD dst_unused:UNUSED_PAD src0_sel:DWORD src1_sel:WORD_1
v_mfma_f32_16x16x32_bf16 v[152:155],v[64:67],v[44:47],v[112:115]
ds_read_u16 v136,v136
v_lshlrev_b32_sdwa v141,1,v141 dst_sel:DWORD dst_unused:UNUSED_PAD src0_sel:DWORD src1_sel:WORD_1
v_mfma_f32_16x16x32_bf16 v[156:159],v[68:71],v[44:47],v[116:119]
ds_read_b64_tr_b16 v[96:97],v3 offset:12384
ds_read_b64_tr_b16 v[98:99],v3 offset:13920
v_lshlrev_b32_sdwa v143,1,v143 dst_sel:DWORD dst_unused:UNUSED_PAD src0_sel:DWORD src1_sel:WORD_1
v_mfma_f32_16x16x32_bf16 v[148:151],v[68:71],v[40:43],v[116:119]
ds_read_u16_d16_hi v138,v138
v_mfma_f32_16x16x32_bf16 v[144:147],v[72:75],v[48:51],v[144:147]
ds_read_u16 v137,v137
v_mfma_f32_16x16x32_bf16 v[152:155],v[72:75],v[52:55],v[152:155]
ds_read_b64_tr_b16 v[100:101],v3 offset:12392
ds_read_b64_tr_b16 v[102:103],v3 offset:13928
v_mfma_f32_16x16x32_bf16 v[156:159],v[76:79],v[52:55],v[156:159]
ds_read_u16_d16_hi v139,v139
v_mfma_f32_16x16x32_bf16 v[148:151],v[76:79],v[48:51],v[148:151]
s_waitcnt lgkmcnt(15)
ds_read_u16 v140,v140
v_lshl_add_u64 v[128:129],v[128:129],0,v[130:131]
v_mfma_f32_16x16x32_bf16 v[144:147],v[80:83],v[56:59],v[144:147]
s_waitcnt lgkmcnt(9)
ds_read_b64_tr_b16 v[104:105],v3 offset:12448
ds_read_b64_tr_b16 v[106:107],v3 offset:13984
v_lshl_add_u64 v[130:131],v[132:133],0,v[134:135]
v_mfma_f32_16x16x32_bf16 v[152:155],v[80:83],v[60:63],v[152:155]
ds_read_u16_d16_hi v142,v142
v_mfma_f32_16x16x32_bf16 v[156:159],v[84:87],v[60:63],v[156:159]
ds_read_u16 v141,v141
v_mfma_f32_16x16x32_bf16 v[148:151],v[84:87],v[56:59],v[148:151]
ds_read_b64_tr_b16 v[108:109],v3 offset:12456
ds_read_b64_tr_b16 v[110:111],v3 offset:13992
v_lshlrev_b32_sdwa v144,1,v144 dst_sel:DWORD dst_unused:UNUSED_PAD src0_sel:DWORD src1_sel:WORD_1
v_lshlrev_b32_sdwa v146,1,v146 dst_sel:DWORD dst_unused:UNUSED_PAD src0_sel:DWORD src1_sel:WORD_1
v_mfma_f32_16x16x32_bf16 v[160:163],v[88:91],v[128:131],v[160:163]
ds_read_u16_d16_hi v143,v143
v_lshlrev_b32_sdwa v145,1,v145 dst_sel:DWORD dst_unused:UNUSED_PAD src0_sel:DWORD src1_sel:WORD_1
v_mfma_f32_16x16x32_bf16 v[168:171],v[92:95],v[128:131],v[168:171]
s_waitcnt lgkmcnt(13)
ds_read_u16 v144,v144
v_lshlrev_b32_sdwa v147,1,v147 dst_sel:DWORD dst_unused:UNUSED_PAD src0_sel:DWORD src1_sel:WORD_1
v_mfma_f32_16x16x32_bf16 v[176:179],v[96:99],v[128:131],v[176:179]
s_waitcnt lgkmcnt(10)
ds_read_b64_tr_b16 v[64:65],v1 offset:12320
ds_read_b64_tr_b16 v[66:67],v1 offset:14368
v_lshlrev_b32_sdwa v148,1,v148 dst_sel:DWORD dst_unused:UNUSED_PAD src0_sel:DWORD src1_sel:WORD_1
v_mfma_f32_16x16x32_bf16 v[184:187],v[100:103],v[128:131],v[184:187]
s_waitcnt lgkmcnt(8)
ds_read_u16_d16_hi v146,v146
v_lshl_add_u64 v[136:137],v[136:137],0,v[138:139]
v_lshlrev_b32_sdwa v150,1,v150 dst_sel:DWORD dst_unused:UNUSED_PAD src0_sel:DWORD src1_sel:WORD_1
v_mfma_f32_16x16x32_bf16 v[192:195],v[104:107],v[128:131],v[192:195]
s_waitcnt lgkmcnt(4)
ds_read_u16 v145,v145
v_lshl_add_u64 v[138:139],v[140:141],0,v[142:143]
v_mfma_f32_16x16x32_bf16 v[200:203],v[108:111],v[128:131],v[200:203]
ds_read_b128 v[112:115],v4 offset:1024
ds_read_b64_tr_b16 v[68:69],v2 offset:12320
v_lshlrev_b32_sdwa v149,1,v149 dst_sel:DWORD dst_unused:UNUSED_PAD src0_sel:DWORD src1_sel:WORD_1
v_mfma_f32_16x16x32_bf16 v[164:167],v[88:91],v[136:139],v[164:167]
ds_read_u16_d16_hi v147,v147
v_lshlrev_b32_sdwa v151,1,v151 dst_sel:DWORD dst_unused:UNUSED_PAD src0_sel:DWORD src1_sel:WORD_1
v_mfma_f32_16x16x32_bf16 v[172:175],v[92:95],v[136:139],v[172:175]
ds_read_u16 v148,v148
ds_read_b64_tr_b16 v[70:71],v2 offset:14368
v_lshlrev_b32_sdwa v152,1,v152 dst_sel:DWORD dst_unused:UNUSED_PAD src0_sel:DWORD src1_sel:WORD_1
v_mfma_f32_16x16x32_bf16 v[180:183],v[96:99],v[136:139],v[180:183]
ds_read_b128 v[116:119],v4 offset:1056
ds_read_b64_tr_b16 v[72:73],v1 offset:16416
v_lshlrev_b32_sdwa v154,1,v154 dst_sel:DWORD dst_unused:UNUSED_PAD src0_sel:DWORD src1_sel:WORD_1
v_mfma_f32_16x16x32_bf16 v[188:191],v[100:103],v[136:139],v[188:191]
ds_read_u16_d16_hi v150,v150
v_lshlrev_b32_sdwa v153,1,v153 dst_sel:DWORD dst_unused:UNUSED_PAD src0_sel:DWORD src1_sel:WORD_1
v_mfma_f32_16x16x32_bf16 v[196:199],v[104:107],v[136:139],v[196:199]
ds_read_u16 v149,v149
v_lshlrev_b32_sdwa v155,1,v155 dst_sel:DWORD dst_unused:UNUSED_PAD src0_sel:DWORD src1_sel:WORD_1
v_mfma_f32_16x16x32_bf16 v[204:207],v[108:111],v[136:139],v[204:207]
s_waitcnt lgkmcnt(8)
ds_read_b64_tr_b16 v[74:75],v1 offset:18464
ds_read_b64_tr_b16 v[76:77],v2 offset:16416
v_lshlrev_b32_sdwa v156,1,v156 dst_sel:DWORD dst_unused:UNUSED_PAD src0_sel:DWORD src1_sel:WORD_1
v_mfma_f32_16x16x32_bf16 v[128:131],v[64:67],v[16:19],v[112:115]
ds_read_u16_d16_hi v151,v151
v_lshlrev_b32_sdwa v158,1,v158 dst_sel:DWORD dst_unused:UNUSED_PAD src0_sel:DWORD src1_sel:WORD_1
v_mfma_f32_16x16x32_bf16 v[136:139],v[64:67],v[20:23],v[112:115]
s_waitcnt lgkmcnt(6)
ds_read_u16 v152,v152
ds_read_b64_tr_b16 v[78:79],v2 offset:18464
v_lshlrev_b32_sdwa v157,1,v157 dst_sel:DWORD dst_unused:UNUSED_PAD src0_sel:DWORD src1_sel:WORD_1
v_mfma_f32_16x16x32_bf16 v[140:143],v[68:71],v[20:23],v[116:119]
ds_read_b64_tr_b16 v[80:81],v1 offset:20512
ds_read_b64_tr_b16 v[82:83],v1 offset:22560
v_lshlrev_b32_sdwa v159,1,v159 dst_sel:DWORD dst_unused:UNUSED_PAD src0_sel:DWORD src1_sel:WORD_1
v_mfma_f32_16x16x32_bf16 v[132:135],v[68:71],v[16:19],v[116:119]
s_waitcnt lgkmcnt(6)
ds_read_u16_d16_hi v154,v154
v_mfma_f32_16x16x32_bf16 v[128:131],v[72:75],v[24:27],v[128:131]
ds_read_u16 v153,v153
v_mfma_f32_16x16x32_bf16 v[136:139],v[72:75],v[28:31],v[136:139]
s_waitcnt lgkmcnt(4)
ds_read_b64_tr_b16 v[84:85],v2 offset:20512
ds_read_b64_tr_b16 v[86:87],v2 offset:22560
v_mfma_f32_16x16x32_bf16 v[140:143],v[76:79],v[28:31],v[140:143]
ds_read_u16_d16_hi v155,v155
v_mfma_f32_16x16x32_bf16 v[132:135],v[76:79],v[24:27],v[132:135]
s_waitcnt lgkmcnt(5)
ds_read_u16 v156,v156
v_lshl_add_u64 v[144:145],v[144:145],0,v[146:147]
v_mfma_f32_16x16x32_bf16 v[128:131],v[80:83],v[32:35],v[128:131]
v_lshl_add_u64 v[146:147],v[148:149],0,v[150:151]
v_mfma_f32_16x16x32_bf16 v[136:139],v[80:83],v[36:39],v[136:139]
s_waitcnt lgkmcnt(2)
ds_read_u16_d16_hi v158,v158
v_mfma_f32_16x16x32_bf16 v[140:143],v[84:87],v[36:39],v[140:143]
ds_read_u16 v157,v157
v_mfma_f32_16x16x32_bf16 v[132:135],v[84:87],v[32:35],v[132:135]
s_nop 0
v_lshlrev_b32_sdwa v128,1,v128 dst_sel:DWORD dst_unused:UNUSED_PAD src0_sel:DWORD src1_sel:WORD_1
v_lshlrev_b32_sdwa v130,1,v130 dst_sel:DWORD dst_unused:UNUSED_PAD src0_sel:DWORD src1_sel:WORD_1
v_mfma_f32_16x16x32_bf16 v[208:211],v[88:91],v[144:147],v[208:211]
ds_read_u16_d16_hi v159,v159
v_lshlrev_b32_sdwa v129,1,v129 dst_sel:DWORD dst_unused:UNUSED_PAD src0_sel:DWORD src1_sel:WORD_1
v_mfma_f32_16x16x32_bf16 v[216:219],v[92:95],v[144:147],v[216:219]
ds_read_u16 v128,v128
v_lshlrev_b32_sdwa v131,1,v131 dst_sel:DWORD dst_unused:UNUSED_PAD src0_sel:DWORD src1_sel:WORD_1
v_mfma_f32_16x16x32_bf16 v[224:227],v[96:99],v[144:147],v[224:227]
v_lshlrev_b32_sdwa v132,1,v132 dst_sel:DWORD dst_unused:UNUSED_PAD src0_sel:DWORD src1_sel:WORD_1
v_mfma_f32_16x16x32_bf16 v[232:235],v[100:103],v[144:147],v[232:235]
s_waitcnt lgkmcnt(5)
ds_read_u16_d16_hi v130,v130
v_lshl_add_u64 v[152:153],v[152:153],0,v[154:155]
v_lshlrev_b32_sdwa v134,1,v134 dst_sel:DWORD dst_unused:UNUSED_PAD src0_sel:DWORD src1_sel:WORD_1
v_mfma_f32_16x16x32_bf16 v[240:243],v[104:107],v[144:147],v[240:243]
s_waitcnt lgkmcnt(2)
ds_read_u16 v129,v129
v_lshl_add_u64 v[154:155],v[156:157],0,v[158:159]
v_mfma_f32_16x16x32_bf16 v[248:251],v[108:111],v[144:147],v[248:251]
v_lshlrev_b32_sdwa v133,1,v133 dst_sel:DWORD dst_unused:UNUSED_PAD src0_sel:DWORD src1_sel:WORD_1
v_mfma_f32_16x16x32_bf16 v[212:215],v[88:91],v[152:155],v[212:215]
ds_read_u16_d16_hi v131,v131
v_lshlrev_b32_sdwa v135,1,v135 dst_sel:DWORD dst_unused:UNUSED_PAD src0_sel:DWORD src1_sel:WORD_1
v_mfma_f32_16x16x32_bf16 v[220:223],v[92:95],v[152:155],v[220:223]
ds_read_u16 v132,v132
v_lshlrev_b32_sdwa v136,1,v136 dst_sel:DWORD dst_unused:UNUSED_PAD src0_sel:DWORD src1_sel:WORD_1
v_mfma_f32_16x16x32_bf16 v[228:231],v[96:99],v[152:155],v[228:231]
ds_read_b64_tr_b16 v[88:89],v3 offset:18480
ds_read_b64_tr_b16 v[90:91],v3 offset:20016
v_lshlrev_b32_sdwa v138,1,v138 dst_sel:DWORD dst_unused:UNUSED_PAD src0_sel:DWORD src1_sel:WORD_1
v_mfma_f32_16x16x32_bf16 v[236:239],v[100:103],v[152:155],v[236:239]
ds_read_u16_d16_hi v134,v134
v_lshlrev_b32_sdwa v137,1,v137 dst_sel:DWORD dst_unused:UNUSED_PAD src0_sel:DWORD src1_sel:WORD_1
v_mfma_f32_16x16x32_bf16 v[244:247],v[104:107],v[152:155],v[244:247]
ds_read_u16 v133,v133
v_lshlrev_b32_sdwa v139,1,v139 dst_sel:DWORD dst_unused:UNUSED_PAD src0_sel:DWORD src1_sel:WORD_1
v_mfma_f32_16x16x32_bf16 v[252:255],v[108:111],v[152:155],v[252:255]
ds_read_b64_tr_b16 v[92:93],v3 offset:18488
ds_read_b64_tr_b16 v[94:95],v3 offset:20024
v_lshlrev_b32_sdwa v140,1,v140 dst_sel:DWORD dst_unused:UNUSED_PAD src0_sel:DWORD src1_sel:WORD_1
v_mfma_f32_16x16x32_bf16 v[144:147],v[64:67],v[40:43],v[112:115]
ds_read_u16_d16_hi v135,v135
v_lshlrev_b32_sdwa v142,1,v142 dst_sel:DWORD dst_unused:UNUSED_PAD src0_sel:DWORD src1_sel:WORD_1
v_mfma_f32_16x16x32_bf16 v[152:155],v[64:67],v[44:47],v[112:115]
ds_read_u16 v136,v136
v_lshlrev_b32_sdwa v141,1,v141 dst_sel:DWORD dst_unused:UNUSED_PAD src0_sel:DWORD src1_sel:WORD_1
v_mfma_f32_16x16x32_bf16 v[156:159],v[68:71],v[44:47],v[116:119]
ds_read_b64_tr_b16 v[96:97],v3 offset:18544
ds_read_b64_tr_b16 v[98:99],v3 offset:20080
v_lshlrev_b32_sdwa v143,1,v143 dst_sel:DWORD dst_unused:UNUSED_PAD src0_sel:DWORD src1_sel:WORD_1
v_mfma_f32_16x16x32_bf16 v[148:151],v[68:71],v[40:43],v[116:119]
ds_read_u16_d16_hi v138,v138
v_mfma_f32_16x16x32_bf16 v[144:147],v[72:75],v[48:51],v[144:147]
ds_read_u16 v137,v137
v_mfma_f32_16x16x32_bf16 v[152:155],v[72:75],v[52:55],v[152:155]
ds_read_b64_tr_b16 v[100:101],v3 offset:18552
ds_read_b64_tr_b16 v[102:103],v3 offset:20088
v_mfma_f32_16x16x32_bf16 v[156:159],v[76:79],v[52:55],v[156:159]
ds_read_u16_d16_hi v139,v139
v_mfma_f32_16x16x32_bf16 v[148:151],v[76:79],v[48:51],v[148:151]
s_waitcnt lgkmcnt(15)
ds_read_u16 v140,v140
v_lshl_add_u64 v[128:129],v[128:129],0,v[130:131]
v_mfma_f32_16x16x32_bf16 v[144:147],v[80:83],v[56:59],v[144:147]
s_waitcnt lgkmcnt(9)
ds_read_b64_tr_b16 v[104:105],v3 offset:18608
ds_read_b64_tr_b16 v[106:107],v3 offset:20144
v_lshl_add_u64 v[130:131],v[132:133],0,v[134:135]
v_mfma_f32_16x16x32_bf16 v[152:155],v[80:83],v[60:63],v[152:155]
ds_read_u16_d16_hi v142,v142
v_mfma_f32_16x16x32_bf16 v[156:159],v[84:87],v[60:63],v[156:159]
ds_read_u16 v141,v141
v_mfma_f32_16x16x32_bf16 v[148:151],v[84:87],v[56:59],v[148:151]
ds_read_b64_tr_b16 v[108:109],v3 offset:18616
ds_read_b64_tr_b16 v[110:111],v3 offset:20152
v_lshlrev_b32_sdwa v144,1,v144 dst_sel:DWORD dst_unused:UNUSED_PAD src0_sel:DWORD src1_sel:WORD_1
v_lshlrev_b32_sdwa v146,1,v146 dst_sel:DWORD dst_unused:UNUSED_PAD src0_sel:DWORD src1_sel:WORD_1
v_mfma_f32_16x16x32_bf16 v[160:163],v[88:91],v[128:131],v[160:163]
ds_read_u16_d16_hi v143,v143
v_lshlrev_b32_sdwa v145,1,v145 dst_sel:DWORD dst_unused:UNUSED_PAD src0_sel:DWORD src1_sel:WORD_1
v_mfma_f32_16x16x32_bf16 v[168:171],v[92:95],v[128:131],v[168:171]
s_waitcnt lgkmcnt(13)
ds_read_u16 v144,v144
v_lshlrev_b32_sdwa v147,1,v147 dst_sel:DWORD dst_unused:UNUSED_PAD src0_sel:DWORD src1_sel:WORD_1
v_mfma_f32_16x16x32_bf16 v[176:179],v[96:99],v[128:131],v[176:179]
s_waitcnt lgkmcnt(10)
ds_read_b64_tr_b16 v[64:65],v1 offset:12832
ds_read_b64_tr_b16 v[66:67],v1 offset:14880
v_lshlrev_b32_sdwa v148,1,v148 dst_sel:DWORD dst_unused:UNUSED_PAD src0_sel:DWORD src1_sel:WORD_1
v_mfma_f32_16x16x32_bf16 v[184:187],v[100:103],v[128:131],v[184:187]
s_waitcnt lgkmcnt(8)
ds_read_u16_d16_hi v146,v146
v_lshl_add_u64 v[136:137],v[136:137],0,v[138:139]
v_lshlrev_b32_sdwa v150,1,v150 dst_sel:DWORD dst_unused:UNUSED_PAD src0_sel:DWORD src1_sel:WORD_1
v_mfma_f32_16x16x32_bf16 v[192:195],v[104:107],v[128:131],v[192:195]
s_waitcnt lgkmcnt(4)
ds_read_u16 v145,v145
v_lshl_add_u64 v[138:139],v[140:141],0,v[142:143]
v_mfma_f32_16x16x32_bf16 v[200:203],v[108:111],v[128:131],v[200:203]
ds_read_b128 v[112:115],v4 offset:1152
ds_read_b64_tr_b16 v[68:69],v2 offset:12832
v_lshlrev_b32_sdwa v149,1,v149 dst_sel:DWORD dst_unused:UNUSED_PAD src0_sel:DWORD src1_sel:WORD_1
v_mfma_f32_16x16x32_bf16 v[164:167],v[88:91],v[136:139],v[164:167]
ds_read_u16_d16_hi v147,v147
v_lshlrev_b32_sdwa v151,1,v151 dst_sel:DWORD dst_unused:UNUSED_PAD src0_sel:DWORD src1_sel:WORD_1
v_mfma_f32_16x16x32_bf16 v[172:175],v[92:95],v[136:139],v[172:175]
ds_read_u16 v148,v148
ds_read_b64_tr_b16 v[70:71],v2 offset:14880
v_lshlrev_b32_sdwa v152,1,v152 dst_sel:DWORD dst_unused:UNUSED_PAD src0_sel:DWORD src1_sel:WORD_1
v_mfma_f32_16x16x32_bf16 v[180:183],v[96:99],v[136:139],v[180:183]
ds_read_b128 v[116:119],v4 offset:1184
ds_read_b64_tr_b16 v[72:73],v1 offset:16928
v_lshlrev_b32_sdwa v154,1,v154 dst_sel:DWORD dst_unused:UNUSED_PAD src0_sel:DWORD src1_sel:WORD_1
v_mfma_f32_16x16x32_bf16 v[188:191],v[100:103],v[136:139],v[188:191]
ds_read_u16_d16_hi v150,v150
v_lshlrev_b32_sdwa v153,1,v153 dst_sel:DWORD dst_unused:UNUSED_PAD src0_sel:DWORD src1_sel:WORD_1
v_mfma_f32_16x16x32_bf16 v[196:199],v[104:107],v[136:139],v[196:199]
ds_read_u16 v149,v149
v_lshlrev_b32_sdwa v155,1,v155 dst_sel:DWORD dst_unused:UNUSED_PAD src0_sel:DWORD src1_sel:WORD_1
v_mfma_f32_16x16x32_bf16 v[204:207],v[108:111],v[136:139],v[204:207]
s_waitcnt lgkmcnt(8)
ds_read_b64_tr_b16 v[74:75],v1 offset:18976
ds_read_b64_tr_b16 v[76:77],v2 offset:16928
v_lshlrev_b32_sdwa v156,1,v156 dst_sel:DWORD dst_unused:UNUSED_PAD src0_sel:DWORD src1_sel:WORD_1
v_mfma_f32_16x16x32_bf16 v[128:131],v[64:67],v[16:19],v[112:115]
ds_read_u16_d16_hi v151,v151
v_lshlrev_b32_sdwa v158,1,v158 dst_sel:DWORD dst_unused:UNUSED_PAD src0_sel:DWORD src1_sel:WORD_1
v_mfma_f32_16x16x32_bf16 v[136:139],v[64:67],v[20:23],v[112:115]
s_waitcnt lgkmcnt(6)
ds_read_u16 v152,v152
ds_read_b64_tr_b16 v[78:79],v2 offset:18976
v_lshlrev_b32_sdwa v157,1,v157 dst_sel:DWORD dst_unused:UNUSED_PAD src0_sel:DWORD src1_sel:WORD_1
v_mfma_f32_16x16x32_bf16 v[140:143],v[68:71],v[20:23],v[116:119]
ds_read_b64_tr_b16 v[80:81],v1 offset:21024
ds_read_b64_tr_b16 v[82:83],v1 offset:23072
v_lshlrev_b32_sdwa v159,1,v159 dst_sel:DWORD dst_unused:UNUSED_PAD src0_sel:DWORD src1_sel:WORD_1
v_mfma_f32_16x16x32_bf16 v[132:135],v[68:71],v[16:19],v[116:119]
s_waitcnt lgkmcnt(6)
ds_read_u16_d16_hi v154,v154
v_mfma_f32_16x16x32_bf16 v[128:131],v[72:75],v[24:27],v[128:131]
ds_read_u16 v153,v153
v_mfma_f32_16x16x32_bf16 v[136:139],v[72:75],v[28:31],v[136:139]
s_waitcnt lgkmcnt(4)
ds_read_b64_tr_b16 v[84:85],v2 offset:21024
ds_read_b64_tr_b16 v[86:87],v2 offset:23072
v_mfma_f32_16x16x32_bf16 v[140:143],v[76:79],v[28:31],v[140:143]
ds_read_u16_d16_hi v155,v155
v_mfma_f32_16x16x32_bf16 v[132:135],v[76:79],v[24:27],v[132:135]
s_waitcnt lgkmcnt(5)
ds_read_u16 v156,v156
v_lshl_add_u64 v[144:145],v[144:145],0,v[146:147]
v_mfma_f32_16x16x32_bf16 v[128:131],v[80:83],v[32:35],v[128:131]
v_lshl_add_u64 v[146:147],v[148:149],0,v[150:151]
v_mfma_f32_16x16x32_bf16 v[136:139],v[80:83],v[36:39],v[136:139]
s_waitcnt lgkmcnt(2)
ds_read_u16_d16_hi v158,v158
v_mfma_f32_16x16x32_bf16 v[140:143],v[84:87],v[36:39],v[140:143]
ds_read_u16 v157,v157
v_mfma_f32_16x16x32_bf16 v[132:135],v[84:87],v[32:35],v[132:135]
s_nop 0
v_lshlrev_b32_sdwa v128,1,v128 dst_sel:DWORD dst_unused:UNUSED_PAD src0_sel:DWORD src1_sel:WORD_1
v_lshlrev_b32_sdwa v130,1,v130 dst_sel:DWORD dst_unused:UNUSED_PAD src0_sel:DWORD src1_sel:WORD_1
v_mfma_f32_16x16x32_bf16 v[208:211],v[88:91],v[144:147],v[208:211]
s_barrier
s_setprio 3
ds_read_u16_d16_hi v159,v159
v_lshlrev_b32_sdwa v129,1,v129 dst_sel:DWORD dst_unused:UNUSED_PAD src0_sel:DWORD src1_sel:WORD_1
v_lshlrev_b32_sdwa v131,1,v131 dst_sel:DWORD dst_unused:UNUSED_PAD src0_sel:DWORD src1_sel:WORD_1
v_mfma_f32_16x16x32_bf16 v[216:219],v[92:95],v[144:147],v[216:219]
v_fma_mixlo_f16 v120,v140,s99,0
v_fma_mixlo_f16 v122,v141,s99,0
v_mfma_f32_16x16x32_bf16 v[224:227],v[96:99],v[144:147],v[224:227]
s_cbranch_vccz RA1_%=
s_add_u32 m0,s58,12320
s_mov_b32 s69,s60
buffer_load_dwordx4 v5,s[44:47],s69 offen lds
s_add_u32 m0,m0,1024
s_add_u32 s69,s69,s53
buffer_load_dwordx4 v6,s[44:47],s69 offen lds
RA1_%=:
ds_read_u16 v128,v128
v_lshlrev_b32_sdwa v132,1,v132 dst_sel:DWORD dst_unused:UNUSED_PAD src0_sel:DWORD src1_sel:WORD_1
v_lshlrev_b32_sdwa v134,1,v134 dst_sel:DWORD dst_unused:UNUSED_PAD src0_sel:DWORD src1_sel:WORD_1
v_mfma_f32_16x16x32_bf16 v[232:235],v[100:103],v[144:147],v[232:235]
s_waitcnt lgkmcnt(5)
v_lshl_add_u64 v[152:153],v[152:153],0,v[154:155]
v_mfma_f32_16x16x32_bf16 v[240:243],v[104:107],v[144:147],v[240:243]
s_waitcnt lgkmcnt(1)
ds_read_u16_d16_hi v130,v130
v_lshl_add_u64 v[154:155],v[156:157],0,v[158:159]
v_mfma_f32_16x16x32_bf16 v[248:251],v[108:111],v[144:147],v[248:251]
v_fma_mixhi_f16 v120,v142,s99,0
v_fma_mixhi_f16 v122,v143,s99,0
v_mfma_f32_16x16x32_bf16 v[212:215],v[88:91],v[152:155],v[212:215]
s_cbranch_vccz RA2_%=
s_add_u32 m0,m0,1024
s_add_u32 s69,s69,s53
buffer_load_dwordx4 v5,s[44:47],s69 offen lds
s_add_u32 m0,m0,s50
s_add_u32 s69,s69,s53
buffer_load_dwordx4 v6,s[44:47],s69 offen lds
RA2_%=:
ds_read_u16 v129,v129
v_lshlrev_b32_sdwa v133,1,v133 dst_sel:DWORD dst_unused:UNUSED_PAD src0_sel:DWORD src1_sel:WORD_1
v_lshlrev_b32_sdwa v135,1,v135 dst_sel:DWORD dst_unused:UNUSED_PAD src0_sel:DWORD src1_sel:WORD_1
v_mfma_f32_16x16x32_bf16 v[220:223],v[92:95],v[152:155],v[220:223]
ds_read_b64_tr_b16 v[88:89],v3 offset:0
v_pk_fma_f16 v121,v120,v120,s54
v_pk_fma_f16 v123,v122,v122,s54
v_mfma_f32_16x16x32_bf16 v[228:231],v[96:99],v[152:155],v[228:231]
ds_read_u16_d16_hi v131,v131
v_pk_mul_f16 v121,v120,v121
v_pk_mul_f16 v123,v122,v123
v_mfma_f32_16x16x32_bf16 v[236:239],v[100:103],v[152:155],v[236:239]
ds_read_b64_tr_b16 v[90:91],v3 offset:1536
v_lshlrev_b32_sdwa v136,1,v136 dst_sel:DWORD dst_unused:UNUSED_PAD src0_sel:DWORD src1_sel:WORD_1
v_lshlrev_b32_sdwa v138,1,v138 dst_sel:DWORD dst_unused:UNUSED_PAD src0_sel:DWORD src1_sel:WORD_1
v_mfma_f32_16x16x32_bf16 v[244:247],v[104:107],v[152:155],v[244:247]
s_cbranch_vccz RA3_%=
s_add_u32 m0,m0,1024
s_add_u32 s69,s69,s53
buffer_load_dwordx4 v5,s[44:47],s69 offen lds
s_add_u32 m0,m0,1024
s_add_u32 s69,s69,s53
buffer_load_dwordx4 v6,s[44:47],s69 offen lds
RA3_%=:
ds_read_u16 v132,v132
v_exp_f16 v120,v121
v_mfma_f32_16x16x32_bf16 v[252:255],v[108:111],v[152:155],v[252:255]
ds_read_b64_tr_b16 v[92:93],v3 offset:8
v_exp_f16 v122,v123
v_mfma_f32_16x16x32_bf16 v[144:147],v[64:67],v[40:43],v[112:115]
ds_read_u16_d16_hi v134,v134
v_lshlrev_b32_sdwa v137,1,v137 dst_sel:DWORD dst_unused:UNUSED_PAD src0_sel:DWORD src1_sel:WORD_1
v_lshlrev_b32_sdwa v139,1,v139 dst_sel:DWORD dst_unused:UNUSED_PAD src0_sel:DWORD src1_sel:WORD_1
v_mfma_f32_16x16x32_bf16 v[152:155],v[64:67],v[44:47],v[112:115]
s_cbranch_vccz RA4_%=
s_add_u32 m0,m0,s50
s_add_u32 s69,s69,s51
buffer_load_dwordx4 v5,s[44:47],s69 offen lds
s_add_u32 m0,m0,1024
s_add_u32 s69,s69,s53
buffer_load_dwordx4 v6,s[44:47],s69 offen lds
RA4_%=:
ds_read_b64_tr_b16 v[94:95],v3 offset:1544
v_exp_f16_sdwa v120,v121 dst_sel:WORD_1 dst_unused:UNUSED_PRESERVE src0_sel:WORD_1
v_mfma_f32_16x16x32_bf16 v[156:159],v[68:71],v[44:47],v[116:119]
ds_read_u16 v133,v133
v_exp_f16_sdwa v122,v123 dst_sel:WORD_1 dst_unused:UNUSED_PRESERVE src0_sel:WORD_1
v_mfma_f32_16x16x32_bf16 v[148:151],v[68:71],v[40:43],v[116:119]
ds_read_b64_tr_b16 v[96:97],v3 offset:64
v_pk_fma_f16 v120,v120,s55,s55
v_pk_fma_f16 v122,v122,s55,s55
v_mfma_f32_16x16x32_bf16 v[144:147],v[72:75],v[48:51],v[144:147]
ds_read_u16_d16_hi v135,v135
v_rcp_f16 v121,v120
v_mfma_f32_16x16x32_bf16 v[152:155],v[72:75],v[52:55],v[152:155]
s_cbranch_vccz RA5_%=
s_add_u32 m0,m0,1024
s_add_u32 s69,s69,s53
buffer_load_dwordx4 v5,s[44:47],s69 offen lds
s_add_u32 m0,m0,s50
s_add_u32 s69,s69,s53
buffer_load_dwordx4 v6,s[44:47],s69 offen lds
RA5_%=:
ds_read_b64_tr_b16 v[98:99],v3 offset:1600
ds_read_b64_tr_b16 v[100:101],v3 offset:72
v_rcp_f16 v123,v122
v_mfma_f32_16x16x32_bf16 v[156:159],v[76:79],v[52:55],v[156:159]
ds_read_u16 v136,v136
v_rcp_f16_sdwa v121,v120 dst_sel:WORD_1 dst_unused:UNUSED_PRESERVE src0_sel:WORD_1
v_mfma_f32_16x16x32_bf16 v[148:151],v[76:79],v[48:51],v[148:151]
ds_read_b64_tr_b16 v[102:103],v3 offset:1608
ds_read_b64_tr_b16 v[104:105],v3 offset:128
v_rcp_f16_sdwa v123,v122 dst_sel:WORD_1 dst_unused:UNUSED_PRESERVE src0_sel:WORD_1
v_mfma_f32_16x16x32_bf16 v[144:147],v[80:83],v[56:59],v[144:147]
ds_read_u16_d16_hi v138,v138
v_fma_mix_f32 v140,v140,v121,0 op_sel_hi:[0,1,0]
v_fma_mix_f32 v142,v142,v121,0 op_sel:[0,1,0] op_sel_hi:[0,1,0]
v_mfma_f32_16x16x32_bf16 v[152:155],v[80:83],v[60:63],v[152:155]
s_waitcnt lgkmcnt(6)
s_cbranch_vccz RA6_%=
s_add_u32 m0,m0,1024
s_add_u32 s69,s69,s53
buffer_load_dwordx4 v5,s[44:47],s69 offen lds
s_add_u32 m0,m0,1024
s_add_u32 s69,s69,s53
buffer_load_dwordx4 v6,s[44:47],s69 offen lds
RA6_%=:
ds_read_b64_tr_b16 v[106:107],v3 offset:1664
ds_read_b64_tr_b16 v[108:109],v3 offset:136
v_lshl_add_u64 v[128:129],v[128:129],0,v[130:131]
v_lshl_add_u64 v[130:131],v[132:133],0,v[134:135]
v_mfma_f32_16x16x32_bf16 v[156:159],v[84:87],v[60:63],v[156:159]
ds_read_u16 v137,v137
v_fma_mix_f32 v141,v141,v123,0 op_sel_hi:[0,1,0]
v_fma_mix_f32 v143,v143,v123,0 op_sel:[0,1,0] op_sel_hi:[0,1,0]
v_mfma_f32_16x16x32_bf16 v[148:151],v[84:87],v[56:59],v[148:151]
ds_read_b64_tr_b16 v[110:111],v3 offset:1672
ds_read_b64_tr_b16 v[64:65],v1 offset:0
v_lshlrev_b32_sdwa v144,1,v144 dst_sel:DWORD dst_unused:UNUSED_PAD src0_sel:DWORD src1_sel:WORD_1
v_lshlrev_b32_sdwa v146,1,v146 dst_sel:DWORD dst_unused:UNUSED_PAD src0_sel:DWORD src1_sel:WORD_1
v_mfma_f32_16x16x32_bf16 v[160:163],v[88:91],v[128:131],v[160:163]
ds_read_u16_d16_hi v139,v139
v_lshlrev_b32_sdwa v145,1,v145 dst_sel:DWORD dst_unused:UNUSED_PAD src0_sel:DWORD src1_sel:WORD_1
v_lshlrev_b32_sdwa v147,1,v147 dst_sel:DWORD dst_unused:UNUSED_PAD src0_sel:DWORD src1_sel:WORD_1
v_mfma_f32_16x16x32_bf16 v[168:171],v[92:95],v[128:131],v[168:171]
s_waitcnt lgkmcnt(11)
ds_read_b64_tr_b16 v[66:67],v1 offset:2048
ds_read_b128 v[112:115],v4 offset:1280
v_fma_mixlo_f16 v120,v156,s99,0
v_fma_mixlo_f16 v122,v157,s99,0
v_mfma_f32_16x16x32_bf16 v[176:179],v[96:99],v[128:131],v[176:179]
s_waitcnt lgkmcnt(10)
ds_read_u16 v144,v144
v_lshlrev_b32_sdwa v148,1,v148 dst_sel:DWORD dst_unused:UNUSED_PAD src0_sel:DWORD src1_sel:WORD_1
v_lshlrev_b32_sdwa v150,1,v150 dst_sel:DWORD dst_unused:UNUSED_PAD src0_sel:DWORD src1_sel:WORD_1
v_mfma_f32_16x16x32_bf16 v[184:187],v[100:103],v[128:131],v[184:187]
s_waitcnt lgkmcnt(3)
ds_read_b64_tr_b16 v[68:69],v2 offset:0
ds_read_b64_tr_b16 v[70:71],v2 offset:2048
v_lshl_add_u64 v[136:137],v[136:137],0,v[138:139]
v_mfma_f32_16x16x32_bf16 v[192:195],v[104:107],v[128:131],v[192:195]
ds_read_u16_d16_hi v146,v146
v_cvt_pk_bf16_f32 v138,v140,v142
v_cvt_pk_bf16_f32 v139,v141,v143
v_mfma_f32_16x16x32_bf16 v[200:203],v[108:111],v[128:131],v[200:203]
ds_read_b128 v[116:119],v4 offset:1312
ds_read_b64_tr_b16 v[72:73],v1 offset:4096
v_fma_mixhi_f16 v120,v158,s99,0
v_fma_mixhi_f16 v122,v159,s99,0
v_mfma_f32_16x16x32_bf16 v[164:167],v[88:91],v[136:139],v[164:167]
ds_read_u16 v145,v145
v_lshlrev_b32_sdwa v149,1,v149 dst_sel:DWORD dst_unused:UNUSED_PAD src0_sel:DWORD src1_sel:WORD_1
v_lshlrev_b32_sdwa v151,1,v151 dst_sel:DWORD dst_unused:UNUSED_PAD src0_sel:DWORD src1_sel:WORD_1
v_mfma_f32_16x16x32_bf16 v[172:175],v[92:95],v[136:139],v[172:175]
ds_read_b64_tr_b16 v[74:75],v1 offset:6144
ds_read_b64_tr_b16 v[76:77],v2 offset:4096
v_pk_fma_f16 v121,v120,v120,s54
v_pk_fma_f16 v123,v122,v122,s54
v_mfma_f32_16x16x32_bf16 v[180:183],v[96:99],v[136:139],v[180:183]
ds_read_u16_d16_hi v147,v147
v_pk_mul_f16 v121,v120,v121
v_pk_mul_f16 v123,v122,v123
v_mfma_f32_16x16x32_bf16 v[188:191],v[100:103],v[136:139],v[188:191]
ds_read_b64_tr_b16 v[78:79],v2 offset:6144
v_lshlrev_b32_sdwa v152,1,v152 dst_sel:DWORD dst_unused:UNUSED_PAD src0_sel:DWORD src1_sel:WORD_1
v_lshlrev_b32_sdwa v154,1,v154 dst_sel:DWORD dst_unused:UNUSED_PAD src0_sel:DWORD src1_sel:WORD_1
v_mfma_f32_16x16x32_bf16 v[196:199],v[104:107],v[136:139],v[196:199]
ds_read_u16 v148,v148
v_exp_f16 v120,v121
v_mfma_f32_16x16x32_bf16 v[204:207],v[108:111],v[136:139],v[204:207]
s_waitcnt lgkmcnt(12)
ds_read_b64_tr_b16 v[80:81],v1 offset:8192
v_exp_f16 v122,v123
v_mfma_f32_16x16x32_bf16 v[128:131],v[64:67],v[16:19],v[112:115]
ds_read_u16_d16_hi v150,v150
v_lshlrev_b32_sdwa v153,1,v153 dst_sel:DWORD dst_unused:UNUSED_PAD src0_sel:DWORD src1_sel:WORD_1
v_lshlrev_b32_sdwa v155,1,v155 dst_sel:DWORD dst_unused:UNUSED_PAD src0_sel:DWORD src1_sel:WORD_1
v_mfma_f32_16x16x32_bf16 v[136:139],v[64:67],v[20:23],v[112:115]
s_waitcnt lgkmcnt(9)
ds_read_b64_tr_b16 v[82:83],v1 offset:10240
v_exp_f16_sdwa v120,v121 dst_sel:WORD_1 dst_unused:UNUSED_PRESERVE src0_sel:WORD_1
v_mfma_f32_16x16x32_bf16 v[140:143],v[68:71],v[20:23],v[116:119]
ds_read_u16 v149,v149
v_exp_f16_sdwa v122,v123 dst_sel:WORD_1 dst_unused:UNUSED_PRESERVE src0_sel:WORD_1
v_mfma_f32_16x16x32_bf16 v[132:135],v[68:71],v[16:19],v[116:119]
s_waitcnt lgkmcnt(8)
ds_read_b64_tr_b16 v[84:85],v2 offset:8192
v_pk_fma_f16 v120,v120,s55,s55
v_pk_fma_f16 v122,v122,s55,s55
v_mfma_f32_16x16x32_bf16 v[128:131],v[72:75],v[24:27],v[128:131]
ds_read_u16_d16_hi v151,v151
v_rcp_f16 v121,v120
v_mfma_f32_16x16x32_bf16 v[136:139],v[72:75],v[28:31],v[136:139]
s_waitcnt lgkmcnt(7)
ds_read_b64_tr_b16 v[86:87],v2 offset:10240
v_rcp_f16 v123,v122
v_mfma_f32_16x16x32_bf16 v[140:143],v[76:79],v[28:31],v[140:143]
ds_read_u16 v152,v152
v_rcp_f16_sdwa v121,v120 dst_sel:WORD_1 dst_unused:UNUSED_PRESERVE src0_sel:WORD_1
v_mfma_f32_16x16x32_bf16 v[132:135],v[76:79],v[24:27],v[132:135]
s_waitcnt lgkmcnt(5)
v_rcp_f16_sdwa v123,v122 dst_sel:WORD_1 dst_unused:UNUSED_PRESERVE src0_sel:WORD_1
v_mfma_f32_16x16x32_bf16 v[128:131],v[80:83],v[32:35],v[128:131]
ds_read_u16_d16_hi v154,v154
v_fma_mix_f32 v156,v156,v121,0 op_sel_hi:[0,1,0]
v_fma_mix_f32 v158,v158,v121,0 op_sel:[0,1,0] op_sel_hi:[0,1,0]
v_mfma_f32_16x16x32_bf16 v[136:139],v[80:83],v[36:39],v[136:139]
s_waitcnt lgkmcnt(2)
v_lshl_add_u64 v[144:145],v[144:145],0,v[146:147]
v_lshl_add_u64 v[146:147],v[148:149],0,v[150:151]
v_mfma_f32_16x16x32_bf16 v[140:143],v[84:87],v[36:39],v[140:143]
ds_read_u16 v153,v153
v_fma_mix_f32 v157,v157,v123,0 op_sel_hi:[0,1,0]
v_fma_mix_f32 v159,v159,v123,0 op_sel:[0,1,0] op_sel_hi:[0,1,0]
v_mfma_f32_16x16x32_bf16 v[132:135],v[84:87],v[32:35],v[132:135]
v_lshlrev_b32_sdwa v128,1,v128 dst_sel:DWORD dst_unused:UNUSED_PAD src0_sel:DWORD src1_sel:WORD_1
v_lshlrev_b32_sdwa v130,1,v130 dst_sel:DWORD dst_unused:UNUSED_PAD src0_sel:DWORD src1_sel:WORD_1
v_mfma_f32_16x16x32_bf16 v[208:211],v[88:91],v[144:147],v[208:211]
ds_read_u16_d16_hi v155,v155
v_lshlrev_b32_sdwa v129,1,v129 dst_sel:DWORD dst_unused:UNUSED_PAD src0_sel:DWORD src1_sel:WORD_1
v_lshlrev_b32_sdwa v131,1,v131 dst_sel:DWORD dst_unused:UNUSED_PAD src0_sel:DWORD src1_sel:WORD_1
v_mfma_f32_16x16x32_bf16 v[216:219],v[92:95],v[144:147],v[216:219]
v_fma_mixlo_f16 v120,v140,s99,0
v_fma_mixlo_f16 v122,v141,s99,0
v_mfma_f32_16x16x32_bf16 v[224:227],v[96:99],v[144:147],v[224:227]
ds_read_u16 v128,v128
v_lshlrev_b32_sdwa v132,1,v132 dst_sel:DWORD dst_unused:UNUSED_PAD src0_sel:DWORD src1_sel:WORD_1
v_lshlrev_b32_sdwa v134,1,v134 dst_sel:DWORD dst_unused:UNUSED_PAD src0_sel:DWORD src1_sel:WORD_1
v_mfma_f32_16x16x32_bf16 v[232:235],v[100:103],v[144:147],v[232:235]
s_waitcnt lgkmcnt(1)
v_lshl_add_u64 v[152:153],v[152:153],0,v[154:155]
v_mfma_f32_16x16x32_bf16 v[240:243],v[104:107],v[144:147],v[240:243]
ds_read_u16_d16_hi v130,v130
v_cvt_pk_bf16_f32 v154,v156,v158
v_cvt_pk_bf16_f32 v155,v157,v159
v_mfma_f32_16x16x32_bf16 v[248:251],v[108:111],v[144:147],v[248:251]
v_fma_mixhi_f16 v120,v142,s99,0
v_fma_mixhi_f16 v122,v143,s99,0
v_mfma_f32_16x16x32_bf16 v[212:215],v[88:91],v[152:155],v[212:215]
s_setprio 2
ds_read_u16 v129,v129
v_lshlrev_b32_sdwa v133,1,v133 dst_sel:DWORD dst_unused:UNUSED_PAD src0_sel:DWORD src1_sel:WORD_1
v_lshlrev_b32_sdwa v135,1,v135 dst_sel:DWORD dst_unused:UNUSED_PAD src0_sel:DWORD src1_sel:WORD_1
v_mfma_f32_16x16x32_bf16 v[220:223],v[92:95],v[152:155],v[220:223]
ds_read_b64_tr_b16 v[88:89],v3 offset:6160
v_pk_fma_f16 v121,v120,v120,s54
v_pk_fma_f16 v123,v122,v122,s54
v_mfma_f32_16x16x32_bf16 v[228:231],v[96:99],v[152:155],v[228:231]
ds_read_u16_d16_hi v131,v131
v_pk_mul_f16 v121,v120,v121
v_pk_mul_f16 v123,v122,v123
v_mfma_f32_16x16x32_bf16 v[236:239],v[100:103],v[152:155],v[236:239]
ds_read_b64_tr_b16 v[90:91],v3 offset:7696
v_lshlrev_b32_sdwa v136,1,v136 dst_sel:DWORD dst_unused:UNUSED_PAD src0_sel:DWORD src1_sel:WORD_1
v_lshlrev_b32_sdwa v138,1,v138 dst_sel:DWORD dst_unused:UNUSED_PAD src0_sel:DWORD src1_sel:WORD_1
v_mfma_f32_16x16x32_bf16 v[244:247],v[104:107],v[152:155],v[244:247]
ds_read_u16 v132,v132
v_exp_f16 v120,v121
v_mfma_f32_16x16x32_bf16 v[252:255],v[108:111],v[152:155],v[252:255]
ds_read_b64_tr_b16 v[92:93],v3 offset:6168
v_exp_f16 v122,v123
v_mfma_f32_16x16x32_bf16 v[144:147],v[64:67],v[40:43],v[112:115]
ds_read_u16_d16_hi v134,v134
v_lshlrev_b32_sdwa v137,1,v137 dst_sel:DWORD dst_unused:UNUSED_PAD src0_sel:DWORD src1_sel:WORD_1
v_lshlrev_b32_sdwa v139,1,v139 dst_sel:DWORD dst_unused:UNUSED_PAD src0_sel:DWORD src1_sel:WORD_1
v_mfma_f32_16x16x32_bf16 v[152:155],v[64:67],v[44:47],v[112:115]
ds_read_b64_tr_b16 v[94:95],v3 offset:7704
v_exp_f16_sdwa v120,v121 dst_sel:WORD_1 dst_unused:UNUSED_PRESERVE src0_sel:WORD_1
v_mfma_f32_16x16x32_bf16 v[156:159],v[68:71],v[44:47],v[116:119]
ds_read_u16 v133,v133
v_exp_f16_sdwa v122,v123 dst_sel:WORD_1 dst_unused:UNUSED_PRESERVE src0_sel:WORD_1
v_mfma_f32_16x16x32_bf16 v[148:151],v[68:71],v[40:43],v[116:119]
ds_read_b64_tr_b16 v[96:97],v3 offset:6224
v_pk_fma_f16 v120,v120,s55,s55
v_pk_fma_f16 v122,v122,s55,s55
v_mfma_f32_16x16x32_bf16 v[144:147],v[72:75],v[48:51],v[144:147]
ds_read_u16_d16_hi v135,v135
v_rcp_f16 v121,v120
v_mfma_f32_16x16x32_bf16 v[152:155],v[72:75],v[52:55],v[152:155]
ds_read_b64_tr_b16 v[98:99],v3 offset:7760
ds_read_b64_tr_b16 v[100:101],v3 offset:6232
v_rcp_f16 v123,v122
v_mfma_f32_16x16x32_bf16 v[156:159],v[76:79],v[52:55],v[156:159]
ds_read_u16 v136,v136
v_rcp_f16_sdwa v121,v120 dst_sel:WORD_1 dst_unused:UNUSED_PRESERVE src0_sel:WORD_1
v_mfma_f32_16x16x32_bf16 v[148:151],v[76:79],v[48:51],v[148:151]
ds_read_b64_tr_b16 v[102:103],v3 offset:7768
ds_read_b64_tr_b16 v[104:105],v3 offset:6288
v_rcp_f16_sdwa v123,v122 dst_sel:WORD_1 dst_unused:UNUSED_PRESERVE src0_sel:WORD_1
v_mfma_f32_16x16x32_bf16 v[144:147],v[80:83],v[56:59],v[144:147]
ds_read_u16_d16_hi v138,v138
v_fma_mix_f32 v140,v140,v121,0 op_sel_hi:[0,1,0]
v_fma_mix_f32 v142,v142,v121,0 op_sel:[0,1,0] op_sel_hi:[0,1,0]
v_mfma_f32_16x16x32_bf16 v[152:155],v[80:83],v[60:63],v[152:155]
s_waitcnt lgkmcnt(6)
ds_read_b64_tr_b16 v[106:107],v3 offset:7824
ds_read_b64_tr_b16 v[108:109],v3 offset:6296
v_lshl_add_u64 v[128:129],v[128:129],0,v[130:131]
v_lshl_add_u64 v[130:131],v[132:133],0,v[134:135]
v_mfma_f32_16x16x32_bf16 v[156:159],v[84:87],v[60:63],v[156:159]
ds_read_u16 v137,v137
v_fma_mix_f32 v141,v141,v123,0 op_sel_hi:[0,1,0]
v_fma_mix_f32 v143,v143,v123,0 op_sel:[0,1,0] op_sel_hi:[0,1,0]
v_mfma_f32_16x16x32_bf16 v[148:151],v[84:87],v[56:59],v[148:151]
ds_read_b64_tr_b16 v[110:111],v3 offset:7832
ds_read_b64_tr_b16 v[64:65],v1 offset:512
v_lshlrev_b32_sdwa v144,1,v144 dst_sel:DWORD dst_unused:UNUSED_PAD src0_sel:DWORD src1_sel:WORD_1
v_lshlrev_b32_sdwa v146,1,v146 dst_sel:DWORD dst_unused:UNUSED_PAD src0_sel:DWORD src1_sel:WORD_1
v_mfma_f32_16x16x32_bf16 v[160:163],v[88:91],v[128:131],v[160:163]
ds_read_u16_d16_hi v139,v139
v_lshlrev_b32_sdwa v145,1,v145 dst_sel:DWORD dst_unused:UNUSED_PAD src0_sel:DWORD src1_sel:WORD_1
v_lshlrev_b32_sdwa v147,1,v147 dst_sel:DWORD dst_unused:UNUSED_PAD src0_sel:DWORD src1_sel:WORD_1
v_mfma_f32_16x16x32_bf16 v[168:171],v[92:95],v[128:131],v[168:171]
s_waitcnt lgkmcnt(11)
ds_read_b64_tr_b16 v[66:67],v1 offset:2560
ds_read_b128 v[112:115],v4 offset:1408
v_fma_mixlo_f16 v120,v156,s99,0
v_fma_mixlo_f16 v122,v157,s99,0
v_mfma_f32_16x16x32_bf16 v[176:179],v[96:99],v[128:131],v[176:179]
s_waitcnt lgkmcnt(10)
ds_read_u16 v144,v144
v_lshlrev_b32_sdwa v148,1,v148 dst_sel:DWORD dst_unused:UNUSED_PAD src0_sel:DWORD src1_sel:WORD_1
v_lshlrev_b32_sdwa v150,1,v150 dst_sel:DWORD dst_unused:UNUSED_PAD src0_sel:DWORD src1_sel:WORD_1
v_mfma_f32_16x16x32_bf16 v[184:187],v[100:103],v[128:131],v[184:187]
s_waitcnt lgkmcnt(3)
ds_read_b64_tr_b16 v[68:69],v2 offset:512
ds_read_b64_tr_b16 v[70:71],v2 offset:2560
v_lshl_add_u64 v[136:137],v[136:137],0,v[138:139]
v_mfma_f32_16x16x32_bf16 v[192:195],v[104:107],v[128:131],v[192:195]
ds_read_u16_d16_hi v146,v146
v_cvt_pk_bf16_f32 v138,v140,v142
v_cvt_pk_bf16_f32 v139,v141,v143
v_mfma_f32_16x16x32_bf16 v[200:203],v[108:111],v[128:131],v[200:203]
ds_read_b128 v[116:119],v4 offset:1440
ds_read_b64_tr_b16 v[72:73],v1 offset:4608
v_fma_mixhi_f16 v120,v158,s99,0
v_fma_mixhi_f16 v122,v159,s99,0
v_mfma_f32_16x16x32_bf16 v[164:167],v[88:91],v[136:139],v[164:167]
ds_read_u16 v145,v145
v_lshlrev_b32_sdwa v149,1,v149 dst_sel:DWORD dst_unused:UNUSED_PAD src0_sel:DWORD src1_sel:WORD_1
v_lshlrev_b32_sdwa v151,1,v151 dst_sel:DWORD dst_unused:UNUSED_PAD src0_sel:DWORD src1_sel:WORD_1
v_mfma_f32_16x16x32_bf16 v[172:175],v[92:95],v[136:139],v[172:175]
s_setprio 1
ds_read_b64_tr_b16 v[74:75],v1 offset:6656
ds_read_b64_tr_b16 v[76:77],v2 offset:4608
v_pk_fma_f16 v121,v120,v120,s54
v_pk_fma_f16 v123,v122,v122,s54
v_mfma_f32_16x16x32_bf16 v[180:183],v[96:99],v[136:139],v[180:183]
ds_read_u16_d16_hi v147,v147
v_pk_mul_f16 v121,v120,v121
v_pk_mul_f16 v123,v122,v123
v_mfma_f32_16x16x32_bf16 v[188:191],v[100:103],v[136:139],v[188:191]
ds_read_b64_tr_b16 v[78:79],v2 offset:6656
v_lshlrev_b32_sdwa v152,1,v152 dst_sel:DWORD dst_unused:UNUSED_PAD src0_sel:DWORD src1_sel:WORD_1
v_lshlrev_b32_sdwa v154,1,v154 dst_sel:DWORD dst_unused:UNUSED_PAD src0_sel:DWORD src1_sel:WORD_1
v_mfma_f32_16x16x32_bf16 v[196:199],v[104:107],v[136:139],v[196:199]
ds_read_u16 v148,v148
v_exp_f16 v120,v121
v_mfma_f32_16x16x32_bf16 v[204:207],v[108:111],v[136:139],v[204:207]
s_waitcnt lgkmcnt(12)
ds_read_b64_tr_b16 v[80:81],v1 offset:8704
v_exp_f16 v122,v123
v_mfma_f32_16x16x32_bf16 v[128:131],v[64:67],v[16:19],v[112:115]
ds_read_u16_d16_hi v150,v150
v_lshlrev_b32_sdwa v153,1,v153 dst_sel:DWORD dst_unused:UNUSED_PAD src0_sel:DWORD src1_sel:WORD_1
v_lshlrev_b32_sdwa v155,1,v155 dst_sel:DWORD dst_unused:UNUSED_PAD src0_sel:DWORD src1_sel:WORD_1
v_mfma_f32_16x16x32_bf16 v[136:139],v[64:67],v[20:23],v[112:115]
s_waitcnt lgkmcnt(9)
ds_read_b64_tr_b16 v[82:83],v1 offset:10752
v_exp_f16_sdwa v120,v121 dst_sel:WORD_1 dst_unused:UNUSED_PRESERVE src0_sel:WORD_1
v_mfma_f32_16x16x32_bf16 v[140:143],v[68:71],v[20:23],v[116:119]
ds_read_u16 v149,v149
v_exp_f16_sdwa v122,v123 dst_sel:WORD_1 dst_unused:UNUSED_PRESERVE src0_sel:WORD_1
v_mfma_f32_16x16x32_bf16 v[132:135],v[68:71],v[16:19],v[116:119]
s_waitcnt lgkmcnt(8)
ds_read_b64_tr_b16 v[84:85],v2 offset:8704
v_pk_fma_f16 v120,v120,s55,s55
v_pk_fma_f16 v122,v122,s55,s55
v_mfma_f32_16x16x32_bf16 v[128:131],v[72:75],v[24:27],v[128:131]
ds_read_u16_d16_hi v151,v151
v_rcp_f16 v121,v120
v_mfma_f32_16x16x32_bf16 v[136:139],v[72:75],v[28:31],v[136:139]
s_waitcnt lgkmcnt(7)
ds_read_b64_tr_b16 v[86:87],v2 offset:10752
v_rcp_f16 v123,v122
v_mfma_f32_16x16x32_bf16 v[140:143],v[76:79],v[28:31],v[140:143]
ds_read_u16 v152,v152
v_rcp_f16_sdwa v121,v120 dst_sel:WORD_1 dst_unused:UNUSED_PRESERVE src0_sel:WORD_1
v_mfma_f32_16x16x32_bf16 v[132:135],v[76:79],v[24:27],v[132:135]
s_waitcnt lgkmcnt(5)
v_rcp_f16_sdwa v123,v122 dst_sel:WORD_1 dst_unused:UNUSED_PRESERVE src0_sel:WORD_1
v_mfma_f32_16x16x32_bf16 v[128:131],v[80:83],v[32:35],v[128:131]
ds_read_u16_d16_hi v154,v154
v_fma_mix_f32 v156,v156,v121,0 op_sel_hi:[0,1,0]
v_fma_mix_f32 v158,v158,v121,0 op_sel:[0,1,0] op_sel_hi:[0,1,0]
v_mfma_f32_16x16x32_bf16 v[136:139],v[80:83],v[36:39],v[136:139]
s_waitcnt lgkmcnt(2)
s_setprio 0
v_lshl_add_u64 v[144:145],v[144:145],0,v[146:147]
v_lshl_add_u64 v[146:147],v[148:149],0,v[150:151]
v_mfma_f32_16x16x32_bf16 v[140:143],v[84:87],v[36:39],v[140:143]
ds_read_u16 v153,v153
v_fma_mix_f32 v157,v157,v123,0 op_sel_hi:[0,1,0]
v_fma_mix_f32 v159,v159,v123,0 op_sel:[0,1,0] op_sel_hi:[0,1,0]
v_mfma_f32_16x16x32_bf16 v[132:135],v[84:87],v[32:35],v[132:135]
v_lshlrev_b32_sdwa v128,1,v128 dst_sel:DWORD dst_unused:UNUSED_PAD src0_sel:DWORD src1_sel:WORD_1
v_lshlrev_b32_sdwa v130,1,v130 dst_sel:DWORD dst_unused:UNUSED_PAD src0_sel:DWORD src1_sel:WORD_1
v_mfma_f32_16x16x32_bf16 v[208:211],v[88:91],v[144:147],v[208:211]
s_waitcnt vmcnt(0)
s_barrier
s_setprio 3
ds_read_u16_d16_hi v155,v155
v_lshlrev_b32_sdwa v129,1,v129 dst_sel:DWORD dst_unused:UNUSED_PAD src0_sel:DWORD src1_sel:WORD_1
v_lshlrev_b32_sdwa v131,1,v131 dst_sel:DWORD dst_unused:UNUSED_PAD src0_sel:DWORD src1_sel:WORD_1
v_mfma_f32_16x16x32_bf16 v[216:219],v[92:95],v[144:147],v[216:219]
v_fma_mixlo_f16 v120,v140,s99,0
v_fma_mixlo_f16 v122,v141,s99,0
v_mfma_f32_16x16x32_bf16 v[224:227],v[96:99],v[144:147],v[224:227]
s_cbranch_vccz RA7_%=
s_add_u32 m0,s58,0
s_mul_i32 s69,s59,1
s_add_u32 s69,s69,s97
buffer_load_dwordx4 v5,s[44:47],s69 offen lds
s_add_u32 m0,m0,1024
s_add_u32 s69,s69,s53
buffer_load_dwordx4 v6,s[44:47],s69 offen lds
RA7_%=:
ds_read_u16 v128,v128
v_lshlrev_b32_sdwa v132,1,v132 dst_sel:DWORD dst_unused:UNUSED_PAD src0_sel:DWORD src1_sel:WORD_1
v_lshlrev_b32_sdwa v134,1,v134 dst_sel:DWORD dst_unused:UNUSED_PAD src0_sel:DWORD src1_sel:WORD_1
v_mfma_f32_16x16x32_bf16 v[232:235],v[100:103],v[144:147],v[232:235]
s_waitcnt lgkmcnt(1)
v_lshl_add_u64 v[152:153],v[152:153],0,v[154:155]
v_mfma_f32_16x16x32_bf16 v[240:243],v[104:107],v[144:147],v[240:243]
ds_read_u16_d16_hi v130,v130
v_cvt_pk_bf16_f32 v154,v156,v158
v_cvt_pk_bf16_f32 v155,v157,v159
v_mfma_f32_16x16x32_bf16 v[248:251],v[108:111],v[144:147],v[248:251]
v_fma_mixhi_f16 v120,v142,s99,0
v_fma_mixhi_f16 v122,v143,s99,0
v_mfma_f32_16x16x32_bf16 v[212:215],v[88:91],v[152:155],v[212:215]
s_cbranch_vccz RA8_%=
s_add_u32 m0,m0,1024
s_add_u32 s69,s69,s53
buffer_load_dwordx4 v5,s[44:47],s69 offen lds
s_add_u32 m0,m0,s50
s_add_u32 s69,s69,s53
buffer_load_dwordx4 v6,s[44:47],s69 offen lds
RA8_%=:
ds_read_u16 v129,v129
v_lshlrev_b32_sdwa v133,1,v133 dst_sel:DWORD dst_unused:UNUSED_PAD src0_sel:DWORD src1_sel:WORD_1
v_lshlrev_b32_sdwa v135,1,v135 dst_sel:DWORD dst_unused:UNUSED_PAD src0_sel:DWORD src1_sel:WORD_1
v_mfma_f32_16x16x32_bf16 v[220:223],v[92:95],v[152:155],v[220:223]
ds_read_b64_tr_b16 v[88:89],v3 offset:12320
v_pk_fma_f16 v121,v120,v120,s54
v_pk_fma_f16 v123,v122,v122,s54
v_mfma_f32_16x16x32_bf16 v[228:231],v[96:99],v[152:155],v[228:231]
ds_read_u16_d16_hi v131,v131
v_pk_mul_f16 v121,v120,v121
v_pk_mul_f16 v123,v122,v123
v_mfma_f32_16x16x32_bf16 v[236:239],v[100:103],v[152:155],v[236:239]
s_cbranch_vccz RA9_%=
s_add_u32 m0,m0,1024
s_add_u32 s69,s69,s53
buffer_load_dwordx4 v5,s[44:47],s69 offen lds
s_add_u32 m0,m0,1024
s_add_u32 s69,s69,s53
buffer_load_dwordx4 v6,s[44:47],s69 offen lds
RA9_%=:
ds_read_b64_tr_b16 v[90:91],v3 offset:13856
v_lshlrev_b32_sdwa v136,1,v136 dst_sel:DWORD dst_unused:UNUSED_PAD src0_sel:DWORD src1_sel:WORD_1
v_lshlrev_b32_sdwa v138,1,v138 dst_sel:DWORD dst_unused:UNUSED_PAD src0_sel:DWORD src1_sel:WORD_1
v_mfma_f32_16x16x32_bf16 v[244:247],v[104:107],v[152:155],v[244:247]
ds_read_u16 v132,v132
v_exp_f16 v120,v121
v_mfma_f32_16x16x32_bf16 v[252:255],v[108:111],v[152:155],v[252:255]
ds_read_b64_tr_b16 v[92:93],v3 offset:12328
v_exp_f16 v122,v123
v_mfma_f32_16x16x32_bf16 v[144:147],v[64:67],v[40:43],v[112:115]
ds_read_u16_d16_hi v134,v134
v_lshlrev_b32_sdwa v137,1,v137 dst_sel:DWORD dst_unused:UNUSED_PAD src0_sel:DWORD src1_sel:WORD_1
v_lshlrev_b32_sdwa v139,1,v139 dst_sel:DWORD dst_unused:UNUSED_PAD src0_sel:DWORD src1_sel:WORD_1
v_mfma_f32_16x16x32_bf16 v[152:155],v[64:67],v[44:47],v[112:115]
s_cbranch_vccz RA10_%=
s_add_u32 m0,m0,s50
s_add_u32 s69,s69,s53
buffer_load_dwordx4 v5,s[44:47],s69 offen lds
s_add_u32 m0,m0,1024
s_add_u32 s69,s69,s53
buffer_load_dwordx4 v6,s[44:47],s69 offen lds
RA10_%=:
ds_read_b64_tr_b16 v[94:95],v3 offset:13864
v_exp_f16_sdwa v120,v121 dst_sel:WORD_1 dst_unused:UNUSED_PRESERVE src0_sel:WORD_1
v_mfma_f32_16x16x32_bf16 v[156:159],v[68:71],v[44:47],v[116:119]
ds_read_u16 v133,v133
v_exp_f16_sdwa v122,v123 dst_sel:WORD_1 dst_unused:UNUSED_PRESERVE src0_sel:WORD_1
v_mfma_f32_16x16x32_bf16 v[148:151],v[68:71],v[40:43],v[116:119]
ds_read_b64_tr_b16 v[96:97],v3 offset:12384
v_pk_fma_f16 v120,v120,s55,s55
v_pk_fma_f16 v122,v122,s55,s55
v_mfma_f32_16x16x32_bf16 v[144:147],v[72:75],v[48:51],v[144:147]
ds_read_u16_d16_hi v135,v135
v_rcp_f16 v121,v120
v_mfma_f32_16x16x32_bf16 v[152:155],v[72:75],v[52:55],v[152:155]
s_cbranch_vccz RA11_%=
s_add_u32 m0,m0,1024
s_add_u32 s69,s69,s53
buffer_load_dwordx4 v5,s[44:47],s69 offen lds
s_add_u32 m0,m0,s50
s_add_u32 s69,s69,s53
buffer_load_dwordx4 v6,s[44:47],s69 offen lds
RA11_%=:
ds_read_b64_tr_b16 v[98:99],v3 offset:13920
ds_read_b64_tr_b16 v[100:101],v3 offset:12392
v_rcp_f16 v123,v122
v_mfma_f32_16x16x32_bf16 v[156:159],v[76:79],v[52:55],v[156:159]
ds_read_u16 v136,v136
v_rcp_f16_sdwa v121,v120 dst_sel:WORD_1 dst_unused:UNUSED_PRESERVE src0_sel:WORD_1
v_mfma_f32_16x16x32_bf16 v[148:151],v[76:79],v[48:51],v[148:151]
ds_read_b64_tr_b16 v[102:103],v3 offset:13928
ds_read_b64_tr_b16 v[104:105],v3 offset:12448
v_rcp_f16_sdwa v123,v122 dst_sel:WORD_1 dst_unused:UNUSED_PRESERVE src0_sel:WORD_1
v_mfma_f32_16x16x32_bf16 v[144:147],v[80:83],v[56:59],v[144:147]
ds_read_u16_d16_hi v138,v138
v_fma_mix_f32 v140,v140,v121,0 op_sel_hi:[0,1,0]
v_fma_mix_f32 v142,v142,v121,0 op_sel:[0,1,0] op_sel_hi:[0,1,0]
v_mfma_f32_16x16x32_bf16 v[152:155],v[80:83],v[60:63],v[152:155]
s_waitcnt lgkmcnt(6)
s_cbranch_vccz RA12_%=
s_add_u32 m0,m0,1024
s_add_u32 s69,s69,s53
buffer_load_dwordx4 v5,s[44:47],s69 offen lds
s_add_u32 m0,m0,1024
s_add_u32 s69,s69,s53
buffer_load_dwordx4 v6,s[44:47],s69 offen lds
RA12_%=:
ds_read_b64_tr_b16 v[106:107],v3 offset:13984
ds_read_b64_tr_b16 v[108:109],v3 offset:12456
v_lshl_add_u64 v[128:129],v[128:129],0,v[130:131]
v_lshl_add_u64 v[130:131],v[132:133],0,v[134:135]
v_mfma_f32_16x16x32_bf16 v[156:159],v[84:87],v[60:63],v[156:159]
ds_read_u16 v137,v137
v_fma_mix_f32 v141,v141,v123,0 op_sel_hi:[0,1,0]
v_fma_mix_f32 v143,v143,v123,0 op_sel:[0,1,0] op_sel_hi:[0,1,0]
v_mfma_f32_16x16x32_bf16 v[148:151],v[84:87],v[56:59],v[148:151]
ds_read_b64_tr_b16 v[110:111],v3 offset:13992
ds_read_b64_tr_b16 v[64:65],v1 offset:12320
v_lshlrev_b32_sdwa v144,1,v144 dst_sel:DWORD dst_unused:UNUSED_PAD src0_sel:DWORD src1_sel:WORD_1
v_lshlrev_b32_sdwa v146,1,v146 dst_sel:DWORD dst_unused:UNUSED_PAD src0_sel:DWORD src1_sel:WORD_1
v_mfma_f32_16x16x32_bf16 v[160:163],v[88:91],v[128:131],v[160:163]
ds_read_u16_d16_hi v139,v139
v_lshlrev_b32_sdwa v145,1,v145 dst_sel:DWORD dst_unused:UNUSED_PAD src0_sel:DWORD src1_sel:WORD_1
v_lshlrev_b32_sdwa v147,1,v147 dst_sel:DWORD dst_unused:UNUSED_PAD src0_sel:DWORD src1_sel:WORD_1
v_mfma_f32_16x16x32_bf16 v[168:171],v[92:95],v[128:131],v[168:171]
s_waitcnt lgkmcnt(11)
ds_read_b64_tr_b16 v[66:67],v1 offset:14368
ds_read_b128 v[112:115],v4 offset:0
v_fma_mixlo_f16 v120,v156,s99,0
v_fma_mixlo_f16 v122,v157,s99,0
v_mfma_f32_16x16x32_bf16 v[176:179],v[96:99],v[128:131],v[176:179]
s_waitcnt lgkmcnt(10)
ds_read_u16 v144,v144
v_lshlrev_b32_sdwa v148,1,v148 dst_sel:DWORD dst_unused:UNUSED_PAD src0_sel:DWORD src1_sel:WORD_1
v_lshlrev_b32_sdwa v150,1,v150 dst_sel:DWORD dst_unused:UNUSED_PAD src0_sel:DWORD src1_sel:WORD_1
v_mfma_f32_16x16x32_bf16 v[184:187],v[100:103],v[128:131],v[184:187]
s_waitcnt lgkmcnt(3)
ds_read_b64_tr_b16 v[68:69],v2 offset:12320
ds_read_b64_tr_b16 v[70:71],v2 offset:14368
v_lshl_add_u64 v[136:137],v[136:137],0,v[138:139]
v_mfma_f32_16x16x32_bf16 v[192:195],v[104:107],v[128:131],v[192:195]
ds_read_u16_d16_hi v146,v146
v_cvt_pk_bf16_f32 v138,v140,v142
v_cvt_pk_bf16_f32 v139,v141,v143
v_mfma_f32_16x16x32_bf16 v[200:203],v[108:111],v[128:131],v[200:203]
ds_read_b128 v[116:119],v4 offset:32
ds_read_b64_tr_b16 v[72:73],v1 offset:16416
v_fma_mixhi_f16 v120,v158,s99,0
v_fma_mixhi_f16 v122,v159,s99,0
v_mfma_f32_16x16x32_bf16 v[164:167],v[88:91],v[136:139],v[164:167]
ds_read_u16 v145,v145
v_lshlrev_b32_sdwa v149,1,v149 dst_sel:DWORD dst_unused:UNUSED_PAD src0_sel:DWORD src1_sel:WORD_1
v_lshlrev_b32_sdwa v151,1,v151 dst_sel:DWORD dst_unused:UNUSED_PAD src0_sel:DWORD src1_sel:WORD_1
v_mfma_f32_16x16x32_bf16 v[172:175],v[92:95],v[136:139],v[172:175]
ds_read_b64_tr_b16 v[74:75],v1 offset:18464
ds_read_b64_tr_b16 v[76:77],v2 offset:16416
v_pk_fma_f16 v121,v120,v120,s54
v_pk_fma_f16 v123,v122,v122,s54
v_mfma_f32_16x16x32_bf16 v[180:183],v[96:99],v[136:139],v[180:183]
ds_read_u16_d16_hi v147,v147
v_pk_mul_f16 v121,v120,v121
v_pk_mul_f16 v123,v122,v123
v_mfma_f32_16x16x32_bf16 v[188:191],v[100:103],v[136:139],v[188:191]
ds_read_b64_tr_b16 v[78:79],v2 offset:18464
v_lshlrev_b32_sdwa v152,1,v152 dst_sel:DWORD dst_unused:UNUSED_PAD src0_sel:DWORD src1_sel:WORD_1
v_lshlrev_b32_sdwa v154,1,v154 dst_sel:DWORD dst_unused:UNUSED_PAD src0_sel:DWORD src1_sel:WORD_1
v_mfma_f32_16x16x32_bf16 v[196:199],v[104:107],v[136:139],v[196:199]
ds_read_u16 v148,v148
v_exp_f16 v120,v121
v_mfma_f32_16x16x32_bf16 v[204:207],v[108:111],v[136:139],v[204:207]
s_waitcnt lgkmcnt(12)
ds_read_b64_tr_b16 v[80:81],v1 offset:20512
v_exp_f16 v122,v123
v_mfma_f32_16x16x32_bf16 v[128:131],v[64:67],v[16:19],v[112:115]
ds_read_u16_d16_hi v150,v150
v_lshlrev_b32_sdwa v153,1,v153 dst_sel:DWORD dst_unused:UNUSED_PAD src0_sel:DWORD src1_sel:WORD_1
v_lshlrev_b32_sdwa v155,1,v155 dst_sel:DWORD dst_unused:UNUSED_PAD src0_sel:DWORD src1_sel:WORD_1
v_mfma_f32_16x16x32_bf16 v[136:139],v[64:67],v[20:23],v[112:115]
s_waitcnt lgkmcnt(9)
ds_read_b64_tr_b16 v[82:83],v1 offset:22560
v_exp_f16_sdwa v120,v121 dst_sel:WORD_1 dst_unused:UNUSED_PRESERVE src0_sel:WORD_1
v_mfma_f32_16x16x32_bf16 v[140:143],v[68:71],v[20:23],v[116:119]
ds_read_u16 v149,v149
v_exp_f16_sdwa v122,v123 dst_sel:WORD_1 dst_unused:UNUSED_PRESERVE src0_sel:WORD_1
v_mfma_f32_16x16x32_bf16 v[132:135],v[68:71],v[16:19],v[116:119]
s_waitcnt lgkmcnt(8)
ds_read_b64_tr_b16 v[84:85],v2 offset:20512
v_pk_fma_f16 v120,v120,s55,s55
v_pk_fma_f16 v122,v122,s55,s55
v_mfma_f32_16x16x32_bf16 v[128:131],v[72:75],v[24:27],v[128:131]
ds_read_u16_d16_hi v151,v151
v_rcp_f16 v121,v120
v_mfma_f32_16x16x32_bf16 v[136:139],v[72:75],v[28:31],v[136:139]
s_waitcnt lgkmcnt(7)
ds_read_b64_tr_b16 v[86:87],v2 offset:22560
v_rcp_f16 v123,v122
v_mfma_f32_16x16x32_bf16 v[140:143],v[76:79],v[28:31],v[140:143]
ds_read_u16 v152,v152
v_rcp_f16_sdwa v121,v120 dst_sel:WORD_1 dst_unused:UNUSED_PRESERVE src0_sel:WORD_1
v_mfma_f32_16x16x32_bf16 v[132:135],v[76:79],v[24:27],v[132:135]
s_waitcnt lgkmcnt(5)
v_rcp_f16_sdwa v123,v122 dst_sel:WORD_1 dst_unused:UNUSED_PRESERVE src0_sel:WORD_1
v_mfma_f32_16x16x32_bf16 v[128:131],v[80:83],v[32:35],v[128:131]
ds_read_u16_d16_hi v154,v154
v_fma_mix_f32 v156,v156,v121,0 op_sel_hi:[0,1,0]
v_fma_mix_f32 v158,v158,v121,0 op_sel:[0,1,0] op_sel_hi:[0,1,0]
v_mfma_f32_16x16x32_bf16 v[136:139],v[80:83],v[36:39],v[136:139]
s_waitcnt lgkmcnt(2)
v_lshl_add_u64 v[144:145],v[144:145],0,v[146:147]
v_lshl_add_u64 v[146:147],v[148:149],0,v[150:151]
v_mfma_f32_16x16x32_bf16 v[140:143],v[84:87],v[36:39],v[140:143]
ds_read_u16 v153,v153
v_fma_mix_f32 v157,v157,v123,0 op_sel_hi:[0,1,0]
v_fma_mix_f32 v159,v159,v123,0 op_sel:[0,1,0] op_sel_hi:[0,1,0]
v_mfma_f32_16x16x32_bf16 v[132:135],v[84:87],v[32:35],v[132:135]
v_lshlrev_b32_sdwa v128,1,v128 dst_sel:DWORD dst_unused:UNUSED_PAD src0_sel:DWORD src1_sel:WORD_1
v_lshlrev_b32_sdwa v130,1,v130 dst_sel:DWORD dst_unused:UNUSED_PAD src0_sel:DWORD src1_sel:WORD_1
v_mfma_f32_16x16x32_bf16 v[208:211],v[88:91],v[144:147],v[208:211]
ds_read_u16_d16_hi v155,v155
v_lshlrev_b32_sdwa v129,1,v129 dst_sel:DWORD dst_unused:UNUSED_PAD src0_sel:DWORD src1_sel:WORD_1
v_lshlrev_b32_sdwa v131,1,v131 dst_sel:DWORD dst_unused:UNUSED_PAD src0_sel:DWORD src1_sel:WORD_1
v_mfma_f32_16x16x32_bf16 v[216:219],v[92:95],v[144:147],v[216:219]
v_fma_mixlo_f16 v120,v140,s99,0
v_fma_mixlo_f16 v122,v141,s99,0
v_mfma_f32_16x16x32_bf16 v[224:227],v[96:99],v[144:147],v[224:227]
s_setprio 2
ds_read_u16 v128,v128
v_lshlrev_b32_sdwa v132,1,v132 dst_sel:DWORD dst_unused:UNUSED_PAD src0_sel:DWORD src1_sel:WORD_1
v_lshlrev_b32_sdwa v134,1,v134 dst_sel:DWORD dst_unused:UNUSED_PAD src0_sel:DWORD src1_sel:WORD_1
v_mfma_f32_16x16x32_bf16 v[232:235],v[100:103],v[144:147],v[232:235]
s_waitcnt lgkmcnt(1)
v_lshl_add_u64 v[152:153],v[152:153],0,v[154:155]
v_mfma_f32_16x16x32_bf16 v[240:243],v[104:107],v[144:147],v[240:243]
ds_read_u16_d16_hi v130,v130
v_cvt_pk_bf16_f32 v154,v156,v158
v_cvt_pk_bf16_f32 v155,v157,v159
v_mfma_f32_16x16x32_bf16 v[248:251],v[108:111],v[144:147],v[248:251]
v_fma_mixhi_f16 v120,v142,s99,0
v_fma_mixhi_f16 v122,v143,s99,0
v_mfma_f32_16x16x32_bf16 v[212:215],v[88:91],v[152:155],v[212:215]
ds_read_u16 v129,v129
v_lshlrev_b32_sdwa v133,1,v133 dst_sel:DWORD dst_unused:UNUSED_PAD src0_sel:DWORD src1_sel:WORD_1
v_lshlrev_b32_sdwa v135,1,v135 dst_sel:DWORD dst_unused:UNUSED_PAD src0_sel:DWORD src1_sel:WORD_1
v_mfma_f32_16x16x32_bf16 v[220:223],v[92:95],v[152:155],v[220:223]
ds_read_b64_tr_b16 v[88:89],v3 offset:18480
v_pk_fma_f16 v121,v120,v120,s54
v_pk_fma_f16 v123,v122,v122,s54
v_mfma_f32_16x16x32_bf16 v[228:231],v[96:99],v[152:155],v[228:231]
ds_read_u16_d16_hi v131,v131
v_pk_mul_f16 v121,v120,v121
v_pk_mul_f16 v123,v122,v123
v_mfma_f32_16x16x32_bf16 v[236:239],v[100:103],v[152:155],v[236:239]
ds_read_b64_tr_b16 v[90:91],v3 offset:20016
v_lshlrev_b32_sdwa v136,1,v136 dst_sel:DWORD dst_unused:UNUSED_PAD src0_sel:DWORD src1_sel:WORD_1
v_lshlrev_b32_sdwa v138,1,v138 dst_sel:DWORD dst_unused:UNUSED_PAD src0_sel:DWORD src1_sel:WORD_1
v_mfma_f32_16x16x32_bf16 v[244:247],v[104:107],v[152:155],v[244:247]
ds_read_u16 v132,v132
v_exp_f16 v120,v121
v_mfma_f32_16x16x32_bf16 v[252:255],v[108:111],v[152:155],v[252:255]
ds_read_b64_tr_b16 v[92:93],v3 offset:18488
v_exp_f16 v122,v123
v_mfma_f32_16x16x32_bf16 v[144:147],v[64:67],v[40:43],v[112:115]
ds_read_u16_d16_hi v134,v134
v_lshlrev_b32_sdwa v137,1,v137 dst_sel:DWORD dst_unused:UNUSED_PAD src0_sel:DWORD src1_sel:WORD_1
v_lshlrev_b32_sdwa v139,1,v139 dst_sel:DWORD dst_unused:UNUSED_PAD src0_sel:DWORD src1_sel:WORD_1
v_mfma_f32_16x16x32_bf16 v[152:155],v[64:67],v[44:47],v[112:115]
ds_read_b64_tr_b16 v[94:95],v3 offset:20024
v_exp_f16_sdwa v120,v121 dst_sel:WORD_1 dst_unused:UNUSED_PRESERVE src0_sel:WORD_1
v_mfma_f32_16x16x32_bf16 v[156:159],v[68:71],v[44:47],v[116:119]
ds_read_u16 v133,v133
v_exp_f16_sdwa v122,v123 dst_sel:WORD_1 dst_unused:UNUSED_PRESERVE src0_sel:WORD_1
v_mfma_f32_16x16x32_bf16 v[148:151],v[68:71],v[40:43],v[116:119]
ds_read_b64_tr_b16 v[96:97],v3 offset:18544
v_pk_fma_f16 v120,v120,s55,s55
v_pk_fma_f16 v122,v122,s55,s55
v_mfma_f32_16x16x32_bf16 v[144:147],v[72:75],v[48:51],v[144:147]
ds_read_u16_d16_hi v135,v135
v_rcp_f16 v121,v120
v_mfma_f32_16x16x32_bf16 v[152:155],v[72:75],v[52:55],v[152:155]
ds_read_b64_tr_b16 v[98:99],v3 offset:20080
ds_read_b64_tr_b16 v[100:101],v3 offset:18552
v_rcp_f16 v123,v122
v_mfma_f32_16x16x32_bf16 v[156:159],v[76:79],v[52:55],v[156:159]
ds_read_u16 v136,v136
v_rcp_f16_sdwa v121,v120 dst_sel:WORD_1 dst_unused:UNUSED_PRESERVE src0_sel:WORD_1
v_mfma_f32_16x16x32_bf16 v[148:151],v[76:79],v[48:51],v[148:151]
ds_read_b64_tr_b16 v[102:103],v3 offset:20088
ds_read_b64_tr_b16 v[104:105],v3 offset:18608
v_rcp_f16_sdwa v123,v122 dst_sel:WORD_1 dst_unused:UNUSED_PRESERVE src0_sel:WORD_1
v_mfma_f32_16x16x32_bf16 v[144:147],v[80:83],v[56:59],v[144:147]
ds_read_u16_d16_hi v138,v138
v_fma_mix_f32 v140,v140,v121,0 op_sel_hi:[0,1,0]
v_fma_mix_f32 v142,v142,v121,0 op_sel:[0,1,0] op_sel_hi:[0,1,0]
v_mfma_f32_16x16x32_bf16 v[152:155],v[80:83],v[60:63],v[152:155]
s_waitcnt lgkmcnt(6)
ds_read_b64_tr_b16 v[106:107],v3 offset:20144
ds_read_b64_tr_b16 v[108:109],v3 offset:18616
v_lshl_add_u64 v[128:129],v[128:129],0,v[130:131]
v_lshl_add_u64 v[130:131],v[132:133],0,v[134:135]
v_mfma_f32_16x16x32_bf16 v[156:159],v[84:87],v[60:63],v[156:159]
ds_read_u16 v137,v137
v_fma_mix_f32 v141,v141,v123,0 op_sel_hi:[0,1,0]
v_fma_mix_f32 v143,v143,v123,0 op_sel:[0,1,0] op_sel_hi:[0,1,0]
v_mfma_f32_16x16x32_bf16 v[148:151],v[84:87],v[56:59],v[148:151]
ds_read_b64_tr_b16 v[110:111],v3 offset:20152
ds_read_b128 v[64:67],v4 offset:1536
v_lshlrev_b32_sdwa v144,1,v144 dst_sel:DWORD dst_unused:UNUSED_PAD src0_sel:DWORD src1_sel:WORD_1
v_lshlrev_b32_sdwa v146,1,v146 dst_sel:DWORD dst_unused:UNUSED_PAD src0_sel:DWORD src1_sel:WORD_1
v_mfma_f32_16x16x32_bf16 v[160:163],v[88:91],v[128:131],v[160:163]
ds_read_u16_d16_hi v139,v139
ds_read_b128 v[68:71],v4 offset:1568
v_lshlrev_b32_sdwa v145,1,v145 dst_sel:DWORD dst_unused:UNUSED_PAD src0_sel:DWORD src1_sel:WORD_1
v_lshlrev_b32_sdwa v147,1,v147 dst_sel:DWORD dst_unused:UNUSED_PAD src0_sel:DWORD src1_sel:WORD_1
v_fma_mixlo_f16 v120,v156,s99,0
v_fma_mixlo_f16 v122,v157,s99,0
v_mfma_f32_16x16x32_bf16 v[168:171],v[92:95],v[128:131],v[168:171]
s_waitcnt lgkmcnt(12)
ds_read_u16 v144,v144
ds_read_b128 v[72:75],v4 offset:1664
v_lshlrev_b32_sdwa v148,1,v148 dst_sel:DWORD dst_unused:UNUSED_PAD src0_sel:DWORD src1_sel:WORD_1
v_lshlrev_b32_sdwa v150,1,v150 dst_sel:DWORD dst_unused:UNUSED_PAD src0_sel:DWORD src1_sel:WORD_1
s_waitcnt lgkmcnt(3)
v_lshl_add_u64 v[136:137],v[136:137],0,v[138:139]
v_cvt_pk_bf16_f32 v138,v140,v142
v_mfma_f32_16x16x32_bf16 v[176:179],v[96:99],v[128:131],v[176:179]
s_setprio 1
ds_read_u16_d16_hi v146,v146
ds_read_b128 v[76:79],v4 offset:1696
v_cvt_pk_bf16_f32 v139,v141,v143
v_fma_mixhi_f16 v120,v158,s99,0
v_fma_mixhi_f16 v122,v159,s99,0
v_lshlrev_b32_sdwa v149,1,v149 dst_sel:DWORD dst_unused:UNUSED_PAD src0_sel:DWORD src1_sel:WORD_1
v_mfma_f32_16x16x32_bf16 v[184:187],v[100:103],v[128:131],v[184:187]
ds_read_u16 v145,v145
ds_read_b128 v[80:83],v4 offset:1792
v_lshlrev_b32_sdwa v151,1,v151 dst_sel:DWORD dst_unused:UNUSED_PAD src0_sel:DWORD src1_sel:WORD_1
v_pk_fma_f16 v121,v120,v120,s54
v_pk_fma_f16 v123,v122,v122,s54
v_pk_mul_f16 v121,v120,v121
v_mfma_f32_16x16x32_bf16 v[192:195],v[104:107],v[128:131],v[192:195]
ds_read_u16_d16_hi v147,v147
ds_read_b128 v[84:87],v4 offset:1824
v_pk_mul_f16 v123,v122,v123
v_lshlrev_b32_sdwa v152,1,v152 dst_sel:DWORD dst_unused:UNUSED_PAD src0_sel:DWORD src1_sel:WORD_1
v_lshlrev_b32_sdwa v154,1,v154 dst_sel:DWORD dst_unused:UNUSED_PAD src0_sel:DWORD src1_sel:WORD_1
v_exp_f16 v120,v121
v_mfma_f32_16x16x32_bf16 v[200:203],v[108:111],v[128:131],v[200:203]
ds_read_u16 v148,v148
v_exp_f16 v122,v123
v_lshlrev_b32_sdwa v153,1,v153 dst_sel:DWORD dst_unused:UNUSED_PAD src0_sel:DWORD src1_sel:WORD_1
v_mfma_f32_16x16x32_bf16 v[164:167],v[88:91],v[136:139],v[164:167]
ds_read_u16_d16_hi v150,v150
v_lshlrev_b32_sdwa v155,1,v155 dst_sel:DWORD dst_unused:UNUSED_PAD src0_sel:DWORD src1_sel:WORD_1
v_exp_f16_sdwa v120,v121 dst_sel:WORD_1 dst_unused:UNUSED_PRESERVE src0_sel:WORD_1
v_exp_f16_sdwa v122,v123 dst_sel:WORD_1 dst_unused:UNUSED_PRESERVE src0_sel:WORD_1
v_mfma_f32_16x16x32_bf16 v[172:175],v[92:95],v[136:139],v[172:175]
ds_read_u16 v149,v149
v_pk_fma_f16 v120,v120,s55,s55
v_pk_fma_f16 v122,v122,s55,s55
v_rcp_f16 v121,v120
v_mfma_f32_16x16x32_bf16 v[180:183],v[96:99],v[136:139],v[180:183]
ds_read_u16_d16_hi v151,v151
v_rcp_f16 v123,v122
v_rcp_f16_sdwa v121,v120 dst_sel:WORD_1 dst_unused:UNUSED_PRESERVE src0_sel:WORD_1
v_mfma_f32_16x16x32_bf16 v[188:191],v[100:103],v[136:139],v[188:191]
ds_read_u16 v152,v152
v_rcp_f16_sdwa v123,v122 dst_sel:WORD_1 dst_unused:UNUSED_PRESERVE src0_sel:WORD_1
v_fma_mix_f32 v156,v156,v121,0 op_sel_hi:[0,1,0]
v_mfma_f32_16x16x32_bf16 v[196:199],v[104:107],v[136:139],v[196:199]
s_setprio 0
ds_read_u16_d16_hi v154,v154
v_fma_mix_f32 v158,v158,v121,0 op_sel:[0,1,0] op_sel_hi:[0,1,0]
s_waitcnt lgkmcnt(7)
v_lshl_add_u64 v[144:145],v[144:145],0,v[146:147]
s_waitcnt lgkmcnt(2)
v_lshl_add_u64 v[146:147],v[148:149],0,v[150:151]
v_fma_mix_f32 v157,v157,v123,0 op_sel_hi:[0,1,0]
v_mfma_f32_16x16x32_bf16 v[204:207],v[108:111],v[136:139],v[204:207]
ds_read_u16 v153,v153
v_fma_mix_f32 v159,v159,v123,0 op_sel:[0,1,0] op_sel_hi:[0,1,0]
ds_read_u16_d16_hi v155,v155
s_waitcnt lgkmcnt(0)
v_lshl_add_u64 v[152:153],v[152:153],0,v[154:155]
v_cvt_pk_bf16_f32 v154,v156,v158
v_cvt_pk_bf16_f32 v155,v157,v159
v_mfma_f32_16x16x32_bf16 v[208:211],v[88:91],v[144:147],v[208:211]
s_waitcnt vmcnt(0)
s_barrier
s_setprio 0
v_xor_b32 v9,64,v10
v_mul_f32 v160,v160,v64
v_mul_f32 v161,v161,v65
v_mul_f32 v162,v162,v66
v_mul_f32 v163,v163,v67
v_mfma_f32_16x16x32_bf16 v[216:219],v[92:95],v[144:147],v[216:219]
v_mul_f32 v168,v168,v68
v_mul_f32 v169,v169,v69
v_mul_f32 v170,v170,v70
v_mul_f32 v171,v171,v71
v_cvt_pk_bf16_f32 v16,v160,v161
v_mfma_f32_16x16x32_bf16 v[224:227],v[96:99],v[144:147],v[224:227]
v_cvt_pk_bf16_f32 v17,v162,v163
v_cvt_pk_bf16_f32 v18,v168,v169
v_cvt_pk_bf16_f32 v19,v170,v171
v_pk_maximum3_f16 v7,v7,v16,v17
v_mfma_f32_16x16x32_bf16 v[232:235],v[100:103],v[144:147],v[232:235]
v_pk_maximum3_f16 v7,v7,v18,v19
v_mul_f32_dpp v176,v176,v72 quad_perm:[1,0,3,2] row_mask:0xf bank_mask:0xf
v_mul_f32_dpp v177,v177,v73 quad_perm:[1,0,3,2] row_mask:0xf bank_mask:0xf
v_mul_f32_dpp v178,v178,v74 quad_perm:[1,0,3,2] row_mask:0xf bank_mask:0xf
v_mul_f32_dpp v179,v179,v75 quad_perm:[1,0,3,2] row_mask:0xf bank_mask:0xf
v_mfma_f32_16x16x32_bf16 v[240:243],v[104:107],v[144:147],v[240:243]
v_mul_f32_dpp v184,v184,v76 quad_perm:[1,0,3,2] row_mask:0xf bank_mask:0xf
v_mul_f32_dpp v185,v185,v77 quad_perm:[1,0,3,2] row_mask:0xf bank_mask:0xf
v_mul_f32_dpp v186,v186,v78 quad_perm:[1,0,3,2] row_mask:0xf bank_mask:0xf
v_mul_f32_dpp v187,v187,v79 quad_perm:[1,0,3,2] row_mask:0xf bank_mask:0xf
v_mfma_f32_16x16x32_bf16 v[248:251],v[108:111],v[144:147],v[248:251]
v_cvt_pk_bf16_f32 v24,v176,v177
v_cvt_pk_bf16_f32 v25,v178,v179
v_cvt_pk_bf16_f32 v26,v184,v185
v_cvt_pk_bf16_f32 v27,v186,v187
v_pk_maximum3_f16 v7,v7,v24,v25
v_mfma_f32_16x16x32_bf16 v[212:215],v[88:91],v[152:155],v[212:215]
v_pk_maximum3_f16 v7,v7,v26,v27
s_mov_b64 exec,s[12:13]
v_swap_b32 v16,v24
v_swap_b32 v17,v25
v_swap_b32 v18,v26
v_swap_b32 v19,v27
s_mov_b64 exec,-1
v_mfma_f32_16x16x32_bf16 v[220:223],v[92:95],v[152:155],v[220:223]
s_add_u32 s65,s61,0
buffer_store_dwordx4 v[16:19],v10,s[40:43],s65 offen offset:0 sc1
v_mfma_f32_16x16x32_bf16 v[228:231],v[96:99],v[152:155],v[228:231]
v_mul_f32 v192,v192,v80
v_mul_f32 v193,v193,v81
v_mul_f32 v194,v194,v82
v_mul_f32 v195,v195,v83
v_mfma_f32_16x16x32_bf16 v[236:239],v[100:103],v[152:155],v[236:239]
v_mul_f32 v200,v200,v84
v_mul_f32 v201,v201,v85
v_mul_f32 v202,v202,v86
v_mul_f32 v203,v203,v87
v_cvt_pk_bf16_f32 v32,v192,v193
v_mfma_f32_16x16x32_bf16 v[244:247],v[104:107],v[152:155],v[244:247]
v_cvt_pk_bf16_f32 v33,v194,v195
v_cvt_pk_bf16_f32 v34,v200,v201
v_cvt_pk_bf16_f32 v35,v202,v203
v_pk_maximum3_f16 v7,v7,v32,v33
v_mfma_f32_16x16x32_bf16 v[252:255],v[108:111],v[152:155],v[252:255]
v_pk_maximum3_f16 v7,v7,v34,v35
v_mul_f32_dpp v164,v164,v64 quad_perm:[1,0,3,2] row_mask:0xf bank_mask:0xf
s_add_u32 s65,s61,0
buffer_store_dwordx4 v[24:27],v9,s[40:43],s65 offen offset:384 sc1
v_mul_f32_dpp v165,v165,v65 quad_perm:[1,0,3,2] row_mask:0xf bank_mask:0xf
v_mul_f32_dpp v166,v166,v66 quad_perm:[1,0,3,2] row_mask:0xf bank_mask:0xf
v_mul_f32_dpp v167,v167,v67 quad_perm:[1,0,3,2] row_mask:0xf bank_mask:0xf
v_mul_f32_dpp v172,v172,v68 quad_perm:[1,0,3,2] row_mask:0xf bank_mask:0xf
v_mul_f32_dpp v173,v173,v69 quad_perm:[1,0,3,2] row_mask:0xf bank_mask:0xf
v_mul_f32_dpp v174,v174,v70 quad_perm:[1,0,3,2] row_mask:0xf bank_mask:0xf
v_mul_f32_dpp v175,v175,v71 quad_perm:[1,0,3,2] row_mask:0xf bank_mask:0xf
v_cvt_pk_bf16_f32 v20,v164,v165
v_cvt_pk_bf16_f32 v21,v166,v167
v_cvt_pk_bf16_f32 v22,v172,v173
v_cvt_pk_bf16_f32 v23,v174,v175
v_pk_maximum3_f16 v7,v7,v20,v21
v_pk_maximum3_f16 v7,v7,v22,v23
s_mov_b64 exec,s[12:13]
v_swap_b32 v32,v20
v_swap_b32 v33,v21
v_swap_b32 v34,v22
v_swap_b32 v35,v23
s_mov_b64 exec,-1
v_mul_f32 v180,v180,v72
v_mul_f32 v181,v181,v73
v_mul_f32 v182,v182,v74
v_mul_f32 v183,v183,v75
s_add_u32 s65,s61,0
buffer_store_dwordx4 v[32:35],v10,s[40:43],s65 offen offset:128 sc1
v_mul_f32 v188,v188,v76
v_mul_f32 v189,v189,v77
v_mul_f32 v190,v190,v78
v_mul_f32 v191,v191,v79
v_cvt_pk_bf16_f32 v28,v180,v181
v_cvt_pk_bf16_f32 v29,v182,v183
v_cvt_pk_bf16_f32 v30,v188,v189
v_cvt_pk_bf16_f32 v31,v190,v191
v_pk_maximum3_f16 v7,v7,v28,v29
v_pk_maximum3_f16 v7,v7,v30,v31
v_mul_f32_dpp v196,v196,v80 quad_perm:[1,0,3,2] row_mask:0xf bank_mask:0xf
v_mul_f32_dpp v197,v197,v81 quad_perm:[1,0,3,2] row_mask:0xf bank_mask:0xf
v_mul_f32_dpp v198,v198,v82 quad_perm:[1,0,3,2] row_mask:0xf bank_mask:0xf
v_mul_f32_dpp v199,v199,v83 quad_perm:[1,0,3,2] row_mask:0xf bank_mask:0xf
v_mul_f32_dpp v204,v204,v84 quad_perm:[1,0,3,2] row_mask:0xf bank_mask:0xf
v_mul_f32_dpp v205,v205,v85 quad_perm:[1,0,3,2] row_mask:0xf bank_mask:0xf
v_mul_f32_dpp v206,v206,v86 quad_perm:[1,0,3,2] row_mask:0xf bank_mask:0xf
v_mul_f32_dpp v207,v207,v87 quad_perm:[1,0,3,2] row_mask:0xf bank_mask:0xf
v_cvt_pk_bf16_f32 v36,v196,v197
v_cvt_pk_bf16_f32 v37,v198,v199
v_cvt_pk_bf16_f32 v38,v204,v205
v_cvt_pk_bf16_f32 v39,v206,v207
v_pk_maximum3_f16 v7,v7,v36,v37
v_pk_maximum3_f16 v7,v7,v38,v39
s_mov_b64 exec,s[12:13]
v_swap_b32 v28,v36
v_swap_b32 v29,v37
v_swap_b32 v30,v38
v_swap_b32 v31,v39
s_mov_b64 exec,-1
s_add_u32 s65,s61,0
buffer_store_dwordx4 v[20:23],v9,s[40:43],s65 offen offset:512 sc1
v_mul_f32 v208,v208,v64
v_mul_f32 v209,v209,v65
v_mul_f32 v210,v210,v66
v_mul_f32 v211,v211,v67
v_mul_f32 v216,v216,v68
v_mul_f32 v217,v217,v69
v_mul_f32 v218,v218,v70
v_mul_f32 v219,v219,v71
v_cvt_pk_bf16_f32 v40,v208,v209
v_cvt_pk_bf16_f32 v41,v210,v211
v_cvt_pk_bf16_f32 v42,v216,v217
v_cvt_pk_bf16_f32 v43,v218,v219
v_pk_maximum3_f16 v7,v7,v40,v41
v_pk_maximum3_f16 v7,v7,v42,v43
v_mul_f32_dpp v224,v224,v72 quad_perm:[1,0,3,2] row_mask:0xf bank_mask:0xf
v_mul_f32_dpp v225,v225,v73 quad_perm:[1,0,3,2] row_mask:0xf bank_mask:0xf
v_mul_f32_dpp v226,v226,v74 quad_perm:[1,0,3,2] row_mask:0xf bank_mask:0xf
v_mul_f32_dpp v227,v227,v75 quad_perm:[1,0,3,2] row_mask:0xf bank_mask:0xf
s_add_u32 s65,s61,0
buffer_store_dwordx4 v[28:31],v10,s[40:43],s65 offen offset:256 sc1
v_mul_f32_dpp v232,v232,v76 quad_perm:[1,0,3,2] row_mask:0xf bank_mask:0xf
v_mul_f32_dpp v233,v233,v77 quad_perm:[1,0,3,2] row_mask:0xf bank_mask:0xf
v_mul_f32_dpp v234,v234,v78 quad_perm:[1,0,3,2] row_mask:0xf bank_mask:0xf
v_mul_f32_dpp v235,v235,v79 quad_perm:[1,0,3,2] row_mask:0xf bank_mask:0xf
v_cvt_pk_bf16_f32 v48,v224,v225
v_cvt_pk_bf16_f32 v49,v226,v227
v_cvt_pk_bf16_f32 v50,v232,v233
v_cvt_pk_bf16_f32 v51,v234,v235
v_pk_maximum3_f16 v7,v7,v48,v49
v_pk_maximum3_f16 v7,v7,v50,v51
s_mov_b64 exec,s[12:13]
v_swap_b32 v40,v48
v_swap_b32 v41,v49
v_swap_b32 v42,v50
v_swap_b32 v43,v51
s_mov_b64 exec,-1
v_mul_f32 v240,v240,v80
v_mul_f32 v241,v241,v81
v_mul_f32 v242,v242,v82
v_mul_f32 v243,v243,v83
v_mul_f32 v248,v248,v84
v_mul_f32 v249,v249,v85
v_mul_f32 v250,v250,v86
s_add_u32 s65,s61,0
buffer_store_dwordx4 v[36:39],v9,s[40:43],s65 offen offset:640 sc1
v_mul_f32 v251,v251,v87
v_cvt_pk_bf16_f32 v56,v240,v241
v_cvt_pk_bf16_f32 v57,v242,v243
v_cvt_pk_bf16_f32 v58,v248,v249
v_cvt_pk_bf16_f32 v59,v250,v251
v_pk_maximum3_f16 v7,v7,v56,v57
v_pk_maximum3_f16 v7,v7,v58,v59
v_mul_f32_dpp v212,v212,v64 quad_perm:[1,0,3,2] row_mask:0xf bank_mask:0xf
v_mul_f32_dpp v213,v213,v65 quad_perm:[1,0,3,2] row_mask:0xf bank_mask:0xf
v_mul_f32_dpp v214,v214,v66 quad_perm:[1,0,3,2] row_mask:0xf bank_mask:0xf
v_mul_f32_dpp v215,v215,v67 quad_perm:[1,0,3,2] row_mask:0xf bank_mask:0xf
v_mul_f32_dpp v220,v220,v68 quad_perm:[1,0,3,2] row_mask:0xf bank_mask:0xf
v_mul_f32_dpp v221,v221,v69 quad_perm:[1,0,3,2] row_mask:0xf bank_mask:0xf
v_mul_f32_dpp v222,v222,v70 quad_perm:[1,0,3,2] row_mask:0xf bank_mask:0xf
v_mul_f32_dpp v223,v223,v71 quad_perm:[1,0,3,2] row_mask:0xf bank_mask:0xf
v_cvt_pk_bf16_f32 v44,v212,v213
v_cvt_pk_bf16_f32 v45,v214,v215
v_cvt_pk_bf16_f32 v46,v220,v221
v_cvt_pk_bf16_f32 v47,v222,v223
v_pk_maximum3_f16 v7,v7,v44,v45
v_pk_maximum3_f16 v7,v7,v46,v47
s_mov_b64 exec,s[12:13]
v_swap_b32 v56,v44
v_swap_b32 v57,v45
v_swap_b32 v58,v46
v_swap_b32 v59,v47
s_mov_b64 exec,-1
s_add_u32 s65,s61,98304
buffer_store_dwordx4 v[40:43],v10,s[40:43],s65 offen offset:0 sc1
v_mul_f32 v228,v228,v72
v_mul_f32 v229,v229,v73
v_mul_f32 v230,v230,v74
v_mul_f32 v231,v231,v75
v_mul_f32 v236,v236,v76
v_mul_f32 v237,v237,v77
v_mul_f32 v238,v238,v78
v_mul_f32 v239,v239,v79
v_cvt_pk_bf16_f32 v52,v228,v229
v_cvt_pk_bf16_f32 v53,v230,v231
v_cvt_pk_bf16_f32 v54,v236,v237
v_cvt_pk_bf16_f32 v55,v238,v239
v_pk_maximum3_f16 v7,v7,v52,v53
v_pk_maximum3_f16 v7,v7,v54,v55
v_mul_f32_dpp v244,v244,v80 quad_perm:[1,0,3,2] row_mask:0xf bank_mask:0xf
v_mul_f32_dpp v245,v245,v81 quad_perm:[1,0,3,2] row_mask:0xf bank_mask:0xf
v_mul_f32_dpp v246,v246,v82 quad_perm:[1,0,3,2] row_mask:0xf bank_mask:0xf
v_mul_f32_dpp v247,v247,v83 quad_perm:[1,0,3,2] row_mask:0xf bank_mask:0xf
v_mul_f32_dpp v252,v252,v84 quad_perm:[1,0,3,2] row_mask:0xf bank_mask:0xf
v_mul_f32_dpp v253,v253,v85 quad_perm:[1,0,3,2] row_mask:0xf bank_mask:0xf
v_mul_f32_dpp v254,v254,v86 quad_perm:[1,0,3,2] row_mask:0xf bank_mask:0xf
s_add_u32 s65,s61,98304
buffer_store_dwordx4 v[48:51],v9,s[40:43],s65 offen offset:384 sc1
v_mul_f32_dpp v255,v255,v87 quad_perm:[1,0,3,2] row_mask:0xf bank_mask:0xf
v_cvt_pk_bf16_f32 v60,v244,v245
v_cvt_pk_bf16_f32 v61,v246,v247
v_cvt_pk_bf16_f32 v62,v252,v253
v_cvt_pk_bf16_f32 v63,v254,v255
v_pk_maximum3_f16 v7,v7,v60,v61
v_pk_maximum3_f16 v7,v7,v62,v63
s_mov_b64 exec,s[12:13]
v_swap_b32 v52,v60
v_swap_b32 v53,v61
v_swap_b32 v54,v62
v_swap_b32 v55,v63
s_mov_b64 exec,-1
s_add_u32 s4,s10,12
s_cmp_ge_u32 s4,20
s_cselect_b32 s7,20,0
s_sub_u32 s4,s4,s7
s_mul_i32 s4,s4,3488
s_add_u32 s5,s10,14
s_cmp_ge_u32 s5,20
s_cselect_b32 s7,20,0
s_sub_u32 s5,s5,s7
s_mul_i32 s5,s5,3488
s_add_u32 s6,s10,16
s_cmp_ge_u32 s6,20
s_cselect_b32 s7,20,0
s_sub_u32 s6,s6,s7
s_mul_i32 s6,s6,3488
v_add_u32 v112,s4,v15
v_add_u32 v116,s5,v15
s_mov_b64 exec,s[8:9]
v_add_u32 v112,s5,v15
v_add_u32 v116,s6,v15
s_mov_b64 exec,-1
v_xor_b32 v113,16,v112
v_xor_b32 v117,16,v116
v_xor_b32 v114,32,v112
v_xor_b32 v118,32,v116
v_xor_b32 v115,48,v112
v_xor_b32 v119,48,v116
s_barrier
s_setprio 3
ds_read_b128 v[136:139],v116 offset:0
ds_read_b128 v[88:91],v11 offset:768
ds_read_b128 v[140:143],v116 offset:3488
ds_read_b128 v[128:131],v112 offset:0
ds_read_b128 v[132:135],v112 offset:3488
ds_read_b128 v[92:95],v12 offset:0
ds_read_b128 v[152:155],v117 offset:0
ds_read_b128 v[96:99],v11 offset:1920
ds_read_b128 v[156:159],v117 offset:3488
ds_read_b128 v[144:147],v113 offset:0
ds_read_b128 v[148:151],v113 offset:3488
ds_read_b128 v[100:103],v12 offset:1152
s_waitcnt lgkmcnt(10)
v_mfma_f32_16x16x32_bf16 v[160:163],v[88:91],v[136:139],0
s_waitcnt lgkmcnt(9)
s_add_u32 s65,s61,98304
buffer_store_dwordx4 v[56:59],v10,s[40:43],s65 offen offset:128 sc1
v_mfma_f32_16x16x32_bf16 v[184:187],v[88:91],v[140:143],0
s_waitcnt lgkmcnt(6)
ds_read_b128 v[140:143],v116 offset:3552
ds_read_b128 v[88:91],v11 offset:5376
v_smfmac_f32_16x16x64_bf16 v[160:163],v[92:95],v[128:135],v13
ds_read_b128 v[128:131],v112 offset:64
v_smfmac_f32_16x16x64_bf16 v[184:187],v[92:95],v[132:139],v13
s_waitcnt lgkmcnt(7)
ds_read_b128 v[136:139],v116 offset:64
ds_read_b128 v[132:135],v112 offset:3552
v_mfma_f32_16x16x32_bf16 v[164:167],v[96:99],v[152:155],0
s_waitcnt lgkmcnt(8)
ds_read_b128 v[92:95],v12 offset:4608
v_add_f32 v120,v160,v161
v_mul_f32 v124,v160,v160
v_fmac_f32 v124,v161,v161
v_mfma_f32_16x16x32_bf16 v[188:191],v[96:99],v[156:159],0
s_waitcnt lgkmcnt(6)
ds_read_b128 v[156:159],v117 offset:3552
ds_read_b128 v[96:99],v11 offset:6528
v_add_f32 v121,v162,v163
v_mul_f32 v125,v162,v162
v_fmac_f32 v125,v163,v163
v_smfmac_f32_16x16x64_bf16 v[164:167],v[100:103],v[144:151],v13
ds_read_b128 v[144:147],v113 offset:64
v_add_f32 v122,v184,v185
v_mul_f32 v126,v184,v184
v_fmac_f32 v126,v185,v185
v_smfmac_f32_16x16x64_bf16 v[188:191],v[100:103],v[148:155],v13
s_waitcnt lgkmcnt(5)
s_add_u32 s65,s61,98304
buffer_store_dwordx4 v[44:47],v9,s[40:43],s65 offen offset:512 sc1
ds_read_b128 v[152:155],v117 offset:64
ds_read_b128 v[148:151],v113 offset:3552
v_add_f32 v123,v186,v187
v_mul_f32 v127,v186,v186
v_fmac_f32 v127,v187,v187
v_mfma_f32_16x16x32_bf16 v[168:171],v[88:91],v[136:139],0
ds_read_b128 v[100:103],v12 offset:5760
v_add_f32 v120,v120,v164
v_fmac_f32 v124,v164,v164
v_add_f32 v120,v120,v165
v_fmac_f32 v124,v165,v165
v_mfma_f32_16x16x32_bf16 v[192:195],v[88:91],v[140:143],0
s_waitcnt lgkmcnt(6)
ds_read_b128 v[140:143],v116 offset:3616
ds_read_b128 v[88:91],v11 offset:9984
v_add_f32 v121,v121,v166
v_fmac_f32 v125,v166,v166
v_add_f32 v121,v121,v167
v_fmac_f32 v125,v167,v167
v_smfmac_f32_16x16x64_bf16 v[168:171],v[92:95],v[128:135],v13
ds_read_b128 v[128:131],v112 offset:128
v_add_f32 v122,v122,v188
v_fmac_f32 v126,v188,v188
v_add_f32 v122,v122,v189
v_fmac_f32 v126,v189,v189
v_smfmac_f32_16x16x64_bf16 v[192:195],v[92:95],v[132:139],v13
s_waitcnt lgkmcnt(5)
s_add_u32 s65,s61,98304
buffer_store_dwordx4 v[52:55],v10,s[40:43],s65 offen offset:256 sc1
ds_read_b128 v[136:139],v116 offset:128
ds_read_b128 v[132:135],v112 offset:3616
v_add_f32 v123,v123,v190
v_fmac_f32 v127,v190,v190
v_add_f32 v123,v123,v191
v_fmac_f32 v127,v191,v191
v_mfma_f32_16x16x32_bf16 v[172:175],v[96:99],v[152:155],0
ds_read_b128 v[92:95],v12 offset:9216
v_add_f32 v120,v120,v168
v_fmac_f32 v124,v168,v168
v_add_f32 v120,v120,v169
v_fmac_f32 v124,v169,v169
v_mfma_f32_16x16x32_bf16 v[196:199],v[96:99],v[156:159],0
s_waitcnt lgkmcnt(6)
ds_read_b128 v[156:159],v117 offset:3616
ds_read_b128 v[96:99],v11 offset:11136
v_add_f32 v121,v121,v170
v_fmac_f32 v125,v170,v170
v_add_f32 v121,v121,v171
v_fmac_f32 v125,v171,v171
v_smfmac_f32_16x16x64_bf16 v[172:175],v[100:103],v[144:151],v13
ds_read_b128 v[144:147],v113 offset:128
v_add_f32 v122,v122,v192
v_fmac_f32 v126,v192,v192
v_add_f32 v122,v122,v193
v_fmac_f32 v126,v193,v193
v_smfmac_f32_16x16x64_bf16 v[196:199],v[100:103],v[148:155],v13
s_waitcnt lgkmcnt(5)
s_add_u32 s65,s61,98304
buffer_store_dwordx4 v[60:63],v9,s[40:43],s65 offen offset:640 sc1
ds_read_b128 v[152:155],v117 offset:128
ds_read_b128 v[148:151],v113 offset:3616
v_add_f32 v123,v123,v194
v_fmac_f32 v127,v194,v194
v_add_f32 v123,v123,v195
v_fmac_f32 v127,v195,v195
v_mfma_f32_16x16x32_bf16 v[176:179],v[88:91],v[136:139],0
ds_read_b128 v[100:103],v12 offset:10368
v_add_f32 v120,v120,v172
v_fmac_f32 v124,v172,v172
v_add_f32 v120,v120,v173
v_fmac_f32 v124,v173,v173
v_mfma_f32_16x16x32_bf16 v[200:203],v[88:91],v[140:143],0
s_waitcnt lgkmcnt(6)
ds_read_b128 v[140:143],v118 offset:3488
ds_read_b128 v[88:91],v11 offset:3072
v_add_f32 v121,v121,v174
v_fmac_f32 v125,v174,v174
v_add_f32 v121,v121,v175
v_fmac_f32 v125,v175,v175
v_smfmac_f32_16x16x64_bf16 v[176:179],v[92:95],v[128:135],v13
ds_read_b128 v[128:131],v114 offset:0
v_add_f32 v122,v122,v196
v_fmac_f32 v126,v196,v196
v_add_f32 v122,v122,v197
v_fmac_f32 v126,v197,v197
v_smfmac_f32_16x16x64_bf16 v[200:203],v[92:95],v[132:139],v13
s_waitcnt lgkmcnt(5)
ds_read_b128 v[136:139],v118 offset:0
ds_read_b128 v[132:135],v114 offset:3488
v_add_f32 v123,v123,v198
v_fmac_f32 v127,v198,v198
v_add_f32 v123,v123,v199
v_fmac_f32 v127,v199,v199
v_mfma_f32_16x16x32_bf16 v[180:183],v[96:99],v[152:155],0
ds_read_b128 v[92:95],v12 offset:2304
v_add_f32 v120,v120,v176
v_fmac_f32 v124,v176,v176
v_add_f32 v120,v120,v177
v_fmac_f32 v124,v177,v177
v_mfma_f32_16x16x32_bf16 v[204:207],v[96:99],v[156:159],0
s_waitcnt lgkmcnt(6)
ds_read_b128 v[156:159],v119 offset:3488
ds_read_b128 v[96:99],v11 offset:4224
v_add_f32 v121,v121,v178
v_fmac_f32 v125,v178,v178
v_add_f32 v121,v121,v179
v_fmac_f32 v125,v179,v179
v_smfmac_f32_16x16x64_bf16 v[180:183],v[100:103],v[144:151],v13
ds_read_b128 v[144:147],v115 offset:0
v_add_f32 v122,v122,v200
v_fmac_f32 v126,v200,v200
v_add_f32 v122,v122,v201
v_fmac_f32 v126,v201,v201
v_smfmac_f32_16x16x64_bf16 v[204:207],v[100:103],v[148:155],v13
s_waitcnt lgkmcnt(5)
ds_read_b128 v[152:155],v119 offset:0
ds_read_b128 v[148:151],v115 offset:3488
v_add_f32 v123,v123,v202
v_fmac_f32 v127,v202,v202
v_add_f32 v123,v123,v203
v_fmac_f32 v127,v203,v203
v_mfma_f32_16x16x32_bf16 v[208:211],v[88:91],v[136:139],0
ds_read_b128 v[100:103],v12 offset:3456
v_add_f32 v120,v120,v180
v_fmac_f32 v124,v180,v180
v_add_f32 v120,v120,v181
v_fmac_f32 v124,v181,v181
v_mfma_f32_16x16x32_bf16 v[232:235],v[88:91],v[140:143],0
s_waitcnt lgkmcnt(6)
ds_read_b128 v[140:143],v118 offset:3552
ds_read_b128 v[88:91],v11 offset:7680
v_add_f32 v121,v121,v182
v_fmac_f32 v125,v182,v182
v_add_f32 v121,v121,v183
v_fmac_f32 v125,v183,v183
v_smfmac_f32_16x16x64_bf16 v[208:211],v[92:95],v[128:135],v13
ds_read_b128 v[128:131],v114 offset:64
v_add_f32 v122,v122,v204
v_fmac_f32 v126,v204,v204
v_add_f32 v122,v122,v205
v_fmac_f32 v126,v205,v205
v_smfmac_f32_16x16x64_bf16 v[232:235],v[92:95],v[132:139],v13
s_waitcnt lgkmcnt(5)
ds_read_b128 v[136:139],v118 offset:64
ds_read_b128 v[132:135],v114 offset:3552
v_add_f32 v123,v123,v206
v_fmac_f32 v127,v206,v206
v_add_f32 v123,v123,v207
v_fmac_f32 v127,v207,v207
v_mfma_f32_16x16x32_bf16 v[212:215],v[96:99],v[152:155],0
ds_read_b128 v[92:95],v12 offset:6912
v_add_f32 v120,v120,v208
v_fmac_f32 v124,v208,v208
v_add_f32 v120,v120,v209
v_fmac_f32 v124,v209,v209
v_mfma_f32_16x16x32_bf16 v[236:239],v[96:99],v[156:159],0
s_waitcnt lgkmcnt(6)
s_setprio 2
ds_read_b128 v[156:159],v119 offset:3552
ds_read_b128 v[96:99],v11 offset:8832
v_add_f32 v121,v121,v210
v_fmac_f32 v125,v210,v210
v_add_f32 v121,v121,v211
v_fmac_f32 v125,v211,v211
v_smfmac_f32_16x16x64_bf16 v[212:215],v[100:103],v[144:151],v13
ds_read_b128 v[144:147],v115 offset:64
v_add_f32 v122,v122,v232
v_fmac_f32 v126,v232,v232
v_add_f32 v122,v122,v233
v_fmac_f32 v126,v233,v233
v_smfmac_f32_16x16x64_bf16 v[236:239],v[100:103],v[148:155],v13
s_waitcnt lgkmcnt(5)
ds_read_b128 v[152:155],v119 offset:64
ds_read_b128 v[148:151],v115 offset:3552
v_add_f32 v123,v123,v234
v_fmac_f32 v127,v234,v234
v_add_f32 v123,v123,v235
v_fmac_f32 v127,v235,v235
v_mfma_f32_16x16x32_bf16 v[216:219],v[88:91],v[136:139],0
ds_read_b128 v[100:103],v12 offset:8064
v_add_f32 v120,v120,v212
v_fmac_f32 v124,v212,v212
v_add_f32 v120,v120,v213
v_fmac_f32 v124,v213,v213
v_add_f32 v121,v121,v214
v_mfma_f32_16x16x32_bf16 v[240:243],v[88:91],v[140:143],0
s_waitcnt lgkmcnt(6)
ds_read_b128 v[140:143],v118 offset:3616
ds_read_b128 v[88:91],v11 offset:12288
v_fmac_f32 v125,v214,v214
v_add_f32 v121,v121,v215
v_fmac_f32 v125,v215,v215
v_add_f32 v122,v122,v236
v_smfmac_f32_16x16x64_bf16 v[216:219],v[92:95],v[128:135],v13
ds_read_b128 v[128:131],v114 offset:128
v_fmac_f32 v126,v236,v236
v_add_f32 v122,v122,v237
v_fmac_f32 v126,v237,v237
v_add_f32 v123,v123,v238
v_fmac_f32 v127,v238,v238
v_smfmac_f32_16x16x64_bf16 v[240:243],v[92:95],v[132:139],v13
s_waitcnt lgkmcnt(5)
ds_read_b128 v[136:139],v118 offset:128
ds_read_b128 v[132:135],v114 offset:3616
v_add_f32 v123,v123,v239
v_fmac_f32 v127,v239,v239
v_add_u32 v112,v112,v8
v_add_u32 v116,v116,v8
v_mfma_f32_16x16x32_bf16 v[220:223],v[96:99],v[152:155],0
ds_read_b128 v[92:95],v12 offset:11520
ds_read_b128 v[104:107],v14 offset:0
v_add_f32 v120,v120,v216
v_fmac_f32 v124,v216,v216
v_add_f32 v120,v120,v217
v_fmac_f32 v124,v217,v217
v_mfma_f32_16x16x32_bf16 v[244:247],v[96:99],v[156:159],0
s_waitcnt lgkmcnt(7)
ds_read_b128 v[108:111],v4 offset:3584
ds_read_b128 v[156:159],v119 offset:3616
v_add_f32 v121,v121,v218
v_fmac_f32 v125,v218,v218
v_add_f32 v121,v121,v219
v_fmac_f32 v125,v219,v219
v_smfmac_f32_16x16x64_bf16 v[220:223],v[100:103],v[144:151],v13
ds_read_b128 v[96:99],v11 offset:13440
ds_read_b128 v[144:147],v115 offset:128
v_add_f32 v122,v122,v240
v_fmac_f32 v126,v240,v240
v_add_f32 v122,v122,v241
v_fmac_f32 v126,v241,v241
v_smfmac_f32_16x16x64_bf16 v[244:247],v[100:103],v[148:155],v13
s_waitcnt lgkmcnt(7)
ds_read_b128 v[152:155],v119 offset:128
ds_read_b128 v[148:151],v115 offset:3616
v_add_f32 v123,v123,v242
v_fmac_f32 v127,v242,v242
v_add_f32 v123,v123,v243
v_fmac_f32 v127,v243,v243
v_mfma_f32_16x16x32_bf16 v[224:227],v[88:91],v[136:139],0
ds_read_b128 v[100:103],v12 offset:12672
v_add_f32 v120,v120,v220
v_fmac_f32 v124,v220,v220
v_add_f32 v120,v120,v221
v_fmac_f32 v124,v221,v221
v_mfma_f32_16x16x32_bf16 v[248:251],v[88:91],v[140:143],0
s_waitcnt lgkmcnt(8)
s_setprio 1
ds_read_b128 v[140:143],v116 offset:192
ds_read_b128 v[88:91],v112 offset:3616
v_add_f32 v121,v121,v222
v_fmac_f32 v125,v222,v222
v_add_f32 v121,v121,v223
v_fmac_f32 v125,v223,v223
v_smfmac_f32_16x16x64_bf16 v[224:227],v[92:95],v[128:135],v13
ds_read_b128 v[128:131],v112 offset:3488
v_add_f32 v122,v122,v244
v_fmac_f32 v126,v244,v244
v_add_f32 v122,v122,v245
v_fmac_f32 v126,v245,v245
v_smfmac_f32_16x16x64_bf16 v[248:251],v[92:95],v[132:139],v13
s_waitcnt lgkmcnt(5)
ds_read_b128 v[136:139],v116 offset:0
ds_read_b128 v[132:135],v112 offset:3680
v_add_f32 v123,v123,v246
v_fmac_f32 v127,v246,v246
v_add_f32 v123,v123,v247
v_fmac_f32 v127,v247,v247
v_mfma_f32_16x16x32_bf16 v[228:231],v[96:99],v[152:155],0
ds_read_b128 v[92:95],v112 offset:3808
v_add_f32 v120,v120,v224
v_fmac_f32 v124,v224,v224
v_add_f32 v120,v120,v225
v_fmac_f32 v124,v225,v225
v_mfma_f32_16x16x32_bf16 v[252:255],v[96:99],v[156:159],0
s_waitcnt lgkmcnt(6)
ds_read_b128 v[156:159],v116 offset:256
ds_read_b128 v[96:99],v116 offset:128
v_add_f32 v121,v121,v226
v_fmac_f32 v125,v226,v226
v_add_f32 v121,v121,v227
v_fmac_f32 v125,v227,v227
v_smfmac_f32_16x16x64_bf16 v[228:231],v[100:103],v[144:151],v13
ds_read_b128 v[144:147],v112 offset:3552
v_add_f32 v122,v122,v248
v_fmac_f32 v126,v248,v248
v_add_f32 v122,v122,v249
v_fmac_f32 v126,v249,v249
v_smfmac_f32_16x16x64_bf16 v[252:255],v[100:103],v[148:155],v13
s_setprio 0
ds_read_b128 v[152:155],v116 offset:64
ds_read_b128 v[148:151],v112 offset:3744
v_add_f32 v123,v123,v250
v_fmac_f32 v127,v250,v250
v_add_f32 v123,v123,v251
v_fmac_f32 v127,v251,v251
ds_read_b128 v[100:103],v116 offset:320
v_add_f32 v120,v120,v228
v_fmac_f32 v124,v228,v228
v_add_f32 v120,v120,v229
v_fmac_f32 v124,v229,v229
v_add_f32 v121,v121,v230
v_fmac_f32 v125,v230,v230
v_add_f32 v121,v121,v231
v_fmac_f32 v125,v231,v231
s_waitcnt lgkmcnt(0)
s_barrier
s_setprio 0
v_add_f32 v122,v122,v252
v_fmac_f32 v126,v252,v252
v_add_f32 v122,v122,v253
v_fmac_f32 v126,v253,v253
v_add_f32 v123,v123,v254
v_fmac_f32 v127,v254,v254
v_add_f32 v123,v123,v255
v_fmac_f32 v127,v255,v255
s_setprio 3
ds_read_b128 v[56:59],v14 offset:256
ds_read_b128 v[60:63],v4 offset:3616
s_setprio 0
v_permlane32_swap_b32 v120,v122
v_permlane32_swap_b32 v121,v123
v_permlane32_swap_b32 v124,v126
v_permlane32_swap_b32 v125,v127
v_add_f32 v120,v120,v122
v_add_f32 v121,v121,v123
v_add_f32 v124,v124,v126
v_add_f32 v125,v125,v127
v_permlane16_swap_b32 v120,v121
s_nop 0
v_permlane16_swap_b32 v124,v125
v_add_f32 v113,v120,v121
v_add_f32 v114,v124,v125
v_mul_f32 v113,0x3c2aaaab,v113
v_mul_f32 v114,0x3c2aaaab,v114
v_mul_f32 v115,0x5e000000,v114
v_fma_f32 v114,-v113,v113,v114
v_pk_maximum3_f16 v7,v7,v115,v115 op_sel:[0,1,1] op_sel_hi:[1,1,1]
v_max_f32 v114,0,v114
v_add_f32 v114,0x3727c5ac,v114
v_rsq_f32 v114,v114
s_nop 0
v_mul_f32 v120,0x8befe000,v114
v_mul_f32 v121,0x8befe000,v114
v_mul_f32 v124,-v113,v120
v_mul_f32 v125,-v113,v120
v_permlane16_swap_b32 v120,v121
s_nop 0
v_permlane16_swap_b32 v124,v125
v_mov_b32 v122,v120
v_mov_b32 v123,v121
v_mov_b32 v126,v124
v_mov_b32 v127,v125
v_permlane32_swap_b32 v120,v122
v_permlane32_swap_b32 v124,v126
v_permlane32_swap_b32 v121,v123
v_permlane32_swap_b32 v125,v127
v_add_u32 v8,94432,v1
v_add_u32 v9,94432,v2
v_fma_f32 v160,v160,v120,v124
s_setprio 3
ds_read_b64_tr_b16 v[64:65],v1 offset:512
s_setprio 0
v_fma_f32 v161,v161,v120,v124
s_setprio 3
ds_read_b64_tr_b16 v[66:67],v1 offset:2560
s_setprio 0
v_fma_f32 v162,v162,v121,v125
s_setprio 3
ds_read_b128 v[112:115],v4 offset:896
s_setprio 0
v_fma_f32 v163,v163,v121,v125
s_setprio 3
ds_read_b64_tr_b16 v[68:69],v2 offset:512
s_setprio 0
v_fma_f32 v164,v164,v120,v124
s_setprio 3
ds_read_b64_tr_b16 v[70:71],v2 offset:2560
s_setprio 0
v_fma_f32 v165,v165,v120,v124
s_setprio 3
ds_read_b128 v[116:119],v4 offset:928
s_setprio 0
v_fma_f32 v166,v166,v121,v125
s_setprio 3
ds_read_b64_tr_b16 v[72:73],v1 offset:4608
s_setprio 0
v_fma_f32 v167,v167,v121,v125
s_setprio 3
ds_read_b64_tr_b16 v[74:75],v1 offset:6656
s_setprio 0
v_fma_f32 v208,v208,v120,v124
s_setprio 3
ds_read_b64_tr_b16 v[76:77],v2 offset:4608
s_setprio 0
v_fma_f32 v209,v209,v120,v124
s_setprio 3
ds_read_b64_tr_b16 v[78:79],v2 offset:6656
s_setprio 0
v_fma_f32 v210,v210,v121,v125
s_setprio 3
ds_read_b64_tr_b16 v[80:81],v1 offset:8704
s_setprio 0
v_fma_f32 v211,v211,v121,v125
s_setprio 3
ds_read_b64_tr_b16 v[82:83],v1 offset:10752
s_setprio 0
v_fma_f32 v212,v212,v120,v124
s_setprio 3
ds_read_b64_tr_b16 v[84:85],v2 offset:8704
s_setprio 0
v_fma_f32 v213,v213,v120,v124
s_setprio 3
ds_read_b64_tr_b16 v[86:87],v2 offset:10752
s_setprio 0
v_fma_f32 v214,v214,v121,v125
v_fma_f32 v215,v215,v121,v125
v_cvt_pk_bf16_f32 v16,v160,v161
v_cvt_pk_bf16_f32 v20,v162,v163
v_cvt_pk_bf16_f32 v17,v164,v165
v_cvt_pk_bf16_f32 v21,v166,v167
v_cvt_pk_bf16_f32 v18,v208,v209
v_cvt_pk_bf16_f32 v22,v210,v211
v_cvt_pk_bf16_f32 v19,v212,v213
v_cvt_pk_bf16_f32 v23,v214,v215
v_fma_f32 v168,v168,v120,v124
v_fma_f32 v169,v169,v120,v124
v_mfma_f32_16x16x32_bf16 v[160:163],v[104:107],v[128:131],v[108:111]
v_fma_f32 v170,v170,v121,v125
v_fma_f32 v171,v171,v121,v125
v_fma_f32 v172,v172,v120,v124
v_fma_f32 v173,v173,v120,v124
v_fma_f32 v174,v174,v121,v125
v_fma_f32 v175,v175,v121,v125
v_mfma_f32_16x16x32_bf16 v[164:167],v[104:107],v[132:135],v[108:111]
v_fma_f32 v216,v216,v120,v124
v_fma_f32 v217,v217,v120,v124
v_fma_f32 v218,v218,v121,v125
v_fma_f32 v219,v219,v121,v125
v_fma_f32 v220,v220,v120,v124
v_mfma_f32_16x16x32_bf16 v[208:211],v[104:107],v[136:139],v[108:111]
v_fma_f32 v221,v221,v120,v124
v_fma_f32 v222,v222,v121,v125
v_fma_f32 v223,v223,v121,v125
v_cvt_pk_bf16_f32 v24,v168,v169
v_cvt_pk_bf16_f32 v28,v170,v171
s_setprio 3
v_mfma_f32_16x16x32_bf16 v[212:215],v[104:107],v[140:143],v[108:111]
ds_read_b128 v[104:107],v14 offset:512
ds_read_b128 v[108:111],v4 offset:3712
s_setprio 0
v_cvt_pk_bf16_f32 v25,v172,v173
v_cvt_pk_bf16_f32 v29,v174,v175
v_cvt_pk_bf16_f32 v26,v216,v217
v_cvt_pk_bf16_f32 v30,v218,v219
v_cvt_pk_bf16_f32 v27,v220,v221
v_cvt_pk_bf16_f32 v31,v222,v223
v_fma_f32 v176,v176,v120,v124
v_fma_f32 v177,v177,v120,v124
s_waitcnt lgkmcnt(15)
v_mfma_f32_16x16x32_bf16 v[168:171],v[56:59],v[128:131],v[60:63]
v_fma_f32 v178,v178,v121,v125
v_fma_f32 v179,v179,v121,v125
v_fma_f32 v180,v180,v120,v124
v_fma_f32 v181,v181,v120,v124
v_fma_f32 v182,v182,v121,v125
v_fma_f32 v183,v183,v121,v125
v_mfma_f32_16x16x32_bf16 v[172:175],v[56:59],v[132:135],v[60:63]
v_fma_f32 v224,v224,v120,v124
v_fma_f32 v225,v225,v120,v124
v_fma_f32 v226,v226,v121,v125
v_fma_f32 v227,v227,v121,v125
v_fma_f32 v228,v228,v120,v124
v_mfma_f32_16x16x32_bf16 v[216:219],v[56:59],v[136:139],v[60:63]
v_fma_f32 v229,v229,v120,v124
v_fma_f32 v230,v230,v121,v125
v_fma_f32 v231,v231,v121,v125
v_cvt_pk_bf16_f32 v32,v176,v177
v_cvt_pk_bf16_f32 v36,v178,v179
s_setprio 3
v_mfma_f32_16x16x32_bf16 v[220:223],v[56:59],v[140:143],v[60:63]
ds_read_b128 v[56:59],v14 offset:768
ds_read_b128 v[60:63],v4 offset:3744
s_setprio 0
v_cvt_pk_bf16_f32 v33,v180,v181
v_cvt_pk_bf16_f32 v37,v182,v183
v_cvt_pk_bf16_f32 v34,v224,v225
v_cvt_pk_bf16_f32 v38,v226,v227
v_cvt_pk_bf16_f32 v35,v228,v229
v_cvt_pk_bf16_f32 v39,v230,v231
v_fma_f32 v184,v184,v122,v126
s_waitcnt lgkmcnt(2)
v_mfma_f32_16x16x32_bf16 v[176:179],v[104:107],v[144:147],v[108:111]
v_fma_f32 v185,v185,v122,v126
v_fma_f32 v186,v186,v123,v127
v_fma_f32 v187,v187,v123,v127
v_mfma_f32_16x16x32_bf16 v[128:131],v[64:67],v[16:19],v[112:115]
v_fma_f32 v188,v188,v122,v126
v_fma_f32 v189,v189,v122,v126
v_fma_f32 v190,v190,v123,v127
v_mfma_f32_16x16x32_bf16 v[180:183],v[104:107],v[148:151],v[108:111]
v_fma_f32 v191,v191,v123,v127
v_fma_f32 v232,v232,v122,v126
v_fma_f32 v233,v233,v122,v126
v_mfma_f32_16x16x32_bf16 v[136:139],v[64:67],v[20:23],v[112:115]
v_fma_f32 v234,v234,v123,v127
v_fma_f32 v235,v235,v123,v127
v_fma_f32 v236,v236,v122,v126
v_mfma_f32_16x16x32_bf16 v[224:227],v[104:107],v[152:155],v[108:111]
v_fma_f32 v237,v237,v122,v126
v_fma_f32 v238,v238,v123,v127
v_fma_f32 v239,v239,v123,v127
v_mfma_f32_16x16x32_bf16 v[140:143],v[68:71],v[20:23],v[116:119]
v_cvt_pk_bf16_f32 v40,v184,v185
v_cvt_pk_bf16_f32 v44,v186,v187
v_cvt_pk_bf16_f32 v41,v188,v189
s_setprio 3
v_mfma_f32_16x16x32_bf16 v[228:231],v[104:107],v[156:159],v[108:111]
ds_read_b128 v[104:107],v14 offset:1024
ds_read_b128 v[108:111],v4 offset:3840
s_setprio 0
v_cvt_pk_bf16_f32 v45,v190,v191
v_cvt_pk_bf16_f32 v42,v232,v233
v_cvt_pk_bf16_f32 v46,v234,v235
v_mfma_f32_16x16x32_bf16 v[132:135],v[68:71],v[16:19],v[116:119]
v_cvt_pk_bf16_f32 v43,v236,v237
v_cvt_pk_bf16_f32 v47,v238,v239
v_fma_f32 v192,v192,v122,v126
s_waitcnt lgkmcnt(2)
v_mfma_f32_16x16x32_bf16 v[184:187],v[56:59],v[144:147],v[60:63]
v_fma_f32 v193,v193,v122,v126
v_fma_f32 v194,v194,v123,v127
v_mfma_f32_16x16x32_bf16 v[128:131],v[72:75],v[24:27],v[128:131]
v_fma_f32 v195,v195,v123,v127
v_fma_f32 v196,v196,v122,v126
v_mfma_f32_16x16x32_bf16 v[188:191],v[56:59],v[148:151],v[60:63]
v_fma_f32 v197,v197,v122,v126
v_fma_f32 v198,v198,v123,v127
v_mfma_f32_16x16x32_bf16 v[136:139],v[72:75],v[28:31],v[136:139]
v_fma_f32 v199,v199,v123,v127
v_fma_f32 v240,v240,v122,v126
v_mfma_f32_16x16x32_bf16 v[232:235],v[56:59],v[152:155],v[60:63]
v_fma_f32 v241,v241,v122,v126
v_fma_f32 v242,v242,v123,v127
v_mfma_f32_16x16x32_bf16 v[140:143],v[76:79],v[28:31],v[140:143]
v_fma_f32 v243,v243,v123,v127
v_fma_f32 v244,v244,v122,v126
s_setprio 3
v_mfma_f32_16x16x32_bf16 v[236:239],v[56:59],v[156:159],v[60:63]
ds_read_b128 v[144:147],v14 offset:1280
ds_read_b128 v[148:151],v4 offset:3872
s_setprio 0
v_fma_f32 v245,v245,v122,v126
v_fma_f32 v246,v246,v123,v127
v_mfma_f32_16x16x32_bf16 v[132:135],v[76:79],v[24:27],v[132:135]
v_fma_f32 v247,v247,v123,v127
v_cvt_pk_bf16_f32 v48,v192,v193
v_cvt_pk_bf16_f32 v52,v194,v195
v_cvt_pk_bf16_f32 v49,v196,v197
v_cvt_pk_bf16_f32 v53,v198,v199
v_cvt_pk_bf16_f32 v50,v240,v241
v_cvt_pk_bf16_f32 v54,v242,v243
v_cvt_pk_bf16_f32 v51,v244,v245
v_cvt_pk_bf16_f32 v55,v246,v247
v_fma_f32 v200,v200,v122,v126
s_waitcnt lgkmcnt(2)
v_mfma_f32_16x16x32_bf16 v[192:195],v[104:107],v[88:91],v[108:111]
v_fma_f32 v201,v201,v122,v126
v_fma_f32 v202,v202,v123,v127
v_fma_f32 v203,v203,v123,v127
v_mfma_f32_16x16x32_bf16 v[128:131],v[80:83],v[32:35],v[128:131]
v_fma_f32 v204,v204,v122,v126
v_fma_f32 v205,v205,v122,v126
v_fma_f32 v206,v206,v123,v127
v_mfma_f32_16x16x32_bf16 v[196:199],v[104:107],v[92:95],v[108:111]
v_fma_f32 v207,v207,v123,v127
v_fma_f32 v248,v248,v122,v126
v_fma_f32 v249,v249,v122,v126
v_mfma_f32_16x16x32_bf16 v[136:139],v[80:83],v[36:39],v[136:139]
v_fma_f32 v250,v250,v123,v127
v_fma_f32 v251,v251,v123,v127
v_fma_f32 v252,v252,v122,v126
v_mfma_f32_16x16x32_bf16 v[240:243],v[104:107],v[96:99],v[108:111]
v_fma_f32 v253,v253,v122,v126
v_fma_f32 v254,v254,v123,v127
v_fma_f32 v255,v255,v123,v127
v_mfma_f32_16x16x32_bf16 v[140:143],v[84:87],v[36:39],v[140:143]
v_cvt_pk_bf16_f32 v56,v200,v201
v_cvt_pk_bf16_f32 v60,v202,v203
v_cvt_pk_bf16_f32 v57,v204,v205
v_mfma_f32_16x16x32_bf16 v[244:247],v[104:107],v[100:103],v[108:111]
v_cvt_pk_bf16_f32 v61,v206,v207
v_cvt_pk_bf16_f32 v58,v248,v249
v_cvt_pk_bf16_f32 v62,v250,v251
v_mfma_f32_16x16x32_bf16 v[132:135],v[84:87],v[32:35],v[132:135]
v_cvt_pk_bf16_f32 v59,v252,v253
v_cvt_pk_bf16_f32 v63,v254,v255
s_waitcnt lgkmcnt(0)
v_mfma_f32_16x16x32_bf16 v[200:203],v[144:147],v[88:91],v[148:151]
s_barrier
s_setprio 3
ds_read_b64_tr_b16 v[104:105],v3 offset:12448
v_lshlrev_b32_sdwa v128,1,v128 dst_sel:DWORD dst_unused:UNUSED_PAD src0_sel:DWORD src1_sel:WORD_1
v_lshlrev_b32_sdwa v130,1,v130 dst_sel:DWORD dst_unused:UNUSED_PAD src0_sel:DWORD src1_sel:WORD_1
v_lshlrev_b32_sdwa v129,1,v129 dst_sel:DWORD dst_unused:UNUSED_PAD src0_sel:DWORD src1_sel:WORD_1
v_mfma_f32_16x16x32_bf16 v[204:207],v[144:147],v[92:95],v[148:151]
s_cbranch_vccz RA13_%=
s_add_u32 m0,s58,0
s_mul_i32 s69,s59,9
s_add_u32 s69,s69,s97
buffer_load_dwordx4 v5,s[44:47],s69 offen lds
s_add_u32 m0,m0,1024
s_add_u32 s69,s69,s53
buffer_load_dwordx4 v6,s[44:47],s69 offen lds
RA13_%=:
ds_read_u16 v128,v128
ds_read_b64_tr_b16 v[106:107],v3 offset:13984
v_lshlrev_b32_sdwa v131,1,v131 dst_sel:DWORD dst_unused:UNUSED_PAD src0_sel:DWORD src1_sel:WORD_1
v_fma_mixlo_f16 v120,v140,s99,0
v_fma_mixlo_f16 v122,v141,s99,0
v_mfma_f32_16x16x32_bf16 v[248:251],v[144:147],v[96:99],v[148:151]
ds_read_u16_d16_hi v130,v130
ds_read_b64_tr_b16 v[108:109],v3 offset:12456
v_lshlrev_b32_sdwa v132,1,v132 dst_sel:DWORD dst_unused:UNUSED_PAD src0_sel:DWORD src1_sel:WORD_1
v_lshlrev_b32_sdwa v134,1,v134 dst_sel:DWORD dst_unused:UNUSED_PAD src0_sel:DWORD src1_sel:WORD_1
v_mfma_f32_16x16x32_bf16 v[252:255],v[144:147],v[100:103],v[148:151]
ds_read_u16 v129,v129
ds_read_b64_tr_b16 v[110:111],v3 offset:13992
v_fma_mixhi_f16 v120,v142,s99,0
v_fma_mixhi_f16 v122,v143,s99,0
v_lshlrev_b32_sdwa v133,1,v133 dst_sel:DWORD dst_unused:UNUSED_PAD src0_sel:DWORD src1_sel:WORD_1
v_lshlrev_b32_sdwa v135,1,v135 dst_sel:DWORD dst_unused:UNUSED_PAD src0_sel:DWORD src1_sel:WORD_1
v_mfma_f32_16x16x32_bf16 v[144:147],v[64:67],v[40:43],v[112:115]
s_cbranch_vccz RA14_%=
s_add_u32 m0,m0,1024
s_add_u32 s69,s69,s53
buffer_load_dwordx4 v5,s[44:47],s69 offen lds
s_add_u32 m0,m0,s50
s_add_u32 s69,s69,s53
buffer_load_dwordx4 v6,s[44:47],s69 offen lds
RA14_%=:
ds_read_u16_d16_hi v131,v131
ds_read_b64_tr_b16 v[88:89],v3 offset:12320
v_pk_fma_f16 v121,v120,v120,s54
v_pk_fma_f16 v123,v122,v122,s54
v_pk_mul_f16 v121,v120,v121
v_mfma_f32_16x16x32_bf16 v[152:155],v[64:67],v[44:47],v[112:115]
ds_read_u16 v132,v132
ds_read_b64_tr_b16 v[90:91],v3 offset:13856
v_pk_mul_f16 v123,v122,v123
v_lshlrev_b32_sdwa v136,1,v136 dst_sel:DWORD dst_unused:UNUSED_PAD src0_sel:DWORD src1_sel:WORD_1
v_mfma_f32_16x16x32_bf16 v[156:159],v[68:71],v[44:47],v[116:119]
ds_read_u16_d16_hi v134,v134
ds_read_b64_tr_b16 v[92:93],v3 offset:12328
v_lshlrev_b32_sdwa v138,1,v138 dst_sel:DWORD dst_unused:UNUSED_PAD src0_sel:DWORD src1_sel:WORD_1
v_exp_f16 v120,v121
v_mfma_f32_16x16x32_bf16 v[148:151],v[68:71],v[40:43],v[116:119]
ds_read_u16 v133,v133
ds_read_b64_tr_b16 v[94:95],v3 offset:13864
v_exp_f16 v122,v123
v_lshlrev_b32_sdwa v137,1,v137 dst_sel:DWORD dst_unused:UNUSED_PAD src0_sel:DWORD src1_sel:WORD_1
v_mfma_f32_16x16x32_bf16 v[144:147],v[72:75],v[48:51],v[144:147]
s_cbranch_vccz RA15_%=
s_add_u32 m0,m0,1024
s_add_u32 s69,s69,s53
buffer_load_dwordx4 v5,s[44:47],s69 offen lds
s_add_u32 m0,m0,1024
s_add_u32 s69,s69,s53
buffer_load_dwordx4 v6,s[44:47],s69 offen lds
RA15_%=:
ds_read_u16_d16_hi v135,v135
ds_read_b64_tr_b16 v[96:97],v3 offset:12384
v_lshlrev_b32_sdwa v139,1,v139 dst_sel:DWORD dst_unused:UNUSED_PAD src0_sel:DWORD src1_sel:WORD_1
v_exp_f16_sdwa v120,v121 dst_sel:WORD_1 dst_unused:UNUSED_PRESERVE src0_sel:WORD_1
v_mfma_f32_16x16x32_bf16 v[152:155],v[72:75],v[52:55],v[152:155]
ds_read_u16 v136,v136
ds_read_b64_tr_b16 v[98:99],v3 offset:13920
v_exp_f16_sdwa v122,v123 dst_sel:WORD_1 dst_unused:UNUSED_PRESERVE src0_sel:WORD_1
v_mfma_f32_16x16x32_bf16 v[156:159],v[76:79],v[52:55],v[156:159]
ds_read_u16_d16_hi v138,v138
ds_read_b64_tr_b16 v[100:101],v3 offset:12392
v_pk_fma_f16 v120,v120,s55,s55
v_pk_fma_f16 v122,v122,s55,s55
v_rcp_f16 v121,v120
v_mfma_f32_16x16x32_bf16 v[148:151],v[76:79],v[48:51],v[148:151]
s_cbranch_vccz RA16_%=
s_add_u32 m0,m0,s50
s_add_u32 s69,s69,s53
buffer_load_dwordx4 v5,s[44:47],s69 offen lds
s_add_u32 m0,m0,1024
s_add_u32 s69,s69,s53
buffer_load_dwordx4 v6,s[44:47],s69 offen lds
RA16_%=:
ds_read_u16 v137,v137
ds_read_b64_tr_b16 v[102:103],v3 offset:13928
v_rcp_f16 v123,v122
v_mfma_f32_16x16x32_bf16 v[144:147],v[80:83],v[56:59],v[144:147]
ds_read_u16_d16_hi v139,v139
ds_read_b64_tr_b16 v[64:65],v1 offset:12320
v_rcp_f16_sdwa v121,v120 dst_sel:WORD_1 dst_unused:UNUSED_PRESERVE src0_sel:WORD_1
v_mfma_f32_16x16x32_bf16 v[152:155],v[80:83],v[60:63],v[152:155]
ds_read_b64_tr_b16 v[66:67],v1 offset:14368
v_rcp_f16_sdwa v123,v122 dst_sel:WORD_1 dst_unused:UNUSED_PRESERVE src0_sel:WORD_1
v_fma_mix_f32 v140,v140,v121,0 op_sel_hi:[0,1,0]
v_mfma_f32_16x16x32_bf16 v[156:159],v[84:87],v[60:63],v[156:159]
s_cbranch_vccz RA17_%=
s_add_u32 m0,m0,1024
s_add_u32 s69,s69,s53
buffer_load_dwordx4 v5,s[44:47],s69 offen lds
s_add_u32 m0,m0,s50
s_add_u32 s69,s69,s53
buffer_load_dwordx4 v6,s[44:47],s69 offen lds
RA17_%=:
ds_read_b128 v[112:115],v4 offset:1024
v_fma_mix_f32 v142,v142,v121,0 op_sel:[0,1,0] op_sel_hi:[0,1,0]
s_waitcnt lgkmcnt(15)
v_lshl_add_u64 v[128:129],v[128:129],0,v[130:131]
s_waitcnt lgkmcnt(11)
v_lshl_add_u64 v[130:131],v[132:133],0,v[134:135]
v_mfma_f32_16x16x32_bf16 v[148:151],v[84:87],v[56:59],v[148:151]
ds_read_b64_tr_b16 v[68:69],v2 offset:12320
v_fma_mix_f32 v141,v141,v123,0 op_sel_hi:[0,1,0]
v_fma_mix_f32 v143,v143,v123,0 op_sel:[0,1,0] op_sel_hi:[0,1,0]
v_lshlrev_b32_sdwa v144,1,v144 dst_sel:DWORD dst_unused:UNUSED_PAD src0_sel:DWORD src1_sel:WORD_1
v_lshlrev_b32_sdwa v146,1,v146 dst_sel:DWORD dst_unused:UNUSED_PAD src0_sel:DWORD src1_sel:WORD_1
v_mfma_f32_16x16x32_bf16 v[160:163],v[88:91],v[128:131],v[160:163]
ds_read_u16 v144,v144
v_lshlrev_b32_sdwa v145,1,v145 dst_sel:DWORD dst_unused:UNUSED_PAD src0_sel:DWORD src1_sel:WORD_1
v_lshlrev_b32_sdwa v147,1,v147 dst_sel:DWORD dst_unused:UNUSED_PAD src0_sel:DWORD src1_sel:WORD_1
v_mfma_f32_16x16x32_bf16 v[168:171],v[92:95],v[128:131],v[168:171]
s_waitcnt lgkmcnt(10)
s_cbranch_vccz RA18_%=
s_add_u32 m0,m0,1024
s_add_u32 s69,s69,s53
buffer_load_dwordx4 v5,s[44:47],s69 offen lds
s_add_u32 m0,m0,1024
s_add_u32 s69,s69,s53
buffer_load_dwordx4 v6,s[44:47],s69 offen lds
RA18_%=:
ds_read_b64_tr_b16 v[70:71],v2 offset:14368
ds_read_b128 v[116:119],v4 offset:1056
v_fma_mixlo_f16 v120,v156,s99,0
v_fma_mixlo_f16 v122,v157,s99,0
v_mfma_f32_16x16x32_bf16 v[176:179],v[96:99],v[128:131],v[176:179]
s_waitcnt lgkmcnt(8)
ds_read_u16_d16_hi v146,v146
v_lshlrev_b32_sdwa v148,1,v148 dst_sel:DWORD dst_unused:UNUSED_PAD src0_sel:DWORD src1_sel:WORD_1
v_lshlrev_b32_sdwa v150,1,v150 dst_sel:DWORD dst_unused:UNUSED_PAD src0_sel:DWORD src1_sel:WORD_1
v_mfma_f32_16x16x32_bf16 v[184:187],v[100:103],v[128:131],v[184:187]
s_waitcnt lgkmcnt(8)
ds_read_b64_tr_b16 v[72:73],v1 offset:16416
ds_read_b64_tr_b16 v[74:75],v1 offset:18464
v_lshl_add_u64 v[136:137],v[136:137],0,v[138:139]
v_mfma_f32_16x16x32_bf16 v[192:195],v[104:107],v[128:131],v[192:195]
ds_read_u16 v145,v145
v_cvt_pk_bf16_f32 v138,v140,v142
v_cvt_pk_bf16_f32 v139,v141,v143
v_mfma_f32_16x16x32_bf16 v[200:203],v[108:111],v[128:131],v[200:203]
ds_read_b64_tr_b16 v[76:77],v2 offset:16416
ds_read_b64_tr_b16 v[78:79],v2 offset:18464
v_fma_mixhi_f16 v120,v158,s99,0
v_fma_mixhi_f16 v122,v159,s99,0
v_mfma_f32_16x16x32_bf16 v[164:167],v[88:91],v[136:139],v[164:167]
ds_read_u16_d16_hi v147,v147
v_lshlrev_b32_sdwa v149,1,v149 dst_sel:DWORD dst_unused:UNUSED_PAD src0_sel:DWORD src1_sel:WORD_1
v_lshlrev_b32_sdwa v151,1,v151 dst_sel:DWORD dst_unused:UNUSED_PAD src0_sel:DWORD src1_sel:WORD_1
v_mfma_f32_16x16x32_bf16 v[172:175],v[92:95],v[136:139],v[172:175]
ds_read_b64_tr_b16 v[80:81],v1 offset:20512
ds_read_b64_tr_b16 v[82:83],v1 offset:22560
v_pk_fma_f16 v121,v120,v120,s54
v_pk_fma_f16 v123,v122,v122,s54
v_mfma_f32_16x16x32_bf16 v[180:183],v[96:99],v[136:139],v[180:183]
ds_read_u16 v148,v148
v_pk_mul_f16 v121,v120,v121
v_pk_mul_f16 v123,v122,v123
v_mfma_f32_16x16x32_bf16 v[188:191],v[100:103],v[136:139],v[188:191]
ds_read_b64_tr_b16 v[84:85],v2 offset:20512
v_lshlrev_b32_sdwa v152,1,v152 dst_sel:DWORD dst_unused:UNUSED_PAD src0_sel:DWORD src1_sel:WORD_1
v_lshlrev_b32_sdwa v154,1,v154 dst_sel:DWORD dst_unused:UNUSED_PAD src0_sel:DWORD src1_sel:WORD_1
v_mfma_f32_16x16x32_bf16 v[196:199],v[104:107],v[136:139],v[196:199]
ds_read_u16_d16_hi v150,v150
v_exp_f16 v120,v121
v_mfma_f32_16x16x32_bf16 v[204:207],v[108:111],v[136:139],v[204:207]
s_waitcnt lgkmcnt(15)
ds_read_b64_tr_b16 v[86:87],v2 offset:22560
v_exp_f16 v122,v123
v_mfma_f32_16x16x32_bf16 v[128:131],v[64:67],v[16:19],v[112:115]
ds_read_u16 v149,v149
v_lshlrev_b32_sdwa v153,1,v153 dst_sel:DWORD dst_unused:UNUSED_PAD src0_sel:DWORD src1_sel:WORD_1
v_lshlrev_b32_sdwa v155,1,v155 dst_sel:DWORD dst_unused:UNUSED_PAD src0_sel:DWORD src1_sel:WORD_1
v_mfma_f32_16x16x32_bf16 v[136:139],v[64:67],v[20:23],v[112:115]
s_waitcnt lgkmcnt(14)
v_exp_f16_sdwa v120,v121 dst_sel:WORD_1 dst_unused:UNUSED_PRESERVE src0_sel:WORD_1
v_mfma_f32_16x16x32_bf16 v[140:143],v[68:71],v[20:23],v[116:119]
ds_read_u16_d16_hi v151,v151
v_exp_f16_sdwa v122,v123 dst_sel:WORD_1 dst_unused:UNUSED_PRESERVE src0_sel:WORD_1
v_mfma_f32_16x16x32_bf16 v[132:135],v[68:71],v[16:19],v[116:119]
s_waitcnt lgkmcnt(12)
v_pk_fma_f16 v120,v120,s55,s55
v_pk_fma_f16 v122,v122,s55,s55
v_mfma_f32_16x16x32_bf16 v[128:131],v[72:75],v[24:27],v[128:131]
ds_read_u16 v152,v152
v_rcp_f16 v121,v120
v_mfma_f32_16x16x32_bf16 v[136:139],v[72:75],v[28:31],v[136:139]
s_waitcnt lgkmcnt(10)
v_rcp_f16 v123,v122
v_mfma_f32_16x16x32_bf16 v[140:143],v[76:79],v[28:31],v[140:143]
ds_read_u16_d16_hi v154,v154
v_rcp_f16_sdwa v121,v120 dst_sel:WORD_1 dst_unused:UNUSED_PRESERVE src0_sel:WORD_1
v_mfma_f32_16x16x32_bf16 v[132:135],v[76:79],v[24:27],v[132:135]
s_waitcnt lgkmcnt(8)
v_rcp_f16_sdwa v123,v122 dst_sel:WORD_1 dst_unused:UNUSED_PRESERVE src0_sel:WORD_1
v_mfma_f32_16x16x32_bf16 v[128:131],v[80:83],v[32:35],v[128:131]
ds_read_u16 v153,v153
v_fma_mix_f32 v156,v156,v121,0 op_sel_hi:[0,1,0]
v_fma_mix_f32 v158,v158,v121,0 op_sel:[0,1,0] op_sel_hi:[0,1,0]
v_mfma_f32_16x16x32_bf16 v[136:139],v[80:83],v[36:39],v[136:139]
s_waitcnt lgkmcnt(3)
v_lshl_add_u64 v[144:145],v[144:145],0,v[146:147]
v_lshl_add_u64 v[146:147],v[148:149],0,v[150:151]
v_mfma_f32_16x16x32_bf16 v[140:143],v[84:87],v[36:39],v[140:143]
ds_read_u16_d16_hi v155,v155
v_fma_mix_f32 v157,v157,v123,0 op_sel_hi:[0,1,0]
v_fma_mix_f32 v159,v159,v123,0 op_sel:[0,1,0] op_sel_hi:[0,1,0]
v_mfma_f32_16x16x32_bf16 v[132:135],v[84:87],v[32:35],v[132:135]
v_lshlrev_b32_sdwa v128,1,v128 dst_sel:DWORD dst_unused:UNUSED_PAD src0_sel:DWORD src1_sel:WORD_1
v_lshlrev_b32_sdwa v130,1,v130 dst_sel:DWORD dst_unused:UNUSED_PAD src0_sel:DWORD src1_sel:WORD_1
v_mfma_f32_16x16x32_bf16 v[208:211],v[88:91],v[144:147],v[208:211]
ds_read_u16 v128,v128
v_lshlrev_b32_sdwa v129,1,v129 dst_sel:DWORD dst_unused:UNUSED_PAD src0_sel:DWORD src1_sel:WORD_1
v_lshlrev_b32_sdwa v131,1,v131 dst_sel:DWORD dst_unused:UNUSED_PAD src0_sel:DWORD src1_sel:WORD_1
v_mfma_f32_16x16x32_bf16 v[216:219],v[92:95],v[144:147],v[216:219]
v_fma_mixlo_f16 v120,v140,s99,0
v_fma_mixlo_f16 v122,v141,s99,0
v_mfma_f32_16x16x32_bf16 v[224:227],v[96:99],v[144:147],v[224:227]
ds_read_u16_d16_hi v130,v130
v_lshlrev_b32_sdwa v132,1,v132 dst_sel:DWORD dst_unused:UNUSED_PAD src0_sel:DWORD src1_sel:WORD_1
v_lshlrev_b32_sdwa v134,1,v134 dst_sel:DWORD dst_unused:UNUSED_PAD src0_sel:DWORD src1_sel:WORD_1
v_mfma_f32_16x16x32_bf16 v[232:235],v[100:103],v[144:147],v[232:235]
s_waitcnt lgkmcnt(2)
v_lshl_add_u64 v[152:153],v[152:153],0,v[154:155]
v_mfma_f32_16x16x32_bf16 v[240:243],v[104:107],v[144:147],v[240:243]
ds_read_u16 v129,v129
v_cvt_pk_bf16_f32 v154,v156,v158
v_cvt_pk_bf16_f32 v155,v157,v159
v_mfma_f32_16x16x32_bf16 v[248:251],v[108:111],v[144:147],v[248:251]
v_fma_mixhi_f16 v120,v142,s99,0
v_fma_mixhi_f16 v122,v143,s99,0
v_mfma_f32_16x16x32_bf16 v[212:215],v[88:91],v[152:155],v[212:215]
ds_read_u16_d16_hi v131,v131
v_lshlrev_b32_sdwa v133,1,v133 dst_sel:DWORD dst_unused:UNUSED_PAD src0_sel:DWORD src1_sel:WORD_1
v_lshlrev_b32_sdwa v135,1,v135 dst_sel:DWORD dst_unused:UNUSED_PAD src0_sel:DWORD src1_sel:WORD_1
v_mfma_f32_16x16x32_bf16 v[220:223],v[92:95],v[152:155],v[220:223]
s_setprio 2
ds_read_b64_tr_b16 v[88:89],v3 offset:18480
v_pk_fma_f16 v121,v120,v120,s54
v_pk_fma_f16 v123,v122,v122,s54
v_mfma_f32_16x16x32_bf16 v[228:231],v[96:99],v[152:155],v[228:231]
ds_read_u16 v132,v132
v_pk_mul_f16 v121,v120,v121
v_pk_mul_f16 v123,v122,v123
v_mfma_f32_16x16x32_bf16 v[236:239],v[100:103],v[152:155],v[236:239]
ds_read_b64_tr_b16 v[90:91],v3 offset:20016
v_lshlrev_b32_sdwa v136,1,v136 dst_sel:DWORD dst_unused:UNUSED_PAD src0_sel:DWORD src1_sel:WORD_1
v_lshlrev_b32_sdwa v138,1,v138 dst_sel:DWORD dst_unused:UNUSED_PAD src0_sel:DWORD src1_sel:WORD_1
v_mfma_f32_16x16x32_bf16 v[244:247],v[104:107],v[152:155],v[244:247]
ds_read_u16_d16_hi v134,v134
v_exp_f16 v120,v121
v_mfma_f32_16x16x32_bf16 v[252:255],v[108:111],v[152:155],v[252:255]
ds_read_b64_tr_b16 v[92:93],v3 offset:18488
v_exp_f16 v122,v123
v_mfma_f32_16x16x32_bf16 v[144:147],v[64:67],v[40:43],v[112:115]
ds_read_u16 v133,v133
v_lshlrev_b32_sdwa v137,1,v137 dst_sel:DWORD dst_unused:UNUSED_PAD src0_sel:DWORD src1_sel:WORD_1
v_lshlrev_b32_sdwa v139,1,v139 dst_sel:DWORD dst_unused:UNUSED_PAD src0_sel:DWORD src1_sel:WORD_1
v_mfma_f32_16x16x32_bf16 v[152:155],v[64:67],v[44:47],v[112:115]
ds_read_b64_tr_b16 v[94:95],v3 offset:20024
v_exp_f16_sdwa v120,v121 dst_sel:WORD_1 dst_unused:UNUSED_PRESERVE src0_sel:WORD_1
v_mfma_f32_16x16x32_bf16 v[156:159],v[68:71],v[44:47],v[116:119]
ds_read_u16_d16_hi v135,v135
v_exp_f16_sdwa v122,v123 dst_sel:WORD_1 dst_unused:UNUSED_PRESERVE src0_sel:WORD_1
v_mfma_f32_16x16x32_bf16 v[148:151],v[68:71],v[40:43],v[116:119]
ds_read_b64_tr_b16 v[96:97],v3 offset:18544
v_pk_fma_f16 v120,v120,s55,s55
v_pk_fma_f16 v122,v122,s55,s55
v_mfma_f32_16x16x32_bf16 v[144:147],v[72:75],v[48:51],v[144:147]
ds_read_u16 v136,v136
v_rcp_f16 v121,v120
v_mfma_f32_16x16x32_bf16 v[152:155],v[72:75],v[52:55],v[152:155]
ds_read_b64_tr_b16 v[98:99],v3 offset:20080
ds_read_b64_tr_b16 v[100:101],v3 offset:18552
v_rcp_f16 v123,v122
v_mfma_f32_16x16x32_bf16 v[156:159],v[76:79],v[52:55],v[156:159]
ds_read_u16_d16_hi v138,v138
v_rcp_f16_sdwa v121,v120 dst_sel:WORD_1 dst_unused:UNUSED_PRESERVE src0_sel:WORD_1
v_mfma_f32_16x16x32_bf16 v[148:151],v[76:79],v[48:51],v[148:151]
ds_read_b64_tr_b16 v[102:103],v3 offset:20088
ds_read_b64_tr_b16 v[104:105],v3 offset:18608
v_rcp_f16_sdwa v123,v122 dst_sel:WORD_1 dst_unused:UNUSED_PRESERVE src0_sel:WORD_1
v_mfma_f32_16x16x32_bf16 v[144:147],v[80:83],v[56:59],v[144:147]
ds_read_u16 v137,v137
v_fma_mix_f32 v140,v140,v121,0 op_sel_hi:[0,1,0]
v_fma_mix_f32 v142,v142,v121,0 op_sel:[0,1,0] op_sel_hi:[0,1,0]
v_mfma_f32_16x16x32_bf16 v[152:155],v[80:83],v[60:63],v[152:155]
s_waitcnt lgkmcnt(8)
ds_read_b64_tr_b16 v[106:107],v3 offset:20144
ds_read_b64_tr_b16 v[108:109],v3 offset:18616
v_lshl_add_u64 v[128:129],v[128:129],0,v[130:131]
v_lshl_add_u64 v[130:131],v[132:133],0,v[134:135]
v_mfma_f32_16x16x32_bf16 v[156:159],v[84:87],v[60:63],v[156:159]
ds_read_u16_d16_hi v139,v139
v_fma_mix_f32 v141,v141,v123,0 op_sel_hi:[0,1,0]
v_fma_mix_f32 v143,v143,v123,0 op_sel:[0,1,0] op_sel_hi:[0,1,0]
v_mfma_f32_16x16x32_bf16 v[148:151],v[84:87],v[56:59],v[148:151]
ds_read_b64_tr_b16 v[110:111],v3 offset:20152
ds_read_b64_tr_b16 v[64:65],v1 offset:12832
v_lshlrev_b32_sdwa v144,1,v144 dst_sel:DWORD dst_unused:UNUSED_PAD src0_sel:DWORD src1_sel:WORD_1
v_lshlrev_b32_sdwa v146,1,v146 dst_sel:DWORD dst_unused:UNUSED_PAD src0_sel:DWORD src1_sel:WORD_1
v_mfma_f32_16x16x32_bf16 v[160:163],v[88:91],v[128:131],v[160:163]
ds_read_u16 v144,v144
v_lshlrev_b32_sdwa v145,1,v145 dst_sel:DWORD dst_unused:UNUSED_PAD src0_sel:DWORD src1_sel:WORD_1
v_lshlrev_b32_sdwa v147,1,v147 dst_sel:DWORD dst_unused:UNUSED_PAD src0_sel:DWORD src1_sel:WORD_1
v_mfma_f32_16x16x32_bf16 v[168:171],v[92:95],v[128:131],v[168:171]
s_waitcnt lgkmcnt(11)
ds_read_b64_tr_b16 v[66:67],v1 offset:14880
ds_read_b128 v[112:115],v4 offset:1152
v_fma_mixlo_f16 v120,v156,s99,0
v_fma_mixlo_f16 v122,v157,s99,0
v_mfma_f32_16x16x32_bf16 v[176:179],v[96:99],v[128:131],v[176:179]
s_waitcnt lgkmcnt(10)
ds_read_u16_d16_hi v146,v146
v_lshlrev_b32_sdwa v148,1,v148 dst_sel:DWORD dst_unused:UNUSED_PAD src0_sel:DWORD src1_sel:WORD_1
v_lshlrev_b32_sdwa v150,1,v150 dst_sel:DWORD dst_unused:UNUSED_PAD src0_sel:DWORD src1_sel:WORD_1
v_mfma_f32_16x16x32_bf16 v[184:187],v[100:103],v[128:131],v[184:187]
s_waitcnt lgkmcnt(6)
ds_read_b64_tr_b16 v[68:69],v2 offset:12832
ds_read_b64_tr_b16 v[70:71],v2 offset:14880
v_lshl_add_u64 v[136:137],v[136:137],0,v[138:139]
v_mfma_f32_16x16x32_bf16 v[192:195],v[104:107],v[128:131],v[192:195]
s_waitcnt lgkmcnt(7)
ds_read_u16 v145,v145
v_cvt_pk_bf16_f32 v138,v140,v142
v_cvt_pk_bf16_f32 v139,v141,v143
v_mfma_f32_16x16x32_bf16 v[200:203],v[108:111],v[128:131],v[200:203]
ds_read_b128 v[116:119],v4 offset:1184
ds_read_b64_tr_b16 v[72:73],v1 offset:16928
v_fma_mixhi_f16 v120,v158,s99,0
v_fma_mixhi_f16 v122,v159,s99,0
v_mfma_f32_16x16x32_bf16 v[164:167],v[88:91],v[136:139],v[164:167]
ds_read_u16_d16_hi v147,v147
v_lshlrev_b32_sdwa v149,1,v149 dst_sel:DWORD dst_unused:UNUSED_PAD src0_sel:DWORD src1_sel:WORD_1
v_lshlrev_b32_sdwa v151,1,v151 dst_sel:DWORD dst_unused:UNUSED_PAD src0_sel:DWORD src1_sel:WORD_1
v_mfma_f32_16x16x32_bf16 v[172:175],v[92:95],v[136:139],v[172:175]
ds_read_b64_tr_b16 v[74:75],v1 offset:18976
ds_read_b64_tr_b16 v[76:77],v2 offset:16928
v_pk_fma_f16 v121,v120,v120,s54
v_pk_fma_f16 v123,v122,v122,s54
v_mfma_f32_16x16x32_bf16 v[180:183],v[96:99],v[136:139],v[180:183]
s_setprio 1
ds_read_u16 v148,v148
v_pk_mul_f16 v121,v120,v121
v_pk_mul_f16 v123,v122,v123
v_mfma_f32_16x16x32_bf16 v[188:191],v[100:103],v[136:139],v[188:191]
ds_read_b64_tr_b16 v[78:79],v2 offset:18976
v_lshlrev_b32_sdwa v152,1,v152 dst_sel:DWORD dst_unused:UNUSED_PAD src0_sel:DWORD src1_sel:WORD_1
v_lshlrev_b32_sdwa v154,1,v154 dst_sel:DWORD dst_unused:UNUSED_PAD src0_sel:DWORD src1_sel:WORD_1
v_mfma_f32_16x16x32_bf16 v[196:199],v[104:107],v[136:139],v[196:199]
ds_read_u16_d16_hi v150,v150
v_exp_f16 v120,v121
v_mfma_f32_16x16x32_bf16 v[204:207],v[108:111],v[136:139],v[204:207]
s_waitcnt lgkmcnt(12)
ds_read_b64_tr_b16 v[80:81],v1 offset:21024
v_exp_f16 v122,v123
v_mfma_f32_16x16x32_bf16 v[128:131],v[64:67],v[16:19],v[112:115]
ds_read_u16 v149,v149
v_lshlrev_b32_sdwa v153,1,v153 dst_sel:DWORD dst_unused:UNUSED_PAD src0_sel:DWORD src1_sel:WORD_1
v_lshlrev_b32_sdwa v155,1,v155 dst_sel:DWORD dst_unused:UNUSED_PAD src0_sel:DWORD src1_sel:WORD_1
v_mfma_f32_16x16x32_bf16 v[136:139],v[64:67],v[20:23],v[112:115]
s_waitcnt lgkmcnt(9)
ds_read_b64_tr_b16 v[82:83],v1 offset:23072
v_exp_f16_sdwa v120,v121 dst_sel:WORD_1 dst_unused:UNUSED_PRESERVE src0_sel:WORD_1
v_mfma_f32_16x16x32_bf16 v[140:143],v[68:71],v[20:23],v[116:119]
ds_read_u16_d16_hi v151,v151
v_exp_f16_sdwa v122,v123 dst_sel:WORD_1 dst_unused:UNUSED_PRESERVE src0_sel:WORD_1
v_mfma_f32_16x16x32_bf16 v[132:135],v[68:71],v[16:19],v[116:119]
s_waitcnt lgkmcnt(8)
ds_read_b64_tr_b16 v[84:85],v2 offset:21024
v_pk_fma_f16 v120,v120,s55,s55
v_pk_fma_f16 v122,v122,s55,s55
v_mfma_f32_16x16x32_bf16 v[128:131],v[72:75],v[24:27],v[128:131]
ds_read_u16 v152,v152
v_rcp_f16 v121,v120
v_mfma_f32_16x16x32_bf16 v[136:139],v[72:75],v[28:31],v[136:139]
s_waitcnt lgkmcnt(7)
ds_read_b64_tr_b16 v[86:87],v2 offset:23072
v_rcp_f16 v123,v122
v_mfma_f32_16x16x32_bf16 v[140:143],v[76:79],v[28:31],v[140:143]
ds_read_u16_d16_hi v154,v154
v_rcp_f16_sdwa v121,v120 dst_sel:WORD_1 dst_unused:UNUSED_PRESERVE src0_sel:WORD_1
v_mfma_f32_16x16x32_bf16 v[132:135],v[76:79],v[24:27],v[132:135]
s_waitcnt lgkmcnt(5)
v_rcp_f16_sdwa v123,v122 dst_sel:WORD_1 dst_unused:UNUSED_PRESERVE src0_sel:WORD_1
v_mfma_f32_16x16x32_bf16 v[128:131],v[80:83],v[32:35],v[128:131]
ds_read_u16 v153,v153
v_fma_mix_f32 v156,v156,v121,0 op_sel_hi:[0,1,0]
v_fma_mix_f32 v158,v158,v121,0 op_sel:[0,1,0] op_sel_hi:[0,1,0]
v_mfma_f32_16x16x32_bf16 v[136:139],v[80:83],v[36:39],v[136:139]
s_waitcnt lgkmcnt(2)
s_setprio 0
v_lshl_add_u64 v[144:145],v[144:145],0,v[146:147]
v_lshl_add_u64 v[146:147],v[148:149],0,v[150:151]
v_mfma_f32_16x16x32_bf16 v[140:143],v[84:87],v[36:39],v[140:143]
ds_read_u16_d16_hi v155,v155
v_fma_mix_f32 v157,v157,v123,0 op_sel_hi:[0,1,0]
v_fma_mix_f32 v159,v159,v123,0 op_sel:[0,1,0] op_sel_hi:[0,1,0]
v_mfma_f32_16x16x32_bf16 v[132:135],v[84:87],v[32:35],v[132:135]
v_lshlrev_b32_sdwa v128,1,v128 dst_sel:DWORD dst_unused:UNUSED_PAD src0_sel:DWORD src1_sel:WORD_1
v_lshlrev_b32_sdwa v130,1,v130 dst_sel:DWORD dst_unused:UNUSED_PAD src0_sel:DWORD src1_sel:WORD_1
v_mfma_f32_16x16x32_bf16 v[208:211],v[88:91],v[144:147],v[208:211]
s_cbranch_vccz RA19_%=
s_waitcnt vmcnt(0)
RA19_%=:
s_barrier
s_setprio 2
ds_read_u16 v128,v128
v_lshlrev_b32_sdwa v129,1,v129 dst_sel:DWORD dst_unused:UNUSED_PAD src0_sel:DWORD src1_sel:WORD_1
v_mfma_f32_16x16x32_bf16 v[216:219],v[92:95],v[144:147],v[216:219]
ds_read_u16_d16_hi v130,v130
v_lshlrev_b32_sdwa v131,1,v131 dst_sel:DWORD dst_unused:UNUSED_PAD src0_sel:DWORD src1_sel:WORD_1
v_mfma_f32_16x16x32_bf16 v[224:227],v[96:99],v[144:147],v[224:227]
s_cbranch_vccz RA20_%=
s_add_u32 m0,s58,12320
s_mov_b32 s69,s60
buffer_load_dwordx4 v5,s[44:47],s69 offen lds
s_add_u32 m0,m0,1024
s_add_u32 s69,s69,s53
buffer_load_dwordx4 v6,s[44:47],s69 offen lds
RA20_%=:
v_lshlrev_b32_sdwa v132,1,v132 dst_sel:DWORD dst_unused:UNUSED_PAD src0_sel:DWORD src1_sel:WORD_1
v_mfma_f32_16x16x32_bf16 v[232:235],v[100:103],v[144:147],v[232:235]
s_waitcnt lgkmcnt(2)
ds_read_u16 v129,v129
v_lshl_add_u64 v[152:153],v[152:153],0,v[154:155]
v_lshlrev_b32_sdwa v134,1,v134 dst_sel:DWORD dst_unused:UNUSED_PAD src0_sel:DWORD src1_sel:WORD_1
v_mfma_f32_16x16x32_bf16 v[240:243],v[104:107],v[144:147],v[240:243]
ds_read_u16_d16_hi v131,v131
v_cvt_pk_bf16_f32 v154,v156,v158
v_cvt_pk_bf16_f32 v155,v157,v159
v_mfma_f32_16x16x32_bf16 v[248:251],v[108:111],v[144:147],v[248:251]
v_lshlrev_b32_sdwa v133,1,v133 dst_sel:DWORD dst_unused:UNUSED_PAD src0_sel:DWORD src1_sel:WORD_1
v_mfma_f32_16x16x32_bf16 v[212:215],v[88:91],v[152:155],v[212:215]
s_cbranch_vccz RA21_%=
s_add_u32 m0,m0,1024
s_add_u32 s69,s69,s53
buffer_load_dwordx4 v5,s[44:47],s69 offen lds
s_add_u32 m0,m0,s50
s_add_u32 s69,s69,s53
buffer_load_dwordx4 v6,s[44:47],s69 offen lds
RA21_%=:
ds_read_u16 v132,v132
v_lshlrev_b32_sdwa v135,1,v135 dst_sel:DWORD dst_unused:UNUSED_PAD src0_sel:DWORD src1_sel:WORD_1
v_mfma_f32_16x16x32_bf16 v[220:223],v[92:95],v[152:155],v[220:223]
ds_read_u16_d16_hi v134,v134
v_lshlrev_b32_sdwa v136,1,v136 dst_sel:DWORD dst_unused:UNUSED_PAD src0_sel:DWORD src1_sel:WORD_1
v_mfma_f32_16x16x32_bf16 v[228:231],v[96:99],v[152:155],v[228:231]
ds_read_b64_tr_b16 v[88:89],v3 offset:0
ds_read_b64_tr_b16 v[90:91],v3 offset:1536
v_lshlrev_b32_sdwa v138,1,v138 dst_sel:DWORD dst_unused:UNUSED_PAD src0_sel:DWORD src1_sel:WORD_1
v_mfma_f32_16x16x32_bf16 v[236:239],v[100:103],v[152:155],v[236:239]
ds_read_u16 v133,v133
v_lshlrev_b32_sdwa v137,1,v137 dst_sel:DWORD dst_unused:UNUSED_PAD src0_sel:DWORD src1_sel:WORD_1
v_mfma_f32_16x16x32_bf16 v[244:247],v[104:107],v[152:155],v[244:247]
ds_read_u16_d16_hi v135,v135
v_lshlrev_b32_sdwa v139,1,v139 dst_sel:DWORD dst_unused:UNUSED_PAD src0_sel:DWORD src1_sel:WORD_1
v_mfma_f32_16x16x32_bf16 v[252:255],v[108:111],v[152:155],v[252:255]
s_cbranch_vccz RA22_%=
s_add_u32 m0,m0,1024
s_add_u32 s69,s69,s53
buffer_load_dwordx4 v5,s[44:47],s69 offen lds
s_add_u32 m0,m0,1024
s_add_u32 s69,s69,s53
buffer_load_dwordx4 v6,s[44:47],s69 offen lds
RA22_%=:
ds_read_b64_tr_b16 v[92:93],v3 offset:8
ds_read_b64_tr_b16 v[94:95],v3 offset:1544
v_lshlrev_b32_sdwa v140,1,v140 dst_sel:DWORD dst_unused:UNUSED_PAD src0_sel:DWORD src1_sel:WORD_1
v_mfma_f32_16x16x32_bf16 v[144:147],v[64:67],v[40:43],v[112:115]
ds_read_u16 v136,v136
v_lshlrev_b32_sdwa v142,1,v142 dst_sel:DWORD dst_unused:UNUSED_PAD src0_sel:DWORD src1_sel:WORD_1
v_mfma_f32_16x16x32_bf16 v[152:155],v[64:67],v[44:47],v[112:115]
ds_read_u16_d16_hi v138,v138
v_lshlrev_b32_sdwa v141,1,v141 dst_sel:DWORD dst_unused:UNUSED_PAD src0_sel:DWORD src1_sel:WORD_1
v_mfma_f32_16x16x32_bf16 v[156:159],v[68:71],v[44:47],v[116:119]
ds_read_b64_tr_b16 v[96:97],v3 offset:64
ds_read_b64_tr_b16 v[98:99],v3 offset:1600
v_lshlrev_b32_sdwa v143,1,v143 dst_sel:DWORD dst_unused:UNUSED_PAD src0_sel:DWORD src1_sel:WORD_1
v_mfma_f32_16x16x32_bf16 v[148:151],v[68:71],v[40:43],v[116:119]
ds_read_u16 v137,v137
v_mfma_f32_16x16x32_bf16 v[144:147],v[72:75],v[48:51],v[144:147]
ds_read_u16_d16_hi v139,v139
v_mfma_f32_16x16x32_bf16 v[152:155],v[72:75],v[52:55],v[152:155]
s_cbranch_vccz RA23_%=
s_add_u32 m0,m0,s50
s_add_u32 s69,s69,s51
buffer_load_dwordx4 v5,s[44:47],s69 offen lds
s_add_u32 m0,m0,1024
s_add_u32 s69,s69,s53
buffer_load_dwordx4 v6,s[44:47],s69 offen lds
RA23_%=:
ds_read_b64_tr_b16 v[100:101],v3 offset:72
ds_read_b64_tr_b16 v[102:103],v3 offset:1608
v_mfma_f32_16x16x32_bf16 v[156:159],v[76:79],v[52:55],v[156:159]
ds_read_u16 v140,v140
v_mfma_f32_16x16x32_bf16 v[148:151],v[76:79],v[48:51],v[148:151]
s_waitcnt lgkmcnt(15)
ds_read_u16_d16_hi v142,v142
v_lshl_add_u64 v[128:129],v[128:129],0,v[130:131]
v_mfma_f32_16x16x32_bf16 v[144:147],v[80:83],v[56:59],v[144:147]
s_waitcnt lgkmcnt(12)
ds_read_b64_tr_b16 v[104:105],v3 offset:128
ds_read_b64_tr_b16 v[106:107],v3 offset:1664
v_lshl_add_u64 v[130:131],v[132:133],0,v[134:135]
v_mfma_f32_16x16x32_bf16 v[152:155],v[80:83],v[60:63],v[152:155]
ds_read_u16 v141,v141
v_mfma_f32_16x16x32_bf16 v[156:159],v[84:87],v[60:63],v[156:159]
ds_read_u16_d16_hi v143,v143
v_mfma_f32_16x16x32_bf16 v[148:151],v[84:87],v[56:59],v[148:151]
s_cbranch_vccz RA24_%=
s_add_u32 m0,m0,1024
s_add_u32 s69,s69,s53
buffer_load_dwordx4 v5,s[44:47],s69 offen lds
s_add_u32 m0,m0,s50
s_add_u32 s69,s69,s53
buffer_load_dwordx4 v6,s[44:47],s69 offen lds
RA24_%=:
ds_read_b64_tr_b16 v[108:109],v3 offset:136
ds_read_b64_tr_b16 v[110:111],v3 offset:1672
v_lshlrev_b32_sdwa v144,1,v144 dst_sel:DWORD dst_unused:UNUSED_PAD src0_sel:DWORD src1_sel:WORD_1
v_lshlrev_b32_sdwa v146,1,v146 dst_sel:DWORD dst_unused:UNUSED_PAD src0_sel:DWORD src1_sel:WORD_1
v_mfma_f32_16x16x32_bf16 v[160:163],v[88:91],v[128:131],v[160:163]
s_waitcnt lgkmcnt(15)
ds_read_u16 v144,v144
v_lshlrev_b32_sdwa v145,1,v145 dst_sel:DWORD dst_unused:UNUSED_PAD src0_sel:DWORD src1_sel:WORD_1
v_mfma_f32_16x16x32_bf16 v[168:171],v[92:95],v[128:131],v[168:171]
s_waitcnt lgkmcnt(13)
ds_read_u16_d16_hi v146,v146
v_lshlrev_b32_sdwa v147,1,v147 dst_sel:DWORD dst_unused:UNUSED_PAD src0_sel:DWORD src1_sel:WORD_1
v_mfma_f32_16x16x32_bf16 v[176:179],v[96:99],v[128:131],v[176:179]
s_waitcnt lgkmcnt(10)
ds_read_b64_tr_b16 v[64:65],v1 offset:0
ds_read_b64_tr_b16 v[66:67],v1 offset:2048
v_lshlrev_b32_sdwa v148,1,v148 dst_sel:DWORD dst_unused:UNUSED_PAD src0_sel:DWORD src1_sel:WORD_1
v_mfma_f32_16x16x32_bf16 v[184:187],v[100:103],v[128:131],v[184:187]
s_waitcnt lgkmcnt(8)
ds_read_u16 v145,v145
v_lshl_add_u64 v[136:137],v[136:137],0,v[138:139]
v_lshlrev_b32_sdwa v150,1,v150 dst_sel:DWORD dst_unused:UNUSED_PAD src0_sel:DWORD src1_sel:WORD_1
v_mfma_f32_16x16x32_bf16 v[192:195],v[104:107],v[128:131],v[192:195]
s_waitcnt lgkmcnt(5)
s_cbranch_vccz RA25_%=
s_add_u32 m0,m0,1024
s_add_u32 s69,s69,s53
buffer_load_dwordx4 v5,s[44:47],s69 offen lds
s_add_u32 m0,m0,1024
s_add_u32 s69,s69,s53
buffer_load_dwordx4 v6,s[44:47],s69 offen lds
RA25_%=:
ds_read_u16_d16_hi v147,v147
v_lshl_add_u64 v[138:139],v[140:141],0,v[142:143]
v_mfma_f32_16x16x32_bf16 v[200:203],v[108:111],v[128:131],v[200:203]
ds_read_b128 v[112:115],v4 offset:1280
ds_read_b64_tr_b16 v[68:69],v2 offset:0
v_lshlrev_b32_sdwa v149,1,v149 dst_sel:DWORD dst_unused:UNUSED_PAD src0_sel:DWORD src1_sel:WORD_1
v_mfma_f32_16x16x32_bf16 v[164:167],v[88:91],v[136:139],v[164:167]
ds_read_u16 v148,v148
v_lshlrev_b32_sdwa v151,1,v151 dst_sel:DWORD dst_unused:UNUSED_PAD src0_sel:DWORD src1_sel:WORD_1
v_mfma_f32_16x16x32_bf16 v[172:175],v[92:95],v[136:139],v[172:175]
ds_read_u16_d16_hi v150,v150
ds_read_b64_tr_b16 v[70:71],v2 offset:2048
v_lshlrev_b32_sdwa v152,1,v152 dst_sel:DWORD dst_unused:UNUSED_PAD src0_sel:DWORD src1_sel:WORD_1
v_mfma_f32_16x16x32_bf16 v[180:183],v[96:99],v[136:139],v[180:183]
ds_read_b128 v[116:119],v4 offset:1312
ds_read_b64_tr_b16 v[72:73],v1 offset:4096
v_lshlrev_b32_sdwa v154,1,v154 dst_sel:DWORD dst_unused:UNUSED_PAD src0_sel:DWORD src1_sel:WORD_1
v_mfma_f32_16x16x32_bf16 v[188:191],v[100:103],v[136:139],v[188:191]
ds_read_u16 v149,v149
v_lshlrev_b32_sdwa v153,1,v153 dst_sel:DWORD dst_unused:UNUSED_PAD src0_sel:DWORD src1_sel:WORD_1
v_mfma_f32_16x16x32_bf16 v[196:199],v[104:107],v[136:139],v[196:199]
ds_read_u16_d16_hi v151,v151
v_lshlrev_b32_sdwa v155,1,v155 dst_sel:DWORD dst_unused:UNUSED_PAD src0_sel:DWORD src1_sel:WORD_1
v_mfma_f32_16x16x32_bf16 v[204:207],v[108:111],v[136:139],v[204:207]
s_waitcnt lgkmcnt(8)
ds_read_b64_tr_b16 v[74:75],v1 offset:6144
ds_read_b64_tr_b16 v[76:77],v2 offset:4096
v_lshlrev_b32_sdwa v156,1,v156 dst_sel:DWORD dst_unused:UNUSED_PAD src0_sel:DWORD src1_sel:WORD_1
v_mfma_f32_16x16x32_bf16 v[128:131],v[64:67],v[16:19],v[112:115]
ds_read_u16 v152,v152
v_lshlrev_b32_sdwa v158,1,v158 dst_sel:DWORD dst_unused:UNUSED_PAD src0_sel:DWORD src1_sel:WORD_1
v_mfma_f32_16x16x32_bf16 v[136:139],v[64:67],v[20:23],v[112:115]
s_waitcnt lgkmcnt(6)
ds_read_u16_d16_hi v154,v154
ds_read_b64_tr_b16 v[78:79],v2 offset:6144
v_lshlrev_b32_sdwa v157,1,v157 dst_sel:DWORD dst_unused:UNUSED_PAD src0_sel:DWORD src1_sel:WORD_1
v_mfma_f32_16x16x32_bf16 v[140:143],v[68:71],v[20:23],v[116:119]
ds_read_b64_tr_b16 v[80:81],v1 offset:8192
ds_read_b64_tr_b16 v[82:83],v1 offset:10240
v_lshlrev_b32_sdwa v159,1,v159 dst_sel:DWORD dst_unused:UNUSED_PAD src0_sel:DWORD src1_sel:WORD_1
v_mfma_f32_16x16x32_bf16 v[132:135],v[68:71],v[16:19],v[116:119]
s_waitcnt lgkmcnt(6)
ds_read_u16 v153,v153
v_mfma_f32_16x16x32_bf16 v[128:131],v[72:75],v[24:27],v[128:131]
ds_read_u16_d16_hi v155,v155
v_mfma_f32_16x16x32_bf16 v[136:139],v[72:75],v[28:31],v[136:139]
s_waitcnt lgkmcnt(4)
ds_read_b64_tr_b16 v[84:85],v2 offset:8192
ds_read_b64_tr_b16 v[86:87],v2 offset:10240
v_mfma_f32_16x16x32_bf16 v[140:143],v[76:79],v[28:31],v[140:143]
ds_read_u16 v156,v156
v_mfma_f32_16x16x32_bf16 v[132:135],v[76:79],v[24:27],v[132:135]
s_waitcnt lgkmcnt(5)
ds_read_u16_d16_hi v158,v158
v_lshl_add_u64 v[144:145],v[144:145],0,v[146:147]
v_mfma_f32_16x16x32_bf16 v[128:131],v[80:83],v[32:35],v[128:131]
v_lshl_add_u64 v[146:147],v[148:149],0,v[150:151]
v_mfma_f32_16x16x32_bf16 v[136:139],v[80:83],v[36:39],v[136:139]
s_waitcnt lgkmcnt(2)
ds_read_u16 v157,v157
v_mfma_f32_16x16x32_bf16 v[140:143],v[84:87],v[36:39],v[140:143]
ds_read_u16_d16_hi v159,v159
v_mfma_f32_16x16x32_bf16 v[132:135],v[84:87],v[32:35],v[132:135]
s_nop 0
v_lshlrev_b32_sdwa v128,1,v128 dst_sel:DWORD dst_unused:UNUSED_PAD src0_sel:DWORD src1_sel:WORD_1
v_lshlrev_b32_sdwa v130,1,v130 dst_sel:DWORD dst_unused:UNUSED_PAD src0_sel:DWORD src1_sel:WORD_1
v_mfma_f32_16x16x32_bf16 v[208:211],v[88:91],v[144:147],v[208:211]
ds_read_u16 v128,v128
v_lshlrev_b32_sdwa v129,1,v129 dst_sel:DWORD dst_unused:UNUSED_PAD src0_sel:DWORD src1_sel:WORD_1
v_mfma_f32_16x16x32_bf16 v[216:219],v[92:95],v[144:147],v[216:219]
ds_read_u16_d16_hi v130,v130
v_lshlrev_b32_sdwa v131,1,v131 dst_sel:DWORD dst_unused:UNUSED_PAD src0_sel:DWORD src1_sel:WORD_1
v_mfma_f32_16x16x32_bf16 v[224:227],v[96:99],v[144:147],v[224:227]
v_lshlrev_b32_sdwa v132,1,v132 dst_sel:DWORD dst_unused:UNUSED_PAD src0_sel:DWORD src1_sel:WORD_1
v_mfma_f32_16x16x32_bf16 v[232:235],v[100:103],v[144:147],v[232:235]
ds_read_u16 v129,v129
v_lshl_add_u64 v[152:153],v[152:153],0,v[154:155]
v_lshlrev_b32_sdwa v134,1,v134 dst_sel:DWORD dst_unused:UNUSED_PAD src0_sel:DWORD src1_sel:WORD_1
v_mfma_f32_16x16x32_bf16 v[240:243],v[104:107],v[144:147],v[240:243]
s_waitcnt lgkmcnt(3)
ds_read_u16_d16_hi v131,v131
v_lshl_add_u64 v[154:155],v[156:157],0,v[158:159]
v_mfma_f32_16x16x32_bf16 v[248:251],v[108:111],v[144:147],v[248:251]
v_lshlrev_b32_sdwa v133,1,v133 dst_sel:DWORD dst_unused:UNUSED_PAD src0_sel:DWORD src1_sel:WORD_1
v_mfma_f32_16x16x32_bf16 v[212:215],v[88:91],v[152:155],v[212:215]
ds_read_u16 v132,v132
v_lshlrev_b32_sdwa v135,1,v135 dst_sel:DWORD dst_unused:UNUSED_PAD src0_sel:DWORD src1_sel:WORD_1
v_mfma_f32_16x16x32_bf16 v[220:223],v[92:95],v[152:155],v[220:223]
ds_read_u16_d16_hi v134,v134
v_lshlrev_b32_sdwa v136,1,v136 dst_sel:DWORD dst_unused:UNUSED_PAD src0_sel:DWORD src1_sel:WORD_1
v_mfma_f32_16x16x32_bf16 v[228:231],v[96:99],v[152:155],v[228:231]
ds_read_b64_tr_b16 v[88:89],v3 offset:6160
ds_read_b64_tr_b16 v[90:91],v3 offset:7696
v_lshlrev_b32_sdwa v138,1,v138 dst_sel:DWORD dst_unused:UNUSED_PAD src0_sel:DWORD src1_sel:WORD_1
v_mfma_f32_16x16x32_bf16 v[236:239],v[100:103],v[152:155],v[236:239]
ds_read_u16 v133,v133
v_lshlrev_b32_sdwa v137,1,v137 dst_sel:DWORD dst_unused:UNUSED_PAD src0_sel:DWORD src1_sel:WORD_1
v_mfma_f32_16x16x32_bf16 v[244:247],v[104:107],v[152:155],v[244:247]
ds_read_u16_d16_hi v135,v135
v_lshlrev_b32_sdwa v139,1,v139 dst_sel:DWORD dst_unused:UNUSED_PAD src0_sel:DWORD src1_sel:WORD_1
v_mfma_f32_16x16x32_bf16 v[252:255],v[108:111],v[152:155],v[252:255]
ds_read_b64_tr_b16 v[92:93],v3 offset:6168
ds_read_b64_tr_b16 v[94:95],v3 offset:7704
v_lshlrev_b32_sdwa v140,1,v140 dst_sel:DWORD dst_unused:UNUSED_PAD src0_sel:DWORD src1_sel:WORD_1
v_mfma_f32_16x16x32_bf16 v[144:147],v[64:67],v[40:43],v[112:115]
ds_read_u16 v136,v136
v_lshlrev_b32_sdwa v142,1,v142 dst_sel:DWORD dst_unused:UNUSED_PAD src0_sel:DWORD src1_sel:WORD_1
v_mfma_f32_16x16x32_bf16 v[152:155],v[64:67],v[44:47],v[112:115]
ds_read_u16_d16_hi v138,v138
v_lshlrev_b32_sdwa v141,1,v141 dst_sel:DWORD dst_unused:UNUSED_PAD src0_sel:DWORD src1_sel:WORD_1
v_mfma_f32_16x16x32_bf16 v[156:159],v[68:71],v[44:47],v[116:119]
ds_read_b64_tr_b16 v[96:97],v3 offset:6224
ds_read_b64_tr_b16 v[98:99],v3 offset:7760
v_lshlrev_b32_sdwa v143,1,v143 dst_sel:DWORD dst_unused:UNUSED_PAD src0_sel:DWORD src1_sel:WORD_1
v_mfma_f32_16x16x32_bf16 v[148:151],v[68:71],v[40:43],v[116:119]
ds_read_u16 v137,v137
v_mfma_f32_16x16x32_bf16 v[144:147],v[72:75],v[48:51],v[144:147]
ds_read_u16_d16_hi v139,v139
v_mfma_f32_16x16x32_bf16 v[152:155],v[72:75],v[52:55],v[152:155]
ds_read_b64_tr_b16 v[100:101],v3 offset:6232
ds_read_b64_tr_b16 v[102:103],v3 offset:7768
v_mfma_f32_16x16x32_bf16 v[156:159],v[76:79],v[52:55],v[156:159]
ds_read_u16 v140,v140
v_mfma_f32_16x16x32_bf16 v[148:151],v[76:79],v[48:51],v[148:151]
s_waitcnt lgkmcnt(15)
ds_read_u16_d16_hi v142,v142
v_lshl_add_u64 v[128:129],v[128:129],0,v[130:131]
v_mfma_f32_16x16x32_bf16 v[144:147],v[80:83],v[56:59],v[144:147]
s_waitcnt lgkmcnt(12)
ds_read_b64_tr_b16 v[104:105],v3 offset:6288
ds_read_b64_tr_b16 v[106:107],v3 offset:7824
v_lshl_add_u64 v[130:131],v[132:133],0,v[134:135]
v_mfma_f32_16x16x32_bf16 v[152:155],v[80:83],v[60:63],v[152:155]
ds_read_u16 v141,v141
v_mfma_f32_16x16x32_bf16 v[156:159],v[84:87],v[60:63],v[156:159]
ds_read_u16_d16_hi v143,v143
v_mfma_f32_16x16x32_bf16 v[148:151],v[84:87],v[56:59],v[148:151]
ds_read_b64_tr_b16 v[108:109],v3 offset:6296
ds_read_b64_tr_b16 v[110:111],v3 offset:7832
v_lshlrev_b32_sdwa v144,1,v144 dst_sel:DWORD dst_unused:UNUSED_PAD src0_sel:DWORD src1_sel:WORD_1
v_lshlrev_b32_sdwa v146,1,v146 dst_sel:DWORD dst_unused:UNUSED_PAD src0_sel:DWORD src1_sel:WORD_1
v_mfma_f32_16x16x32_bf16 v[160:163],v[88:91],v[128:131],v[160:163]
s_waitcnt lgkmcnt(15)
ds_read_u16 v144,v144
v_lshlrev_b32_sdwa v145,1,v145 dst_sel:DWORD dst_unused:UNUSED_PAD src0_sel:DWORD src1_sel:WORD_1
v_mfma_f32_16x16x32_bf16 v[168:171],v[92:95],v[128:131],v[168:171]
s_waitcnt lgkmcnt(13)
ds_read_u16_d16_hi v146,v146
v_lshlrev_b32_sdwa v147,1,v147 dst_sel:DWORD dst_unused:UNUSED_PAD src0_sel:DWORD src1_sel:WORD_1
v_mfma_f32_16x16x32_bf16 v[176:179],v[96:99],v[128:131],v[176:179]
s_waitcnt lgkmcnt(10)
ds_read_b64_tr_b16 v[64:65],v1 offset:512
ds_read_b64_tr_b16 v[66:67],v1 offset:2560
v_lshlrev_b32_sdwa v148,1,v148 dst_sel:DWORD dst_unused:UNUSED_PAD src0_sel:DWORD src1_sel:WORD_1
v_mfma_f32_16x16x32_bf16 v[184:187],v[100:103],v[128:131],v[184:187]
s_waitcnt lgkmcnt(8)
ds_read_u16 v145,v145
v_lshl_add_u64 v[136:137],v[136:137],0,v[138:139]
v_lshlrev_b32_sdwa v150,1,v150 dst_sel:DWORD dst_unused:UNUSED_PAD src0_sel:DWORD src1_sel:WORD_1
v_mfma_f32_16x16x32_bf16 v[192:195],v[104:107],v[128:131],v[192:195]
s_waitcnt lgkmcnt(5)
ds_read_u16_d16_hi v147,v147
v_lshl_add_u64 v[138:139],v[140:141],0,v[142:143]
v_mfma_f32_16x16x32_bf16 v[200:203],v[108:111],v[128:131],v[200:203]
ds_read_b128 v[112:115],v4 offset:1408
ds_read_b64_tr_b16 v[68:69],v2 offset:512
v_lshlrev_b32_sdwa v149,1,v149 dst_sel:DWORD dst_unused:UNUSED_PAD src0_sel:DWORD src1_sel:WORD_1
v_mfma_f32_16x16x32_bf16 v[164:167],v[88:91],v[136:139],v[164:167]
ds_read_u16 v148,v148
v_lshlrev_b32_sdwa v151,1,v151 dst_sel:DWORD dst_unused:UNUSED_PAD src0_sel:DWORD src1_sel:WORD_1
v_mfma_f32_16x16x32_bf16 v[172:175],v[92:95],v[136:139],v[172:175]
ds_read_u16_d16_hi v150,v150
ds_read_b64_tr_b16 v[70:71],v2 offset:2560
v_lshlrev_b32_sdwa v152,1,v152 dst_sel:DWORD dst_unused:UNUSED_PAD src0_sel:DWORD src1_sel:WORD_1
v_mfma_f32_16x16x32_bf16 v[180:183],v[96:99],v[136:139],v[180:183]
ds_read_b128 v[116:119],v4 offset:1440
ds_read_b64_tr_b16 v[72:73],v1 offset:4608
v_lshlrev_b32_sdwa v154,1,v154 dst_sel:DWORD dst_unused:UNUSED_PAD src0_sel:DWORD src1_sel:WORD_1
v_mfma_f32_16x16x32_bf16 v[188:191],v[100:103],v[136:139],v[188:191]
ds_read_u16 v149,v149
v_lshlrev_b32_sdwa v153,1,v153 dst_sel:DWORD dst_unused:UNUSED_PAD src0_sel:DWORD src1_sel:WORD_1
v_mfma_f32_16x16x32_bf16 v[196:199],v[104:107],v[136:139],v[196:199]
ds_read_u16_d16_hi v151,v151
v_lshlrev_b32_sdwa v155,1,v155 dst_sel:DWORD dst_unused:UNUSED_PAD src0_sel:DWORD src1_sel:WORD_1
v_mfma_f32_16x16x32_bf16 v[204:207],v[108:111],v[136:139],v[204:207]
s_waitcnt lgkmcnt(8)
ds_read_b64_tr_b16 v[74:75],v1 offset:6656
ds_read_b64_tr_b16 v[76:77],v2 offset:4608
v_lshlrev_b32_sdwa v156,1,v156 dst_sel:DWORD dst_unused:UNUSED_PAD src0_sel:DWORD src1_sel:WORD_1
v_mfma_f32_16x16x32_bf16 v[128:131],v[64:67],v[16:19],v[112:115]
ds_read_u16 v152,v152
v_lshlrev_b32_sdwa v158,1,v158 dst_sel:DWORD dst_unused:UNUSED_PAD src0_sel:DWORD src1_sel:WORD_1
v_mfma_f32_16x16x32_bf16 v[136:139],v[64:67],v[20:23],v[112:115]
s_waitcnt lgkmcnt(6)
ds_read_u16_d16_hi v154,v154
ds_read_b64_tr_b16 v[78:79],v2 offset:6656
v_lshlrev_b32_sdwa v157,1,v157 dst_sel:DWORD dst_unused:UNUSED_PAD src0_sel:DWORD src1_sel:WORD_1
v_mfma_f32_16x16x32_bf16 v[140:143],v[68:71],v[20:23],v[116:119]
ds_read_b64_tr_b16 v[80:81],v1 offset:8704
ds_read_b64_tr_b16 v[82:83],v1 offset:10752
v_lshlrev_b32_sdwa v159,1,v159 dst_sel:DWORD dst_unused:UNUSED_PAD src0_sel:DWORD src1_sel:WORD_1
v_mfma_f32_16x16x32_bf16 v[132:135],v[68:71],v[16:19],v[116:119]
s_waitcnt lgkmcnt(6)
ds_read_u16 v153,v153
v_mfma_f32_16x16x32_bf16 v[128:131],v[72:75],v[24:27],v[128:131]
ds_read_u16_d16_hi v155,v155
v_mfma_f32_16x16x32_bf16 v[136:139],v[72:75],v[28:31],v[136:139]
s_waitcnt lgkmcnt(4)
ds_read_b64_tr_b16 v[84:85],v2 offset:8704
ds_read_b64_tr_b16 v[86:87],v2 offset:10752
v_mfma_f32_16x16x32_bf16 v[140:143],v[76:79],v[28:31],v[140:143]
ds_read_u16 v156,v156
v_mfma_f32_16x16x32_bf16 v[132:135],v[76:79],v[24:27],v[132:135]
s_waitcnt lgkmcnt(5)
ds_read_u16_d16_hi v158,v158
v_lshl_add_u64 v[144:145],v[144:145],0,v[146:147]
v_mfma_f32_16x16x32_bf16 v[128:131],v[80:83],v[32:35],v[128:131]
v_lshl_add_u64 v[146:147],v[148:149],0,v[150:151]
v_mfma_f32_16x16x32_bf16 v[136:139],v[80:83],v[36:39],v[136:139]
s_waitcnt lgkmcnt(2)
ds_read_u16 v157,v157
v_mfma_f32_16x16x32_bf16 v[140:143],v[84:87],v[36:39],v[140:143]
ds_read_u16_d16_hi v159,v159
v_mfma_f32_16x16x32_bf16 v[132:135],v[84:87],v[32:35],v[132:135]
s_nop 0
v_lshlrev_b32_sdwa v128,1,v128 dst_sel:DWORD dst_unused:UNUSED_PAD src0_sel:DWORD src1_sel:WORD_1
v_lshlrev_b32_sdwa v130,1,v130 dst_sel:DWORD dst_unused:UNUSED_PAD src0_sel:DWORD src1_sel:WORD_1
v_mfma_f32_16x16x32_bf16 v[208:211],v[88:91],v[144:147],v[208:211]
s_cbranch_vccz RA26_%=
s_waitcnt vmcnt(0)
RA26_%=:
s_barrier
s_setprio 3
ds_read_u16 v128,v128
v_lshlrev_b32_sdwa v129,1,v129 dst_sel:DWORD dst_unused:UNUSED_PAD src0_sel:DWORD src1_sel:WORD_1
v_lshlrev_b32_sdwa v131,1,v131 dst_sel:DWORD dst_unused:UNUSED_PAD src0_sel:DWORD src1_sel:WORD_1
v_mfma_f32_16x16x32_bf16 v[216:219],v[92:95],v[144:147],v[216:219]
v_fma_mixlo_f16 v120,v140,s99,0
v_fma_mixlo_f16 v122,v141,s99,0
v_mfma_f32_16x16x32_bf16 v[224:227],v[96:99],v[144:147],v[224:227]
s_cbranch_vccz RA27_%=
s_add_u32 m0,s58,0
s_mul_i32 s69,s59,1
s_add_u32 s69,s69,s97
buffer_load_dwordx4 v5,s[44:47],s69 offen lds
s_add_u32 m0,m0,1024
s_add_u32 s69,s69,s53
buffer_load_dwordx4 v6,s[44:47],s69 offen lds
RA27_%=:
ds_read_u16_d16_hi v130,v130
v_lshlrev_b32_sdwa v132,1,v132 dst_sel:DWORD dst_unused:UNUSED_PAD src0_sel:DWORD src1_sel:WORD_1
v_lshlrev_b32_sdwa v134,1,v134 dst_sel:DWORD dst_unused:UNUSED_PAD src0_sel:DWORD src1_sel:WORD_1
v_mfma_f32_16x16x32_bf16 v[232:235],v[100:103],v[144:147],v[232:235]
v_lshl_add_u64 v[152:153],v[152:153],0,v[154:155]
v_mfma_f32_16x16x32_bf16 v[240:243],v[104:107],v[144:147],v[240:243]
s_waitcnt lgkmcnt(2)
ds_read_u16 v129,v129
v_lshl_add_u64 v[154:155],v[156:157],0,v[158:159]
v_mfma_f32_16x16x32_bf16 v[248:251],v[108:111],v[144:147],v[248:251]
v_fma_mixhi_f16 v120,v142,s99,0
v_fma_mixhi_f16 v122,v143,s99,0
v_mfma_f32_16x16x32_bf16 v[212:215],v[88:91],v[152:155],v[212:215]
s_cbranch_vccz RA28_%=
s_add_u32 m0,m0,1024
s_add_u32 s69,s69,s53
buffer_load_dwordx4 v5,s[44:47],s69 offen lds
s_add_u32 m0,m0,s50
s_add_u32 s69,s69,s53
buffer_load_dwordx4 v6,s[44:47],s69 offen lds
RA28_%=:
ds_read_u16_d16_hi v131,v131
v_lshlrev_b32_sdwa v133,1,v133 dst_sel:DWORD dst_unused:UNUSED_PAD src0_sel:DWORD src1_sel:WORD_1
v_lshlrev_b32_sdwa v135,1,v135 dst_sel:DWORD dst_unused:UNUSED_PAD src0_sel:DWORD src1_sel:WORD_1
v_mfma_f32_16x16x32_bf16 v[220:223],v[92:95],v[152:155],v[220:223]
ds_read_b64_tr_b16 v[88:89],v3 offset:12320
v_pk_fma_f16 v121,v120,v120,s54
v_pk_fma_f16 v123,v122,v122,s54
v_mfma_f32_16x16x32_bf16 v[228:231],v[96:99],v[152:155],v[228:231]
ds_read_u16 v132,v132
v_pk_mul_f16 v121,v120,v121
v_pk_mul_f16 v123,v122,v123
v_mfma_f32_16x16x32_bf16 v[236:239],v[100:103],v[152:155],v[236:239]
ds_read_b64_tr_b16 v[90:91],v3 offset:13856
v_lshlrev_b32_sdwa v136,1,v136 dst_sel:DWORD dst_unused:UNUSED_PAD src0_sel:DWORD src1_sel:WORD_1
v_lshlrev_b32_sdwa v138,1,v138 dst_sel:DWORD dst_unused:UNUSED_PAD src0_sel:DWORD src1_sel:WORD_1
v_mfma_f32_16x16x32_bf16 v[244:247],v[104:107],v[152:155],v[244:247]
s_cbranch_vccz RA29_%=
s_add_u32 m0,m0,1024
s_add_u32 s69,s69,s53
buffer_load_dwordx4 v5,s[44:47],s69 offen lds
s_add_u32 m0,m0,1024
s_add_u32 s69,s69,s53
buffer_load_dwordx4 v6,s[44:47],s69 offen lds
RA29_%=:
ds_read_u16_d16_hi v134,v134
v_exp_f16 v120,v121
v_mfma_f32_16x16x32_bf16 v[252:255],v[108:111],v[152:155],v[252:255]
ds_read_b64_tr_b16 v[92:93],v3 offset:12328
v_exp_f16 v122,v123
v_mfma_f32_16x16x32_bf16 v[144:147],v[64:67],v[40:43],v[112:115]
ds_read_u16 v133,v133
v_lshlrev_b32_sdwa v137,1,v137 dst_sel:DWORD dst_unused:UNUSED_PAD src0_sel:DWORD src1_sel:WORD_1
v_lshlrev_b32_sdwa v139,1,v139 dst_sel:DWORD dst_unused:UNUSED_PAD src0_sel:DWORD src1_sel:WORD_1
v_mfma_f32_16x16x32_bf16 v[152:155],v[64:67],v[44:47],v[112:115]
s_cbranch_vccz RA30_%=
s_add_u32 m0,m0,s50
s_add_u32 s69,s69,s53
buffer_load_dwordx4 v5,s[44:47],s69 offen lds
s_add_u32 m0,m0,1024
s_add_u32 s69,s69,s53
buffer_load_dwordx4 v6,s[44:47],s69 offen lds
RA30_%=:
ds_read_b64_tr_b16 v[94:95],v3 offset:13864
v_exp_f16_sdwa v120,v121 dst_sel:WORD_1 dst_unused:UNUSED_PRESERVE src0_sel:WORD_1
v_mfma_f32_16x16x32_bf16 v[156:159],v[68:71],v[44:47],v[116:119]
ds_read_u16_d16_hi v135,v135
v_exp_f16_sdwa v122,v123 dst_sel:WORD_1 dst_unused:UNUSED_PRESERVE src0_sel:WORD_1
v_mfma_f32_16x16x32_bf16 v[148:151],v[68:71],v[40:43],v[116:119]
ds_read_b64_tr_b16 v[96:97],v3 offset:12384
v_pk_fma_f16 v120,v120,s55,s55
v_pk_fma_f16 v122,v122,s55,s55
v_mfma_f32_16x16x32_bf16 v[144:147],v[72:75],v[48:51],v[144:147]
ds_read_u16 v136,v136
v_rcp_f16 v121,v120
v_mfma_f32_16x16x32_bf16 v[152:155],v[72:75],v[52:55],v[152:155]
s_cbranch_vccz RA31_%=
s_add_u32 m0,m0,1024
s_add_u32 s69,s69,s53
buffer_load_dwordx4 v5,s[44:47],s69 offen lds
s_add_u32 m0,m0,s50
s_add_u32 s69,s69,s53
buffer_load_dwordx4 v6,s[44:47],s69 offen lds
RA31_%=:
ds_read_b64_tr_b16 v[98:99],v3 offset:13920
ds_read_b64_tr_b16 v[100:101],v3 offset:12392
v_rcp_f16 v123,v122
v_mfma_f32_16x16x32_bf16 v[156:159],v[76:79],v[52:55],v[156:159]
ds_read_u16_d16_hi v138,v138
v_rcp_f16_sdwa v121,v120 dst_sel:WORD_1 dst_unused:UNUSED_PRESERVE src0_sel:WORD_1
v_mfma_f32_16x16x32_bf16 v[148:151],v[76:79],v[48:51],v[148:151]
ds_read_b64_tr_b16 v[102:103],v3 offset:13928
ds_read_b64_tr_b16 v[104:105],v3 offset:12448
v_rcp_f16_sdwa v123,v122 dst_sel:WORD_1 dst_unused:UNUSED_PRESERVE src0_sel:WORD_1
v_mfma_f32_16x16x32_bf16 v[144:147],v[80:83],v[56:59],v[144:147]
ds_read_u16 v137,v137
v_fma_mix_f32 v140,v140,v121,0 op_sel_hi:[0,1,0]
v_fma_mix_f32 v142,v142,v121,0 op_sel:[0,1,0] op_sel_hi:[0,1,0]
v_mfma_f32_16x16x32_bf16 v[152:155],v[80:83],v[60:63],v[152:155]
s_waitcnt lgkmcnt(8)
s_cbranch_vccz RA32_%=
s_add_u32 m0,m0,1024
s_add_u32 s69,s69,s53
buffer_load_dwordx4 v5,s[44:47],s69 offen lds
s_add_u32 m0,m0,1024
s_add_u32 s69,s69,s53
buffer_load_dwordx4 v6,s[44:47],s69 offen lds
RA32_%=:
ds_read_b64_tr_b16 v[106:107],v3 offset:13984
ds_read_b64_tr_b16 v[108:109],v3 offset:12456
v_lshl_add_u64 v[128:129],v[128:129],0,v[130:131]
v_lshl_add_u64 v[130:131],v[132:133],0,v[134:135]
v_mfma_f32_16x16x32_bf16 v[156:159],v[84:87],v[60:63],v[156:159]
ds_read_u16_d16_hi v139,v139
v_fma_mix_f32 v141,v141,v123,0 op_sel_hi:[0,1,0]
v_fma_mix_f32 v143,v143,v123,0 op_sel:[0,1,0] op_sel_hi:[0,1,0]
v_mfma_f32_16x16x32_bf16 v[148:151],v[84:87],v[56:59],v[148:151]
ds_read_b64_tr_b16 v[110:111],v3 offset:13992
ds_read_b64_tr_b16 v[64:65],v1 offset:12320
v_lshlrev_b32_sdwa v144,1,v144 dst_sel:DWORD dst_unused:UNUSED_PAD src0_sel:DWORD src1_sel:WORD_1
v_lshlrev_b32_sdwa v146,1,v146 dst_sel:DWORD dst_unused:UNUSED_PAD src0_sel:DWORD src1_sel:WORD_1
v_mfma_f32_16x16x32_bf16 v[160:163],v[88:91],v[128:131],v[160:163]
ds_read_u16 v144,v144
v_lshlrev_b32_sdwa v145,1,v145 dst_sel:DWORD dst_unused:UNUSED_PAD src0_sel:DWORD src1_sel:WORD_1
v_lshlrev_b32_sdwa v147,1,v147 dst_sel:DWORD dst_unused:UNUSED_PAD src0_sel:DWORD src1_sel:WORD_1
v_mfma_f32_16x16x32_bf16 v[168:171],v[92:95],v[128:131],v[168:171]
s_waitcnt lgkmcnt(11)
ds_read_b64_tr_b16 v[66:67],v1 offset:14368
ds_read_b128 v[112:115],v4 offset:0
v_fma_mixlo_f16 v120,v156,s99,0
v_fma_mixlo_f16 v122,v157,s99,0
v_mfma_f32_16x16x32_bf16 v[176:179],v[96:99],v[128:131],v[176:179]
s_waitcnt lgkmcnt(10)
ds_read_u16_d16_hi v146,v146
v_lshlrev_b32_sdwa v148,1,v148 dst_sel:DWORD dst_unused:UNUSED_PAD src0_sel:DWORD src1_sel:WORD_1
v_lshlrev_b32_sdwa v150,1,v150 dst_sel:DWORD dst_unused:UNUSED_PAD src0_sel:DWORD src1_sel:WORD_1
v_mfma_f32_16x16x32_bf16 v[184:187],v[100:103],v[128:131],v[184:187]
s_waitcnt lgkmcnt(6)
ds_read_b64_tr_b16 v[68:69],v2 offset:12320
ds_read_b64_tr_b16 v[70:71],v2 offset:14368
v_lshl_add_u64 v[136:137],v[136:137],0,v[138:139]
v_mfma_f32_16x16x32_bf16 v[192:195],v[104:107],v[128:131],v[192:195]
s_waitcnt lgkmcnt(7)
ds_read_u16 v145,v145
v_cvt_pk_bf16_f32 v138,v140,v142
v_cvt_pk_bf16_f32 v139,v141,v143
v_mfma_f32_16x16x32_bf16 v[200:203],v[108:111],v[128:131],v[200:203]
ds_read_b128 v[116:119],v4 offset:32
ds_read_b64_tr_b16 v[72:73],v1 offset:16416
v_fma_mixhi_f16 v120,v158,s99,0
v_fma_mixhi_f16 v122,v159,s99,0
v_mfma_f32_16x16x32_bf16 v[164:167],v[88:91],v[136:139],v[164:167]
ds_read_u16_d16_hi v147,v147
v_lshlrev_b32_sdwa v149,1,v149 dst_sel:DWORD dst_unused:UNUSED_PAD src0_sel:DWORD src1_sel:WORD_1
v_lshlrev_b32_sdwa v151,1,v151 dst_sel:DWORD dst_unused:UNUSED_PAD src0_sel:DWORD src1_sel:WORD_1
v_mfma_f32_16x16x32_bf16 v[172:175],v[92:95],v[136:139],v[172:175]
ds_read_b64_tr_b16 v[74:75],v1 offset:18464
ds_read_b64_tr_b16 v[76:77],v2 offset:16416
v_pk_fma_f16 v121,v120,v120,s54
v_pk_fma_f16 v123,v122,v122,s54
v_mfma_f32_16x16x32_bf16 v[180:183],v[96:99],v[136:139],v[180:183]
ds_read_u16 v148,v148
v_pk_mul_f16 v121,v120,v121
v_pk_mul_f16 v123,v122,v123
v_mfma_f32_16x16x32_bf16 v[188:191],v[100:103],v[136:139],v[188:191]
ds_read_b64_tr_b16 v[78:79],v2 offset:18464
v_lshlrev_b32_sdwa v152,1,v152 dst_sel:DWORD dst_unused:UNUSED_PAD src0_sel:DWORD src1_sel:WORD_1
v_lshlrev_b32_sdwa v154,1,v154 dst_sel:DWORD dst_unused:UNUSED_PAD src0_sel:DWORD src1_sel:WORD_1
v_mfma_f32_16x16x32_bf16 v[196:199],v[104:107],v[136:139],v[196:199]
ds_read_u16_d16_hi v150,v150
v_exp_f16 v120,v121
v_mfma_f32_16x16x32_bf16 v[204:207],v[108:111],v[136:139],v[204:207]
s_waitcnt lgkmcnt(12)
ds_read_b64_tr_b16 v[80:81],v1 offset:20512
v_exp_f16 v122,v123
v_mfma_f32_16x16x32_bf16 v[128:131],v[64:67],v[16:19],v[112:115]
ds_read_u16 v149,v149
v_lshlrev_b32_sdwa v153,1,v153 dst_sel:DWORD dst_unused:UNUSED_PAD src0_sel:DWORD src1_sel:WORD_1
v_lshlrev_b32_sdwa v155,1,v155 dst_sel:DWORD dst_unused:UNUSED_PAD src0_sel:DWORD src1_sel:WORD_1
v_mfma_f32_16x16x32_bf16 v[136:139],v[64:67],v[20:23],v[112:115]
s_waitcnt lgkmcnt(9)
ds_read_b64_tr_b16 v[82:83],v1 offset:22560
v_exp_f16_sdwa v120,v121 dst_sel:WORD_1 dst_unused:UNUSED_PRESERVE src0_sel:WORD_1
v_mfma_f32_16x16x32_bf16 v[140:143],v[68:71],v[20:23],v[116:119]
ds_read_u16_d16_hi v151,v151
v_exp_f16_sdwa v122,v123 dst_sel:WORD_1 dst_unused:UNUSED_PRESERVE src0_sel:WORD_1
v_mfma_f32_16x16x32_bf16 v[132:135],v[68:71],v[16:19],v[116:119]
s_waitcnt lgkmcnt(8)
ds_read_b64_tr_b16 v[84:85],v2 offset:20512
v_pk_fma_f16 v120,v120,s55,s55
v_pk_fma_f16 v122,v122,s55,s55
v_mfma_f32_16x16x32_bf16 v[128:131],v[72:75],v[24:27],v[128:131]
ds_read_u16 v152,v152
v_rcp_f16 v121,v120
v_mfma_f32_16x16x32_bf16 v[136:139],v[72:75],v[28:31],v[136:139]
s_waitcnt lgkmcnt(7)
ds_read_b64_tr_b16 v[86:87],v2 offset:22560
v_rcp_f16 v123,v122
v_mfma_f32_16x16x32_bf16 v[140:143],v[76:79],v[28:31],v[140:143]
ds_read_u16_d16_hi v154,v154
v_rcp_f16_sdwa v121,v120 dst_sel:WORD_1 dst_unused:UNUSED_PRESERVE src0_sel:WORD_1
v_mfma_f32_16x16x32_bf16 v[132:135],v[76:79],v[24:27],v[132:135]
s_waitcnt lgkmcnt(5)
v_rcp_f16_sdwa v123,v122 dst_sel:WORD_1 dst_unused:UNUSED_PRESERVE src0_sel:WORD_1
v_mfma_f32_16x16x32_bf16 v[128:131],v[80:83],v[32:35],v[128:131]
ds_read_u16 v153,v153
v_fma_mix_f32 v156,v156,v121,0 op_sel_hi:[0,1,0]
v_fma_mix_f32 v158,v158,v121,0 op_sel:[0,1,0] op_sel_hi:[0,1,0]
v_mfma_f32_16x16x32_bf16 v[136:139],v[80:83],v[36:39],v[136:139]
s_waitcnt lgkmcnt(2)
v_lshl_add_u64 v[144:145],v[144:145],0,v[146:147]
v_lshl_add_u64 v[146:147],v[148:149],0,v[150:151]
v_mfma_f32_16x16x32_bf16 v[140:143],v[84:87],v[36:39],v[140:143]
ds_read_u16_d16_hi v155,v155
v_fma_mix_f32 v157,v157,v123,0 op_sel_hi:[0,1,0]
v_fma_mix_f32 v159,v159,v123,0 op_sel:[0,1,0] op_sel_hi:[0,1,0]
v_mfma_f32_16x16x32_bf16 v[132:135],v[84:87],v[32:35],v[132:135]
v_lshlrev_b32_sdwa v128,1,v128 dst_sel:DWORD dst_unused:UNUSED_PAD src0_sel:DWORD src1_sel:WORD_1
v_lshlrev_b32_sdwa v130,1,v130 dst_sel:DWORD dst_unused:UNUSED_PAD src0_sel:DWORD src1_sel:WORD_1
v_mfma_f32_16x16x32_bf16 v[208:211],v[88:91],v[144:147],v[208:211]
ds_read_u16 v128,v128
v_lshlrev_b32_sdwa v129,1,v129 dst_sel:DWORD dst_unused:UNUSED_PAD src0_sel:DWORD src1_sel:WORD_1
v_lshlrev_b32_sdwa v131,1,v131 dst_sel:DWORD dst_unused:UNUSED_PAD src0_sel:DWORD src1_sel:WORD_1
v_mfma_f32_16x16x32_bf16 v[216:219],v[92:95],v[144:147],v[216:219]
v_fma_mixlo_f16 v120,v140,s99,0
v_fma_mixlo_f16 v122,v141,s99,0
v_mfma_f32_16x16x32_bf16 v[224:227],v[96:99],v[144:147],v[224:227]
ds_read_u16_d16_hi v130,v130
v_lshlrev_b32_sdwa v132,1,v132 dst_sel:DWORD dst_unused:UNUSED_PAD src0_sel:DWORD src1_sel:WORD_1
v_lshlrev_b32_sdwa v134,1,v134 dst_sel:DWORD dst_unused:UNUSED_PAD src0_sel:DWORD src1_sel:WORD_1
v_mfma_f32_16x16x32_bf16 v[232:235],v[100:103],v[144:147],v[232:235]
s_waitcnt lgkmcnt(2)
v_lshl_add_u64 v[152:153],v[152:153],0,v[154:155]
v_mfma_f32_16x16x32_bf16 v[240:243],v[104:107],v[144:147],v[240:243]
ds_read_u16 v129,v129
v_cvt_pk_bf16_f32 v154,v156,v158
v_cvt_pk_bf16_f32 v155,v157,v159
v_mfma_f32_16x16x32_bf16 v[248:251],v[108:111],v[144:147],v[248:251]
v_fma_mixhi_f16 v120,v142,s99,0
v_fma_mixhi_f16 v122,v143,s99,0
v_mfma_f32_16x16x32_bf16 v[212:215],v[88:91],v[152:155],v[212:215]
s_setprio 2
ds_read_u16_d16_hi v131,v131
v_lshlrev_b32_sdwa v133,1,v133 dst_sel:DWORD dst_unused:UNUSED_PAD src0_sel:DWORD src1_sel:WORD_1
v_lshlrev_b32_sdwa v135,1,v135 dst_sel:DWORD dst_unused:UNUSED_PAD src0_sel:DWORD src1_sel:WORD_1
v_mfma_f32_16x16x32_bf16 v[220:223],v[92:95],v[152:155],v[220:223]
ds_read_b64_tr_b16 v[88:89],v3 offset:18480
v_pk_fma_f16 v121,v120,v120,s54
v_pk_fma_f16 v123,v122,v122,s54
v_mfma_f32_16x16x32_bf16 v[228:231],v[96:99],v[152:155],v[228:231]
ds_read_u16 v132,v132
v_pk_mul_f16 v121,v120,v121
v_pk_mul_f16 v123,v122,v123
v_mfma_f32_16x16x32_bf16 v[236:239],v[100:103],v[152:155],v[236:239]
ds_read_b64_tr_b16 v[90:91],v3 offset:20016
v_lshlrev_b32_sdwa v136,1,v136 dst_sel:DWORD dst_unused:UNUSED_PAD src0_sel:DWORD src1_sel:WORD_1
v_lshlrev_b32_sdwa v138,1,v138 dst_sel:DWORD dst_unused:UNUSED_PAD src0_sel:DWORD src1_sel:WORD_1
v_mfma_f32_16x16x32_bf16 v[244:247],v[104:107],v[152:155],v[244:247]
ds_read_u16_d16_hi v134,v134
v_exp_f16 v120,v121
v_mfma_f32_16x16x32_bf16 v[252:255],v[108:111],v[152:155],v[252:255]
ds_read_b64_tr_b16 v[92:93],v3 offset:18488
v_exp_f16 v122,v123
v_mfma_f32_16x16x32_bf16 v[144:147],v[64:67],v[40:43],v[112:115]
ds_read_u16 v133,v133
v_lshlrev_b32_sdwa v137,1,v137 dst_sel:DWORD dst_unused:UNUSED_PAD src0_sel:DWORD src1_sel:WORD_1
v_lshlrev_b32_sdwa v139,1,v139 dst_sel:DWORD dst_unused:UNUSED_PAD src0_sel:DWORD src1_sel:WORD_1
v_mfma_f32_16x16x32_bf16 v[152:155],v[64:67],v[44:47],v[112:115]
ds_read_b64_tr_b16 v[94:95],v3 offset:20024
v_exp_f16_sdwa v120,v121 dst_sel:WORD_1 dst_unused:UNUSED_PRESERVE src0_sel:WORD_1
v_mfma_f32_16x16x32_bf16 v[156:159],v[68:71],v[44:47],v[116:119]
ds_read_u16_d16_hi v135,v135
v_exp_f16_sdwa v122,v123 dst_sel:WORD_1 dst_unused:UNUSED_PRESERVE src0_sel:WORD_1
v_mfma_f32_16x16x32_bf16 v[148:151],v[68:71],v[40:43],v[116:119]
ds_read_b64_tr_b16 v[96:97],v3 offset:18544
v_pk_fma_f16 v120,v120,s55,s55
v_pk_fma_f16 v122,v122,s55,s55
v_mfma_f32_16x16x32_bf16 v[144:147],v[72:75],v[48:51],v[144:147]
ds_read_u16 v136,v136
v_rcp_f16 v121,v120
v_mfma_f32_16x16x32_bf16 v[152:155],v[72:75],v[52:55],v[152:155]
ds_read_b64_tr_b16 v[98:99],v3 offset:20080
ds_read_b64_tr_b16 v[100:101],v3 offset:18552
v_rcp_f16 v123,v122
v_mfma_f32_16x16x32_bf16 v[156:159],v[76:79],v[52:55],v[156:159]
ds_read_u16_d16_hi v138,v138
v_rcp_f16_sdwa v121,v120 dst_sel:WORD_1 dst_unused:UNUSED_PRESERVE src0_sel:WORD_1
v_mfma_f32_16x16x32_bf16 v[148:151],v[76:79],v[48:51],v[148:151]
ds_read_b64_tr_b16 v[102:103],v3 offset:20088
ds_read_b64_tr_b16 v[104:105],v3 offset:18608
v_rcp_f16_sdwa v123,v122 dst_sel:WORD_1 dst_unused:UNUSED_PRESERVE src0_sel:WORD_1
v_mfma_f32_16x16x32_bf16 v[144:147],v[80:83],v[56:59],v[144:147]
ds_read_u16 v137,v137
v_fma_mix_f32 v140,v140,v121,0 op_sel_hi:[0,1,0]
v_fma_mix_f32 v142,v142,v121,0 op_sel:[0,1,0] op_sel_hi:[0,1,0]
v_mfma_f32_16x16x32_bf16 v[152:155],v[80:83],v[60:63],v[152:155]
s_waitcnt lgkmcnt(8)
ds_read_b64_tr_b16 v[106:107],v3 offset:20144
ds_read_b64_tr_b16 v[108:109],v3 offset:18616
v_lshl_add_u64 v[128:129],v[128:129],0,v[130:131]
v_lshl_add_u64 v[130:131],v[132:133],0,v[134:135]
v_mfma_f32_16x16x32_bf16 v[156:159],v[84:87],v[60:63],v[156:159]
ds_read_u16_d16_hi v139,v139
v_fma_mix_f32 v141,v141,v123,0 op_sel_hi:[0,1,0]
v_fma_mix_f32 v143,v143,v123,0 op_sel:[0,1,0] op_sel_hi:[0,1,0]
v_mfma_f32_16x16x32_bf16 v[148:151],v[84:87],v[56:59],v[148:151]
ds_read_b64_tr_b16 v[110:111],v3 offset:20152
ds_read_b64_tr_b16 v[64:65],v1 offset:12832
v_lshlrev_b32_sdwa v144,1,v144 dst_sel:DWORD dst_unused:UNUSED_PAD src0_sel:DWORD src1_sel:WORD_1
v_lshlrev_b32_sdwa v146,1,v146 dst_sel:DWORD dst_unused:UNUSED_PAD src0_sel:DWORD src1_sel:WORD_1
v_mfma_f32_16x16x32_bf16 v[160:163],v[88:91],v[128:131],v[160:163]
ds_read_u16 v144,v144
v_lshlrev_b32_sdwa v145,1,v145 dst_sel:DWORD dst_unused:UNUSED_PAD src0_sel:DWORD src1_sel:WORD_1
v_lshlrev_b32_sdwa v147,1,v147 dst_sel:DWORD dst_unused:UNUSED_PAD src0_sel:DWORD src1_sel:WORD_1
v_mfma_f32_16x16x32_bf16 v[168:171],v[92:95],v[128:131],v[168:171]
s_waitcnt lgkmcnt(11)
ds_read_b64_tr_b16 v[66:67],v1 offset:14880
ds_read_b128 v[112:115],v4 offset:128
v_fma_mixlo_f16 v120,v156,s99,0
v_fma_mixlo_f16 v122,v157,s99,0
v_mfma_f32_16x16x32_bf16 v[176:179],v[96:99],v[128:131],v[176:179]
s_waitcnt lgkmcnt(10)
ds_read_u16_d16_hi v146,v146
v_lshlrev_b32_sdwa v148,1,v148 dst_sel:DWORD dst_unused:UNUSED_PAD src0_sel:DWORD src1_sel:WORD_1
v_lshlrev_b32_sdwa v150,1,v150 dst_sel:DWORD dst_unused:UNUSED_PAD src0_sel:DWORD src1_sel:WORD_1
v_mfma_f32_16x16x32_bf16 v[184:187],v[100:103],v[128:131],v[184:187]
s_waitcnt lgkmcnt(6)
ds_read_b64_tr_b16 v[68:69],v2 offset:12832
ds_read_b64_tr_b16 v[70:71],v2 offset:14880
v_lshl_add_u64 v[136:137],v[136:137],0,v[138:139]
v_mfma_f32_16x16x32_bf16 v[192:195],v[104:107],v[128:131],v[192:195]
s_waitcnt lgkmcnt(7)
ds_read_u16 v145,v145
v_cvt_pk_bf16_f32 v138,v140,v142
v_cvt_pk_bf16_f32 v139,v141,v143
v_mfma_f32_16x16x32_bf16 v[200:203],v[108:111],v[128:131],v[200:203]
ds_read_b128 v[116:119],v4 offset:160
ds_read_b64_tr_b16 v[72:73],v1 offset:16928
v_fma_mixhi_f16 v120,v158,s99,0
v_fma_mixhi_f16 v122,v159,s99,0
v_mfma_f32_16x16x32_bf16 v[164:167],v[88:91],v[136:139],v[164:167]
ds_read_u16_d16_hi v147,v147
v_lshlrev_b32_sdwa v149,1,v149 dst_sel:DWORD dst_unused:UNUSED_PAD src0_sel:DWORD src1_sel:WORD_1
v_lshlrev_b32_sdwa v151,1,v151 dst_sel:DWORD dst_unused:UNUSED_PAD src0_sel:DWORD src1_sel:WORD_1
v_mfma_f32_16x16x32_bf16 v[172:175],v[92:95],v[136:139],v[172:175]
s_setprio 1
ds_read_b64_tr_b16 v[74:75],v1 offset:18976
ds_read_b64_tr_b16 v[76:77],v2 offset:16928
v_pk_fma_f16 v121,v120,v120,s54
v_pk_fma_f16 v123,v122,v122,s54
v_mfma_f32_16x16x32_bf16 v[180:183],v[96:99],v[136:139],v[180:183]
ds_read_u16 v148,v148
v_pk_mul_f16 v121,v120,v121
v_pk_mul_f16 v123,v122,v123
v_mfma_f32_16x16x32_bf16 v[188:191],v[100:103],v[136:139],v[188:191]
ds_read_b64_tr_b16 v[78:79],v2 offset:18976
v_lshlrev_b32_sdwa v152,1,v152 dst_sel:DWORD dst_unused:UNUSED_PAD src0_sel:DWORD src1_sel:WORD_1
v_lshlrev_b32_sdwa v154,1,v154 dst_sel:DWORD dst_unused:UNUSED_PAD src0_sel:DWORD src1_sel:WORD_1
v_mfma_f32_16x16x32_bf16 v[196:199],v[104:107],v[136:139],v[196:199]
ds_read_u16_d16_hi v150,v150
v_exp_f16 v120,v121
v_mfma_f32_16x16x32_bf16 v[204:207],v[108:111],v[136:139],v[204:207]
s_waitcnt lgkmcnt(12)
ds_read_b64_tr_b16 v[80:81],v1 offset:21024
v_exp_f16 v122,v123
v_mfma_f32_16x16x32_bf16 v[128:131],v[64:67],v[16:19],v[112:115]
ds_read_u16 v149,v149
v_lshlrev_b32_sdwa v153,1,v153 dst_sel:DWORD dst_unused:UNUSED_PAD src0_sel:DWORD src1_sel:WORD_1
v_lshlrev_b32_sdwa v155,1,v155 dst_sel:DWORD dst_unused:UNUSED_PAD src0_sel:DWORD src1_sel:WORD_1
v_mfma_f32_16x16x32_bf16 v[136:139],v[64:67],v[20:23],v[112:115]
s_waitcnt lgkmcnt(9)
ds_read_b64_tr_b16 v[82:83],v1 offset:23072
v_exp_f16_sdwa v120,v121 dst_sel:WORD_1 dst_unused:UNUSED_PRESERVE src0_sel:WORD_1
v_mfma_f32_16x16x32_bf16 v[140:143],v[68:71],v[20:23],v[116:119]
ds_read_u16_d16_hi v151,v151
v_exp_f16_sdwa v122,v123 dst_sel:WORD_1 dst_unused:UNUSED_PRESERVE src0_sel:WORD_1
v_mfma_f32_16x16x32_bf16 v[132:135],v[68:71],v[16:19],v[116:119]
s_waitcnt lgkmcnt(8)
ds_read_b64_tr_b16 v[84:85],v2 offset:21024
v_pk_fma_f16 v120,v120,s55,s55
v_pk_fma_f16 v122,v122,s55,s55
v_mfma_f32_16x16x32_bf16 v[128:131],v[72:75],v[24:27],v[128:131]
ds_read_u16 v152,v152
v_rcp_f16 v121,v120
v_mfma_f32_16x16x32_bf16 v[136:139],v[72:75],v[28:31],v[136:139]
s_waitcnt lgkmcnt(7)
ds_read_b64_tr_b16 v[86:87],v2 offset:23072
v_rcp_f16 v123,v122
v_mfma_f32_16x16x32_bf16 v[140:143],v[76:79],v[28:31],v[140:143]
ds_read_u16_d16_hi v154,v154
v_rcp_f16_sdwa v121,v120 dst_sel:WORD_1 dst_unused:UNUSED_PRESERVE src0_sel:WORD_1
v_mfma_f32_16x16x32_bf16 v[132:135],v[76:79],v[24:27],v[132:135]
s_waitcnt lgkmcnt(5)
v_rcp_f16_sdwa v123,v122 dst_sel:WORD_1 dst_unused:UNUSED_PRESERVE src0_sel:WORD_1
v_mfma_f32_16x16x32_bf16 v[128:131],v[80:83],v[32:35],v[128:131]
ds_read_u16 v153,v153
v_fma_mix_f32 v156,v156,v121,0 op_sel_hi:[0,1,0]
v_fma_mix_f32 v158,v158,v121,0 op_sel:[0,1,0] op_sel_hi:[0,1,0]
v_mfma_f32_16x16x32_bf16 v[136:139],v[80:83],v[36:39],v[136:139]
s_waitcnt lgkmcnt(2)
s_setprio 0
v_lshl_add_u64 v[144:145],v[144:145],0,v[146:147]
v_lshl_add_u64 v[146:147],v[148:149],0,v[150:151]
v_mfma_f32_16x16x32_bf16 v[140:143],v[84:87],v[36:39],v[140:143]
ds_read_u16_d16_hi v155,v155
v_fma_mix_f32 v157,v157,v123,0 op_sel_hi:[0,1,0]
v_fma_mix_f32 v159,v159,v123,0 op_sel:[0,1,0] op_sel_hi:[0,1,0]
v_mfma_f32_16x16x32_bf16 v[132:135],v[84:87],v[32:35],v[132:135]
v_lshlrev_b32_sdwa v128,1,v128 dst_sel:DWORD dst_unused:UNUSED_PAD src0_sel:DWORD src1_sel:WORD_1
v_lshlrev_b32_sdwa v130,1,v130 dst_sel:DWORD dst_unused:UNUSED_PAD src0_sel:DWORD src1_sel:WORD_1
v_mfma_f32_16x16x32_bf16 v[208:211],v[88:91],v[144:147],v[208:211]
s_cbranch_vccz RA33_%=
s_waitcnt vmcnt(0)
RA33_%=:
s_barrier
s_setprio 2
ds_read_u16 v128,v128
v_lshlrev_b32_sdwa v129,1,v129 dst_sel:DWORD dst_unused:UNUSED_PAD src0_sel:DWORD src1_sel:WORD_1
v_mfma_f32_16x16x32_bf16 v[216:219],v[92:95],v[144:147],v[216:219]
ds_read_u16_d16_hi v130,v130
v_lshlrev_b32_sdwa v131,1,v131 dst_sel:DWORD dst_unused:UNUSED_PAD src0_sel:DWORD src1_sel:WORD_1
v_mfma_f32_16x16x32_bf16 v[224:227],v[96:99],v[144:147],v[224:227]
s_cbranch_vccz RA34_%=
s_add_u32 m0,s58,12320
s_mul_i32 s69,s59,3
s_add_u32 s69,s69,s97
buffer_load_dwordx4 v5,s[44:47],s69 offen lds
s_add_u32 m0,m0,1024
s_add_u32 s69,s69,s53
buffer_load_dwordx4 v6,s[44:47],s69 offen lds
RA34_%=:
v_lshlrev_b32_sdwa v132,1,v132 dst_sel:DWORD dst_unused:UNUSED_PAD src0_sel:DWORD src1_sel:WORD_1
v_mfma_f32_16x16x32_bf16 v[232:235],v[100:103],v[144:147],v[232:235]
s_waitcnt lgkmcnt(2)
ds_read_u16 v129,v129
v_lshl_add_u64 v[152:153],v[152:153],0,v[154:155]
v_lshlrev_b32_sdwa v134,1,v134 dst_sel:DWORD dst_unused:UNUSED_PAD src0_sel:DWORD src1_sel:WORD_1
v_mfma_f32_16x16x32_bf16 v[240:243],v[104:107],v[144:147],v[240:243]
ds_read_u16_d16_hi v131,v131
v_cvt_pk_bf16_f32 v154,v156,v158
v_cvt_pk_bf16_f32 v155,v157,v159
v_mfma_f32_16x16x32_bf16 v[248:251],v[108:111],v[144:147],v[248:251]
v_lshlrev_b32_sdwa v133,1,v133 dst_sel:DWORD dst_unused:UNUSED_PAD src0_sel:DWORD src1_sel:WORD_1
v_mfma_f32_16x16x32_bf16 v[212:215],v[88:91],v[152:155],v[212:215]
s_cbranch_vccz RA35_%=
s_add_u32 m0,m0,1024
s_add_u32 s69,s69,s53
buffer_load_dwordx4 v5,s[44:47],s69 offen lds
s_add_u32 m0,m0,s50
s_add_u32 s69,s69,s53
buffer_load_dwordx4 v6,s[44:47],s69 offen lds
RA35_%=:
ds_read_u16 v132,v132
v_lshlrev_b32_sdwa v135,1,v135 dst_sel:DWORD dst_unused:UNUSED_PAD src0_sel:DWORD src1_sel:WORD_1
v_mfma_f32_16x16x32_bf16 v[220:223],v[92:95],v[152:155],v[220:223]
ds_read_u16_d16_hi v134,v134
v_lshlrev_b32_sdwa v136,1,v136 dst_sel:DWORD dst_unused:UNUSED_PAD src0_sel:DWORD src1_sel:WORD_1
v_mfma_f32_16x16x32_bf16 v[228:231],v[96:99],v[152:155],v[228:231]
ds_read_b64_tr_b16 v[88:89],v3 offset:0
ds_read_b64_tr_b16 v[90:91],v3 offset:1536
v_lshlrev_b32_sdwa v138,1,v138 dst_sel:DWORD dst_unused:UNUSED_PAD src0_sel:DWORD src1_sel:WORD_1
v_mfma_f32_16x16x32_bf16 v[236:239],v[100:103],v[152:155],v[236:239]
ds_read_u16 v133,v133
v_lshlrev_b32_sdwa v137,1,v137 dst_sel:DWORD dst_unused:UNUSED_PAD src0_sel:DWORD src1_sel:WORD_1
v_mfma_f32_16x16x32_bf16 v[244:247],v[104:107],v[152:155],v[244:247]
ds_read_u16_d16_hi v135,v135
v_lshlrev_b32_sdwa v139,1,v139 dst_sel:DWORD dst_unused:UNUSED_PAD src0_sel:DWORD src1_sel:WORD_1
v_mfma_f32_16x16x32_bf16 v[252:255],v[108:111],v[152:155],v[252:255]
s_cbranch_vccz RA36_%=
s_add_u32 m0,m0,1024
s_add_u32 s69,s69,s53
buffer_load_dwordx4 v5,s[44:47],s69 offen lds
s_add_u32 m0,m0,1024
s_add_u32 s69,s69,s53
buffer_load_dwordx4 v6,s[44:47],s69 offen lds
RA36_%=:
ds_read_b64_tr_b16 v[92:93],v3 offset:8
ds_read_b64_tr_b16 v[94:95],v3 offset:1544
v_lshlrev_b32_sdwa v140,1,v140 dst_sel:DWORD dst_unused:UNUSED_PAD src0_sel:DWORD src1_sel:WORD_1
v_mfma_f32_16x16x32_bf16 v[144:147],v[64:67],v[40:43],v[112:115]
ds_read_u16 v136,v136
v_lshlrev_b32_sdwa v142,1,v142 dst_sel:DWORD dst_unused:UNUSED_PAD src0_sel:DWORD src1_sel:WORD_1
v_mfma_f32_16x16x32_bf16 v[152:155],v[64:67],v[44:47],v[112:115]
ds_read_u16_d16_hi v138,v138
v_lshlrev_b32_sdwa v141,1,v141 dst_sel:DWORD dst_unused:UNUSED_PAD src0_sel:DWORD src1_sel:WORD_1
v_mfma_f32_16x16x32_bf16 v[156:159],v[68:71],v[44:47],v[116:119]
ds_read_b64_tr_b16 v[96:97],v3 offset:64
ds_read_b64_tr_b16 v[98:99],v3 offset:1600
v_lshlrev_b32_sdwa v143,1,v143 dst_sel:DWORD dst_unused:UNUSED_PAD src0_sel:DWORD src1_sel:WORD_1
v_mfma_f32_16x16x32_bf16 v[148:151],v[68:71],v[40:43],v[116:119]
ds_read_u16 v137,v137
v_mfma_f32_16x16x32_bf16 v[144:147],v[72:75],v[48:51],v[144:147]
ds_read_u16_d16_hi v139,v139
v_mfma_f32_16x16x32_bf16 v[152:155],v[72:75],v[52:55],v[152:155]
s_cbranch_vccz RA37_%=
s_add_u32 m0,m0,s50
s_add_u32 s69,s69,s53
buffer_load_dwordx4 v5,s[44:47],s69 offen lds
s_add_u32 m0,m0,1024
s_add_u32 s69,s69,s53
buffer_load_dwordx4 v6,s[44:47],s69 offen lds
RA37_%=:
ds_read_b64_tr_b16 v[100:101],v3 offset:72
ds_read_b64_tr_b16 v[102:103],v3 offset:1608
v_mfma_f32_16x16x32_bf16 v[156:159],v[76:79],v[52:55],v[156:159]
ds_read_u16 v140,v140
v_mfma_f32_16x16x32_bf16 v[148:151],v[76:79],v[48:51],v[148:151]
s_waitcnt lgkmcnt(15)
ds_read_u16_d16_hi v142,v142
v_lshl_add_u64 v[128:129],v[128:129],0,v[130:131]
v_mfma_f32_16x16x32_bf16 v[144:147],v[80:83],v[56:59],v[144:147]
s_waitcnt lgkmcnt(12)
ds_read_b64_tr_b16 v[104:105],v3 offset:128
ds_read_b64_tr_b16 v[106:107],v3 offset:1664
v_lshl_add_u64 v[130:131],v[132:133],0,v[134:135]
v_mfma_f32_16x16x32_bf16 v[152:155],v[80:83],v[60:63],v[152:155]
ds_read_u16 v141,v141
v_mfma_f32_16x16x32_bf16 v[156:159],v[84:87],v[60:63],v[156:159]
ds_read_u16_d16_hi v143,v143
v_mfma_f32_16x16x32_bf16 v[148:151],v[84:87],v[56:59],v[148:151]
s_cbranch_vccz RA38_%=
s_add_u32 m0,m0,1024
s_add_u32 s69,s69,s53
buffer_load_dwordx4 v5,s[44:47],s69 offen lds
s_add_u32 m0,m0,s50
s_add_u32 s69,s69,s53
buffer_load_dwordx4 v6,s[44:47],s69 offen lds
RA38_%=:
ds_read_b64_tr_b16 v[108:109],v3 offset:136
ds_read_b64_tr_b16 v[110:111],v3 offset:1672
v_lshlrev_b32_sdwa v144,1,v144 dst_sel:DWORD dst_unused:UNUSED_PAD src0_sel:DWORD src1_sel:WORD_1
v_lshlrev_b32_sdwa v146,1,v146 dst_sel:DWORD dst_unused:UNUSED_PAD src0_sel:DWORD src1_sel:WORD_1
v_mfma_f32_16x16x32_bf16 v[160:163],v[88:91],v[128:131],v[160:163]
s_waitcnt lgkmcnt(15)
ds_read_u16 v144,v144
v_lshlrev_b32_sdwa v145,1,v145 dst_sel:DWORD dst_unused:UNUSED_PAD src0_sel:DWORD src1_sel:WORD_1
v_mfma_f32_16x16x32_bf16 v[168:171],v[92:95],v[128:131],v[168:171]
s_waitcnt lgkmcnt(13)
ds_read_u16_d16_hi v146,v146
v_lshlrev_b32_sdwa v147,1,v147 dst_sel:DWORD dst_unused:UNUSED_PAD src0_sel:DWORD src1_sel:WORD_1
v_mfma_f32_16x16x32_bf16 v[176:179],v[96:99],v[128:131],v[176:179]
s_waitcnt lgkmcnt(10)
ds_read_b64_tr_b16 v[64:65],v1 offset:0
ds_read_b64_tr_b16 v[66:67],v1 offset:2048
v_lshlrev_b32_sdwa v148,1,v148 dst_sel:DWORD dst_unused:UNUSED_PAD src0_sel:DWORD src1_sel:WORD_1
v_mfma_f32_16x16x32_bf16 v[184:187],v[100:103],v[128:131],v[184:187]
s_waitcnt lgkmcnt(8)
ds_read_u16 v145,v145
v_lshl_add_u64 v[136:137],v[136:137],0,v[138:139]
v_lshlrev_b32_sdwa v150,1,v150 dst_sel:DWORD dst_unused:UNUSED_PAD src0_sel:DWORD src1_sel:WORD_1
v_mfma_f32_16x16x32_bf16 v[192:195],v[104:107],v[128:131],v[192:195]
s_waitcnt lgkmcnt(5)
s_cbranch_vccz RA39_%=
s_add_u32 m0,m0,1024
s_add_u32 s69,s69,s53
buffer_load_dwordx4 v5,s[44:47],s69 offen lds
s_add_u32 m0,m0,1024
s_add_u32 s69,s69,s53
buffer_load_dwordx4 v6,s[44:47],s69 offen lds
RA39_%=:
ds_read_u16_d16_hi v147,v147
v_lshl_add_u64 v[138:139],v[140:141],0,v[142:143]
v_mfma_f32_16x16x32_bf16 v[200:203],v[108:111],v[128:131],v[200:203]
ds_read_b128 v[112:115],v4 offset:256
ds_read_b64_tr_b16 v[68:69],v2 offset:0
v_lshlrev_b32_sdwa v149,1,v149 dst_sel:DWORD dst_unused:UNUSED_PAD src0_sel:DWORD src1_sel:WORD_1
v_mfma_f32_16x16x32_bf16 v[164:167],v[88:91],v[136:139],v[164:167]
ds_read_u16 v148,v148
v_lshlrev_b32_sdwa v151,1,v151 dst_sel:DWORD dst_unused:UNUSED_PAD src0_sel:DWORD src1_sel:WORD_1
v_mfma_f32_16x16x32_bf16 v[172:175],v[92:95],v[136:139],v[172:175]
ds_read_u16_d16_hi v150,v150
ds_read_b64_tr_b16 v[70:71],v2 offset:2048
v_lshlrev_b32_sdwa v152,1,v152 dst_sel:DWORD dst_unused:UNUSED_PAD src0_sel:DWORD src1_sel:WORD_1
v_mfma_f32_16x16x32_bf16 v[180:183],v[96:99],v[136:139],v[180:183]
ds_read_b128 v[116:119],v4 offset:288
ds_read_b64_tr_b16 v[72:73],v1 offset:4096
v_lshlrev_b32_sdwa v154,1,v154 dst_sel:DWORD dst_unused:UNUSED_PAD src0_sel:DWORD src1_sel:WORD_1
v_mfma_f32_16x16x32_bf16 v[188:191],v[100:103],v[136:139],v[188:191]
ds_read_u16 v149,v149
v_lshlrev_b32_sdwa v153,1,v153 dst_sel:DWORD dst_unused:UNUSED_PAD src0_sel:DWORD src1_sel:WORD_1
v_mfma_f32_16x16x32_bf16 v[196:199],v[104:107],v[136:139],v[196:199]
ds_read_u16_d16_hi v151,v151
v_lshlrev_b32_sdwa v155,1,v155 dst_sel:DWORD dst_unused:UNUSED_PAD src0_sel:DWORD src1_sel:WORD_1
v_mfma_f32_16x16x32_bf16 v[204:207],v[108:111],v[136:139],v[204:207]
s_waitcnt lgkmcnt(8)
ds_read_b64_tr_b16 v[74:75],v1 offset:6144
ds_read_b64_tr_b16 v[76:77],v2 offset:4096
v_lshlrev_b32_sdwa v156,1,v156 dst_sel:DWORD dst_unused:UNUSED_PAD src0_sel:DWORD src1_sel:WORD_1
v_mfma_f32_16x16x32_bf16 v[128:131],v[64:67],v[16:19],v[112:115]
ds_read_u16 v152,v152
v_lshlrev_b32_sdwa v158,1,v158 dst_sel:DWORD dst_unused:UNUSED_PAD src0_sel:DWORD src1_sel:WORD_1
v_mfma_f32_16x16x32_bf16 v[136:139],v[64:67],v[20:23],v[112:115]
s_waitcnt lgkmcnt(6)
ds_read_u16_d16_hi v154,v154
ds_read_b64_tr_b16 v[78:79],v2 offset:6144
v_lshlrev_b32_sdwa v157,1,v157 dst_sel:DWORD dst_unused:UNUSED_PAD src0_sel:DWORD src1_sel:WORD_1
v_mfma_f32_16x16x32_bf16 v[140:143],v[68:71],v[20:23],v[116:119]
ds_read_b64_tr_b16 v[80:81],v1 offset:8192
ds_read_b64_tr_b16 v[82:83],v1 offset:10240
v_lshlrev_b32_sdwa v159,1,v159 dst_sel:DWORD dst_unused:UNUSED_PAD src0_sel:DWORD src1_sel:WORD_1
v_mfma_f32_16x16x32_bf16 v[132:135],v[68:71],v[16:19],v[116:119]
s_waitcnt lgkmcnt(6)
ds_read_u16 v153,v153
v_mfma_f32_16x16x32_bf16 v[128:131],v[72:75],v[24:27],v[128:131]
ds_read_u16_d16_hi v155,v155
v_mfma_f32_16x16x32_bf16 v[136:139],v[72:75],v[28:31],v[136:139]
s_waitcnt lgkmcnt(4)
ds_read_b64_tr_b16 v[84:85],v2 offset:8192
ds_read_b64_tr_b16 v[86:87],v2 offset:10240
v_mfma_f32_16x16x32_bf16 v[140:143],v[76:79],v[28:31],v[140:143]
ds_read_u16 v156,v156
v_mfma_f32_16x16x32_bf16 v[132:135],v[76:79],v[24:27],v[132:135]
s_waitcnt lgkmcnt(5)
ds_read_u16_d16_hi v158,v158
v_lshl_add_u64 v[144:145],v[144:145],0,v[146:147]
v_mfma_f32_16x16x32_bf16 v[128:131],v[80:83],v[32:35],v[128:131]
v_lshl_add_u64 v[146:147],v[148:149],0,v[150:151]
v_mfma_f32_16x16x32_bf16 v[136:139],v[80:83],v[36:39],v[136:139]
s_waitcnt lgkmcnt(2)
ds_read_u16 v157,v157
v_mfma_f32_16x16x32_bf16 v[140:143],v[84:87],v[36:39],v[140:143]
ds_read_u16_d16_hi v159,v159
v_mfma_f32_16x16x32_bf16 v[132:135],v[84:87],v[32:35],v[132:135]
s_nop 0
v_lshlrev_b32_sdwa v128,1,v128 dst_sel:DWORD dst_unused:UNUSED_PAD src0_sel:DWORD src1_sel:WORD_1
v_lshlrev_b32_sdwa v130,1,v130 dst_sel:DWORD dst_unused:UNUSED_PAD src0_sel:DWORD src1_sel:WORD_1
v_mfma_f32_16x16x32_bf16 v[208:211],v[88:91],v[144:147],v[208:211]
ds_read_u16 v128,v128
v_lshlrev_b32_sdwa v129,1,v129 dst_sel:DWORD dst_unused:UNUSED_PAD src0_sel:DWORD src1_sel:WORD_1
v_mfma_f32_16x16x32_bf16 v[216:219],v[92:95],v[144:147],v[216:219]
ds_read_u16_d16_hi v130,v130
v_lshlrev_b32_sdwa v131,1,v131 dst_sel:DWORD dst_unused:UNUSED_PAD src0_sel:DWORD src1_sel:WORD_1
v_mfma_f32_16x16x32_bf16 v[224:227],v[96:99],v[144:147],v[224:227]
v_lshlrev_b32_sdwa v132,1,v132 dst_sel:DWORD dst_unused:UNUSED_PAD src0_sel:DWORD src1_sel:WORD_1
v_mfma_f32_16x16x32_bf16 v[232:235],v[100:103],v[144:147],v[232:235]
ds_read_u16 v129,v129
v_lshl_add_u64 v[152:153],v[152:153],0,v[154:155]
v_lshlrev_b32_sdwa v134,1,v134 dst_sel:DWORD dst_unused:UNUSED_PAD src0_sel:DWORD src1_sel:WORD_1
v_mfma_f32_16x16x32_bf16 v[240:243],v[104:107],v[144:147],v[240:243]
s_waitcnt lgkmcnt(3)
ds_read_u16_d16_hi v131,v131
v_lshl_add_u64 v[154:155],v[156:157],0,v[158:159]
v_mfma_f32_16x16x32_bf16 v[248:251],v[108:111],v[144:147],v[248:251]
v_lshlrev_b32_sdwa v133,1,v133 dst_sel:DWORD dst_unused:UNUSED_PAD src0_sel:DWORD src1_sel:WORD_1
v_mfma_f32_16x16x32_bf16 v[212:215],v[88:91],v[152:155],v[212:215]
ds_read_u16 v132,v132
v_lshlrev_b32_sdwa v135,1,v135 dst_sel:DWORD dst_unused:UNUSED_PAD src0_sel:DWORD src1_sel:WORD_1
v_mfma_f32_16x16x32_bf16 v[220:223],v[92:95],v[152:155],v[220:223]
ds_read_u16_d16_hi v134,v134
v_lshlrev_b32_sdwa v136,1,v136 dst_sel:DWORD dst_unused:UNUSED_PAD src0_sel:DWORD src1_sel:WORD_1
v_mfma_f32_16x16x32_bf16 v[228:231],v[96:99],v[152:155],v[228:231]
ds_read_b64_tr_b16 v[88:89],v3 offset:6160
ds_read_b64_tr_b16 v[90:91],v3 offset:7696
v_lshlrev_b32_sdwa v138,1,v138 dst_sel:DWORD dst_unused:UNUSED_PAD src0_sel:DWORD src1_sel:WORD_1
v_mfma_f32_16x16x32_bf16 v[236:239],v[100:103],v[152:155],v[236:239]
ds_read_u16 v133,v133
v_lshlrev_b32_sdwa v137,1,v137 dst_sel:DWORD dst_unused:UNUSED_PAD src0_sel:DWORD src1_sel:WORD_1
v_mfma_f32_16x16x32_bf16 v[244:247],v[104:107],v[152:155],v[244:247]
ds_read_u16_d16_hi v135,v135
v_lshlrev_b32_sdwa v139,1,v139 dst_sel:DWORD dst_unused:UNUSED_PAD src0_sel:DWORD src1_sel:WORD_1
v_mfma_f32_16x16x32_bf16 v[252:255],v[108:111],v[152:155],v[252:255]
ds_read_b64_tr_b16 v[92:93],v3 offset:6168
ds_read_b64_tr_b16 v[94:95],v3 offset:7704
v_lshlrev_b32_sdwa v140,1,v140 dst_sel:DWORD dst_unused:UNUSED_PAD src0_sel:DWORD src1_sel:WORD_1
v_mfma_f32_16x16x32_bf16 v[144:147],v[64:67],v[40:43],v[112:115]
ds_read_u16 v136,v136
v_lshlrev_b32_sdwa v142,1,v142 dst_sel:DWORD dst_unused:UNUSED_PAD src0_sel:DWORD src1_sel:WORD_1
v_mfma_f32_16x16x32_bf16 v[152:155],v[64:67],v[44:47],v[112:115]
ds_read_u16_d16_hi v138,v138
v_lshlrev_b32_sdwa v141,1,v141 dst_sel:DWORD dst_unused:UNUSED_PAD src0_sel:DWORD src1_sel:WORD_1
v_mfma_f32_16x16x32_bf16 v[156:159],v[68:71],v[44:47],v[116:119]
ds_read_b64_tr_b16 v[96:97],v3 offset:6224
ds_read_b64_tr_b16 v[98:99],v3 offset:7760
v_lshlrev_b32_sdwa v143,1,v143 dst_sel:DWORD dst_unused:UNUSED_PAD src0_sel:DWORD src1_sel:WORD_1
v_mfma_f32_16x16x32_bf16 v[148:151],v[68:71],v[40:43],v[116:119]
ds_read_u16 v137,v137
v_mfma_f32_16x16x32_bf16 v[144:147],v[72:75],v[48:51],v[144:147]
ds_read_u16_d16_hi v139,v139
v_mfma_f32_16x16x32_bf16 v[152:155],v[72:75],v[52:55],v[152:155]
ds_read_b64_tr_b16 v[100:101],v3 offset:6232
ds_read_b64_tr_b16 v[102:103],v3 offset:7768
v_mfma_f32_16x16x32_bf16 v[156:159],v[76:79],v[52:55],v[156:159]
ds_read_u16 v140,v140
v_mfma_f32_16x16x32_bf16 v[148:151],v[76:79],v[48:51],v[148:151]
s_waitcnt lgkmcnt(15)
ds_read_u16_d16_hi v142,v142
v_lshl_add_u64 v[128:129],v[128:129],0,v[130:131]
v_mfma_f32_16x16x32_bf16 v[144:147],v[80:83],v[56:59],v[144:147]
s_waitcnt lgkmcnt(12)
ds_read_b64_tr_b16 v[104:105],v3 offset:6288
ds_read_b64_tr_b16 v[106:107],v3 offset:7824
v_lshl_add_u64 v[130:131],v[132:133],0,v[134:135]
v_mfma_f32_16x16x32_bf16 v[152:155],v[80:83],v[60:63],v[152:155]
ds_read_u16 v141,v141
v_mfma_f32_16x16x32_bf16 v[156:159],v[84:87],v[60:63],v[156:159]
ds_read_u16_d16_hi v143,v143
v_mfma_f32_16x16x32_bf16 v[148:151],v[84:87],v[56:59],v[148:151]
ds_read_b64_tr_b16 v[108:109],v3 offset:6296
ds_read_b64_tr_b16 v[110:111],v3 offset:7832
v_lshlrev_b32_sdwa v144,1,v144 dst_sel:DWORD dst_unused:UNUSED_PAD src0_sel:DWORD src1_sel:WORD_1
v_lshlrev_b32_sdwa v146,1,v146 dst_sel:DWORD dst_unused:UNUSED_PAD src0_sel:DWORD src1_sel:WORD_1
v_mfma_f32_16x16x32_bf16 v[160:163],v[88:91],v[128:131],v[160:163]
s_waitcnt lgkmcnt(15)
ds_read_u16 v144,v144
v_lshlrev_b32_sdwa v145,1,v145 dst_sel:DWORD dst_unused:UNUSED_PAD src0_sel:DWORD src1_sel:WORD_1
v_mfma_f32_16x16x32_bf16 v[168:171],v[92:95],v[128:131],v[168:171]
s_waitcnt lgkmcnt(13)
ds_read_u16_d16_hi v146,v146
v_lshlrev_b32_sdwa v147,1,v147 dst_sel:DWORD dst_unused:UNUSED_PAD src0_sel:DWORD src1_sel:WORD_1
v_mfma_f32_16x16x32_bf16 v[176:179],v[96:99],v[128:131],v[176:179]
s_waitcnt lgkmcnt(10)
ds_read_b64_tr_b16 v[64:65],v1 offset:512
ds_read_b64_tr_b16 v[66:67],v1 offset:2560
v_lshlrev_b32_sdwa v148,1,v148 dst_sel:DWORD dst_unused:UNUSED_PAD src0_sel:DWORD src1_sel:WORD_1
v_mfma_f32_16x16x32_bf16 v[184:187],v[100:103],v[128:131],v[184:187]
s_waitcnt lgkmcnt(8)
ds_read_u16 v145,v145
v_lshl_add_u64 v[136:137],v[136:137],0,v[138:139]
v_lshlrev_b32_sdwa v150,1,v150 dst_sel:DWORD dst_unused:UNUSED_PAD src0_sel:DWORD src1_sel:WORD_1
v_mfma_f32_16x16x32_bf16 v[192:195],v[104:107],v[128:131],v[192:195]
s_waitcnt lgkmcnt(5)
ds_read_u16_d16_hi v147,v147
v_lshl_add_u64 v[138:139],v[140:141],0,v[142:143]
v_mfma_f32_16x16x32_bf16 v[200:203],v[108:111],v[128:131],v[200:203]
ds_read_b128 v[112:115],v4 offset:384
ds_read_b64_tr_b16 v[68:69],v2 offset:512
v_lshlrev_b32_sdwa v149,1,v149 dst_sel:DWORD dst_unused:UNUSED_PAD src0_sel:DWORD src1_sel:WORD_1
v_mfma_f32_16x16x32_bf16 v[164:167],v[88:91],v[136:139],v[164:167]
ds_read_u16 v148,v148
v_lshlrev_b32_sdwa v151,1,v151 dst_sel:DWORD dst_unused:UNUSED_PAD src0_sel:DWORD src1_sel:WORD_1
v_mfma_f32_16x16x32_bf16 v[172:175],v[92:95],v[136:139],v[172:175]
ds_read_u16_d16_hi v150,v150
ds_read_b64_tr_b16 v[70:71],v2 offset:2560
v_lshlrev_b32_sdwa v152,1,v152 dst_sel:DWORD dst_unused:UNUSED_PAD src0_sel:DWORD src1_sel:WORD_1
v_mfma_f32_16x16x32_bf16 v[180:183],v[96:99],v[136:139],v[180:183]
ds_read_b128 v[116:119],v4 offset:416
ds_read_b64_tr_b16 v[72:73],v1 offset:4608
v_lshlrev_b32_sdwa v154,1,v154 dst_sel:DWORD dst_unused:UNUSED_PAD src0_sel:DWORD src1_sel:WORD_1
v_mfma_f32_16x16x32_bf16 v[188:191],v[100:103],v[136:139],v[188:191]
ds_read_u16 v149,v149
v_lshlrev_b32_sdwa v153,1,v153 dst_sel:DWORD dst_unused:UNUSED_PAD src0_sel:DWORD src1_sel:WORD_1
v_mfma_f32_16x16x32_bf16 v[196:199],v[104:107],v[136:139],v[196:199]
ds_read_u16_d16_hi v151,v151
v_lshlrev_b32_sdwa v155,1,v155 dst_sel:DWORD dst_unused:UNUSED_PAD src0_sel:DWORD src1_sel:WORD_1
v_mfma_f32_16x16x32_bf16 v[204:207],v[108:111],v[136:139],v[204:207]
s_waitcnt lgkmcnt(8)
ds_read_b64_tr_b16 v[74:75],v1 offset:6656
ds_read_b64_tr_b16 v[76:77],v2 offset:4608
v_lshlrev_b32_sdwa v156,1,v156 dst_sel:DWORD dst_unused:UNUSED_PAD src0_sel:DWORD src1_sel:WORD_1
v_mfma_f32_16x16x32_bf16 v[128:131],v[64:67],v[16:19],v[112:115]
ds_read_u16 v152,v152
v_lshlrev_b32_sdwa v158,1,v158 dst_sel:DWORD dst_unused:UNUSED_PAD src0_sel:DWORD src1_sel:WORD_1
v_mfma_f32_16x16x32_bf16 v[136:139],v[64:67],v[20:23],v[112:115]
s_waitcnt lgkmcnt(6)
ds_read_u16_d16_hi v154,v154
ds_read_b64_tr_b16 v[78:79],v2 offset:6656
v_lshlrev_b32_sdwa v157,1,v157 dst_sel:DWORD dst_unused:UNUSED_PAD src0_sel:DWORD src1_sel:WORD_1
v_mfma_f32_16x16x32_bf16 v[140:143],v[68:71],v[20:23],v[116:119]
ds_read_b64_tr_b16 v[80:81],v1 offset:8704
ds_read_b64_tr_b16 v[82:83],v1 offset:10752
v_lshlrev_b32_sdwa v159,1,v159 dst_sel:DWORD dst_unused:UNUSED_PAD src0_sel:DWORD src1_sel:WORD_1
v_mfma_f32_16x16x32_bf16 v[132:135],v[68:71],v[16:19],v[116:119]
s_waitcnt lgkmcnt(6)
ds_read_u16 v153,v153
v_mfma_f32_16x16x32_bf16 v[128:131],v[72:75],v[24:27],v[128:131]
ds_read_u16_d16_hi v155,v155
v_mfma_f32_16x16x32_bf16 v[136:139],v[72:75],v[28:31],v[136:139]
s_waitcnt lgkmcnt(4)
ds_read_b64_tr_b16 v[84:85],v2 offset:8704
ds_read_b64_tr_b16 v[86:87],v2 offset:10752
v_mfma_f32_16x16x32_bf16 v[140:143],v[76:79],v[28:31],v[140:143]
ds_read_u16 v156,v156
v_mfma_f32_16x16x32_bf16 v[132:135],v[76:79],v[24:27],v[132:135]
s_waitcnt lgkmcnt(5)
ds_read_u16_d16_hi v158,v158
v_lshl_add_u64 v[144:145],v[144:145],0,v[146:147]
v_mfma_f32_16x16x32_bf16 v[128:131],v[80:83],v[32:35],v[128:131]
v_lshl_add_u64 v[146:147],v[148:149],0,v[150:151]
v_mfma_f32_16x16x32_bf16 v[136:139],v[80:83],v[36:39],v[136:139]
s_waitcnt lgkmcnt(2)
ds_read_u16 v157,v157
v_mfma_f32_16x16x32_bf16 v[140:143],v[84:87],v[36:39],v[140:143]
ds_read_u16_d16_hi v159,v159
v_mfma_f32_16x16x32_bf16 v[132:135],v[84:87],v[32:35],v[132:135]
s_nop 0
v_lshlrev_b32_sdwa v128,1,v128 dst_sel:DWORD dst_unused:UNUSED_PAD src0_sel:DWORD src1_sel:WORD_1
v_lshlrev_b32_sdwa v130,1,v130 dst_sel:DWORD dst_unused:UNUSED_PAD src0_sel:DWORD src1_sel:WORD_1
v_mfma_f32_16x16x32_bf16 v[208:211],v[88:91],v[144:147],v[208:211]
s_cbranch_vccz RA40_%=
s_waitcnt vmcnt(0)
RA40_%=:
s_barrier
s_setprio 3
ds_read_u16 v128,v128
v_lshlrev_b32_sdwa v129,1,v129 dst_sel:DWORD dst_unused:UNUSED_PAD src0_sel:DWORD src1_sel:WORD_1
v_lshlrev_b32_sdwa v131,1,v131 dst_sel:DWORD dst_unused:UNUSED_PAD src0_sel:DWORD src1_sel:WORD_1
v_mfma_f32_16x16x32_bf16 v[216:219],v[92:95],v[144:147],v[216:219]
v_fma_mixlo_f16 v120,v140,s99,0
v_fma_mixlo_f16 v122,v141,s99,0
v_mfma_f32_16x16x32_bf16 v[224:227],v[96:99],v[144:147],v[224:227]
s_cbranch_vccz RA41_%=
s_add_u32 m0,s58,0
s_mul_i32 s69,s59,5
s_add_u32 s69,s69,s97
buffer_load_dwordx4 v5,s[44:47],s69 offen lds
s_add_u32 m0,m0,1024
s_add_u32 s69,s69,s53
buffer_load_dwordx4 v6,s[44:47],s69 offen lds
RA41_%=:
ds_read_u16_d16_hi v130,v130
v_lshlrev_b32_sdwa v132,1,v132 dst_sel:DWORD dst_unused:UNUSED_PAD src0_sel:DWORD src1_sel:WORD_1
v_lshlrev_b32_sdwa v134,1,v134 dst_sel:DWORD dst_unused:UNUSED_PAD src0_sel:DWORD src1_sel:WORD_1
v_mfma_f32_16x16x32_bf16 v[232:235],v[100:103],v[144:147],v[232:235]
v_lshl_add_u64 v[152:153],v[152:153],0,v[154:155]
v_mfma_f32_16x16x32_bf16 v[240:243],v[104:107],v[144:147],v[240:243]
s_waitcnt lgkmcnt(2)
ds_read_u16 v129,v129
v_lshl_add_u64 v[154:155],v[156:157],0,v[158:159]
v_mfma_f32_16x16x32_bf16 v[248:251],v[108:111],v[144:147],v[248:251]
v_fma_mixhi_f16 v120,v142,s99,0
v_fma_mixhi_f16 v122,v143,s99,0
v_mfma_f32_16x16x32_bf16 v[212:215],v[88:91],v[152:155],v[212:215]
s_cbranch_vccz RA42_%=
s_add_u32 m0,m0,1024
s_add_u32 s69,s69,s53
buffer_load_dwordx4 v5,s[44:47],s69 offen lds
s_add_u32 m0,m0,s50
s_add_u32 s69,s69,s53
buffer_load_dwordx4 v6,s[44:47],s69 offen lds
RA42_%=:
ds_read_u16_d16_hi v131,v131
v_lshlrev_b32_sdwa v133,1,v133 dst_sel:DWORD dst_unused:UNUSED_PAD src0_sel:DWORD src1_sel:WORD_1
v_lshlrev_b32_sdwa v135,1,v135 dst_sel:DWORD dst_unused:UNUSED_PAD src0_sel:DWORD src1_sel:WORD_1
v_mfma_f32_16x16x32_bf16 v[220:223],v[92:95],v[152:155],v[220:223]
ds_read_b64_tr_b16 v[88:89],v3 offset:12320
v_pk_fma_f16 v121,v120,v120,s54
v_pk_fma_f16 v123,v122,v122,s54
v_mfma_f32_16x16x32_bf16 v[228:231],v[96:99],v[152:155],v[228:231]
ds_read_u16 v132,v132
v_pk_mul_f16 v121,v120,v121
v_pk_mul_f16 v123,v122,v123
v_mfma_f32_16x16x32_bf16 v[236:239],v[100:103],v[152:155],v[236:239]
ds_read_b64_tr_b16 v[90:91],v3 offset:13856
v_lshlrev_b32_sdwa v136,1,v136 dst_sel:DWORD dst_unused:UNUSED_PAD src0_sel:DWORD src1_sel:WORD_1
v_lshlrev_b32_sdwa v138,1,v138 dst_sel:DWORD dst_unused:UNUSED_PAD src0_sel:DWORD src1_sel:WORD_1
v_mfma_f32_16x16x32_bf16 v[244:247],v[104:107],v[152:155],v[244:247]
s_cbranch_vccz RA43_%=
s_add_u32 m0,m0,1024
s_add_u32 s69,s69,s53
buffer_load_dwordx4 v5,s[44:47],s69 offen lds
s_add_u32 m0,m0,1024
s_add_u32 s69,s69,s53
buffer_load_dwordx4 v6,s[44:47],s69 offen lds
RA43_%=:
ds_read_u16_d16_hi v134,v134
v_exp_f16 v120,v121
v_mfma_f32_16x16x32_bf16 v[252:255],v[108:111],v[152:155],v[252:255]
ds_read_b64_tr_b16 v[92:93],v3 offset:12328
v_exp_f16 v122,v123
v_mfma_f32_16x16x32_bf16 v[144:147],v[64:67],v[40:43],v[112:115]
ds_read_u16 v133,v133
v_lshlrev_b32_sdwa v137,1,v137 dst_sel:DWORD dst_unused:UNUSED_PAD src0_sel:DWORD src1_sel:WORD_1
v_lshlrev_b32_sdwa v139,1,v139 dst_sel:DWORD dst_unused:UNUSED_PAD src0_sel:DWORD src1_sel:WORD_1
v_mfma_f32_16x16x32_bf16 v[152:155],v[64:67],v[44:47],v[112:115]
s_cbranch_vccz RA44_%=
s_add_u32 m0,m0,s50
s_add_u32 s69,s69,s53
buffer_load_dwordx4 v5,s[44:47],s69 offen lds
s_add_u32 m0,m0,1024
s_add_u32 s69,s69,s53
buffer_load_dwordx4 v6,s[44:47],s69 offen lds
RA44_%=:
ds_read_b64_tr_b16 v[94:95],v3 offset:13864
v_exp_f16_sdwa v120,v121 dst_sel:WORD_1 dst_unused:UNUSED_PRESERVE src0_sel:WORD_1
v_mfma_f32_16x16x32_bf16 v[156:159],v[68:71],v[44:47],v[116:119]
ds_read_u16_d16_hi v135,v135
v_exp_f16_sdwa v122,v123 dst_sel:WORD_1 dst_unused:UNUSED_PRESERVE src0_sel:WORD_1
v_mfma_f32_16x16x32_bf16 v[148:151],v[68:71],v[40:43],v[116:119]
ds_read_b64_tr_b16 v[96:97],v3 offset:12384
v_pk_fma_f16 v120,v120,s55,s55
v_pk_fma_f16 v122,v122,s55,s55
v_mfma_f32_16x16x32_bf16 v[144:147],v[72:75],v[48:51],v[144:147]
ds_read_u16 v136,v136
v_rcp_f16 v121,v120
v_mfma_f32_16x16x32_bf16 v[152:155],v[72:75],v[52:55],v[152:155]
s_cbranch_vccz RA45_%=
s_add_u32 m0,m0,1024
s_add_u32 s69,s69,s53
buffer_load_dwordx4 v5,s[44:47],s69 offen lds
s_add_u32 m0,m0,s50
s_add_u32 s69,s69,s53
buffer_load_dwordx4 v6,s[44:47],s69 offen lds
RA45_%=:
ds_read_b64_tr_b16 v[98:99],v3 offset:13920
ds_read_b64_tr_b16 v[100:101],v3 offset:12392
v_rcp_f16 v123,v122
v_mfma_f32_16x16x32_bf16 v[156:159],v[76:79],v[52:55],v[156:159]
ds_read_u16_d16_hi v138,v138
v_rcp_f16_sdwa v121,v120 dst_sel:WORD_1 dst_unused:UNUSED_PRESERVE src0_sel:WORD_1
v_mfma_f32_16x16x32_bf16 v[148:151],v[76:79],v[48:51],v[148:151]
ds_read_b64_tr_b16 v[102:103],v3 offset:13928
ds_read_b64_tr_b16 v[104:105],v3 offset:12448
v_rcp_f16_sdwa v123,v122 dst_sel:WORD_1 dst_unused:UNUSED_PRESERVE src0_sel:WORD_1
v_mfma_f32_16x16x32_bf16 v[144:147],v[80:83],v[56:59],v[144:147]
ds_read_u16 v137,v137
v_fma_mix_f32 v140,v140,v121,0 op_sel_hi:[0,1,0]
v_fma_mix_f32 v142,v142,v121,0 op_sel:[0,1,0] op_sel_hi:[0,1,0]
v_mfma_f32_16x16x32_bf16 v[152:155],v[80:83],v[60:63],v[152:155]
s_waitcnt lgkmcnt(8)
s_cbranch_vccz RA46_%=
s_add_u32 m0,m0,1024
s_add_u32 s69,s69,s53
buffer_load_dwordx4 v5,s[44:47],s69 offen lds
s_add_u32 m0,m0,1024
s_add_u32 s69,s69,s53
buffer_load_dwordx4 v6,s[44:47],s69 offen lds
RA46_%=:
ds_read_b64_tr_b16 v[106:107],v3 offset:13984
ds_read_b64_tr_b16 v[108:109],v3 offset:12456
v_lshl_add_u64 v[128:129],v[128:129],0,v[130:131]
v_lshl_add_u64 v[130:131],v[132:133],0,v[134:135]
v_mfma_f32_16x16x32_bf16 v[156:159],v[84:87],v[60:63],v[156:159]
ds_read_u16_d16_hi v139,v139
v_fma_mix_f32 v141,v141,v123,0 op_sel_hi:[0,1,0]
v_fma_mix_f32 v143,v143,v123,0 op_sel:[0,1,0] op_sel_hi:[0,1,0]
v_mfma_f32_16x16x32_bf16 v[148:151],v[84:87],v[56:59],v[148:151]
ds_read_b64_tr_b16 v[110:111],v3 offset:13992
ds_read_b64_tr_b16 v[64:65],v1 offset:12320
v_lshlrev_b32_sdwa v144,1,v144 dst_sel:DWORD dst_unused:UNUSED_PAD src0_sel:DWORD src1_sel:WORD_1
v_lshlrev_b32_sdwa v146,1,v146 dst_sel:DWORD dst_unused:UNUSED_PAD src0_sel:DWORD src1_sel:WORD_1
v_mfma_f32_16x16x32_bf16 v[160:163],v[88:91],v[128:131],v[160:163]
ds_read_u16 v144,v144
v_lshlrev_b32_sdwa v145,1,v145 dst_sel:DWORD dst_unused:UNUSED_PAD src0_sel:DWORD src1_sel:WORD_1
v_lshlrev_b32_sdwa v147,1,v147 dst_sel:DWORD dst_unused:UNUSED_PAD src0_sel:DWORD src1_sel:WORD_1
v_mfma_f32_16x16x32_bf16 v[168:171],v[92:95],v[128:131],v[168:171]
s_waitcnt lgkmcnt(11)
ds_read_b64_tr_b16 v[66:67],v1 offset:14368
ds_read_b128 v[112:115],v4 offset:512
v_fma_mixlo_f16 v120,v156,s99,0
v_fma_mixlo_f16 v122,v157,s99,0
v_mfma_f32_16x16x32_bf16 v[176:179],v[96:99],v[128:131],v[176:179]
s_waitcnt lgkmcnt(10)
ds_read_u16_d16_hi v146,v146
v_lshlrev_b32_sdwa v148,1,v148 dst_sel:DWORD dst_unused:UNUSED_PAD src0_sel:DWORD src1_sel:WORD_1
v_lshlrev_b32_sdwa v150,1,v150 dst_sel:DWORD dst_unused:UNUSED_PAD src0_sel:DWORD src1_sel:WORD_1
v_mfma_f32_16x16x32_bf16 v[184:187],v[100:103],v[128:131],v[184:187]
s_waitcnt lgkmcnt(6)
ds_read_b64_tr_b16 v[68:69],v2 offset:12320
ds_read_b64_tr_b16 v[70:71],v2 offset:14368
v_lshl_add_u64 v[136:137],v[136:137],0,v[138:139]
v_mfma_f32_16x16x32_bf16 v[192:195],v[104:107],v[128:131],v[192:195]
s_waitcnt lgkmcnt(7)
ds_read_u16 v145,v145
v_cvt_pk_bf16_f32 v138,v140,v142
v_cvt_pk_bf16_f32 v139,v141,v143
v_mfma_f32_16x16x32_bf16 v[200:203],v[108:111],v[128:131],v[200:203]
ds_read_b128 v[116:119],v4 offset:544
ds_read_b64_tr_b16 v[72:73],v1 offset:16416
v_fma_mixhi_f16 v120,v158,s99,0
v_fma_mixhi_f16 v122,v159,s99,0
v_mfma_f32_16x16x32_bf16 v[164:167],v[88:91],v[136:139],v[164:167]
ds_read_u16_d16_hi v147,v147
v_lshlrev_b32_sdwa v149,1,v149 dst_sel:DWORD dst_unused:UNUSED_PAD src0_sel:DWORD src1_sel:WORD_1
v_lshlrev_b32_sdwa v151,1,v151 dst_sel:DWORD dst_unused:UNUSED_PAD src0_sel:DWORD src1_sel:WORD_1
v_mfma_f32_16x16x32_bf16 v[172:175],v[92:95],v[136:139],v[172:175]
ds_read_b64_tr_b16 v[74:75],v1 offset:18464
ds_read_b64_tr_b16 v[76:77],v2 offset:16416
v_pk_fma_f16 v121,v120,v120,s54
v_pk_fma_f16 v123,v122,v122,s54
v_mfma_f32_16x16x32_bf16 v[180:183],v[96:99],v[136:139],v[180:183]
ds_read_u16 v148,v148
v_pk_mul_f16 v121,v120,v121
v_pk_mul_f16 v123,v122,v123
v_mfma_f32_16x16x32_bf16 v[188:191],v[100:103],v[136:139],v[188:191]
ds_read_b64_tr_b16 v[78:79],v2 offset:18464
v_lshlrev_b32_sdwa v152,1,v152 dst_sel:DWORD dst_unused:UNUSED_PAD src0_sel:DWORD src1_sel:WORD_1
v_lshlrev_b32_sdwa v154,1,v154 dst_sel:DWORD dst_unused:UNUSED_PAD src0_sel:DWORD src1_sel:WORD_1
v_mfma_f32_16x16x32_bf16 v[196:199],v[104:107],v[136:139],v[196:199]
ds_read_u16_d16_hi v150,v150
v_exp_f16 v120,v121
v_mfma_f32_16x16x32_bf16 v[204:207],v[108:111],v[136:139],v[204:207]
s_waitcnt lgkmcnt(12)
ds_read_b64_tr_b16 v[80:81],v1 offset:20512
v_exp_f16 v122,v123
v_mfma_f32_16x16x32_bf16 v[128:131],v[64:67],v[16:19],v[112:115]
ds_read_u16 v149,v149
v_lshlrev_b32_sdwa v153,1,v153 dst_sel:DWORD dst_unused:UNUSED_PAD src0_sel:DWORD src1_sel:WORD_1
v_lshlrev_b32_sdwa v155,1,v155 dst_sel:DWORD dst_unused:UNUSED_PAD src0_sel:DWORD src1_sel:WORD_1
v_mfma_f32_16x16x32_bf16 v[136:139],v[64:67],v[20:23],v[112:115]
s_waitcnt lgkmcnt(9)
ds_read_b64_tr_b16 v[82:83],v1 offset:22560
v_exp_f16_sdwa v120,v121 dst_sel:WORD_1 dst_unused:UNUSED_PRESERVE src0_sel:WORD_1
v_mfma_f32_16x16x32_bf16 v[140:143],v[68:71],v[20:23],v[116:119]
ds_read_u16_d16_hi v151,v151
v_exp_f16_sdwa v122,v123 dst_sel:WORD_1 dst_unused:UNUSED_PRESERVE src0_sel:WORD_1
v_mfma_f32_16x16x32_bf16 v[132:135],v[68:71],v[16:19],v[116:119]
s_waitcnt lgkmcnt(8)
ds_read_b64_tr_b16 v[84:85],v2 offset:20512
v_pk_fma_f16 v120,v120,s55,s55
v_pk_fma_f16 v122,v122,s55,s55
v_mfma_f32_16x16x32_bf16 v[128:131],v[72:75],v[24:27],v[128:131]
ds_read_u16 v152,v152
v_rcp_f16 v121,v120
v_mfma_f32_16x16x32_bf16 v[136:139],v[72:75],v[28:31],v[136:139]
s_waitcnt lgkmcnt(7)
ds_read_b64_tr_b16 v[86:87],v2 offset:22560
v_rcp_f16 v123,v122
v_mfma_f32_16x16x32_bf16 v[140:143],v[76:79],v[28:31],v[140:143]
ds_read_u16_d16_hi v154,v154
v_rcp_f16_sdwa v121,v120 dst_sel:WORD_1 dst_unused:UNUSED_PRESERVE src0_sel:WORD_1
v_mfma_f32_16x16x32_bf16 v[132:135],v[76:79],v[24:27],v[132:135]
s_waitcnt lgkmcnt(5)
v_rcp_f16_sdwa v123,v122 dst_sel:WORD_1 dst_unused:UNUSED_PRESERVE src0_sel:WORD_1
v_mfma_f32_16x16x32_bf16 v[128:131],v[80:83],v[32:35],v[128:131]
ds_read_u16 v153,v153
v_fma_mix_f32 v156,v156,v121,0 op_sel_hi:[0,1,0]
v_fma_mix_f32 v158,v158,v121,0 op_sel:[0,1,0] op_sel_hi:[0,1,0]
v_mfma_f32_16x16x32_bf16 v[136:139],v[80:83],v[36:39],v[136:139]
s_waitcnt lgkmcnt(2)
v_lshl_add_u64 v[144:145],v[144:145],0,v[146:147]
v_lshl_add_u64 v[146:147],v[148:149],0,v[150:151]
v_mfma_f32_16x16x32_bf16 v[140:143],v[84:87],v[36:39],v[140:143]
ds_read_u16_d16_hi v155,v155
v_fma_mix_f32 v157,v157,v123,0 op_sel_hi:[0,1,0]
v_fma_mix_f32 v159,v159,v123,0 op_sel:[0,1,0] op_sel_hi:[0,1,0]
v_mfma_f32_16x16x32_bf16 v[132:135],v[84:87],v[32:35],v[132:135]
v_lshlrev_b32_sdwa v128,1,v128 dst_sel:DWORD dst_unused:UNUSED_PAD src0_sel:DWORD src1_sel:WORD_1
v_lshlrev_b32_sdwa v130,1,v130 dst_sel:DWORD dst_unused:UNUSED_PAD src0_sel:DWORD src1_sel:WORD_1
v_mfma_f32_16x16x32_bf16 v[208:211],v[88:91],v[144:147],v[208:211]
ds_read_u16 v128,v128
v_lshlrev_b32_sdwa v129,1,v129 dst_sel:DWORD dst_unused:UNUSED_PAD src0_sel:DWORD src1_sel:WORD_1
v_lshlrev_b32_sdwa v131,1,v131 dst_sel:DWORD dst_unused:UNUSED_PAD src0_sel:DWORD src1_sel:WORD_1
v_mfma_f32_16x16x32_bf16 v[216:219],v[92:95],v[144:147],v[216:219]
v_fma_mixlo_f16 v120,v140,s99,0
v_fma_mixlo_f16 v122,v141,s99,0
v_mfma_f32_16x16x32_bf16 v[224:227],v[96:99],v[144:147],v[224:227]
ds_read_u16_d16_hi v130,v130
v_lshlrev_b32_sdwa v132,1,v132 dst_sel:DWORD dst_unused:UNUSED_PAD src0_sel:DWORD src1_sel:WORD_1
v_lshlrev_b32_sdwa v134,1,v134 dst_sel:DWORD dst_unused:UNUSED_PAD src0_sel:DWORD src1_sel:WORD_1
v_mfma_f32_16x16x32_bf16 v[232:235],v[100:103],v[144:147],v[232:235]
s_waitcnt lgkmcnt(2)
v_lshl_add_u64 v[152:153],v[152:153],0,v[154:155]
v_mfma_f32_16x16x32_bf16 v[240:243],v[104:107],v[144:147],v[240:243]
ds_read_u16 v129,v129
v_cvt_pk_bf16_f32 v154,v156,v158
v_cvt_pk_bf16_f32 v155,v157,v159
v_mfma_f32_16x16x32_bf16 v[248:251],v[108:111],v[144:147],v[248:251]
v_fma_mixhi_f16 v120,v142,s99,0
v_fma_mixhi_f16 v122,v143,s99,0
v_mfma_f32_16x16x32_bf16 v[212:215],v[88:91],v[152:155],v[212:215]
s_setprio 2
ds_read_u16_d16_hi v131,v131
v_lshlrev_b32_sdwa v133,1,v133 dst_sel:DWORD dst_unused:UNUSED_PAD src0_sel:DWORD src1_sel:WORD_1
v_lshlrev_b32_sdwa v135,1,v135 dst_sel:DWORD dst_unused:UNUSED_PAD src0_sel:DWORD src1_sel:WORD_1
v_mfma_f32_16x16x32_bf16 v[220:223],v[92:95],v[152:155],v[220:223]
ds_read_b64_tr_b16 v[88:89],v3 offset:18480
v_pk_fma_f16 v121,v120,v120,s54
v_pk_fma_f16 v123,v122,v122,s54
v_mfma_f32_16x16x32_bf16 v[228:231],v[96:99],v[152:155],v[228:231]
ds_read_u16 v132,v132
v_pk_mul_f16 v121,v120,v121
v_pk_mul_f16 v123,v122,v123
v_mfma_f32_16x16x32_bf16 v[236:239],v[100:103],v[152:155],v[236:239]
ds_read_b64_tr_b16 v[90:91],v3 offset:20016
v_lshlrev_b32_sdwa v136,1,v136 dst_sel:DWORD dst_unused:UNUSED_PAD src0_sel:DWORD src1_sel:WORD_1
v_lshlrev_b32_sdwa v138,1,v138 dst_sel:DWORD dst_unused:UNUSED_PAD src0_sel:DWORD src1_sel:WORD_1
v_mfma_f32_16x16x32_bf16 v[244:247],v[104:107],v[152:155],v[244:247]
ds_read_u16_d16_hi v134,v134
v_exp_f16 v120,v121
v_mfma_f32_16x16x32_bf16 v[252:255],v[108:111],v[152:155],v[252:255]
ds_read_b64_tr_b16 v[92:93],v3 offset:18488
v_exp_f16 v122,v123
v_mfma_f32_16x16x32_bf16 v[144:147],v[64:67],v[40:43],v[112:115]
ds_read_u16 v133,v133
v_lshlrev_b32_sdwa v137,1,v137 dst_sel:DWORD dst_unused:UNUSED_PAD src0_sel:DWORD src1_sel:WORD_1
v_lshlrev_b32_sdwa v139,1,v139 dst_sel:DWORD dst_unused:UNUSED_PAD src0_sel:DWORD src1_sel:WORD_1
v_mfma_f32_16x16x32_bf16 v[152:155],v[64:67],v[44:47],v[112:115]
ds_read_b64_tr_b16 v[94:95],v3 offset:20024
v_exp_f16_sdwa v120,v121 dst_sel:WORD_1 dst_unused:UNUSED_PRESERVE src0_sel:WORD_1
v_mfma_f32_16x16x32_bf16 v[156:159],v[68:71],v[44:47],v[116:119]
ds_read_u16_d16_hi v135,v135
v_exp_f16_sdwa v122,v123 dst_sel:WORD_1 dst_unused:UNUSED_PRESERVE src0_sel:WORD_1
v_mfma_f32_16x16x32_bf16 v[148:151],v[68:71],v[40:43],v[116:119]
ds_read_b64_tr_b16 v[96:97],v3 offset:18544
v_pk_fma_f16 v120,v120,s55,s55
v_pk_fma_f16 v122,v122,s55,s55
v_mfma_f32_16x16x32_bf16 v[144:147],v[72:75],v[48:51],v[144:147]
ds_read_u16 v136,v136
v_rcp_f16 v121,v120
v_mfma_f32_16x16x32_bf16 v[152:155],v[72:75],v[52:55],v[152:155]
ds_read_b64_tr_b16 v[98:99],v3 offset:20080
ds_read_b64_tr_b16 v[100:101],v3 offset:18552
v_rcp_f16 v123,v122
v_mfma_f32_16x16x32_bf16 v[156:159],v[76:79],v[52:55],v[156:159]
ds_read_u16_d16_hi v138,v138
v_rcp_f16_sdwa v121,v120 dst_sel:WORD_1 dst_unused:UNUSED_PRESERVE src0_sel:WORD_1
v_mfma_f32_16x16x32_bf16 v[148:151],v[76:79],v[48:51],v[148:151]
ds_read_b64_tr_b16 v[102:103],v3 offset:20088
ds_read_b64_tr_b16 v[104:105],v3 offset:18608
v_rcp_f16_sdwa v123,v122 dst_sel:WORD_1 dst_unused:UNUSED_PRESERVE src0_sel:WORD_1
v_mfma_f32_16x16x32_bf16 v[144:147],v[80:83],v[56:59],v[144:147]
ds_read_u16 v137,v137
v_fma_mix_f32 v140,v140,v121,0 op_sel_hi:[0,1,0]
v_fma_mix_f32 v142,v142,v121,0 op_sel:[0,1,0] op_sel_hi:[0,1,0]
v_mfma_f32_16x16x32_bf16 v[152:155],v[80:83],v[60:63],v[152:155]
s_waitcnt lgkmcnt(8)
ds_read_b64_tr_b16 v[106:107],v3 offset:20144
ds_read_b64_tr_b16 v[108:109],v3 offset:18616
v_lshl_add_u64 v[128:129],v[128:129],0,v[130:131]
v_lshl_add_u64 v[130:131],v[132:133],0,v[134:135]
v_mfma_f32_16x16x32_bf16 v[156:159],v[84:87],v[60:63],v[156:159]
ds_read_u16_d16_hi v139,v139
v_fma_mix_f32 v141,v141,v123,0 op_sel_hi:[0,1,0]
v_fma_mix_f32 v143,v143,v123,0 op_sel:[0,1,0] op_sel_hi:[0,1,0]
v_mfma_f32_16x16x32_bf16 v[148:151],v[84:87],v[56:59],v[148:151]
ds_read_b64_tr_b16 v[110:111],v3 offset:20152
ds_read_b64_tr_b16 v[64:65],v1 offset:12832
v_lshlrev_b32_sdwa v144,1,v144 dst_sel:DWORD dst_unused:UNUSED_PAD src0_sel:DWORD src1_sel:WORD_1
v_lshlrev_b32_sdwa v146,1,v146 dst_sel:DWORD dst_unused:UNUSED_PAD src0_sel:DWORD src1_sel:WORD_1
v_mfma_f32_16x16x32_bf16 v[160:163],v[88:91],v[128:131],v[160:163]
ds_read_u16 v144,v144
v_lshlrev_b32_sdwa v145,1,v145 dst_sel:DWORD dst_unused:UNUSED_PAD src0_sel:DWORD src1_sel:WORD_1
v_lshlrev_b32_sdwa v147,1,v147 dst_sel:DWORD dst_unused:UNUSED_PAD src0_sel:DWORD src1_sel:WORD_1
v_mfma_f32_16x16x32_bf16 v[168:171],v[92:95],v[128:131],v[168:171]
s_waitcnt lgkmcnt(11)
ds_read_b64_tr_b16 v[66:67],v1 offset:14880
ds_read_b128 v[112:115],v4 offset:640
v_fma_mixlo_f16 v120,v156,s99,0
v_fma_mixlo_f16 v122,v157,s99,0
v_mfma_f32_16x16x32_bf16 v[176:179],v[96:99],v[128:131],v[176:179]
s_waitcnt lgkmcnt(10)
ds_read_u16_d16_hi v146,v146
v_lshlrev_b32_sdwa v148,1,v148 dst_sel:DWORD dst_unused:UNUSED_PAD src0_sel:DWORD src1_sel:WORD_1
v_lshlrev_b32_sdwa v150,1,v150 dst_sel:DWORD dst_unused:UNUSED_PAD src0_sel:DWORD src1_sel:WORD_1
v_mfma_f32_16x16x32_bf16 v[184:187],v[100:103],v[128:131],v[184:187]
s_waitcnt lgkmcnt(6)
ds_read_b64_tr_b16 v[68:69],v2 offset:12832
ds_read_b64_tr_b16 v[70:71],v2 offset:14880
v_lshl_add_u64 v[136:137],v[136:137],0,v[138:139]
v_mfma_f32_16x16x32_bf16 v[192:195],v[104:107],v[128:131],v[192:195]
s_waitcnt lgkmcnt(7)
ds_read_u16 v145,v145
v_cvt_pk_bf16_f32 v138,v140,v142
v_cvt_pk_bf16_f32 v139,v141,v143
v_mfma_f32_16x16x32_bf16 v[200:203],v[108:111],v[128:131],v[200:203]
ds_read_b128 v[116:119],v4 offset:672
ds_read_b64_tr_b16 v[72:73],v1 offset:16928
v_fma_mixhi_f16 v120,v158,s99,0
v_fma_mixhi_f16 v122,v159,s99,0
v_mfma_f32_16x16x32_bf16 v[164:167],v[88:91],v[136:139],v[164:167]
ds_read_u16_d16_hi v147,v147
v_lshlrev_b32_sdwa v149,1,v149 dst_sel:DWORD dst_unused:UNUSED_PAD src0_sel:DWORD src1_sel:WORD_1
v_lshlrev_b32_sdwa v151,1,v151 dst_sel:DWORD dst_unused:UNUSED_PAD src0_sel:DWORD src1_sel:WORD_1
v_mfma_f32_16x16x32_bf16 v[172:175],v[92:95],v[136:139],v[172:175]
s_setprio 1
ds_read_b64_tr_b16 v[74:75],v1 offset:18976
ds_read_b64_tr_b16 v[76:77],v2 offset:16928
v_pk_fma_f16 v121,v120,v120,s54
v_pk_fma_f16 v123,v122,v122,s54
v_mfma_f32_16x16x32_bf16 v[180:183],v[96:99],v[136:139],v[180:183]
ds_read_u16 v148,v148
v_pk_mul_f16 v121,v120,v121
v_pk_mul_f16 v123,v122,v123
v_mfma_f32_16x16x32_bf16 v[188:191],v[100:103],v[136:139],v[188:191]
ds_read_b64_tr_b16 v[78:79],v2 offset:18976
v_lshlrev_b32_sdwa v152,1,v152 dst_sel:DWORD dst_unused:UNUSED_PAD src0_sel:DWORD src1_sel:WORD_1
v_lshlrev_b32_sdwa v154,1,v154 dst_sel:DWORD dst_unused:UNUSED_PAD src0_sel:DWORD src1_sel:WORD_1
v_mfma_f32_16x16x32_bf16 v[196:199],v[104:107],v[136:139],v[196:199]
ds_read_u16_d16_hi v150,v150
v_exp_f16 v120,v121
v_mfma_f32_16x16x32_bf16 v[204:207],v[108:111],v[136:139],v[204:207]
s_waitcnt lgkmcnt(12)
ds_read_b64_tr_b16 v[80:81],v1 offset:21024
v_exp_f16 v122,v123
v_mfma_f32_16x16x32_bf16 v[128:131],v[64:67],v[16:19],v[112:115]
ds_read_u16 v149,v149
v_lshlrev_b32_sdwa v153,1,v153 dst_sel:DWORD dst_unused:UNUSED_PAD src0_sel:DWORD src1_sel:WORD_1
v_lshlrev_b32_sdwa v155,1,v155 dst_sel:DWORD dst_unused:UNUSED_PAD src0_sel:DWORD src1_sel:WORD_1
v_mfma_f32_16x16x32_bf16 v[136:139],v[64:67],v[20:23],v[112:115]
s_waitcnt lgkmcnt(9)
ds_read_b64_tr_b16 v[82:83],v1 offset:23072
v_exp_f16_sdwa v120,v121 dst_sel:WORD_1 dst_unused:UNUSED_PRESERVE src0_sel:WORD_1
v_mfma_f32_16x16x32_bf16 v[140:143],v[68:71],v[20:23],v[116:119]
ds_read_u16_d16_hi v151,v151
v_exp_f16_sdwa v122,v123 dst_sel:WORD_1 dst_unused:UNUSED_PRESERVE src0_sel:WORD_1
v_mfma_f32_16x16x32_bf16 v[132:135],v[68:71],v[16:19],v[116:119]
s_waitcnt lgkmcnt(8)
ds_read_b64_tr_b16 v[84:85],v2 offset:21024
v_pk_fma_f16 v120,v120,s55,s55
v_pk_fma_f16 v122,v122,s55,s55
v_mfma_f32_16x16x32_bf16 v[128:131],v[72:75],v[24:27],v[128:131]
ds_read_u16 v152,v152
v_rcp_f16 v121,v120
v_mfma_f32_16x16x32_bf16 v[136:139],v[72:75],v[28:31],v[136:139]
s_waitcnt lgkmcnt(7)
ds_read_b64_tr_b16 v[86:87],v2 offset:23072
v_rcp_f16 v123,v122
v_mfma_f32_16x16x32_bf16 v[140:143],v[76:79],v[28:31],v[140:143]
ds_read_u16_d16_hi v154,v154
v_rcp_f16_sdwa v121,v120 dst_sel:WORD_1 dst_unused:UNUSED_PRESERVE src0_sel:WORD_1
v_mfma_f32_16x16x32_bf16 v[132:135],v[76:79],v[24:27],v[132:135]
s_waitcnt lgkmcnt(5)
v_rcp_f16_sdwa v123,v122 dst_sel:WORD_1 dst_unused:UNUSED_PRESERVE src0_sel:WORD_1
v_mfma_f32_16x16x32_bf16 v[128:131],v[80:83],v[32:35],v[128:131]
ds_read_u16 v153,v153
v_fma_mix_f32 v156,v156,v121,0 op_sel_hi:[0,1,0]
v_fma_mix_f32 v158,v158,v121,0 op_sel:[0,1,0] op_sel_hi:[0,1,0]
v_mfma_f32_16x16x32_bf16 v[136:139],v[80:83],v[36:39],v[136:139]
s_waitcnt lgkmcnt(2)
s_setprio 0
v_lshl_add_u64 v[144:145],v[144:145],0,v[146:147]
v_lshl_add_u64 v[146:147],v[148:149],0,v[150:151]
v_mfma_f32_16x16x32_bf16 v[140:143],v[84:87],v[36:39],v[140:143]
ds_read_u16_d16_hi v155,v155
v_fma_mix_f32 v157,v157,v123,0 op_sel_hi:[0,1,0]
v_fma_mix_f32 v159,v159,v123,0 op_sel:[0,1,0] op_sel_hi:[0,1,0]
v_mfma_f32_16x16x32_bf16 v[132:135],v[84:87],v[32:35],v[132:135]
v_lshlrev_b32_sdwa v128,1,v128 dst_sel:DWORD dst_unused:UNUSED_PAD src0_sel:DWORD src1_sel:WORD_1
v_lshlrev_b32_sdwa v130,1,v130 dst_sel:DWORD dst_unused:UNUSED_PAD src0_sel:DWORD src1_sel:WORD_1
v_mfma_f32_16x16x32_bf16 v[208:211],v[88:91],v[144:147],v[208:211]
s_cbranch_vccz RA47_%=
s_waitcnt vmcnt(0)
RA47_%=:
s_barrier
s_setprio 3
ds_read_u16 v128,v128
v_lshlrev_b32_sdwa v129,1,v129 dst_sel:DWORD dst_unused:UNUSED_PAD src0_sel:DWORD src1_sel:WORD_1
v_lshlrev_b32_sdwa v131,1,v131 dst_sel:DWORD dst_unused:UNUSED_PAD src0_sel:DWORD src1_sel:WORD_1
v_mfma_f32_16x16x32_bf16 v[216:219],v[92:95],v[144:147],v[216:219]
v_fma_mixlo_f16 v120,v140,s99,0
v_fma_mixlo_f16 v122,v141,s99,0
v_mfma_f32_16x16x32_bf16 v[224:227],v[96:99],v[144:147],v[224:227]
s_cbranch_vccz RA48_%=
s_add_u32 m0,s58,12320
s_mul_i32 s69,s59,7
s_add_u32 s69,s69,s97
buffer_load_dwordx4 v5,s[44:47],s69 offen lds
s_add_u32 m0,m0,1024
s_add_u32 s69,s69,s53
buffer_load_dwordx4 v6,s[44:47],s69 offen lds
RA48_%=:
ds_read_u16_d16_hi v130,v130
v_lshlrev_b32_sdwa v132,1,v132 dst_sel:DWORD dst_unused:UNUSED_PAD src0_sel:DWORD src1_sel:WORD_1
v_lshlrev_b32_sdwa v134,1,v134 dst_sel:DWORD dst_unused:UNUSED_PAD src0_sel:DWORD src1_sel:WORD_1
v_mfma_f32_16x16x32_bf16 v[232:235],v[100:103],v[144:147],v[232:235]
s_waitcnt lgkmcnt(2)
v_lshl_add_u64 v[152:153],v[152:153],0,v[154:155]
v_mfma_f32_16x16x32_bf16 v[240:243],v[104:107],v[144:147],v[240:243]
ds_read_u16 v129,v129
v_cvt_pk_bf16_f32 v154,v156,v158
v_cvt_pk_bf16_f32 v155,v157,v159
v_mfma_f32_16x16x32_bf16 v[248:251],v[108:111],v[144:147],v[248:251]
v_fma_mixhi_f16 v120,v142,s99,0
v_fma_mixhi_f16 v122,v143,s99,0
v_mfma_f32_16x16x32_bf16 v[212:215],v[88:91],v[152:155],v[212:215]
s_cbranch_vccz RA49_%=
s_add_u32 m0,m0,1024
s_add_u32 s69,s69,s53
buffer_load_dwordx4 v5,s[44:47],s69 offen lds
s_add_u32 m0,m0,s50
s_add_u32 s69,s69,s53
buffer_load_dwordx4 v6,s[44:47],s69 offen lds
RA49_%=:
ds_read_u16_d16_hi v131,v131
v_lshlrev_b32_sdwa v133,1,v133 dst_sel:DWORD dst_unused:UNUSED_PAD src0_sel:DWORD src1_sel:WORD_1
v_lshlrev_b32_sdwa v135,1,v135 dst_sel:DWORD dst_unused:UNUSED_PAD src0_sel:DWORD src1_sel:WORD_1
v_mfma_f32_16x16x32_bf16 v[220:223],v[92:95],v[152:155],v[220:223]
ds_read_b64_tr_b16 v[88:89],v3 offset:0
v_pk_fma_f16 v121,v120,v120,s54
v_pk_fma_f16 v123,v122,v122,s54
v_mfma_f32_16x16x32_bf16 v[228:231],v[96:99],v[152:155],v[228:231]
ds_read_u16 v132,v132
v_pk_mul_f16 v121,v120,v121
v_pk_mul_f16 v123,v122,v123
v_mfma_f32_16x16x32_bf16 v[236:239],v[100:103],v[152:155],v[236:239]
s_cbranch_vccz RA50_%=
s_add_u32 m0,m0,1024
s_add_u32 s69,s69,s53
buffer_load_dwordx4 v5,s[44:47],s69 offen lds
s_add_u32 m0,m0,1024
s_add_u32 s69,s69,s53
buffer_load_dwordx4 v6,s[44:47],s69 offen lds
RA50_%=:
ds_read_b64_tr_b16 v[90:91],v3 offset:1536
v_lshlrev_b32_sdwa v136,1,v136 dst_sel:DWORD dst_unused:UNUSED_PAD src0_sel:DWORD src1_sel:WORD_1
v_lshlrev_b32_sdwa v138,1,v138 dst_sel:DWORD dst_unused:UNUSED_PAD src0_sel:DWORD src1_sel:WORD_1
v_mfma_f32_16x16x32_bf16 v[244:247],v[104:107],v[152:155],v[244:247]
ds_read_u16_d16_hi v134,v134
v_exp_f16 v120,v121
v_mfma_f32_16x16x32_bf16 v[252:255],v[108:111],v[152:155],v[252:255]
ds_read_b64_tr_b16 v[92:93],v3 offset:8
v_exp_f16 v122,v123
v_mfma_f32_16x16x32_bf16 v[144:147],v[64:67],v[40:43],v[112:115]
ds_read_u16 v133,v133
v_lshlrev_b32_sdwa v137,1,v137 dst_sel:DWORD dst_unused:UNUSED_PAD src0_sel:DWORD src1_sel:WORD_1
v_lshlrev_b32_sdwa v139,1,v139 dst_sel:DWORD dst_unused:UNUSED_PAD src0_sel:DWORD src1_sel:WORD_1
v_mfma_f32_16x16x32_bf16 v[152:155],v[64:67],v[44:47],v[112:115]
s_cbranch_vccz RA51_%=
s_add_u32 m0,m0,s50
s_add_u32 s69,s69,s53
buffer_load_dwordx4 v5,s[44:47],s69 offen lds
s_add_u32 m0,m0,1024
s_add_u32 s69,s69,s53
buffer_load_dwordx4 v6,s[44:47],s69 offen lds
RA51_%=:
ds_read_b64_tr_b16 v[94:95],v3 offset:1544
v_exp_f16_sdwa v120,v121 dst_sel:WORD_1 dst_unused:UNUSED_PRESERVE src0_sel:WORD_1
v_mfma_f32_16x16x32_bf16 v[156:159],v[68:71],v[44:47],v[116:119]
ds_read_u16_d16_hi v135,v135
v_exp_f16_sdwa v122,v123 dst_sel:WORD_1 dst_unused:UNUSED_PRESERVE src0_sel:WORD_1
v_mfma_f32_16x16x32_bf16 v[148:151],v[68:71],v[40:43],v[116:119]
ds_read_b64_tr_b16 v[96:97],v3 offset:64
v_pk_fma_f16 v120,v120,s55,s55
v_pk_fma_f16 v122,v122,s55,s55
v_mfma_f32_16x16x32_bf16 v[144:147],v[72:75],v[48:51],v[144:147]
ds_read_u16 v136,v136
v_rcp_f16 v121,v120
v_mfma_f32_16x16x32_bf16 v[152:155],v[72:75],v[52:55],v[152:155]
s_cbranch_vccz RA52_%=
s_add_u32 m0,m0,1024
s_add_u32 s69,s69,s53
buffer_load_dwordx4 v5,s[44:47],s69 offen lds
s_add_u32 m0,m0,s50
s_add_u32 s69,s69,s53
buffer_load_dwordx4 v6,s[44:47],s69 offen lds
RA52_%=:
ds_read_b64_tr_b16 v[98:99],v3 offset:1600
ds_read_b64_tr_b16 v[100:101],v3 offset:72
v_rcp_f16 v123,v122
v_mfma_f32_16x16x32_bf16 v[156:159],v[76:79],v[52:55],v[156:159]
ds_read_u16_d16_hi v138,v138
v_rcp_f16_sdwa v121,v120 dst_sel:WORD_1 dst_unused:UNUSED_PRESERVE src0_sel:WORD_1
v_mfma_f32_16x16x32_bf16 v[148:151],v[76:79],v[48:51],v[148:151]
ds_read_b64_tr_b16 v[102:103],v3 offset:1608
ds_read_b64_tr_b16 v[104:105],v3 offset:128
v_rcp_f16_sdwa v123,v122 dst_sel:WORD_1 dst_unused:UNUSED_PRESERVE src0_sel:WORD_1
v_mfma_f32_16x16x32_bf16 v[144:147],v[80:83],v[56:59],v[144:147]
ds_read_u16 v137,v137
v_fma_mix_f32 v140,v140,v121,0 op_sel_hi:[0,1,0]
v_fma_mix_f32 v142,v142,v121,0 op_sel:[0,1,0] op_sel_hi:[0,1,0]
v_mfma_f32_16x16x32_bf16 v[152:155],v[80:83],v[60:63],v[152:155]
s_waitcnt lgkmcnt(8)
s_cbranch_vccz RA53_%=
s_add_u32 m0,m0,1024
s_add_u32 s69,s69,s53
buffer_load_dwordx4 v5,s[44:47],s69 offen lds
s_add_u32 m0,m0,1024
s_add_u32 s69,s69,s53
buffer_load_dwordx4 v6,s[44:47],s69 offen lds
RA53_%=:
ds_read_b64_tr_b16 v[106:107],v3 offset:1664
ds_read_b64_tr_b16 v[108:109],v3 offset:136
v_lshl_add_u64 v[128:129],v[128:129],0,v[130:131]
v_lshl_add_u64 v[130:131],v[132:133],0,v[134:135]
v_mfma_f32_16x16x32_bf16 v[156:159],v[84:87],v[60:63],v[156:159]
ds_read_u16_d16_hi v139,v139
v_fma_mix_f32 v141,v141,v123,0 op_sel_hi:[0,1,0]
v_fma_mix_f32 v143,v143,v123,0 op_sel:[0,1,0] op_sel_hi:[0,1,0]
v_mfma_f32_16x16x32_bf16 v[148:151],v[84:87],v[56:59],v[148:151]
ds_read_b64_tr_b16 v[110:111],v3 offset:1672
ds_read_b64_tr_b16 v[64:65],v1 offset:0
v_lshlrev_b32_sdwa v144,1,v144 dst_sel:DWORD dst_unused:UNUSED_PAD src0_sel:DWORD src1_sel:WORD_1
v_lshlrev_b32_sdwa v146,1,v146 dst_sel:DWORD dst_unused:UNUSED_PAD src0_sel:DWORD src1_sel:WORD_1
v_mfma_f32_16x16x32_bf16 v[160:163],v[88:91],v[128:131],v[160:163]
s_cbranch_vccz RA54_%=
s_add_u32 m0,s100,0
s_mul_i32 s69,s59,9
s_add_u32 s69,s69,s97
buffer_load_dwordx4 v5,s[44:47],s69 offen lds
s_add_u32 m0,m0,1024
s_add_u32 s69,s69,s53
buffer_load_dwordx4 v6,s[44:47],s69 offen lds
RA54_%=:
ds_read_u16 v144,v144
v_lshlrev_b32_sdwa v145,1,v145 dst_sel:DWORD dst_unused:UNUSED_PAD src0_sel:DWORD src1_sel:WORD_1
v_lshlrev_b32_sdwa v147,1,v147 dst_sel:DWORD dst_unused:UNUSED_PAD src0_sel:DWORD src1_sel:WORD_1
v_mfma_f32_16x16x32_bf16 v[168:171],v[92:95],v[128:131],v[168:171]
s_waitcnt lgkmcnt(11)
ds_read_b64_tr_b16 v[66:67],v1 offset:2048
ds_read_b128 v[112:115],v4 offset:768
v_fma_mixlo_f16 v120,v156,s99,0
v_fma_mixlo_f16 v122,v157,s99,0
v_mfma_f32_16x16x32_bf16 v[176:179],v[96:99],v[128:131],v[176:179]
s_waitcnt lgkmcnt(10)
ds_read_u16_d16_hi v146,v146
v_lshlrev_b32_sdwa v148,1,v148 dst_sel:DWORD dst_unused:UNUSED_PAD src0_sel:DWORD src1_sel:WORD_1
v_lshlrev_b32_sdwa v150,1,v150 dst_sel:DWORD dst_unused:UNUSED_PAD src0_sel:DWORD src1_sel:WORD_1
v_mfma_f32_16x16x32_bf16 v[184:187],v[100:103],v[128:131],v[184:187]
s_waitcnt lgkmcnt(6)
ds_read_b64_tr_b16 v[68:69],v2 offset:0
ds_read_b64_tr_b16 v[70:71],v2 offset:2048
v_lshl_add_u64 v[136:137],v[136:137],0,v[138:139]
v_mfma_f32_16x16x32_bf16 v[192:195],v[104:107],v[128:131],v[192:195]
s_waitcnt lgkmcnt(7)
s_cbranch_vccz RA55_%=
s_add_u32 m0,m0,1024
s_add_u32 s69,s69,s53
buffer_load_dwordx4 v5,s[44:47],s69 offen lds
s_add_u32 m0,m0,s50
s_add_u32 s69,s69,s53
buffer_load_dwordx4 v6,s[44:47],s69 offen lds
RA55_%=:
ds_read_u16 v145,v145
v_cvt_pk_bf16_f32 v138,v140,v142
v_cvt_pk_bf16_f32 v139,v141,v143
v_mfma_f32_16x16x32_bf16 v[200:203],v[108:111],v[128:131],v[200:203]
ds_read_b128 v[116:119],v4 offset:800
ds_read_b64_tr_b16 v[72:73],v1 offset:4096
v_fma_mixhi_f16 v120,v158,s99,0
v_fma_mixhi_f16 v122,v159,s99,0
v_mfma_f32_16x16x32_bf16 v[164:167],v[88:91],v[136:139],v[164:167]
ds_read_u16_d16_hi v147,v147
v_lshlrev_b32_sdwa v149,1,v149 dst_sel:DWORD dst_unused:UNUSED_PAD src0_sel:DWORD src1_sel:WORD_1
v_lshlrev_b32_sdwa v151,1,v151 dst_sel:DWORD dst_unused:UNUSED_PAD src0_sel:DWORD src1_sel:WORD_1
v_mfma_f32_16x16x32_bf16 v[172:175],v[92:95],v[136:139],v[172:175]
ds_read_b64_tr_b16 v[74:75],v1 offset:6144
ds_read_b64_tr_b16 v[76:77],v2 offset:4096
v_pk_fma_f16 v121,v120,v120,s54
v_pk_fma_f16 v123,v122,v122,s54
v_mfma_f32_16x16x32_bf16 v[180:183],v[96:99],v[136:139],v[180:183]
s_cbranch_vccz RA56_%=
s_add_u32 m0,m0,1024
s_add_u32 s69,s69,s53
buffer_load_dwordx4 v5,s[44:47],s69 offen lds
s_add_u32 m0,m0,1024
s_add_u32 s69,s69,s53
buffer_load_dwordx4 v6,s[44:47],s69 offen lds
RA56_%=:
ds_read_u16 v148,v148
v_pk_mul_f16 v121,v120,v121
v_pk_mul_f16 v123,v122,v123
v_mfma_f32_16x16x32_bf16 v[188:191],v[100:103],v[136:139],v[188:191]
ds_read_b64_tr_b16 v[78:79],v2 offset:6144
v_lshlrev_b32_sdwa v152,1,v152 dst_sel:DWORD dst_unused:UNUSED_PAD src0_sel:DWORD src1_sel:WORD_1
v_lshlrev_b32_sdwa v154,1,v154 dst_sel:DWORD dst_unused:UNUSED_PAD src0_sel:DWORD src1_sel:WORD_1
v_mfma_f32_16x16x32_bf16 v[196:199],v[104:107],v[136:139],v[196:199]
ds_read_u16_d16_hi v150,v150
v_exp_f16 v120,v121
v_mfma_f32_16x16x32_bf16 v[204:207],v[108:111],v[136:139],v[204:207]
s_waitcnt lgkmcnt(12)
ds_read_b64_tr_b16 v[80:81],v1 offset:8192
v_exp_f16 v122,v123
v_mfma_f32_16x16x32_bf16 v[128:131],v[64:67],v[16:19],v[112:115]
s_cbranch_vccz RA57_%=
s_add_u32 m0,m0,s50
s_add_u32 s69,s69,s53
buffer_load_dwordx4 v5,s[44:47],s69 offen lds
s_add_u32 m0,m0,1024
s_add_u32 s69,s69,s53
buffer_load_dwordx4 v6,s[44:47],s69 offen lds
RA57_%=:
ds_read_u16 v149,v149
v_lshlrev_b32_sdwa v153,1,v153 dst_sel:DWORD dst_unused:UNUSED_PAD src0_sel:DWORD src1_sel:WORD_1
v_lshlrev_b32_sdwa v155,1,v155 dst_sel:DWORD dst_unused:UNUSED_PAD src0_sel:DWORD src1_sel:WORD_1
v_mfma_f32_16x16x32_bf16 v[136:139],v[64:67],v[20:23],v[112:115]
s_waitcnt lgkmcnt(9)
ds_read_b64_tr_b16 v[82:83],v1 offset:10240
v_exp_f16_sdwa v120,v121 dst_sel:WORD_1 dst_unused:UNUSED_PRESERVE src0_sel:WORD_1
v_mfma_f32_16x16x32_bf16 v[140:143],v[68:71],v[20:23],v[116:119]
ds_read_u16_d16_hi v151,v151
v_exp_f16_sdwa v122,v123 dst_sel:WORD_1 dst_unused:UNUSED_PRESERVE src0_sel:WORD_1
v_mfma_f32_16x16x32_bf16 v[132:135],v[68:71],v[16:19],v[116:119]
s_waitcnt lgkmcnt(8)
ds_read_b64_tr_b16 v[84:85],v2 offset:8192
v_pk_fma_f16 v120,v120,s55,s55
v_pk_fma_f16 v122,v122,s55,s55
v_mfma_f32_16x16x32_bf16 v[128:131],v[72:75],v[24:27],v[128:131]
s_cbranch_vccz RA58_%=
s_add_u32 m0,m0,1024
s_add_u32 s69,s69,s53
buffer_load_dwordx4 v5,s[44:47],s69 offen lds
s_add_u32 m0,m0,s50
s_add_u32 s69,s69,s53
buffer_load_dwordx4 v6,s[44:47],s69 offen lds
RA58_%=:
ds_read_u16 v152,v152
v_rcp_f16 v121,v120
v_mfma_f32_16x16x32_bf16 v[136:139],v[72:75],v[28:31],v[136:139]
s_waitcnt lgkmcnt(7)
ds_read_b64_tr_b16 v[86:87],v2 offset:10240
v_rcp_f16 v123,v122
v_mfma_f32_16x16x32_bf16 v[140:143],v[76:79],v[28:31],v[140:143]
ds_read_u16_d16_hi v154,v154
v_rcp_f16_sdwa v121,v120 dst_sel:WORD_1 dst_unused:UNUSED_PRESERVE src0_sel:WORD_1
v_mfma_f32_16x16x32_bf16 v[132:135],v[76:79],v[24:27],v[132:135]
s_waitcnt lgkmcnt(5)
s_cbranch_vccz RA59_%=
s_add_u32 m0,m0,1024
s_add_u32 s69,s69,s53
buffer_load_dwordx4 v5,s[44:47],s69 offen lds
s_add_u32 m0,m0,1024
s_add_u32 s69,s69,s53
buffer_load_dwordx4 v6,s[44:47],s69 offen lds
RA59_%=:
v_rcp_f16_sdwa v123,v122 dst_sel:WORD_1 dst_unused:UNUSED_PRESERVE src0_sel:WORD_1
v_mfma_f32_16x16x32_bf16 v[128:131],v[80:83],v[32:35],v[128:131]
ds_read_u16 v153,v153
v_fma_mix_f32 v156,v156,v121,0 op_sel_hi:[0,1,0]
v_fma_mix_f32 v158,v158,v121,0 op_sel:[0,1,0] op_sel_hi:[0,1,0]
v_mfma_f32_16x16x32_bf16 v[136:139],v[80:83],v[36:39],v[136:139]
s_waitcnt lgkmcnt(2)
v_lshl_add_u64 v[144:145],v[144:145],0,v[146:147]
v_lshl_add_u64 v[146:147],v[148:149],0,v[150:151]
v_mfma_f32_16x16x32_bf16 v[140:143],v[84:87],v[36:39],v[140:143]
ds_read_u16_d16_hi v155,v155
v_fma_mix_f32 v157,v157,v123,0 op_sel_hi:[0,1,0]
v_fma_mix_f32 v159,v159,v123,0 op_sel:[0,1,0] op_sel_hi:[0,1,0]
v_mfma_f32_16x16x32_bf16 v[132:135],v[84:87],v[32:35],v[132:135]
s_cbranch_vccz RA60_%=
s_add_u32 m0,s100,12320
s_mov_b32 s69,s60
buffer_load_dwordx4 v5,s[44:47],s69 offen lds
s_add_u32 m0,m0,1024
s_add_u32 s69,s69,s53
buffer_load_dwordx4 v6,s[44:47],s69 offen lds
RA60_%=:
v_lshlrev_b32_sdwa v128,1,v128 dst_sel:DWORD dst_unused:UNUSED_PAD src0_sel:DWORD src1_sel:WORD_1
v_lshlrev_b32_sdwa v130,1,v130 dst_sel:DWORD dst_unused:UNUSED_PAD src0_sel:DWORD src1_sel:WORD_1
v_mfma_f32_16x16x32_bf16 v[208:211],v[88:91],v[144:147],v[208:211]
ds_read_u16 v128,v128
v_lshlrev_b32_sdwa v129,1,v129 dst_sel:DWORD dst_unused:UNUSED_PAD src0_sel:DWORD src1_sel:WORD_1
v_lshlrev_b32_sdwa v131,1,v131 dst_sel:DWORD dst_unused:UNUSED_PAD src0_sel:DWORD src1_sel:WORD_1
v_mfma_f32_16x16x32_bf16 v[216:219],v[92:95],v[144:147],v[216:219]
v_fma_mixlo_f16 v120,v140,s99,0
v_fma_mixlo_f16 v122,v141,s99,0
v_mfma_f32_16x16x32_bf16 v[224:227],v[96:99],v[144:147],v[224:227]
s_setprio 2
ds_read_u16_d16_hi v130,v130
v_lshlrev_b32_sdwa v132,1,v132 dst_sel:DWORD dst_unused:UNUSED_PAD src0_sel:DWORD src1_sel:WORD_1
v_lshlrev_b32_sdwa v134,1,v134 dst_sel:DWORD dst_unused:UNUSED_PAD src0_sel:DWORD src1_sel:WORD_1
v_mfma_f32_16x16x32_bf16 v[232:235],v[100:103],v[144:147],v[232:235]
s_waitcnt lgkmcnt(2)
s_cbranch_vccz RA61_%=
s_add_u32 m0,m0,1024
s_add_u32 s69,s69,s53
buffer_load_dwordx4 v5,s[44:47],s69 offen lds
s_add_u32 m0,m0,s50
s_add_u32 s69,s69,s53
buffer_load_dwordx4 v6,s[44:47],s69 offen lds
RA61_%=:
v_lshl_add_u64 v[152:153],v[152:153],0,v[154:155]
v_mfma_f32_16x16x32_bf16 v[240:243],v[104:107],v[144:147],v[240:243]
ds_read_u16 v129,v129
v_cvt_pk_bf16_f32 v154,v156,v158
v_cvt_pk_bf16_f32 v155,v157,v159
v_mfma_f32_16x16x32_bf16 v[248:251],v[108:111],v[144:147],v[248:251]
v_fma_mixhi_f16 v120,v142,s99,0
v_fma_mixhi_f16 v122,v143,s99,0
v_mfma_f32_16x16x32_bf16 v[212:215],v[88:91],v[152:155],v[212:215]
ds_read_u16_d16_hi v131,v131
v_lshlrev_b32_sdwa v133,1,v133 dst_sel:DWORD dst_unused:UNUSED_PAD src0_sel:DWORD src1_sel:WORD_1
v_lshlrev_b32_sdwa v135,1,v135 dst_sel:DWORD dst_unused:UNUSED_PAD src0_sel:DWORD src1_sel:WORD_1
v_mfma_f32_16x16x32_bf16 v[220:223],v[92:95],v[152:155],v[220:223]
s_cbranch_vccz RA62_%=
s_add_u32 m0,m0,1024
s_add_u32 s69,s69,s53
buffer_load_dwordx4 v5,s[44:47],s69 offen lds
s_add_u32 m0,m0,1024
s_add_u32 s69,s69,s53
buffer_load_dwordx4 v6,s[44:47],s69 offen lds
RA62_%=:
ds_read_b64_tr_b16 v[88:89],v3 offset:6160
v_pk_fma_f16 v121,v120,v120,s54
v_pk_fma_f16 v123,v122,v122,s54
v_mfma_f32_16x16x32_bf16 v[228:231],v[96:99],v[152:155],v[228:231]
ds_read_u16 v132,v132
v_pk_mul_f16 v121,v120,v121
v_pk_mul_f16 v123,v122,v123
v_mfma_f32_16x16x32_bf16 v[236:239],v[100:103],v[152:155],v[236:239]
ds_read_b64_tr_b16 v[90:91],v3 offset:7696
v_lshlrev_b32_sdwa v136,1,v136 dst_sel:DWORD dst_unused:UNUSED_PAD src0_sel:DWORD src1_sel:WORD_1
v_lshlrev_b32_sdwa v138,1,v138 dst_sel:DWORD dst_unused:UNUSED_PAD src0_sel:DWORD src1_sel:WORD_1
v_mfma_f32_16x16x32_bf16 v[244:247],v[104:107],v[152:155],v[244:247]
ds_read_u16_d16_hi v134,v134
v_exp_f16 v120,v121
v_mfma_f32_16x16x32_bf16 v[252:255],v[108:111],v[152:155],v[252:255]
s_cbranch_vccz RA63_%=
s_add_u32 m0,m0,s50
s_add_u32 s69,s69,s51
buffer_load_dwordx4 v5,s[44:47],s69 offen lds
s_add_u32 m0,m0,1024
s_add_u32 s69,s69,s53
buffer_load_dwordx4 v6,s[44:47],s69 offen lds
RA63_%=:
ds_read_b64_tr_b16 v[92:93],v3 offset:6168
v_exp_f16 v122,v123
v_mfma_f32_16x16x32_bf16 v[144:147],v[64:67],v[40:43],v[112:115]
ds_read_u16 v133,v133
v_lshlrev_b32_sdwa v137,1,v137 dst_sel:DWORD dst_unused:UNUSED_PAD src0_sel:DWORD src1_sel:WORD_1
v_lshlrev_b32_sdwa v139,1,v139 dst_sel:DWORD dst_unused:UNUSED_PAD src0_sel:DWORD src1_sel:WORD_1
v_mfma_f32_16x16x32_bf16 v[152:155],v[64:67],v[44:47],v[112:115]
ds_read_b64_tr_b16 v[94:95],v3 offset:7704
v_exp_f16_sdwa v120,v121 dst_sel:WORD_1 dst_unused:UNUSED_PRESERVE src0_sel:WORD_1
v_mfma_f32_16x16x32_bf16 v[156:159],v[68:71],v[44:47],v[116:119]
s_cbranch_vccz RA64_%=
s_add_u32 m0,m0,1024
s_add_u32 s69,s69,s53
buffer_load_dwordx4 v5,s[44:47],s69 offen lds
s_add_u32 m0,m0,s50
s_add_u32 s69,s69,s53
buffer_load_dwordx4 v6,s[44:47],s69 offen lds
RA64_%=:
ds_read_u16_d16_hi v135,v135
v_exp_f16_sdwa v122,v123 dst_sel:WORD_1 dst_unused:UNUSED_PRESERVE src0_sel:WORD_1
v_mfma_f32_16x16x32_bf16 v[148:151],v[68:71],v[40:43],v[116:119]
ds_read_b64_tr_b16 v[96:97],v3 offset:6224
v_pk_fma_f16 v120,v120,s55,s55
v_pk_fma_f16 v122,v122,s55,s55
v_mfma_f32_16x16x32_bf16 v[144:147],v[72:75],v[48:51],v[144:147]
ds_read_u16 v136,v136
v_rcp_f16 v121,v120
v_mfma_f32_16x16x32_bf16 v[152:155],v[72:75],v[52:55],v[152:155]
ds_read_b64_tr_b16 v[98:99],v3 offset:7760
ds_read_b64_tr_b16 v[100:101],v3 offset:6232
v_rcp_f16 v123,v122
v_mfma_f32_16x16x32_bf16 v[156:159],v[76:79],v[52:55],v[156:159]
s_cbranch_vccz RA65_%=
s_add_u32 m0,m0,1024
s_add_u32 s69,s69,s53
buffer_load_dwordx4 v5,s[44:47],s69 offen lds
s_add_u32 m0,m0,1024
s_add_u32 s69,s69,s53
buffer_load_dwordx4 v6,s[44:47],s69 offen lds
RA65_%=:
ds_read_u16_d16_hi v138,v138
v_rcp_f16_sdwa v121,v120 dst_sel:WORD_1 dst_unused:UNUSED_PRESERVE src0_sel:WORD_1
v_mfma_f32_16x16x32_bf16 v[148:151],v[76:79],v[48:51],v[148:151]
ds_read_b64_tr_b16 v[102:103],v3 offset:7768
ds_read_b64_tr_b16 v[104:105],v3 offset:6288
v_rcp_f16_sdwa v123,v122 dst_sel:WORD_1 dst_unused:UNUSED_PRESERVE src0_sel:WORD_1
v_mfma_f32_16x16x32_bf16 v[144:147],v[80:83],v[56:59],v[144:147]
ds_read_u16 v137,v137
v_fma_mix_f32 v140,v140,v121,0 op_sel_hi:[0,1,0]
v_fma_mix_f32 v142,v142,v121,0 op_sel:[0,1,0] op_sel_hi:[0,1,0]
v_mfma_f32_16x16x32_bf16 v[152:155],v[80:83],v[60:63],v[152:155]
s_waitcnt lgkmcnt(8)
ds_read_b64_tr_b16 v[106:107],v3 offset:7824
ds_read_b64_tr_b16 v[108:109],v3 offset:6296
v_lshl_add_u64 v[128:129],v[128:129],0,v[130:131]
v_lshl_add_u64 v[130:131],v[132:133],0,v[134:135]
v_mfma_f32_16x16x32_bf16 v[156:159],v[84:87],v[60:63],v[156:159]
s_cbranch_vccz RA66_%=
s_add_u32 m0,s100,24640
s_mul_i32 s69,s59,1
s_add_u32 s69,s69,s97
buffer_load_dwordx4 v5,s[44:47],s69 offen lds
s_add_u32 m0,m0,1024
s_add_u32 s69,s69,s53
buffer_load_dwordx4 v6,s[44:47],s69 offen lds
RA66_%=:
ds_read_u16_d16_hi v139,v139
v_fma_mix_f32 v141,v141,v123,0 op_sel_hi:[0,1,0]
v_fma_mix_f32 v143,v143,v123,0 op_sel:[0,1,0] op_sel_hi:[0,1,0]
v_mfma_f32_16x16x32_bf16 v[148:151],v[84:87],v[56:59],v[148:151]
ds_read_b64_tr_b16 v[110:111],v3 offset:7832
ds_read_b128 v[64:67],v4 offset:1536
v_lshlrev_b32_sdwa v144,1,v144 dst_sel:DWORD dst_unused:UNUSED_PAD src0_sel:DWORD src1_sel:WORD_1
v_lshlrev_b32_sdwa v146,1,v146 dst_sel:DWORD dst_unused:UNUSED_PAD src0_sel:DWORD src1_sel:WORD_1
v_mfma_f32_16x16x32_bf16 v[160:163],v[88:91],v[128:131],v[160:163]
ds_read_u16 v144,v144
ds_read_b128 v[68:71],v4 offset:1568
v_lshlrev_b32_sdwa v145,1,v145 dst_sel:DWORD dst_unused:UNUSED_PAD src0_sel:DWORD src1_sel:WORD_1
v_lshlrev_b32_sdwa v147,1,v147 dst_sel:DWORD dst_unused:UNUSED_PAD src0_sel:DWORD src1_sel:WORD_1
v_fma_mixlo_f16 v120,v156,s99,0
v_fma_mixlo_f16 v122,v157,s99,0
v_mfma_f32_16x16x32_bf16 v[168:171],v[92:95],v[128:131],v[168:171]
s_waitcnt lgkmcnt(12)
s_cbranch_vccz RA67_%=
s_add_u32 m0,m0,1024
s_add_u32 s69,s69,s53
buffer_load_dwordx4 v5,s[44:47],s69 offen lds
s_add_u32 m0,m0,s50
s_add_u32 s69,s69,s53
buffer_load_dwordx4 v6,s[44:47],s69 offen lds
RA67_%=:
ds_read_u16_d16_hi v146,v146
ds_read_b128 v[72:75],v4 offset:1664
v_lshlrev_b32_sdwa v148,1,v148 dst_sel:DWORD dst_unused:UNUSED_PAD src0_sel:DWORD src1_sel:WORD_1
v_lshlrev_b32_sdwa v150,1,v150 dst_sel:DWORD dst_unused:UNUSED_PAD src0_sel:DWORD src1_sel:WORD_1
s_waitcnt lgkmcnt(6)
v_lshl_add_u64 v[136:137],v[136:137],0,v[138:139]
v_cvt_pk_bf16_f32 v138,v140,v142
v_mfma_f32_16x16x32_bf16 v[176:179],v[96:99],v[128:131],v[176:179]
s_setprio 1
ds_read_u16 v145,v145
ds_read_b128 v[76:79],v4 offset:1696
v_cvt_pk_bf16_f32 v139,v141,v143
v_fma_mixhi_f16 v120,v158,s99,0
v_fma_mixhi_f16 v122,v159,s99,0
v_lshlrev_b32_sdwa v149,1,v149 dst_sel:DWORD dst_unused:UNUSED_PAD src0_sel:DWORD src1_sel:WORD_1
v_mfma_f32_16x16x32_bf16 v[184:187],v[100:103],v[128:131],v[184:187]
ds_read_u16_d16_hi v147,v147
ds_read_b128 v[80:83],v4 offset:1792
v_lshlrev_b32_sdwa v151,1,v151 dst_sel:DWORD dst_unused:UNUSED_PAD src0_sel:DWORD src1_sel:WORD_1
v_pk_fma_f16 v121,v120,v120,s54
v_pk_fma_f16 v123,v122,v122,s54
v_pk_mul_f16 v121,v120,v121
v_mfma_f32_16x16x32_bf16 v[192:195],v[104:107],v[128:131],v[192:195]
s_waitcnt lgkmcnt(9)
s_cbranch_vccz RA68_%=
s_add_u32 m0,m0,1024
s_add_u32 s69,s69,s53
buffer_load_dwordx4 v5,s[44:47],s69 offen lds
s_add_u32 m0,m0,1024
s_add_u32 s69,s69,s53
buffer_load_dwordx4 v6,s[44:47],s69 offen lds
RA68_%=:
ds_read_u16 v148,v148
ds_read_b128 v[84:87],v4 offset:1824
v_pk_mul_f16 v123,v122,v123
v_lshlrev_b32_sdwa v152,1,v152 dst_sel:DWORD dst_unused:UNUSED_PAD src0_sel:DWORD src1_sel:WORD_1
v_lshlrev_b32_sdwa v154,1,v154 dst_sel:DWORD dst_unused:UNUSED_PAD src0_sel:DWORD src1_sel:WORD_1
v_exp_f16 v120,v121
v_mfma_f32_16x16x32_bf16 v[200:203],v[108:111],v[128:131],v[200:203]
ds_read_u16_d16_hi v150,v150
v_exp_f16 v122,v123
v_lshlrev_b32_sdwa v153,1,v153 dst_sel:DWORD dst_unused:UNUSED_PAD src0_sel:DWORD src1_sel:WORD_1
v_mfma_f32_16x16x32_bf16 v[164:167],v[88:91],v[136:139],v[164:167]
s_cbranch_vccz RA69_%=
s_add_u32 m0,m0,s50
s_add_u32 s69,s69,s53
buffer_load_dwordx4 v5,s[44:47],s69 offen lds
s_add_u32 m0,m0,1024
s_add_u32 s69,s69,s53
buffer_load_dwordx4 v6,s[44:47],s69 offen lds
RA69_%=:
ds_read_u16 v149,v149
v_lshlrev_b32_sdwa v155,1,v155 dst_sel:DWORD dst_unused:UNUSED_PAD src0_sel:DWORD src1_sel:WORD_1
v_exp_f16_sdwa v120,v121 dst_sel:WORD_1 dst_unused:UNUSED_PRESERVE src0_sel:WORD_1
v_exp_f16_sdwa v122,v123 dst_sel:WORD_1 dst_unused:UNUSED_PRESERVE src0_sel:WORD_1
v_mfma_f32_16x16x32_bf16 v[172:175],v[92:95],v[136:139],v[172:175]
ds_read_u16_d16_hi v151,v151
v_pk_fma_f16 v120,v120,s55,s55
v_pk_fma_f16 v122,v122,s55,s55
v_rcp_f16 v121,v120
v_mfma_f32_16x16x32_bf16 v[180:183],v[96:99],v[136:139],v[180:183]
ds_read_u16 v152,v152
v_rcp_f16 v123,v122
v_rcp_f16_sdwa v121,v120 dst_sel:WORD_1 dst_unused:UNUSED_PRESERVE src0_sel:WORD_1
v_mfma_f32_16x16x32_bf16 v[188:191],v[100:103],v[136:139],v[188:191]
s_cbranch_vccz RA70_%=
s_add_u32 m0,m0,1024
s_add_u32 s69,s69,s53
buffer_load_dwordx4 v5,s[44:47],s69 offen lds
s_add_u32 m0,m0,s50
s_add_u32 s69,s69,s53
buffer_load_dwordx4 v6,s[44:47],s69 offen lds
RA70_%=:
ds_read_u16_d16_hi v154,v154
v_rcp_f16_sdwa v123,v122 dst_sel:WORD_1 dst_unused:UNUSED_PRESERVE src0_sel:WORD_1
v_fma_mix_f32 v156,v156,v121,0 op_sel_hi:[0,1,0]
v_mfma_f32_16x16x32_bf16 v[196:199],v[104:107],v[136:139],v[196:199]
s_setprio 0
ds_read_u16 v153,v153
v_fma_mix_f32 v158,v158,v121,0 op_sel:[0,1,0] op_sel_hi:[0,1,0]
s_waitcnt lgkmcnt(9)
v_lshl_add_u64 v[144:145],v[144:145],0,v[146:147]
s_waitcnt lgkmcnt(3)
v_lshl_add_u64 v[146:147],v[148:149],0,v[150:151]
v_fma_mix_f32 v157,v157,v123,0 op_sel_hi:[0,1,0]
v_mfma_f32_16x16x32_bf16 v[204:207],v[108:111],v[136:139],v[204:207]
s_cbranch_vccz RA71_%=
s_add_u32 m0,m0,1024
s_add_u32 s69,s69,s53
buffer_load_dwordx4 v5,s[44:47],s69 offen lds
s_add_u32 m0,m0,1024
s_add_u32 s69,s69,s53
buffer_load_dwordx4 v6,s[44:47],s69 offen lds
RA71_%=:
ds_read_u16_d16_hi v155,v155
v_fma_mix_f32 v159,v159,v123,0 op_sel:[0,1,0] op_sel_hi:[0,1,0]
s_waitcnt lgkmcnt(0)
v_lshl_add_u64 v[152:153],v[152:153],0,v[154:155]
v_cvt_pk_bf16_f32 v154,v156,v158
v_cvt_pk_bf16_f32 v155,v157,v159
v_mfma_f32_16x16x32_bf16 v[208:211],v[88:91],v[144:147],v[208:211]
s_waitcnt vmcnt(36)
s_barrier
s_setprio 0
v_xor_b32 v15,64,v10
v_mul_f32 v160,v160,v64
v_mul_f32 v161,v161,v65
v_mul_f32 v162,v162,v66
v_mul_f32 v163,v163,v67
v_mfma_f32_16x16x32_bf16 v[216:219],v[92:95],v[144:147],v[216:219]
v_mul_f32 v168,v168,v68
v_mul_f32 v169,v169,v69
v_mul_f32 v170,v170,v70
v_mul_f32 v171,v171,v71
v_cvt_pk_bf16_f32 v16,v160,v161
v_mfma_f32_16x16x32_bf16 v[224:227],v[96:99],v[144:147],v[224:227]
v_cvt_pk_bf16_f32 v17,v162,v163
v_cvt_pk_bf16_f32 v18,v168,v169
v_cvt_pk_bf16_f32 v19,v170,v171
v_pk_maximum3_f16 v7,v7,v16,v17
v_mfma_f32_16x16x32_bf16 v[232:235],v[100:103],v[144:147],v[232:235]
v_pk_maximum3_f16 v7,v7,v18,v19
v_mul_f32_dpp v176,v176,v72 quad_perm:[1,0,3,2] row_mask:0xf bank_mask:0xf
v_mul_f32_dpp v177,v177,v73 quad_perm:[1,0,3,2] row_mask:0xf bank_mask:0xf
v_mul_f32_dpp v178,v178,v74 quad_perm:[1,0,3,2] row_mask:0xf bank_mask:0xf
v_mul_f32_dpp v179,v179,v75 quad_perm:[1,0,3,2] row_mask:0xf bank_mask:0xf
v_mfma_f32_16x16x32_bf16 v[240:243],v[104:107],v[144:147],v[240:243]
v_mul_f32_dpp v184,v184,v76 quad_perm:[1,0,3,2] row_mask:0xf bank_mask:0xf
v_mul_f32_dpp v185,v185,v77 quad_perm:[1,0,3,2] row_mask:0xf bank_mask:0xf
v_mul_f32_dpp v186,v186,v78 quad_perm:[1,0,3,2] row_mask:0xf bank_mask:0xf
v_mul_f32_dpp v187,v187,v79 quad_perm:[1,0,3,2] row_mask:0xf bank_mask:0xf
v_mfma_f32_16x16x32_bf16 v[248:251],v[108:111],v[144:147],v[248:251]
v_cvt_pk_bf16_f32 v24,v176,v177
v_cvt_pk_bf16_f32 v25,v178,v179
v_cvt_pk_bf16_f32 v26,v184,v185
v_cvt_pk_bf16_f32 v27,v186,v187
v_pk_maximum3_f16 v7,v7,v24,v25
v_mfma_f32_16x16x32_bf16 v[212:215],v[88:91],v[152:155],v[212:215]
v_pk_maximum3_f16 v7,v7,v26,v27
s_mov_b64 exec,s[12:13]
v_swap_b32 v16,v24
v_swap_b32 v17,v25
v_swap_b32 v18,v26
v_swap_b32 v19,v27
s_mov_b64 exec,-1
v_mfma_f32_16x16x32_bf16 v[220:223],v[92:95],v[152:155],v[220:223]
s_add_u32 s65,s61,3145728
buffer_store_dwordx4 v[16:19],v10,s[40:43],s65 offen offset:0 sc1
v_mfma_f32_16x16x32_bf16 v[228:231],v[96:99],v[152:155],v[228:231]
v_mul_f32 v192,v192,v80
v_mul_f32 v193,v193,v81
v_mul_f32 v194,v194,v82
v_mul_f32 v195,v195,v83
v_mfma_f32_16x16x32_bf16 v[236:239],v[100:103],v[152:155],v[236:239]
v_mul_f32 v200,v200,v84
v_mul_f32 v201,v201,v85
v_mul_f32 v202,v202,v86
v_mul_f32 v203,v203,v87
v_cvt_pk_bf16_f32 v32,v192,v193
v_mfma_f32_16x16x32_bf16 v[244:247],v[104:107],v[152:155],v[244:247]
v_cvt_pk_bf16_f32 v33,v194,v195
v_cvt_pk_bf16_f32 v34,v200,v201
v_cvt_pk_bf16_f32 v35,v202,v203
v_pk_maximum3_f16 v7,v7,v32,v33
v_mfma_f32_16x16x32_bf16 v[252:255],v[108:111],v[152:155],v[252:255]
v_pk_maximum3_f16 v7,v7,v34,v35
v_mul_f32_dpp v164,v164,v64 quad_perm:[1,0,3,2] row_mask:0xf bank_mask:0xf
v_mul_f32_dpp v165,v165,v65 quad_perm:[1,0,3,2] row_mask:0xf bank_mask:0xf
v_mul_f32_dpp v166,v166,v66 quad_perm:[1,0,3,2] row_mask:0xf bank_mask:0xf
v_mul_f32_dpp v167,v167,v67 quad_perm:[1,0,3,2] row_mask:0xf bank_mask:0xf
v_mul_f32_dpp v172,v172,v68 quad_perm:[1,0,3,2] row_mask:0xf bank_mask:0xf
v_mul_f32_dpp v173,v173,v69 quad_perm:[1,0,3,2] row_mask:0xf bank_mask:0xf
v_mul_f32_dpp v174,v174,v70 quad_perm:[1,0,3,2] row_mask:0xf bank_mask:0xf
v_mul_f32_dpp v175,v175,v71 quad_perm:[1,0,3,2] row_mask:0xf bank_mask:0xf
v_cvt_pk_bf16_f32 v20,v164,v165
v_cvt_pk_bf16_f32 v21,v166,v167
v_cvt_pk_bf16_f32 v22,v172,v173
v_cvt_pk_bf16_f32 v23,v174,v175
v_pk_maximum3_f16 v7,v7,v20,v21
v_pk_maximum3_f16 v7,v7,v22,v23
s_mov_b64 exec,s[12:13]
v_swap_b32 v32,v20
v_swap_b32 v33,v21
v_swap_b32 v34,v22
v_swap_b32 v35,v23
s_mov_b64 exec,-1
v_mul_f32 v180,v180,v72
v_mul_f32 v181,v181,v73
v_mul_f32 v182,v182,v74
v_mul_f32 v183,v183,v75
v_mul_f32 v188,v188,v76
v_mul_f32 v189,v189,v77
v_mul_f32 v190,v190,v78
v_mul_f32 v191,v191,v79
v_cvt_pk_bf16_f32 v28,v180,v181
v_cvt_pk_bf16_f32 v29,v182,v183
v_cvt_pk_bf16_f32 v30,v188,v189
v_cvt_pk_bf16_f32 v31,v190,v191
v_pk_maximum3_f16 v7,v7,v28,v29
v_pk_maximum3_f16 v7,v7,v30,v31
v_mul_f32_dpp v196,v196,v80 quad_perm:[1,0,3,2] row_mask:0xf bank_mask:0xf
v_mul_f32_dpp v197,v197,v81 quad_perm:[1,0,3,2] row_mask:0xf bank_mask:0xf
v_mul_f32_dpp v198,v198,v82 quad_perm:[1,0,3,2] row_mask:0xf bank_mask:0xf
v_mul_f32_dpp v199,v199,v83 quad_perm:[1,0,3,2] row_mask:0xf bank_mask:0xf
v_mul_f32_dpp v204,v204,v84 quad_perm:[1,0,3,2] row_mask:0xf bank_mask:0xf
v_mul_f32_dpp v205,v205,v85 quad_perm:[1,0,3,2] row_mask:0xf bank_mask:0xf
v_mul_f32_dpp v206,v206,v86 quad_perm:[1,0,3,2] row_mask:0xf bank_mask:0xf
v_mul_f32_dpp v207,v207,v87 quad_perm:[1,0,3,2] row_mask:0xf bank_mask:0xf
v_cvt_pk_bf16_f32 v36,v196,v197
v_cvt_pk_bf16_f32 v37,v198,v199
v_cvt_pk_bf16_f32 v38,v204,v205
v_cvt_pk_bf16_f32 v39,v206,v207
v_pk_maximum3_f16 v7,v7,v36,v37
v_pk_maximum3_f16 v7,v7,v38,v39
s_mov_b64 exec,s[12:13]
v_swap_b32 v28,v36
v_swap_b32 v29,v37
v_swap_b32 v30,v38
v_swap_b32 v31,v39
s_mov_b64 exec,-1
s_add_u32 s65,s61,3145728
buffer_store_dwordx4 v[24:27],v15,s[40:43],s65 offen offset:384 sc1
v_mul_f32 v208,v208,v64
v_mul_f32 v209,v209,v65
v_mul_f32 v210,v210,v66
v_mul_f32 v211,v211,v67
v_mul_f32 v216,v216,v68
v_mul_f32 v217,v217,v69
v_mul_f32 v218,v218,v70
v_mul_f32 v219,v219,v71
v_cvt_pk_bf16_f32 v40,v208,v209
v_cvt_pk_bf16_f32 v41,v210,v211
v_cvt_pk_bf16_f32 v42,v216,v217
v_cvt_pk_bf16_f32 v43,v218,v219
v_pk_maximum3_f16 v7,v7,v40,v41
v_pk_maximum3_f16 v7,v7,v42,v43
v_mul_f32_dpp v224,v224,v72 quad_perm:[1,0,3,2] row_mask:0xf bank_mask:0xf
v_mul_f32_dpp v225,v225,v73 quad_perm:[1,0,3,2] row_mask:0xf bank_mask:0xf
v_mul_f32_dpp v226,v226,v74 quad_perm:[1,0,3,2] row_mask:0xf bank_mask:0xf
v_mul_f32_dpp v227,v227,v75 quad_perm:[1,0,3,2] row_mask:0xf bank_mask:0xf
v_mul_f32_dpp v232,v232,v76 quad_perm:[1,0,3,2] row_mask:0xf bank_mask:0xf
v_mul_f32_dpp v233,v233,v77 quad_perm:[1,0,3,2] row_mask:0xf bank_mask:0xf
v_mul_f32_dpp v234,v234,v78 quad_perm:[1,0,3,2] row_mask:0xf bank_mask:0xf
v_mul_f32_dpp v235,v235,v79 quad_perm:[1,0,3,2] row_mask:0xf bank_mask:0xf
v_cvt_pk_bf16_f32 v48,v224,v225
v_cvt_pk_bf16_f32 v49,v226,v227
v_cvt_pk_bf16_f32 v50,v232,v233
v_cvt_pk_bf16_f32 v51,v234,v235
v_pk_maximum3_f16 v7,v7,v48,v49
v_pk_maximum3_f16 v7,v7,v50,v51
s_mov_b64 exec,s[12:13]
v_swap_b32 v40,v48
v_swap_b32 v41,v49
v_swap_b32 v42,v50
v_swap_b32 v43,v51
s_mov_b64 exec,-1
v_mul_f32 v240,v240,v80
v_mul_f32 v241,v241,v81
v_mul_f32 v242,v242,v82
v_mul_f32 v243,v243,v83
v_mul_f32 v248,v248,v84
v_mul_f32 v249,v249,v85
v_mul_f32 v250,v250,v86
v_mul_f32 v251,v251,v87
v_cvt_pk_bf16_f32 v56,v240,v241
v_cvt_pk_bf16_f32 v57,v242,v243
v_cvt_pk_bf16_f32 v58,v248,v249
v_cvt_pk_bf16_f32 v59,v250,v251
v_pk_maximum3_f16 v7,v7,v56,v57
v_pk_maximum3_f16 v7,v7,v58,v59
v_mul_f32_dpp v212,v212,v64 quad_perm:[1,0,3,2] row_mask:0xf bank_mask:0xf
v_mul_f32_dpp v213,v213,v65 quad_perm:[1,0,3,2] row_mask:0xf bank_mask:0xf
v_mul_f32_dpp v214,v214,v66 quad_perm:[1,0,3,2] row_mask:0xf bank_mask:0xf
v_mul_f32_dpp v215,v215,v67 quad_perm:[1,0,3,2] row_mask:0xf bank_mask:0xf
v_mul_f32_dpp v220,v220,v68 quad_perm:[1,0,3,2] row_mask:0xf bank_mask:0xf
v_mul_f32_dpp v221,v221,v69 quad_perm:[1,0,3,2] row_mask:0xf bank_mask:0xf
v_mul_f32_dpp v222,v222,v70 quad_perm:[1,0,3,2] row_mask:0xf bank_mask:0xf
v_mul_f32_dpp v223,v223,v71 quad_perm:[1,0,3,2] row_mask:0xf bank_mask:0xf
v_cvt_pk_bf16_f32 v44,v212,v213
v_cvt_pk_bf16_f32 v45,v214,v215
v_cvt_pk_bf16_f32 v46,v220,v221
v_cvt_pk_bf16_f32 v47,v222,v223
v_pk_maximum3_f16 v7,v7,v44,v45
v_pk_maximum3_f16 v7,v7,v46,v47
s_mov_b64 exec,s[12:13]
v_swap_b32 v56,v44
v_swap_b32 v57,v45
v_swap_b32 v58,v46
v_swap_b32 v59,v47
s_mov_b64 exec,-1
s_add_u32 s65,s61,3145728
buffer_store_dwordx4 v[32:35],v10,s[40:43],s65 offen offset:128 sc1
v_mul_f32 v228,v228,v72
v_mul_f32 v229,v229,v73
v_mul_f32 v230,v230,v74
v_mul_f32 v231,v231,v75
v_mul_f32 v236,v236,v76
v_mul_f32 v237,v237,v77
v_mul_f32 v238,v238,v78
v_mul_f32 v239,v239,v79
v_cvt_pk_bf16_f32 v52,v228,v229
v_cvt_pk_bf16_f32 v53,v230,v231
v_cvt_pk_bf16_f32 v54,v236,v237
v_cvt_pk_bf16_f32 v55,v238,v239
v_pk_maximum3_f16 v7,v7,v52,v53
v_pk_maximum3_f16 v7,v7,v54,v55
v_mul_f32_dpp v244,v244,v80 quad_perm:[1,0,3,2] row_mask:0xf bank_mask:0xf
v_mul_f32_dpp v245,v245,v81 quad_perm:[1,0,3,2] row_mask:0xf bank_mask:0xf
v_mul_f32_dpp v246,v246,v82 quad_perm:[1,0,3,2] row_mask:0xf bank_mask:0xf
v_mul_f32_dpp v247,v247,v83 quad_perm:[1,0,3,2] row_mask:0xf bank_mask:0xf
v_mul_f32_dpp v252,v252,v84 quad_perm:[1,0,3,2] row_mask:0xf bank_mask:0xf
v_mul_f32_dpp v253,v253,v85 quad_perm:[1,0,3,2] row_mask:0xf bank_mask:0xf
v_mul_f32_dpp v254,v254,v86 quad_perm:[1,0,3,2] row_mask:0xf bank_mask:0xf
v_mul_f32_dpp v255,v255,v87 quad_perm:[1,0,3,2] row_mask:0xf bank_mask:0xf
v_cvt_pk_bf16_f32 v60,v244,v245
v_cvt_pk_bf16_f32 v61,v246,v247
v_cvt_pk_bf16_f32 v62,v252,v253
v_cvt_pk_bf16_f32 v63,v254,v255
v_pk_maximum3_f16 v7,v7,v60,v61
v_pk_maximum3_f16 v7,v7,v62,v63
s_mov_b64 exec,s[12:13]
v_swap_b32 v52,v60
v_swap_b32 v53,v61
v_swap_b32 v54,v62
v_swap_b32 v55,v63
s_mov_b64 exec,-1
s_cbranch_vccz RA72_%=
s_waitcnt vmcnt(3)
RA72_%=:
s_barrier
s_sleep 2
s_add_u32 s65,s61,3145728
buffer_store_dwordx4 v[20:23],v15,s[40:43],s65 offen offset:512 sc1
s_sleep 4
s_add_u32 s65,s61,3145728
buffer_store_dwordx4 v[28:31],v10,s[40:43],s65 offen offset:256 sc1
s_sleep 4
s_add_u32 s65,s61,3145728
buffer_store_dwordx4 v[36:39],v15,s[40:43],s65 offen offset:640 sc1
s_sleep 4
s_add_u32 s65,s61,3244032
buffer_store_dwordx4 v[40:43],v10,s[40:43],s65 offen offset:0 sc1
s_sleep 4
s_add_u32 s65,s61,3244032
buffer_store_dwordx4 v[48:51],v15,s[40:43],s65 offen offset:384 sc1
s_sleep 4
s_add_u32 s65,s61,3244032
buffer_store_dwordx4 v[56:59],v10,s[40:43],s65 offen offset:128 sc1
s_sleep 4
s_add_u32 s65,s61,3244032
buffer_store_dwordx4 v[44:47],v15,s[40:43],s65 offen offset:512 sc1
s_sleep 4
s_add_u32 s65,s61,3244032
buffer_store_dwordx4 v[52:55],v10,s[40:43],s65 offen offset:256 sc1
s_sleep 4
s_add_u32 s65,s61,3244032
buffer_store_dwordx4 v[60:63],v15,s[40:43],s65 offen offset:640 sc1
v_and_b32 v232,63,%1
s_cmp_lg_u32 s52,1
s_cbranch_scc1 N_s0_%=
v_min_u32 v236,47,v232
v_lshlrev_b32 v152,4,v236
v_lshlrev_b32 v153,2,v236
v_min_u32 v236,59,v232
v_mul_u32_u24 v237,0xaaab,v236
v_lshrrev_b32 v237,19,v237
v_mul_u32_u24 v237,14592,v237
v_lshl_add_u32 v148,v236,4,v237
s_add_u32 s64,s24,0
s_addc_u32 s65,s25,0
global_load_dwordx4 v[112:115],v152,s[64:65]
s_add_u32 s64,s24,9216
s_addc_u32 s65,s25,0
global_load_dwordx4 v[116:119],v152,s[64:65]
s_add_u32 s64,s24,18432
s_addc_u32 s65,s25,0
global_load_dwordx4 v[120:123],v152,s[64:65]
s_add_u32 s64,s24,27648
s_addc_u32 s65,s25,0
global_load_dwordx4 v[124:127],v152,s[64:65]
s_add_u32 s64,s24,36864
s_addc_u32 s65,s25,0
global_load_dwordx4 v[128:131],v152,s[64:65]
s_add_u32 s64,s24,46080
s_addc_u32 s65,s25,0
global_load_dwordx4 v[132:135],v152,s[64:65]
s_add_u32 s64,s24,55296
s_addc_u32 s65,s25,0
global_load_dwordx4 v[136:139],v152,s[64:65]
s_add_u32 s64,s24,64512
s_addc_u32 s65,s25,0
global_load_dwordx4 v[140:143],v152,s[64:65]
global_load_dwordx4 v[144:147],v148,s[28:29]
global_load_dwordx4 v[148:151],v148,s[28:29] offset:3648
global_load_dwordx4 v[156:159],v152,s[26:27]
global_load_dword v152,v153,s[32:33]
global_load_dword v153,v153,s[30:31]
N_s0_%=:
s_and_b32 s64,s52,3
s_mul_i32 s64,s64,18432
s_add_u32 s64,s64,4096
v_lshl_add_u32 v255,v232,4,s64
global_load_dwordx4 v[160:163],v255,s[24:25] offset:-4096
global_load_dwordx4 v[164:167],v255,s[24:25] offset:-3072
global_load_dwordx4 v[168:171],v255,s[24:25] offset:-2048
global_load_dwordx4 v[172:175],v255,s[24:25] offset:-1024
global_load_dwordx4 v[176:179],v255,s[24:25] offset:0
global_load_dwordx4 v[180:183],v255,s[24:25] offset:1024
global_load_dwordx4 v[184:187],v255,s[24:25] offset:2048
global_load_dwordx4 v[188:191],v255,s[24:25] offset:3072
v_add_u32 v255,8192,v255
global_load_dwordx4 v[192:195],v255,s[24:25] offset:-4096
global_load_dwordx4 v[196:199],v255,s[24:25] offset:-3072
global_load_dwordx4 v[200:203],v255,s[24:25] offset:-2048
global_load_dwordx4 v[204:207],v255,s[24:25] offset:-1024
global_load_dwordx4 v[208:211],v255,s[24:25] offset:0
global_load_dwordx4 v[212:215],v255,s[24:25] offset:1024
global_load_dwordx4 v[216:219],v255,s[24:25] offset:2048
global_load_dwordx4 v[220:223],v255,s[24:25] offset:3072
v_add_u32 v255,8192,v255
global_load_dwordx4 v[224:227],v255,s[24:25] offset:-4096
global_load_dwordx4 v[228:231],v255,s[24:25] offset:-3072
s_waitcnt vmcnt(0)
v_or3_b32 v160,v160,v164,v165
v_or3_b32 v162,v162,v166,v167
v_or3_b32 v160,v160,v168,v169
v_or3_b32 v162,v162,v170,v171
v_or3_b32 v160,v160,v172,v173
v_or3_b32 v162,v162,v174,v175
v_or3_b32 v160,v160,v176,v177
v_or3_b32 v162,v162,v178,v179
v_or3_b32 v160,v160,v180,v181
v_or3_b32 v162,v162,v182,v183
v_or3_b32 v160,v160,v184,v185
v_or3_b32 v162,v162,v186,v187
v_or3_b32 v160,v160,v188,v189
v_or3_b32 v162,v162,v190,v191
v_or3_b32 v160,v160,v192,v193
v_or3_b32 v162,v162,v194,v195
v_or3_b32 v160,v160,v196,v197
v_or3_b32 v162,v162,v198,v199
v_or3_b32 v160,v160,v200,v201
v_or3_b32 v162,v162,v202,v203
v_or3_b32 v160,v160,v204,v205
v_or3_b32 v162,v162,v206,v207
v_or3_b32 v160,v160,v208,v209
v_or3_b32 v162,v162,v210,v211
v_or3_b32 v160,v160,v212,v213
v_or3_b32 v162,v162,v214,v215
v_or3_b32 v160,v160,v216,v217
v_or3_b32 v162,v162,v218,v219
v_or3_b32 v160,v160,v220,v221
v_or3_b32 v162,v162,v222,v223
v_or3_b32 v160,v160,v224,v225
v_or3_b32 v162,v162,v226,v227
v_or3_b32 v160,v160,v228,v229
v_or3_b32 v162,v162,v230,v231
v_or3_b32 v160,v160,v161,v162
v_or_b32 v160,v160,v163
s_mov_b32 s64,0x7bff7bff
v_and_b32 v160,0x40004000,v160
v_pk_mul_f16 v160,v160,s64
v_or_b32 v7,v7,v160
s_cmp_lg_u32 s52,1
s_cbranch_scc1 N_s1_%=
s_mov_b32 s65,0x7fff7fff
v_and_b32 v112,s65,v112
v_and_b32 v113,s65,v113
v_and_b32 v114,s65,v114
v_and_b32 v115,s65,v115
v_and_b32 v116,s65,v116
v_and_b32 v117,s65,v117
v_and_b32 v118,s65,v118
v_and_b32 v119,s65,v119
v_and_b32 v120,s65,v120
v_and_b32 v121,s65,v121
v_and_b32 v122,s65,v122
v_and_b32 v123,s65,v123
v_and_b32 v124,s65,v124
v_and_b32 v125,s65,v125
v_and_b32 v126,s65,v126
v_and_b32 v127,s65,v127
v_and_b32 v128,s65,v128
v_and_b32 v129,s65,v129
v_and_b32 v130,s65,v130
v_and_b32 v131,s65,v131
v_and_b32 v132,s65,v132
v_and_b32 v133,s65,v133
v_and_b32 v134,s65,v134
v_and_b32 v135,s65,v135
v_and_b32 v136,s65,v136
v_and_b32 v137,s65,v137
v_and_b32 v138,s65,v138
v_and_b32 v139,s65,v139
v_and_b32 v140,s65,v140
v_and_b32 v141,s65,v141
v_and_b32 v142,s65,v142
v_and_b32 v143,s65,v143
v_pk_max_u16 v100,v112,v116
v_pk_max_u16 v101,v113,v117
v_pk_max_u16 v102,v114,v118
v_pk_max_u16 v103,v115,v119
v_pk_max_u16 v100,v100,v120
v_pk_max_u16 v101,v101,v121
v_pk_max_u16 v102,v102,v122
v_pk_max_u16 v103,v103,v123
v_pk_max_u16 v100,v100,v124
v_pk_max_u16 v101,v101,v125
v_pk_max_u16 v102,v102,v126
v_pk_max_u16 v103,v103,v127
v_pk_max_u16 v100,v100,v128
v_pk_max_u16 v101,v101,v129
v_pk_max_u16 v102,v102,v130
v_pk_max_u16 v103,v103,v131
v_pk_max_u16 v100,v100,v132
v_pk_max_u16 v101,v101,v133
v_pk_max_u16 v102,v102,v134
v_pk_max_u16 v103,v103,v135
v_pk_max_u16 v100,v100,v136
v_pk_max_u16 v101,v101,v137
v_pk_max_u16 v102,v102,v138
v_pk_max_u16 v103,v103,v139
v_pk_max_u16 v100,v100,v140
v_pk_max_u16 v101,v101,v141
v_pk_max_u16 v102,v102,v142
v_pk_max_u16 v103,v103,v143
v_pk_min_u16 v100,v100,v101
v_pk_min_u16 v102,v102,v103
v_pk_min_u16 v100,v100,v102
v_lshrrev_b32 v101,16,v100
v_and_b32 v100,0xffff,v100
v_min_u32 v100,v100,v101
v_mov_b32 v101,0x3a00
v_cmp_lt_u32_e64 s[92:93],v100,v101
s_nop 0
s_or_b32 s64,s92,s93
v_and_b32 v152,s65,v152
v_lshrrev_b32 v101,16,v152
v_and_b32 v152,0xffff,v152
v_max_u32 v152,v152,v101
v_mov_b32 v101,0x4580
v_cmp_le_u32_e64 s[92:93],v101,v152
s_nop 0
s_or_b32 s64,s64,s92
s_or_b32 s64,s64,s93
v_and_b32 v153,s65,v153
v_and_b32 v144,s65,v144
v_pk_max_u16 v153,v153,v144
v_and_b32 v145,s65,v145
v_pk_max_u16 v153,v153,v145
v_and_b32 v146,s65,v146
v_pk_max_u16 v153,v153,v146
v_and_b32 v147,s65,v147
v_pk_max_u16 v153,v153,v147
v_and_b32 v148,s65,v148
v_pk_max_u16 v153,v153,v148
v_and_b32 v149,s65,v149
v_pk_max_u16 v153,v153,v149
v_and_b32 v150,s65,v150
v_pk_max_u16 v153,v153,v150
v_and_b32 v151,s65,v151
v_pk_max_u16 v153,v153,v151
v_lshrrev_b32 v101,16,v153
v_and_b32 v153,0xffff,v153
v_max_u32 v153,v153,v101
v_mov_b32 v101,0x5380
v_cmp_le_u32_e64 s[92:93],v101,v153
s_nop 0
s_or_b32 s64,s64,s92
s_or_b32 s64,s64,s93
v_and_b32 v156,s65,v156
v_and_b32 v157,s65,v157
v_and_b32 v158,s65,v158
v_and_b32 v159,s65,v159
v_pk_max_u16 v156,v156,v157
v_pk_max_u16 v156,v156,v158
v_pk_max_u16 v156,v156,v159
v_lshrrev_b32 v101,16,v156
v_and_b32 v156,0xffff,v156
v_max_u32 v156,v156,v101
v_mov_b32 v101,0x4280
v_cmp_le_u32_e64 s[92:93],v101,v156
s_nop 0
s_or_b32 s64,s64,s92
s_or_b32 s64,s64,s93
s_cmp_eq_u32 s64,0
s_cbranch_scc1 N_s1_%=
v_mov_b32 v7,0x7c007c00
N_s1_%=:
v_and_b32 v1,0x7c007c00,v7
v_lshrrev_b32 v2,16,v1
v_and_b32 v1,0xffff,v1
v_max_u32 v1,v1,v2
v_cmp_eq_u32 vcc,0x7c00,v1
s_or_b32 s64,vcc_lo,vcc_hi
v_mov_b32 v1,s64
v_mov_b32 v2,8192
s_mov_b64 exec,1
ds_or_b32 v2,v1 offset:3456
s_mov_b64 exec,-1
s_waitcnt lgkmcnt(0)
s_barrier
s_setprio 3
ds_read_b32 v1,v2 offset:3456
s_waitcnt lgkmcnt(0)
s_setprio 0
v_readfirstlane_b32 s64,v1
s_cmp_lg_u32 s64,0
s_cbranch_scc1 N_fallback_%=
s_endpgm
s_nop 4
N_B_%=:
v_mov_b32 v192,0x409d3ef8
v_and_b32 v248,255,%1
v_lshlrev_b32 v198,4,v248
v_lshlrev_b32 v248,19,v248
v_cmp_eq_u32_e64 s[94:95],0,v248
v_add_u32 v199,0x8000,v198
s_mov_b32 s4,0x3f000000
s_mov_b32 s5,0x3fc00000
s_mov_b32 s6,0x40200000
s_mov_b32 s7,0x40600000
s_mov_b32 s8,0x40900000
s_mov_b32 s9,0x40b00000
s_mov_b32 s10,0x40d00000
s_mov_b32 s11,0x40f00000
s_mov_b32 s12,0x3f80222e
s_mov_b32 s13,0x13089abf
s_mov_b32 s96,0xebefe000
s_mov_b32 s15,0
N_tl_%=:
v_add_u32 v237,0x10000,v248
v_mul_f32 v194,s99,v248
v_fma_f32 v196,v237,s99,-v194
v_pk_fma_f32 v[160:161],v[196:197],s[4:5],v[194:195] op_sel_hi:[0,1,0]
v_pk_fma_f32 v[162:163],v[196:197],s[6:7],v[194:195] op_sel_hi:[0,1,0]
v_pk_fma_f32 v[164:165],v[196:197],s[8:9],v[194:195] op_sel_hi:[0,1,0]
v_pk_fma_f32 v[166:167],v[196:197],s[10:11],v[194:195] op_sel_hi:[0,1,0]
v_cndmask_b32_e64 v160,v160,0,s[94:95]
v_pk_mul_f32 v[168:169],v[160:161],v[160:161]
v_pk_mul_f32 v[170:171],v[162:163],v[162:163]
v_pk_mul_f32 v[172:173],v[164:165],v[164:165]
v_pk_mul_f32 v[174:175],v[166:167],v[166:167]
v_pk_fma_f32 v[168:169],v[168:169],s[12:13],v[192:193] op_sel_hi:[1,0,0]
v_pk_fma_f32 v[170:171],v[170:171],s[12:13],v[192:193] op_sel_hi:[1,0,0]
v_pk_fma_f32 v[172:173],v[172:173],s[12:13],v[192:193] op_sel_hi:[1,0,0]
v_pk_fma_f32 v[174:175],v[174:175],s[12:13],v[192:193] op_sel_hi:[1,0,0]
v_pk_mul_f32 v[168:169],v[168:169],v[160:161]
v_pk_mul_f32 v[170:171],v[170:171],v[162:163]
v_pk_mul_f32 v[172:173],v[172:173],v[164:165]
v_pk_mul_f32 v[174:175],v[174:175],v[166:167]
v_exp_f32 v168,v168
v_exp_f32 v169,v169
v_exp_f32 v170,v170
v_exp_f32 v171,v171
v_exp_f32 v172,v172
v_exp_f32 v173,v173
v_exp_f32 v174,v174
v_exp_f32 v175,v175
v_pk_fma_f32 v[168:169],v[168:169],s[96:97],s[96:97] op_sel_hi:[1,0,0]
v_pk_fma_f32 v[170:171],v[170:171],s[96:97],s[96:97] op_sel_hi:[1,0,0]
v_pk_fma_f32 v[172:173],v[172:173],s[96:97],s[96:97] op_sel_hi:[1,0,0]
v_pk_fma_f32 v[174:175],v[174:175],s[96:97],s[96:97] op_sel_hi:[1,0,0]
v_rcp_f32 v168,v168
v_rcp_f32 v169,v169
v_rcp_f32 v170,v170
v_rcp_f32 v171,v171
v_rcp_f32 v172,v172
v_rcp_f32 v173,v173
v_rcp_f32 v174,v174
v_rcp_f32 v175,v175
v_pk_mul_f32 v[168:169],v[168:169],v[160:161]
v_pk_mul_f32 v[170:171],v[170:171],v[162:163]
v_pk_mul_f32 v[172:173],v[172:173],v[164:165]
v_pk_mul_f32 v[174:175],v[174:175],v[166:167]
v_pk_fma_f32 v[160:161],v[160:161],s[12:13],v[168:169] op_sel:[0,1,0] op_sel_hi:[1,1,1]
v_pk_fma_f32 v[162:163],v[162:163],s[12:13],v[170:171] op_sel:[0,1,0] op_sel_hi:[1,1,1]
v_pk_fma_f32 v[164:165],v[164:165],s[12:13],v[172:173] op_sel:[0,1,0] op_sel_hi:[1,1,1]
v_pk_fma_f32 v[166:167],v[166:167],s[12:13],v[174:175] op_sel:[0,1,0] op_sel_hi:[1,1,1]
v_cvt_pk_bf16_f32 v176,v168,v169
v_cvt_pk_bf16_f32 v177,v170,v171
v_cvt_pk_bf16_f32 v178,v172,v173
v_cvt_pk_bf16_f32 v179,v174,v175
v_cvt_pk_bf16_f32 v180,v160,v161
v_cvt_pk_bf16_f32 v181,v162,v163
v_cvt_pk_bf16_f32 v182,v164,v165
v_cvt_pk_bf16_f32 v183,v166,v167
ds_write_b128 v198,v[176:179] offset:0
ds_write_b128 v199,v[180:183] offset:32768
s_mov_b64 s[94:95],0
v_add_u32 v248,0x8000000,v248
v_add_u32 v198,4096,v198
v_add_u32 v199,4096,v199
s_add_u32 s15,s15,1
s_cmp_lt_u32 s15,2
s_cbranch_scc1 N_tl_%=
v_bfe_u32 v236,v232,2,3
v_mul_u32_u24 v236,768,v236
v_and_b32 v237,3,v232
v_lshrrev_b32 v238,5,v232
v_lshl_add_u32 v237,v238,2,v237
v_lshl_add_u32 v236,v237,4,v236
v_lshlrev_b32 v237,4,v232
s_bitcmp0_b32 s52,0
s_mov_b32 s58,40896
s_cselect_b32 s58,16256,s58
s_cselect_b64 s[92:93],-1,0
s_cselect_b32 s59,64,6144
s_movk_i32 s53,1024
s_cselect_b32 s53,6144,s53
s_cselect_b32 s60,0,67584
s_cselect_b32 s97,64,0
s_cselect_b32 s69,0,0x70000000
s_mov_b32 s100,73728
s_cselect_b32 s100,110688,s100
s_cselect_b32 s64,16,0
s_movk_i32 s50,1032
s_cselect_b32 s50,1024,s50
s_cselect_b32 s51,s53,-72704
v_cndmask_b32_e64 v236,v237,v236,s[92:93]
v_cndmask_b32_e64 v5,v241,v236,vcc
v_xor_b32 v236,s64,v236
v_cndmask_b32_e64 v6,v6,v236,vcc
s_mov_b32 s46,73728
s_mov_b32 s47,0x00020000
s_waitcnt lgkmcnt(0)
s_sub_u32 s36,s20,192
s_subb_u32 s37,s21,0
s_and_b32 s37,s37,0xffff
s_bitcmp0_b32 s52,0
s_cselect_b64 s[44:45],s[24:25],s[28:29]
s_and_b32 s45,s45,0xffff
s_cbranch_vccz RB1_%=
s_add_u32 m0,s58,12320
s_nop 0
buffer_load_dwordx4 v5,s[44:47],s69 offen lds
s_add_u32 m0,m0,1024
s_add_u32 s69,s69,s53
buffer_load_dwordx4 v6,s[44:47],s69 offen lds
s_add_u32 m0,m0,1024
s_add_u32 s69,s69,s53
buffer_load_dwordx4 v5,s[44:47],s69 offen lds
s_add_u32 m0,m0,s50
s_add_u32 s69,s69,s53
buffer_load_dwordx4 v6,s[44:47],s69 offen lds
s_add_u32 m0,m0,1024
s_add_u32 s69,s69,s53
buffer_load_dwordx4 v5,s[44:47],s69 offen lds
s_add_u32 m0,m0,1024
s_add_u32 s69,s69,s53
buffer_load_dwordx4 v6,s[44:47],s69 offen lds
s_add_u32 m0,m0,s50
s_add_u32 s69,s69,s51
buffer_load_dwordx4 v5,s[44:47],s69 offen lds
s_add_u32 m0,m0,1024
s_add_u32 s69,s69,s53
buffer_load_dwordx4 v6,s[44:47],s69 offen lds
s_add_u32 m0,m0,1024
s_add_u32 s69,s69,s53
buffer_load_dwordx4 v5,s[44:47],s69 offen lds
s_add_u32 m0,m0,s50
s_add_u32 s69,s69,s53
buffer_load_dwordx4 v6,s[44:47],s69 offen lds
s_add_u32 m0,m0,1024
s_add_u32 s69,s69,s53
buffer_load_dwordx4 v5,s[44:47],s69 offen lds
s_add_u32 m0,m0,1024
s_add_u32 s69,s69,s53
buffer_load_dwordx4 v6,s[44:47],s69 offen lds
RB1_%=:
s_and_b32 s64,s52,3
s_mul_i32 s67,s64,54
s_lshl_b32 s10,s64,2
s_cmp_eq_u32 s70,31
s_cselect_b32 s66,17,99
v_add_u32 v236,s67,v232
v_add_u32 v236,108,v236
v_mul_u32_u24 v237,0xaaab,v236
v_lshrrev_b32 v237,19,v237
v_mul_u32_u24 v238,12,v237
v_sub_u32 v238,v236,v238
v_add_u32 v236,1,v237
v_bfe_u32 v236,v236,2,2
v_xor_b32 v238,v238,v236
v_mul_u32_u24 v236,192,v237
v_lshl_add_u32 v239,v238,4,v236
v_cmp_eq_u32_e64 s[92:93],s66,v237
v_mov_b32 v236,0x7fff0000
s_nop 0
v_cndmask_b32_e64 v239,v239,v236,s[92:93]
v_cndmask_b32_e64 v6,v239,v6,vcc
s_add_u32 s64,s10,s71
v_and_b32 v233,15,v232
v_lshrrev_b32 v234,4,v232
s_mul_i32 s61,s64,98304
s_add_u32 s61,s61,s68
v_and_b32 v236,1,v234
v_lshlrev_b32 v236,1,v236
v_lshrrev_b32 v237,5,v232
v_or_b32 v235,v236,v237
s_mov_b32 s54,0x44ea44ea
s_mov_b32 s55,0x877f877f
s_mov_b32 s8,0x0ff00ff0
s_mov_b32 s9,0x0ff00ff0
v_mov_b32 v7,0
v_mov_b32 v239,0x70000
v_bfe_u32 v236,v233,1,1
v_sub_u32 v236,v235,v236
v_cmp_gt_u32_e64 s[92:93],3,v236
v_lshrrev_b32 v237,2,v233
v_lshlrev_b32 v237,1,v237
v_and_b32 v238,1,v233
v_or_b32 v237,v237,v238
v_lshl_add_u32 v236,v236,3,v237
v_lshlrev_b32 v236,4,v236
v_add_u32 v236,143488,v236
v_cndmask_b32_e64 v11,v239,v236,s[92:93]
v_and_b32 v13,3,v237
v_lshlrev_b32 v236,1,v233
v_and_b32 v236,4,v236
v_lshl_add_u32 v236,v234,3,v236
v_mov_b32 v238,0x54250310
v_mul_u32_u24 v13,0x5555,v13
v_bfe_u32 v236,v238,v236,3
v_lshl_add_u32 v236,v236,3,v237
v_lshlrev_b32 v236,4,v236
v_add_u32 v12,143488,v236
v_lshrrev_b32 v237,2,v233
v_cmp_eq_u32_e64 s[92:93],v234,v237
v_lshlrev_b32 v236,4,v233
v_add_u32 v236,10112,v236
v_cndmask_b32_e64 v14,v239,v236,s[92:93]
v_and_b32 v239,7,v233
v_lshl_add_u32 v236,v239,1,v235
v_add_u32 v237,1,v236
v_bfe_u32 v237,v237,2,2
v_mul_u32_u24 v236,192,v236
v_lshl_add_u32 v236,v237,4,v236
v_add_u32 v237,1,v239
v_bfe_u32 v237,v237,1,2
v_xor_b32 v237,v237,v234
v_mul_u32_u24 v239,384,v239
v_lshl_add_u32 v237,v237,4,v239
v_sub_u32 v237,v237,v236
v_add_u32 v8,192,v237
v_add_u32 v15,73728,v236
v_and_b32 v238,1,v233
v_mul_u32_u24 v238,320,v238
v_sub_u32 v239,v239,v238
s_mov_b32 s12,0xaaaaaaaa
s_mov_b32 s13,0xaaaaaaaa
v_lshl_add_u32 v10,v234,4,v239
s_mov_b64 exec,s[8:9]
v_add_u32 v10,196608,v10
s_mov_b64 exec,-1
v_lshrrev_b32 v236,3,v233
v_bfe_u32 v237,v233,2,1
v_lshl_add_u32 v237,v236,4,v237
v_lshl_add_u32 v237,v234,1,v237
v_lshlrev_b32 v237,6,v237
v_lshl_add_u32 v237,v236,4,v237
v_and_b32 v238,1,v233
v_lshl_add_u32 v237,v238,5,v237
v_bfe_u32 v238,v233,1,1
v_lshl_add_u32 v237,v238,3,v237
v_xor_b32 v238,16,v237
v_add_u32 v1,16256,v237
v_add_u32 v2,16256,v238
v_lshrrev_b32 v236,3,v233
v_bfe_u32 v237,v233,2,1
v_lshl_add_u32 v236,v237,1,v236
v_lshrrev_b32 v237,1,v234
v_lshl_add_u32 v236,v237,2,v236
v_mul_u32_u24 v236,192,v236
v_and_b32 v238,1,v234
v_mul_u32_u24 v239,3080,v238
v_add_u32 v236,v236,v239
v_and_b32 v239,3,v233
v_lshl_add_u32 v236,v239,4,v236
v_add_u32 v3,40896,v236
v_lshlrev_b32 v238,6,v238
v_lshl_add_u32 v238,v237,4,v238
v_add_u32 v4,8192,v238
s_mov_b64 s[40:41],s[34:35]
s_and_b32 s41,s41,0xffff
s_mov_b32 s42,50331648
s_mov_b32 s43,0x00020000
s_waitcnt lgkmcnt(0)
s_barrier
s_setprio 0
s_cbranch_vccz RB2_%=
s_add_u32 m0,s58,0
s_mul_i32 s69,s59,1
s_add_u32 s69,s69,s97
buffer_load_dwordx4 v5,s[44:47],s69 offen lds
s_add_u32 m0,m0,1024
s_add_u32 s69,s69,s53
buffer_load_dwordx4 v6,s[44:47],s69 offen lds
s_add_u32 m0,m0,1024
s_add_u32 s69,s69,s53
buffer_load_dwordx4 v5,s[44:47],s69 offen lds
s_add_u32 m0,m0,s50
s_add_u32 s69,s69,s53
buffer_load_dwordx4 v6,s[44:47],s69 offen lds
s_add_u32 m0,m0,1024
s_add_u32 s69,s69,s53
buffer_load_dwordx4 v5,s[44:47],s69 offen lds
s_add_u32 m0,m0,1024
s_add_u32 s69,s69,s53
buffer_load_dwordx4 v6,s[44:47],s69 offen lds
s_add_u32 m0,m0,s50
s_add_u32 s69,s69,s53
buffer_load_dwordx4 v5,s[44:47],s69 offen lds
s_add_u32 m0,m0,1024
s_add_u32 s69,s69,s53
buffer_load_dwordx4 v6,s[44:47],s69 offen lds
s_add_u32 m0,m0,1024
s_add_u32 s69,s69,s53
buffer_load_dwordx4 v5,s[44:47],s69 offen lds
s_add_u32 m0,m0,s50
s_add_u32 s69,s69,s53
buffer_load_dwordx4 v6,s[44:47],s69 offen lds
s_add_u32 m0,m0,1024
s_add_u32 s69,s69,s53
buffer_load_dwordx4 v5,s[44:47],s69 offen lds
s_add_u32 m0,m0,1024
s_add_u32 s69,s69,s53
buffer_load_dwordx4 v6,s[44:47],s69 offen lds
RB2_%=:
v_lshlrev_b32 v253,7,v232
s_bfe_u32 s64,%0,0x20003
s_and_b32 s65,s52,3
s_lshl_b32 s65,s65,2
s_add_u32 s64,s64,s65
s_lshl_b32 s65,s64,13
v_add_u32 v252,s65,v253
s_getpc_b64 s[14:15]
N_pf_%=:
s_sub_u32 s14,s14,N_pf_%=-N_main_%=
s_subb_u32 s15,s15,0
v_min_u32 v252,N_mainend_%=-N_main_%=,v252
global_load_dword v254,v252,s[14:15]
s_cbranch_vccnz RB3_%=
s_mov_b64 exec,s[62:63]
s_add_u32 m0,s57,62784
s_add_u32 s64,s56,1769472
buffer_load_dwordx4 v5,s[36:39],s64 offen lds
s_add_u32 m0,s57,64512
s_nop 0
buffer_load_dwordx4 v6,s[36:39],s64 offen lds
s_mov_b64 exec,-1
RB3_%=:
s_cbranch_vccnz RB4_%=
s_mov_b64 exec,s[62:63]
s_add_u32 m0,s57,66272
s_add_u32 s64,s56,1867776
buffer_load_dwordx4 v5,s[36:39],s64 offen lds
s_add_u32 m0,s57,68000
s_nop 0
buffer_load_dwordx4 v6,s[36:39],s64 offen lds
s_mov_b64 exec,-1
RB4_%=:
s_waitcnt vmcnt(13)
s_barrier
s_cbranch_vccnz RB5_%=
s_mov_b64 exec,s[62:63]
s_add_u32 m0,s57,0
s_add_u32 s64,s56,1966080
buffer_load_dwordx4 v5,s[36:39],s64 offen lds
s_add_u32 m0,s57,1728
s_nop 0
buffer_load_dwordx4 v6,s[36:39],s64 offen lds
s_mov_b64 exec,-1
RB5_%=:
s_sleep 4
s_cbranch_vccnz RB6_%=
s_mov_b64 exec,s[62:63]
s_add_u32 m0,s57,3488
s_add_u32 s64,s56,2064384
buffer_load_dwordx4 v5,s[36:39],s64 offen lds
s_add_u32 m0,s57,5216
s_nop 0
buffer_load_dwordx4 v6,s[36:39],s64 offen lds
s_mov_b64 exec,-1
RB6_%=:
s_sleep 4
s_cbranch_vccnz RB7_%=
s_mov_b64 exec,s[62:63]
s_add_u32 m0,s57,6976
s_add_u32 s64,s56,2162688
buffer_load_dwordx4 v5,s[36:39],s64 offen lds
s_add_u32 m0,s57,8704
s_nop 0
buffer_load_dwordx4 v6,s[36:39],s64 offen lds
s_mov_b64 exec,-1
RB7_%=:
s_sleep 4
s_cbranch_vccnz RB8_%=
s_mov_b64 exec,s[62:63]
s_add_u32 m0,s57,10464
s_add_u32 s64,s56,2260992
buffer_load_dwordx4 v5,s[36:39],s64 offen lds
s_add_u32 m0,s57,12192
s_nop 0
buffer_load_dwordx4 v6,s[36:39],s64 offen lds
s_mov_b64 exec,-1
RB8_%=:
s_cbranch_vccz RB9_%=
s_waitcnt vmcnt(1)
RB9_%=:
s_barrier
s_cbranch_vccz RB10_%=
s_add_u32 m0,s58,12320
s_mul_i32 s69,s59,3
s_add_u32 s69,s69,s97
buffer_load_dwordx4 v5,s[44:47],s69 offen lds
s_add_u32 m0,m0,1024
s_add_u32 s69,s69,s53
buffer_load_dwordx4 v6,s[44:47],s69 offen lds
RB10_%=:
s_cbranch_vccz RB11_%=
s_add_u32 m0,m0,1024
s_add_u32 s69,s69,s53
buffer_load_dwordx4 v5,s[44:47],s69 offen lds
s_add_u32 m0,m0,s50
s_add_u32 s69,s69,s53
buffer_load_dwordx4 v6,s[44:47],s69 offen lds
RB11_%=:
s_cbranch_vccnz RB12_%=
s_mov_b64 exec,s[62:63]
s_add_u32 m0,s57,13952
s_add_u32 s64,s56,2359296
buffer_load_dwordx4 v5,s[36:39],s64 offen lds
s_add_u32 m0,s57,15680
s_nop 0
buffer_load_dwordx4 v6,s[36:39],s64 offen lds
s_mov_b64 exec,-1
RB12_%=:
s_cbranch_vccz RB13_%=
s_add_u32 m0,m0,1024
s_add_u32 s69,s69,s53
buffer_load_dwordx4 v5,s[44:47],s69 offen lds
s_add_u32 m0,m0,1024
s_add_u32 s69,s69,s53
buffer_load_dwordx4 v6,s[44:47],s69 offen lds
RB13_%=:
s_cbranch_vccz RB14_%=
s_add_u32 m0,m0,s50
s_add_u32 s69,s69,s53
buffer_load_dwordx4 v5,s[44:47],s69 offen lds
s_add_u32 m0,m0,1024
s_add_u32 s69,s69,s53
buffer_load_dwordx4 v6,s[44:47],s69 offen lds
RB14_%=:
s_cbranch_vccz RB15_%=
s_add_u32 m0,m0,1024
s_add_u32 s69,s69,s53
buffer_load_dwordx4 v5,s[44:47],s69 offen lds
s_add_u32 m0,m0,s50
s_add_u32 s69,s69,s53
buffer_load_dwordx4 v6,s[44:47],s69 offen lds
RB15_%=:
s_cbranch_vccz RB16_%=
s_add_u32 m0,m0,1024
s_add_u32 s69,s69,s53
buffer_load_dwordx4 v5,s[44:47],s69 offen lds
s_add_u32 m0,m0,1024
s_add_u32 s69,s69,s53
buffer_load_dwordx4 v6,s[44:47],s69 offen lds
RB16_%=:
s_cbranch_vccnz RB17_%=
s_mov_b64 exec,s[62:63]
s_add_u32 m0,s57,17440
s_add_u32 s64,s56,2457600
buffer_load_dwordx4 v5,s[36:39],s64 offen lds
s_add_u32 m0,s57,19168
s_nop 0
buffer_load_dwordx4 v6,s[36:39],s64 offen lds
s_mov_b64 exec,-1
RB17_%=:
s_sleep 4
s_cbranch_vccnz RB18_%=
s_mov_b64 exec,s[62:63]
s_add_u32 m0,s57,20928
s_add_u32 s64,s56,2555904
buffer_load_dwordx4 v5,s[36:39],s64 offen lds
s_add_u32 m0,s57,22656
s_nop 0
buffer_load_dwordx4 v6,s[36:39],s64 offen lds
s_mov_b64 exec,-1
RB18_%=:
s_sleep 4
s_cbranch_vccnz RB19_%=
s_mov_b64 exec,s[62:63]
s_add_u32 m0,s57,24416
s_add_u32 s64,s56,2654208
buffer_load_dwordx4 v5,s[36:39],s64 offen lds
s_add_u32 m0,s57,26144
s_nop 0
buffer_load_dwordx4 v6,s[36:39],s64 offen lds
s_mov_b64 exec,-1
RB19_%=:
s_sleep 4
s_cbranch_vccnz RB20_%=
s_mov_b64 exec,s[62:63]
s_add_u32 m0,s57,27904
s_add_u32 s64,s56,2752512
buffer_load_dwordx4 v5,s[36:39],s64 offen lds
s_add_u32 m0,s57,29632
s_nop 0
buffer_load_dwordx4 v6,s[36:39],s64 offen lds
s_mov_b64 exec,-1
RB20_%=:
s_sleep 4
s_cbranch_vccnz RB21_%=
s_mov_b64 exec,s[62:63]
s_add_u32 m0,s57,31392
s_add_u32 s64,s56,2850816
buffer_load_dwordx4 v5,s[36:39],s64 offen lds
s_add_u32 m0,s57,33120
s_nop 0
buffer_load_dwordx4 v6,s[36:39],s64 offen lds
s_mov_b64 exec,-1
RB21_%=:
s_sleep 4
s_cbranch_vccnz RB22_%=
s_mov_b64 exec,s[62:63]
s_add_u32 m0,s57,34880
s_add_u32 s64,s56,2949120
buffer_load_dwordx4 v5,s[36:39],s64 offen lds
s_add_u32 m0,s57,36608
s_nop 0
buffer_load_dwordx4 v6,s[36:39],s64 offen lds
s_mov_b64 exec,-1
RB22_%=:
s_sleep 4
s_cbranch_vccnz RB23_%=
s_mov_b64 exec,s[62:63]
s_add_u32 m0,s57,38368
s_add_u32 s64,s56,3047424
buffer_load_dwordx4 v5,s[36:39],s64 offen lds
s_add_u32 m0,s57,40096
s_nop 0
buffer_load_dwordx4 v6,s[36:39],s64 offen lds
s_mov_b64 exec,-1
RB23_%=:
s_cbranch_vccz RB24_%=
s_waitcnt vmcnt(0)
RB24_%=:
s_barrier
s_cbranch_vccz RB25_%=
s_add_u32 m0,s58,0
s_mul_i32 s69,s59,5
s_add_u32 s69,s69,s97
buffer_load_dwordx4 v5,s[44:47],s69 offen lds
s_add_u32 m0,m0,1024
s_add_u32 s69,s69,s53
buffer_load_dwordx4 v6,s[44:47],s69 offen lds
RB25_%=:
s_cbranch_vccz RB26_%=
s_add_u32 m0,m0,1024
s_add_u32 s69,s69,s53
buffer_load_dwordx4 v5,s[44:47],s69 offen lds
s_add_u32 m0,m0,s50
s_add_u32 s69,s69,s53
buffer_load_dwordx4 v6,s[44:47],s69 offen lds
RB26_%=:
s_cbranch_vccz RB27_%=
s_add_u32 m0,m0,1024
s_add_u32 s69,s69,s53
buffer_load_dwordx4 v5,s[44:47],s69 offen lds
s_add_u32 m0,m0,1024
s_add_u32 s69,s69,s53
buffer_load_dwordx4 v6,s[44:47],s69 offen lds
RB27_%=:
s_cbranch_vccz RB28_%=
s_add_u32 m0,m0,s50
s_add_u32 s69,s69,s53
buffer_load_dwordx4 v5,s[44:47],s69 offen lds
s_add_u32 m0,m0,1024
s_add_u32 s69,s69,s53
buffer_load_dwordx4 v6,s[44:47],s69 offen lds
RB28_%=:
s_cbranch_vccz RB29_%=
s_add_u32 m0,m0,1024
s_add_u32 s69,s69,s53
buffer_load_dwordx4 v5,s[44:47],s69 offen lds
s_add_u32 m0,m0,s50
s_add_u32 s69,s69,s53
buffer_load_dwordx4 v6,s[44:47],s69 offen lds
RB29_%=:
s_cbranch_vccnz RB30_%=
s_mov_b64 exec,s[62:63]
s_add_u32 m0,s57,41856
s_add_u32 s64,s56,3145728
buffer_load_dwordx4 v5,s[36:39],s64 offen lds
s_add_u32 m0,s57,43584
s_nop 0
buffer_load_dwordx4 v6,s[36:39],s64 offen lds
s_mov_b64 exec,-1
RB30_%=:
s_cbranch_vccz RB31_%=
s_add_u32 m0,m0,1024
s_add_u32 s69,s69,s53
buffer_load_dwordx4 v5,s[44:47],s69 offen lds
s_add_u32 m0,m0,1024
s_add_u32 s69,s69,s53
buffer_load_dwordx4 v6,s[44:47],s69 offen lds
RB31_%=:
s_sleep 3
s_cbranch_vccnz RB32_%=
s_mov_b64 exec,s[62:63]
s_add_u32 m0,s57,45344
s_add_u32 s64,s56,3244032
buffer_load_dwordx4 v5,s[36:39],s64 offen lds
s_add_u32 m0,s57,47072
s_nop 0
buffer_load_dwordx4 v6,s[36:39],s64 offen lds
s_mov_b64 exec,-1
RB32_%=:
s_add_u32 s4,s10,16
s_cmp_ge_u32 s4,20
s_cselect_b32 s7,20,0
s_sub_u32 s4,s4,s7
s_mul_i32 s4,s4,3488
s_add_u32 s5,s10,18
s_cmp_ge_u32 s5,20
s_cselect_b32 s7,20,0
s_sub_u32 s5,s5,s7
s_mul_i32 s5,s5,3488
s_add_u32 s6,s10,0
s_cmp_ge_u32 s6,20
s_cselect_b32 s7,20,0
s_sub_u32 s6,s6,s7
s_mul_i32 s6,s6,3488
v_add_u32 v112,s4,v15
v_add_u32 v116,s5,v15
s_mov_b64 exec,s[8:9]
v_add_u32 v112,s5,v15
v_add_u32 v116,s6,v15
s_mov_b64 exec,-1
v_xor_b32 v113,16,v112
v_xor_b32 v117,16,v116
v_xor_b32 v114,32,v112
v_xor_b32 v118,32,v116
v_xor_b32 v115,48,v112
v_xor_b32 v119,48,v116
s_waitcnt vmcnt(0)
s_barrier
s_setprio 3
ds_read_b128 v[136:139],v116 offset:0
ds_read_b128 v[88:91],v11 offset:768
ds_read_b128 v[140:143],v116 offset:3488
ds_read_b128 v[128:131],v112 offset:0
ds_read_b128 v[132:135],v112 offset:3488
ds_read_b128 v[92:95],v12 offset:0
ds_read_b128 v[152:155],v117 offset:0
ds_read_b128 v[96:99],v11 offset:1920
ds_read_b128 v[156:159],v117 offset:3488
ds_read_b128 v[144:147],v113 offset:0
ds_read_b128 v[148:151],v113 offset:3488
ds_read_b128 v[100:103],v12 offset:1152
s_waitcnt lgkmcnt(10)
v_mfma_f32_16x16x32_bf16 v[160:163],v[88:91],v[136:139],0
s_waitcnt lgkmcnt(9)
v_mfma_f32_16x16x32_bf16 v[184:187],v[88:91],v[140:143],0
s_waitcnt lgkmcnt(6)
s_cbranch_vccnz RB33_%=
s_mov_b64 exec,s[62:63]
s_add_u32 m0,s57,48832
s_add_u32 s64,s56,3342336
buffer_load_dwordx4 v5,s[36:39],s64 offen lds
s_add_u32 m0,s57,50560
s_nop 0
buffer_load_dwordx4 v6,s[36:39],s64 offen lds
s_mov_b64 exec,-1
RB33_%=:
ds_read_b128 v[140:143],v116 offset:3552
ds_read_b128 v[88:91],v11 offset:5376
v_smfmac_f32_16x16x64_bf16 v[160:163],v[92:95],v[128:135],v13
s_cbranch_vccz RB34_%=
s_add_u32 m0,s58,12320
s_mul_i32 s69,s59,7
s_add_u32 s69,s69,s97
buffer_load_dwordx4 v5,s[44:47],s69 offen lds
s_add_u32 m0,m0,1024
s_add_u32 s69,s69,s53
buffer_load_dwordx4 v6,s[44:47],s69 offen lds
RB34_%=:
ds_read_b128 v[128:131],v112 offset:64
v_smfmac_f32_16x16x64_bf16 v[184:187],v[92:95],v[132:139],v13
s_waitcnt lgkmcnt(7)
ds_read_b128 v[136:139],v116 offset:64
ds_read_b128 v[132:135],v112 offset:3552
v_mfma_f32_16x16x32_bf16 v[164:167],v[96:99],v[152:155],0
s_waitcnt lgkmcnt(8)
ds_read_b128 v[92:95],v12 offset:4608
v_add_f32 v120,v160,v161
v_mul_f32 v124,v160,v160
v_fmac_f32 v124,v161,v161
v_mfma_f32_16x16x32_bf16 v[188:191],v[96:99],v[156:159],0
s_waitcnt lgkmcnt(6)
ds_read_b128 v[156:159],v117 offset:3552
ds_read_b128 v[96:99],v11 offset:6528
v_add_f32 v121,v162,v163
v_mul_f32 v125,v162,v162
v_fmac_f32 v125,v163,v163
v_smfmac_f32_16x16x64_bf16 v[164:167],v[100:103],v[144:151],v13
s_cbranch_vccz RB35_%=
s_add_u32 m0,m0,1024
s_add_u32 s69,s69,s53
buffer_load_dwordx4 v5,s[44:47],s69 offen lds
s_add_u32 m0,m0,s50
s_add_u32 s69,s69,s53
buffer_load_dwordx4 v6,s[44:47],s69 offen lds
RB35_%=:
ds_read_b128 v[144:147],v113 offset:64
v_add_f32 v122,v184,v185
v_mul_f32 v126,v184,v184
v_fmac_f32 v126,v185,v185
v_smfmac_f32_16x16x64_bf16 v[188:191],v[100:103],v[148:155],v13
s_waitcnt lgkmcnt(5)
s_cbranch_vccnz RB36_%=
s_mov_b64 exec,s[62:63]
s_add_u32 m0,s57,52320
s_add_u32 s64,s56,3440640
buffer_load_dwordx4 v5,s[36:39],s64 offen lds
s_add_u32 m0,s57,54048
s_nop 0
buffer_load_dwordx4 v6,s[36:39],s64 offen lds
s_mov_b64 exec,-1
RB36_%=:
ds_read_b128 v[152:155],v117 offset:64
ds_read_b128 v[148:151],v113 offset:3552
v_add_f32 v123,v186,v187
v_mul_f32 v127,v186,v186
v_fmac_f32 v127,v187,v187
v_mfma_f32_16x16x32_bf16 v[168:171],v[88:91],v[136:139],0
ds_read_b128 v[100:103],v12 offset:5760
v_add_f32 v120,v120,v164
v_fmac_f32 v124,v164,v164
v_add_f32 v120,v120,v165
v_fmac_f32 v124,v165,v165
v_mfma_f32_16x16x32_bf16 v[192:195],v[88:91],v[140:143],0
s_waitcnt lgkmcnt(6)
s_cbranch_vccz RB37_%=
s_add_u32 m0,m0,1024
s_add_u32 s69,s69,s53
buffer_load_dwordx4 v5,s[44:47],s69 offen lds
s_add_u32 m0,m0,1024
s_add_u32 s69,s69,s53
buffer_load_dwordx4 v6,s[44:47],s69 offen lds
RB37_%=:
ds_read_b128 v[140:143],v116 offset:3616
ds_read_b128 v[88:91],v11 offset:9984
v_add_f32 v121,v121,v166
v_fmac_f32 v125,v166,v166
v_add_f32 v121,v121,v167
v_fmac_f32 v125,v167,v167
v_smfmac_f32_16x16x64_bf16 v[168:171],v[92:95],v[128:135],v13
ds_read_b128 v[128:131],v112 offset:128
v_add_f32 v122,v122,v188
v_fmac_f32 v126,v188,v188
v_add_f32 v122,v122,v189
v_fmac_f32 v126,v189,v189
v_smfmac_f32_16x16x64_bf16 v[192:195],v[92:95],v[132:139],v13
s_waitcnt lgkmcnt(5)
s_cbranch_vccz RB38_%=
s_add_u32 m0,m0,s50
s_add_u32 s69,s69,s53
buffer_load_dwordx4 v5,s[44:47],s69 offen lds
s_add_u32 m0,m0,1024
s_add_u32 s69,s69,s53
buffer_load_dwordx4 v6,s[44:47],s69 offen lds
RB38_%=:
ds_read_b128 v[136:139],v116 offset:128
ds_read_b128 v[132:135],v112 offset:3616
v_add_f32 v123,v123,v190
v_fmac_f32 v127,v190,v190
v_add_f32 v123,v123,v191
v_fmac_f32 v127,v191,v191
v_mfma_f32_16x16x32_bf16 v[172:175],v[96:99],v[152:155],0
ds_read_b128 v[92:95],v12 offset:9216
v_add_f32 v120,v120,v168
v_fmac_f32 v124,v168,v168
v_add_f32 v120,v120,v169
v_fmac_f32 v124,v169,v169
v_mfma_f32_16x16x32_bf16 v[196:199],v[96:99],v[156:159],0
s_waitcnt lgkmcnt(6)
s_cbranch_vccz RB39_%=
s_add_u32 m0,m0,1024
s_add_u32 s69,s69,s53
buffer_load_dwordx4 v5,s[44:47],s69 offen lds
s_add_u32 m0,m0,s50
s_add_u32 s69,s69,s53
buffer_load_dwordx4 v6,s[44:47],s69 offen lds
RB39_%=:
ds_read_b128 v[156:159],v117 offset:3616
ds_read_b128 v[96:99],v11 offset:11136
v_add_f32 v121,v121,v170
v_fmac_f32 v125,v170,v170
v_add_f32 v121,v121,v171
v_fmac_f32 v125,v171,v171
v_smfmac_f32_16x16x64_bf16 v[172:175],v[100:103],v[144:151],v13
ds_read_b128 v[144:147],v113 offset:128
v_add_f32 v122,v122,v192
v_fmac_f32 v126,v192,v192
v_add_f32 v122,v122,v193
v_fmac_f32 v126,v193,v193
v_smfmac_f32_16x16x64_bf16 v[196:199],v[100:103],v[148:155],v13
s_waitcnt lgkmcnt(5)
s_cbranch_vccz RB40_%=
s_add_u32 m0,m0,1024
s_add_u32 s69,s69,s53
buffer_load_dwordx4 v5,s[44:47],s69 offen lds
s_add_u32 m0,m0,1024
s_add_u32 s69,s69,s53
buffer_load_dwordx4 v6,s[44:47],s69 offen lds
RB40_%=:
ds_read_b128 v[152:155],v117 offset:128
ds_read_b128 v[148:151],v113 offset:3616
v_add_f32 v123,v123,v194
v_fmac_f32 v127,v194,v194
v_add_f32 v123,v123,v195
v_fmac_f32 v127,v195,v195
v_mfma_f32_16x16x32_bf16 v[176:179],v[88:91],v[136:139],0
ds_read_b128 v[100:103],v12 offset:10368
v_add_f32 v120,v120,v172
v_fmac_f32 v124,v172,v172
v_add_f32 v120,v120,v173
v_fmac_f32 v124,v173,v173
v_mfma_f32_16x16x32_bf16 v[200:203],v[88:91],v[140:143],0
s_waitcnt lgkmcnt(6)
ds_read_b128 v[140:143],v118 offset:3488
ds_read_b128 v[88:91],v11 offset:3072
v_add_f32 v121,v121,v174
v_fmac_f32 v125,v174,v174
v_add_f32 v121,v121,v175
v_fmac_f32 v125,v175,v175
v_smfmac_f32_16x16x64_bf16 v[176:179],v[92:95],v[128:135],v13
ds_read_b128 v[128:131],v114 offset:0
v_add_f32 v122,v122,v196
v_fmac_f32 v126,v196,v196
v_add_f32 v122,v122,v197
v_fmac_f32 v126,v197,v197
v_smfmac_f32_16x16x64_bf16 v[200:203],v[92:95],v[132:139],v13
s_waitcnt lgkmcnt(5)
ds_read_b128 v[136:139],v118 offset:0
ds_read_b128 v[132:135],v114 offset:3488
v_add_f32 v123,v123,v198
v_fmac_f32 v127,v198,v198
v_add_f32 v123,v123,v199
v_fmac_f32 v127,v199,v199
v_mfma_f32_16x16x32_bf16 v[180:183],v[96:99],v[152:155],0
ds_read_b128 v[92:95],v12 offset:2304
v_add_f32 v120,v120,v176
v_fmac_f32 v124,v176,v176
v_add_f32 v120,v120,v177
v_fmac_f32 v124,v177,v177
v_mfma_f32_16x16x32_bf16 v[204:207],v[96:99],v[156:159],0
s_waitcnt lgkmcnt(6)
ds_read_b128 v[156:159],v119 offset:3488
ds_read_b128 v[96:99],v11 offset:4224
v_add_f32 v121,v121,v178
v_fmac_f32 v125,v178,v178
v_add_f32 v121,v121,v179
v_fmac_f32 v125,v179,v179
v_smfmac_f32_16x16x64_bf16 v[180:183],v[100:103],v[144:151],v13
ds_read_b128 v[144:147],v115 offset:0
v_add_f32 v122,v122,v200
v_fmac_f32 v126,v200,v200
v_add_f32 v122,v122,v201
v_fmac_f32 v126,v201,v201
v_smfmac_f32_16x16x64_bf16 v[204:207],v[100:103],v[148:155],v13
s_waitcnt lgkmcnt(5)
ds_read_b128 v[152:155],v119 offset:0
ds_read_b128 v[148:151],v115 offset:3488
v_add_f32 v123,v123,v202
v_fmac_f32 v127,v202,v202
v_add_f32 v123,v123,v203
v_fmac_f32 v127,v203,v203
v_mfma_f32_16x16x32_bf16 v[208:211],v[88:91],v[136:139],0
ds_read_b128 v[100:103],v12 offset:3456
v_add_f32 v120,v120,v180
v_fmac_f32 v124,v180,v180
v_add_f32 v120,v120,v181
v_fmac_f32 v124,v181,v181
v_mfma_f32_16x16x32_bf16 v[232:235],v[88:91],v[140:143],0
s_waitcnt lgkmcnt(6)
ds_read_b128 v[140:143],v118 offset:3552
ds_read_b128 v[88:91],v11 offset:7680
v_add_f32 v121,v121,v182
v_fmac_f32 v125,v182,v182
v_add_f32 v121,v121,v183
v_fmac_f32 v125,v183,v183
v_smfmac_f32_16x16x64_bf16 v[208:211],v[92:95],v[128:135],v13
ds_read_b128 v[128:131],v114 offset:64
v_add_f32 v122,v122,v204
v_fmac_f32 v126,v204,v204
v_add_f32 v122,v122,v205
v_fmac_f32 v126,v205,v205
v_smfmac_f32_16x16x64_bf16 v[232:235],v[92:95],v[132:139],v13
s_waitcnt lgkmcnt(5)
ds_read_b128 v[136:139],v118 offset:64
ds_read_b128 v[132:135],v114 offset:3552
v_add_f32 v123,v123,v206
v_fmac_f32 v127,v206,v206
v_add_f32 v123,v123,v207
v_fmac_f32 v127,v207,v207
v_mfma_f32_16x16x32_bf16 v[212:215],v[96:99],v[152:155],0
ds_read_b128 v[92:95],v12 offset:6912
v_add_f32 v120,v120,v208
v_fmac_f32 v124,v208,v208
v_add_f32 v120,v120,v209
v_fmac_f32 v124,v209,v209
v_mfma_f32_16x16x32_bf16 v[236:239],v[96:99],v[156:159],0
s_waitcnt lgkmcnt(6)
s_setprio 2
ds_read_b128 v[156:159],v119 offset:3552
ds_read_b128 v[96:99],v11 offset:8832
v_add_f32 v121,v121,v210
v_fmac_f32 v125,v210,v210
v_add_f32 v121,v121,v211
v_fmac_f32 v125,v211,v211
v_smfmac_f32_16x16x64_bf16 v[212:215],v[100:103],v[144:151],v13
ds_read_b128 v[144:147],v115 offset:64
v_add_f32 v122,v122,v232
v_fmac_f32 v126,v232,v232
v_add_f32 v122,v122,v233
v_fmac_f32 v126,v233,v233
v_smfmac_f32_16x16x64_bf16 v[236:239],v[100:103],v[148:155],v13
s_waitcnt lgkmcnt(5)
ds_read_b128 v[152:155],v119 offset:64
ds_read_b128 v[148:151],v115 offset:3552
v_add_f32 v123,v123,v234
v_fmac_f32 v127,v234,v234
v_add_f32 v123,v123,v235
v_fmac_f32 v127,v235,v235
v_mfma_f32_16x16x32_bf16 v[216:219],v[88:91],v[136:139],0
ds_read_b128 v[100:103],v12 offset:8064
v_add_f32 v120,v120,v212
v_fmac_f32 v124,v212,v212
v_add_f32 v120,v120,v213
v_fmac_f32 v124,v213,v213
v_add_f32 v121,v121,v214
v_mfma_f32_16x16x32_bf16 v[240:243],v[88:91],v[140:143],0
s_waitcnt lgkmcnt(6)
ds_read_b128 v[140:143],v118 offset:3616
ds_read_b128 v[88:91],v11 offset:12288
v_fmac_f32 v125,v214,v214
v_add_f32 v121,v121,v215
v_fmac_f32 v125,v215,v215
v_add_f32 v122,v122,v236
v_smfmac_f32_16x16x64_bf16 v[216:219],v[92:95],v[128:135],v13
ds_read_b128 v[128:131],v114 offset:128
v_fmac_f32 v126,v236,v236
v_add_f32 v122,v122,v237
v_fmac_f32 v126,v237,v237
v_add_f32 v123,v123,v238
v_fmac_f32 v127,v238,v238
v_smfmac_f32_16x16x64_bf16 v[240:243],v[92:95],v[132:139],v13
s_waitcnt lgkmcnt(5)
ds_read_b128 v[136:139],v118 offset:128
ds_read_b128 v[132:135],v114 offset:3616
v_add_f32 v123,v123,v239
v_fmac_f32 v127,v239,v239
v_add_u32 v112,v112,v8
v_add_u32 v116,v116,v8
v_mfma_f32_16x16x32_bf16 v[220:223],v[96:99],v[152:155],0
ds_read_b128 v[92:95],v12 offset:11520
ds_read_b128 v[104:107],v14 offset:0
v_add_f32 v120,v120,v216
v_fmac_f32 v124,v216,v216
v_add_f32 v120,v120,v217
v_fmac_f32 v124,v217,v217
v_mfma_f32_16x16x32_bf16 v[244:247],v[96:99],v[156:159],0
s_waitcnt lgkmcnt(7)
ds_read_b128 v[108:111],v4 offset:3584
ds_read_b128 v[156:159],v119 offset:3616
v_add_f32 v121,v121,v218
v_fmac_f32 v125,v218,v218
v_add_f32 v121,v121,v219
v_fmac_f32 v125,v219,v219
v_smfmac_f32_16x16x64_bf16 v[220:223],v[100:103],v[144:151],v13
ds_read_b128 v[96:99],v11 offset:13440
ds_read_b128 v[144:147],v115 offset:128
v_add_f32 v122,v122,v240
v_fmac_f32 v126,v240,v240
v_add_f32 v122,v122,v241
v_fmac_f32 v126,v241,v241
v_smfmac_f32_16x16x64_bf16 v[244:247],v[100:103],v[148:155],v13
s_waitcnt lgkmcnt(7)
ds_read_b128 v[152:155],v119 offset:128
ds_read_b128 v[148:151],v115 offset:3616
v_add_f32 v123,v123,v242
v_fmac_f32 v127,v242,v242
v_add_f32 v123,v123,v243
v_fmac_f32 v127,v243,v243
v_mfma_f32_16x16x32_bf16 v[224:227],v[88:91],v[136:139],0
ds_read_b128 v[100:103],v12 offset:12672
v_add_f32 v120,v120,v220
v_fmac_f32 v124,v220,v220
v_add_f32 v120,v120,v221
v_fmac_f32 v124,v221,v221
v_mfma_f32_16x16x32_bf16 v[248:251],v[88:91],v[140:143],0
s_waitcnt lgkmcnt(8)
s_setprio 1
ds_read_b128 v[140:143],v116 offset:192
ds_read_b128 v[88:91],v112 offset:3616
v_add_f32 v121,v121,v222
v_fmac_f32 v125,v222,v222
v_add_f32 v121,v121,v223
v_fmac_f32 v125,v223,v223
v_smfmac_f32_16x16x64_bf16 v[224:227],v[92:95],v[128:135],v13
ds_read_b128 v[128:131],v112 offset:3488
v_add_f32 v122,v122,v244
v_fmac_f32 v126,v244,v244
v_add_f32 v122,v122,v245
v_fmac_f32 v126,v245,v245
v_smfmac_f32_16x16x64_bf16 v[248:251],v[92:95],v[132:139],v13
s_waitcnt lgkmcnt(5)
ds_read_b128 v[136:139],v116 offset:0
ds_read_b128 v[132:135],v112 offset:3680
v_add_f32 v123,v123,v246
v_fmac_f32 v127,v246,v246
v_add_f32 v123,v123,v247
v_fmac_f32 v127,v247,v247
v_mfma_f32_16x16x32_bf16 v[228:231],v[96:99],v[152:155],0
ds_read_b128 v[92:95],v112 offset:3808
v_add_f32 v120,v120,v224
v_fmac_f32 v124,v224,v224
v_add_f32 v120,v120,v225
v_fmac_f32 v124,v225,v225
v_mfma_f32_16x16x32_bf16 v[252:255],v[96:99],v[156:159],0
s_waitcnt lgkmcnt(6)
ds_read_b128 v[156:159],v116 offset:256
ds_read_b128 v[96:99],v116 offset:128
v_add_f32 v121,v121,v226
v_fmac_f32 v125,v226,v226
v_add_f32 v121,v121,v227
v_fmac_f32 v125,v227,v227
v_smfmac_f32_16x16x64_bf16 v[228:231],v[100:103],v[144:151],v13
ds_read_b128 v[144:147],v112 offset:3552
v_add_f32 v122,v122,v248
v_fmac_f32 v126,v248,v248
v_add_f32 v122,v122,v249
v_fmac_f32 v126,v249,v249
v_smfmac_f32_16x16x64_bf16 v[252:255],v[100:103],v[148:155],v13
s_setprio 0
ds_read_b128 v[152:155],v116 offset:64
ds_read_b128 v[148:151],v112 offset:3744
v_add_f32 v123,v123,v250
v_fmac_f32 v127,v250,v250
v_add_f32 v123,v123,v251
v_fmac_f32 v127,v251,v251
ds_read_b128 v[100:103],v116 offset:320
v_add_f32 v120,v120,v228
v_fmac_f32 v124,v228,v228
v_add_f32 v120,v120,v229
v_fmac_f32 v124,v229,v229
v_add_f32 v121,v121,v230
v_fmac_f32 v125,v230,v230
v_add_f32 v121,v121,v231
v_fmac_f32 v125,v231,v231
s_waitcnt lgkmcnt(0)
s_cbranch_vccz RB41_%=
s_waitcnt vmcnt(0)
RB41_%=:
s_barrier
s_setprio 0
v_add_f32 v122,v122,v252
v_fmac_f32 v126,v252,v252
v_add_f32 v122,v122,v253
s_cbranch_vccnz RB42_%=
s_mov_b64 exec,s[62:63]
s_add_u32 m0,s57,55808
s_add_u32 s64,s56,3538944
buffer_load_dwordx4 v5,s[36:39],s64 offen lds
s_add_u32 m0,s57,57536
s_nop 0
buffer_load_dwordx4 v6,s[36:39],s64 offen lds
s_mov_b64 exec,-1
RB42_%=:
v_fmac_f32 v126,v253,v253
v_add_f32 v123,v123,v254
s_cbranch_vccz RB43_%=
s_add_u32 m0,s58,0
s_mul_i32 s69,s59,9
s_add_u32 s69,s69,s97
buffer_load_dwordx4 v5,s[44:47],s69 offen lds
s_add_u32 m0,m0,1024
s_add_u32 s69,s69,s53
buffer_load_dwordx4 v6,s[44:47],s69 offen lds
RB43_%=:
v_fmac_f32 v127,v254,v254
v_add_f32 v123,v123,v255
v_fmac_f32 v127,v255,v255
s_setprio 3
ds_read_b128 v[56:59],v14 offset:256
ds_read_b128 v[60:63],v4 offset:3616
s_setprio 0
v_permlane32_swap_b32 v120,v122
v_permlane32_swap_b32 v121,v123
v_permlane32_swap_b32 v124,v126
v_permlane32_swap_b32 v125,v127
v_add_f32 v120,v120,v122
v_add_f32 v121,v121,v123
v_add_f32 v124,v124,v126
v_add_f32 v125,v125,v127
s_cbranch_vccz RB44_%=
s_add_u32 m0,m0,1024
s_add_u32 s69,s69,s53
buffer_load_dwordx4 v5,s[44:47],s69 offen lds
s_add_u32 m0,m0,s50
s_add_u32 s69,s69,s53
buffer_load_dwordx4 v6,s[44:47],s69 offen lds
RB44_%=:
v_permlane16_swap_b32 v120,v121
v_permlane16_swap_b32 v124,v125
v_add_f32 v113,v120,v121
v_add_f32 v114,v124,v125
v_mul_f32 v113,0x3c2aaaab,v113
v_mul_f32 v114,0x3c2aaaab,v114
v_mul_f32 v115,0x5e000000,v114
v_fma_f32 v114,-v113,v113,v114
v_pk_maximum3_f16 v7,v7,v115,v115 op_sel:[0,1,1] op_sel_hi:[1,1,1]
v_max_f32 v114,0,v114
v_add_f32 v114,0x3727c5ac,v114
v_rsq_f32 v114,v114
s_cbranch_vccz RB45_%=
s_add_u32 m0,m0,1024
s_add_u32 s69,s69,s53
buffer_load_dwordx4 v5,s[44:47],s69 offen lds
s_add_u32 m0,m0,1024
s_add_u32 s69,s69,s53
buffer_load_dwordx4 v6,s[44:47],s69 offen lds
RB45_%=:
v_mul_f32 v120,0x8befe000,v114
v_mul_f32 v121,0x8befe000,v114
v_mul_f32 v124,-v113,v120
v_mul_f32 v125,-v113,v120
v_permlane16_swap_b32 v120,v121
s_nop 0
v_permlane16_swap_b32 v124,v125
v_mov_b32 v122,v120
v_mov_b32 v123,v121
v_mov_b32 v126,v124
v_mov_b32 v127,v125
v_permlane32_swap_b32 v120,v122
v_permlane32_swap_b32 v124,v126
s_cbranch_vccz RB46_%=
s_add_u32 m0,m0,s50
s_add_u32 s69,s69,s53
buffer_load_dwordx4 v5,s[44:47],s69 offen lds
s_add_u32 m0,m0,1024
s_add_u32 s69,s69,s53
buffer_load_dwordx4 v6,s[44:47],s69 offen lds
RB46_%=:
v_permlane32_swap_b32 v121,v123
v_permlane32_swap_b32 v125,v127
v_fma_f32 v160,v160,v120,v124
s_setprio 3
ds_read_b64_tr_b16 v[64:65],v1 offset:12832
s_setprio 0
v_fma_f32 v161,v161,v120,v124
s_setprio 3
ds_read_b64_tr_b16 v[66:67],v1 offset:14880
s_setprio 0
v_fma_f32 v162,v162,v121,v125
s_setprio 3
ds_read_b128 v[112:115],v4 offset:1152
s_setprio 0
v_fma_f32 v163,v163,v121,v125
s_setprio 3
ds_read_b64_tr_b16 v[68:69],v2 offset:12832
s_setprio 0
v_fma_f32 v164,v164,v120,v124
s_setprio 3
ds_read_b64_tr_b16 v[70:71],v2 offset:14880
s_setprio 0
v_fma_f32 v165,v165,v120,v124
s_setprio 3
ds_read_b128 v[116:119],v4 offset:1184
s_setprio 0
v_fma_f32 v166,v166,v121,v125
s_setprio 3
ds_read_b64_tr_b16 v[72:73],v1 offset:16928
s_setprio 0
v_fma_f32 v167,v167,v121,v125
s_setprio 3
ds_read_b64_tr_b16 v[74:75],v1 offset:18976
s_setprio 0
v_fma_f32 v208,v208,v120,v124
s_setprio 3
ds_read_b64_tr_b16 v[76:77],v2 offset:16928
s_setprio 0
v_fma_f32 v209,v209,v120,v124
s_cbranch_vccz RB47_%=
s_add_u32 m0,m0,1024
s_add_u32 s69,s69,s53
buffer_load_dwordx4 v5,s[44:47],s69 offen lds
s_add_u32 m0,m0,s50
s_add_u32 s69,s69,s53
buffer_load_dwordx4 v6,s[44:47],s69 offen lds
RB47_%=:
s_setprio 3
ds_read_b64_tr_b16 v[78:79],v2 offset:18976
s_setprio 0
v_fma_f32 v210,v210,v121,v125
s_setprio 3
ds_read_b64_tr_b16 v[80:81],v1 offset:21024
s_setprio 0
v_fma_f32 v211,v211,v121,v125
s_setprio 3
ds_read_b64_tr_b16 v[82:83],v1 offset:23072
s_setprio 0
v_fma_f32 v212,v212,v120,v124
s_setprio 3
ds_read_b64_tr_b16 v[84:85],v2 offset:21024
s_setprio 0
v_fma_f32 v213,v213,v120,v124
s_setprio 3
ds_read_b64_tr_b16 v[86:87],v2 offset:23072
s_setprio 0
v_fma_f32 v214,v214,v121,v125
v_fma_f32 v215,v215,v121,v125
v_cvt_pk_bf16_f32 v16,v160,v161
s_cbranch_vccnz RB48_%=
s_mov_b64 exec,s[62:63]
s_add_u32 m0,s57,59296
s_add_u32 s64,s56,3637248
buffer_load_dwordx4 v5,s[36:39],s64 offen lds
s_add_u32 m0,s57,61024
s_nop 0
buffer_load_dwordx4 v6,s[36:39],s64 offen lds
s_mov_b64 exec,-1
RB48_%=:
v_cvt_pk_bf16_f32 v20,v162,v163
v_cvt_pk_bf16_f32 v17,v164,v165
v_cvt_pk_bf16_f32 v21,v166,v167
v_cvt_pk_bf16_f32 v18,v208,v209
v_cvt_pk_bf16_f32 v22,v210,v211
v_cvt_pk_bf16_f32 v19,v212,v213
v_cvt_pk_bf16_f32 v23,v214,v215
v_fma_f32 v168,v168,v120,v124
v_fma_f32 v169,v169,v120,v124
v_mfma_f32_16x16x32_bf16 v[160:163],v[104:107],v[128:131],v[108:111]
s_cbranch_vccz RB49_%=
s_add_u32 m0,m0,1024
s_add_u32 s69,s69,s53
buffer_load_dwordx4 v5,s[44:47],s69 offen lds
s_add_u32 m0,m0,1024
s_add_u32 s69,s69,s53
buffer_load_dwordx4 v6,s[44:47],s69 offen lds
RB49_%=:
v_fma_f32 v170,v170,v121,v125
v_fma_f32 v171,v171,v121,v125
v_fma_f32 v172,v172,v120,v124
v_fma_f32 v173,v173,v120,v124
v_fma_f32 v174,v174,v121,v125
v_fma_f32 v175,v175,v121,v125
v_mfma_f32_16x16x32_bf16 v[164:167],v[104:107],v[132:135],v[108:111]
v_fma_f32 v216,v216,v120,v124
v_fma_f32 v217,v217,v120,v124
v_fma_f32 v218,v218,v121,v125
v_fma_f32 v219,v219,v121,v125
v_fma_f32 v220,v220,v120,v124
v_mfma_f32_16x16x32_bf16 v[208:211],v[104:107],v[136:139],v[108:111]
v_fma_f32 v221,v221,v120,v124
v_fma_f32 v222,v222,v121,v125
v_fma_f32 v223,v223,v121,v125
v_cvt_pk_bf16_f32 v24,v168,v169
v_cvt_pk_bf16_f32 v28,v170,v171
s_setprio 3
v_mfma_f32_16x16x32_bf16 v[212:215],v[104:107],v[140:143],v[108:111]
ds_read_b128 v[104:107],v14 offset:512
ds_read_b128 v[108:111],v4 offset:3712
s_setprio 0
v_cvt_pk_bf16_f32 v25,v172,v173
v_cvt_pk_bf16_f32 v29,v174,v175
v_cvt_pk_bf16_f32 v26,v216,v217
v_cvt_pk_bf16_f32 v30,v218,v219
v_cvt_pk_bf16_f32 v27,v220,v221
v_cvt_pk_bf16_f32 v31,v222,v223
v_fma_f32 v176,v176,v120,v124
v_fma_f32 v177,v177,v120,v124
s_waitcnt lgkmcnt(15)
v_mfma_f32_16x16x32_bf16 v[168:171],v[56:59],v[128:131],v[60:63]
v_fma_f32 v178,v178,v121,v125
v_fma_f32 v179,v179,v121,v125
v_fma_f32 v180,v180,v120,v124
v_fma_f32 v181,v181,v120,v124
v_fma_f32 v182,v182,v121,v125
v_fma_f32 v183,v183,v121,v125
v_mfma_f32_16x16x32_bf16 v[172:175],v[56:59],v[132:135],v[60:63]
v_fma_f32 v224,v224,v120,v124
v_fma_f32 v225,v225,v120,v124
v_fma_f32 v226,v226,v121,v125
v_fma_f32 v227,v227,v121,v125
v_fma_f32 v228,v228,v120,v124
v_mfma_f32_16x16x32_bf16 v[216:219],v[56:59],v[136:139],v[60:63]
v_fma_f32 v229,v229,v120,v124
v_fma_f32 v230,v230,v121,v125
v_fma_f32 v231,v231,v121,v125
v_cvt_pk_bf16_f32 v32,v176,v177
v_cvt_pk_bf16_f32 v36,v178,v179
s_setprio 3
v_mfma_f32_16x16x32_bf16 v[220:223],v[56:59],v[140:143],v[60:63]
ds_read_b128 v[56:59],v14 offset:768
ds_read_b128 v[60:63],v4 offset:3744
s_setprio 0
v_cvt_pk_bf16_f32 v33,v180,v181
v_cvt_pk_bf16_f32 v37,v182,v183
v_cvt_pk_bf16_f32 v34,v224,v225
v_cvt_pk_bf16_f32 v38,v226,v227
v_cvt_pk_bf16_f32 v35,v228,v229
v_cvt_pk_bf16_f32 v39,v230,v231
v_fma_f32 v184,v184,v122,v126
s_waitcnt lgkmcnt(2)
v_mfma_f32_16x16x32_bf16 v[176:179],v[104:107],v[144:147],v[108:111]
s_cbranch_vccnz RB50_%=
s_mov_b64 exec,s[62:63]
s_add_u32 m0,s57,62784
s_add_u32 s64,s56,3735552
buffer_load_dwordx4 v5,s[36:39],s64 offen lds
s_add_u32 m0,s57,64512
s_nop 0
buffer_load_dwordx4 v6,s[36:39],s64 offen lds
s_mov_b64 exec,-1
RB50_%=:
v_fma_f32 v185,v185,v122,v126
v_fma_f32 v186,v186,v123,v127
v_fma_f32 v187,v187,v123,v127
v_mfma_f32_16x16x32_bf16 v[128:131],v[64:67],v[16:19],v[112:115]
v_fma_f32 v188,v188,v122,v126
v_fma_f32 v189,v189,v122,v126
v_fma_f32 v190,v190,v123,v127
v_mfma_f32_16x16x32_bf16 v[180:183],v[104:107],v[148:151],v[108:111]
v_fma_f32 v191,v191,v123,v127
v_fma_f32 v232,v232,v122,v126
v_fma_f32 v233,v233,v122,v126
v_mfma_f32_16x16x32_bf16 v[136:139],v[64:67],v[20:23],v[112:115]
v_fma_f32 v234,v234,v123,v127
v_fma_f32 v235,v235,v123,v127
v_fma_f32 v236,v236,v122,v126
v_mfma_f32_16x16x32_bf16 v[224:227],v[104:107],v[152:155],v[108:111]
v_fma_f32 v237,v237,v122,v126
v_fma_f32 v238,v238,v123,v127
v_fma_f32 v239,v239,v123,v127
v_mfma_f32_16x16x32_bf16 v[140:143],v[68:71],v[20:23],v[116:119]
v_cvt_pk_bf16_f32 v40,v184,v185
v_cvt_pk_bf16_f32 v44,v186,v187
v_cvt_pk_bf16_f32 v41,v188,v189
s_setprio 3
v_mfma_f32_16x16x32_bf16 v[228:231],v[104:107],v[156:159],v[108:111]
ds_read_b128 v[104:107],v14 offset:1024
ds_read_b128 v[108:111],v4 offset:3840
s_setprio 0
v_cvt_pk_bf16_f32 v45,v190,v191
v_cvt_pk_bf16_f32 v42,v232,v233
v_cvt_pk_bf16_f32 v46,v234,v235
v_mfma_f32_16x16x32_bf16 v[132:135],v[68:71],v[16:19],v[116:119]
v_cvt_pk_bf16_f32 v43,v236,v237
v_cvt_pk_bf16_f32 v47,v238,v239
v_fma_f32 v192,v192,v122,v126
s_waitcnt lgkmcnt(2)
v_mfma_f32_16x16x32_bf16 v[184:187],v[56:59],v[144:147],v[60:63]
v_fma_f32 v193,v193,v122,v126
v_fma_f32 v194,v194,v123,v127
v_mfma_f32_16x16x32_bf16 v[128:131],v[72:75],v[24:27],v[128:131]
v_fma_f32 v195,v195,v123,v127
v_fma_f32 v196,v196,v122,v126
v_mfma_f32_16x16x32_bf16 v[188:191],v[56:59],v[148:151],v[60:63]
v_fma_f32 v197,v197,v122,v126
v_fma_f32 v198,v198,v123,v127
v_mfma_f32_16x16x32_bf16 v[136:139],v[72:75],v[28:31],v[136:139]
v_fma_f32 v199,v199,v123,v127
v_fma_f32 v240,v240,v122,v126
v_mfma_f32_16x16x32_bf16 v[232:235],v[56:59],v[152:155],v[60:63]
v_fma_f32 v241,v241,v122,v126
v_fma_f32 v242,v242,v123,v127
v_mfma_f32_16x16x32_bf16 v[140:143],v[76:79],v[28:31],v[140:143]
v_fma_f32 v243,v243,v123,v127
v_fma_f32 v244,v244,v122,v126
v_mfma_f32_16x16x32_bf16 v[236:239],v[56:59],v[156:159],v[60:63]
s_cbranch_vccnz RB51_%=
s_mov_b64 exec,s[62:63]
s_add_u32 m0,s57,66272
s_add_u32 s64,s56,3833856
buffer_load_dwordx4 v5,s[36:39],s64 offen lds
s_add_u32 m0,s57,68000
s_nop 0
buffer_load_dwordx4 v6,s[36:39],s64 offen lds
s_mov_b64 exec,-1
RB51_%=:
s_setprio 3
ds_read_b128 v[144:147],v14 offset:1280
ds_read_b128 v[148:151],v4 offset:3872
s_setprio 0
v_fma_f32 v245,v245,v122,v126
v_fma_f32 v246,v246,v123,v127
v_mfma_f32_16x16x32_bf16 v[132:135],v[76:79],v[24:27],v[132:135]
v_fma_f32 v247,v247,v123,v127
v_cvt_pk_bf16_f32 v48,v192,v193
v_cvt_pk_bf16_f32 v52,v194,v195
v_cvt_pk_bf16_f32 v49,v196,v197
v_cvt_pk_bf16_f32 v53,v198,v199
v_cvt_pk_bf16_f32 v50,v240,v241
v_cvt_pk_bf16_f32 v54,v242,v243
v_cvt_pk_bf16_f32 v51,v244,v245
v_cvt_pk_bf16_f32 v55,v246,v247
v_fma_f32 v200,v200,v122,v126
s_waitcnt lgkmcnt(2)
v_mfma_f32_16x16x32_bf16 v[192:195],v[104:107],v[88:91],v[108:111]
v_fma_f32 v201,v201,v122,v126
v_fma_f32 v202,v202,v123,v127
v_fma_f32 v203,v203,v123,v127
v_mfma_f32_16x16x32_bf16 v[128:131],v[80:83],v[32:35],v[128:131]
v_fma_f32 v204,v204,v122,v126
v_fma_f32 v205,v205,v122,v126
v_fma_f32 v206,v206,v123,v127
v_mfma_f32_16x16x32_bf16 v[196:199],v[104:107],v[92:95],v[108:111]
v_fma_f32 v207,v207,v123,v127
v_fma_f32 v248,v248,v122,v126
v_fma_f32 v249,v249,v122,v126
v_mfma_f32_16x16x32_bf16 v[136:139],v[80:83],v[36:39],v[136:139]
v_fma_f32 v250,v250,v123,v127
v_fma_f32 v251,v251,v123,v127
v_fma_f32 v252,v252,v122,v126
v_mfma_f32_16x16x32_bf16 v[240:243],v[104:107],v[96:99],v[108:111]
v_fma_f32 v253,v253,v122,v126
v_fma_f32 v254,v254,v123,v127
v_fma_f32 v255,v255,v123,v127
v_mfma_f32_16x16x32_bf16 v[140:143],v[84:87],v[36:39],v[140:143]
v_cvt_pk_bf16_f32 v56,v200,v201
v_cvt_pk_bf16_f32 v60,v202,v203
v_cvt_pk_bf16_f32 v57,v204,v205
v_mfma_f32_16x16x32_bf16 v[244:247],v[104:107],v[100:103],v[108:111]
v_cvt_pk_bf16_f32 v61,v206,v207
v_cvt_pk_bf16_f32 v58,v248,v249
v_cvt_pk_bf16_f32 v62,v250,v251
v_mfma_f32_16x16x32_bf16 v[132:135],v[84:87],v[32:35],v[132:135]
v_cvt_pk_bf16_f32 v59,v252,v253
v_cvt_pk_bf16_f32 v63,v254,v255
s_waitcnt lgkmcnt(0)
v_mfma_f32_16x16x32_bf16 v[200:203],v[144:147],v[88:91],v[148:151]
s_cbranch_vccz RB52_%=
s_waitcnt vmcnt(0)
RB52_%=:
s_barrier
s_setprio 3
ds_read_b64_tr_b16 v[104:105],v3 offset:128
v_lshlrev_b32_sdwa v128,1,v128 dst_sel:DWORD dst_unused:UNUSED_PAD src0_sel:DWORD src1_sel:WORD_1
v_lshlrev_b32_sdwa v130,1,v130 dst_sel:DWORD dst_unused:UNUSED_PAD src0_sel:DWORD src1_sel:WORD_1
v_lshlrev_b32_sdwa v129,1,v129 dst_sel:DWORD dst_unused:UNUSED_PAD src0_sel:DWORD src1_sel:WORD_1
v_mfma_f32_16x16x32_bf16 v[204:207],v[144:147],v[92:95],v[148:151]
ds_read_u16 v128,v128
ds_read_b64_tr_b16 v[106:107],v3 offset:1664
v_lshlrev_b32_sdwa v131,1,v131 dst_sel:DWORD dst_unused:UNUSED_PAD src0_sel:DWORD src1_sel:WORD_1
v_fma_mixlo_f16 v120,v140,s99,0
v_fma_mixlo_f16 v122,v141,s99,0
v_mfma_f32_16x16x32_bf16 v[248:251],v[144:147],v[96:99],v[148:151]
ds_read_u16_d16_hi v130,v130
ds_read_b64_tr_b16 v[108:109],v3 offset:136
v_lshlrev_b32_sdwa v132,1,v132 dst_sel:DWORD dst_unused:UNUSED_PAD src0_sel:DWORD src1_sel:WORD_1
v_lshlrev_b32_sdwa v134,1,v134 dst_sel:DWORD dst_unused:UNUSED_PAD src0_sel:DWORD src1_sel:WORD_1
v_mfma_f32_16x16x32_bf16 v[252:255],v[144:147],v[100:103],v[148:151]
s_cbranch_vccnz RB53_%=
s_mov_b64 exec,s[62:63]
s_add_u32 m0,s57,0
s_add_u32 s64,s56,3932160
buffer_load_dwordx4 v5,s[36:39],s64 offen lds
s_add_u32 m0,s57,1728
s_nop 0
buffer_load_dwordx4 v6,s[36:39],s64 offen lds
s_mov_b64 exec,-1
RB53_%=:
ds_read_u16 v129,v129
ds_read_b64_tr_b16 v[110:111],v3 offset:1672
v_fma_mixhi_f16 v120,v142,s99,0
v_fma_mixhi_f16 v122,v143,s99,0
v_lshlrev_b32_sdwa v133,1,v133 dst_sel:DWORD dst_unused:UNUSED_PAD src0_sel:DWORD src1_sel:WORD_1
v_lshlrev_b32_sdwa v135,1,v135 dst_sel:DWORD dst_unused:UNUSED_PAD src0_sel:DWORD src1_sel:WORD_1
v_mfma_f32_16x16x32_bf16 v[144:147],v[64:67],v[40:43],v[112:115]
ds_read_u16_d16_hi v131,v131
ds_read_b64_tr_b16 v[88:89],v3 offset:0
v_pk_fma_f16 v121,v120,v120,s54
v_pk_fma_f16 v123,v122,v122,s54
v_pk_mul_f16 v121,v120,v121
v_mfma_f32_16x16x32_bf16 v[152:155],v[64:67],v[44:47],v[112:115]
ds_read_u16 v132,v132
ds_read_b64_tr_b16 v[90:91],v3 offset:1536
v_pk_mul_f16 v123,v122,v123
v_lshlrev_b32_sdwa v136,1,v136 dst_sel:DWORD dst_unused:UNUSED_PAD src0_sel:DWORD src1_sel:WORD_1
v_mfma_f32_16x16x32_bf16 v[156:159],v[68:71],v[44:47],v[116:119]
ds_read_u16_d16_hi v134,v134
ds_read_b64_tr_b16 v[92:93],v3 offset:8
v_lshlrev_b32_sdwa v138,1,v138 dst_sel:DWORD dst_unused:UNUSED_PAD src0_sel:DWORD src1_sel:WORD_1
v_exp_f16 v120,v121
v_mfma_f32_16x16x32_bf16 v[148:151],v[68:71],v[40:43],v[116:119]
ds_read_u16 v133,v133
ds_read_b64_tr_b16 v[94:95],v3 offset:1544
v_exp_f16 v122,v123
v_lshlrev_b32_sdwa v137,1,v137 dst_sel:DWORD dst_unused:UNUSED_PAD src0_sel:DWORD src1_sel:WORD_1
v_mfma_f32_16x16x32_bf16 v[144:147],v[72:75],v[48:51],v[144:147]
ds_read_u16_d16_hi v135,v135
ds_read_b64_tr_b16 v[96:97],v3 offset:64
v_lshlrev_b32_sdwa v139,1,v139 dst_sel:DWORD dst_unused:UNUSED_PAD src0_sel:DWORD src1_sel:WORD_1
v_exp_f16_sdwa v120,v121 dst_sel:WORD_1 dst_unused:UNUSED_PRESERVE src0_sel:WORD_1
v_mfma_f32_16x16x32_bf16 v[152:155],v[72:75],v[52:55],v[152:155]
ds_read_u16 v136,v136
ds_read_b64_tr_b16 v[98:99],v3 offset:1600
v_exp_f16_sdwa v122,v123 dst_sel:WORD_1 dst_unused:UNUSED_PRESERVE src0_sel:WORD_1
v_mfma_f32_16x16x32_bf16 v[156:159],v[76:79],v[52:55],v[156:159]
ds_read_u16_d16_hi v138,v138
ds_read_b64_tr_b16 v[100:101],v3 offset:72
v_pk_fma_f16 v120,v120,s55,s55
v_pk_fma_f16 v122,v122,s55,s55
v_rcp_f16 v121,v120
v_mfma_f32_16x16x32_bf16 v[148:151],v[76:79],v[48:51],v[148:151]
ds_read_u16 v137,v137
ds_read_b64_tr_b16 v[102:103],v3 offset:1608
v_rcp_f16 v123,v122
v_mfma_f32_16x16x32_bf16 v[144:147],v[80:83],v[56:59],v[144:147]
ds_read_u16_d16_hi v139,v139
ds_read_b64_tr_b16 v[64:65],v1 offset:0
v_rcp_f16_sdwa v121,v120 dst_sel:WORD_1 dst_unused:UNUSED_PRESERVE src0_sel:WORD_1
v_mfma_f32_16x16x32_bf16 v[152:155],v[80:83],v[60:63],v[152:155]
ds_read_b64_tr_b16 v[66:67],v1 offset:2048
v_rcp_f16_sdwa v123,v122 dst_sel:WORD_1 dst_unused:UNUSED_PRESERVE src0_sel:WORD_1
v_fma_mix_f32 v140,v140,v121,0 op_sel_hi:[0,1,0]
v_mfma_f32_16x16x32_bf16 v[156:159],v[84:87],v[60:63],v[156:159]
ds_read_b128 v[112:115],v4 offset:1280
v_fma_mix_f32 v142,v142,v121,0 op_sel:[0,1,0] op_sel_hi:[0,1,0]
s_waitcnt lgkmcnt(15)
v_lshl_add_u64 v[128:129],v[128:129],0,v[130:131]
s_waitcnt lgkmcnt(11)
v_lshl_add_u64 v[130:131],v[132:133],0,v[134:135]
v_mfma_f32_16x16x32_bf16 v[148:151],v[84:87],v[56:59],v[148:151]
ds_read_b64_tr_b16 v[68:69],v2 offset:0
v_fma_mix_f32 v141,v141,v123,0 op_sel_hi:[0,1,0]
v_fma_mix_f32 v143,v143,v123,0 op_sel:[0,1,0] op_sel_hi:[0,1,0]
v_lshlrev_b32_sdwa v144,1,v144 dst_sel:DWORD dst_unused:UNUSED_PAD src0_sel:DWORD src1_sel:WORD_1
v_lshlrev_b32_sdwa v146,1,v146 dst_sel:DWORD dst_unused:UNUSED_PAD src0_sel:DWORD src1_sel:WORD_1
v_mfma_f32_16x16x32_bf16 v[160:163],v[88:91],v[128:131],v[160:163]
ds_read_u16 v144,v144
v_lshlrev_b32_sdwa v145,1,v145 dst_sel:DWORD dst_unused:UNUSED_PAD src0_sel:DWORD src1_sel:WORD_1
v_lshlrev_b32_sdwa v147,1,v147 dst_sel:DWORD dst_unused:UNUSED_PAD src0_sel:DWORD src1_sel:WORD_1
v_mfma_f32_16x16x32_bf16 v[168:171],v[92:95],v[128:131],v[168:171]
s_waitcnt lgkmcnt(10)
s_cbranch_vccnz RB54_%=
s_mov_b64 exec,s[62:63]
s_add_u32 m0,s57,3488
s_add_u32 s64,s56,4030464
buffer_load_dwordx4 v5,s[36:39],s64 offen lds
s_add_u32 m0,s57,5216
s_nop 0
buffer_load_dwordx4 v6,s[36:39],s64 offen lds
s_mov_b64 exec,-1
RB54_%=:
ds_read_b64_tr_b16 v[70:71],v2 offset:2048
ds_read_b128 v[116:119],v4 offset:1312
v_fma_mixlo_f16 v120,v156,s99,0
v_fma_mixlo_f16 v122,v157,s99,0
v_mfma_f32_16x16x32_bf16 v[176:179],v[96:99],v[128:131],v[176:179]
s_waitcnt lgkmcnt(8)
ds_read_u16_d16_hi v146,v146
v_lshlrev_b32_sdwa v148,1,v148 dst_sel:DWORD dst_unused:UNUSED_PAD src0_sel:DWORD src1_sel:WORD_1
v_lshlrev_b32_sdwa v150,1,v150 dst_sel:DWORD dst_unused:UNUSED_PAD src0_sel:DWORD src1_sel:WORD_1
v_mfma_f32_16x16x32_bf16 v[184:187],v[100:103],v[128:131],v[184:187]
s_waitcnt lgkmcnt(8)
ds_read_b64_tr_b16 v[72:73],v1 offset:4096
ds_read_b64_tr_b16 v[74:75],v1 offset:6144
v_lshl_add_u64 v[136:137],v[136:137],0,v[138:139]
v_mfma_f32_16x16x32_bf16 v[192:195],v[104:107],v[128:131],v[192:195]
ds_read_u16 v145,v145
v_cvt_pk_bf16_f32 v138,v140,v142
v_cvt_pk_bf16_f32 v139,v141,v143
v_mfma_f32_16x16x32_bf16 v[200:203],v[108:111],v[128:131],v[200:203]
ds_read_b64_tr_b16 v[76:77],v2 offset:4096
ds_read_b64_tr_b16 v[78:79],v2 offset:6144
v_fma_mixhi_f16 v120,v158,s99,0
v_fma_mixhi_f16 v122,v159,s99,0
v_mfma_f32_16x16x32_bf16 v[164:167],v[88:91],v[136:139],v[164:167]
ds_read_u16_d16_hi v147,v147
v_lshlrev_b32_sdwa v149,1,v149 dst_sel:DWORD dst_unused:UNUSED_PAD src0_sel:DWORD src1_sel:WORD_1
v_lshlrev_b32_sdwa v151,1,v151 dst_sel:DWORD dst_unused:UNUSED_PAD src0_sel:DWORD src1_sel:WORD_1
v_mfma_f32_16x16x32_bf16 v[172:175],v[92:95],v[136:139],v[172:175]
ds_read_b64_tr_b16 v[80:81],v1 offset:8192
ds_read_b64_tr_b16 v[82:83],v1 offset:10240
v_pk_fma_f16 v121,v120,v120,s54
v_pk_fma_f16 v123,v122,v122,s54
v_mfma_f32_16x16x32_bf16 v[180:183],v[96:99],v[136:139],v[180:183]
ds_read_u16 v148,v148
v_pk_mul_f16 v121,v120,v121
v_pk_mul_f16 v123,v122,v123
v_mfma_f32_16x16x32_bf16 v[188:191],v[100:103],v[136:139],v[188:191]
ds_read_b64_tr_b16 v[84:85],v2 offset:8192
v_lshlrev_b32_sdwa v152,1,v152 dst_sel:DWORD dst_unused:UNUSED_PAD src0_sel:DWORD src1_sel:WORD_1
v_lshlrev_b32_sdwa v154,1,v154 dst_sel:DWORD dst_unused:UNUSED_PAD src0_sel:DWORD src1_sel:WORD_1
v_mfma_f32_16x16x32_bf16 v[196:199],v[104:107],v[136:139],v[196:199]
ds_read_u16_d16_hi v150,v150
v_exp_f16 v120,v121
v_mfma_f32_16x16x32_bf16 v[204:207],v[108:111],v[136:139],v[204:207]
s_waitcnt lgkmcnt(15)
ds_read_b64_tr_b16 v[86:87],v2 offset:10240
v_exp_f16 v122,v123
v_mfma_f32_16x16x32_bf16 v[128:131],v[64:67],v[16:19],v[112:115]
ds_read_u16 v149,v149
v_lshlrev_b32_sdwa v153,1,v153 dst_sel:DWORD dst_unused:UNUSED_PAD src0_sel:DWORD src1_sel:WORD_1
v_lshlrev_b32_sdwa v155,1,v155 dst_sel:DWORD dst_unused:UNUSED_PAD src0_sel:DWORD src1_sel:WORD_1
v_mfma_f32_16x16x32_bf16 v[136:139],v[64:67],v[20:23],v[112:115]
s_waitcnt lgkmcnt(14)
v_exp_f16_sdwa v120,v121 dst_sel:WORD_1 dst_unused:UNUSED_PRESERVE src0_sel:WORD_1
v_mfma_f32_16x16x32_bf16 v[140:143],v[68:71],v[20:23],v[116:119]
ds_read_u16_d16_hi v151,v151
v_exp_f16_sdwa v122,v123 dst_sel:WORD_1 dst_unused:UNUSED_PRESERVE src0_sel:WORD_1
v_mfma_f32_16x16x32_bf16 v[132:135],v[68:71],v[16:19],v[116:119]
s_waitcnt lgkmcnt(12)
v_pk_fma_f16 v120,v120,s55,s55
v_pk_fma_f16 v122,v122,s55,s55
v_mfma_f32_16x16x32_bf16 v[128:131],v[72:75],v[24:27],v[128:131]
ds_read_u16 v152,v152
v_rcp_f16 v121,v120
v_mfma_f32_16x16x32_bf16 v[136:139],v[72:75],v[28:31],v[136:139]
s_waitcnt lgkmcnt(10)
v_rcp_f16 v123,v122
v_mfma_f32_16x16x32_bf16 v[140:143],v[76:79],v[28:31],v[140:143]
ds_read_u16_d16_hi v154,v154
v_rcp_f16_sdwa v121,v120 dst_sel:WORD_1 dst_unused:UNUSED_PRESERVE src0_sel:WORD_1
v_mfma_f32_16x16x32_bf16 v[132:135],v[76:79],v[24:27],v[132:135]
s_waitcnt lgkmcnt(8)
s_cbranch_vccnz RB55_%=
s_mov_b64 exec,s[62:63]
s_add_u32 m0,s57,6976
s_add_u32 s64,s56,4128768
buffer_load_dwordx4 v5,s[36:39],s64 offen lds
s_add_u32 m0,s57,8704
s_nop 0
buffer_load_dwordx4 v6,s[36:39],s64 offen lds
s_mov_b64 exec,-1
RB55_%=:
v_rcp_f16_sdwa v123,v122 dst_sel:WORD_1 dst_unused:UNUSED_PRESERVE src0_sel:WORD_1
v_mfma_f32_16x16x32_bf16 v[128:131],v[80:83],v[32:35],v[128:131]
ds_read_u16 v153,v153
v_fma_mix_f32 v156,v156,v121,0 op_sel_hi:[0,1,0]
v_fma_mix_f32 v158,v158,v121,0 op_sel:[0,1,0] op_sel_hi:[0,1,0]
v_mfma_f32_16x16x32_bf16 v[136:139],v[80:83],v[36:39],v[136:139]
s_waitcnt lgkmcnt(3)
v_lshl_add_u64 v[144:145],v[144:145],0,v[146:147]
v_lshl_add_u64 v[146:147],v[148:149],0,v[150:151]
v_mfma_f32_16x16x32_bf16 v[140:143],v[84:87],v[36:39],v[140:143]
ds_read_u16_d16_hi v155,v155
v_fma_mix_f32 v157,v157,v123,0 op_sel_hi:[0,1,0]
v_fma_mix_f32 v159,v159,v123,0 op_sel:[0,1,0] op_sel_hi:[0,1,0]
v_mfma_f32_16x16x32_bf16 v[132:135],v[84:87],v[32:35],v[132:135]
v_lshlrev_b32_sdwa v128,1,v128 dst_sel:DWORD dst_unused:UNUSED_PAD src0_sel:DWORD src1_sel:WORD_1
v_lshlrev_b32_sdwa v130,1,v130 dst_sel:DWORD dst_unused:UNUSED_PAD src0_sel:DWORD src1_sel:WORD_1
v_mfma_f32_16x16x32_bf16 v[208:211],v[88:91],v[144:147],v[208:211]
ds_read_u16 v128,v128
v_lshlrev_b32_sdwa v129,1,v129 dst_sel:DWORD dst_unused:UNUSED_PAD src0_sel:DWORD src1_sel:WORD_1
v_lshlrev_b32_sdwa v131,1,v131 dst_sel:DWORD dst_unused:UNUSED_PAD src0_sel:DWORD src1_sel:WORD_1
v_mfma_f32_16x16x32_bf16 v[216:219],v[92:95],v[144:147],v[216:219]
v_fma_mixlo_f16 v120,v140,s99,0
v_fma_mixlo_f16 v122,v141,s99,0
v_mfma_f32_16x16x32_bf16 v[224:227],v[96:99],v[144:147],v[224:227]
ds_read_u16_d16_hi v130,v130
v_lshlrev_b32_sdwa v132,1,v132 dst_sel:DWORD dst_unused:UNUSED_PAD src0_sel:DWORD src1_sel:WORD_1
v_lshlrev_b32_sdwa v134,1,v134 dst_sel:DWORD dst_unused:UNUSED_PAD src0_sel:DWORD src1_sel:WORD_1
v_mfma_f32_16x16x32_bf16 v[232:235],v[100:103],v[144:147],v[232:235]
s_waitcnt lgkmcnt(2)
v_lshl_add_u64 v[152:153],v[152:153],0,v[154:155]
v_mfma_f32_16x16x32_bf16 v[240:243],v[104:107],v[144:147],v[240:243]
ds_read_u16 v129,v129
v_cvt_pk_bf16_f32 v154,v156,v158
v_cvt_pk_bf16_f32 v155,v157,v159
v_mfma_f32_16x16x32_bf16 v[248:251],v[108:111],v[144:147],v[248:251]
v_fma_mixhi_f16 v120,v142,s99,0
v_fma_mixhi_f16 v122,v143,s99,0
v_mfma_f32_16x16x32_bf16 v[212:215],v[88:91],v[152:155],v[212:215]
ds_read_u16_d16_hi v131,v131
v_lshlrev_b32_sdwa v133,1,v133 dst_sel:DWORD dst_unused:UNUSED_PAD src0_sel:DWORD src1_sel:WORD_1
v_lshlrev_b32_sdwa v135,1,v135 dst_sel:DWORD dst_unused:UNUSED_PAD src0_sel:DWORD src1_sel:WORD_1
v_mfma_f32_16x16x32_bf16 v[220:223],v[92:95],v[152:155],v[220:223]
s_setprio 2
ds_read_b64_tr_b16 v[88:89],v3 offset:6160
v_pk_fma_f16 v121,v120,v120,s54
v_pk_fma_f16 v123,v122,v122,s54
v_mfma_f32_16x16x32_bf16 v[228:231],v[96:99],v[152:155],v[228:231]
ds_read_u16 v132,v132
v_pk_mul_f16 v121,v120,v121
v_pk_mul_f16 v123,v122,v123
v_mfma_f32_16x16x32_bf16 v[236:239],v[100:103],v[152:155],v[236:239]
ds_read_b64_tr_b16 v[90:91],v3 offset:7696
v_lshlrev_b32_sdwa v136,1,v136 dst_sel:DWORD dst_unused:UNUSED_PAD src0_sel:DWORD src1_sel:WORD_1
v_lshlrev_b32_sdwa v138,1,v138 dst_sel:DWORD dst_unused:UNUSED_PAD src0_sel:DWORD src1_sel:WORD_1
v_mfma_f32_16x16x32_bf16 v[244:247],v[104:107],v[152:155],v[244:247]
ds_read_u16_d16_hi v134,v134
v_exp_f16 v120,v121
v_mfma_f32_16x16x32_bf16 v[252:255],v[108:111],v[152:155],v[252:255]
ds_read_b64_tr_b16 v[92:93],v3 offset:6168
v_exp_f16 v122,v123
v_mfma_f32_16x16x32_bf16 v[144:147],v[64:67],v[40:43],v[112:115]
s_cbranch_vccnz RB56_%=
s_mov_b64 exec,s[62:63]
s_add_u32 m0,s57,10464
s_add_u32 s64,s56,4227072
buffer_load_dwordx4 v5,s[36:39],s64 offen lds
s_add_u32 m0,s57,12192
s_nop 0
buffer_load_dwordx4 v6,s[36:39],s64 offen lds
s_mov_b64 exec,-1
RB56_%=:
ds_read_u16 v133,v133
v_lshlrev_b32_sdwa v137,1,v137 dst_sel:DWORD dst_unused:UNUSED_PAD src0_sel:DWORD src1_sel:WORD_1
v_lshlrev_b32_sdwa v139,1,v139 dst_sel:DWORD dst_unused:UNUSED_PAD src0_sel:DWORD src1_sel:WORD_1
v_mfma_f32_16x16x32_bf16 v[152:155],v[64:67],v[44:47],v[112:115]
ds_read_b64_tr_b16 v[94:95],v3 offset:7704
v_exp_f16_sdwa v120,v121 dst_sel:WORD_1 dst_unused:UNUSED_PRESERVE src0_sel:WORD_1
v_mfma_f32_16x16x32_bf16 v[156:159],v[68:71],v[44:47],v[116:119]
ds_read_u16_d16_hi v135,v135
v_exp_f16_sdwa v122,v123 dst_sel:WORD_1 dst_unused:UNUSED_PRESERVE src0_sel:WORD_1
v_mfma_f32_16x16x32_bf16 v[148:151],v[68:71],v[40:43],v[116:119]
ds_read_b64_tr_b16 v[96:97],v3 offset:6224
v_pk_fma_f16 v120,v120,s55,s55
v_pk_fma_f16 v122,v122,s55,s55
v_mfma_f32_16x16x32_bf16 v[144:147],v[72:75],v[48:51],v[144:147]
ds_read_u16 v136,v136
v_rcp_f16 v121,v120
v_mfma_f32_16x16x32_bf16 v[152:155],v[72:75],v[52:55],v[152:155]
ds_read_b64_tr_b16 v[98:99],v3 offset:7760
ds_read_b64_tr_b16 v[100:101],v3 offset:6232
v_rcp_f16 v123,v122
v_mfma_f32_16x16x32_bf16 v[156:159],v[76:79],v[52:55],v[156:159]
ds_read_u16_d16_hi v138,v138
v_rcp_f16_sdwa v121,v120 dst_sel:WORD_1 dst_unused:UNUSED_PRESERVE src0_sel:WORD_1
v_mfma_f32_16x16x32_bf16 v[148:151],v[76:79],v[48:51],v[148:151]
ds_read_b64_tr_b16 v[102:103],v3 offset:7768
ds_read_b64_tr_b16 v[104:105],v3 offset:6288
v_rcp_f16_sdwa v123,v122 dst_sel:WORD_1 dst_unused:UNUSED_PRESERVE src0_sel:WORD_1
v_mfma_f32_16x16x32_bf16 v[144:147],v[80:83],v[56:59],v[144:147]
ds_read_u16 v137,v137
v_fma_mix_f32 v140,v140,v121,0 op_sel_hi:[0,1,0]
v_fma_mix_f32 v142,v142,v121,0 op_sel:[0,1,0] op_sel_hi:[0,1,0]
v_mfma_f32_16x16x32_bf16 v[152:155],v[80:83],v[60:63],v[152:155]
s_waitcnt lgkmcnt(8)
ds_read_b64_tr_b16 v[106:107],v3 offset:7824
ds_read_b64_tr_b16 v[108:109],v3 offset:6296
v_lshl_add_u64 v[128:129],v[128:129],0,v[130:131]
v_lshl_add_u64 v[130:131],v[132:133],0,v[134:135]
v_mfma_f32_16x16x32_bf16 v[156:159],v[84:87],v[60:63],v[156:159]
ds_read_u16_d16_hi v139,v139
v_fma_mix_f32 v141,v141,v123,0 op_sel_hi:[0,1,0]
v_fma_mix_f32 v143,v143,v123,0 op_sel:[0,1,0] op_sel_hi:[0,1,0]
v_mfma_f32_16x16x32_bf16 v[148:151],v[84:87],v[56:59],v[148:151]
ds_read_b64_tr_b16 v[110:111],v3 offset:7832
ds_read_b64_tr_b16 v[64:65],v1 offset:512
v_lshlrev_b32_sdwa v144,1,v144 dst_sel:DWORD dst_unused:UNUSED_PAD src0_sel:DWORD src1_sel:WORD_1
v_lshlrev_b32_sdwa v146,1,v146 dst_sel:DWORD dst_unused:UNUSED_PAD src0_sel:DWORD src1_sel:WORD_1
v_mfma_f32_16x16x32_bf16 v[160:163],v[88:91],v[128:131],v[160:163]
ds_read_u16 v144,v144
v_lshlrev_b32_sdwa v145,1,v145 dst_sel:DWORD dst_unused:UNUSED_PAD src0_sel:DWORD src1_sel:WORD_1
v_lshlrev_b32_sdwa v147,1,v147 dst_sel:DWORD dst_unused:UNUSED_PAD src0_sel:DWORD src1_sel:WORD_1
v_mfma_f32_16x16x32_bf16 v[168:171],v[92:95],v[128:131],v[168:171]
s_waitcnt lgkmcnt(11)
ds_read_b64_tr_b16 v[66:67],v1 offset:2560
ds_read_b128 v[112:115],v4 offset:1408
v_fma_mixlo_f16 v120,v156,s99,0
v_fma_mixlo_f16 v122,v157,s99,0
v_mfma_f32_16x16x32_bf16 v[176:179],v[96:99],v[128:131],v[176:179]
s_waitcnt lgkmcnt(10)
ds_read_u16_d16_hi v146,v146
v_lshlrev_b32_sdwa v148,1,v148 dst_sel:DWORD dst_unused:UNUSED_PAD src0_sel:DWORD src1_sel:WORD_1
v_lshlrev_b32_sdwa v150,1,v150 dst_sel:DWORD dst_unused:UNUSED_PAD src0_sel:DWORD src1_sel:WORD_1
v_mfma_f32_16x16x32_bf16 v[184:187],v[100:103],v[128:131],v[184:187]
s_waitcnt lgkmcnt(6)
ds_read_b64_tr_b16 v[68:69],v2 offset:512
ds_read_b64_tr_b16 v[70:71],v2 offset:2560
v_lshl_add_u64 v[136:137],v[136:137],0,v[138:139]
v_mfma_f32_16x16x32_bf16 v[192:195],v[104:107],v[128:131],v[192:195]
s_waitcnt lgkmcnt(7)
ds_read_u16 v145,v145
v_cvt_pk_bf16_f32 v138,v140,v142
v_cvt_pk_bf16_f32 v139,v141,v143
v_mfma_f32_16x16x32_bf16 v[200:203],v[108:111],v[128:131],v[200:203]
ds_read_b128 v[116:119],v4 offset:1440
ds_read_b64_tr_b16 v[72:73],v1 offset:4608
v_fma_mixhi_f16 v120,v158,s99,0
v_fma_mixhi_f16 v122,v159,s99,0
v_mfma_f32_16x16x32_bf16 v[164:167],v[88:91],v[136:139],v[164:167]
s_cbranch_vccnz RB57_%=
s_mov_b64 exec,s[62:63]
s_add_u32 m0,s57,13952
s_add_u32 s64,s56,4325376
buffer_load_dwordx4 v5,s[36:39],s64 offen lds
s_add_u32 m0,s57,15680
s_nop 0
buffer_load_dwordx4 v6,s[36:39],s64 offen lds
s_mov_b64 exec,-1
RB57_%=:
ds_read_u16_d16_hi v147,v147
v_lshlrev_b32_sdwa v149,1,v149 dst_sel:DWORD dst_unused:UNUSED_PAD src0_sel:DWORD src1_sel:WORD_1
v_lshlrev_b32_sdwa v151,1,v151 dst_sel:DWORD dst_unused:UNUSED_PAD src0_sel:DWORD src1_sel:WORD_1
v_mfma_f32_16x16x32_bf16 v[172:175],v[92:95],v[136:139],v[172:175]
ds_read_b64_tr_b16 v[74:75],v1 offset:6656
ds_read_b64_tr_b16 v[76:77],v2 offset:4608
v_pk_fma_f16 v121,v120,v120,s54
v_pk_fma_f16 v123,v122,v122,s54
v_mfma_f32_16x16x32_bf16 v[180:183],v[96:99],v[136:139],v[180:183]
s_setprio 1
ds_read_u16 v148,v148
v_pk_mul_f16 v121,v120,v121
v_pk_mul_f16 v123,v122,v123
v_mfma_f32_16x16x32_bf16 v[188:191],v[100:103],v[136:139],v[188:191]
ds_read_b64_tr_b16 v[78:79],v2 offset:6656
v_lshlrev_b32_sdwa v152,1,v152 dst_sel:DWORD dst_unused:UNUSED_PAD src0_sel:DWORD src1_sel:WORD_1
v_lshlrev_b32_sdwa v154,1,v154 dst_sel:DWORD dst_unused:UNUSED_PAD src0_sel:DWORD src1_sel:WORD_1
v_mfma_f32_16x16x32_bf16 v[196:199],v[104:107],v[136:139],v[196:199]
ds_read_u16_d16_hi v150,v150
v_exp_f16 v120,v121
v_mfma_f32_16x16x32_bf16 v[204:207],v[108:111],v[136:139],v[204:207]
s_waitcnt lgkmcnt(12)
ds_read_b64_tr_b16 v[80:81],v1 offset:8704
v_exp_f16 v122,v123
v_mfma_f32_16x16x32_bf16 v[128:131],v[64:67],v[16:19],v[112:115]
ds_read_u16 v149,v149
v_lshlrev_b32_sdwa v153,1,v153 dst_sel:DWORD dst_unused:UNUSED_PAD src0_sel:DWORD src1_sel:WORD_1
v_lshlrev_b32_sdwa v155,1,v155 dst_sel:DWORD dst_unused:UNUSED_PAD src0_sel:DWORD src1_sel:WORD_1
v_mfma_f32_16x16x32_bf16 v[136:139],v[64:67],v[20:23],v[112:115]
s_waitcnt lgkmcnt(9)
ds_read_b64_tr_b16 v[82:83],v1 offset:10752
v_exp_f16_sdwa v120,v121 dst_sel:WORD_1 dst_unused:UNUSED_PRESERVE src0_sel:WORD_1
v_mfma_f32_16x16x32_bf16 v[140:143],v[68:71],v[20:23],v[116:119]
ds_read_u16_d16_hi v151,v151
v_exp_f16_sdwa v122,v123 dst_sel:WORD_1 dst_unused:UNUSED_PRESERVE src0_sel:WORD_1
v_mfma_f32_16x16x32_bf16 v[132:135],v[68:71],v[16:19],v[116:119]
s_waitcnt lgkmcnt(8)
ds_read_b64_tr_b16 v[84:85],v2 offset:8704
v_pk_fma_f16 v120,v120,s55,s55
v_pk_fma_f16 v122,v122,s55,s55
v_mfma_f32_16x16x32_bf16 v[128:131],v[72:75],v[24:27],v[128:131]
ds_read_u16 v152,v152
v_rcp_f16 v121,v120
v_mfma_f32_16x16x32_bf16 v[136:139],v[72:75],v[28:31],v[136:139]
s_waitcnt lgkmcnt(7)
ds_read_b64_tr_b16 v[86:87],v2 offset:10752
v_rcp_f16 v123,v122
v_mfma_f32_16x16x32_bf16 v[140:143],v[76:79],v[28:31],v[140:143]
ds_read_u16_d16_hi v154,v154
v_rcp_f16_sdwa v121,v120 dst_sel:WORD_1 dst_unused:UNUSED_PRESERVE src0_sel:WORD_1
v_mfma_f32_16x16x32_bf16 v[132:135],v[76:79],v[24:27],v[132:135]
s_waitcnt lgkmcnt(5)
v_rcp_f16_sdwa v123,v122 dst_sel:WORD_1 dst_unused:UNUSED_PRESERVE src0_sel:WORD_1
v_mfma_f32_16x16x32_bf16 v[128:131],v[80:83],v[32:35],v[128:131]
ds_read_u16 v153,v153
v_fma_mix_f32 v156,v156,v121,0 op_sel_hi:[0,1,0]
v_fma_mix_f32 v158,v158,v121,0 op_sel:[0,1,0] op_sel_hi:[0,1,0]
v_mfma_f32_16x16x32_bf16 v[136:139],v[80:83],v[36:39],v[136:139]
s_waitcnt lgkmcnt(2)
s_setprio 0
v_lshl_add_u64 v[144:145],v[144:145],0,v[146:147]
v_lshl_add_u64 v[146:147],v[148:149],0,v[150:151]
v_mfma_f32_16x16x32_bf16 v[140:143],v[84:87],v[36:39],v[140:143]
ds_read_u16_d16_hi v155,v155
v_fma_mix_f32 v157,v157,v123,0 op_sel_hi:[0,1,0]
v_fma_mix_f32 v159,v159,v123,0 op_sel:[0,1,0] op_sel_hi:[0,1,0]
v_mfma_f32_16x16x32_bf16 v[132:135],v[84:87],v[32:35],v[132:135]
s_cbranch_vccnz RB58_%=
s_mov_b64 exec,s[62:63]
s_add_u32 m0,s57,17440
s_add_u32 s64,s56,4423680
buffer_load_dwordx4 v5,s[36:39],s64 offen lds
s_add_u32 m0,s57,19168
s_nop 0
buffer_load_dwordx4 v6,s[36:39],s64 offen lds
s_mov_b64 exec,-1
RB58_%=:
v_lshlrev_b32_sdwa v128,1,v128 dst_sel:DWORD dst_unused:UNUSED_PAD src0_sel:DWORD src1_sel:WORD_1
v_lshlrev_b32_sdwa v130,1,v130 dst_sel:DWORD dst_unused:UNUSED_PAD src0_sel:DWORD src1_sel:WORD_1
v_mfma_f32_16x16x32_bf16 v[208:211],v[88:91],v[144:147],v[208:211]
s_barrier
s_setprio 3
ds_read_u16 v128,v128
v_lshlrev_b32_sdwa v129,1,v129 dst_sel:DWORD dst_unused:UNUSED_PAD src0_sel:DWORD src1_sel:WORD_1
v_lshlrev_b32_sdwa v131,1,v131 dst_sel:DWORD dst_unused:UNUSED_PAD src0_sel:DWORD src1_sel:WORD_1
v_mfma_f32_16x16x32_bf16 v[216:219],v[92:95],v[144:147],v[216:219]
v_fma_mixlo_f16 v120,v140,s99,0
v_fma_mixlo_f16 v122,v141,s99,0
v_mfma_f32_16x16x32_bf16 v[224:227],v[96:99],v[144:147],v[224:227]
ds_read_u16_d16_hi v130,v130
v_lshlrev_b32_sdwa v132,1,v132 dst_sel:DWORD dst_unused:UNUSED_PAD src0_sel:DWORD src1_sel:WORD_1
v_lshlrev_b32_sdwa v134,1,v134 dst_sel:DWORD dst_unused:UNUSED_PAD src0_sel:DWORD src1_sel:WORD_1
v_mfma_f32_16x16x32_bf16 v[232:235],v[100:103],v[144:147],v[232:235]
s_waitcnt lgkmcnt(2)
v_lshl_add_u64 v[152:153],v[152:153],0,v[154:155]
v_mfma_f32_16x16x32_bf16 v[240:243],v[104:107],v[144:147],v[240:243]
ds_read_u16 v129,v129
v_cvt_pk_bf16_f32 v154,v156,v158
v_cvt_pk_bf16_f32 v155,v157,v159
v_mfma_f32_16x16x32_bf16 v[248:251],v[108:111],v[144:147],v[248:251]
v_fma_mixhi_f16 v120,v142,s99,0
v_fma_mixhi_f16 v122,v143,s99,0
v_mfma_f32_16x16x32_bf16 v[212:215],v[88:91],v[152:155],v[212:215]
ds_read_u16_d16_hi v131,v131
v_lshlrev_b32_sdwa v133,1,v133 dst_sel:DWORD dst_unused:UNUSED_PAD src0_sel:DWORD src1_sel:WORD_1
v_lshlrev_b32_sdwa v135,1,v135 dst_sel:DWORD dst_unused:UNUSED_PAD src0_sel:DWORD src1_sel:WORD_1
v_mfma_f32_16x16x32_bf16 v[220:223],v[92:95],v[152:155],v[220:223]
ds_read_b64_tr_b16 v[88:89],v3 offset:12320
v_pk_fma_f16 v121,v120,v120,s54
v_pk_fma_f16 v123,v122,v122,s54
v_mfma_f32_16x16x32_bf16 v[228:231],v[96:99],v[152:155],v[228:231]
ds_read_u16 v132,v132
v_pk_mul_f16 v121,v120,v121
v_pk_mul_f16 v123,v122,v123
v_mfma_f32_16x16x32_bf16 v[236:239],v[100:103],v[152:155],v[236:239]
ds_read_b64_tr_b16 v[90:91],v3 offset:13856
v_lshlrev_b32_sdwa v136,1,v136 dst_sel:DWORD dst_unused:UNUSED_PAD src0_sel:DWORD src1_sel:WORD_1
v_lshlrev_b32_sdwa v138,1,v138 dst_sel:DWORD dst_unused:UNUSED_PAD src0_sel:DWORD src1_sel:WORD_1
v_mfma_f32_16x16x32_bf16 v[244:247],v[104:107],v[152:155],v[244:247]
ds_read_u16_d16_hi v134,v134
v_exp_f16 v120,v121
v_mfma_f32_16x16x32_bf16 v[252:255],v[108:111],v[152:155],v[252:255]
ds_read_b64_tr_b16 v[92:93],v3 offset:12328
v_exp_f16 v122,v123
v_mfma_f32_16x16x32_bf16 v[144:147],v[64:67],v[40:43],v[112:115]
ds_read_u16 v133,v133
v_lshlrev_b32_sdwa v137,1,v137 dst_sel:DWORD dst_unused:UNUSED_PAD src0_sel:DWORD src1_sel:WORD_1
v_lshlrev_b32_sdwa v139,1,v139 dst_sel:DWORD dst_unused:UNUSED_PAD src0_sel:DWORD src1_sel:WORD_1
v_mfma_f32_16x16x32_bf16 v[152:155],v[64:67],v[44:47],v[112:115]
ds_read_b64_tr_b16 v[94:95],v3 offset:13864
v_exp_f16_sdwa v120,v121 dst_sel:WORD_1 dst_unused:UNUSED_PRESERVE src0_sel:WORD_1
v_mfma_f32_16x16x32_bf16 v[156:159],v[68:71],v[44:47],v[116:119]
ds_read_u16_d16_hi v135,v135
v_exp_f16_sdwa v122,v123 dst_sel:WORD_1 dst_unused:UNUSED_PRESERVE src0_sel:WORD_1
v_mfma_f32_16x16x32_bf16 v[148:151],v[68:71],v[40:43],v[116:119]
ds_read_b64_tr_b16 v[96:97],v3 offset:12384
v_pk_fma_f16 v120,v120,s55,s55
v_pk_fma_f16 v122,v122,s55,s55
v_mfma_f32_16x16x32_bf16 v[144:147],v[72:75],v[48:51],v[144:147]
s_cbranch_vccnz RB59_%=
s_mov_b64 exec,s[62:63]
s_add_u32 m0,s57,20928
s_add_u32 s64,s56,4521984
buffer_load_dwordx4 v5,s[36:39],s64 offen lds
s_add_u32 m0,s57,22656
s_nop 0
buffer_load_dwordx4 v6,s[36:39],s64 offen lds
s_mov_b64 exec,-1
RB59_%=:
ds_read_u16 v136,v136
v_rcp_f16 v121,v120
v_mfma_f32_16x16x32_bf16 v[152:155],v[72:75],v[52:55],v[152:155]
ds_read_b64_tr_b16 v[98:99],v3 offset:13920
ds_read_b64_tr_b16 v[100:101],v3 offset:12392
v_rcp_f16 v123,v122
v_mfma_f32_16x16x32_bf16 v[156:159],v[76:79],v[52:55],v[156:159]
ds_read_u16_d16_hi v138,v138
v_rcp_f16_sdwa v121,v120 dst_sel:WORD_1 dst_unused:UNUSED_PRESERVE src0_sel:WORD_1
v_mfma_f32_16x16x32_bf16 v[148:151],v[76:79],v[48:51],v[148:151]
ds_read_b64_tr_b16 v[102:103],v3 offset:13928
ds_read_b64_tr_b16 v[104:105],v3 offset:12448
v_rcp_f16_sdwa v123,v122 dst_sel:WORD_1 dst_unused:UNUSED_PRESERVE src0_sel:WORD_1
v_mfma_f32_16x16x32_bf16 v[144:147],v[80:83],v[56:59],v[144:147]
ds_read_u16 v137,v137
v_fma_mix_f32 v140,v140,v121,0 op_sel_hi:[0,1,0]
v_fma_mix_f32 v142,v142,v121,0 op_sel:[0,1,0] op_sel_hi:[0,1,0]
v_mfma_f32_16x16x32_bf16 v[152:155],v[80:83],v[60:63],v[152:155]
s_waitcnt lgkmcnt(8)
ds_read_b64_tr_b16 v[106:107],v3 offset:13984
ds_read_b64_tr_b16 v[108:109],v3 offset:12456
v_lshl_add_u64 v[128:129],v[128:129],0,v[130:131]
v_lshl_add_u64 v[130:131],v[132:133],0,v[134:135]
v_mfma_f32_16x16x32_bf16 v[156:159],v[84:87],v[60:63],v[156:159]
ds_read_u16_d16_hi v139,v139
v_fma_mix_f32 v141,v141,v123,0 op_sel_hi:[0,1,0]
v_fma_mix_f32 v143,v143,v123,0 op_sel:[0,1,0] op_sel_hi:[0,1,0]
v_mfma_f32_16x16x32_bf16 v[148:151],v[84:87],v[56:59],v[148:151]
ds_read_b64_tr_b16 v[110:111],v3 offset:13992
ds_read_b64_tr_b16 v[64:65],v1 offset:12320
v_lshlrev_b32_sdwa v144,1,v144 dst_sel:DWORD dst_unused:UNUSED_PAD src0_sel:DWORD src1_sel:WORD_1
v_lshlrev_b32_sdwa v146,1,v146 dst_sel:DWORD dst_unused:UNUSED_PAD src0_sel:DWORD src1_sel:WORD_1
v_mfma_f32_16x16x32_bf16 v[160:163],v[88:91],v[128:131],v[160:163]
ds_read_u16 v144,v144
v_lshlrev_b32_sdwa v145,1,v145 dst_sel:DWORD dst_unused:UNUSED_PAD src0_sel:DWORD src1_sel:WORD_1
v_lshlrev_b32_sdwa v147,1,v147 dst_sel:DWORD dst_unused:UNUSED_PAD src0_sel:DWORD src1_sel:WORD_1
v_mfma_f32_16x16x32_bf16 v[168:171],v[92:95],v[128:131],v[168:171]
s_waitcnt lgkmcnt(11)
ds_read_b64_tr_b16 v[66:67],v1 offset:14368
ds_read_b128 v[112:115],v4 offset:0
v_fma_mixlo_f16 v120,v156,s99,0
v_fma_mixlo_f16 v122,v157,s99,0
v_mfma_f32_16x16x32_bf16 v[176:179],v[96:99],v[128:131],v[176:179]
s_waitcnt lgkmcnt(10)
ds_read_u16_d16_hi v146,v146
v_lshlrev_b32_sdwa v148,1,v148 dst_sel:DWORD dst_unused:UNUSED_PAD src0_sel:DWORD src1_sel:WORD_1
v_lshlrev_b32_sdwa v150,1,v150 dst_sel:DWORD dst_unused:UNUSED_PAD src0_sel:DWORD src1_sel:WORD_1
v_mfma_f32_16x16x32_bf16 v[184:187],v[100:103],v[128:131],v[184:187]
s_waitcnt lgkmcnt(6)
ds_read_b64_tr_b16 v[68:69],v2 offset:12320
ds_read_b64_tr_b16 v[70:71],v2 offset:14368
v_lshl_add_u64 v[136:137],v[136:137],0,v[138:139]
v_mfma_f32_16x16x32_bf16 v[192:195],v[104:107],v[128:131],v[192:195]
s_waitcnt lgkmcnt(7)
ds_read_u16 v145,v145
v_cvt_pk_bf16_f32 v138,v140,v142
v_cvt_pk_bf16_f32 v139,v141,v143
v_mfma_f32_16x16x32_bf16 v[200:203],v[108:111],v[128:131],v[200:203]
ds_read_b128 v[116:119],v4 offset:32
ds_read_b64_tr_b16 v[72:73],v1 offset:16416
v_fma_mixhi_f16 v120,v158,s99,0
v_fma_mixhi_f16 v122,v159,s99,0
v_mfma_f32_16x16x32_bf16 v[164:167],v[88:91],v[136:139],v[164:167]
ds_read_u16_d16_hi v147,v147
v_lshlrev_b32_sdwa v149,1,v149 dst_sel:DWORD dst_unused:UNUSED_PAD src0_sel:DWORD src1_sel:WORD_1
v_lshlrev_b32_sdwa v151,1,v151 dst_sel:DWORD dst_unused:UNUSED_PAD src0_sel:DWORD src1_sel:WORD_1
v_mfma_f32_16x16x32_bf16 v[172:175],v[92:95],v[136:139],v[172:175]
ds_read_b64_tr_b16 v[74:75],v1 offset:18464
ds_read_b64_tr_b16 v[76:77],v2 offset:16416
v_pk_fma_f16 v121,v120,v120,s54
v_pk_fma_f16 v123,v122,v122,s54
v_mfma_f32_16x16x32_bf16 v[180:183],v[96:99],v[136:139],v[180:183]
ds_read_u16 v148,v148
v_pk_mul_f16 v121,v120,v121
v_pk_mul_f16 v123,v122,v123
v_mfma_f32_16x16x32_bf16 v[188:191],v[100:103],v[136:139],v[188:191]
ds_read_b64_tr_b16 v[78:79],v2 offset:18464
v_lshlrev_b32_sdwa v152,1,v152 dst_sel:DWORD dst_unused:UNUSED_PAD src0_sel:DWORD src1_sel:WORD_1
v_lshlrev_b32_sdwa v154,1,v154 dst_sel:DWORD dst_unused:UNUSED_PAD src0_sel:DWORD src1_sel:WORD_1
v_mfma_f32_16x16x32_bf16 v[196:199],v[104:107],v[136:139],v[196:199]
s_cbranch_vccnz RB60_%=
s_mov_b64 exec,s[62:63]
s_add_u32 m0,s57,24416
s_add_u32 s64,s56,4620288
buffer_load_dwordx4 v5,s[36:39],s64 offen lds
s_add_u32 m0,s57,26144
s_nop 0
buffer_load_dwordx4 v6,s[36:39],s64 offen lds
s_mov_b64 exec,-1
RB60_%=:
ds_read_u16_d16_hi v150,v150
v_exp_f16 v120,v121
v_mfma_f32_16x16x32_bf16 v[204:207],v[108:111],v[136:139],v[204:207]
s_waitcnt lgkmcnt(12)
ds_read_b64_tr_b16 v[80:81],v1 offset:20512
v_exp_f16 v122,v123
v_mfma_f32_16x16x32_bf16 v[128:131],v[64:67],v[16:19],v[112:115]
ds_read_u16 v149,v149
v_lshlrev_b32_sdwa v153,1,v153 dst_sel:DWORD dst_unused:UNUSED_PAD src0_sel:DWORD src1_sel:WORD_1
v_lshlrev_b32_sdwa v155,1,v155 dst_sel:DWORD dst_unused:UNUSED_PAD src0_sel:DWORD src1_sel:WORD_1
v_mfma_f32_16x16x32_bf16 v[136:139],v[64:67],v[20:23],v[112:115]
s_waitcnt lgkmcnt(9)
ds_read_b64_tr_b16 v[82:83],v1 offset:22560
v_exp_f16_sdwa v120,v121 dst_sel:WORD_1 dst_unused:UNUSED_PRESERVE src0_sel:WORD_1
v_mfma_f32_16x16x32_bf16 v[140:143],v[68:71],v[20:23],v[116:119]
ds_read_u16_d16_hi v151,v151
v_exp_f16_sdwa v122,v123 dst_sel:WORD_1 dst_unused:UNUSED_PRESERVE src0_sel:WORD_1
v_mfma_f32_16x16x32_bf16 v[132:135],v[68:71],v[16:19],v[116:119]
s_waitcnt lgkmcnt(8)
ds_read_b64_tr_b16 v[84:85],v2 offset:20512
v_pk_fma_f16 v120,v120,s55,s55
v_pk_fma_f16 v122,v122,s55,s55
v_mfma_f32_16x16x32_bf16 v[128:131],v[72:75],v[24:27],v[128:131]
ds_read_u16 v152,v152
v_rcp_f16 v121,v120
v_mfma_f32_16x16x32_bf16 v[136:139],v[72:75],v[28:31],v[136:139]
s_waitcnt lgkmcnt(7)
ds_read_b64_tr_b16 v[86:87],v2 offset:22560
v_rcp_f16 v123,v122
v_mfma_f32_16x16x32_bf16 v[140:143],v[76:79],v[28:31],v[140:143]
ds_read_u16_d16_hi v154,v154
v_rcp_f16_sdwa v121,v120 dst_sel:WORD_1 dst_unused:UNUSED_PRESERVE src0_sel:WORD_1
v_mfma_f32_16x16x32_bf16 v[132:135],v[76:79],v[24:27],v[132:135]
s_waitcnt lgkmcnt(5)
v_rcp_f16_sdwa v123,v122 dst_sel:WORD_1 dst_unused:UNUSED_PRESERVE src0_sel:WORD_1
v_mfma_f32_16x16x32_bf16 v[128:131],v[80:83],v[32:35],v[128:131]
ds_read_u16 v153,v153
v_fma_mix_f32 v156,v156,v121,0 op_sel_hi:[0,1,0]
v_fma_mix_f32 v158,v158,v121,0 op_sel:[0,1,0] op_sel_hi:[0,1,0]
v_mfma_f32_16x16x32_bf16 v[136:139],v[80:83],v[36:39],v[136:139]
s_waitcnt lgkmcnt(2)
v_lshl_add_u64 v[144:145],v[144:145],0,v[146:147]
v_lshl_add_u64 v[146:147],v[148:149],0,v[150:151]
v_mfma_f32_16x16x32_bf16 v[140:143],v[84:87],v[36:39],v[140:143]
ds_read_u16_d16_hi v155,v155
v_fma_mix_f32 v157,v157,v123,0 op_sel_hi:[0,1,0]
v_fma_mix_f32 v159,v159,v123,0 op_sel:[0,1,0] op_sel_hi:[0,1,0]
v_mfma_f32_16x16x32_bf16 v[132:135],v[84:87],v[32:35],v[132:135]
v_lshlrev_b32_sdwa v128,1,v128 dst_sel:DWORD dst_unused:UNUSED_PAD src0_sel:DWORD src1_sel:WORD_1
v_lshlrev_b32_sdwa v130,1,v130 dst_sel:DWORD dst_unused:UNUSED_PAD src0_sel:DWORD src1_sel:WORD_1
v_mfma_f32_16x16x32_bf16 v[208:211],v[88:91],v[144:147],v[208:211]
ds_read_u16 v128,v128
v_lshlrev_b32_sdwa v129,1,v129 dst_sel:DWORD dst_unused:UNUSED_PAD src0_sel:DWORD src1_sel:WORD_1
v_lshlrev_b32_sdwa v131,1,v131 dst_sel:DWORD dst_unused:UNUSED_PAD src0_sel:DWORD src1_sel:WORD_1
v_mfma_f32_16x16x32_bf16 v[216:219],v[92:95],v[144:147],v[216:219]
v_fma_mixlo_f16 v120,v140,s99,0
v_fma_mixlo_f16 v122,v141,s99,0
v_mfma_f32_16x16x32_bf16 v[224:227],v[96:99],v[144:147],v[224:227]
ds_read_u16_d16_hi v130,v130
v_lshlrev_b32_sdwa v132,1,v132 dst_sel:DWORD dst_unused:UNUSED_PAD src0_sel:DWORD src1_sel:WORD_1
v_lshlrev_b32_sdwa v134,1,v134 dst_sel:DWORD dst_unused:UNUSED_PAD src0_sel:DWORD src1_sel:WORD_1
v_mfma_f32_16x16x32_bf16 v[232:235],v[100:103],v[144:147],v[232:235]
s_waitcnt lgkmcnt(2)
s_cbranch_vccnz RB61_%=
s_mov_b64 exec,s[62:63]
s_add_u32 m0,s57,27904
s_add_u32 s64,s56,4718592
buffer_load_dwordx4 v5,s[36:39],s64 offen lds
s_add_u32 m0,s57,29632
s_nop 0
buffer_load_dwordx4 v6,s[36:39],s64 offen lds
s_mov_b64 exec,-1
RB61_%=:
v_lshl_add_u64 v[152:153],v[152:153],0,v[154:155]
v_mfma_f32_16x16x32_bf16 v[240:243],v[104:107],v[144:147],v[240:243]
ds_read_u16 v129,v129
v_cvt_pk_bf16_f32 v154,v156,v158
v_cvt_pk_bf16_f32 v155,v157,v159
v_mfma_f32_16x16x32_bf16 v[248:251],v[108:111],v[144:147],v[248:251]
s_setprio 2
v_fma_mixhi_f16 v120,v142,s99,0
v_fma_mixhi_f16 v122,v143,s99,0
v_mfma_f32_16x16x32_bf16 v[212:215],v[88:91],v[152:155],v[212:215]
ds_read_u16_d16_hi v131,v131
v_lshlrev_b32_sdwa v133,1,v133 dst_sel:DWORD dst_unused:UNUSED_PAD src0_sel:DWORD src1_sel:WORD_1
v_lshlrev_b32_sdwa v135,1,v135 dst_sel:DWORD dst_unused:UNUSED_PAD src0_sel:DWORD src1_sel:WORD_1
v_mfma_f32_16x16x32_bf16 v[220:223],v[92:95],v[152:155],v[220:223]
ds_read_b64_tr_b16 v[88:89],v3 offset:18480
v_pk_fma_f16 v121,v120,v120,s54
v_pk_fma_f16 v123,v122,v122,s54
v_mfma_f32_16x16x32_bf16 v[228:231],v[96:99],v[152:155],v[228:231]
ds_read_u16 v132,v132
v_pk_mul_f16 v121,v120,v121
v_pk_mul_f16 v123,v122,v123
v_mfma_f32_16x16x32_bf16 v[236:239],v[100:103],v[152:155],v[236:239]
ds_read_b64_tr_b16 v[90:91],v3 offset:20016
v_lshlrev_b32_sdwa v136,1,v136 dst_sel:DWORD dst_unused:UNUSED_PAD src0_sel:DWORD src1_sel:WORD_1
v_lshlrev_b32_sdwa v138,1,v138 dst_sel:DWORD dst_unused:UNUSED_PAD src0_sel:DWORD src1_sel:WORD_1
v_mfma_f32_16x16x32_bf16 v[244:247],v[104:107],v[152:155],v[244:247]
ds_read_u16_d16_hi v134,v134
v_exp_f16 v120,v121
v_mfma_f32_16x16x32_bf16 v[252:255],v[108:111],v[152:155],v[252:255]
ds_read_b64_tr_b16 v[92:93],v3 offset:18488
v_exp_f16 v122,v123
v_mfma_f32_16x16x32_bf16 v[144:147],v[64:67],v[40:43],v[112:115]
ds_read_u16 v133,v133
v_lshlrev_b32_sdwa v137,1,v137 dst_sel:DWORD dst_unused:UNUSED_PAD src0_sel:DWORD src1_sel:WORD_1
v_lshlrev_b32_sdwa v139,1,v139 dst_sel:DWORD dst_unused:UNUSED_PAD src0_sel:DWORD src1_sel:WORD_1
v_mfma_f32_16x16x32_bf16 v[152:155],v[64:67],v[44:47],v[112:115]
ds_read_b64_tr_b16 v[94:95],v3 offset:20024
v_exp_f16_sdwa v120,v121 dst_sel:WORD_1 dst_unused:UNUSED_PRESERVE src0_sel:WORD_1
v_mfma_f32_16x16x32_bf16 v[156:159],v[68:71],v[44:47],v[116:119]
ds_read_u16_d16_hi v135,v135
v_exp_f16_sdwa v122,v123 dst_sel:WORD_1 dst_unused:UNUSED_PRESERVE src0_sel:WORD_1
v_mfma_f32_16x16x32_bf16 v[148:151],v[68:71],v[40:43],v[116:119]
ds_read_b64_tr_b16 v[96:97],v3 offset:18544
v_pk_fma_f16 v120,v120,s55,s55
v_pk_fma_f16 v122,v122,s55,s55
v_mfma_f32_16x16x32_bf16 v[144:147],v[72:75],v[48:51],v[144:147]
ds_read_u16 v136,v136
v_rcp_f16 v121,v120
v_mfma_f32_16x16x32_bf16 v[152:155],v[72:75],v[52:55],v[152:155]
ds_read_b64_tr_b16 v[98:99],v3 offset:20080
ds_read_b64_tr_b16 v[100:101],v3 offset:18552
v_rcp_f16 v123,v122
v_mfma_f32_16x16x32_bf16 v[156:159],v[76:79],v[52:55],v[156:159]
ds_read_u16_d16_hi v138,v138
v_rcp_f16_sdwa v121,v120 dst_sel:WORD_1 dst_unused:UNUSED_PRESERVE src0_sel:WORD_1
v_mfma_f32_16x16x32_bf16 v[148:151],v[76:79],v[48:51],v[148:151]
ds_read_b64_tr_b16 v[102:103],v3 offset:20088
ds_read_b64_tr_b16 v[104:105],v3 offset:18608
v_rcp_f16_sdwa v123,v122 dst_sel:WORD_1 dst_unused:UNUSED_PRESERVE src0_sel:WORD_1
v_mfma_f32_16x16x32_bf16 v[144:147],v[80:83],v[56:59],v[144:147]
ds_read_u16 v137,v137
v_fma_mix_f32 v140,v140,v121,0 op_sel_hi:[0,1,0]
v_fma_mix_f32 v142,v142,v121,0 op_sel:[0,1,0] op_sel_hi:[0,1,0]
v_mfma_f32_16x16x32_bf16 v[152:155],v[80:83],v[60:63],v[152:155]
s_waitcnt lgkmcnt(8)
s_cbranch_vccnz RB62_%=
s_mov_b64 exec,s[62:63]
s_add_u32 m0,s57,31392
s_add_u32 s64,s56,4816896
buffer_load_dwordx4 v5,s[36:39],s64 offen lds
s_add_u32 m0,s57,33120
s_nop 0
buffer_load_dwordx4 v6,s[36:39],s64 offen lds
s_mov_b64 exec,-1
RB62_%=:
ds_read_b64_tr_b16 v[106:107],v3 offset:20144
ds_read_b64_tr_b16 v[108:109],v3 offset:18616
v_lshl_add_u64 v[128:129],v[128:129],0,v[130:131]
v_lshl_add_u64 v[130:131],v[132:133],0,v[134:135]
v_mfma_f32_16x16x32_bf16 v[156:159],v[84:87],v[60:63],v[156:159]
ds_read_u16_d16_hi v139,v139
v_fma_mix_f32 v141,v141,v123,0 op_sel_hi:[0,1,0]
v_fma_mix_f32 v143,v143,v123,0 op_sel:[0,1,0] op_sel_hi:[0,1,0]
v_mfma_f32_16x16x32_bf16 v[148:151],v[84:87],v[56:59],v[148:151]
ds_read_b64_tr_b16 v[110:111],v3 offset:20152
ds_read_b64_tr_b16 v[64:65],v1 offset:12832
v_lshlrev_b32_sdwa v144,1,v144 dst_sel:DWORD dst_unused:UNUSED_PAD src0_sel:DWORD src1_sel:WORD_1
v_lshlrev_b32_sdwa v146,1,v146 dst_sel:DWORD dst_unused:UNUSED_PAD src0_sel:DWORD src1_sel:WORD_1
v_mfma_f32_16x16x32_bf16 v[160:163],v[88:91],v[128:131],v[160:163]
ds_read_u16 v144,v144
v_lshlrev_b32_sdwa v145,1,v145 dst_sel:DWORD dst_unused:UNUSED_PAD src0_sel:DWORD src1_sel:WORD_1
v_lshlrev_b32_sdwa v147,1,v147 dst_sel:DWORD dst_unused:UNUSED_PAD src0_sel:DWORD src1_sel:WORD_1
v_mfma_f32_16x16x32_bf16 v[168:171],v[92:95],v[128:131],v[168:171]
s_waitcnt lgkmcnt(11)
ds_read_b64_tr_b16 v[66:67],v1 offset:14880
ds_read_b128 v[112:115],v4 offset:128
v_fma_mixlo_f16 v120,v156,s99,0
v_fma_mixlo_f16 v122,v157,s99,0
v_mfma_f32_16x16x32_bf16 v[176:179],v[96:99],v[128:131],v[176:179]
s_waitcnt lgkmcnt(10)
ds_read_u16_d16_hi v146,v146
v_lshlrev_b32_sdwa v148,1,v148 dst_sel:DWORD dst_unused:UNUSED_PAD src0_sel:DWORD src1_sel:WORD_1
v_lshlrev_b32_sdwa v150,1,v150 dst_sel:DWORD dst_unused:UNUSED_PAD src0_sel:DWORD src1_sel:WORD_1
v_mfma_f32_16x16x32_bf16 v[184:187],v[100:103],v[128:131],v[184:187]
s_waitcnt lgkmcnt(6)
ds_read_b64_tr_b16 v[68:69],v2 offset:12832
ds_read_b64_tr_b16 v[70:71],v2 offset:14880
v_lshl_add_u64 v[136:137],v[136:137],0,v[138:139]
v_mfma_f32_16x16x32_bf16 v[192:195],v[104:107],v[128:131],v[192:195]
s_waitcnt lgkmcnt(7)
ds_read_u16 v145,v145
v_cvt_pk_bf16_f32 v138,v140,v142
v_cvt_pk_bf16_f32 v139,v141,v143
v_mfma_f32_16x16x32_bf16 v[200:203],v[108:111],v[128:131],v[200:203]
ds_read_b128 v[116:119],v4 offset:160
ds_read_b64_tr_b16 v[72:73],v1 offset:16928
v_fma_mixhi_f16 v120,v158,s99,0
v_fma_mixhi_f16 v122,v159,s99,0
v_mfma_f32_16x16x32_bf16 v[164:167],v[88:91],v[136:139],v[164:167]
ds_read_u16_d16_hi v147,v147
v_lshlrev_b32_sdwa v149,1,v149 dst_sel:DWORD dst_unused:UNUSED_PAD src0_sel:DWORD src1_sel:WORD_1
v_lshlrev_b32_sdwa v151,1,v151 dst_sel:DWORD dst_unused:UNUSED_PAD src0_sel:DWORD src1_sel:WORD_1
v_mfma_f32_16x16x32_bf16 v[172:175],v[92:95],v[136:139],v[172:175]
s_setprio 1
ds_read_b64_tr_b16 v[74:75],v1 offset:18976
ds_read_b64_tr_b16 v[76:77],v2 offset:16928
v_pk_fma_f16 v121,v120,v120,s54
v_pk_fma_f16 v123,v122,v122,s54
v_mfma_f32_16x16x32_bf16 v[180:183],v[96:99],v[136:139],v[180:183]
ds_read_u16 v148,v148
v_pk_mul_f16 v121,v120,v121
v_pk_mul_f16 v123,v122,v123
v_mfma_f32_16x16x32_bf16 v[188:191],v[100:103],v[136:139],v[188:191]
ds_read_b64_tr_b16 v[78:79],v2 offset:18976
v_lshlrev_b32_sdwa v152,1,v152 dst_sel:DWORD dst_unused:UNUSED_PAD src0_sel:DWORD src1_sel:WORD_1
v_lshlrev_b32_sdwa v154,1,v154 dst_sel:DWORD dst_unused:UNUSED_PAD src0_sel:DWORD src1_sel:WORD_1
v_mfma_f32_16x16x32_bf16 v[196:199],v[104:107],v[136:139],v[196:199]
ds_read_u16_d16_hi v150,v150
v_exp_f16 v120,v121
v_mfma_f32_16x16x32_bf16 v[204:207],v[108:111],v[136:139],v[204:207]
s_waitcnt lgkmcnt(12)
ds_read_b64_tr_b16 v[80:81],v1 offset:21024
v_exp_f16 v122,v123
v_mfma_f32_16x16x32_bf16 v[128:131],v[64:67],v[16:19],v[112:115]
ds_read_u16 v149,v149
v_lshlrev_b32_sdwa v153,1,v153 dst_sel:DWORD dst_unused:UNUSED_PAD src0_sel:DWORD src1_sel:WORD_1
v_lshlrev_b32_sdwa v155,1,v155 dst_sel:DWORD dst_unused:UNUSED_PAD src0_sel:DWORD src1_sel:WORD_1
v_mfma_f32_16x16x32_bf16 v[136:139],v[64:67],v[20:23],v[112:115]
s_waitcnt lgkmcnt(9)
ds_read_b64_tr_b16 v[82:83],v1 offset:23072
v_exp_f16_sdwa v120,v121 dst_sel:WORD_1 dst_unused:UNUSED_PRESERVE src0_sel:WORD_1
v_mfma_f32_16x16x32_bf16 v[140:143],v[68:71],v[20:23],v[116:119]
ds_read_u16_d16_hi v151,v151
v_exp_f16_sdwa v122,v123 dst_sel:WORD_1 dst_unused:UNUSED_PRESERVE src0_sel:WORD_1
v_mfma_f32_16x16x32_bf16 v[132:135],v[68:71],v[16:19],v[116:119]
s_waitcnt lgkmcnt(8)
ds_read_b64_tr_b16 v[84:85],v2 offset:21024
v_pk_fma_f16 v120,v120,s55,s55
v_pk_fma_f16 v122,v122,s55,s55
v_mfma_f32_16x16x32_bf16 v[128:131],v[72:75],v[24:27],v[128:131]
ds_read_u16 v152,v152
v_rcp_f16 v121,v120
v_mfma_f32_16x16x32_bf16 v[136:139],v[72:75],v[28:31],v[136:139]
s_waitcnt lgkmcnt(7)
ds_read_b64_tr_b16 v[86:87],v2 offset:23072
v_rcp_f16 v123,v122
v_mfma_f32_16x16x32_bf16 v[140:143],v[76:79],v[28:31],v[140:143]
ds_read_u16_d16_hi v154,v154
v_rcp_f16_sdwa v121,v120 dst_sel:WORD_1 dst_unused:UNUSED_PRESERVE src0_sel:WORD_1
v_mfma_f32_16x16x32_bf16 v[132:135],v[76:79],v[24:27],v[132:135]
s_waitcnt lgkmcnt(5)
v_rcp_f16_sdwa v123,v122 dst_sel:WORD_1 dst_unused:UNUSED_PRESERVE src0_sel:WORD_1
v_mfma_f32_16x16x32_bf16 v[128:131],v[80:83],v[32:35],v[128:131]
ds_read_u16 v153,v153
v_fma_mix_f32 v156,v156,v121,0 op_sel_hi:[0,1,0]
v_fma_mix_f32 v158,v158,v121,0 op_sel:[0,1,0] op_sel_hi:[0,1,0]
v_mfma_f32_16x16x32_bf16 v[136:139],v[80:83],v[36:39],v[136:139]
s_waitcnt lgkmcnt(2)
s_setprio 0
v_lshl_add_u64 v[144:145],v[144:145],0,v[146:147]
v_lshl_add_u64 v[146:147],v[148:149],0,v[150:151]
v_mfma_f32_16x16x32_bf16 v[140:143],v[84:87],v[36:39],v[140:143]
ds_read_u16_d16_hi v155,v155
v_fma_mix_f32 v157,v157,v123,0 op_sel_hi:[0,1,0]
v_fma_mix_f32 v159,v159,v123,0 op_sel:[0,1,0] op_sel_hi:[0,1,0]
v_mfma_f32_16x16x32_bf16 v[132:135],v[84:87],v[32:35],v[132:135]
v_lshlrev_b32_sdwa v128,1,v128 dst_sel:DWORD dst_unused:UNUSED_PAD src0_sel:DWORD src1_sel:WORD_1
v_lshlrev_b32_sdwa v130,1,v130 dst_sel:DWORD dst_unused:UNUSED_PAD src0_sel:DWORD src1_sel:WORD_1
v_mfma_f32_16x16x32_bf16 v[208:211],v[88:91],v[144:147],v[208:211]
s_barrier
s_setprio 2
ds_read_u16 v128,v128
v_lshlrev_b32_sdwa v129,1,v129 dst_sel:DWORD dst_unused:UNUSED_PAD src0_sel:DWORD src1_sel:WORD_1
v_mfma_f32_16x16x32_bf16 v[216:219],v[92:95],v[144:147],v[216:219]
ds_read_u16_d16_hi v130,v130
v_lshlrev_b32_sdwa v131,1,v131 dst_sel:DWORD dst_unused:UNUSED_PAD src0_sel:DWORD src1_sel:WORD_1
v_mfma_f32_16x16x32_bf16 v[224:227],v[96:99],v[144:147],v[224:227]
s_cbranch_vccz RB63_%=
s_add_u32 m0,s58,12320
s_mul_i32 s69,s59,3
s_add_u32 s69,s69,s97
buffer_load_dwordx4 v5,s[44:47],s69 offen lds
s_add_u32 m0,m0,1024
s_add_u32 s69,s69,s53
buffer_load_dwordx4 v6,s[44:47],s69 offen lds
RB63_%=:
v_lshlrev_b32_sdwa v132,1,v132 dst_sel:DWORD dst_unused:UNUSED_PAD src0_sel:DWORD src1_sel:WORD_1
v_mfma_f32_16x16x32_bf16 v[232:235],v[100:103],v[144:147],v[232:235]
s_waitcnt lgkmcnt(2)
ds_read_u16 v129,v129
v_lshl_add_u64 v[152:153],v[152:153],0,v[154:155]
v_lshlrev_b32_sdwa v134,1,v134 dst_sel:DWORD dst_unused:UNUSED_PAD src0_sel:DWORD src1_sel:WORD_1
v_mfma_f32_16x16x32_bf16 v[240:243],v[104:107],v[144:147],v[240:243]
ds_read_u16_d16_hi v131,v131
v_cvt_pk_bf16_f32 v154,v156,v158
v_cvt_pk_bf16_f32 v155,v157,v159
v_mfma_f32_16x16x32_bf16 v[248:251],v[108:111],v[144:147],v[248:251]
v_lshlrev_b32_sdwa v133,1,v133 dst_sel:DWORD dst_unused:UNUSED_PAD src0_sel:DWORD src1_sel:WORD_1
v_mfma_f32_16x16x32_bf16 v[212:215],v[88:91],v[152:155],v[212:215]
s_cbranch_vccz RB64_%=
s_add_u32 m0,m0,1024
s_add_u32 s69,s69,s53
buffer_load_dwordx4 v5,s[44:47],s69 offen lds
s_add_u32 m0,m0,s50
s_add_u32 s69,s69,s53
buffer_load_dwordx4 v6,s[44:47],s69 offen lds
RB64_%=:
ds_read_u16 v132,v132
v_lshlrev_b32_sdwa v135,1,v135 dst_sel:DWORD dst_unused:UNUSED_PAD src0_sel:DWORD src1_sel:WORD_1
v_mfma_f32_16x16x32_bf16 v[220:223],v[92:95],v[152:155],v[220:223]
ds_read_u16_d16_hi v134,v134
v_lshlrev_b32_sdwa v136,1,v136 dst_sel:DWORD dst_unused:UNUSED_PAD src0_sel:DWORD src1_sel:WORD_1
v_mfma_f32_16x16x32_bf16 v[228:231],v[96:99],v[152:155],v[228:231]
ds_read_b64_tr_b16 v[88:89],v3 offset:0
ds_read_b64_tr_b16 v[90:91],v3 offset:1536
v_lshlrev_b32_sdwa v138,1,v138 dst_sel:DWORD dst_unused:UNUSED_PAD src0_sel:DWORD src1_sel:WORD_1
v_mfma_f32_16x16x32_bf16 v[236:239],v[100:103],v[152:155],v[236:239]
ds_read_u16 v133,v133
v_lshlrev_b32_sdwa v137,1,v137 dst_sel:DWORD dst_unused:UNUSED_PAD src0_sel:DWORD src1_sel:WORD_1
v_mfma_f32_16x16x32_bf16 v[244:247],v[104:107],v[152:155],v[244:247]
ds_read_u16_d16_hi v135,v135
v_lshlrev_b32_sdwa v139,1,v139 dst_sel:DWORD dst_unused:UNUSED_PAD src0_sel:DWORD src1_sel:WORD_1
v_mfma_f32_16x16x32_bf16 v[252:255],v[108:111],v[152:155],v[252:255]
s_cbranch_vccz RB65_%=
s_add_u32 m0,m0,1024
s_add_u32 s69,s69,s53
buffer_load_dwordx4 v5,s[44:47],s69 offen lds
s_add_u32 m0,m0,1024
s_add_u32 s69,s69,s53
buffer_load_dwordx4 v6,s[44:47],s69 offen lds
RB65_%=:
ds_read_b64_tr_b16 v[92:93],v3 offset:8
ds_read_b64_tr_b16 v[94:95],v3 offset:1544
v_lshlrev_b32_sdwa v140,1,v140 dst_sel:DWORD dst_unused:UNUSED_PAD src0_sel:DWORD src1_sel:WORD_1
v_mfma_f32_16x16x32_bf16 v[144:147],v[64:67],v[40:43],v[112:115]
ds_read_u16 v136,v136
v_lshlrev_b32_sdwa v142,1,v142 dst_sel:DWORD dst_unused:UNUSED_PAD src0_sel:DWORD src1_sel:WORD_1
v_mfma_f32_16x16x32_bf16 v[152:155],v[64:67],v[44:47],v[112:115]
ds_read_u16_d16_hi v138,v138
v_lshlrev_b32_sdwa v141,1,v141 dst_sel:DWORD dst_unused:UNUSED_PAD src0_sel:DWORD src1_sel:WORD_1
v_mfma_f32_16x16x32_bf16 v[156:159],v[68:71],v[44:47],v[116:119]
ds_read_b64_tr_b16 v[96:97],v3 offset:64
ds_read_b64_tr_b16 v[98:99],v3 offset:1600
v_lshlrev_b32_sdwa v143,1,v143 dst_sel:DWORD dst_unused:UNUSED_PAD src0_sel:DWORD src1_sel:WORD_1
v_mfma_f32_16x16x32_bf16 v[148:151],v[68:71],v[40:43],v[116:119]
ds_read_u16 v137,v137
v_mfma_f32_16x16x32_bf16 v[144:147],v[72:75],v[48:51],v[144:147]
ds_read_u16_d16_hi v139,v139
v_mfma_f32_16x16x32_bf16 v[152:155],v[72:75],v[52:55],v[152:155]
s_cbranch_vccz RB66_%=
s_add_u32 m0,m0,s50
s_add_u32 s69,s69,s53
buffer_load_dwordx4 v5,s[44:47],s69 offen lds
s_add_u32 m0,m0,1024
s_add_u32 s69,s69,s53
buffer_load_dwordx4 v6,s[44:47],s69 offen lds
RB66_%=:
ds_read_b64_tr_b16 v[100:101],v3 offset:72
ds_read_b64_tr_b16 v[102:103],v3 offset:1608
v_mfma_f32_16x16x32_bf16 v[156:159],v[76:79],v[52:55],v[156:159]
ds_read_u16 v140,v140
v_mfma_f32_16x16x32_bf16 v[148:151],v[76:79],v[48:51],v[148:151]
s_waitcnt lgkmcnt(15)
ds_read_u16_d16_hi v142,v142
v_lshl_add_u64 v[128:129],v[128:129],0,v[130:131]
v_mfma_f32_16x16x32_bf16 v[144:147],v[80:83],v[56:59],v[144:147]
s_waitcnt lgkmcnt(12)
ds_read_b64_tr_b16 v[104:105],v3 offset:128
ds_read_b64_tr_b16 v[106:107],v3 offset:1664
v_lshl_add_u64 v[130:131],v[132:133],0,v[134:135]
v_mfma_f32_16x16x32_bf16 v[152:155],v[80:83],v[60:63],v[152:155]
ds_read_u16 v141,v141
v_mfma_f32_16x16x32_bf16 v[156:159],v[84:87],v[60:63],v[156:159]
ds_read_u16_d16_hi v143,v143
v_mfma_f32_16x16x32_bf16 v[148:151],v[84:87],v[56:59],v[148:151]
s_cbranch_vccz RB67_%=
s_add_u32 m0,m0,1024
s_add_u32 s69,s69,s53
buffer_load_dwordx4 v5,s[44:47],s69 offen lds
s_add_u32 m0,m0,s50
s_add_u32 s69,s69,s53
buffer_load_dwordx4 v6,s[44:47],s69 offen lds
RB67_%=:
ds_read_b64_tr_b16 v[108:109],v3 offset:136
ds_read_b64_tr_b16 v[110:111],v3 offset:1672
v_lshlrev_b32_sdwa v144,1,v144 dst_sel:DWORD dst_unused:UNUSED_PAD src0_sel:DWORD src1_sel:WORD_1
v_lshlrev_b32_sdwa v146,1,v146 dst_sel:DWORD dst_unused:UNUSED_PAD src0_sel:DWORD src1_sel:WORD_1
v_mfma_f32_16x16x32_bf16 v[160:163],v[88:91],v[128:131],v[160:163]
s_waitcnt lgkmcnt(15)
ds_read_u16 v144,v144
v_lshlrev_b32_sdwa v145,1,v145 dst_sel:DWORD dst_unused:UNUSED_PAD src0_sel:DWORD src1_sel:WORD_1
v_mfma_f32_16x16x32_bf16 v[168:171],v[92:95],v[128:131],v[168:171]
s_waitcnt lgkmcnt(13)
ds_read_u16_d16_hi v146,v146
v_lshlrev_b32_sdwa v147,1,v147 dst_sel:DWORD dst_unused:UNUSED_PAD src0_sel:DWORD src1_sel:WORD_1
v_mfma_f32_16x16x32_bf16 v[176:179],v[96:99],v[128:131],v[176:179]
s_waitcnt lgkmcnt(10)
ds_read_b64_tr_b16 v[64:65],v1 offset:0
ds_read_b64_tr_b16 v[66:67],v1 offset:2048
v_lshlrev_b32_sdwa v148,1,v148 dst_sel:DWORD dst_unused:UNUSED_PAD src0_sel:DWORD src1_sel:WORD_1
v_mfma_f32_16x16x32_bf16 v[184:187],v[100:103],v[128:131],v[184:187]
s_waitcnt lgkmcnt(8)
ds_read_u16 v145,v145
v_lshl_add_u64 v[136:137],v[136:137],0,v[138:139]
v_lshlrev_b32_sdwa v150,1,v150 dst_sel:DWORD dst_unused:UNUSED_PAD src0_sel:DWORD src1_sel:WORD_1
v_mfma_f32_16x16x32_bf16 v[192:195],v[104:107],v[128:131],v[192:195]
s_waitcnt lgkmcnt(5)
s_cbranch_vccz RB68_%=
s_add_u32 m0,m0,1024
s_add_u32 s69,s69,s53
buffer_load_dwordx4 v5,s[44:47],s69 offen lds
s_add_u32 m0,m0,1024
s_add_u32 s69,s69,s53
buffer_load_dwordx4 v6,s[44:47],s69 offen lds
RB68_%=:
ds_read_u16_d16_hi v147,v147
v_lshl_add_u64 v[138:139],v[140:141],0,v[142:143]
v_mfma_f32_16x16x32_bf16 v[200:203],v[108:111],v[128:131],v[200:203]
ds_read_b128 v[112:115],v4 offset:256
ds_read_b64_tr_b16 v[68:69],v2 offset:0
v_lshlrev_b32_sdwa v149,1,v149 dst_sel:DWORD dst_unused:UNUSED_PAD src0_sel:DWORD src1_sel:WORD_1
v_mfma_f32_16x16x32_bf16 v[164:167],v[88:91],v[136:139],v[164:167]
ds_read_u16 v148,v148
v_lshlrev_b32_sdwa v151,1,v151 dst_sel:DWORD dst_unused:UNUSED_PAD src0_sel:DWORD src1_sel:WORD_1
v_mfma_f32_16x16x32_bf16 v[172:175],v[92:95],v[136:139],v[172:175]
ds_read_u16_d16_hi v150,v150
ds_read_b64_tr_b16 v[70:71],v2 offset:2048
v_lshlrev_b32_sdwa v152,1,v152 dst_sel:DWORD dst_unused:UNUSED_PAD src0_sel:DWORD src1_sel:WORD_1
v_mfma_f32_16x16x32_bf16 v[180:183],v[96:99],v[136:139],v[180:183]
ds_read_b128 v[116:119],v4 offset:288
ds_read_b64_tr_b16 v[72:73],v1 offset:4096
v_lshlrev_b32_sdwa v154,1,v154 dst_sel:DWORD dst_unused:UNUSED_PAD src0_sel:DWORD src1_sel:WORD_1
v_mfma_f32_16x16x32_bf16 v[188:191],v[100:103],v[136:139],v[188:191]
ds_read_u16 v149,v149
v_lshlrev_b32_sdwa v153,1,v153 dst_sel:DWORD dst_unused:UNUSED_PAD src0_sel:DWORD src1_sel:WORD_1
v_mfma_f32_16x16x32_bf16 v[196:199],v[104:107],v[136:139],v[196:199]
ds_read_u16_d16_hi v151,v151
v_lshlrev_b32_sdwa v155,1,v155 dst_sel:DWORD dst_unused:UNUSED_PAD src0_sel:DWORD src1_sel:WORD_1
v_mfma_f32_16x16x32_bf16 v[204:207],v[108:111],v[136:139],v[204:207]
s_waitcnt lgkmcnt(8)
ds_read_b64_tr_b16 v[74:75],v1 offset:6144
ds_read_b64_tr_b16 v[76:77],v2 offset:4096
v_lshlrev_b32_sdwa v156,1,v156 dst_sel:DWORD dst_unused:UNUSED_PAD src0_sel:DWORD src1_sel:WORD_1
v_mfma_f32_16x16x32_bf16 v[128:131],v[64:67],v[16:19],v[112:115]
ds_read_u16 v152,v152
v_lshlrev_b32_sdwa v158,1,v158 dst_sel:DWORD dst_unused:UNUSED_PAD src0_sel:DWORD src1_sel:WORD_1
v_mfma_f32_16x16x32_bf16 v[136:139],v[64:67],v[20:23],v[112:115]
s_waitcnt lgkmcnt(6)
ds_read_u16_d16_hi v154,v154
ds_read_b64_tr_b16 v[78:79],v2 offset:6144
v_lshlrev_b32_sdwa v157,1,v157 dst_sel:DWORD dst_unused:UNUSED_PAD src0_sel:DWORD src1_sel:WORD_1
v_mfma_f32_16x16x32_bf16 v[140:143],v[68:71],v[20:23],v[116:119]
ds_read_b64_tr_b16 v[80:81],v1 offset:8192
ds_read_b64_tr_b16 v[82:83],v1 offset:10240
v_lshlrev_b32_sdwa v159,1,v159 dst_sel:DWORD dst_unused:UNUSED_PAD src0_sel:DWORD src1_sel:WORD_1
v_mfma_f32_16x16x32_bf16 v[132:135],v[68:71],v[16:19],v[116:119]
s_waitcnt lgkmcnt(6)
ds_read_u16 v153,v153
v_mfma_f32_16x16x32_bf16 v[128:131],v[72:75],v[24:27],v[128:131]
ds_read_u16_d16_hi v155,v155
v_mfma_f32_16x16x32_bf16 v[136:139],v[72:75],v[28:31],v[136:139]
s_waitcnt lgkmcnt(4)
ds_read_b64_tr_b16 v[84:85],v2 offset:8192
ds_read_b64_tr_b16 v[86:87],v2 offset:10240
v_mfma_f32_16x16x32_bf16 v[140:143],v[76:79],v[28:31],v[140:143]
ds_read_u16 v156,v156
v_mfma_f32_16x16x32_bf16 v[132:135],v[76:79],v[24:27],v[132:135]
s_waitcnt lgkmcnt(5)
ds_read_u16_d16_hi v158,v158
v_lshl_add_u64 v[144:145],v[144:145],0,v[146:147]
v_mfma_f32_16x16x32_bf16 v[128:131],v[80:83],v[32:35],v[128:131]
v_lshl_add_u64 v[146:147],v[148:149],0,v[150:151]
v_mfma_f32_16x16x32_bf16 v[136:139],v[80:83],v[36:39],v[136:139]
s_waitcnt lgkmcnt(2)
ds_read_u16 v157,v157
v_mfma_f32_16x16x32_bf16 v[140:143],v[84:87],v[36:39],v[140:143]
ds_read_u16_d16_hi v159,v159
v_mfma_f32_16x16x32_bf16 v[132:135],v[84:87],v[32:35],v[132:135]
s_nop 0
v_lshlrev_b32_sdwa v128,1,v128 dst_sel:DWORD dst_unused:UNUSED_PAD src0_sel:DWORD src1_sel:WORD_1
v_lshlrev_b32_sdwa v130,1,v130 dst_sel:DWORD dst_unused:UNUSED_PAD src0_sel:DWORD src1_sel:WORD_1
v_mfma_f32_16x16x32_bf16 v[208:211],v[88:91],v[144:147],v[208:211]
ds_read_u16 v128,v128
v_lshlrev_b32_sdwa v129,1,v129 dst_sel:DWORD dst_unused:UNUSED_PAD src0_sel:DWORD src1_sel:WORD_1
v_mfma_f32_16x16x32_bf16 v[216:219],v[92:95],v[144:147],v[216:219]
ds_read_u16_d16_hi v130,v130
v_lshlrev_b32_sdwa v131,1,v131 dst_sel:DWORD dst_unused:UNUSED_PAD src0_sel:DWORD src1_sel:WORD_1
v_mfma_f32_16x16x32_bf16 v[224:227],v[96:99],v[144:147],v[224:227]
v_lshlrev_b32_sdwa v132,1,v132 dst_sel:DWORD dst_unused:UNUSED_PAD src0_sel:DWORD src1_sel:WORD_1
v_mfma_f32_16x16x32_bf16 v[232:235],v[100:103],v[144:147],v[232:235]
ds_read_u16 v129,v129
v_lshl_add_u64 v[152:153],v[152:153],0,v[154:155]
v_lshlrev_b32_sdwa v134,1,v134 dst_sel:DWORD dst_unused:UNUSED_PAD src0_sel:DWORD src1_sel:WORD_1
v_mfma_f32_16x16x32_bf16 v[240:243],v[104:107],v[144:147],v[240:243]
s_waitcnt lgkmcnt(3)
ds_read_u16_d16_hi v131,v131
v_lshl_add_u64 v[154:155],v[156:157],0,v[158:159]
v_mfma_f32_16x16x32_bf16 v[248:251],v[108:111],v[144:147],v[248:251]
v_lshlrev_b32_sdwa v133,1,v133 dst_sel:DWORD dst_unused:UNUSED_PAD src0_sel:DWORD src1_sel:WORD_1
v_mfma_f32_16x16x32_bf16 v[212:215],v[88:91],v[152:155],v[212:215]
ds_read_u16 v132,v132
v_lshlrev_b32_sdwa v135,1,v135 dst_sel:DWORD dst_unused:UNUSED_PAD src0_sel:DWORD src1_sel:WORD_1
v_mfma_f32_16x16x32_bf16 v[220:223],v[92:95],v[152:155],v[220:223]
ds_read_u16_d16_hi v134,v134
v_lshlrev_b32_sdwa v136,1,v136 dst_sel:DWORD dst_unused:UNUSED_PAD src0_sel:DWORD src1_sel:WORD_1
v_mfma_f32_16x16x32_bf16 v[228:231],v[96:99],v[152:155],v[228:231]
ds_read_b64_tr_b16 v[88:89],v3 offset:6160
ds_read_b64_tr_b16 v[90:91],v3 offset:7696
v_lshlrev_b32_sdwa v138,1,v138 dst_sel:DWORD dst_unused:UNUSED_PAD src0_sel:DWORD src1_sel:WORD_1
v_mfma_f32_16x16x32_bf16 v[236:239],v[100:103],v[152:155],v[236:239]
ds_read_u16 v133,v133
v_lshlrev_b32_sdwa v137,1,v137 dst_sel:DWORD dst_unused:UNUSED_PAD src0_sel:DWORD src1_sel:WORD_1
v_mfma_f32_16x16x32_bf16 v[244:247],v[104:107],v[152:155],v[244:247]
ds_read_u16_d16_hi v135,v135
v_lshlrev_b32_sdwa v139,1,v139 dst_sel:DWORD dst_unused:UNUSED_PAD src0_sel:DWORD src1_sel:WORD_1
v_mfma_f32_16x16x32_bf16 v[252:255],v[108:111],v[152:155],v[252:255]
ds_read_b64_tr_b16 v[92:93],v3 offset:6168
ds_read_b64_tr_b16 v[94:95],v3 offset:7704
v_lshlrev_b32_sdwa v140,1,v140 dst_sel:DWORD dst_unused:UNUSED_PAD src0_sel:DWORD src1_sel:WORD_1
v_mfma_f32_16x16x32_bf16 v[144:147],v[64:67],v[40:43],v[112:115]
ds_read_u16 v136,v136
v_lshlrev_b32_sdwa v142,1,v142 dst_sel:DWORD dst_unused:UNUSED_PAD src0_sel:DWORD src1_sel:WORD_1
v_mfma_f32_16x16x32_bf16 v[152:155],v[64:67],v[44:47],v[112:115]
ds_read_u16_d16_hi v138,v138
v_lshlrev_b32_sdwa v141,1,v141 dst_sel:DWORD dst_unused:UNUSED_PAD src0_sel:DWORD src1_sel:WORD_1
v_mfma_f32_16x16x32_bf16 v[156:159],v[68:71],v[44:47],v[116:119]
ds_read_b64_tr_b16 v[96:97],v3 offset:6224
ds_read_b64_tr_b16 v[98:99],v3 offset:7760
v_lshlrev_b32_sdwa v143,1,v143 dst_sel:DWORD dst_unused:UNUSED_PAD src0_sel:DWORD src1_sel:WORD_1
v_mfma_f32_16x16x32_bf16 v[148:151],v[68:71],v[40:43],v[116:119]
ds_read_u16 v137,v137
v_mfma_f32_16x16x32_bf16 v[144:147],v[72:75],v[48:51],v[144:147]
ds_read_u16_d16_hi v139,v139
v_mfma_f32_16x16x32_bf16 v[152:155],v[72:75],v[52:55],v[152:155]
ds_read_b64_tr_b16 v[100:101],v3 offset:6232
ds_read_b64_tr_b16 v[102:103],v3 offset:7768
v_mfma_f32_16x16x32_bf16 v[156:159],v[76:79],v[52:55],v[156:159]
ds_read_u16 v140,v140
v_mfma_f32_16x16x32_bf16 v[148:151],v[76:79],v[48:51],v[148:151]
s_waitcnt lgkmcnt(15)
ds_read_u16_d16_hi v142,v142
v_lshl_add_u64 v[128:129],v[128:129],0,v[130:131]
v_mfma_f32_16x16x32_bf16 v[144:147],v[80:83],v[56:59],v[144:147]
s_waitcnt lgkmcnt(12)
ds_read_b64_tr_b16 v[104:105],v3 offset:6288
ds_read_b64_tr_b16 v[106:107],v3 offset:7824
v_lshl_add_u64 v[130:131],v[132:133],0,v[134:135]
v_mfma_f32_16x16x32_bf16 v[152:155],v[80:83],v[60:63],v[152:155]
ds_read_u16 v141,v141
v_mfma_f32_16x16x32_bf16 v[156:159],v[84:87],v[60:63],v[156:159]
ds_read_u16_d16_hi v143,v143
v_mfma_f32_16x16x32_bf16 v[148:151],v[84:87],v[56:59],v[148:151]
ds_read_b64_tr_b16 v[108:109],v3 offset:6296
ds_read_b64_tr_b16 v[110:111],v3 offset:7832
v_lshlrev_b32_sdwa v144,1,v144 dst_sel:DWORD dst_unused:UNUSED_PAD src0_sel:DWORD src1_sel:WORD_1
v_lshlrev_b32_sdwa v146,1,v146 dst_sel:DWORD dst_unused:UNUSED_PAD src0_sel:DWORD src1_sel:WORD_1
v_mfma_f32_16x16x32_bf16 v[160:163],v[88:91],v[128:131],v[160:163]
s_waitcnt lgkmcnt(15)
ds_read_u16 v144,v144
v_lshlrev_b32_sdwa v145,1,v145 dst_sel:DWORD dst_unused:UNUSED_PAD src0_sel:DWORD src1_sel:WORD_1
v_mfma_f32_16x16x32_bf16 v[168:171],v[92:95],v[128:131],v[168:171]
s_waitcnt lgkmcnt(13)
ds_read_u16_d16_hi v146,v146
v_lshlrev_b32_sdwa v147,1,v147 dst_sel:DWORD dst_unused:UNUSED_PAD src0_sel:DWORD src1_sel:WORD_1
v_mfma_f32_16x16x32_bf16 v[176:179],v[96:99],v[128:131],v[176:179]
s_waitcnt lgkmcnt(10)
ds_read_b64_tr_b16 v[64:65],v1 offset:512
ds_read_b64_tr_b16 v[66:67],v1 offset:2560
v_lshlrev_b32_sdwa v148,1,v148 dst_sel:DWORD dst_unused:UNUSED_PAD src0_sel:DWORD src1_sel:WORD_1
v_mfma_f32_16x16x32_bf16 v[184:187],v[100:103],v[128:131],v[184:187]
s_waitcnt lgkmcnt(8)
ds_read_u16 v145,v145
v_lshl_add_u64 v[136:137],v[136:137],0,v[138:139]
v_lshlrev_b32_sdwa v150,1,v150 dst_sel:DWORD dst_unused:UNUSED_PAD src0_sel:DWORD src1_sel:WORD_1
v_mfma_f32_16x16x32_bf16 v[192:195],v[104:107],v[128:131],v[192:195]
s_waitcnt lgkmcnt(5)
ds_read_u16_d16_hi v147,v147
v_lshl_add_u64 v[138:139],v[140:141],0,v[142:143]
v_mfma_f32_16x16x32_bf16 v[200:203],v[108:111],v[128:131],v[200:203]
ds_read_b128 v[112:115],v4 offset:384
ds_read_b64_tr_b16 v[68:69],v2 offset:512
v_lshlrev_b32_sdwa v149,1,v149 dst_sel:DWORD dst_unused:UNUSED_PAD src0_sel:DWORD src1_sel:WORD_1
v_mfma_f32_16x16x32_bf16 v[164:167],v[88:91],v[136:139],v[164:167]
ds_read_u16 v148,v148
v_lshlrev_b32_sdwa v151,1,v151 dst_sel:DWORD dst_unused:UNUSED_PAD src0_sel:DWORD src1_sel:WORD_1
v_mfma_f32_16x16x32_bf16 v[172:175],v[92:95],v[136:139],v[172:175]
ds_read_u16_d16_hi v150,v150
ds_read_b64_tr_b16 v[70:71],v2 offset:2560
v_lshlrev_b32_sdwa v152,1,v152 dst_sel:DWORD dst_unused:UNUSED_PAD src0_sel:DWORD src1_sel:WORD_1
v_mfma_f32_16x16x32_bf16 v[180:183],v[96:99],v[136:139],v[180:183]
ds_read_b128 v[116:119],v4 offset:416
ds_read_b64_tr_b16 v[72:73],v1 offset:4608
v_lshlrev_b32_sdwa v154,1,v154 dst_sel:DWORD dst_unused:UNUSED_PAD src0_sel:DWORD src1_sel:WORD_1
v_mfma_f32_16x16x32_bf16 v[188:191],v[100:103],v[136:139],v[188:191]
ds_read_u16 v149,v149
v_lshlrev_b32_sdwa v153,1,v153 dst_sel:DWORD dst_unused:UNUSED_PAD src0_sel:DWORD src1_sel:WORD_1
v_mfma_f32_16x16x32_bf16 v[196:199],v[104:107],v[136:139],v[196:199]
ds_read_u16_d16_hi v151,v151
v_lshlrev_b32_sdwa v155,1,v155 dst_sel:DWORD dst_unused:UNUSED_PAD src0_sel:DWORD src1_sel:WORD_1
v_mfma_f32_16x16x32_bf16 v[204:207],v[108:111],v[136:139],v[204:207]
s_waitcnt lgkmcnt(8)
ds_read_b64_tr_b16 v[74:75],v1 offset:6656
ds_read_b64_tr_b16 v[76:77],v2 offset:4608
v_lshlrev_b32_sdwa v156,1,v156 dst_sel:DWORD dst_unused:UNUSED_PAD src0_sel:DWORD src1_sel:WORD_1
v_mfma_f32_16x16x32_bf16 v[128:131],v[64:67],v[16:19],v[112:115]
ds_read_u16 v152,v152
v_lshlrev_b32_sdwa v158,1,v158 dst_sel:DWORD dst_unused:UNUSED_PAD src0_sel:DWORD src1_sel:WORD_1
v_mfma_f32_16x16x32_bf16 v[136:139],v[64:67],v[20:23],v[112:115]
s_waitcnt lgkmcnt(6)
ds_read_u16_d16_hi v154,v154
ds_read_b64_tr_b16 v[78:79],v2 offset:6656
v_lshlrev_b32_sdwa v157,1,v157 dst_sel:DWORD dst_unused:UNUSED_PAD src0_sel:DWORD src1_sel:WORD_1
v_mfma_f32_16x16x32_bf16 v[140:143],v[68:71],v[20:23],v[116:119]
ds_read_b64_tr_b16 v[80:81],v1 offset:8704
ds_read_b64_tr_b16 v[82:83],v1 offset:10752
v_lshlrev_b32_sdwa v159,1,v159 dst_sel:DWORD dst_unused:UNUSED_PAD src0_sel:DWORD src1_sel:WORD_1
v_mfma_f32_16x16x32_bf16 v[132:135],v[68:71],v[16:19],v[116:119]
s_waitcnt lgkmcnt(6)
ds_read_u16 v153,v153
v_mfma_f32_16x16x32_bf16 v[128:131],v[72:75],v[24:27],v[128:131]
ds_read_u16_d16_hi v155,v155
v_mfma_f32_16x16x32_bf16 v[136:139],v[72:75],v[28:31],v[136:139]
s_waitcnt lgkmcnt(4)
ds_read_b64_tr_b16 v[84:85],v2 offset:8704
ds_read_b64_tr_b16 v[86:87],v2 offset:10752
v_mfma_f32_16x16x32_bf16 v[140:143],v[76:79],v[28:31],v[140:143]
ds_read_u16 v156,v156
v_mfma_f32_16x16x32_bf16 v[132:135],v[76:79],v[24:27],v[132:135]
s_waitcnt lgkmcnt(5)
ds_read_u16_d16_hi v158,v158
v_lshl_add_u64 v[144:145],v[144:145],0,v[146:147]
v_mfma_f32_16x16x32_bf16 v[128:131],v[80:83],v[32:35],v[128:131]
v_lshl_add_u64 v[146:147],v[148:149],0,v[150:151]
v_mfma_f32_16x16x32_bf16 v[136:139],v[80:83],v[36:39],v[136:139]
s_waitcnt lgkmcnt(2)
ds_read_u16 v157,v157
v_mfma_f32_16x16x32_bf16 v[140:143],v[84:87],v[36:39],v[140:143]
ds_read_u16_d16_hi v159,v159
v_mfma_f32_16x16x32_bf16 v[132:135],v[84:87],v[32:35],v[132:135]
s_nop 0
v_lshlrev_b32_sdwa v128,1,v128 dst_sel:DWORD dst_unused:UNUSED_PAD src0_sel:DWORD src1_sel:WORD_1
v_lshlrev_b32_sdwa v130,1,v130 dst_sel:DWORD dst_unused:UNUSED_PAD src0_sel:DWORD src1_sel:WORD_1
v_mfma_f32_16x16x32_bf16 v[208:211],v[88:91],v[144:147],v[208:211]
s_waitcnt vmcnt(0)
s_barrier
s_setprio 3
ds_read_u16 v128,v128
v_lshlrev_b32_sdwa v129,1,v129 dst_sel:DWORD dst_unused:UNUSED_PAD src0_sel:DWORD src1_sel:WORD_1
v_lshlrev_b32_sdwa v131,1,v131 dst_sel:DWORD dst_unused:UNUSED_PAD src0_sel:DWORD src1_sel:WORD_1
v_mfma_f32_16x16x32_bf16 v[216:219],v[92:95],v[144:147],v[216:219]
s_cbranch_vccnz RB69_%=
s_mov_b64 exec,s[62:63]
s_add_u32 m0,s57,34880
s_add_u32 s64,s56,4915200
buffer_load_dwordx4 v5,s[36:39],s64 offen lds
s_add_u32 m0,s57,36608
s_nop 0
buffer_load_dwordx4 v6,s[36:39],s64 offen lds
s_mov_b64 exec,-1
RB69_%=:
v_fma_mixlo_f16 v120,v140,s99,0
v_fma_mixlo_f16 v122,v141,s99,0
v_mfma_f32_16x16x32_bf16 v[224:227],v[96:99],v[144:147],v[224:227]
s_cbranch_vccz RB70_%=
s_add_u32 m0,s58,0
s_mul_i32 s69,s59,5
s_add_u32 s69,s69,s97
buffer_load_dwordx4 v5,s[44:47],s69 offen lds
s_add_u32 m0,m0,1024
s_add_u32 s69,s69,s53
buffer_load_dwordx4 v6,s[44:47],s69 offen lds
RB70_%=:
ds_read_u16_d16_hi v130,v130
v_lshlrev_b32_sdwa v132,1,v132 dst_sel:DWORD dst_unused:UNUSED_PAD src0_sel:DWORD src1_sel:WORD_1
v_lshlrev_b32_sdwa v134,1,v134 dst_sel:DWORD dst_unused:UNUSED_PAD src0_sel:DWORD src1_sel:WORD_1
v_mfma_f32_16x16x32_bf16 v[232:235],v[100:103],v[144:147],v[232:235]
v_lshl_add_u64 v[152:153],v[152:153],0,v[154:155]
v_mfma_f32_16x16x32_bf16 v[240:243],v[104:107],v[144:147],v[240:243]
s_waitcnt lgkmcnt(2)
ds_read_u16 v129,v129
v_lshl_add_u64 v[154:155],v[156:157],0,v[158:159]
v_mfma_f32_16x16x32_bf16 v[248:251],v[108:111],v[144:147],v[248:251]
v_fma_mixhi_f16 v120,v142,s99,0
v_fma_mixhi_f16 v122,v143,s99,0
v_mfma_f32_16x16x32_bf16 v[212:215],v[88:91],v[152:155],v[212:215]
s_cbranch_vccz RB71_%=
s_add_u32 m0,m0,1024
s_add_u32 s69,s69,s53
buffer_load_dwordx4 v5,s[44:47],s69 offen lds
s_add_u32 m0,m0,s50
s_add_u32 s69,s69,s53
buffer_load_dwordx4 v6,s[44:47],s69 offen lds
RB71_%=:
ds_read_u16_d16_hi v131,v131
v_lshlrev_b32_sdwa v133,1,v133 dst_sel:DWORD dst_unused:UNUSED_PAD src0_sel:DWORD src1_sel:WORD_1
v_lshlrev_b32_sdwa v135,1,v135 dst_sel:DWORD dst_unused:UNUSED_PAD src0_sel:DWORD src1_sel:WORD_1
v_mfma_f32_16x16x32_bf16 v[220:223],v[92:95],v[152:155],v[220:223]
ds_read_b64_tr_b16 v[88:89],v3 offset:12320
v_pk_fma_f16 v121,v120,v120,s54
v_pk_fma_f16 v123,v122,v122,s54
v_mfma_f32_16x16x32_bf16 v[228:231],v[96:99],v[152:155],v[228:231]
s_cbranch_vccnz RB72_%=
s_mov_b64 exec,s[62:63]
s_add_u32 m0,s57,38368
s_add_u32 s64,s56,5013504
buffer_load_dwordx4 v5,s[36:39],s64 offen lds
s_add_u32 m0,s57,40096
s_nop 0
buffer_load_dwordx4 v6,s[36:39],s64 offen lds
s_mov_b64 exec,-1
RB72_%=:
ds_read_u16 v132,v132
v_pk_mul_f16 v121,v120,v121
v_pk_mul_f16 v123,v122,v123
v_mfma_f32_16x16x32_bf16 v[236:239],v[100:103],v[152:155],v[236:239]
ds_read_b64_tr_b16 v[90:91],v3 offset:13856
v_lshlrev_b32_sdwa v136,1,v136 dst_sel:DWORD dst_unused:UNUSED_PAD src0_sel:DWORD src1_sel:WORD_1
v_lshlrev_b32_sdwa v138,1,v138 dst_sel:DWORD dst_unused:UNUSED_PAD src0_sel:DWORD src1_sel:WORD_1
v_mfma_f32_16x16x32_bf16 v[244:247],v[104:107],v[152:155],v[244:247]
s_cbranch_vccz RB73_%=
s_add_u32 m0,m0,1024
s_add_u32 s69,s69,s53
buffer_load_dwordx4 v5,s[44:47],s69 offen lds
s_add_u32 m0,m0,1024
s_add_u32 s69,s69,s53
buffer_load_dwordx4 v6,s[44:47],s69 offen lds
RB73_%=:
ds_read_u16_d16_hi v134,v134
v_exp_f16 v120,v121
v_mfma_f32_16x16x32_bf16 v[252:255],v[108:111],v[152:155],v[252:255]
ds_read_b64_tr_b16 v[92:93],v3 offset:12328
v_exp_f16 v122,v123
v_mfma_f32_16x16x32_bf16 v[144:147],v[64:67],v[40:43],v[112:115]
ds_read_u16 v133,v133
v_lshlrev_b32_sdwa v137,1,v137 dst_sel:DWORD dst_unused:UNUSED_PAD src0_sel:DWORD src1_sel:WORD_1
v_lshlrev_b32_sdwa v139,1,v139 dst_sel:DWORD dst_unused:UNUSED_PAD src0_sel:DWORD src1_sel:WORD_1
v_mfma_f32_16x16x32_bf16 v[152:155],v[64:67],v[44:47],v[112:115]
s_cbranch_vccz RB74_%=
s_add_u32 m0,m0,s50
s_add_u32 s69,s69,s53
buffer_load_dwordx4 v5,s[44:47],s69 offen lds
s_add_u32 m0,m0,1024
s_add_u32 s69,s69,s53
buffer_load_dwordx4 v6,s[44:47],s69 offen lds
RB74_%=:
ds_read_b64_tr_b16 v[94:95],v3 offset:13864
v_exp_f16_sdwa v120,v121 dst_sel:WORD_1 dst_unused:UNUSED_PRESERVE src0_sel:WORD_1
v_mfma_f32_16x16x32_bf16 v[156:159],v[68:71],v[44:47],v[116:119]
ds_read_u16_d16_hi v135,v135
v_exp_f16_sdwa v122,v123 dst_sel:WORD_1 dst_unused:UNUSED_PRESERVE src0_sel:WORD_1
v_mfma_f32_16x16x32_bf16 v[148:151],v[68:71],v[40:43],v[116:119]
ds_read_b64_tr_b16 v[96:97],v3 offset:12384
v_pk_fma_f16 v120,v120,s55,s55
v_pk_fma_f16 v122,v122,s55,s55
v_mfma_f32_16x16x32_bf16 v[144:147],v[72:75],v[48:51],v[144:147]
ds_read_u16 v136,v136
v_rcp_f16 v121,v120
v_mfma_f32_16x16x32_bf16 v[152:155],v[72:75],v[52:55],v[152:155]
s_cbranch_vccz RB75_%=
s_add_u32 m0,m0,1024
s_add_u32 s69,s69,s53
buffer_load_dwordx4 v5,s[44:47],s69 offen lds
s_add_u32 m0,m0,s50
s_add_u32 s69,s69,s53
buffer_load_dwordx4 v6,s[44:47],s69 offen lds
RB75_%=:
ds_read_b64_tr_b16 v[98:99],v3 offset:13920
ds_read_b64_tr_b16 v[100:101],v3 offset:12392
v_rcp_f16 v123,v122
v_mfma_f32_16x16x32_bf16 v[156:159],v[76:79],v[52:55],v[156:159]
ds_read_u16_d16_hi v138,v138
v_rcp_f16_sdwa v121,v120 dst_sel:WORD_1 dst_unused:UNUSED_PRESERVE src0_sel:WORD_1
v_mfma_f32_16x16x32_bf16 v[148:151],v[76:79],v[48:51],v[148:151]
ds_read_b64_tr_b16 v[102:103],v3 offset:13928
ds_read_b64_tr_b16 v[104:105],v3 offset:12448
v_rcp_f16_sdwa v123,v122 dst_sel:WORD_1 dst_unused:UNUSED_PRESERVE src0_sel:WORD_1
v_mfma_f32_16x16x32_bf16 v[144:147],v[80:83],v[56:59],v[144:147]
ds_read_u16 v137,v137
v_fma_mix_f32 v140,v140,v121,0 op_sel_hi:[0,1,0]
v_fma_mix_f32 v142,v142,v121,0 op_sel:[0,1,0] op_sel_hi:[0,1,0]
v_mfma_f32_16x16x32_bf16 v[152:155],v[80:83],v[60:63],v[152:155]
s_waitcnt lgkmcnt(8)
s_cbranch_vccz RB76_%=
s_add_u32 m0,m0,1024
s_add_u32 s69,s69,s53
buffer_load_dwordx4 v5,s[44:47],s69 offen lds
s_add_u32 m0,m0,1024
s_add_u32 s69,s69,s53
buffer_load_dwordx4 v6,s[44:47],s69 offen lds
RB76_%=:
ds_read_b64_tr_b16 v[106:107],v3 offset:13984
ds_read_b64_tr_b16 v[108:109],v3 offset:12456
v_lshl_add_u64 v[128:129],v[128:129],0,v[130:131]
v_lshl_add_u64 v[130:131],v[132:133],0,v[134:135]
v_mfma_f32_16x16x32_bf16 v[156:159],v[84:87],v[60:63],v[156:159]
ds_read_u16_d16_hi v139,v139
v_fma_mix_f32 v141,v141,v123,0 op_sel_hi:[0,1,0]
v_fma_mix_f32 v143,v143,v123,0 op_sel:[0,1,0] op_sel_hi:[0,1,0]
v_mfma_f32_16x16x32_bf16 v[148:151],v[84:87],v[56:59],v[148:151]
ds_read_b64_tr_b16 v[110:111],v3 offset:13992
ds_read_b64_tr_b16 v[64:65],v1 offset:12320
v_lshlrev_b32_sdwa v144,1,v144 dst_sel:DWORD dst_unused:UNUSED_PAD src0_sel:DWORD src1_sel:WORD_1
v_lshlrev_b32_sdwa v146,1,v146 dst_sel:DWORD dst_unused:UNUSED_PAD src0_sel:DWORD src1_sel:WORD_1
v_mfma_f32_16x16x32_bf16 v[160:163],v[88:91],v[128:131],v[160:163]
ds_read_u16 v144,v144
v_lshlrev_b32_sdwa v145,1,v145 dst_sel:DWORD dst_unused:UNUSED_PAD src0_sel:DWORD src1_sel:WORD_1
v_lshlrev_b32_sdwa v147,1,v147 dst_sel:DWORD dst_unused:UNUSED_PAD src0_sel:DWORD src1_sel:WORD_1
v_mfma_f32_16x16x32_bf16 v[168:171],v[92:95],v[128:131],v[168:171]
s_waitcnt lgkmcnt(11)
ds_read_b64_tr_b16 v[66:67],v1 offset:14368
ds_read_b128 v[112:115],v4 offset:512
v_fma_mixlo_f16 v120,v156,s99,0
v_fma_mixlo_f16 v122,v157,s99,0
v_mfma_f32_16x16x32_bf16 v[176:179],v[96:99],v[128:131],v[176:179]
s_waitcnt lgkmcnt(10)
ds_read_u16_d16_hi v146,v146
v_lshlrev_b32_sdwa v148,1,v148 dst_sel:DWORD dst_unused:UNUSED_PAD src0_sel:DWORD src1_sel:WORD_1
v_lshlrev_b32_sdwa v150,1,v150 dst_sel:DWORD dst_unused:UNUSED_PAD src0_sel:DWORD src1_sel:WORD_1
v_mfma_f32_16x16x32_bf16 v[184:187],v[100:103],v[128:131],v[184:187]
s_waitcnt lgkmcnt(6)
ds_read_b64_tr_b16 v[68:69],v2 offset:12320
ds_read_b64_tr_b16 v[70:71],v2 offset:14368
v_lshl_add_u64 v[136:137],v[136:137],0,v[138:139]
v_mfma_f32_16x16x32_bf16 v[192:195],v[104:107],v[128:131],v[192:195]
s_waitcnt lgkmcnt(7)
ds_read_u16 v145,v145
v_cvt_pk_bf16_f32 v138,v140,v142
v_cvt_pk_bf16_f32 v139,v141,v143
v_mfma_f32_16x16x32_bf16 v[200:203],v[108:111],v[128:131],v[200:203]
ds_read_b128 v[116:119],v4 offset:544
ds_read_b64_tr_b16 v[72:73],v1 offset:16416
v_fma_mixhi_f16 v120,v158,s99,0
v_fma_mixhi_f16 v122,v159,s99,0
v_mfma_f32_16x16x32_bf16 v[164:167],v[88:91],v[136:139],v[164:167]
ds_read_u16_d16_hi v147,v147
v_lshlrev_b32_sdwa v149,1,v149 dst_sel:DWORD dst_unused:UNUSED_PAD src0_sel:DWORD src1_sel:WORD_1
v_lshlrev_b32_sdwa v151,1,v151 dst_sel:DWORD dst_unused:UNUSED_PAD src0_sel:DWORD src1_sel:WORD_1
v_mfma_f32_16x16x32_bf16 v[172:175],v[92:95],v[136:139],v[172:175]
ds_read_b64_tr_b16 v[74:75],v1 offset:18464
ds_read_b64_tr_b16 v[76:77],v2 offset:16416
v_pk_fma_f16 v121,v120,v120,s54
v_pk_fma_f16 v123,v122,v122,s54
v_mfma_f32_16x16x32_bf16 v[180:183],v[96:99],v[136:139],v[180:183]
ds_read_u16 v148,v148
v_pk_mul_f16 v121,v120,v121
v_pk_mul_f16 v123,v122,v123
v_mfma_f32_16x16x32_bf16 v[188:191],v[100:103],v[136:139],v[188:191]
ds_read_b64_tr_b16 v[78:79],v2 offset:18464
v_lshlrev_b32_sdwa v152,1,v152 dst_sel:DWORD dst_unused:UNUSED_PAD src0_sel:DWORD src1_sel:WORD_1
v_lshlrev_b32_sdwa v154,1,v154 dst_sel:DWORD dst_unused:UNUSED_PAD src0_sel:DWORD src1_sel:WORD_1
v_mfma_f32_16x16x32_bf16 v[196:199],v[104:107],v[136:139],v[196:199]
ds_read_u16_d16_hi v150,v150
v_exp_f16 v120,v121
v_mfma_f32_16x16x32_bf16 v[204:207],v[108:111],v[136:139],v[204:207]
s_waitcnt lgkmcnt(12)
ds_read_b64_tr_b16 v[80:81],v1 offset:20512
v_exp_f16 v122,v123
v_mfma_f32_16x16x32_bf16 v[128:131],v[64:67],v[16:19],v[112:115]
ds_read_u16 v149,v149
v_lshlrev_b32_sdwa v153,1,v153 dst_sel:DWORD dst_unused:UNUSED_PAD src0_sel:DWORD src1_sel:WORD_1
v_lshlrev_b32_sdwa v155,1,v155 dst_sel:DWORD dst_unused:UNUSED_PAD src0_sel:DWORD src1_sel:WORD_1
v_mfma_f32_16x16x32_bf16 v[136:139],v[64:67],v[20:23],v[112:115]
s_waitcnt lgkmcnt(9)
ds_read_b64_tr_b16 v[82:83],v1 offset:22560
v_exp_f16_sdwa v120,v121 dst_sel:WORD_1 dst_unused:UNUSED_PRESERVE src0_sel:WORD_1
v_mfma_f32_16x16x32_bf16 v[140:143],v[68:71],v[20:23],v[116:119]
ds_read_u16_d16_hi v151,v151
v_exp_f16_sdwa v122,v123 dst_sel:WORD_1 dst_unused:UNUSED_PRESERVE src0_sel:WORD_1
v_mfma_f32_16x16x32_bf16 v[132:135],v[68:71],v[16:19],v[116:119]
s_waitcnt lgkmcnt(8)
ds_read_b64_tr_b16 v[84:85],v2 offset:20512
v_pk_fma_f16 v120,v120,s55,s55
v_pk_fma_f16 v122,v122,s55,s55
v_mfma_f32_16x16x32_bf16 v[128:131],v[72:75],v[24:27],v[128:131]
ds_read_u16 v152,v152
v_rcp_f16 v121,v120
v_mfma_f32_16x16x32_bf16 v[136:139],v[72:75],v[28:31],v[136:139]
s_waitcnt lgkmcnt(7)
ds_read_b64_tr_b16 v[86:87],v2 offset:22560
v_rcp_f16 v123,v122
v_mfma_f32_16x16x32_bf16 v[140:143],v[76:79],v[28:31],v[140:143]
ds_read_u16_d16_hi v154,v154
v_rcp_f16_sdwa v121,v120 dst_sel:WORD_1 dst_unused:UNUSED_PRESERVE src0_sel:WORD_1
v_mfma_f32_16x16x32_bf16 v[132:135],v[76:79],v[24:27],v[132:135]
s_waitcnt lgkmcnt(5)
v_rcp_f16_sdwa v123,v122 dst_sel:WORD_1 dst_unused:UNUSED_PRESERVE src0_sel:WORD_1
v_mfma_f32_16x16x32_bf16 v[128:131],v[80:83],v[32:35],v[128:131]
ds_read_u16 v153,v153
v_fma_mix_f32 v156,v156,v121,0 op_sel_hi:[0,1,0]
v_fma_mix_f32 v158,v158,v121,0 op_sel:[0,1,0] op_sel_hi:[0,1,0]
v_mfma_f32_16x16x32_bf16 v[136:139],v[80:83],v[36:39],v[136:139]
s_waitcnt lgkmcnt(2)
v_lshl_add_u64 v[144:145],v[144:145],0,v[146:147]
v_lshl_add_u64 v[146:147],v[148:149],0,v[150:151]
v_mfma_f32_16x16x32_bf16 v[140:143],v[84:87],v[36:39],v[140:143]
ds_read_u16_d16_hi v155,v155
v_fma_mix_f32 v157,v157,v123,0 op_sel_hi:[0,1,0]
v_fma_mix_f32 v159,v159,v123,0 op_sel:[0,1,0] op_sel_hi:[0,1,0]
v_mfma_f32_16x16x32_bf16 v[132:135],v[84:87],v[32:35],v[132:135]
v_lshlrev_b32_sdwa v128,1,v128 dst_sel:DWORD dst_unused:UNUSED_PAD src0_sel:DWORD src1_sel:WORD_1
v_lshlrev_b32_sdwa v130,1,v130 dst_sel:DWORD dst_unused:UNUSED_PAD src0_sel:DWORD src1_sel:WORD_1
v_mfma_f32_16x16x32_bf16 v[208:211],v[88:91],v[144:147],v[208:211]
ds_read_u16 v128,v128
v_lshlrev_b32_sdwa v129,1,v129 dst_sel:DWORD dst_unused:UNUSED_PAD src0_sel:DWORD src1_sel:WORD_1
v_lshlrev_b32_sdwa v131,1,v131 dst_sel:DWORD dst_unused:UNUSED_PAD src0_sel:DWORD src1_sel:WORD_1
v_mfma_f32_16x16x32_bf16 v[216:219],v[92:95],v[144:147],v[216:219]
v_fma_mixlo_f16 v120,v140,s99,0
v_fma_mixlo_f16 v122,v141,s99,0
v_mfma_f32_16x16x32_bf16 v[224:227],v[96:99],v[144:147],v[224:227]
ds_read_u16_d16_hi v130,v130
v_lshlrev_b32_sdwa v132,1,v132 dst_sel:DWORD dst_unused:UNUSED_PAD src0_sel:DWORD src1_sel:WORD_1
v_lshlrev_b32_sdwa v134,1,v134 dst_sel:DWORD dst_unused:UNUSED_PAD src0_sel:DWORD src1_sel:WORD_1
v_mfma_f32_16x16x32_bf16 v[232:235],v[100:103],v[144:147],v[232:235]
s_waitcnt lgkmcnt(2)
v_lshl_add_u64 v[152:153],v[152:153],0,v[154:155]
v_mfma_f32_16x16x32_bf16 v[240:243],v[104:107],v[144:147],v[240:243]
ds_read_u16 v129,v129
v_cvt_pk_bf16_f32 v154,v156,v158
v_cvt_pk_bf16_f32 v155,v157,v159
v_mfma_f32_16x16x32_bf16 v[248:251],v[108:111],v[144:147],v[248:251]
v_fma_mixhi_f16 v120,v142,s99,0
v_fma_mixhi_f16 v122,v143,s99,0
v_mfma_f32_16x16x32_bf16 v[212:215],v[88:91],v[152:155],v[212:215]
s_setprio 2
ds_read_u16_d16_hi v131,v131
v_lshlrev_b32_sdwa v133,1,v133 dst_sel:DWORD dst_unused:UNUSED_PAD src0_sel:DWORD src1_sel:WORD_1
v_lshlrev_b32_sdwa v135,1,v135 dst_sel:DWORD dst_unused:UNUSED_PAD src0_sel:DWORD src1_sel:WORD_1
v_mfma_f32_16x16x32_bf16 v[220:223],v[92:95],v[152:155],v[220:223]
ds_read_b64_tr_b16 v[88:89],v3 offset:18480
v_pk_fma_f16 v121,v120,v120,s54
v_pk_fma_f16 v123,v122,v122,s54
v_mfma_f32_16x16x32_bf16 v[228:231],v[96:99],v[152:155],v[228:231]
ds_read_u16 v132,v132
v_pk_mul_f16 v121,v120,v121
v_pk_mul_f16 v123,v122,v123
v_mfma_f32_16x16x32_bf16 v[236:239],v[100:103],v[152:155],v[236:239]
ds_read_b64_tr_b16 v[90:91],v3 offset:20016
v_lshlrev_b32_sdwa v136,1,v136 dst_sel:DWORD dst_unused:UNUSED_PAD src0_sel:DWORD src1_sel:WORD_1
v_lshlrev_b32_sdwa v138,1,v138 dst_sel:DWORD dst_unused:UNUSED_PAD src0_sel:DWORD src1_sel:WORD_1
v_mfma_f32_16x16x32_bf16 v[244:247],v[104:107],v[152:155],v[244:247]
ds_read_u16_d16_hi v134,v134
v_exp_f16 v120,v121
v_mfma_f32_16x16x32_bf16 v[252:255],v[108:111],v[152:155],v[252:255]
ds_read_b64_tr_b16 v[92:93],v3 offset:18488
v_exp_f16 v122,v123
v_mfma_f32_16x16x32_bf16 v[144:147],v[64:67],v[40:43],v[112:115]
ds_read_u16 v133,v133
v_lshlrev_b32_sdwa v137,1,v137 dst_sel:DWORD dst_unused:UNUSED_PAD src0_sel:DWORD src1_sel:WORD_1
v_lshlrev_b32_sdwa v139,1,v139 dst_sel:DWORD dst_unused:UNUSED_PAD src0_sel:DWORD src1_sel:WORD_1
v_mfma_f32_16x16x32_bf16 v[152:155],v[64:67],v[44:47],v[112:115]
ds_read_b64_tr_b16 v[94:95],v3 offset:20024
v_exp_f16_sdwa v120,v121 dst_sel:WORD_1 dst_unused:UNUSED_PRESERVE src0_sel:WORD_1
v_mfma_f32_16x16x32_bf16 v[156:159],v[68:71],v[44:47],v[116:119]
ds_read_u16_d16_hi v135,v135
v_exp_f16_sdwa v122,v123 dst_sel:WORD_1 dst_unused:UNUSED_PRESERVE src0_sel:WORD_1
v_mfma_f32_16x16x32_bf16 v[148:151],v[68:71],v[40:43],v[116:119]
ds_read_b64_tr_b16 v[96:97],v3 offset:18544
v_pk_fma_f16 v120,v120,s55,s55
v_pk_fma_f16 v122,v122,s55,s55
v_mfma_f32_16x16x32_bf16 v[144:147],v[72:75],v[48:51],v[144:147]
ds_read_u16 v136,v136
v_rcp_f16 v121,v120
v_mfma_f32_16x16x32_bf16 v[152:155],v[72:75],v[52:55],v[152:155]
ds_read_b64_tr_b16 v[98:99],v3 offset:20080
ds_read_b64_tr_b16 v[100:101],v3 offset:18552
v_rcp_f16 v123,v122
v_mfma_f32_16x16x32_bf16 v[156:159],v[76:79],v[52:55],v[156:159]
ds_read_u16_d16_hi v138,v138
v_rcp_f16_sdwa v121,v120 dst_sel:WORD_1 dst_unused:UNUSED_PRESERVE src0_sel:WORD_1
v_mfma_f32_16x16x32_bf16 v[148:151],v[76:79],v[48:51],v[148:151]
ds_read_b64_tr_b16 v[102:103],v3 offset:20088
ds_read_b64_tr_b16 v[104:105],v3 offset:18608
v_rcp_f16_sdwa v123,v122 dst_sel:WORD_1 dst_unused:UNUSED_PRESERVE src0_sel:WORD_1
v_mfma_f32_16x16x32_bf16 v[144:147],v[80:83],v[56:59],v[144:147]
ds_read_u16 v137,v137
v_fma_mix_f32 v140,v140,v121,0 op_sel_hi:[0,1,0]
v_fma_mix_f32 v142,v142,v121,0 op_sel:[0,1,0] op_sel_hi:[0,1,0]
v_mfma_f32_16x16x32_bf16 v[152:155],v[80:83],v[60:63],v[152:155]
s_waitcnt lgkmcnt(8)
ds_read_b64_tr_b16 v[106:107],v3 offset:20144
ds_read_b64_tr_b16 v[108:109],v3 offset:18616
v_lshl_add_u64 v[128:129],v[128:129],0,v[130:131]
v_lshl_add_u64 v[130:131],v[132:133],0,v[134:135]
v_mfma_f32_16x16x32_bf16 v[156:159],v[84:87],v[60:63],v[156:159]
ds_read_u16_d16_hi v139,v139
v_fma_mix_f32 v141,v141,v123,0 op_sel_hi:[0,1,0]
v_fma_mix_f32 v143,v143,v123,0 op_sel:[0,1,0] op_sel_hi:[0,1,0]
v_mfma_f32_16x16x32_bf16 v[148:151],v[84:87],v[56:59],v[148:151]
ds_read_b64_tr_b16 v[110:111],v3 offset:20152
ds_read_b64_tr_b16 v[64:65],v1 offset:12832
v_lshlrev_b32_sdwa v144,1,v144 dst_sel:DWORD dst_unused:UNUSED_PAD src0_sel:DWORD src1_sel:WORD_1
v_lshlrev_b32_sdwa v146,1,v146 dst_sel:DWORD dst_unused:UNUSED_PAD src0_sel:DWORD src1_sel:WORD_1
v_mfma_f32_16x16x32_bf16 v[160:163],v[88:91],v[128:131],v[160:163]
ds_read_u16 v144,v144
v_lshlrev_b32_sdwa v145,1,v145 dst_sel:DWORD dst_unused:UNUSED_PAD src0_sel:DWORD src1_sel:WORD_1
v_lshlrev_b32_sdwa v147,1,v147 dst_sel:DWORD dst_unused:UNUSED_PAD src0_sel:DWORD src1_sel:WORD_1
v_mfma_f32_16x16x32_bf16 v[168:171],v[92:95],v[128:131],v[168:171]
s_waitcnt lgkmcnt(11)
ds_read_b64_tr_b16 v[66:67],v1 offset:14880
ds_read_b128 v[112:115],v4 offset:640
v_fma_mixlo_f16 v120,v156,s99,0
v_fma_mixlo_f16 v122,v157,s99,0
v_mfma_f32_16x16x32_bf16 v[176:179],v[96:99],v[128:131],v[176:179]
s_waitcnt lgkmcnt(10)
ds_read_u16_d16_hi v146,v146
v_lshlrev_b32_sdwa v148,1,v148 dst_sel:DWORD dst_unused:UNUSED_PAD src0_sel:DWORD src1_sel:WORD_1
v_lshlrev_b32_sdwa v150,1,v150 dst_sel:DWORD dst_unused:UNUSED_PAD src0_sel:DWORD src1_sel:WORD_1
v_mfma_f32_16x16x32_bf16 v[184:187],v[100:103],v[128:131],v[184:187]
s_waitcnt lgkmcnt(6)
ds_read_b64_tr_b16 v[68:69],v2 offset:12832
ds_read_b64_tr_b16 v[70:71],v2 offset:14880
v_lshl_add_u64 v[136:137],v[136:137],0,v[138:139]
v_mfma_f32_16x16x32_bf16 v[192:195],v[104:107],v[128:131],v[192:195]
s_waitcnt lgkmcnt(7)
ds_read_u16 v145,v145
v_cvt_pk_bf16_f32 v138,v140,v142
v_cvt_pk_bf16_f32 v139,v141,v143
v_mfma_f32_16x16x32_bf16 v[200:203],v[108:111],v[128:131],v[200:203]
ds_read_b128 v[116:119],v4 offset:672
ds_read_b64_tr_b16 v[72:73],v1 offset:16928
v_fma_mixhi_f16 v120,v158,s99,0
v_fma_mixhi_f16 v122,v159,s99,0
v_mfma_f32_16x16x32_bf16 v[164:167],v[88:91],v[136:139],v[164:167]
ds_read_u16_d16_hi v147,v147
v_lshlrev_b32_sdwa v149,1,v149 dst_sel:DWORD dst_unused:UNUSED_PAD src0_sel:DWORD src1_sel:WORD_1
v_lshlrev_b32_sdwa v151,1,v151 dst_sel:DWORD dst_unused:UNUSED_PAD src0_sel:DWORD src1_sel:WORD_1
v_mfma_f32_16x16x32_bf16 v[172:175],v[92:95],v[136:139],v[172:175]
s_setprio 1
ds_read_b64_tr_b16 v[74:75],v1 offset:18976
ds_read_b64_tr_b16 v[76:77],v2 offset:16928
v_pk_fma_f16 v121,v120,v120,s54
v_pk_fma_f16 v123,v122,v122,s54
v_mfma_f32_16x16x32_bf16 v[180:183],v[96:99],v[136:139],v[180:183]
ds_read_u16 v148,v148
v_pk_mul_f16 v121,v120,v121
v_pk_mul_f16 v123,v122,v123
v_mfma_f32_16x16x32_bf16 v[188:191],v[100:103],v[136:139],v[188:191]
ds_read_b64_tr_b16 v[78:79],v2 offset:18976
v_lshlrev_b32_sdwa v152,1,v152 dst_sel:DWORD dst_unused:UNUSED_PAD src0_sel:DWORD src1_sel:WORD_1
v_lshlrev_b32_sdwa v154,1,v154 dst_sel:DWORD dst_unused:UNUSED_PAD src0_sel:DWORD src1_sel:WORD_1
v_mfma_f32_16x16x32_bf16 v[196:199],v[104:107],v[136:139],v[196:199]
ds_read_u16_d16_hi v150,v150
v_exp_f16 v120,v121
v_mfma_f32_16x16x32_bf16 v[204:207],v[108:111],v[136:139],v[204:207]
s_waitcnt lgkmcnt(12)
ds_read_b64_tr_b16 v[80:81],v1 offset:21024
v_exp_f16 v122,v123
v_mfma_f32_16x16x32_bf16 v[128:131],v[64:67],v[16:19],v[112:115]
ds_read_u16 v149,v149
v_lshlrev_b32_sdwa v153,1,v153 dst_sel:DWORD dst_unused:UNUSED_PAD src0_sel:DWORD src1_sel:WORD_1
v_lshlrev_b32_sdwa v155,1,v155 dst_sel:DWORD dst_unused:UNUSED_PAD src0_sel:DWORD src1_sel:WORD_1
v_mfma_f32_16x16x32_bf16 v[136:139],v[64:67],v[20:23],v[112:115]
s_waitcnt lgkmcnt(9)
ds_read_b64_tr_b16 v[82:83],v1 offset:23072
v_exp_f16_sdwa v120,v121 dst_sel:WORD_1 dst_unused:UNUSED_PRESERVE src0_sel:WORD_1
v_mfma_f32_16x16x32_bf16 v[140:143],v[68:71],v[20:23],v[116:119]
ds_read_u16_d16_hi v151,v151
v_exp_f16_sdwa v122,v123 dst_sel:WORD_1 dst_unused:UNUSED_PRESERVE src0_sel:WORD_1
v_mfma_f32_16x16x32_bf16 v[132:135],v[68:71],v[16:19],v[116:119]
s_waitcnt lgkmcnt(8)
ds_read_b64_tr_b16 v[84:85],v2 offset:21024
v_pk_fma_f16 v120,v120,s55,s55
v_pk_fma_f16 v122,v122,s55,s55
v_mfma_f32_16x16x32_bf16 v[128:131],v[72:75],v[24:27],v[128:131]
ds_read_u16 v152,v152
v_rcp_f16 v121,v120
v_mfma_f32_16x16x32_bf16 v[136:139],v[72:75],v[28:31],v[136:139]
s_waitcnt lgkmcnt(7)
ds_read_b64_tr_b16 v[86:87],v2 offset:23072
v_rcp_f16 v123,v122
v_mfma_f32_16x16x32_bf16 v[140:143],v[76:79],v[28:31],v[140:143]
ds_read_u16_d16_hi v154,v154
v_rcp_f16_sdwa v121,v120 dst_sel:WORD_1 dst_unused:UNUSED_PRESERVE src0_sel:WORD_1
v_mfma_f32_16x16x32_bf16 v[132:135],v[76:79],v[24:27],v[132:135]
s_waitcnt lgkmcnt(5)
v_rcp_f16_sdwa v123,v122 dst_sel:WORD_1 dst_unused:UNUSED_PRESERVE src0_sel:WORD_1
v_mfma_f32_16x16x32_bf16 v[128:131],v[80:83],v[32:35],v[128:131]
ds_read_u16 v153,v153
v_fma_mix_f32 v156,v156,v121,0 op_sel_hi:[0,1,0]
v_fma_mix_f32 v158,v158,v121,0 op_sel:[0,1,0] op_sel_hi:[0,1,0]
v_mfma_f32_16x16x32_bf16 v[136:139],v[80:83],v[36:39],v[136:139]
s_waitcnt lgkmcnt(2)
s_setprio 0
v_lshl_add_u64 v[144:145],v[144:145],0,v[146:147]
v_lshl_add_u64 v[146:147],v[148:149],0,v[150:151]
v_mfma_f32_16x16x32_bf16 v[140:143],v[84:87],v[36:39],v[140:143]
ds_read_u16_d16_hi v155,v155
v_fma_mix_f32 v157,v157,v123,0 op_sel_hi:[0,1,0]
v_fma_mix_f32 v159,v159,v123,0 op_sel:[0,1,0] op_sel_hi:[0,1,0]
v_mfma_f32_16x16x32_bf16 v[132:135],v[84:87],v[32:35],v[132:135]
v_lshlrev_b32_sdwa v128,1,v128 dst_sel:DWORD dst_unused:UNUSED_PAD src0_sel:DWORD src1_sel:WORD_1
v_lshlrev_b32_sdwa v130,1,v130 dst_sel:DWORD dst_unused:UNUSED_PAD src0_sel:DWORD src1_sel:WORD_1
v_mfma_f32_16x16x32_bf16 v[208:211],v[88:91],v[144:147],v[208:211]
s_cbranch_vccz RB77_%=
s_waitcnt vmcnt(0)
RB77_%=:
s_barrier
s_setprio 2
ds_read_u16 v128,v128
v_lshlrev_b32_sdwa v129,1,v129 dst_sel:DWORD dst_unused:UNUSED_PAD src0_sel:DWORD src1_sel:WORD_1
v_mfma_f32_16x16x32_bf16 v[216:219],v[92:95],v[144:147],v[216:219]
s_cbranch_vccnz RB78_%=
s_mov_b64 exec,s[62:63]
s_add_u32 m0,s57,41856
s_add_u32 s64,s56,5111808
buffer_load_dwordx4 v5,s[36:39],s64 offen lds
s_add_u32 m0,s57,43584
s_nop 0
buffer_load_dwordx4 v6,s[36:39],s64 offen lds
s_mov_b64 exec,-1
RB78_%=:
ds_read_u16_d16_hi v130,v130
v_lshlrev_b32_sdwa v131,1,v131 dst_sel:DWORD dst_unused:UNUSED_PAD src0_sel:DWORD src1_sel:WORD_1
v_mfma_f32_16x16x32_bf16 v[224:227],v[96:99],v[144:147],v[224:227]
s_cbranch_vccz RB79_%=
s_add_u32 m0,s58,12320
s_mul_i32 s69,s59,7
s_add_u32 s69,s69,s97
buffer_load_dwordx4 v5,s[44:47],s69 offen lds
s_add_u32 m0,m0,1024
s_add_u32 s69,s69,s53
buffer_load_dwordx4 v6,s[44:47],s69 offen lds
RB79_%=:
v_lshlrev_b32_sdwa v132,1,v132 dst_sel:DWORD dst_unused:UNUSED_PAD src0_sel:DWORD src1_sel:WORD_1
v_mfma_f32_16x16x32_bf16 v[232:235],v[100:103],v[144:147],v[232:235]
s_waitcnt lgkmcnt(2)
ds_read_u16 v129,v129
v_lshl_add_u64 v[152:153],v[152:153],0,v[154:155]
v_lshlrev_b32_sdwa v134,1,v134 dst_sel:DWORD dst_unused:UNUSED_PAD src0_sel:DWORD src1_sel:WORD_1
v_mfma_f32_16x16x32_bf16 v[240:243],v[104:107],v[144:147],v[240:243]
ds_read_u16_d16_hi v131,v131
v_cvt_pk_bf16_f32 v154,v156,v158
v_cvt_pk_bf16_f32 v155,v157,v159
v_mfma_f32_16x16x32_bf16 v[248:251],v[108:111],v[144:147],v[248:251]
v_lshlrev_b32_sdwa v133,1,v133 dst_sel:DWORD dst_unused:UNUSED_PAD src0_sel:DWORD src1_sel:WORD_1
v_mfma_f32_16x16x32_bf16 v[212:215],v[88:91],v[152:155],v[212:215]
s_cbranch_vccz RB80_%=
s_add_u32 m0,m0,1024
s_add_u32 s69,s69,s53
buffer_load_dwordx4 v5,s[44:47],s69 offen lds
s_add_u32 m0,m0,s50
s_add_u32 s69,s69,s53
buffer_load_dwordx4 v6,s[44:47],s69 offen lds
RB80_%=:
ds_read_u16 v132,v132
v_lshlrev_b32_sdwa v135,1,v135 dst_sel:DWORD dst_unused:UNUSED_PAD src0_sel:DWORD src1_sel:WORD_1
v_mfma_f32_16x16x32_bf16 v[220:223],v[92:95],v[152:155],v[220:223]
ds_read_u16_d16_hi v134,v134
v_lshlrev_b32_sdwa v136,1,v136 dst_sel:DWORD dst_unused:UNUSED_PAD src0_sel:DWORD src1_sel:WORD_1
v_mfma_f32_16x16x32_bf16 v[228:231],v[96:99],v[152:155],v[228:231]
ds_read_b64_tr_b16 v[88:89],v3 offset:0
ds_read_b64_tr_b16 v[90:91],v3 offset:1536
v_lshlrev_b32_sdwa v138,1,v138 dst_sel:DWORD dst_unused:UNUSED_PAD src0_sel:DWORD src1_sel:WORD_1
v_mfma_f32_16x16x32_bf16 v[236:239],v[100:103],v[152:155],v[236:239]
ds_read_u16 v133,v133
v_lshlrev_b32_sdwa v137,1,v137 dst_sel:DWORD dst_unused:UNUSED_PAD src0_sel:DWORD src1_sel:WORD_1
v_mfma_f32_16x16x32_bf16 v[244:247],v[104:107],v[152:155],v[244:247]
ds_read_u16_d16_hi v135,v135
v_lshlrev_b32_sdwa v139,1,v139 dst_sel:DWORD dst_unused:UNUSED_PAD src0_sel:DWORD src1_sel:WORD_1
v_mfma_f32_16x16x32_bf16 v[252:255],v[108:111],v[152:155],v[252:255]
s_cbranch_vccz RB81_%=
s_add_u32 m0,m0,1024
s_add_u32 s69,s69,s53
buffer_load_dwordx4 v5,s[44:47],s69 offen lds
s_add_u32 m0,m0,1024
s_add_u32 s69,s69,s53
buffer_load_dwordx4 v6,s[44:47],s69 offen lds
RB81_%=:
ds_read_b64_tr_b16 v[92:93],v3 offset:8
ds_read_b64_tr_b16 v[94:95],v3 offset:1544
v_lshlrev_b32_sdwa v140,1,v140 dst_sel:DWORD dst_unused:UNUSED_PAD src0_sel:DWORD src1_sel:WORD_1
v_mfma_f32_16x16x32_bf16 v[144:147],v[64:67],v[40:43],v[112:115]
ds_read_u16 v136,v136
v_lshlrev_b32_sdwa v142,1,v142 dst_sel:DWORD dst_unused:UNUSED_PAD src0_sel:DWORD src1_sel:WORD_1
v_mfma_f32_16x16x32_bf16 v[152:155],v[64:67],v[44:47],v[112:115]
ds_read_u16_d16_hi v138,v138
v_lshlrev_b32_sdwa v141,1,v141 dst_sel:DWORD dst_unused:UNUSED_PAD src0_sel:DWORD src1_sel:WORD_1
v_mfma_f32_16x16x32_bf16 v[156:159],v[68:71],v[44:47],v[116:119]
ds_read_b64_tr_b16 v[96:97],v3 offset:64
ds_read_b64_tr_b16 v[98:99],v3 offset:1600
v_lshlrev_b32_sdwa v143,1,v143 dst_sel:DWORD dst_unused:UNUSED_PAD src0_sel:DWORD src1_sel:WORD_1
v_mfma_f32_16x16x32_bf16 v[148:151],v[68:71],v[40:43],v[116:119]
ds_read_u16 v137,v137
v_mfma_f32_16x16x32_bf16 v[144:147],v[72:75],v[48:51],v[144:147]
ds_read_u16_d16_hi v139,v139
v_mfma_f32_16x16x32_bf16 v[152:155],v[72:75],v[52:55],v[152:155]
s_cbranch_vccnz RB82_%=
s_mov_b64 exec,s[62:63]
s_add_u32 m0,s57,45344
s_add_u32 s64,s56,5210112
buffer_load_dwordx4 v5,s[36:39],s64 offen lds
s_add_u32 m0,s57,47072
s_nop 0
buffer_load_dwordx4 v6,s[36:39],s64 offen lds
s_mov_b64 exec,-1
RB82_%=:
ds_read_b64_tr_b16 v[100:101],v3 offset:72
ds_read_b64_tr_b16 v[102:103],v3 offset:1608
v_mfma_f32_16x16x32_bf16 v[156:159],v[76:79],v[52:55],v[156:159]
s_cbranch_vccz RB83_%=
s_add_u32 m0,m0,s50
s_add_u32 s69,s69,s53
buffer_load_dwordx4 v5,s[44:47],s69 offen lds
s_add_u32 m0,m0,1024
s_add_u32 s69,s69,s53
buffer_load_dwordx4 v6,s[44:47],s69 offen lds
RB83_%=:
ds_read_u16 v140,v140
v_mfma_f32_16x16x32_bf16 v[148:151],v[76:79],v[48:51],v[148:151]
s_waitcnt lgkmcnt(15)
ds_read_u16_d16_hi v142,v142
v_lshl_add_u64 v[128:129],v[128:129],0,v[130:131]
v_mfma_f32_16x16x32_bf16 v[144:147],v[80:83],v[56:59],v[144:147]
s_waitcnt lgkmcnt(12)
ds_read_b64_tr_b16 v[104:105],v3 offset:128
ds_read_b64_tr_b16 v[106:107],v3 offset:1664
v_lshl_add_u64 v[130:131],v[132:133],0,v[134:135]
v_mfma_f32_16x16x32_bf16 v[152:155],v[80:83],v[60:63],v[152:155]
ds_read_u16 v141,v141
v_mfma_f32_16x16x32_bf16 v[156:159],v[84:87],v[60:63],v[156:159]
ds_read_u16_d16_hi v143,v143
v_mfma_f32_16x16x32_bf16 v[148:151],v[84:87],v[56:59],v[148:151]
s_cbranch_vccz RB84_%=
s_add_u32 m0,m0,1024
s_add_u32 s69,s69,s53
buffer_load_dwordx4 v5,s[44:47],s69 offen lds
s_add_u32 m0,m0,s50
s_add_u32 s69,s69,s53
buffer_load_dwordx4 v6,s[44:47],s69 offen lds
RB84_%=:
ds_read_b64_tr_b16 v[108:109],v3 offset:136
ds_read_b64_tr_b16 v[110:111],v3 offset:1672
v_lshlrev_b32_sdwa v144,1,v144 dst_sel:DWORD dst_unused:UNUSED_PAD src0_sel:DWORD src1_sel:WORD_1
v_lshlrev_b32_sdwa v146,1,v146 dst_sel:DWORD dst_unused:UNUSED_PAD src0_sel:DWORD src1_sel:WORD_1
v_mfma_f32_16x16x32_bf16 v[160:163],v[88:91],v[128:131],v[160:163]
s_waitcnt lgkmcnt(15)
ds_read_u16 v144,v144
v_lshlrev_b32_sdwa v145,1,v145 dst_sel:DWORD dst_unused:UNUSED_PAD src0_sel:DWORD src1_sel:WORD_1
v_mfma_f32_16x16x32_bf16 v[168:171],v[92:95],v[128:131],v[168:171]
s_waitcnt lgkmcnt(13)
ds_read_u16_d16_hi v146,v146
v_lshlrev_b32_sdwa v147,1,v147 dst_sel:DWORD dst_unused:UNUSED_PAD src0_sel:DWORD src1_sel:WORD_1
v_mfma_f32_16x16x32_bf16 v[176:179],v[96:99],v[128:131],v[176:179]
s_waitcnt lgkmcnt(10)
ds_read_b64_tr_b16 v[64:65],v1 offset:0
ds_read_b64_tr_b16 v[66:67],v1 offset:2048
v_lshlrev_b32_sdwa v148,1,v148 dst_sel:DWORD dst_unused:UNUSED_PAD src0_sel:DWORD src1_sel:WORD_1
v_mfma_f32_16x16x32_bf16 v[184:187],v[100:103],v[128:131],v[184:187]
s_waitcnt lgkmcnt(8)
ds_read_u16 v145,v145
v_lshl_add_u64 v[136:137],v[136:137],0,v[138:139]
v_lshlrev_b32_sdwa v150,1,v150 dst_sel:DWORD dst_unused:UNUSED_PAD src0_sel:DWORD src1_sel:WORD_1
v_mfma_f32_16x16x32_bf16 v[192:195],v[104:107],v[128:131],v[192:195]
s_waitcnt lgkmcnt(5)
s_cbranch_vccz RB85_%=
s_add_u32 m0,m0,1024
s_add_u32 s69,s69,s53
buffer_load_dwordx4 v5,s[44:47],s69 offen lds
s_add_u32 m0,m0,1024
s_add_u32 s69,s69,s53
buffer_load_dwordx4 v6,s[44:47],s69 offen lds
RB85_%=:
ds_read_u16_d16_hi v147,v147
v_lshl_add_u64 v[138:139],v[140:141],0,v[142:143]
v_mfma_f32_16x16x32_bf16 v[200:203],v[108:111],v[128:131],v[200:203]
ds_read_b128 v[112:115],v4 offset:768
ds_read_b64_tr_b16 v[68:69],v2 offset:0
v_lshlrev_b32_sdwa v149,1,v149 dst_sel:DWORD dst_unused:UNUSED_PAD src0_sel:DWORD src1_sel:WORD_1
v_mfma_f32_16x16x32_bf16 v[164:167],v[88:91],v[136:139],v[164:167]
ds_read_u16 v148,v148
v_lshlrev_b32_sdwa v151,1,v151 dst_sel:DWORD dst_unused:UNUSED_PAD src0_sel:DWORD src1_sel:WORD_1
v_mfma_f32_16x16x32_bf16 v[172:175],v[92:95],v[136:139],v[172:175]
ds_read_u16_d16_hi v150,v150
ds_read_b64_tr_b16 v[70:71],v2 offset:2048
v_lshlrev_b32_sdwa v152,1,v152 dst_sel:DWORD dst_unused:UNUSED_PAD src0_sel:DWORD src1_sel:WORD_1
v_mfma_f32_16x16x32_bf16 v[180:183],v[96:99],v[136:139],v[180:183]
ds_read_b128 v[116:119],v4 offset:800
ds_read_b64_tr_b16 v[72:73],v1 offset:4096
v_lshlrev_b32_sdwa v154,1,v154 dst_sel:DWORD dst_unused:UNUSED_PAD src0_sel:DWORD src1_sel:WORD_1
v_mfma_f32_16x16x32_bf16 v[188:191],v[100:103],v[136:139],v[188:191]
s_cbranch_vccnz RB86_%=
s_mov_b64 exec,s[62:63]
s_add_u32 m0,s57,48832
s_add_u32 s64,s56,5308416
buffer_load_dwordx4 v5,s[36:39],s64 offen lds
s_add_u32 m0,s57,50560
s_nop 0
buffer_load_dwordx4 v6,s[36:39],s64 offen lds
s_mov_b64 exec,-1
RB86_%=:
ds_read_u16 v149,v149
v_lshlrev_b32_sdwa v153,1,v153 dst_sel:DWORD dst_unused:UNUSED_PAD src0_sel:DWORD src1_sel:WORD_1
v_mfma_f32_16x16x32_bf16 v[196:199],v[104:107],v[136:139],v[196:199]
ds_read_u16_d16_hi v151,v151
v_lshlrev_b32_sdwa v155,1,v155 dst_sel:DWORD dst_unused:UNUSED_PAD src0_sel:DWORD src1_sel:WORD_1
v_mfma_f32_16x16x32_bf16 v[204:207],v[108:111],v[136:139],v[204:207]
s_waitcnt lgkmcnt(8)
ds_read_b64_tr_b16 v[74:75],v1 offset:6144
ds_read_b64_tr_b16 v[76:77],v2 offset:4096
v_lshlrev_b32_sdwa v156,1,v156 dst_sel:DWORD dst_unused:UNUSED_PAD src0_sel:DWORD src1_sel:WORD_1
v_mfma_f32_16x16x32_bf16 v[128:131],v[64:67],v[16:19],v[112:115]
ds_read_u16 v152,v152
v_lshlrev_b32_sdwa v158,1,v158 dst_sel:DWORD dst_unused:UNUSED_PAD src0_sel:DWORD src1_sel:WORD_1
v_mfma_f32_16x16x32_bf16 v[136:139],v[64:67],v[20:23],v[112:115]
s_waitcnt lgkmcnt(6)
ds_read_u16_d16_hi v154,v154
ds_read_b64_tr_b16 v[78:79],v2 offset:6144
v_lshlrev_b32_sdwa v157,1,v157 dst_sel:DWORD dst_unused:UNUSED_PAD src0_sel:DWORD src1_sel:WORD_1
v_mfma_f32_16x16x32_bf16 v[140:143],v[68:71],v[20:23],v[116:119]
ds_read_b64_tr_b16 v[80:81],v1 offset:8192
ds_read_b64_tr_b16 v[82:83],v1 offset:10240
v_lshlrev_b32_sdwa v159,1,v159 dst_sel:DWORD dst_unused:UNUSED_PAD src0_sel:DWORD src1_sel:WORD_1
v_mfma_f32_16x16x32_bf16 v[132:135],v[68:71],v[16:19],v[116:119]
s_waitcnt lgkmcnt(6)
ds_read_u16 v153,v153
v_mfma_f32_16x16x32_bf16 v[128:131],v[72:75],v[24:27],v[128:131]
ds_read_u16_d16_hi v155,v155
v_mfma_f32_16x16x32_bf16 v[136:139],v[72:75],v[28:31],v[136:139]
s_waitcnt lgkmcnt(4)
ds_read_b64_tr_b16 v[84:85],v2 offset:8192
ds_read_b64_tr_b16 v[86:87],v2 offset:10240
v_mfma_f32_16x16x32_bf16 v[140:143],v[76:79],v[28:31],v[140:143]
ds_read_u16 v156,v156
v_mfma_f32_16x16x32_bf16 v[132:135],v[76:79],v[24:27],v[132:135]
s_waitcnt lgkmcnt(5)
ds_read_u16_d16_hi v158,v158
v_lshl_add_u64 v[144:145],v[144:145],0,v[146:147]
v_mfma_f32_16x16x32_bf16 v[128:131],v[80:83],v[32:35],v[128:131]
v_lshl_add_u64 v[146:147],v[148:149],0,v[150:151]
v_mfma_f32_16x16x32_bf16 v[136:139],v[80:83],v[36:39],v[136:139]
s_waitcnt lgkmcnt(2)
ds_read_u16 v157,v157
v_mfma_f32_16x16x32_bf16 v[140:143],v[84:87],v[36:39],v[140:143]
ds_read_u16_d16_hi v159,v159
v_mfma_f32_16x16x32_bf16 v[132:135],v[84:87],v[32:35],v[132:135]
s_nop 0
v_lshlrev_b32_sdwa v128,1,v128 dst_sel:DWORD dst_unused:UNUSED_PAD src0_sel:DWORD src1_sel:WORD_1
v_lshlrev_b32_sdwa v130,1,v130 dst_sel:DWORD dst_unused:UNUSED_PAD src0_sel:DWORD src1_sel:WORD_1
v_mfma_f32_16x16x32_bf16 v[208:211],v[88:91],v[144:147],v[208:211]
ds_read_u16 v128,v128
v_lshlrev_b32_sdwa v129,1,v129 dst_sel:DWORD dst_unused:UNUSED_PAD src0_sel:DWORD src1_sel:WORD_1
v_mfma_f32_16x16x32_bf16 v[216:219],v[92:95],v[144:147],v[216:219]
ds_read_u16_d16_hi v130,v130
v_lshlrev_b32_sdwa v131,1,v131 dst_sel:DWORD dst_unused:UNUSED_PAD src0_sel:DWORD src1_sel:WORD_1
v_mfma_f32_16x16x32_bf16 v[224:227],v[96:99],v[144:147],v[224:227]
v_lshlrev_b32_sdwa v132,1,v132 dst_sel:DWORD dst_unused:UNUSED_PAD src0_sel:DWORD src1_sel:WORD_1
v_mfma_f32_16x16x32_bf16 v[232:235],v[100:103],v[144:147],v[232:235]
s_cbranch_vccnz RB87_%=
s_mov_b64 exec,s[62:63]
s_add_u32 m0,s57,52320
s_add_u32 s64,s56,5406720
buffer_load_dwordx4 v5,s[36:39],s64 offen lds
s_add_u32 m0,s57,54048
s_nop 0
buffer_load_dwordx4 v6,s[36:39],s64 offen lds
s_mov_b64 exec,-1
RB87_%=:
ds_read_u16 v129,v129
v_lshl_add_u64 v[152:153],v[152:153],0,v[154:155]
v_lshlrev_b32_sdwa v134,1,v134 dst_sel:DWORD dst_unused:UNUSED_PAD src0_sel:DWORD src1_sel:WORD_1
v_mfma_f32_16x16x32_bf16 v[240:243],v[104:107],v[144:147],v[240:243]
s_waitcnt lgkmcnt(3)
ds_read_u16_d16_hi v131,v131
v_lshl_add_u64 v[154:155],v[156:157],0,v[158:159]
v_mfma_f32_16x16x32_bf16 v[248:251],v[108:111],v[144:147],v[248:251]
v_lshlrev_b32_sdwa v133,1,v133 dst_sel:DWORD dst_unused:UNUSED_PAD src0_sel:DWORD src1_sel:WORD_1
v_mfma_f32_16x16x32_bf16 v[212:215],v[88:91],v[152:155],v[212:215]
ds_read_u16 v132,v132
v_lshlrev_b32_sdwa v135,1,v135 dst_sel:DWORD dst_unused:UNUSED_PAD src0_sel:DWORD src1_sel:WORD_1
v_mfma_f32_16x16x32_bf16 v[220:223],v[92:95],v[152:155],v[220:223]
ds_read_u16_d16_hi v134,v134
v_lshlrev_b32_sdwa v136,1,v136 dst_sel:DWORD dst_unused:UNUSED_PAD src0_sel:DWORD src1_sel:WORD_1
v_mfma_f32_16x16x32_bf16 v[228:231],v[96:99],v[152:155],v[228:231]
ds_read_b64_tr_b16 v[88:89],v3 offset:6160
ds_read_b64_tr_b16 v[90:91],v3 offset:7696
v_lshlrev_b32_sdwa v138,1,v138 dst_sel:DWORD dst_unused:UNUSED_PAD src0_sel:DWORD src1_sel:WORD_1
v_mfma_f32_16x16x32_bf16 v[236:239],v[100:103],v[152:155],v[236:239]
ds_read_u16 v133,v133
v_lshlrev_b32_sdwa v137,1,v137 dst_sel:DWORD dst_unused:UNUSED_PAD src0_sel:DWORD src1_sel:WORD_1
v_mfma_f32_16x16x32_bf16 v[244:247],v[104:107],v[152:155],v[244:247]
ds_read_u16_d16_hi v135,v135
v_lshlrev_b32_sdwa v139,1,v139 dst_sel:DWORD dst_unused:UNUSED_PAD src0_sel:DWORD src1_sel:WORD_1
v_mfma_f32_16x16x32_bf16 v[252:255],v[108:111],v[152:155],v[252:255]
ds_read_b64_tr_b16 v[92:93],v3 offset:6168
ds_read_b64_tr_b16 v[94:95],v3 offset:7704
v_lshlrev_b32_sdwa v140,1,v140 dst_sel:DWORD dst_unused:UNUSED_PAD src0_sel:DWORD src1_sel:WORD_1
v_mfma_f32_16x16x32_bf16 v[144:147],v[64:67],v[40:43],v[112:115]
ds_read_u16 v136,v136
v_lshlrev_b32_sdwa v142,1,v142 dst_sel:DWORD dst_unused:UNUSED_PAD src0_sel:DWORD src1_sel:WORD_1
v_mfma_f32_16x16x32_bf16 v[152:155],v[64:67],v[44:47],v[112:115]
ds_read_u16_d16_hi v138,v138
v_lshlrev_b32_sdwa v141,1,v141 dst_sel:DWORD dst_unused:UNUSED_PAD src0_sel:DWORD src1_sel:WORD_1
v_mfma_f32_16x16x32_bf16 v[156:159],v[68:71],v[44:47],v[116:119]
ds_read_b64_tr_b16 v[96:97],v3 offset:6224
ds_read_b64_tr_b16 v[98:99],v3 offset:7760
v_lshlrev_b32_sdwa v143,1,v143 dst_sel:DWORD dst_unused:UNUSED_PAD src0_sel:DWORD src1_sel:WORD_1
v_mfma_f32_16x16x32_bf16 v[148:151],v[68:71],v[40:43],v[116:119]
ds_read_u16 v137,v137
v_mfma_f32_16x16x32_bf16 v[144:147],v[72:75],v[48:51],v[144:147]
ds_read_u16_d16_hi v139,v139
v_mfma_f32_16x16x32_bf16 v[152:155],v[72:75],v[52:55],v[152:155]
ds_read_b64_tr_b16 v[100:101],v3 offset:6232
ds_read_b64_tr_b16 v[102:103],v3 offset:7768
v_mfma_f32_16x16x32_bf16 v[156:159],v[76:79],v[52:55],v[156:159]
ds_read_u16 v140,v140
v_mfma_f32_16x16x32_bf16 v[148:151],v[76:79],v[48:51],v[148:151]
s_waitcnt lgkmcnt(15)
ds_read_u16_d16_hi v142,v142
v_lshl_add_u64 v[128:129],v[128:129],0,v[130:131]
v_mfma_f32_16x16x32_bf16 v[144:147],v[80:83],v[56:59],v[144:147]
s_waitcnt lgkmcnt(12)
s_cbranch_vccnz RB88_%=
s_mov_b64 exec,s[62:63]
s_add_u32 m0,s57,55808
s_add_u32 s64,s56,5505024
buffer_load_dwordx4 v5,s[36:39],s64 offen lds
s_add_u32 m0,s57,57536
s_nop 0
buffer_load_dwordx4 v6,s[36:39],s64 offen lds
s_mov_b64 exec,-1
RB88_%=:
ds_read_b64_tr_b16 v[104:105],v3 offset:6288
ds_read_b64_tr_b16 v[106:107],v3 offset:7824
v_lshl_add_u64 v[130:131],v[132:133],0,v[134:135]
v_mfma_f32_16x16x32_bf16 v[152:155],v[80:83],v[60:63],v[152:155]
ds_read_u16 v141,v141
v_mfma_f32_16x16x32_bf16 v[156:159],v[84:87],v[60:63],v[156:159]
ds_read_u16_d16_hi v143,v143
v_mfma_f32_16x16x32_bf16 v[148:151],v[84:87],v[56:59],v[148:151]
ds_read_b64_tr_b16 v[108:109],v3 offset:6296
ds_read_b64_tr_b16 v[110:111],v3 offset:7832
v_lshlrev_b32_sdwa v144,1,v144 dst_sel:DWORD dst_unused:UNUSED_PAD src0_sel:DWORD src1_sel:WORD_1
v_lshlrev_b32_sdwa v146,1,v146 dst_sel:DWORD dst_unused:UNUSED_PAD src0_sel:DWORD src1_sel:WORD_1
v_mfma_f32_16x16x32_bf16 v[160:163],v[88:91],v[128:131],v[160:163]
s_waitcnt lgkmcnt(15)
ds_read_u16 v144,v144
v_lshlrev_b32_sdwa v145,1,v145 dst_sel:DWORD dst_unused:UNUSED_PAD src0_sel:DWORD src1_sel:WORD_1
v_mfma_f32_16x16x32_bf16 v[168:171],v[92:95],v[128:131],v[168:171]
s_waitcnt lgkmcnt(13)
ds_read_u16_d16_hi v146,v146
v_lshlrev_b32_sdwa v147,1,v147 dst_sel:DWORD dst_unused:UNUSED_PAD src0_sel:DWORD src1_sel:WORD_1
v_mfma_f32_16x16x32_bf16 v[176:179],v[96:99],v[128:131],v[176:179]
s_waitcnt lgkmcnt(10)
ds_read_b64_tr_b16 v[64:65],v1 offset:512
ds_read_b64_tr_b16 v[66:67],v1 offset:2560
v_lshlrev_b32_sdwa v148,1,v148 dst_sel:DWORD dst_unused:UNUSED_PAD src0_sel:DWORD src1_sel:WORD_1
v_mfma_f32_16x16x32_bf16 v[184:187],v[100:103],v[128:131],v[184:187]
s_waitcnt lgkmcnt(8)
ds_read_u16 v145,v145
v_lshl_add_u64 v[136:137],v[136:137],0,v[138:139]
v_lshlrev_b32_sdwa v150,1,v150 dst_sel:DWORD dst_unused:UNUSED_PAD src0_sel:DWORD src1_sel:WORD_1
v_mfma_f32_16x16x32_bf16 v[192:195],v[104:107],v[128:131],v[192:195]
s_waitcnt lgkmcnt(5)
ds_read_u16_d16_hi v147,v147
v_lshl_add_u64 v[138:139],v[140:141],0,v[142:143]
v_mfma_f32_16x16x32_bf16 v[200:203],v[108:111],v[128:131],v[200:203]
ds_read_b128 v[112:115],v4 offset:896
ds_read_b64_tr_b16 v[68:69],v2 offset:512
v_lshlrev_b32_sdwa v149,1,v149 dst_sel:DWORD dst_unused:UNUSED_PAD src0_sel:DWORD src1_sel:WORD_1
v_mfma_f32_16x16x32_bf16 v[164:167],v[88:91],v[136:139],v[164:167]
ds_read_u16 v148,v148
v_lshlrev_b32_sdwa v151,1,v151 dst_sel:DWORD dst_unused:UNUSED_PAD src0_sel:DWORD src1_sel:WORD_1
v_mfma_f32_16x16x32_bf16 v[172:175],v[92:95],v[136:139],v[172:175]
ds_read_u16_d16_hi v150,v150
ds_read_b64_tr_b16 v[70:71],v2 offset:2560
v_lshlrev_b32_sdwa v152,1,v152 dst_sel:DWORD dst_unused:UNUSED_PAD src0_sel:DWORD src1_sel:WORD_1
v_mfma_f32_16x16x32_bf16 v[180:183],v[96:99],v[136:139],v[180:183]
ds_read_b128 v[116:119],v4 offset:928
ds_read_b64_tr_b16 v[72:73],v1 offset:4608
v_lshlrev_b32_sdwa v154,1,v154 dst_sel:DWORD dst_unused:UNUSED_PAD src0_sel:DWORD src1_sel:WORD_1
v_mfma_f32_16x16x32_bf16 v[188:191],v[100:103],v[136:139],v[188:191]
ds_read_u16 v149,v149
v_lshlrev_b32_sdwa v153,1,v153 dst_sel:DWORD dst_unused:UNUSED_PAD src0_sel:DWORD src1_sel:WORD_1
v_mfma_f32_16x16x32_bf16 v[196:199],v[104:107],v[136:139],v[196:199]
ds_read_u16_d16_hi v151,v151
v_lshlrev_b32_sdwa v155,1,v155 dst_sel:DWORD dst_unused:UNUSED_PAD src0_sel:DWORD src1_sel:WORD_1
v_mfma_f32_16x16x32_bf16 v[204:207],v[108:111],v[136:139],v[204:207]
s_waitcnt lgkmcnt(8)
s_cbranch_vccnz RB89_%=
s_mov_b64 exec,s[62:63]
s_add_u32 m0,s57,59296
s_add_u32 s64,s56,5603328
buffer_load_dwordx4 v5,s[36:39],s64 offen lds
s_add_u32 m0,s57,61024
s_nop 0
buffer_load_dwordx4 v6,s[36:39],s64 offen lds
s_mov_b64 exec,-1
RB89_%=:
ds_read_b64_tr_b16 v[74:75],v1 offset:6656
ds_read_b64_tr_b16 v[76:77],v2 offset:4608
v_lshlrev_b32_sdwa v156,1,v156 dst_sel:DWORD dst_unused:UNUSED_PAD src0_sel:DWORD src1_sel:WORD_1
v_mfma_f32_16x16x32_bf16 v[128:131],v[64:67],v[16:19],v[112:115]
ds_read_u16 v152,v152
v_lshlrev_b32_sdwa v158,1,v158 dst_sel:DWORD dst_unused:UNUSED_PAD src0_sel:DWORD src1_sel:WORD_1
v_mfma_f32_16x16x32_bf16 v[136:139],v[64:67],v[20:23],v[112:115]
s_waitcnt lgkmcnt(6)
ds_read_u16_d16_hi v154,v154
ds_read_b64_tr_b16 v[78:79],v2 offset:6656
v_lshlrev_b32_sdwa v157,1,v157 dst_sel:DWORD dst_unused:UNUSED_PAD src0_sel:DWORD src1_sel:WORD_1
v_mfma_f32_16x16x32_bf16 v[140:143],v[68:71],v[20:23],v[116:119]
ds_read_b64_tr_b16 v[80:81],v1 offset:8704
ds_read_b64_tr_b16 v[82:83],v1 offset:10752
v_lshlrev_b32_sdwa v159,1,v159 dst_sel:DWORD dst_unused:UNUSED_PAD src0_sel:DWORD src1_sel:WORD_1
v_mfma_f32_16x16x32_bf16 v[132:135],v[68:71],v[16:19],v[116:119]
s_waitcnt lgkmcnt(6)
ds_read_u16 v153,v153
v_mfma_f32_16x16x32_bf16 v[128:131],v[72:75],v[24:27],v[128:131]
ds_read_u16_d16_hi v155,v155
v_mfma_f32_16x16x32_bf16 v[136:139],v[72:75],v[28:31],v[136:139]
s_waitcnt lgkmcnt(4)
ds_read_b64_tr_b16 v[84:85],v2 offset:8704
ds_read_b64_tr_b16 v[86:87],v2 offset:10752
v_mfma_f32_16x16x32_bf16 v[140:143],v[76:79],v[28:31],v[140:143]
ds_read_u16 v156,v156
v_mfma_f32_16x16x32_bf16 v[132:135],v[76:79],v[24:27],v[132:135]
s_waitcnt lgkmcnt(5)
ds_read_u16_d16_hi v158,v158
v_lshl_add_u64 v[144:145],v[144:145],0,v[146:147]
v_mfma_f32_16x16x32_bf16 v[128:131],v[80:83],v[32:35],v[128:131]
v_lshl_add_u64 v[146:147],v[148:149],0,v[150:151]
v_mfma_f32_16x16x32_bf16 v[136:139],v[80:83],v[36:39],v[136:139]
s_waitcnt lgkmcnt(2)
ds_read_u16 v157,v157
v_mfma_f32_16x16x32_bf16 v[140:143],v[84:87],v[36:39],v[140:143]
ds_read_u16_d16_hi v159,v159
v_mfma_f32_16x16x32_bf16 v[132:135],v[84:87],v[32:35],v[132:135]
s_nop 0
v_lshlrev_b32_sdwa v128,1,v128 dst_sel:DWORD dst_unused:UNUSED_PAD src0_sel:DWORD src1_sel:WORD_1
v_lshlrev_b32_sdwa v130,1,v130 dst_sel:DWORD dst_unused:UNUSED_PAD src0_sel:DWORD src1_sel:WORD_1
v_mfma_f32_16x16x32_bf16 v[208:211],v[88:91],v[144:147],v[208:211]
s_cbranch_vccz RB90_%=
s_waitcnt vmcnt(0)
RB90_%=:
s_barrier
s_setprio 3
ds_read_u16 v128,v128
v_lshlrev_b32_sdwa v129,1,v129 dst_sel:DWORD dst_unused:UNUSED_PAD src0_sel:DWORD src1_sel:WORD_1
v_lshlrev_b32_sdwa v131,1,v131 dst_sel:DWORD dst_unused:UNUSED_PAD src0_sel:DWORD src1_sel:WORD_1
v_mfma_f32_16x16x32_bf16 v[216:219],v[92:95],v[144:147],v[216:219]
v_fma_mixlo_f16 v120,v140,s99,0
v_fma_mixlo_f16 v122,v141,s99,0
v_mfma_f32_16x16x32_bf16 v[224:227],v[96:99],v[144:147],v[224:227]
ds_read_u16_d16_hi v130,v130
v_lshlrev_b32_sdwa v132,1,v132 dst_sel:DWORD dst_unused:UNUSED_PAD src0_sel:DWORD src1_sel:WORD_1
v_lshlrev_b32_sdwa v134,1,v134 dst_sel:DWORD dst_unused:UNUSED_PAD src0_sel:DWORD src1_sel:WORD_1
v_mfma_f32_16x16x32_bf16 v[232:235],v[100:103],v[144:147],v[232:235]
v_lshl_add_u64 v[152:153],v[152:153],0,v[154:155]
v_mfma_f32_16x16x32_bf16 v[240:243],v[104:107],v[144:147],v[240:243]
s_waitcnt lgkmcnt(2)
s_cbranch_vccnz RB91_%=
s_mov_b64 exec,s[62:63]
s_add_u32 m0,s57,62784
s_add_u32 s64,s56,5701632
buffer_load_dwordx4 v5,s[36:39],s64 offen lds
s_add_u32 m0,s57,64512
s_nop 0
buffer_load_dwordx4 v6,s[36:39],s64 offen lds
s_mov_b64 exec,-1
RB91_%=:
ds_read_u16 v129,v129
v_lshl_add_u64 v[154:155],v[156:157],0,v[158:159]
v_mfma_f32_16x16x32_bf16 v[248:251],v[108:111],v[144:147],v[248:251]
v_fma_mixhi_f16 v120,v142,s99,0
v_fma_mixhi_f16 v122,v143,s99,0
v_mfma_f32_16x16x32_bf16 v[212:215],v[88:91],v[152:155],v[212:215]
ds_read_u16_d16_hi v131,v131
v_lshlrev_b32_sdwa v133,1,v133 dst_sel:DWORD dst_unused:UNUSED_PAD src0_sel:DWORD src1_sel:WORD_1
v_lshlrev_b32_sdwa v135,1,v135 dst_sel:DWORD dst_unused:UNUSED_PAD src0_sel:DWORD src1_sel:WORD_1
v_mfma_f32_16x16x32_bf16 v[220:223],v[92:95],v[152:155],v[220:223]
ds_read_b64_tr_b16 v[88:89],v3 offset:12320
v_pk_fma_f16 v121,v120,v120,s54
v_pk_fma_f16 v123,v122,v122,s54
v_mfma_f32_16x16x32_bf16 v[228:231],v[96:99],v[152:155],v[228:231]
ds_read_u16 v132,v132
v_pk_mul_f16 v121,v120,v121
v_pk_mul_f16 v123,v122,v123
v_mfma_f32_16x16x32_bf16 v[236:239],v[100:103],v[152:155],v[236:239]
ds_read_b64_tr_b16 v[90:91],v3 offset:13856
v_lshlrev_b32_sdwa v136,1,v136 dst_sel:DWORD dst_unused:UNUSED_PAD src0_sel:DWORD src1_sel:WORD_1
v_lshlrev_b32_sdwa v138,1,v138 dst_sel:DWORD dst_unused:UNUSED_PAD src0_sel:DWORD src1_sel:WORD_1
v_mfma_f32_16x16x32_bf16 v[244:247],v[104:107],v[152:155],v[244:247]
ds_read_u16_d16_hi v134,v134
v_exp_f16 v120,v121
v_mfma_f32_16x16x32_bf16 v[252:255],v[108:111],v[152:155],v[252:255]
ds_read_b64_tr_b16 v[92:93],v3 offset:12328
v_exp_f16 v122,v123
v_mfma_f32_16x16x32_bf16 v[144:147],v[64:67],v[40:43],v[112:115]
ds_read_u16 v133,v133
v_lshlrev_b32_sdwa v137,1,v137 dst_sel:DWORD dst_unused:UNUSED_PAD src0_sel:DWORD src1_sel:WORD_1
v_lshlrev_b32_sdwa v139,1,v139 dst_sel:DWORD dst_unused:UNUSED_PAD src0_sel:DWORD src1_sel:WORD_1
v_mfma_f32_16x16x32_bf16 v[152:155],v[64:67],v[44:47],v[112:115]
ds_read_b64_tr_b16 v[94:95],v3 offset:13864
v_exp_f16_sdwa v120,v121 dst_sel:WORD_1 dst_unused:UNUSED_PRESERVE src0_sel:WORD_1
v_mfma_f32_16x16x32_bf16 v[156:159],v[68:71],v[44:47],v[116:119]
ds_read_u16_d16_hi v135,v135
v_exp_f16_sdwa v122,v123 dst_sel:WORD_1 dst_unused:UNUSED_PRESERVE src0_sel:WORD_1
v_mfma_f32_16x16x32_bf16 v[148:151],v[68:71],v[40:43],v[116:119]
ds_read_b64_tr_b16 v[96:97],v3 offset:12384
v_pk_fma_f16 v120,v120,s55,s55
v_pk_fma_f16 v122,v122,s55,s55
v_mfma_f32_16x16x32_bf16 v[144:147],v[72:75],v[48:51],v[144:147]
s_cbranch_vccnz RB92_%=
s_mov_b64 exec,s[62:63]
s_add_u32 m0,s57,66272
s_add_u32 s64,s56,5799936
buffer_load_dwordx4 v5,s[36:39],s64 offen lds
s_add_u32 m0,s57,68000
s_nop 0
buffer_load_dwordx4 v6,s[36:39],s64 offen lds
s_mov_b64 exec,-1
RB92_%=:
ds_read_u16 v136,v136
v_rcp_f16 v121,v120
v_mfma_f32_16x16x32_bf16 v[152:155],v[72:75],v[52:55],v[152:155]
ds_read_b64_tr_b16 v[98:99],v3 offset:13920
ds_read_b64_tr_b16 v[100:101],v3 offset:12392
v_rcp_f16 v123,v122
v_mfma_f32_16x16x32_bf16 v[156:159],v[76:79],v[52:55],v[156:159]
ds_read_u16_d16_hi v138,v138
v_rcp_f16_sdwa v121,v120 dst_sel:WORD_1 dst_unused:UNUSED_PRESERVE src0_sel:WORD_1
v_mfma_f32_16x16x32_bf16 v[148:151],v[76:79],v[48:51],v[148:151]
ds_read_b64_tr_b16 v[102:103],v3 offset:13928
ds_read_b64_tr_b16 v[104:105],v3 offset:12448
v_rcp_f16_sdwa v123,v122 dst_sel:WORD_1 dst_unused:UNUSED_PRESERVE src0_sel:WORD_1
v_mfma_f32_16x16x32_bf16 v[144:147],v[80:83],v[56:59],v[144:147]
ds_read_u16 v137,v137
v_fma_mix_f32 v140,v140,v121,0 op_sel_hi:[0,1,0]
v_fma_mix_f32 v142,v142,v121,0 op_sel:[0,1,0] op_sel_hi:[0,1,0]
v_mfma_f32_16x16x32_bf16 v[152:155],v[80:83],v[60:63],v[152:155]
s_waitcnt lgkmcnt(8)
ds_read_b64_tr_b16 v[106:107],v3 offset:13984
ds_read_b64_tr_b16 v[108:109],v3 offset:12456
v_lshl_add_u64 v[128:129],v[128:129],0,v[130:131]
v_lshl_add_u64 v[130:131],v[132:133],0,v[134:135]
v_mfma_f32_16x16x32_bf16 v[156:159],v[84:87],v[60:63],v[156:159]
ds_read_u16_d16_hi v139,v139
v_fma_mix_f32 v141,v141,v123,0 op_sel_hi:[0,1,0]
v_fma_mix_f32 v143,v143,v123,0 op_sel:[0,1,0] op_sel_hi:[0,1,0]
v_mfma_f32_16x16x32_bf16 v[148:151],v[84:87],v[56:59],v[148:151]
ds_read_b64_tr_b16 v[110:111],v3 offset:13992
ds_read_b64_tr_b16 v[64:65],v1 offset:12320
v_lshlrev_b32_sdwa v144,1,v144 dst_sel:DWORD dst_unused:UNUSED_PAD src0_sel:DWORD src1_sel:WORD_1
v_lshlrev_b32_sdwa v146,1,v146 dst_sel:DWORD dst_unused:UNUSED_PAD src0_sel:DWORD src1_sel:WORD_1
v_mfma_f32_16x16x32_bf16 v[160:163],v[88:91],v[128:131],v[160:163]
ds_read_u16 v144,v144
v_lshlrev_b32_sdwa v145,1,v145 dst_sel:DWORD dst_unused:UNUSED_PAD src0_sel:DWORD src1_sel:WORD_1
v_lshlrev_b32_sdwa v147,1,v147 dst_sel:DWORD dst_unused:UNUSED_PAD src0_sel:DWORD src1_sel:WORD_1
v_mfma_f32_16x16x32_bf16 v[168:171],v[92:95],v[128:131],v[168:171]
s_waitcnt lgkmcnt(11)
ds_read_b64_tr_b16 v[66:67],v1 offset:14368
ds_read_b128 v[112:115],v4 offset:1024
v_fma_mixlo_f16 v120,v156,s99,0
v_fma_mixlo_f16 v122,v157,s99,0
v_mfma_f32_16x16x32_bf16 v[176:179],v[96:99],v[128:131],v[176:179]
s_waitcnt lgkmcnt(10)
ds_read_u16_d16_hi v146,v146
v_lshlrev_b32_sdwa v148,1,v148 dst_sel:DWORD dst_unused:UNUSED_PAD src0_sel:DWORD src1_sel:WORD_1
v_lshlrev_b32_sdwa v150,1,v150 dst_sel:DWORD dst_unused:UNUSED_PAD src0_sel:DWORD src1_sel:WORD_1
v_mfma_f32_16x16x32_bf16 v[184:187],v[100:103],v[128:131],v[184:187]
s_waitcnt lgkmcnt(6)
ds_read_b64_tr_b16 v[68:69],v2 offset:12320
ds_read_b64_tr_b16 v[70:71],v2 offset:14368
v_lshl_add_u64 v[136:137],v[136:137],0,v[138:139]
v_mfma_f32_16x16x32_bf16 v[192:195],v[104:107],v[128:131],v[192:195]
s_waitcnt lgkmcnt(7)
s_cbranch_vccnz RB93_%=
s_mov_b64 exec,s[62:63]
s_add_u32 m0,s57,0
s_add_u32 s64,s56,5898240
buffer_load_dwordx4 v5,s[36:39],s64 offen lds
s_add_u32 m0,s57,1728
s_nop 0
buffer_load_dwordx4 v6,s[36:39],s64 offen lds
s_mov_b64 exec,-1
RB93_%=:
ds_read_u16 v145,v145
v_cvt_pk_bf16_f32 v138,v140,v142
v_cvt_pk_bf16_f32 v139,v141,v143
v_mfma_f32_16x16x32_bf16 v[200:203],v[108:111],v[128:131],v[200:203]
ds_read_b128 v[116:119],v4 offset:1056
ds_read_b64_tr_b16 v[72:73],v1 offset:16416
v_fma_mixhi_f16 v120,v158,s99,0
v_fma_mixhi_f16 v122,v159,s99,0
v_mfma_f32_16x16x32_bf16 v[164:167],v[88:91],v[136:139],v[164:167]
ds_read_u16_d16_hi v147,v147
v_lshlrev_b32_sdwa v149,1,v149 dst_sel:DWORD dst_unused:UNUSED_PAD src0_sel:DWORD src1_sel:WORD_1
v_lshlrev_b32_sdwa v151,1,v151 dst_sel:DWORD dst_unused:UNUSED_PAD src0_sel:DWORD src1_sel:WORD_1
v_mfma_f32_16x16x32_bf16 v[172:175],v[92:95],v[136:139],v[172:175]
ds_read_b64_tr_b16 v[74:75],v1 offset:18464
ds_read_b64_tr_b16 v[76:77],v2 offset:16416
v_pk_fma_f16 v121,v120,v120,s54
v_pk_fma_f16 v123,v122,v122,s54
v_mfma_f32_16x16x32_bf16 v[180:183],v[96:99],v[136:139],v[180:183]
ds_read_u16 v148,v148
v_pk_mul_f16 v121,v120,v121
v_pk_mul_f16 v123,v122,v123
v_mfma_f32_16x16x32_bf16 v[188:191],v[100:103],v[136:139],v[188:191]
ds_read_b64_tr_b16 v[78:79],v2 offset:18464
v_lshlrev_b32_sdwa v152,1,v152 dst_sel:DWORD dst_unused:UNUSED_PAD src0_sel:DWORD src1_sel:WORD_1
v_lshlrev_b32_sdwa v154,1,v154 dst_sel:DWORD dst_unused:UNUSED_PAD src0_sel:DWORD src1_sel:WORD_1
v_mfma_f32_16x16x32_bf16 v[196:199],v[104:107],v[136:139],v[196:199]
ds_read_u16_d16_hi v150,v150
v_exp_f16 v120,v121
v_mfma_f32_16x16x32_bf16 v[204:207],v[108:111],v[136:139],v[204:207]
s_waitcnt lgkmcnt(12)
ds_read_b64_tr_b16 v[80:81],v1 offset:20512
v_exp_f16 v122,v123
v_mfma_f32_16x16x32_bf16 v[128:131],v[64:67],v[16:19],v[112:115]
ds_read_u16 v149,v149
v_lshlrev_b32_sdwa v153,1,v153 dst_sel:DWORD dst_unused:UNUSED_PAD src0_sel:DWORD src1_sel:WORD_1
v_lshlrev_b32_sdwa v155,1,v155 dst_sel:DWORD dst_unused:UNUSED_PAD src0_sel:DWORD src1_sel:WORD_1
v_mfma_f32_16x16x32_bf16 v[136:139],v[64:67],v[20:23],v[112:115]
s_waitcnt lgkmcnt(9)
ds_read_b64_tr_b16 v[82:83],v1 offset:22560
v_exp_f16_sdwa v120,v121 dst_sel:WORD_1 dst_unused:UNUSED_PRESERVE src0_sel:WORD_1
v_mfma_f32_16x16x32_bf16 v[140:143],v[68:71],v[20:23],v[116:119]
ds_read_u16_d16_hi v151,v151
v_exp_f16_sdwa v122,v123 dst_sel:WORD_1 dst_unused:UNUSED_PRESERVE src0_sel:WORD_1
v_mfma_f32_16x16x32_bf16 v[132:135],v[68:71],v[16:19],v[116:119]
s_waitcnt lgkmcnt(8)
ds_read_b64_tr_b16 v[84:85],v2 offset:20512
v_pk_fma_f16 v120,v120,s55,s55
v_pk_fma_f16 v122,v122,s55,s55
v_mfma_f32_16x16x32_bf16 v[128:131],v[72:75],v[24:27],v[128:131]
s_cbranch_vccnz RB94_%=
s_mov_b64 exec,s[62:63]
s_add_u32 m0,s57,3488
s_add_u32 s64,s56,5996544
buffer_load_dwordx4 v5,s[36:39],s64 offen lds
s_add_u32 m0,s57,5216
s_nop 0
buffer_load_dwordx4 v6,s[36:39],s64 offen lds
s_mov_b64 exec,-1
RB94_%=:
ds_read_u16 v152,v152
v_rcp_f16 v121,v120
v_mfma_f32_16x16x32_bf16 v[136:139],v[72:75],v[28:31],v[136:139]
s_waitcnt lgkmcnt(7)
ds_read_b64_tr_b16 v[86:87],v2 offset:22560
v_rcp_f16 v123,v122
v_mfma_f32_16x16x32_bf16 v[140:143],v[76:79],v[28:31],v[140:143]
ds_read_u16_d16_hi v154,v154
v_rcp_f16_sdwa v121,v120 dst_sel:WORD_1 dst_unused:UNUSED_PRESERVE src0_sel:WORD_1
v_mfma_f32_16x16x32_bf16 v[132:135],v[76:79],v[24:27],v[132:135]
s_waitcnt lgkmcnt(5)
v_rcp_f16_sdwa v123,v122 dst_sel:WORD_1 dst_unused:UNUSED_PRESERVE src0_sel:WORD_1
v_mfma_f32_16x16x32_bf16 v[128:131],v[80:83],v[32:35],v[128:131]
ds_read_u16 v153,v153
v_fma_mix_f32 v156,v156,v121,0 op_sel_hi:[0,1,0]
v_fma_mix_f32 v158,v158,v121,0 op_sel:[0,1,0] op_sel_hi:[0,1,0]
v_mfma_f32_16x16x32_bf16 v[136:139],v[80:83],v[36:39],v[136:139]
s_waitcnt lgkmcnt(2)
v_lshl_add_u64 v[144:145],v[144:145],0,v[146:147]
v_lshl_add_u64 v[146:147],v[148:149],0,v[150:151]
v_mfma_f32_16x16x32_bf16 v[140:143],v[84:87],v[36:39],v[140:143]
ds_read_u16_d16_hi v155,v155
v_fma_mix_f32 v157,v157,v123,0 op_sel_hi:[0,1,0]
v_fma_mix_f32 v159,v159,v123,0 op_sel:[0,1,0] op_sel_hi:[0,1,0]
v_mfma_f32_16x16x32_bf16 v[132:135],v[84:87],v[32:35],v[132:135]
v_lshlrev_b32_sdwa v128,1,v128 dst_sel:DWORD dst_unused:UNUSED_PAD src0_sel:DWORD src1_sel:WORD_1
v_lshlrev_b32_sdwa v130,1,v130 dst_sel:DWORD dst_unused:UNUSED_PAD src0_sel:DWORD src1_sel:WORD_1
v_mfma_f32_16x16x32_bf16 v[208:211],v[88:91],v[144:147],v[208:211]
ds_read_u16 v128,v128
v_lshlrev_b32_sdwa v129,1,v129 dst_sel:DWORD dst_unused:UNUSED_PAD src0_sel:DWORD src1_sel:WORD_1
v_lshlrev_b32_sdwa v131,1,v131 dst_sel:DWORD dst_unused:UNUSED_PAD src0_sel:DWORD src1_sel:WORD_1
v_mfma_f32_16x16x32_bf16 v[216:219],v[92:95],v[144:147],v[216:219]
v_fma_mixlo_f16 v120,v140,s99,0
v_fma_mixlo_f16 v122,v141,s99,0
v_mfma_f32_16x16x32_bf16 v[224:227],v[96:99],v[144:147],v[224:227]
s_setprio 2
ds_read_u16_d16_hi v130,v130
v_lshlrev_b32_sdwa v132,1,v132 dst_sel:DWORD dst_unused:UNUSED_PAD src0_sel:DWORD src1_sel:WORD_1
v_lshlrev_b32_sdwa v134,1,v134 dst_sel:DWORD dst_unused:UNUSED_PAD src0_sel:DWORD src1_sel:WORD_1
v_mfma_f32_16x16x32_bf16 v[232:235],v[100:103],v[144:147],v[232:235]
s_waitcnt lgkmcnt(2)
s_cbranch_vccnz RB95_%=
s_mov_b64 exec,s[62:63]
s_add_u32 m0,s57,6976
s_add_u32 s64,s56,6094848
buffer_load_dwordx4 v5,s[36:39],s64 offen lds
s_add_u32 m0,s57,8704
s_nop 0
buffer_load_dwordx4 v6,s[36:39],s64 offen lds
s_mov_b64 exec,-1
RB95_%=:
v_lshl_add_u64 v[152:153],v[152:153],0,v[154:155]
v_mfma_f32_16x16x32_bf16 v[240:243],v[104:107],v[144:147],v[240:243]
ds_read_u16 v129,v129
v_cvt_pk_bf16_f32 v154,v156,v158
v_cvt_pk_bf16_f32 v155,v157,v159
v_mfma_f32_16x16x32_bf16 v[248:251],v[108:111],v[144:147],v[248:251]
v_fma_mixhi_f16 v120,v142,s99,0
v_fma_mixhi_f16 v122,v143,s99,0
v_mfma_f32_16x16x32_bf16 v[212:215],v[88:91],v[152:155],v[212:215]
ds_read_u16_d16_hi v131,v131
v_lshlrev_b32_sdwa v133,1,v133 dst_sel:DWORD dst_unused:UNUSED_PAD src0_sel:DWORD src1_sel:WORD_1
v_lshlrev_b32_sdwa v135,1,v135 dst_sel:DWORD dst_unused:UNUSED_PAD src0_sel:DWORD src1_sel:WORD_1
v_mfma_f32_16x16x32_bf16 v[220:223],v[92:95],v[152:155],v[220:223]
ds_read_b64_tr_b16 v[88:89],v3 offset:18480
v_pk_fma_f16 v121,v120,v120,s54
v_pk_fma_f16 v123,v122,v122,s54
v_mfma_f32_16x16x32_bf16 v[228:231],v[96:99],v[152:155],v[228:231]
ds_read_u16 v132,v132
v_pk_mul_f16 v121,v120,v121
v_pk_mul_f16 v123,v122,v123
v_mfma_f32_16x16x32_bf16 v[236:239],v[100:103],v[152:155],v[236:239]
ds_read_b64_tr_b16 v[90:91],v3 offset:20016
v_lshlrev_b32_sdwa v136,1,v136 dst_sel:DWORD dst_unused:UNUSED_PAD src0_sel:DWORD src1_sel:WORD_1
v_lshlrev_b32_sdwa v138,1,v138 dst_sel:DWORD dst_unused:UNUSED_PAD src0_sel:DWORD src1_sel:WORD_1
v_mfma_f32_16x16x32_bf16 v[244:247],v[104:107],v[152:155],v[244:247]
ds_read_u16_d16_hi v134,v134
v_exp_f16 v120,v121
v_mfma_f32_16x16x32_bf16 v[252:255],v[108:111],v[152:155],v[252:255]
ds_read_b64_tr_b16 v[92:93],v3 offset:18488
v_exp_f16 v122,v123
v_mfma_f32_16x16x32_bf16 v[144:147],v[64:67],v[40:43],v[112:115]
ds_read_u16 v133,v133
v_lshlrev_b32_sdwa v137,1,v137 dst_sel:DWORD dst_unused:UNUSED_PAD src0_sel:DWORD src1_sel:WORD_1
v_lshlrev_b32_sdwa v139,1,v139 dst_sel:DWORD dst_unused:UNUSED_PAD src0_sel:DWORD src1_sel:WORD_1
v_mfma_f32_16x16x32_bf16 v[152:155],v[64:67],v[44:47],v[112:115]
ds_read_b64_tr_b16 v[94:95],v3 offset:20024
v_exp_f16_sdwa v120,v121 dst_sel:WORD_1 dst_unused:UNUSED_PRESERVE src0_sel:WORD_1
v_mfma_f32_16x16x32_bf16 v[156:159],v[68:71],v[44:47],v[116:119]
ds_read_u16_d16_hi v135,v135
v_exp_f16_sdwa v122,v123 dst_sel:WORD_1 dst_unused:UNUSED_PRESERVE src0_sel:WORD_1
v_mfma_f32_16x16x32_bf16 v[148:151],v[68:71],v[40:43],v[116:119]
ds_read_b64_tr_b16 v[96:97],v3 offset:18544
v_pk_fma_f16 v120,v120,s55,s55
v_pk_fma_f16 v122,v122,s55,s55
v_mfma_f32_16x16x32_bf16 v[144:147],v[72:75],v[48:51],v[144:147]
s_cbranch_vccnz RB96_%=
s_mov_b64 exec,s[62:63]
s_add_u32 m0,s57,10464
s_add_u32 s64,s56,6193152
buffer_load_dwordx4 v5,s[36:39],s64 offen lds
s_add_u32 m0,s57,12192
s_nop 0
buffer_load_dwordx4 v6,s[36:39],s64 offen lds
s_mov_b64 exec,-1
RB96_%=:
ds_read_u16 v136,v136
v_rcp_f16 v121,v120
v_mfma_f32_16x16x32_bf16 v[152:155],v[72:75],v[52:55],v[152:155]
ds_read_b64_tr_b16 v[98:99],v3 offset:20080
ds_read_b64_tr_b16 v[100:101],v3 offset:18552
v_rcp_f16 v123,v122
v_mfma_f32_16x16x32_bf16 v[156:159],v[76:79],v[52:55],v[156:159]
ds_read_u16_d16_hi v138,v138
v_rcp_f16_sdwa v121,v120 dst_sel:WORD_1 dst_unused:UNUSED_PRESERVE src0_sel:WORD_1
v_mfma_f32_16x16x32_bf16 v[148:151],v[76:79],v[48:51],v[148:151]
ds_read_b64_tr_b16 v[102:103],v3 offset:20088
ds_read_b64_tr_b16 v[104:105],v3 offset:18608
v_rcp_f16_sdwa v123,v122 dst_sel:WORD_1 dst_unused:UNUSED_PRESERVE src0_sel:WORD_1
v_mfma_f32_16x16x32_bf16 v[144:147],v[80:83],v[56:59],v[144:147]
ds_read_u16 v137,v137
v_fma_mix_f32 v140,v140,v121,0 op_sel_hi:[0,1,0]
v_fma_mix_f32 v142,v142,v121,0 op_sel:[0,1,0] op_sel_hi:[0,1,0]
v_mfma_f32_16x16x32_bf16 v[152:155],v[80:83],v[60:63],v[152:155]
s_waitcnt lgkmcnt(8)
ds_read_b64_tr_b16 v[106:107],v3 offset:20144
ds_read_b64_tr_b16 v[108:109],v3 offset:18616
v_lshl_add_u64 v[128:129],v[128:129],0,v[130:131]
v_lshl_add_u64 v[130:131],v[132:133],0,v[134:135]
v_mfma_f32_16x16x32_bf16 v[156:159],v[84:87],v[60:63],v[156:159]
ds_read_u16_d16_hi v139,v139
v_fma_mix_f32 v141,v141,v123,0 op_sel_hi:[0,1,0]
v_fma_mix_f32 v143,v143,v123,0 op_sel:[0,1,0] op_sel_hi:[0,1,0]
v_mfma_f32_16x16x32_bf16 v[148:151],v[84:87],v[56:59],v[148:151]
ds_read_b64_tr_b16 v[110:111],v3 offset:20152
ds_read_b128 v[64:67],v4 offset:1536
v_lshlrev_b32_sdwa v144,1,v144 dst_sel:DWORD dst_unused:UNUSED_PAD src0_sel:DWORD src1_sel:WORD_1
v_lshlrev_b32_sdwa v146,1,v146 dst_sel:DWORD dst_unused:UNUSED_PAD src0_sel:DWORD src1_sel:WORD_1
v_mfma_f32_16x16x32_bf16 v[160:163],v[88:91],v[128:131],v[160:163]
ds_read_u16 v144,v144
ds_read_b128 v[68:71],v4 offset:1568
v_lshlrev_b32_sdwa v145,1,v145 dst_sel:DWORD dst_unused:UNUSED_PAD src0_sel:DWORD src1_sel:WORD_1
v_lshlrev_b32_sdwa v147,1,v147 dst_sel:DWORD dst_unused:UNUSED_PAD src0_sel:DWORD src1_sel:WORD_1
v_fma_mixlo_f16 v120,v156,s99,0
v_fma_mixlo_f16 v122,v157,s99,0
v_mfma_f32_16x16x32_bf16 v[168:171],v[92:95],v[128:131],v[168:171]
s_waitcnt lgkmcnt(12)
ds_read_u16_d16_hi v146,v146
ds_read_b128 v[72:75],v4 offset:1664
v_lshlrev_b32_sdwa v148,1,v148 dst_sel:DWORD dst_unused:UNUSED_PAD src0_sel:DWORD src1_sel:WORD_1
v_lshlrev_b32_sdwa v150,1,v150 dst_sel:DWORD dst_unused:UNUSED_PAD src0_sel:DWORD src1_sel:WORD_1
s_waitcnt lgkmcnt(6)
v_lshl_add_u64 v[136:137],v[136:137],0,v[138:139]
v_cvt_pk_bf16_f32 v138,v140,v142
v_mfma_f32_16x16x32_bf16 v[176:179],v[96:99],v[128:131],v[176:179]
s_setprio 1
s_cbranch_vccnz RB97_%=
s_mov_b64 exec,s[62:63]
s_add_u32 m0,s57,13952
s_add_u32 s64,s56,6291456
buffer_load_dwordx4 v5,s[36:39],s64 offen lds
s_add_u32 m0,s57,15680
s_nop 0
buffer_load_dwordx4 v6,s[36:39],s64 offen lds
s_mov_b64 exec,-1
RB97_%=:
ds_read_u16 v145,v145
ds_read_b128 v[76:79],v4 offset:1696
v_cvt_pk_bf16_f32 v139,v141,v143
v_fma_mixhi_f16 v120,v158,s99,0
v_fma_mixhi_f16 v122,v159,s99,0
v_lshlrev_b32_sdwa v149,1,v149 dst_sel:DWORD dst_unused:UNUSED_PAD src0_sel:DWORD src1_sel:WORD_1
v_mfma_f32_16x16x32_bf16 v[184:187],v[100:103],v[128:131],v[184:187]
ds_read_u16_d16_hi v147,v147
ds_read_b128 v[80:83],v4 offset:1792
v_lshlrev_b32_sdwa v151,1,v151 dst_sel:DWORD dst_unused:UNUSED_PAD src0_sel:DWORD src1_sel:WORD_1
v_pk_fma_f16 v121,v120,v120,s54
v_pk_fma_f16 v123,v122,v122,s54
v_pk_mul_f16 v121,v120,v121
v_mfma_f32_16x16x32_bf16 v[192:195],v[104:107],v[128:131],v[192:195]
s_waitcnt lgkmcnt(9)
ds_read_u16 v148,v148
ds_read_b128 v[84:87],v4 offset:1824
v_pk_mul_f16 v123,v122,v123
v_lshlrev_b32_sdwa v152,1,v152 dst_sel:DWORD dst_unused:UNUSED_PAD src0_sel:DWORD src1_sel:WORD_1
v_lshlrev_b32_sdwa v154,1,v154 dst_sel:DWORD dst_unused:UNUSED_PAD src0_sel:DWORD src1_sel:WORD_1
v_exp_f16 v120,v121
v_mfma_f32_16x16x32_bf16 v[200:203],v[108:111],v[128:131],v[200:203]
ds_read_u16_d16_hi v150,v150
v_exp_f16 v122,v123
v_lshlrev_b32_sdwa v153,1,v153 dst_sel:DWORD dst_unused:UNUSED_PAD src0_sel:DWORD src1_sel:WORD_1
v_mfma_f32_16x16x32_bf16 v[164:167],v[88:91],v[136:139],v[164:167]
ds_read_u16 v149,v149
v_lshlrev_b32_sdwa v155,1,v155 dst_sel:DWORD dst_unused:UNUSED_PAD src0_sel:DWORD src1_sel:WORD_1
v_exp_f16_sdwa v120,v121 dst_sel:WORD_1 dst_unused:UNUSED_PRESERVE src0_sel:WORD_1
v_exp_f16_sdwa v122,v123 dst_sel:WORD_1 dst_unused:UNUSED_PRESERVE src0_sel:WORD_1
v_mfma_f32_16x16x32_bf16 v[172:175],v[92:95],v[136:139],v[172:175]
ds_read_u16_d16_hi v151,v151
v_pk_fma_f16 v120,v120,s55,s55
v_pk_fma_f16 v122,v122,s55,s55
v_rcp_f16 v121,v120
v_mfma_f32_16x16x32_bf16 v[180:183],v[96:99],v[136:139],v[180:183]
ds_read_u16 v152,v152
v_rcp_f16 v123,v122
v_rcp_f16_sdwa v121,v120 dst_sel:WORD_1 dst_unused:UNUSED_PRESERVE src0_sel:WORD_1
v_mfma_f32_16x16x32_bf16 v[188:191],v[100:103],v[136:139],v[188:191]
ds_read_u16_d16_hi v154,v154
v_rcp_f16_sdwa v123,v122 dst_sel:WORD_1 dst_unused:UNUSED_PRESERVE src0_sel:WORD_1
v_fma_mix_f32 v156,v156,v121,0 op_sel_hi:[0,1,0]
v_mfma_f32_16x16x32_bf16 v[196:199],v[104:107],v[136:139],v[196:199]
s_setprio 0
s_cbranch_vccnz RB98_%=
s_mov_b64 exec,s[62:63]
s_add_u32 m0,s57,17440
s_add_u32 s64,s56,6389760
buffer_load_dwordx4 v5,s[36:39],s64 offen lds
s_add_u32 m0,s57,19168
s_nop 0
buffer_load_dwordx4 v6,s[36:39],s64 offen lds
s_mov_b64 exec,-1
RB98_%=:
ds_read_u16 v153,v153
v_fma_mix_f32 v158,v158,v121,0 op_sel:[0,1,0] op_sel_hi:[0,1,0]
s_waitcnt lgkmcnt(9)
v_lshl_add_u64 v[144:145],v[144:145],0,v[146:147]
s_waitcnt lgkmcnt(3)
v_lshl_add_u64 v[146:147],v[148:149],0,v[150:151]
v_fma_mix_f32 v157,v157,v123,0 op_sel_hi:[0,1,0]
v_mfma_f32_16x16x32_bf16 v[204:207],v[108:111],v[136:139],v[204:207]
ds_read_u16_d16_hi v155,v155
v_fma_mix_f32 v159,v159,v123,0 op_sel:[0,1,0] op_sel_hi:[0,1,0]
s_waitcnt lgkmcnt(0)
v_lshl_add_u64 v[152:153],v[152:153],0,v[154:155]
v_cvt_pk_bf16_f32 v154,v156,v158
v_cvt_pk_bf16_f32 v155,v157,v159
v_mfma_f32_16x16x32_bf16 v[208:211],v[88:91],v[144:147],v[208:211]
s_barrier
s_setprio 0
v_xor_b32 v9,64,v10
v_mul_f32 v160,v160,v64
v_mul_f32 v161,v161,v65
v_mul_f32 v162,v162,v66
v_mul_f32 v163,v163,v67
v_mfma_f32_16x16x32_bf16 v[216:219],v[92:95],v[144:147],v[216:219]
v_mul_f32 v168,v168,v68
v_mul_f32 v169,v169,v69
v_mul_f32 v170,v170,v70
v_mul_f32 v171,v171,v71
v_cvt_pk_bf16_f32 v16,v160,v161
v_mfma_f32_16x16x32_bf16 v[224:227],v[96:99],v[144:147],v[224:227]
v_cvt_pk_bf16_f32 v17,v162,v163
v_cvt_pk_bf16_f32 v18,v168,v169
v_cvt_pk_bf16_f32 v19,v170,v171
v_pk_maximum3_f16 v7,v7,v16,v17
v_mfma_f32_16x16x32_bf16 v[232:235],v[100:103],v[144:147],v[232:235]
v_pk_maximum3_f16 v7,v7,v18,v19
v_mul_f32_dpp v176,v176,v72 quad_perm:[1,0,3,2] row_mask:0xf bank_mask:0xf
v_mul_f32_dpp v177,v177,v73 quad_perm:[1,0,3,2] row_mask:0xf bank_mask:0xf
v_mul_f32_dpp v178,v178,v74 quad_perm:[1,0,3,2] row_mask:0xf bank_mask:0xf
v_mul_f32_dpp v179,v179,v75 quad_perm:[1,0,3,2] row_mask:0xf bank_mask:0xf
v_mfma_f32_16x16x32_bf16 v[240:243],v[104:107],v[144:147],v[240:243]
v_mul_f32_dpp v184,v184,v76 quad_perm:[1,0,3,2] row_mask:0xf bank_mask:0xf
v_mul_f32_dpp v185,v185,v77 quad_perm:[1,0,3,2] row_mask:0xf bank_mask:0xf
v_mul_f32_dpp v186,v186,v78 quad_perm:[1,0,3,2] row_mask:0xf bank_mask:0xf
v_mul_f32_dpp v187,v187,v79 quad_perm:[1,0,3,2] row_mask:0xf bank_mask:0xf
v_mfma_f32_16x16x32_bf16 v[248:251],v[108:111],v[144:147],v[248:251]
v_cvt_pk_bf16_f32 v24,v176,v177
v_cvt_pk_bf16_f32 v25,v178,v179
v_cvt_pk_bf16_f32 v26,v184,v185
v_cvt_pk_bf16_f32 v27,v186,v187
v_pk_maximum3_f16 v7,v7,v24,v25
v_mfma_f32_16x16x32_bf16 v[212:215],v[88:91],v[152:155],v[212:215]
v_pk_maximum3_f16 v7,v7,v26,v27
s_mov_b64 exec,s[12:13]
v_swap_b32 v16,v24
v_swap_b32 v17,v25
v_swap_b32 v18,v26
v_swap_b32 v19,v27
s_mov_b64 exec,-1
v_mfma_f32_16x16x32_bf16 v[220:223],v[92:95],v[152:155],v[220:223]
s_add_u32 s65,s61,1572864
buffer_store_dwordx4 v[16:19],v10,s[40:43],s65 offen offset:0 sc1
v_mfma_f32_16x16x32_bf16 v[228:231],v[96:99],v[152:155],v[228:231]
v_mul_f32 v192,v192,v80
v_mul_f32 v193,v193,v81
v_mul_f32 v194,v194,v82
v_mul_f32 v195,v195,v83
v_mfma_f32_16x16x32_bf16 v[236:239],v[100:103],v[152:155],v[236:239]
v_mul_f32 v200,v200,v84
v_mul_f32 v201,v201,v85
v_mul_f32 v202,v202,v86
v_mul_f32 v203,v203,v87
v_cvt_pk_bf16_f32 v32,v192,v193
v_mfma_f32_16x16x32_bf16 v[244:247],v[104:107],v[152:155],v[244:247]
v_cvt_pk_bf16_f32 v33,v194,v195
v_cvt_pk_bf16_f32 v34,v200,v201
v_cvt_pk_bf16_f32 v35,v202,v203
v_pk_maximum3_f16 v7,v7,v32,v33
v_mfma_f32_16x16x32_bf16 v[252:255],v[108:111],v[152:155],v[252:255]
v_pk_maximum3_f16 v7,v7,v34,v35
v_mul_f32_dpp v164,v164,v64 quad_perm:[1,0,3,2] row_mask:0xf bank_mask:0xf
s_add_u32 s65,s61,1572864
buffer_store_dwordx4 v[24:27],v9,s[40:43],s65 offen offset:384 sc1
v_mul_f32_dpp v165,v165,v65 quad_perm:[1,0,3,2] row_mask:0xf bank_mask:0xf
v_mul_f32_dpp v166,v166,v66 quad_perm:[1,0,3,2] row_mask:0xf bank_mask:0xf
v_mul_f32_dpp v167,v167,v67 quad_perm:[1,0,3,2] row_mask:0xf bank_mask:0xf
v_mul_f32_dpp v172,v172,v68 quad_perm:[1,0,3,2] row_mask:0xf bank_mask:0xf
v_mul_f32_dpp v173,v173,v69 quad_perm:[1,0,3,2] row_mask:0xf bank_mask:0xf
v_mul_f32_dpp v174,v174,v70 quad_perm:[1,0,3,2] row_mask:0xf bank_mask:0xf
v_mul_f32_dpp v175,v175,v71 quad_perm:[1,0,3,2] row_mask:0xf bank_mask:0xf
v_cvt_pk_bf16_f32 v20,v164,v165
v_cvt_pk_bf16_f32 v21,v166,v167
v_cvt_pk_bf16_f32 v22,v172,v173
v_cvt_pk_bf16_f32 v23,v174,v175
v_pk_maximum3_f16 v7,v7,v20,v21
v_pk_maximum3_f16 v7,v7,v22,v23
s_mov_b64 exec,s[12:13]
v_swap_b32 v32,v20
v_swap_b32 v33,v21
v_swap_b32 v34,v22
v_swap_b32 v35,v23
s_mov_b64 exec,-1
v_mul_f32 v180,v180,v72
v_mul_f32 v181,v181,v73
v_mul_f32 v182,v182,v74
v_mul_f32 v183,v183,v75
s_add_u32 s65,s61,1572864
buffer_store_dwordx4 v[32:35],v10,s[40:43],s65 offen offset:128 sc1
v_mul_f32 v188,v188,v76
v_mul_f32 v189,v189,v77
v_mul_f32 v190,v190,v78
v_mul_f32 v191,v191,v79
v_cvt_pk_bf16_f32 v28,v180,v181
v_cvt_pk_bf16_f32 v29,v182,v183
v_cvt_pk_bf16_f32 v30,v188,v189
v_cvt_pk_bf16_f32 v31,v190,v191
v_pk_maximum3_f16 v7,v7,v28,v29
v_pk_maximum3_f16 v7,v7,v30,v31
v_mul_f32_dpp v196,v196,v80 quad_perm:[1,0,3,2] row_mask:0xf bank_mask:0xf
v_mul_f32_dpp v197,v197,v81 quad_perm:[1,0,3,2] row_mask:0xf bank_mask:0xf
v_mul_f32_dpp v198,v198,v82 quad_perm:[1,0,3,2] row_mask:0xf bank_mask:0xf
v_mul_f32_dpp v199,v199,v83 quad_perm:[1,0,3,2] row_mask:0xf bank_mask:0xf
v_mul_f32_dpp v204,v204,v84 quad_perm:[1,0,3,2] row_mask:0xf bank_mask:0xf
v_mul_f32_dpp v205,v205,v85 quad_perm:[1,0,3,2] row_mask:0xf bank_mask:0xf
v_mul_f32_dpp v206,v206,v86 quad_perm:[1,0,3,2] row_mask:0xf bank_mask:0xf
v_mul_f32_dpp v207,v207,v87 quad_perm:[1,0,3,2] row_mask:0xf bank_mask:0xf
v_cvt_pk_bf16_f32 v36,v196,v197
v_cvt_pk_bf16_f32 v37,v198,v199
v_cvt_pk_bf16_f32 v38,v204,v205
v_cvt_pk_bf16_f32 v39,v206,v207
v_pk_maximum3_f16 v7,v7,v36,v37
v_pk_maximum3_f16 v7,v7,v38,v39
s_mov_b64 exec,s[12:13]
v_swap_b32 v28,v36
v_swap_b32 v29,v37
v_swap_b32 v30,v38
v_swap_b32 v31,v39
s_mov_b64 exec,-1
s_add_u32 s65,s61,1572864
buffer_store_dwordx4 v[20:23],v9,s[40:43],s65 offen offset:512 sc1
v_mul_f32 v208,v208,v64
v_mul_f32 v209,v209,v65
v_mul_f32 v210,v210,v66
v_mul_f32 v211,v211,v67
v_mul_f32 v216,v216,v68
v_mul_f32 v217,v217,v69
v_mul_f32 v218,v218,v70
v_mul_f32 v219,v219,v71
v_cvt_pk_bf16_f32 v40,v208,v209
v_cvt_pk_bf16_f32 v41,v210,v211
v_cvt_pk_bf16_f32 v42,v216,v217
v_cvt_pk_bf16_f32 v43,v218,v219
v_pk_maximum3_f16 v7,v7,v40,v41
v_pk_maximum3_f16 v7,v7,v42,v43
v_mul_f32_dpp v224,v224,v72 quad_perm:[1,0,3,2] row_mask:0xf bank_mask:0xf
v_mul_f32_dpp v225,v225,v73 quad_perm:[1,0,3,2] row_mask:0xf bank_mask:0xf
v_mul_f32_dpp v226,v226,v74 quad_perm:[1,0,3,2] row_mask:0xf bank_mask:0xf
v_mul_f32_dpp v227,v227,v75 quad_perm:[1,0,3,2] row_mask:0xf bank_mask:0xf
s_add_u32 s65,s61,1572864
buffer_store_dwordx4 v[28:31],v10,s[40:43],s65 offen offset:256 sc1
v_mul_f32_dpp v232,v232,v76 quad_perm:[1,0,3,2] row_mask:0xf bank_mask:0xf
v_mul_f32_dpp v233,v233,v77 quad_perm:[1,0,3,2] row_mask:0xf bank_mask:0xf
v_mul_f32_dpp v234,v234,v78 quad_perm:[1,0,3,2] row_mask:0xf bank_mask:0xf
v_mul_f32_dpp v235,v235,v79 quad_perm:[1,0,3,2] row_mask:0xf bank_mask:0xf
v_cvt_pk_bf16_f32 v48,v224,v225
v_cvt_pk_bf16_f32 v49,v226,v227
v_cvt_pk_bf16_f32 v50,v232,v233
v_cvt_pk_bf16_f32 v51,v234,v235
v_pk_maximum3_f16 v7,v7,v48,v49
v_pk_maximum3_f16 v7,v7,v50,v51
s_mov_b64 exec,s[12:13]
v_swap_b32 v40,v48
v_swap_b32 v41,v49
v_swap_b32 v42,v50
v_swap_b32 v43,v51
s_mov_b64 exec,-1
v_mul_f32 v240,v240,v80
v_mul_f32 v241,v241,v81
v_mul_f32 v242,v242,v82
v_mul_f32 v243,v243,v83
v_mul_f32 v248,v248,v84
v_mul_f32 v249,v249,v85
v_mul_f32 v250,v250,v86
s_add_u32 s65,s61,1572864
buffer_store_dwordx4 v[36:39],v9,s[40:43],s65 offen offset:640 sc1
v_mul_f32 v251,v251,v87
v_cvt_pk_bf16_f32 v56,v240,v241
v_cvt_pk_bf16_f32 v57,v242,v243
v_cvt_pk_bf16_f32 v58,v248,v249
v_cvt_pk_bf16_f32 v59,v250,v251
v_pk_maximum3_f16 v7,v7,v56,v57
v_pk_maximum3_f16 v7,v7,v58,v59
v_mul_f32_dpp v212,v212,v64 quad_perm:[1,0,3,2] row_mask:0xf bank_mask:0xf
v_mul_f32_dpp v213,v213,v65 quad_perm:[1,0,3,2] row_mask:0xf bank_mask:0xf
v_mul_f32_dpp v214,v214,v66 quad_perm:[1,0,3,2] row_mask:0xf bank_mask:0xf
v_mul_f32_dpp v215,v215,v67 quad_perm:[1,0,3,2] row_mask:0xf bank_mask:0xf
v_mul_f32_dpp v220,v220,v68 quad_perm:[1,0,3,2] row_mask:0xf bank_mask:0xf
v_mul_f32_dpp v221,v221,v69 quad_perm:[1,0,3,2] row_mask:0xf bank_mask:0xf
v_mul_f32_dpp v222,v222,v70 quad_perm:[1,0,3,2] row_mask:0xf bank_mask:0xf
v_mul_f32_dpp v223,v223,v71 quad_perm:[1,0,3,2] row_mask:0xf bank_mask:0xf
v_cvt_pk_bf16_f32 v44,v212,v213
v_cvt_pk_bf16_f32 v45,v214,v215
v_cvt_pk_bf16_f32 v46,v220,v221
v_cvt_pk_bf16_f32 v47,v222,v223
v_pk_maximum3_f16 v7,v7,v44,v45
v_pk_maximum3_f16 v7,v7,v46,v47
s_mov_b64 exec,s[12:13]
v_swap_b32 v56,v44
v_swap_b32 v57,v45
v_swap_b32 v58,v46
v_swap_b32 v59,v47
s_mov_b64 exec,-1
s_add_u32 s65,s61,1671168
buffer_store_dwordx4 v[40:43],v10,s[40:43],s65 offen offset:0 sc1
v_mul_f32 v228,v228,v72
v_mul_f32 v229,v229,v73
v_mul_f32 v230,v230,v74
v_mul_f32 v231,v231,v75
v_mul_f32 v236,v236,v76
v_mul_f32 v237,v237,v77
v_mul_f32 v238,v238,v78
v_mul_f32 v239,v239,v79
v_cvt_pk_bf16_f32 v52,v228,v229
v_cvt_pk_bf16_f32 v53,v230,v231
v_cvt_pk_bf16_f32 v54,v236,v237
v_cvt_pk_bf16_f32 v55,v238,v239
v_pk_maximum3_f16 v7,v7,v52,v53
v_pk_maximum3_f16 v7,v7,v54,v55
v_mul_f32_dpp v244,v244,v80 quad_perm:[1,0,3,2] row_mask:0xf bank_mask:0xf
v_mul_f32_dpp v245,v245,v81 quad_perm:[1,0,3,2] row_mask:0xf bank_mask:0xf
v_mul_f32_dpp v246,v246,v82 quad_perm:[1,0,3,2] row_mask:0xf bank_mask:0xf
v_mul_f32_dpp v247,v247,v83 quad_perm:[1,0,3,2] row_mask:0xf bank_mask:0xf
v_mul_f32_dpp v252,v252,v84 quad_perm:[1,0,3,2] row_mask:0xf bank_mask:0xf
v_mul_f32_dpp v253,v253,v85 quad_perm:[1,0,3,2] row_mask:0xf bank_mask:0xf
v_mul_f32_dpp v254,v254,v86 quad_perm:[1,0,3,2] row_mask:0xf bank_mask:0xf
s_add_u32 s65,s61,1671168
buffer_store_dwordx4 v[48:51],v9,s[40:43],s65 offen offset:384 sc1
v_mul_f32_dpp v255,v255,v87 quad_perm:[1,0,3,2] row_mask:0xf bank_mask:0xf
v_cvt_pk_bf16_f32 v60,v244,v245
v_cvt_pk_bf16_f32 v61,v246,v247
v_cvt_pk_bf16_f32 v62,v252,v253
v_cvt_pk_bf16_f32 v63,v254,v255
v_pk_maximum3_f16 v7,v7,v60,v61
v_pk_maximum3_f16 v7,v7,v62,v63
s_mov_b64 exec,s[12:13]
v_swap_b32 v52,v60
v_swap_b32 v53,v61
v_swap_b32 v54,v62
v_swap_b32 v55,v63
s_mov_b64 exec,-1
s_add_u32 s4,s10,8
s_cmp_ge_u32 s4,20
s_cselect_b32 s7,20,0
s_sub_u32 s4,s4,s7
s_mul_i32 s4,s4,3488
s_add_u32 s5,s10,10
s_cmp_ge_u32 s5,20
s_cselect_b32 s7,20,0
s_sub_u32 s5,s5,s7
s_mul_i32 s5,s5,3488
s_add_u32 s6,s10,12
s_cmp_ge_u32 s6,20
s_cselect_b32 s7,20,0
s_sub_u32 s6,s6,s7
s_mul_i32 s6,s6,3488
v_add_u32 v112,s4,v15
v_add_u32 v116,s5,v15
s_mov_b64 exec,s[8:9]
v_add_u32 v112,s5,v15
v_add_u32 v116,s6,v15
s_mov_b64 exec,-1
v_xor_b32 v113,16,v112
v_xor_b32 v117,16,v116
v_xor_b32 v114,32,v112
v_xor_b32 v118,32,v116
v_xor_b32 v115,48,v112
v_xor_b32 v119,48,v116
s_waitcnt vmcnt(8)
s_barrier
s_setprio 3
ds_read_b128 v[136:139],v116 offset:0
ds_read_b128 v[88:91],v11 offset:768
ds_read_b128 v[140:143],v116 offset:3488
ds_read_b128 v[128:131],v112 offset:0
ds_read_b128 v[132:135],v112 offset:3488
ds_read_b128 v[92:95],v12 offset:0
ds_read_b128 v[152:155],v117 offset:0
ds_read_b128 v[96:99],v11 offset:1920
ds_read_b128 v[156:159],v117 offset:3488
ds_read_b128 v[144:147],v113 offset:0
ds_read_b128 v[148:151],v113 offset:3488
ds_read_b128 v[100:103],v12 offset:1152
s_waitcnt lgkmcnt(10)
v_mfma_f32_16x16x32_bf16 v[160:163],v[88:91],v[136:139],0
s_waitcnt lgkmcnt(9)
s_add_u32 s65,s61,1671168
buffer_store_dwordx4 v[56:59],v10,s[40:43],s65 offen offset:128 sc1
v_mfma_f32_16x16x32_bf16 v[184:187],v[88:91],v[140:143],0
s_waitcnt lgkmcnt(6)
ds_read_b128 v[140:143],v116 offset:3552
ds_read_b128 v[88:91],v11 offset:5376
v_smfmac_f32_16x16x64_bf16 v[160:163],v[92:95],v[128:135],v13
ds_read_b128 v[128:131],v112 offset:64
v_smfmac_f32_16x16x64_bf16 v[184:187],v[92:95],v[132:139],v13
s_waitcnt lgkmcnt(7)
ds_read_b128 v[136:139],v116 offset:64
ds_read_b128 v[132:135],v112 offset:3552
v_mfma_f32_16x16x32_bf16 v[164:167],v[96:99],v[152:155],0
s_waitcnt lgkmcnt(8)
ds_read_b128 v[92:95],v12 offset:4608
v_add_f32 v120,v160,v161
v_mul_f32 v124,v160,v160
v_fmac_f32 v124,v161,v161
v_mfma_f32_16x16x32_bf16 v[188:191],v[96:99],v[156:159],0
s_waitcnt lgkmcnt(6)
ds_read_b128 v[156:159],v117 offset:3552
ds_read_b128 v[96:99],v11 offset:6528
v_add_f32 v121,v162,v163
v_mul_f32 v125,v162,v162
v_fmac_f32 v125,v163,v163
v_smfmac_f32_16x16x64_bf16 v[164:167],v[100:103],v[144:151],v13
ds_read_b128 v[144:147],v113 offset:64
v_add_f32 v122,v184,v185
v_mul_f32 v126,v184,v184
v_fmac_f32 v126,v185,v185
v_smfmac_f32_16x16x64_bf16 v[188:191],v[100:103],v[148:155],v13
s_waitcnt lgkmcnt(5)
s_add_u32 s65,s61,1671168
buffer_store_dwordx4 v[44:47],v9,s[40:43],s65 offen offset:512 sc1
ds_read_b128 v[152:155],v117 offset:64
ds_read_b128 v[148:151],v113 offset:3552
v_add_f32 v123,v186,v187
v_mul_f32 v127,v186,v186
v_fmac_f32 v127,v187,v187
v_mfma_f32_16x16x32_bf16 v[168:171],v[88:91],v[136:139],0
ds_read_b128 v[100:103],v12 offset:5760
v_add_f32 v120,v120,v164
v_fmac_f32 v124,v164,v164
v_add_f32 v120,v120,v165
v_fmac_f32 v124,v165,v165
v_mfma_f32_16x16x32_bf16 v[192:195],v[88:91],v[140:143],0
s_waitcnt lgkmcnt(6)
ds_read_b128 v[140:143],v116 offset:3616
ds_read_b128 v[88:91],v11 offset:9984
v_add_f32 v121,v121,v166
v_fmac_f32 v125,v166,v166
v_add_f32 v121,v121,v167
v_fmac_f32 v125,v167,v167
v_smfmac_f32_16x16x64_bf16 v[168:171],v[92:95],v[128:135],v13
ds_read_b128 v[128:131],v112 offset:128
v_add_f32 v122,v122,v188
v_fmac_f32 v126,v188,v188
v_add_f32 v122,v122,v189
v_fmac_f32 v126,v189,v189
v_smfmac_f32_16x16x64_bf16 v[192:195],v[92:95],v[132:139],v13
s_waitcnt lgkmcnt(5)
s_add_u32 s65,s61,1671168
buffer_store_dwordx4 v[52:55],v10,s[40:43],s65 offen offset:256 sc1
ds_read_b128 v[136:139],v116 offset:128
ds_read_b128 v[132:135],v112 offset:3616
v_add_f32 v123,v123,v190
v_fmac_f32 v127,v190,v190
v_add_f32 v123,v123,v191
v_fmac_f32 v127,v191,v191
v_mfma_f32_16x16x32_bf16 v[172:175],v[96:99],v[152:155],0
ds_read_b128 v[92:95],v12 offset:9216
v_add_f32 v120,v120,v168
v_fmac_f32 v124,v168,v168
v_add_f32 v120,v120,v169
v_fmac_f32 v124,v169,v169
v_mfma_f32_16x16x32_bf16 v[196:199],v[96:99],v[156:159],0
s_waitcnt lgkmcnt(6)
ds_read_b128 v[156:159],v117 offset:3616
ds_read_b128 v[96:99],v11 offset:11136
v_add_f32 v121,v121,v170
v_fmac_f32 v125,v170,v170
v_add_f32 v121,v121,v171
v_fmac_f32 v125,v171,v171
v_smfmac_f32_16x16x64_bf16 v[172:175],v[100:103],v[144:151],v13
ds_read_b128 v[144:147],v113 offset:128
v_add_f32 v122,v122,v192
v_fmac_f32 v126,v192,v192
v_add_f32 v122,v122,v193
v_fmac_f32 v126,v193,v193
v_smfmac_f32_16x16x64_bf16 v[196:199],v[100:103],v[148:155],v13
s_waitcnt lgkmcnt(5)
s_add_u32 s65,s61,1671168
buffer_store_dwordx4 v[60:63],v9,s[40:43],s65 offen offset:640 sc1
ds_read_b128 v[152:155],v117 offset:128
ds_read_b128 v[148:151],v113 offset:3616
v_add_f32 v123,v123,v194
v_fmac_f32 v127,v194,v194
v_add_f32 v123,v123,v195
v_fmac_f32 v127,v195,v195
v_mfma_f32_16x16x32_bf16 v[176:179],v[88:91],v[136:139],0
ds_read_b128 v[100:103],v12 offset:10368
v_add_f32 v120,v120,v172
v_fmac_f32 v124,v172,v172
v_add_f32 v120,v120,v173
v_fmac_f32 v124,v173,v173
v_mfma_f32_16x16x32_bf16 v[200:203],v[88:91],v[140:143],0
s_waitcnt lgkmcnt(6)
ds_read_b128 v[140:143],v118 offset:3488
ds_read_b128 v[88:91],v11 offset:3072
v_add_f32 v121,v121,v174
v_fmac_f32 v125,v174,v174
v_add_f32 v121,v121,v175
v_fmac_f32 v125,v175,v175
v_smfmac_f32_16x16x64_bf16 v[176:179],v[92:95],v[128:135],v13
ds_read_b128 v[128:131],v114 offset:0
v_add_f32 v122,v122,v196
v_fmac_f32 v126,v196,v196
v_add_f32 v122,v122,v197
v_fmac_f32 v126,v197,v197
v_smfmac_f32_16x16x64_bf16 v[200:203],v[92:95],v[132:139],v13
s_waitcnt lgkmcnt(5)
ds_read_b128 v[136:139],v118 offset:0
ds_read_b128 v[132:135],v114 offset:3488
v_add_f32 v123,v123,v198
v_fmac_f32 v127,v198,v198
v_add_f32 v123,v123,v199
v_fmac_f32 v127,v199,v199
v_mfma_f32_16x16x32_bf16 v[180:183],v[96:99],v[152:155],0
ds_read_b128 v[92:95],v12 offset:2304
v_add_f32 v120,v120,v176
v_fmac_f32 v124,v176,v176
v_add_f32 v120,v120,v177
v_fmac_f32 v124,v177,v177
v_mfma_f32_16x16x32_bf16 v[204:207],v[96:99],v[156:159],0
s_waitcnt lgkmcnt(6)
ds_read_b128 v[156:159],v119 offset:3488
ds_read_b128 v[96:99],v11 offset:4224
v_add_f32 v121,v121,v178
v_fmac_f32 v125,v178,v178
v_add_f32 v121,v121,v179
v_fmac_f32 v125,v179,v179
v_smfmac_f32_16x16x64_bf16 v[180:183],v[100:103],v[144:151],v13
ds_read_b128 v[144:147],v115 offset:0
v_add_f32 v122,v122,v200
v_fmac_f32 v126,v200,v200
v_add_f32 v122,v122,v201
v_fmac_f32 v126,v201,v201
v_smfmac_f32_16x16x64_bf16 v[204:207],v[100:103],v[148:155],v13
s_waitcnt lgkmcnt(5)
ds_read_b128 v[152:155],v119 offset:0
ds_read_b128 v[148:151],v115 offset:3488
v_add_f32 v123,v123,v202
v_fmac_f32 v127,v202,v202
v_add_f32 v123,v123,v203
v_fmac_f32 v127,v203,v203
v_mfma_f32_16x16x32_bf16 v[208:211],v[88:91],v[136:139],0
ds_read_b128 v[100:103],v12 offset:3456
v_add_f32 v120,v120,v180
v_fmac_f32 v124,v180,v180
v_add_f32 v120,v120,v181
v_fmac_f32 v124,v181,v181
v_mfma_f32_16x16x32_bf16 v[232:235],v[88:91],v[140:143],0
s_waitcnt lgkmcnt(6)
ds_read_b128 v[140:143],v118 offset:3552
ds_read_b128 v[88:91],v11 offset:7680
v_add_f32 v121,v121,v182
v_fmac_f32 v125,v182,v182
v_add_f32 v121,v121,v183
v_fmac_f32 v125,v183,v183
v_smfmac_f32_16x16x64_bf16 v[208:211],v[92:95],v[128:135],v13
ds_read_b128 v[128:131],v114 offset:64
v_add_f32 v122,v122,v204
v_fmac_f32 v126,v204,v204
v_add_f32 v122,v122,v205
v_fmac_f32 v126,v205,v205
v_smfmac_f32_16x16x64_bf16 v[232:235],v[92:95],v[132:139],v13
s_waitcnt lgkmcnt(5)
ds_read_b128 v[136:139],v118 offset:64
ds_read_b128 v[132:135],v114 offset:3552
v_add_f32 v123,v123,v206
v_fmac_f32 v127,v206,v206
v_add_f32 v123,v123,v207
v_fmac_f32 v127,v207,v207
v_mfma_f32_16x16x32_bf16 v[212:215],v[96:99],v[152:155],0
ds_read_b128 v[92:95],v12 offset:6912
v_add_f32 v120,v120,v208
v_fmac_f32 v124,v208,v208
v_add_f32 v120,v120,v209
v_fmac_f32 v124,v209,v209
v_mfma_f32_16x16x32_bf16 v[236:239],v[96:99],v[156:159],0
s_waitcnt lgkmcnt(6)
s_setprio 2
ds_read_b128 v[156:159],v119 offset:3552
ds_read_b128 v[96:99],v11 offset:8832
v_add_f32 v121,v121,v210
v_fmac_f32 v125,v210,v210
v_add_f32 v121,v121,v211
v_fmac_f32 v125,v211,v211
v_smfmac_f32_16x16x64_bf16 v[212:215],v[100:103],v[144:151],v13
ds_read_b128 v[144:147],v115 offset:64
v_add_f32 v122,v122,v232
v_fmac_f32 v126,v232,v232
v_add_f32 v122,v122,v233
v_fmac_f32 v126,v233,v233
v_smfmac_f32_16x16x64_bf16 v[236:239],v[100:103],v[148:155],v13
s_waitcnt lgkmcnt(5)
ds_read_b128 v[152:155],v119 offset:64
ds_read_b128 v[148:151],v115 offset:3552
v_add_f32 v123,v123,v234
v_fmac_f32 v127,v234,v234
v_add_f32 v123,v123,v235
v_fmac_f32 v127,v235,v235
v_mfma_f32_16x16x32_bf16 v[216:219],v[88:91],v[136:139],0
ds_read_b128 v[100:103],v12 offset:8064
v_add_f32 v120,v120,v212
v_fmac_f32 v124,v212,v212
v_add_f32 v120,v120,v213
v_fmac_f32 v124,v213,v213
v_add_f32 v121,v121,v214
v_mfma_f32_16x16x32_bf16 v[240:243],v[88:91],v[140:143],0
s_waitcnt lgkmcnt(6)
ds_read_b128 v[140:143],v118 offset:3616
ds_read_b128 v[88:91],v11 offset:12288
v_fmac_f32 v125,v214,v214
v_add_f32 v121,v121,v215
v_fmac_f32 v125,v215,v215
v_add_f32 v122,v122,v236
v_smfmac_f32_16x16x64_bf16 v[216:219],v[92:95],v[128:135],v13
ds_read_b128 v[128:131],v114 offset:128
v_fmac_f32 v126,v236,v236
v_add_f32 v122,v122,v237
v_fmac_f32 v126,v237,v237
v_add_f32 v123,v123,v238
v_fmac_f32 v127,v238,v238
v_smfmac_f32_16x16x64_bf16 v[240:243],v[92:95],v[132:139],v13
s_waitcnt lgkmcnt(5)
ds_read_b128 v[136:139],v118 offset:128
ds_read_b128 v[132:135],v114 offset:3616
v_add_f32 v123,v123,v239
v_fmac_f32 v127,v239,v239
v_add_u32 v112,v112,v8
v_add_u32 v116,v116,v8
v_mfma_f32_16x16x32_bf16 v[220:223],v[96:99],v[152:155],0
ds_read_b128 v[92:95],v12 offset:11520
ds_read_b128 v[104:107],v14 offset:0
v_add_f32 v120,v120,v216
v_fmac_f32 v124,v216,v216
v_add_f32 v120,v120,v217
v_fmac_f32 v124,v217,v217
v_mfma_f32_16x16x32_bf16 v[244:247],v[96:99],v[156:159],0
s_waitcnt lgkmcnt(7)
ds_read_b128 v[108:111],v4 offset:3584
ds_read_b128 v[156:159],v119 offset:3616
v_add_f32 v121,v121,v218
v_fmac_f32 v125,v218,v218
v_add_f32 v121,v121,v219
v_fmac_f32 v125,v219,v219
v_smfmac_f32_16x16x64_bf16 v[220:223],v[100:103],v[144:151],v13
ds_read_b128 v[96:99],v11 offset:13440
ds_read_b128 v[144:147],v115 offset:128
v_add_f32 v122,v122,v240
v_fmac_f32 v126,v240,v240
v_add_f32 v122,v122,v241
v_fmac_f32 v126,v241,v241
v_smfmac_f32_16x16x64_bf16 v[244:247],v[100:103],v[148:155],v13
s_waitcnt lgkmcnt(7)
ds_read_b128 v[152:155],v119 offset:128
ds_read_b128 v[148:151],v115 offset:3616
v_add_f32 v123,v123,v242
v_fmac_f32 v127,v242,v242
v_add_f32 v123,v123,v243
v_fmac_f32 v127,v243,v243
v_mfma_f32_16x16x32_bf16 v[224:227],v[88:91],v[136:139],0
ds_read_b128 v[100:103],v12 offset:12672
v_add_f32 v120,v120,v220
v_fmac_f32 v124,v220,v220
v_add_f32 v120,v120,v221
v_fmac_f32 v124,v221,v221
v_mfma_f32_16x16x32_bf16 v[248:251],v[88:91],v[140:143],0
s_waitcnt lgkmcnt(8)
s_setprio 1
ds_read_b128 v[140:143],v116 offset:192
ds_read_b128 v[88:91],v112 offset:3616
v_add_f32 v121,v121,v222
v_fmac_f32 v125,v222,v222
v_add_f32 v121,v121,v223
v_fmac_f32 v125,v223,v223
v_smfmac_f32_16x16x64_bf16 v[224:227],v[92:95],v[128:135],v13
ds_read_b128 v[128:131],v112 offset:3488
v_add_f32 v122,v122,v244
v_fmac_f32 v126,v244,v244
v_add_f32 v122,v122,v245
v_fmac_f32 v126,v245,v245
v_smfmac_f32_16x16x64_bf16 v[248:251],v[92:95],v[132:139],v13
s_waitcnt lgkmcnt(5)
ds_read_b128 v[136:139],v116 offset:0
ds_read_b128 v[132:135],v112 offset:3680
v_add_f32 v123,v123,v246
v_fmac_f32 v127,v246,v246
v_add_f32 v123,v123,v247
v_fmac_f32 v127,v247,v247
v_mfma_f32_16x16x32_bf16 v[228:231],v[96:99],v[152:155],0
ds_read_b128 v[92:95],v112 offset:3808
v_add_f32 v120,v120,v224
v_fmac_f32 v124,v224,v224
v_add_f32 v120,v120,v225
v_fmac_f32 v124,v225,v225
v_mfma_f32_16x16x32_bf16 v[252:255],v[96:99],v[156:159],0
s_waitcnt lgkmcnt(6)
ds_read_b128 v[156:159],v116 offset:256
ds_read_b128 v[96:99],v116 offset:128
v_add_f32 v121,v121,v226
v_fmac_f32 v125,v226,v226
v_add_f32 v121,v121,v227
v_fmac_f32 v125,v227,v227
v_smfmac_f32_16x16x64_bf16 v[228:231],v[100:103],v[144:151],v13
ds_read_b128 v[144:147],v112 offset:3552
v_add_f32 v122,v122,v248
v_fmac_f32 v126,v248,v248
v_add_f32 v122,v122,v249
v_fmac_f32 v126,v249,v249
v_smfmac_f32_16x16x64_bf16 v[252:255],v[100:103],v[148:155],v13
s_setprio 0
ds_read_b128 v[152:155],v116 offset:64
ds_read_b128 v[148:151],v112 offset:3744
v_add_f32 v123,v123,v250
v_fmac_f32 v127,v250,v250
v_add_f32 v123,v123,v251
v_fmac_f32 v127,v251,v251
ds_read_b128 v[100:103],v116 offset:320
v_add_f32 v120,v120,v228
v_fmac_f32 v124,v228,v228
v_add_f32 v120,v120,v229
v_fmac_f32 v124,v229,v229
v_add_f32 v121,v121,v230
v_fmac_f32 v125,v230,v230
v_add_f32 v121,v121,v231
v_fmac_f32 v125,v231,v231
s_waitcnt lgkmcnt(0)
s_barrier
s_setprio 0
v_add_f32 v122,v122,v252
v_fmac_f32 v126,v252,v252
v_add_f32 v122,v122,v253
v_fmac_f32 v126,v253,v253
v_add_f32 v123,v123,v254
v_fmac_f32 v127,v254,v254
v_add_f32 v123,v123,v255
v_fmac_f32 v127,v255,v255
s_setprio 3
ds_read_b128 v[56:59],v14 offset:256
ds_read_b128 v[60:63],v4 offset:3616
s_setprio 0
v_permlane32_swap_b32 v120,v122
v_permlane32_swap_b32 v121,v123
v_permlane32_swap_b32 v124,v126
v_permlane32_swap_b32 v125,v127
v_add_f32 v120,v120,v122
v_add_f32 v121,v121,v123
v_add_f32 v124,v124,v126
v_add_f32 v125,v125,v127
v_permlane16_swap_b32 v120,v121
s_nop 0
v_permlane16_swap_b32 v124,v125
v_add_f32 v113,v120,v121
v_add_f32 v114,v124,v125
v_mul_f32 v113,0x3c2aaaab,v113
v_mul_f32 v114,0x3c2aaaab,v114
v_mul_f32 v115,0x5e000000,v114
v_fma_f32 v114,-v113,v113,v114
v_pk_maximum3_f16 v7,v7,v115,v115 op_sel:[0,1,1] op_sel_hi:[1,1,1]
v_max_f32 v114,0,v114
v_add_f32 v114,0x3727c5ac,v114
v_rsq_f32 v114,v114
s_nop 0
v_mul_f32 v120,0x8befe000,v114
v_mul_f32 v121,0x8befe000,v114
v_mul_f32 v124,-v113,v120
v_mul_f32 v125,-v113,v120
v_permlane16_swap_b32 v120,v121
s_nop 0
v_permlane16_swap_b32 v124,v125
v_mov_b32 v122,v120
v_mov_b32 v123,v121
v_mov_b32 v126,v124
v_mov_b32 v127,v125
v_permlane32_swap_b32 v120,v122
v_permlane32_swap_b32 v124,v126
v_permlane32_swap_b32 v121,v123
v_permlane32_swap_b32 v125,v127
v_add_u32 v8,94432,v1
v_add_u32 v9,94432,v2
v_fma_f32 v160,v160,v120,v124
s_setprio 3
ds_read_b64_tr_b16 v[64:65],v1 offset:512
s_setprio 0
v_fma_f32 v161,v161,v120,v124
s_setprio 3
ds_read_b64_tr_b16 v[66:67],v1 offset:2560
s_setprio 0
v_fma_f32 v162,v162,v121,v125
s_setprio 3
ds_read_b128 v[112:115],v4 offset:384
s_setprio 0
v_fma_f32 v163,v163,v121,v125
s_setprio 3
ds_read_b64_tr_b16 v[68:69],v2 offset:512
s_setprio 0
v_fma_f32 v164,v164,v120,v124
s_setprio 3
ds_read_b64_tr_b16 v[70:71],v2 offset:2560
s_setprio 0
v_fma_f32 v165,v165,v120,v124
s_setprio 3
ds_read_b128 v[116:119],v4 offset:416
s_setprio 0
v_fma_f32 v166,v166,v121,v125
s_setprio 3
ds_read_b64_tr_b16 v[72:73],v1 offset:4608
s_setprio 0
v_fma_f32 v167,v167,v121,v125
s_setprio 3
ds_read_b64_tr_b16 v[74:75],v1 offset:6656
s_setprio 0
v_fma_f32 v208,v208,v120,v124
s_setprio 3
ds_read_b64_tr_b16 v[76:77],v2 offset:4608
s_setprio 0
v_fma_f32 v209,v209,v120,v124
s_setprio 3
ds_read_b64_tr_b16 v[78:79],v2 offset:6656
s_setprio 0
v_fma_f32 v210,v210,v121,v125
s_setprio 3
ds_read_b64_tr_b16 v[80:81],v1 offset:8704
s_setprio 0
v_fma_f32 v211,v211,v121,v125
s_setprio 3
ds_read_b64_tr_b16 v[82:83],v1 offset:10752
s_setprio 0
v_fma_f32 v212,v212,v120,v124
s_setprio 3
ds_read_b64_tr_b16 v[84:85],v2 offset:8704
s_setprio 0
v_fma_f32 v213,v213,v120,v124
s_setprio 3
ds_read_b64_tr_b16 v[86:87],v2 offset:10752
s_setprio 0
v_fma_f32 v214,v214,v121,v125
v_fma_f32 v215,v215,v121,v125
v_cvt_pk_bf16_f32 v16,v160,v161
v_cvt_pk_bf16_f32 v20,v162,v163
v_cvt_pk_bf16_f32 v17,v164,v165
v_cvt_pk_bf16_f32 v21,v166,v167
v_cvt_pk_bf16_f32 v18,v208,v209
v_cvt_pk_bf16_f32 v22,v210,v211
v_cvt_pk_bf16_f32 v19,v212,v213
v_cvt_pk_bf16_f32 v23,v214,v215
v_fma_f32 v168,v168,v120,v124
v_fma_f32 v169,v169,v120,v124
v_mfma_f32_16x16x32_bf16 v[160:163],v[104:107],v[128:131],v[108:111]
v_fma_f32 v170,v170,v121,v125
v_fma_f32 v171,v171,v121,v125
v_fma_f32 v172,v172,v120,v124
v_fma_f32 v173,v173,v120,v124
v_fma_f32 v174,v174,v121,v125
v_fma_f32 v175,v175,v121,v125
v_mfma_f32_16x16x32_bf16 v[164:167],v[104:107],v[132:135],v[108:111]
v_fma_f32 v216,v216,v120,v124
v_fma_f32 v217,v217,v120,v124
v_fma_f32 v218,v218,v121,v125
v_fma_f32 v219,v219,v121,v125
v_fma_f32 v220,v220,v120,v124
v_mfma_f32_16x16x32_bf16 v[208:211],v[104:107],v[136:139],v[108:111]
v_fma_f32 v221,v221,v120,v124
v_fma_f32 v222,v222,v121,v125
v_fma_f32 v223,v223,v121,v125
v_cvt_pk_bf16_f32 v24,v168,v169
v_cvt_pk_bf16_f32 v28,v170,v171
s_setprio 3
v_mfma_f32_16x16x32_bf16 v[212:215],v[104:107],v[140:143],v[108:111]
ds_read_b128 v[104:107],v14 offset:512
ds_read_b128 v[108:111],v4 offset:3712
s_setprio 0
v_cvt_pk_bf16_f32 v25,v172,v173
v_cvt_pk_bf16_f32 v29,v174,v175
v_cvt_pk_bf16_f32 v26,v216,v217
v_cvt_pk_bf16_f32 v30,v218,v219
v_cvt_pk_bf16_f32 v27,v220,v221
v_cvt_pk_bf16_f32 v31,v222,v223
v_fma_f32 v176,v176,v120,v124
v_fma_f32 v177,v177,v120,v124
s_waitcnt lgkmcnt(15)
v_mfma_f32_16x16x32_bf16 v[168:171],v[56:59],v[128:131],v[60:63]
v_fma_f32 v178,v178,v121,v125
v_fma_f32 v179,v179,v121,v125
v_fma_f32 v180,v180,v120,v124
v_fma_f32 v181,v181,v120,v124
v_fma_f32 v182,v182,v121,v125
v_fma_f32 v183,v183,v121,v125
v_mfma_f32_16x16x32_bf16 v[172:175],v[56:59],v[132:135],v[60:63]
v_fma_f32 v224,v224,v120,v124
v_fma_f32 v225,v225,v120,v124
v_fma_f32 v226,v226,v121,v125
v_fma_f32 v227,v227,v121,v125
v_fma_f32 v228,v228,v120,v124
v_mfma_f32_16x16x32_bf16 v[216:219],v[56:59],v[136:139],v[60:63]
v_fma_f32 v229,v229,v120,v124
v_fma_f32 v230,v230,v121,v125
v_fma_f32 v231,v231,v121,v125
v_cvt_pk_bf16_f32 v32,v176,v177
v_cvt_pk_bf16_f32 v36,v178,v179
s_setprio 3
v_mfma_f32_16x16x32_bf16 v[220:223],v[56:59],v[140:143],v[60:63]
ds_read_b128 v[56:59],v14 offset:768
ds_read_b128 v[60:63],v4 offset:3744
s_setprio 0
v_cvt_pk_bf16_f32 v33,v180,v181
v_cvt_pk_bf16_f32 v37,v182,v183
v_cvt_pk_bf16_f32 v34,v224,v225
v_cvt_pk_bf16_f32 v38,v226,v227
v_cvt_pk_bf16_f32 v35,v228,v229
v_cvt_pk_bf16_f32 v39,v230,v231
v_fma_f32 v184,v184,v122,v126
s_waitcnt lgkmcnt(2)
v_mfma_f32_16x16x32_bf16 v[176:179],v[104:107],v[144:147],v[108:111]
v_fma_f32 v185,v185,v122,v126
v_fma_f32 v186,v186,v123,v127
v_fma_f32 v187,v187,v123,v127
v_mfma_f32_16x16x32_bf16 v[128:131],v[64:67],v[16:19],v[112:115]
v_fma_f32 v188,v188,v122,v126
v_fma_f32 v189,v189,v122,v126
v_fma_f32 v190,v190,v123,v127
v_mfma_f32_16x16x32_bf16 v[180:183],v[104:107],v[148:151],v[108:111]
v_fma_f32 v191,v191,v123,v127
v_fma_f32 v232,v232,v122,v126
v_fma_f32 v233,v233,v122,v126
v_mfma_f32_16x16x32_bf16 v[136:139],v[64:67],v[20:23],v[112:115]
v_fma_f32 v234,v234,v123,v127
v_fma_f32 v235,v235,v123,v127
v_fma_f32 v236,v236,v122,v126
v_mfma_f32_16x16x32_bf16 v[224:227],v[104:107],v[152:155],v[108:111]
v_fma_f32 v237,v237,v122,v126
v_fma_f32 v238,v238,v123,v127
v_fma_f32 v239,v239,v123,v127
v_mfma_f32_16x16x32_bf16 v[140:143],v[68:71],v[20:23],v[116:119]
v_cvt_pk_bf16_f32 v40,v184,v185
v_cvt_pk_bf16_f32 v44,v186,v187
v_cvt_pk_bf16_f32 v41,v188,v189
s_setprio 3
v_mfma_f32_16x16x32_bf16 v[228:231],v[104:107],v[156:159],v[108:111]
ds_read_b128 v[104:107],v14 offset:1024
ds_read_b128 v[108:111],v4 offset:3840
s_setprio 0
v_cvt_pk_bf16_f32 v45,v190,v191
v_cvt_pk_bf16_f32 v42,v232,v233
v_cvt_pk_bf16_f32 v46,v234,v235
v_mfma_f32_16x16x32_bf16 v[132:135],v[68:71],v[16:19],v[116:119]
v_cvt_pk_bf16_f32 v43,v236,v237
v_cvt_pk_bf16_f32 v47,v238,v239
v_fma_f32 v192,v192,v122,v126
s_waitcnt lgkmcnt(2)
v_mfma_f32_16x16x32_bf16 v[184:187],v[56:59],v[144:147],v[60:63]
v_fma_f32 v193,v193,v122,v126
v_fma_f32 v194,v194,v123,v127
v_mfma_f32_16x16x32_bf16 v[128:131],v[72:75],v[24:27],v[128:131]
v_fma_f32 v195,v195,v123,v127
v_fma_f32 v196,v196,v122,v126
v_mfma_f32_16x16x32_bf16 v[188:191],v[56:59],v[148:151],v[60:63]
v_fma_f32 v197,v197,v122,v126
v_fma_f32 v198,v198,v123,v127
v_mfma_f32_16x16x32_bf16 v[136:139],v[72:75],v[28:31],v[136:139]
v_fma_f32 v199,v199,v123,v127
v_fma_f32 v240,v240,v122,v126
v_mfma_f32_16x16x32_bf16 v[232:235],v[56:59],v[152:155],v[60:63]
v_fma_f32 v241,v241,v122,v126
v_fma_f32 v242,v242,v123,v127
v_mfma_f32_16x16x32_bf16 v[140:143],v[76:79],v[28:31],v[140:143]
v_fma_f32 v243,v243,v123,v127
v_fma_f32 v244,v244,v122,v126
s_setprio 3
v_mfma_f32_16x16x32_bf16 v[236:239],v[56:59],v[156:159],v[60:63]
ds_read_b128 v[144:147],v14 offset:1280
ds_read_b128 v[148:151],v4 offset:3872
s_setprio 0
v_fma_f32 v245,v245,v122,v126
v_fma_f32 v246,v246,v123,v127
v_mfma_f32_16x16x32_bf16 v[132:135],v[76:79],v[24:27],v[132:135]
v_fma_f32 v247,v247,v123,v127
v_cvt_pk_bf16_f32 v48,v192,v193
v_cvt_pk_bf16_f32 v52,v194,v195
v_cvt_pk_bf16_f32 v49,v196,v197
v_cvt_pk_bf16_f32 v53,v198,v199
v_cvt_pk_bf16_f32 v50,v240,v241
v_cvt_pk_bf16_f32 v54,v242,v243
v_cvt_pk_bf16_f32 v51,v244,v245
v_cvt_pk_bf16_f32 v55,v246,v247
v_fma_f32 v200,v200,v122,v126
s_waitcnt lgkmcnt(2)
v_mfma_f32_16x16x32_bf16 v[192:195],v[104:107],v[88:91],v[108:111]
v_fma_f32 v201,v201,v122,v126
v_fma_f32 v202,v202,v123,v127
v_fma_f32 v203,v203,v123,v127
v_mfma_f32_16x16x32_bf16 v[128:131],v[80:83],v[32:35],v[128:131]
v_fma_f32 v204,v204,v122,v126
v_fma_f32 v205,v205,v122,v126
v_fma_f32 v206,v206,v123,v127
v_mfma_f32_16x16x32_bf16 v[196:199],v[104:107],v[92:95],v[108:111]
v_fma_f32 v207,v207,v123,v127
v_fma_f32 v248,v248,v122,v126
v_fma_f32 v249,v249,v122,v126
v_mfma_f32_16x16x32_bf16 v[136:139],v[80:83],v[36:39],v[136:139]
v_fma_f32 v250,v250,v123,v127
v_fma_f32 v251,v251,v123,v127
v_fma_f32 v252,v252,v122,v126
v_mfma_f32_16x16x32_bf16 v[240:243],v[104:107],v[96:99],v[108:111]
v_fma_f32 v253,v253,v122,v126
v_fma_f32 v254,v254,v123,v127
v_fma_f32 v255,v255,v123,v127
v_mfma_f32_16x16x32_bf16 v[140:143],v[84:87],v[36:39],v[140:143]
v_cvt_pk_bf16_f32 v56,v200,v201
v_cvt_pk_bf16_f32 v60,v202,v203
v_cvt_pk_bf16_f32 v57,v204,v205
v_mfma_f32_16x16x32_bf16 v[244:247],v[104:107],v[100:103],v[108:111]
v_cvt_pk_bf16_f32 v61,v206,v207
v_cvt_pk_bf16_f32 v58,v248,v249
v_cvt_pk_bf16_f32 v62,v250,v251
v_mfma_f32_16x16x32_bf16 v[132:135],v[84:87],v[32:35],v[132:135]
v_cvt_pk_bf16_f32 v59,v252,v253
v_cvt_pk_bf16_f32 v63,v254,v255
s_waitcnt lgkmcnt(0)
v_mfma_f32_16x16x32_bf16 v[200:203],v[144:147],v[88:91],v[148:151]
s_barrier
s_setprio 3
ds_read_b64_tr_b16 v[104:105],v3 offset:12448
v_lshlrev_b32_sdwa v128,1,v128 dst_sel:DWORD dst_unused:UNUSED_PAD src0_sel:DWORD src1_sel:WORD_1
v_lshlrev_b32_sdwa v130,1,v130 dst_sel:DWORD dst_unused:UNUSED_PAD src0_sel:DWORD src1_sel:WORD_1
v_lshlrev_b32_sdwa v129,1,v129 dst_sel:DWORD dst_unused:UNUSED_PAD src0_sel:DWORD src1_sel:WORD_1
v_mfma_f32_16x16x32_bf16 v[204:207],v[144:147],v[92:95],v[148:151]
ds_read_u16 v128,v128
ds_read_b64_tr_b16 v[106:107],v3 offset:13984
v_lshlrev_b32_sdwa v131,1,v131 dst_sel:DWORD dst_unused:UNUSED_PAD src0_sel:DWORD src1_sel:WORD_1
v_fma_mixlo_f16 v120,v140,s99,0
v_fma_mixlo_f16 v122,v141,s99,0
v_mfma_f32_16x16x32_bf16 v[248:251],v[144:147],v[96:99],v[148:151]
ds_read_u16_d16_hi v130,v130
ds_read_b64_tr_b16 v[108:109],v3 offset:12456
v_lshlrev_b32_sdwa v132,1,v132 dst_sel:DWORD dst_unused:UNUSED_PAD src0_sel:DWORD src1_sel:WORD_1
v_lshlrev_b32_sdwa v134,1,v134 dst_sel:DWORD dst_unused:UNUSED_PAD src0_sel:DWORD src1_sel:WORD_1
v_mfma_f32_16x16x32_bf16 v[252:255],v[144:147],v[100:103],v[148:151]
ds_read_u16 v129,v129
ds_read_b64_tr_b16 v[110:111],v3 offset:13992
v_fma_mixhi_f16 v120,v142,s99,0
v_fma_mixhi_f16 v122,v143,s99,0
v_lshlrev_b32_sdwa v133,1,v133 dst_sel:DWORD dst_unused:UNUSED_PAD src0_sel:DWORD src1_sel:WORD_1
v_lshlrev_b32_sdwa v135,1,v135 dst_sel:DWORD dst_unused:UNUSED_PAD src0_sel:DWORD src1_sel:WORD_1
v_mfma_f32_16x16x32_bf16 v[144:147],v[64:67],v[40:43],v[112:115]
ds_read_u16_d16_hi v131,v131
ds_read_b64_tr_b16 v[88:89],v3 offset:12320
v_pk_fma_f16 v121,v120,v120,s54
v_pk_fma_f16 v123,v122,v122,s54
v_pk_mul_f16 v121,v120,v121
v_mfma_f32_16x16x32_bf16 v[152:155],v[64:67],v[44:47],v[112:115]
ds_read_u16 v132,v132
ds_read_b64_tr_b16 v[90:91],v3 offset:13856
v_pk_mul_f16 v123,v122,v123
v_lshlrev_b32_sdwa v136,1,v136 dst_sel:DWORD dst_unused:UNUSED_PAD src0_sel:DWORD src1_sel:WORD_1
v_mfma_f32_16x16x32_bf16 v[156:159],v[68:71],v[44:47],v[116:119]
ds_read_u16_d16_hi v134,v134
ds_read_b64_tr_b16 v[92:93],v3 offset:12328
v_lshlrev_b32_sdwa v138,1,v138 dst_sel:DWORD dst_unused:UNUSED_PAD src0_sel:DWORD src1_sel:WORD_1
v_exp_f16 v120,v121
v_mfma_f32_16x16x32_bf16 v[148:151],v[68:71],v[40:43],v[116:119]
ds_read_u16 v133,v133
ds_read_b64_tr_b16 v[94:95],v3 offset:13864
v_exp_f16 v122,v123
v_lshlrev_b32_sdwa v137,1,v137 dst_sel:DWORD dst_unused:UNUSED_PAD src0_sel:DWORD src1_sel:WORD_1
v_mfma_f32_16x16x32_bf16 v[144:147],v[72:75],v[48:51],v[144:147]
ds_read_u16_d16_hi v135,v135
ds_read_b64_tr_b16 v[96:97],v3 offset:12384
v_lshlrev_b32_sdwa v139,1,v139 dst_sel:DWORD dst_unused:UNUSED_PAD src0_sel:DWORD src1_sel:WORD_1
v_exp_f16_sdwa v120,v121 dst_sel:WORD_1 dst_unused:UNUSED_PRESERVE src0_sel:WORD_1
v_mfma_f32_16x16x32_bf16 v[152:155],v[72:75],v[52:55],v[152:155]
ds_read_u16 v136,v136
ds_read_b64_tr_b16 v[98:99],v3 offset:13920
v_exp_f16_sdwa v122,v123 dst_sel:WORD_1 dst_unused:UNUSED_PRESERVE src0_sel:WORD_1
v_mfma_f32_16x16x32_bf16 v[156:159],v[76:79],v[52:55],v[156:159]
ds_read_u16_d16_hi v138,v138
ds_read_b64_tr_b16 v[100:101],v3 offset:12392
v_pk_fma_f16 v120,v120,s55,s55
v_pk_fma_f16 v122,v122,s55,s55
v_rcp_f16 v121,v120
v_mfma_f32_16x16x32_bf16 v[148:151],v[76:79],v[48:51],v[148:151]
ds_read_u16 v137,v137
ds_read_b64_tr_b16 v[102:103],v3 offset:13928
v_rcp_f16 v123,v122
v_mfma_f32_16x16x32_bf16 v[144:147],v[80:83],v[56:59],v[144:147]
ds_read_u16_d16_hi v139,v139
ds_read_b64_tr_b16 v[64:65],v1 offset:12320
v_rcp_f16_sdwa v121,v120 dst_sel:WORD_1 dst_unused:UNUSED_PRESERVE src0_sel:WORD_1
v_mfma_f32_16x16x32_bf16 v[152:155],v[80:83],v[60:63],v[152:155]
ds_read_b64_tr_b16 v[66:67],v1 offset:14368
v_rcp_f16_sdwa v123,v122 dst_sel:WORD_1 dst_unused:UNUSED_PRESERVE src0_sel:WORD_1
v_fma_mix_f32 v140,v140,v121,0 op_sel_hi:[0,1,0]
v_mfma_f32_16x16x32_bf16 v[156:159],v[84:87],v[60:63],v[156:159]
ds_read_b128 v[112:115],v4 offset:512
v_fma_mix_f32 v142,v142,v121,0 op_sel:[0,1,0] op_sel_hi:[0,1,0]
s_waitcnt lgkmcnt(15)
v_lshl_add_u64 v[128:129],v[128:129],0,v[130:131]
s_waitcnt lgkmcnt(11)
v_lshl_add_u64 v[130:131],v[132:133],0,v[134:135]
v_mfma_f32_16x16x32_bf16 v[148:151],v[84:87],v[56:59],v[148:151]
ds_read_b64_tr_b16 v[68:69],v2 offset:12320
v_fma_mix_f32 v141,v141,v123,0 op_sel_hi:[0,1,0]
v_fma_mix_f32 v143,v143,v123,0 op_sel:[0,1,0] op_sel_hi:[0,1,0]
v_lshlrev_b32_sdwa v144,1,v144 dst_sel:DWORD dst_unused:UNUSED_PAD src0_sel:DWORD src1_sel:WORD_1
v_lshlrev_b32_sdwa v146,1,v146 dst_sel:DWORD dst_unused:UNUSED_PAD src0_sel:DWORD src1_sel:WORD_1
v_mfma_f32_16x16x32_bf16 v[160:163],v[88:91],v[128:131],v[160:163]
ds_read_u16 v144,v144
v_lshlrev_b32_sdwa v145,1,v145 dst_sel:DWORD dst_unused:UNUSED_PAD src0_sel:DWORD src1_sel:WORD_1
v_lshlrev_b32_sdwa v147,1,v147 dst_sel:DWORD dst_unused:UNUSED_PAD src0_sel:DWORD src1_sel:WORD_1
v_mfma_f32_16x16x32_bf16 v[168:171],v[92:95],v[128:131],v[168:171]
s_waitcnt lgkmcnt(10)
ds_read_b64_tr_b16 v[70:71],v2 offset:14368
ds_read_b128 v[116:119],v4 offset:544
v_fma_mixlo_f16 v120,v156,s99,0
v_fma_mixlo_f16 v122,v157,s99,0
v_mfma_f32_16x16x32_bf16 v[176:179],v[96:99],v[128:131],v[176:179]
s_waitcnt lgkmcnt(8)
ds_read_u16_d16_hi v146,v146
v_lshlrev_b32_sdwa v148,1,v148 dst_sel:DWORD dst_unused:UNUSED_PAD src0_sel:DWORD src1_sel:WORD_1
v_lshlrev_b32_sdwa v150,1,v150 dst_sel:DWORD dst_unused:UNUSED_PAD src0_sel:DWORD src1_sel:WORD_1
v_mfma_f32_16x16x32_bf16 v[184:187],v[100:103],v[128:131],v[184:187]
s_waitcnt lgkmcnt(8)
ds_read_b64_tr_b16 v[72:73],v1 offset:16416
ds_read_b64_tr_b16 v[74:75],v1 offset:18464
v_lshl_add_u64 v[136:137],v[136:137],0,v[138:139]
v_mfma_f32_16x16x32_bf16 v[192:195],v[104:107],v[128:131],v[192:195]
ds_read_u16 v145,v145
v_cvt_pk_bf16_f32 v138,v140,v142
v_cvt_pk_bf16_f32 v139,v141,v143
v_mfma_f32_16x16x32_bf16 v[200:203],v[108:111],v[128:131],v[200:203]
ds_read_b64_tr_b16 v[76:77],v2 offset:16416
ds_read_b64_tr_b16 v[78:79],v2 offset:18464
v_fma_mixhi_f16 v120,v158,s99,0
v_fma_mixhi_f16 v122,v159,s99,0
v_mfma_f32_16x16x32_bf16 v[164:167],v[88:91],v[136:139],v[164:167]
ds_read_u16_d16_hi v147,v147
v_lshlrev_b32_sdwa v149,1,v149 dst_sel:DWORD dst_unused:UNUSED_PAD src0_sel:DWORD src1_sel:WORD_1
v_lshlrev_b32_sdwa v151,1,v151 dst_sel:DWORD dst_unused:UNUSED_PAD src0_sel:DWORD src1_sel:WORD_1
v_mfma_f32_16x16x32_bf16 v[172:175],v[92:95],v[136:139],v[172:175]
ds_read_b64_tr_b16 v[80:81],v1 offset:20512
ds_read_b64_tr_b16 v[82:83],v1 offset:22560
v_pk_fma_f16 v121,v120,v120,s54
v_pk_fma_f16 v123,v122,v122,s54
v_mfma_f32_16x16x32_bf16 v[180:183],v[96:99],v[136:139],v[180:183]
ds_read_u16 v148,v148
v_pk_mul_f16 v121,v120,v121
v_pk_mul_f16 v123,v122,v123
v_mfma_f32_16x16x32_bf16 v[188:191],v[100:103],v[136:139],v[188:191]
ds_read_b64_tr_b16 v[84:85],v2 offset:20512
v_lshlrev_b32_sdwa v152,1,v152 dst_sel:DWORD dst_unused:UNUSED_PAD src0_sel:DWORD src1_sel:WORD_1
v_lshlrev_b32_sdwa v154,1,v154 dst_sel:DWORD dst_unused:UNUSED_PAD src0_sel:DWORD src1_sel:WORD_1
v_mfma_f32_16x16x32_bf16 v[196:199],v[104:107],v[136:139],v[196:199]
ds_read_u16_d16_hi v150,v150
v_exp_f16 v120,v121
v_mfma_f32_16x16x32_bf16 v[204:207],v[108:111],v[136:139],v[204:207]
s_waitcnt lgkmcnt(15)
ds_read_b64_tr_b16 v[86:87],v2 offset:22560
v_exp_f16 v122,v123
v_mfma_f32_16x16x32_bf16 v[128:131],v[64:67],v[16:19],v[112:115]
ds_read_u16 v149,v149
v_lshlrev_b32_sdwa v153,1,v153 dst_sel:DWORD dst_unused:UNUSED_PAD src0_sel:DWORD src1_sel:WORD_1
v_lshlrev_b32_sdwa v155,1,v155 dst_sel:DWORD dst_unused:UNUSED_PAD src0_sel:DWORD src1_sel:WORD_1
v_mfma_f32_16x16x32_bf16 v[136:139],v[64:67],v[20:23],v[112:115]
s_waitcnt lgkmcnt(14)
v_exp_f16_sdwa v120,v121 dst_sel:WORD_1 dst_unused:UNUSED_PRESERVE src0_sel:WORD_1
v_mfma_f32_16x16x32_bf16 v[140:143],v[68:71],v[20:23],v[116:119]
ds_read_u16_d16_hi v151,v151
v_exp_f16_sdwa v122,v123 dst_sel:WORD_1 dst_unused:UNUSED_PRESERVE src0_sel:WORD_1
v_mfma_f32_16x16x32_bf16 v[132:135],v[68:71],v[16:19],v[116:119]
s_waitcnt lgkmcnt(12)
v_pk_fma_f16 v120,v120,s55,s55
v_pk_fma_f16 v122,v122,s55,s55
v_mfma_f32_16x16x32_bf16 v[128:131],v[72:75],v[24:27],v[128:131]
ds_read_u16 v152,v152
v_rcp_f16 v121,v120
v_mfma_f32_16x16x32_bf16 v[136:139],v[72:75],v[28:31],v[136:139]
s_waitcnt lgkmcnt(10)
v_rcp_f16 v123,v122
v_mfma_f32_16x16x32_bf16 v[140:143],v[76:79],v[28:31],v[140:143]
ds_read_u16_d16_hi v154,v154
v_rcp_f16_sdwa v121,v120 dst_sel:WORD_1 dst_unused:UNUSED_PRESERVE src0_sel:WORD_1
v_mfma_f32_16x16x32_bf16 v[132:135],v[76:79],v[24:27],v[132:135]
s_waitcnt lgkmcnt(8)
v_rcp_f16_sdwa v123,v122 dst_sel:WORD_1 dst_unused:UNUSED_PRESERVE src0_sel:WORD_1
v_mfma_f32_16x16x32_bf16 v[128:131],v[80:83],v[32:35],v[128:131]
ds_read_u16 v153,v153
v_fma_mix_f32 v156,v156,v121,0 op_sel_hi:[0,1,0]
v_fma_mix_f32 v158,v158,v121,0 op_sel:[0,1,0] op_sel_hi:[0,1,0]
v_mfma_f32_16x16x32_bf16 v[136:139],v[80:83],v[36:39],v[136:139]
s_waitcnt lgkmcnt(3)
v_lshl_add_u64 v[144:145],v[144:145],0,v[146:147]
v_lshl_add_u64 v[146:147],v[148:149],0,v[150:151]
v_mfma_f32_16x16x32_bf16 v[140:143],v[84:87],v[36:39],v[140:143]
ds_read_u16_d16_hi v155,v155
v_fma_mix_f32 v157,v157,v123,0 op_sel_hi:[0,1,0]
v_fma_mix_f32 v159,v159,v123,0 op_sel:[0,1,0] op_sel_hi:[0,1,0]
v_mfma_f32_16x16x32_bf16 v[132:135],v[84:87],v[32:35],v[132:135]
v_lshlrev_b32_sdwa v128,1,v128 dst_sel:DWORD dst_unused:UNUSED_PAD src0_sel:DWORD src1_sel:WORD_1
v_lshlrev_b32_sdwa v130,1,v130 dst_sel:DWORD dst_unused:UNUSED_PAD src0_sel:DWORD src1_sel:WORD_1
v_mfma_f32_16x16x32_bf16 v[208:211],v[88:91],v[144:147],v[208:211]
ds_read_u16 v128,v128
v_lshlrev_b32_sdwa v129,1,v129 dst_sel:DWORD dst_unused:UNUSED_PAD src0_sel:DWORD src1_sel:WORD_1
v_lshlrev_b32_sdwa v131,1,v131 dst_sel:DWORD dst_unused:UNUSED_PAD src0_sel:DWORD src1_sel:WORD_1
v_mfma_f32_16x16x32_bf16 v[216:219],v[92:95],v[144:147],v[216:219]
v_fma_mixlo_f16 v120,v140,s99,0
v_fma_mixlo_f16 v122,v141,s99,0
v_mfma_f32_16x16x32_bf16 v[224:227],v[96:99],v[144:147],v[224:227]
ds_read_u16_d16_hi v130,v130
v_lshlrev_b32_sdwa v132,1,v132 dst_sel:DWORD dst_unused:UNUSED_PAD src0_sel:DWORD src1_sel:WORD_1
v_lshlrev_b32_sdwa v134,1,v134 dst_sel:DWORD dst_unused:UNUSED_PAD src0_sel:DWORD src1_sel:WORD_1
v_mfma_f32_16x16x32_bf16 v[232:235],v[100:103],v[144:147],v[232:235]
s_waitcnt lgkmcnt(2)
v_lshl_add_u64 v[152:153],v[152:153],0,v[154:155]
v_mfma_f32_16x16x32_bf16 v[240:243],v[104:107],v[144:147],v[240:243]
ds_read_u16 v129,v129
v_cvt_pk_bf16_f32 v154,v156,v158
v_cvt_pk_bf16_f32 v155,v157,v159
v_mfma_f32_16x16x32_bf16 v[248:251],v[108:111],v[144:147],v[248:251]
v_fma_mixhi_f16 v120,v142,s99,0
v_fma_mixhi_f16 v122,v143,s99,0
v_mfma_f32_16x16x32_bf16 v[212:215],v[88:91],v[152:155],v[212:215]
ds_read_u16_d16_hi v131,v131
v_lshlrev_b32_sdwa v133,1,v133 dst_sel:DWORD dst_unused:UNUSED_PAD src0_sel:DWORD src1_sel:WORD_1
v_lshlrev_b32_sdwa v135,1,v135 dst_sel:DWORD dst_unused:UNUSED_PAD src0_sel:DWORD src1_sel:WORD_1
v_mfma_f32_16x16x32_bf16 v[220:223],v[92:95],v[152:155],v[220:223]
s_setprio 2
ds_read_b64_tr_b16 v[88:89],v3 offset:18480
v_pk_fma_f16 v121,v120,v120,s54
v_pk_fma_f16 v123,v122,v122,s54
v_mfma_f32_16x16x32_bf16 v[228:231],v[96:99],v[152:155],v[228:231]
ds_read_u16 v132,v132
v_pk_mul_f16 v121,v120,v121
v_pk_mul_f16 v123,v122,v123
v_mfma_f32_16x16x32_bf16 v[236:239],v[100:103],v[152:155],v[236:239]
ds_read_b64_tr_b16 v[90:91],v3 offset:20016
v_lshlrev_b32_sdwa v136,1,v136 dst_sel:DWORD dst_unused:UNUSED_PAD src0_sel:DWORD src1_sel:WORD_1
v_lshlrev_b32_sdwa v138,1,v138 dst_sel:DWORD dst_unused:UNUSED_PAD src0_sel:DWORD src1_sel:WORD_1
v_mfma_f32_16x16x32_bf16 v[244:247],v[104:107],v[152:155],v[244:247]
ds_read_u16_d16_hi v134,v134
v_exp_f16 v120,v121
v_mfma_f32_16x16x32_bf16 v[252:255],v[108:111],v[152:155],v[252:255]
ds_read_b64_tr_b16 v[92:93],v3 offset:18488
v_exp_f16 v122,v123
v_mfma_f32_16x16x32_bf16 v[144:147],v[64:67],v[40:43],v[112:115]
ds_read_u16 v133,v133
v_lshlrev_b32_sdwa v137,1,v137 dst_sel:DWORD dst_unused:UNUSED_PAD src0_sel:DWORD src1_sel:WORD_1
v_lshlrev_b32_sdwa v139,1,v139 dst_sel:DWORD dst_unused:UNUSED_PAD src0_sel:DWORD src1_sel:WORD_1
v_mfma_f32_16x16x32_bf16 v[152:155],v[64:67],v[44:47],v[112:115]
ds_read_b64_tr_b16 v[94:95],v3 offset:20024
v_exp_f16_sdwa v120,v121 dst_sel:WORD_1 dst_unused:UNUSED_PRESERVE src0_sel:WORD_1
v_mfma_f32_16x16x32_bf16 v[156:159],v[68:71],v[44:47],v[116:119]
ds_read_u16_d16_hi v135,v135
v_exp_f16_sdwa v122,v123 dst_sel:WORD_1 dst_unused:UNUSED_PRESERVE src0_sel:WORD_1
v_mfma_f32_16x16x32_bf16 v[148:151],v[68:71],v[40:43],v[116:119]
ds_read_b64_tr_b16 v[96:97],v3 offset:18544
v_pk_fma_f16 v120,v120,s55,s55
v_pk_fma_f16 v122,v122,s55,s55
v_mfma_f32_16x16x32_bf16 v[144:147],v[72:75],v[48:51],v[144:147]
ds_read_u16 v136,v136
v_rcp_f16 v121,v120
v_mfma_f32_16x16x32_bf16 v[152:155],v[72:75],v[52:55],v[152:155]
ds_read_b64_tr_b16 v[98:99],v3 offset:20080
ds_read_b64_tr_b16 v[100:101],v3 offset:18552
v_rcp_f16 v123,v122
v_mfma_f32_16x16x32_bf16 v[156:159],v[76:79],v[52:55],v[156:159]
ds_read_u16_d16_hi v138,v138
v_rcp_f16_sdwa v121,v120 dst_sel:WORD_1 dst_unused:UNUSED_PRESERVE src0_sel:WORD_1
v_mfma_f32_16x16x32_bf16 v[148:151],v[76:79],v[48:51],v[148:151]
ds_read_b64_tr_b16 v[102:103],v3 offset:20088
ds_read_b64_tr_b16 v[104:105],v3 offset:18608
v_rcp_f16_sdwa v123,v122 dst_sel:WORD_1 dst_unused:UNUSED_PRESERVE src0_sel:WORD_1
v_mfma_f32_16x16x32_bf16 v[144:147],v[80:83],v[56:59],v[144:147]
ds_read_u16 v137,v137
v_fma_mix_f32 v140,v140,v121,0 op_sel_hi:[0,1,0]
v_fma_mix_f32 v142,v142,v121,0 op_sel:[0,1,0] op_sel_hi:[0,1,0]
v_mfma_f32_16x16x32_bf16 v[152:155],v[80:83],v[60:63],v[152:155]
s_waitcnt lgkmcnt(8)
ds_read_b64_tr_b16 v[106:107],v3 offset:20144
ds_read_b64_tr_b16 v[108:109],v3 offset:18616
v_lshl_add_u64 v[128:129],v[128:129],0,v[130:131]
v_lshl_add_u64 v[130:131],v[132:133],0,v[134:135]
v_mfma_f32_16x16x32_bf16 v[156:159],v[84:87],v[60:63],v[156:159]
ds_read_u16_d16_hi v139,v139
v_fma_mix_f32 v141,v141,v123,0 op_sel_hi:[0,1,0]
v_fma_mix_f32 v143,v143,v123,0 op_sel:[0,1,0] op_sel_hi:[0,1,0]
v_mfma_f32_16x16x32_bf16 v[148:151],v[84:87],v[56:59],v[148:151]
ds_read_b64_tr_b16 v[110:111],v3 offset:20152
ds_read_b64_tr_b16 v[64:65],v1 offset:12832
v_lshlrev_b32_sdwa v144,1,v144 dst_sel:DWORD dst_unused:UNUSED_PAD src0_sel:DWORD src1_sel:WORD_1
v_lshlrev_b32_sdwa v146,1,v146 dst_sel:DWORD dst_unused:UNUSED_PAD src0_sel:DWORD src1_sel:WORD_1
v_mfma_f32_16x16x32_bf16 v[160:163],v[88:91],v[128:131],v[160:163]
ds_read_u16 v144,v144
v_lshlrev_b32_sdwa v145,1,v145 dst_sel:DWORD dst_unused:UNUSED_PAD src0_sel:DWORD src1_sel:WORD_1
v_lshlrev_b32_sdwa v147,1,v147 dst_sel:DWORD dst_unused:UNUSED_PAD src0_sel:DWORD src1_sel:WORD_1
v_mfma_f32_16x16x32_bf16 v[168:171],v[92:95],v[128:131],v[168:171]
s_waitcnt lgkmcnt(11)
ds_read_b64_tr_b16 v[66:67],v1 offset:14880
ds_read_b128 v[112:115],v4 offset:640
v_fma_mixlo_f16 v120,v156,s99,0
v_fma_mixlo_f16 v122,v157,s99,0
v_mfma_f32_16x16x32_bf16 v[176:179],v[96:99],v[128:131],v[176:179]
s_waitcnt lgkmcnt(10)
ds_read_u16_d16_hi v146,v146
v_lshlrev_b32_sdwa v148,1,v148 dst_sel:DWORD dst_unused:UNUSED_PAD src0_sel:DWORD src1_sel:WORD_1
v_lshlrev_b32_sdwa v150,1,v150 dst_sel:DWORD dst_unused:UNUSED_PAD src0_sel:DWORD src1_sel:WORD_1
v_mfma_f32_16x16x32_bf16 v[184:187],v[100:103],v[128:131],v[184:187]
s_waitcnt lgkmcnt(6)
ds_read_b64_tr_b16 v[68:69],v2 offset:12832
ds_read_b64_tr_b16 v[70:71],v2 offset:14880
v_lshl_add_u64 v[136:137],v[136:137],0,v[138:139]
v_mfma_f32_16x16x32_bf16 v[192:195],v[104:107],v[128:131],v[192:195]
s_waitcnt lgkmcnt(7)
ds_read_u16 v145,v145
v_cvt_pk_bf16_f32 v138,v140,v142
v_cvt_pk_bf16_f32 v139,v141,v143
v_mfma_f32_16x16x32_bf16 v[200:203],v[108:111],v[128:131],v[200:203]
ds_read_b128 v[116:119],v4 offset:672
ds_read_b64_tr_b16 v[72:73],v1 offset:16928
v_fma_mixhi_f16 v120,v158,s99,0
v_fma_mixhi_f16 v122,v159,s99,0
v_mfma_f32_16x16x32_bf16 v[164:167],v[88:91],v[136:139],v[164:167]
ds_read_u16_d16_hi v147,v147
v_lshlrev_b32_sdwa v149,1,v149 dst_sel:DWORD dst_unused:UNUSED_PAD src0_sel:DWORD src1_sel:WORD_1
v_lshlrev_b32_sdwa v151,1,v151 dst_sel:DWORD dst_unused:UNUSED_PAD src0_sel:DWORD src1_sel:WORD_1
v_mfma_f32_16x16x32_bf16 v[172:175],v[92:95],v[136:139],v[172:175]
ds_read_b64_tr_b16 v[74:75],v1 offset:18976
ds_read_b64_tr_b16 v[76:77],v2 offset:16928
v_pk_fma_f16 v121,v120,v120,s54
v_pk_fma_f16 v123,v122,v122,s54
v_mfma_f32_16x16x32_bf16 v[180:183],v[96:99],v[136:139],v[180:183]
s_setprio 1
ds_read_u16 v148,v148
v_pk_mul_f16 v121,v120,v121
v_pk_mul_f16 v123,v122,v123
v_mfma_f32_16x16x32_bf16 v[188:191],v[100:103],v[136:139],v[188:191]
ds_read_b64_tr_b16 v[78:79],v2 offset:18976
v_lshlrev_b32_sdwa v152,1,v152 dst_sel:DWORD dst_unused:UNUSED_PAD src0_sel:DWORD src1_sel:WORD_1
v_lshlrev_b32_sdwa v154,1,v154 dst_sel:DWORD dst_unused:UNUSED_PAD src0_sel:DWORD src1_sel:WORD_1
v_mfma_f32_16x16x32_bf16 v[196:199],v[104:107],v[136:139],v[196:199]
ds_read_u16_d16_hi v150,v150
v_exp_f16 v120,v121
v_mfma_f32_16x16x32_bf16 v[204:207],v[108:111],v[136:139],v[204:207]
s_waitcnt lgkmcnt(12)
ds_read_b64_tr_b16 v[80:81],v1 offset:21024
v_exp_f16 v122,v123
v_mfma_f32_16x16x32_bf16 v[128:131],v[64:67],v[16:19],v[112:115]
ds_read_u16 v149,v149
v_lshlrev_b32_sdwa v153,1,v153 dst_sel:DWORD dst_unused:UNUSED_PAD src0_sel:DWORD src1_sel:WORD_1
v_lshlrev_b32_sdwa v155,1,v155 dst_sel:DWORD dst_unused:UNUSED_PAD src0_sel:DWORD src1_sel:WORD_1
v_mfma_f32_16x16x32_bf16 v[136:139],v[64:67],v[20:23],v[112:115]
s_waitcnt lgkmcnt(9)
ds_read_b64_tr_b16 v[82:83],v1 offset:23072
v_exp_f16_sdwa v120,v121 dst_sel:WORD_1 dst_unused:UNUSED_PRESERVE src0_sel:WORD_1
v_mfma_f32_16x16x32_bf16 v[140:143],v[68:71],v[20:23],v[116:119]
ds_read_u16_d16_hi v151,v151
v_exp_f16_sdwa v122,v123 dst_sel:WORD_1 dst_unused:UNUSED_PRESERVE src0_sel:WORD_1
v_mfma_f32_16x16x32_bf16 v[132:135],v[68:71],v[16:19],v[116:119]
s_waitcnt lgkmcnt(8)
ds_read_b64_tr_b16 v[84:85],v2 offset:21024
v_pk_fma_f16 v120,v120,s55,s55
v_pk_fma_f16 v122,v122,s55,s55
v_mfma_f32_16x16x32_bf16 v[128:131],v[72:75],v[24:27],v[128:131]
ds_read_u16 v152,v152
v_rcp_f16 v121,v120
v_mfma_f32_16x16x32_bf16 v[136:139],v[72:75],v[28:31],v[136:139]
s_waitcnt lgkmcnt(7)
ds_read_b64_tr_b16 v[86:87],v2 offset:23072
v_rcp_f16 v123,v122
v_mfma_f32_16x16x32_bf16 v[140:143],v[76:79],v[28:31],v[140:143]
ds_read_u16_d16_hi v154,v154
v_rcp_f16_sdwa v121,v120 dst_sel:WORD_1 dst_unused:UNUSED_PRESERVE src0_sel:WORD_1
v_mfma_f32_16x16x32_bf16 v[132:135],v[76:79],v[24:27],v[132:135]
s_waitcnt lgkmcnt(5)
v_rcp_f16_sdwa v123,v122 dst_sel:WORD_1 dst_unused:UNUSED_PRESERVE src0_sel:WORD_1
v_mfma_f32_16x16x32_bf16 v[128:131],v[80:83],v[32:35],v[128:131]
ds_read_u16 v153,v153
v_fma_mix_f32 v156,v156,v121,0 op_sel_hi:[0,1,0]
v_fma_mix_f32 v158,v158,v121,0 op_sel:[0,1,0] op_sel_hi:[0,1,0]
v_mfma_f32_16x16x32_bf16 v[136:139],v[80:83],v[36:39],v[136:139]
s_waitcnt lgkmcnt(2)
s_setprio 0
v_lshl_add_u64 v[144:145],v[144:145],0,v[146:147]
v_lshl_add_u64 v[146:147],v[148:149],0,v[150:151]
v_mfma_f32_16x16x32_bf16 v[140:143],v[84:87],v[36:39],v[140:143]
ds_read_u16_d16_hi v155,v155
v_fma_mix_f32 v157,v157,v123,0 op_sel_hi:[0,1,0]
v_fma_mix_f32 v159,v159,v123,0 op_sel:[0,1,0] op_sel_hi:[0,1,0]
v_mfma_f32_16x16x32_bf16 v[132:135],v[84:87],v[32:35],v[132:135]
v_lshlrev_b32_sdwa v128,1,v128 dst_sel:DWORD dst_unused:UNUSED_PAD src0_sel:DWORD src1_sel:WORD_1
v_lshlrev_b32_sdwa v130,1,v130 dst_sel:DWORD dst_unused:UNUSED_PAD src0_sel:DWORD src1_sel:WORD_1
v_mfma_f32_16x16x32_bf16 v[208:211],v[88:91],v[144:147],v[208:211]
s_barrier
s_setprio 3
ds_read_u16 v128,v128
v_lshlrev_b32_sdwa v129,1,v129 dst_sel:DWORD dst_unused:UNUSED_PAD src0_sel:DWORD src1_sel:WORD_1
v_lshlrev_b32_sdwa v131,1,v131 dst_sel:DWORD dst_unused:UNUSED_PAD src0_sel:DWORD src1_sel:WORD_1
v_mfma_f32_16x16x32_bf16 v[216:219],v[92:95],v[144:147],v[216:219]
v_fma_mixlo_f16 v120,v140,s99,0
v_fma_mixlo_f16 v122,v141,s99,0
v_mfma_f32_16x16x32_bf16 v[224:227],v[96:99],v[144:147],v[224:227]
ds_read_u16_d16_hi v130,v130
v_lshlrev_b32_sdwa v132,1,v132 dst_sel:DWORD dst_unused:UNUSED_PAD src0_sel:DWORD src1_sel:WORD_1
v_lshlrev_b32_sdwa v134,1,v134 dst_sel:DWORD dst_unused:UNUSED_PAD src0_sel:DWORD src1_sel:WORD_1
v_mfma_f32_16x16x32_bf16 v[232:235],v[100:103],v[144:147],v[232:235]
s_waitcnt lgkmcnt(2)
v_lshl_add_u64 v[152:153],v[152:153],0,v[154:155]
v_mfma_f32_16x16x32_bf16 v[240:243],v[104:107],v[144:147],v[240:243]
ds_read_u16 v129,v129
v_cvt_pk_bf16_f32 v154,v156,v158
v_cvt_pk_bf16_f32 v155,v157,v159
v_mfma_f32_16x16x32_bf16 v[248:251],v[108:111],v[144:147],v[248:251]
v_fma_mixhi_f16 v120,v142,s99,0
v_fma_mixhi_f16 v122,v143,s99,0
v_mfma_f32_16x16x32_bf16 v[212:215],v[88:91],v[152:155],v[212:215]
ds_read_u16_d16_hi v131,v131
v_lshlrev_b32_sdwa v133,1,v133 dst_sel:DWORD dst_unused:UNUSED_PAD src0_sel:DWORD src1_sel:WORD_1
v_lshlrev_b32_sdwa v135,1,v135 dst_sel:DWORD dst_unused:UNUSED_PAD src0_sel:DWORD src1_sel:WORD_1
v_mfma_f32_16x16x32_bf16 v[220:223],v[92:95],v[152:155],v[220:223]
ds_read_b64_tr_b16 v[88:89],v3 offset:0
v_pk_fma_f16 v121,v120,v120,s54
v_pk_fma_f16 v123,v122,v122,s54
v_mfma_f32_16x16x32_bf16 v[228:231],v[96:99],v[152:155],v[228:231]
ds_read_u16 v132,v132
v_pk_mul_f16 v121,v120,v121
v_pk_mul_f16 v123,v122,v123
v_mfma_f32_16x16x32_bf16 v[236:239],v[100:103],v[152:155],v[236:239]
ds_read_b64_tr_b16 v[90:91],v3 offset:1536
v_lshlrev_b32_sdwa v136,1,v136 dst_sel:DWORD dst_unused:UNUSED_PAD src0_sel:DWORD src1_sel:WORD_1
v_lshlrev_b32_sdwa v138,1,v138 dst_sel:DWORD dst_unused:UNUSED_PAD src0_sel:DWORD src1_sel:WORD_1
v_mfma_f32_16x16x32_bf16 v[244:247],v[104:107],v[152:155],v[244:247]
ds_read_u16_d16_hi v134,v134
v_exp_f16 v120,v121
v_mfma_f32_16x16x32_bf16 v[252:255],v[108:111],v[152:155],v[252:255]
ds_read_b64_tr_b16 v[92:93],v3 offset:8
v_exp_f16 v122,v123
v_mfma_f32_16x16x32_bf16 v[144:147],v[64:67],v[40:43],v[112:115]
ds_read_u16 v133,v133
v_lshlrev_b32_sdwa v137,1,v137 dst_sel:DWORD dst_unused:UNUSED_PAD src0_sel:DWORD src1_sel:WORD_1
v_lshlrev_b32_sdwa v139,1,v139 dst_sel:DWORD dst_unused:UNUSED_PAD src0_sel:DWORD src1_sel:WORD_1
v_mfma_f32_16x16x32_bf16 v[152:155],v[64:67],v[44:47],v[112:115]
ds_read_b64_tr_b16 v[94:95],v3 offset:1544
v_exp_f16_sdwa v120,v121 dst_sel:WORD_1 dst_unused:UNUSED_PRESERVE src0_sel:WORD_1
v_mfma_f32_16x16x32_bf16 v[156:159],v[68:71],v[44:47],v[116:119]
ds_read_u16_d16_hi v135,v135
v_exp_f16_sdwa v122,v123 dst_sel:WORD_1 dst_unused:UNUSED_PRESERVE src0_sel:WORD_1
v_mfma_f32_16x16x32_bf16 v[148:151],v[68:71],v[40:43],v[116:119]
ds_read_b64_tr_b16 v[96:97],v3 offset:64
v_pk_fma_f16 v120,v120,s55,s55
v_pk_fma_f16 v122,v122,s55,s55
v_mfma_f32_16x16x32_bf16 v[144:147],v[72:75],v[48:51],v[144:147]
ds_read_u16 v136,v136
v_rcp_f16 v121,v120
v_mfma_f32_16x16x32_bf16 v[152:155],v[72:75],v[52:55],v[152:155]
ds_read_b64_tr_b16 v[98:99],v3 offset:1600
ds_read_b64_tr_b16 v[100:101],v3 offset:72
v_rcp_f16 v123,v122
v_mfma_f32_16x16x32_bf16 v[156:159],v[76:79],v[52:55],v[156:159]
ds_read_u16_d16_hi v138,v138
v_rcp_f16_sdwa v121,v120 dst_sel:WORD_1 dst_unused:UNUSED_PRESERVE src0_sel:WORD_1
v_mfma_f32_16x16x32_bf16 v[148:151],v[76:79],v[48:51],v[148:151]
ds_read_b64_tr_b16 v[102:103],v3 offset:1608
ds_read_b64_tr_b16 v[104:105],v3 offset:128
v_rcp_f16_sdwa v123,v122 dst_sel:WORD_1 dst_unused:UNUSED_PRESERVE src0_sel:WORD_1
v_mfma_f32_16x16x32_bf16 v[144:147],v[80:83],v[56:59],v[144:147]
ds_read_u16 v137,v137
v_fma_mix_f32 v140,v140,v121,0 op_sel_hi:[0,1,0]
v_fma_mix_f32 v142,v142,v121,0 op_sel:[0,1,0] op_sel_hi:[0,1,0]
v_mfma_f32_16x16x32_bf16 v[152:155],v[80:83],v[60:63],v[152:155]
s_waitcnt lgkmcnt(8)
ds_read_b64_tr_b16 v[106:107],v3 offset:1664
ds_read_b64_tr_b16 v[108:109],v3 offset:136
v_lshl_add_u64 v[128:129],v[128:129],0,v[130:131]
v_lshl_add_u64 v[130:131],v[132:133],0,v[134:135]
v_mfma_f32_16x16x32_bf16 v[156:159],v[84:87],v[60:63],v[156:159]
ds_read_u16_d16_hi v139,v139
v_fma_mix_f32 v141,v141,v123,0 op_sel_hi:[0,1,0]
v_fma_mix_f32 v143,v143,v123,0 op_sel:[0,1,0] op_sel_hi:[0,1,0]
v_mfma_f32_16x16x32_bf16 v[148:151],v[84:87],v[56:59],v[148:151]
ds_read_b64_tr_b16 v[110:111],v3 offset:1672
ds_read_b64_tr_b16 v[64:65],v1 offset:0
v_lshlrev_b32_sdwa v144,1,v144 dst_sel:DWORD dst_unused:UNUSED_PAD src0_sel:DWORD src1_sel:WORD_1
v_lshlrev_b32_sdwa v146,1,v146 dst_sel:DWORD dst_unused:UNUSED_PAD src0_sel:DWORD src1_sel:WORD_1
v_mfma_f32_16x16x32_bf16 v[160:163],v[88:91],v[128:131],v[160:163]
ds_read_u16 v144,v144
v_lshlrev_b32_sdwa v145,1,v145 dst_sel:DWORD dst_unused:UNUSED_PAD src0_sel:DWORD src1_sel:WORD_1
v_lshlrev_b32_sdwa v147,1,v147 dst_sel:DWORD dst_unused:UNUSED_PAD src0_sel:DWORD src1_sel:WORD_1
v_mfma_f32_16x16x32_bf16 v[168:171],v[92:95],v[128:131],v[168:171]
s_waitcnt lgkmcnt(11)
ds_read_b64_tr_b16 v[66:67],v1 offset:2048
ds_read_b128 v[112:115],v4 offset:768
v_fma_mixlo_f16 v120,v156,s99,0
v_fma_mixlo_f16 v122,v157,s99,0
v_mfma_f32_16x16x32_bf16 v[176:179],v[96:99],v[128:131],v[176:179]
s_waitcnt lgkmcnt(10)
ds_read_u16_d16_hi v146,v146
v_lshlrev_b32_sdwa v148,1,v148 dst_sel:DWORD dst_unused:UNUSED_PAD src0_sel:DWORD src1_sel:WORD_1
v_lshlrev_b32_sdwa v150,1,v150 dst_sel:DWORD dst_unused:UNUSED_PAD src0_sel:DWORD src1_sel:WORD_1
v_mfma_f32_16x16x32_bf16 v[184:187],v[100:103],v[128:131],v[184:187]
s_waitcnt lgkmcnt(6)
ds_read_b64_tr_b16 v[68:69],v2 offset:0
ds_read_b64_tr_b16 v[70:71],v2 offset:2048
v_lshl_add_u64 v[136:137],v[136:137],0,v[138:139]
v_mfma_f32_16x16x32_bf16 v[192:195],v[104:107],v[128:131],v[192:195]
s_waitcnt lgkmcnt(7)
ds_read_u16 v145,v145
v_cvt_pk_bf16_f32 v138,v140,v142
v_cvt_pk_bf16_f32 v139,v141,v143
v_mfma_f32_16x16x32_bf16 v[200:203],v[108:111],v[128:131],v[200:203]
ds_read_b128 v[116:119],v4 offset:800
ds_read_b64_tr_b16 v[72:73],v1 offset:4096
v_fma_mixhi_f16 v120,v158,s99,0
v_fma_mixhi_f16 v122,v159,s99,0
v_mfma_f32_16x16x32_bf16 v[164:167],v[88:91],v[136:139],v[164:167]
ds_read_u16_d16_hi v147,v147
v_lshlrev_b32_sdwa v149,1,v149 dst_sel:DWORD dst_unused:UNUSED_PAD src0_sel:DWORD src1_sel:WORD_1
v_lshlrev_b32_sdwa v151,1,v151 dst_sel:DWORD dst_unused:UNUSED_PAD src0_sel:DWORD src1_sel:WORD_1
v_mfma_f32_16x16x32_bf16 v[172:175],v[92:95],v[136:139],v[172:175]
ds_read_b64_tr_b16 v[74:75],v1 offset:6144
ds_read_b64_tr_b16 v[76:77],v2 offset:4096
v_pk_fma_f16 v121,v120,v120,s54
v_pk_fma_f16 v123,v122,v122,s54
v_mfma_f32_16x16x32_bf16 v[180:183],v[96:99],v[136:139],v[180:183]
ds_read_u16 v148,v148
v_pk_mul_f16 v121,v120,v121
v_pk_mul_f16 v123,v122,v123
v_mfma_f32_16x16x32_bf16 v[188:191],v[100:103],v[136:139],v[188:191]
ds_read_b64_tr_b16 v[78:79],v2 offset:6144
v_lshlrev_b32_sdwa v152,1,v152 dst_sel:DWORD dst_unused:UNUSED_PAD src0_sel:DWORD src1_sel:WORD_1
v_lshlrev_b32_sdwa v154,1,v154 dst_sel:DWORD dst_unused:UNUSED_PAD src0_sel:DWORD src1_sel:WORD_1
v_mfma_f32_16x16x32_bf16 v[196:199],v[104:107],v[136:139],v[196:199]
ds_read_u16_d16_hi v150,v150
v_exp_f16 v120,v121
v_mfma_f32_16x16x32_bf16 v[204:207],v[108:111],v[136:139],v[204:207]
s_waitcnt lgkmcnt(12)
ds_read_b64_tr_b16 v[80:81],v1 offset:8192
v_exp_f16 v122,v123
v_mfma_f32_16x16x32_bf16 v[128:131],v[64:67],v[16:19],v[112:115]
ds_read_u16 v149,v149
v_lshlrev_b32_sdwa v153,1,v153 dst_sel:DWORD dst_unused:UNUSED_PAD src0_sel:DWORD src1_sel:WORD_1
v_lshlrev_b32_sdwa v155,1,v155 dst_sel:DWORD dst_unused:UNUSED_PAD src0_sel:DWORD src1_sel:WORD_1
v_mfma_f32_16x16x32_bf16 v[136:139],v[64:67],v[20:23],v[112:115]
s_waitcnt lgkmcnt(9)
ds_read_b64_tr_b16 v[82:83],v1 offset:10240
v_exp_f16_sdwa v120,v121 dst_sel:WORD_1 dst_unused:UNUSED_PRESERVE src0_sel:WORD_1
v_mfma_f32_16x16x32_bf16 v[140:143],v[68:71],v[20:23],v[116:119]
ds_read_u16_d16_hi v151,v151
v_exp_f16_sdwa v122,v123 dst_sel:WORD_1 dst_unused:UNUSED_PRESERVE src0_sel:WORD_1
v_mfma_f32_16x16x32_bf16 v[132:135],v[68:71],v[16:19],v[116:119]
s_waitcnt lgkmcnt(8)
ds_read_b64_tr_b16 v[84:85],v2 offset:8192
v_pk_fma_f16 v120,v120,s55,s55
v_pk_fma_f16 v122,v122,s55,s55
v_mfma_f32_16x16x32_bf16 v[128:131],v[72:75],v[24:27],v[128:131]
ds_read_u16 v152,v152
v_rcp_f16 v121,v120
v_mfma_f32_16x16x32_bf16 v[136:139],v[72:75],v[28:31],v[136:139]
s_waitcnt lgkmcnt(7)
ds_read_b64_tr_b16 v[86:87],v2 offset:10240
v_rcp_f16 v123,v122
v_mfma_f32_16x16x32_bf16 v[140:143],v[76:79],v[28:31],v[140:143]
ds_read_u16_d16_hi v154,v154
v_rcp_f16_sdwa v121,v120 dst_sel:WORD_1 dst_unused:UNUSED_PRESERVE src0_sel:WORD_1
v_mfma_f32_16x16x32_bf16 v[132:135],v[76:79],v[24:27],v[132:135]
s_waitcnt lgkmcnt(5)
v_rcp_f16_sdwa v123,v122 dst_sel:WORD_1 dst_unused:UNUSED_PRESERVE src0_sel:WORD_1
v_mfma_f32_16x16x32_bf16 v[128:131],v[80:83],v[32:35],v[128:131]
ds_read_u16 v153,v153
v_fma_mix_f32 v156,v156,v121,0 op_sel_hi:[0,1,0]
v_fma_mix_f32 v158,v158,v121,0 op_sel:[0,1,0] op_sel_hi:[0,1,0]
v_mfma_f32_16x16x32_bf16 v[136:139],v[80:83],v[36:39],v[136:139]
s_waitcnt lgkmcnt(2)
v_lshl_add_u64 v[144:145],v[144:145],0,v[146:147]
v_lshl_add_u64 v[146:147],v[148:149],0,v[150:151]
v_mfma_f32_16x16x32_bf16 v[140:143],v[84:87],v[36:39],v[140:143]
ds_read_u16_d16_hi v155,v155
v_fma_mix_f32 v157,v157,v123,0 op_sel_hi:[0,1,0]
v_fma_mix_f32 v159,v159,v123,0 op_sel:[0,1,0] op_sel_hi:[0,1,0]
v_mfma_f32_16x16x32_bf16 v[132:135],v[84:87],v[32:35],v[132:135]
v_lshlrev_b32_sdwa v128,1,v128 dst_sel:DWORD dst_unused:UNUSED_PAD src0_sel:DWORD src1_sel:WORD_1
v_lshlrev_b32_sdwa v130,1,v130 dst_sel:DWORD dst_unused:UNUSED_PAD src0_sel:DWORD src1_sel:WORD_1
v_mfma_f32_16x16x32_bf16 v[208:211],v[88:91],v[144:147],v[208:211]
ds_read_u16 v128,v128
v_lshlrev_b32_sdwa v129,1,v129 dst_sel:DWORD dst_unused:UNUSED_PAD src0_sel:DWORD src1_sel:WORD_1
v_lshlrev_b32_sdwa v131,1,v131 dst_sel:DWORD dst_unused:UNUSED_PAD src0_sel:DWORD src1_sel:WORD_1
v_mfma_f32_16x16x32_bf16 v[216:219],v[92:95],v[144:147],v[216:219]
v_fma_mixlo_f16 v120,v140,s99,0
v_fma_mixlo_f16 v122,v141,s99,0
v_mfma_f32_16x16x32_bf16 v[224:227],v[96:99],v[144:147],v[224:227]
ds_read_u16_d16_hi v130,v130
v_lshlrev_b32_sdwa v132,1,v132 dst_sel:DWORD dst_unused:UNUSED_PAD src0_sel:DWORD src1_sel:WORD_1
v_lshlrev_b32_sdwa v134,1,v134 dst_sel:DWORD dst_unused:UNUSED_PAD src0_sel:DWORD src1_sel:WORD_1
v_mfma_f32_16x16x32_bf16 v[232:235],v[100:103],v[144:147],v[232:235]
s_waitcnt lgkmcnt(2)
v_lshl_add_u64 v[152:153],v[152:153],0,v[154:155]
v_mfma_f32_16x16x32_bf16 v[240:243],v[104:107],v[144:147],v[240:243]
ds_read_u16 v129,v129
v_cvt_pk_bf16_f32 v154,v156,v158
v_cvt_pk_bf16_f32 v155,v157,v159
v_mfma_f32_16x16x32_bf16 v[248:251],v[108:111],v[144:147],v[248:251]
s_setprio 2
v_fma_mixhi_f16 v120,v142,s99,0
v_fma_mixhi_f16 v122,v143,s99,0
v_mfma_f32_16x16x32_bf16 v[212:215],v[88:91],v[152:155],v[212:215]
ds_read_u16_d16_hi v131,v131
v_lshlrev_b32_sdwa v133,1,v133 dst_sel:DWORD dst_unused:UNUSED_PAD src0_sel:DWORD src1_sel:WORD_1
v_lshlrev_b32_sdwa v135,1,v135 dst_sel:DWORD dst_unused:UNUSED_PAD src0_sel:DWORD src1_sel:WORD_1
v_mfma_f32_16x16x32_bf16 v[220:223],v[92:95],v[152:155],v[220:223]
ds_read_b64_tr_b16 v[88:89],v3 offset:6160
v_pk_fma_f16 v121,v120,v120,s54
v_pk_fma_f16 v123,v122,v122,s54
v_mfma_f32_16x16x32_bf16 v[228:231],v[96:99],v[152:155],v[228:231]
ds_read_u16 v132,v132
v_pk_mul_f16 v121,v120,v121
v_pk_mul_f16 v123,v122,v123
v_mfma_f32_16x16x32_bf16 v[236:239],v[100:103],v[152:155],v[236:239]
ds_read_b64_tr_b16 v[90:91],v3 offset:7696
v_lshlrev_b32_sdwa v136,1,v136 dst_sel:DWORD dst_unused:UNUSED_PAD src0_sel:DWORD src1_sel:WORD_1
v_lshlrev_b32_sdwa v138,1,v138 dst_sel:DWORD dst_unused:UNUSED_PAD src0_sel:DWORD src1_sel:WORD_1
v_mfma_f32_16x16x32_bf16 v[244:247],v[104:107],v[152:155],v[244:247]
ds_read_u16_d16_hi v134,v134
v_exp_f16 v120,v121
v_mfma_f32_16x16x32_bf16 v[252:255],v[108:111],v[152:155],v[252:255]
ds_read_b64_tr_b16 v[92:93],v3 offset:6168
v_exp_f16 v122,v123
v_mfma_f32_16x16x32_bf16 v[144:147],v[64:67],v[40:43],v[112:115]
ds_read_u16 v133,v133
v_lshlrev_b32_sdwa v137,1,v137 dst_sel:DWORD dst_unused:UNUSED_PAD src0_sel:DWORD src1_sel:WORD_1
v_lshlrev_b32_sdwa v139,1,v139 dst_sel:DWORD dst_unused:UNUSED_PAD src0_sel:DWORD src1_sel:WORD_1
v_mfma_f32_16x16x32_bf16 v[152:155],v[64:67],v[44:47],v[112:115]
ds_read_b64_tr_b16 v[94:95],v3 offset:7704
v_exp_f16_sdwa v120,v121 dst_sel:WORD_1 dst_unused:UNUSED_PRESERVE src0_sel:WORD_1
v_mfma_f32_16x16x32_bf16 v[156:159],v[68:71],v[44:47],v[116:119]
ds_read_u16_d16_hi v135,v135
v_exp_f16_sdwa v122,v123 dst_sel:WORD_1 dst_unused:UNUSED_PRESERVE src0_sel:WORD_1
v_mfma_f32_16x16x32_bf16 v[148:151],v[68:71],v[40:43],v[116:119]
ds_read_b64_tr_b16 v[96:97],v3 offset:6224
v_pk_fma_f16 v120,v120,s55,s55
v_pk_fma_f16 v122,v122,s55,s55
v_mfma_f32_16x16x32_bf16 v[144:147],v[72:75],v[48:51],v[144:147]
ds_read_u16 v136,v136
v_rcp_f16 v121,v120
v_mfma_f32_16x16x32_bf16 v[152:155],v[72:75],v[52:55],v[152:155]
ds_read_b64_tr_b16 v[98:99],v3 offset:7760
ds_read_b64_tr_b16 v[100:101],v3 offset:6232
v_rcp_f16 v123,v122
v_mfma_f32_16x16x32_bf16 v[156:159],v[76:79],v[52:55],v[156:159]
ds_read_u16_d16_hi v138,v138
v_rcp_f16_sdwa v121,v120 dst_sel:WORD_1 dst_unused:UNUSED_PRESERVE src0_sel:WORD_1
v_mfma_f32_16x16x32_bf16 v[148:151],v[76:79],v[48:51],v[148:151]
ds_read_b64_tr_b16 v[102:103],v3 offset:7768
ds_read_b64_tr_b16 v[104:105],v3 offset:6288
v_rcp_f16_sdwa v123,v122 dst_sel:WORD_1 dst_unused:UNUSED_PRESERVE src0_sel:WORD_1
v_mfma_f32_16x16x32_bf16 v[144:147],v[80:83],v[56:59],v[144:147]
ds_read_u16 v137,v137
v_fma_mix_f32 v140,v140,v121,0 op_sel_hi:[0,1,0]
v_fma_mix_f32 v142,v142,v121,0 op_sel:[0,1,0] op_sel_hi:[0,1,0]
v_mfma_f32_16x16x32_bf16 v[152:155],v[80:83],v[60:63],v[152:155]
s_waitcnt lgkmcnt(8)
ds_read_b64_tr_b16 v[106:107],v3 offset:7824
ds_read_b64_tr_b16 v[108:109],v3 offset:6296
v_lshl_add_u64 v[128:129],v[128:129],0,v[130:131]
v_lshl_add_u64 v[130:131],v[132:133],0,v[134:135]
v_mfma_f32_16x16x32_bf16 v[156:159],v[84:87],v[60:63],v[156:159]
ds_read_u16_d16_hi v139,v139
v_fma_mix_f32 v141,v141,v123,0 op_sel_hi:[0,1,0]
v_fma_mix_f32 v143,v143,v123,0 op_sel:[0,1,0] op_sel_hi:[0,1,0]
v_mfma_f32_16x16x32_bf16 v[148:151],v[84:87],v[56:59],v[148:151]
ds_read_b64_tr_b16 v[110:111],v3 offset:7832
ds_read_b64_tr_b16 v[64:65],v1 offset:512
v_lshlrev_b32_sdwa v144,1,v144 dst_sel:DWORD dst_unused:UNUSED_PAD src0_sel:DWORD src1_sel:WORD_1
v_lshlrev_b32_sdwa v146,1,v146 dst_sel:DWORD dst_unused:UNUSED_PAD src0_sel:DWORD src1_sel:WORD_1
v_mfma_f32_16x16x32_bf16 v[160:163],v[88:91],v[128:131],v[160:163]
ds_read_u16 v144,v144
v_lshlrev_b32_sdwa v145,1,v145 dst_sel:DWORD dst_unused:UNUSED_PAD src0_sel:DWORD src1_sel:WORD_1
v_lshlrev_b32_sdwa v147,1,v147 dst_sel:DWORD dst_unused:UNUSED_PAD src0_sel:DWORD src1_sel:WORD_1
v_mfma_f32_16x16x32_bf16 v[168:171],v[92:95],v[128:131],v[168:171]
s_waitcnt lgkmcnt(11)
ds_read_b64_tr_b16 v[66:67],v1 offset:2560
ds_read_b128 v[112:115],v4 offset:896
v_fma_mixlo_f16 v120,v156,s99,0
v_fma_mixlo_f16 v122,v157,s99,0
v_mfma_f32_16x16x32_bf16 v[176:179],v[96:99],v[128:131],v[176:179]
s_waitcnt lgkmcnt(10)
ds_read_u16_d16_hi v146,v146
v_lshlrev_b32_sdwa v148,1,v148 dst_sel:DWORD dst_unused:UNUSED_PAD src0_sel:DWORD src1_sel:WORD_1
v_lshlrev_b32_sdwa v150,1,v150 dst_sel:DWORD dst_unused:UNUSED_PAD src0_sel:DWORD src1_sel:WORD_1
v_mfma_f32_16x16x32_bf16 v[184:187],v[100:103],v[128:131],v[184:187]
s_waitcnt lgkmcnt(6)
ds_read_b64_tr_b16 v[68:69],v2 offset:512
ds_read_b64_tr_b16 v[70:71],v2 offset:2560
v_lshl_add_u64 v[136:137],v[136:137],0,v[138:139]
v_mfma_f32_16x16x32_bf16 v[192:195],v[104:107],v[128:131],v[192:195]
s_waitcnt lgkmcnt(7)
ds_read_u16 v145,v145
v_cvt_pk_bf16_f32 v138,v140,v142
v_cvt_pk_bf16_f32 v139,v141,v143
v_mfma_f32_16x16x32_bf16 v[200:203],v[108:111],v[128:131],v[200:203]
ds_read_b128 v[116:119],v4 offset:928
ds_read_b64_tr_b16 v[72:73],v1 offset:4608
v_fma_mixhi_f16 v120,v158,s99,0
v_fma_mixhi_f16 v122,v159,s99,0
v_mfma_f32_16x16x32_bf16 v[164:167],v[88:91],v[136:139],v[164:167]
ds_read_u16_d16_hi v147,v147
v_lshlrev_b32_sdwa v149,1,v149 dst_sel:DWORD dst_unused:UNUSED_PAD src0_sel:DWORD src1_sel:WORD_1
v_lshlrev_b32_sdwa v151,1,v151 dst_sel:DWORD dst_unused:UNUSED_PAD src0_sel:DWORD src1_sel:WORD_1
v_mfma_f32_16x16x32_bf16 v[172:175],v[92:95],v[136:139],v[172:175]
s_setprio 1
ds_read_b64_tr_b16 v[74:75],v1 offset:6656
ds_read_b64_tr_b16 v[76:77],v2 offset:4608
v_pk_fma_f16 v121,v120,v120,s54
v_pk_fma_f16 v123,v122,v122,s54
v_mfma_f32_16x16x32_bf16 v[180:183],v[96:99],v[136:139],v[180:183]
ds_read_u16 v148,v148
v_pk_mul_f16 v121,v120,v121
v_pk_mul_f16 v123,v122,v123
v_mfma_f32_16x16x32_bf16 v[188:191],v[100:103],v[136:139],v[188:191]
ds_read_b64_tr_b16 v[78:79],v2 offset:6656
v_lshlrev_b32_sdwa v152,1,v152 dst_sel:DWORD dst_unused:UNUSED_PAD src0_sel:DWORD src1_sel:WORD_1
v_lshlrev_b32_sdwa v154,1,v154 dst_sel:DWORD dst_unused:UNUSED_PAD src0_sel:DWORD src1_sel:WORD_1
v_mfma_f32_16x16x32_bf16 v[196:199],v[104:107],v[136:139],v[196:199]
ds_read_u16_d16_hi v150,v150
v_exp_f16 v120,v121
v_mfma_f32_16x16x32_bf16 v[204:207],v[108:111],v[136:139],v[204:207]
s_waitcnt lgkmcnt(12)
ds_read_b64_tr_b16 v[80:81],v1 offset:8704
v_exp_f16 v122,v123
v_mfma_f32_16x16x32_bf16 v[128:131],v[64:67],v[16:19],v[112:115]
ds_read_u16 v149,v149
v_lshlrev_b32_sdwa v153,1,v153 dst_sel:DWORD dst_unused:UNUSED_PAD src0_sel:DWORD src1_sel:WORD_1
v_lshlrev_b32_sdwa v155,1,v155 dst_sel:DWORD dst_unused:UNUSED_PAD src0_sel:DWORD src1_sel:WORD_1
v_mfma_f32_16x16x32_bf16 v[136:139],v[64:67],v[20:23],v[112:115]
s_waitcnt lgkmcnt(9)
ds_read_b64_tr_b16 v[82:83],v1 offset:10752
v_exp_f16_sdwa v120,v121 dst_sel:WORD_1 dst_unused:UNUSED_PRESERVE src0_sel:WORD_1
v_mfma_f32_16x16x32_bf16 v[140:143],v[68:71],v[20:23],v[116:119]
ds_read_u16_d16_hi v151,v151
v_exp_f16_sdwa v122,v123 dst_sel:WORD_1 dst_unused:UNUSED_PRESERVE src0_sel:WORD_1
v_mfma_f32_16x16x32_bf16 v[132:135],v[68:71],v[16:19],v[116:119]
s_waitcnt lgkmcnt(8)
ds_read_b64_tr_b16 v[84:85],v2 offset:8704
v_pk_fma_f16 v120,v120,s55,s55
v_pk_fma_f16 v122,v122,s55,s55
v_mfma_f32_16x16x32_bf16 v[128:131],v[72:75],v[24:27],v[128:131]
ds_read_u16 v152,v152
v_rcp_f16 v121,v120
v_mfma_f32_16x16x32_bf16 v[136:139],v[72:75],v[28:31],v[136:139]
s_waitcnt lgkmcnt(7)
ds_read_b64_tr_b16 v[86:87],v2 offset:10752
v_rcp_f16 v123,v122
v_mfma_f32_16x16x32_bf16 v[140:143],v[76:79],v[28:31],v[140:143]
ds_read_u16_d16_hi v154,v154
v_rcp_f16_sdwa v121,v120 dst_sel:WORD_1 dst_unused:UNUSED_PRESERVE src0_sel:WORD_1
v_mfma_f32_16x16x32_bf16 v[132:135],v[76:79],v[24:27],v[132:135]
s_waitcnt lgkmcnt(5)
v_rcp_f16_sdwa v123,v122 dst_sel:WORD_1 dst_unused:UNUSED_PRESERVE src0_sel:WORD_1
v_mfma_f32_16x16x32_bf16 v[128:131],v[80:83],v[32:35],v[128:131]
ds_read_u16 v153,v153
v_fma_mix_f32 v156,v156,v121,0 op_sel_hi:[0,1,0]
v_fma_mix_f32 v158,v158,v121,0 op_sel:[0,1,0] op_sel_hi:[0,1,0]
v_mfma_f32_16x16x32_bf16 v[136:139],v[80:83],v[36:39],v[136:139]
s_waitcnt lgkmcnt(2)
s_setprio 0
v_lshl_add_u64 v[144:145],v[144:145],0,v[146:147]
v_lshl_add_u64 v[146:147],v[148:149],0,v[150:151]
v_mfma_f32_16x16x32_bf16 v[140:143],v[84:87],v[36:39],v[140:143]
ds_read_u16_d16_hi v155,v155
v_fma_mix_f32 v157,v157,v123,0 op_sel_hi:[0,1,0]
v_fma_mix_f32 v159,v159,v123,0 op_sel:[0,1,0] op_sel_hi:[0,1,0]
v_mfma_f32_16x16x32_bf16 v[132:135],v[84:87],v[32:35],v[132:135]
v_lshlrev_b32_sdwa v128,1,v128 dst_sel:DWORD dst_unused:UNUSED_PAD src0_sel:DWORD src1_sel:WORD_1
v_lshlrev_b32_sdwa v130,1,v130 dst_sel:DWORD dst_unused:UNUSED_PAD src0_sel:DWORD src1_sel:WORD_1
v_mfma_f32_16x16x32_bf16 v[208:211],v[88:91],v[144:147],v[208:211]
s_barrier
s_setprio 2
ds_read_u16 v128,v128
v_lshlrev_b32_sdwa v129,1,v129 dst_sel:DWORD dst_unused:UNUSED_PAD src0_sel:DWORD src1_sel:WORD_1
v_mfma_f32_16x16x32_bf16 v[216:219],v[92:95],v[144:147],v[216:219]
ds_read_u16_d16_hi v130,v130
v_lshlrev_b32_sdwa v131,1,v131 dst_sel:DWORD dst_unused:UNUSED_PAD src0_sel:DWORD src1_sel:WORD_1
v_mfma_f32_16x16x32_bf16 v[224:227],v[96:99],v[144:147],v[224:227]
v_lshlrev_b32_sdwa v132,1,v132 dst_sel:DWORD dst_unused:UNUSED_PAD src0_sel:DWORD src1_sel:WORD_1
v_mfma_f32_16x16x32_bf16 v[232:235],v[100:103],v[144:147],v[232:235]
s_waitcnt lgkmcnt(2)
ds_read_u16 v129,v129
v_lshl_add_u64 v[152:153],v[152:153],0,v[154:155]
v_lshlrev_b32_sdwa v134,1,v134 dst_sel:DWORD dst_unused:UNUSED_PAD src0_sel:DWORD src1_sel:WORD_1
v_mfma_f32_16x16x32_bf16 v[240:243],v[104:107],v[144:147],v[240:243]
ds_read_u16_d16_hi v131,v131
v_cvt_pk_bf16_f32 v154,v156,v158
v_cvt_pk_bf16_f32 v155,v157,v159
v_mfma_f32_16x16x32_bf16 v[248:251],v[108:111],v[144:147],v[248:251]
v_lshlrev_b32_sdwa v133,1,v133 dst_sel:DWORD dst_unused:UNUSED_PAD src0_sel:DWORD src1_sel:WORD_1
v_mfma_f32_16x16x32_bf16 v[212:215],v[88:91],v[152:155],v[212:215]
ds_read_u16 v132,v132
v_lshlrev_b32_sdwa v135,1,v135 dst_sel:DWORD dst_unused:UNUSED_PAD src0_sel:DWORD src1_sel:WORD_1
v_mfma_f32_16x16x32_bf16 v[220:223],v[92:95],v[152:155],v[220:223]
ds_read_u16_d16_hi v134,v134
v_lshlrev_b32_sdwa v136,1,v136 dst_sel:DWORD dst_unused:UNUSED_PAD src0_sel:DWORD src1_sel:WORD_1
v_mfma_f32_16x16x32_bf16 v[228:231],v[96:99],v[152:155],v[228:231]
ds_read_b64_tr_b16 v[88:89],v3 offset:12320
ds_read_b64_tr_b16 v[90:91],v3 offset:13856
v_lshlrev_b32_sdwa v138,1,v138 dst_sel:DWORD dst_unused:UNUSED_PAD src0_sel:DWORD src1_sel:WORD_1
v_mfma_f32_16x16x32_bf16 v[236:239],v[100:103],v[152:155],v[236:239]
ds_read_u16 v133,v133
v_lshlrev_b32_sdwa v137,1,v137 dst_sel:DWORD dst_unused:UNUSED_PAD src0_sel:DWORD src1_sel:WORD_1
v_mfma_f32_16x16x32_bf16 v[244:247],v[104:107],v[152:155],v[244:247]
ds_read_u16_d16_hi v135,v135
v_lshlrev_b32_sdwa v139,1,v139 dst_sel:DWORD dst_unused:UNUSED_PAD src0_sel:DWORD src1_sel:WORD_1
v_mfma_f32_16x16x32_bf16 v[252:255],v[108:111],v[152:155],v[252:255]
ds_read_b64_tr_b16 v[92:93],v3 offset:12328
ds_read_b64_tr_b16 v[94:95],v3 offset:13864
v_lshlrev_b32_sdwa v140,1,v140 dst_sel:DWORD dst_unused:UNUSED_PAD src0_sel:DWORD src1_sel:WORD_1
v_mfma_f32_16x16x32_bf16 v[144:147],v[64:67],v[40:43],v[112:115]
ds_read_u16 v136,v136
v_lshlrev_b32_sdwa v142,1,v142 dst_sel:DWORD dst_unused:UNUSED_PAD src0_sel:DWORD src1_sel:WORD_1
v_mfma_f32_16x16x32_bf16 v[152:155],v[64:67],v[44:47],v[112:115]
ds_read_u16_d16_hi v138,v138
v_lshlrev_b32_sdwa v141,1,v141 dst_sel:DWORD dst_unused:UNUSED_PAD src0_sel:DWORD src1_sel:WORD_1
v_mfma_f32_16x16x32_bf16 v[156:159],v[68:71],v[44:47],v[116:119]
ds_read_b64_tr_b16 v[96:97],v3 offset:12384
ds_read_b64_tr_b16 v[98:99],v3 offset:13920
v_lshlrev_b32_sdwa v143,1,v143 dst_sel:DWORD dst_unused:UNUSED_PAD src0_sel:DWORD src1_sel:WORD_1
v_mfma_f32_16x16x32_bf16 v[148:151],v[68:71],v[40:43],v[116:119]
ds_read_u16 v137,v137
v_mfma_f32_16x16x32_bf16 v[144:147],v[72:75],v[48:51],v[144:147]
ds_read_u16_d16_hi v139,v139
v_mfma_f32_16x16x32_bf16 v[152:155],v[72:75],v[52:55],v[152:155]
ds_read_b64_tr_b16 v[100:101],v3 offset:12392
ds_read_b64_tr_b16 v[102:103],v3 offset:13928
v_mfma_f32_16x16x32_bf16 v[156:159],v[76:79],v[52:55],v[156:159]
ds_read_u16 v140,v140
v_mfma_f32_16x16x32_bf16 v[148:151],v[76:79],v[48:51],v[148:151]
s_waitcnt lgkmcnt(15)
ds_read_u16_d16_hi v142,v142
v_lshl_add_u64 v[128:129],v[128:129],0,v[130:131]
v_mfma_f32_16x16x32_bf16 v[144:147],v[80:83],v[56:59],v[144:147]
s_waitcnt lgkmcnt(12)
ds_read_b64_tr_b16 v[104:105],v3 offset:12448
ds_read_b64_tr_b16 v[106:107],v3 offset:13984
v_lshl_add_u64 v[130:131],v[132:133],0,v[134:135]
v_mfma_f32_16x16x32_bf16 v[152:155],v[80:83],v[60:63],v[152:155]
ds_read_u16 v141,v141
v_mfma_f32_16x16x32_bf16 v[156:159],v[84:87],v[60:63],v[156:159]
ds_read_u16_d16_hi v143,v143
v_mfma_f32_16x16x32_bf16 v[148:151],v[84:87],v[56:59],v[148:151]
ds_read_b64_tr_b16 v[108:109],v3 offset:12456
ds_read_b64_tr_b16 v[110:111],v3 offset:13992
v_lshlrev_b32_sdwa v144,1,v144 dst_sel:DWORD dst_unused:UNUSED_PAD src0_sel:DWORD src1_sel:WORD_1
v_lshlrev_b32_sdwa v146,1,v146 dst_sel:DWORD dst_unused:UNUSED_PAD src0_sel:DWORD src1_sel:WORD_1
v_mfma_f32_16x16x32_bf16 v[160:163],v[88:91],v[128:131],v[160:163]
s_waitcnt lgkmcnt(15)
ds_read_u16 v144,v144
v_lshlrev_b32_sdwa v145,1,v145 dst_sel:DWORD dst_unused:UNUSED_PAD src0_sel:DWORD src1_sel:WORD_1
v_mfma_f32_16x16x32_bf16 v[168:171],v[92:95],v[128:131],v[168:171]
s_waitcnt lgkmcnt(13)
ds_read_u16_d16_hi v146,v146
v_lshlrev_b32_sdwa v147,1,v147 dst_sel:DWORD dst_unused:UNUSED_PAD src0_sel:DWORD src1_sel:WORD_1
v_mfma_f32_16x16x32_bf16 v[176:179],v[96:99],v[128:131],v[176:179]
s_waitcnt lgkmcnt(10)
ds_read_b64_tr_b16 v[64:65],v1 offset:12320
ds_read_b64_tr_b16 v[66:67],v1 offset:14368
v_lshlrev_b32_sdwa v148,1,v148 dst_sel:DWORD dst_unused:UNUSED_PAD src0_sel:DWORD src1_sel:WORD_1
v_mfma_f32_16x16x32_bf16 v[184:187],v[100:103],v[128:131],v[184:187]
s_waitcnt lgkmcnt(8)
ds_read_u16 v145,v145
v_lshl_add_u64 v[136:137],v[136:137],0,v[138:139]
v_lshlrev_b32_sdwa v150,1,v150 dst_sel:DWORD dst_unused:UNUSED_PAD src0_sel:DWORD src1_sel:WORD_1
v_mfma_f32_16x16x32_bf16 v[192:195],v[104:107],v[128:131],v[192:195]
s_waitcnt lgkmcnt(5)
ds_read_u16_d16_hi v147,v147
v_lshl_add_u64 v[138:139],v[140:141],0,v[142:143]
v_mfma_f32_16x16x32_bf16 v[200:203],v[108:111],v[128:131],v[200:203]
ds_read_b128 v[112:115],v4 offset:1024
ds_read_b64_tr_b16 v[68:69],v2 offset:12320
v_lshlrev_b32_sdwa v149,1,v149 dst_sel:DWORD dst_unused:UNUSED_PAD src0_sel:DWORD src1_sel:WORD_1
v_mfma_f32_16x16x32_bf16 v[164:167],v[88:91],v[136:139],v[164:167]
ds_read_u16 v148,v148
v_lshlrev_b32_sdwa v151,1,v151 dst_sel:DWORD dst_unused:UNUSED_PAD src0_sel:DWORD src1_sel:WORD_1
v_mfma_f32_16x16x32_bf16 v[172:175],v[92:95],v[136:139],v[172:175]
ds_read_u16_d16_hi v150,v150
ds_read_b64_tr_b16 v[70:71],v2 offset:14368
v_lshlrev_b32_sdwa v152,1,v152 dst_sel:DWORD dst_unused:UNUSED_PAD src0_sel:DWORD src1_sel:WORD_1
v_mfma_f32_16x16x32_bf16 v[180:183],v[96:99],v[136:139],v[180:183]
ds_read_b128 v[116:119],v4 offset:1056
ds_read_b64_tr_b16 v[72:73],v1 offset:16416
v_lshlrev_b32_sdwa v154,1,v154 dst_sel:DWORD dst_unused:UNUSED_PAD src0_sel:DWORD src1_sel:WORD_1
v_mfma_f32_16x16x32_bf16 v[188:191],v[100:103],v[136:139],v[188:191]
ds_read_u16 v149,v149
v_lshlrev_b32_sdwa v153,1,v153 dst_sel:DWORD dst_unused:UNUSED_PAD src0_sel:DWORD src1_sel:WORD_1
v_mfma_f32_16x16x32_bf16 v[196:199],v[104:107],v[136:139],v[196:199]
ds_read_u16_d16_hi v151,v151
v_lshlrev_b32_sdwa v155,1,v155 dst_sel:DWORD dst_unused:UNUSED_PAD src0_sel:DWORD src1_sel:WORD_1
v_mfma_f32_16x16x32_bf16 v[204:207],v[108:111],v[136:139],v[204:207]
s_waitcnt lgkmcnt(8)
ds_read_b64_tr_b16 v[74:75],v1 offset:18464
ds_read_b64_tr_b16 v[76:77],v2 offset:16416
v_lshlrev_b32_sdwa v156,1,v156 dst_sel:DWORD dst_unused:UNUSED_PAD src0_sel:DWORD src1_sel:WORD_1
v_mfma_f32_16x16x32_bf16 v[128:131],v[64:67],v[16:19],v[112:115]
ds_read_u16 v152,v152
v_lshlrev_b32_sdwa v158,1,v158 dst_sel:DWORD dst_unused:UNUSED_PAD src0_sel:DWORD src1_sel:WORD_1
v_mfma_f32_16x16x32_bf16 v[136:139],v[64:67],v[20:23],v[112:115]
s_waitcnt lgkmcnt(6)
ds_read_u16_d16_hi v154,v154
ds_read_b64_tr_b16 v[78:79],v2 offset:18464
v_lshlrev_b32_sdwa v157,1,v157 dst_sel:DWORD dst_unused:UNUSED_PAD src0_sel:DWORD src1_sel:WORD_1
v_mfma_f32_16x16x32_bf16 v[140:143],v[68:71],v[20:23],v[116:119]
ds_read_b64_tr_b16 v[80:81],v1 offset:20512
ds_read_b64_tr_b16 v[82:83],v1 offset:22560
v_lshlrev_b32_sdwa v159,1,v159 dst_sel:DWORD dst_unused:UNUSED_PAD src0_sel:DWORD src1_sel:WORD_1
v_mfma_f32_16x16x32_bf16 v[132:135],v[68:71],v[16:19],v[116:119]
s_waitcnt lgkmcnt(6)
ds_read_u16 v153,v153
v_mfma_f32_16x16x32_bf16 v[128:131],v[72:75],v[24:27],v[128:131]
ds_read_u16_d16_hi v155,v155
v_mfma_f32_16x16x32_bf16 v[136:139],v[72:75],v[28:31],v[136:139]
s_waitcnt lgkmcnt(4)
ds_read_b64_tr_b16 v[84:85],v2 offset:20512
ds_read_b64_tr_b16 v[86:87],v2 offset:22560
v_mfma_f32_16x16x32_bf16 v[140:143],v[76:79],v[28:31],v[140:143]
ds_read_u16 v156,v156
v_mfma_f32_16x16x32_bf16 v[132:135],v[76:79],v[24:27],v[132:135]
s_waitcnt lgkmcnt(5)
ds_read_u16_d16_hi v158,v158
v_lshl_add_u64 v[144:145],v[144:145],0,v[146:147]
v_mfma_f32_16x16x32_bf16 v[128:131],v[80:83],v[32:35],v[128:131]
v_lshl_add_u64 v[146:147],v[148:149],0,v[150:151]
v_mfma_f32_16x16x32_bf16 v[136:139],v[80:83],v[36:39],v[136:139]
s_waitcnt lgkmcnt(2)
ds_read_u16 v157,v157
v_mfma_f32_16x16x32_bf16 v[140:143],v[84:87],v[36:39],v[140:143]
ds_read_u16_d16_hi v159,v159
v_mfma_f32_16x16x32_bf16 v[132:135],v[84:87],v[32:35],v[132:135]
s_nop 0
v_lshlrev_b32_sdwa v128,1,v128 dst_sel:DWORD dst_unused:UNUSED_PAD src0_sel:DWORD src1_sel:WORD_1
v_lshlrev_b32_sdwa v130,1,v130 dst_sel:DWORD dst_unused:UNUSED_PAD src0_sel:DWORD src1_sel:WORD_1
v_mfma_f32_16x16x32_bf16 v[208:211],v[88:91],v[144:147],v[208:211]
ds_read_u16 v128,v128
v_lshlrev_b32_sdwa v129,1,v129 dst_sel:DWORD dst_unused:UNUSED_PAD src0_sel:DWORD src1_sel:WORD_1
v_mfma_f32_16x16x32_bf16 v[216:219],v[92:95],v[144:147],v[216:219]
ds_read_u16_d16_hi v130,v130
v_lshlrev_b32_sdwa v131,1,v131 dst_sel:DWORD dst_unused:UNUSED_PAD src0_sel:DWORD src1_sel:WORD_1
v_mfma_f32_16x16x32_bf16 v[224:227],v[96:99],v[144:147],v[224:227]
v_lshlrev_b32_sdwa v132,1,v132 dst_sel:DWORD dst_unused:UNUSED_PAD src0_sel:DWORD src1_sel:WORD_1
v_mfma_f32_16x16x32_bf16 v[232:235],v[100:103],v[144:147],v[232:235]
ds_read_u16 v129,v129
v_lshl_add_u64 v[152:153],v[152:153],0,v[154:155]
v_lshlrev_b32_sdwa v134,1,v134 dst_sel:DWORD dst_unused:UNUSED_PAD src0_sel:DWORD src1_sel:WORD_1
v_mfma_f32_16x16x32_bf16 v[240:243],v[104:107],v[144:147],v[240:243]
s_waitcnt lgkmcnt(3)
ds_read_u16_d16_hi v131,v131
v_lshl_add_u64 v[154:155],v[156:157],0,v[158:159]
v_mfma_f32_16x16x32_bf16 v[248:251],v[108:111],v[144:147],v[248:251]
v_lshlrev_b32_sdwa v133,1,v133 dst_sel:DWORD dst_unused:UNUSED_PAD src0_sel:DWORD src1_sel:WORD_1
v_mfma_f32_16x16x32_bf16 v[212:215],v[88:91],v[152:155],v[212:215]
ds_read_u16 v132,v132
v_lshlrev_b32_sdwa v135,1,v135 dst_sel:DWORD dst_unused:UNUSED_PAD src0_sel:DWORD src1_sel:WORD_1
v_mfma_f32_16x16x32_bf16 v[220:223],v[92:95],v[152:155],v[220:223]
ds_read_u16_d16_hi v134,v134
v_lshlrev_b32_sdwa v136,1,v136 dst_sel:DWORD dst_unused:UNUSED_PAD src0_sel:DWORD src1_sel:WORD_1
v_mfma_f32_16x16x32_bf16 v[228:231],v[96:99],v[152:155],v[228:231]
ds_read_b64_tr_b16 v[88:89],v3 offset:18480
ds_read_b64_tr_b16 v[90:91],v3 offset:20016
v_lshlrev_b32_sdwa v138,1,v138 dst_sel:DWORD dst_unused:UNUSED_PAD src0_sel:DWORD src1_sel:WORD_1
v_mfma_f32_16x16x32_bf16 v[236:239],v[100:103],v[152:155],v[236:239]
ds_read_u16 v133,v133
v_lshlrev_b32_sdwa v137,1,v137 dst_sel:DWORD dst_unused:UNUSED_PAD src0_sel:DWORD src1_sel:WORD_1
v_mfma_f32_16x16x32_bf16 v[244:247],v[104:107],v[152:155],v[244:247]
ds_read_u16_d16_hi v135,v135
v_lshlrev_b32_sdwa v139,1,v139 dst_sel:DWORD dst_unused:UNUSED_PAD src0_sel:DWORD src1_sel:WORD_1
v_mfma_f32_16x16x32_bf16 v[252:255],v[108:111],v[152:155],v[252:255]
ds_read_b64_tr_b16 v[92:93],v3 offset:18488
ds_read_b64_tr_b16 v[94:95],v3 offset:20024
v_lshlrev_b32_sdwa v140,1,v140 dst_sel:DWORD dst_unused:UNUSED_PAD src0_sel:DWORD src1_sel:WORD_1
v_mfma_f32_16x16x32_bf16 v[144:147],v[64:67],v[40:43],v[112:115]
ds_read_u16 v136,v136
v_lshlrev_b32_sdwa v142,1,v142 dst_sel:DWORD dst_unused:UNUSED_PAD src0_sel:DWORD src1_sel:WORD_1
v_mfma_f32_16x16x32_bf16 v[152:155],v[64:67],v[44:47],v[112:115]
ds_read_u16_d16_hi v138,v138
v_lshlrev_b32_sdwa v141,1,v141 dst_sel:DWORD dst_unused:UNUSED_PAD src0_sel:DWORD src1_sel:WORD_1
v_mfma_f32_16x16x32_bf16 v[156:159],v[68:71],v[44:47],v[116:119]
ds_read_b64_tr_b16 v[96:97],v3 offset:18544
ds_read_b64_tr_b16 v[98:99],v3 offset:20080
v_lshlrev_b32_sdwa v143,1,v143 dst_sel:DWORD dst_unused:UNUSED_PAD src0_sel:DWORD src1_sel:WORD_1
v_mfma_f32_16x16x32_bf16 v[148:151],v[68:71],v[40:43],v[116:119]
ds_read_u16 v137,v137
v_mfma_f32_16x16x32_bf16 v[144:147],v[72:75],v[48:51],v[144:147]
ds_read_u16_d16_hi v139,v139
v_mfma_f32_16x16x32_bf16 v[152:155],v[72:75],v[52:55],v[152:155]
ds_read_b64_tr_b16 v[100:101],v3 offset:18552
ds_read_b64_tr_b16 v[102:103],v3 offset:20088
v_mfma_f32_16x16x32_bf16 v[156:159],v[76:79],v[52:55],v[156:159]
ds_read_u16 v140,v140
v_mfma_f32_16x16x32_bf16 v[148:151],v[76:79],v[48:51],v[148:151]
s_waitcnt lgkmcnt(15)
ds_read_u16_d16_hi v142,v142
v_lshl_add_u64 v[128:129],v[128:129],0,v[130:131]
v_mfma_f32_16x16x32_bf16 v[144:147],v[80:83],v[56:59],v[144:147]
s_waitcnt lgkmcnt(12)
ds_read_b64_tr_b16 v[104:105],v3 offset:18608
ds_read_b64_tr_b16 v[106:107],v3 offset:20144
v_lshl_add_u64 v[130:131],v[132:133],0,v[134:135]
v_mfma_f32_16x16x32_bf16 v[152:155],v[80:83],v[60:63],v[152:155]
ds_read_u16 v141,v141
v_mfma_f32_16x16x32_bf16 v[156:159],v[84:87],v[60:63],v[156:159]
ds_read_u16_d16_hi v143,v143
v_mfma_f32_16x16x32_bf16 v[148:151],v[84:87],v[56:59],v[148:151]
ds_read_b64_tr_b16 v[108:109],v3 offset:18616
ds_read_b64_tr_b16 v[110:111],v3 offset:20152
v_lshlrev_b32_sdwa v144,1,v144 dst_sel:DWORD dst_unused:UNUSED_PAD src0_sel:DWORD src1_sel:WORD_1
v_lshlrev_b32_sdwa v146,1,v146 dst_sel:DWORD dst_unused:UNUSED_PAD src0_sel:DWORD src1_sel:WORD_1
v_mfma_f32_16x16x32_bf16 v[160:163],v[88:91],v[128:131],v[160:163]
s_waitcnt lgkmcnt(15)
ds_read_u16 v144,v144
v_lshlrev_b32_sdwa v145,1,v145 dst_sel:DWORD dst_unused:UNUSED_PAD src0_sel:DWORD src1_sel:WORD_1
v_mfma_f32_16x16x32_bf16 v[168:171],v[92:95],v[128:131],v[168:171]
s_waitcnt lgkmcnt(13)
ds_read_u16_d16_hi v146,v146
v_lshlrev_b32_sdwa v147,1,v147 dst_sel:DWORD dst_unused:UNUSED_PAD src0_sel:DWORD src1_sel:WORD_1
v_mfma_f32_16x16x32_bf16 v[176:179],v[96:99],v[128:131],v[176:179]
s_waitcnt lgkmcnt(10)
ds_read_b64_tr_b16 v[64:65],v1 offset:12832
ds_read_b64_tr_b16 v[66:67],v1 offset:14880
v_lshlrev_b32_sdwa v148,1,v148 dst_sel:DWORD dst_unused:UNUSED_PAD src0_sel:DWORD src1_sel:WORD_1
v_mfma_f32_16x16x32_bf16 v[184:187],v[100:103],v[128:131],v[184:187]
s_waitcnt lgkmcnt(8)
ds_read_u16 v145,v145
v_lshl_add_u64 v[136:137],v[136:137],0,v[138:139]
v_lshlrev_b32_sdwa v150,1,v150 dst_sel:DWORD dst_unused:UNUSED_PAD src0_sel:DWORD src1_sel:WORD_1
v_mfma_f32_16x16x32_bf16 v[192:195],v[104:107],v[128:131],v[192:195]
s_waitcnt lgkmcnt(5)
ds_read_u16_d16_hi v147,v147
v_lshl_add_u64 v[138:139],v[140:141],0,v[142:143]
v_mfma_f32_16x16x32_bf16 v[200:203],v[108:111],v[128:131],v[200:203]
ds_read_b128 v[112:115],v4 offset:1152
ds_read_b64_tr_b16 v[68:69],v2 offset:12832
v_lshlrev_b32_sdwa v149,1,v149 dst_sel:DWORD dst_unused:UNUSED_PAD src0_sel:DWORD src1_sel:WORD_1
v_mfma_f32_16x16x32_bf16 v[164:167],v[88:91],v[136:139],v[164:167]
ds_read_u16 v148,v148
v_lshlrev_b32_sdwa v151,1,v151 dst_sel:DWORD dst_unused:UNUSED_PAD src0_sel:DWORD src1_sel:WORD_1
v_mfma_f32_16x16x32_bf16 v[172:175],v[92:95],v[136:139],v[172:175]
ds_read_u16_d16_hi v150,v150
ds_read_b64_tr_b16 v[70:71],v2 offset:14880
v_lshlrev_b32_sdwa v152,1,v152 dst_sel:DWORD dst_unused:UNUSED_PAD src0_sel:DWORD src1_sel:WORD_1
v_mfma_f32_16x16x32_bf16 v[180:183],v[96:99],v[136:139],v[180:183]
ds_read_b128 v[116:119],v4 offset:1184
ds_read_b64_tr_b16 v[72:73],v1 offset:16928
v_lshlrev_b32_sdwa v154,1,v154 dst_sel:DWORD dst_unused:UNUSED_PAD src0_sel:DWORD src1_sel:WORD_1
v_mfma_f32_16x16x32_bf16 v[188:191],v[100:103],v[136:139],v[188:191]
ds_read_u16 v149,v149
v_lshlrev_b32_sdwa v153,1,v153 dst_sel:DWORD dst_unused:UNUSED_PAD src0_sel:DWORD src1_sel:WORD_1
v_mfma_f32_16x16x32_bf16 v[196:199],v[104:107],v[136:139],v[196:199]
ds_read_u16_d16_hi v151,v151
v_lshlrev_b32_sdwa v155,1,v155 dst_sel:DWORD dst_unused:UNUSED_PAD src0_sel:DWORD src1_sel:WORD_1
v_mfma_f32_16x16x32_bf16 v[204:207],v[108:111],v[136:139],v[204:207]
s_waitcnt lgkmcnt(8)
ds_read_b64_tr_b16 v[74:75],v1 offset:18976
ds_read_b64_tr_b16 v[76:77],v2 offset:16928
v_lshlrev_b32_sdwa v156,1,v156 dst_sel:DWORD dst_unused:UNUSED_PAD src0_sel:DWORD src1_sel:WORD_1
v_mfma_f32_16x16x32_bf16 v[128:131],v[64:67],v[16:19],v[112:115]
ds_read_u16 v152,v152
v_lshlrev_b32_sdwa v158,1,v158 dst_sel:DWORD dst_unused:UNUSED_PAD src0_sel:DWORD src1_sel:WORD_1
v_mfma_f32_16x16x32_bf16 v[136:139],v[64:67],v[20:23],v[112:115]
s_waitcnt lgkmcnt(6)
ds_read_u16_d16_hi v154,v154
ds_read_b64_tr_b16 v[78:79],v2 offset:18976
v_lshlrev_b32_sdwa v157,1,v157 dst_sel:DWORD dst_unused:UNUSED_PAD src0_sel:DWORD src1_sel:WORD_1
v_mfma_f32_16x16x32_bf16 v[140:143],v[68:71],v[20:23],v[116:119]
ds_read_b64_tr_b16 v[80:81],v1 offset:21024
ds_read_b64_tr_b16 v[82:83],v1 offset:23072
v_lshlrev_b32_sdwa v159,1,v159 dst_sel:DWORD dst_unused:UNUSED_PAD src0_sel:DWORD src1_sel:WORD_1
v_mfma_f32_16x16x32_bf16 v[132:135],v[68:71],v[16:19],v[116:119]
s_waitcnt lgkmcnt(6)
ds_read_u16 v153,v153
v_mfma_f32_16x16x32_bf16 v[128:131],v[72:75],v[24:27],v[128:131]
ds_read_u16_d16_hi v155,v155
v_mfma_f32_16x16x32_bf16 v[136:139],v[72:75],v[28:31],v[136:139]
s_waitcnt lgkmcnt(4)
ds_read_b64_tr_b16 v[84:85],v2 offset:21024
ds_read_b64_tr_b16 v[86:87],v2 offset:23072
v_mfma_f32_16x16x32_bf16 v[140:143],v[76:79],v[28:31],v[140:143]
ds_read_u16 v156,v156
v_mfma_f32_16x16x32_bf16 v[132:135],v[76:79],v[24:27],v[132:135]
s_waitcnt lgkmcnt(5)
ds_read_u16_d16_hi v158,v158
v_lshl_add_u64 v[144:145],v[144:145],0,v[146:147]
v_mfma_f32_16x16x32_bf16 v[128:131],v[80:83],v[32:35],v[128:131]
v_lshl_add_u64 v[146:147],v[148:149],0,v[150:151]
v_mfma_f32_16x16x32_bf16 v[136:139],v[80:83],v[36:39],v[136:139]
s_waitcnt lgkmcnt(2)
ds_read_u16 v157,v157
v_mfma_f32_16x16x32_bf16 v[140:143],v[84:87],v[36:39],v[140:143]
ds_read_u16_d16_hi v159,v159
v_mfma_f32_16x16x32_bf16 v[132:135],v[84:87],v[32:35],v[132:135]
s_nop 0
v_lshlrev_b32_sdwa v128,1,v128 dst_sel:DWORD dst_unused:UNUSED_PAD src0_sel:DWORD src1_sel:WORD_1
v_lshlrev_b32_sdwa v130,1,v130 dst_sel:DWORD dst_unused:UNUSED_PAD src0_sel:DWORD src1_sel:WORD_1
v_mfma_f32_16x16x32_bf16 v[208:211],v[88:91],v[144:147],v[208:211]
s_barrier
ds_read_u16 v128,v128
v_lshlrev_b32_sdwa v129,1,v129 dst_sel:DWORD dst_unused:UNUSED_PAD src0_sel:DWORD src1_sel:WORD_1
v_mfma_f32_16x16x32_bf16 v[216:219],v[92:95],v[144:147],v[216:219]
ds_read_u16_d16_hi v130,v130
v_lshlrev_b32_sdwa v131,1,v131 dst_sel:DWORD dst_unused:UNUSED_PAD src0_sel:DWORD src1_sel:WORD_1
v_mfma_f32_16x16x32_bf16 v[224:227],v[96:99],v[144:147],v[224:227]
v_lshlrev_b32_sdwa v132,1,v132 dst_sel:DWORD dst_unused:UNUSED_PAD src0_sel:DWORD src1_sel:WORD_1
v_mfma_f32_16x16x32_bf16 v[232:235],v[100:103],v[144:147],v[232:235]
ds_read_u16 v129,v129
v_lshl_add_u64 v[152:153],v[152:153],0,v[154:155]
v_lshlrev_b32_sdwa v134,1,v134 dst_sel:DWORD dst_unused:UNUSED_PAD src0_sel:DWORD src1_sel:WORD_1
v_mfma_f32_16x16x32_bf16 v[240:243],v[104:107],v[144:147],v[240:243]
s_waitcnt lgkmcnt(3)
ds_read_u16_d16_hi v131,v131
v_lshl_add_u64 v[154:155],v[156:157],0,v[158:159]
v_mfma_f32_16x16x32_bf16 v[248:251],v[108:111],v[144:147],v[248:251]
v_lshlrev_b32_sdwa v133,1,v133 dst_sel:DWORD dst_unused:UNUSED_PAD src0_sel:DWORD src1_sel:WORD_1
v_mfma_f32_16x16x32_bf16 v[212:215],v[88:91],v[152:155],v[212:215]
ds_read_u16 v132,v132
v_lshlrev_b32_sdwa v135,1,v135 dst_sel:DWORD dst_unused:UNUSED_PAD src0_sel:DWORD src1_sel:WORD_1
v_mfma_f32_16x16x32_bf16 v[220:223],v[92:95],v[152:155],v[220:223]
ds_read_u16_d16_hi v134,v134
v_lshlrev_b32_sdwa v136,1,v136 dst_sel:DWORD dst_unused:UNUSED_PAD src0_sel:DWORD src1_sel:WORD_1
v_mfma_f32_16x16x32_bf16 v[228:231],v[96:99],v[152:155],v[228:231]
ds_read_b64_tr_b16 v[88:89],v3 offset:32832
ds_read_b64_tr_b16 v[90:91],v3 offset:34368
v_lshlrev_b32_sdwa v138,1,v138 dst_sel:DWORD dst_unused:UNUSED_PAD src0_sel:DWORD src1_sel:WORD_1
v_mfma_f32_16x16x32_bf16 v[236:239],v[100:103],v[152:155],v[236:239]
ds_read_u16 v133,v133
v_lshlrev_b32_sdwa v137,1,v137 dst_sel:DWORD dst_unused:UNUSED_PAD src0_sel:DWORD src1_sel:WORD_1
v_mfma_f32_16x16x32_bf16 v[244:247],v[104:107],v[152:155],v[244:247]
ds_read_u16_d16_hi v135,v135
v_lshlrev_b32_sdwa v139,1,v139 dst_sel:DWORD dst_unused:UNUSED_PAD src0_sel:DWORD src1_sel:WORD_1
v_mfma_f32_16x16x32_bf16 v[252:255],v[108:111],v[152:155],v[252:255]
ds_read_b64_tr_b16 v[92:93],v3 offset:32840
ds_read_b64_tr_b16 v[94:95],v3 offset:34376
v_lshlrev_b32_sdwa v140,1,v140 dst_sel:DWORD dst_unused:UNUSED_PAD src0_sel:DWORD src1_sel:WORD_1
v_mfma_f32_16x16x32_bf16 v[144:147],v[64:67],v[40:43],v[112:115]
ds_read_u16 v136,v136
v_lshlrev_b32_sdwa v142,1,v142 dst_sel:DWORD dst_unused:UNUSED_PAD src0_sel:DWORD src1_sel:WORD_1
v_mfma_f32_16x16x32_bf16 v[152:155],v[64:67],v[44:47],v[112:115]
ds_read_u16_d16_hi v138,v138
v_lshlrev_b32_sdwa v141,1,v141 dst_sel:DWORD dst_unused:UNUSED_PAD src0_sel:DWORD src1_sel:WORD_1
v_mfma_f32_16x16x32_bf16 v[156:159],v[68:71],v[44:47],v[116:119]
ds_read_b64_tr_b16 v[96:97],v3 offset:32896
ds_read_b64_tr_b16 v[98:99],v3 offset:34432
v_lshlrev_b32_sdwa v143,1,v143 dst_sel:DWORD dst_unused:UNUSED_PAD src0_sel:DWORD src1_sel:WORD_1
v_mfma_f32_16x16x32_bf16 v[148:151],v[68:71],v[40:43],v[116:119]
ds_read_u16 v137,v137
v_mfma_f32_16x16x32_bf16 v[144:147],v[72:75],v[48:51],v[144:147]
ds_read_u16_d16_hi v139,v139
v_mfma_f32_16x16x32_bf16 v[152:155],v[72:75],v[52:55],v[152:155]
ds_read_b64_tr_b16 v[100:101],v3 offset:32904
ds_read_b64_tr_b16 v[102:103],v3 offset:34440
v_mfma_f32_16x16x32_bf16 v[156:159],v[76:79],v[52:55],v[156:159]
ds_read_u16 v140,v140
v_mfma_f32_16x16x32_bf16 v[148:151],v[76:79],v[48:51],v[148:151]
s_waitcnt lgkmcnt(15)
ds_read_u16_d16_hi v142,v142
v_lshl_add_u64 v[128:129],v[128:129],0,v[130:131]
v_mfma_f32_16x16x32_bf16 v[144:147],v[80:83],v[56:59],v[144:147]
s_waitcnt lgkmcnt(12)
ds_read_b64_tr_b16 v[104:105],v3 offset:32960
ds_read_b64_tr_b16 v[106:107],v3 offset:34496
v_lshl_add_u64 v[130:131],v[132:133],0,v[134:135]
v_mfma_f32_16x16x32_bf16 v[152:155],v[80:83],v[60:63],v[152:155]
ds_read_u16 v141,v141
v_mfma_f32_16x16x32_bf16 v[156:159],v[84:87],v[60:63],v[156:159]
ds_read_u16_d16_hi v143,v143
v_mfma_f32_16x16x32_bf16 v[148:151],v[84:87],v[56:59],v[148:151]
ds_read_b64_tr_b16 v[108:109],v3 offset:32968
ds_read_b64_tr_b16 v[110:111],v3 offset:34504
v_lshlrev_b32_sdwa v144,1,v144 dst_sel:DWORD dst_unused:UNUSED_PAD src0_sel:DWORD src1_sel:WORD_1
v_lshlrev_b32_sdwa v146,1,v146 dst_sel:DWORD dst_unused:UNUSED_PAD src0_sel:DWORD src1_sel:WORD_1
v_mfma_f32_16x16x32_bf16 v[160:163],v[88:91],v[128:131],v[160:163]
s_waitcnt lgkmcnt(15)
ds_read_u16 v144,v144
v_lshlrev_b32_sdwa v145,1,v145 dst_sel:DWORD dst_unused:UNUSED_PAD src0_sel:DWORD src1_sel:WORD_1
v_mfma_f32_16x16x32_bf16 v[168:171],v[92:95],v[128:131],v[168:171]
s_waitcnt lgkmcnt(13)
ds_read_u16_d16_hi v146,v146
v_lshlrev_b32_sdwa v147,1,v147 dst_sel:DWORD dst_unused:UNUSED_PAD src0_sel:DWORD src1_sel:WORD_1
v_mfma_f32_16x16x32_bf16 v[176:179],v[96:99],v[128:131],v[176:179]
s_waitcnt lgkmcnt(10)
ds_read_b64_tr_b16 v[64:65],v8 offset:0
ds_read_b64_tr_b16 v[66:67],v8 offset:2048
v_lshlrev_b32_sdwa v148,1,v148 dst_sel:DWORD dst_unused:UNUSED_PAD src0_sel:DWORD src1_sel:WORD_1
v_mfma_f32_16x16x32_bf16 v[184:187],v[100:103],v[128:131],v[184:187]
s_waitcnt lgkmcnt(8)
ds_read_u16 v145,v145
v_lshl_add_u64 v[136:137],v[136:137],0,v[138:139]
v_lshlrev_b32_sdwa v150,1,v150 dst_sel:DWORD dst_unused:UNUSED_PAD src0_sel:DWORD src1_sel:WORD_1
v_mfma_f32_16x16x32_bf16 v[192:195],v[104:107],v[128:131],v[192:195]
s_waitcnt lgkmcnt(5)
ds_read_u16_d16_hi v147,v147
v_lshl_add_u64 v[138:139],v[140:141],0,v[142:143]
v_mfma_f32_16x16x32_bf16 v[200:203],v[108:111],v[128:131],v[200:203]
ds_read_b128 v[112:115],v4 offset:1280
ds_read_b64_tr_b16 v[68:69],v9 offset:0
v_lshlrev_b32_sdwa v149,1,v149 dst_sel:DWORD dst_unused:UNUSED_PAD src0_sel:DWORD src1_sel:WORD_1
v_mfma_f32_16x16x32_bf16 v[164:167],v[88:91],v[136:139],v[164:167]
ds_read_u16 v148,v148
v_lshlrev_b32_sdwa v151,1,v151 dst_sel:DWORD dst_unused:UNUSED_PAD src0_sel:DWORD src1_sel:WORD_1
v_mfma_f32_16x16x32_bf16 v[172:175],v[92:95],v[136:139],v[172:175]
ds_read_u16_d16_hi v150,v150
ds_read_b64_tr_b16 v[70:71],v9 offset:2048
v_lshlrev_b32_sdwa v152,1,v152 dst_sel:DWORD dst_unused:UNUSED_PAD src0_sel:DWORD src1_sel:WORD_1
v_mfma_f32_16x16x32_bf16 v[180:183],v[96:99],v[136:139],v[180:183]
ds_read_b128 v[116:119],v4 offset:1312
ds_read_b64_tr_b16 v[72:73],v8 offset:4096
v_lshlrev_b32_sdwa v154,1,v154 dst_sel:DWORD dst_unused:UNUSED_PAD src0_sel:DWORD src1_sel:WORD_1
v_mfma_f32_16x16x32_bf16 v[188:191],v[100:103],v[136:139],v[188:191]
ds_read_u16 v149,v149
v_lshlrev_b32_sdwa v153,1,v153 dst_sel:DWORD dst_unused:UNUSED_PAD src0_sel:DWORD src1_sel:WORD_1
v_mfma_f32_16x16x32_bf16 v[196:199],v[104:107],v[136:139],v[196:199]
ds_read_u16_d16_hi v151,v151
v_lshlrev_b32_sdwa v155,1,v155 dst_sel:DWORD dst_unused:UNUSED_PAD src0_sel:DWORD src1_sel:WORD_1
v_mfma_f32_16x16x32_bf16 v[204:207],v[108:111],v[136:139],v[204:207]
s_waitcnt lgkmcnt(8)
ds_read_b64_tr_b16 v[74:75],v8 offset:6144
ds_read_b64_tr_b16 v[76:77],v9 offset:4096
v_lshlrev_b32_sdwa v156,1,v156 dst_sel:DWORD dst_unused:UNUSED_PAD src0_sel:DWORD src1_sel:WORD_1
v_mfma_f32_16x16x32_bf16 v[128:131],v[64:67],v[16:19],v[112:115]
ds_read_u16 v152,v152
v_lshlrev_b32_sdwa v158,1,v158 dst_sel:DWORD dst_unused:UNUSED_PAD src0_sel:DWORD src1_sel:WORD_1
v_mfma_f32_16x16x32_bf16 v[136:139],v[64:67],v[20:23],v[112:115]
s_waitcnt lgkmcnt(6)
ds_read_u16_d16_hi v154,v154
ds_read_b64_tr_b16 v[78:79],v9 offset:6144
v_lshlrev_b32_sdwa v157,1,v157 dst_sel:DWORD dst_unused:UNUSED_PAD src0_sel:DWORD src1_sel:WORD_1
v_mfma_f32_16x16x32_bf16 v[140:143],v[68:71],v[20:23],v[116:119]
ds_read_b64_tr_b16 v[80:81],v8 offset:8192
ds_read_b64_tr_b16 v[82:83],v8 offset:10240
v_lshlrev_b32_sdwa v159,1,v159 dst_sel:DWORD dst_unused:UNUSED_PAD src0_sel:DWORD src1_sel:WORD_1
v_mfma_f32_16x16x32_bf16 v[132:135],v[68:71],v[16:19],v[116:119]
s_waitcnt lgkmcnt(6)
ds_read_u16 v153,v153
v_mfma_f32_16x16x32_bf16 v[128:131],v[72:75],v[24:27],v[128:131]
ds_read_u16_d16_hi v155,v155
v_mfma_f32_16x16x32_bf16 v[136:139],v[72:75],v[28:31],v[136:139]
s_waitcnt lgkmcnt(4)
ds_read_b64_tr_b16 v[84:85],v9 offset:8192
ds_read_b64_tr_b16 v[86:87],v9 offset:10240
v_mfma_f32_16x16x32_bf16 v[140:143],v[76:79],v[28:31],v[140:143]
ds_read_u16 v156,v156
v_mfma_f32_16x16x32_bf16 v[132:135],v[76:79],v[24:27],v[132:135]
s_waitcnt lgkmcnt(5)
ds_read_u16_d16_hi v158,v158
v_lshl_add_u64 v[144:145],v[144:145],0,v[146:147]
v_mfma_f32_16x16x32_bf16 v[128:131],v[80:83],v[32:35],v[128:131]
v_lshl_add_u64 v[146:147],v[148:149],0,v[150:151]
v_mfma_f32_16x16x32_bf16 v[136:139],v[80:83],v[36:39],v[136:139]
s_waitcnt lgkmcnt(2)
ds_read_u16 v157,v157
v_mfma_f32_16x16x32_bf16 v[140:143],v[84:87],v[36:39],v[140:143]
ds_read_u16_d16_hi v159,v159
v_mfma_f32_16x16x32_bf16 v[132:135],v[84:87],v[32:35],v[132:135]
s_nop 0
v_lshlrev_b32_sdwa v128,1,v128 dst_sel:DWORD dst_unused:UNUSED_PAD src0_sel:DWORD src1_sel:WORD_1
v_lshlrev_b32_sdwa v130,1,v130 dst_sel:DWORD dst_unused:UNUSED_PAD src0_sel:DWORD src1_sel:WORD_1
v_mfma_f32_16x16x32_bf16 v[208:211],v[88:91],v[144:147],v[208:211]
ds_read_u16 v128,v128
v_lshlrev_b32_sdwa v129,1,v129 dst_sel:DWORD dst_unused:UNUSED_PAD src0_sel:DWORD src1_sel:WORD_1
v_mfma_f32_16x16x32_bf16 v[216:219],v[92:95],v[144:147],v[216:219]
ds_read_u16_d16_hi v130,v130
v_lshlrev_b32_sdwa v131,1,v131 dst_sel:DWORD dst_unused:UNUSED_PAD src0_sel:DWORD src1_sel:WORD_1
v_mfma_f32_16x16x32_bf16 v[224:227],v[96:99],v[144:147],v[224:227]
v_lshlrev_b32_sdwa v132,1,v132 dst_sel:DWORD dst_unused:UNUSED_PAD src0_sel:DWORD src1_sel:WORD_1
v_mfma_f32_16x16x32_bf16 v[232:235],v[100:103],v[144:147],v[232:235]
ds_read_u16 v129,v129
v_lshl_add_u64 v[152:153],v[152:153],0,v[154:155]
v_lshlrev_b32_sdwa v134,1,v134 dst_sel:DWORD dst_unused:UNUSED_PAD src0_sel:DWORD src1_sel:WORD_1
v_mfma_f32_16x16x32_bf16 v[240:243],v[104:107],v[144:147],v[240:243]
s_waitcnt lgkmcnt(3)
ds_read_u16_d16_hi v131,v131
v_lshl_add_u64 v[154:155],v[156:157],0,v[158:159]
v_mfma_f32_16x16x32_bf16 v[248:251],v[108:111],v[144:147],v[248:251]
v_lshlrev_b32_sdwa v133,1,v133 dst_sel:DWORD dst_unused:UNUSED_PAD src0_sel:DWORD src1_sel:WORD_1
v_mfma_f32_16x16x32_bf16 v[212:215],v[88:91],v[152:155],v[212:215]
ds_read_u16 v132,v132
v_lshlrev_b32_sdwa v135,1,v135 dst_sel:DWORD dst_unused:UNUSED_PAD src0_sel:DWORD src1_sel:WORD_1
v_mfma_f32_16x16x32_bf16 v[220:223],v[92:95],v[152:155],v[220:223]
ds_read_u16_d16_hi v134,v134
v_lshlrev_b32_sdwa v136,1,v136 dst_sel:DWORD dst_unused:UNUSED_PAD src0_sel:DWORD src1_sel:WORD_1
v_mfma_f32_16x16x32_bf16 v[228:231],v[96:99],v[152:155],v[228:231]
ds_read_b64_tr_b16 v[88:89],v3 offset:38992
ds_read_b64_tr_b16 v[90:91],v3 offset:40528
v_lshlrev_b32_sdwa v138,1,v138 dst_sel:DWORD dst_unused:UNUSED_PAD src0_sel:DWORD src1_sel:WORD_1
v_mfma_f32_16x16x32_bf16 v[236:239],v[100:103],v[152:155],v[236:239]
ds_read_u16 v133,v133
v_lshlrev_b32_sdwa v137,1,v137 dst_sel:DWORD dst_unused:UNUSED_PAD src0_sel:DWORD src1_sel:WORD_1
v_mfma_f32_16x16x32_bf16 v[244:247],v[104:107],v[152:155],v[244:247]
ds_read_u16_d16_hi v135,v135
v_lshlrev_b32_sdwa v139,1,v139 dst_sel:DWORD dst_unused:UNUSED_PAD src0_sel:DWORD src1_sel:WORD_1
v_mfma_f32_16x16x32_bf16 v[252:255],v[108:111],v[152:155],v[252:255]
ds_read_b64_tr_b16 v[92:93],v3 offset:39000
ds_read_b64_tr_b16 v[94:95],v3 offset:40536
v_lshlrev_b32_sdwa v140,1,v140 dst_sel:DWORD dst_unused:UNUSED_PAD src0_sel:DWORD src1_sel:WORD_1
v_mfma_f32_16x16x32_bf16 v[144:147],v[64:67],v[40:43],v[112:115]
ds_read_u16 v136,v136
v_lshlrev_b32_sdwa v142,1,v142 dst_sel:DWORD dst_unused:UNUSED_PAD src0_sel:DWORD src1_sel:WORD_1
v_mfma_f32_16x16x32_bf16 v[152:155],v[64:67],v[44:47],v[112:115]
ds_read_u16_d16_hi v138,v138
v_lshlrev_b32_sdwa v141,1,v141 dst_sel:DWORD dst_unused:UNUSED_PAD src0_sel:DWORD src1_sel:WORD_1
v_mfma_f32_16x16x32_bf16 v[156:159],v[68:71],v[44:47],v[116:119]
ds_read_b64_tr_b16 v[96:97],v3 offset:39056
ds_read_b64_tr_b16 v[98:99],v3 offset:40592
v_lshlrev_b32_sdwa v143,1,v143 dst_sel:DWORD dst_unused:UNUSED_PAD src0_sel:DWORD src1_sel:WORD_1
v_mfma_f32_16x16x32_bf16 v[148:151],v[68:71],v[40:43],v[116:119]
ds_read_u16 v137,v137
v_mfma_f32_16x16x32_bf16 v[144:147],v[72:75],v[48:51],v[144:147]
ds_read_u16_d16_hi v139,v139
v_mfma_f32_16x16x32_bf16 v[152:155],v[72:75],v[52:55],v[152:155]
ds_read_b64_tr_b16 v[100:101],v3 offset:39064
ds_read_b64_tr_b16 v[102:103],v3 offset:40600
v_mfma_f32_16x16x32_bf16 v[156:159],v[76:79],v[52:55],v[156:159]
ds_read_u16 v140,v140
v_mfma_f32_16x16x32_bf16 v[148:151],v[76:79],v[48:51],v[148:151]
s_waitcnt lgkmcnt(15)
ds_read_u16_d16_hi v142,v142
v_lshl_add_u64 v[128:129],v[128:129],0,v[130:131]
v_mfma_f32_16x16x32_bf16 v[144:147],v[80:83],v[56:59],v[144:147]
s_waitcnt lgkmcnt(12)
ds_read_b64_tr_b16 v[104:105],v3 offset:39120
ds_read_b64_tr_b16 v[106:107],v3 offset:40656
v_lshl_add_u64 v[130:131],v[132:133],0,v[134:135]
v_mfma_f32_16x16x32_bf16 v[152:155],v[80:83],v[60:63],v[152:155]
ds_read_u16 v141,v141
v_mfma_f32_16x16x32_bf16 v[156:159],v[84:87],v[60:63],v[156:159]
ds_read_u16_d16_hi v143,v143
v_mfma_f32_16x16x32_bf16 v[148:151],v[84:87],v[56:59],v[148:151]
ds_read_b64_tr_b16 v[108:109],v3 offset:39128
ds_read_b64_tr_b16 v[110:111],v3 offset:40664
v_lshlrev_b32_sdwa v144,1,v144 dst_sel:DWORD dst_unused:UNUSED_PAD src0_sel:DWORD src1_sel:WORD_1
v_lshlrev_b32_sdwa v146,1,v146 dst_sel:DWORD dst_unused:UNUSED_PAD src0_sel:DWORD src1_sel:WORD_1
v_mfma_f32_16x16x32_bf16 v[160:163],v[88:91],v[128:131],v[160:163]
s_waitcnt lgkmcnt(15)
ds_read_u16 v144,v144
v_lshlrev_b32_sdwa v145,1,v145 dst_sel:DWORD dst_unused:UNUSED_PAD src0_sel:DWORD src1_sel:WORD_1
v_mfma_f32_16x16x32_bf16 v[168:171],v[92:95],v[128:131],v[168:171]
s_waitcnt lgkmcnt(13)
ds_read_u16_d16_hi v146,v146
v_lshlrev_b32_sdwa v147,1,v147 dst_sel:DWORD dst_unused:UNUSED_PAD src0_sel:DWORD src1_sel:WORD_1
v_mfma_f32_16x16x32_bf16 v[176:179],v[96:99],v[128:131],v[176:179]
s_waitcnt lgkmcnt(10)
ds_read_b64_tr_b16 v[64:65],v8 offset:512
ds_read_b64_tr_b16 v[66:67],v8 offset:2560
v_lshlrev_b32_sdwa v148,1,v148 dst_sel:DWORD dst_unused:UNUSED_PAD src0_sel:DWORD src1_sel:WORD_1
v_mfma_f32_16x16x32_bf16 v[184:187],v[100:103],v[128:131],v[184:187]
s_waitcnt lgkmcnt(8)
ds_read_u16 v145,v145
v_lshl_add_u64 v[136:137],v[136:137],0,v[138:139]
v_lshlrev_b32_sdwa v150,1,v150 dst_sel:DWORD dst_unused:UNUSED_PAD src0_sel:DWORD src1_sel:WORD_1
v_mfma_f32_16x16x32_bf16 v[192:195],v[104:107],v[128:131],v[192:195]
s_waitcnt lgkmcnt(5)
ds_read_u16_d16_hi v147,v147
v_lshl_add_u64 v[138:139],v[140:141],0,v[142:143]
v_mfma_f32_16x16x32_bf16 v[200:203],v[108:111],v[128:131],v[200:203]
ds_read_b128 v[112:115],v4 offset:1408
ds_read_b64_tr_b16 v[68:69],v9 offset:512
v_lshlrev_b32_sdwa v149,1,v149 dst_sel:DWORD dst_unused:UNUSED_PAD src0_sel:DWORD src1_sel:WORD_1
v_mfma_f32_16x16x32_bf16 v[164:167],v[88:91],v[136:139],v[164:167]
ds_read_u16 v148,v148
v_lshlrev_b32_sdwa v151,1,v151 dst_sel:DWORD dst_unused:UNUSED_PAD src0_sel:DWORD src1_sel:WORD_1
v_mfma_f32_16x16x32_bf16 v[172:175],v[92:95],v[136:139],v[172:175]
ds_read_u16_d16_hi v150,v150
ds_read_b64_tr_b16 v[70:71],v9 offset:2560
v_lshlrev_b32_sdwa v152,1,v152 dst_sel:DWORD dst_unused:UNUSED_PAD src0_sel:DWORD src1_sel:WORD_1
v_mfma_f32_16x16x32_bf16 v[180:183],v[96:99],v[136:139],v[180:183]
ds_read_b128 v[116:119],v4 offset:1440
ds_read_b64_tr_b16 v[72:73],v8 offset:4608
v_lshlrev_b32_sdwa v154,1,v154 dst_sel:DWORD dst_unused:UNUSED_PAD src0_sel:DWORD src1_sel:WORD_1
v_mfma_f32_16x16x32_bf16 v[188:191],v[100:103],v[136:139],v[188:191]
ds_read_u16 v149,v149
v_lshlrev_b32_sdwa v153,1,v153 dst_sel:DWORD dst_unused:UNUSED_PAD src0_sel:DWORD src1_sel:WORD_1
v_mfma_f32_16x16x32_bf16 v[196:199],v[104:107],v[136:139],v[196:199]
ds_read_u16_d16_hi v151,v151
v_lshlrev_b32_sdwa v155,1,v155 dst_sel:DWORD dst_unused:UNUSED_PAD src0_sel:DWORD src1_sel:WORD_1
v_mfma_f32_16x16x32_bf16 v[204:207],v[108:111],v[136:139],v[204:207]
s_waitcnt lgkmcnt(8)
ds_read_b64_tr_b16 v[74:75],v8 offset:6656
ds_read_b64_tr_b16 v[76:77],v9 offset:4608
v_lshlrev_b32_sdwa v156,1,v156 dst_sel:DWORD dst_unused:UNUSED_PAD src0_sel:DWORD src1_sel:WORD_1
v_mfma_f32_16x16x32_bf16 v[128:131],v[64:67],v[16:19],v[112:115]
ds_read_u16 v152,v152
v_lshlrev_b32_sdwa v158,1,v158 dst_sel:DWORD dst_unused:UNUSED_PAD src0_sel:DWORD src1_sel:WORD_1
v_mfma_f32_16x16x32_bf16 v[136:139],v[64:67],v[20:23],v[112:115]
s_waitcnt lgkmcnt(6)
ds_read_u16_d16_hi v154,v154
ds_read_b64_tr_b16 v[78:79],v9 offset:6656
v_lshlrev_b32_sdwa v157,1,v157 dst_sel:DWORD dst_unused:UNUSED_PAD src0_sel:DWORD src1_sel:WORD_1
v_mfma_f32_16x16x32_bf16 v[140:143],v[68:71],v[20:23],v[116:119]
ds_read_b64_tr_b16 v[80:81],v8 offset:8704
ds_read_b64_tr_b16 v[82:83],v8 offset:10752
v_lshlrev_b32_sdwa v159,1,v159 dst_sel:DWORD dst_unused:UNUSED_PAD src0_sel:DWORD src1_sel:WORD_1
v_mfma_f32_16x16x32_bf16 v[132:135],v[68:71],v[16:19],v[116:119]
s_waitcnt lgkmcnt(6)
ds_read_u16 v153,v153
v_mfma_f32_16x16x32_bf16 v[128:131],v[72:75],v[24:27],v[128:131]
ds_read_u16_d16_hi v155,v155
v_mfma_f32_16x16x32_bf16 v[136:139],v[72:75],v[28:31],v[136:139]
s_waitcnt lgkmcnt(4)
ds_read_b64_tr_b16 v[84:85],v9 offset:8704
ds_read_b64_tr_b16 v[86:87],v9 offset:10752
v_mfma_f32_16x16x32_bf16 v[140:143],v[76:79],v[28:31],v[140:143]
ds_read_u16 v156,v156
v_mfma_f32_16x16x32_bf16 v[132:135],v[76:79],v[24:27],v[132:135]
s_waitcnt lgkmcnt(5)
ds_read_u16_d16_hi v158,v158
v_lshl_add_u64 v[144:145],v[144:145],0,v[146:147]
v_mfma_f32_16x16x32_bf16 v[128:131],v[80:83],v[32:35],v[128:131]
v_lshl_add_u64 v[146:147],v[148:149],0,v[150:151]
v_mfma_f32_16x16x32_bf16 v[136:139],v[80:83],v[36:39],v[136:139]
s_waitcnt lgkmcnt(2)
ds_read_u16 v157,v157
v_mfma_f32_16x16x32_bf16 v[140:143],v[84:87],v[36:39],v[140:143]
ds_read_u16_d16_hi v159,v159
v_mfma_f32_16x16x32_bf16 v[132:135],v[84:87],v[32:35],v[132:135]
s_nop 0
v_lshlrev_b32_sdwa v128,1,v128 dst_sel:DWORD dst_unused:UNUSED_PAD src0_sel:DWORD src1_sel:WORD_1
v_lshlrev_b32_sdwa v130,1,v130 dst_sel:DWORD dst_unused:UNUSED_PAD src0_sel:DWORD src1_sel:WORD_1
v_mfma_f32_16x16x32_bf16 v[208:211],v[88:91],v[144:147],v[208:211]
ds_read_u16 v128,v128
v_lshlrev_b32_sdwa v129,1,v129 dst_sel:DWORD dst_unused:UNUSED_PAD src0_sel:DWORD src1_sel:WORD_1
v_mfma_f32_16x16x32_bf16 v[216:219],v[92:95],v[144:147],v[216:219]
ds_read_u16_d16_hi v130,v130
v_lshlrev_b32_sdwa v131,1,v131 dst_sel:DWORD dst_unused:UNUSED_PAD src0_sel:DWORD src1_sel:WORD_1
v_mfma_f32_16x16x32_bf16 v[224:227],v[96:99],v[144:147],v[224:227]
v_lshlrev_b32_sdwa v132,1,v132 dst_sel:DWORD dst_unused:UNUSED_PAD src0_sel:DWORD src1_sel:WORD_1
v_mfma_f32_16x16x32_bf16 v[232:235],v[100:103],v[144:147],v[232:235]
ds_read_u16 v129,v129
v_lshl_add_u64 v[152:153],v[152:153],0,v[154:155]
v_lshlrev_b32_sdwa v134,1,v134 dst_sel:DWORD dst_unused:UNUSED_PAD src0_sel:DWORD src1_sel:WORD_1
v_mfma_f32_16x16x32_bf16 v[240:243],v[104:107],v[144:147],v[240:243]
s_waitcnt lgkmcnt(3)
ds_read_u16_d16_hi v131,v131
v_lshl_add_u64 v[154:155],v[156:157],0,v[158:159]
v_mfma_f32_16x16x32_bf16 v[248:251],v[108:111],v[144:147],v[248:251]
v_lshlrev_b32_sdwa v133,1,v133 dst_sel:DWORD dst_unused:UNUSED_PAD src0_sel:DWORD src1_sel:WORD_1
v_mfma_f32_16x16x32_bf16 v[212:215],v[88:91],v[152:155],v[212:215]
ds_read_u16 v132,v132
v_lshlrev_b32_sdwa v135,1,v135 dst_sel:DWORD dst_unused:UNUSED_PAD src0_sel:DWORD src1_sel:WORD_1
v_mfma_f32_16x16x32_bf16 v[220:223],v[92:95],v[152:155],v[220:223]
ds_read_u16_d16_hi v134,v134
v_lshlrev_b32_sdwa v136,1,v136 dst_sel:DWORD dst_unused:UNUSED_PAD src0_sel:DWORD src1_sel:WORD_1
v_mfma_f32_16x16x32_bf16 v[228:231],v[96:99],v[152:155],v[228:231]
ds_read_b64_tr_b16 v[88:89],v3 offset:45152
ds_read_b64_tr_b16 v[90:91],v3 offset:46688
v_lshlrev_b32_sdwa v138,1,v138 dst_sel:DWORD dst_unused:UNUSED_PAD src0_sel:DWORD src1_sel:WORD_1
v_mfma_f32_16x16x32_bf16 v[236:239],v[100:103],v[152:155],v[236:239]
ds_read_u16 v133,v133
v_lshlrev_b32_sdwa v137,1,v137 dst_sel:DWORD dst_unused:UNUSED_PAD src0_sel:DWORD src1_sel:WORD_1
v_mfma_f32_16x16x32_bf16 v[244:247],v[104:107],v[152:155],v[244:247]
ds_read_u16_d16_hi v135,v135
v_lshlrev_b32_sdwa v139,1,v139 dst_sel:DWORD dst_unused:UNUSED_PAD src0_sel:DWORD src1_sel:WORD_1
v_mfma_f32_16x16x32_bf16 v[252:255],v[108:111],v[152:155],v[252:255]
ds_read_b64_tr_b16 v[92:93],v3 offset:45160
ds_read_b64_tr_b16 v[94:95],v3 offset:46696
v_lshlrev_b32_sdwa v140,1,v140 dst_sel:DWORD dst_unused:UNUSED_PAD src0_sel:DWORD src1_sel:WORD_1
v_mfma_f32_16x16x32_bf16 v[144:147],v[64:67],v[40:43],v[112:115]
ds_read_u16 v136,v136
v_lshlrev_b32_sdwa v142,1,v142 dst_sel:DWORD dst_unused:UNUSED_PAD src0_sel:DWORD src1_sel:WORD_1
v_mfma_f32_16x16x32_bf16 v[152:155],v[64:67],v[44:47],v[112:115]
ds_read_u16_d16_hi v138,v138
v_lshlrev_b32_sdwa v141,1,v141 dst_sel:DWORD dst_unused:UNUSED_PAD src0_sel:DWORD src1_sel:WORD_1
v_mfma_f32_16x16x32_bf16 v[156:159],v[68:71],v[44:47],v[116:119]
ds_read_b64_tr_b16 v[96:97],v3 offset:45216
ds_read_b64_tr_b16 v[98:99],v3 offset:46752
v_lshlrev_b32_sdwa v143,1,v143 dst_sel:DWORD dst_unused:UNUSED_PAD src0_sel:DWORD src1_sel:WORD_1
v_mfma_f32_16x16x32_bf16 v[148:151],v[68:71],v[40:43],v[116:119]
ds_read_u16 v137,v137
v_mfma_f32_16x16x32_bf16 v[144:147],v[72:75],v[48:51],v[144:147]
ds_read_u16_d16_hi v139,v139
v_mfma_f32_16x16x32_bf16 v[152:155],v[72:75],v[52:55],v[152:155]
ds_read_b64_tr_b16 v[100:101],v3 offset:45224
ds_read_b64_tr_b16 v[102:103],v3 offset:46760
v_mfma_f32_16x16x32_bf16 v[156:159],v[76:79],v[52:55],v[156:159]
ds_read_u16 v140,v140
v_mfma_f32_16x16x32_bf16 v[148:151],v[76:79],v[48:51],v[148:151]
s_waitcnt lgkmcnt(15)
ds_read_u16_d16_hi v142,v142
v_lshl_add_u64 v[128:129],v[128:129],0,v[130:131]
v_mfma_f32_16x16x32_bf16 v[144:147],v[80:83],v[56:59],v[144:147]
s_waitcnt lgkmcnt(12)
ds_read_b64_tr_b16 v[104:105],v3 offset:45280
ds_read_b64_tr_b16 v[106:107],v3 offset:46816
v_lshl_add_u64 v[130:131],v[132:133],0,v[134:135]
v_mfma_f32_16x16x32_bf16 v[152:155],v[80:83],v[60:63],v[152:155]
ds_read_u16 v141,v141
v_mfma_f32_16x16x32_bf16 v[156:159],v[84:87],v[60:63],v[156:159]
ds_read_u16_d16_hi v143,v143
v_mfma_f32_16x16x32_bf16 v[148:151],v[84:87],v[56:59],v[148:151]
ds_read_b64_tr_b16 v[108:109],v3 offset:45288
ds_read_b64_tr_b16 v[110:111],v3 offset:46824
v_lshlrev_b32_sdwa v144,1,v144 dst_sel:DWORD dst_unused:UNUSED_PAD src0_sel:DWORD src1_sel:WORD_1
v_lshlrev_b32_sdwa v146,1,v146 dst_sel:DWORD dst_unused:UNUSED_PAD src0_sel:DWORD src1_sel:WORD_1
v_mfma_f32_16x16x32_bf16 v[160:163],v[88:91],v[128:131],v[160:163]
s_waitcnt lgkmcnt(15)
ds_read_u16 v144,v144
v_lshlrev_b32_sdwa v145,1,v145 dst_sel:DWORD dst_unused:UNUSED_PAD src0_sel:DWORD src1_sel:WORD_1
v_mfma_f32_16x16x32_bf16 v[168:171],v[92:95],v[128:131],v[168:171]
s_waitcnt lgkmcnt(13)
ds_read_u16_d16_hi v146,v146
v_lshlrev_b32_sdwa v147,1,v147 dst_sel:DWORD dst_unused:UNUSED_PAD src0_sel:DWORD src1_sel:WORD_1
v_mfma_f32_16x16x32_bf16 v[176:179],v[96:99],v[128:131],v[176:179]
s_waitcnt lgkmcnt(10)
ds_read_b64_tr_b16 v[64:65],v8 offset:12320
ds_read_b64_tr_b16 v[66:67],v8 offset:14368
v_lshlrev_b32_sdwa v148,1,v148 dst_sel:DWORD dst_unused:UNUSED_PAD src0_sel:DWORD src1_sel:WORD_1
v_mfma_f32_16x16x32_bf16 v[184:187],v[100:103],v[128:131],v[184:187]
s_waitcnt lgkmcnt(8)
ds_read_u16 v145,v145
v_lshl_add_u64 v[136:137],v[136:137],0,v[138:139]
v_lshlrev_b32_sdwa v150,1,v150 dst_sel:DWORD dst_unused:UNUSED_PAD src0_sel:DWORD src1_sel:WORD_1
v_mfma_f32_16x16x32_bf16 v[192:195],v[104:107],v[128:131],v[192:195]
s_waitcnt lgkmcnt(5)
ds_read_u16_d16_hi v147,v147
v_lshl_add_u64 v[138:139],v[140:141],0,v[142:143]
v_mfma_f32_16x16x32_bf16 v[200:203],v[108:111],v[128:131],v[200:203]
ds_read_b128 v[112:115],v4 offset:0
ds_read_b64_tr_b16 v[68:69],v9 offset:12320
v_lshlrev_b32_sdwa v149,1,v149 dst_sel:DWORD dst_unused:UNUSED_PAD src0_sel:DWORD src1_sel:WORD_1
v_mfma_f32_16x16x32_bf16 v[164:167],v[88:91],v[136:139],v[164:167]
ds_read_u16 v148,v148
v_lshlrev_b32_sdwa v151,1,v151 dst_sel:DWORD dst_unused:UNUSED_PAD src0_sel:DWORD src1_sel:WORD_1
v_mfma_f32_16x16x32_bf16 v[172:175],v[92:95],v[136:139],v[172:175]
ds_read_u16_d16_hi v150,v150
ds_read_b64_tr_b16 v[70:71],v9 offset:14368
v_lshlrev_b32_sdwa v152,1,v152 dst_sel:DWORD dst_unused:UNUSED_PAD src0_sel:DWORD src1_sel:WORD_1
v_mfma_f32_16x16x32_bf16 v[180:183],v[96:99],v[136:139],v[180:183]
ds_read_b128 v[116:119],v4 offset:32
ds_read_b64_tr_b16 v[72:73],v8 offset:16416
v_lshlrev_b32_sdwa v154,1,v154 dst_sel:DWORD dst_unused:UNUSED_PAD src0_sel:DWORD src1_sel:WORD_1
v_mfma_f32_16x16x32_bf16 v[188:191],v[100:103],v[136:139],v[188:191]
ds_read_u16 v149,v149
v_lshlrev_b32_sdwa v153,1,v153 dst_sel:DWORD dst_unused:UNUSED_PAD src0_sel:DWORD src1_sel:WORD_1
v_mfma_f32_16x16x32_bf16 v[196:199],v[104:107],v[136:139],v[196:199]
ds_read_u16_d16_hi v151,v151
v_lshlrev_b32_sdwa v155,1,v155 dst_sel:DWORD dst_unused:UNUSED_PAD src0_sel:DWORD src1_sel:WORD_1
v_mfma_f32_16x16x32_bf16 v[204:207],v[108:111],v[136:139],v[204:207]
s_waitcnt lgkmcnt(8)
ds_read_b64_tr_b16 v[74:75],v8 offset:18464
ds_read_b64_tr_b16 v[76:77],v9 offset:16416
v_lshlrev_b32_sdwa v156,1,v156 dst_sel:DWORD dst_unused:UNUSED_PAD src0_sel:DWORD src1_sel:WORD_1
v_mfma_f32_16x16x32_bf16 v[128:131],v[64:67],v[16:19],v[112:115]
ds_read_u16 v152,v152
v_lshlrev_b32_sdwa v158,1,v158 dst_sel:DWORD dst_unused:UNUSED_PAD src0_sel:DWORD src1_sel:WORD_1
v_mfma_f32_16x16x32_bf16 v[136:139],v[64:67],v[20:23],v[112:115]
s_waitcnt lgkmcnt(6)
ds_read_u16_d16_hi v154,v154
ds_read_b64_tr_b16 v[78:79],v9 offset:18464
v_lshlrev_b32_sdwa v157,1,v157 dst_sel:DWORD dst_unused:UNUSED_PAD src0_sel:DWORD src1_sel:WORD_1
v_mfma_f32_16x16x32_bf16 v[140:143],v[68:71],v[20:23],v[116:119]
ds_read_b64_tr_b16 v[80:81],v8 offset:20512
ds_read_b64_tr_b16 v[82:83],v8 offset:22560
v_lshlrev_b32_sdwa v159,1,v159 dst_sel:DWORD dst_unused:UNUSED_PAD src0_sel:DWORD src1_sel:WORD_1
v_mfma_f32_16x16x32_bf16 v[132:135],v[68:71],v[16:19],v[116:119]
s_waitcnt lgkmcnt(6)
ds_read_u16 v153,v153
v_mfma_f32_16x16x32_bf16 v[128:131],v[72:75],v[24:27],v[128:131]
ds_read_u16_d16_hi v155,v155
v_mfma_f32_16x16x32_bf16 v[136:139],v[72:75],v[28:31],v[136:139]
s_waitcnt lgkmcnt(4)
ds_read_b64_tr_b16 v[84:85],v9 offset:20512
ds_read_b64_tr_b16 v[86:87],v9 offset:22560
v_mfma_f32_16x16x32_bf16 v[140:143],v[76:79],v[28:31],v[140:143]
ds_read_u16 v156,v156
v_mfma_f32_16x16x32_bf16 v[132:135],v[76:79],v[24:27],v[132:135]
s_waitcnt lgkmcnt(5)
ds_read_u16_d16_hi v158,v158
v_lshl_add_u64 v[144:145],v[144:145],0,v[146:147]
v_mfma_f32_16x16x32_bf16 v[128:131],v[80:83],v[32:35],v[128:131]
v_lshl_add_u64 v[146:147],v[148:149],0,v[150:151]
v_mfma_f32_16x16x32_bf16 v[136:139],v[80:83],v[36:39],v[136:139]
s_waitcnt lgkmcnt(2)
ds_read_u16 v157,v157
v_mfma_f32_16x16x32_bf16 v[140:143],v[84:87],v[36:39],v[140:143]
ds_read_u16_d16_hi v159,v159
v_mfma_f32_16x16x32_bf16 v[132:135],v[84:87],v[32:35],v[132:135]
s_nop 0
v_lshlrev_b32_sdwa v128,1,v128 dst_sel:DWORD dst_unused:UNUSED_PAD src0_sel:DWORD src1_sel:WORD_1
v_lshlrev_b32_sdwa v130,1,v130 dst_sel:DWORD dst_unused:UNUSED_PAD src0_sel:DWORD src1_sel:WORD_1
v_mfma_f32_16x16x32_bf16 v[208:211],v[88:91],v[144:147],v[208:211]
ds_read_u16 v128,v128
v_lshlrev_b32_sdwa v129,1,v129 dst_sel:DWORD dst_unused:UNUSED_PAD src0_sel:DWORD src1_sel:WORD_1
v_mfma_f32_16x16x32_bf16 v[216:219],v[92:95],v[144:147],v[216:219]
ds_read_u16_d16_hi v130,v130
v_lshlrev_b32_sdwa v131,1,v131 dst_sel:DWORD dst_unused:UNUSED_PAD src0_sel:DWORD src1_sel:WORD_1
v_mfma_f32_16x16x32_bf16 v[224:227],v[96:99],v[144:147],v[224:227]
v_lshlrev_b32_sdwa v132,1,v132 dst_sel:DWORD dst_unused:UNUSED_PAD src0_sel:DWORD src1_sel:WORD_1
v_mfma_f32_16x16x32_bf16 v[232:235],v[100:103],v[144:147],v[232:235]
ds_read_u16 v129,v129
v_lshl_add_u64 v[152:153],v[152:153],0,v[154:155]
v_lshlrev_b32_sdwa v134,1,v134 dst_sel:DWORD dst_unused:UNUSED_PAD src0_sel:DWORD src1_sel:WORD_1
v_mfma_f32_16x16x32_bf16 v[240:243],v[104:107],v[144:147],v[240:243]
s_waitcnt lgkmcnt(3)
ds_read_u16_d16_hi v131,v131
v_lshl_add_u64 v[154:155],v[156:157],0,v[158:159]
v_mfma_f32_16x16x32_bf16 v[248:251],v[108:111],v[144:147],v[248:251]
v_lshlrev_b32_sdwa v133,1,v133 dst_sel:DWORD dst_unused:UNUSED_PAD src0_sel:DWORD src1_sel:WORD_1
v_mfma_f32_16x16x32_bf16 v[212:215],v[88:91],v[152:155],v[212:215]
ds_read_u16 v132,v132
v_lshlrev_b32_sdwa v135,1,v135 dst_sel:DWORD dst_unused:UNUSED_PAD src0_sel:DWORD src1_sel:WORD_1
v_mfma_f32_16x16x32_bf16 v[220:223],v[92:95],v[152:155],v[220:223]
ds_read_u16_d16_hi v134,v134
v_lshlrev_b32_sdwa v136,1,v136 dst_sel:DWORD dst_unused:UNUSED_PAD src0_sel:DWORD src1_sel:WORD_1
v_mfma_f32_16x16x32_bf16 v[228:231],v[96:99],v[152:155],v[228:231]
ds_read_b64_tr_b16 v[88:89],v3 offset:51312
ds_read_b64_tr_b16 v[90:91],v3 offset:52848
v_lshlrev_b32_sdwa v138,1,v138 dst_sel:DWORD dst_unused:UNUSED_PAD src0_sel:DWORD src1_sel:WORD_1
v_mfma_f32_16x16x32_bf16 v[236:239],v[100:103],v[152:155],v[236:239]
ds_read_u16 v133,v133
v_lshlrev_b32_sdwa v137,1,v137 dst_sel:DWORD dst_unused:UNUSED_PAD src0_sel:DWORD src1_sel:WORD_1
v_mfma_f32_16x16x32_bf16 v[244:247],v[104:107],v[152:155],v[244:247]
ds_read_u16_d16_hi v135,v135
v_lshlrev_b32_sdwa v139,1,v139 dst_sel:DWORD dst_unused:UNUSED_PAD src0_sel:DWORD src1_sel:WORD_1
v_mfma_f32_16x16x32_bf16 v[252:255],v[108:111],v[152:155],v[252:255]
ds_read_b64_tr_b16 v[92:93],v3 offset:51320
ds_read_b64_tr_b16 v[94:95],v3 offset:52856
v_lshlrev_b32_sdwa v140,1,v140 dst_sel:DWORD dst_unused:UNUSED_PAD src0_sel:DWORD src1_sel:WORD_1
v_mfma_f32_16x16x32_bf16 v[144:147],v[64:67],v[40:43],v[112:115]
ds_read_u16 v136,v136
v_lshlrev_b32_sdwa v142,1,v142 dst_sel:DWORD dst_unused:UNUSED_PAD src0_sel:DWORD src1_sel:WORD_1
v_mfma_f32_16x16x32_bf16 v[152:155],v[64:67],v[44:47],v[112:115]
ds_read_u16_d16_hi v138,v138
v_lshlrev_b32_sdwa v141,1,v141 dst_sel:DWORD dst_unused:UNUSED_PAD src0_sel:DWORD src1_sel:WORD_1
v_mfma_f32_16x16x32_bf16 v[156:159],v[68:71],v[44:47],v[116:119]
ds_read_b64_tr_b16 v[96:97],v3 offset:51376
ds_read_b64_tr_b16 v[98:99],v3 offset:52912
v_lshlrev_b32_sdwa v143,1,v143 dst_sel:DWORD dst_unused:UNUSED_PAD src0_sel:DWORD src1_sel:WORD_1
v_mfma_f32_16x16x32_bf16 v[148:151],v[68:71],v[40:43],v[116:119]
ds_read_u16 v137,v137
v_mfma_f32_16x16x32_bf16 v[144:147],v[72:75],v[48:51],v[144:147]
ds_read_u16_d16_hi v139,v139
v_mfma_f32_16x16x32_bf16 v[152:155],v[72:75],v[52:55],v[152:155]
ds_read_b64_tr_b16 v[100:101],v3 offset:51384
ds_read_b64_tr_b16 v[102:103],v3 offset:52920
v_mfma_f32_16x16x32_bf16 v[156:159],v[76:79],v[52:55],v[156:159]
ds_read_u16 v140,v140
v_mfma_f32_16x16x32_bf16 v[148:151],v[76:79],v[48:51],v[148:151]
s_waitcnt lgkmcnt(15)
ds_read_u16_d16_hi v142,v142
v_lshl_add_u64 v[128:129],v[128:129],0,v[130:131]
v_mfma_f32_16x16x32_bf16 v[144:147],v[80:83],v[56:59],v[144:147]
s_waitcnt lgkmcnt(12)
ds_read_b64_tr_b16 v[104:105],v3 offset:51440
ds_read_b64_tr_b16 v[106:107],v3 offset:52976
v_lshl_add_u64 v[130:131],v[132:133],0,v[134:135]
v_mfma_f32_16x16x32_bf16 v[152:155],v[80:83],v[60:63],v[152:155]
ds_read_u16 v141,v141
v_mfma_f32_16x16x32_bf16 v[156:159],v[84:87],v[60:63],v[156:159]
ds_read_u16_d16_hi v143,v143
v_mfma_f32_16x16x32_bf16 v[148:151],v[84:87],v[56:59],v[148:151]
ds_read_b64_tr_b16 v[108:109],v3 offset:51448
ds_read_b64_tr_b16 v[110:111],v3 offset:52984
v_lshlrev_b32_sdwa v144,1,v144 dst_sel:DWORD dst_unused:UNUSED_PAD src0_sel:DWORD src1_sel:WORD_1
v_lshlrev_b32_sdwa v146,1,v146 dst_sel:DWORD dst_unused:UNUSED_PAD src0_sel:DWORD src1_sel:WORD_1
v_mfma_f32_16x16x32_bf16 v[160:163],v[88:91],v[128:131],v[160:163]
s_waitcnt lgkmcnt(15)
ds_read_u16 v144,v144
v_lshlrev_b32_sdwa v145,1,v145 dst_sel:DWORD dst_unused:UNUSED_PAD src0_sel:DWORD src1_sel:WORD_1
v_mfma_f32_16x16x32_bf16 v[168:171],v[92:95],v[128:131],v[168:171]
s_waitcnt lgkmcnt(13)
ds_read_u16_d16_hi v146,v146
v_lshlrev_b32_sdwa v147,1,v147 dst_sel:DWORD dst_unused:UNUSED_PAD src0_sel:DWORD src1_sel:WORD_1
v_mfma_f32_16x16x32_bf16 v[176:179],v[96:99],v[128:131],v[176:179]
s_waitcnt lgkmcnt(10)
ds_read_b64_tr_b16 v[64:65],v8 offset:12832
ds_read_b64_tr_b16 v[66:67],v8 offset:14880
v_lshlrev_b32_sdwa v148,1,v148 dst_sel:DWORD dst_unused:UNUSED_PAD src0_sel:DWORD src1_sel:WORD_1
v_mfma_f32_16x16x32_bf16 v[184:187],v[100:103],v[128:131],v[184:187]
s_waitcnt lgkmcnt(8)
ds_read_u16 v145,v145
v_lshl_add_u64 v[136:137],v[136:137],0,v[138:139]
v_lshlrev_b32_sdwa v150,1,v150 dst_sel:DWORD dst_unused:UNUSED_PAD src0_sel:DWORD src1_sel:WORD_1
v_mfma_f32_16x16x32_bf16 v[192:195],v[104:107],v[128:131],v[192:195]
s_waitcnt lgkmcnt(5)
ds_read_u16_d16_hi v147,v147
v_lshl_add_u64 v[138:139],v[140:141],0,v[142:143]
v_mfma_f32_16x16x32_bf16 v[200:203],v[108:111],v[128:131],v[200:203]
ds_read_b128 v[112:115],v4 offset:128
ds_read_b64_tr_b16 v[68:69],v9 offset:12832
v_lshlrev_b32_sdwa v149,1,v149 dst_sel:DWORD dst_unused:UNUSED_PAD src0_sel:DWORD src1_sel:WORD_1
v_mfma_f32_16x16x32_bf16 v[164:167],v[88:91],v[136:139],v[164:167]
ds_read_u16 v148,v148
v_lshlrev_b32_sdwa v151,1,v151 dst_sel:DWORD dst_unused:UNUSED_PAD src0_sel:DWORD src1_sel:WORD_1
v_mfma_f32_16x16x32_bf16 v[172:175],v[92:95],v[136:139],v[172:175]
ds_read_u16_d16_hi v150,v150
ds_read_b64_tr_b16 v[70:71],v9 offset:14880
v_lshlrev_b32_sdwa v152,1,v152 dst_sel:DWORD dst_unused:UNUSED_PAD src0_sel:DWORD src1_sel:WORD_1
v_mfma_f32_16x16x32_bf16 v[180:183],v[96:99],v[136:139],v[180:183]
ds_read_b128 v[116:119],v4 offset:160
ds_read_b64_tr_b16 v[72:73],v8 offset:16928
v_lshlrev_b32_sdwa v154,1,v154 dst_sel:DWORD dst_unused:UNUSED_PAD src0_sel:DWORD src1_sel:WORD_1
v_mfma_f32_16x16x32_bf16 v[188:191],v[100:103],v[136:139],v[188:191]
ds_read_u16 v149,v149
v_lshlrev_b32_sdwa v153,1,v153 dst_sel:DWORD dst_unused:UNUSED_PAD src0_sel:DWORD src1_sel:WORD_1
v_mfma_f32_16x16x32_bf16 v[196:199],v[104:107],v[136:139],v[196:199]
ds_read_u16_d16_hi v151,v151
v_lshlrev_b32_sdwa v155,1,v155 dst_sel:DWORD dst_unused:UNUSED_PAD src0_sel:DWORD src1_sel:WORD_1
v_mfma_f32_16x16x32_bf16 v[204:207],v[108:111],v[136:139],v[204:207]
s_waitcnt lgkmcnt(8)
ds_read_b64_tr_b16 v[74:75],v8 offset:18976
ds_read_b64_tr_b16 v[76:77],v9 offset:16928
v_lshlrev_b32_sdwa v156,1,v156 dst_sel:DWORD dst_unused:UNUSED_PAD src0_sel:DWORD src1_sel:WORD_1
v_mfma_f32_16x16x32_bf16 v[128:131],v[64:67],v[16:19],v[112:115]
ds_read_u16 v152,v152
v_lshlrev_b32_sdwa v158,1,v158 dst_sel:DWORD dst_unused:UNUSED_PAD src0_sel:DWORD src1_sel:WORD_1
v_mfma_f32_16x16x32_bf16 v[136:139],v[64:67],v[20:23],v[112:115]
s_waitcnt lgkmcnt(6)
ds_read_u16_d16_hi v154,v154
ds_read_b64_tr_b16 v[78:79],v9 offset:18976
v_lshlrev_b32_sdwa v157,1,v157 dst_sel:DWORD dst_unused:UNUSED_PAD src0_sel:DWORD src1_sel:WORD_1
v_mfma_f32_16x16x32_bf16 v[140:143],v[68:71],v[20:23],v[116:119]
ds_read_b64_tr_b16 v[80:81],v8 offset:21024
ds_read_b64_tr_b16 v[82:83],v8 offset:23072
v_lshlrev_b32_sdwa v159,1,v159 dst_sel:DWORD dst_unused:UNUSED_PAD src0_sel:DWORD src1_sel:WORD_1
v_mfma_f32_16x16x32_bf16 v[132:135],v[68:71],v[16:19],v[116:119]
s_waitcnt lgkmcnt(6)
ds_read_u16 v153,v153
v_mfma_f32_16x16x32_bf16 v[128:131],v[72:75],v[24:27],v[128:131]
ds_read_u16_d16_hi v155,v155
v_mfma_f32_16x16x32_bf16 v[136:139],v[72:75],v[28:31],v[136:139]
s_waitcnt lgkmcnt(4)
ds_read_b64_tr_b16 v[84:85],v9 offset:21024
ds_read_b64_tr_b16 v[86:87],v9 offset:23072
v_mfma_f32_16x16x32_bf16 v[140:143],v[76:79],v[28:31],v[140:143]
ds_read_u16 v156,v156
v_mfma_f32_16x16x32_bf16 v[132:135],v[76:79],v[24:27],v[132:135]
s_waitcnt lgkmcnt(5)
ds_read_u16_d16_hi v158,v158
v_lshl_add_u64 v[144:145],v[144:145],0,v[146:147]
v_mfma_f32_16x16x32_bf16 v[128:131],v[80:83],v[32:35],v[128:131]
v_lshl_add_u64 v[146:147],v[148:149],0,v[150:151]
v_mfma_f32_16x16x32_bf16 v[136:139],v[80:83],v[36:39],v[136:139]
s_waitcnt lgkmcnt(2)
ds_read_u16 v157,v157
v_mfma_f32_16x16x32_bf16 v[140:143],v[84:87],v[36:39],v[140:143]
ds_read_u16_d16_hi v159,v159
v_mfma_f32_16x16x32_bf16 v[132:135],v[84:87],v[32:35],v[132:135]
s_nop 0
v_lshlrev_b32_sdwa v128,1,v128 dst_sel:DWORD dst_unused:UNUSED_PAD src0_sel:DWORD src1_sel:WORD_1
v_lshlrev_b32_sdwa v130,1,v130 dst_sel:DWORD dst_unused:UNUSED_PAD src0_sel:DWORD src1_sel:WORD_1
v_mfma_f32_16x16x32_bf16 v[208:211],v[88:91],v[144:147],v[208:211]
ds_read_u16 v128,v128
v_lshlrev_b32_sdwa v129,1,v129 dst_sel:DWORD dst_unused:UNUSED_PAD src0_sel:DWORD src1_sel:WORD_1
v_mfma_f32_16x16x32_bf16 v[216:219],v[92:95],v[144:147],v[216:219]
ds_read_u16_d16_hi v130,v130
v_lshlrev_b32_sdwa v131,1,v131 dst_sel:DWORD dst_unused:UNUSED_PAD src0_sel:DWORD src1_sel:WORD_1
v_mfma_f32_16x16x32_bf16 v[224:227],v[96:99],v[144:147],v[224:227]
v_lshlrev_b32_sdwa v132,1,v132 dst_sel:DWORD dst_unused:UNUSED_PAD src0_sel:DWORD src1_sel:WORD_1
v_mfma_f32_16x16x32_bf16 v[232:235],v[100:103],v[144:147],v[232:235]
ds_read_u16 v129,v129
v_lshl_add_u64 v[152:153],v[152:153],0,v[154:155]
v_lshlrev_b32_sdwa v134,1,v134 dst_sel:DWORD dst_unused:UNUSED_PAD src0_sel:DWORD src1_sel:WORD_1
v_mfma_f32_16x16x32_bf16 v[240:243],v[104:107],v[144:147],v[240:243]
s_waitcnt lgkmcnt(3)
ds_read_u16_d16_hi v131,v131
v_lshl_add_u64 v[154:155],v[156:157],0,v[158:159]
v_mfma_f32_16x16x32_bf16 v[248:251],v[108:111],v[144:147],v[248:251]
v_lshlrev_b32_sdwa v133,1,v133 dst_sel:DWORD dst_unused:UNUSED_PAD src0_sel:DWORD src1_sel:WORD_1
v_mfma_f32_16x16x32_bf16 v[212:215],v[88:91],v[152:155],v[212:215]
ds_read_u16 v132,v132
v_lshlrev_b32_sdwa v135,1,v135 dst_sel:DWORD dst_unused:UNUSED_PAD src0_sel:DWORD src1_sel:WORD_1
v_mfma_f32_16x16x32_bf16 v[220:223],v[92:95],v[152:155],v[220:223]
ds_read_u16_d16_hi v134,v134
v_lshlrev_b32_sdwa v136,1,v136 dst_sel:DWORD dst_unused:UNUSED_PAD src0_sel:DWORD src1_sel:WORD_1
v_mfma_f32_16x16x32_bf16 v[228:231],v[96:99],v[152:155],v[228:231]
ds_read_b64_tr_b16 v[88:89],v3 offset:57472
ds_read_b64_tr_b16 v[90:91],v3 offset:59008
v_lshlrev_b32_sdwa v138,1,v138 dst_sel:DWORD dst_unused:UNUSED_PAD src0_sel:DWORD src1_sel:WORD_1
v_mfma_f32_16x16x32_bf16 v[236:239],v[100:103],v[152:155],v[236:239]
ds_read_u16 v133,v133
v_lshlrev_b32_sdwa v137,1,v137 dst_sel:DWORD dst_unused:UNUSED_PAD src0_sel:DWORD src1_sel:WORD_1
v_mfma_f32_16x16x32_bf16 v[244:247],v[104:107],v[152:155],v[244:247]
ds_read_u16_d16_hi v135,v135
v_lshlrev_b32_sdwa v139,1,v139 dst_sel:DWORD dst_unused:UNUSED_PAD src0_sel:DWORD src1_sel:WORD_1
v_mfma_f32_16x16x32_bf16 v[252:255],v[108:111],v[152:155],v[252:255]
ds_read_b64_tr_b16 v[92:93],v3 offset:57480
ds_read_b64_tr_b16 v[94:95],v3 offset:59016
v_lshlrev_b32_sdwa v140,1,v140 dst_sel:DWORD dst_unused:UNUSED_PAD src0_sel:DWORD src1_sel:WORD_1
v_mfma_f32_16x16x32_bf16 v[144:147],v[64:67],v[40:43],v[112:115]
ds_read_u16 v136,v136
v_lshlrev_b32_sdwa v142,1,v142 dst_sel:DWORD dst_unused:UNUSED_PAD src0_sel:DWORD src1_sel:WORD_1
v_mfma_f32_16x16x32_bf16 v[152:155],v[64:67],v[44:47],v[112:115]
ds_read_u16_d16_hi v138,v138
v_lshlrev_b32_sdwa v141,1,v141 dst_sel:DWORD dst_unused:UNUSED_PAD src0_sel:DWORD src1_sel:WORD_1
v_mfma_f32_16x16x32_bf16 v[156:159],v[68:71],v[44:47],v[116:119]
ds_read_b64_tr_b16 v[96:97],v3 offset:57536
ds_read_b64_tr_b16 v[98:99],v3 offset:59072
v_lshlrev_b32_sdwa v143,1,v143 dst_sel:DWORD dst_unused:UNUSED_PAD src0_sel:DWORD src1_sel:WORD_1
v_mfma_f32_16x16x32_bf16 v[148:151],v[68:71],v[40:43],v[116:119]
ds_read_u16 v137,v137
v_mfma_f32_16x16x32_bf16 v[144:147],v[72:75],v[48:51],v[144:147]
ds_read_u16_d16_hi v139,v139
v_mfma_f32_16x16x32_bf16 v[152:155],v[72:75],v[52:55],v[152:155]
ds_read_b64_tr_b16 v[100:101],v3 offset:57544
ds_read_b64_tr_b16 v[102:103],v3 offset:59080
v_mfma_f32_16x16x32_bf16 v[156:159],v[76:79],v[52:55],v[156:159]
ds_read_u16 v140,v140
v_mfma_f32_16x16x32_bf16 v[148:151],v[76:79],v[48:51],v[148:151]
s_waitcnt lgkmcnt(15)
ds_read_u16_d16_hi v142,v142
v_lshl_add_u64 v[128:129],v[128:129],0,v[130:131]
v_mfma_f32_16x16x32_bf16 v[144:147],v[80:83],v[56:59],v[144:147]
s_waitcnt lgkmcnt(12)
ds_read_b64_tr_b16 v[104:105],v3 offset:57600
ds_read_b64_tr_b16 v[106:107],v3 offset:59136
v_lshl_add_u64 v[130:131],v[132:133],0,v[134:135]
v_mfma_f32_16x16x32_bf16 v[152:155],v[80:83],v[60:63],v[152:155]
ds_read_u16 v141,v141
v_mfma_f32_16x16x32_bf16 v[156:159],v[84:87],v[60:63],v[156:159]
ds_read_u16_d16_hi v143,v143
v_mfma_f32_16x16x32_bf16 v[148:151],v[84:87],v[56:59],v[148:151]
ds_read_b64_tr_b16 v[108:109],v3 offset:57608
ds_read_b64_tr_b16 v[110:111],v3 offset:59144
v_lshlrev_b32_sdwa v144,1,v144 dst_sel:DWORD dst_unused:UNUSED_PAD src0_sel:DWORD src1_sel:WORD_1
v_lshlrev_b32_sdwa v146,1,v146 dst_sel:DWORD dst_unused:UNUSED_PAD src0_sel:DWORD src1_sel:WORD_1
v_mfma_f32_16x16x32_bf16 v[160:163],v[88:91],v[128:131],v[160:163]
s_waitcnt lgkmcnt(15)
ds_read_u16 v144,v144
v_lshlrev_b32_sdwa v145,1,v145 dst_sel:DWORD dst_unused:UNUSED_PAD src0_sel:DWORD src1_sel:WORD_1
v_mfma_f32_16x16x32_bf16 v[168:171],v[92:95],v[128:131],v[168:171]
s_waitcnt lgkmcnt(13)
ds_read_u16_d16_hi v146,v146
v_lshlrev_b32_sdwa v147,1,v147 dst_sel:DWORD dst_unused:UNUSED_PAD src0_sel:DWORD src1_sel:WORD_1
v_mfma_f32_16x16x32_bf16 v[176:179],v[96:99],v[128:131],v[176:179]
s_waitcnt lgkmcnt(10)
ds_read_b64_tr_b16 v[64:65],v8 offset:24640
ds_read_b64_tr_b16 v[66:67],v8 offset:26688
v_lshlrev_b32_sdwa v148,1,v148 dst_sel:DWORD dst_unused:UNUSED_PAD src0_sel:DWORD src1_sel:WORD_1
v_mfma_f32_16x16x32_bf16 v[184:187],v[100:103],v[128:131],v[184:187]
s_waitcnt lgkmcnt(8)
ds_read_u16 v145,v145
v_lshl_add_u64 v[136:137],v[136:137],0,v[138:139]
v_lshlrev_b32_sdwa v150,1,v150 dst_sel:DWORD dst_unused:UNUSED_PAD src0_sel:DWORD src1_sel:WORD_1
v_mfma_f32_16x16x32_bf16 v[192:195],v[104:107],v[128:131],v[192:195]
s_waitcnt lgkmcnt(5)
ds_read_u16_d16_hi v147,v147
v_lshl_add_u64 v[138:139],v[140:141],0,v[142:143]
v_mfma_f32_16x16x32_bf16 v[200:203],v[108:111],v[128:131],v[200:203]
ds_read_b128 v[112:115],v4 offset:256
ds_read_b64_tr_b16 v[68:69],v9 offset:24640
v_lshlrev_b32_sdwa v149,1,v149 dst_sel:DWORD dst_unused:UNUSED_PAD src0_sel:DWORD src1_sel:WORD_1
v_mfma_f32_16x16x32_bf16 v[164:167],v[88:91],v[136:139],v[164:167]
ds_read_u16 v148,v148
v_lshlrev_b32_sdwa v151,1,v151 dst_sel:DWORD dst_unused:UNUSED_PAD src0_sel:DWORD src1_sel:WORD_1
v_mfma_f32_16x16x32_bf16 v[172:175],v[92:95],v[136:139],v[172:175]
ds_read_u16_d16_hi v150,v150
ds_read_b64_tr_b16 v[70:71],v9 offset:26688
v_lshlrev_b32_sdwa v152,1,v152 dst_sel:DWORD dst_unused:UNUSED_PAD src0_sel:DWORD src1_sel:WORD_1
v_mfma_f32_16x16x32_bf16 v[180:183],v[96:99],v[136:139],v[180:183]
ds_read_b128 v[116:119],v4 offset:288
ds_read_b64_tr_b16 v[72:73],v8 offset:28736
v_lshlrev_b32_sdwa v154,1,v154 dst_sel:DWORD dst_unused:UNUSED_PAD src0_sel:DWORD src1_sel:WORD_1
v_mfma_f32_16x16x32_bf16 v[188:191],v[100:103],v[136:139],v[188:191]
ds_read_u16 v149,v149
v_lshlrev_b32_sdwa v153,1,v153 dst_sel:DWORD dst_unused:UNUSED_PAD src0_sel:DWORD src1_sel:WORD_1
v_mfma_f32_16x16x32_bf16 v[196:199],v[104:107],v[136:139],v[196:199]
ds_read_u16_d16_hi v151,v151
v_lshlrev_b32_sdwa v155,1,v155 dst_sel:DWORD dst_unused:UNUSED_PAD src0_sel:DWORD src1_sel:WORD_1
v_mfma_f32_16x16x32_bf16 v[204:207],v[108:111],v[136:139],v[204:207]
s_waitcnt lgkmcnt(8)
ds_read_b64_tr_b16 v[74:75],v8 offset:30784
ds_read_b64_tr_b16 v[76:77],v9 offset:28736
v_lshlrev_b32_sdwa v156,1,v156 dst_sel:DWORD dst_unused:UNUSED_PAD src0_sel:DWORD src1_sel:WORD_1
v_mfma_f32_16x16x32_bf16 v[128:131],v[64:67],v[16:19],v[112:115]
ds_read_u16 v152,v152
v_lshlrev_b32_sdwa v158,1,v158 dst_sel:DWORD dst_unused:UNUSED_PAD src0_sel:DWORD src1_sel:WORD_1
v_mfma_f32_16x16x32_bf16 v[136:139],v[64:67],v[20:23],v[112:115]
s_waitcnt lgkmcnt(6)
ds_read_u16_d16_hi v154,v154
ds_read_b64_tr_b16 v[78:79],v9 offset:30784
v_lshlrev_b32_sdwa v157,1,v157 dst_sel:DWORD dst_unused:UNUSED_PAD src0_sel:DWORD src1_sel:WORD_1
v_mfma_f32_16x16x32_bf16 v[140:143],v[68:71],v[20:23],v[116:119]
ds_read_b64_tr_b16 v[80:81],v8 offset:32832
ds_read_b64_tr_b16 v[82:83],v8 offset:34880
v_lshlrev_b32_sdwa v159,1,v159 dst_sel:DWORD dst_unused:UNUSED_PAD src0_sel:DWORD src1_sel:WORD_1
v_mfma_f32_16x16x32_bf16 v[132:135],v[68:71],v[16:19],v[116:119]
s_waitcnt lgkmcnt(6)
ds_read_u16 v153,v153
v_mfma_f32_16x16x32_bf16 v[128:131],v[72:75],v[24:27],v[128:131]
ds_read_u16_d16_hi v155,v155
v_mfma_f32_16x16x32_bf16 v[136:139],v[72:75],v[28:31],v[136:139]
s_waitcnt lgkmcnt(4)
ds_read_b64_tr_b16 v[84:85],v9 offset:32832
ds_read_b64_tr_b16 v[86:87],v9 offset:34880
v_mfma_f32_16x16x32_bf16 v[140:143],v[76:79],v[28:31],v[140:143]
ds_read_u16 v156,v156
v_mfma_f32_16x16x32_bf16 v[132:135],v[76:79],v[24:27],v[132:135]
s_waitcnt lgkmcnt(5)
ds_read_u16_d16_hi v158,v158
v_lshl_add_u64 v[144:145],v[144:145],0,v[146:147]
v_mfma_f32_16x16x32_bf16 v[128:131],v[80:83],v[32:35],v[128:131]
v_lshl_add_u64 v[146:147],v[148:149],0,v[150:151]
v_mfma_f32_16x16x32_bf16 v[136:139],v[80:83],v[36:39],v[136:139]
s_waitcnt lgkmcnt(2)
ds_read_u16 v157,v157
v_mfma_f32_16x16x32_bf16 v[140:143],v[84:87],v[36:39],v[140:143]
ds_read_u16_d16_hi v159,v159
v_mfma_f32_16x16x32_bf16 v[132:135],v[84:87],v[32:35],v[132:135]
s_nop 0
v_lshlrev_b32_sdwa v128,1,v128 dst_sel:DWORD dst_unused:UNUSED_PAD src0_sel:DWORD src1_sel:WORD_1
v_lshlrev_b32_sdwa v130,1,v130 dst_sel:DWORD dst_unused:UNUSED_PAD src0_sel:DWORD src1_sel:WORD_1
v_mfma_f32_16x16x32_bf16 v[208:211],v[88:91],v[144:147],v[208:211]
ds_read_u16 v128,v128
v_lshlrev_b32_sdwa v129,1,v129 dst_sel:DWORD dst_unused:UNUSED_PAD src0_sel:DWORD src1_sel:WORD_1
v_mfma_f32_16x16x32_bf16 v[216:219],v[92:95],v[144:147],v[216:219]
ds_read_u16_d16_hi v130,v130
v_lshlrev_b32_sdwa v131,1,v131 dst_sel:DWORD dst_unused:UNUSED_PAD src0_sel:DWORD src1_sel:WORD_1
v_mfma_f32_16x16x32_bf16 v[224:227],v[96:99],v[144:147],v[224:227]
v_lshlrev_b32_sdwa v132,1,v132 dst_sel:DWORD dst_unused:UNUSED_PAD src0_sel:DWORD src1_sel:WORD_1
v_mfma_f32_16x16x32_bf16 v[232:235],v[100:103],v[144:147],v[232:235]
ds_read_u16 v129,v129
v_lshl_add_u64 v[152:153],v[152:153],0,v[154:155]
v_lshlrev_b32_sdwa v134,1,v134 dst_sel:DWORD dst_unused:UNUSED_PAD src0_sel:DWORD src1_sel:WORD_1
v_mfma_f32_16x16x32_bf16 v[240:243],v[104:107],v[144:147],v[240:243]
s_waitcnt lgkmcnt(3)
ds_read_u16_d16_hi v131,v131
v_lshl_add_u64 v[154:155],v[156:157],0,v[158:159]
v_mfma_f32_16x16x32_bf16 v[248:251],v[108:111],v[144:147],v[248:251]
v_lshlrev_b32_sdwa v133,1,v133 dst_sel:DWORD dst_unused:UNUSED_PAD src0_sel:DWORD src1_sel:WORD_1
v_mfma_f32_16x16x32_bf16 v[212:215],v[88:91],v[152:155],v[212:215]
ds_read_u16 v132,v132
v_lshlrev_b32_sdwa v135,1,v135 dst_sel:DWORD dst_unused:UNUSED_PAD src0_sel:DWORD src1_sel:WORD_1
v_mfma_f32_16x16x32_bf16 v[220:223],v[92:95],v[152:155],v[220:223]
ds_read_u16_d16_hi v134,v134
v_lshlrev_b32_sdwa v136,1,v136 dst_sel:DWORD dst_unused:UNUSED_PAD src0_sel:DWORD src1_sel:WORD_1
v_mfma_f32_16x16x32_bf16 v[228:231],v[96:99],v[152:155],v[228:231]
ds_read_b64_tr_b16 v[88:89],v3 offset:63632
ds_read_b64_tr_b16 v[90:91],v3 offset:65168
v_lshlrev_b32_sdwa v138,1,v138 dst_sel:DWORD dst_unused:UNUSED_PAD src0_sel:DWORD src1_sel:WORD_1
v_mfma_f32_16x16x32_bf16 v[236:239],v[100:103],v[152:155],v[236:239]
ds_read_u16 v133,v133
v_lshlrev_b32_sdwa v137,1,v137 dst_sel:DWORD dst_unused:UNUSED_PAD src0_sel:DWORD src1_sel:WORD_1
v_mfma_f32_16x16x32_bf16 v[244:247],v[104:107],v[152:155],v[244:247]
ds_read_u16_d16_hi v135,v135
v_lshlrev_b32_sdwa v139,1,v139 dst_sel:DWORD dst_unused:UNUSED_PAD src0_sel:DWORD src1_sel:WORD_1
v_mfma_f32_16x16x32_bf16 v[252:255],v[108:111],v[152:155],v[252:255]
ds_read_b64_tr_b16 v[92:93],v3 offset:63640
ds_read_b64_tr_b16 v[94:95],v3 offset:65176
v_lshlrev_b32_sdwa v140,1,v140 dst_sel:DWORD dst_unused:UNUSED_PAD src0_sel:DWORD src1_sel:WORD_1
v_mfma_f32_16x16x32_bf16 v[144:147],v[64:67],v[40:43],v[112:115]
ds_read_u16 v136,v136
v_lshlrev_b32_sdwa v142,1,v142 dst_sel:DWORD dst_unused:UNUSED_PAD src0_sel:DWORD src1_sel:WORD_1
v_mfma_f32_16x16x32_bf16 v[152:155],v[64:67],v[44:47],v[112:115]
ds_read_u16_d16_hi v138,v138
v_lshlrev_b32_sdwa v141,1,v141 dst_sel:DWORD dst_unused:UNUSED_PAD src0_sel:DWORD src1_sel:WORD_1
v_mfma_f32_16x16x32_bf16 v[156:159],v[68:71],v[44:47],v[116:119]
ds_read_b64_tr_b16 v[96:97],v3 offset:63696
ds_read_b64_tr_b16 v[98:99],v3 offset:65232
v_lshlrev_b32_sdwa v143,1,v143 dst_sel:DWORD dst_unused:UNUSED_PAD src0_sel:DWORD src1_sel:WORD_1
v_mfma_f32_16x16x32_bf16 v[148:151],v[68:71],v[40:43],v[116:119]
ds_read_u16 v137,v137
v_mfma_f32_16x16x32_bf16 v[144:147],v[72:75],v[48:51],v[144:147]
ds_read_u16_d16_hi v139,v139
v_mfma_f32_16x16x32_bf16 v[152:155],v[72:75],v[52:55],v[152:155]
ds_read_b64_tr_b16 v[100:101],v3 offset:63704
ds_read_b64_tr_b16 v[102:103],v3 offset:65240
v_mfma_f32_16x16x32_bf16 v[156:159],v[76:79],v[52:55],v[156:159]
ds_read_u16 v140,v140
v_mfma_f32_16x16x32_bf16 v[148:151],v[76:79],v[48:51],v[148:151]
s_waitcnt lgkmcnt(15)
ds_read_u16_d16_hi v142,v142
v_lshl_add_u64 v[128:129],v[128:129],0,v[130:131]
v_mfma_f32_16x16x32_bf16 v[144:147],v[80:83],v[56:59],v[144:147]
s_waitcnt lgkmcnt(12)
ds_read_b64_tr_b16 v[104:105],v3 offset:63760
ds_read_b64_tr_b16 v[106:107],v3 offset:65296
v_lshl_add_u64 v[130:131],v[132:133],0,v[134:135]
v_mfma_f32_16x16x32_bf16 v[152:155],v[80:83],v[60:63],v[152:155]
ds_read_u16 v141,v141
v_mfma_f32_16x16x32_bf16 v[156:159],v[84:87],v[60:63],v[156:159]
ds_read_u16_d16_hi v143,v143
v_mfma_f32_16x16x32_bf16 v[148:151],v[84:87],v[56:59],v[148:151]
ds_read_b64_tr_b16 v[108:109],v3 offset:63768
ds_read_b64_tr_b16 v[110:111],v3 offset:65304
v_lshlrev_b32_sdwa v144,1,v144 dst_sel:DWORD dst_unused:UNUSED_PAD src0_sel:DWORD src1_sel:WORD_1
v_lshlrev_b32_sdwa v146,1,v146 dst_sel:DWORD dst_unused:UNUSED_PAD src0_sel:DWORD src1_sel:WORD_1
v_mfma_f32_16x16x32_bf16 v[160:163],v[88:91],v[128:131],v[160:163]
s_waitcnt lgkmcnt(15)
ds_read_u16 v144,v144
ds_read_b128 v[64:67],v4 offset:1536
v_lshlrev_b32_sdwa v145,1,v145 dst_sel:DWORD dst_unused:UNUSED_PAD src0_sel:DWORD src1_sel:WORD_1
v_lshlrev_b32_sdwa v147,1,v147 dst_sel:DWORD dst_unused:UNUSED_PAD src0_sel:DWORD src1_sel:WORD_1
v_mfma_f32_16x16x32_bf16 v[168:171],v[92:95],v[128:131],v[168:171]
s_waitcnt lgkmcnt(14)
ds_read_u16_d16_hi v146,v146
ds_read_b128 v[68:71],v4 offset:1568
v_lshlrev_b32_sdwa v148,1,v148 dst_sel:DWORD dst_unused:UNUSED_PAD src0_sel:DWORD src1_sel:WORD_1
s_waitcnt lgkmcnt(14)
v_lshl_add_u64 v[136:137],v[136:137],0,v[138:139]
v_mfma_f32_16x16x32_bf16 v[176:179],v[96:99],v[128:131],v[176:179]
s_waitcnt lgkmcnt(12)
ds_read_u16 v145,v145
ds_read_b128 v[72:75],v4 offset:1664
v_lshlrev_b32_sdwa v150,1,v150 dst_sel:DWORD dst_unused:UNUSED_PAD src0_sel:DWORD src1_sel:WORD_1
v_mfma_f32_16x16x32_bf16 v[184:187],v[100:103],v[128:131],v[184:187]
s_waitcnt lgkmcnt(10)
ds_read_u16_d16_hi v147,v147
ds_read_b128 v[76:79],v4 offset:1696
s_waitcnt lgkmcnt(10)
v_lshl_add_u64 v[138:139],v[140:141],0,v[142:143]
v_lshlrev_b32_sdwa v149,1,v149 dst_sel:DWORD dst_unused:UNUSED_PAD src0_sel:DWORD src1_sel:WORD_1
v_mfma_f32_16x16x32_bf16 v[192:195],v[104:107],v[128:131],v[192:195]
s_waitcnt lgkmcnt(8)
ds_read_u16 v148,v148
ds_read_b128 v[80:83],v4 offset:1792
v_lshlrev_b32_sdwa v151,1,v151 dst_sel:DWORD dst_unused:UNUSED_PAD src0_sel:DWORD src1_sel:WORD_1
v_lshlrev_b32_sdwa v152,1,v152 dst_sel:DWORD dst_unused:UNUSED_PAD src0_sel:DWORD src1_sel:WORD_1
v_mfma_f32_16x16x32_bf16 v[200:203],v[108:111],v[128:131],v[200:203]
ds_read_u16_d16_hi v150,v150
ds_read_b128 v[84:87],v4 offset:1824
v_lshlrev_b32_sdwa v154,1,v154 dst_sel:DWORD dst_unused:UNUSED_PAD src0_sel:DWORD src1_sel:WORD_1
v_mfma_f32_16x16x32_bf16 v[164:167],v[88:91],v[136:139],v[164:167]
ds_read_u16 v149,v149
v_lshlrev_b32_sdwa v153,1,v153 dst_sel:DWORD dst_unused:UNUSED_PAD src0_sel:DWORD src1_sel:WORD_1
v_lshlrev_b32_sdwa v155,1,v155 dst_sel:DWORD dst_unused:UNUSED_PAD src0_sel:DWORD src1_sel:WORD_1
v_mfma_f32_16x16x32_bf16 v[172:175],v[92:95],v[136:139],v[172:175]
ds_read_u16_d16_hi v151,v151
v_lshlrev_b32_sdwa v156,1,v156 dst_sel:DWORD dst_unused:UNUSED_PAD src0_sel:DWORD src1_sel:WORD_1
v_lshlrev_b32_sdwa v158,1,v158 dst_sel:DWORD dst_unused:UNUSED_PAD src0_sel:DWORD src1_sel:WORD_1
v_mfma_f32_16x16x32_bf16 v[180:183],v[96:99],v[136:139],v[180:183]
ds_read_u16 v152,v152
v_lshlrev_b32_sdwa v157,1,v157 dst_sel:DWORD dst_unused:UNUSED_PAD src0_sel:DWORD src1_sel:WORD_1
v_mfma_f32_16x16x32_bf16 v[188:191],v[100:103],v[136:139],v[188:191]
ds_read_u16_d16_hi v154,v154
v_lshlrev_b32_sdwa v159,1,v159 dst_sel:DWORD dst_unused:UNUSED_PAD src0_sel:DWORD src1_sel:WORD_1
s_waitcnt lgkmcnt(9)
v_lshl_add_u64 v[144:145],v[144:145],0,v[146:147]
v_mfma_f32_16x16x32_bf16 v[196:199],v[104:107],v[136:139],v[196:199]
ds_read_u16 v153,v153
s_waitcnt lgkmcnt(3)
v_lshl_add_u64 v[146:147],v[148:149],0,v[150:151]
v_mfma_f32_16x16x32_bf16 v[204:207],v[108:111],v[136:139],v[204:207]
ds_read_u16_d16_hi v155,v155
s_waitcnt lgkmcnt(0)
v_lshl_add_u64 v[152:153],v[152:153],0,v[154:155]
ds_read_u16 v156,v156
ds_read_u16_d16_hi v158,v158
ds_read_u16 v157,v157
ds_read_u16_d16_hi v159,v159
s_waitcnt lgkmcnt(0)
v_lshl_add_u64 v[154:155],v[156:157],0,v[158:159]
v_mfma_f32_16x16x32_bf16 v[208:211],v[88:91],v[144:147],v[208:211]
v_xor_b32 v15,64,v10
v_mul_f32 v160,v160,v64
v_mul_f32 v161,v161,v65
v_mul_f32 v162,v162,v66
v_mul_f32 v163,v163,v67
v_mfma_f32_16x16x32_bf16 v[216:219],v[92:95],v[144:147],v[216:219]
v_mul_f32 v168,v168,v68
v_mul_f32 v169,v169,v69
v_mul_f32 v170,v170,v70
v_mul_f32 v171,v171,v71
v_cvt_pk_bf16_f32 v16,v160,v161
v_mfma_f32_16x16x32_bf16 v[224:227],v[96:99],v[144:147],v[224:227]
v_cvt_pk_bf16_f32 v17,v162,v163
v_cvt_pk_bf16_f32 v18,v168,v169
v_cvt_pk_bf16_f32 v19,v170,v171
v_pk_maximum3_f16 v7,v7,v16,v17
v_mfma_f32_16x16x32_bf16 v[232:235],v[100:103],v[144:147],v[232:235]
v_pk_maximum3_f16 v7,v7,v18,v19
v_mul_f32_dpp v176,v176,v72 quad_perm:[1,0,3,2] row_mask:0xf bank_mask:0xf
v_mul_f32_dpp v177,v177,v73 quad_perm:[1,0,3,2] row_mask:0xf bank_mask:0xf
v_mul_f32_dpp v178,v178,v74 quad_perm:[1,0,3,2] row_mask:0xf bank_mask:0xf
v_mul_f32_dpp v179,v179,v75 quad_perm:[1,0,3,2] row_mask:0xf bank_mask:0xf
v_mfma_f32_16x16x32_bf16 v[240:243],v[104:107],v[144:147],v[240:243]
v_mul_f32_dpp v184,v184,v76 quad_perm:[1,0,3,2] row_mask:0xf bank_mask:0xf
v_mul_f32_dpp v185,v185,v77 quad_perm:[1,0,3,2] row_mask:0xf bank_mask:0xf
v_mul_f32_dpp v186,v186,v78 quad_perm:[1,0,3,2] row_mask:0xf bank_mask:0xf
v_mul_f32_dpp v187,v187,v79 quad_perm:[1,0,3,2] row_mask:0xf bank_mask:0xf
v_mfma_f32_16x16x32_bf16 v[248:251],v[108:111],v[144:147],v[248:251]
v_cvt_pk_bf16_f32 v24,v176,v177
v_cvt_pk_bf16_f32 v25,v178,v179
v_cvt_pk_bf16_f32 v26,v184,v185
v_cvt_pk_bf16_f32 v27,v186,v187
v_pk_maximum3_f16 v7,v7,v24,v25
v_mfma_f32_16x16x32_bf16 v[212:215],v[88:91],v[152:155],v[212:215]
v_pk_maximum3_f16 v7,v7,v26,v27
s_mov_b64 exec,s[12:13]
v_swap_b32 v16,v24
v_swap_b32 v17,v25
v_swap_b32 v18,v26
v_swap_b32 v19,v27
s_mov_b64 exec,-1
s_add_u32 s65,s61,4718592
buffer_store_dwordx4 v[16:19],v10,s[40:43],s65 offen offset:0 sc1
s_add_u32 s65,s61,4718592
buffer_store_dwordx4 v[24:27],v15,s[40:43],s65 offen offset:384 sc1
v_mfma_f32_16x16x32_bf16 v[220:223],v[92:95],v[152:155],v[220:223]
v_mfma_f32_16x16x32_bf16 v[228:231],v[96:99],v[152:155],v[228:231]
v_mul_f32 v192,v192,v80
v_mul_f32 v193,v193,v81
v_mul_f32 v194,v194,v82
v_mul_f32 v195,v195,v83
v_mfma_f32_16x16x32_bf16 v[236:239],v[100:103],v[152:155],v[236:239]
v_mul_f32 v200,v200,v84
v_mul_f32 v201,v201,v85
v_mul_f32 v202,v202,v86
v_mul_f32 v203,v203,v87
v_cvt_pk_bf16_f32 v32,v192,v193
v_mfma_f32_16x16x32_bf16 v[244:247],v[104:107],v[152:155],v[244:247]
v_cvt_pk_bf16_f32 v33,v194,v195
v_cvt_pk_bf16_f32 v34,v200,v201
v_cvt_pk_bf16_f32 v35,v202,v203
v_pk_maximum3_f16 v7,v7,v32,v33
v_mfma_f32_16x16x32_bf16 v[252:255],v[108:111],v[152:155],v[252:255]
v_pk_maximum3_f16 v7,v7,v34,v35
v_mul_f32_dpp v164,v164,v64 quad_perm:[1,0,3,2] row_mask:0xf bank_mask:0xf
v_mul_f32_dpp v165,v165,v65 quad_perm:[1,0,3,2] row_mask:0xf bank_mask:0xf
v_mul_f32_dpp v166,v166,v66 quad_perm:[1,0,3,2] row_mask:0xf bank_mask:0xf
v_mul_f32_dpp v167,v167,v67 quad_perm:[1,0,3,2] row_mask:0xf bank_mask:0xf
v_mul_f32_dpp v172,v172,v68 quad_perm:[1,0,3,2] row_mask:0xf bank_mask:0xf
v_mul_f32_dpp v173,v173,v69 quad_perm:[1,0,3,2] row_mask:0xf bank_mask:0xf
v_mul_f32_dpp v174,v174,v70 quad_perm:[1,0,3,2] row_mask:0xf bank_mask:0xf
v_mul_f32_dpp v175,v175,v71 quad_perm:[1,0,3,2] row_mask:0xf bank_mask:0xf
v_cvt_pk_bf16_f32 v20,v164,v165
v_cvt_pk_bf16_f32 v21,v166,v167
v_cvt_pk_bf16_f32 v22,v172,v173
v_cvt_pk_bf16_f32 v23,v174,v175
v_pk_maximum3_f16 v7,v7,v20,v21
v_pk_maximum3_f16 v7,v7,v22,v23
s_mov_b64 exec,s[12:13]
v_swap_b32 v32,v20
v_swap_b32 v33,v21
v_swap_b32 v34,v22
v_swap_b32 v35,v23
s_mov_b64 exec,-1
s_add_u32 s65,s61,4718592
buffer_store_dwordx4 v[32:35],v10,s[40:43],s65 offen offset:128 sc1
s_add_u32 s65,s61,4718592
buffer_store_dwordx4 v[20:23],v15,s[40:43],s65 offen offset:512 sc1
v_mul_f32 v180,v180,v72
v_mul_f32 v181,v181,v73
v_mul_f32 v182,v182,v74
v_mul_f32 v183,v183,v75
v_mul_f32 v188,v188,v76
v_mul_f32 v189,v189,v77
v_mul_f32 v190,v190,v78
v_mul_f32 v191,v191,v79
v_cvt_pk_bf16_f32 v28,v180,v181
v_cvt_pk_bf16_f32 v29,v182,v183
v_cvt_pk_bf16_f32 v30,v188,v189
v_cvt_pk_bf16_f32 v31,v190,v191
v_pk_maximum3_f16 v7,v7,v28,v29
v_pk_maximum3_f16 v7,v7,v30,v31
v_mul_f32_dpp v196,v196,v80 quad_perm:[1,0,3,2] row_mask:0xf bank_mask:0xf
v_mul_f32_dpp v197,v197,v81 quad_perm:[1,0,3,2] row_mask:0xf bank_mask:0xf
v_mul_f32_dpp v198,v198,v82 quad_perm:[1,0,3,2] row_mask:0xf bank_mask:0xf
v_mul_f32_dpp v199,v199,v83 quad_perm:[1,0,3,2] row_mask:0xf bank_mask:0xf
v_mul_f32_dpp v204,v204,v84 quad_perm:[1,0,3,2] row_mask:0xf bank_mask:0xf
v_mul_f32_dpp v205,v205,v85 quad_perm:[1,0,3,2] row_mask:0xf bank_mask:0xf
v_mul_f32_dpp v206,v206,v86 quad_perm:[1,0,3,2] row_mask:0xf bank_mask:0xf
v_mul_f32_dpp v207,v207,v87 quad_perm:[1,0,3,2] row_mask:0xf bank_mask:0xf
v_cvt_pk_bf16_f32 v36,v196,v197
v_cvt_pk_bf16_f32 v37,v198,v199
v_cvt_pk_bf16_f32 v38,v204,v205
v_cvt_pk_bf16_f32 v39,v206,v207
v_pk_maximum3_f16 v7,v7,v36,v37
v_pk_maximum3_f16 v7,v7,v38,v39
s_mov_b64 exec,s[12:13]
v_swap_b32 v28,v36
v_swap_b32 v29,v37
v_swap_b32 v30,v38
v_swap_b32 v31,v39
s_mov_b64 exec,-1
s_add_u32 s65,s61,4718592
buffer_store_dwordx4 v[28:31],v10,s[40:43],s65 offen offset:256 sc1
s_add_u32 s65,s61,4718592
buffer_store_dwordx4 v[36:39],v15,s[40:43],s65 offen offset:640 sc1
v_mul_f32 v208,v208,v64
v_mul_f32 v209,v209,v65
v_mul_f32 v210,v210,v66
v_mul_f32 v211,v211,v67
v_mul_f32 v216,v216,v68
v_mul_f32 v217,v217,v69
v_mul_f32 v218,v218,v70
v_mul_f32 v219,v219,v71
v_cvt_pk_bf16_f32 v40,v208,v209
v_cvt_pk_bf16_f32 v41,v210,v211
v_cvt_pk_bf16_f32 v42,v216,v217
v_cvt_pk_bf16_f32 v43,v218,v219
v_pk_maximum3_f16 v7,v7,v40,v41
v_pk_maximum3_f16 v7,v7,v42,v43
v_mul_f32_dpp v224,v224,v72 quad_perm:[1,0,3,2] row_mask:0xf bank_mask:0xf
v_mul_f32_dpp v225,v225,v73 quad_perm:[1,0,3,2] row_mask:0xf bank_mask:0xf
v_mul_f32_dpp v226,v226,v74 quad_perm:[1,0,3,2] row_mask:0xf bank_mask:0xf
v_mul_f32_dpp v227,v227,v75 quad_perm:[1,0,3,2] row_mask:0xf bank_mask:0xf
v_mul_f32_dpp v232,v232,v76 quad_perm:[1,0,3,2] row_mask:0xf bank_mask:0xf
v_mul_f32_dpp v233,v233,v77 quad_perm:[1,0,3,2] row_mask:0xf bank_mask:0xf
v_mul_f32_dpp v234,v234,v78 quad_perm:[1,0,3,2] row_mask:0xf bank_mask:0xf
v_mul_f32_dpp v235,v235,v79 quad_perm:[1,0,3,2] row_mask:0xf bank_mask:0xf
v_cvt_pk_bf16_f32 v48,v224,v225
v_cvt_pk_bf16_f32 v49,v226,v227
v_cvt_pk_bf16_f32 v50,v232,v233
v_cvt_pk_bf16_f32 v51,v234,v235
v_pk_maximum3_f16 v7,v7,v48,v49
v_pk_maximum3_f16 v7,v7,v50,v51
s_mov_b64 exec,s[12:13]
v_swap_b32 v40,v48
v_swap_b32 v41,v49
v_swap_b32 v42,v50
v_swap_b32 v43,v51
s_mov_b64 exec,-1
s_add_u32 s65,s61,4816896
buffer_store_dwordx4 v[40:43],v10,s[40:43],s65 offen offset:0 sc1
s_add_u32 s65,s61,4816896
buffer_store_dwordx4 v[48:51],v15,s[40:43],s65 offen offset:384 sc1
v_mul_f32 v240,v240,v80
v_mul_f32 v241,v241,v81
v_mul_f32 v242,v242,v82
v_mul_f32 v243,v243,v83
v_mul_f32 v248,v248,v84
v_mul_f32 v249,v249,v85
v_mul_f32 v250,v250,v86
v_mul_f32 v251,v251,v87
v_cvt_pk_bf16_f32 v56,v240,v241
v_cvt_pk_bf16_f32 v57,v242,v243
v_cvt_pk_bf16_f32 v58,v248,v249
v_cvt_pk_bf16_f32 v59,v250,v251
v_pk_maximum3_f16 v7,v7,v56,v57
v_pk_maximum3_f16 v7,v7,v58,v59
v_mul_f32_dpp v212,v212,v64 quad_perm:[1,0,3,2] row_mask:0xf bank_mask:0xf
v_mul_f32_dpp v213,v213,v65 quad_perm:[1,0,3,2] row_mask:0xf bank_mask:0xf
v_mul_f32_dpp v214,v214,v66 quad_perm:[1,0,3,2] row_mask:0xf bank_mask:0xf
v_mul_f32_dpp v215,v215,v67 quad_perm:[1,0,3,2] row_mask:0xf bank_mask:0xf
v_mul_f32_dpp v220,v220,v68 quad_perm:[1,0,3,2] row_mask:0xf bank_mask:0xf
v_mul_f32_dpp v221,v221,v69 quad_perm:[1,0,3,2] row_mask:0xf bank_mask:0xf
v_mul_f32_dpp v222,v222,v70 quad_perm:[1,0,3,2] row_mask:0xf bank_mask:0xf
v_mul_f32_dpp v223,v223,v71 quad_perm:[1,0,3,2] row_mask:0xf bank_mask:0xf
v_cvt_pk_bf16_f32 v44,v212,v213
v_cvt_pk_bf16_f32 v45,v214,v215
v_cvt_pk_bf16_f32 v46,v220,v221
v_cvt_pk_bf16_f32 v47,v222,v223
v_pk_maximum3_f16 v7,v7,v44,v45
v_pk_maximum3_f16 v7,v7,v46,v47
s_mov_b64 exec,s[12:13]
v_swap_b32 v56,v44
v_swap_b32 v57,v45
v_swap_b32 v58,v46
v_swap_b32 v59,v47
s_mov_b64 exec,-1
s_add_u32 s65,s61,4816896
buffer_store_dwordx4 v[56:59],v10,s[40:43],s65 offen offset:128 sc1
s_add_u32 s65,s61,4816896
buffer_store_dwordx4 v[44:47],v15,s[40:43],s65 offen offset:512 sc1
v_mul_f32 v228,v228,v72
v_mul_f32 v229,v229,v73
v_mul_f32 v230,v230,v74
v_mul_f32 v231,v231,v75
v_mul_f32 v236,v236,v76
v_mul_f32 v237,v237,v77
v_mul_f32 v238,v238,v78
v_mul_f32 v239,v239,v79
v_cvt_pk_bf16_f32 v52,v228,v229
v_cvt_pk_bf16_f32 v53,v230,v231
v_cvt_pk_bf16_f32 v54,v236,v237
v_cvt_pk_bf16_f32 v55,v238,v239
v_pk_maximum3_f16 v7,v7,v52,v53
v_pk_maximum3_f16 v7,v7,v54,v55
v_mul_f32_dpp v244,v244,v80 quad_perm:[1,0,3,2] row_mask:0xf bank_mask:0xf
v_mul_f32_dpp v245,v245,v81 quad_perm:[1,0,3,2] row_mask:0xf bank_mask:0xf
v_mul_f32_dpp v246,v246,v82 quad_perm:[1,0,3,2] row_mask:0xf bank_mask:0xf
v_mul_f32_dpp v247,v247,v83 quad_perm:[1,0,3,2] row_mask:0xf bank_mask:0xf
v_mul_f32_dpp v252,v252,v84 quad_perm:[1,0,3,2] row_mask:0xf bank_mask:0xf
v_mul_f32_dpp v253,v253,v85 quad_perm:[1,0,3,2] row_mask:0xf bank_mask:0xf
v_mul_f32_dpp v254,v254,v86 quad_perm:[1,0,3,2] row_mask:0xf bank_mask:0xf
v_mul_f32_dpp v255,v255,v87 quad_perm:[1,0,3,2] row_mask:0xf bank_mask:0xf
v_cvt_pk_bf16_f32 v60,v244,v245
v_cvt_pk_bf16_f32 v61,v246,v247
v_cvt_pk_bf16_f32 v62,v252,v253
v_cvt_pk_bf16_f32 v63,v254,v255
v_pk_maximum3_f16 v7,v7,v60,v61
v_pk_maximum3_f16 v7,v7,v62,v63
s_mov_b64 exec,s[12:13]
v_swap_b32 v52,v60
v_swap_b32 v53,v61
v_swap_b32 v54,v62
v_swap_b32 v55,v63
s_mov_b64 exec,-1
s_add_u32 s65,s61,4816896
buffer_store_dwordx4 v[52:55],v10,s[40:43],s65 offen offset:256 sc1
s_add_u32 s65,s61,4816896
buffer_store_dwordx4 v[60:63],v15,s[40:43],s65 offen offset:640 sc1
v_and_b32 v1,0x7c007c00,v7
v_lshrrev_b32 v2,16,v1
v_and_b32 v1,0xffff,v1
v_max_u32 v1,v1,v2
v_cmp_eq_u32 vcc,0x7c00,v1
s_or_b32 s64,vcc_lo,vcc_hi
v_mov_b32 v1,s64
v_mov_b32 v2,8192
s_mov_b64 exec,1
ds_or_b32 v2,v1 offset:3456
s_mov_b64 exec,-1
s_waitcnt lgkmcnt(0)
s_barrier
ds_read_b32 v1,v2 offset:3456
s_waitcnt lgkmcnt(0)
v_readfirstlane_b32 s64,v1
s_cmp_lg_u32 s64,0
s_cbranch_scc1 N_fallback_%=
s_endpgm
N_mainend_%=:
N_fallback_%=:
s_setprio 0
s_waitcnt vmcnt(0) lgkmcnt(0)
s_barrier
v_lshrrev_b32 v237,6,%1
v_and_b32 v232,63,%1
v_readfirstlane_b32 s52,v237
v_lshlrev_b32 v248,4,v232
v_min_u32 v244,383,%1
v_lshlrev_b32 v238,1,v244
global_load_ushort v244,v238,s[26:27]
v_min_u32 v245,95,%1
v_lshlrev_b32 v239,1,v245
global_load_ushort v245,v239,s[30:31]
global_load_ushort v246,v239,s[32:33]
s_mov_b64 s[92:93],s[22:23]
s_and_b32 s93,s93,0xffff
s_mov_b32 s94,1728
s_mov_b32 s95,0x00020000
s_lshl_b32 s71,s52,11
s_add_u32 s71,s71,62208
s_mov_b32 m0,s71
s_movk_i32 s64,1024
buffer_load_dwordx4 v248,s[92:95],0 offen lds
s_add_u32 m0,s71,1024
s_nop 0
buffer_load_dwordx4 v248,s[92:95],s64 offen lds
v_mov_b32 v250,s71
s_mov_b64 s[36:37],s[20:21]
s_mov_b64 s[40:41],s[34:35]
s_mov_b64 s[44:45],s[24:25]
s_mov_b64 s[48:49],s[28:29]
s_sub_u32 s36,s36,192
s_subb_u32 s37,s37,0
s_and_b32 s37,s37,0xffff
s_mov_b32 s38,50331840
s_mov_b32 s39,0x00020000
s_and_b32 s41,s41,0xffff
s_mov_b32 s42,50331648
s_mov_b32 s43,0x00020000
s_and_b32 s45,s45,0xffff
s_mov_b32 s46,73728
s_mov_b32 s47,0x00020000
s_and_b32 s49,s49,0xffff
s_mov_b32 s50,73728
s_mov_b32 s51,0x00020000
s_mov_b32 s60,-1
s_mov_b32 s61,0xffff
s_mov_b32 s62,-1
s_mov_b32 s63,0x3fffff
s_and_b32 s64,%0,7
s_lshr_b32 s70,%0,3
s_lshl_b32 s56,s64,6
s_mul_i32 s57,s70,3072
s_and_b32 s66,s52,3
s_mul_i32 s68,s66,864
s_mul_i32 s67,s66,54
s_mul_i32 s58,s66,1536
v_add_u32 v237,s67,v232
v_mul_u32_u24 v238,0xaaab,v237
v_lshrrev_b32 v238,19,v238
v_mul_u32_u24 v239,12,v238
v_sub_u32 v239,v237,v239
v_bfe_u32 v237,v238,2,2
v_xor_b32 v239,v239,v237
v_lshlrev_b32 v239,4,v239
v_mul_u32_u24 v237,192,v238
v_add_u32 v247,v237,v239
s_cmp_eq_u32 s70,0
s_cselect_b32 s71,0,99
s_cmp_eq_u32 s70,31
s_cselect_b32 s71,17,s71
v_cmp_eq_u32 vcc,s71,v238
v_mov_b32 v237,0x7fff0000
s_sub_u32 s67,s56,1
s_mul_i32 s59,s67,98304
s_add_u32 s59,s59,s57
v_cndmask_b32 v247,v247,v237,vcc
s_lshr_b32 s66,s52,2
s_mul_i32 s64,s66,3456
s_add_u32 s64,s64,s68
s_mul_i32 s65,s66,98304
s_add_u32 s65,s65,s59
s_add_u32 s67,s67,s66
s_cmp_lt_i32 s67,0
s_cselect_b32 s71,0x70000000,s65
s_mov_b64 exec,s[62:63]
s_add_u32 m0,s64,0
s_nop 0
buffer_load_dwordx4 v247,s[36:39],s71 offen lds
s_add_u32 m0,s64,6912
s_add_u32 s71,s65,196608
buffer_load_dwordx4 v247,s[36:39],s71 offen lds
s_add_u32 m0,s64,13824
s_add_u32 s71,s65,393216
buffer_load_dwordx4 v247,s[36:39],s71 offen lds
s_add_u32 m0,s64,20736
s_add_u32 s71,s65,589824
buffer_load_dwordx4 v247,s[36:39],s71 offen lds
s_add_u32 m0,s64,27648
s_add_u32 s71,s65,786432
buffer_load_dwordx4 v247,s[36:39],s71 offen lds
s_add_u32 m0,s64,34560
s_add_u32 s71,s65,983040
buffer_load_dwordx4 v247,s[36:39],s71 offen lds
s_add_u32 m0,s64,41472
s_add_u32 s71,s65,1179648
buffer_load_dwordx4 v247,s[36:39],s71 offen lds
s_add_u32 m0,s64,48384
s_add_u32 s71,s65,1376256
buffer_load_dwordx4 v247,s[36:39],s71 offen lds
s_add_u32 m0,s64,55296
s_add_u32 s71,s65,1572864
buffer_load_dwordx4 v247,s[36:39],s71 offen lds
s_mov_b64 exec,-1
v_lshrrev_b32 v237,2,v232
v_mul_u32_u24 v237,768,v237
v_and_b32 v238,3,v232
v_lshl_add_u32 v237,v238,4,v237
s_and_b32 s66,s52,3
s_mul_i32 s71,s66,18432
v_add_u32 v27,s71,v237
v_add_u32 v28,s58,v248
s_mov_b32 s54,62208
s_mov_b32 s53,0
s_mov_b32 s96,0x70000000
s_add_u32 s68,s68,s54
s_add_u32 s59,s59,1572864
s_lshl_b32 s66,s52,1
s_add_u32 s66,s66,s56
s_mul_i32 s69,s66,98304
s_add_u32 s69,s69,s57
s_cmp_ge_u32 s52,4
s_cbranch_scc1 L_noW_%=
s_mov_b64 exec,s[60:61]
s_add_u32 m0,s58,124416
s_movk_i32 s64,0
buffer_load_dwordx4 v27,s[44:47],s64 offen lds
s_add_u32 m0,s58,125184
s_movk_i32 s65,9216
buffer_load_dwordx4 v27,s[44:47],s65 offen lds
s_mov_b64 exec,-1
s_mov_b64 exec,s[60:61]
s_add_u32 m0,s58,142848
s_mov_b32 s64,0
buffer_load_dwordx4 v28,s[48:51],s64 offen lds
s_add_u32 m0,s58,143616
s_mov_b32 s65,768
buffer_load_dwordx4 v28,s[48:51],s65 offen lds
s_mov_b64 exec,-1
s_mov_b64 exec,s[60:61]
s_add_u32 m0,s58,130560
s_movk_i32 s64,64
buffer_load_dwordx4 v27,s[44:47],s64 offen lds
s_add_u32 m0,s58,131328
s_movk_i32 s65,9280
buffer_load_dwordx4 v27,s[44:47],s65 offen lds
s_mov_b64 exec,-1
s_mov_b64 exec,s[60:61]
s_add_u32 m0,s58,136704
s_movk_i32 s64,128
buffer_load_dwordx4 v27,s[44:47],s64 offen lds
s_add_u32 m0,s58,137472
s_movk_i32 s65,9344
buffer_load_dwordx4 v27,s[44:47],s65 offen lds
s_mov_b64 exec,-1
s_branch L_W_%=
L_noW_%=:
v_mov_b32 v27,v247
s_mul_i32 s64,s70,2304
s_and_b32 s65,s52,3
s_mul_i32 s65,s65,576
s_add_u32 s64,s64,s65
v_min_u32 v237,560,v248
buffer_load_dwordx4 v[252:255],v237,s[44:47],s64 offen
buffer_load_dwordx4 v[252:255],v237,s[48:51],s64 offen
L_W_%=:
v_and_b32 v233,15,v232
v_lshrrev_b32 v234,4,v232
v_lshrrev_b32 v235,5,v232
v_and_b32 v237,1,v234
v_lshlrev_b32 v237,1,v237
v_or_b32 v236,v237,v235
v_lshrrev_b32 v237,3,v233
v_lshlrev_b32 v237,2,v237
v_and_b32 v238,3,v233
v_or_b32 v241,v237,v238
v_lshrrev_b32 v237,2,v233
v_and_b32 v242,1,v237
v_sub_u32 v237,v236,v242
v_cmp_gt_u32 vcc,3,v237
v_mov_b32 v238,0
v_mov_b32 v239,-1
v_cndmask_b32 v237,v238,v237,vcc
v_cndmask_b32 v249,v238,v239,vcc
v_mul_u32_u24 v237,576,v237
v_lshl_add_u32 v237,v241,1,v237
v_add_u32 v250,v250,v237
v_lshrrev_b32 v237,1,v241
v_cmp_eq_u32 vcc,0,v237
s_nop 1
v_cndmask_b32 v32,v238,v249,vcc
v_cmp_eq_u32 vcc,1,v237
s_nop 1
v_cndmask_b32 v33,v238,v249,vcc
v_cmp_eq_u32 vcc,2,v237
s_nop 1
v_cndmask_b32 v34,v238,v249,vcc
v_cmp_eq_u32 vcc,3,v237
s_nop 1
v_cndmask_b32 v35,v238,v249,vcc
v_and_b32 v243,1,v241
v_lshlrev_b32 v243,4,v243
s_mul_i32 s71,s52,6912
v_add_u32 v237,0,v233
v_bfe_u32 v238,v237,2,2
v_mul_u32_u24 v239,18,v236
v_add_u32 v239,v239,v237
v_mul_u32_u24 v239,192,v239
v_add_u32 v239,s71,v239
v_xor_b32 v240,0,v238
v_lshl_add_u32 v4,v240,4,v239
v_xor_b32 v240,1,v238
v_lshl_add_u32 v5,v240,4,v239
v_xor_b32 v240,2,v238
v_lshl_add_u32 v6,v240,4,v239
v_xor_b32 v240,3,v238
v_lshl_add_u32 v7,v240,4,v239
v_add_u32 v237,1,v233
v_bfe_u32 v238,v237,2,2
v_mul_u32_u24 v239,18,v236
v_add_u32 v239,v239,v237
v_mul_u32_u24 v239,192,v239
v_add_u32 v239,s71,v239
v_xor_b32 v240,0,v238
v_lshl_add_u32 v8,v240,4,v239
v_xor_b32 v240,1,v238
v_lshl_add_u32 v9,v240,4,v239
v_xor_b32 v240,2,v238
v_lshl_add_u32 v10,v240,4,v239
v_xor_b32 v240,3,v238
v_lshl_add_u32 v11,v240,4,v239
v_add_u32 v237,2,v233
v_bfe_u32 v238,v237,2,2
v_mul_u32_u24 v239,18,v236
v_add_u32 v239,v239,v237
v_mul_u32_u24 v239,192,v239
v_add_u32 v239,s71,v239
v_xor_b32 v240,0,v238
v_lshl_add_u32 v12,v240,4,v239
v_xor_b32 v240,1,v238
v_lshl_add_u32 v13,v240,4,v239
v_xor_b32 v240,2,v238
v_lshl_add_u32 v14,v240,4,v239
v_xor_b32 v240,3,v238
v_lshl_add_u32 v15,v240,4,v239
v_lshrrev_b32 v237,2,v233
v_lshl_add_u32 v237,v235,2,v237
v_and_b32 v238,1,v234
v_lshlrev_b32 v238,5,v238
v_and_b32 v239,3,v233
v_lshl_add_u32 v238,v239,3,v238
v_add_u32 v238,124416,v238
v_lshl_add_u32 v16,v237,6,v238
v_mul_u32_u24 v237,192,v237
v_add_u32 v17,v237,v238
v_add_u32 v17,18432,v17
v_lshlrev_b32 v237,4,v235
v_add_u32 v18,161280,v237
v_add_u32 v237,s71,v248
v_add_u32 v19,3648,v237
v_and_b32 v237,1,v234
v_add_u32 v237,1,v237
v_mul_u32_u24 v237,18,v237
v_add_u32 v238,1,v233
v_add_u32 v237,v237,v238
v_mul_u32_u24 v237,192,v237
v_add_u32 v237,s71,v237
v_lshl_add_u32 v237,v235,3,v237
v_bfe_u32 v238,v238,2,2
v_xor_b32 v240,0,v238
v_lshl_add_u32 v20,v240,4,v237
v_xor_b32 v240,1,v238
v_lshl_add_u32 v21,v240,4,v237
v_xor_b32 v240,2,v238
v_lshl_add_u32 v22,v240,4,v237
v_xor_b32 v240,3,v238
v_lshl_add_u32 v23,v240,4,v237
v_add_u32 v237,0,v232
v_mul_u32_u24 v238,0xaaab,v237
v_lshrrev_b32 v238,19,v238
v_mul_u32_u24 v239,12,v238
v_sub_u32 v239,v237,v239
v_add_u32 v237,1,v238
v_bfe_u32 v237,v237,2,2
v_xor_b32 v239,v239,v237
v_lshlrev_b32 v239,4,v239
v_mul_u32_u24 v237,192,v238
v_add_u32 v24,v237,v239
v_add_u32 v237,64,v232
v_mul_u32_u24 v238,0xaaab,v237
v_lshrrev_b32 v238,19,v238
v_mul_u32_u24 v239,12,v238
v_sub_u32 v239,v237,v239
v_add_u32 v237,1,v238
v_bfe_u32 v237,v237,2,2
v_xor_b32 v239,v239,v237
v_lshlrev_b32 v239,4,v239
v_mul_u32_u24 v237,192,v238
v_add_u32 v25,v237,v239
v_add_u32 v237,128,v232
v_mul_u32_u24 v238,0xaaab,v237
v_lshrrev_b32 v238,19,v238
v_mul_u32_u24 v239,12,v238
v_sub_u32 v239,v237,v239
v_add_u32 v237,1,v238
v_bfe_u32 v237,v237,2,2
v_xor_b32 v239,v239,v237
v_lshlrev_b32 v239,4,v239
v_mul_u32_u24 v237,192,v238
v_add_u32 v26,v237,v239
s_cmp_ge_u32 s52,4
s_cbranch_scc1 L_wB_%=
s_waitcnt vmcnt(17)
s_branch L_wA_%=
L_wB_%=:
s_waitcnt vmcnt(11)
L_wA_%=:
ds_read_u16 v36,v250 offset:0
ds_read_u16 v37,v250 offset:16
ds_read_u16 v38,v250 offset:32
ds_read_u16 v39,v250 offset:48
ds_read_u16 v40,v250 offset:64
ds_read_u16 v41,v250 offset:80
ds_read_u16 v42,v250 offset:96
ds_read_u16 v43,v250 offset:112
ds_read_u16 v44,v250 offset:128
ds_read_u16 v45,v250 offset:144
ds_read_u16 v46,v250 offset:160
ds_read_u16 v47,v250 offset:176
ds_read_u16 v48,v250 offset:192
ds_read_u16 v49,v250 offset:208
ds_read_u16 v50,v250 offset:224
ds_read_u16 v51,v250 offset:240
ds_read_u16 v52,v250 offset:256
ds_read_u16 v53,v250 offset:272
ds_read_u16 v54,v250 offset:288
ds_read_u16 v55,v250 offset:304
ds_read_u16 v56,v250 offset:320
ds_read_u16 v57,v250 offset:336
ds_read_u16 v58,v250 offset:352
ds_read_u16 v59,v250 offset:368
ds_read_u16 v60,v250 offset:384
ds_read_u16 v61,v250 offset:400
ds_read_u16 v62,v250 offset:416
ds_read_u16 v63,v250 offset:432
ds_read_u16 v64,v250 offset:448
ds_read_u16 v65,v250 offset:464
ds_read_u16 v66,v250 offset:480
ds_read_u16 v67,v250 offset:496
ds_read_u16 v68,v250 offset:512
ds_read_u16 v69,v250 offset:528
ds_read_u16 v70,v250 offset:544
ds_read_u16 v71,v250 offset:560
v_lshlrev_b32 v244,16,v244
v_lshlrev_b32 v245,16,v245
v_lshlrev_b32 v246,16,v246
v_mul_f32 v244,0xbeeff558,v244
v_mul_f32 v245,0xbeeff558,v245
v_mul_f32 v246,0xc0088e99,v246
v_min_u32 v237,383,%1
v_lshlrev_b32 v237,2,v237
v_add_u32 v237,161280,v237
ds_write_b32 v237,v244 offset:0
v_min_u32 v237,95,%1
v_lshlrev_b32 v237,2,v237
v_add_u32 v237,161280,v237
ds_write_b32 v237,v245 offset:1920
ds_write_b32 v237,v246 offset:1536
s_waitcnt lgkmcnt(0)
v_lshlrev_b32 v36,v243,v36
v_lshlrev_b32 v37,v243,v37
v_lshlrev_b32 v38,v243,v38
v_lshlrev_b32 v39,v243,v39
v_lshlrev_b32 v40,v243,v40
v_lshlrev_b32 v41,v243,v41
v_lshlrev_b32 v42,v243,v42
v_lshlrev_b32 v43,v243,v43
v_lshlrev_b32 v44,v243,v44
v_lshlrev_b32 v45,v243,v45
v_lshlrev_b32 v46,v243,v46
v_lshlrev_b32 v47,v243,v47
v_lshlrev_b32 v48,v243,v48
v_lshlrev_b32 v49,v243,v49
v_lshlrev_b32 v50,v243,v50
v_lshlrev_b32 v51,v243,v51
v_lshlrev_b32 v52,v243,v52
v_lshlrev_b32 v53,v243,v53
v_lshlrev_b32 v54,v243,v54
v_lshlrev_b32 v55,v243,v55
v_lshlrev_b32 v56,v243,v56
v_lshlrev_b32 v57,v243,v57
v_lshlrev_b32 v58,v243,v58
v_lshlrev_b32 v59,v243,v59
v_lshlrev_b32 v60,v243,v60
v_lshlrev_b32 v61,v243,v61
v_lshlrev_b32 v62,v243,v62
v_lshlrev_b32 v63,v243,v63
v_lshlrev_b32 v64,v243,v64
v_lshlrev_b32 v65,v243,v65
v_lshlrev_b32 v66,v243,v66
v_lshlrev_b32 v67,v243,v67
v_lshlrev_b32 v68,v243,v68
v_lshlrev_b32 v69,v243,v69
v_lshlrev_b32 v70,v243,v70
v_lshlrev_b32 v71,v243,v71
s_cmp_ge_u32 s52,4
s_cbranch_scc1 L_fB_%=
s_waitcnt vmcnt(6)
s_branch L_fA_%=
L_fB_%=:
s_waitcnt vmcnt(2)
L_fA_%=:
s_cmp_ge_u32 s52,4
s_cbranch_scc1 L_pass1_%=
L_pass0_%=:
s_barrier
ds_read_b128 v[200:203],v4 offset:0
ds_read_b128 v[204:207],v8 offset:0
ds_read_b128 v[208:211],v12 offset:0
ds_read_b128 v[212:215],v5 offset:0
ds_read_b128 v[216:219],v9 offset:0
ds_read_b128 v[220:223],v13 offset:0
ds_read_b128 v[96:99],v18 offset:1920
ds_read_b128 v[100:103],v18 offset:1952
ds_read_b128 v[104:107],v18 offset:1984
ds_read_b128 v[108:111],v18 offset:2016
ds_read_b128 v[112:115],v18 offset:2048
ds_read_b128 v[116:119],v18 offset:2080
ds_read_b128 v[120:123],v18 offset:2112
ds_read_b128 v[124:127],v18 offset:2144
ds_read_b128 v[128:131],v18 offset:2176
ds_read_b128 v[132:135],v18 offset:2208
ds_read_b128 v[136:139],v18 offset:2240
ds_read_b128 v[140:143],v18 offset:2272
v_and_b32 v72,v32,v36
v_and_b32 v73,v33,v36
v_and_b32 v74,v34,v36
v_and_b32 v75,v35,v36
v_and_b32 v76,v32,v48
v_and_b32 v77,v33,v48
v_and_b32 v78,v34,v48
v_and_b32 v79,v35,v48
v_and_b32 v80,v32,v60
v_and_b32 v81,v33,v60
v_and_b32 v82,v34,v60
v_and_b32 v83,v35,v60
v_and_b32 v84,v32,v37
v_and_b32 v85,v33,v37
v_and_b32 v86,v34,v37
v_and_b32 v87,v35,v37
v_and_b32 v88,v32,v49
v_and_b32 v89,v33,v49
v_and_b32 v90,v34,v49
v_and_b32 v91,v35,v49
v_and_b32 v92,v32,v61
v_and_b32 v93,v33,v61
v_and_b32 v94,v34,v61
v_and_b32 v95,v35,v61
s_setprio 1
s_waitcnt lgkmcnt(15)
v_mfma_f32_16x16x32_bf16 v[144:147],v[72:75],v[200:203],0
v_mfma_f32_16x16x32_bf16 v[144:147],v[76:79],v[204:207],v[144:147]
v_mfma_f32_16x16x32_bf16 v[144:147],v[80:83],v[208:211],v[144:147]
ds_read_b128 v[200:203],v6 offset:0
ds_read_b128 v[204:207],v10 offset:0
ds_read_b128 v[208:211],v14 offset:0
s_setprio 0
v_and_b32 v72,v32,v38
v_and_b32 v73,v33,v38
v_and_b32 v74,v34,v38
v_and_b32 v75,v35,v38
v_and_b32 v76,v32,v50
v_and_b32 v77,v33,v50
v_and_b32 v78,v34,v50
v_and_b32 v79,v35,v50
v_and_b32 v80,v32,v62
v_and_b32 v81,v33,v62
v_and_b32 v82,v34,v62
v_and_b32 v83,v35,v62
v_mul_f32 v237,v144,v144
v_mul_f32 v236,v145,v145
v_mul_f32 v239,v146,v146
v_mul_f32 v238,v147,v147
s_setprio 1
s_waitcnt lgkmcnt(15)
v_mfma_f32_16x16x32_bf16 v[148:151],v[84:87],v[212:215],0
v_mfma_f32_16x16x32_bf16 v[148:151],v[88:91],v[216:219],v[148:151]
v_mfma_f32_16x16x32_bf16 v[148:151],v[92:95],v[220:223],v[148:151]
ds_read_b128 v[212:215],v7 offset:0
ds_read_b128 v[216:219],v11 offset:0
ds_read_b128 v[220:223],v15 offset:0
s_setprio 0
v_and_b32 v84,v32,v39
v_and_b32 v85,v33,v39
v_and_b32 v86,v34,v39
v_and_b32 v87,v35,v39
v_and_b32 v88,v32,v51
v_and_b32 v89,v33,v51
v_and_b32 v90,v34,v51
v_and_b32 v91,v35,v51
v_and_b32 v92,v32,v63
v_and_b32 v93,v33,v63
v_and_b32 v94,v34,v63
v_and_b32 v95,v35,v63
v_add_f32 v232,v144,v148
v_fmac_f32 v237,v148,v148
v_add_f32 v233,v145,v149
v_fmac_f32 v236,v149,v149
v_add_f32 v234,v146,v150
v_fmac_f32 v239,v150,v150
v_add_f32 v235,v147,v151
v_fmac_f32 v238,v151,v151
s_setprio 1
s_waitcnt lgkmcnt(3)
v_mfma_f32_16x16x32_bf16 v[152:155],v[72:75],v[200:203],0
v_mfma_f32_16x16x32_bf16 v[152:155],v[76:79],v[204:207],v[152:155]
v_mfma_f32_16x16x32_bf16 v[152:155],v[80:83],v[208:211],v[152:155]
ds_read_b128 v[200:203],v4 offset:64
ds_read_b128 v[204:207],v8 offset:64
ds_read_b128 v[208:211],v12 offset:64
s_setprio 0
v_and_b32 v72,v32,v40
v_and_b32 v73,v33,v40
v_and_b32 v74,v34,v40
v_and_b32 v75,v35,v40
v_and_b32 v76,v32,v52
v_and_b32 v77,v33,v52
v_and_b32 v78,v34,v52
v_and_b32 v79,v35,v52
v_and_b32 v80,v32,v64
v_and_b32 v81,v33,v64
v_and_b32 v82,v34,v64
v_and_b32 v83,v35,v64
v_add_f32 v232,v232,v152
v_fmac_f32 v237,v152,v152
v_add_f32 v233,v233,v153
v_fmac_f32 v236,v153,v153
v_add_f32 v234,v234,v154
v_fmac_f32 v239,v154,v154
v_add_f32 v235,v235,v155
v_fmac_f32 v238,v155,v155
s_setprio 1
s_waitcnt lgkmcnt(3)
v_mfma_f32_16x16x32_bf16 v[156:159],v[84:87],v[212:215],0
v_mfma_f32_16x16x32_bf16 v[156:159],v[88:91],v[216:219],v[156:159]
v_mfma_f32_16x16x32_bf16 v[156:159],v[92:95],v[220:223],v[156:159]
ds_read_b128 v[212:215],v5 offset:64
ds_read_b128 v[216:219],v9 offset:64
ds_read_b128 v[220:223],v13 offset:64
s_setprio 0
v_and_b32 v84,v32,v41
v_and_b32 v85,v33,v41
v_and_b32 v86,v34,v41
v_and_b32 v87,v35,v41
v_and_b32 v88,v32,v53
v_and_b32 v89,v33,v53
v_and_b32 v90,v34,v53
v_and_b32 v91,v35,v53
v_and_b32 v92,v32,v65
v_and_b32 v93,v33,v65
v_and_b32 v94,v34,v65
v_and_b32 v95,v35,v65
v_add_f32 v232,v232,v156
v_fmac_f32 v237,v156,v156
v_add_f32 v233,v233,v157
v_fmac_f32 v236,v157,v157
v_add_f32 v234,v234,v158
v_fmac_f32 v239,v158,v158
v_add_f32 v235,v235,v159
v_fmac_f32 v238,v159,v159
s_setprio 1
s_waitcnt lgkmcnt(3)
v_mfma_f32_16x16x32_bf16 v[160:163],v[72:75],v[200:203],0
v_mfma_f32_16x16x32_bf16 v[160:163],v[76:79],v[204:207],v[160:163]
v_mfma_f32_16x16x32_bf16 v[160:163],v[80:83],v[208:211],v[160:163]
ds_read_b128 v[200:203],v6 offset:64
ds_read_b128 v[204:207],v10 offset:64
ds_read_b128 v[208:211],v14 offset:64
s_setprio 0
v_and_b32 v72,v32,v42
v_and_b32 v73,v33,v42
v_and_b32 v74,v34,v42
v_and_b32 v75,v35,v42
v_and_b32 v76,v32,v54
v_and_b32 v77,v33,v54
v_and_b32 v78,v34,v54
v_and_b32 v79,v35,v54
v_and_b32 v80,v32,v66
v_and_b32 v81,v33,v66
v_and_b32 v82,v34,v66
v_and_b32 v83,v35,v66
v_add_f32 v232,v232,v160
v_fmac_f32 v237,v160,v160
v_add_f32 v233,v233,v161
v_fmac_f32 v236,v161,v161
v_add_f32 v234,v234,v162
v_fmac_f32 v239,v162,v162
v_add_f32 v235,v235,v163
v_fmac_f32 v238,v163,v163
s_setprio 1
s_waitcnt lgkmcnt(3)
v_mfma_f32_16x16x32_bf16 v[164:167],v[84:87],v[212:215],0
v_mfma_f32_16x16x32_bf16 v[164:167],v[88:91],v[216:219],v[164:167]
v_mfma_f32_16x16x32_bf16 v[164:167],v[92:95],v[220:223],v[164:167]
ds_read_b128 v[212:215],v7 offset:64
ds_read_b128 v[216:219],v11 offset:64
ds_read_b128 v[220:223],v15 offset:64
s_setprio 0
v_and_b32 v84,v32,v43
v_and_b32 v85,v33,v43
v_and_b32 v86,v34,v43
v_and_b32 v87,v35,v43
v_and_b32 v88,v32,v55
v_and_b32 v89,v33,v55
v_and_b32 v90,v34,v55
v_and_b32 v91,v35,v55
v_and_b32 v92,v32,v67
v_and_b32 v93,v33,v67
v_and_b32 v94,v34,v67
v_and_b32 v95,v35,v67
v_add_f32 v232,v232,v164
v_fmac_f32 v237,v164,v164
v_add_f32 v233,v233,v165
v_fmac_f32 v236,v165,v165
v_add_f32 v234,v234,v166
v_fmac_f32 v239,v166,v166
v_add_f32 v235,v235,v167
v_fmac_f32 v238,v167,v167
s_setprio 1
s_waitcnt lgkmcnt(3)
v_mfma_f32_16x16x32_bf16 v[168:171],v[72:75],v[200:203],0
v_mfma_f32_16x16x32_bf16 v[168:171],v[76:79],v[204:207],v[168:171]
v_mfma_f32_16x16x32_bf16 v[168:171],v[80:83],v[208:211],v[168:171]
ds_read_b128 v[200:203],v4 offset:128
ds_read_b128 v[204:207],v8 offset:128
ds_read_b128 v[208:211],v12 offset:128
s_setprio 0
v_and_b32 v72,v32,v44
v_and_b32 v73,v33,v44
v_and_b32 v74,v34,v44
v_and_b32 v75,v35,v44
v_and_b32 v76,v32,v56
v_and_b32 v77,v33,v56
v_and_b32 v78,v34,v56
v_and_b32 v79,v35,v56
v_and_b32 v80,v32,v68
v_and_b32 v81,v33,v68
v_and_b32 v82,v34,v68
v_and_b32 v83,v35,v68
v_add_f32 v232,v232,v168
v_fmac_f32 v237,v168,v168
v_add_f32 v233,v233,v169
v_fmac_f32 v236,v169,v169
v_add_f32 v234,v234,v170
v_fmac_f32 v239,v170,v170
v_add_f32 v235,v235,v171
v_fmac_f32 v238,v171,v171
s_setprio 1
s_waitcnt lgkmcnt(3)
v_mfma_f32_16x16x32_bf16 v[172:175],v[84:87],v[212:215],0
v_mfma_f32_16x16x32_bf16 v[172:175],v[88:91],v[216:219],v[172:175]
v_mfma_f32_16x16x32_bf16 v[172:175],v[92:95],v[220:223],v[172:175]
ds_read_b128 v[212:215],v5 offset:128
ds_read_b128 v[216:219],v9 offset:128
ds_read_b128 v[220:223],v13 offset:128
s_setprio 0
v_and_b32 v84,v32,v45
v_and_b32 v85,v33,v45
v_and_b32 v86,v34,v45
v_and_b32 v87,v35,v45
v_and_b32 v88,v32,v57
v_and_b32 v89,v33,v57
v_and_b32 v90,v34,v57
v_and_b32 v91,v35,v57
v_and_b32 v92,v32,v69
v_and_b32 v93,v33,v69
v_and_b32 v94,v34,v69
v_and_b32 v95,v35,v69
v_add_f32 v232,v232,v172
v_fmac_f32 v237,v172,v172
v_add_f32 v233,v233,v173
v_fmac_f32 v236,v173,v173
v_add_f32 v234,v234,v174
v_fmac_f32 v239,v174,v174
v_add_f32 v235,v235,v175
v_fmac_f32 v238,v175,v175
s_setprio 1
s_waitcnt lgkmcnt(3)
v_mfma_f32_16x16x32_bf16 v[176:179],v[72:75],v[200:203],0
v_mfma_f32_16x16x32_bf16 v[176:179],v[76:79],v[204:207],v[176:179]
v_mfma_f32_16x16x32_bf16 v[176:179],v[80:83],v[208:211],v[176:179]
ds_read_b128 v[200:203],v6 offset:128
ds_read_b128 v[204:207],v10 offset:128
ds_read_b128 v[208:211],v14 offset:128
s_setprio 0
v_and_b32 v72,v32,v46
v_and_b32 v73,v33,v46
v_and_b32 v74,v34,v46
v_and_b32 v75,v35,v46
v_and_b32 v76,v32,v58
v_and_b32 v77,v33,v58
v_and_b32 v78,v34,v58
v_and_b32 v79,v35,v58
v_and_b32 v80,v32,v70
v_and_b32 v81,v33,v70
v_and_b32 v82,v34,v70
v_and_b32 v83,v35,v70
v_add_f32 v232,v232,v176
v_fmac_f32 v237,v176,v176
v_add_f32 v233,v233,v177
v_fmac_f32 v236,v177,v177
v_add_f32 v234,v234,v178
v_fmac_f32 v239,v178,v178
v_add_f32 v235,v235,v179
v_fmac_f32 v238,v179,v179
s_setprio 1
s_waitcnt lgkmcnt(3)
v_mfma_f32_16x16x32_bf16 v[180:183],v[84:87],v[212:215],0
v_mfma_f32_16x16x32_bf16 v[180:183],v[88:91],v[216:219],v[180:183]
v_mfma_f32_16x16x32_bf16 v[180:183],v[92:95],v[220:223],v[180:183]
ds_read_b128 v[212:215],v7 offset:128
ds_read_b128 v[216:219],v11 offset:128
ds_read_b128 v[220:223],v15 offset:128
s_setprio 0
v_and_b32 v84,v32,v47
v_and_b32 v85,v33,v47
v_and_b32 v86,v34,v47
v_and_b32 v87,v35,v47
v_and_b32 v88,v32,v59
v_and_b32 v89,v33,v59
v_and_b32 v90,v34,v59
v_and_b32 v91,v35,v59
v_and_b32 v92,v32,v71
v_and_b32 v93,v33,v71
v_and_b32 v94,v34,v71
v_and_b32 v95,v35,v71
v_add_f32 v232,v232,v180
v_fmac_f32 v237,v180,v180
v_add_f32 v233,v233,v181
v_fmac_f32 v236,v181,v181
v_add_f32 v234,v234,v182
v_fmac_f32 v239,v182,v182
v_add_f32 v235,v235,v183
v_fmac_f32 v238,v183,v183
s_setprio 1
s_waitcnt lgkmcnt(3)
v_mfma_f32_16x16x32_bf16 v[184:187],v[72:75],v[200:203],0
v_mfma_f32_16x16x32_bf16 v[184:187],v[76:79],v[204:207],v[184:187]
v_mfma_f32_16x16x32_bf16 v[184:187],v[80:83],v[208:211],v[184:187]
s_setprio 0
s_nop 7
v_add_f32 v232,v232,v184
v_fmac_f32 v237,v184,v184
v_add_f32 v233,v233,v185
v_fmac_f32 v236,v185,v185
v_add_f32 v234,v234,v186
v_fmac_f32 v239,v186,v186
v_add_f32 v235,v235,v187
v_fmac_f32 v238,v187,v187
s_setprio 1
s_waitcnt lgkmcnt(0)
v_mfma_f32_16x16x32_bf16 v[188:191],v[84:87],v[212:215],0
v_mfma_f32_16x16x32_bf16 v[188:191],v[88:91],v[216:219],v[188:191]
v_mfma_f32_16x16x32_bf16 v[188:191],v[92:95],v[220:223],v[188:191]
s_setprio 0
ds_read_b64_tr_b16 v[200:201],v16 offset:0
ds_read_b64_tr_b16 v[202:203],v16 offset:512
ds_read_b64_tr_b16 v[204:205],v16 offset:1024
ds_read_b64_tr_b16 v[206:207],v16 offset:1536
ds_read_b64_tr_b16 v[208:209],v16 offset:2048
ds_read_b64_tr_b16 v[210:211],v16 offset:2560
ds_read_b64_tr_b16 v[212:213],v16 offset:3072
ds_read_b64_tr_b16 v[214:215],v16 offset:3584
ds_read_b64_tr_b16 v[216:217],v16 offset:4096
ds_read_b64_tr_b16 v[218:219],v16 offset:4608
ds_read_b64_tr_b16 v[220:221],v16 offset:5120
ds_read_b64_tr_b16 v[222:223],v16 offset:5632
v_add_f32 v232,v232,v188
v_fmac_f32 v237,v188,v188
v_add_f32 v233,v233,v189
v_fmac_f32 v236,v189,v189
v_add_f32 v234,v234,v190
v_fmac_f32 v239,v190,v190
v_add_f32 v235,v235,v191
v_fmac_f32 v238,v191,v191
v_add_f32 v232,v232,v233
v_add_f32 v234,v234,v235
v_add_f32 v237,v237,v236
v_add_f32 v239,v239,v238
v_add_f32 v240,v232,v234
v_add_f32 v241,v237,v239
s_nop 1
v_permlane32_swap_b32 v240,v241
v_add_f32 v240,v240,v241
v_mov_b32 v241,v240
s_nop 1
v_permlane32_swap_b32 v240,v241
v_mul_f32 v240,0x3c2aaaab,v240
v_mul_f32 v241,0x3c2aaaab,v241
v_fma_f32 v241,-v240,v240,v241
v_max_f32 v241,0,v241
v_add_f32 v241,0x3727c5ac,v241
v_rsq_f32 v241,v241
s_nop 0
v_mul_f32 v242,0xbeeff558,v241
v_mul_f32 v243,0x3eeff558,v241
v_mul_f32 v243,v240,v243
v_fma_f32 v144,v144,v242,v243
v_fma_f32 v145,v145,v242,v243
v_fma_f32 v146,v146,v242,v243
v_fma_f32 v147,v147,v242,v243
v_fma_f32 v148,v148,v242,v243
v_fma_f32 v149,v149,v242,v243
v_fma_f32 v150,v150,v242,v243
v_fma_f32 v151,v151,v242,v243
v_fma_f32 v152,v152,v242,v243
v_fma_f32 v153,v153,v242,v243
v_fma_f32 v154,v154,v242,v243
v_fma_f32 v155,v155,v242,v243
v_fma_f32 v156,v156,v242,v243
v_fma_f32 v157,v157,v242,v243
v_fma_f32 v158,v158,v242,v243
v_fma_f32 v159,v159,v242,v243
v_fma_f32 v160,v160,v242,v243
v_fma_f32 v161,v161,v242,v243
v_fma_f32 v162,v162,v242,v243
v_fma_f32 v163,v163,v242,v243
v_fma_f32 v164,v164,v242,v243
v_fma_f32 v165,v165,v242,v243
v_fma_f32 v166,v166,v242,v243
v_fma_f32 v167,v167,v242,v243
v_fma_f32 v168,v168,v242,v243
v_fma_f32 v169,v169,v242,v243
v_fma_f32 v170,v170,v242,v243
v_fma_f32 v171,v171,v242,v243
v_fma_f32 v172,v172,v242,v243
v_fma_f32 v173,v173,v242,v243
v_fma_f32 v174,v174,v242,v243
v_fma_f32 v175,v175,v242,v243
v_fma_f32 v176,v176,v242,v243
v_fma_f32 v177,v177,v242,v243
v_fma_f32 v178,v178,v242,v243
v_fma_f32 v179,v179,v242,v243
v_fma_f32 v180,v180,v242,v243
v_fma_f32 v181,v181,v242,v243
v_fma_f32 v182,v182,v242,v243
v_fma_f32 v183,v183,v242,v243
v_fma_f32 v184,v184,v242,v243
v_fma_f32 v185,v185,v242,v243
v_fma_f32 v186,v186,v242,v243
v_fma_f32 v187,v187,v242,v243
v_fma_f32 v188,v188,v242,v243
v_fma_f32 v189,v189,v242,v243
v_fma_f32 v190,v190,v242,v243
v_fma_f32 v191,v191,v242,v243
s_setprio 1
v_cvt_pk_bf16_f32 v72,v144,v145
v_cvt_pk_bf16_f32 v73,v146,v147
v_cvt_pk_bf16_f32 v74,v148,v149
v_cvt_pk_bf16_f32 v75,v150,v151
v_cvt_pk_bf16_f32 v76,v152,v153
v_cvt_pk_bf16_f32 v77,v154,v155
v_cvt_pk_bf16_f32 v78,v156,v157
v_cvt_pk_bf16_f32 v79,v158,v159
ds_read_b128 v[144:147],v18 offset:0
ds_read_b128 v[148:151],v18 offset:32
ds_read_b128 v[152:155],v18 offset:64
ds_read_b128 v[156:159],v18 offset:96
v_cvt_pk_bf16_f32 v80,v160,v161
v_cvt_pk_bf16_f32 v81,v162,v163
v_cvt_pk_bf16_f32 v82,v164,v165
v_cvt_pk_bf16_f32 v83,v166,v167
v_cvt_pk_bf16_f32 v84,v168,v169
v_cvt_pk_bf16_f32 v85,v170,v171
v_cvt_pk_bf16_f32 v86,v172,v173
v_cvt_pk_bf16_f32 v87,v174,v175
s_waitcnt lgkmcnt(0)
v_mfma_f32_32x32x16_bf16 v[144:159],v[200:203],v[72:75],v[144:159]
v_cvt_pk_bf16_f32 v88,v176,v177
v_cvt_pk_bf16_f32 v89,v178,v179
v_cvt_pk_bf16_f32 v90,v180,v181
v_cvt_pk_bf16_f32 v91,v182,v183
v_mfma_f32_32x32x16_bf16 v[144:159],v[204:207],v[76:79],v[144:159]
v_cvt_pk_bf16_f32 v92,v184,v185
v_cvt_pk_bf16_f32 v93,v186,v187
v_cvt_pk_bf16_f32 v94,v188,v189
v_cvt_pk_bf16_f32 v95,v190,v191
v_mfma_f32_32x32x16_bf16 v[144:159],v[208:211],v[80:83],v[144:159]
v_mfma_f32_32x32x16_bf16 v[144:159],v[212:215],v[84:87],v[144:159]
v_mfma_f32_32x32x16_bf16 v[144:159],v[216:219],v[88:91],v[144:159]
v_mfma_f32_32x32x16_bf16 v[144:159],v[220:223],v[92:95],v[144:159]
s_setprio 0
s_nop 7
s_nop 1
s_waitcnt vmcnt(2)
s_barrier
v_fmaak_f32 v176,v144,v144,0x409d30fc
v_fmaak_f32 v177,v145,v145,0x409d30fc
v_fmaak_f32 v178,v146,v146,0x409d30fc
v_fmaak_f32 v179,v147,v147,0x409d30fc
v_fmaak_f32 v180,v148,v148,0x409d30fc
v_fmaak_f32 v181,v149,v149,0x409d30fc
v_fmaak_f32 v182,v150,v150,0x409d30fc
v_fmaak_f32 v183,v151,v151,0x409d30fc
v_fmaak_f32 v184,v152,v152,0x409d30fc
v_fmaak_f32 v185,v153,v153,0x409d30fc
v_fmaak_f32 v186,v154,v154,0x409d30fc
v_fmaak_f32 v187,v155,v155,0x409d30fc
v_fmaak_f32 v188,v156,v156,0x409d30fc
v_fmaak_f32 v189,v157,v157,0x409d30fc
v_fmaak_f32 v190,v158,v158,0x409d30fc
v_fmaak_f32 v191,v159,v159,0x409d30fc
v_mul_f32 v176,v144,v176
v_mul_f32 v177,v145,v177
v_mul_f32 v178,v146,v178
v_mul_f32 v179,v147,v179
v_mul_f32 v180,v148,v180
v_mul_f32 v181,v149,v181
v_mul_f32 v182,v150,v182
v_mul_f32 v183,v151,v183
v_mul_f32 v184,v152,v184
v_mul_f32 v185,v153,v185
v_mul_f32 v186,v154,v186
v_mul_f32 v187,v155,v187
v_mul_f32 v188,v156,v188
v_mul_f32 v189,v157,v189
v_mul_f32 v190,v158,v190
v_mul_f32 v191,v159,v191
s_setprio 1
v_exp_f32 v176,v176
v_exp_f32 v177,v177
v_exp_f32 v178,v178
v_exp_f32 v179,v179
v_exp_f32 v180,v180
v_exp_f32 v181,v181
v_exp_f32 v182,v182
v_exp_f32 v183,v183
v_exp_f32 v184,v184
v_exp_f32 v185,v185
v_exp_f32 v186,v186
v_exp_f32 v187,v187
v_exp_f32 v188,v188
v_exp_f32 v189,v189
v_exp_f32 v190,v190
v_exp_f32 v191,v191
ds_read_b64_tr_b16 v[200:201],v16 offset:6144
ds_read_b64_tr_b16 v[202:203],v16 offset:6656
ds_read_b64_tr_b16 v[204:205],v16 offset:7168
ds_read_b64_tr_b16 v[206:207],v16 offset:7680
ds_read_b64_tr_b16 v[208:209],v16 offset:8192
ds_read_b64_tr_b16 v[210:211],v16 offset:8704
ds_read_b64_tr_b16 v[212:213],v16 offset:9216
ds_read_b64_tr_b16 v[214:215],v16 offset:9728
ds_read_b64_tr_b16 v[216:217],v16 offset:10240
ds_read_b64_tr_b16 v[218:219],v16 offset:10752
ds_read_b64_tr_b16 v[220:221],v16 offset:11264
ds_read_b64_tr_b16 v[222:223],v16 offset:11776
ds_read_b128 v[160:163],v18 offset:128
ds_read_b128 v[164:167],v18 offset:160
ds_read_b128 v[168:171],v18 offset:192
ds_read_b128 v[172:175],v18 offset:224
s_mov_b64 exec,s[60:61]
s_add_u32 m0,s58,148992
s_mov_b32 s64,6144
buffer_load_dwordx4 v28,s[48:51],s64 offen lds
s_add_u32 m0,s58,149760
s_mov_b32 s65,6912
buffer_load_dwordx4 v28,s[48:51],s65 offen lds
s_mov_b64 exec,-1
s_setprio 0
v_add_f32 v176,1.0,v176
v_add_f32 v177,1.0,v177
v_add_f32 v178,1.0,v178
v_add_f32 v179,1.0,v179
v_add_f32 v180,1.0,v180
v_add_f32 v181,1.0,v181
v_add_f32 v182,1.0,v182
v_add_f32 v183,1.0,v183
v_add_f32 v184,1.0,v184
v_add_f32 v185,1.0,v185
v_add_f32 v186,1.0,v186
v_add_f32 v187,1.0,v187
v_add_f32 v188,1.0,v188
v_add_f32 v189,1.0,v189
v_add_f32 v190,1.0,v190
v_add_f32 v191,1.0,v191
s_setprio 1
s_waitcnt lgkmcnt(0)
v_mfma_f32_32x32x16_bf16 v[160:175],v[200:203],v[72:75],v[160:175]
v_rcp_f32 v176,v176
v_rcp_f32 v177,v177
v_rcp_f32 v178,v178
v_mfma_f32_32x32x16_bf16 v[160:175],v[204:207],v[76:79],v[160:175]
v_rcp_f32 v179,v179
v_rcp_f32 v180,v180
v_rcp_f32 v181,v181
v_mfma_f32_32x32x16_bf16 v[160:175],v[208:211],v[80:83],v[160:175]
v_rcp_f32 v182,v182
v_rcp_f32 v183,v183
v_rcp_f32 v184,v184
v_mfma_f32_32x32x16_bf16 v[160:175],v[212:215],v[84:87],v[160:175]
v_rcp_f32 v185,v185
v_rcp_f32 v186,v186
v_rcp_f32 v187,v187
v_mfma_f32_32x32x16_bf16 v[160:175],v[216:219],v[88:91],v[160:175]
v_rcp_f32 v188,v188
v_rcp_f32 v189,v189
v_mfma_f32_32x32x16_bf16 v[160:175],v[220:223],v[92:95],v[160:175]
v_rcp_f32 v190,v190
v_rcp_f32 v191,v191
ds_read_b64_tr_b16 v[200:201],v17 offset:0
ds_read_b64_tr_b16 v[202:203],v17 offset:1536
ds_read_b64_tr_b16 v[204:205],v17 offset:3072
ds_read_b64_tr_b16 v[206:207],v17 offset:4608
ds_read_b64_tr_b16 v[208:209],v17 offset:64
ds_read_b64_tr_b16 v[210:211],v17 offset:1600
ds_read_b64_tr_b16 v[212:213],v17 offset:3136
ds_read_b64_tr_b16 v[214:215],v17 offset:4672
ds_read_b64_tr_b16 v[216:217],v17 offset:128
ds_read_b64_tr_b16 v[218:219],v17 offset:1664
ds_read_b64_tr_b16 v[220:221],v17 offset:3200
ds_read_b64_tr_b16 v[222:223],v17 offset:4736
s_mov_b64 exec,s[60:61]
s_add_u32 m0,s58,124416
s_movk_i32 s64,192
buffer_load_dwordx4 v27,s[44:47],s64 offen lds
s_add_u32 m0,s58,125184
s_movk_i32 s65,9408
buffer_load_dwordx4 v27,s[44:47],s65 offen lds
s_mov_b64 exec,-1
s_setprio 0
v_mul_f32 v176,v144,v176
v_mul_f32 v177,v145,v177
v_mul_f32 v178,v146,v178
v_mul_f32 v179,v147,v179
v_mul_f32 v180,v148,v180
v_mul_f32 v181,v149,v181
v_mul_f32 v182,v150,v182
v_mul_f32 v183,v151,v183
v_mul_f32 v184,v152,v184
v_mul_f32 v185,v153,v185
v_mul_f32 v186,v154,v186
v_mul_f32 v187,v155,v187
v_mul_f32 v188,v156,v188
v_mul_f32 v189,v157,v189
v_mul_f32 v190,v158,v190
v_mul_f32 v191,v159,v191
v_cvt_pk_bf16_f32 v192,v176,v177
v_cvt_pk_bf16_f32 v193,v178,v179
v_cvt_pk_bf16_f32 v194,v180,v181
v_cvt_pk_bf16_f32 v195,v182,v183
v_cvt_pk_bf16_f32 v196,v184,v185
v_cvt_pk_bf16_f32 v197,v186,v187
v_cvt_pk_bf16_f32 v198,v188,v189
v_cvt_pk_bf16_f32 v199,v190,v191
s_waitcnt lgkmcnt(0)
s_waitcnt vmcnt(2)
s_barrier
v_fmaak_f32 v176,v160,v160,0x409d30fc
v_fmaak_f32 v177,v161,v161,0x409d30fc
v_fmaak_f32 v178,v162,v162,0x409d30fc
v_fmaak_f32 v179,v163,v163,0x409d30fc
v_fmaak_f32 v180,v164,v164,0x409d30fc
v_fmaak_f32 v181,v165,v165,0x409d30fc
v_fmaak_f32 v182,v166,v166,0x409d30fc
v_fmaak_f32 v183,v167,v167,0x409d30fc
v_fmaak_f32 v184,v168,v168,0x409d30fc
v_fmaak_f32 v185,v169,v169,0x409d30fc
v_fmaak_f32 v186,v170,v170,0x409d30fc
v_fmaak_f32 v187,v171,v171,0x409d30fc
v_fmaak_f32 v188,v172,v172,0x409d30fc
v_fmaak_f32 v189,v173,v173,0x409d30fc
v_fmaak_f32 v190,v174,v174,0x409d30fc
v_fmaak_f32 v191,v175,v175,0x409d30fc
v_mul_f32 v176,v160,v176
v_mul_f32 v177,v161,v177
v_mul_f32 v178,v162,v178
v_mul_f32 v179,v163,v179
v_mul_f32 v180,v164,v180
v_mul_f32 v181,v165,v181
v_mul_f32 v182,v166,v182
v_mul_f32 v183,v167,v183
v_mul_f32 v184,v168,v184
v_mul_f32 v185,v169,v185
v_mul_f32 v186,v170,v186
v_mul_f32 v187,v171,v187
v_mul_f32 v188,v172,v188
v_mul_f32 v189,v173,v189
v_mul_f32 v190,v174,v190
v_mul_f32 v191,v175,v191
s_setprio 1
v_mfma_f32_32x32x16_bf16 v[96:111],v[200:203],v[192:195],v[96:111]
v_exp_f32 v176,v176
v_exp_f32 v177,v177
v_exp_f32 v178,v178
v_mfma_f32_32x32x16_bf16 v[96:111],v[204:207],v[196:199],v[96:111]
v_exp_f32 v179,v179
v_exp_f32 v180,v180
v_exp_f32 v181,v181
v_mfma_f32_32x32x16_bf16 v[112:127],v[208:211],v[192:195],v[112:127]
v_exp_f32 v182,v182
v_exp_f32 v183,v183
v_exp_f32 v184,v184
v_mfma_f32_32x32x16_bf16 v[112:127],v[212:215],v[196:199],v[112:127]
v_exp_f32 v185,v185
v_exp_f32 v186,v186
v_exp_f32 v187,v187
v_mfma_f32_32x32x16_bf16 v[128:143],v[216:219],v[192:195],v[128:143]
v_exp_f32 v188,v188
v_exp_f32 v189,v189
v_mfma_f32_32x32x16_bf16 v[128:143],v[220:223],v[196:199],v[128:143]
v_exp_f32 v190,v190
v_exp_f32 v191,v191
ds_read_b64_tr_b16 v[200:201],v16 offset:12288
ds_read_b64_tr_b16 v[202:203],v16 offset:12800
ds_read_b64_tr_b16 v[204:205],v16 offset:13312
ds_read_b64_tr_b16 v[206:207],v16 offset:13824
ds_read_b64_tr_b16 v[208:209],v16 offset:14336
ds_read_b64_tr_b16 v[210:211],v16 offset:14848
ds_read_b64_tr_b16 v[212:213],v16 offset:15360
ds_read_b64_tr_b16 v[214:215],v16 offset:15872
ds_read_b64_tr_b16 v[216:217],v16 offset:16384
ds_read_b64_tr_b16 v[218:219],v16 offset:16896
ds_read_b64_tr_b16 v[220:221],v16 offset:17408
ds_read_b64_tr_b16 v[222:223],v16 offset:17920
ds_read_b128 v[144:147],v18 offset:256
ds_read_b128 v[148:151],v18 offset:288
ds_read_b128 v[152:155],v18 offset:320
ds_read_b128 v[156:159],v18 offset:352
s_mov_b64 exec,s[60:61]
s_add_u32 m0,s58,155136
s_mov_b32 s64,12288
buffer_load_dwordx4 v28,s[48:51],s64 offen lds
s_add_u32 m0,s58,155904
s_mov_b32 s65,13056
buffer_load_dwordx4 v28,s[48:51],s65 offen lds
s_mov_b64 exec,-1
s_setprio 0
v_add_f32 v176,1.0,v176
v_add_f32 v177,1.0,v177
v_add_f32 v178,1.0,v178
v_add_f32 v179,1.0,v179
v_add_f32 v180,1.0,v180
v_add_f32 v181,1.0,v181
v_add_f32 v182,1.0,v182
v_add_f32 v183,1.0,v183
v_add_f32 v184,1.0,v184
v_add_f32 v185,1.0,v185
v_add_f32 v186,1.0,v186
v_add_f32 v187,1.0,v187
v_add_f32 v188,1.0,v188
v_add_f32 v189,1.0,v189
v_add_f32 v190,1.0,v190
v_add_f32 v191,1.0,v191
s_setprio 1
s_waitcnt lgkmcnt(0)
v_mfma_f32_32x32x16_bf16 v[144:159],v[200:203],v[72:75],v[144:159]
v_rcp_f32 v176,v176
v_rcp_f32 v177,v177
v_rcp_f32 v178,v178
v_mfma_f32_32x32x16_bf16 v[144:159],v[204:207],v[76:79],v[144:159]
v_rcp_f32 v179,v179
v_rcp_f32 v180,v180
v_rcp_f32 v181,v181
v_mfma_f32_32x32x16_bf16 v[144:159],v[208:211],v[80:83],v[144:159]
v_rcp_f32 v182,v182
v_rcp_f32 v183,v183
v_rcp_f32 v184,v184
v_mfma_f32_32x32x16_bf16 v[144:159],v[212:215],v[84:87],v[144:159]
v_rcp_f32 v185,v185
v_rcp_f32 v186,v186
v_rcp_f32 v187,v187
v_mfma_f32_32x32x16_bf16 v[144:159],v[216:219],v[88:91],v[144:159]
v_rcp_f32 v188,v188
v_rcp_f32 v189,v189
v_mfma_f32_32x32x16_bf16 v[144:159],v[220:223],v[92:95],v[144:159]
v_rcp_f32 v190,v190
v_rcp_f32 v191,v191
ds_read_b64_tr_b16 v[200:201],v17 offset:6144
ds_read_b64_tr_b16 v[202:203],v17 offset:7680
ds_read_b64_tr_b16 v[204:205],v17 offset:9216
ds_read_b64_tr_b16 v[206:207],v17 offset:10752
ds_read_b64_tr_b16 v[208:209],v17 offset:6208
ds_read_b64_tr_b16 v[210:211],v17 offset:7744
ds_read_b64_tr_b16 v[212:213],v17 offset:9280
ds_read_b64_tr_b16 v[214:215],v17 offset:10816
ds_read_b64_tr_b16 v[216:217],v17 offset:6272
ds_read_b64_tr_b16 v[218:219],v17 offset:7808
ds_read_b64_tr_b16 v[220:221],v17 offset:9344
ds_read_b64_tr_b16 v[222:223],v17 offset:10880
s_mov_b64 exec,s[60:61]
s_add_u32 m0,s58,130560
s_movk_i32 s64,256
buffer_load_dwordx4 v27,s[44:47],s64 offen lds
s_add_u32 m0,s58,131328
s_movk_i32 s65,9472
buffer_load_dwordx4 v27,s[44:47],s65 offen lds
s_mov_b64 exec,-1
s_setprio 0
v_mul_f32 v176,v160,v176
v_mul_f32 v177,v161,v177
v_mul_f32 v178,v162,v178
v_mul_f32 v179,v163,v179
v_mul_f32 v180,v164,v180
v_mul_f32 v181,v165,v181
v_mul_f32 v182,v166,v182
v_mul_f32 v183,v167,v183
v_mul_f32 v184,v168,v184
v_mul_f32 v185,v169,v185
v_mul_f32 v186,v170,v186
v_mul_f32 v187,v171,v187
v_mul_f32 v188,v172,v188
v_mul_f32 v189,v173,v189
v_mul_f32 v190,v174,v190
v_mul_f32 v191,v175,v191
v_cvt_pk_bf16_f32 v192,v176,v177
v_cvt_pk_bf16_f32 v193,v178,v179
v_cvt_pk_bf16_f32 v194,v180,v181
v_cvt_pk_bf16_f32 v195,v182,v183
v_cvt_pk_bf16_f32 v196,v184,v185
v_cvt_pk_bf16_f32 v197,v186,v187
v_cvt_pk_bf16_f32 v198,v188,v189
v_cvt_pk_bf16_f32 v199,v190,v191
s_waitcnt lgkmcnt(0)
s_waitcnt vmcnt(2)
s_barrier
v_fmaak_f32 v176,v144,v144,0x409d30fc
v_fmaak_f32 v177,v145,v145,0x409d30fc
v_fmaak_f32 v178,v146,v146,0x409d30fc
v_fmaak_f32 v179,v147,v147,0x409d30fc
v_fmaak_f32 v180,v148,v148,0x409d30fc
v_fmaak_f32 v181,v149,v149,0x409d30fc
v_fmaak_f32 v182,v150,v150,0x409d30fc
v_fmaak_f32 v183,v151,v151,0x409d30fc
v_fmaak_f32 v184,v152,v152,0x409d30fc
v_fmaak_f32 v185,v153,v153,0x409d30fc
v_fmaak_f32 v186,v154,v154,0x409d30fc
v_fmaak_f32 v187,v155,v155,0x409d30fc
v_fmaak_f32 v188,v156,v156,0x409d30fc
v_fmaak_f32 v189,v157,v157,0x409d30fc
v_fmaak_f32 v190,v158,v158,0x409d30fc
v_fmaak_f32 v191,v159,v159,0x409d30fc
v_mul_f32 v176,v144,v176
v_mul_f32 v177,v145,v177
v_mul_f32 v178,v146,v178
v_mul_f32 v179,v147,v179
v_mul_f32 v180,v148,v180
v_mul_f32 v181,v149,v181
v_mul_f32 v182,v150,v182
v_mul_f32 v183,v151,v183
v_mul_f32 v184,v152,v184
v_mul_f32 v185,v153,v185
v_mul_f32 v186,v154,v186
v_mul_f32 v187,v155,v187
v_mul_f32 v188,v156,v188
v_mul_f32 v189,v157,v189
v_mul_f32 v190,v158,v190
v_mul_f32 v191,v159,v191
s_setprio 1
v_mfma_f32_32x32x16_bf16 v[96:111],v[200:203],v[192:195],v[96:111]
v_exp_f32 v176,v176
v_exp_f32 v177,v177
v_exp_f32 v178,v178
v_mfma_f32_32x32x16_bf16 v[96:111],v[204:207],v[196:199],v[96:111]
v_exp_f32 v179,v179
v_exp_f32 v180,v180
v_exp_f32 v181,v181
v_mfma_f32_32x32x16_bf16 v[112:127],v[208:211],v[192:195],v[112:127]
v_exp_f32 v182,v182
v_exp_f32 v183,v183
v_exp_f32 v184,v184
v_mfma_f32_32x32x16_bf16 v[112:127],v[212:215],v[196:199],v[112:127]
v_exp_f32 v185,v185
v_exp_f32 v186,v186
v_exp_f32 v187,v187
v_mfma_f32_32x32x16_bf16 v[128:143],v[216:219],v[192:195],v[128:143]
v_exp_f32 v188,v188
v_exp_f32 v189,v189
v_mfma_f32_32x32x16_bf16 v[128:143],v[220:223],v[196:199],v[128:143]
v_exp_f32 v190,v190
v_exp_f32 v191,v191
ds_read_b64_tr_b16 v[200:201],v16 offset:0
ds_read_b64_tr_b16 v[202:203],v16 offset:512
ds_read_b64_tr_b16 v[204:205],v16 offset:1024
ds_read_b64_tr_b16 v[206:207],v16 offset:1536
ds_read_b64_tr_b16 v[208:209],v16 offset:2048
ds_read_b64_tr_b16 v[210:211],v16 offset:2560
ds_read_b64_tr_b16 v[212:213],v16 offset:3072
ds_read_b64_tr_b16 v[214:215],v16 offset:3584
ds_read_b64_tr_b16 v[216:217],v16 offset:4096
ds_read_b64_tr_b16 v[218:219],v16 offset:4608
ds_read_b64_tr_b16 v[220:221],v16 offset:5120
ds_read_b64_tr_b16 v[222:223],v16 offset:5632
ds_read_b128 v[160:163],v18 offset:384
ds_read_b128 v[164:167],v18 offset:416
ds_read_b128 v[168:171],v18 offset:448
ds_read_b128 v[172:175],v18 offset:480
s_mov_b64 exec,s[60:61]
s_add_u32 m0,s58,142848
s_mov_b32 s64,18432
buffer_load_dwordx4 v28,s[48:51],s64 offen lds
s_add_u32 m0,s58,143616
s_mov_b32 s65,19200
buffer_load_dwordx4 v28,s[48:51],s65 offen lds
s_mov_b64 exec,-1
s_setprio 0
v_add_f32 v176,1.0,v176
v_add_f32 v177,1.0,v177
v_add_f32 v178,1.0,v178
v_add_f32 v179,1.0,v179
v_add_f32 v180,1.0,v180
v_add_f32 v181,1.0,v181
v_add_f32 v182,1.0,v182
v_add_f32 v183,1.0,v183
v_add_f32 v184,1.0,v184
v_add_f32 v185,1.0,v185
v_add_f32 v186,1.0,v186
v_add_f32 v187,1.0,v187
v_add_f32 v188,1.0,v188
v_add_f32 v189,1.0,v189
v_add_f32 v190,1.0,v190
v_add_f32 v191,1.0,v191
s_setprio 1
s_waitcnt lgkmcnt(0)
v_mfma_f32_32x32x16_bf16 v[160:175],v[200:203],v[72:75],v[160:175]
v_rcp_f32 v176,v176
v_rcp_f32 v177,v177
v_rcp_f32 v178,v178
v_mfma_f32_32x32x16_bf16 v[160:175],v[204:207],v[76:79],v[160:175]
v_rcp_f32 v179,v179
v_rcp_f32 v180,v180
v_rcp_f32 v181,v181
v_mfma_f32_32x32x16_bf16 v[160:175],v[208:211],v[80:83],v[160:175]
v_rcp_f32 v182,v182
v_rcp_f32 v183,v183
v_rcp_f32 v184,v184
v_mfma_f32_32x32x16_bf16 v[160:175],v[212:215],v[84:87],v[160:175]
v_rcp_f32 v185,v185
v_rcp_f32 v186,v186
v_rcp_f32 v187,v187
v_mfma_f32_32x32x16_bf16 v[160:175],v[216:219],v[88:91],v[160:175]
v_rcp_f32 v188,v188
v_rcp_f32 v189,v189
v_mfma_f32_32x32x16_bf16 v[160:175],v[220:223],v[92:95],v[160:175]
v_rcp_f32 v190,v190
v_rcp_f32 v191,v191
ds_read_b64_tr_b16 v[200:201],v17 offset:12288
ds_read_b64_tr_b16 v[202:203],v17 offset:13824
ds_read_b64_tr_b16 v[204:205],v17 offset:15360
ds_read_b64_tr_b16 v[206:207],v17 offset:16896
ds_read_b64_tr_b16 v[208:209],v17 offset:12352
ds_read_b64_tr_b16 v[210:211],v17 offset:13888
ds_read_b64_tr_b16 v[212:213],v17 offset:15424
ds_read_b64_tr_b16 v[214:215],v17 offset:16960
ds_read_b64_tr_b16 v[216:217],v17 offset:12416
ds_read_b64_tr_b16 v[218:219],v17 offset:13952
ds_read_b64_tr_b16 v[220:221],v17 offset:15488
ds_read_b64_tr_b16 v[222:223],v17 offset:17024
s_mov_b64 exec,s[60:61]
s_add_u32 m0,s58,136704
s_movk_i32 s64,320
buffer_load_dwordx4 v27,s[44:47],s64 offen lds
s_add_u32 m0,s58,137472
s_movk_i32 s65,9536
buffer_load_dwordx4 v27,s[44:47],s65 offen lds
s_mov_b64 exec,-1
s_setprio 0
v_mul_f32 v176,v144,v176
v_mul_f32 v177,v145,v177
v_mul_f32 v178,v146,v178
v_mul_f32 v179,v147,v179
v_mul_f32 v180,v148,v180
v_mul_f32 v181,v149,v181
v_mul_f32 v182,v150,v182
v_mul_f32 v183,v151,v183
v_mul_f32 v184,v152,v184
v_mul_f32 v185,v153,v185
v_mul_f32 v186,v154,v186
v_mul_f32 v187,v155,v187
v_mul_f32 v188,v156,v188
v_mul_f32 v189,v157,v189
v_mul_f32 v190,v158,v190
v_mul_f32 v191,v159,v191
v_cvt_pk_bf16_f32 v192,v176,v177
v_cvt_pk_bf16_f32 v193,v178,v179
v_cvt_pk_bf16_f32 v194,v180,v181
v_cvt_pk_bf16_f32 v195,v182,v183
v_cvt_pk_bf16_f32 v196,v184,v185
v_cvt_pk_bf16_f32 v197,v186,v187
v_cvt_pk_bf16_f32 v198,v188,v189
v_cvt_pk_bf16_f32 v199,v190,v191
s_waitcnt lgkmcnt(0)
s_waitcnt vmcnt(2)
s_barrier
v_fmaak_f32 v176,v160,v160,0x409d30fc
v_fmaak_f32 v177,v161,v161,0x409d30fc
v_fmaak_f32 v178,v162,v162,0x409d30fc
v_fmaak_f32 v179,v163,v163,0x409d30fc
v_fmaak_f32 v180,v164,v164,0x409d30fc
v_fmaak_f32 v181,v165,v165,0x409d30fc
v_fmaak_f32 v182,v166,v166,0x409d30fc
v_fmaak_f32 v183,v167,v167,0x409d30fc
v_fmaak_f32 v184,v168,v168,0x409d30fc
v_fmaak_f32 v185,v169,v169,0x409d30fc
v_fmaak_f32 v186,v170,v170,0x409d30fc
v_fmaak_f32 v187,v171,v171,0x409d30fc
v_fmaak_f32 v188,v172,v172,0x409d30fc
v_fmaak_f32 v189,v173,v173,0x409d30fc
v_fmaak_f32 v190,v174,v174,0x409d30fc
v_fmaak_f32 v191,v175,v175,0x409d30fc
v_mul_f32 v176,v160,v176
v_mul_f32 v177,v161,v177
v_mul_f32 v178,v162,v178
v_mul_f32 v179,v163,v179
v_mul_f32 v180,v164,v180
v_mul_f32 v181,v165,v181
v_mul_f32 v182,v166,v182
v_mul_f32 v183,v167,v183
v_mul_f32 v184,v168,v184
v_mul_f32 v185,v169,v185
v_mul_f32 v186,v170,v186
v_mul_f32 v187,v171,v187
v_mul_f32 v188,v172,v188
v_mul_f32 v189,v173,v189
v_mul_f32 v190,v174,v190
v_mul_f32 v191,v175,v191
s_setprio 1
v_mfma_f32_32x32x16_bf16 v[96:111],v[200:203],v[192:195],v[96:111]
v_exp_f32 v176,v176
v_exp_f32 v177,v177
v_exp_f32 v178,v178
v_mfma_f32_32x32x16_bf16 v[96:111],v[204:207],v[196:199],v[96:111]
v_exp_f32 v179,v179
v_exp_f32 v180,v180
v_exp_f32 v181,v181
v_mfma_f32_32x32x16_bf16 v[112:127],v[208:211],v[192:195],v[112:127]
v_exp_f32 v182,v182
v_exp_f32 v183,v183
v_exp_f32 v184,v184
v_mfma_f32_32x32x16_bf16 v[112:127],v[212:215],v[196:199],v[112:127]
v_exp_f32 v185,v185
v_exp_f32 v186,v186
v_exp_f32 v187,v187
v_mfma_f32_32x32x16_bf16 v[128:143],v[216:219],v[192:195],v[128:143]
v_exp_f32 v188,v188
v_exp_f32 v189,v189
v_mfma_f32_32x32x16_bf16 v[128:143],v[220:223],v[196:199],v[128:143]
v_exp_f32 v190,v190
v_exp_f32 v191,v191
ds_read_b64_tr_b16 v[200:201],v16 offset:6144
ds_read_b64_tr_b16 v[202:203],v16 offset:6656
ds_read_b64_tr_b16 v[204:205],v16 offset:7168
ds_read_b64_tr_b16 v[206:207],v16 offset:7680
ds_read_b64_tr_b16 v[208:209],v16 offset:8192
ds_read_b64_tr_b16 v[210:211],v16 offset:8704
ds_read_b64_tr_b16 v[212:213],v16 offset:9216
ds_read_b64_tr_b16 v[214:215],v16 offset:9728
ds_read_b64_tr_b16 v[216:217],v16 offset:10240
ds_read_b64_tr_b16 v[218:219],v16 offset:10752
ds_read_b64_tr_b16 v[220:221],v16 offset:11264
ds_read_b64_tr_b16 v[222:223],v16 offset:11776
ds_read_b128 v[144:147],v18 offset:512
ds_read_b128 v[148:151],v18 offset:544
ds_read_b128 v[152:155],v18 offset:576
ds_read_b128 v[156:159],v18 offset:608
s_mov_b64 exec,s[60:61]
s_add_u32 m0,s58,148992
s_mov_b32 s64,24576
buffer_load_dwordx4 v28,s[48:51],s64 offen lds
s_add_u32 m0,s58,149760
s_mov_b32 s65,25344
buffer_load_dwordx4 v28,s[48:51],s65 offen lds
s_mov_b64 exec,-1
s_setprio 0
v_add_f32 v176,1.0,v176
v_add_f32 v177,1.0,v177
v_add_f32 v178,1.0,v178
v_add_f32 v179,1.0,v179
v_add_f32 v180,1.0,v180
v_add_f32 v181,1.0,v181
v_add_f32 v182,1.0,v182
v_add_f32 v183,1.0,v183
v_add_f32 v184,1.0,v184
v_add_f32 v185,1.0,v185
v_add_f32 v186,1.0,v186
v_add_f32 v187,1.0,v187
v_add_f32 v188,1.0,v188
v_add_f32 v189,1.0,v189
v_add_f32 v190,1.0,v190
v_add_f32 v191,1.0,v191
s_setprio 1
s_waitcnt lgkmcnt(0)
v_mfma_f32_32x32x16_bf16 v[144:159],v[200:203],v[72:75],v[144:159]
v_rcp_f32 v176,v176
v_rcp_f32 v177,v177
v_rcp_f32 v178,v178
v_mfma_f32_32x32x16_bf16 v[144:159],v[204:207],v[76:79],v[144:159]
v_rcp_f32 v179,v179
v_rcp_f32 v180,v180
v_rcp_f32 v181,v181
v_mfma_f32_32x32x16_bf16 v[144:159],v[208:211],v[80:83],v[144:159]
v_rcp_f32 v182,v182
v_rcp_f32 v183,v183
v_rcp_f32 v184,v184
v_mfma_f32_32x32x16_bf16 v[144:159],v[212:215],v[84:87],v[144:159]
v_rcp_f32 v185,v185
v_rcp_f32 v186,v186
v_rcp_f32 v187,v187
v_mfma_f32_32x32x16_bf16 v[144:159],v[216:219],v[88:91],v[144:159]
v_rcp_f32 v188,v188
v_rcp_f32 v189,v189
v_mfma_f32_32x32x16_bf16 v[144:159],v[220:223],v[92:95],v[144:159]
v_rcp_f32 v190,v190
v_rcp_f32 v191,v191
ds_read_b64_tr_b16 v[200:201],v17 offset:0
ds_read_b64_tr_b16 v[202:203],v17 offset:1536
ds_read_b64_tr_b16 v[204:205],v17 offset:3072
ds_read_b64_tr_b16 v[206:207],v17 offset:4608
ds_read_b64_tr_b16 v[208:209],v17 offset:64
ds_read_b64_tr_b16 v[210:211],v17 offset:1600
ds_read_b64_tr_b16 v[212:213],v17 offset:3136
ds_read_b64_tr_b16 v[214:215],v17 offset:4672
ds_read_b64_tr_b16 v[216:217],v17 offset:128
ds_read_b64_tr_b16 v[218:219],v17 offset:1664
ds_read_b64_tr_b16 v[220:221],v17 offset:3200
ds_read_b64_tr_b16 v[222:223],v17 offset:4736
s_mov_b64 exec,s[60:61]
s_add_u32 m0,s58,124416
s_movk_i32 s64,384
buffer_load_dwordx4 v27,s[44:47],s64 offen lds
s_add_u32 m0,s58,125184
s_movk_i32 s65,9600
buffer_load_dwordx4 v27,s[44:47],s65 offen lds
s_mov_b64 exec,-1
s_setprio 0
v_mul_f32 v176,v160,v176
v_mul_f32 v177,v161,v177
v_mul_f32 v178,v162,v178
v_mul_f32 v179,v163,v179
v_mul_f32 v180,v164,v180
v_mul_f32 v181,v165,v181
v_mul_f32 v182,v166,v182
v_mul_f32 v183,v167,v183
v_mul_f32 v184,v168,v184
v_mul_f32 v185,v169,v185
v_mul_f32 v186,v170,v186
v_mul_f32 v187,v171,v187
v_mul_f32 v188,v172,v188
v_mul_f32 v189,v173,v189
v_mul_f32 v190,v174,v190
v_mul_f32 v191,v175,v191
v_cvt_pk_bf16_f32 v192,v176,v177
v_cvt_pk_bf16_f32 v193,v178,v179
v_cvt_pk_bf16_f32 v194,v180,v181
v_cvt_pk_bf16_f32 v195,v182,v183
v_cvt_pk_bf16_f32 v196,v184,v185
v_cvt_pk_bf16_f32 v197,v186,v187
v_cvt_pk_bf16_f32 v198,v188,v189
v_cvt_pk_bf16_f32 v199,v190,v191
s_waitcnt lgkmcnt(0)
s_waitcnt vmcnt(2)
s_barrier
v_fmaak_f32 v176,v144,v144,0x409d30fc
v_fmaak_f32 v177,v145,v145,0x409d30fc
v_fmaak_f32 v178,v146,v146,0x409d30fc
v_fmaak_f32 v179,v147,v147,0x409d30fc
v_fmaak_f32 v180,v148,v148,0x409d30fc
v_fmaak_f32 v181,v149,v149,0x409d30fc
v_fmaak_f32 v182,v150,v150,0x409d30fc
v_fmaak_f32 v183,v151,v151,0x409d30fc
v_fmaak_f32 v184,v152,v152,0x409d30fc
v_fmaak_f32 v185,v153,v153,0x409d30fc
v_fmaak_f32 v186,v154,v154,0x409d30fc
v_fmaak_f32 v187,v155,v155,0x409d30fc
v_fmaak_f32 v188,v156,v156,0x409d30fc
v_fmaak_f32 v189,v157,v157,0x409d30fc
v_fmaak_f32 v190,v158,v158,0x409d30fc
v_fmaak_f32 v191,v159,v159,0x409d30fc
v_mul_f32 v176,v144,v176
v_mul_f32 v177,v145,v177
v_mul_f32 v178,v146,v178
v_mul_f32 v179,v147,v179
v_mul_f32 v180,v148,v180
v_mul_f32 v181,v149,v181
v_mul_f32 v182,v150,v182
v_mul_f32 v183,v151,v183
v_mul_f32 v184,v152,v184
v_mul_f32 v185,v153,v185
v_mul_f32 v186,v154,v186
v_mul_f32 v187,v155,v187
v_mul_f32 v188,v156,v188
v_mul_f32 v189,v157,v189
v_mul_f32 v190,v158,v190
v_mul_f32 v191,v159,v191
s_setprio 1
v_mfma_f32_32x32x16_bf16 v[96:111],v[200:203],v[192:195],v[96:111]
v_exp_f32 v176,v176
v_exp_f32 v177,v177
v_exp_f32 v178,v178
v_mfma_f32_32x32x16_bf16 v[96:111],v[204:207],v[196:199],v[96:111]
v_exp_f32 v179,v179
v_exp_f32 v180,v180
v_exp_f32 v181,v181
v_mfma_f32_32x32x16_bf16 v[112:127],v[208:211],v[192:195],v[112:127]
v_exp_f32 v182,v182
v_exp_f32 v183,v183
v_exp_f32 v184,v184
v_mfma_f32_32x32x16_bf16 v[112:127],v[212:215],v[196:199],v[112:127]
v_exp_f32 v185,v185
v_exp_f32 v186,v186
v_exp_f32 v187,v187
v_mfma_f32_32x32x16_bf16 v[128:143],v[216:219],v[192:195],v[128:143]
v_exp_f32 v188,v188
v_exp_f32 v189,v189
v_mfma_f32_32x32x16_bf16 v[128:143],v[220:223],v[196:199],v[128:143]
v_exp_f32 v190,v190
v_exp_f32 v191,v191
ds_read_b64_tr_b16 v[200:201],v16 offset:12288
ds_read_b64_tr_b16 v[202:203],v16 offset:12800
ds_read_b64_tr_b16 v[204:205],v16 offset:13312
ds_read_b64_tr_b16 v[206:207],v16 offset:13824
ds_read_b64_tr_b16 v[208:209],v16 offset:14336
ds_read_b64_tr_b16 v[210:211],v16 offset:14848
ds_read_b64_tr_b16 v[212:213],v16 offset:15360
ds_read_b64_tr_b16 v[214:215],v16 offset:15872
ds_read_b64_tr_b16 v[216:217],v16 offset:16384
ds_read_b64_tr_b16 v[218:219],v16 offset:16896
ds_read_b64_tr_b16 v[220:221],v16 offset:17408
ds_read_b64_tr_b16 v[222:223],v16 offset:17920
ds_read_b128 v[160:163],v18 offset:640
ds_read_b128 v[164:167],v18 offset:672
ds_read_b128 v[168:171],v18 offset:704
ds_read_b128 v[172:175],v18 offset:736
s_mov_b64 exec,s[60:61]
s_add_u32 m0,s58,155136
s_mov_b32 s64,30720
buffer_load_dwordx4 v28,s[48:51],s64 offen lds
s_add_u32 m0,s58,155904
s_mov_b32 s65,31488
buffer_load_dwordx4 v28,s[48:51],s65 offen lds
s_mov_b64 exec,-1
s_setprio 0
v_add_f32 v176,1.0,v176
v_add_f32 v177,1.0,v177
v_add_f32 v178,1.0,v178
v_add_f32 v179,1.0,v179
v_add_f32 v180,1.0,v180
v_add_f32 v181,1.0,v181
v_add_f32 v182,1.0,v182
v_add_f32 v183,1.0,v183
v_add_f32 v184,1.0,v184
v_add_f32 v185,1.0,v185
v_add_f32 v186,1.0,v186
v_add_f32 v187,1.0,v187
v_add_f32 v188,1.0,v188
v_add_f32 v189,1.0,v189
v_add_f32 v190,1.0,v190
v_add_f32 v191,1.0,v191
s_setprio 1
s_waitcnt lgkmcnt(0)
v_mfma_f32_32x32x16_bf16 v[160:175],v[200:203],v[72:75],v[160:175]
v_rcp_f32 v176,v176
v_rcp_f32 v177,v177
v_rcp_f32 v178,v178
v_mfma_f32_32x32x16_bf16 v[160:175],v[204:207],v[76:79],v[160:175]
v_rcp_f32 v179,v179
v_rcp_f32 v180,v180
v_rcp_f32 v181,v181
v_mfma_f32_32x32x16_bf16 v[160:175],v[208:211],v[80:83],v[160:175]
v_rcp_f32 v182,v182
v_rcp_f32 v183,v183
v_rcp_f32 v184,v184
v_mfma_f32_32x32x16_bf16 v[160:175],v[212:215],v[84:87],v[160:175]
v_rcp_f32 v185,v185
v_rcp_f32 v186,v186
v_rcp_f32 v187,v187
v_mfma_f32_32x32x16_bf16 v[160:175],v[216:219],v[88:91],v[160:175]
v_rcp_f32 v188,v188
v_rcp_f32 v189,v189
v_mfma_f32_32x32x16_bf16 v[160:175],v[220:223],v[92:95],v[160:175]
v_rcp_f32 v190,v190
v_rcp_f32 v191,v191
ds_read_b64_tr_b16 v[200:201],v17 offset:6144
ds_read_b64_tr_b16 v[202:203],v17 offset:7680
ds_read_b64_tr_b16 v[204:205],v17 offset:9216
ds_read_b64_tr_b16 v[206:207],v17 offset:10752
ds_read_b64_tr_b16 v[208:209],v17 offset:6208
ds_read_b64_tr_b16 v[210:211],v17 offset:7744
ds_read_b64_tr_b16 v[212:213],v17 offset:9280
ds_read_b64_tr_b16 v[214:215],v17 offset:10816
ds_read_b64_tr_b16 v[216:217],v17 offset:6272
ds_read_b64_tr_b16 v[218:219],v17 offset:7808
ds_read_b64_tr_b16 v[220:221],v17 offset:9344
ds_read_b64_tr_b16 v[222:223],v17 offset:10880
s_mov_b64 exec,s[60:61]
s_add_u32 m0,s58,130560
s_movk_i32 s64,448
buffer_load_dwordx4 v27,s[44:47],s64 offen lds
s_add_u32 m0,s58,131328
s_movk_i32 s65,9664
buffer_load_dwordx4 v27,s[44:47],s65 offen lds
s_mov_b64 exec,-1
s_setprio 0
v_mul_f32 v176,v144,v176
v_mul_f32 v177,v145,v177
v_mul_f32 v178,v146,v178
v_mul_f32 v179,v147,v179
v_mul_f32 v180,v148,v180
v_mul_f32 v181,v149,v181
v_mul_f32 v182,v150,v182
v_mul_f32 v183,v151,v183
v_mul_f32 v184,v152,v184
v_mul_f32 v185,v153,v185
v_mul_f32 v186,v154,v186
v_mul_f32 v187,v155,v187
v_mul_f32 v188,v156,v188
v_mul_f32 v189,v157,v189
v_mul_f32 v190,v158,v190
v_mul_f32 v191,v159,v191
v_cvt_pk_bf16_f32 v192,v176,v177
v_cvt_pk_bf16_f32 v193,v178,v179
v_cvt_pk_bf16_f32 v194,v180,v181
v_cvt_pk_bf16_f32 v195,v182,v183
v_cvt_pk_bf16_f32 v196,v184,v185
v_cvt_pk_bf16_f32 v197,v186,v187
v_cvt_pk_bf16_f32 v198,v188,v189
v_cvt_pk_bf16_f32 v199,v190,v191
s_waitcnt lgkmcnt(0)
s_waitcnt vmcnt(2)
s_barrier
v_fmaak_f32 v176,v160,v160,0x409d30fc
v_fmaak_f32 v177,v161,v161,0x409d30fc
v_fmaak_f32 v178,v162,v162,0x409d30fc
v_fmaak_f32 v179,v163,v163,0x409d30fc
v_fmaak_f32 v180,v164,v164,0x409d30fc
v_fmaak_f32 v181,v165,v165,0x409d30fc
v_fmaak_f32 v182,v166,v166,0x409d30fc
v_fmaak_f32 v183,v167,v167,0x409d30fc
v_fmaak_f32 v184,v168,v168,0x409d30fc
v_fmaak_f32 v185,v169,v169,0x409d30fc
v_fmaak_f32 v186,v170,v170,0x409d30fc
v_fmaak_f32 v187,v171,v171,0x409d30fc
v_fmaak_f32 v188,v172,v172,0x409d30fc
v_fmaak_f32 v189,v173,v173,0x409d30fc
v_fmaak_f32 v190,v174,v174,0x409d30fc
v_fmaak_f32 v191,v175,v175,0x409d30fc
v_mul_f32 v176,v160,v176
v_mul_f32 v177,v161,v177
v_mul_f32 v178,v162,v178
v_mul_f32 v179,v163,v179
v_mul_f32 v180,v164,v180
v_mul_f32 v181,v165,v181
v_mul_f32 v182,v166,v182
v_mul_f32 v183,v167,v183
v_mul_f32 v184,v168,v184
v_mul_f32 v185,v169,v185
v_mul_f32 v186,v170,v186
v_mul_f32 v187,v171,v187
v_mul_f32 v188,v172,v188
v_mul_f32 v189,v173,v189
v_mul_f32 v190,v174,v190
v_mul_f32 v191,v175,v191
s_setprio 1
v_mfma_f32_32x32x16_bf16 v[96:111],v[200:203],v[192:195],v[96:111]
v_exp_f32 v176,v176
v_exp_f32 v177,v177
v_exp_f32 v178,v178
v_mfma_f32_32x32x16_bf16 v[96:111],v[204:207],v[196:199],v[96:111]
v_exp_f32 v179,v179
v_exp_f32 v180,v180
v_exp_f32 v181,v181
v_mfma_f32_32x32x16_bf16 v[112:127],v[208:211],v[192:195],v[112:127]
v_exp_f32 v182,v182
v_exp_f32 v183,v183
v_exp_f32 v184,v184
v_mfma_f32_32x32x16_bf16 v[112:127],v[212:215],v[196:199],v[112:127]
v_exp_f32 v185,v185
v_exp_f32 v186,v186
v_exp_f32 v187,v187
v_mfma_f32_32x32x16_bf16 v[128:143],v[216:219],v[192:195],v[128:143]
v_exp_f32 v188,v188
v_exp_f32 v189,v189
v_mfma_f32_32x32x16_bf16 v[128:143],v[220:223],v[196:199],v[128:143]
v_exp_f32 v190,v190
v_exp_f32 v191,v191
ds_read_b64_tr_b16 v[200:201],v16 offset:0
ds_read_b64_tr_b16 v[202:203],v16 offset:512
ds_read_b64_tr_b16 v[204:205],v16 offset:1024
ds_read_b64_tr_b16 v[206:207],v16 offset:1536
ds_read_b64_tr_b16 v[208:209],v16 offset:2048
ds_read_b64_tr_b16 v[210:211],v16 offset:2560
ds_read_b64_tr_b16 v[212:213],v16 offset:3072
ds_read_b64_tr_b16 v[214:215],v16 offset:3584
ds_read_b64_tr_b16 v[216:217],v16 offset:4096
ds_read_b64_tr_b16 v[218:219],v16 offset:4608
ds_read_b64_tr_b16 v[220:221],v16 offset:5120
ds_read_b64_tr_b16 v[222:223],v16 offset:5632
ds_read_b128 v[144:147],v18 offset:768
ds_read_b128 v[148:151],v18 offset:800
ds_read_b128 v[152:155],v18 offset:832
ds_read_b128 v[156:159],v18 offset:864
s_mov_b64 exec,s[60:61]
s_add_u32 m0,s58,142848
s_mov_b32 s64,36864
buffer_load_dwordx4 v28,s[48:51],s64 offen lds
s_add_u32 m0,s58,143616
s_mov_b32 s65,37632
buffer_load_dwordx4 v28,s[48:51],s65 offen lds
s_mov_b64 exec,-1
s_setprio 0
v_add_f32 v176,1.0,v176
v_add_f32 v177,1.0,v177
v_add_f32 v178,1.0,v178
v_add_f32 v179,1.0,v179
v_add_f32 v180,1.0,v180
v_add_f32 v181,1.0,v181
v_add_f32 v182,1.0,v182
v_add_f32 v183,1.0,v183
v_add_f32 v184,1.0,v184
v_add_f32 v185,1.0,v185
v_add_f32 v186,1.0,v186
v_add_f32 v187,1.0,v187
v_add_f32 v188,1.0,v188
v_add_f32 v189,1.0,v189
v_add_f32 v190,1.0,v190
v_add_f32 v191,1.0,v191
s_setprio 1
s_waitcnt lgkmcnt(0)
v_mfma_f32_32x32x16_bf16 v[144:159],v[200:203],v[72:75],v[144:159]
v_rcp_f32 v176,v176
v_rcp_f32 v177,v177
v_rcp_f32 v178,v178
v_mfma_f32_32x32x16_bf16 v[144:159],v[204:207],v[76:79],v[144:159]
v_rcp_f32 v179,v179
v_rcp_f32 v180,v180
v_rcp_f32 v181,v181
v_mfma_f32_32x32x16_bf16 v[144:159],v[208:211],v[80:83],v[144:159]
v_rcp_f32 v182,v182
v_rcp_f32 v183,v183
v_rcp_f32 v184,v184
v_mfma_f32_32x32x16_bf16 v[144:159],v[212:215],v[84:87],v[144:159]
v_rcp_f32 v185,v185
v_rcp_f32 v186,v186
v_rcp_f32 v187,v187
v_mfma_f32_32x32x16_bf16 v[144:159],v[216:219],v[88:91],v[144:159]
v_rcp_f32 v188,v188
v_rcp_f32 v189,v189
v_mfma_f32_32x32x16_bf16 v[144:159],v[220:223],v[92:95],v[144:159]
v_rcp_f32 v190,v190
v_rcp_f32 v191,v191
ds_read_b64_tr_b16 v[200:201],v17 offset:12288
ds_read_b64_tr_b16 v[202:203],v17 offset:13824
ds_read_b64_tr_b16 v[204:205],v17 offset:15360
ds_read_b64_tr_b16 v[206:207],v17 offset:16896
ds_read_b64_tr_b16 v[208:209],v17 offset:12352
ds_read_b64_tr_b16 v[210:211],v17 offset:13888
ds_read_b64_tr_b16 v[212:213],v17 offset:15424
ds_read_b64_tr_b16 v[214:215],v17 offset:16960
ds_read_b64_tr_b16 v[216:217],v17 offset:12416
ds_read_b64_tr_b16 v[218:219],v17 offset:13952
ds_read_b64_tr_b16 v[220:221],v17 offset:15488
ds_read_b64_tr_b16 v[222:223],v17 offset:17024
s_mov_b64 exec,s[60:61]
s_add_u32 m0,s58,136704
s_movk_i32 s64,512
buffer_load_dwordx4 v27,s[44:47],s64 offen lds
s_add_u32 m0,s58,137472
s_movk_i32 s65,9728
buffer_load_dwordx4 v27,s[44:47],s65 offen lds
s_mov_b64 exec,-1
s_setprio 0
v_mul_f32 v176,v160,v176
v_mul_f32 v177,v161,v177
v_mul_f32 v178,v162,v178
v_mul_f32 v179,v163,v179
v_mul_f32 v180,v164,v180
v_mul_f32 v181,v165,v181
v_mul_f32 v182,v166,v182
v_mul_f32 v183,v167,v183
v_mul_f32 v184,v168,v184
v_mul_f32 v185,v169,v185
v_mul_f32 v186,v170,v186
v_mul_f32 v187,v171,v187
v_mul_f32 v188,v172,v188
v_mul_f32 v189,v173,v189
v_mul_f32 v190,v174,v190
v_mul_f32 v191,v175,v191
v_cvt_pk_bf16_f32 v192,v176,v177
v_cvt_pk_bf16_f32 v193,v178,v179
v_cvt_pk_bf16_f32 v194,v180,v181
v_cvt_pk_bf16_f32 v195,v182,v183
v_cvt_pk_bf16_f32 v196,v184,v185
v_cvt_pk_bf16_f32 v197,v186,v187
v_cvt_pk_bf16_f32 v198,v188,v189
v_cvt_pk_bf16_f32 v199,v190,v191
s_waitcnt lgkmcnt(0)
s_waitcnt vmcnt(2)
s_barrier
v_fmaak_f32 v176,v144,v144,0x409d30fc
v_fmaak_f32 v177,v145,v145,0x409d30fc
v_fmaak_f32 v178,v146,v146,0x409d30fc
v_fmaak_f32 v179,v147,v147,0x409d30fc
v_fmaak_f32 v180,v148,v148,0x409d30fc
v_fmaak_f32 v181,v149,v149,0x409d30fc
v_fmaak_f32 v182,v150,v150,0x409d30fc
v_fmaak_f32 v183,v151,v151,0x409d30fc
v_fmaak_f32 v184,v152,v152,0x409d30fc
v_fmaak_f32 v185,v153,v153,0x409d30fc
v_fmaak_f32 v186,v154,v154,0x409d30fc
v_fmaak_f32 v187,v155,v155,0x409d30fc
v_fmaak_f32 v188,v156,v156,0x409d30fc
v_fmaak_f32 v189,v157,v157,0x409d30fc
v_fmaak_f32 v190,v158,v158,0x409d30fc
v_fmaak_f32 v191,v159,v159,0x409d30fc
v_mul_f32 v176,v144,v176
v_mul_f32 v177,v145,v177
v_mul_f32 v178,v146,v178
v_mul_f32 v179,v147,v179
v_mul_f32 v180,v148,v180
v_mul_f32 v181,v149,v181
v_mul_f32 v182,v150,v182
v_mul_f32 v183,v151,v183
v_mul_f32 v184,v152,v184
v_mul_f32 v185,v153,v185
v_mul_f32 v186,v154,v186
v_mul_f32 v187,v155,v187
v_mul_f32 v188,v156,v188
v_mul_f32 v189,v157,v189
v_mul_f32 v190,v158,v190
v_mul_f32 v191,v159,v191
s_setprio 1
v_mfma_f32_32x32x16_bf16 v[96:111],v[200:203],v[192:195],v[96:111]
v_exp_f32 v176,v176
v_exp_f32 v177,v177
v_exp_f32 v178,v178
v_mfma_f32_32x32x16_bf16 v[96:111],v[204:207],v[196:199],v[96:111]
v_exp_f32 v179,v179
v_exp_f32 v180,v180
v_exp_f32 v181,v181
v_mfma_f32_32x32x16_bf16 v[112:127],v[208:211],v[192:195],v[112:127]
v_exp_f32 v182,v182
v_exp_f32 v183,v183
v_exp_f32 v184,v184
v_mfma_f32_32x32x16_bf16 v[112:127],v[212:215],v[196:199],v[112:127]
v_exp_f32 v185,v185
v_exp_f32 v186,v186
v_exp_f32 v187,v187
v_mfma_f32_32x32x16_bf16 v[128:143],v[216:219],v[192:195],v[128:143]
v_exp_f32 v188,v188
v_exp_f32 v189,v189
v_mfma_f32_32x32x16_bf16 v[128:143],v[220:223],v[196:199],v[128:143]
v_exp_f32 v190,v190
v_exp_f32 v191,v191
ds_read_b64_tr_b16 v[200:201],v16 offset:6144
ds_read_b64_tr_b16 v[202:203],v16 offset:6656
ds_read_b64_tr_b16 v[204:205],v16 offset:7168
ds_read_b64_tr_b16 v[206:207],v16 offset:7680
ds_read_b64_tr_b16 v[208:209],v16 offset:8192
ds_read_b64_tr_b16 v[210:211],v16 offset:8704
ds_read_b64_tr_b16 v[212:213],v16 offset:9216
ds_read_b64_tr_b16 v[214:215],v16 offset:9728
ds_read_b64_tr_b16 v[216:217],v16 offset:10240
ds_read_b64_tr_b16 v[218:219],v16 offset:10752
ds_read_b64_tr_b16 v[220:221],v16 offset:11264
ds_read_b64_tr_b16 v[222:223],v16 offset:11776
ds_read_b128 v[160:163],v18 offset:896
ds_read_b128 v[164:167],v18 offset:928
ds_read_b128 v[168:171],v18 offset:960
ds_read_b128 v[172:175],v18 offset:992
s_mov_b64 exec,s[60:61]
s_add_u32 m0,s58,148992
s_mov_b32 s64,43008
buffer_load_dwordx4 v28,s[48:51],s64 offen lds
s_add_u32 m0,s58,149760
s_mov_b32 s65,43776
buffer_load_dwordx4 v28,s[48:51],s65 offen lds
s_mov_b64 exec,-1
s_setprio 0
v_add_f32 v176,1.0,v176
v_add_f32 v177,1.0,v177
v_add_f32 v178,1.0,v178
v_add_f32 v179,1.0,v179
v_add_f32 v180,1.0,v180
v_add_f32 v181,1.0,v181
v_add_f32 v182,1.0,v182
v_add_f32 v183,1.0,v183
v_add_f32 v184,1.0,v184
v_add_f32 v185,1.0,v185
v_add_f32 v186,1.0,v186
v_add_f32 v187,1.0,v187
v_add_f32 v188,1.0,v188
v_add_f32 v189,1.0,v189
v_add_f32 v190,1.0,v190
v_add_f32 v191,1.0,v191
s_setprio 1
s_waitcnt lgkmcnt(0)
v_mfma_f32_32x32x16_bf16 v[160:175],v[200:203],v[72:75],v[160:175]
v_rcp_f32 v176,v176
v_rcp_f32 v177,v177
v_rcp_f32 v178,v178
v_mfma_f32_32x32x16_bf16 v[160:175],v[204:207],v[76:79],v[160:175]
v_rcp_f32 v179,v179
v_rcp_f32 v180,v180
v_rcp_f32 v181,v181
v_mfma_f32_32x32x16_bf16 v[160:175],v[208:211],v[80:83],v[160:175]
v_rcp_f32 v182,v182
v_rcp_f32 v183,v183
v_rcp_f32 v184,v184
v_mfma_f32_32x32x16_bf16 v[160:175],v[212:215],v[84:87],v[160:175]
v_rcp_f32 v185,v185
v_rcp_f32 v186,v186
v_rcp_f32 v187,v187
v_mfma_f32_32x32x16_bf16 v[160:175],v[216:219],v[88:91],v[160:175]
v_rcp_f32 v188,v188
v_rcp_f32 v189,v189
v_mfma_f32_32x32x16_bf16 v[160:175],v[220:223],v[92:95],v[160:175]
v_rcp_f32 v190,v190
v_rcp_f32 v191,v191
ds_read_b64_tr_b16 v[200:201],v17 offset:0
ds_read_b64_tr_b16 v[202:203],v17 offset:1536
ds_read_b64_tr_b16 v[204:205],v17 offset:3072
ds_read_b64_tr_b16 v[206:207],v17 offset:4608
ds_read_b64_tr_b16 v[208:209],v17 offset:64
ds_read_b64_tr_b16 v[210:211],v17 offset:1600
ds_read_b64_tr_b16 v[212:213],v17 offset:3136
ds_read_b64_tr_b16 v[214:215],v17 offset:4672
ds_read_b64_tr_b16 v[216:217],v17 offset:128
ds_read_b64_tr_b16 v[218:219],v17 offset:1664
ds_read_b64_tr_b16 v[220:221],v17 offset:3200
ds_read_b64_tr_b16 v[222:223],v17 offset:4736
s_mov_b64 exec,s[60:61]
s_add_u32 m0,s58,124416
s_movk_i32 s64,576
buffer_load_dwordx4 v27,s[44:47],s64 offen lds
s_add_u32 m0,s58,125184
s_movk_i32 s65,9792
buffer_load_dwordx4 v27,s[44:47],s65 offen lds
s_mov_b64 exec,-1
s_setprio 0
v_mul_f32 v176,v144,v176
v_mul_f32 v177,v145,v177
v_mul_f32 v178,v146,v178
v_mul_f32 v179,v147,v179
v_mul_f32 v180,v148,v180
v_mul_f32 v181,v149,v181
v_mul_f32 v182,v150,v182
v_mul_f32 v183,v151,v183
v_mul_f32 v184,v152,v184
v_mul_f32 v185,v153,v185
v_mul_f32 v186,v154,v186
v_mul_f32 v187,v155,v187
v_mul_f32 v188,v156,v188
v_mul_f32 v189,v157,v189
v_mul_f32 v190,v158,v190
v_mul_f32 v191,v159,v191
v_cvt_pk_bf16_f32 v192,v176,v177
v_cvt_pk_bf16_f32 v193,v178,v179
v_cvt_pk_bf16_f32 v194,v180,v181
v_cvt_pk_bf16_f32 v195,v182,v183
v_cvt_pk_bf16_f32 v196,v184,v185
v_cvt_pk_bf16_f32 v197,v186,v187
v_cvt_pk_bf16_f32 v198,v188,v189
v_cvt_pk_bf16_f32 v199,v190,v191
s_waitcnt lgkmcnt(0)
s_waitcnt vmcnt(2)
s_barrier
v_fmaak_f32 v176,v160,v160,0x409d30fc
v_fmaak_f32 v177,v161,v161,0x409d30fc
v_fmaak_f32 v178,v162,v162,0x409d30fc
v_fmaak_f32 v179,v163,v163,0x409d30fc
v_fmaak_f32 v180,v164,v164,0x409d30fc
v_fmaak_f32 v181,v165,v165,0x409d30fc
v_fmaak_f32 v182,v166,v166,0x409d30fc
v_fmaak_f32 v183,v167,v167,0x409d30fc
v_fmaak_f32 v184,v168,v168,0x409d30fc
v_fmaak_f32 v185,v169,v169,0x409d30fc
v_fmaak_f32 v186,v170,v170,0x409d30fc
v_fmaak_f32 v187,v171,v171,0x409d30fc
v_fmaak_f32 v188,v172,v172,0x409d30fc
v_fmaak_f32 v189,v173,v173,0x409d30fc
v_fmaak_f32 v190,v174,v174,0x409d30fc
v_fmaak_f32 v191,v175,v175,0x409d30fc
v_mul_f32 v176,v160,v176
v_mul_f32 v177,v161,v177
v_mul_f32 v178,v162,v178
v_mul_f32 v179,v163,v179
v_mul_f32 v180,v164,v180
v_mul_f32 v181,v165,v181
v_mul_f32 v182,v166,v182
v_mul_f32 v183,v167,v183
v_mul_f32 v184,v168,v184
v_mul_f32 v185,v169,v185
v_mul_f32 v186,v170,v186
v_mul_f32 v187,v171,v187
v_mul_f32 v188,v172,v188
v_mul_f32 v189,v173,v189
v_mul_f32 v190,v174,v190
v_mul_f32 v191,v175,v191
s_setprio 1
v_mfma_f32_32x32x16_bf16 v[96:111],v[200:203],v[192:195],v[96:111]
v_exp_f32 v176,v176
v_exp_f32 v177,v177
v_exp_f32 v178,v178
v_mfma_f32_32x32x16_bf16 v[96:111],v[204:207],v[196:199],v[96:111]
v_exp_f32 v179,v179
v_exp_f32 v180,v180
v_exp_f32 v181,v181
v_mfma_f32_32x32x16_bf16 v[112:127],v[208:211],v[192:195],v[112:127]
v_exp_f32 v182,v182
v_exp_f32 v183,v183
v_exp_f32 v184,v184
v_mfma_f32_32x32x16_bf16 v[112:127],v[212:215],v[196:199],v[112:127]
v_exp_f32 v185,v185
v_exp_f32 v186,v186
v_exp_f32 v187,v187
v_mfma_f32_32x32x16_bf16 v[128:143],v[216:219],v[192:195],v[128:143]
v_exp_f32 v188,v188
v_exp_f32 v189,v189
v_mfma_f32_32x32x16_bf16 v[128:143],v[220:223],v[196:199],v[128:143]
v_exp_f32 v190,v190
v_exp_f32 v191,v191
ds_read_b64_tr_b16 v[200:201],v16 offset:12288
ds_read_b64_tr_b16 v[202:203],v16 offset:12800
ds_read_b64_tr_b16 v[204:205],v16 offset:13312
ds_read_b64_tr_b16 v[206:207],v16 offset:13824
ds_read_b64_tr_b16 v[208:209],v16 offset:14336
ds_read_b64_tr_b16 v[210:211],v16 offset:14848
ds_read_b64_tr_b16 v[212:213],v16 offset:15360
ds_read_b64_tr_b16 v[214:215],v16 offset:15872
ds_read_b64_tr_b16 v[216:217],v16 offset:16384
ds_read_b64_tr_b16 v[218:219],v16 offset:16896
ds_read_b64_tr_b16 v[220:221],v16 offset:17408
ds_read_b64_tr_b16 v[222:223],v16 offset:17920
ds_read_b128 v[144:147],v18 offset:1024
ds_read_b128 v[148:151],v18 offset:1056
ds_read_b128 v[152:155],v18 offset:1088
ds_read_b128 v[156:159],v18 offset:1120
s_mov_b64 exec,s[60:61]
s_add_u32 m0,s58,155136
s_mov_b32 s64,49152
buffer_load_dwordx4 v28,s[48:51],s64 offen lds
s_add_u32 m0,s58,155904
s_mov_b32 s65,49920
buffer_load_dwordx4 v28,s[48:51],s65 offen lds
s_mov_b64 exec,-1
s_setprio 0
v_add_f32 v176,1.0,v176
v_add_f32 v177,1.0,v177
v_add_f32 v178,1.0,v178
v_add_f32 v179,1.0,v179
v_add_f32 v180,1.0,v180
v_add_f32 v181,1.0,v181
v_add_f32 v182,1.0,v182
v_add_f32 v183,1.0,v183
v_add_f32 v184,1.0,v184
v_add_f32 v185,1.0,v185
v_add_f32 v186,1.0,v186
v_add_f32 v187,1.0,v187
v_add_f32 v188,1.0,v188
v_add_f32 v189,1.0,v189
v_add_f32 v190,1.0,v190
v_add_f32 v191,1.0,v191
s_setprio 1
s_waitcnt lgkmcnt(0)
v_mfma_f32_32x32x16_bf16 v[144:159],v[200:203],v[72:75],v[144:159]
v_rcp_f32 v176,v176
v_rcp_f32 v177,v177
v_rcp_f32 v178,v178
v_mfma_f32_32x32x16_bf16 v[144:159],v[204:207],v[76:79],v[144:159]
v_rcp_f32 v179,v179
v_rcp_f32 v180,v180
v_rcp_f32 v181,v181
v_mfma_f32_32x32x16_bf16 v[144:159],v[208:211],v[80:83],v[144:159]
v_rcp_f32 v182,v182
v_rcp_f32 v183,v183
v_rcp_f32 v184,v184
v_mfma_f32_32x32x16_bf16 v[144:159],v[212:215],v[84:87],v[144:159]
v_rcp_f32 v185,v185
v_rcp_f32 v186,v186
v_rcp_f32 v187,v187
v_mfma_f32_32x32x16_bf16 v[144:159],v[216:219],v[88:91],v[144:159]
v_rcp_f32 v188,v188
v_rcp_f32 v189,v189
v_mfma_f32_32x32x16_bf16 v[144:159],v[220:223],v[92:95],v[144:159]
v_rcp_f32 v190,v190
v_rcp_f32 v191,v191
ds_read_b64_tr_b16 v[200:201],v17 offset:6144
ds_read_b64_tr_b16 v[202:203],v17 offset:7680
ds_read_b64_tr_b16 v[204:205],v17 offset:9216
ds_read_b64_tr_b16 v[206:207],v17 offset:10752
ds_read_b64_tr_b16 v[208:209],v17 offset:6208
ds_read_b64_tr_b16 v[210:211],v17 offset:7744
ds_read_b64_tr_b16 v[212:213],v17 offset:9280
ds_read_b64_tr_b16 v[214:215],v17 offset:10816
ds_read_b64_tr_b16 v[216:217],v17 offset:6272
ds_read_b64_tr_b16 v[218:219],v17 offset:7808
ds_read_b64_tr_b16 v[220:221],v17 offset:9344
ds_read_b64_tr_b16 v[222:223],v17 offset:10880
s_mov_b64 exec,s[60:61]
s_add_u32 m0,s58,130560
s_movk_i32 s64,640
buffer_load_dwordx4 v27,s[44:47],s64 offen lds
s_add_u32 m0,s58,131328
s_movk_i32 s65,9856
buffer_load_dwordx4 v27,s[44:47],s65 offen lds
s_mov_b64 exec,-1
s_setprio 0
v_mul_f32 v176,v160,v176
v_mul_f32 v177,v161,v177
v_mul_f32 v178,v162,v178
v_mul_f32 v179,v163,v179
v_mul_f32 v180,v164,v180
v_mul_f32 v181,v165,v181
v_mul_f32 v182,v166,v182
v_mul_f32 v183,v167,v183
v_mul_f32 v184,v168,v184
v_mul_f32 v185,v169,v185
v_mul_f32 v186,v170,v186
v_mul_f32 v187,v171,v187
v_mul_f32 v188,v172,v188
v_mul_f32 v189,v173,v189
v_mul_f32 v190,v174,v190
v_mul_f32 v191,v175,v191
v_cvt_pk_bf16_f32 v192,v176,v177
v_cvt_pk_bf16_f32 v193,v178,v179
v_cvt_pk_bf16_f32 v194,v180,v181
v_cvt_pk_bf16_f32 v195,v182,v183
v_cvt_pk_bf16_f32 v196,v184,v185
v_cvt_pk_bf16_f32 v197,v186,v187
v_cvt_pk_bf16_f32 v198,v188,v189
v_cvt_pk_bf16_f32 v199,v190,v191
s_waitcnt lgkmcnt(0)
s_waitcnt vmcnt(2)
s_barrier
v_fmaak_f32 v176,v144,v144,0x409d30fc
v_fmaak_f32 v177,v145,v145,0x409d30fc
v_fmaak_f32 v178,v146,v146,0x409d30fc
v_fmaak_f32 v179,v147,v147,0x409d30fc
v_fmaak_f32 v180,v148,v148,0x409d30fc
v_fmaak_f32 v181,v149,v149,0x409d30fc
v_fmaak_f32 v182,v150,v150,0x409d30fc
v_fmaak_f32 v183,v151,v151,0x409d30fc
v_fmaak_f32 v184,v152,v152,0x409d30fc
v_fmaak_f32 v185,v153,v153,0x409d30fc
v_fmaak_f32 v186,v154,v154,0x409d30fc
v_fmaak_f32 v187,v155,v155,0x409d30fc
v_fmaak_f32 v188,v156,v156,0x409d30fc
v_fmaak_f32 v189,v157,v157,0x409d30fc
v_fmaak_f32 v190,v158,v158,0x409d30fc
v_fmaak_f32 v191,v159,v159,0x409d30fc
v_mul_f32 v176,v144,v176
v_mul_f32 v177,v145,v177
v_mul_f32 v178,v146,v178
v_mul_f32 v179,v147,v179
v_mul_f32 v180,v148,v180
v_mul_f32 v181,v149,v181
v_mul_f32 v182,v150,v182
v_mul_f32 v183,v151,v183
v_mul_f32 v184,v152,v184
v_mul_f32 v185,v153,v185
v_mul_f32 v186,v154,v186
v_mul_f32 v187,v155,v187
v_mul_f32 v188,v156,v188
v_mul_f32 v189,v157,v189
v_mul_f32 v190,v158,v190
v_mul_f32 v191,v159,v191
s_setprio 1
v_mfma_f32_32x32x16_bf16 v[96:111],v[200:203],v[192:195],v[96:111]
v_exp_f32 v176,v176
v_exp_f32 v177,v177
v_exp_f32 v178,v178
v_mfma_f32_32x32x16_bf16 v[96:111],v[204:207],v[196:199],v[96:111]
v_exp_f32 v179,v179
v_exp_f32 v180,v180
v_exp_f32 v181,v181
v_mfma_f32_32x32x16_bf16 v[112:127],v[208:211],v[192:195],v[112:127]
v_exp_f32 v182,v182
v_exp_f32 v183,v183
v_exp_f32 v184,v184
v_mfma_f32_32x32x16_bf16 v[112:127],v[212:215],v[196:199],v[112:127]
v_exp_f32 v185,v185
v_exp_f32 v186,v186
v_exp_f32 v187,v187
v_mfma_f32_32x32x16_bf16 v[128:143],v[216:219],v[192:195],v[128:143]
v_exp_f32 v188,v188
v_exp_f32 v189,v189
v_mfma_f32_32x32x16_bf16 v[128:143],v[220:223],v[196:199],v[128:143]
v_exp_f32 v190,v190
v_exp_f32 v191,v191
ds_read_b64_tr_b16 v[200:201],v16 offset:0
ds_read_b64_tr_b16 v[202:203],v16 offset:512
ds_read_b64_tr_b16 v[204:205],v16 offset:1024
ds_read_b64_tr_b16 v[206:207],v16 offset:1536
ds_read_b64_tr_b16 v[208:209],v16 offset:2048
ds_read_b64_tr_b16 v[210:211],v16 offset:2560
ds_read_b64_tr_b16 v[212:213],v16 offset:3072
ds_read_b64_tr_b16 v[214:215],v16 offset:3584
ds_read_b64_tr_b16 v[216:217],v16 offset:4096
ds_read_b64_tr_b16 v[218:219],v16 offset:4608
ds_read_b64_tr_b16 v[220:221],v16 offset:5120
ds_read_b64_tr_b16 v[222:223],v16 offset:5632
ds_read_b128 v[160:163],v18 offset:1152
ds_read_b128 v[164:167],v18 offset:1184
ds_read_b128 v[168:171],v18 offset:1216
ds_read_b128 v[172:175],v18 offset:1248
s_mov_b64 exec,s[60:61]
s_add_u32 m0,s58,142848
s_mov_b32 s64,55296
buffer_load_dwordx4 v28,s[48:51],s64 offen lds
s_add_u32 m0,s58,143616
s_mov_b32 s65,56064
buffer_load_dwordx4 v28,s[48:51],s65 offen lds
s_mov_b64 exec,-1
s_setprio 0
v_add_f32 v176,1.0,v176
v_add_f32 v177,1.0,v177
v_add_f32 v178,1.0,v178
v_add_f32 v179,1.0,v179
v_add_f32 v180,1.0,v180
v_add_f32 v181,1.0,v181
v_add_f32 v182,1.0,v182
v_add_f32 v183,1.0,v183
v_add_f32 v184,1.0,v184
v_add_f32 v185,1.0,v185
v_add_f32 v186,1.0,v186
v_add_f32 v187,1.0,v187
v_add_f32 v188,1.0,v188
v_add_f32 v189,1.0,v189
v_add_f32 v190,1.0,v190
v_add_f32 v191,1.0,v191
s_setprio 1
s_waitcnt lgkmcnt(0)
v_mfma_f32_32x32x16_bf16 v[160:175],v[200:203],v[72:75],v[160:175]
v_rcp_f32 v176,v176
v_rcp_f32 v177,v177
v_rcp_f32 v178,v178
v_mfma_f32_32x32x16_bf16 v[160:175],v[204:207],v[76:79],v[160:175]
v_rcp_f32 v179,v179
v_rcp_f32 v180,v180
v_rcp_f32 v181,v181
v_mfma_f32_32x32x16_bf16 v[160:175],v[208:211],v[80:83],v[160:175]
v_rcp_f32 v182,v182
v_rcp_f32 v183,v183
v_rcp_f32 v184,v184
v_mfma_f32_32x32x16_bf16 v[160:175],v[212:215],v[84:87],v[160:175]
v_rcp_f32 v185,v185
v_rcp_f32 v186,v186
v_rcp_f32 v187,v187
v_mfma_f32_32x32x16_bf16 v[160:175],v[216:219],v[88:91],v[160:175]
v_rcp_f32 v188,v188
v_rcp_f32 v189,v189
v_mfma_f32_32x32x16_bf16 v[160:175],v[220:223],v[92:95],v[160:175]
v_rcp_f32 v190,v190
v_rcp_f32 v191,v191
ds_read_b64_tr_b16 v[200:201],v17 offset:12288
ds_read_b64_tr_b16 v[202:203],v17 offset:13824
ds_read_b64_tr_b16 v[204:205],v17 offset:15360
ds_read_b64_tr_b16 v[206:207],v17 offset:16896
ds_read_b64_tr_b16 v[208:209],v17 offset:12352
ds_read_b64_tr_b16 v[210:211],v17 offset:13888
ds_read_b64_tr_b16 v[212:213],v17 offset:15424
ds_read_b64_tr_b16 v[214:215],v17 offset:16960
ds_read_b64_tr_b16 v[216:217],v17 offset:12416
ds_read_b64_tr_b16 v[218:219],v17 offset:13952
ds_read_b64_tr_b16 v[220:221],v17 offset:15488
ds_read_b64_tr_b16 v[222:223],v17 offset:17024
s_mov_b64 exec,s[60:61]
s_add_u32 m0,s58,136704
s_movk_i32 s64,704
buffer_load_dwordx4 v27,s[44:47],s64 offen lds
s_add_u32 m0,s58,137472
s_movk_i32 s65,9920
buffer_load_dwordx4 v27,s[44:47],s65 offen lds
s_mov_b64 exec,-1
s_setprio 0
v_mul_f32 v176,v144,v176
v_mul_f32 v177,v145,v177
v_mul_f32 v178,v146,v178
v_mul_f32 v179,v147,v179
v_mul_f32 v180,v148,v180
v_mul_f32 v181,v149,v181
v_mul_f32 v182,v150,v182
v_mul_f32 v183,v151,v183
v_mul_f32 v184,v152,v184
v_mul_f32 v185,v153,v185
v_mul_f32 v186,v154,v186
v_mul_f32 v187,v155,v187
v_mul_f32 v188,v156,v188
v_mul_f32 v189,v157,v189
v_mul_f32 v190,v158,v190
v_mul_f32 v191,v159,v191
v_cvt_pk_bf16_f32 v192,v176,v177
v_cvt_pk_bf16_f32 v193,v178,v179
v_cvt_pk_bf16_f32 v194,v180,v181
v_cvt_pk_bf16_f32 v195,v182,v183
v_cvt_pk_bf16_f32 v196,v184,v185
v_cvt_pk_bf16_f32 v197,v186,v187
v_cvt_pk_bf16_f32 v198,v188,v189
v_cvt_pk_bf16_f32 v199,v190,v191
s_waitcnt lgkmcnt(0)
s_waitcnt vmcnt(2)
s_barrier
v_fmaak_f32 v176,v160,v160,0x409d30fc
v_fmaak_f32 v177,v161,v161,0x409d30fc
v_fmaak_f32 v178,v162,v162,0x409d30fc
v_fmaak_f32 v179,v163,v163,0x409d30fc
v_fmaak_f32 v180,v164,v164,0x409d30fc
v_fmaak_f32 v181,v165,v165,0x409d30fc
v_fmaak_f32 v182,v166,v166,0x409d30fc
v_fmaak_f32 v183,v167,v167,0x409d30fc
v_fmaak_f32 v184,v168,v168,0x409d30fc
v_fmaak_f32 v185,v169,v169,0x409d30fc
v_fmaak_f32 v186,v170,v170,0x409d30fc
v_fmaak_f32 v187,v171,v171,0x409d30fc
v_fmaak_f32 v188,v172,v172,0x409d30fc
v_fmaak_f32 v189,v173,v173,0x409d30fc
v_fmaak_f32 v190,v174,v174,0x409d30fc
v_fmaak_f32 v191,v175,v175,0x409d30fc
v_mul_f32 v176,v160,v176
v_mul_f32 v177,v161,v177
v_mul_f32 v178,v162,v178
v_mul_f32 v179,v163,v179
v_mul_f32 v180,v164,v180
v_mul_f32 v181,v165,v181
v_mul_f32 v182,v166,v182
v_mul_f32 v183,v167,v183
v_mul_f32 v184,v168,v184
v_mul_f32 v185,v169,v185
v_mul_f32 v186,v170,v186
v_mul_f32 v187,v171,v187
v_mul_f32 v188,v172,v188
v_mul_f32 v189,v173,v189
v_mul_f32 v190,v174,v190
v_mul_f32 v191,v175,v191
s_setprio 1
v_mfma_f32_32x32x16_bf16 v[96:111],v[200:203],v[192:195],v[96:111]
v_exp_f32 v176,v176
v_exp_f32 v177,v177
v_exp_f32 v178,v178
v_mfma_f32_32x32x16_bf16 v[96:111],v[204:207],v[196:199],v[96:111]
v_exp_f32 v179,v179
v_exp_f32 v180,v180
v_exp_f32 v181,v181
v_mfma_f32_32x32x16_bf16 v[112:127],v[208:211],v[192:195],v[112:127]
v_exp_f32 v182,v182
v_exp_f32 v183,v183
v_exp_f32 v184,v184
v_mfma_f32_32x32x16_bf16 v[112:127],v[212:215],v[196:199],v[112:127]
v_exp_f32 v185,v185
v_exp_f32 v186,v186
v_exp_f32 v187,v187
v_mfma_f32_32x32x16_bf16 v[128:143],v[216:219],v[192:195],v[128:143]
v_exp_f32 v188,v188
v_exp_f32 v189,v189
v_mfma_f32_32x32x16_bf16 v[128:143],v[220:223],v[196:199],v[128:143]
v_exp_f32 v190,v190
v_exp_f32 v191,v191
ds_read_b64_tr_b16 v[200:201],v16 offset:6144
ds_read_b64_tr_b16 v[202:203],v16 offset:6656
ds_read_b64_tr_b16 v[204:205],v16 offset:7168
ds_read_b64_tr_b16 v[206:207],v16 offset:7680
ds_read_b64_tr_b16 v[208:209],v16 offset:8192
ds_read_b64_tr_b16 v[210:211],v16 offset:8704
ds_read_b64_tr_b16 v[212:213],v16 offset:9216
ds_read_b64_tr_b16 v[214:215],v16 offset:9728
ds_read_b64_tr_b16 v[216:217],v16 offset:10240
ds_read_b64_tr_b16 v[218:219],v16 offset:10752
ds_read_b64_tr_b16 v[220:221],v16 offset:11264
ds_read_b64_tr_b16 v[222:223],v16 offset:11776
ds_read_b128 v[144:147],v18 offset:1280
ds_read_b128 v[148:151],v18 offset:1312
ds_read_b128 v[152:155],v18 offset:1344
ds_read_b128 v[156:159],v18 offset:1376
s_mov_b64 exec,s[60:61]
s_add_u32 m0,s58,148992
s_mov_b32 s64,61440
buffer_load_dwordx4 v28,s[48:51],s64 offen lds
s_add_u32 m0,s58,149760
s_mov_b32 s65,62208
buffer_load_dwordx4 v28,s[48:51],s65 offen lds
s_mov_b64 exec,-1
s_setprio 0
v_add_f32 v176,1.0,v176
v_add_f32 v177,1.0,v177
v_add_f32 v178,1.0,v178
v_add_f32 v179,1.0,v179
v_add_f32 v180,1.0,v180
v_add_f32 v181,1.0,v181
v_add_f32 v182,1.0,v182
v_add_f32 v183,1.0,v183
v_add_f32 v184,1.0,v184
v_add_f32 v185,1.0,v185
v_add_f32 v186,1.0,v186
v_add_f32 v187,1.0,v187
v_add_f32 v188,1.0,v188
v_add_f32 v189,1.0,v189
v_add_f32 v190,1.0,v190
v_add_f32 v191,1.0,v191
s_setprio 1
s_waitcnt lgkmcnt(0)
v_mfma_f32_32x32x16_bf16 v[144:159],v[200:203],v[72:75],v[144:159]
v_rcp_f32 v176,v176
v_rcp_f32 v177,v177
v_rcp_f32 v178,v178
v_mfma_f32_32x32x16_bf16 v[144:159],v[204:207],v[76:79],v[144:159]
v_rcp_f32 v179,v179
v_rcp_f32 v180,v180
v_rcp_f32 v181,v181
v_mfma_f32_32x32x16_bf16 v[144:159],v[208:211],v[80:83],v[144:159]
v_rcp_f32 v182,v182
v_rcp_f32 v183,v183
v_rcp_f32 v184,v184
v_mfma_f32_32x32x16_bf16 v[144:159],v[212:215],v[84:87],v[144:159]
v_rcp_f32 v185,v185
v_rcp_f32 v186,v186
v_rcp_f32 v187,v187
v_mfma_f32_32x32x16_bf16 v[144:159],v[216:219],v[88:91],v[144:159]
v_rcp_f32 v188,v188
v_rcp_f32 v189,v189
v_mfma_f32_32x32x16_bf16 v[144:159],v[220:223],v[92:95],v[144:159]
v_rcp_f32 v190,v190
v_rcp_f32 v191,v191
ds_read_b64_tr_b16 v[200:201],v17 offset:0
ds_read_b64_tr_b16 v[202:203],v17 offset:1536
ds_read_b64_tr_b16 v[204:205],v17 offset:3072
ds_read_b64_tr_b16 v[206:207],v17 offset:4608
ds_read_b64_tr_b16 v[208:209],v17 offset:64
ds_read_b64_tr_b16 v[210:211],v17 offset:1600
ds_read_b64_tr_b16 v[212:213],v17 offset:3136
ds_read_b64_tr_b16 v[214:215],v17 offset:4672
ds_read_b64_tr_b16 v[216:217],v17 offset:128
ds_read_b64_tr_b16 v[218:219],v17 offset:1664
ds_read_b64_tr_b16 v[220:221],v17 offset:3200
ds_read_b64_tr_b16 v[222:223],v17 offset:4736
s_mov_b64 exec,s[60:61]
s_add_u32 m0,s58,124416
s_movk_i32 s64,0
buffer_load_dwordx4 v27,s[44:47],s64 offen lds
s_add_u32 m0,s58,125184
s_movk_i32 s65,9216
buffer_load_dwordx4 v27,s[44:47],s65 offen lds
s_mov_b64 exec,-1
s_setprio 0
v_mul_f32 v176,v160,v176
v_mul_f32 v177,v161,v177
v_mul_f32 v178,v162,v178
v_mul_f32 v179,v163,v179
v_mul_f32 v180,v164,v180
v_mul_f32 v181,v165,v181
v_mul_f32 v182,v166,v182
v_mul_f32 v183,v167,v183
v_mul_f32 v184,v168,v184
v_mul_f32 v185,v169,v185
v_mul_f32 v186,v170,v186
v_mul_f32 v187,v171,v187
v_mul_f32 v188,v172,v188
v_mul_f32 v189,v173,v189
v_mul_f32 v190,v174,v190
v_mul_f32 v191,v175,v191
v_cvt_pk_bf16_f32 v192,v176,v177
v_cvt_pk_bf16_f32 v193,v178,v179
v_cvt_pk_bf16_f32 v194,v180,v181
v_cvt_pk_bf16_f32 v195,v182,v183
v_cvt_pk_bf16_f32 v196,v184,v185
v_cvt_pk_bf16_f32 v197,v186,v187
v_cvt_pk_bf16_f32 v198,v188,v189
v_cvt_pk_bf16_f32 v199,v190,v191
s_waitcnt lgkmcnt(0)
s_waitcnt vmcnt(2)
s_barrier
v_fmaak_f32 v176,v144,v144,0x409d30fc
v_fmaak_f32 v177,v145,v145,0x409d30fc
v_fmaak_f32 v178,v146,v146,0x409d30fc
v_fmaak_f32 v179,v147,v147,0x409d30fc
v_fmaak_f32 v180,v148,v148,0x409d30fc
v_fmaak_f32 v181,v149,v149,0x409d30fc
v_fmaak_f32 v182,v150,v150,0x409d30fc
v_fmaak_f32 v183,v151,v151,0x409d30fc
v_fmaak_f32 v184,v152,v152,0x409d30fc
v_fmaak_f32 v185,v153,v153,0x409d30fc
v_fmaak_f32 v186,v154,v154,0x409d30fc
v_fmaak_f32 v187,v155,v155,0x409d30fc
v_fmaak_f32 v188,v156,v156,0x409d30fc
v_fmaak_f32 v189,v157,v157,0x409d30fc
v_fmaak_f32 v190,v158,v158,0x409d30fc
v_fmaak_f32 v191,v159,v159,0x409d30fc
v_mul_f32 v176,v144,v176
v_mul_f32 v177,v145,v177
v_mul_f32 v178,v146,v178
v_mul_f32 v179,v147,v179
v_mul_f32 v180,v148,v180
v_mul_f32 v181,v149,v181
v_mul_f32 v182,v150,v182
v_mul_f32 v183,v151,v183
v_mul_f32 v184,v152,v184
v_mul_f32 v185,v153,v185
v_mul_f32 v186,v154,v186
v_mul_f32 v187,v155,v187
v_mul_f32 v188,v156,v188
v_mul_f32 v189,v157,v189
v_mul_f32 v190,v158,v190
v_mul_f32 v191,v159,v191
s_setprio 1
v_mfma_f32_32x32x16_bf16 v[96:111],v[200:203],v[192:195],v[96:111]
v_exp_f32 v176,v176
v_exp_f32 v177,v177
v_exp_f32 v178,v178
v_mfma_f32_32x32x16_bf16 v[96:111],v[204:207],v[196:199],v[96:111]
v_exp_f32 v179,v179
v_exp_f32 v180,v180
v_exp_f32 v181,v181
v_mfma_f32_32x32x16_bf16 v[112:127],v[208:211],v[192:195],v[112:127]
v_exp_f32 v182,v182
v_exp_f32 v183,v183
v_exp_f32 v184,v184
v_mfma_f32_32x32x16_bf16 v[112:127],v[212:215],v[196:199],v[112:127]
v_exp_f32 v185,v185
v_exp_f32 v186,v186
v_exp_f32 v187,v187
v_mfma_f32_32x32x16_bf16 v[128:143],v[216:219],v[192:195],v[128:143]
v_exp_f32 v188,v188
v_exp_f32 v189,v189
v_mfma_f32_32x32x16_bf16 v[128:143],v[220:223],v[196:199],v[128:143]
v_exp_f32 v190,v190
v_exp_f32 v191,v191
ds_read_b64_tr_b16 v[200:201],v16 offset:12288
ds_read_b64_tr_b16 v[202:203],v16 offset:12800
ds_read_b64_tr_b16 v[204:205],v16 offset:13312
ds_read_b64_tr_b16 v[206:207],v16 offset:13824
ds_read_b64_tr_b16 v[208:209],v16 offset:14336
ds_read_b64_tr_b16 v[210:211],v16 offset:14848
ds_read_b64_tr_b16 v[212:213],v16 offset:15360
ds_read_b64_tr_b16 v[214:215],v16 offset:15872
ds_read_b64_tr_b16 v[216:217],v16 offset:16384
ds_read_b64_tr_b16 v[218:219],v16 offset:16896
ds_read_b64_tr_b16 v[220:221],v16 offset:17408
ds_read_b64_tr_b16 v[222:223],v16 offset:17920
ds_read_b128 v[160:163],v18 offset:1408
ds_read_b128 v[164:167],v18 offset:1440
ds_read_b128 v[168:171],v18 offset:1472
ds_read_b128 v[172:175],v18 offset:1504
s_mov_b64 exec,s[60:61]
s_add_u32 m0,s58,155136
s_mov_b32 s64,67584
buffer_load_dwordx4 v28,s[48:51],s64 offen lds
s_add_u32 m0,s58,155904
s_mov_b32 s65,68352
buffer_load_dwordx4 v28,s[48:51],s65 offen lds
s_mov_b64 exec,-1
s_setprio 0
v_add_f32 v176,1.0,v176
v_add_f32 v177,1.0,v177
v_add_f32 v178,1.0,v178
v_add_f32 v179,1.0,v179
v_add_f32 v180,1.0,v180
v_add_f32 v181,1.0,v181
v_add_f32 v182,1.0,v182
v_add_f32 v183,1.0,v183
v_add_f32 v184,1.0,v184
v_add_f32 v185,1.0,v185
v_add_f32 v186,1.0,v186
v_add_f32 v187,1.0,v187
v_add_f32 v188,1.0,v188
v_add_f32 v189,1.0,v189
v_add_f32 v190,1.0,v190
v_add_f32 v191,1.0,v191
s_setprio 1
s_waitcnt lgkmcnt(0)
v_mfma_f32_32x32x16_bf16 v[160:175],v[200:203],v[72:75],v[160:175]
v_rcp_f32 v176,v176
v_rcp_f32 v177,v177
v_rcp_f32 v178,v178
v_mfma_f32_32x32x16_bf16 v[160:175],v[204:207],v[76:79],v[160:175]
v_rcp_f32 v179,v179
v_rcp_f32 v180,v180
v_rcp_f32 v181,v181
v_mfma_f32_32x32x16_bf16 v[160:175],v[208:211],v[80:83],v[160:175]
v_rcp_f32 v182,v182
v_rcp_f32 v183,v183
v_rcp_f32 v184,v184
v_mfma_f32_32x32x16_bf16 v[160:175],v[212:215],v[84:87],v[160:175]
v_rcp_f32 v185,v185
v_rcp_f32 v186,v186
v_rcp_f32 v187,v187
v_mfma_f32_32x32x16_bf16 v[160:175],v[216:219],v[88:91],v[160:175]
v_rcp_f32 v188,v188
v_rcp_f32 v189,v189
v_mfma_f32_32x32x16_bf16 v[160:175],v[220:223],v[92:95],v[160:175]
v_rcp_f32 v190,v190
v_rcp_f32 v191,v191
ds_read_b64_tr_b16 v[200:201],v17 offset:6144
ds_read_b64_tr_b16 v[202:203],v17 offset:7680
ds_read_b64_tr_b16 v[204:205],v17 offset:9216
ds_read_b64_tr_b16 v[206:207],v17 offset:10752
ds_read_b64_tr_b16 v[208:209],v17 offset:6208
ds_read_b64_tr_b16 v[210:211],v17 offset:7744
ds_read_b64_tr_b16 v[212:213],v17 offset:9280
ds_read_b64_tr_b16 v[214:215],v17 offset:10816
ds_read_b64_tr_b16 v[216:217],v17 offset:6272
ds_read_b64_tr_b16 v[218:219],v17 offset:7808
ds_read_b64_tr_b16 v[220:221],v17 offset:9344
ds_read_b64_tr_b16 v[222:223],v17 offset:10880
s_mov_b64 exec,s[60:61]
s_add_u32 m0,s58,130560
s_movk_i32 s64,64
buffer_load_dwordx4 v27,s[44:47],s64 offen lds
s_add_u32 m0,s58,131328
s_movk_i32 s65,9280
buffer_load_dwordx4 v27,s[44:47],s65 offen lds
s_mov_b64 exec,-1
s_setprio 0
v_mul_f32 v176,v144,v176
v_mul_f32 v177,v145,v177
v_mul_f32 v178,v146,v178
v_mul_f32 v179,v147,v179
v_mul_f32 v180,v148,v180
v_mul_f32 v181,v149,v181
v_mul_f32 v182,v150,v182
v_mul_f32 v183,v151,v183
v_mul_f32 v184,v152,v184
v_mul_f32 v185,v153,v185
v_mul_f32 v186,v154,v186
v_mul_f32 v187,v155,v187
v_mul_f32 v188,v156,v188
v_mul_f32 v189,v157,v189
v_mul_f32 v190,v158,v190
v_mul_f32 v191,v159,v191
v_cvt_pk_bf16_f32 v192,v176,v177
v_cvt_pk_bf16_f32 v193,v178,v179
v_cvt_pk_bf16_f32 v194,v180,v181
v_cvt_pk_bf16_f32 v195,v182,v183
v_cvt_pk_bf16_f32 v196,v184,v185
v_cvt_pk_bf16_f32 v197,v186,v187
v_cvt_pk_bf16_f32 v198,v188,v189
v_cvt_pk_bf16_f32 v199,v190,v191
s_waitcnt lgkmcnt(0)
s_waitcnt vmcnt(2)
s_barrier
v_fmaak_f32 v176,v160,v160,0x409d30fc
v_fmaak_f32 v177,v161,v161,0x409d30fc
v_fmaak_f32 v178,v162,v162,0x409d30fc
v_fmaak_f32 v179,v163,v163,0x409d30fc
v_fmaak_f32 v180,v164,v164,0x409d30fc
v_fmaak_f32 v181,v165,v165,0x409d30fc
v_fmaak_f32 v182,v166,v166,0x409d30fc
v_fmaak_f32 v183,v167,v167,0x409d30fc
v_fmaak_f32 v184,v168,v168,0x409d30fc
v_fmaak_f32 v185,v169,v169,0x409d30fc
v_fmaak_f32 v186,v170,v170,0x409d30fc
v_fmaak_f32 v187,v171,v171,0x409d30fc
v_fmaak_f32 v188,v172,v172,0x409d30fc
v_fmaak_f32 v189,v173,v173,0x409d30fc
v_fmaak_f32 v190,v174,v174,0x409d30fc
v_fmaak_f32 v191,v175,v175,0x409d30fc
v_mul_f32 v176,v160,v176
v_mul_f32 v177,v161,v177
v_mul_f32 v178,v162,v178
v_mul_f32 v179,v163,v179
v_mul_f32 v180,v164,v180
v_mul_f32 v181,v165,v181
v_mul_f32 v182,v166,v182
v_mul_f32 v183,v167,v183
v_mul_f32 v184,v168,v184
v_mul_f32 v185,v169,v185
v_mul_f32 v186,v170,v186
v_mul_f32 v187,v171,v187
v_mul_f32 v188,v172,v188
v_mul_f32 v189,v173,v189
v_mul_f32 v190,v174,v190
v_mul_f32 v191,v175,v191
s_setprio 1
v_mfma_f32_32x32x16_bf16 v[96:111],v[200:203],v[192:195],v[96:111]
v_exp_f32 v176,v176
v_exp_f32 v177,v177
v_exp_f32 v178,v178
v_mfma_f32_32x32x16_bf16 v[96:111],v[204:207],v[196:199],v[96:111]
v_exp_f32 v179,v179
v_exp_f32 v180,v180
v_exp_f32 v181,v181
v_mfma_f32_32x32x16_bf16 v[112:127],v[208:211],v[192:195],v[112:127]
v_exp_f32 v182,v182
v_exp_f32 v183,v183
v_exp_f32 v184,v184
v_mfma_f32_32x32x16_bf16 v[112:127],v[212:215],v[196:199],v[112:127]
v_exp_f32 v185,v185
v_exp_f32 v186,v186
v_exp_f32 v187,v187
v_mfma_f32_32x32x16_bf16 v[128:143],v[216:219],v[192:195],v[128:143]
v_exp_f32 v188,v188
v_exp_f32 v189,v189
v_mfma_f32_32x32x16_bf16 v[128:143],v[220:223],v[196:199],v[128:143]
v_exp_f32 v190,v190
v_exp_f32 v191,v191
ds_read_b64 v[72:73],v20 offset:0
ds_read_b64 v[74:75],v21 offset:0
ds_read_b64 v[76:77],v22 offset:0
ds_read_b64 v[78:79],v23 offset:0
ds_read_b64 v[80:81],v20 offset:64
ds_read_b64 v[82:83],v21 offset:64
ds_read_b64 v[84:85],v22 offset:64
ds_read_b64 v[86:87],v23 offset:64
ds_read_b64 v[88:89],v20 offset:128
ds_read_b64 v[90:91],v21 offset:128
ds_read_b64 v[92:93],v22 offset:128
ds_read_b64 v[94:95],v23 offset:128
s_mov_b64 exec,s[60:61]
s_add_u32 m0,s58,142848
s_mov_b32 s64,0
buffer_load_dwordx4 v28,s[48:51],s64 offen lds
s_add_u32 m0,s58,143616
s_mov_b32 s65,768
buffer_load_dwordx4 v28,s[48:51],s65 offen lds
s_mov_b64 exec,-1
s_setprio 0
v_add_f32 v176,1.0,v176
v_add_f32 v177,1.0,v177
v_add_f32 v178,1.0,v178
v_add_f32 v179,1.0,v179
v_add_f32 v180,1.0,v180
v_add_f32 v181,1.0,v181
v_add_f32 v182,1.0,v182
v_add_f32 v183,1.0,v183
v_add_f32 v184,1.0,v184
v_add_f32 v185,1.0,v185
v_add_f32 v186,1.0,v186
v_add_f32 v187,1.0,v187
v_add_f32 v188,1.0,v188
v_add_f32 v189,1.0,v189
v_add_f32 v190,1.0,v190
v_add_f32 v191,1.0,v191
s_setprio 1
s_waitcnt lgkmcnt(0)
v_rcp_f32 v176,v176
v_rcp_f32 v177,v177
v_rcp_f32 v178,v178
v_rcp_f32 v179,v179
v_rcp_f32 v180,v180
v_rcp_f32 v181,v181
v_rcp_f32 v182,v182
v_rcp_f32 v183,v183
v_rcp_f32 v184,v184
v_rcp_f32 v185,v185
v_rcp_f32 v186,v186
v_rcp_f32 v187,v187
v_rcp_f32 v188,v188
v_rcp_f32 v189,v189
v_rcp_f32 v190,v190
v_rcp_f32 v191,v191
ds_read_b64_tr_b16 v[200:201],v17 offset:12288
ds_read_b64_tr_b16 v[202:203],v17 offset:13824
ds_read_b64_tr_b16 v[204:205],v17 offset:15360
ds_read_b64_tr_b16 v[206:207],v17 offset:16896
ds_read_b64_tr_b16 v[208:209],v17 offset:12352
ds_read_b64_tr_b16 v[210:211],v17 offset:13888
ds_read_b64_tr_b16 v[212:213],v17 offset:15424
ds_read_b64_tr_b16 v[214:215],v17 offset:16960
ds_read_b64_tr_b16 v[216:217],v17 offset:12416
ds_read_b64_tr_b16 v[218:219],v17 offset:13952
ds_read_b64_tr_b16 v[220:221],v17 offset:15488
ds_read_b64_tr_b16 v[222:223],v17 offset:17024
s_mov_b64 exec,s[60:61]
s_add_u32 m0,s58,136704
s_movk_i32 s64,128
buffer_load_dwordx4 v27,s[44:47],s64 offen lds
s_add_u32 m0,s58,137472
s_movk_i32 s65,9344
buffer_load_dwordx4 v27,s[44:47],s65 offen lds
s_mov_b64 exec,-1
s_setprio 0
v_mul_f32 v176,v160,v176
v_mul_f32 v177,v161,v177
v_mul_f32 v178,v162,v178
v_mul_f32 v179,v163,v179
v_mul_f32 v180,v164,v180
v_mul_f32 v181,v165,v181
v_mul_f32 v182,v166,v182
v_mul_f32 v183,v167,v183
v_mul_f32 v184,v168,v184
v_mul_f32 v185,v169,v185
v_mul_f32 v186,v170,v186
v_mul_f32 v187,v171,v187
v_mul_f32 v188,v172,v188
v_mul_f32 v189,v173,v189
v_mul_f32 v190,v174,v190
v_mul_f32 v191,v175,v191
v_cvt_pk_bf16_f32 v192,v176,v177
v_cvt_pk_bf16_f32 v193,v178,v179
v_cvt_pk_bf16_f32 v194,v180,v181
v_cvt_pk_bf16_f32 v195,v182,v183
v_cvt_pk_bf16_f32 v196,v184,v185
v_cvt_pk_bf16_f32 v197,v186,v187
v_cvt_pk_bf16_f32 v198,v188,v189
v_cvt_pk_bf16_f32 v199,v190,v191
ds_read_b128 v[144:147],v18 offset:1536
ds_read_b128 v[148:151],v18 offset:1568
ds_read_b128 v[152:155],v18 offset:1600
ds_read_b128 v[156:159],v18 offset:1632
ds_read_b128 v[160:163],v18 offset:1664
ds_read_b128 v[164:167],v18 offset:1696
ds_read_b128 v[168:171],v18 offset:1728
ds_read_b128 v[172:175],v18 offset:1760
ds_read_b128 v[176:179],v18 offset:1792
ds_read_b128 v[180:183],v18 offset:1824
ds_read_b128 v[184:187],v18 offset:1856
ds_read_b128 v[188:191],v18 offset:1888
s_waitcnt lgkmcnt(12)
s_setprio 1
v_mfma_f32_32x32x16_bf16 v[96:111],v[200:203],v[192:195],v[96:111]
v_lshlrev_b32 v232,16,v72
v_lshlrev_b32 v233,16,v73
v_lshlrev_b32 v234,16,v74
v_lshlrev_b32 v235,16,v75
v_mfma_f32_32x32x16_bf16 v[96:111],v[204:207],v[196:199],v[96:111]
v_lshlrev_b32 v236,16,v76
v_lshlrev_b32 v237,16,v77
v_lshlrev_b32 v238,16,v78
v_lshlrev_b32 v239,16,v79
v_mfma_f32_32x32x16_bf16 v[112:127],v[208:211],v[192:195],v[112:127]
v_lshlrev_b32 v240,16,v80
v_lshlrev_b32 v241,16,v81
v_lshlrev_b32 v242,16,v82
v_lshlrev_b32 v243,16,v83
v_mfma_f32_32x32x16_bf16 v[112:127],v[212:215],v[196:199],v[112:127]
v_lshlrev_b32 v244,16,v84
v_lshlrev_b32 v245,16,v85
v_lshlrev_b32 v246,16,v86
v_lshlrev_b32 v247,16,v87
v_mfma_f32_32x32x16_bf16 v[128:143],v[216:219],v[192:195],v[128:143]
v_lshlrev_b32 v248,16,v88
v_lshlrev_b32 v249,16,v89
v_lshlrev_b32 v250,16,v90
v_lshlrev_b32 v251,16,v91
v_mfma_f32_32x32x16_bf16 v[128:143],v[220:223],v[196:199],v[128:143]
v_lshlrev_b32 v252,16,v92
v_lshlrev_b32 v253,16,v93
v_lshlrev_b32 v254,16,v94
v_lshlrev_b32 v255,16,v95
s_setprio 0
s_waitcnt lgkmcnt(0)
v_and_b32 v72,0xffff0000,v72
v_and_b32 v73,0xffff0000,v73
v_and_b32 v74,0xffff0000,v74
v_and_b32 v75,0xffff0000,v75
v_and_b32 v76,0xffff0000,v76
v_and_b32 v77,0xffff0000,v77
v_and_b32 v78,0xffff0000,v78
v_and_b32 v79,0xffff0000,v79
v_and_b32 v80,0xffff0000,v80
v_and_b32 v81,0xffff0000,v81
v_and_b32 v82,0xffff0000,v82
v_and_b32 v83,0xffff0000,v83
v_and_b32 v84,0xffff0000,v84
v_and_b32 v85,0xffff0000,v85
v_and_b32 v86,0xffff0000,v86
v_and_b32 v87,0xffff0000,v87
v_and_b32 v88,0xffff0000,v88
v_and_b32 v89,0xffff0000,v89
v_and_b32 v90,0xffff0000,v90
v_and_b32 v91,0xffff0000,v91
v_and_b32 v92,0xffff0000,v92
v_and_b32 v93,0xffff0000,v93
v_and_b32 v94,0xffff0000,v94
v_and_b32 v95,0xffff0000,v95
v_fma_f32 v232,v96,v144,v232
v_fma_f32 v72,v97,v145,v72
v_fma_f32 v233,v98,v146,v233
v_fma_f32 v73,v99,v147,v73
v_fma_f32 v234,v100,v148,v234
v_fma_f32 v74,v101,v149,v74
v_fma_f32 v235,v102,v150,v235
v_fma_f32 v75,v103,v151,v75
v_fma_f32 v236,v104,v152,v236
v_fma_f32 v76,v105,v153,v76
v_fma_f32 v237,v106,v154,v237
v_fma_f32 v77,v107,v155,v77
v_fma_f32 v238,v108,v156,v238
v_fma_f32 v78,v109,v157,v78
v_fma_f32 v239,v110,v158,v239
v_fma_f32 v79,v111,v159,v79
v_fma_f32 v240,v112,v160,v240
v_fma_f32 v80,v113,v161,v80
v_fma_f32 v241,v114,v162,v241
v_fma_f32 v81,v115,v163,v81
v_fma_f32 v242,v116,v164,v242
v_fma_f32 v82,v117,v165,v82
v_fma_f32 v243,v118,v166,v243
v_fma_f32 v83,v119,v167,v83
v_fma_f32 v244,v120,v168,v244
v_fma_f32 v84,v121,v169,v84
v_fma_f32 v245,v122,v170,v245
v_fma_f32 v85,v123,v171,v85
v_fma_f32 v246,v124,v172,v246
v_fma_f32 v86,v125,v173,v86
v_fma_f32 v247,v126,v174,v247
v_fma_f32 v87,v127,v175,v87
v_fma_f32 v248,v128,v176,v248
v_fma_f32 v88,v129,v177,v88
v_fma_f32 v249,v130,v178,v249
v_fma_f32 v89,v131,v179,v89
v_fma_f32 v250,v132,v180,v250
v_fma_f32 v90,v133,v181,v90
v_fma_f32 v251,v134,v182,v251
v_fma_f32 v91,v135,v183,v91
v_fma_f32 v252,v136,v184,v252
v_fma_f32 v92,v137,v185,v92
v_fma_f32 v253,v138,v186,v253
v_fma_f32 v93,v139,v187,v93
v_fma_f32 v254,v140,v188,v254
v_fma_f32 v94,v141,v189,v94
v_fma_f32 v255,v142,v190,v255
v_fma_f32 v95,v143,v191,v95
v_add_u32 v4,s54,v4
v_add_u32 v5,s54,v5
v_add_u32 v6,s54,v6
v_add_u32 v7,s54,v7
v_add_u32 v8,s54,v8
v_add_u32 v9,s54,v9
v_add_u32 v10,s54,v10
v_add_u32 v11,s54,v11
v_add_u32 v12,s54,v12
v_add_u32 v13,s54,v13
v_add_u32 v14,s54,v14
v_add_u32 v15,s54,v15
v_cvt_pk_bf16_f32 v200,v232,v72
v_cvt_pk_bf16_f32 v201,v233,v73
v_cvt_pk_bf16_f32 v202,v234,v74
v_cvt_pk_bf16_f32 v203,v235,v75
v_cvt_pk_bf16_f32 v204,v236,v76
v_cvt_pk_bf16_f32 v205,v237,v77
v_cvt_pk_bf16_f32 v206,v238,v78
v_cvt_pk_bf16_f32 v207,v239,v79
v_cvt_pk_bf16_f32 v208,v240,v80
v_cvt_pk_bf16_f32 v209,v241,v81
v_cvt_pk_bf16_f32 v210,v242,v82
v_cvt_pk_bf16_f32 v211,v243,v83
v_cvt_pk_bf16_f32 v212,v244,v84
v_cvt_pk_bf16_f32 v213,v245,v85
v_cvt_pk_bf16_f32 v214,v246,v86
v_cvt_pk_bf16_f32 v215,v247,v87
v_cvt_pk_bf16_f32 v216,v248,v88
v_cvt_pk_bf16_f32 v217,v249,v89
v_cvt_pk_bf16_f32 v218,v250,v90
v_cvt_pk_bf16_f32 v219,v251,v91
v_cvt_pk_bf16_f32 v220,v252,v92
v_cvt_pk_bf16_f32 v221,v253,v93
v_cvt_pk_bf16_f32 v222,v254,v94
v_cvt_pk_bf16_f32 v223,v255,v95
ds_write_b64 v20,v[200:201] offset:0
ds_write_b64 v21,v[202:203] offset:0
ds_write_b64 v22,v[204:205] offset:0
ds_write_b64 v23,v[206:207] offset:0
ds_write_b64 v20,v[208:209] offset:64
ds_write_b64 v21,v[210:211] offset:64
ds_write_b64 v22,v[212:213] offset:64
ds_write_b64 v23,v[214:215] offset:64
ds_write_b64 v20,v[216:217] offset:128
ds_write_b64 v21,v[218:219] offset:128
ds_write_b64 v22,v[220:221] offset:128
ds_write_b64 v23,v[222:223] offset:128
ds_read_b128 v[72:75],v19 offset:0
ds_read_b128 v[76:79],v19 offset:1024
ds_read_b128 v[80:83],v19 offset:2048
ds_read_b128 v[84:87],v19 offset:3456
ds_read_b128 v[88:91],v19 offset:4480
ds_read_b128 v[92:95],v19 offset:5504
v_add_u32 v20,s54,v20
v_add_u32 v21,s54,v21
v_add_u32 v22,s54,v22
v_add_u32 v23,s54,v23
s_sub_u32 s68,s68,s54
s_add_u32 s59,s59,1572864
s_add_u32 s53,s53,1
s_cmp_eq_u32 s53,3
s_cselect_b32 s59,0x70000000,s59
s_add_u32 s67,s69,98304
s_waitcnt lgkmcnt(5)
buffer_store_dwordx4 v[72:75],v24,s[40:43],s69 offen nt
s_waitcnt lgkmcnt(4)
buffer_store_dwordx4 v[76:79],v25,s[40:43],s69 offen nt
s_waitcnt lgkmcnt(3)
buffer_store_dwordx4 v[80:83],v26,s[40:43],s69 offen nt
s_waitcnt lgkmcnt(2)
buffer_store_dwordx4 v[84:87],v24,s[40:43],s67 offen nt
s_waitcnt lgkmcnt(1)
buffer_store_dwordx4 v[88:91],v25,s[40:43],s67 offen nt
s_waitcnt lgkmcnt(0)
buffer_store_dwordx4 v[92:95],v26,s[40:43],s67 offen nt
v_add_u32 v19,s54,v19
s_add_u32 s69,s69,1572864
s_sub_u32 s54,0,s54
s_cmp_lt_u32 s53,4
s_cbranch_scc1 L_pass0_%=
s_branch L_end_%=
L_pass1_%=:
s_waitcnt vmcnt(6)
s_barrier
ds_read_b128 v[200:203],v4 offset:0
ds_read_b128 v[204:207],v8 offset:0
ds_read_b128 v[208:211],v12 offset:0
ds_read_b128 v[212:215],v5 offset:0
ds_read_b128 v[216:219],v9 offset:0
ds_read_b128 v[220:223],v13 offset:0
ds_read_b128 v[96:99],v18 offset:1920
ds_read_b128 v[100:103],v18 offset:1952
ds_read_b128 v[104:107],v18 offset:1984
ds_read_b128 v[108:111],v18 offset:2016
ds_read_b128 v[112:115],v18 offset:2048
ds_read_b128 v[116:119],v18 offset:2080
ds_read_b128 v[120:123],v18 offset:2112
ds_read_b128 v[124:127],v18 offset:2144
ds_read_b128 v[128:131],v18 offset:2176
ds_read_b128 v[132:135],v18 offset:2208
ds_read_b128 v[136:139],v18 offset:2240
ds_read_b128 v[140:143],v18 offset:2272
v_and_b32 v72,v32,v36
v_and_b32 v73,v33,v36
v_and_b32 v74,v34,v36
v_and_b32 v75,v35,v36
v_and_b32 v76,v32,v48
v_and_b32 v77,v33,v48
v_and_b32 v78,v34,v48
v_and_b32 v79,v35,v48
v_and_b32 v80,v32,v60
v_and_b32 v81,v33,v60
v_and_b32 v82,v34,v60
v_and_b32 v83,v35,v60
v_and_b32 v84,v32,v37
v_and_b32 v85,v33,v37
v_and_b32 v86,v34,v37
v_and_b32 v87,v35,v37
v_and_b32 v88,v32,v49
v_and_b32 v89,v33,v49
v_and_b32 v90,v34,v49
v_and_b32 v91,v35,v49
v_and_b32 v92,v32,v61
v_and_b32 v93,v33,v61
v_and_b32 v94,v34,v61
v_and_b32 v95,v35,v61
s_setprio 1
s_waitcnt lgkmcnt(15)
v_mfma_f32_16x16x32_bf16 v[144:147],v[72:75],v[200:203],0
v_mfma_f32_16x16x32_bf16 v[144:147],v[76:79],v[204:207],v[144:147]
v_mfma_f32_16x16x32_bf16 v[144:147],v[80:83],v[208:211],v[144:147]
ds_read_b128 v[200:203],v6 offset:0
ds_read_b128 v[204:207],v10 offset:0
ds_read_b128 v[208:211],v14 offset:0
s_setprio 0
v_and_b32 v72,v32,v38
v_and_b32 v73,v33,v38
v_and_b32 v74,v34,v38
v_and_b32 v75,v35,v38
v_and_b32 v76,v32,v50
v_and_b32 v77,v33,v50
v_and_b32 v78,v34,v50
v_and_b32 v79,v35,v50
v_and_b32 v80,v32,v62
v_and_b32 v81,v33,v62
v_and_b32 v82,v34,v62
v_and_b32 v83,v35,v62
v_mul_f32 v237,v144,v144
v_mul_f32 v236,v145,v145
v_mul_f32 v239,v146,v146
v_mul_f32 v238,v147,v147
s_setprio 1
s_waitcnt lgkmcnt(15)
v_mfma_f32_16x16x32_bf16 v[148:151],v[84:87],v[212:215],0
v_mfma_f32_16x16x32_bf16 v[148:151],v[88:91],v[216:219],v[148:151]
v_mfma_f32_16x16x32_bf16 v[148:151],v[92:95],v[220:223],v[148:151]
ds_read_b128 v[212:215],v7 offset:0
ds_read_b128 v[216:219],v11 offset:0
ds_read_b128 v[220:223],v15 offset:0
s_setprio 0
v_and_b32 v84,v32,v39
v_and_b32 v85,v33,v39
v_and_b32 v86,v34,v39
v_and_b32 v87,v35,v39
v_and_b32 v88,v32,v51
v_and_b32 v89,v33,v51
v_and_b32 v90,v34,v51
v_and_b32 v91,v35,v51
v_and_b32 v92,v32,v63
v_and_b32 v93,v33,v63
v_and_b32 v94,v34,v63
v_and_b32 v95,v35,v63
v_add_f32 v232,v144,v148
v_fmac_f32 v237,v148,v148
v_add_f32 v233,v145,v149
v_fmac_f32 v236,v149,v149
v_add_f32 v234,v146,v150
v_fmac_f32 v239,v150,v150
v_add_f32 v235,v147,v151
v_fmac_f32 v238,v151,v151
s_setprio 1
s_waitcnt lgkmcnt(3)
v_mfma_f32_16x16x32_bf16 v[152:155],v[72:75],v[200:203],0
v_mfma_f32_16x16x32_bf16 v[152:155],v[76:79],v[204:207],v[152:155]
v_mfma_f32_16x16x32_bf16 v[152:155],v[80:83],v[208:211],v[152:155]
ds_read_b128 v[200:203],v4 offset:64
ds_read_b128 v[204:207],v8 offset:64
ds_read_b128 v[208:211],v12 offset:64
s_setprio 0
v_and_b32 v72,v32,v40
v_and_b32 v73,v33,v40
v_and_b32 v74,v34,v40
v_and_b32 v75,v35,v40
v_and_b32 v76,v32,v52
v_and_b32 v77,v33,v52
v_and_b32 v78,v34,v52
v_and_b32 v79,v35,v52
v_and_b32 v80,v32,v64
v_and_b32 v81,v33,v64
v_and_b32 v82,v34,v64
v_and_b32 v83,v35,v64
v_add_f32 v232,v232,v152
v_fmac_f32 v237,v152,v152
v_add_f32 v233,v233,v153
v_fmac_f32 v236,v153,v153
v_add_f32 v234,v234,v154
v_fmac_f32 v239,v154,v154
v_add_f32 v235,v235,v155
v_fmac_f32 v238,v155,v155
s_setprio 1
s_waitcnt lgkmcnt(3)
v_mfma_f32_16x16x32_bf16 v[156:159],v[84:87],v[212:215],0
v_mfma_f32_16x16x32_bf16 v[156:159],v[88:91],v[216:219],v[156:159]
v_mfma_f32_16x16x32_bf16 v[156:159],v[92:95],v[220:223],v[156:159]
ds_read_b128 v[212:215],v5 offset:64
ds_read_b128 v[216:219],v9 offset:64
ds_read_b128 v[220:223],v13 offset:64
s_setprio 0
v_and_b32 v84,v32,v41
v_and_b32 v85,v33,v41
v_and_b32 v86,v34,v41
v_and_b32 v87,v35,v41
v_and_b32 v88,v32,v53
v_and_b32 v89,v33,v53
v_and_b32 v90,v34,v53
v_and_b32 v91,v35,v53
v_and_b32 v92,v32,v65
v_and_b32 v93,v33,v65
v_and_b32 v94,v34,v65
v_and_b32 v95,v35,v65
v_add_f32 v232,v232,v156
v_fmac_f32 v237,v156,v156
v_add_f32 v233,v233,v157
v_fmac_f32 v236,v157,v157
v_add_f32 v234,v234,v158
v_fmac_f32 v239,v158,v158
v_add_f32 v235,v235,v159
v_fmac_f32 v238,v159,v159
s_setprio 1
s_waitcnt lgkmcnt(3)
v_mfma_f32_16x16x32_bf16 v[160:163],v[72:75],v[200:203],0
v_mfma_f32_16x16x32_bf16 v[160:163],v[76:79],v[204:207],v[160:163]
v_mfma_f32_16x16x32_bf16 v[160:163],v[80:83],v[208:211],v[160:163]
ds_read_b128 v[200:203],v6 offset:64
ds_read_b128 v[204:207],v10 offset:64
ds_read_b128 v[208:211],v14 offset:64
s_setprio 0
v_and_b32 v72,v32,v42
v_and_b32 v73,v33,v42
v_and_b32 v74,v34,v42
v_and_b32 v75,v35,v42
v_and_b32 v76,v32,v54
v_and_b32 v77,v33,v54
v_and_b32 v78,v34,v54
v_and_b32 v79,v35,v54
v_and_b32 v80,v32,v66
v_and_b32 v81,v33,v66
v_and_b32 v82,v34,v66
v_and_b32 v83,v35,v66
v_add_f32 v232,v232,v160
v_fmac_f32 v237,v160,v160
v_add_f32 v233,v233,v161
v_fmac_f32 v236,v161,v161
v_add_f32 v234,v234,v162
v_fmac_f32 v239,v162,v162
v_add_f32 v235,v235,v163
v_fmac_f32 v238,v163,v163
s_setprio 1
s_waitcnt lgkmcnt(3)
v_mfma_f32_16x16x32_bf16 v[164:167],v[84:87],v[212:215],0
v_mfma_f32_16x16x32_bf16 v[164:167],v[88:91],v[216:219],v[164:167]
v_mfma_f32_16x16x32_bf16 v[164:167],v[92:95],v[220:223],v[164:167]
ds_read_b128 v[212:215],v7 offset:64
ds_read_b128 v[216:219],v11 offset:64
ds_read_b128 v[220:223],v15 offset:64
s_setprio 0
v_and_b32 v84,v32,v43
v_and_b32 v85,v33,v43
v_and_b32 v86,v34,v43
v_and_b32 v87,v35,v43
v_and_b32 v88,v32,v55
v_and_b32 v89,v33,v55
v_and_b32 v90,v34,v55
v_and_b32 v91,v35,v55
v_and_b32 v92,v32,v67
v_and_b32 v93,v33,v67
v_and_b32 v94,v34,v67
v_and_b32 v95,v35,v67
v_add_f32 v232,v232,v164
v_fmac_f32 v237,v164,v164
v_add_f32 v233,v233,v165
v_fmac_f32 v236,v165,v165
v_add_f32 v234,v234,v166
v_fmac_f32 v239,v166,v166
v_add_f32 v235,v235,v167
v_fmac_f32 v238,v167,v167
s_setprio 1
s_waitcnt lgkmcnt(3)
v_mfma_f32_16x16x32_bf16 v[168:171],v[72:75],v[200:203],0
v_mfma_f32_16x16x32_bf16 v[168:171],v[76:79],v[204:207],v[168:171]
v_mfma_f32_16x16x32_bf16 v[168:171],v[80:83],v[208:211],v[168:171]
ds_read_b128 v[200:203],v4 offset:128
ds_read_b128 v[204:207],v8 offset:128
ds_read_b128 v[208:211],v12 offset:128
s_setprio 0
v_and_b32 v72,v32,v44
v_and_b32 v73,v33,v44
v_and_b32 v74,v34,v44
v_and_b32 v75,v35,v44
v_and_b32 v76,v32,v56
v_and_b32 v77,v33,v56
v_and_b32 v78,v34,v56
v_and_b32 v79,v35,v56
v_and_b32 v80,v32,v68
v_and_b32 v81,v33,v68
v_and_b32 v82,v34,v68
v_and_b32 v83,v35,v68
v_add_f32 v232,v232,v168
v_fmac_f32 v237,v168,v168
v_add_f32 v233,v233,v169
v_fmac_f32 v236,v169,v169
v_add_f32 v234,v234,v170
v_fmac_f32 v239,v170,v170
v_add_f32 v235,v235,v171
v_fmac_f32 v238,v171,v171
s_setprio 1
s_waitcnt lgkmcnt(3)
v_mfma_f32_16x16x32_bf16 v[172:175],v[84:87],v[212:215],0
v_mfma_f32_16x16x32_bf16 v[172:175],v[88:91],v[216:219],v[172:175]
v_mfma_f32_16x16x32_bf16 v[172:175],v[92:95],v[220:223],v[172:175]
ds_read_b128 v[212:215],v5 offset:128
ds_read_b128 v[216:219],v9 offset:128
ds_read_b128 v[220:223],v13 offset:128
s_setprio 0
v_and_b32 v84,v32,v45
v_and_b32 v85,v33,v45
v_and_b32 v86,v34,v45
v_and_b32 v87,v35,v45
v_and_b32 v88,v32,v57
v_and_b32 v89,v33,v57
v_and_b32 v90,v34,v57
v_and_b32 v91,v35,v57
v_and_b32 v92,v32,v69
v_and_b32 v93,v33,v69
v_and_b32 v94,v34,v69
v_and_b32 v95,v35,v69
v_add_f32 v232,v232,v172
v_fmac_f32 v237,v172,v172
v_add_f32 v233,v233,v173
v_fmac_f32 v236,v173,v173
v_add_f32 v234,v234,v174
v_fmac_f32 v239,v174,v174
v_add_f32 v235,v235,v175
v_fmac_f32 v238,v175,v175
s_setprio 1
s_waitcnt lgkmcnt(3)
v_mfma_f32_16x16x32_bf16 v[176:179],v[72:75],v[200:203],0
v_mfma_f32_16x16x32_bf16 v[176:179],v[76:79],v[204:207],v[176:179]
v_mfma_f32_16x16x32_bf16 v[176:179],v[80:83],v[208:211],v[176:179]
ds_read_b128 v[200:203],v6 offset:128
ds_read_b128 v[204:207],v10 offset:128
ds_read_b128 v[208:211],v14 offset:128
s_setprio 0
v_and_b32 v72,v32,v46
v_and_b32 v73,v33,v46
v_and_b32 v74,v34,v46
v_and_b32 v75,v35,v46
v_and_b32 v76,v32,v58
v_and_b32 v77,v33,v58
v_and_b32 v78,v34,v58
v_and_b32 v79,v35,v58
v_and_b32 v80,v32,v70
v_and_b32 v81,v33,v70
v_and_b32 v82,v34,v70
v_and_b32 v83,v35,v70
v_add_f32 v232,v232,v176
v_fmac_f32 v237,v176,v176
v_add_f32 v233,v233,v177
v_fmac_f32 v236,v177,v177
v_add_f32 v234,v234,v178
v_fmac_f32 v239,v178,v178
v_add_f32 v235,v235,v179
v_fmac_f32 v238,v179,v179
s_setprio 1
s_waitcnt lgkmcnt(3)
v_mfma_f32_16x16x32_bf16 v[180:183],v[84:87],v[212:215],0
v_mfma_f32_16x16x32_bf16 v[180:183],v[88:91],v[216:219],v[180:183]
v_mfma_f32_16x16x32_bf16 v[180:183],v[92:95],v[220:223],v[180:183]
ds_read_b128 v[212:215],v7 offset:128
ds_read_b128 v[216:219],v11 offset:128
ds_read_b128 v[220:223],v15 offset:128
s_setprio 0
v_and_b32 v84,v32,v47
v_and_b32 v85,v33,v47
v_and_b32 v86,v34,v47
v_and_b32 v87,v35,v47
v_and_b32 v88,v32,v59
v_and_b32 v89,v33,v59
v_and_b32 v90,v34,v59
v_and_b32 v91,v35,v59
v_and_b32 v92,v32,v71
v_and_b32 v93,v33,v71
v_and_b32 v94,v34,v71
v_and_b32 v95,v35,v71
v_add_f32 v232,v232,v180
v_fmac_f32 v237,v180,v180
v_add_f32 v233,v233,v181
v_fmac_f32 v236,v181,v181
v_add_f32 v234,v234,v182
v_fmac_f32 v239,v182,v182
v_add_f32 v235,v235,v183
v_fmac_f32 v238,v183,v183
s_setprio 1
s_waitcnt lgkmcnt(3)
v_mfma_f32_16x16x32_bf16 v[184:187],v[72:75],v[200:203],0
v_mfma_f32_16x16x32_bf16 v[184:187],v[76:79],v[204:207],v[184:187]
v_mfma_f32_16x16x32_bf16 v[184:187],v[80:83],v[208:211],v[184:187]
s_setprio 0
s_nop 7
v_add_f32 v232,v232,v184
v_fmac_f32 v237,v184,v184
v_add_f32 v233,v233,v185
v_fmac_f32 v236,v185,v185
v_add_f32 v234,v234,v186
v_fmac_f32 v239,v186,v186
v_add_f32 v235,v235,v187
v_fmac_f32 v238,v187,v187
s_setprio 1
s_waitcnt lgkmcnt(0)
v_mfma_f32_16x16x32_bf16 v[188:191],v[84:87],v[212:215],0
v_mfma_f32_16x16x32_bf16 v[188:191],v[88:91],v[216:219],v[188:191]
v_mfma_f32_16x16x32_bf16 v[188:191],v[92:95],v[220:223],v[188:191]
s_setprio 0
ds_read_b64_tr_b16 v[200:201],v16 offset:0
ds_read_b64_tr_b16 v[202:203],v16 offset:512
ds_read_b64_tr_b16 v[204:205],v16 offset:1024
ds_read_b64_tr_b16 v[206:207],v16 offset:1536
ds_read_b64_tr_b16 v[208:209],v16 offset:2048
ds_read_b64_tr_b16 v[210:211],v16 offset:2560
ds_read_b64_tr_b16 v[212:213],v16 offset:3072
ds_read_b64_tr_b16 v[214:215],v16 offset:3584
ds_read_b64_tr_b16 v[216:217],v16 offset:4096
ds_read_b64_tr_b16 v[218:219],v16 offset:4608
ds_read_b64_tr_b16 v[220:221],v16 offset:5120
ds_read_b64_tr_b16 v[222:223],v16 offset:5632
v_add_f32 v232,v232,v188
v_fmac_f32 v237,v188,v188
v_add_f32 v233,v233,v189
v_fmac_f32 v236,v189,v189
v_add_f32 v234,v234,v190
v_fmac_f32 v239,v190,v190
v_add_f32 v235,v235,v191
v_fmac_f32 v238,v191,v191
v_add_f32 v232,v232,v233
v_add_f32 v234,v234,v235
v_add_f32 v237,v237,v236
v_add_f32 v239,v239,v238
v_add_f32 v240,v232,v234
v_add_f32 v241,v237,v239
s_nop 1
v_permlane32_swap_b32 v240,v241
v_add_f32 v240,v240,v241
v_mov_b32 v241,v240
s_nop 1
v_permlane32_swap_b32 v240,v241
v_mul_f32 v240,0x3c2aaaab,v240
v_mul_f32 v241,0x3c2aaaab,v241
v_fma_f32 v241,-v240,v240,v241
v_max_f32 v241,0,v241
v_add_f32 v241,0x3727c5ac,v241
v_rsq_f32 v241,v241
s_nop 0
v_mul_f32 v242,0xbeeff558,v241
v_mul_f32 v243,0x3eeff558,v241
v_mul_f32 v243,v240,v243
v_fma_f32 v144,v144,v242,v243
v_fma_f32 v145,v145,v242,v243
v_fma_f32 v146,v146,v242,v243
v_fma_f32 v147,v147,v242,v243
v_fma_f32 v148,v148,v242,v243
v_fma_f32 v149,v149,v242,v243
v_fma_f32 v150,v150,v242,v243
v_fma_f32 v151,v151,v242,v243
v_fma_f32 v152,v152,v242,v243
v_fma_f32 v153,v153,v242,v243
v_fma_f32 v154,v154,v242,v243
v_fma_f32 v155,v155,v242,v243
v_fma_f32 v156,v156,v242,v243
v_fma_f32 v157,v157,v242,v243
v_fma_f32 v158,v158,v242,v243
v_fma_f32 v159,v159,v242,v243
v_fma_f32 v160,v160,v242,v243
v_fma_f32 v161,v161,v242,v243
v_fma_f32 v162,v162,v242,v243
v_fma_f32 v163,v163,v242,v243
v_fma_f32 v164,v164,v242,v243
v_fma_f32 v165,v165,v242,v243
v_fma_f32 v166,v166,v242,v243
v_fma_f32 v167,v167,v242,v243
v_fma_f32 v168,v168,v242,v243
v_fma_f32 v169,v169,v242,v243
v_fma_f32 v170,v170,v242,v243
v_fma_f32 v171,v171,v242,v243
v_fma_f32 v172,v172,v242,v243
v_fma_f32 v173,v173,v242,v243
v_fma_f32 v174,v174,v242,v243
v_fma_f32 v175,v175,v242,v243
v_fma_f32 v176,v176,v242,v243
v_fma_f32 v177,v177,v242,v243
v_fma_f32 v178,v178,v242,v243
v_fma_f32 v179,v179,v242,v243
v_fma_f32 v180,v180,v242,v243
v_fma_f32 v181,v181,v242,v243
v_fma_f32 v182,v182,v242,v243
v_fma_f32 v183,v183,v242,v243
v_fma_f32 v184,v184,v242,v243
v_fma_f32 v185,v185,v242,v243
v_fma_f32 v186,v186,v242,v243
v_fma_f32 v187,v187,v242,v243
v_fma_f32 v188,v188,v242,v243
v_fma_f32 v189,v189,v242,v243
v_fma_f32 v190,v190,v242,v243
v_fma_f32 v191,v191,v242,v243
s_setprio 1
v_cvt_pk_bf16_f32 v72,v144,v145
v_cvt_pk_bf16_f32 v73,v146,v147
v_cvt_pk_bf16_f32 v74,v148,v149
v_cvt_pk_bf16_f32 v75,v150,v151
v_cvt_pk_bf16_f32 v76,v152,v153
v_cvt_pk_bf16_f32 v77,v154,v155
v_cvt_pk_bf16_f32 v78,v156,v157
v_cvt_pk_bf16_f32 v79,v158,v159
ds_read_b128 v[144:147],v18 offset:0
ds_read_b128 v[148:151],v18 offset:32
ds_read_b128 v[152:155],v18 offset:64
ds_read_b128 v[156:159],v18 offset:96
v_cvt_pk_bf16_f32 v80,v160,v161
v_cvt_pk_bf16_f32 v81,v162,v163
v_cvt_pk_bf16_f32 v82,v164,v165
v_cvt_pk_bf16_f32 v83,v166,v167
v_cvt_pk_bf16_f32 v84,v168,v169
v_cvt_pk_bf16_f32 v85,v170,v171
v_cvt_pk_bf16_f32 v86,v172,v173
v_cvt_pk_bf16_f32 v87,v174,v175
s_waitcnt lgkmcnt(0)
v_mfma_f32_32x32x16_bf16 v[144:159],v[200:203],v[72:75],v[144:159]
v_cvt_pk_bf16_f32 v88,v176,v177
v_cvt_pk_bf16_f32 v89,v178,v179
v_cvt_pk_bf16_f32 v90,v180,v181
v_cvt_pk_bf16_f32 v91,v182,v183
v_mfma_f32_32x32x16_bf16 v[144:159],v[204:207],v[76:79],v[144:159]
v_cvt_pk_bf16_f32 v92,v184,v185
v_cvt_pk_bf16_f32 v93,v186,v187
v_cvt_pk_bf16_f32 v94,v188,v189
v_cvt_pk_bf16_f32 v95,v190,v191
v_mfma_f32_32x32x16_bf16 v[144:159],v[208:211],v[80:83],v[144:159]
v_mfma_f32_32x32x16_bf16 v[144:159],v[212:215],v[84:87],v[144:159]
v_mfma_f32_32x32x16_bf16 v[144:159],v[216:219],v[88:91],v[144:159]
v_mfma_f32_32x32x16_bf16 v[144:159],v[220:223],v[92:95],v[144:159]
s_setprio 0
s_nop 7
s_nop 1
s_waitcnt vmcnt(0)
s_barrier
v_fmaak_f32 v176,v144,v144,0x409d30fc
v_fmaak_f32 v177,v145,v145,0x409d30fc
v_fmaak_f32 v178,v146,v146,0x409d30fc
v_fmaak_f32 v179,v147,v147,0x409d30fc
v_fmaak_f32 v180,v148,v148,0x409d30fc
v_fmaak_f32 v181,v149,v149,0x409d30fc
v_fmaak_f32 v182,v150,v150,0x409d30fc
v_fmaak_f32 v183,v151,v151,0x409d30fc
v_fmaak_f32 v184,v152,v152,0x409d30fc
v_fmaak_f32 v185,v153,v153,0x409d30fc
v_fmaak_f32 v186,v154,v154,0x409d30fc
v_fmaak_f32 v187,v155,v155,0x409d30fc
v_fmaak_f32 v188,v156,v156,0x409d30fc
v_fmaak_f32 v189,v157,v157,0x409d30fc
v_fmaak_f32 v190,v158,v158,0x409d30fc
v_fmaak_f32 v191,v159,v159,0x409d30fc
v_mul_f32 v176,v144,v176
v_mul_f32 v177,v145,v177
v_mul_f32 v178,v146,v178
v_mul_f32 v179,v147,v179
v_mul_f32 v180,v148,v180
v_mul_f32 v181,v149,v181
v_mul_f32 v182,v150,v182
v_mul_f32 v183,v151,v183
v_mul_f32 v184,v152,v184
v_mul_f32 v185,v153,v185
v_mul_f32 v186,v154,v186
v_mul_f32 v187,v155,v187
v_mul_f32 v188,v156,v188
v_mul_f32 v189,v157,v189
v_mul_f32 v190,v158,v190
v_mul_f32 v191,v159,v191
s_setprio 1
v_exp_f32 v176,v176
v_exp_f32 v177,v177
v_exp_f32 v178,v178
v_exp_f32 v179,v179
v_exp_f32 v180,v180
v_exp_f32 v181,v181
v_exp_f32 v182,v182
v_exp_f32 v183,v183
v_exp_f32 v184,v184
v_exp_f32 v185,v185
v_exp_f32 v186,v186
v_exp_f32 v187,v187
v_exp_f32 v188,v188
v_exp_f32 v189,v189
v_exp_f32 v190,v190
v_exp_f32 v191,v191
ds_read_b64_tr_b16 v[200:201],v16 offset:6144
ds_read_b64_tr_b16 v[202:203],v16 offset:6656
ds_read_b64_tr_b16 v[204:205],v16 offset:7168
ds_read_b64_tr_b16 v[206:207],v16 offset:7680
ds_read_b64_tr_b16 v[208:209],v16 offset:8192
ds_read_b64_tr_b16 v[210:211],v16 offset:8704
ds_read_b64_tr_b16 v[212:213],v16 offset:9216
ds_read_b64_tr_b16 v[214:215],v16 offset:9728
ds_read_b64_tr_b16 v[216:217],v16 offset:10240
ds_read_b64_tr_b16 v[218:219],v16 offset:10752
ds_read_b64_tr_b16 v[220:221],v16 offset:11264
ds_read_b64_tr_b16 v[222:223],v16 offset:11776
ds_read_b128 v[160:163],v18 offset:128
ds_read_b128 v[164:167],v18 offset:160
ds_read_b128 v[168:171],v18 offset:192
ds_read_b128 v[172:175],v18 offset:224
s_mov_b64 exec,s[62:63]
s_add_u32 m0,s68,0
s_add_u32 s64,s59,0
buffer_load_dwordx4 v27,s[36:39],s64 offen lds
s_mov_b64 exec,-1
s_setprio 0
v_add_f32 v176,1.0,v176
v_add_f32 v177,1.0,v177
v_add_f32 v178,1.0,v178
v_add_f32 v179,1.0,v179
v_add_f32 v180,1.0,v180
v_add_f32 v181,1.0,v181
v_add_f32 v182,1.0,v182
v_add_f32 v183,1.0,v183
v_add_f32 v184,1.0,v184
v_add_f32 v185,1.0,v185
v_add_f32 v186,1.0,v186
v_add_f32 v187,1.0,v187
v_add_f32 v188,1.0,v188
v_add_f32 v189,1.0,v189
v_add_f32 v190,1.0,v190
v_add_f32 v191,1.0,v191
s_setprio 1
s_waitcnt lgkmcnt(0)
v_mfma_f32_32x32x16_bf16 v[160:175],v[200:203],v[72:75],v[160:175]
v_rcp_f32 v176,v176
v_rcp_f32 v177,v177
v_rcp_f32 v178,v178
v_mfma_f32_32x32x16_bf16 v[160:175],v[204:207],v[76:79],v[160:175]
v_rcp_f32 v179,v179
v_rcp_f32 v180,v180
v_rcp_f32 v181,v181
v_mfma_f32_32x32x16_bf16 v[160:175],v[208:211],v[80:83],v[160:175]
v_rcp_f32 v182,v182
v_rcp_f32 v183,v183
v_rcp_f32 v184,v184
v_mfma_f32_32x32x16_bf16 v[160:175],v[212:215],v[84:87],v[160:175]
v_rcp_f32 v185,v185
v_rcp_f32 v186,v186
v_rcp_f32 v187,v187
v_mfma_f32_32x32x16_bf16 v[160:175],v[216:219],v[88:91],v[160:175]
v_rcp_f32 v188,v188
v_rcp_f32 v189,v189
v_mfma_f32_32x32x16_bf16 v[160:175],v[220:223],v[92:95],v[160:175]
v_rcp_f32 v190,v190
v_rcp_f32 v191,v191
ds_read_b64_tr_b16 v[200:201],v17 offset:0
ds_read_b64_tr_b16 v[202:203],v17 offset:1536
ds_read_b64_tr_b16 v[204:205],v17 offset:3072
ds_read_b64_tr_b16 v[206:207],v17 offset:4608
ds_read_b64_tr_b16 v[208:209],v17 offset:64
ds_read_b64_tr_b16 v[210:211],v17 offset:1600
ds_read_b64_tr_b16 v[212:213],v17 offset:3136
ds_read_b64_tr_b16 v[214:215],v17 offset:4672
ds_read_b64_tr_b16 v[216:217],v17 offset:128
ds_read_b64_tr_b16 v[218:219],v17 offset:1664
ds_read_b64_tr_b16 v[220:221],v17 offset:3200
ds_read_b64_tr_b16 v[222:223],v17 offset:4736
s_mov_b64 exec,s[62:63]
s_add_u32 m0,s68,3456
s_add_u32 s64,s59,98304
buffer_load_dwordx4 v27,s[36:39],s64 offen lds
s_mov_b64 exec,-1
s_setprio 0
v_mul_f32 v176,v144,v176
v_mul_f32 v177,v145,v177
v_mul_f32 v178,v146,v178
v_mul_f32 v179,v147,v179
v_mul_f32 v180,v148,v180
v_mul_f32 v181,v149,v181
v_mul_f32 v182,v150,v182
v_mul_f32 v183,v151,v183
v_mul_f32 v184,v152,v184
v_mul_f32 v185,v153,v185
v_mul_f32 v186,v154,v186
v_mul_f32 v187,v155,v187
v_mul_f32 v188,v156,v188
v_mul_f32 v189,v157,v189
v_mul_f32 v190,v158,v190
v_mul_f32 v191,v159,v191
v_cvt_pk_bf16_f32 v192,v176,v177
v_cvt_pk_bf16_f32 v193,v178,v179
v_cvt_pk_bf16_f32 v194,v180,v181
v_cvt_pk_bf16_f32 v195,v182,v183
v_cvt_pk_bf16_f32 v196,v184,v185
v_cvt_pk_bf16_f32 v197,v186,v187
v_cvt_pk_bf16_f32 v198,v188,v189
v_cvt_pk_bf16_f32 v199,v190,v191
s_waitcnt lgkmcnt(0)
s_barrier
v_fmaak_f32 v176,v160,v160,0x409d30fc
v_fmaak_f32 v177,v161,v161,0x409d30fc
v_fmaak_f32 v178,v162,v162,0x409d30fc
v_fmaak_f32 v179,v163,v163,0x409d30fc
v_fmaak_f32 v180,v164,v164,0x409d30fc
v_fmaak_f32 v181,v165,v165,0x409d30fc
v_fmaak_f32 v182,v166,v166,0x409d30fc
v_fmaak_f32 v183,v167,v167,0x409d30fc
v_fmaak_f32 v184,v168,v168,0x409d30fc
v_fmaak_f32 v185,v169,v169,0x409d30fc
v_fmaak_f32 v186,v170,v170,0x409d30fc
v_fmaak_f32 v187,v171,v171,0x409d30fc
v_fmaak_f32 v188,v172,v172,0x409d30fc
v_fmaak_f32 v189,v173,v173,0x409d30fc
v_fmaak_f32 v190,v174,v174,0x409d30fc
v_fmaak_f32 v191,v175,v175,0x409d30fc
v_mul_f32 v176,v160,v176
v_mul_f32 v177,v161,v177
v_mul_f32 v178,v162,v178
v_mul_f32 v179,v163,v179
v_mul_f32 v180,v164,v180
v_mul_f32 v181,v165,v181
v_mul_f32 v182,v166,v182
v_mul_f32 v183,v167,v183
v_mul_f32 v184,v168,v184
v_mul_f32 v185,v169,v185
v_mul_f32 v186,v170,v186
v_mul_f32 v187,v171,v187
v_mul_f32 v188,v172,v188
v_mul_f32 v189,v173,v189
v_mul_f32 v190,v174,v190
v_mul_f32 v191,v175,v191
s_setprio 1
v_mfma_f32_32x32x16_bf16 v[96:111],v[200:203],v[192:195],v[96:111]
v_exp_f32 v176,v176
v_exp_f32 v177,v177
v_exp_f32 v178,v178
v_mfma_f32_32x32x16_bf16 v[96:111],v[204:207],v[196:199],v[96:111]
v_exp_f32 v179,v179
v_exp_f32 v180,v180
v_exp_f32 v181,v181
v_mfma_f32_32x32x16_bf16 v[112:127],v[208:211],v[192:195],v[112:127]
v_exp_f32 v182,v182
v_exp_f32 v183,v183
v_exp_f32 v184,v184
v_mfma_f32_32x32x16_bf16 v[112:127],v[212:215],v[196:199],v[112:127]
v_exp_f32 v185,v185
v_exp_f32 v186,v186
v_exp_f32 v187,v187
v_mfma_f32_32x32x16_bf16 v[128:143],v[216:219],v[192:195],v[128:143]
v_exp_f32 v188,v188
v_exp_f32 v189,v189
v_mfma_f32_32x32x16_bf16 v[128:143],v[220:223],v[196:199],v[128:143]
v_exp_f32 v190,v190
v_exp_f32 v191,v191
ds_read_b64_tr_b16 v[200:201],v16 offset:12288
ds_read_b64_tr_b16 v[202:203],v16 offset:12800
ds_read_b64_tr_b16 v[204:205],v16 offset:13312
ds_read_b64_tr_b16 v[206:207],v16 offset:13824
ds_read_b64_tr_b16 v[208:209],v16 offset:14336
ds_read_b64_tr_b16 v[210:211],v16 offset:14848
ds_read_b64_tr_b16 v[212:213],v16 offset:15360
ds_read_b64_tr_b16 v[214:215],v16 offset:15872
ds_read_b64_tr_b16 v[216:217],v16 offset:16384
ds_read_b64_tr_b16 v[218:219],v16 offset:16896
ds_read_b64_tr_b16 v[220:221],v16 offset:17408
ds_read_b64_tr_b16 v[222:223],v16 offset:17920
ds_read_b128 v[144:147],v18 offset:256
ds_read_b128 v[148:151],v18 offset:288
ds_read_b128 v[152:155],v18 offset:320
ds_read_b128 v[156:159],v18 offset:352
s_mov_b64 exec,s[62:63]
s_add_u32 m0,s68,6912
s_add_u32 s64,s59,196608
buffer_load_dwordx4 v27,s[36:39],s64 offen lds
s_mov_b64 exec,-1
s_setprio 0
v_add_f32 v176,1.0,v176
v_add_f32 v177,1.0,v177
v_add_f32 v178,1.0,v178
v_add_f32 v179,1.0,v179
v_add_f32 v180,1.0,v180
v_add_f32 v181,1.0,v181
v_add_f32 v182,1.0,v182
v_add_f32 v183,1.0,v183
v_add_f32 v184,1.0,v184
v_add_f32 v185,1.0,v185
v_add_f32 v186,1.0,v186
v_add_f32 v187,1.0,v187
v_add_f32 v188,1.0,v188
v_add_f32 v189,1.0,v189
v_add_f32 v190,1.0,v190
v_add_f32 v191,1.0,v191
s_setprio 1
s_waitcnt lgkmcnt(0)
v_mfma_f32_32x32x16_bf16 v[144:159],v[200:203],v[72:75],v[144:159]
v_rcp_f32 v176,v176
v_rcp_f32 v177,v177
v_rcp_f32 v178,v178
v_mfma_f32_32x32x16_bf16 v[144:159],v[204:207],v[76:79],v[144:159]
v_rcp_f32 v179,v179
v_rcp_f32 v180,v180
v_rcp_f32 v181,v181
v_mfma_f32_32x32x16_bf16 v[144:159],v[208:211],v[80:83],v[144:159]
v_rcp_f32 v182,v182
v_rcp_f32 v183,v183
v_rcp_f32 v184,v184
v_mfma_f32_32x32x16_bf16 v[144:159],v[212:215],v[84:87],v[144:159]
v_rcp_f32 v185,v185
v_rcp_f32 v186,v186
v_rcp_f32 v187,v187
v_mfma_f32_32x32x16_bf16 v[144:159],v[216:219],v[88:91],v[144:159]
v_rcp_f32 v188,v188
v_rcp_f32 v189,v189
v_mfma_f32_32x32x16_bf16 v[144:159],v[220:223],v[92:95],v[144:159]
v_rcp_f32 v190,v190
v_rcp_f32 v191,v191
ds_read_b64_tr_b16 v[200:201],v17 offset:6144
ds_read_b64_tr_b16 v[202:203],v17 offset:7680
ds_read_b64_tr_b16 v[204:205],v17 offset:9216
ds_read_b64_tr_b16 v[206:207],v17 offset:10752
ds_read_b64_tr_b16 v[208:209],v17 offset:6208
ds_read_b64_tr_b16 v[210:211],v17 offset:7744
ds_read_b64_tr_b16 v[212:213],v17 offset:9280
ds_read_b64_tr_b16 v[214:215],v17 offset:10816
ds_read_b64_tr_b16 v[216:217],v17 offset:6272
ds_read_b64_tr_b16 v[218:219],v17 offset:7808
ds_read_b64_tr_b16 v[220:221],v17 offset:9344
ds_read_b64_tr_b16 v[222:223],v17 offset:10880
s_mov_b64 exec,s[62:63]
s_add_u32 m0,s68,10368
s_add_u32 s64,s59,294912
buffer_load_dwordx4 v27,s[36:39],s64 offen lds
s_mov_b64 exec,-1
s_setprio 0
v_mul_f32 v176,v160,v176
v_mul_f32 v177,v161,v177
v_mul_f32 v178,v162,v178
v_mul_f32 v179,v163,v179
v_mul_f32 v180,v164,v180
v_mul_f32 v181,v165,v181
v_mul_f32 v182,v166,v182
v_mul_f32 v183,v167,v183
v_mul_f32 v184,v168,v184
v_mul_f32 v185,v169,v185
v_mul_f32 v186,v170,v186
v_mul_f32 v187,v171,v187
v_mul_f32 v188,v172,v188
v_mul_f32 v189,v173,v189
v_mul_f32 v190,v174,v190
v_mul_f32 v191,v175,v191
v_cvt_pk_bf16_f32 v192,v176,v177
v_cvt_pk_bf16_f32 v193,v178,v179
v_cvt_pk_bf16_f32 v194,v180,v181
v_cvt_pk_bf16_f32 v195,v182,v183
v_cvt_pk_bf16_f32 v196,v184,v185
v_cvt_pk_bf16_f32 v197,v186,v187
v_cvt_pk_bf16_f32 v198,v188,v189
v_cvt_pk_bf16_f32 v199,v190,v191
s_waitcnt lgkmcnt(0)
s_barrier
v_fmaak_f32 v176,v144,v144,0x409d30fc
v_fmaak_f32 v177,v145,v145,0x409d30fc
v_fmaak_f32 v178,v146,v146,0x409d30fc
v_fmaak_f32 v179,v147,v147,0x409d30fc
v_fmaak_f32 v180,v148,v148,0x409d30fc
v_fmaak_f32 v181,v149,v149,0x409d30fc
v_fmaak_f32 v182,v150,v150,0x409d30fc
v_fmaak_f32 v183,v151,v151,0x409d30fc
v_fmaak_f32 v184,v152,v152,0x409d30fc
v_fmaak_f32 v185,v153,v153,0x409d30fc
v_fmaak_f32 v186,v154,v154,0x409d30fc
v_fmaak_f32 v187,v155,v155,0x409d30fc
v_fmaak_f32 v188,v156,v156,0x409d30fc
v_fmaak_f32 v189,v157,v157,0x409d30fc
v_fmaak_f32 v190,v158,v158,0x409d30fc
v_fmaak_f32 v191,v159,v159,0x409d30fc
v_mul_f32 v176,v144,v176
v_mul_f32 v177,v145,v177
v_mul_f32 v178,v146,v178
v_mul_f32 v179,v147,v179
v_mul_f32 v180,v148,v180
v_mul_f32 v181,v149,v181
v_mul_f32 v182,v150,v182
v_mul_f32 v183,v151,v183
v_mul_f32 v184,v152,v184
v_mul_f32 v185,v153,v185
v_mul_f32 v186,v154,v186
v_mul_f32 v187,v155,v187
v_mul_f32 v188,v156,v188
v_mul_f32 v189,v157,v189
v_mul_f32 v190,v158,v190
v_mul_f32 v191,v159,v191
s_setprio 1
v_mfma_f32_32x32x16_bf16 v[96:111],v[200:203],v[192:195],v[96:111]
v_exp_f32 v176,v176
v_exp_f32 v177,v177
v_exp_f32 v178,v178
v_mfma_f32_32x32x16_bf16 v[96:111],v[204:207],v[196:199],v[96:111]
v_exp_f32 v179,v179
v_exp_f32 v180,v180
v_exp_f32 v181,v181
v_mfma_f32_32x32x16_bf16 v[112:127],v[208:211],v[192:195],v[112:127]
v_exp_f32 v182,v182
v_exp_f32 v183,v183
v_exp_f32 v184,v184
v_mfma_f32_32x32x16_bf16 v[112:127],v[212:215],v[196:199],v[112:127]
v_exp_f32 v185,v185
v_exp_f32 v186,v186
v_exp_f32 v187,v187
v_mfma_f32_32x32x16_bf16 v[128:143],v[216:219],v[192:195],v[128:143]
v_exp_f32 v188,v188
v_exp_f32 v189,v189
v_mfma_f32_32x32x16_bf16 v[128:143],v[220:223],v[196:199],v[128:143]
v_exp_f32 v190,v190
v_exp_f32 v191,v191
ds_read_b64_tr_b16 v[200:201],v16 offset:0
ds_read_b64_tr_b16 v[202:203],v16 offset:512
ds_read_b64_tr_b16 v[204:205],v16 offset:1024
ds_read_b64_tr_b16 v[206:207],v16 offset:1536
ds_read_b64_tr_b16 v[208:209],v16 offset:2048
ds_read_b64_tr_b16 v[210:211],v16 offset:2560
ds_read_b64_tr_b16 v[212:213],v16 offset:3072
ds_read_b64_tr_b16 v[214:215],v16 offset:3584
ds_read_b64_tr_b16 v[216:217],v16 offset:4096
ds_read_b64_tr_b16 v[218:219],v16 offset:4608
ds_read_b64_tr_b16 v[220:221],v16 offset:5120
ds_read_b64_tr_b16 v[222:223],v16 offset:5632
ds_read_b128 v[160:163],v18 offset:384
ds_read_b128 v[164:167],v18 offset:416
ds_read_b128 v[168:171],v18 offset:448
ds_read_b128 v[172:175],v18 offset:480
s_mov_b64 exec,s[62:63]
s_add_u32 m0,s68,13824
s_add_u32 s64,s59,393216
buffer_load_dwordx4 v27,s[36:39],s64 offen lds
s_mov_b64 exec,-1
s_setprio 0
v_add_f32 v176,1.0,v176
v_add_f32 v177,1.0,v177
v_add_f32 v178,1.0,v178
v_add_f32 v179,1.0,v179
v_add_f32 v180,1.0,v180
v_add_f32 v181,1.0,v181
v_add_f32 v182,1.0,v182
v_add_f32 v183,1.0,v183
v_add_f32 v184,1.0,v184
v_add_f32 v185,1.0,v185
v_add_f32 v186,1.0,v186
v_add_f32 v187,1.0,v187
v_add_f32 v188,1.0,v188
v_add_f32 v189,1.0,v189
v_add_f32 v190,1.0,v190
v_add_f32 v191,1.0,v191
s_setprio 1
s_waitcnt lgkmcnt(0)
v_mfma_f32_32x32x16_bf16 v[160:175],v[200:203],v[72:75],v[160:175]
v_rcp_f32 v176,v176
v_rcp_f32 v177,v177
v_rcp_f32 v178,v178
v_mfma_f32_32x32x16_bf16 v[160:175],v[204:207],v[76:79],v[160:175]
v_rcp_f32 v179,v179
v_rcp_f32 v180,v180
v_rcp_f32 v181,v181
v_mfma_f32_32x32x16_bf16 v[160:175],v[208:211],v[80:83],v[160:175]
v_rcp_f32 v182,v182
v_rcp_f32 v183,v183
v_rcp_f32 v184,v184
v_mfma_f32_32x32x16_bf16 v[160:175],v[212:215],v[84:87],v[160:175]
v_rcp_f32 v185,v185
v_rcp_f32 v186,v186
v_rcp_f32 v187,v187
v_mfma_f32_32x32x16_bf16 v[160:175],v[216:219],v[88:91],v[160:175]
v_rcp_f32 v188,v188
v_rcp_f32 v189,v189
v_mfma_f32_32x32x16_bf16 v[160:175],v[220:223],v[92:95],v[160:175]
v_rcp_f32 v190,v190
v_rcp_f32 v191,v191
ds_read_b64_tr_b16 v[200:201],v17 offset:12288
ds_read_b64_tr_b16 v[202:203],v17 offset:13824
ds_read_b64_tr_b16 v[204:205],v17 offset:15360
ds_read_b64_tr_b16 v[206:207],v17 offset:16896
ds_read_b64_tr_b16 v[208:209],v17 offset:12352
ds_read_b64_tr_b16 v[210:211],v17 offset:13888
ds_read_b64_tr_b16 v[212:213],v17 offset:15424
ds_read_b64_tr_b16 v[214:215],v17 offset:16960
ds_read_b64_tr_b16 v[216:217],v17 offset:12416
ds_read_b64_tr_b16 v[218:219],v17 offset:13952
ds_read_b64_tr_b16 v[220:221],v17 offset:15488
ds_read_b64_tr_b16 v[222:223],v17 offset:17024
s_mov_b64 exec,s[62:63]
s_add_u32 m0,s68,17280
s_add_u32 s64,s59,491520
buffer_load_dwordx4 v27,s[36:39],s64 offen lds
s_mov_b64 exec,-1
s_setprio 0
v_mul_f32 v176,v144,v176
v_mul_f32 v177,v145,v177
v_mul_f32 v178,v146,v178
v_mul_f32 v179,v147,v179
v_mul_f32 v180,v148,v180
v_mul_f32 v181,v149,v181
v_mul_f32 v182,v150,v182
v_mul_f32 v183,v151,v183
v_mul_f32 v184,v152,v184
v_mul_f32 v185,v153,v185
v_mul_f32 v186,v154,v186
v_mul_f32 v187,v155,v187
v_mul_f32 v188,v156,v188
v_mul_f32 v189,v157,v189
v_mul_f32 v190,v158,v190
v_mul_f32 v191,v159,v191
v_cvt_pk_bf16_f32 v192,v176,v177
v_cvt_pk_bf16_f32 v193,v178,v179
v_cvt_pk_bf16_f32 v194,v180,v181
v_cvt_pk_bf16_f32 v195,v182,v183
v_cvt_pk_bf16_f32 v196,v184,v185
v_cvt_pk_bf16_f32 v197,v186,v187
v_cvt_pk_bf16_f32 v198,v188,v189
v_cvt_pk_bf16_f32 v199,v190,v191
s_waitcnt lgkmcnt(0)
s_barrier
v_fmaak_f32 v176,v160,v160,0x409d30fc
v_fmaak_f32 v177,v161,v161,0x409d30fc
v_fmaak_f32 v178,v162,v162,0x409d30fc
v_fmaak_f32 v179,v163,v163,0x409d30fc
v_fmaak_f32 v180,v164,v164,0x409d30fc
v_fmaak_f32 v181,v165,v165,0x409d30fc
v_fmaak_f32 v182,v166,v166,0x409d30fc
v_fmaak_f32 v183,v167,v167,0x409d30fc
v_fmaak_f32 v184,v168,v168,0x409d30fc
v_fmaak_f32 v185,v169,v169,0x409d30fc
v_fmaak_f32 v186,v170,v170,0x409d30fc
v_fmaak_f32 v187,v171,v171,0x409d30fc
v_fmaak_f32 v188,v172,v172,0x409d30fc
v_fmaak_f32 v189,v173,v173,0x409d30fc
v_fmaak_f32 v190,v174,v174,0x409d30fc
v_fmaak_f32 v191,v175,v175,0x409d30fc
v_mul_f32 v176,v160,v176
v_mul_f32 v177,v161,v177
v_mul_f32 v178,v162,v178
v_mul_f32 v179,v163,v179
v_mul_f32 v180,v164,v180
v_mul_f32 v181,v165,v181
v_mul_f32 v182,v166,v182
v_mul_f32 v183,v167,v183
v_mul_f32 v184,v168,v184
v_mul_f32 v185,v169,v185
v_mul_f32 v186,v170,v186
v_mul_f32 v187,v171,v187
v_mul_f32 v188,v172,v188
v_mul_f32 v189,v173,v189
v_mul_f32 v190,v174,v190
v_mul_f32 v191,v175,v191
s_setprio 1
v_mfma_f32_32x32x16_bf16 v[96:111],v[200:203],v[192:195],v[96:111]
v_exp_f32 v176,v176
v_exp_f32 v177,v177
v_exp_f32 v178,v178
v_mfma_f32_32x32x16_bf16 v[96:111],v[204:207],v[196:199],v[96:111]
v_exp_f32 v179,v179
v_exp_f32 v180,v180
v_exp_f32 v181,v181
v_mfma_f32_32x32x16_bf16 v[112:127],v[208:211],v[192:195],v[112:127]
v_exp_f32 v182,v182
v_exp_f32 v183,v183
v_exp_f32 v184,v184
v_mfma_f32_32x32x16_bf16 v[112:127],v[212:215],v[196:199],v[112:127]
v_exp_f32 v185,v185
v_exp_f32 v186,v186
v_exp_f32 v187,v187
v_mfma_f32_32x32x16_bf16 v[128:143],v[216:219],v[192:195],v[128:143]
v_exp_f32 v188,v188
v_exp_f32 v189,v189
v_mfma_f32_32x32x16_bf16 v[128:143],v[220:223],v[196:199],v[128:143]
v_exp_f32 v190,v190
v_exp_f32 v191,v191
ds_read_b64_tr_b16 v[200:201],v16 offset:6144
ds_read_b64_tr_b16 v[202:203],v16 offset:6656
ds_read_b64_tr_b16 v[204:205],v16 offset:7168
ds_read_b64_tr_b16 v[206:207],v16 offset:7680
ds_read_b64_tr_b16 v[208:209],v16 offset:8192
ds_read_b64_tr_b16 v[210:211],v16 offset:8704
ds_read_b64_tr_b16 v[212:213],v16 offset:9216
ds_read_b64_tr_b16 v[214:215],v16 offset:9728
ds_read_b64_tr_b16 v[216:217],v16 offset:10240
ds_read_b64_tr_b16 v[218:219],v16 offset:10752
ds_read_b64_tr_b16 v[220:221],v16 offset:11264
ds_read_b64_tr_b16 v[222:223],v16 offset:11776
ds_read_b128 v[144:147],v18 offset:512
ds_read_b128 v[148:151],v18 offset:544
ds_read_b128 v[152:155],v18 offset:576
ds_read_b128 v[156:159],v18 offset:608
s_mov_b64 exec,s[62:63]
s_add_u32 m0,s68,20736
s_add_u32 s64,s59,589824
buffer_load_dwordx4 v27,s[36:39],s64 offen lds
s_mov_b64 exec,-1
s_setprio 0
v_add_f32 v176,1.0,v176
v_add_f32 v177,1.0,v177
v_add_f32 v178,1.0,v178
v_add_f32 v179,1.0,v179
v_add_f32 v180,1.0,v180
v_add_f32 v181,1.0,v181
v_add_f32 v182,1.0,v182
v_add_f32 v183,1.0,v183
v_add_f32 v184,1.0,v184
v_add_f32 v185,1.0,v185
v_add_f32 v186,1.0,v186
v_add_f32 v187,1.0,v187
v_add_f32 v188,1.0,v188
v_add_f32 v189,1.0,v189
v_add_f32 v190,1.0,v190
v_add_f32 v191,1.0,v191
s_setprio 1
s_waitcnt lgkmcnt(0)
v_mfma_f32_32x32x16_bf16 v[144:159],v[200:203],v[72:75],v[144:159]
v_rcp_f32 v176,v176
v_rcp_f32 v177,v177
v_rcp_f32 v178,v178
v_mfma_f32_32x32x16_bf16 v[144:159],v[204:207],v[76:79],v[144:159]
v_rcp_f32 v179,v179
v_rcp_f32 v180,v180
v_rcp_f32 v181,v181
v_mfma_f32_32x32x16_bf16 v[144:159],v[208:211],v[80:83],v[144:159]
v_rcp_f32 v182,v182
v_rcp_f32 v183,v183
v_rcp_f32 v184,v184
v_mfma_f32_32x32x16_bf16 v[144:159],v[212:215],v[84:87],v[144:159]
v_rcp_f32 v185,v185
v_rcp_f32 v186,v186
v_rcp_f32 v187,v187
v_mfma_f32_32x32x16_bf16 v[144:159],v[216:219],v[88:91],v[144:159]
v_rcp_f32 v188,v188
v_rcp_f32 v189,v189
v_mfma_f32_32x32x16_bf16 v[144:159],v[220:223],v[92:95],v[144:159]
v_rcp_f32 v190,v190
v_rcp_f32 v191,v191
ds_read_b64_tr_b16 v[200:201],v17 offset:0
ds_read_b64_tr_b16 v[202:203],v17 offset:1536
ds_read_b64_tr_b16 v[204:205],v17 offset:3072
ds_read_b64_tr_b16 v[206:207],v17 offset:4608
ds_read_b64_tr_b16 v[208:209],v17 offset:64
ds_read_b64_tr_b16 v[210:211],v17 offset:1600
ds_read_b64_tr_b16 v[212:213],v17 offset:3136
ds_read_b64_tr_b16 v[214:215],v17 offset:4672
ds_read_b64_tr_b16 v[216:217],v17 offset:128
ds_read_b64_tr_b16 v[218:219],v17 offset:1664
ds_read_b64_tr_b16 v[220:221],v17 offset:3200
ds_read_b64_tr_b16 v[222:223],v17 offset:4736
s_mov_b64 exec,s[62:63]
s_add_u32 m0,s68,24192
s_add_u32 s64,s59,688128
buffer_load_dwordx4 v27,s[36:39],s64 offen lds
s_mov_b64 exec,-1
s_setprio 0
v_mul_f32 v176,v160,v176
v_mul_f32 v177,v161,v177
v_mul_f32 v178,v162,v178
v_mul_f32 v179,v163,v179
v_mul_f32 v180,v164,v180
v_mul_f32 v181,v165,v181
v_mul_f32 v182,v166,v182
v_mul_f32 v183,v167,v183
v_mul_f32 v184,v168,v184
v_mul_f32 v185,v169,v185
v_mul_f32 v186,v170,v186
v_mul_f32 v187,v171,v187
v_mul_f32 v188,v172,v188
v_mul_f32 v189,v173,v189
v_mul_f32 v190,v174,v190
v_mul_f32 v191,v175,v191
v_cvt_pk_bf16_f32 v192,v176,v177
v_cvt_pk_bf16_f32 v193,v178,v179
v_cvt_pk_bf16_f32 v194,v180,v181
v_cvt_pk_bf16_f32 v195,v182,v183
v_cvt_pk_bf16_f32 v196,v184,v185
v_cvt_pk_bf16_f32 v197,v186,v187
v_cvt_pk_bf16_f32 v198,v188,v189
v_cvt_pk_bf16_f32 v199,v190,v191
s_waitcnt lgkmcnt(0)
s_barrier
v_fmaak_f32 v176,v144,v144,0x409d30fc
v_fmaak_f32 v177,v145,v145,0x409d30fc
v_fmaak_f32 v178,v146,v146,0x409d30fc
v_fmaak_f32 v179,v147,v147,0x409d30fc
v_fmaak_f32 v180,v148,v148,0x409d30fc
v_fmaak_f32 v181,v149,v149,0x409d30fc
v_fmaak_f32 v182,v150,v150,0x409d30fc
v_fmaak_f32 v183,v151,v151,0x409d30fc
v_fmaak_f32 v184,v152,v152,0x409d30fc
v_fmaak_f32 v185,v153,v153,0x409d30fc
v_fmaak_f32 v186,v154,v154,0x409d30fc
v_fmaak_f32 v187,v155,v155,0x409d30fc
v_fmaak_f32 v188,v156,v156,0x409d30fc
v_fmaak_f32 v189,v157,v157,0x409d30fc
v_fmaak_f32 v190,v158,v158,0x409d30fc
v_fmaak_f32 v191,v159,v159,0x409d30fc
v_mul_f32 v176,v144,v176
v_mul_f32 v177,v145,v177
v_mul_f32 v178,v146,v178
v_mul_f32 v179,v147,v179
v_mul_f32 v180,v148,v180
v_mul_f32 v181,v149,v181
v_mul_f32 v182,v150,v182
v_mul_f32 v183,v151,v183
v_mul_f32 v184,v152,v184
v_mul_f32 v185,v153,v185
v_mul_f32 v186,v154,v186
v_mul_f32 v187,v155,v187
v_mul_f32 v188,v156,v188
v_mul_f32 v189,v157,v189
v_mul_f32 v190,v158,v190
v_mul_f32 v191,v159,v191
s_setprio 1
v_mfma_f32_32x32x16_bf16 v[96:111],v[200:203],v[192:195],v[96:111]
v_exp_f32 v176,v176
v_exp_f32 v177,v177
v_exp_f32 v178,v178
v_mfma_f32_32x32x16_bf16 v[96:111],v[204:207],v[196:199],v[96:111]
v_exp_f32 v179,v179
v_exp_f32 v180,v180
v_exp_f32 v181,v181
v_mfma_f32_32x32x16_bf16 v[112:127],v[208:211],v[192:195],v[112:127]
v_exp_f32 v182,v182
v_exp_f32 v183,v183
v_exp_f32 v184,v184
v_mfma_f32_32x32x16_bf16 v[112:127],v[212:215],v[196:199],v[112:127]
v_exp_f32 v185,v185
v_exp_f32 v186,v186
v_exp_f32 v187,v187
v_mfma_f32_32x32x16_bf16 v[128:143],v[216:219],v[192:195],v[128:143]
v_exp_f32 v188,v188
v_exp_f32 v189,v189
v_mfma_f32_32x32x16_bf16 v[128:143],v[220:223],v[196:199],v[128:143]
v_exp_f32 v190,v190
v_exp_f32 v191,v191
ds_read_b64_tr_b16 v[200:201],v16 offset:12288
ds_read_b64_tr_b16 v[202:203],v16 offset:12800
ds_read_b64_tr_b16 v[204:205],v16 offset:13312
ds_read_b64_tr_b16 v[206:207],v16 offset:13824
ds_read_b64_tr_b16 v[208:209],v16 offset:14336
ds_read_b64_tr_b16 v[210:211],v16 offset:14848
ds_read_b64_tr_b16 v[212:213],v16 offset:15360
ds_read_b64_tr_b16 v[214:215],v16 offset:15872
ds_read_b64_tr_b16 v[216:217],v16 offset:16384
ds_read_b64_tr_b16 v[218:219],v16 offset:16896
ds_read_b64_tr_b16 v[220:221],v16 offset:17408
ds_read_b64_tr_b16 v[222:223],v16 offset:17920
ds_read_b128 v[160:163],v18 offset:640
ds_read_b128 v[164:167],v18 offset:672
ds_read_b128 v[168:171],v18 offset:704
ds_read_b128 v[172:175],v18 offset:736
s_mov_b64 exec,s[62:63]
s_add_u32 m0,s68,27648
s_add_u32 s64,s59,786432
buffer_load_dwordx4 v27,s[36:39],s64 offen lds
s_mov_b64 exec,-1
s_setprio 0
v_add_f32 v176,1.0,v176
v_add_f32 v177,1.0,v177
v_add_f32 v178,1.0,v178
v_add_f32 v179,1.0,v179
v_add_f32 v180,1.0,v180
v_add_f32 v181,1.0,v181
v_add_f32 v182,1.0,v182
v_add_f32 v183,1.0,v183
v_add_f32 v184,1.0,v184
v_add_f32 v185,1.0,v185
v_add_f32 v186,1.0,v186
v_add_f32 v187,1.0,v187
v_add_f32 v188,1.0,v188
v_add_f32 v189,1.0,v189
v_add_f32 v190,1.0,v190
v_add_f32 v191,1.0,v191
s_setprio 1
s_waitcnt lgkmcnt(0)
v_mfma_f32_32x32x16_bf16 v[160:175],v[200:203],v[72:75],v[160:175]
v_rcp_f32 v176,v176
v_rcp_f32 v177,v177
v_rcp_f32 v178,v178
v_mfma_f32_32x32x16_bf16 v[160:175],v[204:207],v[76:79],v[160:175]
v_rcp_f32 v179,v179
v_rcp_f32 v180,v180
v_rcp_f32 v181,v181
v_mfma_f32_32x32x16_bf16 v[160:175],v[208:211],v[80:83],v[160:175]
v_rcp_f32 v182,v182
v_rcp_f32 v183,v183
v_rcp_f32 v184,v184
v_mfma_f32_32x32x16_bf16 v[160:175],v[212:215],v[84:87],v[160:175]
v_rcp_f32 v185,v185
v_rcp_f32 v186,v186
v_rcp_f32 v187,v187
v_mfma_f32_32x32x16_bf16 v[160:175],v[216:219],v[88:91],v[160:175]
v_rcp_f32 v188,v188
v_rcp_f32 v189,v189
v_mfma_f32_32x32x16_bf16 v[160:175],v[220:223],v[92:95],v[160:175]
v_rcp_f32 v190,v190
v_rcp_f32 v191,v191
ds_read_b64_tr_b16 v[200:201],v17 offset:6144
ds_read_b64_tr_b16 v[202:203],v17 offset:7680
ds_read_b64_tr_b16 v[204:205],v17 offset:9216
ds_read_b64_tr_b16 v[206:207],v17 offset:10752
ds_read_b64_tr_b16 v[208:209],v17 offset:6208
ds_read_b64_tr_b16 v[210:211],v17 offset:7744
ds_read_b64_tr_b16 v[212:213],v17 offset:9280
ds_read_b64_tr_b16 v[214:215],v17 offset:10816
ds_read_b64_tr_b16 v[216:217],v17 offset:6272
ds_read_b64_tr_b16 v[218:219],v17 offset:7808
ds_read_b64_tr_b16 v[220:221],v17 offset:9344
ds_read_b64_tr_b16 v[222:223],v17 offset:10880
s_mov_b64 exec,s[62:63]
s_add_u32 m0,s68,31104
s_add_u32 s64,s59,884736
buffer_load_dwordx4 v27,s[36:39],s64 offen lds
s_mov_b64 exec,-1
s_setprio 0
v_mul_f32 v176,v144,v176
v_mul_f32 v177,v145,v177
v_mul_f32 v178,v146,v178
v_mul_f32 v179,v147,v179
v_mul_f32 v180,v148,v180
v_mul_f32 v181,v149,v181
v_mul_f32 v182,v150,v182
v_mul_f32 v183,v151,v183
v_mul_f32 v184,v152,v184
v_mul_f32 v185,v153,v185
v_mul_f32 v186,v154,v186
v_mul_f32 v187,v155,v187
v_mul_f32 v188,v156,v188
v_mul_f32 v189,v157,v189
v_mul_f32 v190,v158,v190
v_mul_f32 v191,v159,v191
v_cvt_pk_bf16_f32 v192,v176,v177
v_cvt_pk_bf16_f32 v193,v178,v179
v_cvt_pk_bf16_f32 v194,v180,v181
v_cvt_pk_bf16_f32 v195,v182,v183
v_cvt_pk_bf16_f32 v196,v184,v185
v_cvt_pk_bf16_f32 v197,v186,v187
v_cvt_pk_bf16_f32 v198,v188,v189
v_cvt_pk_bf16_f32 v199,v190,v191
s_waitcnt lgkmcnt(0)
s_barrier
v_fmaak_f32 v176,v160,v160,0x409d30fc
v_fmaak_f32 v177,v161,v161,0x409d30fc
v_fmaak_f32 v178,v162,v162,0x409d30fc
v_fmaak_f32 v179,v163,v163,0x409d30fc
v_fmaak_f32 v180,v164,v164,0x409d30fc
v_fmaak_f32 v181,v165,v165,0x409d30fc
v_fmaak_f32 v182,v166,v166,0x409d30fc
v_fmaak_f32 v183,v167,v167,0x409d30fc
v_fmaak_f32 v184,v168,v168,0x409d30fc
v_fmaak_f32 v185,v169,v169,0x409d30fc
v_fmaak_f32 v186,v170,v170,0x409d30fc
v_fmaak_f32 v187,v171,v171,0x409d30fc
v_fmaak_f32 v188,v172,v172,0x409d30fc
v_fmaak_f32 v189,v173,v173,0x409d30fc
v_fmaak_f32 v190,v174,v174,0x409d30fc
v_fmaak_f32 v191,v175,v175,0x409d30fc
v_mul_f32 v176,v160,v176
v_mul_f32 v177,v161,v177
v_mul_f32 v178,v162,v178
v_mul_f32 v179,v163,v179
v_mul_f32 v180,v164,v180
v_mul_f32 v181,v165,v181
v_mul_f32 v182,v166,v182
v_mul_f32 v183,v167,v183
v_mul_f32 v184,v168,v184
v_mul_f32 v185,v169,v185
v_mul_f32 v186,v170,v186
v_mul_f32 v187,v171,v187
v_mul_f32 v188,v172,v188
v_mul_f32 v189,v173,v189
v_mul_f32 v190,v174,v190
v_mul_f32 v191,v175,v191
s_setprio 1
v_mfma_f32_32x32x16_bf16 v[96:111],v[200:203],v[192:195],v[96:111]
v_exp_f32 v176,v176
v_exp_f32 v177,v177
v_exp_f32 v178,v178
v_mfma_f32_32x32x16_bf16 v[96:111],v[204:207],v[196:199],v[96:111]
v_exp_f32 v179,v179
v_exp_f32 v180,v180
v_exp_f32 v181,v181
v_mfma_f32_32x32x16_bf16 v[112:127],v[208:211],v[192:195],v[112:127]
v_exp_f32 v182,v182
v_exp_f32 v183,v183
v_exp_f32 v184,v184
v_mfma_f32_32x32x16_bf16 v[112:127],v[212:215],v[196:199],v[112:127]
v_exp_f32 v185,v185
v_exp_f32 v186,v186
v_exp_f32 v187,v187
v_mfma_f32_32x32x16_bf16 v[128:143],v[216:219],v[192:195],v[128:143]
v_exp_f32 v188,v188
v_exp_f32 v189,v189
v_mfma_f32_32x32x16_bf16 v[128:143],v[220:223],v[196:199],v[128:143]
v_exp_f32 v190,v190
v_exp_f32 v191,v191
ds_read_b64_tr_b16 v[200:201],v16 offset:0
ds_read_b64_tr_b16 v[202:203],v16 offset:512
ds_read_b64_tr_b16 v[204:205],v16 offset:1024
ds_read_b64_tr_b16 v[206:207],v16 offset:1536
ds_read_b64_tr_b16 v[208:209],v16 offset:2048
ds_read_b64_tr_b16 v[210:211],v16 offset:2560
ds_read_b64_tr_b16 v[212:213],v16 offset:3072
ds_read_b64_tr_b16 v[214:215],v16 offset:3584
ds_read_b64_tr_b16 v[216:217],v16 offset:4096
ds_read_b64_tr_b16 v[218:219],v16 offset:4608
ds_read_b64_tr_b16 v[220:221],v16 offset:5120
ds_read_b64_tr_b16 v[222:223],v16 offset:5632
ds_read_b128 v[144:147],v18 offset:768
ds_read_b128 v[148:151],v18 offset:800
ds_read_b128 v[152:155],v18 offset:832
ds_read_b128 v[156:159],v18 offset:864
s_mov_b64 exec,s[62:63]
s_add_u32 m0,s68,34560
s_add_u32 s64,s59,983040
buffer_load_dwordx4 v27,s[36:39],s64 offen lds
s_mov_b64 exec,-1
s_setprio 0
v_add_f32 v176,1.0,v176
v_add_f32 v177,1.0,v177
v_add_f32 v178,1.0,v178
v_add_f32 v179,1.0,v179
v_add_f32 v180,1.0,v180
v_add_f32 v181,1.0,v181
v_add_f32 v182,1.0,v182
v_add_f32 v183,1.0,v183
v_add_f32 v184,1.0,v184
v_add_f32 v185,1.0,v185
v_add_f32 v186,1.0,v186
v_add_f32 v187,1.0,v187
v_add_f32 v188,1.0,v188
v_add_f32 v189,1.0,v189
v_add_f32 v190,1.0,v190
v_add_f32 v191,1.0,v191
s_setprio 1
s_waitcnt lgkmcnt(0)
v_mfma_f32_32x32x16_bf16 v[144:159],v[200:203],v[72:75],v[144:159]
v_rcp_f32 v176,v176
v_rcp_f32 v177,v177
v_rcp_f32 v178,v178
v_mfma_f32_32x32x16_bf16 v[144:159],v[204:207],v[76:79],v[144:159]
v_rcp_f32 v179,v179
v_rcp_f32 v180,v180
v_rcp_f32 v181,v181
v_mfma_f32_32x32x16_bf16 v[144:159],v[208:211],v[80:83],v[144:159]
v_rcp_f32 v182,v182
v_rcp_f32 v183,v183
v_rcp_f32 v184,v184
v_mfma_f32_32x32x16_bf16 v[144:159],v[212:215],v[84:87],v[144:159]
v_rcp_f32 v185,v185
v_rcp_f32 v186,v186
v_rcp_f32 v187,v187
v_mfma_f32_32x32x16_bf16 v[144:159],v[216:219],v[88:91],v[144:159]
v_rcp_f32 v188,v188
v_rcp_f32 v189,v189
v_mfma_f32_32x32x16_bf16 v[144:159],v[220:223],v[92:95],v[144:159]
v_rcp_f32 v190,v190
v_rcp_f32 v191,v191
ds_read_b64_tr_b16 v[200:201],v17 offset:12288
ds_read_b64_tr_b16 v[202:203],v17 offset:13824
ds_read_b64_tr_b16 v[204:205],v17 offset:15360
ds_read_b64_tr_b16 v[206:207],v17 offset:16896
ds_read_b64_tr_b16 v[208:209],v17 offset:12352
ds_read_b64_tr_b16 v[210:211],v17 offset:13888
ds_read_b64_tr_b16 v[212:213],v17 offset:15424
ds_read_b64_tr_b16 v[214:215],v17 offset:16960
ds_read_b64_tr_b16 v[216:217],v17 offset:12416
ds_read_b64_tr_b16 v[218:219],v17 offset:13952
ds_read_b64_tr_b16 v[220:221],v17 offset:15488
ds_read_b64_tr_b16 v[222:223],v17 offset:17024
s_mov_b64 exec,s[62:63]
s_add_u32 m0,s68,38016
s_add_u32 s64,s59,1081344
buffer_load_dwordx4 v27,s[36:39],s64 offen lds
s_mov_b64 exec,-1
s_setprio 0
v_mul_f32 v176,v160,v176
v_mul_f32 v177,v161,v177
v_mul_f32 v178,v162,v178
v_mul_f32 v179,v163,v179
v_mul_f32 v180,v164,v180
v_mul_f32 v181,v165,v181
v_mul_f32 v182,v166,v182
v_mul_f32 v183,v167,v183
v_mul_f32 v184,v168,v184
v_mul_f32 v185,v169,v185
v_mul_f32 v186,v170,v186
v_mul_f32 v187,v171,v187
v_mul_f32 v188,v172,v188
v_mul_f32 v189,v173,v189
v_mul_f32 v190,v174,v190
v_mul_f32 v191,v175,v191
v_cvt_pk_bf16_f32 v192,v176,v177
v_cvt_pk_bf16_f32 v193,v178,v179
v_cvt_pk_bf16_f32 v194,v180,v181
v_cvt_pk_bf16_f32 v195,v182,v183
v_cvt_pk_bf16_f32 v196,v184,v185
v_cvt_pk_bf16_f32 v197,v186,v187
v_cvt_pk_bf16_f32 v198,v188,v189
v_cvt_pk_bf16_f32 v199,v190,v191
s_waitcnt lgkmcnt(0)
s_barrier
v_fmaak_f32 v176,v144,v144,0x409d30fc
v_fmaak_f32 v177,v145,v145,0x409d30fc
v_fmaak_f32 v178,v146,v146,0x409d30fc
v_fmaak_f32 v179,v147,v147,0x409d30fc
v_fmaak_f32 v180,v148,v148,0x409d30fc
v_fmaak_f32 v181,v149,v149,0x409d30fc
v_fmaak_f32 v182,v150,v150,0x409d30fc
v_fmaak_f32 v183,v151,v151,0x409d30fc
v_fmaak_f32 v184,v152,v152,0x409d30fc
v_fmaak_f32 v185,v153,v153,0x409d30fc
v_fmaak_f32 v186,v154,v154,0x409d30fc
v_fmaak_f32 v187,v155,v155,0x409d30fc
v_fmaak_f32 v188,v156,v156,0x409d30fc
v_fmaak_f32 v189,v157,v157,0x409d30fc
v_fmaak_f32 v190,v158,v158,0x409d30fc
v_fmaak_f32 v191,v159,v159,0x409d30fc
v_mul_f32 v176,v144,v176
v_mul_f32 v177,v145,v177
v_mul_f32 v178,v146,v178
v_mul_f32 v179,v147,v179
v_mul_f32 v180,v148,v180
v_mul_f32 v181,v149,v181
v_mul_f32 v182,v150,v182
v_mul_f32 v183,v151,v183
v_mul_f32 v184,v152,v184
v_mul_f32 v185,v153,v185
v_mul_f32 v186,v154,v186
v_mul_f32 v187,v155,v187
v_mul_f32 v188,v156,v188
v_mul_f32 v189,v157,v189
v_mul_f32 v190,v158,v190
v_mul_f32 v191,v159,v191
s_setprio 1
v_mfma_f32_32x32x16_bf16 v[96:111],v[200:203],v[192:195],v[96:111]
v_exp_f32 v176,v176
v_exp_f32 v177,v177
v_exp_f32 v178,v178
v_mfma_f32_32x32x16_bf16 v[96:111],v[204:207],v[196:199],v[96:111]
v_exp_f32 v179,v179
v_exp_f32 v180,v180
v_exp_f32 v181,v181
v_mfma_f32_32x32x16_bf16 v[112:127],v[208:211],v[192:195],v[112:127]
v_exp_f32 v182,v182
v_exp_f32 v183,v183
v_exp_f32 v184,v184
v_mfma_f32_32x32x16_bf16 v[112:127],v[212:215],v[196:199],v[112:127]
v_exp_f32 v185,v185
v_exp_f32 v186,v186
v_exp_f32 v187,v187
v_mfma_f32_32x32x16_bf16 v[128:143],v[216:219],v[192:195],v[128:143]
v_exp_f32 v188,v188
v_exp_f32 v189,v189
v_mfma_f32_32x32x16_bf16 v[128:143],v[220:223],v[196:199],v[128:143]
v_exp_f32 v190,v190
v_exp_f32 v191,v191
ds_read_b64_tr_b16 v[200:201],v16 offset:6144
ds_read_b64_tr_b16 v[202:203],v16 offset:6656
ds_read_b64_tr_b16 v[204:205],v16 offset:7168
ds_read_b64_tr_b16 v[206:207],v16 offset:7680
ds_read_b64_tr_b16 v[208:209],v16 offset:8192
ds_read_b64_tr_b16 v[210:211],v16 offset:8704
ds_read_b64_tr_b16 v[212:213],v16 offset:9216
ds_read_b64_tr_b16 v[214:215],v16 offset:9728
ds_read_b64_tr_b16 v[216:217],v16 offset:10240
ds_read_b64_tr_b16 v[218:219],v16 offset:10752
ds_read_b64_tr_b16 v[220:221],v16 offset:11264
ds_read_b64_tr_b16 v[222:223],v16 offset:11776
ds_read_b128 v[160:163],v18 offset:896
ds_read_b128 v[164:167],v18 offset:928
ds_read_b128 v[168:171],v18 offset:960
ds_read_b128 v[172:175],v18 offset:992
s_mov_b64 exec,s[62:63]
s_add_u32 m0,s68,41472
s_add_u32 s64,s59,1179648
buffer_load_dwordx4 v27,s[36:39],s64 offen lds
s_mov_b64 exec,-1
s_setprio 0
v_add_f32 v176,1.0,v176
v_add_f32 v177,1.0,v177
v_add_f32 v178,1.0,v178
v_add_f32 v179,1.0,v179
v_add_f32 v180,1.0,v180
v_add_f32 v181,1.0,v181
v_add_f32 v182,1.0,v182
v_add_f32 v183,1.0,v183
v_add_f32 v184,1.0,v184
v_add_f32 v185,1.0,v185
v_add_f32 v186,1.0,v186
v_add_f32 v187,1.0,v187
v_add_f32 v188,1.0,v188
v_add_f32 v189,1.0,v189
v_add_f32 v190,1.0,v190
v_add_f32 v191,1.0,v191
s_setprio 1
s_waitcnt lgkmcnt(0)
v_mfma_f32_32x32x16_bf16 v[160:175],v[200:203],v[72:75],v[160:175]
v_rcp_f32 v176,v176
v_rcp_f32 v177,v177
v_rcp_f32 v178,v178
v_mfma_f32_32x32x16_bf16 v[160:175],v[204:207],v[76:79],v[160:175]
v_rcp_f32 v179,v179
v_rcp_f32 v180,v180
v_rcp_f32 v181,v181
v_mfma_f32_32x32x16_bf16 v[160:175],v[208:211],v[80:83],v[160:175]
v_rcp_f32 v182,v182
v_rcp_f32 v183,v183
v_rcp_f32 v184,v184
v_mfma_f32_32x32x16_bf16 v[160:175],v[212:215],v[84:87],v[160:175]
v_rcp_f32 v185,v185
v_rcp_f32 v186,v186
v_rcp_f32 v187,v187
v_mfma_f32_32x32x16_bf16 v[160:175],v[216:219],v[88:91],v[160:175]
v_rcp_f32 v188,v188
v_rcp_f32 v189,v189
v_mfma_f32_32x32x16_bf16 v[160:175],v[220:223],v[92:95],v[160:175]
v_rcp_f32 v190,v190
v_rcp_f32 v191,v191
ds_read_b64_tr_b16 v[200:201],v17 offset:0
ds_read_b64_tr_b16 v[202:203],v17 offset:1536
ds_read_b64_tr_b16 v[204:205],v17 offset:3072
ds_read_b64_tr_b16 v[206:207],v17 offset:4608
ds_read_b64_tr_b16 v[208:209],v17 offset:64
ds_read_b64_tr_b16 v[210:211],v17 offset:1600
ds_read_b64_tr_b16 v[212:213],v17 offset:3136
ds_read_b64_tr_b16 v[214:215],v17 offset:4672
ds_read_b64_tr_b16 v[216:217],v17 offset:128
ds_read_b64_tr_b16 v[218:219],v17 offset:1664
ds_read_b64_tr_b16 v[220:221],v17 offset:3200
ds_read_b64_tr_b16 v[222:223],v17 offset:4736
s_mov_b64 exec,s[62:63]
s_add_u32 m0,s68,44928
s_add_u32 s64,s59,1277952
buffer_load_dwordx4 v27,s[36:39],s64 offen lds
s_mov_b64 exec,-1
s_setprio 0
v_mul_f32 v176,v144,v176
v_mul_f32 v177,v145,v177
v_mul_f32 v178,v146,v178
v_mul_f32 v179,v147,v179
v_mul_f32 v180,v148,v180
v_mul_f32 v181,v149,v181
v_mul_f32 v182,v150,v182
v_mul_f32 v183,v151,v183
v_mul_f32 v184,v152,v184
v_mul_f32 v185,v153,v185
v_mul_f32 v186,v154,v186
v_mul_f32 v187,v155,v187
v_mul_f32 v188,v156,v188
v_mul_f32 v189,v157,v189
v_mul_f32 v190,v158,v190
v_mul_f32 v191,v159,v191
v_cvt_pk_bf16_f32 v192,v176,v177
v_cvt_pk_bf16_f32 v193,v178,v179
v_cvt_pk_bf16_f32 v194,v180,v181
v_cvt_pk_bf16_f32 v195,v182,v183
v_cvt_pk_bf16_f32 v196,v184,v185
v_cvt_pk_bf16_f32 v197,v186,v187
v_cvt_pk_bf16_f32 v198,v188,v189
v_cvt_pk_bf16_f32 v199,v190,v191
s_waitcnt lgkmcnt(0)
s_barrier
v_fmaak_f32 v176,v160,v160,0x409d30fc
v_fmaak_f32 v177,v161,v161,0x409d30fc
v_fmaak_f32 v178,v162,v162,0x409d30fc
v_fmaak_f32 v179,v163,v163,0x409d30fc
v_fmaak_f32 v180,v164,v164,0x409d30fc
v_fmaak_f32 v181,v165,v165,0x409d30fc
v_fmaak_f32 v182,v166,v166,0x409d30fc
v_fmaak_f32 v183,v167,v167,0x409d30fc
v_fmaak_f32 v184,v168,v168,0x409d30fc
v_fmaak_f32 v185,v169,v169,0x409d30fc
v_fmaak_f32 v186,v170,v170,0x409d30fc
v_fmaak_f32 v187,v171,v171,0x409d30fc
v_fmaak_f32 v188,v172,v172,0x409d30fc
v_fmaak_f32 v189,v173,v173,0x409d30fc
v_fmaak_f32 v190,v174,v174,0x409d30fc
v_fmaak_f32 v191,v175,v175,0x409d30fc
v_mul_f32 v176,v160,v176
v_mul_f32 v177,v161,v177
v_mul_f32 v178,v162,v178
v_mul_f32 v179,v163,v179
v_mul_f32 v180,v164,v180
v_mul_f32 v181,v165,v181
v_mul_f32 v182,v166,v182
v_mul_f32 v183,v167,v183
v_mul_f32 v184,v168,v184
v_mul_f32 v185,v169,v185
v_mul_f32 v186,v170,v186
v_mul_f32 v187,v171,v187
v_mul_f32 v188,v172,v188
v_mul_f32 v189,v173,v189
v_mul_f32 v190,v174,v190
v_mul_f32 v191,v175,v191
s_setprio 1
v_mfma_f32_32x32x16_bf16 v[96:111],v[200:203],v[192:195],v[96:111]
v_exp_f32 v176,v176
v_exp_f32 v177,v177
v_exp_f32 v178,v178
v_mfma_f32_32x32x16_bf16 v[96:111],v[204:207],v[196:199],v[96:111]
v_exp_f32 v179,v179
v_exp_f32 v180,v180
v_exp_f32 v181,v181
v_mfma_f32_32x32x16_bf16 v[112:127],v[208:211],v[192:195],v[112:127]
v_exp_f32 v182,v182
v_exp_f32 v183,v183
v_exp_f32 v184,v184
v_mfma_f32_32x32x16_bf16 v[112:127],v[212:215],v[196:199],v[112:127]
v_exp_f32 v185,v185
v_exp_f32 v186,v186
v_exp_f32 v187,v187
v_mfma_f32_32x32x16_bf16 v[128:143],v[216:219],v[192:195],v[128:143]
v_exp_f32 v188,v188
v_exp_f32 v189,v189
v_mfma_f32_32x32x16_bf16 v[128:143],v[220:223],v[196:199],v[128:143]
v_exp_f32 v190,v190
v_exp_f32 v191,v191
ds_read_b64_tr_b16 v[200:201],v16 offset:12288
ds_read_b64_tr_b16 v[202:203],v16 offset:12800
ds_read_b64_tr_b16 v[204:205],v16 offset:13312
ds_read_b64_tr_b16 v[206:207],v16 offset:13824
ds_read_b64_tr_b16 v[208:209],v16 offset:14336
ds_read_b64_tr_b16 v[210:211],v16 offset:14848
ds_read_b64_tr_b16 v[212:213],v16 offset:15360
ds_read_b64_tr_b16 v[214:215],v16 offset:15872
ds_read_b64_tr_b16 v[216:217],v16 offset:16384
ds_read_b64_tr_b16 v[218:219],v16 offset:16896
ds_read_b64_tr_b16 v[220:221],v16 offset:17408
ds_read_b64_tr_b16 v[222:223],v16 offset:17920
ds_read_b128 v[144:147],v18 offset:1024
ds_read_b128 v[148:151],v18 offset:1056
ds_read_b128 v[152:155],v18 offset:1088
ds_read_b128 v[156:159],v18 offset:1120
s_mov_b64 exec,s[62:63]
s_add_u32 m0,s68,48384
s_add_u32 s64,s59,1376256
buffer_load_dwordx4 v27,s[36:39],s64 offen lds
s_mov_b64 exec,-1
s_setprio 0
v_add_f32 v176,1.0,v176
v_add_f32 v177,1.0,v177
v_add_f32 v178,1.0,v178
v_add_f32 v179,1.0,v179
v_add_f32 v180,1.0,v180
v_add_f32 v181,1.0,v181
v_add_f32 v182,1.0,v182
v_add_f32 v183,1.0,v183
v_add_f32 v184,1.0,v184
v_add_f32 v185,1.0,v185
v_add_f32 v186,1.0,v186
v_add_f32 v187,1.0,v187
v_add_f32 v188,1.0,v188
v_add_f32 v189,1.0,v189
v_add_f32 v190,1.0,v190
v_add_f32 v191,1.0,v191
s_setprio 1
s_waitcnt lgkmcnt(0)
v_mfma_f32_32x32x16_bf16 v[144:159],v[200:203],v[72:75],v[144:159]
v_rcp_f32 v176,v176
v_rcp_f32 v177,v177
v_rcp_f32 v178,v178
v_mfma_f32_32x32x16_bf16 v[144:159],v[204:207],v[76:79],v[144:159]
v_rcp_f32 v179,v179
v_rcp_f32 v180,v180
v_rcp_f32 v181,v181
v_mfma_f32_32x32x16_bf16 v[144:159],v[208:211],v[80:83],v[144:159]
v_rcp_f32 v182,v182
v_rcp_f32 v183,v183
v_rcp_f32 v184,v184
v_mfma_f32_32x32x16_bf16 v[144:159],v[212:215],v[84:87],v[144:159]
v_rcp_f32 v185,v185
v_rcp_f32 v186,v186
v_rcp_f32 v187,v187
v_mfma_f32_32x32x16_bf16 v[144:159],v[216:219],v[88:91],v[144:159]
v_rcp_f32 v188,v188
v_rcp_f32 v189,v189
v_mfma_f32_32x32x16_bf16 v[144:159],v[220:223],v[92:95],v[144:159]
v_rcp_f32 v190,v190
v_rcp_f32 v191,v191
ds_read_b64_tr_b16 v[200:201],v17 offset:6144
ds_read_b64_tr_b16 v[202:203],v17 offset:7680
ds_read_b64_tr_b16 v[204:205],v17 offset:9216
ds_read_b64_tr_b16 v[206:207],v17 offset:10752
ds_read_b64_tr_b16 v[208:209],v17 offset:6208
ds_read_b64_tr_b16 v[210:211],v17 offset:7744
ds_read_b64_tr_b16 v[212:213],v17 offset:9280
ds_read_b64_tr_b16 v[214:215],v17 offset:10816
ds_read_b64_tr_b16 v[216:217],v17 offset:6272
ds_read_b64_tr_b16 v[218:219],v17 offset:7808
ds_read_b64_tr_b16 v[220:221],v17 offset:9344
ds_read_b64_tr_b16 v[222:223],v17 offset:10880
s_mov_b64 exec,s[62:63]
s_add_u32 m0,s68,51840
s_add_u32 s64,s59,1474560
buffer_load_dwordx4 v27,s[36:39],s64 offen lds
s_mov_b64 exec,-1
s_setprio 0
v_mul_f32 v176,v160,v176
v_mul_f32 v177,v161,v177
v_mul_f32 v178,v162,v178
v_mul_f32 v179,v163,v179
v_mul_f32 v180,v164,v180
v_mul_f32 v181,v165,v181
v_mul_f32 v182,v166,v182
v_mul_f32 v183,v167,v183
v_mul_f32 v184,v168,v184
v_mul_f32 v185,v169,v185
v_mul_f32 v186,v170,v186
v_mul_f32 v187,v171,v187
v_mul_f32 v188,v172,v188
v_mul_f32 v189,v173,v189
v_mul_f32 v190,v174,v190
v_mul_f32 v191,v175,v191
v_cvt_pk_bf16_f32 v192,v176,v177
v_cvt_pk_bf16_f32 v193,v178,v179
v_cvt_pk_bf16_f32 v194,v180,v181
v_cvt_pk_bf16_f32 v195,v182,v183
v_cvt_pk_bf16_f32 v196,v184,v185
v_cvt_pk_bf16_f32 v197,v186,v187
v_cvt_pk_bf16_f32 v198,v188,v189
v_cvt_pk_bf16_f32 v199,v190,v191
s_waitcnt lgkmcnt(0)
s_barrier
v_fmaak_f32 v176,v144,v144,0x409d30fc
v_fmaak_f32 v177,v145,v145,0x409d30fc
v_fmaak_f32 v178,v146,v146,0x409d30fc
v_fmaak_f32 v179,v147,v147,0x409d30fc
v_fmaak_f32 v180,v148,v148,0x409d30fc
v_fmaak_f32 v181,v149,v149,0x409d30fc
v_fmaak_f32 v182,v150,v150,0x409d30fc
v_fmaak_f32 v183,v151,v151,0x409d30fc
v_fmaak_f32 v184,v152,v152,0x409d30fc
v_fmaak_f32 v185,v153,v153,0x409d30fc
v_fmaak_f32 v186,v154,v154,0x409d30fc
v_fmaak_f32 v187,v155,v155,0x409d30fc
v_fmaak_f32 v188,v156,v156,0x409d30fc
v_fmaak_f32 v189,v157,v157,0x409d30fc
v_fmaak_f32 v190,v158,v158,0x409d30fc
v_fmaak_f32 v191,v159,v159,0x409d30fc
v_mul_f32 v176,v144,v176
v_mul_f32 v177,v145,v177
v_mul_f32 v178,v146,v178
v_mul_f32 v179,v147,v179
v_mul_f32 v180,v148,v180
v_mul_f32 v181,v149,v181
v_mul_f32 v182,v150,v182
v_mul_f32 v183,v151,v183
v_mul_f32 v184,v152,v184
v_mul_f32 v185,v153,v185
v_mul_f32 v186,v154,v186
v_mul_f32 v187,v155,v187
v_mul_f32 v188,v156,v188
v_mul_f32 v189,v157,v189
v_mul_f32 v190,v158,v190
v_mul_f32 v191,v159,v191
s_setprio 1
v_mfma_f32_32x32x16_bf16 v[96:111],v[200:203],v[192:195],v[96:111]
v_exp_f32 v176,v176
v_exp_f32 v177,v177
v_exp_f32 v178,v178
v_mfma_f32_32x32x16_bf16 v[96:111],v[204:207],v[196:199],v[96:111]
v_exp_f32 v179,v179
v_exp_f32 v180,v180
v_exp_f32 v181,v181
v_mfma_f32_32x32x16_bf16 v[112:127],v[208:211],v[192:195],v[112:127]
v_exp_f32 v182,v182
v_exp_f32 v183,v183
v_exp_f32 v184,v184
v_mfma_f32_32x32x16_bf16 v[112:127],v[212:215],v[196:199],v[112:127]
v_exp_f32 v185,v185
v_exp_f32 v186,v186
v_exp_f32 v187,v187
v_mfma_f32_32x32x16_bf16 v[128:143],v[216:219],v[192:195],v[128:143]
v_exp_f32 v188,v188
v_exp_f32 v189,v189
v_mfma_f32_32x32x16_bf16 v[128:143],v[220:223],v[196:199],v[128:143]
v_exp_f32 v190,v190
v_exp_f32 v191,v191
ds_read_b64_tr_b16 v[200:201],v16 offset:0
ds_read_b64_tr_b16 v[202:203],v16 offset:512
ds_read_b64_tr_b16 v[204:205],v16 offset:1024
ds_read_b64_tr_b16 v[206:207],v16 offset:1536
ds_read_b64_tr_b16 v[208:209],v16 offset:2048
ds_read_b64_tr_b16 v[210:211],v16 offset:2560
ds_read_b64_tr_b16 v[212:213],v16 offset:3072
ds_read_b64_tr_b16 v[214:215],v16 offset:3584
ds_read_b64_tr_b16 v[216:217],v16 offset:4096
ds_read_b64_tr_b16 v[218:219],v16 offset:4608
ds_read_b64_tr_b16 v[220:221],v16 offset:5120
ds_read_b64_tr_b16 v[222:223],v16 offset:5632
ds_read_b128 v[160:163],v18 offset:1152
ds_read_b128 v[164:167],v18 offset:1184
ds_read_b128 v[168:171],v18 offset:1216
ds_read_b128 v[172:175],v18 offset:1248
s_mov_b64 exec,s[62:63]
s_add_u32 m0,s68,55296
s_add_u32 s64,s59,1572864
buffer_load_dwordx4 v27,s[36:39],s64 offen lds
s_mov_b64 exec,-1
s_setprio 0
v_add_f32 v176,1.0,v176
v_add_f32 v177,1.0,v177
v_add_f32 v178,1.0,v178
v_add_f32 v179,1.0,v179
v_add_f32 v180,1.0,v180
v_add_f32 v181,1.0,v181
v_add_f32 v182,1.0,v182
v_add_f32 v183,1.0,v183
v_add_f32 v184,1.0,v184
v_add_f32 v185,1.0,v185
v_add_f32 v186,1.0,v186
v_add_f32 v187,1.0,v187
v_add_f32 v188,1.0,v188
v_add_f32 v189,1.0,v189
v_add_f32 v190,1.0,v190
v_add_f32 v191,1.0,v191
s_setprio 1
s_waitcnt lgkmcnt(0)
v_mfma_f32_32x32x16_bf16 v[160:175],v[200:203],v[72:75],v[160:175]
v_rcp_f32 v176,v176
v_rcp_f32 v177,v177
v_rcp_f32 v178,v178
v_mfma_f32_32x32x16_bf16 v[160:175],v[204:207],v[76:79],v[160:175]
v_rcp_f32 v179,v179
v_rcp_f32 v180,v180
v_rcp_f32 v181,v181
v_mfma_f32_32x32x16_bf16 v[160:175],v[208:211],v[80:83],v[160:175]
v_rcp_f32 v182,v182
v_rcp_f32 v183,v183
v_rcp_f32 v184,v184
v_mfma_f32_32x32x16_bf16 v[160:175],v[212:215],v[84:87],v[160:175]
v_rcp_f32 v185,v185
v_rcp_f32 v186,v186
v_rcp_f32 v187,v187
v_mfma_f32_32x32x16_bf16 v[160:175],v[216:219],v[88:91],v[160:175]
v_rcp_f32 v188,v188
v_rcp_f32 v189,v189
v_mfma_f32_32x32x16_bf16 v[160:175],v[220:223],v[92:95],v[160:175]
v_rcp_f32 v190,v190
v_rcp_f32 v191,v191
ds_read_b64_tr_b16 v[200:201],v17 offset:12288
ds_read_b64_tr_b16 v[202:203],v17 offset:13824
ds_read_b64_tr_b16 v[204:205],v17 offset:15360
ds_read_b64_tr_b16 v[206:207],v17 offset:16896
ds_read_b64_tr_b16 v[208:209],v17 offset:12352
ds_read_b64_tr_b16 v[210:211],v17 offset:13888
ds_read_b64_tr_b16 v[212:213],v17 offset:15424
ds_read_b64_tr_b16 v[214:215],v17 offset:16960
ds_read_b64_tr_b16 v[216:217],v17 offset:12416
ds_read_b64_tr_b16 v[218:219],v17 offset:13952
ds_read_b64_tr_b16 v[220:221],v17 offset:15488
ds_read_b64_tr_b16 v[222:223],v17 offset:17024
s_mov_b64 exec,s[62:63]
s_add_u32 m0,s68,58752
s_add_u32 s64,s59,1671168
buffer_load_dwordx4 v27,s[36:39],s64 offen lds
s_mov_b64 exec,-1
s_setprio 0
v_mul_f32 v176,v144,v176
v_mul_f32 v177,v145,v177
v_mul_f32 v178,v146,v178
v_mul_f32 v179,v147,v179
v_mul_f32 v180,v148,v180
v_mul_f32 v181,v149,v181
v_mul_f32 v182,v150,v182
v_mul_f32 v183,v151,v183
v_mul_f32 v184,v152,v184
v_mul_f32 v185,v153,v185
v_mul_f32 v186,v154,v186
v_mul_f32 v187,v155,v187
v_mul_f32 v188,v156,v188
v_mul_f32 v189,v157,v189
v_mul_f32 v190,v158,v190
v_mul_f32 v191,v159,v191
v_cvt_pk_bf16_f32 v192,v176,v177
v_cvt_pk_bf16_f32 v193,v178,v179
v_cvt_pk_bf16_f32 v194,v180,v181
v_cvt_pk_bf16_f32 v195,v182,v183
v_cvt_pk_bf16_f32 v196,v184,v185
v_cvt_pk_bf16_f32 v197,v186,v187
v_cvt_pk_bf16_f32 v198,v188,v189
v_cvt_pk_bf16_f32 v199,v190,v191
s_waitcnt lgkmcnt(0)
s_barrier
v_fmaak_f32 v176,v160,v160,0x409d30fc
v_fmaak_f32 v177,v161,v161,0x409d30fc
v_fmaak_f32 v178,v162,v162,0x409d30fc
v_fmaak_f32 v179,v163,v163,0x409d30fc
v_fmaak_f32 v180,v164,v164,0x409d30fc
v_fmaak_f32 v181,v165,v165,0x409d30fc
v_fmaak_f32 v182,v166,v166,0x409d30fc
v_fmaak_f32 v183,v167,v167,0x409d30fc
v_fmaak_f32 v184,v168,v168,0x409d30fc
v_fmaak_f32 v185,v169,v169,0x409d30fc
v_fmaak_f32 v186,v170,v170,0x409d30fc
v_fmaak_f32 v187,v171,v171,0x409d30fc
v_fmaak_f32 v188,v172,v172,0x409d30fc
v_fmaak_f32 v189,v173,v173,0x409d30fc
v_fmaak_f32 v190,v174,v174,0x409d30fc
v_fmaak_f32 v191,v175,v175,0x409d30fc
v_mul_f32 v176,v160,v176
v_mul_f32 v177,v161,v177
v_mul_f32 v178,v162,v178
v_mul_f32 v179,v163,v179
v_mul_f32 v180,v164,v180
v_mul_f32 v181,v165,v181
v_mul_f32 v182,v166,v182
v_mul_f32 v183,v167,v183
v_mul_f32 v184,v168,v184
v_mul_f32 v185,v169,v185
v_mul_f32 v186,v170,v186
v_mul_f32 v187,v171,v187
v_mul_f32 v188,v172,v188
v_mul_f32 v189,v173,v189
v_mul_f32 v190,v174,v190
v_mul_f32 v191,v175,v191
s_setprio 1
v_mfma_f32_32x32x16_bf16 v[96:111],v[200:203],v[192:195],v[96:111]
v_exp_f32 v176,v176
v_exp_f32 v177,v177
v_exp_f32 v178,v178
v_mfma_f32_32x32x16_bf16 v[96:111],v[204:207],v[196:199],v[96:111]
v_exp_f32 v179,v179
v_exp_f32 v180,v180
v_exp_f32 v181,v181
v_mfma_f32_32x32x16_bf16 v[112:127],v[208:211],v[192:195],v[112:127]
v_exp_f32 v182,v182
v_exp_f32 v183,v183
v_exp_f32 v184,v184
v_mfma_f32_32x32x16_bf16 v[112:127],v[212:215],v[196:199],v[112:127]
v_exp_f32 v185,v185
v_exp_f32 v186,v186
v_exp_f32 v187,v187
v_mfma_f32_32x32x16_bf16 v[128:143],v[216:219],v[192:195],v[128:143]
v_exp_f32 v188,v188
v_exp_f32 v189,v189
v_mfma_f32_32x32x16_bf16 v[128:143],v[220:223],v[196:199],v[128:143]
v_exp_f32 v190,v190
v_exp_f32 v191,v191
ds_read_b64_tr_b16 v[200:201],v16 offset:6144
ds_read_b64_tr_b16 v[202:203],v16 offset:6656
ds_read_b64_tr_b16 v[204:205],v16 offset:7168
ds_read_b64_tr_b16 v[206:207],v16 offset:7680
ds_read_b64_tr_b16 v[208:209],v16 offset:8192
ds_read_b64_tr_b16 v[210:211],v16 offset:8704
ds_read_b64_tr_b16 v[212:213],v16 offset:9216
ds_read_b64_tr_b16 v[214:215],v16 offset:9728
ds_read_b64_tr_b16 v[216:217],v16 offset:10240
ds_read_b64_tr_b16 v[218:219],v16 offset:10752
ds_read_b64_tr_b16 v[220:221],v16 offset:11264
ds_read_b64_tr_b16 v[222:223],v16 offset:11776
ds_read_b128 v[144:147],v18 offset:1280
ds_read_b128 v[148:151],v18 offset:1312
ds_read_b128 v[152:155],v18 offset:1344
ds_read_b128 v[156:159],v18 offset:1376
s_setprio 0
v_add_f32 v176,1.0,v176
v_add_f32 v177,1.0,v177
v_add_f32 v178,1.0,v178
v_add_f32 v179,1.0,v179
v_add_f32 v180,1.0,v180
v_add_f32 v181,1.0,v181
v_add_f32 v182,1.0,v182
v_add_f32 v183,1.0,v183
v_add_f32 v184,1.0,v184
v_add_f32 v185,1.0,v185
v_add_f32 v186,1.0,v186
v_add_f32 v187,1.0,v187
v_add_f32 v188,1.0,v188
v_add_f32 v189,1.0,v189
v_add_f32 v190,1.0,v190
v_add_f32 v191,1.0,v191
s_setprio 1
s_waitcnt lgkmcnt(0)
v_mfma_f32_32x32x16_bf16 v[144:159],v[200:203],v[72:75],v[144:159]
v_rcp_f32 v176,v176
v_rcp_f32 v177,v177
v_rcp_f32 v178,v178
v_mfma_f32_32x32x16_bf16 v[144:159],v[204:207],v[76:79],v[144:159]
v_rcp_f32 v179,v179
v_rcp_f32 v180,v180
v_rcp_f32 v181,v181
v_mfma_f32_32x32x16_bf16 v[144:159],v[208:211],v[80:83],v[144:159]
v_rcp_f32 v182,v182
v_rcp_f32 v183,v183
v_rcp_f32 v184,v184
v_mfma_f32_32x32x16_bf16 v[144:159],v[212:215],v[84:87],v[144:159]
v_rcp_f32 v185,v185
v_rcp_f32 v186,v186
v_rcp_f32 v187,v187
v_mfma_f32_32x32x16_bf16 v[144:159],v[216:219],v[88:91],v[144:159]
v_rcp_f32 v188,v188
v_rcp_f32 v189,v189
v_mfma_f32_32x32x16_bf16 v[144:159],v[220:223],v[92:95],v[144:159]
v_rcp_f32 v190,v190
v_rcp_f32 v191,v191
ds_read_b64_tr_b16 v[200:201],v17 offset:0
ds_read_b64_tr_b16 v[202:203],v17 offset:1536
ds_read_b64_tr_b16 v[204:205],v17 offset:3072
ds_read_b64_tr_b16 v[206:207],v17 offset:4608
ds_read_b64_tr_b16 v[208:209],v17 offset:64
ds_read_b64_tr_b16 v[210:211],v17 offset:1600
ds_read_b64_tr_b16 v[212:213],v17 offset:3136
ds_read_b64_tr_b16 v[214:215],v17 offset:4672
ds_read_b64_tr_b16 v[216:217],v17 offset:128
ds_read_b64_tr_b16 v[218:219],v17 offset:1664
ds_read_b64_tr_b16 v[220:221],v17 offset:3200
ds_read_b64_tr_b16 v[222:223],v17 offset:4736
s_setprio 0
v_mul_f32 v176,v160,v176
v_mul_f32 v177,v161,v177
v_mul_f32 v178,v162,v178
v_mul_f32 v179,v163,v179
v_mul_f32 v180,v164,v180
v_mul_f32 v181,v165,v181
v_mul_f32 v182,v166,v182
v_mul_f32 v183,v167,v183
v_mul_f32 v184,v168,v184
v_mul_f32 v185,v169,v185
v_mul_f32 v186,v170,v186
v_mul_f32 v187,v171,v187
v_mul_f32 v188,v172,v188
v_mul_f32 v189,v173,v189
v_mul_f32 v190,v174,v190
v_mul_f32 v191,v175,v191
v_cvt_pk_bf16_f32 v192,v176,v177
v_cvt_pk_bf16_f32 v193,v178,v179
v_cvt_pk_bf16_f32 v194,v180,v181
v_cvt_pk_bf16_f32 v195,v182,v183
v_cvt_pk_bf16_f32 v196,v184,v185
v_cvt_pk_bf16_f32 v197,v186,v187
v_cvt_pk_bf16_f32 v198,v188,v189
v_cvt_pk_bf16_f32 v199,v190,v191
s_waitcnt lgkmcnt(0)
s_barrier
v_fmaak_f32 v176,v144,v144,0x409d30fc
v_fmaak_f32 v177,v145,v145,0x409d30fc
v_fmaak_f32 v178,v146,v146,0x409d30fc
v_fmaak_f32 v179,v147,v147,0x409d30fc
v_fmaak_f32 v180,v148,v148,0x409d30fc
v_fmaak_f32 v181,v149,v149,0x409d30fc
v_fmaak_f32 v182,v150,v150,0x409d30fc
v_fmaak_f32 v183,v151,v151,0x409d30fc
v_fmaak_f32 v184,v152,v152,0x409d30fc
v_fmaak_f32 v185,v153,v153,0x409d30fc
v_fmaak_f32 v186,v154,v154,0x409d30fc
v_fmaak_f32 v187,v155,v155,0x409d30fc
v_fmaak_f32 v188,v156,v156,0x409d30fc
v_fmaak_f32 v189,v157,v157,0x409d30fc
v_fmaak_f32 v190,v158,v158,0x409d30fc
v_fmaak_f32 v191,v159,v159,0x409d30fc
v_mul_f32 v176,v144,v176
v_mul_f32 v177,v145,v177
v_mul_f32 v178,v146,v178
v_mul_f32 v179,v147,v179
v_mul_f32 v180,v148,v180
v_mul_f32 v181,v149,v181
v_mul_f32 v182,v150,v182
v_mul_f32 v183,v151,v183
v_mul_f32 v184,v152,v184
v_mul_f32 v185,v153,v185
v_mul_f32 v186,v154,v186
v_mul_f32 v187,v155,v187
v_mul_f32 v188,v156,v188
v_mul_f32 v189,v157,v189
v_mul_f32 v190,v158,v190
v_mul_f32 v191,v159,v191
s_setprio 1
v_mfma_f32_32x32x16_bf16 v[96:111],v[200:203],v[192:195],v[96:111]
v_exp_f32 v176,v176
v_exp_f32 v177,v177
v_exp_f32 v178,v178
v_mfma_f32_32x32x16_bf16 v[96:111],v[204:207],v[196:199],v[96:111]
v_exp_f32 v179,v179
v_exp_f32 v180,v180
v_exp_f32 v181,v181
v_mfma_f32_32x32x16_bf16 v[112:127],v[208:211],v[192:195],v[112:127]
v_exp_f32 v182,v182
v_exp_f32 v183,v183
v_exp_f32 v184,v184
v_mfma_f32_32x32x16_bf16 v[112:127],v[212:215],v[196:199],v[112:127]
v_exp_f32 v185,v185
v_exp_f32 v186,v186
v_exp_f32 v187,v187
v_mfma_f32_32x32x16_bf16 v[128:143],v[216:219],v[192:195],v[128:143]
v_exp_f32 v188,v188
v_exp_f32 v189,v189
v_mfma_f32_32x32x16_bf16 v[128:143],v[220:223],v[196:199],v[128:143]
v_exp_f32 v190,v190
v_exp_f32 v191,v191
ds_read_b64_tr_b16 v[200:201],v16 offset:12288
ds_read_b64_tr_b16 v[202:203],v16 offset:12800
ds_read_b64_tr_b16 v[204:205],v16 offset:13312
ds_read_b64_tr_b16 v[206:207],v16 offset:13824
ds_read_b64_tr_b16 v[208:209],v16 offset:14336
ds_read_b64_tr_b16 v[210:211],v16 offset:14848
ds_read_b64_tr_b16 v[212:213],v16 offset:15360
ds_read_b64_tr_b16 v[214:215],v16 offset:15872
ds_read_b64_tr_b16 v[216:217],v16 offset:16384
ds_read_b64_tr_b16 v[218:219],v16 offset:16896
ds_read_b64_tr_b16 v[220:221],v16 offset:17408
ds_read_b64_tr_b16 v[222:223],v16 offset:17920
ds_read_b128 v[160:163],v18 offset:1408
ds_read_b128 v[164:167],v18 offset:1440
ds_read_b128 v[168:171],v18 offset:1472
ds_read_b128 v[172:175],v18 offset:1504
s_setprio 0
v_add_f32 v176,1.0,v176
v_add_f32 v177,1.0,v177
v_add_f32 v178,1.0,v178
v_add_f32 v179,1.0,v179
v_add_f32 v180,1.0,v180
v_add_f32 v181,1.0,v181
v_add_f32 v182,1.0,v182
v_add_f32 v183,1.0,v183
v_add_f32 v184,1.0,v184
v_add_f32 v185,1.0,v185
v_add_f32 v186,1.0,v186
v_add_f32 v187,1.0,v187
v_add_f32 v188,1.0,v188
v_add_f32 v189,1.0,v189
v_add_f32 v190,1.0,v190
v_add_f32 v191,1.0,v191
s_setprio 1
s_waitcnt lgkmcnt(0)
v_mfma_f32_32x32x16_bf16 v[160:175],v[200:203],v[72:75],v[160:175]
v_rcp_f32 v176,v176
v_rcp_f32 v177,v177
v_rcp_f32 v178,v178
v_mfma_f32_32x32x16_bf16 v[160:175],v[204:207],v[76:79],v[160:175]
v_rcp_f32 v179,v179
v_rcp_f32 v180,v180
v_rcp_f32 v181,v181
v_mfma_f32_32x32x16_bf16 v[160:175],v[208:211],v[80:83],v[160:175]
v_rcp_f32 v182,v182
v_rcp_f32 v183,v183
v_rcp_f32 v184,v184
v_mfma_f32_32x32x16_bf16 v[160:175],v[212:215],v[84:87],v[160:175]
v_rcp_f32 v185,v185
v_rcp_f32 v186,v186
v_rcp_f32 v187,v187
v_mfma_f32_32x32x16_bf16 v[160:175],v[216:219],v[88:91],v[160:175]
v_rcp_f32 v188,v188
v_rcp_f32 v189,v189
v_mfma_f32_32x32x16_bf16 v[160:175],v[220:223],v[92:95],v[160:175]
v_rcp_f32 v190,v190
v_rcp_f32 v191,v191
ds_read_b64_tr_b16 v[200:201],v17 offset:6144
ds_read_b64_tr_b16 v[202:203],v17 offset:7680
ds_read_b64_tr_b16 v[204:205],v17 offset:9216
ds_read_b64_tr_b16 v[206:207],v17 offset:10752
ds_read_b64_tr_b16 v[208:209],v17 offset:6208
ds_read_b64_tr_b16 v[210:211],v17 offset:7744
ds_read_b64_tr_b16 v[212:213],v17 offset:9280
ds_read_b64_tr_b16 v[214:215],v17 offset:10816
ds_read_b64_tr_b16 v[216:217],v17 offset:6272
ds_read_b64_tr_b16 v[218:219],v17 offset:7808
ds_read_b64_tr_b16 v[220:221],v17 offset:9344
ds_read_b64_tr_b16 v[222:223],v17 offset:10880
s_setprio 0
v_mul_f32 v176,v144,v176
v_mul_f32 v177,v145,v177
v_mul_f32 v178,v146,v178
v_mul_f32 v179,v147,v179
v_mul_f32 v180,v148,v180
v_mul_f32 v181,v149,v181
v_mul_f32 v182,v150,v182
v_mul_f32 v183,v151,v183
v_mul_f32 v184,v152,v184
v_mul_f32 v185,v153,v185
v_mul_f32 v186,v154,v186
v_mul_f32 v187,v155,v187
v_mul_f32 v188,v156,v188
v_mul_f32 v189,v157,v189
v_mul_f32 v190,v158,v190
v_mul_f32 v191,v159,v191
v_cvt_pk_bf16_f32 v192,v176,v177
v_cvt_pk_bf16_f32 v193,v178,v179
v_cvt_pk_bf16_f32 v194,v180,v181
v_cvt_pk_bf16_f32 v195,v182,v183
v_cvt_pk_bf16_f32 v196,v184,v185
v_cvt_pk_bf16_f32 v197,v186,v187
v_cvt_pk_bf16_f32 v198,v188,v189
v_cvt_pk_bf16_f32 v199,v190,v191
s_waitcnt lgkmcnt(0)
s_barrier
v_fmaak_f32 v176,v160,v160,0x409d30fc
v_fmaak_f32 v177,v161,v161,0x409d30fc
v_fmaak_f32 v178,v162,v162,0x409d30fc
v_fmaak_f32 v179,v163,v163,0x409d30fc
v_fmaak_f32 v180,v164,v164,0x409d30fc
v_fmaak_f32 v181,v165,v165,0x409d30fc
v_fmaak_f32 v182,v166,v166,0x409d30fc
v_fmaak_f32 v183,v167,v167,0x409d30fc
v_fmaak_f32 v184,v168,v168,0x409d30fc
v_fmaak_f32 v185,v169,v169,0x409d30fc
v_fmaak_f32 v186,v170,v170,0x409d30fc
v_fmaak_f32 v187,v171,v171,0x409d30fc
v_fmaak_f32 v188,v172,v172,0x409d30fc
v_fmaak_f32 v189,v173,v173,0x409d30fc
v_fmaak_f32 v190,v174,v174,0x409d30fc
v_fmaak_f32 v191,v175,v175,0x409d30fc
v_mul_f32 v176,v160,v176
v_mul_f32 v177,v161,v177
v_mul_f32 v178,v162,v178
v_mul_f32 v179,v163,v179
v_mul_f32 v180,v164,v180
v_mul_f32 v181,v165,v181
v_mul_f32 v182,v166,v182
v_mul_f32 v183,v167,v183
v_mul_f32 v184,v168,v184
v_mul_f32 v185,v169,v185
v_mul_f32 v186,v170,v186
v_mul_f32 v187,v171,v187
v_mul_f32 v188,v172,v188
v_mul_f32 v189,v173,v189
v_mul_f32 v190,v174,v190
v_mul_f32 v191,v175,v191
s_setprio 1
v_mfma_f32_32x32x16_bf16 v[96:111],v[200:203],v[192:195],v[96:111]
v_exp_f32 v176,v176
v_exp_f32 v177,v177
v_exp_f32 v178,v178
v_mfma_f32_32x32x16_bf16 v[96:111],v[204:207],v[196:199],v[96:111]
v_exp_f32 v179,v179
v_exp_f32 v180,v180
v_exp_f32 v181,v181
v_mfma_f32_32x32x16_bf16 v[112:127],v[208:211],v[192:195],v[112:127]
v_exp_f32 v182,v182
v_exp_f32 v183,v183
v_exp_f32 v184,v184
v_mfma_f32_32x32x16_bf16 v[112:127],v[212:215],v[196:199],v[112:127]
v_exp_f32 v185,v185
v_exp_f32 v186,v186
v_exp_f32 v187,v187
v_mfma_f32_32x32x16_bf16 v[128:143],v[216:219],v[192:195],v[128:143]
v_exp_f32 v188,v188
v_exp_f32 v189,v189
v_mfma_f32_32x32x16_bf16 v[128:143],v[220:223],v[196:199],v[128:143]
v_exp_f32 v190,v190
v_exp_f32 v191,v191
ds_read_b64 v[72:73],v20 offset:0
ds_read_b64 v[74:75],v21 offset:0
ds_read_b64 v[76:77],v22 offset:0
ds_read_b64 v[78:79],v23 offset:0
ds_read_b64 v[80:81],v20 offset:64
ds_read_b64 v[82:83],v21 offset:64
ds_read_b64 v[84:85],v22 offset:64
ds_read_b64 v[86:87],v23 offset:64
ds_read_b64 v[88:89],v20 offset:128
ds_read_b64 v[90:91],v21 offset:128
ds_read_b64 v[92:93],v22 offset:128
ds_read_b64 v[94:95],v23 offset:128
s_setprio 0
v_add_f32 v176,1.0,v176
v_add_f32 v177,1.0,v177
v_add_f32 v178,1.0,v178
v_add_f32 v179,1.0,v179
v_add_f32 v180,1.0,v180
v_add_f32 v181,1.0,v181
v_add_f32 v182,1.0,v182
v_add_f32 v183,1.0,v183
v_add_f32 v184,1.0,v184
v_add_f32 v185,1.0,v185
v_add_f32 v186,1.0,v186
v_add_f32 v187,1.0,v187
v_add_f32 v188,1.0,v188
v_add_f32 v189,1.0,v189
v_add_f32 v190,1.0,v190
v_add_f32 v191,1.0,v191
s_setprio 1
s_waitcnt lgkmcnt(0)
v_rcp_f32 v176,v176
v_rcp_f32 v177,v177
v_rcp_f32 v178,v178
v_rcp_f32 v179,v179
v_rcp_f32 v180,v180
v_rcp_f32 v181,v181
v_rcp_f32 v182,v182
v_rcp_f32 v183,v183
v_rcp_f32 v184,v184
v_rcp_f32 v185,v185
v_rcp_f32 v186,v186
v_rcp_f32 v187,v187
v_rcp_f32 v188,v188
v_rcp_f32 v189,v189
v_rcp_f32 v190,v190
v_rcp_f32 v191,v191
ds_read_b64_tr_b16 v[200:201],v17 offset:12288
ds_read_b64_tr_b16 v[202:203],v17 offset:13824
ds_read_b64_tr_b16 v[204:205],v17 offset:15360
ds_read_b64_tr_b16 v[206:207],v17 offset:16896
ds_read_b64_tr_b16 v[208:209],v17 offset:12352
ds_read_b64_tr_b16 v[210:211],v17 offset:13888
ds_read_b64_tr_b16 v[212:213],v17 offset:15424
ds_read_b64_tr_b16 v[214:215],v17 offset:16960
ds_read_b64_tr_b16 v[216:217],v17 offset:12416
ds_read_b64_tr_b16 v[218:219],v17 offset:13952
ds_read_b64_tr_b16 v[220:221],v17 offset:15488
ds_read_b64_tr_b16 v[222:223],v17 offset:17024
s_setprio 0
v_mul_f32 v176,v160,v176
v_mul_f32 v177,v161,v177
v_mul_f32 v178,v162,v178
v_mul_f32 v179,v163,v179
v_mul_f32 v180,v164,v180
v_mul_f32 v181,v165,v181
v_mul_f32 v182,v166,v182
v_mul_f32 v183,v167,v183
v_mul_f32 v184,v168,v184
v_mul_f32 v185,v169,v185
v_mul_f32 v186,v170,v186
v_mul_f32 v187,v171,v187
v_mul_f32 v188,v172,v188
v_mul_f32 v189,v173,v189
v_mul_f32 v190,v174,v190
v_mul_f32 v191,v175,v191
v_cvt_pk_bf16_f32 v192,v176,v177
v_cvt_pk_bf16_f32 v193,v178,v179
v_cvt_pk_bf16_f32 v194,v180,v181
v_cvt_pk_bf16_f32 v195,v182,v183
v_cvt_pk_bf16_f32 v196,v184,v185
v_cvt_pk_bf16_f32 v197,v186,v187
v_cvt_pk_bf16_f32 v198,v188,v189
v_cvt_pk_bf16_f32 v199,v190,v191
ds_read_b128 v[144:147],v18 offset:1536
ds_read_b128 v[148:151],v18 offset:1568
ds_read_b128 v[152:155],v18 offset:1600
ds_read_b128 v[156:159],v18 offset:1632
ds_read_b128 v[160:163],v18 offset:1664
ds_read_b128 v[164:167],v18 offset:1696
ds_read_b128 v[168:171],v18 offset:1728
ds_read_b128 v[172:175],v18 offset:1760
ds_read_b128 v[176:179],v18 offset:1792
ds_read_b128 v[180:183],v18 offset:1824
ds_read_b128 v[184:187],v18 offset:1856
ds_read_b128 v[188:191],v18 offset:1888
s_waitcnt lgkmcnt(12)
s_setprio 1
v_mfma_f32_32x32x16_bf16 v[96:111],v[200:203],v[192:195],v[96:111]
v_lshlrev_b32 v232,16,v72
v_lshlrev_b32 v233,16,v73
v_lshlrev_b32 v234,16,v74
v_lshlrev_b32 v235,16,v75
v_mfma_f32_32x32x16_bf16 v[96:111],v[204:207],v[196:199],v[96:111]
v_lshlrev_b32 v236,16,v76
v_lshlrev_b32 v237,16,v77
v_lshlrev_b32 v238,16,v78
v_lshlrev_b32 v239,16,v79
v_mfma_f32_32x32x16_bf16 v[112:127],v[208:211],v[192:195],v[112:127]
v_lshlrev_b32 v240,16,v80
v_lshlrev_b32 v241,16,v81
v_lshlrev_b32 v242,16,v82
v_lshlrev_b32 v243,16,v83
v_mfma_f32_32x32x16_bf16 v[112:127],v[212:215],v[196:199],v[112:127]
v_lshlrev_b32 v244,16,v84
v_lshlrev_b32 v245,16,v85
v_lshlrev_b32 v246,16,v86
v_lshlrev_b32 v247,16,v87
v_mfma_f32_32x32x16_bf16 v[128:143],v[216:219],v[192:195],v[128:143]
v_lshlrev_b32 v248,16,v88
v_lshlrev_b32 v249,16,v89
v_lshlrev_b32 v250,16,v90
v_lshlrev_b32 v251,16,v91
v_mfma_f32_32x32x16_bf16 v[128:143],v[220:223],v[196:199],v[128:143]
v_lshlrev_b32 v252,16,v92
v_lshlrev_b32 v253,16,v93
v_lshlrev_b32 v254,16,v94
v_lshlrev_b32 v255,16,v95
s_setprio 0
s_waitcnt lgkmcnt(0)
v_and_b32 v72,0xffff0000,v72
v_and_b32 v73,0xffff0000,v73
v_and_b32 v74,0xffff0000,v74
v_and_b32 v75,0xffff0000,v75
v_and_b32 v76,0xffff0000,v76
v_and_b32 v77,0xffff0000,v77
v_and_b32 v78,0xffff0000,v78
v_and_b32 v79,0xffff0000,v79
v_and_b32 v80,0xffff0000,v80
v_and_b32 v81,0xffff0000,v81
v_and_b32 v82,0xffff0000,v82
v_and_b32 v83,0xffff0000,v83
v_and_b32 v84,0xffff0000,v84
v_and_b32 v85,0xffff0000,v85
v_and_b32 v86,0xffff0000,v86
v_and_b32 v87,0xffff0000,v87
v_and_b32 v88,0xffff0000,v88
v_and_b32 v89,0xffff0000,v89
v_and_b32 v90,0xffff0000,v90
v_and_b32 v91,0xffff0000,v91
v_and_b32 v92,0xffff0000,v92
v_and_b32 v93,0xffff0000,v93
v_and_b32 v94,0xffff0000,v94
v_and_b32 v95,0xffff0000,v95
v_fma_f32 v232,v96,v144,v232
v_fma_f32 v72,v97,v145,v72
v_fma_f32 v233,v98,v146,v233
v_fma_f32 v73,v99,v147,v73
v_fma_f32 v234,v100,v148,v234
v_fma_f32 v74,v101,v149,v74
v_fma_f32 v235,v102,v150,v235
v_fma_f32 v75,v103,v151,v75
v_fma_f32 v236,v104,v152,v236
v_fma_f32 v76,v105,v153,v76
v_fma_f32 v237,v106,v154,v237
v_fma_f32 v77,v107,v155,v77
v_fma_f32 v238,v108,v156,v238
v_fma_f32 v78,v109,v157,v78
v_fma_f32 v239,v110,v158,v239
v_fma_f32 v79,v111,v159,v79
v_fma_f32 v240,v112,v160,v240
v_fma_f32 v80,v113,v161,v80
v_fma_f32 v241,v114,v162,v241
v_fma_f32 v81,v115,v163,v81
v_fma_f32 v242,v116,v164,v242
v_fma_f32 v82,v117,v165,v82
v_fma_f32 v243,v118,v166,v243
v_fma_f32 v83,v119,v167,v83
v_fma_f32 v244,v120,v168,v244
v_fma_f32 v84,v121,v169,v84
v_fma_f32 v245,v122,v170,v245
v_fma_f32 v85,v123,v171,v85
v_fma_f32 v246,v124,v172,v246
v_fma_f32 v86,v125,v173,v86
v_fma_f32 v247,v126,v174,v247
v_fma_f32 v87,v127,v175,v87
v_fma_f32 v248,v128,v176,v248
v_fma_f32 v88,v129,v177,v88
v_fma_f32 v249,v130,v178,v249
v_fma_f32 v89,v131,v179,v89
v_fma_f32 v250,v132,v180,v250
v_fma_f32 v90,v133,v181,v90
v_fma_f32 v251,v134,v182,v251
v_fma_f32 v91,v135,v183,v91
v_fma_f32 v252,v136,v184,v252
v_fma_f32 v92,v137,v185,v92
v_fma_f32 v253,v138,v186,v253
v_fma_f32 v93,v139,v187,v93
v_fma_f32 v254,v140,v188,v254
v_fma_f32 v94,v141,v189,v94
v_fma_f32 v255,v142,v190,v255
v_fma_f32 v95,v143,v191,v95
v_add_u32 v4,s54,v4
v_add_u32 v5,s54,v5
v_add_u32 v6,s54,v6
v_add_u32 v7,s54,v7
v_add_u32 v8,s54,v8
v_add_u32 v9,s54,v9
v_add_u32 v10,s54,v10
v_add_u32 v11,s54,v11
v_add_u32 v12,s54,v12
v_add_u32 v13,s54,v13
v_add_u32 v14,s54,v14
v_add_u32 v15,s54,v15
v_cvt_pk_bf16_f32 v200,v232,v72
v_cvt_pk_bf16_f32 v201,v233,v73
v_cvt_pk_bf16_f32 v202,v234,v74
v_cvt_pk_bf16_f32 v203,v235,v75
v_cvt_pk_bf16_f32 v204,v236,v76
v_cvt_pk_bf16_f32 v205,v237,v77
v_cvt_pk_bf16_f32 v206,v238,v78
v_cvt_pk_bf16_f32 v207,v239,v79
v_cvt_pk_bf16_f32 v208,v240,v80
v_cvt_pk_bf16_f32 v209,v241,v81
v_cvt_pk_bf16_f32 v210,v242,v82
v_cvt_pk_bf16_f32 v211,v243,v83
v_cvt_pk_bf16_f32 v212,v244,v84
v_cvt_pk_bf16_f32 v213,v245,v85
v_cvt_pk_bf16_f32 v214,v246,v86
v_cvt_pk_bf16_f32 v215,v247,v87
v_cvt_pk_bf16_f32 v216,v248,v88
v_cvt_pk_bf16_f32 v217,v249,v89
v_cvt_pk_bf16_f32 v218,v250,v90
v_cvt_pk_bf16_f32 v219,v251,v91
v_cvt_pk_bf16_f32 v220,v252,v92
v_cvt_pk_bf16_f32 v221,v253,v93
v_cvt_pk_bf16_f32 v222,v254,v94
v_cvt_pk_bf16_f32 v223,v255,v95
ds_write_b64 v20,v[200:201] offset:0
ds_write_b64 v21,v[202:203] offset:0
ds_write_b64 v22,v[204:205] offset:0
ds_write_b64 v23,v[206:207] offset:0
ds_write_b64 v20,v[208:209] offset:64
ds_write_b64 v21,v[210:211] offset:64
ds_write_b64 v22,v[212:213] offset:64
ds_write_b64 v23,v[214:215] offset:64
ds_write_b64 v20,v[216:217] offset:128
ds_write_b64 v21,v[218:219] offset:128
ds_write_b64 v22,v[220:221] offset:128
ds_write_b64 v23,v[222:223] offset:128
ds_read_b128 v[72:75],v19 offset:0
ds_read_b128 v[76:79],v19 offset:1024
ds_read_b128 v[80:83],v19 offset:2048
ds_read_b128 v[84:87],v19 offset:3456
ds_read_b128 v[88:91],v19 offset:4480
ds_read_b128 v[92:95],v19 offset:5504
v_add_u32 v20,s54,v20
v_add_u32 v21,s54,v21
v_add_u32 v22,s54,v22
v_add_u32 v23,s54,v23
s_sub_u32 s68,s68,s54
s_add_u32 s59,s59,1572864
s_add_u32 s53,s53,1
s_cmp_eq_u32 s53,3
s_cselect_b32 s59,0x70000000,s59
s_add_u32 s67,s69,98304
s_waitcnt lgkmcnt(5)
buffer_store_dwordx4 v[72:75],v24,s[40:43],s69 offen nt
s_waitcnt lgkmcnt(4)
buffer_store_dwordx4 v[76:79],v25,s[40:43],s69 offen nt
s_waitcnt lgkmcnt(3)
buffer_store_dwordx4 v[80:83],v26,s[40:43],s69 offen nt
s_waitcnt lgkmcnt(2)
buffer_store_dwordx4 v[84:87],v24,s[40:43],s67 offen nt
s_waitcnt lgkmcnt(1)
buffer_store_dwordx4 v[88:91],v25,s[40:43],s67 offen nt
s_waitcnt lgkmcnt(0)
buffer_store_dwordx4 v[92:95],v26,s[40:43],s67 offen nt
v_add_u32 v19,s54,v19
s_add_u32 s69,s69,1572864
s_sub_u32 s54,0,s54
s_cmp_lt_u32 s53,4
s_cbranch_scc1 L_pass1_%=
L_end_%=:
s_waitcnt vmcnt(0) lgkmcnt(0)
s_endpgm
)ASM"
    :: "s"(blockIdx.x), "v"(tid), "s"((unsigned long long)__builtin_amdgcn_kernarg_segment_ptr())
    : "v1", "v2", "v3", "v4", "v5", "v6", "v7", "v8", "v9", "v10", "v11", "v12", "v13", "v14", "v15", "v16", "v17", "v18", "v19", "v20", "v21", "v22", "v23", "v24", "v25", "v26", "v27", "v28", "v29", "v30", "v31", "v32", "v33", "v34", "v35", "v36", "v37", "v38", "v39", "v40", "v41", "v42", "v43", "v44", "v45", "v46", "v47", "v48", "v49", "v50", "v51", "v52", "v53", "v54", "v55", "v56", "v57", "v58", "v59", "v60", "v61", "v62", "v63", "v64", "v65", "v66", "v67", "v68", "v69", "v70", "v71", "v72", "v73", "v74", "v75", "v76", "v77", "v78", "v79", "v80", "v81", "v82", "v83", "v84", "v85", "v86", "v87", "v88", "v89", "v90", "v91", "v92", "v93", "v94", "v95", "v96", "v97", "v98", "v99", "v100", "v101", "v102", "v103", "v104", "v105", "v106", "v107", "v108", "v109", "v110", "v111", "v112", "v113", "v114", "v115", "v116", "v117", "v118", "v119", "v120", "v121", "v122", "v123", "v124", "v125", "v126", "v127", "v128", "v129", "v130", "v131", "v132", "v133", "v134", "v135", "v136", "v137", "v138", "v139", "v140", "v141", "v142", "v143", "v144", "v145", "v146", "v147", "v148", "v149", "v150", "v151", "v152", "v153", "v154", "v155", "v156", "v157", "v158", "v159", "v160", "v161", "v162", "v163", "v164", "v165", "v166", "v167", "v168", "v169", "v170", "v171", "v172", "v173", "v174", "v175", "v176", "v177", "v178", "v179", "v180", "v181", "v182", "v183", "v184", "v185", "v186", "v187", "v188", "v189", "v190", "v191", "v192", "v193", "v194", "v195", "v196", "v197", "v198", "v199", "v200", "v201", "v202", "v203", "v204", "v205", "v206", "v207", "v208", "v209", "v210", "v211", "v212", "v213", "v214", "v215", "v216", "v217", "v218", "v219", "v220", "v221", "v222", "v223", "v224", "v225", "v226", "v227", "v228", "v229", "v230", "v231", "v232", "v233", "v234", "v235", "v236", "v237", "v238", "v239", "v240", "v241", "v242", "v243", "v244", "v245", "v246", "v247", "v248", "v249", "v250", "v251", "v252", "v253", "v254", "v255", "s4", "s5", "s6", "s7", "s8", "s9", "s10", "s11", "s12", "s13", "s14", "s15", "s16", "s17", "s18", "s19", "s20", "s21", "s22", "s23", "s24", "s25", "s26", "s27", "s28", "s29", "s30", "s31", "s32", "s33", "s34", "s35", "s36", "s37", "s38", "s39", "s40", "s41", "s42", "s43", "s44", "s45", "s46", "s47", "s48", "s49", "s50", "s51", "s52", "s53", "s54", "s55", "s56", "s57", "s58", "s59", "s60", "s61", "s62", "s63", "s64", "s65", "s66", "s67", "s68", "s69", "s70", "s71", "s72", "s73", "s74", "s75", "s76", "s77", "s78", "s79", "s80", "s81", "s82", "s83", "s84", "s85", "s86", "s87", "s88", "s89", "s90", "s91", "s92", "s93", "s94", "s95", "s96", "s97", "s98", "s99", "s100", "s101", "vcc", "scc", "memory");
}

extern "C" void kernel_launch(void* const* d_in, const int* in_sizes, int n_in,
                              void* d_out, int out_size, void* d_ws, size_t ws_size,
                              hipStream_t stream) {
    (void)hipFuncSetAttribute((const void*)convnext_kernel, hipFuncAttributeMaxDynamicSharedMemorySize, LDS_BYTES);
    convnext_kernel<<<256, 512, LDS_BYTES, stream>>>(
        (const unsigned short*)d_in[0], (const unsigned short*)d_in[1], (const unsigned short*)d_in[2], (const unsigned short*)d_in[3],
        (const unsigned short*)d_in[4], (const unsigned short*)d_in[5], (const unsigned short*)d_in[6], (unsigned short*)d_out);
}
```

```cpp
#include <hip/hip_runtime.h>
#include <stdint.h>

constexpr int LDS_BYTES = 163584;

__global__ __launch_bounds__(512, 2) void convnext_kernel(
    const unsigned short* __restrict__ x, const unsigned short* __restrict__ dw, const unsigned short* __restrict__ w1,
    const unsigned short* __restrict__ b1, const unsigned short* __restrict__ w2, const unsigned short* __restrict__ b2,
    const unsigned short* __restrict__ gamma, unsigned short* __restrict__ out) {
    const unsigned tid = threadIdx.x;
    asm volatile(
R"ASM(
N_main_%=:
s_load_dwordx16 s[20:35],%2,0
v_lshrrev_b32 v236,6,%1
v_and_b32 v232,63,%1
v_readfirstlane_b32 s52,v236
s_getpc_b64 s[14:15]
N_ep_%=:
v_lshlrev_b32 v252,7,v232
v_lshrrev_b32 v253,8,%1
v_mul_u32_u24 v253,N_B_%=-N_ep_%=,v253
v_add_u32 v252,v252,v253
global_load_dword v254,v252,s[14:15]
v_mov_b32 v228,0
v_mov_b32 v229,0
v_mov_b32 v230,0
v_mov_b32 v231,0
s_and_b32 s64,%0,7
s_lshr_b32 s70,%0,3
s_lshl_b32 s71,s64,6
s_mul_i32 s68,s70,3072
s_and_b32 s64,s52,3
s_mul_i32 s67,s64,54
s_cmp_eq_u32 s70,0
s_cselect_b32 s66,0,99
s_cmp_eq_u32 s70,31
s_cselect_b32 s66,17,s66
v_add_u32 v236,s67,v232
s_mul_i32 s57,s64,864
s_add_u32 s57,s57,73728
v_mul_u32_u24 v237,0xaaab,v236
s_sub_u32 s65,s71,1
v_lshrrev_b32 v237,19,v237
s_mul_i32 s56,s65,98304
v_mul_u32_u24 v238,12,v237
s_add_u32 s56,s56,s68
v_sub_u32 v238,v236,v238
s_cmp_lt_i32 s65,0
s_cselect_b32 s101,0x70000000,s56
v_add_u32 v236,1,v237
s_mov_b32 s62,-1
s_mov_b32 s63,0x3fffff
v_bfe_u32 v236,v236,2,2
s_mov_b32 s38,50331840
s_mov_b32 s39,0x00020000
v_xor_b32 v238,v238,v236
s_mov_b32 s96,0x8befe000
s_mov_b32 s99,0x72800000
v_mul_u32_u24 v236,192,v237
s_mov_b32 s98,0xffff0000
s_bitcmp1_b32 s52,1
s_cselect_b64 vcc,-1,0
v_lshl_add_u32 v241,v238,4,v236
v_cmp_eq_u32_e64 s[92:93],s66,v237
v_mov_b32 v236,0x7fff0000
s_nop 0
v_cndmask_b32_e64 v241,v241,v236,s[92:93]
s_cmp_ge_u32 s52,4
s_cbranch_scc1 N_B_%=
s_setprio 3
s_waitcnt lgkmcnt(0)
s_sub_u32 s36,s20,192
s_subb_u32 s37,s21,0
s_and_b32 s37,s37,0xffff
s_bitcmp0_b32 s52,0
s_cselect_b64 s[44:45],s[24:25],s[28:29]
s_and_b32 s45,s45,0xffff
s_mov_b64 exec,s[62:63]
s_add_u32 m0,s57,0
s_nop 0
buffer_load_dwordx4 v241,s[36:39],s101 offen lds
s_add_u32 m0,s57,3488
s_add_u32 s101,s56,98304
buffer_load_dwordx4 v241,s[36:39],s101 offen lds
s_add_u32 m0,s57,6976
s_add_u32 s101,s56,196608
buffer_load_dwordx4 v241,s[36:39],s101 offen lds
s_add_u32 m0,s57,10464
s_add_u32 s101,s56,294912
buffer_load_dwordx4 v241,s[36:39],s101 offen lds
s_mov_b64 exec,-1
s_and_b32 s66,s52,3
s_cmp_eq_u32 s66,3
s_cselect_b32 s14,1,s66
v_min_u32 v236,47,v232
s_mul_i32 s64,s14,192
s_lshl_b32 s65,s14,7
v_lshl_add_u32 v200,v236,2,s64
s_add_u32 s65,s65,144256
v_and_b32 v199,255,%1
v_min_u32 v237,191,v199
v_lshlrev_b32 v201,2,v237
v_min_u32 v249,95,v199
v_lshlrev_b32 v202,1,v249
s_lshl_b32 s64,s66,3
s_lshr_b32 s67,0x30011100,s64
s_and_b32 s67,s67,0xff
s_lshl_b32 s67,s67,3
s_add_u32 s67,s67,143488
v_lshrrev_b32 v238,2,v236
v_and_b32 v239,3,v236
v_mul_u32_u24 v238,1152,v238
v_lshl_add_u32 v238,v239,5,v238
s_and_b32 s66,s66,1
s_lshl_b32 s64,s66,3
s_cmp_eq_u32 s66,0
s_movk_i32 s66,128
s_cselect_b32 s66,640,s66
v_add_u32 v203,s65,v238
v_add_u32 v209,s67,v238
v_lshl_add_u32 v204,v239,2,v203
v_add_u32 v215,s66,v209
v_xor_b32 v237,s64,v209
v_lshrrev_b32 v239,1,v239
v_xor_b32 v238,s64,v215
ds_write_b128 v203,v[228:231] offset:0
ds_write_b128 v203,v[228:231] offset:16
ds_write2_b64 v209,v[228:229],v[228:229] offset0:0 offset1:2
ds_write2_b64 v215,v[228:229],v[228:229] offset0:0 offset1:2
ds_write2_b64 v237,v[228:229],v[228:229] offset0:0 offset1:2
ds_write2_b64 v238,v[228:229],v[228:229] offset0:0 offset1:2
v_lshl_add_u32 v209,v239,2,v209
v_lshl_add_u32 v215,v239,2,v215
ds_write_b32 v228,v228 offset:11648
global_load_dword v242,v200,s[22:23] offset:0
global_load_dword v243,v200,s[22:23] offset:576
global_load_dword v244,v200,s[22:23] offset:1152
global_load_ushort v247,v202,s[32:33]
global_load_dword v245,v201,s[26:27]
global_load_ushort v246,v202,s[30:31]
s_mov_b64 exec,s[62:63]
s_add_u32 m0,s57,13952
s_add_u32 s101,s56,393216
buffer_load_dwordx4 v241,s[36:39],s101 offen lds
s_add_u32 m0,s57,17440
s_add_u32 s101,s56,491520
buffer_load_dwordx4 v241,s[36:39],s101 offen lds
s_add_u32 m0,s57,20928
s_add_u32 s101,s56,589824
buffer_load_dwordx4 v241,s[36:39],s101 offen lds
s_add_u32 m0,s57,24416
s_add_u32 s101,s56,688128
buffer_load_dwordx4 v241,s[36:39],s101 offen lds
s_add_u32 m0,s57,27904
s_add_u32 s101,s56,786432
buffer_load_dwordx4 v241,s[36:39],s101 offen lds
s_add_u32 m0,s57,31392
s_add_u32 s101,s56,884736
buffer_load_dwordx4 v241,s[36:39],s101 offen lds
s_add_u32 m0,s57,34880
s_add_u32 s101,s56,983040
buffer_load_dwordx4 v241,s[36:39],s101 offen lds
s_add_u32 m0,s57,38368
s_add_u32 s101,s56,1081344
buffer_load_dwordx4 v241,s[36:39],s101 offen lds
s_add_u32 m0,s57,41856
s_add_u32 s101,s56,1179648
buffer_load_dwordx4 v241,s[36:39],s101 offen lds
s_add_u32 m0,s57,45344
s_add_u32 s101,s56,1277952
buffer_load_dwordx4 v241,s[36:39],s101 offen lds
s_add_u32 m0,s57,48832
s_add_u32 s101,s56,1376256
buffer_load_dwordx4 v241,s[36:39],s101 offen lds
s_add_u32 m0,s57,52320
s_add_u32 s101,s56,1474560
buffer_load_dwordx4 v241,s[36:39],s101 offen lds
s_add_u32 m0,s57,55808
s_add_u32 s101,s56,1572864
buffer_load_dwordx4 v241,s[36:39],s101 offen lds
s_add_u32 m0,s57,59296
s_add_u32 s101,s56,1671168
buffer_load_dwordx4 v241,s[36:39],s101 offen lds
s_mov_b64 exec,-1
s_and_b32 s64,s52,3
s_mul_i32 s67,s64,54
s_lshl_b32 s10,s64,2
s_cmp_eq_u32 s70,31
s_cselect_b32 s66,17,99
v_add_u32 v236,s67,v232
v_add_u32 v236,108,v236
v_mul_u32_u24 v237,0xaaab,v236
v_lshrrev_b32 v237,19,v237
v_mul_u32_u24 v238,12,v237
v_sub_u32 v238,v236,v238
v_add_u32 v236,1,v237
v_bfe_u32 v236,v236,2,2
v_xor_b32 v238,v238,v236
v_mul_u32_u24 v236,192,v237
v_lshl_add_u32 v239,v238,4,v236
v_cmp_eq_u32_e64 s[92:93],s66,v237
v_mov_b32 v236,0x7fff0000
s_nop 0
v_cndmask_b32_e64 v239,v239,v236,s[92:93]
v_cndmask_b32_e64 v6,v239,v6,vcc
s_add_u32 s64,s10,s71
v_and_b32 v233,15,v232
v_lshrrev_b32 v234,4,v232
s_mul_i32 s61,s64,98304
s_add_u32 s61,s61,s68
v_and_b32 v236,1,v234
v_lshlrev_b32 v236,1,v236
v_lshrrev_b32 v237,5,v232
v_or_b32 v235,v236,v237
s_mov_b32 s54,0x44ea44ea
s_mov_b32 s55,0x877f877f
s_mov_b32 s8,0x0ff00ff0
s_mov_b32 s9,0x0ff00ff0
v_mov_b32 v7,0
v_mov_b32 v239,0x70000
v_bfe_u32 v236,v233,1,1
v_sub_u32 v236,v235,v236
v_cmp_gt_u32_e64 s[92:93],3,v236
v_lshrrev_b32 v237,2,v233
v_lshlrev_b32 v237,1,v237
v_and_b32 v238,1,v233
v_or_b32 v237,v237,v238
v_lshl_add_u32 v236,v236,3,v237
v_lshlrev_b32 v236,4,v236
v_add_u32 v236,143488,v236
v_cndmask_b32_e64 v11,v239,v236,s[92:93]
v_and_b32 v13,3,v237
v_lshlrev_b32 v236,1,v233
v_and_b32 v236,4,v236
v_lshl_add_u32 v236,v234,3,v236
v_mov_b32 v238,0x54250310
v_mul_u32_u24 v13,0x5555,v13
v_bfe_u32 v236,v238,v236,3
v_lshl_add_u32 v236,v236,3,v237
v_lshlrev_b32 v236,4,v236
v_add_u32 v12,143488,v236
v_lshrrev_b32 v237,2,v233
v_cmp_eq_u32_e64 s[92:93],v234,v237
v_lshlrev_b32 v236,4,v233
v_add_u32 v236,10112,v236
v_cndmask_b32_e64 v14,v239,v236,s[92:93]
v_and_b32 v239,7,v233
v_lshl_add_u32 v236,v239,1,v235
v_add_u32 v237,1,v236
v_bfe_u32 v237,v237,2,2
v_mul_u32_u24 v236,192,v236
v_lshl_add_u32 v236,v237,4,v236
v_add_u32 v237,1,v239
v_bfe_u32 v237,v237,1,2
v_xor_b32 v237,v237,v234
v_mul_u32_u24 v239,384,v239
v_lshl_add_u32 v237,v237,4,v239
v_sub_u32 v237,v237,v236
v_add_u32 v8,192,v237
v_add_u32 v15,73728,v236
v_and_b32 v238,1,v233
v_mul_u32_u24 v238,320,v238
v_sub_u32 v239,v239,v238
s_mov_b32 s12,0xaaaaaaaa
s_mov_b32 s13,0xaaaaaaaa
v_lshl_add_u32 v10,v234,4,v239
s_mov_b64 exec,s[8:9]
v_add_u32 v10,196608,v10
s_mov_b64 exec,-1
v_lshrrev_b32 v236,3,v233
v_bfe_u32 v237,v233,2,1
v_lshl_add_u32 v237,v236,4,v237
v_lshl_add_u32 v237,v234,1,v237
v_lshlrev_b32 v237,6,v237
v_lshl_add_u32 v237,v236,4,v237
v_and_b32 v238,1,v233
v_lshl_add_u32 v237,v238,5,v237
v_bfe_u32 v238,v233,1,1
v_lshl_add_u32 v237,v238,3,v237
v_xor_b32 v238,16,v237
v_add_u32 v1,16256,v237
v_add_u32 v2,16256,v238
v_lshrrev_b32 v236,3,v233
v_bfe_u32 v237,v233,2,1
v_lshl_add_u32 v236,v237,1,v236
v_lshrrev_b32 v237,1,v234
v_lshl_add_u32 v236,v237,2,v236
v_mul_u32_u24 v236,192,v236
v_and_b32 v238,1,v234
v_mul_u32_u24 v239,3080,v238
v_add_u32 v236,v236,v239
v_and_b32 v239,3,v233
v_lshl_add_u32 v236,v239,4,v236
v_add_u32 v3,40896,v236
v_lshlrev_b32 v238,6,v238
v_lshl_add_u32 v238,v237,4,v238
v_add_u32 v4,8192,v238
s_mov_b64 s[40:41],s[34:35]
s_and_b32 s41,s41,0xffff
s_mov_b32 s42,50331648
s_mov_b32 s43,0x00020000
v_lshrrev_b32 v236,5,v249
v_bfe_u32 v237,v249,2,1
v_and_b32 v238,3,v249
v_lshlrev_b32 v206,7,v236
v_lshl_add_u32 v206,v237,5,v206
v_lshl_add_u32 v206,v238,2,v206
v_bfe_u32 v239,v249,3,1
v_lshl_add_u32 v206,v239,6,v206
v_bfe_u32 v239,v249,4,1
v_lshl_add_u32 v206,v239,4,v206
v_add_u32 v206,9728,v206
v_lshl_add_u32 v207,v236,1,v237
v_lshlrev_b32 v207,4,v207
v_bfe_u32 v239,v249,3,2
v_lshl_add_u32 v207,v239,2,v207
v_add_u32 v207,v207,v238
v_lshlrev_b32 v207,4,v207
v_add_u32 v207,10112,v207
v_lshlrev_b32 v208,4,v238
v_cmp_eq_u32_e64 s[48:49],1,v237
v_lshlrev_b32 v205,1,v201
v_add_u32 v205,8192,v205
v_mov_b32 v221,0
v_mov_b32 v222,0x3f800000
s_waitcnt vmcnt(14)
v_and_b32 v212,0xffff,v242
v_and_b32 v242,s98,v242
ds_write2_b32 v209,v212,v242 offset0:0 offset1:4
v_and_b32 v213,0xffff,v243
v_and_b32 v243,s98,v243
ds_write2_b32 v215,v213,v243 offset0:0 offset1:4
v_and_b32 v214,0xffff,v244
v_and_b32 v244,s98,v244
ds_write2_b32 v204,v214,v244 offset0:0 offset1:4
v_lshlrev_b32 v247,16,v247
v_lshlrev_b32 v246,16,v246
v_mul_f32 v247,0x6c800000,v247
v_mul_f32 v246,0x12800000,v246
v_rcp_f32 v224,v247
v_lshlrev_b32 v210,16,v245
v_and_b32 v211,s98,v245
v_med3_f32 v224,v224,v222,-v222
v_pk_mul_f32 v[210:211],v[210:211],s[96:97] op_sel_hi:[1,0]
v_cvt_pk_bf16_f32 v220,v224,v228
ds_write_b64 v205,v[210:211]
ds_write_b32 v206,v246 offset:2048
v_lshlrev_b32 v222,16,v220
v_rcp_f32 v222,v222
v_lshlrev_b64 v[220:221],v208,v[220:221]
v_cndmask_b32_e64 v224,v220,0,s[48:49]
v_cndmask_b32_e64 v226,0,v220,s[48:49]
v_cndmask_b32_e64 v225,v221,0,s[48:49]
v_cndmask_b32_e64 v227,0,v221,s[48:49]
ds_write_b128 v207,v[224:227]
ds_write_b32 v206,v222
v_bfe_u32 v236,v232,2,3
v_mul_u32_u24 v236,768,v236
v_and_b32 v237,3,v232
v_lshrrev_b32 v238,5,v232
v_lshl_add_u32 v237,v238,2,v237
v_lshl_add_u32 v236,v237,4,v236
v_lshlrev_b32 v237,4,v232
s_bitcmp0_b32 s52,0
s_mov_b32 s58,40896
s_cselect_b32 s58,16256,s58
s_cselect_b64 s[92:93],-1,0
s_cselect_b32 s59,64,6144
s_movk_i32 s53,1024
s_cselect_b32 s53,6144,s53
s_cselect_b32 s60,0,67584
s_cselect_b32 s97,64,0
s_cselect_b32 s69,0,0x70000000
s_mov_b32 s100,73728
s_cselect_b32 s100,110688,s100
s_cselect_b32 s64,16,0
s_movk_i32 s50,1032
s_cselect_b32 s50,1024,s50
s_cselect_b32 s51,s53,-72704
v_cndmask_b32_e64 v236,v237,v236,s[92:93]
v_cndmask_b32_e64 v5,v241,v236,vcc
v_xor_b32 v236,s64,v236
v_cndmask_b32_e64 v6,v6,v236,vcc
s_mov_b32 s46,73728
s_mov_b32 s47,0x00020000
s_waitcnt lgkmcnt(0)
s_waitcnt vmcnt(0)
s_barrier
s_setprio 3
s_add_u32 s4,s10,0
s_cmp_ge_u32 s4,20
s_cselect_b32 s7,20,0
s_sub_u32 s4,s4,s7
s_mul_i32 s4,s4,3488
s_add_u32 s5,s10,2
s_cmp_ge_u32 s5,20
s_cselect_b32 s7,20,0
s_sub_u32 s5,s5,s7
s_mul_i32 s5,s5,3488
s_add_u32 s6,s10,4
s_cmp_ge_u32 s6,20
s_cselect_b32 s7,20,0
s_sub_u32 s6,s6,s7
s_mul_i32 s6,s6,3488
v_add_u32 v112,s4,v15
v_add_u32 v116,s5,v15
s_mov_b64 exec,s[8:9]
v_add_u32 v112,s5,v15
v_add_u32 v116,s6,v15
s_mov_b64 exec,-1
v_xor_b32 v113,16,v112
v_xor_b32 v117,16,v116
v_xor_b32 v114,32,v112
v_xor_b32 v118,32,v116
v_xor_b32 v115,48,v112
v_xor_b32 v119,48,v116
ds_read_b128 v[136:139],v116 offset:0
ds_read_b128 v[88:91],v11 offset:768
ds_read_b128 v[140:143],v116 offset:3488
ds_read_b128 v[128:131],v112 offset:0
ds_read_b128 v[132:135],v112 offset:3488
ds_read_b128 v[92:95],v12 offset:0
ds_read_b128 v[152:155],v117 offset:0
ds_read_b128 v[96:99],v11 offset:1920
ds_read_b128 v[156:159],v117 offset:3488
ds_read_b128 v[144:147],v113 offset:0
ds_read_b128 v[148:151],v113 offset:3488
ds_read_b128 v[100:103],v12 offset:1152
s_waitcnt lgkmcnt(10)
v_mfma_f32_16x16x32_bf16 v[160:163],v[88:91],v[136:139],0
s_waitcnt lgkmcnt(9)
v_mfma_f32_16x16x32_bf16 v[184:187],v[88:91],v[140:143],0
s_waitcnt lgkmcnt(6)
ds_read_b128 v[140:143],v116 offset:3552
ds_read_b128 v[88:91],v11 offset:5376
v_smfmac_f32_16x16x64_bf16 v[160:163],v[92:95],v[128:135],v13
ds_read_b128 v[128:131],v112 offset:64
v_smfmac_f32_16x16x64_bf16 v[184:187],v[92:95],v[132:139],v13
s_waitcnt lgkmcnt(7)
ds_read_b128 v[136:139],v116 offset:64
ds_read_b128 v[132:135],v112 offset:3552
v_mfma_f32_16x16x32_bf16 v[164:167],v[96:99],v[152:155],0
s_waitcnt lgkmcnt(8)
ds_read_b128 v[92:95],v12 offset:4608
v_add_f32 v120,v160,v161
v_mul_f32 v124,v160,v160
v_fmac_f32 v124,v161,v161
v_mfma_f32_16x16x32_bf16 v[188:191],v[96:99],v[156:159],0
s_waitcnt lgkmcnt(6)
ds_read_b128 v[156:159],v117 offset:3552
ds_read_b128 v[96:99],v11 offset:6528
v_add_f32 v121,v162,v163
v_mul_f32 v125,v162,v162
v_fmac_f32 v125,v163,v163
v_smfmac_f32_16x16x64_bf16 v[164:167],v[100:103],v[144:151],v13
ds_read_b128 v[144:147],v113 offset:64
v_add_f32 v122,v184,v185
v_mul_f32 v126,v184,v184
v_fmac_f32 v126,v185,v185
v_smfmac_f32_16x16x64_bf16 v[188:191],v[100:103],v[148:155],v13
s_waitcnt lgkmcnt(5)
ds_read_b128 v[152:155],v117 offset:64
ds_read_b128 v[148:151],v113 offset:3552
v_add_f32 v123,v186,v187
v_mul_f32 v127,v186,v186
v_fmac_f32 v127,v187,v187
v_mfma_f32_16x16x32_bf16 v[168:171],v[88:91],v[136:139],0
ds_read_b128 v[100:103],v12 offset:5760
v_add_f32 v120,v120,v164
v_fmac_f32 v124,v164,v164
v_add_f32 v120,v120,v165
v_fmac_f32 v124,v165,v165
v_mfma_f32_16x16x32_bf16 v[192:195],v[88:91],v[140:143],0
s_waitcnt lgkmcnt(6)
ds_read_b128 v[140:143],v116 offset:3616
ds_read_b128 v[88:91],v11 offset:9984
v_add_f32 v121,v121,v166
v_fmac_f32 v125,v166,v166
v_add_f32 v121,v121,v167
v_fmac_f32 v125,v167,v167
v_smfmac_f32_16x16x64_bf16 v[168:171],v[92:95],v[128:135],v13
ds_read_b128 v[128:131],v112 offset:128
v_add_f32 v122,v122,v188
v_fmac_f32 v126,v188,v188
v_add_f32 v122,v122,v189
v_fmac_f32 v126,v189,v189
v_smfmac_f32_16x16x64_bf16 v[192:195],v[92:95],v[132:139],v13
s_waitcnt lgkmcnt(5)
ds_read_b128 v[136:139],v116 offset:128
ds_read_b128 v[132:135],v112 offset:3616
v_add_f32 v123,v123,v190
v_fmac_f32 v127,v190,v190
v_add_f32 v123,v123,v191
v_fmac_f32 v127,v191,v191
v_mfma_f32_16x16x32_bf16 v[172:175],v[96:99],v[152:155],0
ds_read_b128 v[92:95],v12 offset:9216
v_add_f32 v120,v120,v168
v_fmac_f32 v124,v168,v168
v_add_f32 v120,v120,v169
v_fmac_f32 v124,v169,v169
v_mfma_f32_16x16x32_bf16 v[196:199],v[96:99],v[156:159],0
s_waitcnt lgkmcnt(6)
ds_read_b128 v[156:159],v117 offset:3616
ds_read_b128 v[96:99],v11 offset:11136
v_add_f32 v121,v121,v170
v_fmac_f32 v125,v170,v170
v_add_f32 v121,v121,v171
v_fmac_f32 v125,v171,v171
v_smfmac_f32_16x16x64_bf16 v[172:175],v[100:103],v[144:151],v13
ds_read_b128 v[144:147],v113 offset:128
v_add_f32 v122,v122,v192
v_fmac_f32 v126,v192,v192
v_add_f32 v122,v122,v193
v_fmac_f32 v126,v193,v193
v_smfmac_f32_16x16x64_bf16 v[196:199],v[100:103],v[148:155],v13
s_waitcnt lgkmcnt(5)
ds_read_b128 v[152:155],v117 offset:128
ds_read_b128 v[148:151],v113 offset:3616
v_add_f32 v123,v123,v194
v_fmac_f32 v127,v194,v194
v_add_f32 v123,v123,v195
v_fmac_f32 v127,v195,v195
v_mfma_f32_16x16x32_bf16 v[176:179],v[88:91],v[136:139],0
ds_read_b128 v[100:103],v12 offset:10368
v_add_f32 v120,v120,v172
v_fmac_f32 v124,v172,v172
v_add_f32 v120,v120,v173
v_fmac_f32 v124,v173,v173
v_mfma_f32_16x16x32_bf16 v[200:203],v[88:91],v[140:143],0
s_waitcnt lgkmcnt(6)
ds_read_b128 v[140:143],v118 offset:3488
ds_read_b128 v[88:91],v11 offset:3072
v_add_f32 v121,v121,v174
v_fmac_f32 v125,v174,v174
v_add_f32 v121,v121,v175
v_fmac_f32 v125,v175,v175
v_smfmac_f32_16x16x64_bf16 v[176:179],v[92:95],v[128:135],v13
ds_read_b128 v[128:131],v114 offset:0
v_add_f32 v122,v122,v196
v_fmac_f32 v126,v196,v196
v_add_f32 v122,v122,v197
v_fmac_f32 v126,v197,v197
v_smfmac_f32_16x16x64_bf16 v[200:203],v[92:95],v[132:139],v13
s_waitcnt lgkmcnt(5)
ds_read_b128 v[136:139],v118 offset:0
ds_read_b128 v[132:135],v114 offset:3488
v_add_f32 v123,v123,v198
v_fmac_f32 v127,v198,v198
v_add_f32 v123,v123,v199
v_fmac_f32 v127,v199,v199
v_mfma_f32_16x16x32_bf16 v[180:183],v[96:99],v[152:155],0
ds_read_b128 v[92:95],v12 offset:2304
v_add_f32 v120,v120,v176
v_fmac_f32 v124,v176,v176
v_add_f32 v120,v120,v177
v_fmac_f32 v124,v177,v177
v_mfma_f32_16x16x32_bf16 v[204:207],v[96:99],v[156:159],0
s_waitcnt lgkmcnt(6)
ds_read_b128 v[156:159],v119 offset:3488
ds_read_b128 v[96:99],v11 offset:4224
v_add_f32 v121,v121,v178
v_fmac_f32 v125,v178,v178
v_add_f32 v121,v121,v179
v_fmac_f32 v125,v179,v179
v_smfmac_f32_16x16x64_bf16 v[180:183],v[100:103],v[144:151],v13
ds_read_b128 v[144:147],v115 offset:0
v_add_f32 v122,v122,v200
v_fmac_f32 v126,v200,v200
v_add_f32 v122,v122,v201
v_fmac_f32 v126,v201,v201
v_smfmac_f32_16x16x64_bf16 v[204:207],v[100:103],v[148:155],v13
s_waitcnt lgkmcnt(5)
ds_read_b128 v[152:155],v119 offset:0
ds_read_b128 v[148:151],v115 offset:3488
v_add_f32 v123,v123,v202
v_fmac_f32 v127,v202,v202
v_add_f32 v123,v123,v203
v_fmac_f32 v127,v203,v203
v_mfma_f32_16x16x32_bf16 v[208:211],v[88:91],v[136:139],0
ds_read_b128 v[100:103],v12 offset:3456
v_add_f32 v120,v120,v180
v_fmac_f32 v124,v180,v180
v_add_f32 v120,v120,v181
v_fmac_f32 v124,v181,v181
v_mfma_f32_16x16x32_bf16 v[232:235],v[88:91],v[140:143],0
s_waitcnt lgkmcnt(6)
ds_read_b128 v[140:143],v118 offset:3552
ds_read_b128 v[88:91],v11 offset:7680
v_add_f32 v121,v121,v182
v_fmac_f32 v125,v182,v182
v_add_f32 v121,v121,v183
v_fmac_f32 v125,v183,v183
v_smfmac_f32_16x16x64_bf16 v[208:211],v[92:95],v[128:135],v13
ds_read_b128 v[128:131],v114 offset:64
v_add_f32 v122,v122,v204
v_fmac_f32 v126,v204,v204
v_add_f32 v122,v122,v205
v_fmac_f32 v126,v205,v205
v_smfmac_f32_16x16x64_bf16 v[232:235],v[92:95],v[132:139],v13
s_waitcnt lgkmcnt(5)
ds_read_b128 v[136:139],v118 offset:64
ds_read_b128 v[132:135],v114 offset:3552
v_add_f32 v123,v123,v206
v_fmac_f32 v127,v206,v206
v_add_f32 v123,v123,v207
v_fmac_f32 v127,v207,v207
v_mfma_f32_16x16x32_bf16 v[212:215],v[96:99],v[152:155],0
ds_read_b128 v[92:95],v12 offset:6912
v_add_f32 v120,v120,v208
v_fmac_f32 v124,v208,v208
v_add_f32 v120,v120,v209
v_fmac_f32 v124,v209,v209
v_mfma_f32_16x16x32_bf16 v[236:239],v[96:99],v[156:159],0
s_waitcnt lgkmcnt(6)
ds_read_b128 v[156:159],v119 offset:3552
ds_read_b128 v[96:99],v11 offset:8832
v_add_f32 v121,v121,v210
v_fmac_f32 v125,v210,v210
v_add_f32 v121,v121,v211
v_fmac_f32 v125,v211,v211
v_smfmac_f32_16x16x64_bf16 v[212:215],v[100:103],v[144:151],v13
ds_read_b128 v[144:147],v115 offset:64
v_add_f32 v122,v122,v232
v_fmac_f32 v126,v232,v232
v_add_f32 v122,v122,v233
v_fmac_f32 v126,v233,v233
v_smfmac_f32_16x16x64_bf16 v[236:239],v[100:103],v[148:155],v13
s_waitcnt lgkmcnt(5)
ds_read_b128 v[152:155],v119 offset:64
ds_read_b128 v[148:151],v115 offset:3552
v_add_f32 v123,v123,v234
v_fmac_f32 v127,v234,v234
v_add_f32 v123,v123,v235
v_fmac_f32 v127,v235,v235
v_mfma_f32_16x16x32_bf16 v[216:219],v[88:91],v[136:139],0
ds_read_b128 v[100:103],v12 offset:8064
v_add_f32 v120,v120,v212
v_fmac_f32 v124,v212,v212
v_add_f32 v120,v120,v213
v_fmac_f32 v124,v213,v213
v_add_f32 v121,v121,v214
v_mfma_f32_16x16x32_bf16 v[240:243],v[88:91],v[140:143],0
s_waitcnt lgkmcnt(6)
ds_read_b128 v[140:143],v118 offset:3616
ds_read_b128 v[88:91],v11 offset:12288
v_fmac_f32 v125,v214,v214
v_add_f32 v121,v121,v215
v_fmac_f32 v125,v215,v215
v_add_f32 v122,v122,v236
v_smfmac_f32_16x16x64_bf16 v[216:219],v[92:95],v[128:135],v13
ds_read_b128 v[128:131],v114 offset:128
v_fmac_f32 v126,v236,v236
v_add_f32 v122,v122,v237
v_fmac_f32 v126,v237,v237
v_add_f32 v123,v123,v238
v_fmac_f32 v127,v238,v238
v_smfmac_f32_16x16x64_bf16 v[240:243],v[92:95],v[132:139],v13
s_waitcnt lgkmcnt(5)
ds_read_b128 v[136:139],v118 offset:128
ds_read_b128 v[132:135],v114 offset:3616
v_add_f32 v123,v123,v239
v_fmac_f32 v127,v239,v239
v_add_u32 v112,v112,v8
v_add_u32 v116,v116,v8
v_mfma_f32_16x16x32_bf16 v[220:223],v[96:99],v[152:155],0
ds_read_b128 v[92:95],v12 offset:11520
ds_read_b128 v[104:107],v14 offset:0
v_add_f32 v120,v120,v216
v_fmac_f32 v124,v216,v216
v_add_f32 v120,v120,v217
v_fmac_f32 v124,v217,v217
v_mfma_f32_16x16x32_bf16 v[244:247],v[96:99],v[156:159],0
s_waitcnt lgkmcnt(7)
ds_read_b128 v[108:111],v4 offset:3584
ds_read_b128 v[156:159],v119 offset:3616
v_add_f32 v121,v121,v218
v_fmac_f32 v125,v218,v218
v_add_f32 v121,v121,v219
v_fmac_f32 v125,v219,v219
v_smfmac_f32_16x16x64_bf16 v[220:223],v[100:103],v[144:151],v13
ds_read_b128 v[96:99],v11 offset:13440
ds_read_b128 v[144:147],v115 offset:128
v_add_f32 v122,v122,v240
v_fmac_f32 v126,v240,v240
v_add_f32 v122,v122,v241
v_fmac_f32 v126,v241,v241
v_smfmac_f32_16x16x64_bf16 v[244:247],v[100:103],v[148:155],v13
s_waitcnt lgkmcnt(7)
ds_read_b128 v[152:155],v119 offset:128
ds_read_b128 v[148:151],v115 offset:3616
v_add_f32 v123,v123,v242
v_fmac_f32 v127,v242,v242
v_add_f32 v123,v123,v243
v_fmac_f32 v127,v243,v243
v_mfma_f32_16x16x32_bf16 v[224:227],v[88:91],v[136:139],0
ds_read_b128 v[100:103],v12 offset:12672
v_add_f32 v120,v120,v220
s_mov_b32 s86,0xbe801d5a
v_fmac_f32 v124,v220,v220
v_add_f32 v120,v120,v221
v_fmac_f32 v124,v221,v221
v_mfma_f32_16x16x32_bf16 v[248:251],v[88:91],v[140:143],0
s_waitcnt lgkmcnt(8)
ds_read_b128 v[140:143],v116 offset:192
ds_read_b128 v[88:91],v112 offset:3616
v_add_f32 v121,v121,v222
v_fmac_f32 v125,v222,v222
v_add_f32 v121,v121,v223
v_fmac_f32 v125,v223,v223
v_smfmac_f32_16x16x64_bf16 v[224:227],v[92:95],v[128:135],v13
ds_read_b128 v[128:131],v112 offset:3488
v_add_f32 v122,v122,v244
v_fmac_f32 v126,v244,v244
v_add_f32 v122,v122,v245
v_fmac_f32 v126,v245,v245
v_smfmac_f32_16x16x64_bf16 v[248:251],v[92:95],v[132:139],v13
s_waitcnt lgkmcnt(5)
ds_read_b128 v[136:139],v116 offset:0
ds_read_b128 v[132:135],v112 offset:3680
v_add_f32 v123,v123,v246
v_fmac_f32 v127,v246,v246
v_add_f32 v123,v123,v247
v_fmac_f32 v127,v247,v247
v_mfma_f32_16x16x32_bf16 v[228:231],v[96:99],v[152:155],0
ds_read_b128 v[92:95],v112 offset:3808
v_add_f32 v120,v120,v224
v_fmac_f32 v124,v224,v224
v_add_f32 v120,v120,v225
v_fmac_f32 v124,v225,v225
v_mfma_f32_16x16x32_bf16 v[252:255],v[96:99],v[156:159],0
s_waitcnt lgkmcnt(6)
ds_read_b128 v[156:159],v116 offset:256
ds_read_b128 v[96:99],v116 offset:128
v_add_f32 v121,v121,v226
v_fmac_f32 v125,v226,v226
v_add_f32 v121,v121,v227
v_fmac_f32 v125,v227,v227
v_smfmac_f32_16x16x64_bf16 v[228:231],v[100:103],v[144:151],v13
ds_read_b128 v[144:147],v112 offset:3552
v_add_f32 v122,v122,v248
v_fmac_f32 v126,v248,v248
v_add_f32 v122,v122,v249
v_fmac_f32 v126,v249,v249
v_smfmac_f32_16x16x64_bf16 v[252:255],v[100:103],v[148:155],v13
ds_read_b128 v[152:155],v116 offset:64
ds_read_b128 v[148:151],v112 offset:3744
v_add_f32 v123,v123,v250
v_fmac_f32 v127,v250,v250
v_add_f32 v123,v123,v251
v_fmac_f32 v127,v251,v251
ds_read_b128 v[100:103],v116 offset:320
v_add_f32 v120,v120,v228
v_fmac_f32 v124,v228,v228
v_add_f32 v120,v120,v229
v_fmac_f32 v124,v229,v229
v_add_f32 v121,v121,v230
v_fmac_f32 v125,v230,v230
v_add_f32 v121,v121,v231
v_fmac_f32 v125,v231,v231
s_waitcnt lgkmcnt(0)
s_barrier
v_add_f32 v122,v122,v252
v_fmac_f32 v126,v252,v252
v_add_f32 v122,v122,v253
v_fmac_f32 v126,v253,v253
v_add_f32 v123,v123,v254
v_fmac_f32 v127,v254,v254
v_add_f32 v123,v123,v255
v_fmac_f32 v127,v255,v255
ds_read_b128 v[56:59],v14 offset:256
ds_read_b128 v[60:63],v4 offset:3616
v_permlane32_swap_b32 v120,v122
v_permlane32_swap_b32 v121,v123
v_permlane32_swap_b32 v124,v126
v_permlane32_swap_b32 v125,v127
v_add_f32 v120,v120,v122
v_add_f32 v121,v121,v123
v_add_f32 v124,v124,v126
v_add_f32 v125,v125,v127
v_permlane16_swap_b32 v120,v121
s_nop 0
v_permlane16_swap_b32 v124,v125
v_add_f32 v113,v120,v121
v_add_f32 v114,v124,v125
v_mul_f32 v113,0x3c2aaaab,v113
v_mul_f32 v114,0x3c2aaaab,v114
v_mul_f32 v115,0x5e000000,v114
v_fma_f32 v114,-v113,v113,v114
v_pk_maximum3_f16 v7,v7,v115,v115 op_sel:[0,1,1] op_sel_hi:[1,1,1]
v_max_f32 v114,0,v114
v_add_f32 v114,0x3727c5ac,v114
v_rsq_f32 v114,v114
s_nop 0
v_mul_f32 v120,0x8befe000,v114
v_mul_f32 v121,0x8befe000,v114
v_mul_f32 v124,-v113,v120
v_mul_f32 v125,-v113,v120
v_permlane16_swap_b32 v120,v121
s_nop 0
v_permlane16_swap_b32 v124,v125
v_mov_b32 v122,v120
v_mov_b32 v123,v121
v_mov_b32 v126,v124
v_mov_b32 v127,v125
v_permlane32_swap_b32 v120,v122
v_permlane32_swap_b32 v124,v126
v_permlane32_swap_b32 v121,v123
v_permlane32_swap_b32 v125,v127
v_pk_fma_f32 v[160:161],v[160:161],v[120:121],v[124:125] op_sel:[0,0,0] op_sel_hi:[1,0,0]
ds_read_b64_tr_b16 v[64:65],v1 offset:12832
v_pk_fma_f32 v[162:163],v[162:163],v[120:121],v[124:125] op_sel:[0,1,1] op_sel_hi:[1,1,1]
ds_read_b64_tr_b16 v[66:67],v1 offset:14880
v_pk_fma_f32 v[164:165],v[164:165],v[120:121],v[124:125] op_sel:[0,0,0] op_sel_hi:[1,0,0]
ds_read_b128 v[112:115],v4 offset:128
v_pk_fma_f32 v[166:167],v[166:167],v[120:121],v[124:125] op_sel:[0,1,1] op_sel_hi:[1,1,1]
ds_read_b64_tr_b16 v[68:69],v2 offset:12832
v_pk_fma_f32 v[208:209],v[208:209],v[120:121],v[124:125] op_sel:[0,0,0] op_sel_hi:[1,0,0]
ds_read_b64_tr_b16 v[70:71],v2 offset:14880
v_pk_fma_f32 v[210:211],v[210:211],v[120:121],v[124:125] op_sel:[0,1,1] op_sel_hi:[1,1,1]
ds_read_b128 v[116:119],v4 offset:160
v_pk_fma_f32 v[212:213],v[212:213],v[120:121],v[124:125] op_sel:[0,0,0] op_sel_hi:[1,0,0]
ds_read_b64_tr_b16 v[72:73],v1 offset:16928
v_pk_fma_f32 v[214:215],v[214:215],v[120:121],v[124:125] op_sel:[0,1,1] op_sel_hi:[1,1,1]
ds_read_b64_tr_b16 v[74:75],v1 offset:18976
v_pk_fma_f32 v[168:169],v[168:169],v[120:121],v[124:125] op_sel:[0,0,0] op_sel_hi:[1,0,0]
ds_read_b64_tr_b16 v[76:77],v2 offset:16928
v_pk_fma_f32 v[170:171],v[170:171],v[120:121],v[124:125] op_sel:[0,1,1] op_sel_hi:[1,1,1]
ds_read_b64_tr_b16 v[78:79],v2 offset:18976
v_pk_fma_f32 v[172:173],v[172:173],v[120:121],v[124:125] op_sel:[0,0,0] op_sel_hi:[1,0,0]
ds_read_b64_tr_b16 v[80:81],v1 offset:21024
v_pk_fma_f32 v[174:175],v[174:175],v[120:121],v[124:125] op_sel:[0,1,1] op_sel_hi:[1,1,1]
ds_read_b64_tr_b16 v[82:83],v1 offset:23072
v_pk_fma_f32 v[216:217],v[216:217],v[120:121],v[124:125] op_sel:[0,0,0] op_sel_hi:[1,0,0]
ds_read_b64_tr_b16 v[84:85],v2 offset:21024
v_pk_fma_f32 v[218:219],v[218:219],v[120:121],v[124:125] op_sel:[0,1,1] op_sel_hi:[1,1,1]
ds_read_b64_tr_b16 v[86:87],v2 offset:23072
v_pk_fma_f32 v[220:221],v[220:221],v[120:121],v[124:125] op_sel:[0,0,0] op_sel_hi:[1,0,0]
v_pk_fma_f32 v[222:223],v[222:223],v[120:121],v[124:125] op_sel:[0,1,1] op_sel_hi:[1,1,1]
v_pk_fma_f32 v[176:177],v[176:177],v[120:121],v[124:125] op_sel:[0,0,0] op_sel_hi:[1,0,0]
v_pk_fma_f32 v[178:179],v[178:179],v[120:121],v[124:125] op_sel:[0,1,1] op_sel_hi:[1,1,1]
v_pk_fma_f32 v[180:181],v[180:181],v[120:121],v[124:125] op_sel:[0,0,0] op_sel_hi:[1,0,0]
v_pk_fma_f32 v[182:183],v[182:183],v[120:121],v[124:125] op_sel:[0,1,1] op_sel_hi:[1,1,1]
v_pk_fma_f32 v[224:225],v[224:225],v[120:121],v[124:125] op_sel:[0,0,0] op_sel_hi:[1,0,0]
v_pk_fma_f32 v[226:227],v[226:227],v[120:121],v[124:125] op_sel:[0,1,1] op_sel_hi:[1,1,1]
v_pk_fma_f32 v[228:229],v[228:229],v[120:121],v[124:125] op_sel:[0,0,0] op_sel_hi:[1,0,0]
v_pk_fma_f32 v[230:231],v[230:231],v[120:121],v[124:125] op_sel:[0,1,1] op_sel_hi:[1,1,1]
v_pk_fma_f32 v[184:185],v[184:185],v[122:123],v[126:127] op_sel:[0,0,0] op_sel_hi:[1,0,0]
v_pk_fma_f32 v[186:187],v[186:187],v[122:123],v[126:127] op_sel:[0,1,1] op_sel_hi:[1,1,1]
v_pk_fma_f32 v[188:189],v[188:189],v[122:123],v[126:127] op_sel:[0,0,0] op_sel_hi:[1,0,0]
v_pk_fma_f32 v[190:191],v[190:191],v[122:123],v[126:127] op_sel:[0,1,1] op_sel_hi:[1,1,1]
v_pk_fma_f32 v[232:233],v[232:233],v[122:123],v[126:127] op_sel:[0,0,0] op_sel_hi:[1,0,0]
v_pk_fma_f32 v[234:235],v[234:235],v[122:123],v[126:127] op_sel:[0,1,1] op_sel_hi:[1,1,1]
v_pk_fma_f32 v[236:237],v[236:237],v[122:123],v[126:127] op_sel:[0,0,0] op_sel_hi:[1,0,0]
v_pk_fma_f32 v[238:239],v[238:239],v[122:123],v[126:127] op_sel:[0,1,1] op_sel_hi:[1,1,1]
v_pk_fma_f32 v[192:193],v[192:193],v[122:123],v[126:127] op_sel:[0,0,0] op_sel_hi:[1,0,0]
v_pk_fma_f32 v[194:195],v[194:195],v[122:123],v[126:127] op_sel:[0,1,1] op_sel_hi:[1,1,1]
v_pk_fma_f32 v[196:197],v[196:197],v[122:123],v[126:127] op_sel:[0,0,0] op_sel_hi:[1,0,0]
v_pk_fma_f32 v[198:199],v[198:199],v[122:123],v[126:127] op_sel:[0,1,1] op_sel_hi:[1,1,1]
v_pk_fma_f32 v[240:241],v[240:241],v[122:123],v[126:127] op_sel:[0,0,0] op_sel_hi:[1,0,0]
v_pk_fma_f32 v[242:243],v[242:243],v[122:123],v[126:127] op_sel:[0,1,1] op_sel_hi:[1,1,1]
v_pk_fma_f32 v[244:245],v[244:245],v[122:123],v[126:127] op_sel:[0,0,0] op_sel_hi:[1,0,0]
v_pk_fma_f32 v[246:247],v[246:247],v[122:123],v[126:127] op_sel:[0,1,1] op_sel_hi:[1,1,1]
v_pk_fma_f32 v[200:201],v[200:201],v[122:123],v[126:127] op_sel:[0,0,0] op_sel_hi:[1,0,0]
v_pk_fma_f32 v[202:203],v[202:203],v[122:123],v[126:127] op_sel:[0,1,1] op_sel_hi:[1,1,1]
v_pk_fma_f32 v[204:205],v[204:205],v[122:123],v[126:127] op_sel:[0,0,0] op_sel_hi:[1,0,0]
v_pk_fma_f32 v[206:207],v[206:207],v[122:123],v[126:127] op_sel:[0,1,1] op_sel_hi:[1,1,1]
v_pk_fma_f32 v[248:249],v[248:249],v[122:123],v[126:127] op_sel:[0,0,0] op_sel_hi:[1,0,0]
v_pk_fma_f32 v[250:251],v[250:251],v[122:123],v[126:127] op_sel:[0,1,1] op_sel_hi:[1,1,1]
v_pk_fma_f32 v[252:253],v[252:253],v[122:123],v[126:127] op_sel:[0,0,0] op_sel_hi:[1,0,0]
v_pk_fma_f32 v[254:255],v[254:255],v[122:123],v[126:127] op_sel:[0,1,1] op_sel_hi:[1,1,1]
v_cvt_pk_bf16_f32 v16,v160,v161
v_cvt_pk_bf16_f32 v20,v162,v163
v_cvt_pk_bf16_f32 v17,v164,v165
v_cvt_pk_bf16_f32 v21,v166,v167
v_cvt_pk_bf16_f32 v18,v208,v209
v_cvt_pk_bf16_f32 v22,v210,v211
v_cvt_pk_bf16_f32 v19,v212,v213
v_cvt_pk_bf16_f32 v23,v214,v215
v_cvt_pk_bf16_f32 v24,v168,v169
v_mfma_f32_16x16x32_bf16 v[160:163],v[104:107],v[128:131],v[108:111]
v_cvt_pk_bf16_f32 v28,v170,v171
v_cvt_pk_bf16_f32 v25,v172,v173
v_mfma_f32_16x16x32_bf16 v[164:167],v[104:107],v[132:135],v[108:111]
v_cvt_pk_bf16_f32 v29,v174,v175
v_cvt_pk_bf16_f32 v26,v216,v217
v_mfma_f32_16x16x32_bf16 v[208:211],v[104:107],v[136:139],v[108:111]
v_cvt_pk_bf16_f32 v30,v218,v219
v_cvt_pk_bf16_f32 v27,v220,v221
v_mfma_f32_16x16x32_bf16 v[212:215],v[104:107],v[140:143],v[108:111]
ds_read_b128 v[104:107],v14 offset:512
v_cvt_pk_bf16_f32 v31,v222,v223
v_cvt_pk_bf16_f32 v32,v176,v177
s_waitcnt lgkmcnt(15)
v_mfma_f32_16x16x32_bf16 v[168:171],v[56:59],v[128:131],v[60:63]
ds_read_b128 v[108:111],v4 offset:3712
v_cvt_pk_bf16_f32 v36,v178,v179
v_cvt_pk_bf16_f32 v33,v180,v181
v_mfma_f32_16x16x32_bf16 v[172:175],v[56:59],v[132:135],v[60:63]
v_cvt_pk_bf16_f32 v37,v182,v183
v_cvt_pk_bf16_f32 v34,v224,v225
v_mfma_f32_16x16x32_bf16 v[216:219],v[56:59],v[136:139],v[60:63]
v_cvt_pk_bf16_f32 v38,v226,v227
v_cvt_pk_bf16_f32 v35,v228,v229
v_mfma_f32_16x16x32_bf16 v[220:223],v[56:59],v[140:143],v[60:63]
ds_read_b128 v[56:59],v14 offset:768
v_cvt_pk_bf16_f32 v39,v230,v231
s_waitcnt lgkmcnt(1)
v_mfma_f32_16x16x32_bf16 v[176:179],v[104:107],v[144:147],v[108:111]
ds_read_b128 v[60:63],v4 offset:3744
v_cvt_pk_bf16_f32 v40,v184,v185
v_mfma_f32_16x16x32_bf16 v[128:131],v[64:67],v[16:19],v[112:115]
v_cvt_pk_bf16_f32 v44,v186,v187
v_mfma_f32_16x16x32_bf16 v[180:183],v[104:107],v[148:151],v[108:111]
v_cvt_pk_bf16_f32 v41,v188,v189
v_mfma_f32_16x16x32_bf16 v[136:139],v[64:67],v[20:23],v[112:115]
v_cvt_pk_bf16_f32 v45,v190,v191
v_mfma_f32_16x16x32_bf16 v[224:227],v[104:107],v[152:155],v[108:111]
v_cvt_pk_bf16_f32 v42,v232,v233
v_mfma_f32_16x16x32_bf16 v[140:143],v[68:71],v[20:23],v[116:119]
v_cvt_pk_bf16_f32 v46,v234,v235
v_mfma_f32_16x16x32_bf16 v[228:231],v[104:107],v[156:159],v[108:111]
ds_read_b128 v[104:107],v14 offset:1024
v_cvt_pk_bf16_f32 v43,v236,v237
v_mfma_f32_16x16x32_bf16 v[132:135],v[68:71],v[16:19],v[116:119]
ds_read_b128 v[108:111],v4 offset:3840
v_cvt_pk_bf16_f32 v47,v238,v239
s_waitcnt lgkmcnt(2)
v_mfma_f32_16x16x32_bf16 v[184:187],v[56:59],v[144:147],v[60:63]
v_cvt_pk_bf16_f32 v48,v192,v193
v_mfma_f32_16x16x32_bf16 v[128:131],v[72:75],v[24:27],v[128:131]
v_cvt_pk_bf16_f32 v52,v194,v195
v_mfma_f32_16x16x32_bf16 v[188:191],v[56:59],v[148:151],v[60:63]
v_cvt_pk_bf16_f32 v49,v196,v197
v_mfma_f32_16x16x32_bf16 v[136:139],v[72:75],v[28:31],v[136:139]
v_cvt_pk_bf16_f32 v53,v198,v199
v_mfma_f32_16x16x32_bf16 v[232:235],v[56:59],v[152:155],v[60:63]
v_cvt_pk_bf16_f32 v50,v240,v241
v_mfma_f32_16x16x32_bf16 v[140:143],v[76:79],v[28:31],v[140:143]
v_cvt_pk_bf16_f32 v54,v242,v243
v_mfma_f32_16x16x32_bf16 v[236:239],v[56:59],v[156:159],v[60:63]
ds_read_b128 v[144:147],v14 offset:1280
v_cvt_pk_bf16_f32 v51,v244,v245
v_mfma_f32_16x16x32_bf16 v[132:135],v[76:79],v[24:27],v[132:135]
ds_read_b128 v[148:151],v4 offset:3872
v_cvt_pk_bf16_f32 v55,v246,v247
s_waitcnt lgkmcnt(2)
v_mfma_f32_16x16x32_bf16 v[192:195],v[104:107],v[88:91],v[108:111]
v_cvt_pk_bf16_f32 v56,v200,v201
v_mfma_f32_16x16x32_bf16 v[128:131],v[80:83],v[32:35],v[128:131]
v_cvt_pk_bf16_f32 v60,v202,v203
v_mfma_f32_16x16x32_bf16 v[196:199],v[104:107],v[92:95],v[108:111]
v_cvt_pk_bf16_f32 v57,v204,v205
v_mfma_f32_16x16x32_bf16 v[136:139],v[80:83],v[36:39],v[136:139]
v_cvt_pk_bf16_f32 v61,v206,v207
v_mfma_f32_16x16x32_bf16 v[240:243],v[104:107],v[96:99],v[108:111]
v_cvt_pk_bf16_f32 v58,v248,v249
v_mfma_f32_16x16x32_bf16 v[140:143],v[84:87],v[36:39],v[140:143]
v_cvt_pk_bf16_f32 v62,v250,v251
v_mfma_f32_16x16x32_bf16 v[244:247],v[104:107],v[100:103],v[108:111]
v_cvt_pk_bf16_f32 v59,v252,v253
v_mfma_f32_16x16x32_bf16 v[132:135],v[84:87],v[32:35],v[132:135]
v_cvt_pk_bf16_f32 v63,v254,v255
s_waitcnt lgkmcnt(0)
v_mfma_f32_16x16x32_bf16 v[200:203],v[144:147],v[88:91],v[148:151]
s_barrier
s_setprio 2
ds_read_b64_tr_b16 v[104:105],v3 offset:128
v_lshlrev_b32_sdwa v128,1,v128 dst_sel:DWORD dst_unused:UNUSED_PAD src0_sel:DWORD src1_sel:WORD_1
v_lshlrev_b32_sdwa v130,1,v130 dst_sel:DWORD dst_unused:UNUSED_PAD src0_sel:DWORD src1_sel:WORD_1
v_mfma_f32_16x16x32_bf16 v[204:207],v[144:147],v[92:95],v[148:151]
ds_read_u16 v128,v128
ds_read_b64_tr_b16 v[106:107],v3 offset:1664
v_lshlrev_b32_sdwa v129,1,v129 dst_sel:DWORD dst_unused:UNUSED_PAD src0_sel:DWORD src1_sel:WORD_1
v_lshlrev_b32_sdwa v131,1,v131 dst_sel:DWORD dst_unused:UNUSED_PAD src0_sel:DWORD src1_sel:WORD_1
v_mfma_f32_16x16x32_bf16 v[248:251],v[144:147],v[96:99],v[148:151]
ds_read_u16_d16_hi v130,v130
ds_read_b64_tr_b16 v[108:109],v3 offset:136
v_fma_mixlo_f16 v120,v141,s99,0
v_lshlrev_b32_sdwa v132,1,v132 dst_sel:DWORD dst_unused:UNUSED_PAD src0_sel:DWORD src1_sel:WORD_1
v_mfma_f32_16x16x32_bf16 v[252:255],v[144:147],v[100:103],v[148:151]
ds_read_u16 v129,v129
ds_read_b64_tr_b16 v[110:111],v3 offset:1672
v_fma_mixhi_f16 v120,v143,s99,0
v_lshlrev_b32_sdwa v134,1,v134 dst_sel:DWORD dst_unused:UNUSED_PAD src0_sel:DWORD src1_sel:WORD_1
v_pk_fma_f16 v121,v120,v120,s54
v_mfma_f32_16x16x32_bf16 v[144:147],v[64:67],v[40:43],v[112:115]
ds_read_u16_d16_hi v131,v131
ds_read_b64_tr_b16 v[88:89],v3 offset:0
v_lshlrev_b32_sdwa v133,1,v133 dst_sel:DWORD dst_unused:UNUSED_PAD src0_sel:DWORD src1_sel:WORD_1
v_pk_mul_f16 v121,v120,v121
v_mfma_f32_16x16x32_bf16 v[152:155],v[64:67],v[44:47],v[112:115]
ds_read_u16 v132,v132
ds_read_b64_tr_b16 v[90:91],v3 offset:1536
v_lshlrev_b32_sdwa v135,1,v135 dst_sel:DWORD dst_unused:UNUSED_PAD src0_sel:DWORD src1_sel:WORD_1
v_exp_f16 v120,v121
v_mfma_f32_16x16x32_bf16 v[156:159],v[68:71],v[44:47],v[116:119]
ds_read_u16_d16_hi v134,v134
ds_read_b64_tr_b16 v[92:93],v3 offset:8
v_lshlrev_b32_sdwa v136,1,v136 dst_sel:DWORD dst_unused:UNUSED_PAD src0_sel:DWORD src1_sel:WORD_1
v_mfma_f32_16x16x32_bf16 v[148:151],v[68:71],v[40:43],v[116:119]
ds_read_u16 v133,v133
ds_read_b64_tr_b16 v[94:95],v3 offset:1544
v_exp_f16_sdwa v120,v121 dst_sel:WORD_1 dst_unused:UNUSED_PRESERVE src0_sel:WORD_1
v_mfma_f32_16x16x32_bf16 v[144:147],v[72:75],v[48:51],v[144:147]
ds_read_u16_d16_hi v135,v135
ds_read_b64_tr_b16 v[96:97],v3 offset:64
v_lshlrev_b32_sdwa v138,1,v138 dst_sel:DWORD dst_unused:UNUSED_PAD src0_sel:DWORD src1_sel:WORD_1
v_mfma_f32_16x16x32_bf16 v[152:155],v[72:75],v[52:55],v[152:155]
ds_read_u16 v136,v136
ds_read_b64_tr_b16 v[98:99],v3 offset:1600
v_pk_fma_f16 v120,v120,s55,s55
v_lshlrev_b32_sdwa v137,1,v137 dst_sel:DWORD dst_unused:UNUSED_PAD src0_sel:DWORD src1_sel:WORD_1
v_mfma_f32_16x16x32_bf16 v[156:159],v[76:79],v[52:55],v[156:159]
ds_read_u16_d16_hi v138,v138
ds_read_b64_tr_b16 v[100:101],v3 offset:72
v_rcp_f16 v121,v120
v_mfma_f32_16x16x32_bf16 v[148:151],v[76:79],v[48:51],v[148:151]
ds_read_u16 v137,v137
ds_read_b64_tr_b16 v[102:103],v3 offset:1608
v_lshlrev_b32_sdwa v139,1,v139 dst_sel:DWORD dst_unused:UNUSED_PAD src0_sel:DWORD src1_sel:WORD_1
v_rcp_f16_sdwa v121,v120 dst_sel:WORD_1 dst_unused:UNUSED_PRESERVE src0_sel:WORD_1
v_mfma_f32_16x16x32_bf16 v[144:147],v[80:83],v[56:59],v[144:147]
ds_read_u16_d16_hi v139,v139
ds_read_b64_tr_b16 v[64:65],v1 offset:0
v_lshlrev_b32_sdwa v140,1,v140 dst_sel:DWORD dst_unused:UNUSED_PAD src0_sel:DWORD src1_sel:WORD_1
v_mfma_f32_16x16x32_bf16 v[152:155],v[80:83],v[60:63],v[152:155]
ds_read_u16 v140,v140
ds_read_b64_tr_b16 v[66:67],v1 offset:2048
v_fma_mix_f32 v141,v141,v121,0 op_sel_hi:[0,1,0]
v_lshlrev_b32_sdwa v142,1,v142 dst_sel:DWORD dst_unused:UNUSED_PAD src0_sel:DWORD src1_sel:WORD_1
v_mfma_f32_16x16x32_bf16 v[156:159],v[84:87],v[60:63],v[156:159]
ds_read_u16_d16_hi v142,v142
ds_read_b128 v[112:115],v4 offset:256
v_fma_mix_f32 v143,v143,v121,0 op_sel:[0,1,0] op_sel_hi:[0,1,0]
s_waitcnt lgkmcnt(15)
v_lshl_add_u64 v[128:129],v[128:129],0,v[130:131]
v_mfma_f32_16x16x32_bf16 v[148:151],v[84:87],v[56:59],v[148:151]
ds_read_b64_tr_b16 v[68:69],v2 offset:0
s_waitcnt lgkmcnt(14)
v_lshl_add_u64 v[130:131],v[132:133],0,v[134:135]
v_lshlrev_b32_sdwa v144,1,v144 dst_sel:DWORD dst_unused:UNUSED_PAD src0_sel:DWORD src1_sel:WORD_1
v_lshlrev_b32_sdwa v146,1,v146 dst_sel:DWORD dst_unused:UNUSED_PAD src0_sel:DWORD src1_sel:WORD_1
v_mfma_f32_16x16x32_bf16 v[160:163],v[88:91],v[128:131],v[160:163]
ds_read_u16 v144,v144
v_lshlrev_b32_sdwa v145,1,v145 dst_sel:DWORD dst_unused:UNUSED_PAD src0_sel:DWORD src1_sel:WORD_1
v_lshlrev_b32_sdwa v147,1,v147 dst_sel:DWORD dst_unused:UNUSED_PAD src0_sel:DWORD src1_sel:WORD_1
v_mfma_f32_16x16x32_bf16 v[168:171],v[92:95],v[128:131],v[168:171]
s_waitcnt lgkmcnt(12)
ds_read_b64_tr_b16 v[70:71],v2 offset:2048
ds_read_b128 v[116:119],v4 offset:288
v_fma_mixlo_f16 v120,v157,s99,0
v_lshlrev_b32_sdwa v148,1,v148 dst_sel:DWORD dst_unused:UNUSED_PAD src0_sel:DWORD src1_sel:WORD_1
v_mfma_f32_16x16x32_bf16 v[176:179],v[96:99],v[128:131],v[176:179]
s_waitcnt lgkmcnt(10)
ds_read_u16_d16_hi v146,v146
v_fma_mixhi_f16 v120,v159,s99,0
v_lshlrev_b32_sdwa v150,1,v150 dst_sel:DWORD dst_unused:UNUSED_PAD src0_sel:DWORD src1_sel:WORD_1
v_mfma_f32_16x16x32_bf16 v[184:187],v[100:103],v[128:131],v[184:187]
s_waitcnt lgkmcnt(10)
ds_read_u16 v145,v145
v_lshl_add_u64 v[136:137],v[136:137],0,v[138:139]
v_mfma_f32_16x16x32_bf16 v[192:195],v[104:107],v[128:131],v[192:195]
s_waitcnt lgkmcnt(7)
ds_read_b64_tr_b16 v[72:73],v1 offset:4096
ds_read_b64_tr_b16 v[74:75],v1 offset:6144
v_or_b32 v138,v142,v140
v_cvt_pk_bf16_f32 v139,v141,v143
v_mfma_f32_16x16x32_bf16 v[200:203],v[108:111],v[128:131],v[200:203]
ds_read_u16_d16_hi v147,v147
v_pk_fma_f16 v121,v120,v120,s54
v_lshlrev_b32_sdwa v149,1,v149 dst_sel:DWORD dst_unused:UNUSED_PAD src0_sel:DWORD src1_sel:WORD_1
v_mfma_f32_16x16x32_bf16 v[164:167],v[88:91],v[136:139],v[164:167]
ds_read_b64_tr_b16 v[76:77],v2 offset:4096
ds_read_b64_tr_b16 v[78:79],v2 offset:6144
v_pk_mul_f16 v121,v120,v121
v_lshlrev_b32_sdwa v151,1,v151 dst_sel:DWORD dst_unused:UNUSED_PAD src0_sel:DWORD src1_sel:WORD_1
v_mfma_f32_16x16x32_bf16 v[172:175],v[92:95],v[136:139],v[172:175]
ds_read_u16 v148,v148
v_exp_f16 v120,v121
v_mfma_f32_16x16x32_bf16 v[180:183],v[96:99],v[136:139],v[180:183]
ds_read_b64_tr_b16 v[80:81],v1 offset:8192
ds_read_b64_tr_b16 v[82:83],v1 offset:10240
v_lshlrev_b32_sdwa v152,1,v152 dst_sel:DWORD dst_unused:UNUSED_PAD src0_sel:DWORD src1_sel:WORD_1
v_mfma_f32_16x16x32_bf16 v[188:191],v[100:103],v[136:139],v[188:191]
ds_read_u16_d16_hi v150,v150
v_exp_f16_sdwa v120,v121 dst_sel:WORD_1 dst_unused:UNUSED_PRESERVE src0_sel:WORD_1
v_mfma_f32_16x16x32_bf16 v[196:199],v[104:107],v[136:139],v[196:199]
ds_read_u16 v149,v149
v_lshlrev_b32_sdwa v154,1,v154 dst_sel:DWORD dst_unused:UNUSED_PAD src0_sel:DWORD src1_sel:WORD_1
v_mfma_f32_16x16x32_bf16 v[204:207],v[108:111],v[136:139],v[204:207]
s_waitcnt lgkmcnt(15)
ds_read_b64_tr_b16 v[84:85],v2 offset:8192
v_pk_fma_f16 v120,v120,s55,s55
v_lshlrev_b32_sdwa v153,1,v153 dst_sel:DWORD dst_unused:UNUSED_PAD src0_sel:DWORD src1_sel:WORD_1
v_mfma_f32_16x16x32_bf16 v[128:131],v[64:67],v[16:19],v[112:115]
ds_read_u16_d16_hi v151,v151
v_rcp_f16 v121,v120
v_mfma_f32_16x16x32_bf16 v[136:139],v[64:67],v[20:23],v[112:115]
s_waitcnt lgkmcnt(14)
ds_read_b64_tr_b16 v[86:87],v2 offset:10240
v_lshlrev_b32_sdwa v155,1,v155 dst_sel:DWORD dst_unused:UNUSED_PAD src0_sel:DWORD src1_sel:WORD_1
v_mfma_f32_16x16x32_bf16 v[140:143],v[68:71],v[20:23],v[116:119]
ds_read_u16 v152,v152
v_rcp_f16_sdwa v121,v120 dst_sel:WORD_1 dst_unused:UNUSED_PRESERVE src0_sel:WORD_1
v_mfma_f32_16x16x32_bf16 v[132:135],v[68:71],v[16:19],v[116:119]
s_waitcnt lgkmcnt(12)
ds_read_u16_d16_hi v154,v154
v_lshlrev_b32_sdwa v156,1,v156 dst_sel:DWORD dst_unused:UNUSED_PAD src0_sel:DWORD src1_sel:WORD_1
v_mfma_f32_16x16x32_bf16 v[128:131],v[72:75],v[24:27],v[128:131]
v_fma_mix_f32 v157,v157,v121,0 op_sel_hi:[0,1,0]
v_lshlrev_b32_sdwa v158,1,v158 dst_sel:DWORD dst_unused:UNUSED_PAD src0_sel:DWORD src1_sel:WORD_1
v_mfma_f32_16x16x32_bf16 v[136:139],v[72:75],v[28:31],v[136:139]
s_waitcnt lgkmcnt(10)
ds_read_u16 v153,v153
v_fma_mix_f32 v159,v159,v121,0 op_sel:[0,1,0] op_sel_hi:[0,1,0]
v_mfma_f32_16x16x32_bf16 v[140:143],v[76:79],v[28:31],v[140:143]
v_mfma_f32_16x16x32_bf16 v[132:135],v[76:79],v[24:27],v[132:135]
s_waitcnt lgkmcnt(8)
ds_read_u16_d16_hi v155,v155
v_mfma_f32_16x16x32_bf16 v[128:131],v[80:83],v[32:35],v[128:131]
v_mfma_f32_16x16x32_bf16 v[136:139],v[80:83],v[36:39],v[136:139]
s_waitcnt lgkmcnt(4)
ds_read_u16 v156,v156
v_lshl_add_u64 v[144:145],v[144:145],0,v[146:147]
v_lshl_add_u64 v[146:147],v[148:149],0,v[150:151]
v_mfma_f32_16x16x32_bf16 v[140:143],v[84:87],v[36:39],v[140:143]
ds_read_u16_d16_hi v158,v158
v_mfma_f32_16x16x32_bf16 v[132:135],v[84:87],v[32:35],v[132:135]
v_lshlrev_b32_sdwa v128,1,v128 dst_sel:DWORD dst_unused:UNUSED_PAD src0_sel:DWORD src1_sel:WORD_1
v_lshlrev_b32_sdwa v130,1,v130 dst_sel:DWORD dst_unused:UNUSED_PAD src0_sel:DWORD src1_sel:WORD_1
v_mfma_f32_16x16x32_bf16 v[208:211],v[88:91],v[144:147],v[208:211]
ds_read_u16 v128,v128
v_lshlrev_b32_sdwa v129,1,v129 dst_sel:DWORD dst_unused:UNUSED_PAD src0_sel:DWORD src1_sel:WORD_1
v_lshlrev_b32_sdwa v131,1,v131 dst_sel:DWORD dst_unused:UNUSED_PAD src0_sel:DWORD src1_sel:WORD_1
v_mfma_f32_16x16x32_bf16 v[216:219],v[92:95],v[144:147],v[216:219]
v_fma_mixlo_f16 v120,v141,s99,0
v_lshlrev_b32_sdwa v132,1,v132 dst_sel:DWORD dst_unused:UNUSED_PAD src0_sel:DWORD src1_sel:WORD_1
v_mfma_f32_16x16x32_bf16 v[224:227],v[96:99],v[144:147],v[224:227]
ds_read_u16_d16_hi v130,v130
v_fma_mixhi_f16 v120,v143,s99,0
v_lshlrev_b32_sdwa v134,1,v134 dst_sel:DWORD dst_unused:UNUSED_PAD src0_sel:DWORD src1_sel:WORD_1
v_mfma_f32_16x16x32_bf16 v[232:235],v[100:103],v[144:147],v[232:235]
s_waitcnt lgkmcnt(4)
ds_read_u16 v129,v129
v_lshl_add_u64 v[152:153],v[152:153],0,v[154:155]
v_mfma_f32_16x16x32_bf16 v[240:243],v[104:107],v[144:147],v[240:243]
s_waitcnt lgkmcnt(3)
v_or_b32 v154,v158,v156
v_cvt_pk_bf16_f32 v155,v157,v159
v_mfma_f32_16x16x32_bf16 v[248:251],v[108:111],v[144:147],v[248:251]
ds_read_u16_d16_hi v131,v131
v_pk_fma_f16 v121,v120,v120,s54
v_lshlrev_b32_sdwa v133,1,v133 dst_sel:DWORD dst_unused:UNUSED_PAD src0_sel:DWORD src1_sel:WORD_1
v_mfma_f32_16x16x32_bf16 v[212:215],v[88:91],v[152:155],v[212:215]
ds_read_b64_tr_b16 v[88:89],v3 offset:6160
v_pk_mul_f16 v121,v120,v121
v_lshlrev_b32_sdwa v135,1,v135 dst_sel:DWORD dst_unused:UNUSED_PAD src0_sel:DWORD src1_sel:WORD_1
v_mfma_f32_16x16x32_bf16 v[220:223],v[92:95],v[152:155],v[220:223]
ds_read_u16 v132,v132
v_exp_f16 v120,v121
v_mfma_f32_16x16x32_bf16 v[228:231],v[96:99],v[152:155],v[228:231]
ds_read_b64_tr_b16 v[90:91],v3 offset:7696
ds_read_b64_tr_b16 v[92:93],v3 offset:6168
v_lshlrev_b32_sdwa v136,1,v136 dst_sel:DWORD dst_unused:UNUSED_PAD src0_sel:DWORD src1_sel:WORD_1
v_mfma_f32_16x16x32_bf16 v[236:239],v[100:103],v[152:155],v[236:239]
ds_read_u16_d16_hi v134,v134
v_exp_f16_sdwa v120,v121 dst_sel:WORD_1 dst_unused:UNUSED_PRESERVE src0_sel:WORD_1
v_mfma_f32_16x16x32_bf16 v[244:247],v[104:107],v[152:155],v[244:247]
ds_read_u16 v133,v133
v_lshlrev_b32_sdwa v138,1,v138 dst_sel:DWORD dst_unused:UNUSED_PAD src0_sel:DWORD src1_sel:WORD_1
v_mfma_f32_16x16x32_bf16 v[252:255],v[108:111],v[152:155],v[252:255]
ds_read_b64_tr_b16 v[94:95],v3 offset:7704
ds_read_b64_tr_b16 v[96:97],v3 offset:6224
v_pk_fma_f16 v120,v120,s55,s55
v_lshlrev_b32_sdwa v137,1,v137 dst_sel:DWORD dst_unused:UNUSED_PAD src0_sel:DWORD src1_sel:WORD_1
v_mfma_f32_16x16x32_bf16 v[144:147],v[64:67],v[40:43],v[112:115]
ds_read_u16_d16_hi v135,v135
v_rcp_f16 v121,v120
v_mfma_f32_16x16x32_bf16 v[152:155],v[64:67],v[44:47],v[112:115]
ds_read_b64_tr_b16 v[98:99],v3 offset:7760
ds_read_b64_tr_b16 v[100:101],v3 offset:6232
v_lshlrev_b32_sdwa v139,1,v139 dst_sel:DWORD dst_unused:UNUSED_PAD src0_sel:DWORD src1_sel:WORD_1
v_mfma_f32_16x16x32_bf16 v[156:159],v[68:71],v[44:47],v[116:119]
ds_read_u16 v136,v136
v_rcp_f16_sdwa v121,v120 dst_sel:WORD_1 dst_unused:UNUSED_PRESERVE src0_sel:WORD_1
v_mfma_f32_16x16x32_bf16 v[148:151],v[68:71],v[40:43],v[116:119]
ds_read_u16_d16_hi v138,v138
v_lshlrev_b32_sdwa v140,1,v140 dst_sel:DWORD dst_unused:UNUSED_PAD src0_sel:DWORD src1_sel:WORD_1
v_mfma_f32_16x16x32_bf16 v[144:147],v[72:75],v[48:51],v[144:147]
ds_read_b64_tr_b16 v[102:103],v3 offset:7768
ds_read_b64_tr_b16 v[104:105],v3 offset:6288
v_fma_mix_f32 v141,v141,v121,0 op_sel_hi:[0,1,0]
v_lshlrev_b32_sdwa v142,1,v142 dst_sel:DWORD dst_unused:UNUSED_PAD src0_sel:DWORD src1_sel:WORD_1
v_mfma_f32_16x16x32_bf16 v[152:155],v[72:75],v[52:55],v[152:155]
ds_read_u16 v137,v137
v_fma_mix_f32 v143,v143,v121,0 op_sel:[0,1,0] op_sel_hi:[0,1,0]
v_mfma_f32_16x16x32_bf16 v[156:159],v[76:79],v[52:55],v[156:159]
ds_read_b64_tr_b16 v[106:107],v3 offset:7824
ds_read_b64_tr_b16 v[108:109],v3 offset:6296
v_mfma_f32_16x16x32_bf16 v[148:151],v[76:79],v[48:51],v[148:151]
ds_read_u16_d16_hi v139,v139
v_mfma_f32_16x16x32_bf16 v[144:147],v[80:83],v[56:59],v[144:147]
ds_read_b64_tr_b16 v[110:111],v3 offset:7832
ds_read_b64_tr_b16 v[64:65],v1 offset:512
v_mfma_f32_16x16x32_bf16 v[152:155],v[80:83],v[60:63],v[152:155]
s_waitcnt lgkmcnt(12)
ds_read_u16 v140,v140
v_lshl_add_u64 v[128:129],v[128:129],0,v[130:131]
v_lshl_add_u64 v[130:131],v[132:133],0,v[134:135]
v_mfma_f32_16x16x32_bf16 v[156:159],v[84:87],v[60:63],v[156:159]
ds_read_u16_d16_hi v142,v142
v_mfma_f32_16x16x32_bf16 v[148:151],v[84:87],v[56:59],v[148:151]
ds_read_b64_tr_b16 v[66:67],v1 offset:2560
ds_read_b128 v[112:115],v4 offset:384
v_lshlrev_b32_sdwa v144,1,v144 dst_sel:DWORD dst_unused:UNUSED_PAD src0_sel:DWORD src1_sel:WORD_1
v_lshlrev_b32_sdwa v146,1,v146 dst_sel:DWORD dst_unused:UNUSED_PAD src0_sel:DWORD src1_sel:WORD_1
v_mfma_f32_16x16x32_bf16 v[160:163],v[88:91],v[128:131],v[160:163]
ds_read_u16 v144,v144
v_lshlrev_b32_sdwa v145,1,v145 dst_sel:DWORD dst_unused:UNUSED_PAD src0_sel:DWORD src1_sel:WORD_1
v_lshlrev_b32_sdwa v147,1,v147 dst_sel:DWORD dst_unused:UNUSED_PAD src0_sel:DWORD src1_sel:WORD_1
v_mfma_f32_16x16x32_bf16 v[168:171],v[92:95],v[128:131],v[168:171]
s_waitcnt lgkmcnt(15)
ds_read_b64_tr_b16 v[68:69],v2 offset:512
ds_read_b64_tr_b16 v[70:71],v2 offset:2560
v_fma_mixlo_f16 v120,v157,s99,0
v_lshlrev_b32_sdwa v148,1,v148 dst_sel:DWORD dst_unused:UNUSED_PAD src0_sel:DWORD src1_sel:WORD_1
v_mfma_f32_16x16x32_bf16 v[176:179],v[96:99],v[128:131],v[176:179]
s_waitcnt lgkmcnt(14)
ds_read_u16_d16_hi v146,v146
v_fma_mixhi_f16 v120,v159,s99,0
v_lshlrev_b32_sdwa v150,1,v150 dst_sel:DWORD dst_unused:UNUSED_PAD src0_sel:DWORD src1_sel:WORD_1
v_mfma_f32_16x16x32_bf16 v[184:187],v[100:103],v[128:131],v[184:187]
s_waitcnt lgkmcnt(10)
ds_read_u16 v145,v145
v_lshl_add_u64 v[136:137],v[136:137],0,v[138:139]
v_mfma_f32_16x16x32_bf16 v[192:195],v[104:107],v[128:131],v[192:195]
s_waitcnt lgkmcnt(7)
ds_read_b128 v[116:119],v4 offset:416
ds_read_b64_tr_b16 v[72:73],v1 offset:4608
v_or_b32 v138,v142,v140
v_cvt_pk_bf16_f32 v139,v141,v143
v_mfma_f32_16x16x32_bf16 v[200:203],v[108:111],v[128:131],v[200:203]
ds_read_u16_d16_hi v147,v147
v_pk_fma_f16 v121,v120,v120,s54
v_lshlrev_b32_sdwa v149,1,v149 dst_sel:DWORD dst_unused:UNUSED_PAD src0_sel:DWORD src1_sel:WORD_1
v_mfma_f32_16x16x32_bf16 v[164:167],v[88:91],v[136:139],v[164:167]
ds_read_b64_tr_b16 v[74:75],v1 offset:6656
ds_read_b64_tr_b16 v[76:77],v2 offset:4608
v_pk_mul_f16 v121,v120,v121
v_lshlrev_b32_sdwa v151,1,v151 dst_sel:DWORD dst_unused:UNUSED_PAD src0_sel:DWORD src1_sel:WORD_1
v_mfma_f32_16x16x32_bf16 v[172:175],v[92:95],v[136:139],v[172:175]
ds_read_u16 v148,v148
v_exp_f16 v120,v121
v_mfma_f32_16x16x32_bf16 v[180:183],v[96:99],v[136:139],v[180:183]
ds_read_b64_tr_b16 v[78:79],v2 offset:6656
ds_read_b64_tr_b16 v[80:81],v1 offset:8704
v_lshlrev_b32_sdwa v152,1,v152 dst_sel:DWORD dst_unused:UNUSED_PAD src0_sel:DWORD src1_sel:WORD_1
v_mfma_f32_16x16x32_bf16 v[188:191],v[100:103],v[136:139],v[188:191]
ds_read_u16_d16_hi v150,v150
v_exp_f16_sdwa v120,v121 dst_sel:WORD_1 dst_unused:UNUSED_PRESERVE src0_sel:WORD_1
v_mfma_f32_16x16x32_bf16 v[196:199],v[104:107],v[136:139],v[196:199]
ds_read_u16 v149,v149
v_lshlrev_b32_sdwa v154,1,v154 dst_sel:DWORD dst_unused:UNUSED_PAD src0_sel:DWORD src1_sel:WORD_1
v_mfma_f32_16x16x32_bf16 v[204:207],v[108:111],v[136:139],v[204:207]
s_waitcnt lgkmcnt(15)
ds_read_b64_tr_b16 v[82:83],v1 offset:10752
v_pk_fma_f16 v120,v120,s55,s55
s_mov_b32 s86,0xbe801d5a
v_lshlrev_b32_sdwa v153,1,v153 dst_sel:DWORD dst_unused:UNUSED_PAD src0_sel:DWORD src1_sel:WORD_1
v_mfma_f32_16x16x32_bf16 v[128:131],v[64:67],v[16:19],v[112:115]
ds_read_u16_d16_hi v151,v151
v_rcp_f16 v121,v120
v_mfma_f32_16x16x32_bf16 v[136:139],v[64:67],v[20:23],v[112:115]
s_waitcnt lgkmcnt(11)
ds_read_b64_tr_b16 v[84:85],v2 offset:8704
v_lshlrev_b32_sdwa v155,1,v155 dst_sel:DWORD dst_unused:UNUSED_PAD src0_sel:DWORD src1_sel:WORD_1
v_mfma_f32_16x16x32_bf16 v[140:143],v[68:71],v[20:23],v[116:119]
ds_read_u16 v152,v152
v_rcp_f16_sdwa v121,v120 dst_sel:WORD_1 dst_unused:UNUSED_PRESERVE src0_sel:WORD_1
v_mfma_f32_16x16x32_bf16 v[132:135],v[68:71],v[16:19],v[116:119]
s_waitcnt lgkmcnt(10)
ds_read_u16_d16_hi v154,v154
v_lshlrev_b32_sdwa v156,1,v156 dst_sel:DWORD dst_unused:UNUSED_PAD src0_sel:DWORD src1_sel:WORD_1
v_mfma_f32_16x16x32_bf16 v[128:131],v[72:75],v[24:27],v[128:131]
ds_read_b64_tr_b16 v[86:87],v2 offset:10752
v_fma_mix_f32 v157,v157,v121,0 op_sel_hi:[0,1,0]
v_lshlrev_b32_sdwa v158,1,v158 dst_sel:DWORD dst_unused:UNUSED_PAD src0_sel:DWORD src1_sel:WORD_1
v_mfma_f32_16x16x32_bf16 v[136:139],v[72:75],v[28:31],v[136:139]
s_waitcnt lgkmcnt(9)
ds_read_u16 v153,v153
v_fma_mix_f32 v159,v159,v121,0 op_sel:[0,1,0] op_sel_hi:[0,1,0]
v_mfma_f32_16x16x32_bf16 v[140:143],v[76:79],v[28:31],v[140:143]
v_mfma_f32_16x16x32_bf16 v[132:135],v[76:79],v[24:27],v[132:135]
s_waitcnt lgkmcnt(6)
ds_read_u16_d16_hi v155,v155
v_mfma_f32_16x16x32_bf16 v[128:131],v[80:83],v[32:35],v[128:131]
v_mfma_f32_16x16x32_bf16 v[136:139],v[80:83],v[36:39],v[136:139]
s_waitcnt lgkmcnt(2)
ds_read_u16 v156,v156
v_lshl_add_u64 v[144:145],v[144:145],0,v[146:147]
v_lshl_add_u64 v[146:147],v[148:149],0,v[150:151]
v_mfma_f32_16x16x32_bf16 v[140:143],v[84:87],v[36:39],v[140:143]
ds_read_u16_d16_hi v158,v158
v_mfma_f32_16x16x32_bf16 v[132:135],v[84:87],v[32:35],v[132:135]
v_lshlrev_b32_sdwa v128,1,v128 dst_sel:DWORD dst_unused:UNUSED_PAD src0_sel:DWORD src1_sel:WORD_1
v_lshlrev_b32_sdwa v130,1,v130 dst_sel:DWORD dst_unused:UNUSED_PAD src0_sel:DWORD src1_sel:WORD_1
v_mfma_f32_16x16x32_bf16 v[208:211],v[88:91],v[144:147],v[208:211]
s_barrier
ds_read_u16 v128,v128
v_lshlrev_b32_sdwa v129,1,v129 dst_sel:DWORD dst_unused:UNUSED_PAD src0_sel:DWORD src1_sel:WORD_1
v_lshlrev_b32_sdwa v131,1,v131 dst_sel:DWORD dst_unused:UNUSED_PAD src0_sel:DWORD src1_sel:WORD_1
v_mfma_f32_16x16x32_bf16 v[216:219],v[92:95],v[144:147],v[216:219]
v_fma_mixlo_f16 v120,v141,s99,0
v_lshlrev_b32_sdwa v132,1,v132 dst_sel:DWORD dst_unused:UNUSED_PAD src0_sel:DWORD src1_sel:WORD_1
v_mfma_f32_16x16x32_bf16 v[224:227],v[96:99],v[144:147],v[224:227]
ds_read_u16_d16_hi v130,v130
v_fma_mixhi_f16 v120,v143,s99,0
v_lshlrev_b32_sdwa v134,1,v134 dst_sel:DWORD dst_unused:UNUSED_PAD src0_sel:DWORD src1_sel:WORD_1
v_mfma_f32_16x16x32_bf16 v[232:235],v[100:103],v[144:147],v[232:235]
s_waitcnt lgkmcnt(4)
ds_read_u16 v129,v129
v_lshl_add_u64 v[152:153],v[152:153],0,v[154:155]
v_mfma_f32_16x16x32_bf16 v[240:243],v[104:107],v[144:147],v[240:243]
s_waitcnt lgkmcnt(3)
v_or_b32 v154,v158,v156
v_cvt_pk_bf16_f32 v155,v157,v159
v_mfma_f32_16x16x32_bf16 v[248:251],v[108:111],v[144:147],v[248:251]
ds_read_u16_d16_hi v131,v131
v_pk_fma_f16 v121,v120,v120,s54
v_lshlrev_b32_sdwa v133,1,v133 dst_sel:DWORD dst_unused:UNUSED_PAD src0_sel:DWORD src1_sel:WORD_1
v_mfma_f32_16x16x32_bf16 v[212:215],v[88:91],v[152:155],v[212:215]
ds_read_b64_tr_b16 v[88:89],v3 offset:12320
v_pk_mul_f16 v121,v120,v121
v_lshlrev_b32_sdwa v135,1,v135 dst_sel:DWORD dst_unused:UNUSED_PAD src0_sel:DWORD src1_sel:WORD_1
v_mfma_f32_16x16x32_bf16 v[220:223],v[92:95],v[152:155],v[220:223]
ds_read_u16 v132,v132
v_exp_f16 v120,v121
v_mfma_f32_16x16x32_bf16 v[228:231],v[96:99],v[152:155],v[228:231]
ds_read_b64_tr_b16 v[90:91],v3 offset:13856
ds_read_b64_tr_b16 v[92:93],v3 offset:12328
v_lshlrev_b32_sdwa v136,1,v136 dst_sel:DWORD dst_unused:UNUSED_PAD src0_sel:DWORD src1_sel:WORD_1
v_mfma_f32_16x16x32_bf16 v[236:239],v[100:103],v[152:155],v[236:239]
ds_read_u16_d16_hi v134,v134
v_exp_f16_sdwa v120,v121 dst_sel:WORD_1 dst_unused:UNUSED_PRESERVE src0_sel:WORD_1
v_mfma_f32_16x16x32_bf16 v[244:247],v[104:107],v[152:155],v[244:247]
ds_read_u16 v133,v133
v_lshlrev_b32_sdwa v138,1,v138 dst_sel:DWORD dst_unused:UNUSED_PAD src0_sel:DWORD src1_sel:WORD_1
v_mfma_f32_16x16x32_bf16 v[252:255],v[108:111],v[152:155],v[252:255]
ds_read_b64_tr_b16 v[94:95],v3 offset:13864
ds_read_b64_tr_b16 v[96:97],v3 offset:12384
v_pk_fma_f16 v120,v120,s55,s55
v_lshlrev_b32_sdwa v137,1,v137 dst_sel:DWORD dst_unused:UNUSED_PAD src0_sel:DWORD src1_sel:WORD_1
v_mfma_f32_16x16x32_bf16 v[144:147],v[64:67],v[40:43],v[112:115]
ds_read_u16_d16_hi v135,v135
v_rcp_f16 v121,v120
v_mfma_f32_16x16x32_bf16 v[152:155],v[64:67],v[44:47],v[112:115]
ds_read_b64_tr_b16 v[98:99],v3 offset:13920
ds_read_b64_tr_b16 v[100:101],v3 offset:12392
v_lshlrev_b32_sdwa v139,1,v139 dst_sel:DWORD dst_unused:UNUSED_PAD src0_sel:DWORD src1_sel:WORD_1
v_mfma_f32_16x16x32_bf16 v[156:159],v[68:71],v[44:47],v[116:119]
ds_read_u16 v136,v136
v_rcp_f16_sdwa v121,v120 dst_sel:WORD_1 dst_unused:UNUSED_PRESERVE src0_sel:WORD_1
v_mfma_f32_16x16x32_bf16 v[148:151],v[68:71],v[40:43],v[116:119]
ds_read_u16_d16_hi v138,v138
v_lshlrev_b32_sdwa v140,1,v140 dst_sel:DWORD dst_unused:UNUSED_PAD src0_sel:DWORD src1_sel:WORD_1
v_mfma_f32_16x16x32_bf16 v[144:147],v[72:75],v[48:51],v[144:147]
ds_read_b64_tr_b16 v[102:103],v3 offset:13928
ds_read_b64_tr_b16 v[104:105],v3 offset:12448
v_fma_mix_f32 v141,v141,v121,0 op_sel_hi:[0,1,0]
v_lshlrev_b32_sdwa v142,1,v142 dst_sel:DWORD dst_unused:UNUSED_PAD src0_sel:DWORD src1_sel:WORD_1
v_mfma_f32_16x16x32_bf16 v[152:155],v[72:75],v[52:55],v[152:155]
ds_read_u16 v137,v137
v_fma_mix_f32 v143,v143,v121,0 op_sel:[0,1,0] op_sel_hi:[0,1,0]
v_mfma_f32_16x16x32_bf16 v[156:159],v[76:79],v[52:55],v[156:159]
ds_read_b64_tr_b16 v[106:107],v3 offset:13984
ds_read_b64_tr_b16 v[108:109],v3 offset:12456
v_mfma_f32_16x16x32_bf16 v[148:151],v[76:79],v[48:51],v[148:151]
ds_read_u16_d16_hi v139,v139
v_mfma_f32_16x16x32_bf16 v[144:147],v[80:83],v[56:59],v[144:147]
ds_read_b64_tr_b16 v[110:111],v3 offset:13992
ds_read_b64_tr_b16 v[64:65],v1 offset:12320
v_mfma_f32_16x16x32_bf16 v[152:155],v[80:83],v[60:63],v[152:155]
s_waitcnt lgkmcnt(12)
ds_read_u16 v140,v140
v_lshl_add_u64 v[128:129],v[128:129],0,v[130:131]
v_lshl_add_u64 v[130:131],v[132:133],0,v[134:135]
v_mfma_f32_16x16x32_bf16 v[156:159],v[84:87],v[60:63],v[156:159]
ds_read_u16_d16_hi v142,v142
v_mfma_f32_16x16x32_bf16 v[148:151],v[84:87],v[56:59],v[148:151]
ds_read_b64_tr_b16 v[66:67],v1 offset:14368
ds_read_b128 v[112:115],v4 offset:512
v_lshlrev_b32_sdwa v144,1,v144 dst_sel:DWORD dst_unused:UNUSED_PAD src0_sel:DWORD src1_sel:WORD_1
v_lshlrev_b32_sdwa v146,1,v146 dst_sel:DWORD dst_unused:UNUSED_PAD src0_sel:DWORD src1_sel:WORD_1
v_mfma_f32_16x16x32_bf16 v[160:163],v[88:91],v[128:131],v[160:163]
ds_read_u16 v144,v144
v_lshlrev_b32_sdwa v145,1,v145 dst_sel:DWORD dst_unused:UNUSED_PAD src0_sel:DWORD src1_sel:WORD_1
v_lshlrev_b32_sdwa v147,1,v147 dst_sel:DWORD dst_unused:UNUSED_PAD src0_sel:DWORD src1_sel:WORD_1
v_mfma_f32_16x16x32_bf16 v[168:171],v[92:95],v[128:131],v[168:171]
s_waitcnt lgkmcnt(15)
ds_read_b64_tr_b16 v[68:69],v2 offset:12320
ds_read_b64_tr_b16 v[70:71],v2 offset:14368
v_fma_mixlo_f16 v120,v157,s99,0
v_lshlrev_b32_sdwa v148,1,v148 dst_sel:DWORD dst_unused:UNUSED_PAD src0_sel:DWORD src1_sel:WORD_1
v_mfma_f32_16x16x32_bf16 v[176:179],v[96:99],v[128:131],v[176:179]
s_waitcnt lgkmcnt(14)
ds_read_u16_d16_hi v146,v146
v_fma_mixhi_f16 v120,v159,s99,0
v_lshlrev_b32_sdwa v150,1,v150 dst_sel:DWORD dst_unused:UNUSED_PAD src0_sel:DWORD src1_sel:WORD_1
v_mfma_f32_16x16x32_bf16 v[184:187],v[100:103],v[128:131],v[184:187]
s_waitcnt lgkmcnt(10)
ds_read_u16 v145,v145
v_lshl_add_u64 v[136:137],v[136:137],0,v[138:139]
v_mfma_f32_16x16x32_bf16 v[192:195],v[104:107],v[128:131],v[192:195]
s_waitcnt lgkmcnt(7)
ds_read_b128 v[116:119],v4 offset:544
ds_read_b64_tr_b16 v[72:73],v1 offset:16416
v_or_b32 v138,v142,v140
v_cvt_pk_bf16_f32 v139,v141,v143
v_mfma_f32_16x16x32_bf16 v[200:203],v[108:111],v[128:131],v[200:203]
ds_read_u16_d16_hi v147,v147
v_pk_fma_f16 v121,v120,v120,s54
v_lshlrev_b32_sdwa v149,1,v149 dst_sel:DWORD dst_unused:UNUSED_PAD src0_sel:DWORD src1_sel:WORD_1
v_mfma_f32_16x16x32_bf16 v[164:167],v[88:91],v[136:139],v[164:167]
ds_read_b64_tr_b16 v[74:75],v1 offset:18464
ds_read_b64_tr_b16 v[76:77],v2 offset:16416
v_pk_mul_f16 v121,v120,v121
v_lshlrev_b32_sdwa v151,1,v151 dst_sel:DWORD dst_unused:UNUSED_PAD src0_sel:DWORD src1_sel:WORD_1
v_mfma_f32_16x16x32_bf16 v[172:175],v[92:95],v[136:139],v[172:175]
ds_read_u16 v148,v148
v_exp_f16 v120,v121
v_mfma_f32_16x16x32_bf16 v[180:183],v[96:99],v[136:139],v[180:183]
ds_read_b64_tr_b16 v[78:79],v2 offset:18464
ds_read_b64_tr_b16 v[80:81],v1 offset:20512
v_lshlrev_b32_sdwa v152,1,v152 dst_sel:DWORD dst_unused:UNUSED_PAD src0_sel:DWORD src1_sel:WORD_1
v_mfma_f32_16x16x32_bf16 v[188:191],v[100:103],v[136:139],v[188:191]
ds_read_u16_d16_hi v150,v150
v_exp_f16_sdwa v120,v121 dst_sel:WORD_1 dst_unused:UNUSED_PRESERVE src0_sel:WORD_1
v_mfma_f32_16x16x32_bf16 v[196:199],v[104:107],v[136:139],v[196:199]
ds_read_u16 v149,v149
v_lshlrev_b32_sdwa v154,1,v154 dst_sel:DWORD dst_unused:UNUSED_PAD src0_sel:DWORD src1_sel:WORD_1
v_mfma_f32_16x16x32_bf16 v[204:207],v[108:111],v[136:139],v[204:207]
s_waitcnt lgkmcnt(15)
ds_read_b64_tr_b16 v[82:83],v1 offset:22560
v_pk_fma_f16 v120,v120,s55,s55
v_lshlrev_b32_sdwa v153,1,v153 dst_sel:DWORD dst_unused:UNUSED_PAD src0_sel:DWORD src1_sel:WORD_1
v_mfma_f32_16x16x32_bf16 v[128:131],v[64:67],v[16:19],v[112:115]
ds_read_u16_d16_hi v151,v151
v_rcp_f16 v121,v120
v_mfma_f32_16x16x32_bf16 v[136:139],v[64:67],v[20:23],v[112:115]
s_waitcnt lgkmcnt(11)
ds_read_b64_tr_b16 v[84:85],v2 offset:20512
v_lshlrev_b32_sdwa v155,1,v155 dst_sel:DWORD dst_unused:UNUSED_PAD src0_sel:DWORD src1_sel:WORD_1
v_mfma_f32_16x16x32_bf16 v[140:143],v[68:71],v[20:23],v[116:119]
ds_read_u16 v152,v152
v_rcp_f16_sdwa v121,v120 dst_sel:WORD_1 dst_unused:UNUSED_PRESERVE src0_sel:WORD_1
v_mfma_f32_16x16x32_bf16 v[132:135],v[68:71],v[16:19],v[116:119]
s_waitcnt lgkmcnt(10)
ds_read_u16_d16_hi v154,v154
v_lshlrev_b32_sdwa v156,1,v156 dst_sel:DWORD dst_unused:UNUSED_PAD src0_sel:DWORD src1_sel:WORD_1
v_mfma_f32_16x16x32_bf16 v[128:131],v[72:75],v[24:27],v[128:131]
ds_read_b64_tr_b16 v[86:87],v2 offset:22560
v_fma_mix_f32 v157,v157,v121,0 op_sel_hi:[0,1,0]
v_lshlrev_b32_sdwa v158,1,v158 dst_sel:DWORD dst_unused:UNUSED_PAD src0_sel:DWORD src1_sel:WORD_1
v_mfma_f32_16x16x32_bf16 v[136:139],v[72:75],v[28:31],v[136:139]
s_waitcnt lgkmcnt(9)
ds_read_u16 v153,v153
v_fma_mix_f32 v159,v159,v121,0 op_sel:[0,1,0] op_sel_hi:[0,1,0]
v_mfma_f32_16x16x32_bf16 v[140:143],v[76:79],v[28:31],v[140:143]
v_mfma_f32_16x16x32_bf16 v[132:135],v[76:79],v[24:27],v[132:135]
s_waitcnt lgkmcnt(6)
ds_read_u16_d16_hi v155,v155
v_mfma_f32_16x16x32_bf16 v[128:131],v[80:83],v[32:35],v[128:131]
v_mfma_f32_16x16x32_bf16 v[136:139],v[80:83],v[36:39],v[136:139]
s_waitcnt lgkmcnt(2)
ds_read_u16 v156,v156
v_lshl_add_u64 v[144:145],v[144:145],0,v[146:147]
v_lshl_add_u64 v[146:147],v[148:149],0,v[150:151]
v_mfma_f32_16x16x32_bf16 v[140:143],v[84:87],v[36:39],v[140:143]
ds_read_u16_d16_hi v158,v158
v_mfma_f32_16x16x32_bf16 v[132:135],v[84:87],v[32:35],v[132:135]
v_lshlrev_b32_sdwa v128,1,v128 dst_sel:DWORD dst_unused:UNUSED_PAD src0_sel:DWORD src1_sel:WORD_1
v_lshlrev_b32_sdwa v130,1,v130 dst_sel:DWORD dst_unused:UNUSED_PAD src0_sel:DWORD src1_sel:WORD_1
v_mfma_f32_16x16x32_bf16 v[208:211],v[88:91],v[144:147],v[208:211]
ds_read_u16 v128,v128
v_lshlrev_b32_sdwa v129,1,v129 dst_sel:DWORD dst_unused:UNUSED_PAD src0_sel:DWORD src1_sel:WORD_1
v_lshlrev_b32_sdwa v131,1,v131 dst_sel:DWORD dst_unused:UNUSED_PAD src0_sel:DWORD src1_sel:WORD_1
v_mfma_f32_16x16x32_bf16 v[216:219],v[92:95],v[144:147],v[216:219]
v_fma_mixlo_f16 v120,v141,s99,0
v_lshlrev_b32_sdwa v132,1,v132 dst_sel:DWORD dst_unused:UNUSED_PAD src0_sel:DWORD src1_sel:WORD_1
v_mfma_f32_16x16x32_bf16 v[224:227],v[96:99],v[144:147],v[224:227]
ds_read_u16_d16_hi v130,v130
v_fma_mixhi_f16 v120,v143,s99,0
v_lshlrev_b32_sdwa v134,1,v134 dst_sel:DWORD dst_unused:UNUSED_PAD src0_sel:DWORD src1_sel:WORD_1
v_mfma_f32_16x16x32_bf16 v[232:235],v[100:103],v[144:147],v[232:235]
s_waitcnt lgkmcnt(4)
ds_read_u16 v129,v129
v_lshl_add_u64 v[152:153],v[152:153],0,v[154:155]
v_mfma_f32_16x16x32_bf16 v[240:243],v[104:107],v[144:147],v[240:243]
s_waitcnt lgkmcnt(3)
v_or_b32 v154,v158,v156
v_cvt_pk_bf16_f32 v155,v157,v159
v_mfma_f32_16x16x32_bf16 v[248:251],v[108:111],v[144:147],v[248:251]
ds_read_u16_d16_hi v131,v131
v_pk_fma_f16 v121,v120,v120,s54
v_lshlrev_b32_sdwa v133,1,v133 dst_sel:DWORD dst_unused:UNUSED_PAD src0_sel:DWORD src1_sel:WORD_1
v_mfma_f32_16x16x32_bf16 v[212:215],v[88:91],v[152:155],v[212:215]
ds_read_b64_tr_b16 v[88:89],v3 offset:18480
v_pk_mul_f16 v121,v120,v121
v_lshlrev_b32_sdwa v135,1,v135 dst_sel:DWORD dst_unused:UNUSED_PAD src0_sel:DWORD src1_sel:WORD_1
v_mfma_f32_16x16x32_bf16 v[220:223],v[92:95],v[152:155],v[220:223]
ds_read_u16 v132,v132
v_exp_f16 v120,v121
v_mfma_f32_16x16x32_bf16 v[228:231],v[96:99],v[152:155],v[228:231]
ds_read_b64_tr_b16 v[90:91],v3 offset:20016
ds_read_b64_tr_b16 v[92:93],v3 offset:18488
v_lshlrev_b32_sdwa v136,1,v136 dst_sel:DWORD dst_unused:UNUSED_PAD src0_sel:DWORD src1_sel:WORD_1
v_mfma_f32_16x16x32_bf16 v[236:239],v[100:103],v[152:155],v[236:239]
ds_read_u16_d16_hi v134,v134
v_exp_f16_sdwa v120,v121 dst_sel:WORD_1 dst_unused:UNUSED_PRESERVE src0_sel:WORD_1
v_mfma_f32_16x16x32_bf16 v[244:247],v[104:107],v[152:155],v[244:247]
ds_read_u16 v133,v133
v_lshlrev_b32_sdwa v138,1,v138 dst_sel:DWORD dst_unused:UNUSED_PAD src0_sel:DWORD src1_sel:WORD_1
v_mfma_f32_16x16x32_bf16 v[252:255],v[108:111],v[152:155],v[252:255]
ds_read_b64_tr_b16 v[94:95],v3 offset:20024
ds_read_b64_tr_b16 v[96:97],v3 offset:18544
v_pk_fma_f16 v120,v120,s55,s55
v_lshlrev_b32_sdwa v137,1,v137 dst_sel:DWORD dst_unused:UNUSED_PAD src0_sel:DWORD src1_sel:WORD_1
v_mfma_f32_16x16x32_bf16 v[144:147],v[64:67],v[40:43],v[112:115]
ds_read_u16_d16_hi v135,v135
v_rcp_f16 v121,v120
v_mfma_f32_16x16x32_bf16 v[152:155],v[64:67],v[44:47],v[112:115]
ds_read_b64_tr_b16 v[98:99],v3 offset:20080
ds_read_b64_tr_b16 v[100:101],v3 offset:18552
v_lshlrev_b32_sdwa v139,1,v139 dst_sel:DWORD dst_unused:UNUSED_PAD src0_sel:DWORD src1_sel:WORD_1
v_mfma_f32_16x16x32_bf16 v[156:159],v[68:71],v[44:47],v[116:119]
ds_read_u16 v136,v136
v_rcp_f16_sdwa v121,v120 dst_sel:WORD_1 dst_unused:UNUSED_PRESERVE src0_sel:WORD_1
v_mfma_f32_16x16x32_bf16 v[148:151],v[68:71],v[40:43],v[116:119]
ds_read_u16_d16_hi v138,v138
v_lshlrev_b32_sdwa v140,1,v140 dst_sel:DWORD dst_unused:UNUSED_PAD src0_sel:DWORD src1_sel:WORD_1
v_mfma_f32_16x16x32_bf16 v[144:147],v[72:75],v[48:51],v[144:147]
ds_read_b64_tr_b16 v[102:103],v3 offset:20088
ds_read_b64_tr_b16 v[104:105],v3 offset:18608
v_fma_mix_f32 v141,v141,v121,0 op_sel_hi:[0,1,0]
v_lshlrev_b32_sdwa v142,1,v142 dst_sel:DWORD dst_unused:UNUSED_PAD src0_sel:DWORD src1_sel:WORD_1
v_mfma_f32_16x16x32_bf16 v[152:155],v[72:75],v[52:55],v[152:155]
ds_read_u16 v137,v137
v_fma_mix_f32 v143,v143,v121,0 op_sel:[0,1,0] op_sel_hi:[0,1,0]
v_mfma_f32_16x16x32_bf16 v[156:159],v[76:79],v[52:55],v[156:159]
ds_read_b64_tr_b16 v[106:107],v3 offset:20144
ds_read_b64_tr_b16 v[108:109],v3 offset:18616
v_mfma_f32_16x16x32_bf16 v[148:151],v[76:79],v[48:51],v[148:151]
ds_read_u16_d16_hi v139,v139
v_mfma_f32_16x16x32_bf16 v[144:147],v[80:83],v[56:59],v[144:147]
ds_read_b64_tr_b16 v[110:111],v3 offset:20152
ds_read_b64_tr_b16 v[64:65],v1 offset:12832
v_mfma_f32_16x16x32_bf16 v[152:155],v[80:83],v[60:63],v[152:155]
s_waitcnt lgkmcnt(12)
ds_read_u16 v140,v140
v_lshl_add_u64 v[128:129],v[128:129],0,v[130:131]
v_lshl_add_u64 v[130:131],v[132:133],0,v[134:135]
v_mfma_f32_16x16x32_bf16 v[156:159],v[84:87],v[60:63],v[156:159]
ds_read_u16_d16_hi v142,v142
v_mfma_f32_16x16x32_bf16 v[148:151],v[84:87],v[56:59],v[148:151]
ds_read_b64_tr_b16 v[66:67],v1 offset:14880
ds_read_b128 v[112:115],v4 offset:640
v_lshlrev_b32_sdwa v144,1,v144 dst_sel:DWORD dst_unused:UNUSED_PAD src0_sel:DWORD src1_sel:WORD_1
v_lshlrev_b32_sdwa v146,1,v146 dst_sel:DWORD dst_unused:UNUSED_PAD src0_sel:DWORD src1_sel:WORD_1
v_mfma_f32_16x16x32_bf16 v[160:163],v[88:91],v[128:131],v[160:163]
ds_read_u16 v144,v144
v_lshlrev_b32_sdwa v145,1,v145 dst_sel:DWORD dst_unused:UNUSED_PAD src0_sel:DWORD src1_sel:WORD_1
v_lshlrev_b32_sdwa v147,1,v147 dst_sel:DWORD dst_unused:UNUSED_PAD src0_sel:DWORD src1_sel:WORD_1
v_mfma_f32_16x16x32_bf16 v[168:171],v[92:95],v[128:131],v[168:171]
s_waitcnt lgkmcnt(15)
ds_read_b64_tr_b16 v[68:69],v2 offset:12832
ds_read_b64_tr_b16 v[70:71],v2 offset:14880
v_fma_mixlo_f16 v120,v157,s99,0
v_lshlrev_b32_sdwa v148,1,v148 dst_sel:DWORD dst_unused:UNUSED_PAD src0_sel:DWORD src1_sel:WORD_1
v_mfma_f32_16x16x32_bf16 v[176:179],v[96:99],v[128:131],v[176:179]
s_waitcnt lgkmcnt(14)
ds_read_u16_d16_hi v146,v146
v_fma_mixhi_f16 v120,v159,s99,0
v_lshlrev_b32_sdwa v150,1,v150 dst_sel:DWORD dst_unused:UNUSED_PAD src0_sel:DWORD src1_sel:WORD_1
v_mfma_f32_16x16x32_bf16 v[184:187],v[100:103],v[128:131],v[184:187]
s_waitcnt lgkmcnt(10)
ds_read_u16 v145,v145
v_lshl_add_u64 v[136:137],v[136:137],0,v[138:139]
v_mfma_f32_16x16x32_bf16 v[192:195],v[104:107],v[128:131],v[192:195]
s_waitcnt lgkmcnt(7)
ds_read_b128 v[116:119],v4 offset:672
ds_read_b64_tr_b16 v[72:73],v1 offset:16928
v_or_b32 v138,v142,v140
v_cvt_pk_bf16_f32 v139,v141,v143
v_mfma_f32_16x16x32_bf16 v[200:203],v[108:111],v[128:131],v[200:203]
ds_read_u16_d16_hi v147,v147
v_pk_fma_f16 v121,v120,v120,s54
v_lshlrev_b32_sdwa v149,1,v149 dst_sel:DWORD dst_unused:UNUSED_PAD src0_sel:DWORD src1_sel:WORD_1
v_mfma_f32_16x16x32_bf16 v[164:167],v[88:91],v[136:139],v[164:167]
ds_read_b64_tr_b16 v[74:75],v1 offset:18976
ds_read_b64_tr_b16 v[76:77],v2 offset:16928
v_pk_mul_f16 v121,v120,v121
v_lshlrev_b32_sdwa v151,1,v151 dst_sel:DWORD dst_unused:UNUSED_PAD src0_sel:DWORD src1_sel:WORD_1
v_mfma_f32_16x16x32_bf16 v[172:175],v[92:95],v[136:139],v[172:175]
ds_read_u16 v148,v148
v_exp_f16 v120,v121
v_mfma_f32_16x16x32_bf16 v[180:183],v[96:99],v[136:139],v[180:183]
ds_read_b64_tr_b16 v[78:79],v2 offset:18976
ds_read_b64_tr_b16 v[80:81],v1 offset:21024
v_lshlrev_b32_sdwa v152,1,v152 dst_sel:DWORD dst_unused:UNUSED_PAD src0_sel:DWORD src1_sel:WORD_1
v_mfma_f32_16x16x32_bf16 v[188:191],v[100:103],v[136:139],v[188:191]
ds_read_u16_d16_hi v150,v150
v_exp_f16_sdwa v120,v121 dst_sel:WORD_1 dst_unused:UNUSED_PRESERVE src0_sel:WORD_1
v_mfma_f32_16x16x32_bf16 v[196:199],v[104:107],v[136:139],v[196:199]
ds_read_u16 v149,v149
v_lshlrev_b32_sdwa v154,1,v154 dst_sel:DWORD dst_unused:UNUSED_PAD src0_sel:DWORD src1_sel:WORD_1
v_mfma_f32_16x16x32_bf16 v[204:207],v[108:111],v[136:139],v[204:207]
s_waitcnt lgkmcnt(15)
ds_read_b64_tr_b16 v[82:83],v1 offset:23072
v_pk_fma_f16 v120,v120,s55,s55
v_lshlrev_b32_sdwa v153,1,v153 dst_sel:DWORD dst_unused:UNUSED_PAD src0_sel:DWORD src1_sel:WORD_1
v_mfma_f32_16x16x32_bf16 v[128:131],v[64:67],v[16:19],v[112:115]
ds_read_u16_d16_hi v151,v151
v_rcp_f16 v121,v120
v_mfma_f32_16x16x32_bf16 v[136:139],v[64:67],v[20:23],v[112:115]
s_waitcnt lgkmcnt(11)
ds_read_b64_tr_b16 v[84:85],v2 offset:21024
v_lshlrev_b32_sdwa v155,1,v155 dst_sel:DWORD dst_unused:UNUSED_PAD src0_sel:DWORD src1_sel:WORD_1
v_mfma_f32_16x16x32_bf16 v[140:143],v[68:71],v[20:23],v[116:119]
ds_read_u16 v152,v152
v_rcp_f16_sdwa v121,v120 dst_sel:WORD_1 dst_unused:UNUSED_PRESERVE src0_sel:WORD_1
v_mfma_f32_16x16x32_bf16 v[132:135],v[68:71],v[16:19],v[116:119]
s_waitcnt lgkmcnt(10)
ds_read_u16_d16_hi v154,v154
v_lshlrev_b32_sdwa v156,1,v156 dst_sel:DWORD dst_unused:UNUSED_PAD src0_sel:DWORD src1_sel:WORD_1
v_mfma_f32_16x16x32_bf16 v[128:131],v[72:75],v[24:27],v[128:131]
ds_read_b64_tr_b16 v[86:87],v2 offset:23072
v_fma_mix_f32 v157,v157,v121,0 op_sel_hi:[0,1,0]
v_lshlrev_b32_sdwa v158,1,v158 dst_sel:DWORD dst_unused:UNUSED_PAD src0_sel:DWORD src1_sel:WORD_1
v_mfma_f32_16x16x32_bf16 v[136:139],v[72:75],v[28:31],v[136:139]
s_waitcnt lgkmcnt(9)
ds_read_u16 v153,v153
v_fma_mix_f32 v159,v159,v121,0 op_sel:[0,1,0] op_sel_hi:[0,1,0]
v_mfma_f32_16x16x32_bf16 v[140:143],v[76:79],v[28:31],v[140:143]
v_mfma_f32_16x16x32_bf16 v[132:135],v[76:79],v[24:27],v[132:135]
s_waitcnt lgkmcnt(6)
ds_read_u16_d16_hi v155,v155
v_mfma_f32_16x16x32_bf16 v[128:131],v[80:83],v[32:35],v[128:131]
v_mfma_f32_16x16x32_bf16 v[136:139],v[80:83],v[36:39],v[136:139]
s_waitcnt lgkmcnt(2)
ds_read_u16 v156,v156
v_lshl_add_u64 v[144:145],v[144:145],0,v[146:147]
v_lshl_add_u64 v[146:147],v[148:149],0,v[150:151]
v_mfma_f32_16x16x32_bf16 v[140:143],v[84:87],v[36:39],v[140:143]
ds_read_u16_d16_hi v158,v158
v_mfma_f32_16x16x32_bf16 v[132:135],v[84:87],v[32:35],v[132:135]
v_lshlrev_b32_sdwa v128,1,v128 dst_sel:DWORD dst_unused:UNUSED_PAD src0_sel:DWORD src1_sel:WORD_1
v_lshlrev_b32_sdwa v130,1,v130 dst_sel:DWORD dst_unused:UNUSED_PAD src0_sel:DWORD src1_sel:WORD_1
v_mfma_f32_16x16x32_bf16 v[208:211],v[88:91],v[144:147],v[208:211]
s_barrier
s_setprio 3
ds_read_u16 v128,v128
v_lshlrev_b32_sdwa v129,1,v129 dst_sel:DWORD dst_unused:UNUSED_PAD src0_sel:DWORD src1_sel:WORD_1
v_lshlrev_b32_sdwa v131,1,v131 dst_sel:DWORD dst_unused:UNUSED_PAD src0_sel:DWORD src1_sel:WORD_1
v_mfma_f32_16x16x32_bf16 v[216:219],v[92:95],v[144:147],v[216:219]
s_cbranch_vccnz RA1_%=
s_mov_b64 exec,s[62:63]
s_add_u32 m0,s57,48832
s_add_u32 s64,s56,3342336
buffer_load_dwordx4 v5,s[36:39],s64 offen lds
s_add_u32 m0,s57,50560
s_nop 0
buffer_load_dwordx4 v6,s[36:39],s64 offen lds
s_mov_b64 exec,-1
RA1_%=:
v_fma_mixlo_f16 v120,v140,s99,0
v_fma_mixlo_f16 v122,v141,s99,0
v_mfma_f32_16x16x32_bf16 v[224:227],v[96:99],v[144:147],v[224:227]
ds_read_u16_d16_hi v130,v130
v_lshlrev_b32_sdwa v132,1,v132 dst_sel:DWORD dst_unused:UNUSED_PAD src0_sel:DWORD src1_sel:WORD_1
v_lshlrev_b32_sdwa v134,1,v134 dst_sel:DWORD dst_unused:UNUSED_PAD src0_sel:DWORD src1_sel:WORD_1
v_mfma_f32_16x16x32_bf16 v[232:235],v[100:103],v[144:147],v[232:235]
s_waitcnt lgkmcnt(4)
v_lshl_add_u64 v[152:153],v[152:153],0,v[154:155]
v_mfma_f32_16x16x32_bf16 v[240:243],v[104:107],v[144:147],v[240:243]
s_waitcnt lgkmcnt(2)
ds_read_u16 v129,v129
v_or_b32 v154,v158,v156
v_cvt_pk_bf16_f32 v155,v157,v159
v_mfma_f32_16x16x32_bf16 v[248:251],v[108:111],v[144:147],v[248:251]
v_fma_mixhi_f16 v120,v142,s99,0
v_fma_mixhi_f16 v122,v143,s99,0
v_mfma_f32_16x16x32_bf16 v[212:215],v[88:91],v[152:155],v[212:215]
ds_read_u16_d16_hi v131,v131
v_lshlrev_b32_sdwa v133,1,v133 dst_sel:DWORD dst_unused:UNUSED_PAD src0_sel:DWORD src1_sel:WORD_1
v_lshlrev_b32_sdwa v135,1,v135 dst_sel:DWORD dst_unused:UNUSED_PAD src0_sel:DWORD src1_sel:WORD_1
v_mfma_f32_16x16x32_bf16 v[220:223],v[92:95],v[152:155],v[220:223]
ds_read_b64_tr_b16 v[88:89],v3 offset:0
v_pk_fma_f16 v121,v120,v120,s54
v_pk_fma_f16 v123,v122,v122,s54
v_mfma_f32_16x16x32_bf16 v[228:231],v[96:99],v[152:155],v[228:231]
s_cbranch_vccnz RA2_%=
s_mov_b64 exec,s[62:63]
s_add_u32 m0,s57,52320
s_add_u32 s64,s56,3440640
buffer_load_dwordx4 v5,s[36:39],s64 offen lds
s_add_u32 m0,s57,54048
s_nop 0
buffer_load_dwordx4 v6,s[36:39],s64 offen lds
s_mov_b64 exec,-1
RA2_%=:
ds_read_u16 v132,v132
v_pk_mul_f16 v121,v120,v121
v_pk_mul_f16 v123,v122,v123
v_mfma_f32_16x16x32_bf16 v[236:239],v[100:103],v[152:155],v[236:239]
ds_read_b64_tr_b16 v[90:91],v3 offset:1536
v_lshlrev_b32_sdwa v136,1,v136 dst_sel:DWORD dst_unused:UNUSED_PAD src0_sel:DWORD src1_sel:WORD_1
v_lshlrev_b32_sdwa v138,1,v138 dst_sel:DWORD dst_unused:UNUSED_PAD src0_sel:DWORD src1_sel:WORD_1
v_mfma_f32_16x16x32_bf16 v[244:247],v[104:107],v[152:155],v[244:247]
ds_read_u16_d16_hi v134,v134
v_exp_f16 v120,v121
v_mfma_f32_16x16x32_bf16 v[252:255],v[108:111],v[152:155],v[252:255]
ds_read_b64_tr_b16 v[92:93],v3 offset:8
v_exp_f16 v122,v123
v_mfma_f32_16x16x32_bf16 v[144:147],v[64:67],v[40:43],v[112:115]
ds_read_u16 v133,v133
v_lshlrev_b32_sdwa v137,1,v137 dst_sel:DWORD dst_unused:UNUSED_PAD src0_sel:DWORD src1_sel:WORD_1
v_lshlrev_b32_sdwa v139,1,v139 dst_sel:DWORD dst_unused:UNUSED_PAD src0_sel:DWORD src1_sel:WORD_1
v_mfma_f32_16x16x32_bf16 v[152:155],v[64:67],v[44:47],v[112:115]
ds_read_b64_tr_b16 v[94:95],v3 offset:1544
v_exp_f16_sdwa v120,v121 dst_sel:WORD_1 dst_unused:UNUSED_PRESERVE src0_sel:WORD_1
v_mfma_f32_16x16x32_bf16 v[156:159],v[68:71],v[44:47],v[116:119]
ds_read_u16_d16_hi v135,v135
v_exp_f16_sdwa v122,v123 dst_sel:WORD_1 dst_unused:UNUSED_PRESERVE src0_sel:WORD_1
v_mfma_f32_16x16x32_bf16 v[148:151],v[68:71],v[40:43],v[116:119]
ds_read_b64_tr_b16 v[96:97],v3 offset:64
v_pk_fma_f16 v120,v120,s55,s55
v_pk_fma_f16 v122,v122,s55,s55
v_mfma_f32_16x16x32_bf16 v[144:147],v[72:75],v[48:51],v[144:147]
ds_read_u16 v136,v136
v_rcp_f16 v121,v120
v_mfma_f32_16x16x32_bf16 v[152:155],v[72:75],v[52:55],v[152:155]
ds_read_b64_tr_b16 v[98:99],v3 offset:1600
ds_read_b64_tr_b16 v[100:101],v3 offset:72
v_rcp_f16 v123,v122
v_mfma_f32_16x16x32_bf16 v[156:159],v[76:79],v[52:55],v[156:159]
ds_read_u16_d16_hi v138,v138
v_rcp_f16_sdwa v121,v120 dst_sel:WORD_1 dst_unused:UNUSED_PRESERVE src0_sel:WORD_1
v_mfma_f32_16x16x32_bf16 v[148:151],v[76:79],v[48:51],v[148:151]
ds_read_b64_tr_b16 v[102:103],v3 offset:1608
ds_read_b64_tr_b16 v[104:105],v3 offset:128
v_rcp_f16_sdwa v123,v122 dst_sel:WORD_1 dst_unused:UNUSED_PRESERVE src0_sel:WORD_1
v_mfma_f32_16x16x32_bf16 v[144:147],v[80:83],v[56:59],v[144:147]
ds_read_u16 v137,v137
v_fma_mix_f32 v140,v140,v121,0 op_sel_hi:[0,1,0]
v_fma_mix_f32 v142,v142,v121,0 op_sel:[0,1,0] op_sel_hi:[0,1,0]
v_mfma_f32_16x16x32_bf16 v[152:155],v[80:83],v[60:63],v[152:155]
s_waitcnt lgkmcnt(8)
ds_read_b64_tr_b16 v[106:107],v3 offset:1664
ds_read_b64_tr_b16 v[108:109],v3 offset:136
v_lshl_add_u64 v[128:129],v[128:129],0,v[130:131]
v_lshl_add_u64 v[130:131],v[132:133],0,v[134:135]
v_mfma_f32_16x16x32_bf16 v[156:159],v[84:87],v[60:63],v[156:159]
ds_read_u16_d16_hi v139,v139
v_fma_mix_f32 v141,v141,v123,0 op_sel_hi:[0,1,0]
v_fma_mix_f32 v143,v143,v123,0 op_sel:[0,1,0] op_sel_hi:[0,1,0]
v_mfma_f32_16x16x32_bf16 v[148:151],v[84:87],v[56:59],v[148:151]
ds_read_b64_tr_b16 v[110:111],v3 offset:1672
ds_read_b64_tr_b16 v[64:65],v1 offset:0
v_lshlrev_b32_sdwa v144,1,v144 dst_sel:DWORD dst_unused:UNUSED_PAD src0_sel:DWORD src1_sel:WORD_1
v_lshlrev_b32_sdwa v146,1,v146 dst_sel:DWORD dst_unused:UNUSED_PAD src0_sel:DWORD src1_sel:WORD_1
v_mfma_f32_16x16x32_bf16 v[160:163],v[88:91],v[128:131],v[160:163]
ds_read_u16 v144,v144
v_lshlrev_b32_sdwa v145,1,v145 dst_sel:DWORD dst_unused:UNUSED_PAD src0_sel:DWORD src1_sel:WORD_1
v_lshlrev_b32_sdwa v147,1,v147 dst_sel:DWORD dst_unused:UNUSED_PAD src0_sel:DWORD src1_sel:WORD_1
v_mfma_f32_16x16x32_bf16 v[168:171],v[92:95],v[128:131],v[168:171]
s_waitcnt lgkmcnt(11)
ds_read_b64_tr_b16 v[66:67],v1 offset:2048
ds_read_b128 v[112:115],v4 offset:768
v_fma_mixlo_f16 v120,v156,s99,0
v_fma_mixlo_f16 v122,v157,s99,0
v_mfma_f32_16x16x32_bf16 v[176:179],v[96:99],v[128:131],v[176:179]
s_waitcnt lgkmcnt(10)
ds_read_u16_d16_hi v146,v146
v_lshlrev_b32_sdwa v148,1,v148 dst_sel:DWORD dst_unused:UNUSED_PAD src0_sel:DWORD src1_sel:WORD_1
v_lshlrev_b32_sdwa v150,1,v150 dst_sel:DWORD dst_unused:UNUSED_PAD src0_sel:DWORD src1_sel:WORD_1
v_mfma_f32_16x16x32_bf16 v[184:187],v[100:103],v[128:131],v[184:187]
s_waitcnt lgkmcnt(6)
ds_read_b64_tr_b16 v[68:69],v2 offset:0
ds_read_b64_tr_b16 v[70:71],v2 offset:2048
v_lshl_add_u64 v[136:137],v[136:137],0,v[138:139]
v_mfma_f32_16x16x32_bf16 v[192:195],v[104:107],v[128:131],v[192:195]
s_waitcnt lgkmcnt(7)
ds_read_u16 v145,v145
v_cvt_pk_bf16_f32 v138,v140,v142
v_cvt_pk_bf16_f32 v139,v141,v143
v_mfma_f32_16x16x32_bf16 v[200:203],v[108:111],v[128:131],v[200:203]
ds_read_b128 v[116:119],v4 offset:800
ds_read_b64_tr_b16 v[72:73],v1 offset:4096
v_fma_mixhi_f16 v120,v158,s99,0
v_fma_mixhi_f16 v122,v159,s99,0
v_mfma_f32_16x16x32_bf16 v[164:167],v[88:91],v[136:139],v[164:167]
ds_read_u16_d16_hi v147,v147
v_lshlrev_b32_sdwa v149,1,v149 dst_sel:DWORD dst_unused:UNUSED_PAD src0_sel:DWORD src1_sel:WORD_1
v_lshlrev_b32_sdwa v151,1,v151 dst_sel:DWORD dst_unused:UNUSED_PAD src0_sel:DWORD src1_sel:WORD_1
v_mfma_f32_16x16x32_bf16 v[172:175],v[92:95],v[136:139],v[172:175]
ds_read_b64_tr_b16 v[74:75],v1 offset:6144
ds_read_b64_tr_b16 v[76:77],v2 offset:4096
v_pk_fma_f16 v121,v120,v120,s54
v_pk_fma_f16 v123,v122,v122,s54
v_mfma_f32_16x16x32_bf16 v[180:183],v[96:99],v[136:139],v[180:183]
ds_read_u16 v148,v148
v_pk_mul_f16 v121,v120,v121
v_pk_mul_f16 v123,v122,v123
v_mfma_f32_16x16x32_bf16 v[188:191],v[100:103],v[136:139],v[188:191]
s_nop 0
s_mov_b32 s86,0xbe801d5a
ds_read_b64_tr_b16 v[78:79],v2 offset:6144
v_lshlrev_b32_sdwa v152,1,v152 dst_sel:DWORD dst_unused:UNUSED_PAD src0_sel:DWORD src1_sel:WORD_1
v_lshlrev_b32_sdwa v154,1,v154 dst_sel:DWORD dst_unused:UNUSED_PAD src0_sel:DWORD src1_sel:WORD_1
v_mfma_f32_16x16x32_bf16 v[196:199],v[104:107],v[136:139],v[196:199]
ds_read_u16_d16_hi v150,v150
v_exp_f16 v120,v121
v_mfma_f32_16x16x32_bf16 v[204:207],v[108:111],v[136:139],v[204:207]
s_waitcnt lgkmcnt(12)
ds_read_b64_tr_b16 v[80:81],v1 offset:8192
v_exp_f16 v122,v123
v_mfma_f32_16x16x32_bf16 v[128:131],v[64:67],v[16:19],v[112:115]
ds_read_u16 v149,v149
v_lshlrev_b32_sdwa v153,1,v153 dst_sel:DWORD dst_unused:UNUSED_PAD src0_sel:DWORD src1_sel:WORD_1
v_lshlrev_b32_sdwa v155,1,v155 dst_sel:DWORD dst_unused:UNUSED_PAD src0_sel:DWORD src1_sel:WORD_1
v_mfma_f32_16x16x32_bf16 v[136:139],v[64:67],v[20:23],v[112:115]
s_waitcnt lgkmcnt(9)
ds_read_b64_tr_b16 v[82:83],v1 offset:10240
v_exp_f16_sdwa v120,v121 dst_sel:WORD_1 dst_unused:UNUSED_PRESERVE src0_sel:WORD_1
v_mfma_f32_16x16x32_bf16 v[140:143],v[68:71],v[20:23],v[116:119]
ds_read_u16_d16_hi v151,v151
v_exp_f16_sdwa v122,v123 dst_sel:WORD_1 dst_unused:UNUSED_PRESERVE src0_sel:WORD_1
v_mfma_f32_16x16x32_bf16 v[132:135],v[68:71],v[16:19],v[116:119]
s_waitcnt lgkmcnt(8)
ds_read_b64_tr_b16 v[84:85],v2 offset:8192
v_pk_fma_f16 v120,v120,s55,s55
v_pk_fma_f16 v122,v122,s55,s55
v_mfma_f32_16x16x32_bf16 v[128:131],v[72:75],v[24:27],v[128:131]
ds_read_u16 v152,v152
v_rcp_f16 v121,v120
v_mfma_f32_16x16x32_bf16 v[136:139],v[72:75],v[28:31],v[136:139]
s_waitcnt lgkmcnt(7)
ds_read_b64_tr_b16 v[86:87],v2 offset:10240
v_rcp_f16 v123,v122
v_mfma_f32_16x16x32_bf16 v[140:143],v[76:79],v[28:31],v[140:143]
ds_read_u16_d16_hi v154,v154
v_rcp_f16_sdwa v121,v120 dst_sel:WORD_1 dst_unused:UNUSED_PRESERVE src0_sel:WORD_1
v_mfma_f32_16x16x32_bf16 v[132:135],v[76:79],v[24:27],v[132:135]
s_waitcnt lgkmcnt(5)
v_rcp_f16_sdwa v123,v122 dst_sel:WORD_1 dst_unused:UNUSED_PRESERVE src0_sel:WORD_1
v_mfma_f32_16x16x32_bf16 v[128:131],v[80:83],v[32:35],v[128:131]
ds_read_u16 v153,v153
v_fma_mix_f32 v156,v156,v121,0 op_sel_hi:[0,1,0]
v_fma_mix_f32 v158,v158,v121,0 op_sel:[0,1,0] op_sel_hi:[0,1,0]
v_mfma_f32_16x16x32_bf16 v[136:139],v[80:83],v[36:39],v[136:139]
s_waitcnt lgkmcnt(2)
v_lshl_add_u64 v[144:145],v[144:145],0,v[146:147]
v_lshl_add_u64 v[146:147],v[148:149],0,v[150:151]
v_mfma_f32_16x16x32_bf16 v[140:143],v[84:87],v[36:39],v[140:143]
ds_read_u16_d16_hi v155,v155
v_fma_mix_f32 v157,v157,v123,0 op_sel_hi:[0,1,0]
v_fma_mix_f32 v159,v159,v123,0 op_sel:[0,1,0] op_sel_hi:[0,1,0]
v_mfma_f32_16x16x32_bf16 v[132:135],v[84:87],v[32:35],v[132:135]
v_lshlrev_b32_sdwa v128,1,v128 dst_sel:DWORD dst_unused:UNUSED_PAD src0_sel:DWORD src1_sel:WORD_1
v_lshlrev_b32_sdwa v130,1,v130 dst_sel:DWORD dst_unused:UNUSED_PAD src0_sel:DWORD src1_sel:WORD_1
v_mfma_f32_16x16x32_bf16 v[208:211],v[88:91],v[144:147],v[208:211]
ds_read_u16 v128,v128
v_lshlrev_b32_sdwa v129,1,v129 dst_sel:DWORD dst_unused:UNUSED_PAD src0_sel:DWORD src1_sel:WORD_1
v_lshlrev_b32_sdwa v131,1,v131 dst_sel:DWORD dst_unused:UNUSED_PAD src0_sel:DWORD src1_sel:WORD_1
v_mfma_f32_16x16x32_bf16 v[216:219],v[92:95],v[144:147],v[216:219]
v_fma_mixlo_f16 v120,v140,s99,0
v_fma_mixlo_f16 v122,v141,s99,0
v_mfma_f32_16x16x32_bf16 v[224:227],v[96:99],v[144:147],v[224:227]
ds_read_u16_d16_hi v130,v130
v_lshlrev_b32_sdwa v132,1,v132 dst_sel:DWORD dst_unused:UNUSED_PAD src0_sel:DWORD src1_sel:WORD_1
v_lshlrev_b32_sdwa v134,1,v134 dst_sel:DWORD dst_unused:UNUSED_PAD src0_sel:DWORD src1_sel:WORD_1
v_mfma_f32_16x16x32_bf16 v[232:235],v[100:103],v[144:147],v[232:235]
s_waitcnt lgkmcnt(2)
v_lshl_add_u64 v[152:153],v[152:153],0,v[154:155]
v_mfma_f32_16x16x32_bf16 v[240:243],v[104:107],v[144:147],v[240:243]
ds_read_u16 v129,v129
v_cvt_pk_bf16_f32 v154,v156,v158
v_cvt_pk_bf16_f32 v155,v157,v159
v_mfma_f32_16x16x32_bf16 v[248:251],v[108:111],v[144:147],v[248:251]
s_setprio 2
v_fma_mixhi_f16 v120,v142,s99,0
v_fma_mixhi_f16 v122,v143,s99,0
v_mfma_f32_16x16x32_bf16 v[212:215],v[88:91],v[152:155],v[212:215]
ds_read_u16_d16_hi v131,v131
v_lshlrev_b32_sdwa v133,1,v133 dst_sel:DWORD dst_unused:UNUSED_PAD src0_sel:DWORD src1_sel:WORD_1
v_lshlrev_b32_sdwa v135,1,v135 dst_sel:DWORD dst_unused:UNUSED_PAD src0_sel:DWORD src1_sel:WORD_1
v_mfma_f32_16x16x32_bf16 v[220:223],v[92:95],v[152:155],v[220:223]
ds_read_b64_tr_b16 v[88:89],v3 offset:6160
v_pk_fma_f16 v121,v120,v120,s54
v_pk_fma_f16 v123,v122,v122,s54
v_mfma_f32_16x16x32_bf16 v[228:231],v[96:99],v[152:155],v[228:231]
ds_read_u16 v132,v132
v_pk_mul_f16 v121,v120,v121
v_pk_mul_f16 v123,v122,v123
v_mfma_f32_16x16x32_bf16 v[236:239],v[100:103],v[152:155],v[236:239]
ds_read_b64_tr_b16 v[90:91],v3 offset:7696
v_lshlrev_b32_sdwa v136,1,v136 dst_sel:DWORD dst_unused:UNUSED_PAD src0_sel:DWORD src1_sel:WORD_1
v_lshlrev_b32_sdwa v138,1,v138 dst_sel:DWORD dst_unused:UNUSED_PAD src0_sel:DWORD src1_sel:WORD_1
v_mfma_f32_16x16x32_bf16 v[244:247],v[104:107],v[152:155],v[244:247]
ds_read_u16_d16_hi v134,v134
v_exp_f16 v120,v121
v_mfma_f32_16x16x32_bf16 v[252:255],v[108:111],v[152:155],v[252:255]
ds_read_b64_tr_b16 v[92:93],v3 offset:6168
v_exp_f16 v122,v123
v_mfma_f32_16x16x32_bf16 v[144:147],v[64:67],v[40:43],v[112:115]
ds_read_u16 v133,v133
v_lshlrev_b32_sdwa v137,1,v137 dst_sel:DWORD dst_unused:UNUSED_PAD src0_sel:DWORD src1_sel:WORD_1
v_lshlrev_b32_sdwa v139,1,v139 dst_sel:DWORD dst_unused:UNUSED_PAD src0_sel:DWORD src1_sel:WORD_1
v_mfma_f32_16x16x32_bf16 v[152:155],v[64:67],v[44:47],v[112:115]
ds_read_b64_tr_b16 v[94:95],v3 offset:7704
v_exp_f16_sdwa v120,v121 dst_sel:WORD_1 dst_unused:UNUSED_PRESERVE src0_sel:WORD_1
v_mfma_f32_16x16x32_bf16 v[156:159],v[68:71],v[44:47],v[116:119]
ds_read_u16_d16_hi v135,v135
v_exp_f16_sdwa v122,v123 dst_sel:WORD_1 dst_unused:UNUSED_PRESERVE src0_sel:WORD_1
v_mfma_f32_16x16x32_bf16 v[148:151],v[68:71],v[40:43],v[116:119]
ds_read_b64_tr_b16 v[96:97],v3 offset:6224
v_pk_fma_f16 v120,v120,s55,s55
v_pk_fma_f16 v122,v122,s55,s55
v_mfma_f32_16x16x32_bf16 v[144:147],v[72:75],v[48:51],v[144:147]
ds_read_u16 v136,v136
v_rcp_f16 v121,v120
v_mfma_f32_16x16x32_bf16 v[152:155],v[72:75],v[52:55],v[152:155]
ds_read_b64_tr_b16 v[98:99],v3 offset:7760
ds_read_b64_tr_b16 v[100:101],v3 offset:6232
v_rcp_f16 v123,v122
v_mfma_f32_16x16x32_bf16 v[156:159],v[76:79],v[52:55],v[156:159]
ds_read_u16_d16_hi v138,v138
v_rcp_f16_sdwa v121,v120 dst_sel:WORD_1 dst_unused:UNUSED_PRESERVE src0_sel:WORD_1
v_mfma_f32_16x16x32_bf16 v[148:151],v[76:79],v[48:51],v[148:151]
ds_read_b64_tr_b16 v[102:103],v3 offset:7768
ds_read_b64_tr_b16 v[104:105],v3 offset:6288
v_rcp_f16_sdwa v123,v122 dst_sel:WORD_1 dst_unused:UNUSED_PRESERVE src0_sel:WORD_1
v_mfma_f32_16x16x32_bf16 v[144:147],v[80:83],v[56:59],v[144:147]
ds_read_u16 v137,v137
v_fma_mix_f32 v140,v140,v121,0 op_sel_hi:[0,1,0]
v_fma_mix_f32 v142,v142,v121,0 op_sel:[0,1,0] op_sel_hi:[0,1,0]
v_mfma_f32_16x16x32_bf16 v[152:155],v[80:83],v[60:63],v[152:155]
s_waitcnt lgkmcnt(8)
ds_read_b64_tr_b16 v[106:107],v3 offset:7824
ds_read_b64_tr_b16 v[108:109],v3 offset:6296
v_lshl_add_u64 v[128:129],v[128:129],0,v[130:131]
v_lshl_add_u64 v[130:131],v[132:133],0,v[134:135]
v_mfma_f32_16x16x32_bf16 v[156:159],v[84:87],v[60:63],v[156:159]
ds_read_u16_d16_hi v139,v139
v_fma_mix_f32 v141,v141,v123,0 op_sel_hi:[0,1,0]
v_fma_mix_f32 v143,v143,v123,0 op_sel:[0,1,0] op_sel_hi:[0,1,0]
v_mfma_f32_16x16x32_bf16 v[148:151],v[84:87],v[56:59],v[148:151]
ds_read_b64_tr_b16 v[110:111],v3 offset:7832
ds_read_b64_tr_b16 v[64:65],v1 offset:512
v_lshlrev_b32_sdwa v144,1,v144 dst_sel:DWORD dst_unused:UNUSED_PAD src0_sel:DWORD src1_sel:WORD_1
v_lshlrev_b32_sdwa v146,1,v146 dst_sel:DWORD dst_unused:UNUSED_PAD src0_sel:DWORD src1_sel:WORD_1
v_mfma_f32_16x16x32_bf16 v[160:163],v[88:91],v[128:131],v[160:163]
ds_read_u16 v144,v144
v_lshlrev_b32_sdwa v145,1,v145 dst_sel:DWORD dst_unused:UNUSED_PAD src0_sel:DWORD src1_sel:WORD_1
v_lshlrev_b32_sdwa v147,1,v147 dst_sel:DWORD dst_unused:UNUSED_PAD src0_sel:DWORD src1_sel:WORD_1
v_mfma_f32_16x16x32_bf16 v[168:171],v[92:95],v[128:131],v[168:171]
s_waitcnt lgkmcnt(11)
ds_read_b64_tr_b16 v[66:67],v1 offset:2560
ds_read_b128 v[112:115],v4 offset:896
v_fma_mixlo_f16 v120,v156,s99,0
v_fma_mixlo_f16 v122,v157,s99,0
v_mfma_f32_16x16x32_bf16 v[176:179],v[96:99],v[128:131],v[176:179]
s_waitcnt lgkmcnt(10)
ds_read_u16_d16_hi v146,v146
v_lshlrev_b32_sdwa v148,1,v148 dst_sel:DWORD dst_unused:UNUSED_PAD src0_sel:DWORD src1_sel:WORD_1
v_lshlrev_b32_sdwa v150,1,v150 dst_sel:DWORD dst_unused:UNUSED_PAD src0_sel:DWORD src1_sel:WORD_1
v_mfma_f32_16x16x32_bf16 v[184:187],v[100:103],v[128:131],v[184:187]
s_waitcnt lgkmcnt(6)
ds_read_b64_tr_b16 v[68:69],v2 offset:512
ds_read_b64_tr_b16 v[70:71],v2 offset:2560
v_lshl_add_u64 v[136:137],v[136:137],0,v[138:139]
v_mfma_f32_16x16x32_bf16 v[192:195],v[104:107],v[128:131],v[192:195]
s_waitcnt lgkmcnt(7)
ds_read_u16 v145,v145
v_cvt_pk_bf16_f32 v138,v140,v142
v_cvt_pk_bf16_f32 v139,v141,v143
v_mfma_f32_16x16x32_bf16 v[200:203],v[108:111],v[128:131],v[200:203]
ds_read_b128 v[116:119],v4 offset:928
ds_read_b64_tr_b16 v[72:73],v1 offset:4608
v_fma_mixhi_f16 v120,v158,s99,0
v_fma_mixhi_f16 v122,v159,s99,0
v_mfma_f32_16x16x32_bf16 v[164:167],v[88:91],v[136:139],v[164:167]
ds_read_u16_d16_hi v147,v147
v_lshlrev_b32_sdwa v149,1,v149 dst_sel:DWORD dst_unused:UNUSED_PAD src0_sel:DWORD src1_sel:WORD_1
v_lshlrev_b32_sdwa v151,1,v151 dst_sel:DWORD dst_unused:UNUSED_PAD src0_sel:DWORD src1_sel:WORD_1
v_mfma_f32_16x16x32_bf16 v[172:175],v[92:95],v[136:139],v[172:175]
s_setprio 1
ds_read_b64_tr_b16 v[74:75],v1 offset:6656
ds_read_b64_tr_b16 v[76:77],v2 offset:4608
v_pk_fma_f16 v121,v120,v120,s54
v_pk_fma_f16 v123,v122,v122,s54
v_mfma_f32_16x16x32_bf16 v[180:183],v[96:99],v[136:139],v[180:183]
ds_read_u16 v148,v148
v_pk_mul_f16 v121,v120,v121
v_pk_mul_f16 v123,v122,v123
v_mfma_f32_16x16x32_bf16 v[188:191],v[100:103],v[136:139],v[188:191]
ds_read_b64_tr_b16 v[78:79],v2 offset:6656
v_lshlrev_b32_sdwa v152,1,v152 dst_sel:DWORD dst_unused:UNUSED_PAD src0_sel:DWORD src1_sel:WORD_1
v_lshlrev_b32_sdwa v154,1,v154 dst_sel:DWORD dst_unused:UNUSED_PAD src0_sel:DWORD src1_sel:WORD_1
v_mfma_f32_16x16x32_bf16 v[196:199],v[104:107],v[136:139],v[196:199]
ds_read_u16_d16_hi v150,v150
v_exp_f16 v120,v121
v_mfma_f32_16x16x32_bf16 v[204:207],v[108:111],v[136:139],v[204:207]
s_waitcnt lgkmcnt(12)
ds_read_b64_tr_b16 v[80:81],v1 offset:8704
v_exp_f16 v122,v123
v_mfma_f32_16x16x32_bf16 v[128:131],v[64:67],v[16:19],v[112:115]
ds_read_u16 v149,v149
v_lshlrev_b32_sdwa v153,1,v153 dst_sel:DWORD dst_unused:UNUSED_PAD src0_sel:DWORD src1_sel:WORD_1
v_lshlrev_b32_sdwa v155,1,v155 dst_sel:DWORD dst_unused:UNUSED_PAD src0_sel:DWORD src1_sel:WORD_1
v_mfma_f32_16x16x32_bf16 v[136:139],v[64:67],v[20:23],v[112:115]
s_waitcnt lgkmcnt(9)
ds_read_b64_tr_b16 v[82:83],v1 offset:10752
v_exp_f16_sdwa v120,v121 dst_sel:WORD_1 dst_unused:UNUSED_PRESERVE src0_sel:WORD_1
v_mfma_f32_16x16x32_bf16 v[140:143],v[68:71],v[20:23],v[116:119]
ds_read_u16_d16_hi v151,v151
v_exp_f16_sdwa v122,v123 dst_sel:WORD_1 dst_unused:UNUSED_PRESERVE src0_sel:WORD_1
v_mfma_f32_16x16x32_bf16 v[132:135],v[68:71],v[16:19],v[116:119]
s_waitcnt lgkmcnt(8)
ds_read_b64_tr_b16 v[84:85],v2 offset:8704
v_pk_fma_f16 v120,v120,s55,s55
v_pk_fma_f16 v122,v122,s55,s55
v_mfma_f32_16x16x32_bf16 v[128:131],v[72:75],v[24:27],v[128:131]
ds_read_u16 v152,v152
v_rcp_f16 v121,v120
v_mfma_f32_16x16x32_bf16 v[136:139],v[72:75],v[28:31],v[136:139]
s_waitcnt lgkmcnt(7)
ds_read_b64_tr_b16 v[86:87],v2 offset:10752
v_rcp_f16 v123,v122
v_mfma_f32_16x16x32_bf16 v[140:143],v[76:79],v[28:31],v[140:143]
ds_read_u16_d16_hi v154,v154
v_rcp_f16_sdwa v121,v120 dst_sel:WORD_1 dst_unused:UNUSED_PRESERVE src0_sel:WORD_1
v_mfma_f32_16x16x32_bf16 v[132:135],v[76:79],v[24:27],v[132:135]
s_waitcnt lgkmcnt(5)
v_rcp_f16_sdwa v123,v122 dst_sel:WORD_1 dst_unused:UNUSED_PRESERVE src0_sel:WORD_1
v_mfma_f32_16x16x32_bf16 v[128:131],v[80:83],v[32:35],v[128:131]
ds_read_u16 v153,v153
v_fma_mix_f32 v156,v156,v121,0 op_sel_hi:[0,1,0]
v_fma_mix_f32 v158,v158,v121,0 op_sel:[0,1,0] op_sel_hi:[0,1,0]
v_mfma_f32_16x16x32_bf16 v[136:139],v[80:83],v[36:39],v[136:139]
s_waitcnt lgkmcnt(2)
s_setprio 0
v_lshl_add_u64 v[144:145],v[144:145],0,v[146:147]
v_lshl_add_u64 v[146:147],v[148:149],0,v[150:151]
v_mfma_f32_16x16x32_bf16 v[140:143],v[84:87],v[36:39],v[140:143]
ds_read_u16_d16_hi v155,v155
v_fma_mix_f32 v157,v157,v123,0 op_sel_hi:[0,1,0]
v_fma_mix_f32 v159,v159,v123,0 op_sel:[0,1,0] op_sel_hi:[0,1,0]
v_mfma_f32_16x16x32_bf16 v[132:135],v[84:87],v[32:35],v[132:135]
v_lshlrev_b32_sdwa v128,1,v128 dst_sel:DWORD dst_unused:UNUSED_PAD src0_sel:DWORD src1_sel:WORD_1
v_lshlrev_b32_sdwa v130,1,v130 dst_sel:DWORD dst_unused:UNUSED_PAD src0_sel:DWORD src1_sel:WORD_1
v_mfma_f32_16x16x32_bf16 v[208:211],v[88:91],v[144:147],v[208:211]
s_barrier
s_setprio 2
ds_read_u16 v128,v128
v_lshlrev_b32_sdwa v129,1,v129 dst_sel:DWORD dst_unused:UNUSED_PAD src0_sel:DWORD src1_sel:WORD_1
v_mfma_f32_16x16x32_bf16 v[216:219],v[92:95],v[144:147],v[216:219]
s_cbranch_vccnz RA3_%=
s_mov_b64 exec,s[62:63]
s_add_u32 m0,s57,55808
s_add_u32 s64,s56,3538944
buffer_load_dwordx4 v5,s[36:39],s64 offen lds
s_add_u32 m0,s57,57536
s_nop 0
buffer_load_dwordx4 v6,s[36:39],s64 offen lds
s_mov_b64 exec,-1
RA3_%=:
ds_read_u16_d16_hi v130,v130
v_lshlrev_b32_sdwa v131,1,v131 dst_sel:DWORD dst_unused:UNUSED_PAD src0_sel:DWORD src1_sel:WORD_1
v_mfma_f32_16x16x32_bf16 v[224:227],v[96:99],v[144:147],v[224:227]
v_lshlrev_b32_sdwa v132,1,v132 dst_sel:DWORD dst_unused:UNUSED_PAD src0_sel:DWORD src1_sel:WORD_1
v_mfma_f32_16x16x32_bf16 v[232:235],v[100:103],v[144:147],v[232:235]
s_waitcnt lgkmcnt(2)
ds_read_u16 v129,v129
v_lshl_add_u64 v[152:153],v[152:153],0,v[154:155]
v_lshlrev_b32_sdwa v134,1,v134 dst_sel:DWORD dst_unused:UNUSED_PAD src0_sel:DWORD src1_sel:WORD_1
v_mfma_f32_16x16x32_bf16 v[240:243],v[104:107],v[144:147],v[240:243]
ds_read_u16_d16_hi v131,v131
v_cvt_pk_bf16_f32 v154,v156,v158
v_cvt_pk_bf16_f32 v155,v157,v159
v_mfma_f32_16x16x32_bf16 v[248:251],v[108:111],v[144:147],v[248:251]
v_lshlrev_b32_sdwa v133,1,v133 dst_sel:DWORD dst_unused:UNUSED_PAD src0_sel:DWORD src1_sel:WORD_1
v_mfma_f32_16x16x32_bf16 v[212:215],v[88:91],v[152:155],v[212:215]
ds_read_u16 v132,v132
v_lshlrev_b32_sdwa v135,1,v135 dst_sel:DWORD dst_unused:UNUSED_PAD src0_sel:DWORD src1_sel:WORD_1
v_mfma_f32_16x16x32_bf16 v[220:223],v[92:95],v[152:155],v[220:223]
ds_read_u16_d16_hi v134,v134
v_lshlrev_b32_sdwa v136,1,v136 dst_sel:DWORD dst_unused:UNUSED_PAD src0_sel:DWORD src1_sel:WORD_1
v_mfma_f32_16x16x32_bf16 v[228:231],v[96:99],v[152:155],v[228:231]
ds_read_b64_tr_b16 v[88:89],v3 offset:12320
ds_read_b64_tr_b16 v[90:91],v3 offset:13856
v_lshlrev_b32_sdwa v138,1,v138 dst_sel:DWORD dst_unused:UNUSED_PAD src0_sel:DWORD src1_sel:WORD_1
v_mfma_f32_16x16x32_bf16 v[236:239],v[100:103],v[152:155],v[236:239]
ds_read_u16 v133,v133
v_lshlrev_b32_sdwa v137,1,v137 dst_sel:DWORD dst_unused:UNUSED_PAD src0_sel:DWORD src1_sel:WORD_1
v_mfma_f32_16x16x32_bf16 v[244:247],v[104:107],v[152:155],v[244:247]
ds_read_u16_d16_hi v135,v135
v_lshlrev_b32_sdwa v139,1,v139 dst_sel:DWORD dst_unused:UNUSED_PAD src0_sel:DWORD src1_sel:WORD_1
v_mfma_f32_16x16x32_bf16 v[252:255],v[108:111],v[152:155],v[252:255]
ds_read_b64_tr_b16 v[92:93],v3 offset:12328
ds_read_b64_tr_b16 v[94:95],v3 offset:13864
v_lshlrev_b32_sdwa v140,1,v140 dst_sel:DWORD dst_unused:UNUSED_PAD src0_sel:DWORD src1_sel:WORD_1
v_mfma_f32_16x16x32_bf16 v[144:147],v[64:67],v[40:43],v[112:115]
ds_read_u16 v136,v136
v_lshlrev_b32_sdwa v142,1,v142 dst_sel:DWORD dst_unused:UNUSED_PAD src0_sel:DWORD src1_sel:WORD_1
v_mfma_f32_16x16x32_bf16 v[152:155],v[64:67],v[44:47],v[112:115]
ds_read_u16_d16_hi v138,v138
v_lshlrev_b32_sdwa v141,1,v141 dst_sel:DWORD dst_unused:UNUSED_PAD src0_sel:DWORD src1_sel:WORD_1
v_mfma_f32_16x16x32_bf16 v[156:159],v[68:71],v[44:47],v[116:119]
ds_read_b64_tr_b16 v[96:97],v3 offset:12384
ds_read_b64_tr_b16 v[98:99],v3 offset:13920
v_lshlrev_b32_sdwa v143,1,v143 dst_sel:DWORD dst_unused:UNUSED_PAD src0_sel:DWORD src1_sel:WORD_1
v_mfma_f32_16x16x32_bf16 v[148:151],v[68:71],v[40:43],v[116:119]
ds_read_u16 v137,v137
v_mfma_f32_16x16x32_bf16 v[144:147],v[72:75],v[48:51],v[144:147]
ds_read_u16_d16_hi v139,v139
v_mfma_f32_16x16x32_bf16 v[152:155],v[72:75],v[52:55],v[152:155]
ds_read_b64_tr_b16 v[100:101],v3 offset:12392
ds_read_b64_tr_b16 v[102:103],v3 offset:13928
v_mfma_f32_16x16x32_bf16 v[156:159],v[76:79],v[52:55],v[156:159]
ds_read_u16 v140,v140
v_mfma_f32_16x16x32_bf16 v[148:151],v[76:79],v[48:51],v[148:151]
s_waitcnt lgkmcnt(15)
ds_read_u16_d16_hi v142,v142
v_lshl_add_u64 v[128:129],v[128:129],0,v[130:131]
v_mfma_f32_16x16x32_bf16 v[144:147],v[80:83],v[56:59],v[144:147]
s_waitcnt lgkmcnt(12)
ds_read_b64_tr_b16 v[104:105],v3 offset:12448
ds_read_b64_tr_b16 v[106:107],v3 offset:13984
v_lshl_add_u64 v[130:131],v[132:133],0,v[134:135]
v_mfma_f32_16x16x32_bf16 v[152:155],v[80:83],v[60:63],v[152:155]
ds_read_u16 v141,v141
v_mfma_f32_16x16x32_bf16 v[156:159],v[84:87],v[60:63],v[156:159]
ds_read_u16_d16_hi v143,v143
v_mfma_f32_16x16x32_bf16 v[148:151],v[84:87],v[56:59],v[148:151]
ds_read_b64_tr_b16 v[108:109],v3 offset:12456
ds_read_b64_tr_b16 v[110:111],v3 offset:13992
v_lshlrev_b32_sdwa v144,1,v144 dst_sel:DWORD dst_unused:UNUSED_PAD src0_sel:DWORD src1_sel:WORD_1
v_lshlrev_b32_sdwa v146,1,v146 dst_sel:DWORD dst_unused:UNUSED_PAD src0_sel:DWORD src1_sel:WORD_1
v_mfma_f32_16x16x32_bf16 v[160:163],v[88:91],v[128:131],v[160:163]
s_waitcnt lgkmcnt(15)
s_cbranch_vccnz RA4_%=
s_mov_b64 exec,s[62:63]
s_add_u32 m0,s57,59296
s_add_u32 s64,s56,3637248
buffer_load_dwordx4 v5,s[36:39],s64 offen lds
s_add_u32 m0,s57,61024
s_nop 0
buffer_load_dwordx4 v6,s[36:39],s64 offen lds
s_mov_b64 exec,-1
RA4_%=:
ds_read_u16 v144,v144
v_lshlrev_b32_sdwa v145,1,v145 dst_sel:DWORD dst_unused:UNUSED_PAD src0_sel:DWORD src1_sel:WORD_1
v_mfma_f32_16x16x32_bf16 v[168:171],v[92:95],v[128:131],v[168:171]
s_waitcnt lgkmcnt(13)
ds_read_u16_d16_hi v146,v146
v_lshlrev_b32_sdwa v147,1,v147 dst_sel:DWORD dst_unused:UNUSED_PAD src0_sel:DWORD src1_sel:WORD_1
v_mfma_f32_16x16x32_bf16 v[176:179],v[96:99],v[128:131],v[176:179]
s_waitcnt lgkmcnt(10)
ds_read_b64_tr_b16 v[64:65],v1 offset:12320
ds_read_b64_tr_b16 v[66:67],v1 offset:14368
v_lshlrev_b32_sdwa v148,1,v148 dst_sel:DWORD dst_unused:UNUSED_PAD src0_sel:DWORD src1_sel:WORD_1
v_mfma_f32_16x16x32_bf16 v[184:187],v[100:103],v[128:131],v[184:187]
s_waitcnt lgkmcnt(8)
ds_read_u16 v145,v145
v_lshl_add_u64 v[136:137],v[136:137],0,v[138:139]
v_lshlrev_b32_sdwa v150,1,v150 dst_sel:DWORD dst_unused:UNUSED_PAD src0_sel:DWORD src1_sel:WORD_1
v_mfma_f32_16x16x32_bf16 v[192:195],v[104:107],v[128:131],v[192:195]
s_waitcnt lgkmcnt(5)
ds_read_u16_d16_hi v147,v147
v_lshl_add_u64 v[138:139],v[140:141],0,v[142:143]
v_mfma_f32_16x16x32_bf16 v[200:203],v[108:111],v[128:131],v[200:203]
ds_read_b128 v[112:115],v4 offset:1024
ds_read_b64_tr_b16 v[68:69],v2 offset:12320
v_lshlrev_b32_sdwa v149,1,v149 dst_sel:DWORD dst_unused:UNUSED_PAD src0_sel:DWORD src1_sel:WORD_1
v_mfma_f32_16x16x32_bf16 v[164:167],v[88:91],v[136:139],v[164:167]
ds_read_u16 v148,v148
v_lshlrev_b32_sdwa v151,1,v151 dst_sel:DWORD dst_unused:UNUSED_PAD src0_sel:DWORD src1_sel:WORD_1
v_mfma_f32_16x16x32_bf16 v[172:175],v[92:95],v[136:139],v[172:175]
ds_read_u16_d16_hi v150,v150
ds_read_b64_tr_b16 v[70:71],v2 offset:14368
v_lshlrev_b32_sdwa v152,1,v152 dst_sel:DWORD dst_unused:UNUSED_PAD src0_sel:DWORD src1_sel:WORD_1
v_mfma_f32_16x16x32_bf16 v[180:183],v[96:99],v[136:139],v[180:183]
ds_read_b128 v[116:119],v4 offset:1056
ds_read_b64_tr_b16 v[72:73],v1 offset:16416
v_lshlrev_b32_sdwa v154,1,v154 dst_sel:DWORD dst_unused:UNUSED_PAD src0_sel:DWORD src1_sel:WORD_1
v_mfma_f32_16x16x32_bf16 v[188:191],v[100:103],v[136:139],v[188:191]
ds_read_u16 v149,v149
v_lshlrev_b32_sdwa v153,1,v153 dst_sel:DWORD dst_unused:UNUSED_PAD src0_sel:DWORD src1_sel:WORD_1
v_mfma_f32_16x16x32_bf16 v[196:199],v[104:107],v[136:139],v[196:199]
ds_read_u16_d16_hi v151,v151
v_lshlrev_b32_sdwa v155,1,v155 dst_sel:DWORD dst_unused:UNUSED_PAD src0_sel:DWORD src1_sel:WORD_1
v_mfma_f32_16x16x32_bf16 v[204:207],v[108:111],v[136:139],v[204:207]
s_waitcnt lgkmcnt(8)
ds_read_b64_tr_b16 v[74:75],v1 offset:18464
ds_read_b64_tr_b16 v[76:77],v2 offset:16416
v_lshlrev_b32_sdwa v156,1,v156 dst_sel:DWORD dst_unused:UNUSED_PAD src0_sel:DWORD src1_sel:WORD_1
v_mfma_f32_16x16x32_bf16 v[128:131],v[64:67],v[16:19],v[112:115]
ds_read_u16 v152,v152
v_lshlrev_b32_sdwa v158,1,v158 dst_sel:DWORD dst_unused:UNUSED_PAD src0_sel:DWORD src1_sel:WORD_1
v_mfma_f32_16x16x32_bf16 v[136:139],v[64:67],v[20:23],v[112:115]
s_waitcnt lgkmcnt(6)
ds_read_u16_d16_hi v154,v154
ds_read_b64_tr_b16 v[78:79],v2 offset:18464
v_lshlrev_b32_sdwa v157,1,v157 dst_sel:DWORD dst_unused:UNUSED_PAD src0_sel:DWORD src1_sel:WORD_1
v_mfma_f32_16x16x32_bf16 v[140:143],v[68:71],v[20:23],v[116:119]
ds_read_b64_tr_b16 v[80:81],v1 offset:20512
ds_read_b64_tr_b16 v[82:83],v1 offset:22560
v_lshlrev_b32_sdwa v159,1,v159 dst_sel:DWORD dst_unused:UNUSED_PAD src0_sel:DWORD src1_sel:WORD_1
v_mfma_f32_16x16x32_bf16 v[132:135],v[68:71],v[16:19],v[116:119]
s_waitcnt lgkmcnt(6)
ds_read_u16 v153,v153
v_mfma_f32_16x16x32_bf16 v[128:131],v[72:75],v[24:27],v[128:131]
ds_read_u16_d16_hi v155,v155
v_mfma_f32_16x16x32_bf16 v[136:139],v[72:75],v[28:31],v[136:139]
s_waitcnt lgkmcnt(4)
ds_read_b64_tr_b16 v[84:85],v2 offset:20512
ds_read_b64_tr_b16 v[86:87],v2 offset:22560
v_mfma_f32_16x16x32_bf16 v[140:143],v[76:79],v[28:31],v[140:143]
ds_read_u16 v156,v156
v_mfma_f32_16x16x32_bf16 v[132:135],v[76:79],v[24:27],v[132:135]
s_waitcnt lgkmcnt(5)
ds_read_u16_d16_hi v158,v158
v_lshl_add_u64 v[144:145],v[144:145],0,v[146:147]
v_mfma_f32_16x16x32_bf16 v[128:131],v[80:83],v[32:35],v[128:131]
v_lshl_add_u64 v[146:147],v[148:149],0,v[150:151]
v_mfma_f32_16x16x32_bf16 v[136:139],v[80:83],v[36:39],v[136:139]
s_waitcnt lgkmcnt(2)
ds_read_u16 v157,v157
v_mfma_f32_16x16x32_bf16 v[140:143],v[84:87],v[36:39],v[140:143]
ds_read_u16_d16_hi v159,v159
v_mfma_f32_16x16x32_bf16 v[132:135],v[84:87],v[32:35],v[132:135]
s_nop 0
v_lshlrev_b32_sdwa v128,1,v128 dst_sel:DWORD dst_unused:UNUSED_PAD src0_sel:DWORD src1_sel:WORD_1
v_lshlrev_b32_sdwa v130,1,v130 dst_sel:DWORD dst_unused:UNUSED_PAD src0_sel:DWORD src1_sel:WORD_1
v_mfma_f32_16x16x32_bf16 v[208:211],v[88:91],v[144:147],v[208:211]
s_cbranch_vccnz RA5_%=
s_mov_b64 exec,s[62:63]
s_add_u32 m0,s57,62784
s_add_u32 s64,s56,3735552
buffer_load_dwordx4 v5,s[36:39],s64 offen lds
s_add_u32 m0,s57,64512
s_nop 0
buffer_load_dwordx4 v6,s[36:39],s64 offen lds
s_mov_b64 exec,-1
RA5_%=:
ds_read_u16 v128,v128
v_lshlrev_b32_sdwa v129,1,v129 dst_sel:DWORD dst_unused:UNUSED_PAD src0_sel:DWORD src1_sel:WORD_1
v_mfma_f32_16x16x32_bf16 v[216:219],v[92:95],v[144:147],v[216:219]
ds_read_u16_d16_hi v130,v130
v_lshlrev_b32_sdwa v131,1,v131 dst_sel:DWORD dst_unused:UNUSED_PAD src0_sel:DWORD src1_sel:WORD_1
v_mfma_f32_16x16x32_bf16 v[224:227],v[96:99],v[144:147],v[224:227]
v_lshlrev_b32_sdwa v132,1,v132 dst_sel:DWORD dst_unused:UNUSED_PAD src0_sel:DWORD src1_sel:WORD_1
v_mfma_f32_16x16x32_bf16 v[232:235],v[100:103],v[144:147],v[232:235]
ds_read_u16 v129,v129
v_lshl_add_u64 v[152:153],v[152:153],0,v[154:155]
v_lshlrev_b32_sdwa v134,1,v134 dst_sel:DWORD dst_unused:UNUSED_PAD src0_sel:DWORD src1_sel:WORD_1
v_mfma_f32_16x16x32_bf16 v[240:243],v[104:107],v[144:147],v[240:243]
s_waitcnt lgkmcnt(3)
ds_read_u16_d16_hi v131,v131
v_lshl_add_u64 v[154:155],v[156:157],0,v[158:159]
v_mfma_f32_16x16x32_bf16 v[248:251],v[108:111],v[144:147],v[248:251]
v_lshlrev_b32_sdwa v133,1,v133 dst_sel:DWORD dst_unused:UNUSED_PAD src0_sel:DWORD src1_sel:WORD_1
v_mfma_f32_16x16x32_bf16 v[212:215],v[88:91],v[152:155],v[212:215]
ds_read_u16 v132,v132
v_lshlrev_b32_sdwa v135,1,v135 dst_sel:DWORD dst_unused:UNUSED_PAD src0_sel:DWORD src1_sel:WORD_1
v_mfma_f32_16x16x32_bf16 v[220:223],v[92:95],v[152:155],v[220:223]
ds_read_u16_d16_hi v134,v134
v_lshlrev_b32_sdwa v136,1,v136 dst_sel:DWORD dst_unused:UNUSED_PAD src0_sel:DWORD src1_sel:WORD_1
v_mfma_f32_16x16x32_bf16 v[228:231],v[96:99],v[152:155],v[228:231]
ds_read_b64_tr_b16 v[88:89],v3 offset:18480
ds_read_b64_tr_b16 v[90:91],v3 offset:20016
v_lshlrev_b32_sdwa v138,1,v138 dst_sel:DWORD dst_unused:UNUSED_PAD src0_sel:DWORD src1_sel:WORD_1
v_mfma_f32_16x16x32_bf16 v[236:239],v[100:103],v[152:155],v[236:239]
ds_read_u16 v133,v133
v_lshlrev_b32_sdwa v137,1,v137 dst_sel:DWORD dst_unused:UNUSED_PAD src0_sel:DWORD src1_sel:WORD_1
v_mfma_f32_16x16x32_bf16 v[244:247],v[104:107],v[152:155],v[244:247]
ds_read_u16_d16_hi v135,v135
v_lshlrev_b32_sdwa v139,1,v139 dst_sel:DWORD dst_unused:UNUSED_PAD src0_sel:DWORD src1_sel:WORD_1
v_mfma_f32_16x16x32_bf16 v[252:255],v[108:111],v[152:155],v[252:255]
ds_read_b64_tr_b16 v[92:93],v3 offset:18488
ds_read_b64_tr_b16 v[94:95],v3 offset:20024
v_lshlrev_b32_sdwa v140,1,v140 dst_sel:DWORD dst_unused:UNUSED_PAD src0_sel:DWORD src1_sel:WORD_1
v_mfma_f32_16x16x32_bf16 v[144:147],v[64:67],v[40:43],v[112:115]
ds_read_u16 v136,v136
v_lshlrev_b32_sdwa v142,1,v142 dst_sel:DWORD dst_unused:UNUSED_PAD src0_sel:DWORD src1_sel:WORD_1
v_mfma_f32_16x16x32_bf16 v[152:155],v[64:67],v[44:47],v[112:115]
ds_read_u16_d16_hi v138,v138
v_lshlrev_b32_sdwa v141,1,v141 dst_sel:DWORD dst_unused:UNUSED_PAD src0_sel:DWORD src1_sel:WORD_1
v_mfma_f32_16x16x32_bf16 v[156:159],v[68:71],v[44:47],v[116:119]
ds_read_b64_tr_b16 v[96:97],v3 offset:18544
ds_read_b64_tr_b16 v[98:99],v3 offset:20080
v_lshlrev_b32_sdwa v143,1,v143 dst_sel:DWORD dst_unused:UNUSED_PAD src0_sel:DWORD src1_sel:WORD_1
v_mfma_f32_16x16x32_bf16 v[148:151],v[68:71],v[40:43],v[116:119]
ds_read_u16 v137,v137
v_mfma_f32_16x16x32_bf16 v[144:147],v[72:75],v[48:51],v[144:147]
ds_read_u16_d16_hi v139,v139
v_mfma_f32_16x16x32_bf16 v[152:155],v[72:75],v[52:55],v[152:155]
ds_read_b64_tr_b16 v[100:101],v3 offset:18552
ds_read_b64_tr_b16 v[102:103],v3 offset:20088
v_mfma_f32_16x16x32_bf16 v[156:159],v[76:79],v[52:55],v[156:159]
ds_read_u16 v140,v140
v_mfma_f32_16x16x32_bf16 v[148:151],v[76:79],v[48:51],v[148:151]
s_waitcnt lgkmcnt(15)
ds_read_u16_d16_hi v142,v142
v_lshl_add_u64 v[128:129],v[128:129],0,v[130:131]
v_mfma_f32_16x16x32_bf16 v[144:147],v[80:83],v[56:59],v[144:147]
s_waitcnt lgkmcnt(12)
ds_read_b64_tr_b16 v[104:105],v3 offset:18608
ds_read_b64_tr_b16 v[106:107],v3 offset:20144
v_lshl_add_u64 v[130:131],v[132:133],0,v[134:135]
v_mfma_f32_16x16x32_bf16 v[152:155],v[80:83],v[60:63],v[152:155]
ds_read_u16 v141,v141
v_mfma_f32_16x16x32_bf16 v[156:159],v[84:87],v[60:63],v[156:159]
ds_read_u16_d16_hi v143,v143
v_mfma_f32_16x16x32_bf16 v[148:151],v[84:87],v[56:59],v[148:151]
ds_read_b64_tr_b16 v[108:109],v3 offset:18616
ds_read_b64_tr_b16 v[110:111],v3 offset:20152
v_lshlrev_b32_sdwa v144,1,v144 dst_sel:DWORD dst_unused:UNUSED_PAD src0_sel:DWORD src1_sel:WORD_1
v_lshlrev_b32_sdwa v146,1,v146 dst_sel:DWORD dst_unused:UNUSED_PAD src0_sel:DWORD src1_sel:WORD_1
v_mfma_f32_16x16x32_bf16 v[160:163],v[88:91],v[128:131],v[160:163]
s_waitcnt lgkmcnt(15)
s_cbranch_vccnz RA6_%=
s_mov_b64 exec,s[62:63]
s_add_u32 m0,s57,66272
s_add_u32 s64,s56,3833856
buffer_load_dwordx4 v5,s[36:39],s64 offen lds
s_add_u32 m0,s57,68000
s_nop 0
buffer_load_dwordx4 v6,s[36:39],s64 offen lds
s_mov_b64 exec,-1
RA6_%=:
ds_read_u16 v144,v144
v_lshlrev_b32_sdwa v145,1,v145 dst_sel:DWORD dst_unused:UNUSED_PAD src0_sel:DWORD src1_sel:WORD_1
v_mfma_f32_16x16x32_bf16 v[168:171],v[92:95],v[128:131],v[168:171]
s_waitcnt lgkmcnt(13)
ds_read_u16_d16_hi v146,v146
v_lshlrev_b32_sdwa v147,1,v147 dst_sel:DWORD dst_unused:UNUSED_PAD src0_sel:DWORD src1_sel:WORD_1
v_mfma_f32_16x16x32_bf16 v[176:179],v[96:99],v[128:131],v[176:179]
s_waitcnt lgkmcnt(10)
ds_read_b64_tr_b16 v[64:65],v1 offset:12832
ds_read_b64_tr_b16 v[66:67],v1 offset:14880
v_lshlrev_b32_sdwa v148,1,v148 dst_sel:DWORD dst_unused:UNUSED_PAD src0_sel:DWORD src1_sel:WORD_1
v_mfma_f32_16x16x32_bf16 v[184:187],v[100:103],v[128:131],v[184:187]
s_waitcnt lgkmcnt(8)
ds_read_u16 v145,v145
v_lshl_add_u64 v[136:137],v[136:137],0,v[138:139]
v_lshlrev_b32_sdwa v150,1,v150 dst_sel:DWORD dst_unused:UNUSED_PAD src0_sel:DWORD src1_sel:WORD_1
v_mfma_f32_16x16x32_bf16 v[192:195],v[104:107],v[128:131],v[192:195]
s_waitcnt lgkmcnt(5)
ds_read_u16_d16_hi v147,v147
v_lshl_add_u64 v[138:139],v[140:141],0,v[142:143]
v_mfma_f32_16x16x32_bf16 v[200:203],v[108:111],v[128:131],v[200:203]
ds_read_b128 v[112:115],v4 offset:1152
ds_read_b64_tr_b16 v[68:69],v2 offset:12832
v_lshlrev_b32_sdwa v149,1,v149 dst_sel:DWORD dst_unused:UNUSED_PAD src0_sel:DWORD src1_sel:WORD_1
v_mfma_f32_16x16x32_bf16 v[164:167],v[88:91],v[136:139],v[164:167]
ds_read_u16 v148,v148
s_mov_b32 s86,0xbe801d5a
v_lshlrev_b32_sdwa v151,1,v151 dst_sel:DWORD dst_unused:UNUSED_PAD src0_sel:DWORD src1_sel:WORD_1
v_mfma_f32_16x16x32_bf16 v[172:175],v[92:95],v[136:139],v[172:175]
ds_read_u16_d16_hi v150,v150
ds_read_b64_tr_b16 v[70:71],v2 offset:14880
v_lshlrev_b32_sdwa v152,1,v152 dst_sel:DWORD dst_unused:UNUSED_PAD src0_sel:DWORD src1_sel:WORD_1
v_mfma_f32_16x16x32_bf16 v[180:183],v[96:99],v[136:139],v[180:183]
ds_read_b128 v[116:119],v4 offset:1184
ds_read_b64_tr_b16 v[72:73],v1 offset:16928
v_lshlrev_b32_sdwa v154,1,v154 dst_sel:DWORD dst_unused:UNUSED_PAD src0_sel:DWORD src1_sel:WORD_1
v_mfma_f32_16x16x32_bf16 v[188:191],v[100:103],v[136:139],v[188:191]
ds_read_u16 v149,v149
v_lshlrev_b32_sdwa v153,1,v153 dst_sel:DWORD dst_unused:UNUSED_PAD src0_sel:DWORD src1_sel:WORD_1
v_mfma_f32_16x16x32_bf16 v[196:199],v[104:107],v[136:139],v[196:199]
ds_read_u16_d16_hi v151,v151
v_lshlrev_b32_sdwa v155,1,v155 dst_sel:DWORD dst_unused:UNUSED_PAD src0_sel:DWORD src1_sel:WORD_1
v_mfma_f32_16x16x32_bf16 v[204:207],v[108:111],v[136:139],v[204:207]
s_waitcnt lgkmcnt(8)
ds_read_b64_tr_b16 v[74:75],v1 offset:18976
ds_read_b64_tr_b16 v[76:77],v2 offset:16928
v_lshlrev_b32_sdwa v156,1,v156 dst_sel:DWORD dst_unused:UNUSED_PAD src0_sel:DWORD src1_sel:WORD_1
v_mfma_f32_16x16x32_bf16 v[128:131],v[64:67],v[16:19],v[112:115]
ds_read_u16 v152,v152
v_lshlrev_b32_sdwa v158,1,v158 dst_sel:DWORD dst_unused:UNUSED_PAD src0_sel:DWORD src1_sel:WORD_1
v_mfma_f32_16x16x32_bf16 v[136:139],v[64:67],v[20:23],v[112:115]
s_waitcnt lgkmcnt(6)
ds_read_u16_d16_hi v154,v154
ds_read_b64_tr_b16 v[78:79],v2 offset:18976
v_lshlrev_b32_sdwa v157,1,v157 dst_sel:DWORD dst_unused:UNUSED_PAD src0_sel:DWORD src1_sel:WORD_1
v_mfma_f32_16x16x32_bf16 v[140:143],v[68:71],v[20:23],v[116:119]
ds_read_b64_tr_b16 v[80:81],v1 offset:21024
ds_read_b64_tr_b16 v[82:83],v1 offset:23072
v_lshlrev_b32_sdwa v159,1,v159 dst_sel:DWORD dst_unused:UNUSED_PAD src0_sel:DWORD src1_sel:WORD_1
v_mfma_f32_16x16x32_bf16 v[132:135],v[68:71],v[16:19],v[116:119]
s_waitcnt lgkmcnt(6)
ds_read_u16 v153,v153
v_mfma_f32_16x16x32_bf16 v[128:131],v[72:75],v[24:27],v[128:131]
ds_read_u16_d16_hi v155,v155
v_mfma_f32_16x16x32_bf16 v[136:139],v[72:75],v[28:31],v[136:139]
s_waitcnt lgkmcnt(4)
ds_read_b64_tr_b16 v[84:85],v2 offset:21024
ds_read_b64_tr_b16 v[86:87],v2 offset:23072
v_mfma_f32_16x16x32_bf16 v[140:143],v[76:79],v[28:31],v[140:143]
ds_read_u16 v156,v156
v_mfma_f32_16x16x32_bf16 v[132:135],v[76:79],v[24:27],v[132:135]
s_waitcnt lgkmcnt(5)
ds_read_u16_d16_hi v158,v158
v_lshl_add_u64 v[144:145],v[144:145],0,v[146:147]
v_mfma_f32_16x16x32_bf16 v[128:131],v[80:83],v[32:35],v[128:131]
v_lshl_add_u64 v[146:147],v[148:149],0,v[150:151]
v_mfma_f32_16x16x32_bf16 v[136:139],v[80:83],v[36:39],v[136:139]
s_waitcnt lgkmcnt(2)
ds_read_u16 v157,v157
v_mfma_f32_16x16x32_bf16 v[140:143],v[84:87],v[36:39],v[140:143]
ds_read_u16_d16_hi v159,v159
v_mfma_f32_16x16x32_bf16 v[132:135],v[84:87],v[32:35],v[132:135]
s_cbranch_vccnz RA7_%=
s_mov_b64 exec,s[62:63]
s_add_u32 m0,s57,0
s_add_u32 s64,s56,3932160
buffer_load_dwordx4 v5,s[36:39],s64 offen lds
s_add_u32 m0,s57,1728
s_nop 0
buffer_load_dwordx4 v6,s[36:39],s64 offen lds
s_mov_b64 exec,-1
RA7_%=:
v_lshlrev_b32_sdwa v128,1,v128 dst_sel:DWORD dst_unused:UNUSED_PAD src0_sel:DWORD src1_sel:WORD_1
v_lshlrev_b32_sdwa v130,1,v130 dst_sel:DWORD dst_unused:UNUSED_PAD src0_sel:DWORD src1_sel:WORD_1
v_mfma_f32_16x16x32_bf16 v[208:211],v[88:91],v[144:147],v[208:211]
s_barrier
s_setprio 3
ds_read_u16 v128,v128
v_lshlrev_b32_sdwa v129,1,v129 dst_sel:DWORD dst_unused:UNUSED_PAD src0_sel:DWORD src1_sel:WORD_1
v_lshlrev_b32_sdwa v131,1,v131 dst_sel:DWORD dst_unused:UNUSED_PAD src0_sel:DWORD src1_sel:WORD_1
v_mfma_f32_16x16x32_bf16 v[216:219],v[92:95],v[144:147],v[216:219]
v_fma_mixlo_f16 v120,v140,s99,0
v_fma_mixlo_f16 v122,v141,s99,0
v_mfma_f32_16x16x32_bf16 v[224:227],v[96:99],v[144:147],v[224:227]
s_cbranch_vccz RA8_%=
s_add_u32 m0,s58,12320
s_mov_b32 s69,s60
buffer_load_dwordx4 v5,s[44:47],s69 offen lds
s_add_u32 m0,m0,1024
s_add_u32 s69,s69,s53
buffer_load_dwordx4 v6,s[44:47],s69 offen lds
RA8_%=:
ds_read_u16_d16_hi v130,v130
v_lshlrev_b32_sdwa v132,1,v132 dst_sel:DWORD dst_unused:UNUSED_PAD src0_sel:DWORD src1_sel:WORD_1
v_lshlrev_b32_sdwa v134,1,v134 dst_sel:DWORD dst_unused:UNUSED_PAD src0_sel:DWORD src1_sel:WORD_1
v_mfma_f32_16x16x32_bf16 v[232:235],v[100:103],v[144:147],v[232:235]
v_lshl_add_u64 v[152:153],v[152:153],0,v[154:155]
v_mfma_f32_16x16x32_bf16 v[240:243],v[104:107],v[144:147],v[240:243]
s_waitcnt lgkmcnt(2)
ds_read_u16 v129,v129
v_lshl_add_u64 v[154:155],v[156:157],0,v[158:159]
v_mfma_f32_16x16x32_bf16 v[248:251],v[108:111],v[144:147],v[248:251]
v_fma_mixhi_f16 v120,v142,s99,0
v_fma_mixhi_f16 v122,v143,s99,0
v_mfma_f32_16x16x32_bf16 v[212:215],v[88:91],v[152:155],v[212:215]
s_cbranch_vccz RA9_%=
s_add_u32 m0,m0,1024
s_add_u32 s69,s69,s53
buffer_load_dwordx4 v5,s[44:47],s69 offen lds
s_add_u32 m0,m0,s50
s_add_u32 s69,s69,s53
buffer_load_dwordx4 v6,s[44:47],s69 offen lds
RA9_%=:
ds_read_u16_d16_hi v131,v131
v_lshlrev_b32_sdwa v133,1,v133 dst_sel:DWORD dst_unused:UNUSED_PAD src0_sel:DWORD src1_sel:WORD_1
v_lshlrev_b32_sdwa v135,1,v135 dst_sel:DWORD dst_unused:UNUSED_PAD src0_sel:DWORD src1_sel:WORD_1
v_mfma_f32_16x16x32_bf16 v[220:223],v[92:95],v[152:155],v[220:223]
ds_read_b64_tr_b16 v[88:89],v3 offset:0
v_pk_fma_f16 v121,v120,v120,s54
v_pk_fma_f16 v123,v122,v122,s54
v_mfma_f32_16x16x32_bf16 v[228:231],v[96:99],v[152:155],v[228:231]
ds_read_u16 v132,v132
v_pk_mul_f16 v121,v120,v121
v_pk_mul_f16 v123,v122,v123
v_mfma_f32_16x16x32_bf16 v[236:239],v[100:103],v[152:155],v[236:239]
ds_read_b64_tr_b16 v[90:91],v3 offset:1536
v_lshlrev_b32_sdwa v136,1,v136 dst_sel:DWORD dst_unused:UNUSED_PAD src0_sel:DWORD src1_sel:WORD_1
v_lshlrev_b32_sdwa v138,1,v138 dst_sel:DWORD dst_unused:UNUSED_PAD src0_sel:DWORD src1_sel:WORD_1
v_mfma_f32_16x16x32_bf16 v[244:247],v[104:107],v[152:155],v[244:247]
s_cbranch_vccz RA10_%=
s_add_u32 m0,m0,1024
s_add_u32 s69,s69,s53
buffer_load_dwordx4 v5,s[44:47],s69 offen lds
s_add_u32 m0,m0,1024
s_add_u32 s69,s69,s53
buffer_load_dwordx4 v6,s[44:47],s69 offen lds
RA10_%=:
ds_read_u16_d16_hi v134,v134
v_exp_f16 v120,v121
v_mfma_f32_16x16x32_bf16 v[252:255],v[108:111],v[152:155],v[252:255]
ds_read_b64_tr_b16 v[92:93],v3 offset:8
v_exp_f16 v122,v123
v_mfma_f32_16x16x32_bf16 v[144:147],v[64:67],v[40:43],v[112:115]
ds_read_u16 v133,v133
v_lshlrev_b32_sdwa v137,1,v137 dst_sel:DWORD dst_unused:UNUSED_PAD src0_sel:DWORD src1_sel:WORD_1
v_lshlrev_b32_sdwa v139,1,v139 dst_sel:DWORD dst_unused:UNUSED_PAD src0_sel:DWORD src1_sel:WORD_1
v_mfma_f32_16x16x32_bf16 v[152:155],v[64:67],v[44:47],v[112:115]
s_cbranch_vccz RA11_%=
s_add_u32 m0,m0,s50
s_add_u32 s69,s69,s51
buffer_load_dwordx4 v5,s[44:47],s69 offen lds
s_add_u32 m0,m0,1024
s_add_u32 s69,s69,s53
buffer_load_dwordx4 v6,s[44:47],s69 offen lds
RA11_%=:
ds_read_b64_tr_b16 v[94:95],v3 offset:1544
v_exp_f16_sdwa v120,v121 dst_sel:WORD_1 dst_unused:UNUSED_PRESERVE src0_sel:WORD_1
v_mfma_f32_16x16x32_bf16 v[156:159],v[68:71],v[44:47],v[116:119]
ds_read_u16_d16_hi v135,v135
v_exp_f16_sdwa v122,v123 dst_sel:WORD_1 dst_unused:UNUSED_PRESERVE src0_sel:WORD_1
v_mfma_f32_16x16x32_bf16 v[148:151],v[68:71],v[40:43],v[116:119]
ds_read_b64_tr_b16 v[96:97],v3 offset:64
v_pk_fma_f16 v120,v120,s55,s55
v_pk_fma_f16 v122,v122,s55,s55
v_mfma_f32_16x16x32_bf16 v[144:147],v[72:75],v[48:51],v[144:147]
s_cbranch_vccnz RA12_%=
s_mov_b64 exec,s[62:63]
s_add_u32 m0,s57,3488
s_add_u32 s64,s56,4030464
buffer_load_dwordx4 v5,s[36:39],s64 offen lds
s_add_u32 m0,s57,5216
s_nop 0
buffer_load_dwordx4 v6,s[36:39],s64 offen lds
s_mov_b64 exec,-1
RA12_%=:
ds_read_u16 v136,v136
v_rcp_f16 v121,v120
v_mfma_f32_16x16x32_bf16 v[152:155],v[72:75],v[52:55],v[152:155]
s_cbranch_vccz RA13_%=
s_add_u32 m0,m0,1024
s_add_u32 s69,s69,s53
buffer_load_dwordx4 v5,s[44:47],s69 offen lds
s_add_u32 m0,m0,s50
s_add_u32 s69,s69,s53
buffer_load_dwordx4 v6,s[44:47],s69 offen lds
RA13_%=:
ds_read_b64_tr_b16 v[98:99],v3 offset:1600
ds_read_b64_tr_b16 v[100:101],v3 offset:72
v_rcp_f16 v123,v122
v_mfma_f32_16x16x32_bf16 v[156:159],v[76:79],v[52:55],v[156:159]
ds_read_u16_d16_hi v138,v138
v_rcp_f16_sdwa v121,v120 dst_sel:WORD_1 dst_unused:UNUSED_PRESERVE src0_sel:WORD_1
v_mfma_f32_16x16x32_bf16 v[148:151],v[76:79],v[48:51],v[148:151]
ds_read_b64_tr_b16 v[102:103],v3 offset:1608
ds_read_b64_tr_b16 v[104:105],v3 offset:128
v_rcp_f16_sdwa v123,v122 dst_sel:WORD_1 dst_unused:UNUSED_PRESERVE src0_sel:WORD_1
v_mfma_f32_16x16x32_bf16 v[144:147],v[80:83],v[56:59],v[144:147]
ds_read_u16 v137,v137
v_fma_mix_f32 v140,v140,v121,0 op_sel_hi:[0,1,0]
v_fma_mix_f32 v142,v142,v121,0 op_sel:[0,1,0] op_sel_hi:[0,1,0]
v_mfma_f32_16x16x32_bf16 v[152:155],v[80:83],v[60:63],v[152:155]
s_waitcnt lgkmcnt(8)
s_cbranch_vccz RA14_%=
s_add_u32 m0,m0,1024
s_add_u32 s69,s69,s53
buffer_load_dwordx4 v5,s[44:47],s69 offen lds
s_add_u32 m0,m0,1024
s_add_u32 s69,s69,s53
buffer_load_dwordx4 v6,s[44:47],s69 offen lds
RA14_%=:
ds_read_b64_tr_b16 v[106:107],v3 offset:1664
ds_read_b64_tr_b16 v[108:109],v3 offset:136
v_lshl_add_u64 v[128:129],v[128:129],0,v[130:131]
v_lshl_add_u64 v[130:131],v[132:133],0,v[134:135]
v_mfma_f32_16x16x32_bf16 v[156:159],v[84:87],v[60:63],v[156:159]
ds_read_u16_d16_hi v139,v139
v_fma_mix_f32 v141,v141,v123,0 op_sel_hi:[0,1,0]
v_fma_mix_f32 v143,v143,v123,0 op_sel:[0,1,0] op_sel_hi:[0,1,0]
v_mfma_f32_16x16x32_bf16 v[148:151],v[84:87],v[56:59],v[148:151]
ds_read_b64_tr_b16 v[110:111],v3 offset:1672
ds_read_b64_tr_b16 v[64:65],v1 offset:0
v_lshlrev_b32_sdwa v144,1,v144 dst_sel:DWORD dst_unused:UNUSED_PAD src0_sel:DWORD src1_sel:WORD_1
v_lshlrev_b32_sdwa v146,1,v146 dst_sel:DWORD dst_unused:UNUSED_PAD src0_sel:DWORD src1_sel:WORD_1
v_mfma_f32_16x16x32_bf16 v[160:163],v[88:91],v[128:131],v[160:163]
ds_read_u16 v144,v144
v_lshlrev_b32_sdwa v145,1,v145 dst_sel:DWORD dst_unused:UNUSED_PAD src0_sel:DWORD src1_sel:WORD_1
v_lshlrev_b32_sdwa v147,1,v147 dst_sel:DWORD dst_unused:UNUSED_PAD src0_sel:DWORD src1_sel:WORD_1
v_mfma_f32_16x16x32_bf16 v[168:171],v[92:95],v[128:131],v[168:171]
s_waitcnt lgkmcnt(11)
ds_read_b64_tr_b16 v[66:67],v1 offset:2048
ds_read_b128 v[112:115],v4 offset:1280
v_fma_mixlo_f16 v120,v156,s99,0
v_fma_mixlo_f16 v122,v157,s99,0
v_mfma_f32_16x16x32_bf16 v[176:179],v[96:99],v[128:131],v[176:179]
s_waitcnt lgkmcnt(10)
ds_read_u16_d16_hi v146,v146
v_lshlrev_b32_sdwa v148,1,v148 dst_sel:DWORD dst_unused:UNUSED_PAD src0_sel:DWORD src1_sel:WORD_1
v_lshlrev_b32_sdwa v150,1,v150 dst_sel:DWORD dst_unused:UNUSED_PAD src0_sel:DWORD src1_sel:WORD_1
v_mfma_f32_16x16x32_bf16 v[184:187],v[100:103],v[128:131],v[184:187]
s_waitcnt lgkmcnt(6)
ds_read_b64_tr_b16 v[68:69],v2 offset:0
ds_read_b64_tr_b16 v[70:71],v2 offset:2048
v_lshl_add_u64 v[136:137],v[136:137],0,v[138:139]
v_mfma_f32_16x16x32_bf16 v[192:195],v[104:107],v[128:131],v[192:195]
s_waitcnt lgkmcnt(7)
ds_read_u16 v145,v145
v_cvt_pk_bf16_f32 v138,v140,v142
v_cvt_pk_bf16_f32 v139,v141,v143
v_mfma_f32_16x16x32_bf16 v[200:203],v[108:111],v[128:131],v[200:203]
ds_read_b128 v[116:119],v4 offset:1312
ds_read_b64_tr_b16 v[72:73],v1 offset:4096
v_fma_mixhi_f16 v120,v158,s99,0
v_fma_mixhi_f16 v122,v159,s99,0
v_mfma_f32_16x16x32_bf16 v[164:167],v[88:91],v[136:139],v[164:167]
ds_read_u16_d16_hi v147,v147
v_lshlrev_b32_sdwa v149,1,v149 dst_sel:DWORD dst_unused:UNUSED_PAD src0_sel:DWORD src1_sel:WORD_1
v_lshlrev_b32_sdwa v151,1,v151 dst_sel:DWORD dst_unused:UNUSED_PAD src0_sel:DWORD src1_sel:WORD_1
v_mfma_f32_16x16x32_bf16 v[172:175],v[92:95],v[136:139],v[172:175]
ds_read_b64_tr_b16 v[74:75],v1 offset:6144
ds_read_b64_tr_b16 v[76:77],v2 offset:4096
v_pk_fma_f16 v121,v120,v120,s54
v_pk_fma_f16 v123,v122,v122,s54
v_mfma_f32_16x16x32_bf16 v[180:183],v[96:99],v[136:139],v[180:183]
ds_read_u16 v148,v148
v_pk_mul_f16 v121,v120,v121
v_pk_mul_f16 v123,v122,v123
v_mfma_f32_16x16x32_bf16 v[188:191],v[100:103],v[136:139],v[188:191]
s_cbranch_vccnz RA15_%=
s_mov_b64 exec,s[62:63]
s_add_u32 m0,s57,6976
s_add_u32 s64,s56,4128768
buffer_load_dwordx4 v5,s[36:39],s64 offen lds
s_add_u32 m0,s57,8704
s_nop 0
buffer_load_dwordx4 v6,s[36:39],s64 offen lds
s_mov_b64 exec,-1
RA15_%=:
ds_read_b64_tr_b16 v[78:79],v2 offset:6144
v_lshlrev_b32_sdwa v152,1,v152 dst_sel:DWORD dst_unused:UNUSED_PAD src0_sel:DWORD src1_sel:WORD_1
v_lshlrev_b32_sdwa v154,1,v154 dst_sel:DWORD dst_unused:UNUSED_PAD src0_sel:DWORD src1_sel:WORD_1
v_mfma_f32_16x16x32_bf16 v[196:199],v[104:107],v[136:139],v[196:199]
ds_read_u16_d16_hi v150,v150
v_exp_f16 v120,v121
v_mfma_f32_16x16x32_bf16 v[204:207],v[108:111],v[136:139],v[204:207]
s_waitcnt lgkmcnt(12)
ds_read_b64_tr_b16 v[80:81],v1 offset:8192
v_exp_f16 v122,v123
v_mfma_f32_16x16x32_bf16 v[128:131],v[64:67],v[16:19],v[112:115]
ds_read_u16 v149,v149
v_lshlrev_b32_sdwa v153,1,v153 dst_sel:DWORD dst_unused:UNUSED_PAD src0_sel:DWORD src1_sel:WORD_1
v_lshlrev_b32_sdwa v155,1,v155 dst_sel:DWORD dst_unused:UNUSED_PAD src0_sel:DWORD src1_sel:WORD_1
v_mfma_f32_16x16x32_bf16 v[136:139],v[64:67],v[20:23],v[112:115]
s_waitcnt lgkmcnt(9)
ds_read_b64_tr_b16 v[82:83],v1 offset:10240
v_exp_f16_sdwa v120,v121 dst_sel:WORD_1 dst_unused:UNUSED_PRESERVE src0_sel:WORD_1
v_mfma_f32_16x16x32_bf16 v[140:143],v[68:71],v[20:23],v[116:119]
ds_read_u16_d16_hi v151,v151
v_exp_f16_sdwa v122,v123 dst_sel:WORD_1 dst_unused:UNUSED_PRESERVE src0_sel:WORD_1
v_mfma_f32_16x16x32_bf16 v[132:135],v[68:71],v[16:19],v[116:119]
s_waitcnt lgkmcnt(8)
ds_read_b64_tr_b16 v[84:85],v2 offset:8192
v_pk_fma_f16 v120,v120,s55,s55
v_pk_fma_f16 v122,v122,s55,s55
v_mfma_f32_16x16x32_bf16 v[128:131],v[72:75],v[24:27],v[128:131]
ds_read_u16 v152,v152
v_rcp_f16 v121,v120
v_mfma_f32_16x16x32_bf16 v[136:139],v[72:75],v[28:31],v[136:139]
s_waitcnt lgkmcnt(7)
ds_read_b64_tr_b16 v[86:87],v2 offset:10240
v_rcp_f16 v123,v122
v_mfma_f32_16x16x32_bf16 v[140:143],v[76:79],v[28:31],v[140:143]
ds_read_u16_d16_hi v154,v154
v_rcp_f16_sdwa v121,v120 dst_sel:WORD_1 dst_unused:UNUSED_PRESERVE src0_sel:WORD_1
v_mfma_f32_16x16x32_bf16 v[132:135],v[76:79],v[24:27],v[132:135]
s_waitcnt lgkmcnt(5)
v_rcp_f16_sdwa v123,v122 dst_sel:WORD_1 dst_unused:UNUSED_PRESERVE src0_sel:WORD_1
v_mfma_f32_16x16x32_bf16 v[128:131],v[80:83],v[32:35],v[128:131]
ds_read_u16 v153,v153
v_fma_mix_f32 v156,v156,v121,0 op_sel_hi:[0,1,0]
v_fma_mix_f32 v158,v158,v121,0 op_sel:[0,1,0] op_sel_hi:[0,1,0]
v_mfma_f32_16x16x32_bf16 v[136:139],v[80:83],v[36:39],v[136:139]
s_waitcnt lgkmcnt(2)
v_lshl_add_u64 v[144:145],v[144:145],0,v[146:147]
v_lshl_add_u64 v[146:147],v[148:149],0,v[150:151]
v_mfma_f32_16x16x32_bf16 v[140:143],v[84:87],v[36:39],v[140:143]
ds_read_u16_d16_hi v155,v155
v_fma_mix_f32 v157,v157,v123,0 op_sel_hi:[0,1,0]
v_fma_mix_f32 v159,v159,v123,0 op_sel:[0,1,0] op_sel_hi:[0,1,0]
v_mfma_f32_16x16x32_bf16 v[132:135],v[84:87],v[32:35],v[132:135]
v_lshlrev_b32_sdwa v128,1,v128 dst_sel:DWORD dst_unused:UNUSED_PAD src0_sel:DWORD src1_sel:WORD_1
v_lshlrev_b32_sdwa v130,1,v130 dst_sel:DWORD dst_unused:UNUSED_PAD src0_sel:DWORD src1_sel:WORD_1
v_mfma_f32_16x16x32_bf16 v[208:211],v[88:91],v[144:147],v[208:211]
ds_read_u16 v128,v128
v_lshlrev_b32_sdwa v129,1,v129 dst_sel:DWORD dst_unused:UNUSED_PAD src0_sel:DWORD src1_sel:WORD_1
v_lshlrev_b32_sdwa v131,1,v131 dst_sel:DWORD dst_unused:UNUSED_PAD src0_sel:DWORD src1_sel:WORD_1
v_mfma_f32_16x16x32_bf16 v[216:219],v[92:95],v[144:147],v[216:219]
v_fma_mixlo_f16 v120,v140,s99,0
v_fma_mixlo_f16 v122,v141,s99,0
v_mfma_f32_16x16x32_bf16 v[224:227],v[96:99],v[144:147],v[224:227]
s_cbranch_vccnz RA16_%=
s_mov_b64 exec,s[62:63]
s_add_u32 m0,s57,10464
s_add_u32 s64,s56,4227072
buffer_load_dwordx4 v5,s[36:39],s64 offen lds
s_add_u32 m0,s57,12192
s_nop 0
buffer_load_dwordx4 v6,s[36:39],s64 offen lds
s_mov_b64 exec,-1
RA16_%=:
ds_read_u16_d16_hi v130,v130
v_lshlrev_b32_sdwa v132,1,v132 dst_sel:DWORD dst_unused:UNUSED_PAD src0_sel:DWORD src1_sel:WORD_1
v_lshlrev_b32_sdwa v134,1,v134 dst_sel:DWORD dst_unused:UNUSED_PAD src0_sel:DWORD src1_sel:WORD_1
v_mfma_f32_16x16x32_bf16 v[232:235],v[100:103],v[144:147],v[232:235]
s_waitcnt lgkmcnt(2)
v_lshl_add_u64 v[152:153],v[152:153],0,v[154:155]
v_mfma_f32_16x16x32_bf16 v[240:243],v[104:107],v[144:147],v[240:243]
ds_read_u16 v129,v129
v_cvt_pk_bf16_f32 v154,v156,v158
v_cvt_pk_bf16_f32 v155,v157,v159
v_mfma_f32_16x16x32_bf16 v[248:251],v[108:111],v[144:147],v[248:251]
v_fma_mixhi_f16 v120,v142,s99,0
v_fma_mixhi_f16 v122,v143,s99,0
v_mfma_f32_16x16x32_bf16 v[212:215],v[88:91],v[152:155],v[212:215]
s_setprio 2
ds_read_u16_d16_hi v131,v131
v_lshlrev_b32_sdwa v133,1,v133 dst_sel:DWORD dst_unused:UNUSED_PAD src0_sel:DWORD src1_sel:WORD_1
v_lshlrev_b32_sdwa v135,1,v135 dst_sel:DWORD dst_unused:UNUSED_PAD src0_sel:DWORD src1_sel:WORD_1
v_mfma_f32_16x16x32_bf16 v[220:223],v[92:95],v[152:155],v[220:223]
ds_read_b64_tr_b16 v[88:89],v3 offset:6160
v_pk_fma_f16 v121,v120,v120,s54
v_pk_fma_f16 v123,v122,v122,s54
v_mfma_f32_16x16x32_bf16 v[228:231],v[96:99],v[152:155],v[228:231]
ds_read_u16 v132,v132
v_pk_mul_f16 v121,v120,v121
v_pk_mul_f16 v123,v122,v123
v_mfma_f32_16x16x32_bf16 v[236:239],v[100:103],v[152:155],v[236:239]
ds_read_b64_tr_b16 v[90:91],v3 offset:7696
v_lshlrev_b32_sdwa v136,1,v136 dst_sel:DWORD dst_unused:UNUSED_PAD src0_sel:DWORD src1_sel:WORD_1
v_lshlrev_b32_sdwa v138,1,v138 dst_sel:DWORD dst_unused:UNUSED_PAD src0_sel:DWORD src1_sel:WORD_1
v_mfma_f32_16x16x32_bf16 v[244:247],v[104:107],v[152:155],v[244:247]
ds_read_u16_d16_hi v134,v134
v_exp_f16 v120,v121
v_mfma_f32_16x16x32_bf16 v[252:255],v[108:111],v[152:155],v[252:255]
ds_read_b64_tr_b16 v[92:93],v3 offset:6168
v_exp_f16 v122,v123
v_mfma_f32_16x16x32_bf16 v[144:147],v[64:67],v[40:43],v[112:115]
ds_read_u16 v133,v133
v_lshlrev_b32_sdwa v137,1,v137 dst_sel:DWORD dst_unused:UNUSED_PAD src0_sel:DWORD src1_sel:WORD_1
v_lshlrev_b32_sdwa v139,1,v139 dst_sel:DWORD dst_unused:UNUSED_PAD src0_sel:DWORD src1_sel:WORD_1
v_mfma_f32_16x16x32_bf16 v[152:155],v[64:67],v[44:47],v[112:115]
ds_read_b64_tr_b16 v[94:95],v3 offset:7704
v_exp_f16_sdwa v120,v121 dst_sel:WORD_1 dst_unused:UNUSED_PRESERVE src0_sel:WORD_1
v_mfma_f32_16x16x32_bf16 v[156:159],v[68:71],v[44:47],v[116:119]
ds_read_u16_d16_hi v135,v135
v_exp_f16_sdwa v122,v123 dst_sel:WORD_1 dst_unused:UNUSED_PRESERVE src0_sel:WORD_1
v_mfma_f32_16x16x32_bf16 v[148:151],v[68:71],v[40:43],v[116:119]
ds_read_b64_tr_b16 v[96:97],v3 offset:6224
v_pk_fma_f16 v120,v120,s55,s55
v_pk_fma_f16 v122,v122,s55,s55
v_mfma_f32_16x16x32_bf16 v[144:147],v[72:75],v[48:51],v[144:147]
ds_read_u16 v136,v136
v_rcp_f16 v121,v120
v_mfma_f32_16x16x32_bf16 v[152:155],v[72:75],v[52:55],v[152:155]
ds_read_b64_tr_b16 v[98:99],v3 offset:7760
ds_read_b64_tr_b16 v[100:101],v3 offset:6232
v_rcp_f16 v123,v122
v_mfma_f32_16x16x32_bf16 v[156:159],v[76:79],v[52:55],v[156:159]
ds_read_u16_d16_hi v138,v138
v_rcp_f16_sdwa v121,v120 dst_sel:WORD_1 dst_unused:UNUSED_PRESERVE src0_sel:WORD_1
v_mfma_f32_16x16x32_bf16 v[148:151],v[76:79],v[48:51],v[148:151]
s_cbranch_vccnz RA17_%=
s_mov_b64 exec,s[62:63]
s_add_u32 m0,s57,13952
s_add_u32 s64,s56,4325376
buffer_load_dwordx4 v5,s[36:39],s64 offen lds
s_add_u32 m0,s57,15680
s_nop 0
buffer_load_dwordx4 v6,s[36:39],s64 offen lds
s_mov_b64 exec,-1
RA17_%=:
ds_read_b64_tr_b16 v[102:103],v3 offset:7768
ds_read_b64_tr_b16 v[104:105],v3 offset:6288
v_rcp_f16_sdwa v123,v122 dst_sel:WORD_1 dst_unused:UNUSED_PRESERVE src0_sel:WORD_1
v_mfma_f32_16x16x32_bf16 v[144:147],v[80:83],v[56:59],v[144:147]
ds_read_u16 v137,v137
v_fma_mix_f32 v140,v140,v121,0 op_sel_hi:[0,1,0]
v_fma_mix_f32 v142,v142,v121,0 op_sel:[0,1,0] op_sel_hi:[0,1,0]
v_mfma_f32_16x16x32_bf16 v[152:155],v[80:83],v[60:63],v[152:155]
s_waitcnt lgkmcnt(8)
ds_read_b64_tr_b16 v[106:107],v3 offset:7824
ds_read_b64_tr_b16 v[108:109],v3 offset:6296
v_lshl_add_u64 v[128:129],v[128:129],0,v[130:131]
v_lshl_add_u64 v[130:131],v[132:133],0,v[134:135]
v_mfma_f32_16x16x32_bf16 v[156:159],v[84:87],v[60:63],v[156:159]
ds_read_u16_d16_hi v139,v139
v_fma_mix_f32 v141,v141,v123,0 op_sel_hi:[0,1,0]
v_fma_mix_f32 v143,v143,v123,0 op_sel:[0,1,0] op_sel_hi:[0,1,0]
v_mfma_f32_16x16x32_bf16 v[148:151],v[84:87],v[56:59],v[148:151]
ds_read_b64_tr_b16 v[110:111],v3 offset:7832
ds_read_b64_tr_b16 v[64:65],v1 offset:512
v_lshlrev_b32_sdwa v144,1,v144 dst_sel:DWORD dst_unused:UNUSED_PAD src0_sel:DWORD src1_sel:WORD_1
v_lshlrev_b32_sdwa v146,1,v146 dst_sel:DWORD dst_unused:UNUSED_PAD src0_sel:DWORD src1_sel:WORD_1
v_mfma_f32_16x16x32_bf16 v[160:163],v[88:91],v[128:131],v[160:163]
ds_read_u16 v144,v144
v_lshlrev_b32_sdwa v145,1,v145 dst_sel:DWORD dst_unused:UNUSED_PAD src0_sel:DWORD src1_sel:WORD_1
v_lshlrev_b32_sdwa v147,1,v147 dst_sel:DWORD dst_unused:UNUSED_PAD src0_sel:DWORD src1_sel:WORD_1
v_mfma_f32_16x16x32_bf16 v[168:171],v[92:95],v[128:131],v[168:171]
s_waitcnt lgkmcnt(11)
ds_read_b64_tr_b16 v[66:67],v1 offset:2560
ds_read_b128 v[112:115],v4 offset:1408
v_fma_mixlo_f16 v120,v156,s99,0
v_fma_mixlo_f16 v122,v157,s99,0
v_mfma_f32_16x16x32_bf16 v[176:179],v[96:99],v[128:131],v[176:179]
s_waitcnt lgkmcnt(10)
ds_read_u16_d16_hi v146,v146
v_lshlrev_b32_sdwa v148,1,v148 dst_sel:DWORD dst_unused:UNUSED_PAD src0_sel:DWORD src1_sel:WORD_1
v_lshlrev_b32_sdwa v150,1,v150 dst_sel:DWORD dst_unused:UNUSED_PAD src0_sel:DWORD src1_sel:WORD_1
v_mfma_f32_16x16x32_bf16 v[184:187],v[100:103],v[128:131],v[184:187]
s_waitcnt lgkmcnt(6)
ds_read_b64_tr_b16 v[68:69],v2 offset:512
ds_read_b64_tr_b16 v[70:71],v2 offset:2560
v_lshl_add_u64 v[136:137],v[136:137],0,v[138:139]
v_mfma_f32_16x16x32_bf16 v[192:195],v[104:107],v[128:131],v[192:195]
s_waitcnt lgkmcnt(7)
ds_read_u16 v145,v145
v_cvt_pk_bf16_f32 v138,v140,v142
v_cvt_pk_bf16_f32 v139,v141,v143
v_mfma_f32_16x16x32_bf16 v[200:203],v[108:111],v[128:131],v[200:203]
ds_read_b128 v[116:119],v4 offset:1440
ds_read_b64_tr_b16 v[72:73],v1 offset:4608
v_fma_mixhi_f16 v120,v158,s99,0
v_fma_mixhi_f16 v122,v159,s99,0
v_mfma_f32_16x16x32_bf16 v[164:167],v[88:91],v[136:139],v[164:167]
ds_read_u16_d16_hi v147,v147
v_lshlrev_b32_sdwa v149,1,v149 dst_sel:DWORD dst_unused:UNUSED_PAD src0_sel:DWORD src1_sel:WORD_1
v_lshlrev_b32_sdwa v151,1,v151 dst_sel:DWORD dst_unused:UNUSED_PAD src0_sel:DWORD src1_sel:WORD_1
v_mfma_f32_16x16x32_bf16 v[172:175],v[92:95],v[136:139],v[172:175]
s_setprio 1
ds_read_b64_tr_b16 v[74:75],v1 offset:6656
ds_read_b64_tr_b16 v[76:77],v2 offset:4608
v_pk_fma_f16 v121,v120,v120,s54
v_pk_fma_f16 v123,v122,v122,s54
v_mfma_f32_16x16x32_bf16 v[180:183],v[96:99],v[136:139],v[180:183]
ds_read_u16 v148,v148
v_pk_mul_f16 v121,v120,v121
v_pk_mul_f16 v123,v122,v123
v_mfma_f32_16x16x32_bf16 v[188:191],v[100:103],v[136:139],v[188:191]
ds_read_b64_tr_b16 v[78:79],v2 offset:6656
v_lshlrev_b32_sdwa v152,1,v152 dst_sel:DWORD dst_unused:UNUSED_PAD src0_sel:DWORD src1_sel:WORD_1
v_lshlrev_b32_sdwa v154,1,v154 dst_sel:DWORD dst_unused:UNUSED_PAD src0_sel:DWORD src1_sel:WORD_1
v_mfma_f32_16x16x32_bf16 v[196:199],v[104:107],v[136:139],v[196:199]
ds_read_u16_d16_hi v150,v150
v_exp_f16 v120,v121
v_mfma_f32_16x16x32_bf16 v[204:207],v[108:111],v[136:139],v[204:207]
s_waitcnt lgkmcnt(12)
ds_read_b64_tr_b16 v[80:81],v1 offset:8704
v_exp_f16 v122,v123
v_mfma_f32_16x16x32_bf16 v[128:131],v[64:67],v[16:19],v[112:115]
s_cbranch_vccnz RA18_%=
s_mov_b64 exec,s[62:63]
s_add_u32 m0,s57,17440
s_add_u32 s64,s56,4423680
buffer_load_dwordx4 v5,s[36:39],s64 offen lds
s_add_u32 m0,s57,19168
s_nop 0
buffer_load_dwordx4 v6,s[36:39],s64 offen lds
s_mov_b64 exec,-1
RA18_%=:
ds_read_u16 v149,v149
v_lshlrev_b32_sdwa v153,1,v153 dst_sel:DWORD dst_unused:UNUSED_PAD src0_sel:DWORD src1_sel:WORD_1
v_lshlrev_b32_sdwa v155,1,v155 dst_sel:DWORD dst_unused:UNUSED_PAD src0_sel:DWORD src1_sel:WORD_1
v_mfma_f32_16x16x32_bf16 v[136:139],v[64:67],v[20:23],v[112:115]
s_waitcnt lgkmcnt(9)
ds_read_b64_tr_b16 v[82:83],v1 offset:10752
v_exp_f16_sdwa v120,v121 dst_sel:WORD_1 dst_unused:UNUSED_PRESERVE src0_sel:WORD_1
v_mfma_f32_16x16x32_bf16 v[140:143],v[68:71],v[20:23],v[116:119]
ds_read_u16_d16_hi v151,v151
v_exp_f16_sdwa v122,v123 dst_sel:WORD_1 dst_unused:UNUSED_PRESERVE src0_sel:WORD_1
v_mfma_f32_16x16x32_bf16 v[132:135],v[68:71],v[16:19],v[116:119]
s_waitcnt lgkmcnt(8)
ds_read_b64_tr_b16 v[84:85],v2 offset:8704
v_pk_fma_f16 v120,v120,s55,s55
v_pk_fma_f16 v122,v122,s55,s55
v_mfma_f32_16x16x32_bf16 v[128:131],v[72:75],v[24:27],v[128:131]
ds_read_u16 v152,v152
v_rcp_f16 v121,v120
v_mfma_f32_16x16x32_bf16 v[136:139],v[72:75],v[28:31],v[136:139]
s_waitcnt lgkmcnt(7)
ds_read_b64_tr_b16 v[86:87],v2 offset:10752
v_rcp_f16 v123,v122
v_mfma_f32_16x16x32_bf16 v[140:143],v[76:79],v[28:31],v[140:143]
ds_read_u16_d16_hi v154,v154
v_rcp_f16_sdwa v121,v120 dst_sel:WORD_1 dst_unused:UNUSED_PRESERVE src0_sel:WORD_1
v_mfma_f32_16x16x32_bf16 v[132:135],v[76:79],v[24:27],v[132:135]
s_waitcnt lgkmcnt(5)
v_rcp_f16_sdwa v123,v122 dst_sel:WORD_1 dst_unused:UNUSED_PRESERVE src0_sel:WORD_1
v_mfma_f32_16x16x32_bf16 v[128:131],v[80:83],v[32:35],v[128:131]
ds_read_u16 v153,v153
v_fma_mix_f32 v156,v156,v121,0 op_sel_hi:[0,1,0]
v_fma_mix_f32 v158,v158,v121,0 op_sel:[0,1,0] op_sel_hi:[0,1,0]
v_mfma_f32_16x16x32_bf16 v[136:139],v[80:83],v[36:39],v[136:139]
s_waitcnt lgkmcnt(2)
s_setprio 0
v_lshl_add_u64 v[144:145],v[144:145],0,v[146:147]
v_lshl_add_u64 v[146:147],v[148:149],0,v[150:151]
v_mfma_f32_16x16x32_bf16 v[140:143],v[84:87],v[36:39],v[140:143]
ds_read_u16_d16_hi v155,v155
v_fma_mix_f32 v157,v157,v123,0 op_sel_hi:[0,1,0]
v_fma_mix_f32 v159,v159,v123,0 op_sel:[0,1,0] op_sel_hi:[0,1,0]
v_mfma_f32_16x16x32_bf16 v[132:135],v[84:87],v[32:35],v[132:135]
v_lshlrev_b32_sdwa v128,1,v128 dst_sel:DWORD dst_unused:UNUSED_PAD src0_sel:DWORD src1_sel:WORD_1
v_lshlrev_b32_sdwa v130,1,v130 dst_sel:DWORD dst_unused:UNUSED_PAD src0_sel:DWORD src1_sel:WORD_1
v_mfma_f32_16x16x32_bf16 v[208:211],v[88:91],v[144:147],v[208:211]
s_cbranch_vccz RA19_%=
s_waitcnt vmcnt(0)
RA19_%=:
s_barrier
s_setprio 3
ds_read_u16 v128,v128
v_lshlrev_b32_sdwa v129,1,v129 dst_sel:DWORD dst_unused:UNUSED_PAD src0_sel:DWORD src1_sel:WORD_1
v_lshlrev_b32_sdwa v131,1,v131 dst_sel:DWORD dst_unused:UNUSED_PAD src0_sel:DWORD src1_sel:WORD_1
v_mfma_f32_16x16x32_bf16 v[216:219],v[92:95],v[144:147],v[216:219]
v_fma_mixlo_f16 v120,v140,s99,0
v_fma_mixlo_f16 v122,v141,s99,0
v_mfma_f32_16x16x32_bf16 v[224:227],v[96:99],v[144:147],v[224:227]
s_cbranch_vccz RA20_%=
s_add_u32 m0,s58,0
s_mul_i32 s69,s59,1
s_add_u32 s69,s69,s97
buffer_load_dwordx4 v5,s[44:47],s69 offen lds
s_add_u32 m0,m0,1024
s_add_u32 s69,s69,s53
buffer_load_dwordx4 v6,s[44:47],s69 offen lds
RA20_%=:
ds_read_u16_d16_hi v130,v130
v_lshlrev_b32_sdwa v132,1,v132 dst_sel:DWORD dst_unused:UNUSED_PAD src0_sel:DWORD src1_sel:WORD_1
v_lshlrev_b32_sdwa v134,1,v134 dst_sel:DWORD dst_unused:UNUSED_PAD src0_sel:DWORD src1_sel:WORD_1
v_mfma_f32_16x16x32_bf16 v[232:235],v[100:103],v[144:147],v[232:235]
s_waitcnt lgkmcnt(2)
v_lshl_add_u64 v[152:153],v[152:153],0,v[154:155]
v_mfma_f32_16x16x32_bf16 v[240:243],v[104:107],v[144:147],v[240:243]
ds_read_u16 v129,v129
v_cvt_pk_bf16_f32 v154,v156,v158
v_cvt_pk_bf16_f32 v155,v157,v159
v_mfma_f32_16x16x32_bf16 v[248:251],v[108:111],v[144:147],v[248:251]
s_cbranch_vccnz RA21_%=
s_mov_b32 s86,0xbe801d5a
s_mov_b64 exec,s[62:63]
s_add_u32 m0,s57,20928
s_add_u32 s64,s56,4521984
buffer_load_dwordx4 v5,s[36:39],s64 offen lds
s_add_u32 m0,s57,22656
s_nop 0
buffer_load_dwordx4 v6,s[36:39],s64 offen lds
s_mov_b64 exec,-1
RA21_%=:
v_fma_mixhi_f16 v120,v142,s99,0
v_fma_mixhi_f16 v122,v143,s99,0
v_mfma_f32_16x16x32_bf16 v[212:215],v[88:91],v[152:155],v[212:215]
s_cbranch_vccz RA22_%=
s_add_u32 m0,m0,1024
s_add_u32 s69,s69,s53
buffer_load_dwordx4 v5,s[44:47],s69 offen lds
s_add_u32 m0,m0,s50
s_add_u32 s69,s69,s53
buffer_load_dwordx4 v6,s[44:47],s69 offen lds
RA22_%=:
ds_read_u16_d16_hi v131,v131
v_lshlrev_b32_sdwa v133,1,v133 dst_sel:DWORD dst_unused:UNUSED_PAD src0_sel:DWORD src1_sel:WORD_1
v_lshlrev_b32_sdwa v135,1,v135 dst_sel:DWORD dst_unused:UNUSED_PAD src0_sel:DWORD src1_sel:WORD_1
v_mfma_f32_16x16x32_bf16 v[220:223],v[92:95],v[152:155],v[220:223]
ds_read_b64_tr_b16 v[88:89],v3 offset:12320
v_pk_fma_f16 v121,v120,v120,s54
v_pk_fma_f16 v123,v122,v122,s54
v_mfma_f32_16x16x32_bf16 v[228:231],v[96:99],v[152:155],v[228:231]
ds_read_u16 v132,v132
v_pk_mul_f16 v121,v120,v121
v_pk_mul_f16 v123,v122,v123
v_mfma_f32_16x16x32_bf16 v[236:239],v[100:103],v[152:155],v[236:239]
s_cbranch_vccz RA23_%=
s_add_u32 m0,m0,1024
s_add_u32 s69,s69,s53
buffer_load_dwordx4 v5,s[44:47],s69 offen lds
s_add_u32 m0,m0,1024
s_add_u32 s69,s69,s53
buffer_load_dwordx4 v6,s[44:47],s69 offen lds
RA23_%=:
ds_read_b64_tr_b16 v[90:91],v3 offset:13856
v_lshlrev_b32_sdwa v136,1,v136 dst_sel:DWORD dst_unused:UNUSED_PAD src0_sel:DWORD src1_sel:WORD_1
v_lshlrev_b32_sdwa v138,1,v138 dst_sel:DWORD dst_unused:UNUSED_PAD src0_sel:DWORD src1_sel:WORD_1
v_mfma_f32_16x16x32_bf16 v[244:247],v[104:107],v[152:155],v[244:247]
ds_read_u16_d16_hi v134,v134
v_exp_f16 v120,v121
v_mfma_f32_16x16x32_bf16 v[252:255],v[108:111],v[152:155],v[252:255]
ds_read_b64_tr_b16 v[92:93],v3 offset:12328
v_exp_f16 v122,v123
v_mfma_f32_16x16x32_bf16 v[144:147],v[64:67],v[40:43],v[112:115]
ds_read_u16 v133,v133
v_lshlrev_b32_sdwa v137,1,v137 dst_sel:DWORD dst_unused:UNUSED_PAD src0_sel:DWORD src1_sel:WORD_1
v_lshlrev_b32_sdwa v139,1,v139 dst_sel:DWORD dst_unused:UNUSED_PAD src0_sel:DWORD src1_sel:WORD_1
v_mfma_f32_16x16x32_bf16 v[152:155],v[64:67],v[44:47],v[112:115]
s_cbranch_vccz RA24_%=
s_add_u32 m0,m0,s50
s_add_u32 s69,s69,s53
buffer_load_dwordx4 v5,s[44:47],s69 offen lds
s_add_u32 m0,m0,1024
s_add_u32 s69,s69,s53
buffer_load_dwordx4 v6,s[44:47],s69 offen lds
RA24_%=:
ds_read_b64_tr_b16 v[94:95],v3 offset:13864
v_exp_f16_sdwa v120,v121 dst_sel:WORD_1 dst_unused:UNUSED_PRESERVE src0_sel:WORD_1
v_mfma_f32_16x16x32_bf16 v[156:159],v[68:71],v[44:47],v[116:119]
ds_read_u16_d16_hi v135,v135
v_exp_f16_sdwa v122,v123 dst_sel:WORD_1 dst_unused:UNUSED_PRESERVE src0_sel:WORD_1
v_mfma_f32_16x16x32_bf16 v[148:151],v[68:71],v[40:43],v[116:119]
ds_read_b64_tr_b16 v[96:97],v3 offset:12384
v_pk_fma_f16 v120,v120,s55,s55
v_pk_fma_f16 v122,v122,s55,s55
v_mfma_f32_16x16x32_bf16 v[144:147],v[72:75],v[48:51],v[144:147]
ds_read_u16 v136,v136
v_rcp_f16 v121,v120
v_mfma_f32_16x16x32_bf16 v[152:155],v[72:75],v[52:55],v[152:155]
s_cbranch_vccz RA25_%=
s_add_u32 m0,m0,1024
s_add_u32 s69,s69,s53
buffer_load_dwordx4 v5,s[44:47],s69 offen lds
s_add_u32 m0,m0,s50
s_add_u32 s69,s69,s53
buffer_load_dwordx4 v6,s[44:47],s69 offen lds
RA25_%=:
ds_read_b64_tr_b16 v[98:99],v3 offset:13920
ds_read_b64_tr_b16 v[100:101],v3 offset:12392
v_rcp_f16 v123,v122
v_mfma_f32_16x16x32_bf16 v[156:159],v[76:79],v[52:55],v[156:159]
ds_read_u16_d16_hi v138,v138
v_rcp_f16_sdwa v121,v120 dst_sel:WORD_1 dst_unused:UNUSED_PRESERVE src0_sel:WORD_1
v_mfma_f32_16x16x32_bf16 v[148:151],v[76:79],v[48:51],v[148:151]
ds_read_b64_tr_b16 v[102:103],v3 offset:13928
ds_read_b64_tr_b16 v[104:105],v3 offset:12448
v_rcp_f16_sdwa v123,v122 dst_sel:WORD_1 dst_unused:UNUSED_PRESERVE src0_sel:WORD_1
v_mfma_f32_16x16x32_bf16 v[144:147],v[80:83],v[56:59],v[144:147]
ds_read_u16 v137,v137
v_fma_mix_f32 v140,v140,v121,0 op_sel_hi:[0,1,0]
v_fma_mix_f32 v142,v142,v121,0 op_sel:[0,1,0] op_sel_hi:[0,1,0]
v_mfma_f32_16x16x32_bf16 v[152:155],v[80:83],v[60:63],v[152:155]
s_waitcnt lgkmcnt(8)
s_cbranch_vccz RA26_%=
s_add_u32 m0,m0,1024
s_add_u32 s69,s69,s53
buffer_load_dwordx4 v5,s[44:47],s69 offen lds
s_add_u32 m0,m0,1024
s_add_u32 s69,s69,s53
buffer_load_dwordx4 v6,s[44:47],s69 offen lds
RA26_%=:
ds_read_b64_tr_b16 v[106:107],v3 offset:13984
ds_read_b64_tr_b16 v[108:109],v3 offset:12456
v_lshl_add_u64 v[128:129],v[128:129],0,v[130:131]
v_lshl_add_u64 v[130:131],v[132:133],0,v[134:135]
v_mfma_f32_16x16x32_bf16 v[156:159],v[84:87],v[60:63],v[156:159]
s_cbranch_vccnz RA27_%=
s_mov_b64 exec,s[62:63]
s_add_u32 m0,s57,24416
s_add_u32 s64,s56,4620288
buffer_load_dwordx4 v5,s[36:39],s64 offen lds
s_add_u32 m0,s57,26144
s_nop 0
buffer_load_dwordx4 v6,s[36:39],s64 offen lds
s_mov_b64 exec,-1
RA27_%=:
ds_read_u16_d16_hi v139,v139
v_fma_mix_f32 v141,v141,v123,0 op_sel_hi:[0,1,0]
v_fma_mix_f32 v143,v143,v123,0 op_sel:[0,1,0] op_sel_hi:[0,1,0]
v_mfma_f32_16x16x32_bf16 v[148:151],v[84:87],v[56:59],v[148:151]
ds_read_b64_tr_b16 v[110:111],v3 offset:13992
ds_read_b64_tr_b16 v[64:65],v1 offset:12320
v_lshlrev_b32_sdwa v144,1,v144 dst_sel:DWORD dst_unused:UNUSED_PAD src0_sel:DWORD src1_sel:WORD_1
v_lshlrev_b32_sdwa v146,1,v146 dst_sel:DWORD dst_unused:UNUSED_PAD src0_sel:DWORD src1_sel:WORD_1
v_mfma_f32_16x16x32_bf16 v[160:163],v[88:91],v[128:131],v[160:163]
ds_read_u16 v144,v144
v_lshlrev_b32_sdwa v145,1,v145 dst_sel:DWORD dst_unused:UNUSED_PAD src0_sel:DWORD src1_sel:WORD_1
v_lshlrev_b32_sdwa v147,1,v147 dst_sel:DWORD dst_unused:UNUSED_PAD src0_sel:DWORD src1_sel:WORD_1
v_mfma_f32_16x16x32_bf16 v[168:171],v[92:95],v[128:131],v[168:171]
s_waitcnt lgkmcnt(11)
ds_read_b64_tr_b16 v[66:67],v1 offset:14368
ds_read_b128 v[112:115],v4 offset:0
v_fma_mixlo_f16 v120,v156,s99,0
v_fma_mixlo_f16 v122,v157,s99,0
v_mfma_f32_16x16x32_bf16 v[176:179],v[96:99],v[128:131],v[176:179]
s_waitcnt lgkmcnt(10)
ds_read_u16_d16_hi v146,v146
v_lshlrev_b32_sdwa v148,1,v148 dst_sel:DWORD dst_unused:UNUSED_PAD src0_sel:DWORD src1_sel:WORD_1
v_lshlrev_b32_sdwa v150,1,v150 dst_sel:DWORD dst_unused:UNUSED_PAD src0_sel:DWORD src1_sel:WORD_1
v_mfma_f32_16x16x32_bf16 v[184:187],v[100:103],v[128:131],v[184:187]
s_waitcnt lgkmcnt(6)
ds_read_b64_tr_b16 v[68:69],v2 offset:12320
ds_read_b64_tr_b16 v[70:71],v2 offset:14368
v_lshl_add_u64 v[136:137],v[136:137],0,v[138:139]
v_mfma_f32_16x16x32_bf16 v[192:195],v[104:107],v[128:131],v[192:195]
s_waitcnt lgkmcnt(7)
ds_read_u16 v145,v145
v_cvt_pk_bf16_f32 v138,v140,v142
v_cvt_pk_bf16_f32 v139,v141,v143
v_mfma_f32_16x16x32_bf16 v[200:203],v[108:111],v[128:131],v[200:203]
ds_read_b128 v[116:119],v4 offset:32
ds_read_b64_tr_b16 v[72:73],v1 offset:16416
v_fma_mixhi_f16 v120,v158,s99,0
v_fma_mixhi_f16 v122,v159,s99,0
v_mfma_f32_16x16x32_bf16 v[164:167],v[88:91],v[136:139],v[164:167]
ds_read_u16_d16_hi v147,v147
v_lshlrev_b32_sdwa v149,1,v149 dst_sel:DWORD dst_unused:UNUSED_PAD src0_sel:DWORD src1_sel:WORD_1
v_lshlrev_b32_sdwa v151,1,v151 dst_sel:DWORD dst_unused:UNUSED_PAD src0_sel:DWORD src1_sel:WORD_1
v_mfma_f32_16x16x32_bf16 v[172:175],v[92:95],v[136:139],v[172:175]
ds_read_b64_tr_b16 v[74:75],v1 offset:18464
ds_read_b64_tr_b16 v[76:77],v2 offset:16416
v_pk_fma_f16 v121,v120,v120,s54
v_pk_fma_f16 v123,v122,v122,s54
v_mfma_f32_16x16x32_bf16 v[180:183],v[96:99],v[136:139],v[180:183]
ds_read_u16 v148,v148
v_pk_mul_f16 v121,v120,v121
v_pk_mul_f16 v123,v122,v123
v_mfma_f32_16x16x32_bf16 v[188:191],v[100:103],v[136:139],v[188:191]
ds_read_b64_tr_b16 v[78:79],v2 offset:18464
v_lshlrev_b32_sdwa v152,1,v152 dst_sel:DWORD dst_unused:UNUSED_PAD src0_sel:DWORD src1_sel:WORD_1
v_lshlrev_b32_sdwa v154,1,v154 dst_sel:DWORD dst_unused:UNUSED_PAD src0_sel:DWORD src1_sel:WORD_1
v_mfma_f32_16x16x32_bf16 v[196:199],v[104:107],v[136:139],v[196:199]
ds_read_u16_d16_hi v150,v150
v_exp_f16 v120,v121
v_mfma_f32_16x16x32_bf16 v[204:207],v[108:111],v[136:139],v[204:207]
s_waitcnt lgkmcnt(12)
ds_read_b64_tr_b16 v[80:81],v1 offset:20512
v_exp_f16 v122,v123
v_mfma_f32_16x16x32_bf16 v[128:131],v[64:67],v[16:19],v[112:115]
ds_read_u16 v149,v149
v_lshlrev_b32_sdwa v153,1,v153 dst_sel:DWORD dst_unused:UNUSED_PAD src0_sel:DWORD src1_sel:WORD_1
v_lshlrev_b32_sdwa v155,1,v155 dst_sel:DWORD dst_unused:UNUSED_PAD src0_sel:DWORD src1_sel:WORD_1
v_mfma_f32_16x16x32_bf16 v[136:139],v[64:67],v[20:23],v[112:115]
s_waitcnt lgkmcnt(9)
ds_read_b64_tr_b16 v[82:83],v1 offset:22560
v_exp_f16_sdwa v120,v121 dst_sel:WORD_1 dst_unused:UNUSED_PRESERVE src0_sel:WORD_1
v_mfma_f32_16x16x32_bf16 v[140:143],v[68:71],v[20:23],v[116:119]
s_cbranch_vccnz RA28_%=
s_mov_b64 exec,s[62:63]
s_add_u32 m0,s57,27904
s_add_u32 s64,s56,4718592
buffer_load_dwordx4 v5,s[36:39],s64 offen lds
s_add_u32 m0,s57,29632
s_nop 0
buffer_load_dwordx4 v6,s[36:39],s64 offen lds
s_mov_b64 exec,-1
RA28_%=:
ds_read_u16_d16_hi v151,v151
v_exp_f16_sdwa v122,v123 dst_sel:WORD_1 dst_unused:UNUSED_PRESERVE src0_sel:WORD_1
v_mfma_f32_16x16x32_bf16 v[132:135],v[68:71],v[16:19],v[116:119]
s_waitcnt lgkmcnt(8)
ds_read_b64_tr_b16 v[84:85],v2 offset:20512
v_pk_fma_f16 v120,v120,s55,s55
v_pk_fma_f16 v122,v122,s55,s55
v_mfma_f32_16x16x32_bf16 v[128:131],v[72:75],v[24:27],v[128:131]
ds_read_u16 v152,v152
v_rcp_f16 v121,v120
v_mfma_f32_16x16x32_bf16 v[136:139],v[72:75],v[28:31],v[136:139]
s_waitcnt lgkmcnt(7)
ds_read_b64_tr_b16 v[86:87],v2 offset:22560
v_rcp_f16 v123,v122
v_mfma_f32_16x16x32_bf16 v[140:143],v[76:79],v[28:31],v[140:143]
ds_read_u16_d16_hi v154,v154
v_rcp_f16_sdwa v121,v120 dst_sel:WORD_1 dst_unused:UNUSED_PRESERVE src0_sel:WORD_1
v_mfma_f32_16x16x32_bf16 v[132:135],v[76:79],v[24:27],v[132:135]
s_waitcnt lgkmcnt(5)
v_rcp_f16_sdwa v123,v122 dst_sel:WORD_1 dst_unused:UNUSED_PRESERVE src0_sel:WORD_1
v_mfma_f32_16x16x32_bf16 v[128:131],v[80:83],v[32:35],v[128:131]
ds_read_u16 v153,v153
v_fma_mix_f32 v156,v156,v121,0 op_sel_hi:[0,1,0]
v_fma_mix_f32 v158,v158,v121,0 op_sel:[0,1,0] op_sel_hi:[0,1,0]
v_mfma_f32_16x16x32_bf16 v[136:139],v[80:83],v[36:39],v[136:139]
s_waitcnt lgkmcnt(2)
v_lshl_add_u64 v[144:145],v[144:145],0,v[146:147]
v_lshl_add_u64 v[146:147],v[148:149],0,v[150:151]
v_mfma_f32_16x16x32_bf16 v[140:143],v[84:87],v[36:39],v[140:143]
ds_read_u16_d16_hi v155,v155
v_fma_mix_f32 v157,v157,v123,0 op_sel_hi:[0,1,0]
v_fma_mix_f32 v159,v159,v123,0 op_sel:[0,1,0] op_sel_hi:[0,1,0]
v_mfma_f32_16x16x32_bf16 v[132:135],v[84:87],v[32:35],v[132:135]
v_lshlrev_b32_sdwa v128,1,v128 dst_sel:DWORD dst_unused:UNUSED_PAD src0_sel:DWORD src1_sel:WORD_1
v_lshlrev_b32_sdwa v130,1,v130 dst_sel:DWORD dst_unused:UNUSED_PAD src0_sel:DWORD src1_sel:WORD_1
v_mfma_f32_16x16x32_bf16 v[208:211],v[88:91],v[144:147],v[208:211]
ds_read_u16 v128,v128
v_lshlrev_b32_sdwa v129,1,v129 dst_sel:DWORD dst_unused:UNUSED_PAD src0_sel:DWORD src1_sel:WORD_1
v_lshlrev_b32_sdwa v131,1,v131 dst_sel:DWORD dst_unused:UNUSED_PAD src0_sel:DWORD src1_sel:WORD_1
v_mfma_f32_16x16x32_bf16 v[216:219],v[92:95],v[144:147],v[216:219]
v_fma_mixlo_f16 v120,v140,s99,0
v_fma_mixlo_f16 v122,v141,s99,0
v_mfma_f32_16x16x32_bf16 v[224:227],v[96:99],v[144:147],v[224:227]
s_setprio 2
ds_read_u16_d16_hi v130,v130
v_lshlrev_b32_sdwa v132,1,v132 dst_sel:DWORD dst_unused:UNUSED_PAD src0_sel:DWORD src1_sel:WORD_1
v_lshlrev_b32_sdwa v134,1,v134 dst_sel:DWORD dst_unused:UNUSED_PAD src0_sel:DWORD src1_sel:WORD_1
v_mfma_f32_16x16x32_bf16 v[232:235],v[100:103],v[144:147],v[232:235]
s_waitcnt lgkmcnt(2)
v_lshl_add_u64 v[152:153],v[152:153],0,v[154:155]
v_mfma_f32_16x16x32_bf16 v[240:243],v[104:107],v[144:147],v[240:243]
ds_read_u16 v129,v129
v_cvt_pk_bf16_f32 v154,v156,v158
v_cvt_pk_bf16_f32 v155,v157,v159
v_mfma_f32_16x16x32_bf16 v[248:251],v[108:111],v[144:147],v[248:251]
v_fma_mixhi_f16 v120,v142,s99,0
v_fma_mixhi_f16 v122,v143,s99,0
v_mfma_f32_16x16x32_bf16 v[212:215],v[88:91],v[152:155],v[212:215]
ds_read_u16_d16_hi v131,v131
v_lshlrev_b32_sdwa v133,1,v133 dst_sel:DWORD dst_unused:UNUSED_PAD src0_sel:DWORD src1_sel:WORD_1
v_lshlrev_b32_sdwa v135,1,v135 dst_sel:DWORD dst_unused:UNUSED_PAD src0_sel:DWORD src1_sel:WORD_1
v_mfma_f32_16x16x32_bf16 v[220:223],v[92:95],v[152:155],v[220:223]
s_cbranch_vccnz RA29_%=
s_mov_b64 exec,s[62:63]
s_add_u32 m0,s57,31392
s_add_u32 s64,s56,4816896
buffer_load_dwordx4 v5,s[36:39],s64 offen lds
s_add_u32 m0,s57,33120
s_nop 0
buffer_load_dwordx4 v6,s[36:39],s64 offen lds
s_mov_b64 exec,-1
RA29_%=:
ds_read_b64_tr_b16 v[88:89],v3 offset:18480
v_pk_fma_f16 v121,v120,v120,s54
v_pk_fma_f16 v123,v122,v122,s54
v_mfma_f32_16x16x32_bf16 v[228:231],v[96:99],v[152:155],v[228:231]
ds_read_u16 v132,v132
v_pk_mul_f16 v121,v120,v121
v_pk_mul_f16 v123,v122,v123
v_mfma_f32_16x16x32_bf16 v[236:239],v[100:103],v[152:155],v[236:239]
ds_read_b64_tr_b16 v[90:91],v3 offset:20016
v_lshlrev_b32_sdwa v136,1,v136 dst_sel:DWORD dst_unused:UNUSED_PAD src0_sel:DWORD src1_sel:WORD_1
v_lshlrev_b32_sdwa v138,1,v138 dst_sel:DWORD dst_unused:UNUSED_PAD src0_sel:DWORD src1_sel:WORD_1
v_mfma_f32_16x16x32_bf16 v[244:247],v[104:107],v[152:155],v[244:247]
ds_read_u16_d16_hi v134,v134
v_exp_f16 v120,v121
v_mfma_f32_16x16x32_bf16 v[252:255],v[108:111],v[152:155],v[252:255]
ds_read_b64_tr_b16 v[92:93],v3 offset:18488
v_exp_f16 v122,v123
v_mfma_f32_16x16x32_bf16 v[144:147],v[64:67],v[40:43],v[112:115]
ds_read_u16 v133,v133
v_lshlrev_b32_sdwa v137,1,v137 dst_sel:DWORD dst_unused:UNUSED_PAD src0_sel:DWORD src1_sel:WORD_1
v_lshlrev_b32_sdwa v139,1,v139 dst_sel:DWORD dst_unused:UNUSED_PAD src0_sel:DWORD src1_sel:WORD_1
v_mfma_f32_16x16x32_bf16 v[152:155],v[64:67],v[44:47],v[112:115]
ds_read_b64_tr_b16 v[94:95],v3 offset:20024
v_exp_f16_sdwa v120,v121 dst_sel:WORD_1 dst_unused:UNUSED_PRESERVE src0_sel:WORD_1
v_mfma_f32_16x16x32_bf16 v[156:159],v[68:71],v[44:47],v[116:119]
ds_read_u16_d16_hi v135,v135
v_exp_f16_sdwa v122,v123 dst_sel:WORD_1 dst_unused:UNUSED_PRESERVE src0_sel:WORD_1
v_mfma_f32_16x16x32_bf16 v[148:151],v[68:71],v[40:43],v[116:119]
ds_read_b64_tr_b16 v[96:97],v3 offset:18544
v_pk_fma_f16 v120,v120,s55,s55
v_pk_fma_f16 v122,v122,s55,s55
v_mfma_f32_16x16x32_bf16 v[144:147],v[72:75],v[48:51],v[144:147]
ds_read_u16 v136,v136
v_rcp_f16 v121,v120
v_mfma_f32_16x16x32_bf16 v[152:155],v[72:75],v[52:55],v[152:155]
ds_read_b64_tr_b16 v[98:99],v3 offset:20080
ds_read_b64_tr_b16 v[100:101],v3 offset:18552
v_rcp_f16 v123,v122
v_mfma_f32_16x16x32_bf16 v[156:159],v[76:79],v[52:55],v[156:159]
ds_read_u16_d16_hi v138,v138
v_rcp_f16_sdwa v121,v120 dst_sel:WORD_1 dst_unused:UNUSED_PRESERVE src0_sel:WORD_1
v_mfma_f32_16x16x32_bf16 v[148:151],v[76:79],v[48:51],v[148:151]
ds_read_b64_tr_b16 v[102:103],v3 offset:20088
ds_read_b64_tr_b16 v[104:105],v3 offset:18608
v_rcp_f16_sdwa v123,v122 dst_sel:WORD_1 dst_unused:UNUSED_PRESERVE src0_sel:WORD_1
v_mfma_f32_16x16x32_bf16 v[144:147],v[80:83],v[56:59],v[144:147]
ds_read_u16 v137,v137
v_fma_mix_f32 v140,v140,v121,0 op_sel_hi:[0,1,0]
v_fma_mix_f32 v142,v142,v121,0 op_sel:[0,1,0] op_sel_hi:[0,1,0]
v_mfma_f32_16x16x32_bf16 v[152:155],v[80:83],v[60:63],v[152:155]
s_waitcnt lgkmcnt(8)
ds_read_b64_tr_b16 v[106:107],v3 offset:20144
ds_read_b64_tr_b16 v[108:109],v3 offset:18616
v_lshl_add_u64 v[128:129],v[128:129],0,v[130:131]
v_lshl_add_u64 v[130:131],v[132:133],0,v[134:135]
v_mfma_f32_16x16x32_bf16 v[156:159],v[84:87],v[60:63],v[156:159]
ds_read_u16_d16_hi v139,v139
v_fma_mix_f32 v141,v141,v123,0 op_sel_hi:[0,1,0]
v_fma_mix_f32 v143,v143,v123,0 op_sel:[0,1,0] op_sel_hi:[0,1,0]
v_mfma_f32_16x16x32_bf16 v[148:151],v[84:87],v[56:59],v[148:151]
ds_read_b64_tr_b16 v[110:111],v3 offset:20152
ds_read_b128 v[64:67],v4 offset:1536
v_lshlrev_b32_sdwa v144,1,v144 dst_sel:DWORD dst_unused:UNUSED_PAD src0_sel:DWORD src1_sel:WORD_1
v_lshlrev_b32_sdwa v146,1,v146 dst_sel:DWORD dst_unused:UNUSED_PAD src0_sel:DWORD src1_sel:WORD_1
v_mfma_f32_16x16x32_bf16 v[160:163],v[88:91],v[128:131],v[160:163]
ds_read_u16 v144,v144
ds_read_b128 v[68:71],v4 offset:1568
v_lshlrev_b32_sdwa v145,1,v145 dst_sel:DWORD dst_unused:UNUSED_PAD src0_sel:DWORD src1_sel:WORD_1
v_lshlrev_b32_sdwa v147,1,v147 dst_sel:DWORD dst_unused:UNUSED_PAD src0_sel:DWORD src1_sel:WORD_1
v_fma_mixlo_f16 v120,v156,s99,0
v_fma_mixlo_f16 v122,v157,s99,0
v_mfma_f32_16x16x32_bf16 v[168:171],v[92:95],v[128:131],v[168:171]
s_waitcnt lgkmcnt(12)
ds_read_u16_d16_hi v146,v146
ds_read_b128 v[72:75],v4 offset:1664
v_lshlrev_b32_sdwa v148,1,v148 dst_sel:DWORD dst_unused:UNUSED_PAD src0_sel:DWORD src1_sel:WORD_1
v_lshlrev_b32_sdwa v150,1,v150 dst_sel:DWORD dst_unused:UNUSED_PAD src0_sel:DWORD src1_sel:WORD_1
s_waitcnt lgkmcnt(6)
v_lshl_add_u64 v[136:137],v[136:137],0,v[138:139]
v_cvt_pk_bf16_f32 v138,v140,v142
v_mfma_f32_16x16x32_bf16 v[176:179],v[96:99],v[128:131],v[176:179]
s_setprio 1
ds_read_u16 v145,v145
ds_read_b128 v[76:79],v4 offset:1696
v_cvt_pk_bf16_f32 v139,v141,v143
v_fma_mixhi_f16 v120,v158,s99,0
v_fma_mixhi_f16 v122,v159,s99,0
v_lshlrev_b32_sdwa v149,1,v149 dst_sel:DWORD dst_unused:UNUSED_PAD src0_sel:DWORD src1_sel:WORD_1
v_mfma_f32_16x16x32_bf16 v[184:187],v[100:103],v[128:131],v[184:187]
ds_read_u16_d16_hi v147,v147
ds_read_b128 v[80:83],v4 offset:1792
v_lshlrev_b32_sdwa v151,1,v151 dst_sel:DWORD dst_unused:UNUSED_PAD src0_sel:DWORD src1_sel:WORD_1
v_pk_fma_f16 v121,v120,v120,s54
v_pk_fma_f16 v123,v122,v122,s54
v_pk_mul_f16 v121,v120,v121
v_mfma_f32_16x16x32_bf16 v[192:195],v[104:107],v[128:131],v[192:195]
s_waitcnt lgkmcnt(9)
ds_read_u16 v148,v148
ds_read_b128 v[84:87],v4 offset:1824
v_pk_mul_f16 v123,v122,v123
v_lshlrev_b32_sdwa v152,1,v152 dst_sel:DWORD dst_unused:UNUSED_PAD src0_sel:DWORD src1_sel:WORD_1
v_lshlrev_b32_sdwa v154,1,v154 dst_sel:DWORD dst_unused:UNUSED_PAD src0_sel:DWORD src1_sel:WORD_1
v_exp_f16 v120,v121
v_mfma_f32_16x16x32_bf16 v[200:203],v[108:111],v[128:131],v[200:203]
ds_read_u16_d16_hi v150,v150
v_exp_f16 v122,v123
v_lshlrev_b32_sdwa v153,1,v153 dst_sel:DWORD dst_unused:UNUSED_PAD src0_sel:DWORD src1_sel:WORD_1
v_mfma_f32_16x16x32_bf16 v[164:167],v[88:91],v[136:139],v[164:167]
ds_read_u16 v149,v149
v_lshlrev_b32_sdwa v155,1,v155 dst_sel:DWORD dst_unused:UNUSED_PAD src0_sel:DWORD src1_sel:WORD_1
v_exp_f16_sdwa v120,v121 dst_sel:WORD_1 dst_unused:UNUSED_PRESERVE src0_sel:WORD_1
v_exp_f16_sdwa v122,v123 dst_sel:WORD_1 dst_unused:UNUSED_PRESERVE src0_sel:WORD_1
v_mfma_f32_16x16x32_bf16 v[172:175],v[92:95],v[136:139],v[172:175]
ds_read_u16_d16_hi v151,v151
v_pk_fma_f16 v120,v120,s55,s55
v_pk_fma_f16 v122,v122,s55,s55
v_rcp_f16 v121,v120
v_mfma_f32_16x16x32_bf16 v[180:183],v[96:99],v[136:139],v[180:183]
ds_read_u16 v152,v152
v_rcp_f16 v123,v122
v_rcp_f16_sdwa v121,v120 dst_sel:WORD_1 dst_unused:UNUSED_PRESERVE src0_sel:WORD_1
v_mfma_f32_16x16x32_bf16 v[188:191],v[100:103],v[136:139],v[188:191]
ds_read_u16_d16_hi v154,v154
v_rcp_f16_sdwa v123,v122 dst_sel:WORD_1 dst_unused:UNUSED_PRESERVE src0_sel:WORD_1
v_fma_mix_f32 v156,v156,v121,0 op_sel_hi:[0,1,0]
v_mfma_f32_16x16x32_bf16 v[196:199],v[104:107],v[136:139],v[196:199]
s_setprio 0
ds_read_u16 v153,v153
v_fma_mix_f32 v158,v158,v121,0 op_sel:[0,1,0] op_sel_hi:[0,1,0]
s_waitcnt lgkmcnt(9)
v_lshl_add_u64 v[144:145],v[144:145],0,v[146:147]
s_waitcnt lgkmcnt(3)
v_lshl_add_u64 v[146:147],v[148:149],0,v[150:151]
v_fma_mix_f32 v157,v157,v123,0 op_sel_hi:[0,1,0]
v_mfma_f32_16x16x32_bf16 v[204:207],v[108:111],v[136:139],v[204:207]
ds_read_u16_d16_hi v155,v155
v_fma_mix_f32 v159,v159,v123,0 op_sel:[0,1,0] op_sel_hi:[0,1,0]
s_waitcnt lgkmcnt(0)
v_lshl_add_u64 v[152:153],v[152:153],0,v[154:155]
v_cvt_pk_bf16_f32 v154,v156,v158
v_cvt_pk_bf16_f32 v155,v157,v159
v_mfma_f32_16x16x32_bf16 v[208:211],v[88:91],v[144:147],v[208:211]
s_cbranch_vccz RA30_%=
s_waitcnt vmcnt(0)
RA30_%=:
s_barrier
s_setprio 0
v_xor_b32 v9,64,v10
v_mul_f32 v160,v160,v64
v_mul_f32 v161,v161,v65
v_mul_f32 v162,v162,v66
v_mul_f32 v163,v163,v67
v_mfma_f32_16x16x32_bf16 v[216:219],v[92:95],v[144:147],v[216:219]
v_mul_f32 v168,v168,v68
v_mul_f32 v169,v169,v69
v_mul_f32 v170,v170,v70
v_mul_f32 v171,v171,v71
v_cvt_pk_bf16_f32 v16,v160,v161
v_mfma_f32_16x16x32_bf16 v[224:227],v[96:99],v[144:147],v[224:227]
v_cvt_pk_bf16_f32 v17,v162,v163
v_cvt_pk_bf16_f32 v18,v168,v169
v_cvt_pk_bf16_f32 v19,v170,v171
v_mul_f32_dpp v176,v176,v72 quad_perm:[1,0,3,2] row_mask:0xf bank_mask:0xf
v_mfma_f32_16x16x32_bf16 v[232:235],v[100:103],v[144:147],v[232:235]
v_mul_f32_dpp v177,v177,v73 quad_perm:[1,0,3,2] row_mask:0xf bank_mask:0xf
v_mul_f32_dpp v178,v178,v74 quad_perm:[1,0,3,2] row_mask:0xf bank_mask:0xf
v_mul_f32_dpp v179,v179,v75 quad_perm:[1,0,3,2] row_mask:0xf bank_mask:0xf
v_mul_f32_dpp v184,v184,v76 quad_perm:[1,0,3,2] row_mask:0xf bank_mask:0xf
v_mul_f32_dpp v185,v185,v77 quad_perm:[1,0,3,2] row_mask:0xf bank_mask:0xf
v_mfma_f32_16x16x32_bf16 v[240:243],v[104:107],v[144:147],v[240:243]
v_mul_f32_dpp v186,v186,v78 quad_perm:[1,0,3,2] row_mask:0xf bank_mask:0xf
v_mul_f32_dpp v187,v187,v79 quad_perm:[1,0,3,2] row_mask:0xf bank_mask:0xf
v_cvt_pk_bf16_f32 v24,v176,v177
v_cvt_pk_bf16_f32 v25,v178,v179
v_mfma_f32_16x16x32_bf16 v[248:251],v[108:111],v[144:147],v[248:251]
v_cvt_pk_bf16_f32 v26,v184,v185
v_cvt_pk_bf16_f32 v27,v186,v187
s_mov_b64 exec,s[12:13]
v_swap_b32 v16,v24
v_swap_b32 v17,v25
v_swap_b32 v18,v26
v_swap_b32 v19,v27
s_mov_b64 exec,-1
v_mfma_f32_16x16x32_bf16 v[212:215],v[88:91],v[152:155],v[212:215]
s_add_u32 s65,s61,0
buffer_store_dwordx4 v[16:19],v10,s[40:43],s65 offen offset:0 sc1
v_mfma_f32_16x16x32_bf16 v[220:223],v[92:95],v[152:155],v[220:223]
v_mul_f32 v192,v192,v80
v_mul_f32 v193,v193,v81
v_mul_f32 v194,v194,v82
v_mul_f32 v195,v195,v83
v_mfma_f32_16x16x32_bf16 v[228:231],v[96:99],v[152:155],v[228:231]
v_mul_f32 v200,v200,v84
v_mul_f32 v201,v201,v85
v_mul_f32 v202,v202,v86
v_mul_f32 v203,v203,v87
v_mfma_f32_16x16x32_bf16 v[236:239],v[100:103],v[152:155],v[236:239]
v_cvt_pk_bf16_f32 v32,v192,v193
v_cvt_pk_bf16_f32 v33,v194,v195
v_cvt_pk_bf16_f32 v34,v200,v201
v_cvt_pk_bf16_f32 v35,v202,v203
v_mul_f32_dpp v164,v164,v64 quad_perm:[1,0,3,2] row_mask:0xf bank_mask:0xf
v_mfma_f32_16x16x32_bf16 v[244:247],v[104:107],v[152:155],v[244:247]
v_mul_f32_dpp v165,v165,v65 quad_perm:[1,0,3,2] row_mask:0xf bank_mask:0xf
v_mul_f32_dpp v166,v166,v66 quad_perm:[1,0,3,2] row_mask:0xf bank_mask:0xf
v_mul_f32_dpp v167,v167,v67 quad_perm:[1,0,3,2] row_mask:0xf bank_mask:0xf
v_mul_f32_dpp v172,v172,v68 quad_perm:[1,0,3,2] row_mask:0xf bank_mask:0xf
v_mfma_f32_16x16x32_bf16 v[252:255],v[108:111],v[152:155],v[252:255]
s_add_u32 s65,s61,0
buffer_store_dwordx4 v[24:27],v9,s[40:43],s65 offen offset:384 sc1
v_mul_f32_dpp v173,v173,v69 quad_perm:[1,0,3,2] row_mask:0xf bank_mask:0xf
v_mul_f32_dpp v174,v174,v70 quad_perm:[1,0,3,2] row_mask:0xf bank_mask:0xf
v_mul_f32_dpp v175,v175,v71 quad_perm:[1,0,3,2] row_mask:0xf bank_mask:0xf
v_cvt_pk_bf16_f32 v20,v164,v165
v_cvt_pk_bf16_f32 v21,v166,v167
v_cvt_pk_bf16_f32 v22,v172,v173
v_cvt_pk_bf16_f32 v23,v174,v175
s_mov_b64 exec,s[12:13]
v_swap_b32 v32,v20
v_swap_b32 v33,v21
v_swap_b32 v34,v22
v_swap_b32 v35,v23
s_mov_b64 exec,-1
v_mul_f32 v180,v180,v72
v_mul_f32 v181,v181,v73
v_mul_f32 v182,v182,v74
v_mul_f32 v183,v183,v75
v_mul_f32 v188,v188,v76
v_mul_f32 v189,v189,v77
s_add_u32 s65,s61,0
buffer_store_dwordx4 v[32:35],v10,s[40:43],s65 offen offset:128 sc1
v_mul_f32 v190,v190,v78
v_mul_f32 v191,v191,v79
v_cvt_pk_bf16_f32 v28,v180,v181
v_cvt_pk_bf16_f32 v29,v182,v183
v_cvt_pk_bf16_f32 v30,v188,v189
v_cvt_pk_bf16_f32 v31,v190,v191
v_mul_f32_dpp v196,v196,v80 quad_perm:[1,0,3,2] row_mask:0xf bank_mask:0xf
v_mul_f32_dpp v197,v197,v81 quad_perm:[1,0,3,2] row_mask:0xf bank_mask:0xf
v_mul_f32_dpp v198,v198,v82 quad_perm:[1,0,3,2] row_mask:0xf bank_mask:0xf
v_mul_f32_dpp v199,v199,v83 quad_perm:[1,0,3,2] row_mask:0xf bank_mask:0xf
v_mul_f32_dpp v204,v204,v84 quad_perm:[1,0,3,2] row_mask:0xf bank_mask:0xf
v_mul_f32_dpp v205,v205,v85 quad_perm:[1,0,3,2] row_mask:0xf bank_mask:0xf
v_mul_f32_dpp v206,v206,v86 quad_perm:[1,0,3,2] row_mask:0xf bank_mask:0xf
v_mul_f32_dpp v207,v207,v87 quad_perm:[1,0,3,2] row_mask:0xf bank_mask:0xf
v_cvt_pk_bf16_f32 v36,v196,v197
v_cvt_pk_bf16_f32 v37,v198,v199
v_cvt_pk_bf16_f32 v38,v204,v205
v_cvt_pk_bf16_f32 v39,v206,v207
s_mov_b64 exec,s[12:13]
v_swap_b32 v28,v36
v_swap_b32 v29,v37
v_swap_b32 v30,v38
v_swap_b32 v31,v39
s_mov_b64 exec,-1
s_add_u32 s65,s61,0
buffer_store_dwordx4 v[20:23],v9,s[40:43],s65 offen offset:512 sc1
v_mul_f32 v208,v208,v64
v_mul_f32 v209,v209,v65
v_mul_f32 v210,v210,v66
v_mul_f32 v211,v211,v67
v_mul_f32 v216,v216,v68
v_mul_f32 v217,v217,v69
v_mul_f32 v218,v218,v70
v_mul_f32 v219,v219,v71
v_cvt_pk_bf16_f32 v40,v208,v209
v_cvt_pk_bf16_f32 v41,v210,v211
v_cvt_pk_bf16_f32 v42,v216,v217
v_cvt_pk_bf16_f32 v43,v218,v219
v_mul_f32_dpp v224,v224,v72 quad_perm:[1,0,3,2] row_mask:0xf bank_mask:0xf
v_mul_f32_dpp v225,v225,v73 quad_perm:[1,0,3,2] row_mask:0xf bank_mask:0xf
v_mul_f32_dpp v226,v226,v74 quad_perm:[1,0,3,2] row_mask:0xf bank_mask:0xf
v_mul_f32_dpp v227,v227,v75 quad_perm:[1,0,3,2] row_mask:0xf bank_mask:0xf
v_mul_f32_dpp v232,v232,v76 quad_perm:[1,0,3,2] row_mask:0xf bank_mask:0xf
v_mul_f32_dpp v233,v233,v77 quad_perm:[1,0,3,2] row_mask:0xf bank_mask:0xf
v_mul_f32_dpp v234,v234,v78 quad_perm:[1,0,3,2] row_mask:0xf bank_mask:0xf
v_mul_f32_dpp v235,v235,v79 quad_perm:[1,0,3,2] row_mask:0xf bank_mask:0xf
v_cvt_pk_bf16_f32 v48,v224,v225
v_cvt_pk_bf16_f32 v49,v226,v227
v_cvt_pk_bf16_f32 v50,v232,v233
v_cvt_pk_bf16_f32 v51,v234,v235
s_add_u32 s65,s61,0
buffer_store_dwordx4 v[28:31],v10,s[40:43],s65 offen offset:256 sc1
s_mov_b64 exec,s[12:13]
v_swap_b32 v40,v48
v_swap_b32 v41,v49
v_swap_b32 v42,v50
v_swap_b32 v43,v51
s_mov_b64 exec,-1
v_mul_f32 v240,v240,v80
v_mul_f32 v241,v241,v81
v_mul_f32 v242,v242,v82
v_mul_f32 v243,v243,v83
v_mul_f32 v248,v248,v84
v_mul_f32 v249,v249,v85
v_mul_f32 v250,v250,v86
v_mul_f32 v251,v251,v87
v_cvt_pk_bf16_f32 v56,v240,v241
v_cvt_pk_bf16_f32 v57,v242,v243
v_cvt_pk_bf16_f32 v58,v248,v249
v_cvt_pk_bf16_f32 v59,v250,v251
v_mul_f32_dpp v212,v212,v64 quad_perm:[1,0,3,2] row_mask:0xf bank_mask:0xf
v_mul_f32_dpp v213,v213,v65 quad_perm:[1,0,3,2] row_mask:0xf bank_mask:0xf
v_mul_f32_dpp v214,v214,v66 quad_perm:[1,0,3,2] row_mask:0xf bank_mask:0xf
v_mul_f32_dpp v215,v215,v67 quad_perm:[1,0,3,2] row_mask:0xf bank_mask:0xf
v_mul_f32_dpp v220,v220,v68 quad_perm:[1,0,3,2] row_mask:0xf bank_mask:0xf
s_add_u32 s65,s61,0
buffer_store_dwordx4 v[36:39],v9,s[40:43],s65 offen offset:640 sc1
v_mul_f32_dpp v221,v221,v69 quad_perm:[1,0,3,2] row_mask:0xf bank_mask:0xf
v_mul_f32_dpp v222,v222,v70 quad_perm:[1,0,3,2] row_mask:0xf bank_mask:0xf
v_mul_f32_dpp v223,v223,v71 quad_perm:[1,0,3,2] row_mask:0xf bank_mask:0xf
v_cvt_pk_bf16_f32 v44,v212,v213
v_cvt_pk_bf16_f32 v45,v214,v215
v_cvt_pk_bf16_f32 v46,v220,v221
v_cvt_pk_bf16_f32 v47,v222,v223
s_mov_b64 exec,s[12:13]
v_swap_b32 v56,v44
v_swap_b32 v57,v45
v_swap_b32 v58,v46
v_swap_b32 v59,v47
s_mov_b64 exec,-1
v_mul_f32 v228,v228,v72
v_mul_f32 v229,v229,v73
s_mov_b32 s86,0xbe801d5a
v_mul_f32 v230,v230,v74
v_mul_f32 v231,v231,v75
v_mul_f32 v236,v236,v76
v_mul_f32 v237,v237,v77
v_mul_f32 v238,v238,v78
v_mul_f32 v239,v239,v79
v_cvt_pk_bf16_f32 v52,v228,v229
v_cvt_pk_bf16_f32 v53,v230,v231
s_add_u32 s65,s61,98304
buffer_store_dwordx4 v[40:43],v10,s[40:43],s65 offen offset:0 sc1
v_cvt_pk_bf16_f32 v54,v236,v237
v_cvt_pk_bf16_f32 v55,v238,v239
v_mul_f32_dpp v244,v244,v80 quad_perm:[1,0,3,2] row_mask:0xf bank_mask:0xf
v_mul_f32_dpp v245,v245,v81 quad_perm:[1,0,3,2] row_mask:0xf bank_mask:0xf
v_mul_f32_dpp v246,v246,v82 quad_perm:[1,0,3,2] row_mask:0xf bank_mask:0xf
v_mul_f32_dpp v247,v247,v83 quad_perm:[1,0,3,2] row_mask:0xf bank_mask:0xf
v_mul_f32_dpp v252,v252,v84 quad_perm:[1,0,3,2] row_mask:0xf bank_mask:0xf
v_mul_f32_dpp v253,v253,v85 quad_perm:[1,0,3,2] row_mask:0xf bank_mask:0xf
v_mul_f32_dpp v254,v254,v86 quad_perm:[1,0,3,2] row_mask:0xf bank_mask:0xf
v_mul_f32_dpp v255,v255,v87 quad_perm:[1,0,3,2] row_mask:0xf bank_mask:0xf
v_cvt_pk_bf16_f32 v60,v244,v245
v_cvt_pk_bf16_f32 v61,v246,v247
v_cvt_pk_bf16_f32 v62,v252,v253
v_cvt_pk_bf16_f32 v63,v254,v255
s_mov_b64 exec,s[12:13]
v_swap_b32 v52,v60
v_swap_b32 v53,v61
v_swap_b32 v54,v62
v_swap_b32 v55,v63
s_mov_b64 exec,-1
s_cbranch_vccnz RA32_%=
v_and_b32 v112,63,%1
s_bfe_u32 s64,%0,0x20003
s_lshl_b32 s64,s64,13
s_bitcmp1_b32 s52,0
s_mov_b32 s66,28672
s_cselect_b32 s66,69632,s66
s_add_u32 s64,s64,s66
v_lshl_add_u32 v112,v112,7,s64
s_getpc_b64 s[14:15]
N_pgA31_%=:
s_sub_u32 s14,s14,N_pgA31_%=-N_main_%=
s_subb_u32 s15,s15,0
s_mov_b32 m0,157312
v_min_u32 v112,N_mainend_%=-N_main_%=,v112
global_load_lds_dword v112,s[14:15]
RA32_%=:
s_add_u32 s4,s10,12
s_cmp_ge_u32 s4,20
s_cselect_b32 s7,20,0
s_sub_u32 s4,s4,s7
s_mul_i32 s4,s4,3488
s_add_u32 s5,s10,14
s_cmp_ge_u32 s5,20
s_cselect_b32 s7,20,0
s_sub_u32 s5,s5,s7
s_mul_i32 s5,s5,3488
s_add_u32 s6,s10,16
s_cmp_ge_u32 s6,20
s_cselect_b32 s7,20,0
s_sub_u32 s6,s6,s7
s_mul_i32 s6,s6,3488
v_add_u32 v112,s4,v15
v_add_u32 v116,s5,v15
s_mov_b64 exec,s[8:9]
v_add_u32 v112,s5,v15
v_add_u32 v116,s6,v15
s_mov_b64 exec,-1
v_xor_b32 v113,16,v112
v_xor_b32 v117,16,v116
v_xor_b32 v114,32,v112
v_xor_b32 v118,32,v116
v_xor_b32 v115,48,v112
v_xor_b32 v119,48,v116
s_waitcnt vmcnt(8)
s_barrier
s_setprio 3
ds_read_b128 v[136:139],v116 offset:0
ds_read_b128 v[88:91],v11 offset:768
ds_read_b128 v[140:143],v116 offset:3488
ds_read_b128 v[128:131],v112 offset:0
ds_read_b128 v[132:135],v112 offset:3488
ds_read_b128 v[92:95],v12 offset:0
ds_read_b128 v[152:155],v117 offset:0
ds_read_b128 v[96:99],v11 offset:1920
ds_read_b128 v[156:159],v117 offset:3488
ds_read_b128 v[144:147],v113 offset:0
ds_read_b128 v[148:151],v113 offset:3488
ds_read_b128 v[100:103],v12 offset:1152
s_waitcnt lgkmcnt(10)
v_mfma_f32_16x16x32_bf16 v[160:163],v[88:91],v[136:139],0
s_waitcnt lgkmcnt(9)
s_add_u32 s65,s61,98304
buffer_store_dwordx4 v[48:51],v9,s[40:43],s65 offen offset:384 sc1
v_mfma_f32_16x16x32_bf16 v[184:187],v[88:91],v[140:143],0
s_waitcnt lgkmcnt(6)
ds_read_b128 v[140:143],v116 offset:3552
ds_read_b128 v[88:91],v11 offset:5376
v_smfmac_f32_16x16x64_bf16 v[160:163],v[92:95],v[128:135],v13
ds_read_b128 v[128:131],v112 offset:64
v_smfmac_f32_16x16x64_bf16 v[184:187],v[92:95],v[132:139],v13
s_waitcnt lgkmcnt(7)
ds_read_b128 v[136:139],v116 offset:64
ds_read_b128 v[132:135],v112 offset:3552
v_mfma_f32_16x16x32_bf16 v[164:167],v[96:99],v[152:155],0
s_waitcnt lgkmcnt(8)
ds_read_b128 v[92:95],v12 offset:4608
v_add_f32 v120,v160,v161
v_mul_f32 v124,v160,v160
v_fmac_f32 v124,v161,v161
v_mfma_f32_16x16x32_bf16 v[188:191],v[96:99],v[156:159],0
s_waitcnt lgkmcnt(6)
s_add_u32 s65,s61,98304
buffer_store_dwordx4 v[56:59],v10,s[40:43],s65 offen offset:128 sc1
ds_read_b128 v[156:159],v117 offset:3552
ds_read_b128 v[96:99],v11 offset:6528
v_add_f32 v121,v162,v163
v_mul_f32 v125,v162,v162
v_fmac_f32 v125,v163,v163
v_smfmac_f32_16x16x64_bf16 v[164:167],v[100:103],v[144:151],v13
ds_read_b128 v[144:147],v113 offset:64
v_add_f32 v122,v184,v185
v_mul_f32 v126,v184,v184
v_fmac_f32 v126,v185,v185
v_smfmac_f32_16x16x64_bf16 v[188:191],v[100:103],v[148:155],v13
s_waitcnt lgkmcnt(5)
ds_read_b128 v[152:155],v117 offset:64
ds_read_b128 v[148:151],v113 offset:3552
v_add_f32 v123,v186,v187
v_mul_f32 v127,v186,v186
v_fmac_f32 v127,v187,v187
v_mfma_f32_16x16x32_bf16 v[168:171],v[88:91],v[136:139],0
ds_read_b128 v[100:103],v12 offset:5760
v_add_f32 v120,v120,v164
v_fmac_f32 v124,v164,v164
v_add_f32 v120,v120,v165
v_fmac_f32 v124,v165,v165
v_mfma_f32_16x16x32_bf16 v[192:195],v[88:91],v[140:143],0
s_waitcnt lgkmcnt(6)
s_add_u32 s65,s61,98304
buffer_store_dwordx4 v[44:47],v9,s[40:43],s65 offen offset:512 sc1
ds_read_b128 v[140:143],v116 offset:3616
ds_read_b128 v[88:91],v11 offset:9984
v_add_f32 v121,v121,v166
v_fmac_f32 v125,v166,v166
v_add_f32 v121,v121,v167
v_fmac_f32 v125,v167,v167
v_smfmac_f32_16x16x64_bf16 v[168:171],v[92:95],v[128:135],v13
ds_read_b128 v[128:131],v112 offset:128
v_add_f32 v122,v122,v188
v_fmac_f32 v126,v188,v188
v_add_f32 v122,v122,v189
v_fmac_f32 v126,v189,v189
v_smfmac_f32_16x16x64_bf16 v[192:195],v[92:95],v[132:139],v13
s_waitcnt lgkmcnt(5)
ds_read_b128 v[136:139],v116 offset:128
ds_read_b128 v[132:135],v112 offset:3616
v_add_f32 v123,v123,v190
v_fmac_f32 v127,v190,v190
v_add_f32 v123,v123,v191
v_fmac_f32 v127,v191,v191
v_mfma_f32_16x16x32_bf16 v[172:175],v[96:99],v[152:155],0
ds_read_b128 v[92:95],v12 offset:9216
v_add_f32 v120,v120,v168
v_fmac_f32 v124,v168,v168
v_add_f32 v120,v120,v169
v_fmac_f32 v124,v169,v169
v_mfma_f32_16x16x32_bf16 v[196:199],v[96:99],v[156:159],0
s_waitcnt lgkmcnt(6)
s_add_u32 s65,s61,98304
buffer_store_dwordx4 v[52:55],v10,s[40:43],s65 offen offset:256 sc1
ds_read_b128 v[156:159],v117 offset:3616
ds_read_b128 v[96:99],v11 offset:11136
v_add_f32 v121,v121,v170
v_fmac_f32 v125,v170,v170
v_add_f32 v121,v121,v171
v_fmac_f32 v125,v171,v171
v_smfmac_f32_16x16x64_bf16 v[172:175],v[100:103],v[144:151],v13
ds_read_b128 v[144:147],v113 offset:128
v_add_f32 v122,v122,v192
v_fmac_f32 v126,v192,v192
v_add_f32 v122,v122,v193
v_fmac_f32 v126,v193,v193
v_smfmac_f32_16x16x64_bf16 v[196:199],v[100:103],v[148:155],v13
s_waitcnt lgkmcnt(5)
ds_read_b128 v[152:155],v117 offset:128
ds_read_b128 v[148:151],v113 offset:3616
v_add_f32 v123,v123,v194
v_fmac_f32 v127,v194,v194
v_add_f32 v123,v123,v195
v_fmac_f32 v127,v195,v195
v_mfma_f32_16x16x32_bf16 v[176:179],v[88:91],v[136:139],0
ds_read_b128 v[100:103],v12 offset:10368
v_add_f32 v120,v120,v172
v_fmac_f32 v124,v172,v172
v_add_f32 v120,v120,v173
v_fmac_f32 v124,v173,v173
v_mfma_f32_16x16x32_bf16 v[200:203],v[88:91],v[140:143],0
s_waitcnt lgkmcnt(6)
s_add_u32 s65,s61,98304
buffer_store_dwordx4 v[60:63],v9,s[40:43],s65 offen offset:640 sc1
ds_read_b128 v[140:143],v118 offset:3488
ds_read_b128 v[88:91],v11 offset:3072
v_add_f32 v121,v121,v174
v_fmac_f32 v125,v174,v174
v_add_f32 v121,v121,v175
v_fmac_f32 v125,v175,v175
v_smfmac_f32_16x16x64_bf16 v[176:179],v[92:95],v[128:135],v13
ds_read_b128 v[128:131],v114 offset:0
v_add_f32 v122,v122,v196
v_fmac_f32 v126,v196,v196
v_add_f32 v122,v122,v197
v_fmac_f32 v126,v197,v197
v_smfmac_f32_16x16x64_bf16 v[200:203],v[92:95],v[132:139],v13
s_waitcnt lgkmcnt(5)
ds_read_b128 v[136:139],v118 offset:0
ds_read_b128 v[132:135],v114 offset:3488
v_add_f32 v123,v123,v198
v_fmac_f32 v127,v198,v198
v_add_f32 v123,v123,v199
v_fmac_f32 v127,v199,v199
v_mfma_f32_16x16x32_bf16 v[180:183],v[96:99],v[152:155],0
ds_read_b128 v[92:95],v12 offset:2304
v_add_f32 v120,v120,v176
v_fmac_f32 v124,v176,v176
v_add_f32 v120,v120,v177
v_fmac_f32 v124,v177,v177
v_mfma_f32_16x16x32_bf16 v[204:207],v[96:99],v[156:159],0
s_waitcnt lgkmcnt(6)
ds_read_b128 v[156:159],v119 offset:3488
ds_read_b128 v[96:99],v11 offset:4224
v_add_f32 v121,v121,v178
v_fmac_f32 v125,v178,v178
v_add_f32 v121,v121,v179
v_fmac_f32 v125,v179,v179
v_smfmac_f32_16x16x64_bf16 v[180:183],v[100:103],v[144:151],v13
ds_read_b128 v[144:147],v115 offset:0
v_add_f32 v122,v122,v200
v_fmac_f32 v126,v200,v200
v_add_f32 v122,v122,v201
v_fmac_f32 v126,v201,v201
v_smfmac_f32_16x16x64_bf16 v[204:207],v[100:103],v[148:155],v13
s_waitcnt lgkmcnt(5)
ds_read_b128 v[152:155],v119 offset:0
ds_read_b128 v[148:151],v115 offset:3488
v_add_f32 v123,v123,v202
v_fmac_f32 v127,v202,v202
v_add_f32 v123,v123,v203
v_fmac_f32 v127,v203,v203
v_mfma_f32_16x16x32_bf16 v[208:211],v[88:91],v[136:139],0
ds_read_b128 v[100:103],v12 offset:3456
v_add_f32 v120,v120,v180
v_fmac_f32 v124,v180,v180
v_add_f32 v120,v120,v181
v_fmac_f32 v124,v181,v181
v_mfma_f32_16x16x32_bf16 v[232:235],v[88:91],v[140:143],0
s_waitcnt lgkmcnt(6)
ds_read_b128 v[140:143],v118 offset:3552
ds_read_b128 v[88:91],v11 offset:7680
v_add_f32 v121,v121,v182
v_fmac_f32 v125,v182,v182
v_add_f32 v121,v121,v183
v_fmac_f32 v125,v183,v183
v_smfmac_f32_16x16x64_bf16 v[208:211],v[92:95],v[128:135],v13
ds_read_b128 v[128:131],v114 offset:64
v_add_f32 v122,v122,v204
v_fmac_f32 v126,v204,v204
v_add_f32 v122,v122,v205
v_fmac_f32 v126,v205,v205
v_smfmac_f32_16x16x64_bf16 v[232:235],v[92:95],v[132:139],v13
s_waitcnt lgkmcnt(5)
ds_read_b128 v[136:139],v118 offset:64
ds_read_b128 v[132:135],v114 offset:3552
v_add_f32 v123,v123,v206
v_fmac_f32 v127,v206,v206
v_add_f32 v123,v123,v207
v_fmac_f32 v127,v207,v207
v_mfma_f32_16x16x32_bf16 v[212:215],v[96:99],v[152:155],0
ds_read_b128 v[92:95],v12 offset:6912
v_add_f32 v120,v120,v208
v_fmac_f32 v124,v208,v208
v_add_f32 v120,v120,v209
v_fmac_f32 v124,v209,v209
v_mfma_f32_16x16x32_bf16 v[236:239],v[96:99],v[156:159],0
s_waitcnt lgkmcnt(6)
s_setprio 2
ds_read_b128 v[156:159],v119 offset:3552
ds_read_b128 v[96:99],v11 offset:8832
v_add_f32 v121,v121,v210
v_fmac_f32 v125,v210,v210
v_add_f32 v121,v121,v211
v_fmac_f32 v125,v211,v211
v_smfmac_f32_16x16x64_bf16 v[212:215],v[100:103],v[144:151],v13
ds_read_b128 v[144:147],v115 offset:64
v_add_f32 v122,v122,v232
v_fmac_f32 v126,v232,v232
v_add_f32 v122,v122,v233
v_fmac_f32 v126,v233,v233
v_smfmac_f32_16x16x64_bf16 v[236:239],v[100:103],v[148:155],v13
s_waitcnt lgkmcnt(5)
ds_read_b128 v[152:155],v119 offset:64
ds_read_b128 v[148:151],v115 offset:3552
v_add_f32 v123,v123,v234
v_fmac_f32 v127,v234,v234
v_add_f32 v123,v123,v235
v_fmac_f32 v127,v235,v235
v_mfma_f32_16x16x32_bf16 v[216:219],v[88:91],v[136:139],0
ds_read_b128 v[100:103],v12 offset:8064
v_add_f32 v120,v120,v212
v_fmac_f32 v124,v212,v212
v_add_f32 v120,v120,v213
v_fmac_f32 v124,v213,v213
v_add_f32 v121,v121,v214
v_mfma_f32_16x16x32_bf16 v[240:243],v[88:91],v[140:143],0
s_waitcnt lgkmcnt(6)
ds_read_b128 v[140:143],v118 offset:3616
ds_read_b128 v[88:91],v11 offset:12288
v_fmac_f32 v125,v214,v214
v_add_f32 v121,v121,v215
v_fmac_f32 v125,v215,v215
v_add_f32 v122,v122,v236
v_smfmac_f32_16x16x64_bf16 v[216:219],v[92:95],v[128:135],v13
ds_read_b128 v[128:131],v114 offset:128
v_fmac_f32 v126,v236,v236
v_add_f32 v122,v122,v237
v_fmac_f32 v126,v237,v237
v_add_f32 v123,v123,v238
v_fmac_f32 v127,v238,v238
v_smfmac_f32_16x16x64_bf16 v[240:243],v[92:95],v[132:139],v13
s_waitcnt lgkmcnt(5)
ds_read_b128 v[136:139],v118 offset:128
ds_read_b128 v[132:135],v114 offset:3616
v_add_f32 v123,v123,v239
v_fmac_f32 v127,v239,v239
v_add_u32 v112,v112,v8
v_add_u32 v116,v116,v8
v_mfma_f32_16x16x32_bf16 v[220:223],v[96:99],v[152:155],0
ds_read_b128 v[92:95],v12 offset:11520
ds_read_b128 v[104:107],v14 offset:0
v_add_f32 v120,v120,v216
v_fmac_f32 v124,v216,v216
v_add_f32 v120,v120,v217
v_fmac_f32 v124,v217,v217
v_mfma_f32_16x16x32_bf16 v[244:247],v[96:99],v[156:159],0
s_waitcnt lgkmcnt(7)
ds_read_b128 v[108:111],v4 offset:3584
ds_read_b128 v[156:159],v119 offset:3616
v_add_f32 v121,v121,v218
v_fmac_f32 v125,v218,v218
v_add_f32 v121,v121,v219
v_fmac_f32 v125,v219,v219
v_smfmac_f32_16x16x64_bf16 v[220:223],v[100:103],v[144:151],v13
ds_read_b128 v[96:99],v11 offset:13440
ds_read_b128 v[144:147],v115 offset:128
v_add_f32 v122,v122,v240
v_fmac_f32 v126,v240,v240
v_add_f32 v122,v122,v241
v_fmac_f32 v126,v241,v241
v_smfmac_f32_16x16x64_bf16 v[244:247],v[100:103],v[148:155],v13
s_waitcnt lgkmcnt(7)
ds_read_b128 v[152:155],v119 offset:128
ds_read_b128 v[148:151],v115 offset:3616
v_add_f32 v123,v123,v242
v_fmac_f32 v127,v242,v242
v_add_f32 v123,v123,v243
v_fmac_f32 v127,v243,v243
v_mfma_f32_16x16x32_bf16 v[224:227],v[88:91],v[136:139],0
ds_read_b128 v[100:103],v12 offset:12672
v_add_f32 v120,v120,v220
v_fmac_f32 v124,v220,v220
v_add_f32 v120,v120,v221
v_fmac_f32 v124,v221,v221
v_mfma_f32_16x16x32_bf16 v[248:251],v[88:91],v[140:143],0
s_waitcnt lgkmcnt(8)
s_setprio 1
ds_read_b128 v[140:143],v116 offset:192
ds_read_b128 v[88:91],v112 offset:3616
v_add_f32 v121,v121,v222
v_fmac_f32 v125,v222,v222
v_add_f32 v121,v121,v223
v_fmac_f32 v125,v223,v223
v_smfmac_f32_16x16x64_bf16 v[224:227],v[92:95],v[128:135],v13
ds_read_b128 v[128:131],v112 offset:3488
v_add_f32 v122,v122,v244
v_fmac_f32 v126,v244,v244
v_add_f32 v122,v122,v245
v_fmac_f32 v126,v245,v245
v_smfmac_f32_16x16x64_bf16 v[248:251],v[92:95],v[132:139],v13
s_waitcnt lgkmcnt(5)
ds_read_b128 v[136:139],v116 offset:0
ds_read_b128 v[132:135],v112 offset:3680
v_add_f32 v123,v123,v246
v_fmac_f32 v127,v246,v246
v_add_f32 v123,v123,v247
v_fmac_f32 v127,v247,v247
v_mfma_f32_16x16x32_bf16 v[228:231],v[96:99],v[152:155],0
ds_read_b128 v[92:95],v112 offset:3808
v_add_f32 v120,v120,v224
v_fmac_f32 v124,v224,v224
v_add_f32 v120,v120,v225
v_fmac_f32 v124,v225,v225
v_mfma_f32_16x16x32_bf16 v[252:255],v[96:99],v[156:159],0
s_waitcnt lgkmcnt(6)
s_cbranch_vccnz RA33_%=
s_mov_b64 exec,s[62:63]
s_add_u32 m0,s57,34880
s_add_u32 s64,s56,4915200
buffer_load_dwordx4 v5,s[36:39],s64 offen lds
s_add_u32 m0,s57,36608
s_nop 0
buffer_load_dwordx4 v6,s[36:39],s64 offen lds
s_mov_b64 exec,-1
RA33_%=:
ds_read_b128 v[156:159],v116 offset:256
ds_read_b128 v[96:99],v116 offset:128
v_add_f32 v121,v121,v226
v_fmac_f32 v125,v226,v226
v_add_f32 v121,v121,v227
v_fmac_f32 v125,v227,v227
v_smfmac_f32_16x16x64_bf16 v[228:231],v[100:103],v[144:151],v13
ds_read_b128 v[144:147],v112 offset:3552
v_add_f32 v122,v122,v248
v_fmac_f32 v126,v248,v248
v_add_f32 v122,v122,v249
v_fmac_f32 v126,v249,v249
v_smfmac_f32_16x16x64_bf16 v[252:255],v[100:103],v[148:155],v13
s_setprio 0
ds_read_b128 v[152:155],v116 offset:64
ds_read_b128 v[148:151],v112 offset:3744
v_add_f32 v123,v123,v250
v_fmac_f32 v127,v250,v250
v_add_f32 v123,v123,v251
v_fmac_f32 v127,v251,v251
ds_read_b128 v[100:103],v116 offset:320
v_add_f32 v120,v120,v228
v_fmac_f32 v124,v228,v228
v_add_f32 v120,v120,v229
v_fmac_f32 v124,v229,v229
v_add_f32 v121,v121,v230
v_fmac_f32 v125,v230,v230
v_add_f32 v121,v121,v231
v_fmac_f32 v125,v231,v231
s_waitcnt lgkmcnt(0)
s_barrier
s_setprio 0
v_add_f32 v122,v122,v252
s_cbranch_vccnz RA34_%=
s_mov_b64 exec,s[62:63]
s_add_u32 m0,s57,38368
s_add_u32 s64,s56,5013504
buffer_load_dwordx4 v5,s[36:39],s64 offen lds
s_add_u32 m0,s57,40096
s_nop 0
buffer_load_dwordx4 v6,s[36:39],s64 offen lds
s_mov_b64 exec,-1
RA34_%=:
v_fmac_f32 v126,v252,v252
v_add_f32 v122,v122,v253
s_cbranch_vccnz RA35_%=
s_mov_b64 exec,s[62:63]
s_add_u32 m0,s57,41856
s_add_u32 s64,s56,5111808
buffer_load_dwordx4 v5,s[36:39],s64 offen lds
s_add_u32 m0,s57,43584
s_nop 0
buffer_load_dwordx4 v6,s[36:39],s64 offen lds
s_mov_b64 exec,-1
RA35_%=:
v_fmac_f32 v126,v253,v253
v_add_f32 v123,v123,v254
v_fmac_f32 v127,v254,v254
v_add_f32 v123,v123,v255
v_fmac_f32 v127,v255,v255
s_setprio 3
ds_read_b128 v[56:59],v14 offset:256
ds_read_b128 v[60:63],v4 offset:3616
s_setprio 0
v_permlane32_swap_b32 v120,v122
v_permlane32_swap_b32 v121,v123
v_permlane32_swap_b32 v124,v126
v_permlane32_swap_b32 v125,v127
v_add_f32 v120,v120,v122
v_add_f32 v121,v121,v123
v_add_f32 v124,v124,v126
v_add_f32 v125,v125,v127
v_permlane16_swap_b32 v120,v121
s_nop 0
v_permlane16_swap_b32 v124,v125
v_add_f32 v113,v120,v121
v_add_f32 v114,v124,v125
v_mul_f32 v113,0x3c2aaaab,v113
v_mul_f32 v114,0x3c2aaaab,v114
v_mul_f32 v115,0x5e000000,v114
v_fma_f32 v114,-v113,v113,v114
v_pk_maximum3_f16 v7,v7,v115,v115 op_sel:[0,1,1] op_sel_hi:[1,1,1]
v_max_f32 v114,0,v114
v_add_f32 v114,0x3727c5ac,v114
v_rsq_f32 v114,v114
s_nop 0
v_mul_f32 v120,0x8befe000,v114
v_mul_f32 v121,0x8befe000,v114
v_mul_f32 v124,-v113,v120
v_mul_f32 v125,-v113,v120
v_permlane16_swap_b32 v120,v121
s_nop 0
v_permlane16_swap_b32 v124,v125
v_mov_b32 v122,v120
v_mov_b32 v123,v121
v_mov_b32 v126,v124
v_mov_b32 v127,v125
v_permlane32_swap_b32 v120,v122
v_permlane32_swap_b32 v124,v126
s_cbranch_vccnz RA36_%=
s_mov_b64 exec,s[62:63]
s_add_u32 m0,s57,45344
s_add_u32 s64,s56,5210112
buffer_load_dwordx4 v5,s[36:39],s64 offen lds
s_add_u32 m0,s57,47072
s_nop 0
buffer_load_dwordx4 v6,s[36:39],s64 offen lds
s_mov_b64 exec,-1
RA36_%=:
v_permlane32_swap_b32 v121,v123
v_permlane32_swap_b32 v125,v127
v_add_u32 v8,94432,v1
v_add_u32 v9,94432,v2
v_fma_f32 v160,v160,v120,v124
s_setprio 3
ds_read_b64_tr_b16 v[64:65],v1 offset:512
s_setprio 0
v_fma_f32 v161,v161,v120,v124
s_setprio 3
ds_read_b64_tr_b16 v[66:67],v1 offset:2560
s_setprio 0
v_fma_f32 v162,v162,v121,v125
s_setprio 3
ds_read_b128 v[112:115],v4 offset:896
s_setprio 0
v_fma_f32 v163,v163,v121,v125
s_setprio 3
ds_read_b64_tr_b16 v[68:69],v2 offset:512
s_setprio 0
v_fma_f32 v164,v164,v120,v124
s_setprio 3
ds_read_b64_tr_b16 v[70:71],v2 offset:2560
s_setprio 0
v_fma_f32 v165,v165,v120,v124
s_setprio 3
ds_read_b128 v[116:119],v4 offset:928
s_setprio 0
v_fma_f32 v166,v166,v121,v125
s_setprio 3
ds_read_b64_tr_b16 v[72:73],v1 offset:4608
s_setprio 0
v_fma_f32 v167,v167,v121,v125
s_setprio 3
ds_read_b64_tr_b16 v[74:75],v1 offset:6656
s_setprio 0
v_fma_f32 v208,v208,v120,v124
s_setprio 3
ds_read_b64_tr_b16 v[76:77],v2 offset:4608
s_setprio 0
v_fma_f32 v209,v209,v120,v124
s_setprio 3
ds_read_b64_tr_b16 v[78:79],v2 offset:6656
s_setprio 0
v_fma_f32 v210,v210,v121,v125
s_setprio 3
ds_read_b64_tr_b16 v[80:81],v1 offset:8704
s_setprio 0
v_fma_f32 v211,v211,v121,v125
s_setprio 3
ds_read_b64_tr_b16 v[82:83],v1 offset:10752
s_setprio 0
v_fma_f32 v212,v212,v120,v124
s_setprio 3
ds_read_b64_tr_b16 v[84:85],v2 offset:8704
s_setprio 0
v_fma_f32 v213,v213,v120,v124
s_setprio 3
ds_read_b64_tr_b16 v[86:87],v2 offset:10752
s_setprio 0
v_fma_f32 v214,v214,v121,v125
v_fma_f32 v215,v215,v121,v125
v_cvt_pk_bf16_f32 v16,v160,v161
v_cvt_pk_bf16_f32 v20,v162,v163
v_cvt_pk_bf16_f32 v17,v164,v165
v_cvt_pk_bf16_f32 v21,v166,v167
v_cvt_pk_bf16_f32 v18,v208,v209
v_cvt_pk_bf16_f32 v22,v210,v211
v_cvt_pk_bf16_f32 v19,v212,v213
v_cvt_pk_bf16_f32 v23,v214,v215
v_fma_f32 v168,v168,v120,v124
v_fma_f32 v169,v169,v120,v124
v_mfma_f32_16x16x32_bf16 v[160:163],v[104:107],v[128:131],v[108:111]
v_fma_f32 v170,v170,v121,v125
v_fma_f32 v171,v171,v121,v125
v_fma_f32 v172,v172,v120,v124
v_fma_f32 v173,v173,v120,v124
v_fma_f32 v174,v174,v121,v125
v_fma_f32 v175,v175,v121,v125
v_mfma_f32_16x16x32_bf16 v[164:167],v[104:107],v[132:135],v[108:111]
v_fma_f32 v216,v216,v120,v124
v_fma_f32 v217,v217,v120,v124
v_fma_f32 v218,v218,v121,v125
v_fma_f32 v219,v219,v121,v125
v_fma_f32 v220,v220,v120,v124
v_mfma_f32_16x16x32_bf16 v[208:211],v[104:107],v[136:139],v[108:111]
s_cbranch_vccnz RA37_%=
s_mov_b64 exec,s[62:63]
s_add_u32 m0,s57,48832
s_add_u32 s64,s56,5308416
buffer_load_dwordx4 v5,s[36:39],s64 offen lds
s_add_u32 m0,s57,50560
s_nop 0
buffer_load_dwordx4 v6,s[36:39],s64 offen lds
s_mov_b64 exec,-1
RA37_%=:
v_fma_f32 v221,v221,v120,v124
v_fma_f32 v222,v222,v121,v125
v_fma_f32 v223,v223,v121,v125
v_cvt_pk_bf16_f32 v24,v168,v169
v_cvt_pk_bf16_f32 v28,v170,v171
s_setprio 3
v_mfma_f32_16x16x32_bf16 v[212:215],v[104:107],v[140:143],v[108:111]
ds_read_b128 v[104:107],v14 offset:512
s_setprio 0
v_cvt_pk_bf16_f32 v25,v172,v173
v_cvt_pk_bf16_f32 v29,v174,v175
v_cvt_pk_bf16_f32 v26,v216,v217
v_cvt_pk_bf16_f32 v30,v218,v219
v_cvt_pk_bf16_f32 v27,v220,v221
v_cvt_pk_bf16_f32 v31,v222,v223
v_fma_f32 v176,v176,v120,v124
v_fma_f32 v177,v177,v120,v124
s_waitcnt lgkmcnt(15)
s_setprio 3
v_mfma_f32_16x16x32_bf16 v[168:171],v[56:59],v[128:131],v[60:63]
ds_read_b128 v[108:111],v4 offset:3712
s_setprio 0
v_fma_f32 v178,v178,v121,v125
v_fma_f32 v179,v179,v121,v125
v_fma_f32 v180,v180,v120,v124
v_fma_f32 v181,v181,v120,v124
v_fma_f32 v182,v182,v121,v125
v_fma_f32 v183,v183,v121,v125
v_mfma_f32_16x16x32_bf16 v[172:175],v[56:59],v[132:135],v[60:63]
v_fma_f32 v224,v224,v120,v124
v_fma_f32 v225,v225,v120,v124
v_fma_f32 v226,v226,v121,v125
v_fma_f32 v227,v227,v121,v125
v_fma_f32 v228,v228,v120,v124
v_mfma_f32_16x16x32_bf16 v[216:219],v[56:59],v[136:139],v[60:63]
v_fma_f32 v229,v229,v120,v124
v_fma_f32 v230,v230,v121,v125
v_fma_f32 v231,v231,v121,v125
v_cvt_pk_bf16_f32 v32,v176,v177
v_cvt_pk_bf16_f32 v36,v178,v179
s_setprio 3
v_mfma_f32_16x16x32_bf16 v[220:223],v[56:59],v[140:143],v[60:63]
ds_read_b128 v[56:59],v14 offset:768
s_setprio 0
v_cvt_pk_bf16_f32 v33,v180,v181
v_cvt_pk_bf16_f32 v37,v182,v183
v_cvt_pk_bf16_f32 v34,v224,v225
v_cvt_pk_bf16_f32 v38,v226,v227
v_cvt_pk_bf16_f32 v35,v228,v229
v_cvt_pk_bf16_f32 v39,v230,v231
v_fma_f32 v184,v184,v122,v126
s_waitcnt lgkmcnt(1)
v_mfma_f32_16x16x32_bf16 v[176:179],v[104:107],v[144:147],v[108:111]
s_cbranch_vccnz RA38_%=
s_mov_b64 exec,s[62:63]
s_add_u32 m0,s57,52320
s_add_u32 s64,s56,5406720
buffer_load_dwordx4 v5,s[36:39],s64 offen lds
s_add_u32 m0,s57,54048
s_nop 0
buffer_load_dwordx4 v6,s[36:39],s64 offen lds
s_mov_b64 exec,-1
RA38_%=:
s_setprio 3
ds_read_b128 v[60:63],v4 offset:3744
s_setprio 0
v_fma_f32 v185,v185,v122,v126
v_fma_f32 v186,v186,v123,v127
v_fma_f32 v187,v187,v123,v127
v_mfma_f32_16x16x32_bf16 v[128:131],v[64:67],v[16:19],v[112:115]
v_fma_f32 v188,v188,v122,v126
v_fma_f32 v189,v189,v122,v126
v_fma_f32 v190,v190,v123,v127
v_mfma_f32_16x16x32_bf16 v[180:183],v[104:107],v[148:151],v[108:111]
v_fma_f32 v191,v191,v123,v127
v_fma_f32 v232,v232,v122,v126
v_fma_f32 v233,v233,v122,v126
v_mfma_f32_16x16x32_bf16 v[136:139],v[64:67],v[20:23],v[112:115]
v_fma_f32 v234,v234,v123,v127
v_fma_f32 v235,v235,v123,v127
s_nop 0
s_mov_b32 s86,0xbe801d5a
v_fma_f32 v236,v236,v122,v126
v_mfma_f32_16x16x32_bf16 v[224:227],v[104:107],v[152:155],v[108:111]
v_fma_f32 v237,v237,v122,v126
v_fma_f32 v238,v238,v123,v127
v_fma_f32 v239,v239,v123,v127
v_mfma_f32_16x16x32_bf16 v[140:143],v[68:71],v[20:23],v[116:119]
v_cvt_pk_bf16_f32 v40,v184,v185
v_cvt_pk_bf16_f32 v44,v186,v187
v_cvt_pk_bf16_f32 v41,v188,v189
s_setprio 3
v_mfma_f32_16x16x32_bf16 v[228:231],v[104:107],v[156:159],v[108:111]
ds_read_b128 v[104:107],v14 offset:1024
s_setprio 0
v_cvt_pk_bf16_f32 v45,v190,v191
v_cvt_pk_bf16_f32 v42,v232,v233
v_cvt_pk_bf16_f32 v46,v234,v235
s_setprio 3
v_mfma_f32_16x16x32_bf16 v[132:135],v[68:71],v[16:19],v[116:119]
ds_read_b128 v[108:111],v4 offset:3840
s_setprio 0
v_cvt_pk_bf16_f32 v43,v236,v237
v_cvt_pk_bf16_f32 v47,v238,v239
v_fma_f32 v192,v192,v122,v126
s_waitcnt lgkmcnt(2)
v_mfma_f32_16x16x32_bf16 v[184:187],v[56:59],v[144:147],v[60:63]
v_fma_f32 v193,v193,v122,v126
v_fma_f32 v194,v194,v123,v127
v_mfma_f32_16x16x32_bf16 v[128:131],v[72:75],v[24:27],v[128:131]
v_fma_f32 v195,v195,v123,v127
v_fma_f32 v196,v196,v122,v126
v_mfma_f32_16x16x32_bf16 v[188:191],v[56:59],v[148:151],v[60:63]
v_fma_f32 v197,v197,v122,v126
v_fma_f32 v198,v198,v123,v127
v_mfma_f32_16x16x32_bf16 v[136:139],v[72:75],v[28:31],v[136:139]
s_cbranch_vccnz RA39_%=
s_mov_b64 exec,s[62:63]
s_add_u32 m0,s57,55808
s_add_u32 s64,s56,5505024
buffer_load_dwordx4 v5,s[36:39],s64 offen lds
s_add_u32 m0,s57,57536
s_nop 0
buffer_load_dwordx4 v6,s[36:39],s64 offen lds
s_mov_b64 exec,-1
RA39_%=:
v_fma_f32 v199,v199,v123,v127
v_fma_f32 v240,v240,v122,v126
v_mfma_f32_16x16x32_bf16 v[232:235],v[56:59],v[152:155],v[60:63]
v_fma_f32 v241,v241,v122,v126
v_fma_f32 v242,v242,v123,v127
v_mfma_f32_16x16x32_bf16 v[140:143],v[76:79],v[28:31],v[140:143]
v_fma_f32 v243,v243,v123,v127
v_fma_f32 v244,v244,v122,v126
s_setprio 3
v_mfma_f32_16x16x32_bf16 v[236:239],v[56:59],v[156:159],v[60:63]
ds_read_b128 v[144:147],v14 offset:1280
s_setprio 0
v_fma_f32 v245,v245,v122,v126
v_fma_f32 v246,v246,v123,v127
s_setprio 3
v_mfma_f32_16x16x32_bf16 v[132:135],v[76:79],v[24:27],v[132:135]
ds_read_b128 v[148:151],v4 offset:3872
s_setprio 0
v_fma_f32 v247,v247,v123,v127
v_cvt_pk_bf16_f32 v48,v192,v193
v_cvt_pk_bf16_f32 v52,v194,v195
v_cvt_pk_bf16_f32 v49,v196,v197
v_cvt_pk_bf16_f32 v53,v198,v199
v_cvt_pk_bf16_f32 v50,v240,v241
v_cvt_pk_bf16_f32 v54,v242,v243
v_cvt_pk_bf16_f32 v51,v244,v245
v_cvt_pk_bf16_f32 v55,v246,v247
v_fma_f32 v200,v200,v122,v126
s_waitcnt lgkmcnt(2)
v_mfma_f32_16x16x32_bf16 v[192:195],v[104:107],v[88:91],v[108:111]
v_fma_f32 v201,v201,v122,v126
v_fma_f32 v202,v202,v123,v127
v_fma_f32 v203,v203,v123,v127
v_mfma_f32_16x16x32_bf16 v[128:131],v[80:83],v[32:35],v[128:131]
v_fma_f32 v204,v204,v122,v126
v_fma_f32 v205,v205,v122,v126
v_fma_f32 v206,v206,v123,v127
v_mfma_f32_16x16x32_bf16 v[196:199],v[104:107],v[92:95],v[108:111]
v_fma_f32 v207,v207,v123,v127
v_fma_f32 v248,v248,v122,v126
v_fma_f32 v249,v249,v122,v126
v_mfma_f32_16x16x32_bf16 v[136:139],v[80:83],v[36:39],v[136:139]
v_fma_f32 v250,v250,v123,v127
v_fma_f32 v251,v251,v123,v127
v_fma_f32 v252,v252,v122,v126
v_mfma_f32_16x16x32_bf16 v[240:243],v[104:107],v[96:99],v[108:111]
s_cbranch_vccnz RA40_%=
s_mov_b64 exec,s[62:63]
s_add_u32 m0,s57,59296
s_add_u32 s64,s56,5603328
buffer_load_dwordx4 v5,s[36:39],s64 offen lds
s_add_u32 m0,s57,61024
s_nop 0
buffer_load_dwordx4 v6,s[36:39],s64 offen lds
s_mov_b64 exec,-1
RA40_%=:
v_fma_f32 v253,v253,v122,v126
v_fma_f32 v254,v254,v123,v127
v_fma_f32 v255,v255,v123,v127
v_mfma_f32_16x16x32_bf16 v[140:143],v[84:87],v[36:39],v[140:143]
v_cvt_pk_bf16_f32 v56,v200,v201
v_cvt_pk_bf16_f32 v60,v202,v203
v_cvt_pk_bf16_f32 v57,v204,v205
v_mfma_f32_16x16x32_bf16 v[244:247],v[104:107],v[100:103],v[108:111]
v_cvt_pk_bf16_f32 v61,v206,v207
v_cvt_pk_bf16_f32 v58,v248,v249
v_cvt_pk_bf16_f32 v62,v250,v251
v_mfma_f32_16x16x32_bf16 v[132:135],v[84:87],v[32:35],v[132:135]
v_cvt_pk_bf16_f32 v59,v252,v253
v_cvt_pk_bf16_f32 v63,v254,v255
s_waitcnt lgkmcnt(0)
v_mfma_f32_16x16x32_bf16 v[200:203],v[144:147],v[88:91],v[148:151]
s_barrier
s_setprio 3
ds_read_b64_tr_b16 v[104:105],v3 offset:12448
v_lshlrev_b32_sdwa v128,1,v128 dst_sel:DWORD dst_unused:UNUSED_PAD src0_sel:DWORD src1_sel:WORD_1
v_lshlrev_b32_sdwa v130,1,v130 dst_sel:DWORD dst_unused:UNUSED_PAD src0_sel:DWORD src1_sel:WORD_1
v_lshlrev_b32_sdwa v129,1,v129 dst_sel:DWORD dst_unused:UNUSED_PAD src0_sel:DWORD src1_sel:WORD_1
v_mfma_f32_16x16x32_bf16 v[204:207],v[144:147],v[92:95],v[148:151]
s_cbranch_vccz RA41_%=
s_add_u32 m0,s58,0
s_mul_i32 s69,s59,9
s_add_u32 s69,s69,s97
buffer_load_dwordx4 v5,s[44:47],s69 offen lds
s_add_u32 m0,m0,1024
s_add_u32 s69,s69,s53
buffer_load_dwordx4 v6,s[44:47],s69 offen lds
RA41_%=:
ds_read_u16 v128,v128
ds_read_b64_tr_b16 v[106:107],v3 offset:13984
v_lshlrev_b32_sdwa v131,1,v131 dst_sel:DWORD dst_unused:UNUSED_PAD src0_sel:DWORD src1_sel:WORD_1
v_fma_mixlo_f16 v120,v140,s99,0
v_fma_mixlo_f16 v122,v141,s99,0
v_mfma_f32_16x16x32_bf16 v[248:251],v[144:147],v[96:99],v[148:151]
ds_read_u16_d16_hi v130,v130
ds_read_b64_tr_b16 v[108:109],v3 offset:12456
v_lshlrev_b32_sdwa v132,1,v132 dst_sel:DWORD dst_unused:UNUSED_PAD src0_sel:DWORD src1_sel:WORD_1
v_lshlrev_b32_sdwa v134,1,v134 dst_sel:DWORD dst_unused:UNUSED_PAD src0_sel:DWORD src1_sel:WORD_1
v_mfma_f32_16x16x32_bf16 v[252:255],v[144:147],v[100:103],v[148:151]
ds_read_u16 v129,v129
ds_read_b64_tr_b16 v[110:111],v3 offset:13992
v_fma_mixhi_f16 v120,v142,s99,0
v_fma_mixhi_f16 v122,v143,s99,0
v_lshlrev_b32_sdwa v133,1,v133 dst_sel:DWORD dst_unused:UNUSED_PAD src0_sel:DWORD src1_sel:WORD_1
v_lshlrev_b32_sdwa v135,1,v135 dst_sel:DWORD dst_unused:UNUSED_PAD src0_sel:DWORD src1_sel:WORD_1
v_mfma_f32_16x16x32_bf16 v[144:147],v[64:67],v[40:43],v[112:115]
s_cbranch_vccz RA42_%=
s_add_u32 m0,m0,1024
s_add_u32 s69,s69,s53
buffer_load_dwordx4 v5,s[44:47],s69 offen lds
s_add_u32 m0,m0,s50
s_add_u32 s69,s69,s53
buffer_load_dwordx4 v6,s[44:47],s69 offen lds
RA42_%=:
ds_read_u16_d16_hi v131,v131
ds_read_b64_tr_b16 v[88:89],v3 offset:12320
v_pk_fma_f16 v121,v120,v120,s54
v_pk_fma_f16 v123,v122,v122,s54
v_pk_mul_f16 v121,v120,v121
v_mfma_f32_16x16x32_bf16 v[152:155],v[64:67],v[44:47],v[112:115]
ds_read_u16 v132,v132
ds_read_b64_tr_b16 v[90:91],v3 offset:13856
v_pk_mul_f16 v123,v122,v123
v_lshlrev_b32_sdwa v136,1,v136 dst_sel:DWORD dst_unused:UNUSED_PAD src0_sel:DWORD src1_sel:WORD_1
v_mfma_f32_16x16x32_bf16 v[156:159],v[68:71],v[44:47],v[116:119]
s_cbranch_vccnz RA43_%=
s_mov_b64 exec,s[62:63]
s_add_u32 m0,s57,62784
s_add_u32 s64,s56,5701632
buffer_load_dwordx4 v5,s[36:39],s64 offen lds
s_add_u32 m0,s57,64512
s_nop 0
buffer_load_dwordx4 v6,s[36:39],s64 offen lds
s_mov_b64 exec,-1
RA43_%=:
ds_read_u16_d16_hi v134,v134
ds_read_b64_tr_b16 v[92:93],v3 offset:12328
v_lshlrev_b32_sdwa v138,1,v138 dst_sel:DWORD dst_unused:UNUSED_PAD src0_sel:DWORD src1_sel:WORD_1
v_exp_f16 v120,v121
v_mfma_f32_16x16x32_bf16 v[148:151],v[68:71],v[40:43],v[116:119]
ds_read_u16 v133,v133
ds_read_b64_tr_b16 v[94:95],v3 offset:13864
v_exp_f16 v122,v123
v_lshlrev_b32_sdwa v137,1,v137 dst_sel:DWORD dst_unused:UNUSED_PAD src0_sel:DWORD src1_sel:WORD_1
v_mfma_f32_16x16x32_bf16 v[144:147],v[72:75],v[48:51],v[144:147]
s_cbranch_vccz RA44_%=
s_add_u32 m0,m0,1024
s_add_u32 s69,s69,s53
buffer_load_dwordx4 v5,s[44:47],s69 offen lds
s_add_u32 m0,m0,1024
s_add_u32 s69,s69,s53
buffer_load_dwordx4 v6,s[44:47],s69 offen lds
RA44_%=:
ds_read_u16_d16_hi v135,v135
ds_read_b64_tr_b16 v[96:97],v3 offset:12384
v_lshlrev_b32_sdwa v139,1,v139 dst_sel:DWORD dst_unused:UNUSED_PAD src0_sel:DWORD src1_sel:WORD_1
v_exp_f16_sdwa v120,v121 dst_sel:WORD_1 dst_unused:UNUSED_PRESERVE src0_sel:WORD_1
v_mfma_f32_16x16x32_bf16 v[152:155],v[72:75],v[52:55],v[152:155]
ds_read_u16 v136,v136
ds_read_b64_tr_b16 v[98:99],v3 offset:13920
v_exp_f16_sdwa v122,v123 dst_sel:WORD_1 dst_unused:UNUSED_PRESERVE src0_sel:WORD_1
v_mfma_f32_16x16x32_bf16 v[156:159],v[76:79],v[52:55],v[156:159]
ds_read_u16_d16_hi v138,v138
ds_read_b64_tr_b16 v[100:101],v3 offset:12392
v_pk_fma_f16 v120,v120,s55,s55
v_pk_fma_f16 v122,v122,s55,s55
v_rcp_f16 v121,v120
v_mfma_f32_16x16x32_bf16 v[148:151],v[76:79],v[48:51],v[148:151]
s_cbranch_vccz RA45_%=
s_add_u32 m0,m0,s50
s_add_u32 s69,s69,s53
buffer_load_dwordx4 v5,s[44:47],s69 offen lds
s_add_u32 m0,m0,1024
s_add_u32 s69,s69,s53
buffer_load_dwordx4 v6,s[44:47],s69 offen lds
RA45_%=:
ds_read_u16 v137,v137
ds_read_b64_tr_b16 v[102:103],v3 offset:13928
v_rcp_f16 v123,v122
v_mfma_f32_16x16x32_bf16 v[144:147],v[80:83],v[56:59],v[144:147]
ds_read_u16_d16_hi v139,v139
ds_read_b64_tr_b16 v[64:65],v1 offset:12320
v_rcp_f16_sdwa v121,v120 dst_sel:WORD_1 dst_unused:UNUSED_PRESERVE src0_sel:WORD_1
v_mfma_f32_16x16x32_bf16 v[152:155],v[80:83],v[60:63],v[152:155]
ds_read_b64_tr_b16 v[66:67],v1 offset:14368
v_rcp_f16_sdwa v123,v122 dst_sel:WORD_1 dst_unused:UNUSED_PRESERVE src0_sel:WORD_1
v_fma_mix_f32 v140,v140,v121,0 op_sel_hi:[0,1,0]
v_mfma_f32_16x16x32_bf16 v[156:159],v[84:87],v[60:63],v[156:159]
s_cbranch_vccz RA46_%=
s_add_u32 m0,m0,1024
s_add_u32 s69,s69,s53
buffer_load_dwordx4 v5,s[44:47],s69 offen lds
s_add_u32 m0,m0,s50
s_add_u32 s69,s69,s53
buffer_load_dwordx4 v6,s[44:47],s69 offen lds
RA46_%=:
ds_read_b128 v[112:115],v4 offset:1024
v_fma_mix_f32 v142,v142,v121,0 op_sel:[0,1,0] op_sel_hi:[0,1,0]
s_waitcnt lgkmcnt(15)
v_lshl_add_u64 v[128:129],v[128:129],0,v[130:131]
s_waitcnt lgkmcnt(11)
v_lshl_add_u64 v[130:131],v[132:133],0,v[134:135]
v_mfma_f32_16x16x32_bf16 v[148:151],v[84:87],v[56:59],v[148:151]
ds_read_b64_tr_b16 v[68:69],v2 offset:12320
v_fma_mix_f32 v141,v141,v123,0 op_sel_hi:[0,1,0]
v_fma_mix_f32 v143,v143,v123,0 op_sel:[0,1,0] op_sel_hi:[0,1,0]
v_lshlrev_b32_sdwa v144,1,v144 dst_sel:DWORD dst_unused:UNUSED_PAD src0_sel:DWORD src1_sel:WORD_1
v_lshlrev_b32_sdwa v146,1,v146 dst_sel:DWORD dst_unused:UNUSED_PAD src0_sel:DWORD src1_sel:WORD_1
v_mfma_f32_16x16x32_bf16 v[160:163],v[88:91],v[128:131],v[160:163]
s_cbranch_vccnz RA47_%=
s_mov_b64 exec,s[62:63]
s_add_u32 m0,s57,66272
s_add_u32 s64,s56,5799936
buffer_load_dwordx4 v5,s[36:39],s64 offen lds
s_add_u32 m0,s57,68000
s_nop 0
buffer_load_dwordx4 v6,s[36:39],s64 offen lds
s_mov_b64 exec,-1
RA47_%=:
ds_read_u16 v144,v144
v_lshlrev_b32_sdwa v145,1,v145 dst_sel:DWORD dst_unused:UNUSED_PAD src0_sel:DWORD src1_sel:WORD_1
v_lshlrev_b32_sdwa v147,1,v147 dst_sel:DWORD dst_unused:UNUSED_PAD src0_sel:DWORD src1_sel:WORD_1
v_mfma_f32_16x16x32_bf16 v[168:171],v[92:95],v[128:131],v[168:171]
s_waitcnt lgkmcnt(10)
s_cbranch_vccz RA48_%=
s_add_u32 m0,m0,1024
s_add_u32 s69,s69,s53
buffer_load_dwordx4 v5,s[44:47],s69 offen lds
s_add_u32 m0,m0,1024
s_add_u32 s69,s69,s53
buffer_load_dwordx4 v6,s[44:47],s69 offen lds
RA48_%=:
ds_read_b64_tr_b16 v[70:71],v2 offset:14368
ds_read_b128 v[116:119],v4 offset:1056
v_fma_mixlo_f16 v120,v156,s99,0
v_fma_mixlo_f16 v122,v157,s99,0
v_mfma_f32_16x16x32_bf16 v[176:179],v[96:99],v[128:131],v[176:179]
s_waitcnt lgkmcnt(8)
ds_read_u16_d16_hi v146,v146
v_lshlrev_b32_sdwa v148,1,v148 dst_sel:DWORD dst_unused:UNUSED_PAD src0_sel:DWORD src1_sel:WORD_1
v_lshlrev_b32_sdwa v150,1,v150 dst_sel:DWORD dst_unused:UNUSED_PAD src0_sel:DWORD src1_sel:WORD_1
v_mfma_f32_16x16x32_bf16 v[184:187],v[100:103],v[128:131],v[184:187]
s_waitcnt lgkmcnt(8)
ds_read_b64_tr_b16 v[72:73],v1 offset:16416
ds_read_b64_tr_b16 v[74:75],v1 offset:18464
v_lshl_add_u64 v[136:137],v[136:137],0,v[138:139]
v_mfma_f32_16x16x32_bf16 v[192:195],v[104:107],v[128:131],v[192:195]
ds_read_u16 v145,v145
v_cvt_pk_bf16_f32 v138,v140,v142
v_cvt_pk_bf16_f32 v139,v141,v143
v_mfma_f32_16x16x32_bf16 v[200:203],v[108:111],v[128:131],v[200:203]
ds_read_b64_tr_b16 v[76:77],v2 offset:16416
ds_read_b64_tr_b16 v[78:79],v2 offset:18464
v_fma_mixhi_f16 v120,v158,s99,0
v_fma_mixhi_f16 v122,v159,s99,0
v_mfma_f32_16x16x32_bf16 v[164:167],v[88:91],v[136:139],v[164:167]
ds_read_u16_d16_hi v147,v147
v_lshlrev_b32_sdwa v149,1,v149 dst_sel:DWORD dst_unused:UNUSED_PAD src0_sel:DWORD src1_sel:WORD_1
v_lshlrev_b32_sdwa v151,1,v151 dst_sel:DWORD dst_unused:UNUSED_PAD src0_sel:DWORD src1_sel:WORD_1
v_mfma_f32_16x16x32_bf16 v[172:175],v[92:95],v[136:139],v[172:175]
ds_read_b64_tr_b16 v[80:81],v1 offset:20512
ds_read_b64_tr_b16 v[82:83],v1 offset:22560
v_pk_fma_f16 v121,v120,v120,s54
v_pk_fma_f16 v123,v122,v122,s54
v_mfma_f32_16x16x32_bf16 v[180:183],v[96:99],v[136:139],v[180:183]
ds_read_u16 v148,v148
v_pk_mul_f16 v121,v120,v121
v_pk_mul_f16 v123,v122,v123
v_mfma_f32_16x16x32_bf16 v[188:191],v[100:103],v[136:139],v[188:191]
ds_read_b64_tr_b16 v[84:85],v2 offset:20512
v_lshlrev_b32_sdwa v152,1,v152 dst_sel:DWORD dst_unused:UNUSED_PAD src0_sel:DWORD src1_sel:WORD_1
v_lshlrev_b32_sdwa v154,1,v154 dst_sel:DWORD dst_unused:UNUSED_PAD src0_sel:DWORD src1_sel:WORD_1
v_mfma_f32_16x16x32_bf16 v[196:199],v[104:107],v[136:139],v[196:199]
ds_read_u16_d16_hi v150,v150
v_exp_f16 v120,v121
v_mfma_f32_16x16x32_bf16 v[204:207],v[108:111],v[136:139],v[204:207]
s_waitcnt lgkmcnt(15)
ds_read_b64_tr_b16 v[86:87],v2 offset:22560
v_exp_f16 v122,v123
v_mfma_f32_16x16x32_bf16 v[128:131],v[64:67],v[16:19],v[112:115]
s_cbranch_vccnz RA49_%=
s_mov_b64 exec,s[62:63]
s_add_u32 m0,s57,0
s_add_u32 s64,s56,5898240
buffer_load_dwordx4 v5,s[36:39],s64 offen lds
s_add_u32 m0,s57,1728
s_nop 0
buffer_load_dwordx4 v6,s[36:39],s64 offen lds
s_mov_b64 exec,-1
RA49_%=:
ds_read_u16 v149,v149
v_lshlrev_b32_sdwa v153,1,v153 dst_sel:DWORD dst_unused:UNUSED_PAD src0_sel:DWORD src1_sel:WORD_1
v_lshlrev_b32_sdwa v155,1,v155 dst_sel:DWORD dst_unused:UNUSED_PAD src0_sel:DWORD src1_sel:WORD_1
v_mfma_f32_16x16x32_bf16 v[136:139],v[64:67],v[20:23],v[112:115]
s_waitcnt lgkmcnt(14)
v_exp_f16_sdwa v120,v121 dst_sel:WORD_1 dst_unused:UNUSED_PRESERVE src0_sel:WORD_1
v_mfma_f32_16x16x32_bf16 v[140:143],v[68:71],v[20:23],v[116:119]
ds_read_u16_d16_hi v151,v151
v_exp_f16_sdwa v122,v123 dst_sel:WORD_1 dst_unused:UNUSED_PRESERVE src0_sel:WORD_1
v_mfma_f32_16x16x32_bf16 v[132:135],v[68:71],v[16:19],v[116:119]
s_waitcnt lgkmcnt(12)
v_pk_fma_f16 v120,v120,s55,s55
v_pk_fma_f16 v122,v122,s55,s55
v_mfma_f32_16x16x32_bf16 v[128:131],v[72:75],v[24:27],v[128:131]
ds_read_u16 v152,v152
v_rcp_f16 v121,v120
v_mfma_f32_16x16x32_bf16 v[136:139],v[72:75],v[28:31],v[136:139]
s_waitcnt lgkmcnt(10)
v_rcp_f16 v123,v122
v_mfma_f32_16x16x32_bf16 v[140:143],v[76:79],v[28:31],v[140:143]
ds_read_u16_d16_hi v154,v154
v_rcp_f16_sdwa v121,v120 dst_sel:WORD_1 dst_unused:UNUSED_PRESERVE src0_sel:WORD_1
v_mfma_f32_16x16x32_bf16 v[132:135],v[76:79],v[24:27],v[132:135]
s_waitcnt lgkmcnt(8)
v_rcp_f16_sdwa v123,v122 dst_sel:WORD_1 dst_unused:UNUSED_PRESERVE src0_sel:WORD_1
v_mfma_f32_16x16x32_bf16 v[128:131],v[80:83],v[32:35],v[128:131]
ds_read_u16 v153,v153
v_fma_mix_f32 v156,v156,v121,0 op_sel_hi:[0,1,0]
v_fma_mix_f32 v158,v158,v121,0 op_sel:[0,1,0] op_sel_hi:[0,1,0]
v_mfma_f32_16x16x32_bf16 v[136:139],v[80:83],v[36:39],v[136:139]
s_waitcnt lgkmcnt(3)
v_lshl_add_u64 v[144:145],v[144:145],0,v[146:147]
v_lshl_add_u64 v[146:147],v[148:149],0,v[150:151]
v_mfma_f32_16x16x32_bf16 v[140:143],v[84:87],v[36:39],v[140:143]
ds_read_u16_d16_hi v155,v155
v_fma_mix_f32 v157,v157,v123,0 op_sel_hi:[0,1,0]
v_fma_mix_f32 v159,v159,v123,0 op_sel:[0,1,0] op_sel_hi:[0,1,0]
v_mfma_f32_16x16x32_bf16 v[132:135],v[84:87],v[32:35],v[132:135]
v_lshlrev_b32_sdwa v128,1,v128 dst_sel:DWORD dst_unused:UNUSED_PAD src0_sel:DWORD src1_sel:WORD_1
v_lshlrev_b32_sdwa v130,1,v130 dst_sel:DWORD dst_unused:UNUSED_PAD src0_sel:DWORD src1_sel:WORD_1
v_mfma_f32_16x16x32_bf16 v[208:211],v[88:91],v[144:147],v[208:211]
s_cbranch_vccnz RA50_%=
s_mov_b64 exec,s[62:63]
s_add_u32 m0,s57,3488
s_add_u32 s64,s56,5996544
buffer_load_dwordx4 v5,s[36:39],s64 offen lds
s_add_u32 m0,s57,5216
s_nop 0
buffer_load_dwordx4 v6,s[36:39],s64 offen lds
s_mov_b64 exec,-1
RA50_%=:
ds_read_u16 v128,v128
v_lshlrev_b32_sdwa v129,1,v129 dst_sel:DWORD dst_unused:UNUSED_PAD src0_sel:DWORD src1_sel:WORD_1
v_lshlrev_b32_sdwa v131,1,v131 dst_sel:DWORD dst_unused:UNUSED_PAD src0_sel:DWORD src1_sel:WORD_1
v_mfma_f32_16x16x32_bf16 v[216:219],v[92:95],v[144:147],v[216:219]
v_fma_mixlo_f16 v120,v140,s99,0
v_fma_mixlo_f16 v122,v141,s99,0
v_mfma_f32_16x16x32_bf16 v[224:227],v[96:99],v[144:147],v[224:227]
ds_read_u16_d16_hi v130,v130
v_lshlrev_b32_sdwa v132,1,v132 dst_sel:DWORD dst_unused:UNUSED_PAD src0_sel:DWORD src1_sel:WORD_1
v_lshlrev_b32_sdwa v134,1,v134 dst_sel:DWORD dst_unused:UNUSED_PAD src0_sel:DWORD src1_sel:WORD_1
v_mfma_f32_16x16x32_bf16 v[232:235],v[100:103],v[144:147],v[232:235]
s_waitcnt lgkmcnt(2)
v_lshl_add_u64 v[152:153],v[152:153],0,v[154:155]
v_mfma_f32_16x16x32_bf16 v[240:243],v[104:107],v[144:147],v[240:243]
ds_read_u16 v129,v129
v_cvt_pk_bf16_f32 v154,v156,v158
v_cvt_pk_bf16_f32 v155,v157,v159
v_mfma_f32_16x16x32_bf16 v[248:251],v[108:111],v[144:147],v[248:251]
v_fma_mixhi_f16 v120,v142,s99,0
v_fma_mixhi_f16 v122,v143,s99,0
v_mfma_f32_16x16x32_bf16 v[212:215],v[88:91],v[152:155],v[212:215]
ds_read_u16_d16_hi v131,v131
v_lshlrev_b32_sdwa v133,1,v133 dst_sel:DWORD dst_unused:UNUSED_PAD src0_sel:DWORD src1_sel:WORD_1
v_lshlrev_b32_sdwa v135,1,v135 dst_sel:DWORD dst_unused:UNUSED_PAD src0_sel:DWORD src1_sel:WORD_1
v_mfma_f32_16x16x32_bf16 v[220:223],v[92:95],v[152:155],v[220:223]
s_setprio 2
ds_read_b64_tr_b16 v[88:89],v3 offset:18480
v_pk_fma_f16 v121,v120,v120,s54
v_pk_fma_f16 v123,v122,v122,s54
v_mfma_f32_16x16x32_bf16 v[228:231],v[96:99],v[152:155],v[228:231]
ds_read_u16 v132,v132
v_pk_mul_f16 v121,v120,v121
v_pk_mul_f16 v123,v122,v123
v_mfma_f32_16x16x32_bf16 v[236:239],v[100:103],v[152:155],v[236:239]
ds_read_b64_tr_b16 v[90:91],v3 offset:20016
v_lshlrev_b32_sdwa v136,1,v136 dst_sel:DWORD dst_unused:UNUSED_PAD src0_sel:DWORD src1_sel:WORD_1
v_lshlrev_b32_sdwa v138,1,v138 dst_sel:DWORD dst_unused:UNUSED_PAD src0_sel:DWORD src1_sel:WORD_1
v_mfma_f32_16x16x32_bf16 v[244:247],v[104:107],v[152:155],v[244:247]
ds_read_u16_d16_hi v134,v134
v_exp_f16 v120,v121
v_mfma_f32_16x16x32_bf16 v[252:255],v[108:111],v[152:155],v[252:255]
ds_read_b64_tr_b16 v[92:93],v3 offset:18488
v_exp_f16 v122,v123
v_mfma_f32_16x16x32_bf16 v[144:147],v[64:67],v[40:43],v[112:115]
ds_read_u16 v133,v133
v_lshlrev_b32_sdwa v137,1,v137 dst_sel:DWORD dst_unused:UNUSED_PAD src0_sel:DWORD src1_sel:WORD_1
v_lshlrev_b32_sdwa v139,1,v139 dst_sel:DWORD dst_unused:UNUSED_PAD src0_sel:DWORD src1_sel:WORD_1
v_mfma_f32_16x16x32_bf16 v[152:155],v[64:67],v[44:47],v[112:115]
s_cbranch_vccnz RA51_%=
s_mov_b64 exec,s[62:63]
s_add_u32 m0,s57,6976
s_add_u32 s64,s56,6094848
buffer_load_dwordx4 v5,s[36:39],s64 offen lds
s_add_u32 m0,s57,8704
s_nop 0
buffer_load_dwordx4 v6,s[36:39],s64 offen lds
s_mov_b64 exec,-1
RA51_%=:
ds_read_b64_tr_b16 v[94:95],v3 offset:20024
v_exp_f16_sdwa v120,v121 dst_sel:WORD_1 dst_unused:UNUSED_PRESERVE src0_sel:WORD_1
v_mfma_f32_16x16x32_bf16 v[156:159],v[68:71],v[44:47],v[116:119]
ds_read_u16_d16_hi v135,v135
v_exp_f16_sdwa v122,v123 dst_sel:WORD_1 dst_unused:UNUSED_PRESERVE src0_sel:WORD_1
v_mfma_f32_16x16x32_bf16 v[148:151],v[68:71],v[40:43],v[116:119]
ds_read_b64_tr_b16 v[96:97],v3 offset:18544
v_pk_fma_f16 v120,v120,s55,s55
v_pk_fma_f16 v122,v122,s55,s55
v_mfma_f32_16x16x32_bf16 v[144:147],v[72:75],v[48:51],v[144:147]
ds_read_u16 v136,v136
v_rcp_f16 v121,v120
v_mfma_f32_16x16x32_bf16 v[152:155],v[72:75],v[52:55],v[152:155]
ds_read_b64_tr_b16 v[98:99],v3 offset:20080
ds_read_b64_tr_b16 v[100:101],v3 offset:18552
v_rcp_f16 v123,v122
v_mfma_f32_16x16x32_bf16 v[156:159],v[76:79],v[52:55],v[156:159]
ds_read_u16_d16_hi v138,v138
v_rcp_f16_sdwa v121,v120 dst_sel:WORD_1 dst_unused:UNUSED_PRESERVE src0_sel:WORD_1
v_mfma_f32_16x16x32_bf16 v[148:151],v[76:79],v[48:51],v[148:151]
ds_read_b64_tr_b16 v[102:103],v3 offset:20088
ds_read_b64_tr_b16 v[104:105],v3 offset:18608
v_rcp_f16_sdwa v123,v122 dst_sel:WORD_1 dst_unused:UNUSED_PRESERVE src0_sel:WORD_1
v_mfma_f32_16x16x32_bf16 v[144:147],v[80:83],v[56:59],v[144:147]
ds_read_u16 v137,v137
v_fma_mix_f32 v140,v140,v121,0 op_sel_hi:[0,1,0]
v_fma_mix_f32 v142,v142,v121,0 op_sel:[0,1,0] op_sel_hi:[0,1,0]
v_mfma_f32_16x16x32_bf16 v[152:155],v[80:83],v[60:63],v[152:155]
s_waitcnt lgkmcnt(8)
ds_read_b64_tr_b16 v[106:107],v3 offset:20144
ds_read_b64_tr_b16 v[108:109],v3 offset:18616
v_lshl_add_u64 v[128:129],v[128:129],0,v[130:131]
v_lshl_add_u64 v[130:131],v[132:133],0,v[134:135]
v_mfma_f32_16x16x32_bf16 v[156:159],v[84:87],v[60:63],v[156:159]
ds_read_u16_d16_hi v139,v139
v_fma_mix_f32 v141,v141,v123,0 op_sel_hi:[0,1,0]
v_fma_mix_f32 v143,v143,v123,0 op_sel:[0,1,0] op_sel_hi:[0,1,0]
v_mfma_f32_16x16x32_bf16 v[148:151],v[84:87],v[56:59],v[148:151]
ds_read_b64_tr_b16 v[110:111],v3 offset:20152
ds_read_b64_tr_b16 v[64:65],v1 offset:12832
v_lshlrev_b32_sdwa v144,1,v144 dst_sel:DWORD dst_unused:UNUSED_PAD src0_sel:DWORD src1_sel:WORD_1
v_lshlrev_b32_sdwa v146,1,v146 dst_sel:DWORD dst_unused:UNUSED_PAD src0_sel:DWORD src1_sel:WORD_1
v_mfma_f32_16x16x32_bf16 v[160:163],v[88:91],v[128:131],v[160:163]
ds_read_u16 v144,v144
v_lshlrev_b32_sdwa v145,1,v145 dst_sel:DWORD dst_unused:UNUSED_PAD src0_sel:DWORD src1_sel:WORD_1
v_lshlrev_b32_sdwa v147,1,v147 dst_sel:DWORD dst_unused:UNUSED_PAD src0_sel:DWORD src1_sel:WORD_1
v_mfma_f32_16x16x32_bf16 v[168:171],v[92:95],v[128:131],v[168:171]
s_waitcnt lgkmcnt(11)
s_cbranch_vccnz RA52_%=
s_mov_b64 exec,s[62:63]
s_add_u32 m0,s57,10464
s_add_u32 s64,s56,6193152
buffer_load_dwordx4 v5,s[36:39],s64 offen lds
s_add_u32 m0,s57,12192
s_nop 0
buffer_load_dwordx4 v6,s[36:39],s64 offen lds
s_mov_b64 exec,-1
RA52_%=:
ds_read_b64_tr_b16 v[66:67],v1 offset:14880
ds_read_b128 v[112:115],v4 offset:1152
v_fma_mixlo_f16 v120,v156,s99,0
v_fma_mixlo_f16 v122,v157,s99,0
v_mfma_f32_16x16x32_bf16 v[176:179],v[96:99],v[128:131],v[176:179]
s_waitcnt lgkmcnt(10)
ds_read_u16_d16_hi v146,v146
v_lshlrev_b32_sdwa v148,1,v148 dst_sel:DWORD dst_unused:UNUSED_PAD src0_sel:DWORD src1_sel:WORD_1
v_lshlrev_b32_sdwa v150,1,v150 dst_sel:DWORD dst_unused:UNUSED_PAD src0_sel:DWORD src1_sel:WORD_1
v_mfma_f32_16x16x32_bf16 v[184:187],v[100:103],v[128:131],v[184:187]
s_waitcnt lgkmcnt(6)
ds_read_b64_tr_b16 v[68:69],v2 offset:12832
ds_read_b64_tr_b16 v[70:71],v2 offset:14880
v_lshl_add_u64 v[136:137],v[136:137],0,v[138:139]
v_mfma_f32_16x16x32_bf16 v[192:195],v[104:107],v[128:131],v[192:195]
s_waitcnt lgkmcnt(7)
ds_read_u16 v145,v145
v_cvt_pk_bf16_f32 v138,v140,v142
v_cvt_pk_bf16_f32 v139,v141,v143
v_mfma_f32_16x16x32_bf16 v[200:203],v[108:111],v[128:131],v[200:203]
ds_read_b128 v[116:119],v4 offset:1184
ds_read_b64_tr_b16 v[72:73],v1 offset:16928
v_fma_mixhi_f16 v120,v158,s99,0
v_fma_mixhi_f16 v122,v159,s99,0
v_mfma_f32_16x16x32_bf16 v[164:167],v[88:91],v[136:139],v[164:167]
ds_read_u16_d16_hi v147,v147
v_lshlrev_b32_sdwa v149,1,v149 dst_sel:DWORD dst_unused:UNUSED_PAD src0_sel:DWORD src1_sel:WORD_1
v_lshlrev_b32_sdwa v151,1,v151 dst_sel:DWORD dst_unused:UNUSED_PAD src0_sel:DWORD src1_sel:WORD_1
v_mfma_f32_16x16x32_bf16 v[172:175],v[92:95],v[136:139],v[172:175]
ds_read_b64_tr_b16 v[74:75],v1 offset:18976
ds_read_b64_tr_b16 v[76:77],v2 offset:16928
v_pk_fma_f16 v121,v120,v120,s54
v_pk_fma_f16 v123,v122,v122,s54
v_mfma_f32_16x16x32_bf16 v[180:183],v[96:99],v[136:139],v[180:183]
s_setprio 1
ds_read_u16 v148,v148
v_pk_mul_f16 v121,v120,v121
v_pk_mul_f16 v123,v122,v123
v_mfma_f32_16x16x32_bf16 v[188:191],v[100:103],v[136:139],v[188:191]
ds_read_b64_tr_b16 v[78:79],v2 offset:18976
v_lshlrev_b32_sdwa v152,1,v152 dst_sel:DWORD dst_unused:UNUSED_PAD src0_sel:DWORD src1_sel:WORD_1
v_lshlrev_b32_sdwa v154,1,v154 dst_sel:DWORD dst_unused:UNUSED_PAD src0_sel:DWORD src1_sel:WORD_1
v_mfma_f32_16x16x32_bf16 v[196:199],v[104:107],v[136:139],v[196:199]
ds_read_u16_d16_hi v150,v150
v_exp_f16 v120,v121
v_mfma_f32_16x16x32_bf16 v[204:207],v[108:111],v[136:139],v[204:207]
s_waitcnt lgkmcnt(12)
ds_read_b64_tr_b16 v[80:81],v1 offset:21024
v_exp_f16 v122,v123
v_mfma_f32_16x16x32_bf16 v[128:131],v[64:67],v[16:19],v[112:115]
ds_read_u16 v149,v149
v_lshlrev_b32_sdwa v153,1,v153 dst_sel:DWORD dst_unused:UNUSED_PAD src0_sel:DWORD src1_sel:WORD_1
v_lshlrev_b32_sdwa v155,1,v155 dst_sel:DWORD dst_unused:UNUSED_PAD src0_sel:DWORD src1_sel:WORD_1
v_mfma_f32_16x16x32_bf16 v[136:139],v[64:67],v[20:23],v[112:115]
s_waitcnt lgkmcnt(9)
s_cbranch_vccnz RA53_%=
s_mov_b64 exec,s[62:63]
s_add_u32 m0,s57,13952
s_add_u32 s64,s56,6291456
buffer_load_dwordx4 v5,s[36:39],s64 offen lds
s_add_u32 m0,s57,15680
s_nop 0
buffer_load_dwordx4 v6,s[36:39],s64 offen lds
s_mov_b64 exec,-1
RA53_%=:
ds_read_b64_tr_b16 v[82:83],v1 offset:23072
v_exp_f16_sdwa v120,v121 dst_sel:WORD_1 dst_unused:UNUSED_PRESERVE src0_sel:WORD_1
v_mfma_f32_16x16x32_bf16 v[140:143],v[68:71],v[20:23],v[116:119]
ds_read_u16_d16_hi v151,v151
v_exp_f16_sdwa v122,v123 dst_sel:WORD_1 dst_unused:UNUSED_PRESERVE src0_sel:WORD_1
v_mfma_f32_16x16x32_bf16 v[132:135],v[68:71],v[16:19],v[116:119]
s_waitcnt lgkmcnt(8)
ds_read_b64_tr_b16 v[84:85],v2 offset:21024
v_pk_fma_f16 v120,v120,s55,s55
v_pk_fma_f16 v122,v122,s55,s55
v_mfma_f32_16x16x32_bf16 v[128:131],v[72:75],v[24:27],v[128:131]
ds_read_u16 v152,v152
v_rcp_f16 v121,v120
v_mfma_f32_16x16x32_bf16 v[136:139],v[72:75],v[28:31],v[136:139]
s_waitcnt lgkmcnt(7)
s_nop 0
s_mov_b32 s86,0xbe801d5a
ds_read_b64_tr_b16 v[86:87],v2 offset:23072
v_rcp_f16 v123,v122
v_mfma_f32_16x16x32_bf16 v[140:143],v[76:79],v[28:31],v[140:143]
ds_read_u16_d16_hi v154,v154
v_rcp_f16_sdwa v121,v120 dst_sel:WORD_1 dst_unused:UNUSED_PRESERVE src0_sel:WORD_1
v_mfma_f32_16x16x32_bf16 v[132:135],v[76:79],v[24:27],v[132:135]
s_waitcnt lgkmcnt(5)
v_rcp_f16_sdwa v123,v122 dst_sel:WORD_1 dst_unused:UNUSED_PRESERVE src0_sel:WORD_1
v_mfma_f32_16x16x32_bf16 v[128:131],v[80:83],v[32:35],v[128:131]
ds_read_u16 v153,v153
v_fma_mix_f32 v156,v156,v121,0 op_sel_hi:[0,1,0]
v_fma_mix_f32 v158,v158,v121,0 op_sel:[0,1,0] op_sel_hi:[0,1,0]
v_mfma_f32_16x16x32_bf16 v[136:139],v[80:83],v[36:39],v[136:139]
s_waitcnt lgkmcnt(2)
s_setprio 0
v_lshl_add_u64 v[144:145],v[144:145],0,v[146:147]
v_lshl_add_u64 v[146:147],v[148:149],0,v[150:151]
v_mfma_f32_16x16x32_bf16 v[140:143],v[84:87],v[36:39],v[140:143]
ds_read_u16_d16_hi v155,v155
v_fma_mix_f32 v157,v157,v123,0 op_sel_hi:[0,1,0]
v_fma_mix_f32 v159,v159,v123,0 op_sel:[0,1,0] op_sel_hi:[0,1,0]
v_mfma_f32_16x16x32_bf16 v[132:135],v[84:87],v[32:35],v[132:135]
v_lshlrev_b32_sdwa v128,1,v128 dst_sel:DWORD dst_unused:UNUSED_PAD src0_sel:DWORD src1_sel:WORD_1
v_lshlrev_b32_sdwa v130,1,v130 dst_sel:DWORD dst_unused:UNUSED_PAD src0_sel:DWORD src1_sel:WORD_1
v_mfma_f32_16x16x32_bf16 v[208:211],v[88:91],v[144:147],v[208:211]
s_cbranch_vccz RA54_%=
s_waitcnt vmcnt(0)
RA54_%=:
s_barrier
s_setprio 2
ds_read_u16 v128,v128
v_lshlrev_b32_sdwa v129,1,v129 dst_sel:DWORD dst_unused:UNUSED_PAD src0_sel:DWORD src1_sel:WORD_1
v_mfma_f32_16x16x32_bf16 v[216:219],v[92:95],v[144:147],v[216:219]
s_cbranch_vccnz RA55_%=
s_mov_b64 exec,s[62:63]
s_add_u32 m0,s57,17440
s_add_u32 s64,s56,6389760
buffer_load_dwordx4 v5,s[36:39],s64 offen lds
s_add_u32 m0,s57,19168
s_nop 0
buffer_load_dwordx4 v6,s[36:39],s64 offen lds
s_mov_b64 exec,-1
RA55_%=:
ds_read_u16_d16_hi v130,v130
v_lshlrev_b32_sdwa v131,1,v131 dst_sel:DWORD dst_unused:UNUSED_PAD src0_sel:DWORD src1_sel:WORD_1
v_mfma_f32_16x16x32_bf16 v[224:227],v[96:99],v[144:147],v[224:227]
s_cbranch_vccz RA56_%=
s_add_u32 m0,s58,12320
s_mov_b32 s69,s60
buffer_load_dwordx4 v5,s[44:47],s69 offen lds
s_add_u32 m0,m0,1024
s_add_u32 s69,s69,s53
buffer_load_dwordx4 v6,s[44:47],s69 offen lds
RA56_%=:
v_lshlrev_b32_sdwa v132,1,v132 dst_sel:DWORD dst_unused:UNUSED_PAD src0_sel:DWORD src1_sel:WORD_1
v_mfma_f32_16x16x32_bf16 v[232:235],v[100:103],v[144:147],v[232:235]
s_waitcnt lgkmcnt(2)
ds_read_u16 v129,v129
v_lshl_add_u64 v[152:153],v[152:153],0,v[154:155]
v_lshlrev_b32_sdwa v134,1,v134 dst_sel:DWORD dst_unused:UNUSED_PAD src0_sel:DWORD src1_sel:WORD_1
v_mfma_f32_16x16x32_bf16 v[240:243],v[104:107],v[144:147],v[240:243]
ds_read_u16_d16_hi v131,v131
v_cvt_pk_bf16_f32 v154,v156,v158
v_cvt_pk_bf16_f32 v155,v157,v159
v_mfma_f32_16x16x32_bf16 v[248:251],v[108:111],v[144:147],v[248:251]
v_lshlrev_b32_sdwa v133,1,v133 dst_sel:DWORD dst_unused:UNUSED_PAD src0_sel:DWORD src1_sel:WORD_1
v_mfma_f32_16x16x32_bf16 v[212:215],v[88:91],v[152:155],v[212:215]
s_cbranch_vccz RA57_%=
s_add_u32 m0,m0,1024
s_add_u32 s69,s69,s53
buffer_load_dwordx4 v5,s[44:47],s69 offen lds
s_add_u32 m0,m0,s50
s_add_u32 s69,s69,s53
buffer_load_dwordx4 v6,s[44:47],s69 offen lds
RA57_%=:
ds_read_u16 v132,v132
v_lshlrev_b32_sdwa v135,1,v135 dst_sel:DWORD dst_unused:UNUSED_PAD src0_sel:DWORD src1_sel:WORD_1
v_mfma_f32_16x16x32_bf16 v[220:223],v[92:95],v[152:155],v[220:223]
ds_read_u16_d16_hi v134,v134
v_lshlrev_b32_sdwa v136,1,v136 dst_sel:DWORD dst_unused:UNUSED_PAD src0_sel:DWORD src1_sel:WORD_1
v_mfma_f32_16x16x32_bf16 v[228:231],v[96:99],v[152:155],v[228:231]
ds_read_b64_tr_b16 v[88:89],v3 offset:0
ds_read_b64_tr_b16 v[90:91],v3 offset:1536
v_lshlrev_b32_sdwa v138,1,v138 dst_sel:DWORD dst_unused:UNUSED_PAD src0_sel:DWORD src1_sel:WORD_1
v_mfma_f32_16x16x32_bf16 v[236:239],v[100:103],v[152:155],v[236:239]
ds_read_u16 v133,v133
v_lshlrev_b32_sdwa v137,1,v137 dst_sel:DWORD dst_unused:UNUSED_PAD src0_sel:DWORD src1_sel:WORD_1
v_mfma_f32_16x16x32_bf16 v[244:247],v[104:107],v[152:155],v[244:247]
ds_read_u16_d16_hi v135,v135
v_lshlrev_b32_sdwa v139,1,v139 dst_sel:DWORD dst_unused:UNUSED_PAD src0_sel:DWORD src1_sel:WORD_1
v_mfma_f32_16x16x32_bf16 v[252:255],v[108:111],v[152:155],v[252:255]
s_cbranch_vccz RA58_%=
s_add_u32 m0,m0,1024
s_add_u32 s69,s69,s53
buffer_load_dwordx4 v5,s[44:47],s69 offen lds
s_add_u32 m0,m0,1024
s_add_u32 s69,s69,s53
buffer_load_dwordx4 v6,s[44:47],s69 offen lds
RA58_%=:
ds_read_b64_tr_b16 v[92:93],v3 offset:8
ds_read_b64_tr_b16 v[94:95],v3 offset:1544
v_lshlrev_b32_sdwa v140,1,v140 dst_sel:DWORD dst_unused:UNUSED_PAD src0_sel:DWORD src1_sel:WORD_1
v_mfma_f32_16x16x32_bf16 v[144:147],v[64:67],v[40:43],v[112:115]
ds_read_u16 v136,v136
v_lshlrev_b32_sdwa v142,1,v142 dst_sel:DWORD dst_unused:UNUSED_PAD src0_sel:DWORD src1_sel:WORD_1
v_mfma_f32_16x16x32_bf16 v[152:155],v[64:67],v[44:47],v[112:115]
ds_read_u16_d16_hi v138,v138
v_lshlrev_b32_sdwa v141,1,v141 dst_sel:DWORD dst_unused:UNUSED_PAD src0_sel:DWORD src1_sel:WORD_1
v_mfma_f32_16x16x32_bf16 v[156:159],v[68:71],v[44:47],v[116:119]
ds_read_b64_tr_b16 v[96:97],v3 offset:64
ds_read_b64_tr_b16 v[98:99],v3 offset:1600
v_lshlrev_b32_sdwa v143,1,v143 dst_sel:DWORD dst_unused:UNUSED_PAD src0_sel:DWORD src1_sel:WORD_1
v_mfma_f32_16x16x32_bf16 v[148:151],v[68:71],v[40:43],v[116:119]
ds_read_u16 v137,v137
v_mfma_f32_16x16x32_bf16 v[144:147],v[72:75],v[48:51],v[144:147]
ds_read_u16_d16_hi v139,v139
v_mfma_f32_16x16x32_bf16 v[152:155],v[72:75],v[52:55],v[152:155]
s_cbranch_vccz RA59_%=
s_add_u32 m0,m0,s50
s_add_u32 s69,s69,s51
buffer_load_dwordx4 v5,s[44:47],s69 offen lds
s_add_u32 m0,m0,1024
s_add_u32 s69,s69,s53
buffer_load_dwordx4 v6,s[44:47],s69 offen lds
RA59_%=:
ds_read_b64_tr_b16 v[100:101],v3 offset:72
ds_read_b64_tr_b16 v[102:103],v3 offset:1608
v_mfma_f32_16x16x32_bf16 v[156:159],v[76:79],v[52:55],v[156:159]
ds_read_u16 v140,v140
v_mfma_f32_16x16x32_bf16 v[148:151],v[76:79],v[48:51],v[148:151]
s_waitcnt lgkmcnt(15)
ds_read_u16_d16_hi v142,v142
v_lshl_add_u64 v[128:129],v[128:129],0,v[130:131]
v_mfma_f32_16x16x32_bf16 v[144:147],v[80:83],v[56:59],v[144:147]
s_waitcnt lgkmcnt(12)
ds_read_b64_tr_b16 v[104:105],v3 offset:128
ds_read_b64_tr_b16 v[106:107],v3 offset:1664
v_lshl_add_u64 v[130:131],v[132:133],0,v[134:135]
v_mfma_f32_16x16x32_bf16 v[152:155],v[80:83],v[60:63],v[152:155]
ds_read_u16 v141,v141
v_mfma_f32_16x16x32_bf16 v[156:159],v[84:87],v[60:63],v[156:159]
ds_read_u16_d16_hi v143,v143
v_mfma_f32_16x16x32_bf16 v[148:151],v[84:87],v[56:59],v[148:151]
s_cbranch_vccz RA60_%=
s_add_u32 m0,m0,1024
s_add_u32 s69,s69,s53
buffer_load_dwordx4 v5,s[44:47],s69 offen lds
s_add_u32 m0,m0,s50
s_add_u32 s69,s69,s53
buffer_load_dwordx4 v6,s[44:47],s69 offen lds
RA60_%=:
ds_read_b64_tr_b16 v[108:109],v3 offset:136
ds_read_b64_tr_b16 v[110:111],v3 offset:1672
v_lshlrev_b32_sdwa v144,1,v144 dst_sel:DWORD dst_unused:UNUSED_PAD src0_sel:DWORD src1_sel:WORD_1
v_lshlrev_b32_sdwa v146,1,v146 dst_sel:DWORD dst_unused:UNUSED_PAD src0_sel:DWORD src1_sel:WORD_1
v_mfma_f32_16x16x32_bf16 v[160:163],v[88:91],v[128:131],v[160:163]
s_waitcnt lgkmcnt(15)
ds_read_u16 v144,v144
v_lshlrev_b32_sdwa v145,1,v145 dst_sel:DWORD dst_unused:UNUSED_PAD src0_sel:DWORD src1_sel:WORD_1
v_mfma_f32_16x16x32_bf16 v[168:171],v[92:95],v[128:131],v[168:171]
s_waitcnt lgkmcnt(13)
ds_read_u16_d16_hi v146,v146
v_lshlrev_b32_sdwa v147,1,v147 dst_sel:DWORD dst_unused:UNUSED_PAD src0_sel:DWORD src1_sel:WORD_1
v_mfma_f32_16x16x32_bf16 v[176:179],v[96:99],v[128:131],v[176:179]
s_waitcnt lgkmcnt(10)
ds_read_b64_tr_b16 v[64:65],v1 offset:0
ds_read_b64_tr_b16 v[66:67],v1 offset:2048
v_lshlrev_b32_sdwa v148,1,v148 dst_sel:DWORD dst_unused:UNUSED_PAD src0_sel:DWORD src1_sel:WORD_1
v_mfma_f32_16x16x32_bf16 v[184:187],v[100:103],v[128:131],v[184:187]
s_waitcnt lgkmcnt(8)
ds_read_u16 v145,v145
v_lshl_add_u64 v[136:137],v[136:137],0,v[138:139]
v_lshlrev_b32_sdwa v150,1,v150 dst_sel:DWORD dst_unused:UNUSED_PAD src0_sel:DWORD src1_sel:WORD_1
v_mfma_f32_16x16x32_bf16 v[192:195],v[104:107],v[128:131],v[192:195]
s_waitcnt lgkmcnt(5)
s_cbranch_vccz RA61_%=
s_add_u32 m0,m0,1024
s_add_u32 s69,s69,s53
buffer_load_dwordx4 v5,s[44:47],s69 offen lds
s_add_u32 m0,m0,1024
s_add_u32 s69,s69,s53
buffer_load_dwordx4 v6,s[44:47],s69 offen lds
RA61_%=:
ds_read_u16_d16_hi v147,v147
v_lshl_add_u64 v[138:139],v[140:141],0,v[142:143]
v_mfma_f32_16x16x32_bf16 v[200:203],v[108:111],v[128:131],v[200:203]
ds_read_b128 v[112:115],v4 offset:1280
ds_read_b64_tr_b16 v[68:69],v2 offset:0
v_lshlrev_b32_sdwa v149,1,v149 dst_sel:DWORD dst_unused:UNUSED_PAD src0_sel:DWORD src1_sel:WORD_1
v_mfma_f32_16x16x32_bf16 v[164:167],v[88:91],v[136:139],v[164:167]
ds_read_u16 v148,v148
v_lshlrev_b32_sdwa v151,1,v151 dst_sel:DWORD dst_unused:UNUSED_PAD src0_sel:DWORD src1_sel:WORD_1
v_mfma_f32_16x16x32_bf16 v[172:175],v[92:95],v[136:139],v[172:175]
ds_read_u16_d16_hi v150,v150
ds_read_b64_tr_b16 v[70:71],v2 offset:2048
v_lshlrev_b32_sdwa v152,1,v152 dst_sel:DWORD dst_unused:UNUSED_PAD src0_sel:DWORD src1_sel:WORD_1
v_mfma_f32_16x16x32_bf16 v[180:183],v[96:99],v[136:139],v[180:183]
ds_read_b128 v[116:119],v4 offset:1312
ds_read_b64_tr_b16 v[72:73],v1 offset:4096
v_lshlrev_b32_sdwa v154,1,v154 dst_sel:DWORD dst_unused:UNUSED_PAD src0_sel:DWORD src1_sel:WORD_1
v_mfma_f32_16x16x32_bf16 v[188:191],v[100:103],v[136:139],v[188:191]
ds_read_u16 v149,v149
v_lshlrev_b32_sdwa v153,1,v153 dst_sel:DWORD dst_unused:UNUSED_PAD src0_sel:DWORD src1_sel:WORD_1
v_mfma_f32_16x16x32_bf16 v[196:199],v[104:107],v[136:139],v[196:199]
ds_read_u16_d16_hi v151,v151
v_lshlrev_b32_sdwa v155,1,v155 dst_sel:DWORD dst_unused:UNUSED_PAD src0_sel:DWORD src1_sel:WORD_1
v_mfma_f32_16x16x32_bf16 v[204:207],v[108:111],v[136:139],v[204:207]
s_waitcnt lgkmcnt(8)
ds_read_b64_tr_b16 v[74:75],v1 offset:6144
ds_read_b64_tr_b16 v[76:77],v2 offset:4096
v_lshlrev_b32_sdwa v156,1,v156 dst_sel:DWORD dst_unused:UNUSED_PAD src0_sel:DWORD src1_sel:WORD_1
v_mfma_f32_16x16x32_bf16 v[128:131],v[64:67],v[16:19],v[112:115]
ds_read_u16 v152,v152
v_lshlrev_b32_sdwa v158,1,v158 dst_sel:DWORD dst_unused:UNUSED_PAD src0_sel:DWORD src1_sel:WORD_1
v_mfma_f32_16x16x32_bf16 v[136:139],v[64:67],v[20:23],v[112:115]
s_waitcnt lgkmcnt(6)
ds_read_u16_d16_hi v154,v154
ds_read_b64_tr_b16 v[78:79],v2 offset:6144
v_lshlrev_b32_sdwa v157,1,v157 dst_sel:DWORD dst_unused:UNUSED_PAD src0_sel:DWORD src1_sel:WORD_1
v_mfma_f32_16x16x32_bf16 v[140:143],v[68:71],v[20:23],v[116:119]
ds_read_b64_tr_b16 v[80:81],v1 offset:8192
ds_read_b64_tr_b16 v[82:83],v1 offset:10240
v_lshlrev_b32_sdwa v159,1,v159 dst_sel:DWORD dst_unused:UNUSED_PAD src0_sel:DWORD src1_sel:WORD_1
v_mfma_f32_16x16x32_bf16 v[132:135],v[68:71],v[16:19],v[116:119]
s_waitcnt lgkmcnt(6)
ds_read_u16 v153,v153
v_mfma_f32_16x16x32_bf16 v[128:131],v[72:75],v[24:27],v[128:131]
ds_read_u16_d16_hi v155,v155
v_mfma_f32_16x16x32_bf16 v[136:139],v[72:75],v[28:31],v[136:139]
s_waitcnt lgkmcnt(4)
ds_read_b64_tr_b16 v[84:85],v2 offset:8192
ds_read_b64_tr_b16 v[86:87],v2 offset:10240
v_mfma_f32_16x16x32_bf16 v[140:143],v[76:79],v[28:31],v[140:143]
ds_read_u16 v156,v156
v_mfma_f32_16x16x32_bf16 v[132:135],v[76:79],v[24:27],v[132:135]
s_waitcnt lgkmcnt(5)
ds_read_u16_d16_hi v158,v158
v_lshl_add_u64 v[144:145],v[144:145],0,v[146:147]
v_mfma_f32_16x16x32_bf16 v[128:131],v[80:83],v[32:35],v[128:131]
v_lshl_add_u64 v[146:147],v[148:149],0,v[150:151]
v_mfma_f32_16x16x32_bf16 v[136:139],v[80:83],v[36:39],v[136:139]
s_waitcnt lgkmcnt(2)
ds_read_u16 v157,v157
v_mfma_f32_16x16x32_bf16 v[140:143],v[84:87],v[36:39],v[140:143]
ds_read_u16_d16_hi v159,v159
v_mfma_f32_16x16x32_bf16 v[132:135],v[84:87],v[32:35],v[132:135]
s_nop 0
v_lshlrev_b32_sdwa v128,1,v128 dst_sel:DWORD dst_unused:UNUSED_PAD src0_sel:DWORD src1_sel:WORD_1
v_lshlrev_b32_sdwa v130,1,v130 dst_sel:DWORD dst_unused:UNUSED_PAD src0_sel:DWORD src1_sel:WORD_1
v_mfma_f32_16x16x32_bf16 v[208:211],v[88:91],v[144:147],v[208:211]
ds_read_u16 v128,v128
v_lshlrev_b32_sdwa v129,1,v129 dst_sel:DWORD dst_unused:UNUSED_PAD src0_sel:DWORD src1_sel:WORD_1
v_mfma_f32_16x16x32_bf16 v[216:219],v[92:95],v[144:147],v[216:219]
ds_read_u16_d16_hi v130,v130
v_lshlrev_b32_sdwa v131,1,v131 dst_sel:DWORD dst_unused:UNUSED_PAD src0_sel:DWORD src1_sel:WORD_1
v_mfma_f32_16x16x32_bf16 v[224:227],v[96:99],v[144:147],v[224:227]
v_lshlrev_b32_sdwa v132,1,v132 dst_sel:DWORD dst_unused:UNUSED_PAD src0_sel:DWORD src1_sel:WORD_1
v_mfma_f32_16x16x32_bf16 v[232:235],v[100:103],v[144:147],v[232:235]
ds_read_u16 v129,v129
v_lshl_add_u64 v[152:153],v[152:153],0,v[154:155]
v_lshlrev_b32_sdwa v134,1,v134 dst_sel:DWORD dst_unused:UNUSED_PAD src0_sel:DWORD src1_sel:WORD_1
v_mfma_f32_16x16x32_bf16 v[240:243],v[104:107],v[144:147],v[240:243]
s_waitcnt lgkmcnt(3)
ds_read_u16_d16_hi v131,v131
v_lshl_add_u64 v[154:155],v[156:157],0,v[158:159]
v_mfma_f32_16x16x32_bf16 v[248:251],v[108:111],v[144:147],v[248:251]
v_lshlrev_b32_sdwa v133,1,v133 dst_sel:DWORD dst_unused:UNUSED_PAD src0_sel:DWORD src1_sel:WORD_1
v_mfma_f32_16x16x32_bf16 v[212:215],v[88:91],v[152:155],v[212:215]
ds_read_u16 v132,v132
v_lshlrev_b32_sdwa v135,1,v135 dst_sel:DWORD dst_unused:UNUSED_PAD src0_sel:DWORD src1_sel:WORD_1
v_mfma_f32_16x16x32_bf16 v[220:223],v[92:95],v[152:155],v[220:223]
ds_read_u16_d16_hi v134,v134
v_lshlrev_b32_sdwa v136,1,v136 dst_sel:DWORD dst_unused:UNUSED_PAD src0_sel:DWORD src1_sel:WORD_1
v_mfma_f32_16x16x32_bf16 v[228:231],v[96:99],v[152:155],v[228:231]
ds_read_b64_tr_b16 v[88:89],v3 offset:6160
ds_read_b64_tr_b16 v[90:91],v3 offset:7696
v_lshlrev_b32_sdwa v138,1,v138 dst_sel:DWORD dst_unused:UNUSED_PAD src0_sel:DWORD src1_sel:WORD_1
v_mfma_f32_16x16x32_bf16 v[236:239],v[100:103],v[152:155],v[236:239]
ds_read_u16 v133,v133
v_lshlrev_b32_sdwa v137,1,v137 dst_sel:DWORD dst_unused:UNUSED_PAD src0_sel:DWORD src1_sel:WORD_1
v_mfma_f32_16x16x32_bf16 v[244:247],v[104:107],v[152:155],v[244:247]
ds_read_u16_d16_hi v135,v135
v_lshlrev_b32_sdwa v139,1,v139 dst_sel:DWORD dst_unused:UNUSED_PAD src0_sel:DWORD src1_sel:WORD_1
v_mfma_f32_16x16x32_bf16 v[252:255],v[108:111],v[152:155],v[252:255]
ds_read_b64_tr_b16 v[92:93],v3 offset:6168
ds_read_b64_tr_b16 v[94:95],v3 offset:7704
v_lshlrev_b32_sdwa v140,1,v140 dst_sel:DWORD dst_unused:UNUSED_PAD src0_sel:DWORD src1_sel:WORD_1
v_mfma_f32_16x16x32_bf16 v[144:147],v[64:67],v[40:43],v[112:115]
ds_read_u16 v136,v136
v_lshlrev_b32_sdwa v142,1,v142 dst_sel:DWORD dst_unused:UNUSED_PAD src0_sel:DWORD src1_sel:WORD_1
v_mfma_f32_16x16x32_bf16 v[152:155],v[64:67],v[44:47],v[112:115]
ds_read_u16_d16_hi v138,v138
v_lshlrev_b32_sdwa v141,1,v141 dst_sel:DWORD dst_unused:UNUSED_PAD src0_sel:DWORD src1_sel:WORD_1
v_mfma_f32_16x16x32_bf16 v[156:159],v[68:71],v[44:47],v[116:119]
ds_read_b64_tr_b16 v[96:97],v3 offset:6224
ds_read_b64_tr_b16 v[98:99],v3 offset:7760
v_lshlrev_b32_sdwa v143,1,v143 dst_sel:DWORD dst_unused:UNUSED_PAD src0_sel:DWORD src1_sel:WORD_1
v_mfma_f32_16x16x32_bf16 v[148:151],v[68:71],v[40:43],v[116:119]
ds_read_u16 v137,v137
v_mfma_f32_16x16x32_bf16 v[144:147],v[72:75],v[48:51],v[144:147]
ds_read_u16_d16_hi v139,v139
v_mfma_f32_16x16x32_bf16 v[152:155],v[72:75],v[52:55],v[152:155]
ds_read_b64_tr_b16 v[100:101],v3 offset:6232
ds_read_b64_tr_b16 v[102:103],v3 offset:7768
v_mfma_f32_16x16x32_bf16 v[156:159],v[76:79],v[52:55],v[156:159]
ds_read_u16 v140,v140
v_mfma_f32_16x16x32_bf16 v[148:151],v[76:79],v[48:51],v[148:151]
s_waitcnt lgkmcnt(15)
ds_read_u16_d16_hi v142,v142
v_lshl_add_u64 v[128:129],v[128:129],0,v[130:131]
v_mfma_f32_16x16x32_bf16 v[144:147],v[80:83],v[56:59],v[144:147]
s_waitcnt lgkmcnt(12)
ds_read_b64_tr_b16 v[104:105],v3 offset:6288
ds_read_b64_tr_b16 v[106:107],v3 offset:7824
v_lshl_add_u64 v[130:131],v[132:133],0,v[134:135]
v_mfma_f32_16x16x32_bf16 v[152:155],v[80:83],v[60:63],v[152:155]
ds_read_u16 v141,v141
v_mfma_f32_16x16x32_bf16 v[156:159],v[84:87],v[60:63],v[156:159]
ds_read_u16_d16_hi v143,v143
v_mfma_f32_16x16x32_bf16 v[148:151],v[84:87],v[56:59],v[148:151]
ds_read_b64_tr_b16 v[108:109],v3 offset:6296
ds_read_b64_tr_b16 v[110:111],v3 offset:7832
v_lshlrev_b32_sdwa v144,1,v144 dst_sel:DWORD dst_unused:UNUSED_PAD src0_sel:DWORD src1_sel:WORD_1
v_lshlrev_b32_sdwa v146,1,v146 dst_sel:DWORD dst_unused:UNUSED_PAD src0_sel:DWORD src1_sel:WORD_1
v_mfma_f32_16x16x32_bf16 v[160:163],v[88:91],v[128:131],v[160:163]
s_waitcnt lgkmcnt(15)
ds_read_u16 v144,v144
v_lshlrev_b32_sdwa v145,1,v145 dst_sel:DWORD dst_unused:UNUSED_PAD src0_sel:DWORD src1_sel:WORD_1
v_mfma_f32_16x16x32_bf16 v[168:171],v[92:95],v[128:131],v[168:171]
s_waitcnt lgkmcnt(13)
ds_read_u16_d16_hi v146,v146
v_lshlrev_b32_sdwa v147,1,v147 dst_sel:DWORD dst_unused:UNUSED_PAD src0_sel:DWORD src1_sel:WORD_1
v_mfma_f32_16x16x32_bf16 v[176:179],v[96:99],v[128:131],v[176:179]
s_waitcnt lgkmcnt(10)
ds_read_b64_tr_b16 v[64:65],v1 offset:512
ds_read_b64_tr_b16 v[66:67],v1 offset:2560
v_lshlrev_b32_sdwa v148,1,v148 dst_sel:DWORD dst_unused:UNUSED_PAD src0_sel:DWORD src1_sel:WORD_1
v_mfma_f32_16x16x32_bf16 v[184:187],v[100:103],v[128:131],v[184:187]
s_waitcnt lgkmcnt(8)
ds_read_u16 v145,v145
v_lshl_add_u64 v[136:137],v[136:137],0,v[138:139]
v_lshlrev_b32_sdwa v150,1,v150 dst_sel:DWORD dst_unused:UNUSED_PAD src0_sel:DWORD src1_sel:WORD_1
v_mfma_f32_16x16x32_bf16 v[192:195],v[104:107],v[128:131],v[192:195]
s_waitcnt lgkmcnt(5)
ds_read_u16_d16_hi v147,v147
v_lshl_add_u64 v[138:139],v[140:141],0,v[142:143]
v_mfma_f32_16x16x32_bf16 v[200:203],v[108:111],v[128:131],v[200:203]
ds_read_b128 v[112:115],v4 offset:1408
ds_read_b64_tr_b16 v[68:69],v2 offset:512
v_lshlrev_b32_sdwa v149,1,v149 dst_sel:DWORD dst_unused:UNUSED_PAD src0_sel:DWORD src1_sel:WORD_1
v_mfma_f32_16x16x32_bf16 v[164:167],v[88:91],v[136:139],v[164:167]
ds_read_u16 v148,v148
v_lshlrev_b32_sdwa v151,1,v151 dst_sel:DWORD dst_unused:UNUSED_PAD src0_sel:DWORD src1_sel:WORD_1
v_mfma_f32_16x16x32_bf16 v[172:175],v[92:95],v[136:139],v[172:175]
ds_read_u16_d16_hi v150,v150
ds_read_b64_tr_b16 v[70:71],v2 offset:2560
v_lshlrev_b32_sdwa v152,1,v152 dst_sel:DWORD dst_unused:UNUSED_PAD src0_sel:DWORD src1_sel:WORD_1
v_mfma_f32_16x16x32_bf16 v[180:183],v[96:99],v[136:139],v[180:183]
ds_read_b128 v[116:119],v4 offset:1440
ds_read_b64_tr_b16 v[72:73],v1 offset:4608
v_lshlrev_b32_sdwa v154,1,v154 dst_sel:DWORD dst_unused:UNUSED_PAD src0_sel:DWORD src1_sel:WORD_1
v_mfma_f32_16x16x32_bf16 v[188:191],v[100:103],v[136:139],v[188:191]
ds_read_u16 v149,v149
v_lshlrev_b32_sdwa v153,1,v153 dst_sel:DWORD dst_unused:UNUSED_PAD src0_sel:DWORD src1_sel:WORD_1
v_mfma_f32_16x16x32_bf16 v[196:199],v[104:107],v[136:139],v[196:199]
ds_read_u16_d16_hi v151,v151
v_lshlrev_b32_sdwa v155,1,v155 dst_sel:DWORD dst_unused:UNUSED_PAD src0_sel:DWORD src1_sel:WORD_1
v_mfma_f32_16x16x32_bf16 v[204:207],v[108:111],v[136:139],v[204:207]
s_waitcnt lgkmcnt(8)
ds_read_b64_tr_b16 v[74:75],v1 offset:6656
ds_read_b64_tr_b16 v[76:77],v2 offset:4608
v_lshlrev_b32_sdwa v156,1,v156 dst_sel:DWORD dst_unused:UNUSED_PAD src0_sel:DWORD src1_sel:WORD_1
v_mfma_f32_16x16x32_bf16 v[128:131],v[64:67],v[16:19],v[112:115]
ds_read_u16 v152,v152
v_lshlrev_b32_sdwa v158,1,v158 dst_sel:DWORD dst_unused:UNUSED_PAD src0_sel:DWORD src1_sel:WORD_1
v_mfma_f32_16x16x32_bf16 v[136:139],v[64:67],v[20:23],v[112:115]
s_waitcnt lgkmcnt(6)
ds_read_u16_d16_hi v154,v154
ds_read_b64_tr_b16 v[78:79],v2 offset:6656
v_lshlrev_b32_sdwa v157,1,v157 dst_sel:DWORD dst_unused:UNUSED_PAD src0_sel:DWORD src1_sel:WORD_1
v_mfma_f32_16x16x32_bf16 v[140:143],v[68:71],v[20:23],v[116:119]
ds_read_b64_tr_b16 v[80:81],v1 offset:8704
ds_read_b64_tr_b16 v[82:83],v1 offset:10752
v_lshlrev_b32_sdwa v159,1,v159 dst_sel:DWORD dst_unused:UNUSED_PAD src0_sel:DWORD src1_sel:WORD_1
v_mfma_f32_16x16x32_bf16 v[132:135],v[68:71],v[16:19],v[116:119]
s_waitcnt lgkmcnt(6)
ds_read_u16 v153,v153
v_mfma_f32_16x16x32_bf16 v[128:131],v[72:75],v[24:27],v[128:131]
ds_read_u16_d16_hi v155,v155
v_mfma_f32_16x16x32_bf16 v[136:139],v[72:75],v[28:31],v[136:139]
s_waitcnt lgkmcnt(4)
ds_read_b64_tr_b16 v[84:85],v2 offset:8704
ds_read_b64_tr_b16 v[86:87],v2 offset:10752
v_mfma_f32_16x16x32_bf16 v[140:143],v[76:79],v[28:31],v[140:143]
ds_read_u16 v156,v156
v_mfma_f32_16x16x32_bf16 v[132:135],v[76:79],v[24:27],v[132:135]
s_waitcnt lgkmcnt(5)
ds_read_u16_d16_hi v158,v158
v_lshl_add_u64 v[144:145],v[144:145],0,v[146:147]
v_mfma_f32_16x16x32_bf16 v[128:131],v[80:83],v[32:35],v[128:131]
v_lshl_add_u64 v[146:147],v[148:149],0,v[150:151]
v_mfma_f32_16x16x32_bf16 v[136:139],v[80:83],v[36:39],v[136:139]
s_waitcnt lgkmcnt(2)
ds_read_u16 v157,v157
v_mfma_f32_16x16x32_bf16 v[140:143],v[84:87],v[36:39],v[140:143]
ds_read_u16_d16_hi v159,v159
v_mfma_f32_16x16x32_bf16 v[132:135],v[84:87],v[32:35],v[132:135]
s_nop 0
v_lshlrev_b32_sdwa v128,1,v128 dst_sel:DWORD dst_unused:UNUSED_PAD src0_sel:DWORD src1_sel:WORD_1
v_lshlrev_b32_sdwa v130,1,v130 dst_sel:DWORD dst_unused:UNUSED_PAD src0_sel:DWORD src1_sel:WORD_1
v_mfma_f32_16x16x32_bf16 v[208:211],v[88:91],v[144:147],v[208:211]
s_waitcnt vmcnt(0)
s_barrier
s_setprio 3
ds_read_u16 v128,v128
v_lshlrev_b32_sdwa v129,1,v129 dst_sel:DWORD dst_unused:UNUSED_PAD src0_sel:DWORD src1_sel:WORD_1
v_lshlrev_b32_sdwa v131,1,v131 dst_sel:DWORD dst_unused:UNUSED_PAD src0_sel:DWORD src1_sel:WORD_1
v_mfma_f32_16x16x32_bf16 v[216:219],v[92:95],v[144:147],v[216:219]
v_fma_mixlo_f16 v120,v140,s99,0
v_fma_mixlo_f16 v122,v141,s99,0
v_mfma_f32_16x16x32_bf16 v[224:227],v[96:99],v[144:147],v[224:227]
s_cbranch_vccz RA62_%=
s_add_u32 m0,s58,0
s_mul_i32 s69,s59,1
s_add_u32 s69,s69,s97
buffer_load_dwordx4 v5,s[44:47],s69 offen lds
s_add_u32 m0,m0,1024
s_add_u32 s69,s69,s53
buffer_load_dwordx4 v6,s[44:47],s69 offen lds
RA62_%=:
ds_read_u16_d16_hi v130,v130
v_lshlrev_b32_sdwa v132,1,v132 dst_sel:DWORD dst_unused:UNUSED_PAD src0_sel:DWORD src1_sel:WORD_1
v_lshlrev_b32_sdwa v134,1,v134 dst_sel:DWORD dst_unused:UNUSED_PAD src0_sel:DWORD src1_sel:WORD_1
v_mfma_f32_16x16x32_bf16 v[232:235],v[100:103],v[144:147],v[232:235]
v_lshl_add_u64 v[152:153],v[152:153],0,v[154:155]
v_mfma_f32_16x16x32_bf16 v[240:243],v[104:107],v[144:147],v[240:243]
s_waitcnt lgkmcnt(2)
ds_read_u16 v129,v129
v_lshl_add_u64 v[154:155],v[156:157],0,v[158:159]
v_mfma_f32_16x16x32_bf16 v[248:251],v[108:111],v[144:147],v[248:251]
v_fma_mixhi_f16 v120,v142,s99,0
v_fma_mixhi_f16 v122,v143,s99,0
v_mfma_f32_16x16x32_bf16 v[212:215],v[88:91],v[152:155],v[212:215]
s_cbranch_vccz RA63_%=
s_add_u32 m0,m0,1024
s_add_u32 s69,s69,s53
buffer_load_dwordx4 v5,s[44:47],s69 offen lds
s_add_u32 m0,m0,s50
s_add_u32 s69,s69,s53
buffer_load_dwordx4 v6,s[44:47],s69 offen lds
RA63_%=:
ds_read_u16_d16_hi v131,v131
v_lshlrev_b32_sdwa v133,1,v133 dst_sel:DWORD dst_unused:UNUSED_PAD src0_sel:DWORD src1_sel:WORD_1
v_lshlrev_b32_sdwa v135,1,v135 dst_sel:DWORD dst_unused:UNUSED_PAD src0_sel:DWORD src1_sel:WORD_1
v_mfma_f32_16x16x32_bf16 v[220:223],v[92:95],v[152:155],v[220:223]
ds_read_b64_tr_b16 v[88:89],v3 offset:12320
v_pk_fma_f16 v121,v120,v120,s54
v_pk_fma_f16 v123,v122,v122,s54
v_mfma_f32_16x16x32_bf16 v[228:231],v[96:99],v[152:155],v[228:231]
ds_read_u16 v132,v132
v_pk_mul_f16 v121,v120,v121
v_pk_mul_f16 v123,v122,v123
v_mfma_f32_16x16x32_bf16 v[236:239],v[100:103],v[152:155],v[236:239]
ds_read_b64_tr_b16 v[90:91],v3 offset:13856
v_lshlrev_b32_sdwa v136,1,v136 dst_sel:DWORD dst_unused:UNUSED_PAD src0_sel:DWORD src1_sel:WORD_1
v_lshlrev_b32_sdwa v138,1,v138 dst_sel:DWORD dst_unused:UNUSED_PAD src0_sel:DWORD src1_sel:WORD_1
v_mfma_f32_16x16x32_bf16 v[244:247],v[104:107],v[152:155],v[244:247]
s_cbranch_vccz RA64_%=
s_add_u32 m0,m0,1024
s_add_u32 s69,s69,s53
buffer_load_dwordx4 v5,s[44:47],s69 offen lds
s_add_u32 m0,m0,1024
s_add_u32 s69,s69,s53
buffer_load_dwordx4 v6,s[44:47],s69 offen lds
RA64_%=:
ds_read_u16_d16_hi v134,v134
v_exp_f16 v120,v121
v_mfma_f32_16x16x32_bf16 v[252:255],v[108:111],v[152:155],v[252:255]
ds_read_b64_tr_b16 v[92:93],v3 offset:12328
v_exp_f16 v122,v123
v_mfma_f32_16x16x32_bf16 v[144:147],v[64:67],v[40:43],v[112:115]
ds_read_u16 v133,v133
v_lshlrev_b32_sdwa v137,1,v137 dst_sel:DWORD dst_unused:UNUSED_PAD src0_sel:DWORD src1_sel:WORD_1
v_lshlrev_b32_sdwa v139,1,v139 dst_sel:DWORD dst_unused:UNUSED_PAD src0_sel:DWORD src1_sel:WORD_1
v_mfma_f32_16x16x32_bf16 v[152:155],v[64:67],v[44:47],v[112:115]
s_cbranch_vccz RA65_%=
s_add_u32 m0,m0,s50
s_add_u32 s69,s69,s53
buffer_load_dwordx4 v5,s[44:47],s69 offen lds
s_add_u32 m0,m0,1024
s_add_u32 s69,s69,s53
buffer_load_dwordx4 v6,s[44:47],s69 offen lds
RA65_%=:
ds_read_b64_tr_b16 v[94:95],v3 offset:13864
v_exp_f16_sdwa v120,v121 dst_sel:WORD_1 dst_unused:UNUSED_PRESERVE src0_sel:WORD_1
v_mfma_f32_16x16x32_bf16 v[156:159],v[68:71],v[44:47],v[116:119]
ds_read_u16_d16_hi v135,v135
v_exp_f16_sdwa v122,v123 dst_sel:WORD_1 dst_unused:UNUSED_PRESERVE src0_sel:WORD_1
v_mfma_f32_16x16x32_bf16 v[148:151],v[68:71],v[40:43],v[116:119]
ds_read_b64_tr_b16 v[96:97],v3 offset:12384
v_pk_fma_f16 v120,v120,s55,s55
v_pk_fma_f16 v122,v122,s55,s55
v_mfma_f32_16x16x32_bf16 v[144:147],v[72:75],v[48:51],v[144:147]
ds_read_u16 v136,v136
v_rcp_f16 v121,v120
v_mfma_f32_16x16x32_bf16 v[152:155],v[72:75],v[52:55],v[152:155]
s_cbranch_vccz RA66_%=
s_add_u32 m0,m0,1024
s_add_u32 s69,s69,s53
buffer_load_dwordx4 v5,s[44:47],s69 offen lds
s_add_u32 m0,m0,s50
s_add_u32 s69,s69,s53
buffer_load_dwordx4 v6,s[44:47],s69 offen lds
RA66_%=:
ds_read_b64_tr_b16 v[98:99],v3 offset:13920
ds_read_b64_tr_b16 v[100:101],v3 offset:12392
v_rcp_f16 v123,v122
v_mfma_f32_16x16x32_bf16 v[156:159],v[76:79],v[52:55],v[156:159]
ds_read_u16_d16_hi v138,v138
v_rcp_f16_sdwa v121,v120 dst_sel:WORD_1 dst_unused:UNUSED_PRESERVE src0_sel:WORD_1
v_mfma_f32_16x16x32_bf16 v[148:151],v[76:79],v[48:51],v[148:151]
ds_read_b64_tr_b16 v[102:103],v3 offset:13928
ds_read_b64_tr_b16 v[104:105],v3 offset:12448
v_rcp_f16_sdwa v123,v122 dst_sel:WORD_1 dst_unused:UNUSED_PRESERVE src0_sel:WORD_1
v_mfma_f32_16x16x32_bf16 v[144:147],v[80:83],v[56:59],v[144:147]
ds_read_u16 v137,v137
v_fma_mix_f32 v140,v140,v121,0 op_sel_hi:[0,1,0]
v_fma_mix_f32 v142,v142,v121,0 op_sel:[0,1,0] op_sel_hi:[0,1,0]
v_mfma_f32_16x16x32_bf16 v[152:155],v[80:83],v[60:63],v[152:155]
s_waitcnt lgkmcnt(8)
s_cbranch_vccz RA67_%=
s_add_u32 m0,m0,1024
s_add_u32 s69,s69,s53
buffer_load_dwordx4 v5,s[44:47],s69 offen lds
s_add_u32 m0,m0,1024
s_add_u32 s69,s69,s53
buffer_load_dwordx4 v6,s[44:47],s69 offen lds
RA67_%=:
ds_read_b64_tr_b16 v[106:107],v3 offset:13984
ds_read_b64_tr_b16 v[108:109],v3 offset:12456
v_lshl_add_u64 v[128:129],v[128:129],0,v[130:131]
v_lshl_add_u64 v[130:131],v[132:133],0,v[134:135]
v_mfma_f32_16x16x32_bf16 v[156:159],v[84:87],v[60:63],v[156:159]
ds_read_u16_d16_hi v139,v139
v_fma_mix_f32 v141,v141,v123,0 op_sel_hi:[0,1,0]
v_fma_mix_f32 v143,v143,v123,0 op_sel:[0,1,0] op_sel_hi:[0,1,0]
v_mfma_f32_16x16x32_bf16 v[148:151],v[84:87],v[56:59],v[148:151]
ds_read_b64_tr_b16 v[110:111],v3 offset:13992
ds_read_b64_tr_b16 v[64:65],v1 offset:12320
v_lshlrev_b32_sdwa v144,1,v144 dst_sel:DWORD dst_unused:UNUSED_PAD src0_sel:DWORD src1_sel:WORD_1
v_lshlrev_b32_sdwa v146,1,v146 dst_sel:DWORD dst_unused:UNUSED_PAD src0_sel:DWORD src1_sel:WORD_1
v_mfma_f32_16x16x32_bf16 v[160:163],v[88:91],v[128:131],v[160:163]
ds_read_u16 v144,v144
v_lshlrev_b32_sdwa v145,1,v145 dst_sel:DWORD dst_unused:UNUSED_PAD src0_sel:DWORD src1_sel:WORD_1
v_lshlrev_b32_sdwa v147,1,v147 dst_sel:DWORD dst_unused:UNUSED_PAD src0_sel:DWORD src1_sel:WORD_1
v_mfma_f32_16x16x32_bf16 v[168:171],v[92:95],v[128:131],v[168:171]
s_waitcnt lgkmcnt(11)
ds_read_b64_tr_b16 v[66:67],v1 offset:14368
ds_read_b128 v[112:115],v4 offset:0
v_fma_mixlo_f16 v120,v156,s99,0
v_fma_mixlo_f16 v122,v157,s99,0
v_mfma_f32_16x16x32_bf16 v[176:179],v[96:99],v[128:131],v[176:179]
s_waitcnt lgkmcnt(10)
ds_read_u16_d16_hi v146,v146
v_lshlrev_b32_sdwa v148,1,v148 dst_sel:DWORD dst_unused:UNUSED_PAD src0_sel:DWORD src1_sel:WORD_1
v_lshlrev_b32_sdwa v150,1,v150 dst_sel:DWORD dst_unused:UNUSED_PAD src0_sel:DWORD src1_sel:WORD_1
v_mfma_f32_16x16x32_bf16 v[184:187],v[100:103],v[128:131],v[184:187]
s_waitcnt lgkmcnt(6)
ds_read_b64_tr_b16 v[68:69],v2 offset:12320
ds_read_b64_tr_b16 v[70:71],v2 offset:14368
v_lshl_add_u64 v[136:137],v[136:137],0,v[138:139]
v_mfma_f32_16x16x32_bf16 v[192:195],v[104:107],v[128:131],v[192:195]
s_waitcnt lgkmcnt(7)
ds_read_u16 v145,v145
s_nop 0
s_mov_b32 s86,0xbe801d5a
v_cvt_pk_bf16_f32 v138,v140,v142
v_cvt_pk_bf16_f32 v139,v141,v143
v_mfma_f32_16x16x32_bf16 v[200:203],v[108:111],v[128:131],v[200:203]
ds_read_b128 v[116:119],v4 offset:32
ds_read_b64_tr_b16 v[72:73],v1 offset:16416
v_fma_mixhi_f16 v120,v158,s99,0
v_fma_mixhi_f16 v122,v159,s99,0
v_mfma_f32_16x16x32_bf16 v[164:167],v[88:91],v[136:139],v[164:167]
ds_read_u16_d16_hi v147,v147
v_lshlrev_b32_sdwa v149,1,v149 dst_sel:DWORD dst_unused:UNUSED_PAD src0_sel:DWORD src1_sel:WORD_1
v_lshlrev_b32_sdwa v151,1,v151 dst_sel:DWORD dst_unused:UNUSED_PAD src0_sel:DWORD src1_sel:WORD_1
v_mfma_f32_16x16x32_bf16 v[172:175],v[92:95],v[136:139],v[172:175]
ds_read_b64_tr_b16 v[74:75],v1 offset:18464
ds_read_b64_tr_b16 v[76:77],v2 offset:16416
v_pk_fma_f16 v121,v120,v120,s54
v_pk_fma_f16 v123,v122,v122,s54
v_mfma_f32_16x16x32_bf16 v[180:183],v[96:99],v[136:139],v[180:183]
ds_read_u16 v148,v148
v_pk_mul_f16 v121,v120,v121
v_pk_mul_f16 v123,v122,v123
v_mfma_f32_16x16x32_bf16 v[188:191],v[100:103],v[136:139],v[188:191]
ds_read_b64_tr_b16 v[78:79],v2 offset:18464
v_lshlrev_b32_sdwa v152,1,v152 dst_sel:DWORD dst_unused:UNUSED_PAD src0_sel:DWORD src1_sel:WORD_1
v_lshlrev_b32_sdwa v154,1,v154 dst_sel:DWORD dst_unused:UNUSED_PAD src0_sel:DWORD src1_sel:WORD_1
v_mfma_f32_16x16x32_bf16 v[196:199],v[104:107],v[136:139],v[196:199]
ds_read_u16_d16_hi v150,v150
v_exp_f16 v120,v121
v_mfma_f32_16x16x32_bf16 v[204:207],v[108:111],v[136:139],v[204:207]
s_waitcnt lgkmcnt(12)
ds_read_b64_tr_b16 v[80:81],v1 offset:20512
v_exp_f16 v122,v123
v_mfma_f32_16x16x32_bf16 v[128:131],v[64:67],v[16:19],v[112:115]
ds_read_u16 v149,v149
v_lshlrev_b32_sdwa v153,1,v153 dst_sel:DWORD dst_unused:UNUSED_PAD src0_sel:DWORD src1_sel:WORD_1
v_lshlrev_b32_sdwa v155,1,v155 dst_sel:DWORD dst_unused:UNUSED_PAD src0_sel:DWORD src1_sel:WORD_1
v_mfma_f32_16x16x32_bf16 v[136:139],v[64:67],v[20:23],v[112:115]
s_waitcnt lgkmcnt(9)
ds_read_b64_tr_b16 v[82:83],v1 offset:22560
v_exp_f16_sdwa v120,v121 dst_sel:WORD_1 dst_unused:UNUSED_PRESERVE src0_sel:WORD_1
v_mfma_f32_16x16x32_bf16 v[140:143],v[68:71],v[20:23],v[116:119]
ds_read_u16_d16_hi v151,v151
v_exp_f16_sdwa v122,v123 dst_sel:WORD_1 dst_unused:UNUSED_PRESERVE src0_sel:WORD_1
v_mfma_f32_16x16x32_bf16 v[132:135],v[68:71],v[16:19],v[116:119]
s_waitcnt lgkmcnt(8)
ds_read_b64_tr_b16 v[84:85],v2 offset:20512
v_pk_fma_f16 v120,v120,s55,s55
v_pk_fma_f16 v122,v122,s55,s55
v_mfma_f32_16x16x32_bf16 v[128:131],v[72:75],v[24:27],v[128:131]
ds_read_u16 v152,v152
v_rcp_f16 v121,v120
v_mfma_f32_16x16x32_bf16 v[136:139],v[72:75],v[28:31],v[136:139]
s_waitcnt lgkmcnt(7)
ds_read_b64_tr_b16 v[86:87],v2 offset:22560
v_rcp_f16 v123,v122
v_mfma_f32_16x16x32_bf16 v[140:143],v[76:79],v[28:31],v[140:143]
ds_read_u16_d16_hi v154,v154
v_rcp_f16_sdwa v121,v120 dst_sel:WORD_1 dst_unused:UNUSED_PRESERVE src0_sel:WORD_1
v_mfma_f32_16x16x32_bf16 v[132:135],v[76:79],v[24:27],v[132:135]
s_waitcnt lgkmcnt(5)
v_rcp_f16_sdwa v123,v122 dst_sel:WORD_1 dst_unused:UNUSED_PRESERVE src0_sel:WORD_1
v_mfma_f32_16x16x32_bf16 v[128:131],v[80:83],v[32:35],v[128:131]
ds_read_u16 v153,v153
v_fma_mix_f32 v156,v156,v121,0 op_sel_hi:[0,1,0]
v_fma_mix_f32 v158,v158,v121,0 op_sel:[0,1,0] op_sel_hi:[0,1,0]
v_mfma_f32_16x16x32_bf16 v[136:139],v[80:83],v[36:39],v[136:139]
s_waitcnt lgkmcnt(2)
v_lshl_add_u64 v[144:145],v[144:145],0,v[146:147]
v_lshl_add_u64 v[146:147],v[148:149],0,v[150:151]
v_mfma_f32_16x16x32_bf16 v[140:143],v[84:87],v[36:39],v[140:143]
ds_read_u16_d16_hi v155,v155
v_fma_mix_f32 v157,v157,v123,0 op_sel_hi:[0,1,0]
v_fma_mix_f32 v159,v159,v123,0 op_sel:[0,1,0] op_sel_hi:[0,1,0]
v_mfma_f32_16x16x32_bf16 v[132:135],v[84:87],v[32:35],v[132:135]
v_lshlrev_b32_sdwa v128,1,v128 dst_sel:DWORD dst_unused:UNUSED_PAD src0_sel:DWORD src1_sel:WORD_1
v_lshlrev_b32_sdwa v130,1,v130 dst_sel:DWORD dst_unused:UNUSED_PAD src0_sel:DWORD src1_sel:WORD_1
v_mfma_f32_16x16x32_bf16 v[208:211],v[88:91],v[144:147],v[208:211]
ds_read_u16 v128,v128
v_lshlrev_b32_sdwa v129,1,v129 dst_sel:DWORD dst_unused:UNUSED_PAD src0_sel:DWORD src1_sel:WORD_1
v_lshlrev_b32_sdwa v131,1,v131 dst_sel:DWORD dst_unused:UNUSED_PAD src0_sel:DWORD src1_sel:WORD_1
v_mfma_f32_16x16x32_bf16 v[216:219],v[92:95],v[144:147],v[216:219]
v_fma_mixlo_f16 v120,v140,s99,0
v_fma_mixlo_f16 v122,v141,s99,0
v_mfma_f32_16x16x32_bf16 v[224:227],v[96:99],v[144:147],v[224:227]
ds_read_u16_d16_hi v130,v130
v_lshlrev_b32_sdwa v132,1,v132 dst_sel:DWORD dst_unused:UNUSED_PAD src0_sel:DWORD src1_sel:WORD_1
v_lshlrev_b32_sdwa v134,1,v134 dst_sel:DWORD dst_unused:UNUSED_PAD src0_sel:DWORD src1_sel:WORD_1
v_mfma_f32_16x16x32_bf16 v[232:235],v[100:103],v[144:147],v[232:235]
s_waitcnt lgkmcnt(2)
v_lshl_add_u64 v[152:153],v[152:153],0,v[154:155]
v_mfma_f32_16x16x32_bf16 v[240:243],v[104:107],v[144:147],v[240:243]
ds_read_u16 v129,v129
v_cvt_pk_bf16_f32 v154,v156,v158
v_cvt_pk_bf16_f32 v155,v157,v159
v_mfma_f32_16x16x32_bf16 v[248:251],v[108:111],v[144:147],v[248:251]
v_fma_mixhi_f16 v120,v142,s99,0
v_fma_mixhi_f16 v122,v143,s99,0
v_mfma_f32_16x16x32_bf16 v[212:215],v[88:91],v[152:155],v[212:215]
s_setprio 2
ds_read_u16_d16_hi v131,v131
v_lshlrev_b32_sdwa v133,1,v133 dst_sel:DWORD dst_unused:UNUSED_PAD src0_sel:DWORD src1_sel:WORD_1
v_lshlrev_b32_sdwa v135,1,v135 dst_sel:DWORD dst_unused:UNUSED_PAD src0_sel:DWORD src1_sel:WORD_1
v_mfma_f32_16x16x32_bf16 v[220:223],v[92:95],v[152:155],v[220:223]
ds_read_b64_tr_b16 v[88:89],v3 offset:18480
v_pk_fma_f16 v121,v120,v120,s54
v_pk_fma_f16 v123,v122,v122,s54
v_mfma_f32_16x16x32_bf16 v[228:231],v[96:99],v[152:155],v[228:231]
ds_read_u16 v132,v132
v_pk_mul_f16 v121,v120,v121
v_pk_mul_f16 v123,v122,v123
v_mfma_f32_16x16x32_bf16 v[236:239],v[100:103],v[152:155],v[236:239]
ds_read_b64_tr_b16 v[90:91],v3 offset:20016
v_lshlrev_b32_sdwa v136,1,v136 dst_sel:DWORD dst_unused:UNUSED_PAD src0_sel:DWORD src1_sel:WORD_1
v_lshlrev_b32_sdwa v138,1,v138 dst_sel:DWORD dst_unused:UNUSED_PAD src0_sel:DWORD src1_sel:WORD_1
v_mfma_f32_16x16x32_bf16 v[244:247],v[104:107],v[152:155],v[244:247]
ds_read_u16_d16_hi v134,v134
v_exp_f16 v120,v121
v_mfma_f32_16x16x32_bf16 v[252:255],v[108:111],v[152:155],v[252:255]
ds_read_b64_tr_b16 v[92:93],v3 offset:18488
v_exp_f16 v122,v123
v_mfma_f32_16x16x32_bf16 v[144:147],v[64:67],v[40:43],v[112:115]
ds_read_u16 v133,v133
v_lshlrev_b32_sdwa v137,1,v137 dst_sel:DWORD dst_unused:UNUSED_PAD src0_sel:DWORD src1_sel:WORD_1
v_lshlrev_b32_sdwa v139,1,v139 dst_sel:DWORD dst_unused:UNUSED_PAD src0_sel:DWORD src1_sel:WORD_1
v_mfma_f32_16x16x32_bf16 v[152:155],v[64:67],v[44:47],v[112:115]
ds_read_b64_tr_b16 v[94:95],v3 offset:20024
v_exp_f16_sdwa v120,v121 dst_sel:WORD_1 dst_unused:UNUSED_PRESERVE src0_sel:WORD_1
v_mfma_f32_16x16x32_bf16 v[156:159],v[68:71],v[44:47],v[116:119]
ds_read_u16_d16_hi v135,v135
v_exp_f16_sdwa v122,v123 dst_sel:WORD_1 dst_unused:UNUSED_PRESERVE src0_sel:WORD_1
v_mfma_f32_16x16x32_bf16 v[148:151],v[68:71],v[40:43],v[116:119]
ds_read_b64_tr_b16 v[96:97],v3 offset:18544
v_pk_fma_f16 v120,v120,s55,s55
v_pk_fma_f16 v122,v122,s55,s55
v_mfma_f32_16x16x32_bf16 v[144:147],v[72:75],v[48:51],v[144:147]
ds_read_u16 v136,v136
v_rcp_f16 v121,v120
v_mfma_f32_16x16x32_bf16 v[152:155],v[72:75],v[52:55],v[152:155]
ds_read_b64_tr_b16 v[98:99],v3 offset:20080
ds_read_b64_tr_b16 v[100:101],v3 offset:18552
v_rcp_f16 v123,v122
v_mfma_f32_16x16x32_bf16 v[156:159],v[76:79],v[52:55],v[156:159]
ds_read_u16_d16_hi v138,v138
v_rcp_f16_sdwa v121,v120 dst_sel:WORD_1 dst_unused:UNUSED_PRESERVE src0_sel:WORD_1
v_mfma_f32_16x16x32_bf16 v[148:151],v[76:79],v[48:51],v[148:151]
ds_read_b64_tr_b16 v[102:103],v3 offset:20088
ds_read_b64_tr_b16 v[104:105],v3 offset:18608
v_rcp_f16_sdwa v123,v122 dst_sel:WORD_1 dst_unused:UNUSED_PRESERVE src0_sel:WORD_1
v_mfma_f32_16x16x32_bf16 v[144:147],v[80:83],v[56:59],v[144:147]
ds_read_u16 v137,v137
v_fma_mix_f32 v140,v140,v121,0 op_sel_hi:[0,1,0]
v_fma_mix_f32 v142,v142,v121,0 op_sel:[0,1,0] op_sel_hi:[0,1,0]
v_mfma_f32_16x16x32_bf16 v[152:155],v[80:83],v[60:63],v[152:155]
s_waitcnt lgkmcnt(8)
ds_read_b64_tr_b16 v[106:107],v3 offset:20144
ds_read_b64_tr_b16 v[108:109],v3 offset:18616
v_lshl_add_u64 v[128:129],v[128:129],0,v[130:131]
v_lshl_add_u64 v[130:131],v[132:133],0,v[134:135]
v_mfma_f32_16x16x32_bf16 v[156:159],v[84:87],v[60:63],v[156:159]
ds_read_u16_d16_hi v139,v139
v_fma_mix_f32 v141,v141,v123,0 op_sel_hi:[0,1,0]
v_fma_mix_f32 v143,v143,v123,0 op_sel:[0,1,0] op_sel_hi:[0,1,0]
v_mfma_f32_16x16x32_bf16 v[148:151],v[84:87],v[56:59],v[148:151]
ds_read_b64_tr_b16 v[110:111],v3 offset:20152
ds_read_b64_tr_b16 v[64:65],v1 offset:12832
v_lshlrev_b32_sdwa v144,1,v144 dst_sel:DWORD dst_unused:UNUSED_PAD src0_sel:DWORD src1_sel:WORD_1
v_lshlrev_b32_sdwa v146,1,v146 dst_sel:DWORD dst_unused:UNUSED_PAD src0_sel:DWORD src1_sel:WORD_1
v_mfma_f32_16x16x32_bf16 v[160:163],v[88:91],v[128:131],v[160:163]
ds_read_u16 v144,v144
v_lshlrev_b32_sdwa v145,1,v145 dst_sel:DWORD dst_unused:UNUSED_PAD src0_sel:DWORD src1_sel:WORD_1
v_lshlrev_b32_sdwa v147,1,v147 dst_sel:DWORD dst_unused:UNUSED_PAD src0_sel:DWORD src1_sel:WORD_1
v_mfma_f32_16x16x32_bf16 v[168:171],v[92:95],v[128:131],v[168:171]
s_waitcnt lgkmcnt(11)
ds_read_b64_tr_b16 v[66:67],v1 offset:14880
ds_read_b128 v[112:115],v4 offset:128
v_fma_mixlo_f16 v120,v156,s99,0
v_fma_mixlo_f16 v122,v157,s99,0
v_mfma_f32_16x16x32_bf16 v[176:179],v[96:99],v[128:131],v[176:179]
s_waitcnt lgkmcnt(10)
ds_read_u16_d16_hi v146,v146
v_lshlrev_b32_sdwa v148,1,v148 dst_sel:DWORD dst_unused:UNUSED_PAD src0_sel:DWORD src1_sel:WORD_1
v_lshlrev_b32_sdwa v150,1,v150 dst_sel:DWORD dst_unused:UNUSED_PAD src0_sel:DWORD src1_sel:WORD_1
v_mfma_f32_16x16x32_bf16 v[184:187],v[100:103],v[128:131],v[184:187]
s_waitcnt lgkmcnt(6)
ds_read_b64_tr_b16 v[68:69],v2 offset:12832
ds_read_b64_tr_b16 v[70:71],v2 offset:14880
v_lshl_add_u64 v[136:137],v[136:137],0,v[138:139]
v_mfma_f32_16x16x32_bf16 v[192:195],v[104:107],v[128:131],v[192:195]
s_waitcnt lgkmcnt(7)
ds_read_u16 v145,v145
v_cvt_pk_bf16_f32 v138,v140,v142
v_cvt_pk_bf16_f32 v139,v141,v143
v_mfma_f32_16x16x32_bf16 v[200:203],v[108:111],v[128:131],v[200:203]
ds_read_b128 v[116:119],v4 offset:160
ds_read_b64_tr_b16 v[72:73],v1 offset:16928
v_fma_mixhi_f16 v120,v158,s99,0
v_fma_mixhi_f16 v122,v159,s99,0
v_mfma_f32_16x16x32_bf16 v[164:167],v[88:91],v[136:139],v[164:167]
ds_read_u16_d16_hi v147,v147
v_lshlrev_b32_sdwa v149,1,v149 dst_sel:DWORD dst_unused:UNUSED_PAD src0_sel:DWORD src1_sel:WORD_1
v_lshlrev_b32_sdwa v151,1,v151 dst_sel:DWORD dst_unused:UNUSED_PAD src0_sel:DWORD src1_sel:WORD_1
v_mfma_f32_16x16x32_bf16 v[172:175],v[92:95],v[136:139],v[172:175]
s_setprio 1
ds_read_b64_tr_b16 v[74:75],v1 offset:18976
ds_read_b64_tr_b16 v[76:77],v2 offset:16928
v_pk_fma_f16 v121,v120,v120,s54
v_pk_fma_f16 v123,v122,v122,s54
v_mfma_f32_16x16x32_bf16 v[180:183],v[96:99],v[136:139],v[180:183]
ds_read_u16 v148,v148
v_pk_mul_f16 v121,v120,v121
v_pk_mul_f16 v123,v122,v123
v_mfma_f32_16x16x32_bf16 v[188:191],v[100:103],v[136:139],v[188:191]
ds_read_b64_tr_b16 v[78:79],v2 offset:18976
v_lshlrev_b32_sdwa v152,1,v152 dst_sel:DWORD dst_unused:UNUSED_PAD src0_sel:DWORD src1_sel:WORD_1
v_lshlrev_b32_sdwa v154,1,v154 dst_sel:DWORD dst_unused:UNUSED_PAD src0_sel:DWORD src1_sel:WORD_1
v_mfma_f32_16x16x32_bf16 v[196:199],v[104:107],v[136:139],v[196:199]
ds_read_u16_d16_hi v150,v150
v_exp_f16 v120,v121
v_mfma_f32_16x16x32_bf16 v[204:207],v[108:111],v[136:139],v[204:207]
s_waitcnt lgkmcnt(12)
ds_read_b64_tr_b16 v[80:81],v1 offset:21024
v_exp_f16 v122,v123
v_mfma_f32_16x16x32_bf16 v[128:131],v[64:67],v[16:19],v[112:115]
ds_read_u16 v149,v149
v_lshlrev_b32_sdwa v153,1,v153 dst_sel:DWORD dst_unused:UNUSED_PAD src0_sel:DWORD src1_sel:WORD_1
v_lshlrev_b32_sdwa v155,1,v155 dst_sel:DWORD dst_unused:UNUSED_PAD src0_sel:DWORD src1_sel:WORD_1
v_mfma_f32_16x16x32_bf16 v[136:139],v[64:67],v[20:23],v[112:115]
s_waitcnt lgkmcnt(9)
ds_read_b64_tr_b16 v[82:83],v1 offset:23072
v_exp_f16_sdwa v120,v121 dst_sel:WORD_1 dst_unused:UNUSED_PRESERVE src0_sel:WORD_1
v_mfma_f32_16x16x32_bf16 v[140:143],v[68:71],v[20:23],v[116:119]
ds_read_u16_d16_hi v151,v151
v_exp_f16_sdwa v122,v123 dst_sel:WORD_1 dst_unused:UNUSED_PRESERVE src0_sel:WORD_1
v_mfma_f32_16x16x32_bf16 v[132:135],v[68:71],v[16:19],v[116:119]
s_waitcnt lgkmcnt(8)
ds_read_b64_tr_b16 v[84:85],v2 offset:21024
v_pk_fma_f16 v120,v120,s55,s55
v_pk_fma_f16 v122,v122,s55,s55
v_mfma_f32_16x16x32_bf16 v[128:131],v[72:75],v[24:27],v[128:131]
ds_read_u16 v152,v152
v_rcp_f16 v121,v120
v_mfma_f32_16x16x32_bf16 v[136:139],v[72:75],v[28:31],v[136:139]
s_waitcnt lgkmcnt(7)
ds_read_b64_tr_b16 v[86:87],v2 offset:23072
v_rcp_f16 v123,v122
v_mfma_f32_16x16x32_bf16 v[140:143],v[76:79],v[28:31],v[140:143]
ds_read_u16_d16_hi v154,v154
v_rcp_f16_sdwa v121,v120 dst_sel:WORD_1 dst_unused:UNUSED_PRESERVE src0_sel:WORD_1
v_mfma_f32_16x16x32_bf16 v[132:135],v[76:79],v[24:27],v[132:135]
s_waitcnt lgkmcnt(5)
v_rcp_f16_sdwa v123,v122 dst_sel:WORD_1 dst_unused:UNUSED_PRESERVE src0_sel:WORD_1
v_mfma_f32_16x16x32_bf16 v[128:131],v[80:83],v[32:35],v[128:131]
ds_read_u16 v153,v153
v_fma_mix_f32 v156,v156,v121,0 op_sel_hi:[0,1,0]
v_fma_mix_f32 v158,v158,v121,0 op_sel:[0,1,0] op_sel_hi:[0,1,0]
v_mfma_f32_16x16x32_bf16 v[136:139],v[80:83],v[36:39],v[136:139]
s_waitcnt lgkmcnt(2)
s_setprio 0
v_lshl_add_u64 v[144:145],v[144:145],0,v[146:147]
v_lshl_add_u64 v[146:147],v[148:149],0,v[150:151]
v_mfma_f32_16x16x32_bf16 v[140:143],v[84:87],v[36:39],v[140:143]
ds_read_u16_d16_hi v155,v155
v_fma_mix_f32 v157,v157,v123,0 op_sel_hi:[0,1,0]
v_fma_mix_f32 v159,v159,v123,0 op_sel:[0,1,0] op_sel_hi:[0,1,0]
v_mfma_f32_16x16x32_bf16 v[132:135],v[84:87],v[32:35],v[132:135]
v_lshlrev_b32_sdwa v128,1,v128 dst_sel:DWORD dst_unused:UNUSED_PAD src0_sel:DWORD src1_sel:WORD_1
v_lshlrev_b32_sdwa v130,1,v130 dst_sel:DWORD dst_unused:UNUSED_PAD src0_sel:DWORD src1_sel:WORD_1
v_mfma_f32_16x16x32_bf16 v[208:211],v[88:91],v[144:147],v[208:211]
s_waitcnt vmcnt(0)
s_barrier
s_setprio 2
ds_read_u16 v128,v128
v_lshlrev_b32_sdwa v129,1,v129 dst_sel:DWORD dst_unused:UNUSED_PAD src0_sel:DWORD src1_sel:WORD_1
v_mfma_f32_16x16x32_bf16 v[216:219],v[92:95],v[144:147],v[216:219]
ds_read_u16_d16_hi v130,v130
v_lshlrev_b32_sdwa v131,1,v131 dst_sel:DWORD dst_unused:UNUSED_PAD src0_sel:DWORD src1_sel:WORD_1
v_mfma_f32_16x16x32_bf16 v[224:227],v[96:99],v[144:147],v[224:227]
s_cbranch_vccz RA68_%=
s_add_u32 m0,s58,12320
s_mul_i32 s69,s59,3
s_add_u32 s69,s69,s97
buffer_load_dwordx4 v5,s[44:47],s69 offen lds
s_add_u32 m0,m0,1024
s_add_u32 s69,s69,s53
buffer_load_dwordx4 v6,s[44:47],s69 offen lds
RA68_%=:
v_lshlrev_b32_sdwa v132,1,v132 dst_sel:DWORD dst_unused:UNUSED_PAD src0_sel:DWORD src1_sel:WORD_1
v_mfma_f32_16x16x32_bf16 v[232:235],v[100:103],v[144:147],v[232:235]
s_waitcnt lgkmcnt(2)
ds_read_u16 v129,v129
v_lshl_add_u64 v[152:153],v[152:153],0,v[154:155]
v_lshlrev_b32_sdwa v134,1,v134 dst_sel:DWORD dst_unused:UNUSED_PAD src0_sel:DWORD src1_sel:WORD_1
v_mfma_f32_16x16x32_bf16 v[240:243],v[104:107],v[144:147],v[240:243]
ds_read_u16_d16_hi v131,v131
v_cvt_pk_bf16_f32 v154,v156,v158
v_cvt_pk_bf16_f32 v155,v157,v159
v_mfma_f32_16x16x32_bf16 v[248:251],v[108:111],v[144:147],v[248:251]
v_lshlrev_b32_sdwa v133,1,v133 dst_sel:DWORD dst_unused:UNUSED_PAD src0_sel:DWORD src1_sel:WORD_1
v_mfma_f32_16x16x32_bf16 v[212:215],v[88:91],v[152:155],v[212:215]
s_cbranch_vccz RA69_%=
s_add_u32 m0,m0,1024
s_add_u32 s69,s69,s53
buffer_load_dwordx4 v5,s[44:47],s69 offen lds
s_add_u32 m0,m0,s50
s_add_u32 s69,s69,s53
buffer_load_dwordx4 v6,s[44:47],s69 offen lds
RA69_%=:
ds_read_u16 v132,v132
v_lshlrev_b32_sdwa v135,1,v135 dst_sel:DWORD dst_unused:UNUSED_PAD src0_sel:DWORD src1_sel:WORD_1
v_mfma_f32_16x16x32_bf16 v[220:223],v[92:95],v[152:155],v[220:223]
ds_read_u16_d16_hi v134,v134
v_lshlrev_b32_sdwa v136,1,v136 dst_sel:DWORD dst_unused:UNUSED_PAD src0_sel:DWORD src1_sel:WORD_1
v_mfma_f32_16x16x32_bf16 v[228:231],v[96:99],v[152:155],v[228:231]
ds_read_b64_tr_b16 v[88:89],v3 offset:0
ds_read_b64_tr_b16 v[90:91],v3 offset:1536
v_lshlrev_b32_sdwa v138,1,v138 dst_sel:DWORD dst_unused:UNUSED_PAD src0_sel:DWORD src1_sel:WORD_1
v_mfma_f32_16x16x32_bf16 v[236:239],v[100:103],v[152:155],v[236:239]
ds_read_u16 v133,v133
v_lshlrev_b32_sdwa v137,1,v137 dst_sel:DWORD dst_unused:UNUSED_PAD src0_sel:DWORD src1_sel:WORD_1
v_mfma_f32_16x16x32_bf16 v[244:247],v[104:107],v[152:155],v[244:247]
ds_read_u16_d16_hi v135,v135
v_lshlrev_b32_sdwa v139,1,v139 dst_sel:DWORD dst_unused:UNUSED_PAD src0_sel:DWORD src1_sel:WORD_1
v_mfma_f32_16x16x32_bf16 v[252:255],v[108:111],v[152:155],v[252:255]
s_cbranch_vccz RA70_%=
s_add_u32 m0,m0,1024
s_add_u32 s69,s69,s53
buffer_load_dwordx4 v5,s[44:47],s69 offen lds
s_add_u32 m0,m0,1024
s_add_u32 s69,s69,s53
buffer_load_dwordx4 v6,s[44:47],s69 offen lds
RA70_%=:
ds_read_b64_tr_b16 v[92:93],v3 offset:8
ds_read_b64_tr_b16 v[94:95],v3 offset:1544
v_lshlrev_b32_sdwa v140,1,v140 dst_sel:DWORD dst_unused:UNUSED_PAD src0_sel:DWORD src1_sel:WORD_1
v_mfma_f32_16x16x32_bf16 v[144:147],v[64:67],v[40:43],v[112:115]
ds_read_u16 v136,v136
v_lshlrev_b32_sdwa v142,1,v142 dst_sel:DWORD dst_unused:UNUSED_PAD src0_sel:DWORD src1_sel:WORD_1
v_mfma_f32_16x16x32_bf16 v[152:155],v[64:67],v[44:47],v[112:115]
ds_read_u16_d16_hi v138,v138
v_lshlrev_b32_sdwa v141,1,v141 dst_sel:DWORD dst_unused:UNUSED_PAD src0_sel:DWORD src1_sel:WORD_1
v_mfma_f32_16x16x32_bf16 v[156:159],v[68:71],v[44:47],v[116:119]
ds_read_b64_tr_b16 v[96:97],v3 offset:64
ds_read_b64_tr_b16 v[98:99],v3 offset:1600
v_lshlrev_b32_sdwa v143,1,v143 dst_sel:DWORD dst_unused:UNUSED_PAD src0_sel:DWORD src1_sel:WORD_1
v_mfma_f32_16x16x32_bf16 v[148:151],v[68:71],v[40:43],v[116:119]
ds_read_u16 v137,v137
v_mfma_f32_16x16x32_bf16 v[144:147],v[72:75],v[48:51],v[144:147]
ds_read_u16_d16_hi v139,v139
v_mfma_f32_16x16x32_bf16 v[152:155],v[72:75],v[52:55],v[152:155]
s_cbranch_vccz RA71_%=
s_add_u32 m0,m0,s50
s_add_u32 s69,s69,s53
buffer_load_dwordx4 v5,s[44:47],s69 offen lds
s_add_u32 m0,m0,1024
s_add_u32 s69,s69,s53
buffer_load_dwordx4 v6,s[44:47],s69 offen lds
RA71_%=:
ds_read_b64_tr_b16 v[100:101],v3 offset:72
ds_read_b64_tr_b16 v[102:103],v3 offset:1608
v_mfma_f32_16x16x32_bf16 v[156:159],v[76:79],v[52:55],v[156:159]
ds_read_u16 v140,v140
v_mfma_f32_16x16x32_bf16 v[148:151],v[76:79],v[48:51],v[148:151]
s_waitcnt lgkmcnt(15)
ds_read_u16_d16_hi v142,v142
v_lshl_add_u64 v[128:129],v[128:129],0,v[130:131]
v_mfma_f32_16x16x32_bf16 v[144:147],v[80:83],v[56:59],v[144:147]
s_waitcnt lgkmcnt(12)
ds_read_b64_tr_b16 v[104:105],v3 offset:128
ds_read_b64_tr_b16 v[106:107],v3 offset:1664
v_lshl_add_u64 v[130:131],v[132:133],0,v[134:135]
v_mfma_f32_16x16x32_bf16 v[152:155],v[80:83],v[60:63],v[152:155]
ds_read_u16 v141,v141
v_mfma_f32_16x16x32_bf16 v[156:159],v[84:87],v[60:63],v[156:159]
ds_read_u16_d16_hi v143,v143
v_mfma_f32_16x16x32_bf16 v[148:151],v[84:87],v[56:59],v[148:151]
s_cbranch_vccz RA72_%=
s_add_u32 m0,m0,1024
s_add_u32 s69,s69,s53
buffer_load_dwordx4 v5,s[44:47],s69 offen lds
s_add_u32 m0,m0,s50
s_add_u32 s69,s69,s53
buffer_load_dwordx4 v6,s[44:47],s69 offen lds
RA72_%=:
ds_read_b64_tr_b16 v[108:109],v3 offset:136
ds_read_b64_tr_b16 v[110:111],v3 offset:1672
v_lshlrev_b32_sdwa v144,1,v144 dst_sel:DWORD dst_unused:UNUSED_PAD src0_sel:DWORD src1_sel:WORD_1
v_lshlrev_b32_sdwa v146,1,v146 dst_sel:DWORD dst_unused:UNUSED_PAD src0_sel:DWORD src1_sel:WORD_1
v_mfma_f32_16x16x32_bf16 v[160:163],v[88:91],v[128:131],v[160:163]
s_waitcnt lgkmcnt(15)
ds_read_u16 v144,v144
v_lshlrev_b32_sdwa v145,1,v145 dst_sel:DWORD dst_unused:UNUSED_PAD src0_sel:DWORD src1_sel:WORD_1
v_mfma_f32_16x16x32_bf16 v[168:171],v[92:95],v[128:131],v[168:171]
s_waitcnt lgkmcnt(13)
ds_read_u16_d16_hi v146,v146
v_lshlrev_b32_sdwa v147,1,v147 dst_sel:DWORD dst_unused:UNUSED_PAD src0_sel:DWORD src1_sel:WORD_1
v_mfma_f32_16x16x32_bf16 v[176:179],v[96:99],v[128:131],v[176:179]
s_waitcnt lgkmcnt(10)
ds_read_b64_tr_b16 v[64:65],v1 offset:0
ds_read_b64_tr_b16 v[66:67],v1 offset:2048
v_lshlrev_b32_sdwa v148,1,v148 dst_sel:DWORD dst_unused:UNUSED_PAD src0_sel:DWORD src1_sel:WORD_1
v_mfma_f32_16x16x32_bf16 v[184:187],v[100:103],v[128:131],v[184:187]
s_waitcnt lgkmcnt(8)
ds_read_u16 v145,v145
v_lshl_add_u64 v[136:137],v[136:137],0,v[138:139]
v_lshlrev_b32_sdwa v150,1,v150 dst_sel:DWORD dst_unused:UNUSED_PAD src0_sel:DWORD src1_sel:WORD_1
v_mfma_f32_16x16x32_bf16 v[192:195],v[104:107],v[128:131],v[192:195]
s_waitcnt lgkmcnt(5)
s_cbranch_vccz RA73_%=
s_add_u32 m0,m0,1024
s_add_u32 s69,s69,s53
buffer_load_dwordx4 v5,s[44:47],s69 offen lds
s_add_u32 m0,m0,1024
s_add_u32 s69,s69,s53
buffer_load_dwordx4 v6,s[44:47],s69 offen lds
RA73_%=:
ds_read_u16_d16_hi v147,v147
v_lshl_add_u64 v[138:139],v[140:141],0,v[142:143]
v_mfma_f32_16x16x32_bf16 v[200:203],v[108:111],v[128:131],v[200:203]
ds_read_b128 v[112:115],v4 offset:256
ds_read_b64_tr_b16 v[68:69],v2 offset:0
v_lshlrev_b32_sdwa v149,1,v149 dst_sel:DWORD dst_unused:UNUSED_PAD src0_sel:DWORD src1_sel:WORD_1
v_mfma_f32_16x16x32_bf16 v[164:167],v[88:91],v[136:139],v[164:167]
ds_read_u16 v148,v148
v_lshlrev_b32_sdwa v151,1,v151 dst_sel:DWORD dst_unused:UNUSED_PAD src0_sel:DWORD src1_sel:WORD_1
v_mfma_f32_16x16x32_bf16 v[172:175],v[92:95],v[136:139],v[172:175]
ds_read_u16_d16_hi v150,v150
ds_read_b64_tr_b16 v[70:71],v2 offset:2048
v_lshlrev_b32_sdwa v152,1,v152 dst_sel:DWORD dst_unused:UNUSED_PAD src0_sel:DWORD src1_sel:WORD_1
v_mfma_f32_16x16x32_bf16 v[180:183],v[96:99],v[136:139],v[180:183]
ds_read_b128 v[116:119],v4 offset:288
ds_read_b64_tr_b16 v[72:73],v1 offset:4096
v_lshlrev_b32_sdwa v154,1,v154 dst_sel:DWORD dst_unused:UNUSED_PAD src0_sel:DWORD src1_sel:WORD_1
v_mfma_f32_16x16x32_bf16 v[188:191],v[100:103],v[136:139],v[188:191]
ds_read_u16 v149,v149
v_lshlrev_b32_sdwa v153,1,v153 dst_sel:DWORD dst_unused:UNUSED_PAD src0_sel:DWORD src1_sel:WORD_1
v_mfma_f32_16x16x32_bf16 v[196:199],v[104:107],v[136:139],v[196:199]
ds_read_u16_d16_hi v151,v151
v_lshlrev_b32_sdwa v155,1,v155 dst_sel:DWORD dst_unused:UNUSED_PAD src0_sel:DWORD src1_sel:WORD_1
v_mfma_f32_16x16x32_bf16 v[204:207],v[108:111],v[136:139],v[204:207]
s_waitcnt lgkmcnt(8)
ds_read_b64_tr_b16 v[74:75],v1 offset:6144
ds_read_b64_tr_b16 v[76:77],v2 offset:4096
v_lshlrev_b32_sdwa v156,1,v156 dst_sel:DWORD dst_unused:UNUSED_PAD src0_sel:DWORD src1_sel:WORD_1
v_mfma_f32_16x16x32_bf16 v[128:131],v[64:67],v[16:19],v[112:115]
ds_read_u16 v152,v152
v_lshlrev_b32_sdwa v158,1,v158 dst_sel:DWORD dst_unused:UNUSED_PAD src0_sel:DWORD src1_sel:WORD_1
v_mfma_f32_16x16x32_bf16 v[136:139],v[64:67],v[20:23],v[112:115]
s_waitcnt lgkmcnt(6)
ds_read_u16_d16_hi v154,v154
ds_read_b64_tr_b16 v[78:79],v2 offset:6144
v_lshlrev_b32_sdwa v157,1,v157 dst_sel:DWORD dst_unused:UNUSED_PAD src0_sel:DWORD src1_sel:WORD_1
v_mfma_f32_16x16x32_bf16 v[140:143],v[68:71],v[20:23],v[116:119]
ds_read_b64_tr_b16 v[80:81],v1 offset:8192
ds_read_b64_tr_b16 v[82:83],v1 offset:10240
v_lshlrev_b32_sdwa v159,1,v159 dst_sel:DWORD dst_unused:UNUSED_PAD src0_sel:DWORD src1_sel:WORD_1
v_mfma_f32_16x16x32_bf16 v[132:135],v[68:71],v[16:19],v[116:119]
s_waitcnt lgkmcnt(6)
ds_read_u16 v153,v153
v_mfma_f32_16x16x32_bf16 v[128:131],v[72:75],v[24:27],v[128:131]
ds_read_u16_d16_hi v155,v155
v_mfma_f32_16x16x32_bf16 v[136:139],v[72:75],v[28:31],v[136:139]
s_waitcnt lgkmcnt(4)
ds_read_b64_tr_b16 v[84:85],v2 offset:8192
ds_read_b64_tr_b16 v[86:87],v2 offset:10240
v_mfma_f32_16x16x32_bf16 v[140:143],v[76:79],v[28:31],v[140:143]
ds_read_u16 v156,v156
v_mfma_f32_16x16x32_bf16 v[132:135],v[76:79],v[24:27],v[132:135]
s_waitcnt lgkmcnt(5)
ds_read_u16_d16_hi v158,v158
v_lshl_add_u64 v[144:145],v[144:145],0,v[146:147]
v_mfma_f32_16x16x32_bf16 v[128:131],v[80:83],v[32:35],v[128:131]
v_lshl_add_u64 v[146:147],v[148:149],0,v[150:151]
v_mfma_f32_16x16x32_bf16 v[136:139],v[80:83],v[36:39],v[136:139]
s_waitcnt lgkmcnt(2)
ds_read_u16 v157,v157
v_mfma_f32_16x16x32_bf16 v[140:143],v[84:87],v[36:39],v[140:143]
ds_read_u16_d16_hi v159,v159
v_mfma_f32_16x16x32_bf16 v[132:135],v[84:87],v[32:35],v[132:135]
s_nop 0
v_lshlrev_b32_sdwa v128,1,v128 dst_sel:DWORD dst_unused:UNUSED_PAD src0_sel:DWORD src1_sel:WORD_1
v_lshlrev_b32_sdwa v130,1,v130 dst_sel:DWORD dst_unused:UNUSED_PAD src0_sel:DWORD src1_sel:WORD_1
v_mfma_f32_16x16x32_bf16 v[208:211],v[88:91],v[144:147],v[208:211]
ds_read_u16 v128,v128
v_lshlrev_b32_sdwa v129,1,v129 dst_sel:DWORD dst_unused:UNUSED_PAD src0_sel:DWORD src1_sel:WORD_1
v_mfma_f32_16x16x32_bf16 v[216:219],v[92:95],v[144:147],v[216:219]
ds_read_u16_d16_hi v130,v130
v_lshlrev_b32_sdwa v131,1,v131 dst_sel:DWORD dst_unused:UNUSED_PAD src0_sel:DWORD src1_sel:WORD_1
v_mfma_f32_16x16x32_bf16 v[224:227],v[96:99],v[144:147],v[224:227]
v_lshlrev_b32_sdwa v132,1,v132 dst_sel:DWORD dst_unused:UNUSED_PAD src0_sel:DWORD src1_sel:WORD_1
v_mfma_f32_16x16x32_bf16 v[232:235],v[100:103],v[144:147],v[232:235]
ds_read_u16 v129,v129
v_lshl_add_u64 v[152:153],v[152:153],0,v[154:155]
v_lshlrev_b32_sdwa v134,1,v134 dst_sel:DWORD dst_unused:UNUSED_PAD src0_sel:DWORD src1_sel:WORD_1
v_mfma_f32_16x16x32_bf16 v[240:243],v[104:107],v[144:147],v[240:243]
s_waitcnt lgkmcnt(3)
ds_read_u16_d16_hi v131,v131
v_lshl_add_u64 v[154:155],v[156:157],0,v[158:159]
v_mfma_f32_16x16x32_bf16 v[248:251],v[108:111],v[144:147],v[248:251]
v_lshlrev_b32_sdwa v133,1,v133 dst_sel:DWORD dst_unused:UNUSED_PAD src0_sel:DWORD src1_sel:WORD_1
v_mfma_f32_16x16x32_bf16 v[212:215],v[88:91],v[152:155],v[212:215]
ds_read_u16 v132,v132
v_lshlrev_b32_sdwa v135,1,v135 dst_sel:DWORD dst_unused:UNUSED_PAD src0_sel:DWORD src1_sel:WORD_1
v_mfma_f32_16x16x32_bf16 v[220:223],v[92:95],v[152:155],v[220:223]
ds_read_u16_d16_hi v134,v134
v_lshlrev_b32_sdwa v136,1,v136 dst_sel:DWORD dst_unused:UNUSED_PAD src0_sel:DWORD src1_sel:WORD_1
v_mfma_f32_16x16x32_bf16 v[228:231],v[96:99],v[152:155],v[228:231]
ds_read_b64_tr_b16 v[88:89],v3 offset:6160
ds_read_b64_tr_b16 v[90:91],v3 offset:7696
v_lshlrev_b32_sdwa v138,1,v138 dst_sel:DWORD dst_unused:UNUSED_PAD src0_sel:DWORD src1_sel:WORD_1
v_mfma_f32_16x16x32_bf16 v[236:239],v[100:103],v[152:155],v[236:239]
ds_read_u16 v133,v133
v_lshlrev_b32_sdwa v137,1,v137 dst_sel:DWORD dst_unused:UNUSED_PAD src0_sel:DWORD src1_sel:WORD_1
v_mfma_f32_16x16x32_bf16 v[244:247],v[104:107],v[152:155],v[244:247]
ds_read_u16_d16_hi v135,v135
v_lshlrev_b32_sdwa v139,1,v139 dst_sel:DWORD dst_unused:UNUSED_PAD src0_sel:DWORD src1_sel:WORD_1
v_mfma_f32_16x16x32_bf16 v[252:255],v[108:111],v[152:155],v[252:255]
ds_read_b64_tr_b16 v[92:93],v3 offset:6168
ds_read_b64_tr_b16 v[94:95],v3 offset:7704
v_lshlrev_b32_sdwa v140,1,v140 dst_sel:DWORD dst_unused:UNUSED_PAD src0_sel:DWORD src1_sel:WORD_1
v_mfma_f32_16x16x32_bf16 v[144:147],v[64:67],v[40:43],v[112:115]
ds_read_u16 v136,v136
v_lshlrev_b32_sdwa v142,1,v142 dst_sel:DWORD dst_unused:UNUSED_PAD src0_sel:DWORD src1_sel:WORD_1
v_mfma_f32_16x16x32_bf16 v[152:155],v[64:67],v[44:47],v[112:115]
ds_read_u16_d16_hi v138,v138
v_lshlrev_b32_sdwa v141,1,v141 dst_sel:DWORD dst_unused:UNUSED_PAD src0_sel:DWORD src1_sel:WORD_1
v_mfma_f32_16x16x32_bf16 v[156:159],v[68:71],v[44:47],v[116:119]
ds_read_b64_tr_b16 v[96:97],v3 offset:6224
ds_read_b64_tr_b16 v[98:99],v3 offset:7760
v_lshlrev_b32_sdwa v143,1,v143 dst_sel:DWORD dst_unused:UNUSED_PAD src0_sel:DWORD src1_sel:WORD_1
v_mfma_f32_16x16x32_bf16 v[148:151],v[68:71],v[40:43],v[116:119]
ds_read_u16 v137,v137
v_mfma_f32_16x16x32_bf16 v[144:147],v[72:75],v[48:51],v[144:147]
ds_read_u16_d16_hi v139,v139
v_mfma_f32_16x16x32_bf16 v[152:155],v[72:75],v[52:55],v[152:155]
ds_read_b64_tr_b16 v[100:101],v3 offset:6232
ds_read_b64_tr_b16 v[102:103],v3 offset:7768
v_mfma_f32_16x16x32_bf16 v[156:159],v[76:79],v[52:55],v[156:159]
ds_read_u16 v140,v140
v_mfma_f32_16x16x32_bf16 v[148:151],v[76:79],v[48:51],v[148:151]
s_waitcnt lgkmcnt(15)
ds_read_u16_d16_hi v142,v142
v_lshl_add_u64 v[128:129],v[128:129],0,v[130:131]
v_mfma_f32_16x16x32_bf16 v[144:147],v[80:83],v[56:59],v[144:147]
s_waitcnt lgkmcnt(12)
ds_read_b64_tr_b16 v[104:105],v3 offset:6288
ds_read_b64_tr_b16 v[106:107],v3 offset:7824
v_lshl_add_u64 v[130:131],v[132:133],0,v[134:135]
v_mfma_f32_16x16x32_bf16 v[152:155],v[80:83],v[60:63],v[152:155]
ds_read_u16 v141,v141
v_mfma_f32_16x16x32_bf16 v[156:159],v[84:87],v[60:63],v[156:159]
ds_read_u16_d16_hi v143,v143
v_mfma_f32_16x16x32_bf16 v[148:151],v[84:87],v[56:59],v[148:151]
ds_read_b64_tr_b16 v[108:109],v3 offset:6296
ds_read_b64_tr_b16 v[110:111],v3 offset:7832
v_lshlrev_b32_sdwa v144,1,v144 dst_sel:DWORD dst_unused:UNUSED_PAD src0_sel:DWORD src1_sel:WORD_1
v_lshlrev_b32_sdwa v146,1,v146 dst_sel:DWORD dst_unused:UNUSED_PAD src0_sel:DWORD src1_sel:WORD_1
s_mov_b32 s86,0xbe801d5a
v_mfma_f32_16x16x32_bf16 v[160:163],v[88:91],v[128:131],v[160:163]
s_waitcnt lgkmcnt(15)
ds_read_u16 v144,v144
v_lshlrev_b32_sdwa v145,1,v145 dst_sel:DWORD dst_unused:UNUSED_PAD src0_sel:DWORD src1_sel:WORD_1
v_mfma_f32_16x16x32_bf16 v[168:171],v[92:95],v[128:131],v[168:171]
s_waitcnt lgkmcnt(13)
ds_read_u16_d16_hi v146,v146
v_lshlrev_b32_sdwa v147,1,v147 dst_sel:DWORD dst_unused:UNUSED_PAD src0_sel:DWORD src1_sel:WORD_1
v_mfma_f32_16x16x32_bf16 v[176:179],v[96:99],v[128:131],v[176:179]
s_waitcnt lgkmcnt(10)
ds_read_b64_tr_b16 v[64:65],v1 offset:512
ds_read_b64_tr_b16 v[66:67],v1 offset:2560
v_lshlrev_b32_sdwa v148,1,v148 dst_sel:DWORD dst_unused:UNUSED_PAD src0_sel:DWORD src1_sel:WORD_1
v_mfma_f32_16x16x32_bf16 v[184:187],v[100:103],v[128:131],v[184:187]
s_waitcnt lgkmcnt(8)
ds_read_u16 v145,v145
v_lshl_add_u64 v[136:137],v[136:137],0,v[138:139]
v_lshlrev_b32_sdwa v150,1,v150 dst_sel:DWORD dst_unused:UNUSED_PAD src0_sel:DWORD src1_sel:WORD_1
v_mfma_f32_16x16x32_bf16 v[192:195],v[104:107],v[128:131],v[192:195]
s_waitcnt lgkmcnt(5)
ds_read_u16_d16_hi v147,v147
v_lshl_add_u64 v[138:139],v[140:141],0,v[142:143]
v_mfma_f32_16x16x32_bf16 v[200:203],v[108:111],v[128:131],v[200:203]
ds_read_b128 v[112:115],v4 offset:384
ds_read_b64_tr_b16 v[68:69],v2 offset:512
v_lshlrev_b32_sdwa v149,1,v149 dst_sel:DWORD dst_unused:UNUSED_PAD src0_sel:DWORD src1_sel:WORD_1
v_mfma_f32_16x16x32_bf16 v[164:167],v[88:91],v[136:139],v[164:167]
ds_read_u16 v148,v148
v_lshlrev_b32_sdwa v151,1,v151 dst_sel:DWORD dst_unused:UNUSED_PAD src0_sel:DWORD src1_sel:WORD_1
v_mfma_f32_16x16x32_bf16 v[172:175],v[92:95],v[136:139],v[172:175]
ds_read_u16_d16_hi v150,v150
ds_read_b64_tr_b16 v[70:71],v2 offset:2560
v_lshlrev_b32_sdwa v152,1,v152 dst_sel:DWORD dst_unused:UNUSED_PAD src0_sel:DWORD src1_sel:WORD_1
v_mfma_f32_16x16x32_bf16 v[180:183],v[96:99],v[136:139],v[180:183]
ds_read_b128 v[116:119],v4 offset:416
ds_read_b64_tr_b16 v[72:73],v1 offset:4608
v_lshlrev_b32_sdwa v154,1,v154 dst_sel:DWORD dst_unused:UNUSED_PAD src0_sel:DWORD src1_sel:WORD_1
v_mfma_f32_16x16x32_bf16 v[188:191],v[100:103],v[136:139],v[188:191]
ds_read_u16 v149,v149
v_lshlrev_b32_sdwa v153,1,v153 dst_sel:DWORD dst_unused:UNUSED_PAD src0_sel:DWORD src1_sel:WORD_1
v_mfma_f32_16x16x32_bf16 v[196:199],v[104:107],v[136:139],v[196:199]
ds_read_u16_d16_hi v151,v151
v_lshlrev_b32_sdwa v155,1,v155 dst_sel:DWORD dst_unused:UNUSED_PAD src0_sel:DWORD src1_sel:WORD_1
v_mfma_f32_16x16x32_bf16 v[204:207],v[108:111],v[136:139],v[204:207]
s_waitcnt lgkmcnt(8)
ds_read_b64_tr_b16 v[74:75],v1 offset:6656
ds_read_b64_tr_b16 v[76:77],v2 offset:4608
v_lshlrev_b32_sdwa v156,1,v156 dst_sel:DWORD dst_unused:UNUSED_PAD src0_sel:DWORD src1_sel:WORD_1
v_mfma_f32_16x16x32_bf16 v[128:131],v[64:67],v[16:19],v[112:115]
ds_read_u16 v152,v152
v_lshlrev_b32_sdwa v158,1,v158 dst_sel:DWORD dst_unused:UNUSED_PAD src0_sel:DWORD src1_sel:WORD_1
v_mfma_f32_16x16x32_bf16 v[136:139],v[64:67],v[20:23],v[112:115]
s_waitcnt lgkmcnt(6)
ds_read_u16_d16_hi v154,v154
ds_read_b64_tr_b16 v[78:79],v2 offset:6656
v_lshlrev_b32_sdwa v157,1,v157 dst_sel:DWORD dst_unused:UNUSED_PAD src0_sel:DWORD src1_sel:WORD_1
v_mfma_f32_16x16x32_bf16 v[140:143],v[68:71],v[20:23],v[116:119]
ds_read_b64_tr_b16 v[80:81],v1 offset:8704
ds_read_b64_tr_b16 v[82:83],v1 offset:10752
v_lshlrev_b32_sdwa v159,1,v159 dst_sel:DWORD dst_unused:UNUSED_PAD src0_sel:DWORD src1_sel:WORD_1
v_mfma_f32_16x16x32_bf16 v[132:135],v[68:71],v[16:19],v[116:119]
s_waitcnt lgkmcnt(6)
ds_read_u16 v153,v153
v_mfma_f32_16x16x32_bf16 v[128:131],v[72:75],v[24:27],v[128:131]
ds_read_u16_d16_hi v155,v155
v_mfma_f32_16x16x32_bf16 v[136:139],v[72:75],v[28:31],v[136:139]
s_waitcnt lgkmcnt(4)
ds_read_b64_tr_b16 v[84:85],v2 offset:8704
ds_read_b64_tr_b16 v[86:87],v2 offset:10752
v_mfma_f32_16x16x32_bf16 v[140:143],v[76:79],v[28:31],v[140:143]
ds_read_u16 v156,v156
v_mfma_f32_16x16x32_bf16 v[132:135],v[76:79],v[24:27],v[132:135]
s_waitcnt lgkmcnt(5)
ds_read_u16_d16_hi v158,v158
v_lshl_add_u64 v[144:145],v[144:145],0,v[146:147]
v_mfma_f32_16x16x32_bf16 v[128:131],v[80:83],v[32:35],v[128:131]
v_lshl_add_u64 v[146:147],v[148:149],0,v[150:151]
v_mfma_f32_16x16x32_bf16 v[136:139],v[80:83],v[36:39],v[136:139]
s_waitcnt lgkmcnt(2)
ds_read_u16 v157,v157
v_mfma_f32_16x16x32_bf16 v[140:143],v[84:87],v[36:39],v[140:143]
ds_read_u16_d16_hi v159,v159
v_mfma_f32_16x16x32_bf16 v[132:135],v[84:87],v[32:35],v[132:135]
s_nop 0
v_lshlrev_b32_sdwa v128,1,v128 dst_sel:DWORD dst_unused:UNUSED_PAD src0_sel:DWORD src1_sel:WORD_1
v_lshlrev_b32_sdwa v130,1,v130 dst_sel:DWORD dst_unused:UNUSED_PAD src0_sel:DWORD src1_sel:WORD_1
v_mfma_f32_16x16x32_bf16 v[208:211],v[88:91],v[144:147],v[208:211]
s_waitcnt vmcnt(0)
s_barrier
s_setprio 3
ds_read_u16 v128,v128
v_lshlrev_b32_sdwa v129,1,v129 dst_sel:DWORD dst_unused:UNUSED_PAD src0_sel:DWORD src1_sel:WORD_1
v_lshlrev_b32_sdwa v131,1,v131 dst_sel:DWORD dst_unused:UNUSED_PAD src0_sel:DWORD src1_sel:WORD_1
v_mfma_f32_16x16x32_bf16 v[216:219],v[92:95],v[144:147],v[216:219]
v_fma_mixlo_f16 v120,v140,s99,0
v_fma_mixlo_f16 v122,v141,s99,0
v_mfma_f32_16x16x32_bf16 v[224:227],v[96:99],v[144:147],v[224:227]
s_cbranch_vccz RA74_%=
s_add_u32 m0,s58,0
s_mul_i32 s69,s59,5
s_add_u32 s69,s69,s97
buffer_load_dwordx4 v5,s[44:47],s69 offen lds
s_add_u32 m0,m0,1024
s_add_u32 s69,s69,s53
buffer_load_dwordx4 v6,s[44:47],s69 offen lds
RA74_%=:
ds_read_u16_d16_hi v130,v130
v_lshlrev_b32_sdwa v132,1,v132 dst_sel:DWORD dst_unused:UNUSED_PAD src0_sel:DWORD src1_sel:WORD_1
v_lshlrev_b32_sdwa v134,1,v134 dst_sel:DWORD dst_unused:UNUSED_PAD src0_sel:DWORD src1_sel:WORD_1
v_mfma_f32_16x16x32_bf16 v[232:235],v[100:103],v[144:147],v[232:235]
v_lshl_add_u64 v[152:153],v[152:153],0,v[154:155]
v_mfma_f32_16x16x32_bf16 v[240:243],v[104:107],v[144:147],v[240:243]
s_waitcnt lgkmcnt(2)
ds_read_u16 v129,v129
v_lshl_add_u64 v[154:155],v[156:157],0,v[158:159]
v_mfma_f32_16x16x32_bf16 v[248:251],v[108:111],v[144:147],v[248:251]
v_fma_mixhi_f16 v120,v142,s99,0
v_fma_mixhi_f16 v122,v143,s99,0
v_mfma_f32_16x16x32_bf16 v[212:215],v[88:91],v[152:155],v[212:215]
s_cbranch_vccz RA75_%=
s_add_u32 m0,m0,1024
s_add_u32 s69,s69,s53
buffer_load_dwordx4 v5,s[44:47],s69 offen lds
s_add_u32 m0,m0,s50
s_add_u32 s69,s69,s53
buffer_load_dwordx4 v6,s[44:47],s69 offen lds
RA75_%=:
ds_read_u16_d16_hi v131,v131
v_lshlrev_b32_sdwa v133,1,v133 dst_sel:DWORD dst_unused:UNUSED_PAD src0_sel:DWORD src1_sel:WORD_1
v_lshlrev_b32_sdwa v135,1,v135 dst_sel:DWORD dst_unused:UNUSED_PAD src0_sel:DWORD src1_sel:WORD_1
v_mfma_f32_16x16x32_bf16 v[220:223],v[92:95],v[152:155],v[220:223]
ds_read_b64_tr_b16 v[88:89],v3 offset:12320
v_pk_fma_f16 v121,v120,v120,s54
v_pk_fma_f16 v123,v122,v122,s54
v_mfma_f32_16x16x32_bf16 v[228:231],v[96:99],v[152:155],v[228:231]
ds_read_u16 v132,v132
v_pk_mul_f16 v121,v120,v121
v_pk_mul_f16 v123,v122,v123
v_mfma_f32_16x16x32_bf16 v[236:239],v[100:103],v[152:155],v[236:239]
ds_read_b64_tr_b16 v[90:91],v3 offset:13856
v_lshlrev_b32_sdwa v136,1,v136 dst_sel:DWORD dst_unused:UNUSED_PAD src0_sel:DWORD src1_sel:WORD_1
v_lshlrev_b32_sdwa v138,1,v138 dst_sel:DWORD dst_unused:UNUSED_PAD src0_sel:DWORD src1_sel:WORD_1
v_mfma_f32_16x16x32_bf16 v[244:247],v[104:107],v[152:155],v[244:247]
s_cbranch_vccz RA76_%=
s_add_u32 m0,m0,1024
s_add_u32 s69,s69,s53
buffer_load_dwordx4 v5,s[44:47],s69 offen lds
s_add_u32 m0,m0,1024
s_add_u32 s69,s69,s53
buffer_load_dwordx4 v6,s[44:47],s69 offen lds
RA76_%=:
ds_read_u16_d16_hi v134,v134
v_exp_f16 v120,v121
v_mfma_f32_16x16x32_bf16 v[252:255],v[108:111],v[152:155],v[252:255]
ds_read_b64_tr_b16 v[92:93],v3 offset:12328
v_exp_f16 v122,v123
v_mfma_f32_16x16x32_bf16 v[144:147],v[64:67],v[40:43],v[112:115]
ds_read_u16 v133,v133
v_lshlrev_b32_sdwa v137,1,v137 dst_sel:DWORD dst_unused:UNUSED_PAD src0_sel:DWORD src1_sel:WORD_1
v_lshlrev_b32_sdwa v139,1,v139 dst_sel:DWORD dst_unused:UNUSED_PAD src0_sel:DWORD src1_sel:WORD_1
v_mfma_f32_16x16x32_bf16 v[152:155],v[64:67],v[44:47],v[112:115]
s_cbranch_vccz RA77_%=
s_add_u32 m0,m0,s50
s_add_u32 s69,s69,s53
buffer_load_dwordx4 v5,s[44:47],s69 offen lds
s_add_u32 m0,m0,1024
s_add_u32 s69,s69,s53
buffer_load_dwordx4 v6,s[44:47],s69 offen lds
RA77_%=:
ds_read_b64_tr_b16 v[94:95],v3 offset:13864
v_exp_f16_sdwa v120,v121 dst_sel:WORD_1 dst_unused:UNUSED_PRESERVE src0_sel:WORD_1
v_mfma_f32_16x16x32_bf16 v[156:159],v[68:71],v[44:47],v[116:119]
ds_read_u16_d16_hi v135,v135
v_exp_f16_sdwa v122,v123 dst_sel:WORD_1 dst_unused:UNUSED_PRESERVE src0_sel:WORD_1
v_mfma_f32_16x16x32_bf16 v[148:151],v[68:71],v[40:43],v[116:119]
ds_read_b64_tr_b16 v[96:97],v3 offset:12384
v_pk_fma_f16 v120,v120,s55,s55
v_pk_fma_f16 v122,v122,s55,s55
v_mfma_f32_16x16x32_bf16 v[144:147],v[72:75],v[48:51],v[144:147]
ds_read_u16 v136,v136
v_rcp_f16 v121,v120
v_mfma_f32_16x16x32_bf16 v[152:155],v[72:75],v[52:55],v[152:155]
s_cbranch_vccz RA78_%=
s_add_u32 m0,m0,1024
s_add_u32 s69,s69,s53
buffer_load_dwordx4 v5,s[44:47],s69 offen lds
s_add_u32 m0,m0,s50
s_add_u32 s69,s69,s53
buffer_load_dwordx4 v6,s[44:47],s69 offen lds
RA78_%=:
ds_read_b64_tr_b16 v[98:99],v3 offset:13920
ds_read_b64_tr_b16 v[100:101],v3 offset:12392
v_rcp_f16 v123,v122
v_mfma_f32_16x16x32_bf16 v[156:159],v[76:79],v[52:55],v[156:159]
ds_read_u16_d16_hi v138,v138
v_rcp_f16_sdwa v121,v120 dst_sel:WORD_1 dst_unused:UNUSED_PRESERVE src0_sel:WORD_1
v_mfma_f32_16x16x32_bf16 v[148:151],v[76:79],v[48:51],v[148:151]
ds_read_b64_tr_b16 v[102:103],v3 offset:13928
ds_read_b64_tr_b16 v[104:105],v3 offset:12448
v_rcp_f16_sdwa v123,v122 dst_sel:WORD_1 dst_unused:UNUSED_PRESERVE src0_sel:WORD_1
v_mfma_f32_16x16x32_bf16 v[144:147],v[80:83],v[56:59],v[144:147]
ds_read_u16 v137,v137
v_fma_mix_f32 v140,v140,v121,0 op_sel_hi:[0,1,0]
v_fma_mix_f32 v142,v142,v121,0 op_sel:[0,1,0] op_sel_hi:[0,1,0]
v_mfma_f32_16x16x32_bf16 v[152:155],v[80:83],v[60:63],v[152:155]
s_waitcnt lgkmcnt(8)
s_cbranch_vccz RA79_%=
s_add_u32 m0,m0,1024
s_add_u32 s69,s69,s53
buffer_load_dwordx4 v5,s[44:47],s69 offen lds
s_add_u32 m0,m0,1024
s_add_u32 s69,s69,s53
buffer_load_dwordx4 v6,s[44:47],s69 offen lds
RA79_%=:
ds_read_b64_tr_b16 v[106:107],v3 offset:13984
ds_read_b64_tr_b16 v[108:109],v3 offset:12456
v_lshl_add_u64 v[128:129],v[128:129],0,v[130:131]
v_lshl_add_u64 v[130:131],v[132:133],0,v[134:135]
v_mfma_f32_16x16x32_bf16 v[156:159],v[84:87],v[60:63],v[156:159]
ds_read_u16_d16_hi v139,v139
v_fma_mix_f32 v141,v141,v123,0 op_sel_hi:[0,1,0]
v_fma_mix_f32 v143,v143,v123,0 op_sel:[0,1,0] op_sel_hi:[0,1,0]
v_mfma_f32_16x16x32_bf16 v[148:151],v[84:87],v[56:59],v[148:151]
ds_read_b64_tr_b16 v[110:111],v3 offset:13992
ds_read_b64_tr_b16 v[64:65],v1 offset:12320
v_lshlrev_b32_sdwa v144,1,v144 dst_sel:DWORD dst_unused:UNUSED_PAD src0_sel:DWORD src1_sel:WORD_1
v_lshlrev_b32_sdwa v146,1,v146 dst_sel:DWORD dst_unused:UNUSED_PAD src0_sel:DWORD src1_sel:WORD_1
v_mfma_f32_16x16x32_bf16 v[160:163],v[88:91],v[128:131],v[160:163]
ds_read_u16 v144,v144
v_lshlrev_b32_sdwa v145,1,v145 dst_sel:DWORD dst_unused:UNUSED_PAD src0_sel:DWORD src1_sel:WORD_1
v_lshlrev_b32_sdwa v147,1,v147 dst_sel:DWORD dst_unused:UNUSED_PAD src0_sel:DWORD src1_sel:WORD_1
v_mfma_f32_16x16x32_bf16 v[168:171],v[92:95],v[128:131],v[168:171]
s_waitcnt lgkmcnt(11)
ds_read_b64_tr_b16 v[66:67],v1 offset:14368
ds_read_b128 v[112:115],v4 offset:512
v_fma_mixlo_f16 v120,v156,s99,0
v_fma_mixlo_f16 v122,v157,s99,0
v_mfma_f32_16x16x32_bf16 v[176:179],v[96:99],v[128:131],v[176:179]
s_waitcnt lgkmcnt(10)
ds_read_u16_d16_hi v146,v146
v_lshlrev_b32_sdwa v148,1,v148 dst_sel:DWORD dst_unused:UNUSED_PAD src0_sel:DWORD src1_sel:WORD_1
v_lshlrev_b32_sdwa v150,1,v150 dst_sel:DWORD dst_unused:UNUSED_PAD src0_sel:DWORD src1_sel:WORD_1
v_mfma_f32_16x16x32_bf16 v[184:187],v[100:103],v[128:131],v[184:187]
s_waitcnt lgkmcnt(6)
ds_read_b64_tr_b16 v[68:69],v2 offset:12320
ds_read_b64_tr_b16 v[70:71],v2 offset:14368
v_lshl_add_u64 v[136:137],v[136:137],0,v[138:139]
v_mfma_f32_16x16x32_bf16 v[192:195],v[104:107],v[128:131],v[192:195]
s_waitcnt lgkmcnt(7)
ds_read_u16 v145,v145
v_cvt_pk_bf16_f32 v138,v140,v142
v_cvt_pk_bf16_f32 v139,v141,v143
v_mfma_f32_16x16x32_bf16 v[200:203],v[108:111],v[128:131],v[200:203]
ds_read_b128 v[116:119],v4 offset:544
ds_read_b64_tr_b16 v[72:73],v1 offset:16416
v_fma_mixhi_f16 v120,v158,s99,0
v_fma_mixhi_f16 v122,v159,s99,0
v_mfma_f32_16x16x32_bf16 v[164:167],v[88:91],v[136:139],v[164:167]
ds_read_u16_d16_hi v147,v147
v_lshlrev_b32_sdwa v149,1,v149 dst_sel:DWORD dst_unused:UNUSED_PAD src0_sel:DWORD src1_sel:WORD_1
v_lshlrev_b32_sdwa v151,1,v151 dst_sel:DWORD dst_unused:UNUSED_PAD src0_sel:DWORD src1_sel:WORD_1
v_mfma_f32_16x16x32_bf16 v[172:175],v[92:95],v[136:139],v[172:175]
ds_read_b64_tr_b16 v[74:75],v1 offset:18464
ds_read_b64_tr_b16 v[76:77],v2 offset:16416
v_pk_fma_f16 v121,v120,v120,s54
v_pk_fma_f16 v123,v122,v122,s54
v_mfma_f32_16x16x32_bf16 v[180:183],v[96:99],v[136:139],v[180:183]
ds_read_u16 v148,v148
v_pk_mul_f16 v121,v120,v121
v_pk_mul_f16 v123,v122,v123
v_mfma_f32_16x16x32_bf16 v[188:191],v[100:103],v[136:139],v[188:191]
ds_read_b64_tr_b16 v[78:79],v2 offset:18464
v_lshlrev_b32_sdwa v152,1,v152 dst_sel:DWORD dst_unused:UNUSED_PAD src0_sel:DWORD src1_sel:WORD_1
v_lshlrev_b32_sdwa v154,1,v154 dst_sel:DWORD dst_unused:UNUSED_PAD src0_sel:DWORD src1_sel:WORD_1
v_mfma_f32_16x16x32_bf16 v[196:199],v[104:107],v[136:139],v[196:199]
ds_read_u16_d16_hi v150,v150
v_exp_f16 v120,v121
v_mfma_f32_16x16x32_bf16 v[204:207],v[108:111],v[136:139],v[204:207]
s_waitcnt lgkmcnt(12)
ds_read_b64_tr_b16 v[80:81],v1 offset:20512
v_exp_f16 v122,v123
v_mfma_f32_16x16x32_bf16 v[128:131],v[64:67],v[16:19],v[112:115]
ds_read_u16 v149,v149
v_lshlrev_b32_sdwa v153,1,v153 dst_sel:DWORD dst_unused:UNUSED_PAD src0_sel:DWORD src1_sel:WORD_1
v_lshlrev_b32_sdwa v155,1,v155 dst_sel:DWORD dst_unused:UNUSED_PAD src0_sel:DWORD src1_sel:WORD_1
v_mfma_f32_16x16x32_bf16 v[136:139],v[64:67],v[20:23],v[112:115]
s_waitcnt lgkmcnt(9)
ds_read_b64_tr_b16 v[82:83],v1 offset:22560
v_exp_f16_sdwa v120,v121 dst_sel:WORD_1 dst_unused:UNUSED_PRESERVE src0_sel:WORD_1
v_mfma_f32_16x16x32_bf16 v[140:143],v[68:71],v[20:23],v[116:119]
ds_read_u16_d16_hi v151,v151
v_exp_f16_sdwa v122,v123 dst_sel:WORD_1 dst_unused:UNUSED_PRESERVE src0_sel:WORD_1
v_mfma_f32_16x16x32_bf16 v[132:135],v[68:71],v[16:19],v[116:119]
s_waitcnt lgkmcnt(8)
ds_read_b64_tr_b16 v[84:85],v2 offset:20512
v_pk_fma_f16 v120,v120,s55,s55
v_pk_fma_f16 v122,v122,s55,s55
v_mfma_f32_16x16x32_bf16 v[128:131],v[72:75],v[24:27],v[128:131]
ds_read_u16 v152,v152
v_rcp_f16 v121,v120
v_mfma_f32_16x16x32_bf16 v[136:139],v[72:75],v[28:31],v[136:139]
s_waitcnt lgkmcnt(7)
ds_read_b64_tr_b16 v[86:87],v2 offset:22560
v_rcp_f16 v123,v122
v_mfma_f32_16x16x32_bf16 v[140:143],v[76:79],v[28:31],v[140:143]
ds_read_u16_d16_hi v154,v154
v_rcp_f16_sdwa v121,v120 dst_sel:WORD_1 dst_unused:UNUSED_PRESERVE src0_sel:WORD_1
v_mfma_f32_16x16x32_bf16 v[132:135],v[76:79],v[24:27],v[132:135]
s_waitcnt lgkmcnt(5)
v_rcp_f16_sdwa v123,v122 dst_sel:WORD_1 dst_unused:UNUSED_PRESERVE src0_sel:WORD_1
v_mfma_f32_16x16x32_bf16 v[128:131],v[80:83],v[32:35],v[128:131]
ds_read_u16 v153,v153
v_fma_mix_f32 v156,v156,v121,0 op_sel_hi:[0,1,0]
v_fma_mix_f32 v158,v158,v121,0 op_sel:[0,1,0] op_sel_hi:[0,1,0]
v_mfma_f32_16x16x32_bf16 v[136:139],v[80:83],v[36:39],v[136:139]
s_waitcnt lgkmcnt(2)
v_lshl_add_u64 v[144:145],v[144:145],0,v[146:147]
v_lshl_add_u64 v[146:147],v[148:149],0,v[150:151]
v_mfma_f32_16x16x32_bf16 v[140:143],v[84:87],v[36:39],v[140:143]
ds_read_u16_d16_hi v155,v155
v_fma_mix_f32 v157,v157,v123,0 op_sel_hi:[0,1,0]
v_fma_mix_f32 v159,v159,v123,0 op_sel:[0,1,0] op_sel_hi:[0,1,0]
v_mfma_f32_16x16x32_bf16 v[132:135],v[84:87],v[32:35],v[132:135]
v_lshlrev_b32_sdwa v128,1,v128 dst_sel:DWORD dst_unused:UNUSED_PAD src0_sel:DWORD src1_sel:WORD_1
v_lshlrev_b32_sdwa v130,1,v130 dst_sel:DWORD dst_unused:UNUSED_PAD src0_sel:DWORD src1_sel:WORD_1
v_mfma_f32_16x16x32_bf16 v[208:211],v[88:91],v[144:147],v[208:211]
ds_read_u16 v128,v128
v_lshlrev_b32_sdwa v129,1,v129 dst_sel:DWORD dst_unused:UNUSED_PAD src0_sel:DWORD src1_sel:WORD_1
v_lshlrev_b32_sdwa v131,1,v131 dst_sel:DWORD dst_unused:UNUSED_PAD src0_sel:DWORD src1_sel:WORD_1
v_mfma_f32_16x16x32_bf16 v[216:219],v[92:95],v[144:147],v[216:219]
v_fma_mixlo_f16 v120,v140,s99,0
v_fma_mixlo_f16 v122,v141,s99,0
v_mfma_f32_16x16x32_bf16 v[224:227],v[96:99],v[144:147],v[224:227]
ds_read_u16_d16_hi v130,v130
v_lshlrev_b32_sdwa v132,1,v132 dst_sel:DWORD dst_unused:UNUSED_PAD src0_sel:DWORD src1_sel:WORD_1
v_lshlrev_b32_sdwa v134,1,v134 dst_sel:DWORD dst_unused:UNUSED_PAD src0_sel:DWORD src1_sel:WORD_1
v_mfma_f32_16x16x32_bf16 v[232:235],v[100:103],v[144:147],v[232:235]
s_waitcnt lgkmcnt(2)
v_lshl_add_u64 v[152:153],v[152:153],0,v[154:155]
v_mfma_f32_16x16x32_bf16 v[240:243],v[104:107],v[144:147],v[240:243]
ds_read_u16 v129,v129
v_cvt_pk_bf16_f32 v154,v156,v158
v_cvt_pk_bf16_f32 v155,v157,v159
v_mfma_f32_16x16x32_bf16 v[248:251],v[108:111],v[144:147],v[248:251]
v_fma_mixhi_f16 v120,v142,s99,0
v_fma_mixhi_f16 v122,v143,s99,0
v_mfma_f32_16x16x32_bf16 v[212:215],v[88:91],v[152:155],v[212:215]
s_setprio 2
ds_read_u16_d16_hi v131,v131
v_lshlrev_b32_sdwa v133,1,v133 dst_sel:DWORD dst_unused:UNUSED_PAD src0_sel:DWORD src1_sel:WORD_1
v_lshlrev_b32_sdwa v135,1,v135 dst_sel:DWORD dst_unused:UNUSED_PAD src0_sel:DWORD src1_sel:WORD_1
v_mfma_f32_16x16x32_bf16 v[220:223],v[92:95],v[152:155],v[220:223]
ds_read_b64_tr_b16 v[88:89],v3 offset:18480
v_pk_fma_f16 v121,v120,v120,s54
v_pk_fma_f16 v123,v122,v122,s54
v_mfma_f32_16x16x32_bf16 v[228:231],v[96:99],v[152:155],v[228:231]
ds_read_u16 v132,v132
v_pk_mul_f16 v121,v120,v121
v_pk_mul_f16 v123,v122,v123
v_mfma_f32_16x16x32_bf16 v[236:239],v[100:103],v[152:155],v[236:239]
ds_read_b64_tr_b16 v[90:91],v3 offset:20016
v_lshlrev_b32_sdwa v136,1,v136 dst_sel:DWORD dst_unused:UNUSED_PAD src0_sel:DWORD src1_sel:WORD_1
v_lshlrev_b32_sdwa v138,1,v138 dst_sel:DWORD dst_unused:UNUSED_PAD src0_sel:DWORD src1_sel:WORD_1
v_mfma_f32_16x16x32_bf16 v[244:247],v[104:107],v[152:155],v[244:247]
ds_read_u16_d16_hi v134,v134
v_exp_f16 v120,v121
v_mfma_f32_16x16x32_bf16 v[252:255],v[108:111],v[152:155],v[252:255]
ds_read_b64_tr_b16 v[92:93],v3 offset:18488
v_exp_f16 v122,v123
v_mfma_f32_16x16x32_bf16 v[144:147],v[64:67],v[40:43],v[112:115]
ds_read_u16 v133,v133
v_lshlrev_b32_sdwa v137,1,v137 dst_sel:DWORD dst_unused:UNUSED_PAD src0_sel:DWORD src1_sel:WORD_1
v_lshlrev_b32_sdwa v139,1,v139 dst_sel:DWORD dst_unused:UNUSED_PAD src0_sel:DWORD src1_sel:WORD_1
v_mfma_f32_16x16x32_bf16 v[152:155],v[64:67],v[44:47],v[112:115]
ds_read_b64_tr_b16 v[94:95],v3 offset:20024
v_exp_f16_sdwa v120,v121 dst_sel:WORD_1 dst_unused:UNUSED_PRESERVE src0_sel:WORD_1
v_mfma_f32_16x16x32_bf16 v[156:159],v[68:71],v[44:47],v[116:119]
ds_read_u16_d16_hi v135,v135
v_exp_f16_sdwa v122,v123 dst_sel:WORD_1 dst_unused:UNUSED_PRESERVE src0_sel:WORD_1
v_mfma_f32_16x16x32_bf16 v[148:151],v[68:71],v[40:43],v[116:119]
ds_read_b64_tr_b16 v[96:97],v3 offset:18544
v_pk_fma_f16 v120,v120,s55,s55
v_pk_fma_f16 v122,v122,s55,s55
v_mfma_f32_16x16x32_bf16 v[144:147],v[72:75],v[48:51],v[144:147]
ds_read_u16 v136,v136
v_rcp_f16 v121,v120
v_mfma_f32_16x16x32_bf16 v[152:155],v[72:75],v[52:55],v[152:155]
ds_read_b64_tr_b16 v[98:99],v3 offset:20080
ds_read_b64_tr_b16 v[100:101],v3 offset:18552
v_rcp_f16 v123,v122
v_mfma_f32_16x16x32_bf16 v[156:159],v[76:79],v[52:55],v[156:159]
ds_read_u16_d16_hi v138,v138
v_rcp_f16_sdwa v121,v120 dst_sel:WORD_1 dst_unused:UNUSED_PRESERVE src0_sel:WORD_1
v_mfma_f32_16x16x32_bf16 v[148:151],v[76:79],v[48:51],v[148:151]
ds_read_b64_tr_b16 v[102:103],v3 offset:20088
ds_read_b64_tr_b16 v[104:105],v3 offset:18608
v_rcp_f16_sdwa v123,v122 dst_sel:WORD_1 dst_unused:UNUSED_PRESERVE src0_sel:WORD_1
v_mfma_f32_16x16x32_bf16 v[144:147],v[80:83],v[56:59],v[144:147]
ds_read_u16 v137,v137
v_fma_mix_f32 v140,v140,v121,0 op_sel_hi:[0,1,0]
v_fma_mix_f32 v142,v142,v121,0 op_sel:[0,1,0] op_sel_hi:[0,1,0]
v_mfma_f32_16x16x32_bf16 v[152:155],v[80:83],v[60:63],v[152:155]
s_waitcnt lgkmcnt(8)
ds_read_b64_tr_b16 v[106:107],v3 offset:20144
ds_read_b64_tr_b16 v[108:109],v3 offset:18616
v_lshl_add_u64 v[128:129],v[128:129],0,v[130:131]
v_lshl_add_u64 v[130:131],v[132:133],0,v[134:135]
v_mfma_f32_16x16x32_bf16 v[156:159],v[84:87],v[60:63],v[156:159]
ds_read_u16_d16_hi v139,v139
v_fma_mix_f32 v141,v141,v123,0 op_sel_hi:[0,1,0]
v_fma_mix_f32 v143,v143,v123,0 op_sel:[0,1,0] op_sel_hi:[0,1,0]
v_mfma_f32_16x16x32_bf16 v[148:151],v[84:87],v[56:59],v[148:151]
ds_read_b64_tr_b16 v[110:111],v3 offset:20152
ds_read_b64_tr_b16 v[64:65],v1 offset:12832
v_lshlrev_b32_sdwa v144,1,v144 dst_sel:DWORD dst_unused:UNUSED_PAD src0_sel:DWORD src1_sel:WORD_1
v_lshlrev_b32_sdwa v146,1,v146 dst_sel:DWORD dst_unused:UNUSED_PAD src0_sel:DWORD src1_sel:WORD_1
v_mfma_f32_16x16x32_bf16 v[160:163],v[88:91],v[128:131],v[160:163]
ds_read_u16 v144,v144
v_lshlrev_b32_sdwa v145,1,v145 dst_sel:DWORD dst_unused:UNUSED_PAD src0_sel:DWORD src1_sel:WORD_1
v_lshlrev_b32_sdwa v147,1,v147 dst_sel:DWORD dst_unused:UNUSED_PAD src0_sel:DWORD src1_sel:WORD_1
v_mfma_f32_16x16x32_bf16 v[168:171],v[92:95],v[128:131],v[168:171]
s_waitcnt lgkmcnt(11)
ds_read_b64_tr_b16 v[66:67],v1 offset:14880
ds_read_b128 v[112:115],v4 offset:640
v_fma_mixlo_f16 v120,v156,s99,0
v_fma_mixlo_f16 v122,v157,s99,0
v_mfma_f32_16x16x32_bf16 v[176:179],v[96:99],v[128:131],v[176:179]
s_waitcnt lgkmcnt(10)
ds_read_u16_d16_hi v146,v146
v_lshlrev_b32_sdwa v148,1,v148 dst_sel:DWORD dst_unused:UNUSED_PAD src0_sel:DWORD src1_sel:WORD_1
v_lshlrev_b32_sdwa v150,1,v150 dst_sel:DWORD dst_unused:UNUSED_PAD src0_sel:DWORD src1_sel:WORD_1
v_mfma_f32_16x16x32_bf16 v[184:187],v[100:103],v[128:131],v[184:187]
s_waitcnt lgkmcnt(6)
ds_read_b64_tr_b16 v[68:69],v2 offset:12832
ds_read_b64_tr_b16 v[70:71],v2 offset:14880
v_lshl_add_u64 v[136:137],v[136:137],0,v[138:139]
v_mfma_f32_16x16x32_bf16 v[192:195],v[104:107],v[128:131],v[192:195]
s_waitcnt lgkmcnt(7)
ds_read_u16 v145,v145
v_cvt_pk_bf16_f32 v138,v140,v142
v_cvt_pk_bf16_f32 v139,v141,v143
v_mfma_f32_16x16x32_bf16 v[200:203],v[108:111],v[128:131],v[200:203]
ds_read_b128 v[116:119],v4 offset:672
ds_read_b64_tr_b16 v[72:73],v1 offset:16928
v_fma_mixhi_f16 v120,v158,s99,0
v_fma_mixhi_f16 v122,v159,s99,0
v_mfma_f32_16x16x32_bf16 v[164:167],v[88:91],v[136:139],v[164:167]
ds_read_u16_d16_hi v147,v147
v_lshlrev_b32_sdwa v149,1,v149 dst_sel:DWORD dst_unused:UNUSED_PAD src0_sel:DWORD src1_sel:WORD_1
v_lshlrev_b32_sdwa v151,1,v151 dst_sel:DWORD dst_unused:UNUSED_PAD src0_sel:DWORD src1_sel:WORD_1
v_mfma_f32_16x16x32_bf16 v[172:175],v[92:95],v[136:139],v[172:175]
s_setprio 1
ds_read_b64_tr_b16 v[74:75],v1 offset:18976
ds_read_b64_tr_b16 v[76:77],v2 offset:16928
v_pk_fma_f16 v121,v120,v120,s54
v_pk_fma_f16 v123,v122,v122,s54
v_mfma_f32_16x16x32_bf16 v[180:183],v[96:99],v[136:139],v[180:183]
ds_read_u16 v148,v148
v_pk_mul_f16 v121,v120,v121
v_pk_mul_f16 v123,v122,v123
v_mfma_f32_16x16x32_bf16 v[188:191],v[100:103],v[136:139],v[188:191]
ds_read_b64_tr_b16 v[78:79],v2 offset:18976
v_lshlrev_b32_sdwa v152,1,v152 dst_sel:DWORD dst_unused:UNUSED_PAD src0_sel:DWORD src1_sel:WORD_1
v_lshlrev_b32_sdwa v154,1,v154 dst_sel:DWORD dst_unused:UNUSED_PAD src0_sel:DWORD src1_sel:WORD_1
v_mfma_f32_16x16x32_bf16 v[196:199],v[104:107],v[136:139],v[196:199]
ds_read_u16_d16_hi v150,v150
v_exp_f16 v120,v121
v_mfma_f32_16x16x32_bf16 v[204:207],v[108:111],v[136:139],v[204:207]
s_waitcnt lgkmcnt(12)
ds_read_b64_tr_b16 v[80:81],v1 offset:21024
v_exp_f16 v122,v123
v_mfma_f32_16x16x32_bf16 v[128:131],v[64:67],v[16:19],v[112:115]
ds_read_u16 v149,v149
v_lshlrev_b32_sdwa v153,1,v153 dst_sel:DWORD dst_unused:UNUSED_PAD src0_sel:DWORD src1_sel:WORD_1
v_lshlrev_b32_sdwa v155,1,v155 dst_sel:DWORD dst_unused:UNUSED_PAD src0_sel:DWORD src1_sel:WORD_1
v_mfma_f32_16x16x32_bf16 v[136:139],v[64:67],v[20:23],v[112:115]
s_waitcnt lgkmcnt(9)
ds_read_b64_tr_b16 v[82:83],v1 offset:23072
v_exp_f16_sdwa v120,v121 dst_sel:WORD_1 dst_unused:UNUSED_PRESERVE src0_sel:WORD_1
v_mfma_f32_16x16x32_bf16 v[140:143],v[68:71],v[20:23],v[116:119]
ds_read_u16_d16_hi v151,v151
v_exp_f16_sdwa v122,v123 dst_sel:WORD_1 dst_unused:UNUSED_PRESERVE src0_sel:WORD_1
v_mfma_f32_16x16x32_bf16 v[132:135],v[68:71],v[16:19],v[116:119]
s_waitcnt lgkmcnt(8)
ds_read_b64_tr_b16 v[84:85],v2 offset:21024
v_pk_fma_f16 v120,v120,s55,s55
v_pk_fma_f16 v122,v122,s55,s55
v_mfma_f32_16x16x32_bf16 v[128:131],v[72:75],v[24:27],v[128:131]
ds_read_u16 v152,v152
v_rcp_f16 v121,v120
v_mfma_f32_16x16x32_bf16 v[136:139],v[72:75],v[28:31],v[136:139]
s_waitcnt lgkmcnt(7)
ds_read_b64_tr_b16 v[86:87],v2 offset:23072
v_rcp_f16 v123,v122
v_mfma_f32_16x16x32_bf16 v[140:143],v[76:79],v[28:31],v[140:143]
ds_read_u16_d16_hi v154,v154
v_rcp_f16_sdwa v121,v120 dst_sel:WORD_1 dst_unused:UNUSED_PRESERVE src0_sel:WORD_1
v_mfma_f32_16x16x32_bf16 v[132:135],v[76:79],v[24:27],v[132:135]
s_waitcnt lgkmcnt(5)
v_rcp_f16_sdwa v123,v122 dst_sel:WORD_1 dst_unused:UNUSED_PRESERVE src0_sel:WORD_1
v_mfma_f32_16x16x32_bf16 v[128:131],v[80:83],v[32:35],v[128:131]
ds_read_u16 v153,v153
v_fma_mix_f32 v156,v156,v121,0 op_sel_hi:[0,1,0]
v_fma_mix_f32 v158,v158,v121,0 op_sel:[0,1,0] op_sel_hi:[0,1,0]
v_mfma_f32_16x16x32_bf16 v[136:139],v[80:83],v[36:39],v[136:139]
s_waitcnt lgkmcnt(2)
s_setprio 0
v_lshl_add_u64 v[144:145],v[144:145],0,v[146:147]
v_lshl_add_u64 v[146:147],v[148:149],0,v[150:151]
v_mfma_f32_16x16x32_bf16 v[140:143],v[84:87],v[36:39],v[140:143]
ds_read_u16_d16_hi v155,v155
v_fma_mix_f32 v157,v157,v123,0 op_sel_hi:[0,1,0]
v_fma_mix_f32 v159,v159,v123,0 op_sel:[0,1,0] op_sel_hi:[0,1,0]
v_mfma_f32_16x16x32_bf16 v[132:135],v[84:87],v[32:35],v[132:135]
v_lshlrev_b32_sdwa v128,1,v128 dst_sel:DWORD dst_unused:UNUSED_PAD src0_sel:DWORD src1_sel:WORD_1
v_lshlrev_b32_sdwa v130,1,v130 dst_sel:DWORD dst_unused:UNUSED_PAD src0_sel:DWORD src1_sel:WORD_1
v_mfma_f32_16x16x32_bf16 v[208:211],v[88:91],v[144:147],v[208:211]
s_waitcnt vmcnt(0)
s_barrier
s_setprio 3
ds_read_u16 v128,v128
v_lshlrev_b32_sdwa v129,1,v129 dst_sel:DWORD dst_unused:UNUSED_PAD src0_sel:DWORD src1_sel:WORD_1
v_lshlrev_b32_sdwa v131,1,v131 dst_sel:DWORD dst_unused:UNUSED_PAD src0_sel:DWORD src1_sel:WORD_1
v_mfma_f32_16x16x32_bf16 v[216:219],v[92:95],v[144:147],v[216:219]
v_fma_mixlo_f16 v120,v140,s99,0
v_fma_mixlo_f16 v122,v141,s99,0
v_mfma_f32_16x16x32_bf16 v[224:227],v[96:99],v[144:147],v[224:227]
s_cbranch_vccz RA80_%=
s_add_u32 m0,s58,12320
s_mul_i32 s69,s59,7
s_add_u32 s69,s69,s97
buffer_load_dwordx4 v5,s[44:47],s69 offen lds
s_add_u32 m0,m0,1024
s_add_u32 s69,s69,s53
buffer_load_dwordx4 v6,s[44:47],s69 offen lds
RA80_%=:
ds_read_u16_d16_hi v130,v130
v_lshlrev_b32_sdwa v132,1,v132 dst_sel:DWORD dst_unused:UNUSED_PAD src0_sel:DWORD src1_sel:WORD_1
v_lshlrev_b32_sdwa v134,1,v134 dst_sel:DWORD dst_unused:UNUSED_PAD src0_sel:DWORD src1_sel:WORD_1
v_mfma_f32_16x16x32_bf16 v[232:235],v[100:103],v[144:147],v[232:235]
s_waitcnt lgkmcnt(2)
v_lshl_add_u64 v[152:153],v[152:153],0,v[154:155]
v_mfma_f32_16x16x32_bf16 v[240:243],v[104:107],v[144:147],v[240:243]
ds_read_u16 v129,v129
v_cvt_pk_bf16_f32 v154,v156,v158
v_cvt_pk_bf16_f32 v155,v157,v159
v_mfma_f32_16x16x32_bf16 v[248:251],v[108:111],v[144:147],v[248:251]
v_fma_mixhi_f16 v120,v142,s99,0
v_fma_mixhi_f16 v122,v143,s99,0
v_mfma_f32_16x16x32_bf16 v[212:215],v[88:91],v[152:155],v[212:215]
s_cbranch_vccz RA81_%=
s_add_u32 m0,m0,1024
s_add_u32 s69,s69,s53
buffer_load_dwordx4 v5,s[44:47],s69 offen lds
s_add_u32 m0,m0,s50
s_add_u32 s69,s69,s53
buffer_load_dwordx4 v6,s[44:47],s69 offen lds
RA81_%=:
ds_read_u16_d16_hi v131,v131
v_lshlrev_b32_sdwa v133,1,v133 dst_sel:DWORD dst_unused:UNUSED_PAD src0_sel:DWORD src1_sel:WORD_1
v_lshlrev_b32_sdwa v135,1,v135 dst_sel:DWORD dst_unused:UNUSED_PAD src0_sel:DWORD src1_sel:WORD_1
v_mfma_f32_16x16x32_bf16 v[220:223],v[92:95],v[152:155],v[220:223]
ds_read_b64_tr_b16 v[88:89],v3 offset:0
v_pk_fma_f16 v121,v120,v120,s54
v_pk_fma_f16 v123,v122,v122,s54
s_nop 0
s_mov_b32 s86,0xbe801d5a
v_mfma_f32_16x16x32_bf16 v[228:231],v[96:99],v[152:155],v[228:231]
ds_read_u16 v132,v132
v_pk_mul_f16 v121,v120,v121
v_pk_mul_f16 v123,v122,v123
v_mfma_f32_16x16x32_bf16 v[236:239],v[100:103],v[152:155],v[236:239]
s_cbranch_vccz RA82_%=
s_add_u32 m0,m0,1024
s_add_u32 s69,s69,s53
buffer_load_dwordx4 v5,s[44:47],s69 offen lds
s_add_u32 m0,m0,1024
s_add_u32 s69,s69,s53
buffer_load_dwordx4 v6,s[44:47],s69 offen lds
RA82_%=:
ds_read_b64_tr_b16 v[90:91],v3 offset:1536
v_lshlrev_b32_sdwa v136,1,v136 dst_sel:DWORD dst_unused:UNUSED_PAD src0_sel:DWORD src1_sel:WORD_1
v_lshlrev_b32_sdwa v138,1,v138 dst_sel:DWORD dst_unused:UNUSED_PAD src0_sel:DWORD src1_sel:WORD_1
v_mfma_f32_16x16x32_bf16 v[244:247],v[104:107],v[152:155],v[244:247]
ds_read_u16_d16_hi v134,v134
v_exp_f16 v120,v121
v_mfma_f32_16x16x32_bf16 v[252:255],v[108:111],v[152:155],v[252:255]
ds_read_b64_tr_b16 v[92:93],v3 offset:8
v_exp_f16 v122,v123
v_mfma_f32_16x16x32_bf16 v[144:147],v[64:67],v[40:43],v[112:115]
ds_read_u16 v133,v133
v_lshlrev_b32_sdwa v137,1,v137 dst_sel:DWORD dst_unused:UNUSED_PAD src0_sel:DWORD src1_sel:WORD_1
v_lshlrev_b32_sdwa v139,1,v139 dst_sel:DWORD dst_unused:UNUSED_PAD src0_sel:DWORD src1_sel:WORD_1
v_mfma_f32_16x16x32_bf16 v[152:155],v[64:67],v[44:47],v[112:115]
s_cbranch_vccz RA83_%=
s_add_u32 m0,m0,s50
s_add_u32 s69,s69,s53
buffer_load_dwordx4 v5,s[44:47],s69 offen lds
s_add_u32 m0,m0,1024
s_add_u32 s69,s69,s53
buffer_load_dwordx4 v6,s[44:47],s69 offen lds
RA83_%=:
ds_read_b64_tr_b16 v[94:95],v3 offset:1544
v_exp_f16_sdwa v120,v121 dst_sel:WORD_1 dst_unused:UNUSED_PRESERVE src0_sel:WORD_1
v_mfma_f32_16x16x32_bf16 v[156:159],v[68:71],v[44:47],v[116:119]
ds_read_u16_d16_hi v135,v135
v_exp_f16_sdwa v122,v123 dst_sel:WORD_1 dst_unused:UNUSED_PRESERVE src0_sel:WORD_1
v_mfma_f32_16x16x32_bf16 v[148:151],v[68:71],v[40:43],v[116:119]
ds_read_b64_tr_b16 v[96:97],v3 offset:64
v_pk_fma_f16 v120,v120,s55,s55
v_pk_fma_f16 v122,v122,s55,s55
v_mfma_f32_16x16x32_bf16 v[144:147],v[72:75],v[48:51],v[144:147]
ds_read_u16 v136,v136
v_rcp_f16 v121,v120
v_mfma_f32_16x16x32_bf16 v[152:155],v[72:75],v[52:55],v[152:155]
s_cbranch_vccz RA84_%=
s_add_u32 m0,m0,1024
s_add_u32 s69,s69,s53
buffer_load_dwordx4 v5,s[44:47],s69 offen lds
s_add_u32 m0,m0,s50
s_add_u32 s69,s69,s53
buffer_load_dwordx4 v6,s[44:47],s69 offen lds
RA84_%=:
ds_read_b64_tr_b16 v[98:99],v3 offset:1600
ds_read_b64_tr_b16 v[100:101],v3 offset:72
v_rcp_f16 v123,v122
v_mfma_f32_16x16x32_bf16 v[156:159],v[76:79],v[52:55],v[156:159]
ds_read_u16_d16_hi v138,v138
v_rcp_f16_sdwa v121,v120 dst_sel:WORD_1 dst_unused:UNUSED_PRESERVE src0_sel:WORD_1
v_mfma_f32_16x16x32_bf16 v[148:151],v[76:79],v[48:51],v[148:151]
ds_read_b64_tr_b16 v[102:103],v3 offset:1608
ds_read_b64_tr_b16 v[104:105],v3 offset:128
v_rcp_f16_sdwa v123,v122 dst_sel:WORD_1 dst_unused:UNUSED_PRESERVE src0_sel:WORD_1
v_mfma_f32_16x16x32_bf16 v[144:147],v[80:83],v[56:59],v[144:147]
ds_read_u16 v137,v137
v_fma_mix_f32 v140,v140,v121,0 op_sel_hi:[0,1,0]
v_fma_mix_f32 v142,v142,v121,0 op_sel:[0,1,0] op_sel_hi:[0,1,0]
v_mfma_f32_16x16x32_bf16 v[152:155],v[80:83],v[60:63],v[152:155]
s_waitcnt lgkmcnt(8)
s_cbranch_vccz RA85_%=
s_add_u32 m0,m0,1024
s_add_u32 s69,s69,s53
buffer_load_dwordx4 v5,s[44:47],s69 offen lds
s_add_u32 m0,m0,1024
s_add_u32 s69,s69,s53
buffer_load_dwordx4 v6,s[44:47],s69 offen lds
RA85_%=:
ds_read_b64_tr_b16 v[106:107],v3 offset:1664
ds_read_b64_tr_b16 v[108:109],v3 offset:136
v_lshl_add_u64 v[128:129],v[128:129],0,v[130:131]
v_lshl_add_u64 v[130:131],v[132:133],0,v[134:135]
v_mfma_f32_16x16x32_bf16 v[156:159],v[84:87],v[60:63],v[156:159]
ds_read_u16_d16_hi v139,v139
v_fma_mix_f32 v141,v141,v123,0 op_sel_hi:[0,1,0]
v_fma_mix_f32 v143,v143,v123,0 op_sel:[0,1,0] op_sel_hi:[0,1,0]
v_mfma_f32_16x16x32_bf16 v[148:151],v[84:87],v[56:59],v[148:151]
ds_read_b64_tr_b16 v[110:111],v3 offset:1672
ds_read_b64_tr_b16 v[64:65],v1 offset:0
v_lshlrev_b32_sdwa v144,1,v144 dst_sel:DWORD dst_unused:UNUSED_PAD src0_sel:DWORD src1_sel:WORD_1
v_lshlrev_b32_sdwa v146,1,v146 dst_sel:DWORD dst_unused:UNUSED_PAD src0_sel:DWORD src1_sel:WORD_1
v_mfma_f32_16x16x32_bf16 v[160:163],v[88:91],v[128:131],v[160:163]
s_cbranch_vccz RA86_%=
s_add_u32 m0,s100,0
s_mul_i32 s69,s59,9
s_add_u32 s69,s69,s97
buffer_load_dwordx4 v5,s[44:47],s69 offen lds
s_add_u32 m0,m0,1024
s_add_u32 s69,s69,s53
buffer_load_dwordx4 v6,s[44:47],s69 offen lds
RA86_%=:
ds_read_u16 v144,v144
v_lshlrev_b32_sdwa v145,1,v145 dst_sel:DWORD dst_unused:UNUSED_PAD src0_sel:DWORD src1_sel:WORD_1
v_lshlrev_b32_sdwa v147,1,v147 dst_sel:DWORD dst_unused:UNUSED_PAD src0_sel:DWORD src1_sel:WORD_1
v_mfma_f32_16x16x32_bf16 v[168:171],v[92:95],v[128:131],v[168:171]
s_waitcnt lgkmcnt(11)
ds_read_b64_tr_b16 v[66:67],v1 offset:2048
ds_read_b128 v[112:115],v4 offset:768
v_fma_mixlo_f16 v120,v156,s99,0
v_fma_mixlo_f16 v122,v157,s99,0
v_mfma_f32_16x16x32_bf16 v[176:179],v[96:99],v[128:131],v[176:179]
s_waitcnt lgkmcnt(10)
ds_read_u16_d16_hi v146,v146
v_lshlrev_b32_sdwa v148,1,v148 dst_sel:DWORD dst_unused:UNUSED_PAD src0_sel:DWORD src1_sel:WORD_1
v_lshlrev_b32_sdwa v150,1,v150 dst_sel:DWORD dst_unused:UNUSED_PAD src0_sel:DWORD src1_sel:WORD_1
v_mfma_f32_16x16x32_bf16 v[184:187],v[100:103],v[128:131],v[184:187]
s_waitcnt lgkmcnt(6)
ds_read_b64_tr_b16 v[68:69],v2 offset:0
ds_read_b64_tr_b16 v[70:71],v2 offset:2048
v_lshl_add_u64 v[136:137],v[136:137],0,v[138:139]
v_mfma_f32_16x16x32_bf16 v[192:195],v[104:107],v[128:131],v[192:195]
s_waitcnt lgkmcnt(7)
s_cbranch_vccz RA87_%=
s_add_u32 m0,m0,1024
s_add_u32 s69,s69,s53
buffer_load_dwordx4 v5,s[44:47],s69 offen lds
s_add_u32 m0,m0,s50
s_add_u32 s69,s69,s53
buffer_load_dwordx4 v6,s[44:47],s69 offen lds
RA87_%=:
ds_read_u16 v145,v145
v_cvt_pk_bf16_f32 v138,v140,v142
v_cvt_pk_bf16_f32 v139,v141,v143
v_mfma_f32_16x16x32_bf16 v[200:203],v[108:111],v[128:131],v[200:203]
ds_read_b128 v[116:119],v4 offset:800
ds_read_b64_tr_b16 v[72:73],v1 offset:4096
v_fma_mixhi_f16 v120,v158,s99,0
v_fma_mixhi_f16 v122,v159,s99,0
v_mfma_f32_16x16x32_bf16 v[164:167],v[88:91],v[136:139],v[164:167]
ds_read_u16_d16_hi v147,v147
v_lshlrev_b32_sdwa v149,1,v149 dst_sel:DWORD dst_unused:UNUSED_PAD src0_sel:DWORD src1_sel:WORD_1
v_lshlrev_b32_sdwa v151,1,v151 dst_sel:DWORD dst_unused:UNUSED_PAD src0_sel:DWORD src1_sel:WORD_1
v_mfma_f32_16x16x32_bf16 v[172:175],v[92:95],v[136:139],v[172:175]
ds_read_b64_tr_b16 v[74:75],v1 offset:6144
ds_read_b64_tr_b16 v[76:77],v2 offset:4096
v_pk_fma_f16 v121,v120,v120,s54
v_pk_fma_f16 v123,v122,v122,s54
v_mfma_f32_16x16x32_bf16 v[180:183],v[96:99],v[136:139],v[180:183]
s_cbranch_vccz RA88_%=
s_add_u32 m0,m0,1024
s_add_u32 s69,s69,s53
buffer_load_dwordx4 v5,s[44:47],s69 offen lds
s_add_u32 m0,m0,1024
s_add_u32 s69,s69,s53
buffer_load_dwordx4 v6,s[44:47],s69 offen lds
RA88_%=:
ds_read_u16 v148,v148
v_pk_mul_f16 v121,v120,v121
v_pk_mul_f16 v123,v122,v123
v_mfma_f32_16x16x32_bf16 v[188:191],v[100:103],v[136:139],v[188:191]
ds_read_b64_tr_b16 v[78:79],v2 offset:6144
v_lshlrev_b32_sdwa v152,1,v152 dst_sel:DWORD dst_unused:UNUSED_PAD src0_sel:DWORD src1_sel:WORD_1
v_lshlrev_b32_sdwa v154,1,v154 dst_sel:DWORD dst_unused:UNUSED_PAD src0_sel:DWORD src1_sel:WORD_1
v_mfma_f32_16x16x32_bf16 v[196:199],v[104:107],v[136:139],v[196:199]
ds_read_u16_d16_hi v150,v150
v_exp_f16 v120,v121
v_mfma_f32_16x16x32_bf16 v[204:207],v[108:111],v[136:139],v[204:207]
s_waitcnt lgkmcnt(12)
ds_read_b64_tr_b16 v[80:81],v1 offset:8192
v_exp_f16 v122,v123
v_mfma_f32_16x16x32_bf16 v[128:131],v[64:67],v[16:19],v[112:115]
s_cbranch_vccz RA89_%=
s_add_u32 m0,m0,s50
s_add_u32 s69,s69,s53
buffer_load_dwordx4 v5,s[44:47],s69 offen lds
s_add_u32 m0,m0,1024
s_add_u32 s69,s69,s53
buffer_load_dwordx4 v6,s[44:47],s69 offen lds
RA89_%=:
ds_read_u16 v149,v149
v_lshlrev_b32_sdwa v153,1,v153 dst_sel:DWORD dst_unused:UNUSED_PAD src0_sel:DWORD src1_sel:WORD_1
v_lshlrev_b32_sdwa v155,1,v155 dst_sel:DWORD dst_unused:UNUSED_PAD src0_sel:DWORD src1_sel:WORD_1
v_mfma_f32_16x16x32_bf16 v[136:139],v[64:67],v[20:23],v[112:115]
s_waitcnt lgkmcnt(9)
ds_read_b64_tr_b16 v[82:83],v1 offset:10240
v_exp_f16_sdwa v120,v121 dst_sel:WORD_1 dst_unused:UNUSED_PRESERVE src0_sel:WORD_1
v_mfma_f32_16x16x32_bf16 v[140:143],v[68:71],v[20:23],v[116:119]
ds_read_u16_d16_hi v151,v151
v_exp_f16_sdwa v122,v123 dst_sel:WORD_1 dst_unused:UNUSED_PRESERVE src0_sel:WORD_1
v_mfma_f32_16x16x32_bf16 v[132:135],v[68:71],v[16:19],v[116:119]
s_waitcnt lgkmcnt(8)
ds_read_b64_tr_b16 v[84:85],v2 offset:8192
v_pk_fma_f16 v120,v120,s55,s55
v_pk_fma_f16 v122,v122,s55,s55
v_mfma_f32_16x16x32_bf16 v[128:131],v[72:75],v[24:27],v[128:131]
s_cbranch_vccz RA90_%=
s_add_u32 m0,m0,1024
s_add_u32 s69,s69,s53
buffer_load_dwordx4 v5,s[44:47],s69 offen lds
s_add_u32 m0,m0,s50
s_add_u32 s69,s69,s53
buffer_load_dwordx4 v6,s[44:47],s69 offen lds
RA90_%=:
ds_read_u16 v152,v152
v_rcp_f16 v121,v120
v_mfma_f32_16x16x32_bf16 v[136:139],v[72:75],v[28:31],v[136:139]
s_waitcnt lgkmcnt(7)
ds_read_b64_tr_b16 v[86:87],v2 offset:10240
v_rcp_f16 v123,v122
v_mfma_f32_16x16x32_bf16 v[140:143],v[76:79],v[28:31],v[140:143]
ds_read_u16_d16_hi v154,v154
v_rcp_f16_sdwa v121,v120 dst_sel:WORD_1 dst_unused:UNUSED_PRESERVE src0_sel:WORD_1
v_mfma_f32_16x16x32_bf16 v[132:135],v[76:79],v[24:27],v[132:135]
s_waitcnt lgkmcnt(5)
s_cbranch_vccz RA91_%=
s_add_u32 m0,m0,1024
s_add_u32 s69,s69,s53
buffer_load_dwordx4 v5,s[44:47],s69 offen lds
s_add_u32 m0,m0,1024
s_add_u32 s69,s69,s53
buffer_load_dwordx4 v6,s[44:47],s69 offen lds
RA91_%=:
v_rcp_f16_sdwa v123,v122 dst_sel:WORD_1 dst_unused:UNUSED_PRESERVE src0_sel:WORD_1
v_mfma_f32_16x16x32_bf16 v[128:131],v[80:83],v[32:35],v[128:131]
ds_read_u16 v153,v153
v_fma_mix_f32 v156,v156,v121,0 op_sel_hi:[0,1,0]
v_fma_mix_f32 v158,v158,v121,0 op_sel:[0,1,0] op_sel_hi:[0,1,0]
v_mfma_f32_16x16x32_bf16 v[136:139],v[80:83],v[36:39],v[136:139]
s_waitcnt lgkmcnt(2)
v_lshl_add_u64 v[144:145],v[144:145],0,v[146:147]
v_lshl_add_u64 v[146:147],v[148:149],0,v[150:151]
v_mfma_f32_16x16x32_bf16 v[140:143],v[84:87],v[36:39],v[140:143]
ds_read_u16_d16_hi v155,v155
v_fma_mix_f32 v157,v157,v123,0 op_sel_hi:[0,1,0]
v_fma_mix_f32 v159,v159,v123,0 op_sel:[0,1,0] op_sel_hi:[0,1,0]
v_mfma_f32_16x16x32_bf16 v[132:135],v[84:87],v[32:35],v[132:135]
s_cbranch_vccz RA92_%=
s_add_u32 m0,s100,12320
s_mov_b32 s69,s60
buffer_load_dwordx4 v5,s[44:47],s69 offen lds
s_add_u32 m0,m0,1024
s_add_u32 s69,s69,s53
buffer_load_dwordx4 v6,s[44:47],s69 offen lds
RA92_%=:
v_lshlrev_b32_sdwa v128,1,v128 dst_sel:DWORD dst_unused:UNUSED_PAD src0_sel:DWORD src1_sel:WORD_1
v_lshlrev_b32_sdwa v130,1,v130 dst_sel:DWORD dst_unused:UNUSED_PAD src0_sel:DWORD src1_sel:WORD_1
v_mfma_f32_16x16x32_bf16 v[208:211],v[88:91],v[144:147],v[208:211]
ds_read_u16 v128,v128
v_lshlrev_b32_sdwa v129,1,v129 dst_sel:DWORD dst_unused:UNUSED_PAD src0_sel:DWORD src1_sel:WORD_1
v_lshlrev_b32_sdwa v131,1,v131 dst_sel:DWORD dst_unused:UNUSED_PAD src0_sel:DWORD src1_sel:WORD_1
v_mfma_f32_16x16x32_bf16 v[216:219],v[92:95],v[144:147],v[216:219]
v_fma_mixlo_f16 v120,v140,s99,0
v_fma_mixlo_f16 v122,v141,s99,0
v_mfma_f32_16x16x32_bf16 v[224:227],v[96:99],v[144:147],v[224:227]
s_setprio 2
ds_read_u16_d16_hi v130,v130
v_lshlrev_b32_sdwa v132,1,v132 dst_sel:DWORD dst_unused:UNUSED_PAD src0_sel:DWORD src1_sel:WORD_1
v_lshlrev_b32_sdwa v134,1,v134 dst_sel:DWORD dst_unused:UNUSED_PAD src0_sel:DWORD src1_sel:WORD_1
v_mfma_f32_16x16x32_bf16 v[232:235],v[100:103],v[144:147],v[232:235]
s_waitcnt lgkmcnt(2)
s_cbranch_vccz RA93_%=
s_add_u32 m0,m0,1024
s_add_u32 s69,s69,s53
buffer_load_dwordx4 v5,s[44:47],s69 offen lds
s_add_u32 m0,m0,s50
s_add_u32 s69,s69,s53
buffer_load_dwordx4 v6,s[44:47],s69 offen lds
RA93_%=:
v_lshl_add_u64 v[152:153],v[152:153],0,v[154:155]
v_mfma_f32_16x16x32_bf16 v[240:243],v[104:107],v[144:147],v[240:243]
ds_read_u16 v129,v129
v_cvt_pk_bf16_f32 v154,v156,v158
v_cvt_pk_bf16_f32 v155,v157,v159
v_mfma_f32_16x16x32_bf16 v[248:251],v[108:111],v[144:147],v[248:251]
v_fma_mixhi_f16 v120,v142,s99,0
v_fma_mixhi_f16 v122,v143,s99,0
v_mfma_f32_16x16x32_bf16 v[212:215],v[88:91],v[152:155],v[212:215]
ds_read_u16_d16_hi v131,v131
v_lshlrev_b32_sdwa v133,1,v133 dst_sel:DWORD dst_unused:UNUSED_PAD src0_sel:DWORD src1_sel:WORD_1
v_lshlrev_b32_sdwa v135,1,v135 dst_sel:DWORD dst_unused:UNUSED_PAD src0_sel:DWORD src1_sel:WORD_1
v_mfma_f32_16x16x32_bf16 v[220:223],v[92:95],v[152:155],v[220:223]
s_cbranch_vccz RA94_%=
s_add_u32 m0,m0,1024
s_add_u32 s69,s69,s53
buffer_load_dwordx4 v5,s[44:47],s69 offen lds
s_add_u32 m0,m0,1024
s_add_u32 s69,s69,s53
buffer_load_dwordx4 v6,s[44:47],s69 offen lds
RA94_%=:
ds_read_b64_tr_b16 v[88:89],v3 offset:6160
v_pk_fma_f16 v121,v120,v120,s54
v_pk_fma_f16 v123,v122,v122,s54
v_mfma_f32_16x16x32_bf16 v[228:231],v[96:99],v[152:155],v[228:231]
ds_read_u16 v132,v132
v_pk_mul_f16 v121,v120,v121
v_pk_mul_f16 v123,v122,v123
v_mfma_f32_16x16x32_bf16 v[236:239],v[100:103],v[152:155],v[236:239]
ds_read_b64_tr_b16 v[90:91],v3 offset:7696
v_lshlrev_b32_sdwa v136,1,v136 dst_sel:DWORD dst_unused:UNUSED_PAD src0_sel:DWORD src1_sel:WORD_1
v_lshlrev_b32_sdwa v138,1,v138 dst_sel:DWORD dst_unused:UNUSED_PAD src0_sel:DWORD src1_sel:WORD_1
v_mfma_f32_16x16x32_bf16 v[244:247],v[104:107],v[152:155],v[244:247]
ds_read_u16_d16_hi v134,v134
v_exp_f16 v120,v121
v_mfma_f32_16x16x32_bf16 v[252:255],v[108:111],v[152:155],v[252:255]
s_cbranch_vccz RA95_%=
s_add_u32 m0,m0,s50
s_add_u32 s69,s69,s51
buffer_load_dwordx4 v5,s[44:47],s69 offen lds
s_add_u32 m0,m0,1024
s_add_u32 s69,s69,s53
buffer_load_dwordx4 v6,s[44:47],s69 offen lds
RA95_%=:
ds_read_b64_tr_b16 v[92:93],v3 offset:6168
v_exp_f16 v122,v123
v_mfma_f32_16x16x32_bf16 v[144:147],v[64:67],v[40:43],v[112:115]
ds_read_u16 v133,v133
v_lshlrev_b32_sdwa v137,1,v137 dst_sel:DWORD dst_unused:UNUSED_PAD src0_sel:DWORD src1_sel:WORD_1
v_lshlrev_b32_sdwa v139,1,v139 dst_sel:DWORD dst_unused:UNUSED_PAD src0_sel:DWORD src1_sel:WORD_1
v_mfma_f32_16x16x32_bf16 v[152:155],v[64:67],v[44:47],v[112:115]
ds_read_b64_tr_b16 v[94:95],v3 offset:7704
v_exp_f16_sdwa v120,v121 dst_sel:WORD_1 dst_unused:UNUSED_PRESERVE src0_sel:WORD_1
v_mfma_f32_16x16x32_bf16 v[156:159],v[68:71],v[44:47],v[116:119]
s_cbranch_vccz RA96_%=
s_add_u32 m0,m0,1024
s_add_u32 s69,s69,s53
buffer_load_dwordx4 v5,s[44:47],s69 offen lds
s_add_u32 m0,m0,s50
s_add_u32 s69,s69,s53
buffer_load_dwordx4 v6,s[44:47],s69 offen lds
RA96_%=:
ds_read_u16_d16_hi v135,v135
v_exp_f16_sdwa v122,v123 dst_sel:WORD_1 dst_unused:UNUSED_PRESERVE src0_sel:WORD_1
v_mfma_f32_16x16x32_bf16 v[148:151],v[68:71],v[40:43],v[116:119]
ds_read_b64_tr_b16 v[96:97],v3 offset:6224
v_pk_fma_f16 v120,v120,s55,s55
v_pk_fma_f16 v122,v122,s55,s55
v_mfma_f32_16x16x32_bf16 v[144:147],v[72:75],v[48:51],v[144:147]
ds_read_u16 v136,v136
v_rcp_f16 v121,v120
v_mfma_f32_16x16x32_bf16 v[152:155],v[72:75],v[52:55],v[152:155]
ds_read_b64_tr_b16 v[98:99],v3 offset:7760
ds_read_b64_tr_b16 v[100:101],v3 offset:6232
v_rcp_f16 v123,v122
v_mfma_f32_16x16x32_bf16 v[156:159],v[76:79],v[52:55],v[156:159]
s_cbranch_vccz RA97_%=
s_add_u32 m0,m0,1024
s_add_u32 s69,s69,s53
buffer_load_dwordx4 v5,s[44:47],s69 offen lds
s_add_u32 m0,m0,1024
s_add_u32 s69,s69,s53
buffer_load_dwordx4 v6,s[44:47],s69 offen lds
RA97_%=:
ds_read_u16_d16_hi v138,v138
v_rcp_f16_sdwa v121,v120 dst_sel:WORD_1 dst_unused:UNUSED_PRESERVE src0_sel:WORD_1
v_mfma_f32_16x16x32_bf16 v[148:151],v[76:79],v[48:51],v[148:151]
ds_read_b64_tr_b16 v[102:103],v3 offset:7768
ds_read_b64_tr_b16 v[104:105],v3 offset:6288
v_rcp_f16_sdwa v123,v122 dst_sel:WORD_1 dst_unused:UNUSED_PRESERVE src0_sel:WORD_1
v_mfma_f32_16x16x32_bf16 v[144:147],v[80:83],v[56:59],v[144:147]
ds_read_u16 v137,v137
v_fma_mix_f32 v140,v140,v121,0 op_sel_hi:[0,1,0]
v_fma_mix_f32 v142,v142,v121,0 op_sel:[0,1,0] op_sel_hi:[0,1,0]
v_mfma_f32_16x16x32_bf16 v[152:155],v[80:83],v[60:63],v[152:155]
s_waitcnt lgkmcnt(8)
ds_read_b64_tr_b16 v[106:107],v3 offset:7824
ds_read_b64_tr_b16 v[108:109],v3 offset:6296
v_lshl_add_u64 v[128:129],v[128:129],0,v[130:131]
v_lshl_add_u64 v[130:131],v[132:133],0,v[134:135]
v_mfma_f32_16x16x32_bf16 v[156:159],v[84:87],v[60:63],v[156:159]
s_cbranch_vccz RA98_%=
s_add_u32 m0,s100,24640
s_mul_i32 s69,s59,1
s_add_u32 s69,s69,s97
buffer_load_dwordx4 v5,s[44:47],s69 offen lds
s_add_u32 m0,m0,1024
s_add_u32 s69,s69,s53
buffer_load_dwordx4 v6,s[44:47],s69 offen lds
RA98_%=:
ds_read_u16_d16_hi v139,v139
v_fma_mix_f32 v141,v141,v123,0 op_sel_hi:[0,1,0]
v_fma_mix_f32 v143,v143,v123,0 op_sel:[0,1,0] op_sel_hi:[0,1,0]
v_mfma_f32_16x16x32_bf16 v[148:151],v[84:87],v[56:59],v[148:151]
ds_read_b64_tr_b16 v[110:111],v3 offset:7832
ds_read_b128 v[64:67],v4 offset:1536
v_lshlrev_b32_sdwa v144,1,v144 dst_sel:DWORD dst_unused:UNUSED_PAD src0_sel:DWORD src1_sel:WORD_1
v_lshlrev_b32_sdwa v146,1,v146 dst_sel:DWORD dst_unused:UNUSED_PAD src0_sel:DWORD src1_sel:WORD_1
v_mfma_f32_16x16x32_bf16 v[160:163],v[88:91],v[128:131],v[160:163]
ds_read_u16 v144,v144
ds_read_b128 v[68:71],v4 offset:1568
v_lshlrev_b32_sdwa v145,1,v145 dst_sel:DWORD dst_unused:UNUSED_PAD src0_sel:DWORD src1_sel:WORD_1
v_lshlrev_b32_sdwa v147,1,v147 dst_sel:DWORD dst_unused:UNUSED_PAD src0_sel:DWORD src1_sel:WORD_1
v_fma_mixlo_f16 v120,v156,s99,0
v_fma_mixlo_f16 v122,v157,s99,0
v_mfma_f32_16x16x32_bf16 v[168:171],v[92:95],v[128:131],v[168:171]
s_waitcnt lgkmcnt(12)
s_cbranch_vccz RA99_%=
s_add_u32 m0,m0,1024
s_add_u32 s69,s69,s53
buffer_load_dwordx4 v5,s[44:47],s69 offen lds
s_add_u32 m0,m0,s50
s_add_u32 s69,s69,s53
buffer_load_dwordx4 v6,s[44:47],s69 offen lds
RA99_%=:
ds_read_u16_d16_hi v146,v146
ds_read_b128 v[72:75],v4 offset:1664
v_lshlrev_b32_sdwa v148,1,v148 dst_sel:DWORD dst_unused:UNUSED_PAD src0_sel:DWORD src1_sel:WORD_1
v_lshlrev_b32_sdwa v150,1,v150 dst_sel:DWORD dst_unused:UNUSED_PAD src0_sel:DWORD src1_sel:WORD_1
s_waitcnt lgkmcnt(6)
v_lshl_add_u64 v[136:137],v[136:137],0,v[138:139]
v_cvt_pk_bf16_f32 v138,v140,v142
v_mfma_f32_16x16x32_bf16 v[176:179],v[96:99],v[128:131],v[176:179]
s_setprio 1
ds_read_u16 v145,v145
ds_read_b128 v[76:79],v4 offset:1696
v_cvt_pk_bf16_f32 v139,v141,v143
v_fma_mixhi_f16 v120,v158,s99,0
v_fma_mixhi_f16 v122,v159,s99,0
v_lshlrev_b32_sdwa v149,1,v149 dst_sel:DWORD dst_unused:UNUSED_PAD src0_sel:DWORD src1_sel:WORD_1
v_mfma_f32_16x16x32_bf16 v[184:187],v[100:103],v[128:131],v[184:187]
ds_read_u16_d16_hi v147,v147
ds_read_b128 v[80:83],v4 offset:1792
v_lshlrev_b32_sdwa v151,1,v151 dst_sel:DWORD dst_unused:UNUSED_PAD src0_sel:DWORD src1_sel:WORD_1
v_pk_fma_f16 v121,v120,v120,s54
v_pk_fma_f16 v123,v122,v122,s54
v_pk_mul_f16 v121,v120,v121
v_mfma_f32_16x16x32_bf16 v[192:195],v[104:107],v[128:131],v[192:195]
s_waitcnt lgkmcnt(9)
s_cbranch_vccz RA100_%=
s_add_u32 m0,m0,1024
s_add_u32 s69,s69,s53
buffer_load_dwordx4 v5,s[44:47],s69 offen lds
s_add_u32 m0,m0,1024
s_add_u32 s69,s69,s53
buffer_load_dwordx4 v6,s[44:47],s69 offen lds
RA100_%=:
ds_read_u16 v148,v148
ds_read_b128 v[84:87],v4 offset:1824
v_pk_mul_f16 v123,v122,v123
v_lshlrev_b32_sdwa v152,1,v152 dst_sel:DWORD dst_unused:UNUSED_PAD src0_sel:DWORD src1_sel:WORD_1
v_lshlrev_b32_sdwa v154,1,v154 dst_sel:DWORD dst_unused:UNUSED_PAD src0_sel:DWORD src1_sel:WORD_1
v_exp_f16 v120,v121
v_mfma_f32_16x16x32_bf16 v[200:203],v[108:111],v[128:131],v[200:203]
ds_read_u16_d16_hi v150,v150
v_exp_f16 v122,v123
v_lshlrev_b32_sdwa v153,1,v153 dst_sel:DWORD dst_unused:UNUSED_PAD src0_sel:DWORD src1_sel:WORD_1
v_mfma_f32_16x16x32_bf16 v[164:167],v[88:91],v[136:139],v[164:167]
s_cbranch_vccz RA101_%=
s_add_u32 m0,m0,s50
s_add_u32 s69,s69,s53
buffer_load_dwordx4 v5,s[44:47],s69 offen lds
s_add_u32 m0,m0,1024
s_add_u32 s69,s69,s53
buffer_load_dwordx4 v6,s[44:47],s69 offen lds
RA101_%=:
ds_read_u16 v149,v149
v_lshlrev_b32_sdwa v155,1,v155 dst_sel:DWORD dst_unused:UNUSED_PAD src0_sel:DWORD src1_sel:WORD_1
v_exp_f16_sdwa v120,v121 dst_sel:WORD_1 dst_unused:UNUSED_PRESERVE src0_sel:WORD_1
v_exp_f16_sdwa v122,v123 dst_sel:WORD_1 dst_unused:UNUSED_PRESERVE src0_sel:WORD_1
v_mfma_f32_16x16x32_bf16 v[172:175],v[92:95],v[136:139],v[172:175]
ds_read_u16_d16_hi v151,v151
v_pk_fma_f16 v120,v120,s55,s55
v_pk_fma_f16 v122,v122,s55,s55
v_rcp_f16 v121,v120
v_mfma_f32_16x16x32_bf16 v[180:183],v[96:99],v[136:139],v[180:183]
ds_read_u16 v152,v152
v_rcp_f16 v123,v122
v_rcp_f16_sdwa v121,v120 dst_sel:WORD_1 dst_unused:UNUSED_PRESERVE src0_sel:WORD_1
v_mfma_f32_16x16x32_bf16 v[188:191],v[100:103],v[136:139],v[188:191]
s_cbranch_vccz RA102_%=
s_add_u32 m0,m0,1024
s_add_u32 s69,s69,s53
buffer_load_dwordx4 v5,s[44:47],s69 offen lds
s_add_u32 m0,m0,s50
s_add_u32 s69,s69,s53
buffer_load_dwordx4 v6,s[44:47],s69 offen lds
RA102_%=:
ds_read_u16_d16_hi v154,v154
v_rcp_f16_sdwa v123,v122 dst_sel:WORD_1 dst_unused:UNUSED_PRESERVE src0_sel:WORD_1
v_fma_mix_f32 v156,v156,v121,0 op_sel_hi:[0,1,0]
v_mfma_f32_16x16x32_bf16 v[196:199],v[104:107],v[136:139],v[196:199]
s_setprio 0
ds_read_u16 v153,v153
v_fma_mix_f32 v158,v158,v121,0 op_sel:[0,1,0] op_sel_hi:[0,1,0]
s_waitcnt lgkmcnt(9)
v_lshl_add_u64 v[144:145],v[144:145],0,v[146:147]
s_waitcnt lgkmcnt(3)
v_lshl_add_u64 v[146:147],v[148:149],0,v[150:151]
v_fma_mix_f32 v157,v157,v123,0 op_sel_hi:[0,1,0]
v_mfma_f32_16x16x32_bf16 v[204:207],v[108:111],v[136:139],v[204:207]
s_cbranch_vccz RA103_%=
s_add_u32 m0,m0,1024
s_add_u32 s69,s69,s53
buffer_load_dwordx4 v5,s[44:47],s69 offen lds
s_add_u32 m0,m0,1024
s_add_u32 s69,s69,s53
buffer_load_dwordx4 v6,s[44:47],s69 offen lds
RA103_%=:
ds_read_u16_d16_hi v155,v155
v_fma_mix_f32 v159,v159,v123,0 op_sel:[0,1,0] op_sel_hi:[0,1,0]
s_waitcnt lgkmcnt(0)
v_lshl_add_u64 v[152:153],v[152:153],0,v[154:155]
v_cvt_pk_bf16_f32 v154,v156,v158
v_cvt_pk_bf16_f32 v155,v157,v159
v_mfma_f32_16x16x32_bf16 v[208:211],v[88:91],v[144:147],v[208:211]
s_waitcnt vmcnt(36)
s_barrier
s_setprio 0
v_xor_b32 v15,64,v10
v_mul_f32 v160,v160,v64
v_mul_f32 v161,v161,v65
v_mul_f32 v162,v162,v66
v_mul_f32 v163,v163,v67
v_mfma_f32_16x16x32_bf16 v[216:219],v[92:95],v[144:147],v[216:219]
v_mul_f32 v168,v168,v68
v_mul_f32 v169,v169,v69
v_mul_f32 v170,v170,v70
v_mul_f32 v171,v171,v71
v_cvt_pk_bf16_f32 v16,v160,v161
v_mfma_f32_16x16x32_bf16 v[224:227],v[96:99],v[144:147],v[224:227]
v_cvt_pk_bf16_f32 v17,v162,v163
v_cvt_pk_bf16_f32 v18,v168,v169
v_cvt_pk_bf16_f32 v19,v170,v171
v_mul_f32_dpp v176,v176,v72 quad_perm:[1,0,3,2] row_mask:0xf bank_mask:0xf
v_mfma_f32_16x16x32_bf16 v[232:235],v[100:103],v[144:147],v[232:235]
v_mul_f32_dpp v177,v177,v73 quad_perm:[1,0,3,2] row_mask:0xf bank_mask:0xf
v_mul_f32_dpp v178,v178,v74 quad_perm:[1,0,3,2] row_mask:0xf bank_mask:0xf
v_mul_f32_dpp v179,v179,v75 quad_perm:[1,0,3,2] row_mask:0xf bank_mask:0xf
v_mul_f32_dpp v184,v184,v76 quad_perm:[1,0,3,2] row_mask:0xf bank_mask:0xf
v_mul_f32_dpp v185,v185,v77 quad_perm:[1,0,3,2] row_mask:0xf bank_mask:0xf
v_mfma_f32_16x16x32_bf16 v[240:243],v[104:107],v[144:147],v[240:243]
v_mul_f32_dpp v186,v186,v78 quad_perm:[1,0,3,2] row_mask:0xf bank_mask:0xf
v_mul_f32_dpp v187,v187,v79 quad_perm:[1,0,3,2] row_mask:0xf bank_mask:0xf
v_cvt_pk_bf16_f32 v24,v176,v177
v_cvt_pk_bf16_f32 v25,v178,v179
v_mfma_f32_16x16x32_bf16 v[248:251],v[108:111],v[144:147],v[248:251]
v_cvt_pk_bf16_f32 v26,v184,v185
v_cvt_pk_bf16_f32 v27,v186,v187
s_mov_b64 exec,s[12:13]
v_swap_b32 v16,v24
v_swap_b32 v17,v25
v_swap_b32 v18,v26
v_swap_b32 v19,v27
s_mov_b64 exec,-1
v_mfma_f32_16x16x32_bf16 v[212:215],v[88:91],v[152:155],v[212:215]
s_add_u32 s65,s61,3145728
buffer_store_dwordx4 v[16:19],v10,s[40:43],s65 offen offset:0 sc1
v_mfma_f32_16x16x32_bf16 v[220:223],v[92:95],v[152:155],v[220:223]
v_mul_f32 v192,v192,v80
v_mul_f32 v193,v193,v81
v_mul_f32 v194,v194,v82
v_mul_f32 v195,v195,v83
v_mfma_f32_16x16x32_bf16 v[228:231],v[96:99],v[152:155],v[228:231]
v_mul_f32 v200,v200,v84
v_mul_f32 v201,v201,v85
v_mul_f32 v202,v202,v86
v_mul_f32 v203,v203,v87
v_mfma_f32_16x16x32_bf16 v[236:239],v[100:103],v[152:155],v[236:239]
v_cvt_pk_bf16_f32 v32,v192,v193
v_cvt_pk_bf16_f32 v33,v194,v195
v_cvt_pk_bf16_f32 v34,v200,v201
v_cvt_pk_bf16_f32 v35,v202,v203
v_mul_f32_dpp v164,v164,v64 quad_perm:[1,0,3,2] row_mask:0xf bank_mask:0xf
v_mfma_f32_16x16x32_bf16 v[244:247],v[104:107],v[152:155],v[244:247]
v_mul_f32_dpp v165,v165,v65 quad_perm:[1,0,3,2] row_mask:0xf bank_mask:0xf
v_mul_f32_dpp v166,v166,v66 quad_perm:[1,0,3,2] row_mask:0xf bank_mask:0xf
v_mul_f32_dpp v167,v167,v67 quad_perm:[1,0,3,2] row_mask:0xf bank_mask:0xf
v_mul_f32_dpp v172,v172,v68 quad_perm:[1,0,3,2] row_mask:0xf bank_mask:0xf
v_mfma_f32_16x16x32_bf16 v[252:255],v[108:111],v[152:155],v[252:255]
v_mul_f32_dpp v173,v173,v69 quad_perm:[1,0,3,2] row_mask:0xf bank_mask:0xf
v_mul_f32_dpp v174,v174,v70 quad_perm:[1,0,3,2] row_mask:0xf bank_mask:0xf
v_mul_f32_dpp v175,v175,v71 quad_perm:[1,0,3,2] row_mask:0xf bank_mask:0xf
v_cvt_pk_bf16_f32 v20,v164,v165
v_cvt_pk_bf16_f32 v21,v166,v167
v_cvt_pk_bf16_f32 v22,v172,v173
v_cvt_pk_bf16_f32 v23,v174,v175
s_mov_b64 exec,s[12:13]
v_swap_b32 v32,v20
v_swap_b32 v33,v21
v_swap_b32 v34,v22
v_swap_b32 v35,v23
s_mov_b64 exec,-1
s_add_u32 s65,s61,3145728
buffer_store_dwordx4 v[24:27],v15,s[40:43],s65 offen offset:384 sc1
v_mul_f32 v180,v180,v72
v_mul_f32 v181,v181,v73
v_mul_f32 v182,v182,v74
v_mul_f32 v183,v183,v75
v_mul_f32 v188,v188,v76
v_mul_f32 v189,v189,v77
v_mul_f32 v190,v190,v78
v_mul_f32 v191,v191,v79
v_cvt_pk_bf16_f32 v28,v180,v181
v_cvt_pk_bf16_f32 v29,v182,v183
v_cvt_pk_bf16_f32 v30,v188,v189
v_cvt_pk_bf16_f32 v31,v190,v191
v_mul_f32_dpp v196,v196,v80 quad_perm:[1,0,3,2] row_mask:0xf bank_mask:0xf
v_mul_f32_dpp v197,v197,v81 quad_perm:[1,0,3,2] row_mask:0xf bank_mask:0xf
v_mul_f32_dpp v198,v198,v82 quad_perm:[1,0,3,2] row_mask:0xf bank_mask:0xf
v_mul_f32_dpp v199,v199,v83 quad_perm:[1,0,3,2] row_mask:0xf bank_mask:0xf
v_mul_f32_dpp v204,v204,v84 quad_perm:[1,0,3,2] row_mask:0xf bank_mask:0xf
v_mul_f32_dpp v205,v205,v85 quad_perm:[1,0,3,2] row_mask:0xf bank_mask:0xf
v_mul_f32_dpp v206,v206,v86 quad_perm:[1,0,3,2] row_mask:0xf bank_mask:0xf
v_mul_f32_dpp v207,v207,v87 quad_perm:[1,0,3,2] row_mask:0xf bank_mask:0xf
v_cvt_pk_bf16_f32 v36,v196,v197
v_cvt_pk_bf16_f32 v37,v198,v199
v_cvt_pk_bf16_f32 v38,v204,v205
v_cvt_pk_bf16_f32 v39,v206,v207
s_mov_b64 exec,s[12:13]
v_swap_b32 v28,v36
v_swap_b32 v29,v37
v_swap_b32 v30,v38
v_swap_b32 v31,v39
s_mov_b64 exec,-1
s_mov_b32 s86,0xbe801d5a
s_add_u32 s65,s61,3145728
buffer_store_dwordx4 v[32:35],v10,s[40:43],s65 offen offset:128 sc1
v_mul_f32 v208,v208,v64
v_mul_f32 v209,v209,v65
v_mul_f32 v210,v210,v66
v_mul_f32 v211,v211,v67
v_mul_f32 v216,v216,v68
v_mul_f32 v217,v217,v69
v_mul_f32 v218,v218,v70
v_mul_f32 v219,v219,v71
v_cvt_pk_bf16_f32 v40,v208,v209
v_cvt_pk_bf16_f32 v41,v210,v211
v_cvt_pk_bf16_f32 v42,v216,v217
v_cvt_pk_bf16_f32 v43,v218,v219
v_mul_f32_dpp v224,v224,v72 quad_perm:[1,0,3,2] row_mask:0xf bank_mask:0xf
v_mul_f32_dpp v225,v225,v73 quad_perm:[1,0,3,2] row_mask:0xf bank_mask:0xf
v_mul_f32_dpp v226,v226,v74 quad_perm:[1,0,3,2] row_mask:0xf bank_mask:0xf
v_mul_f32_dpp v227,v227,v75 quad_perm:[1,0,3,2] row_mask:0xf bank_mask:0xf
v_mul_f32_dpp v232,v232,v76 quad_perm:[1,0,3,2] row_mask:0xf bank_mask:0xf
v_mul_f32_dpp v233,v233,v77 quad_perm:[1,0,3,2] row_mask:0xf bank_mask:0xf
v_mul_f32_dpp v234,v234,v78 quad_perm:[1,0,3,2] row_mask:0xf bank_mask:0xf
v_mul_f32_dpp v235,v235,v79 quad_perm:[1,0,3,2] row_mask:0xf bank_mask:0xf
v_cvt_pk_bf16_f32 v48,v224,v225
v_cvt_pk_bf16_f32 v49,v226,v227
v_cvt_pk_bf16_f32 v50,v232,v233
v_cvt_pk_bf16_f32 v51,v234,v235
s_mov_b64 exec,s[12:13]
v_swap_b32 v40,v48
v_swap_b32 v41,v49
v_swap_b32 v42,v50
v_swap_b32 v43,v51
s_mov_b64 exec,-1
v_mul_f32 v240,v240,v80
v_mul_f32 v241,v241,v81
v_mul_f32 v242,v242,v82
v_mul_f32 v243,v243,v83
v_mul_f32 v248,v248,v84
s_add_u32 s65,s61,3145728
buffer_store_dwordx4 v[20:23],v15,s[40:43],s65 offen offset:512 sc1
v_mul_f32 v249,v249,v85
v_mul_f32 v250,v250,v86
v_mul_f32 v251,v251,v87
v_cvt_pk_bf16_f32 v56,v240,v241
v_cvt_pk_bf16_f32 v57,v242,v243
v_cvt_pk_bf16_f32 v58,v248,v249
v_cvt_pk_bf16_f32 v59,v250,v251
v_mul_f32_dpp v212,v212,v64 quad_perm:[1,0,3,2] row_mask:0xf bank_mask:0xf
v_mul_f32_dpp v213,v213,v65 quad_perm:[1,0,3,2] row_mask:0xf bank_mask:0xf
v_mul_f32_dpp v214,v214,v66 quad_perm:[1,0,3,2] row_mask:0xf bank_mask:0xf
v_mul_f32_dpp v215,v215,v67 quad_perm:[1,0,3,2] row_mask:0xf bank_mask:0xf
v_mul_f32_dpp v220,v220,v68 quad_perm:[1,0,3,2] row_mask:0xf bank_mask:0xf
v_mul_f32_dpp v221,v221,v69 quad_perm:[1,0,3,2] row_mask:0xf bank_mask:0xf
v_mul_f32_dpp v222,v222,v70 quad_perm:[1,0,3,2] row_mask:0xf bank_mask:0xf
v_mul_f32_dpp v223,v223,v71 quad_perm:[1,0,3,2] row_mask:0xf bank_mask:0xf
v_cvt_pk_bf16_f32 v44,v212,v213
v_cvt_pk_bf16_f32 v45,v214,v215
v_cvt_pk_bf16_f32 v46,v220,v221
v_cvt_pk_bf16_f32 v47,v222,v223
s_mov_b64 exec,s[12:13]
v_swap_b32 v56,v44
v_swap_b32 v57,v45
v_swap_b32 v58,v46
v_swap_b32 v59,v47
s_mov_b64 exec,-1
v_mul_f32 v228,v228,v72
v_mul_f32 v229,v229,v73
v_mul_f32 v230,v230,v74
v_mul_f32 v231,v231,v75
v_mul_f32 v236,v236,v76
v_mul_f32 v237,v237,v77
v_mul_f32 v238,v238,v78
v_mul_f32 v239,v239,v79
v_cvt_pk_bf16_f32 v52,v228,v229
v_cvt_pk_bf16_f32 v53,v230,v231
s_add_u32 s65,s61,3145728
buffer_store_dwordx4 v[28:31],v10,s[40:43],s65 offen offset:256 sc1
v_cvt_pk_bf16_f32 v54,v236,v237
v_cvt_pk_bf16_f32 v55,v238,v239
v_mul_f32_dpp v244,v244,v80 quad_perm:[1,0,3,2] row_mask:0xf bank_mask:0xf
v_mul_f32_dpp v245,v245,v81 quad_perm:[1,0,3,2] row_mask:0xf bank_mask:0xf
v_mul_f32_dpp v246,v246,v82 quad_perm:[1,0,3,2] row_mask:0xf bank_mask:0xf
v_mul_f32_dpp v247,v247,v83 quad_perm:[1,0,3,2] row_mask:0xf bank_mask:0xf
v_mul_f32_dpp v252,v252,v84 quad_perm:[1,0,3,2] row_mask:0xf bank_mask:0xf
v_mul_f32_dpp v253,v253,v85 quad_perm:[1,0,3,2] row_mask:0xf bank_mask:0xf
v_mul_f32_dpp v254,v254,v86 quad_perm:[1,0,3,2] row_mask:0xf bank_mask:0xf
v_mul_f32_dpp v255,v255,v87 quad_perm:[1,0,3,2] row_mask:0xf bank_mask:0xf
v_cvt_pk_bf16_f32 v60,v244,v245
v_cvt_pk_bf16_f32 v61,v246,v247
v_cvt_pk_bf16_f32 v62,v252,v253
v_cvt_pk_bf16_f32 v63,v254,v255
s_mov_b64 exec,s[12:13]
v_swap_b32 v52,v60
v_swap_b32 v53,v61
v_swap_b32 v54,v62
v_swap_b32 v55,v63
s_mov_b64 exec,-1
s_waitcnt vmcnt(5)
s_barrier
s_add_u32 s65,s61,3145728
buffer_store_dwordx4 v[36:39],v15,s[40:43],s65 offen offset:640 sc1
s_sleep 2
s_add_u32 s65,s61,3244032
buffer_store_dwordx4 v[40:43],v10,s[40:43],s65 offen offset:0 sc1
s_sleep 2
s_add_u32 s65,s61,3244032
buffer_store_dwordx4 v[48:51],v15,s[40:43],s65 offen offset:384 sc1
s_sleep 2
s_add_u32 s65,s61,3244032
buffer_store_dwordx4 v[56:59],v10,s[40:43],s65 offen offset:128 sc1
s_sleep 2
s_add_u32 s65,s61,3244032
buffer_store_dwordx4 v[44:47],v15,s[40:43],s65 offen offset:512 sc1
s_sleep 2
s_add_u32 s65,s61,3244032
buffer_store_dwordx4 v[52:55],v10,s[40:43],s65 offen offset:256 sc1
s_sleep 2
s_add_u32 s65,s61,3244032
buffer_store_dwordx4 v[60:63],v15,s[40:43],s65 offen offset:640 sc1
v_and_b32 v232,63,%1
s_getpc_b64 s[88:89]
N_itA104_%=:
s_lshl_b32 s87,s10,10
s_add_u32 s87,s87,86024
s_sub_u32 s88,s88,N_itA104_%=-N_main_%=
s_subb_u32 s89,s89,0
s_add_u32 s88,s88,s87
s_addc_u32 s89,s89,0
s_swappc_b64 s[90:91],s[88:89]
s_cmp_lg_u32 s52,1
s_cbranch_scc1 N_s0_%=
v_min_u32 v236,47,v232
v_lshlrev_b32 v152,4,v236
v_lshlrev_b32 v153,2,v236
v_min_u32 v236,59,v232
v_mul_u32_u24 v237,0xaaab,v236
v_lshrrev_b32 v237,19,v237
v_mul_u32_u24 v237,14592,v237
v_lshl_add_u32 v148,v236,4,v237
s_add_u32 s64,s24,0
s_addc_u32 s65,s25,0
global_load_dwordx4 v[112:115],v152,s[64:65]
s_add_u32 s64,s24,9216
s_addc_u32 s65,s25,0
global_load_dwordx4 v[116:119],v152,s[64:65]
s_add_u32 s64,s24,18432
s_addc_u32 s65,s25,0
global_load_dwordx4 v[120:123],v152,s[64:65]
s_add_u32 s64,s24,27648
s_addc_u32 s65,s25,0
global_load_dwordx4 v[124:127],v152,s[64:65]
s_add_u32 s64,s24,36864
s_addc_u32 s65,s25,0
global_load_dwordx4 v[128:131],v152,s[64:65]
s_add_u32 s64,s24,46080
s_addc_u32 s65,s25,0
global_load_dwordx4 v[132:135],v152,s[64:65]
s_add_u32 s64,s24,55296
s_addc_u32 s65,s25,0
global_load_dwordx4 v[136:139],v152,s[64:65]
s_add_u32 s64,s24,64512
s_addc_u32 s65,s25,0
global_load_dwordx4 v[140:143],v152,s[64:65]
global_load_dwordx4 v[144:147],v148,s[28:29]
global_load_dwordx4 v[148:151],v148,s[28:29] offset:3648
global_load_dwordx4 v[156:159],v152,s[26:27]
global_load_dword v152,v153,s[32:33]
global_load_dword v153,v153,s[30:31]
N_s0_%=:
s_and_b32 s64,s52,3
s_mul_i32 s64,s64,18432
s_add_u32 s64,s64,4096
v_lshl_add_u32 v255,v232,4,s64
global_load_dwordx4 v[160:163],v255,s[24:25] offset:-4096
global_load_dwordx4 v[164:167],v255,s[24:25] offset:-3072
global_load_dwordx4 v[168:171],v255,s[24:25] offset:-2048
global_load_dwordx4 v[172:175],v255,s[24:25] offset:-1024
global_load_dwordx4 v[176:179],v255,s[24:25] offset:0
global_load_dwordx4 v[180:183],v255,s[24:25] offset:1024
global_load_dwordx4 v[184:187],v255,s[24:25] offset:2048
global_load_dwordx4 v[188:191],v255,s[24:25] offset:3072
v_add_u32 v255,8192,v255
global_load_dwordx4 v[192:195],v255,s[24:25] offset:-4096
global_load_dwordx4 v[196:199],v255,s[24:25] offset:-3072
global_load_dwordx4 v[200:203],v255,s[24:25] offset:-2048
global_load_dwordx4 v[204:207],v255,s[24:25] offset:-1024
global_load_dwordx4 v[208:211],v255,s[24:25] offset:0
global_load_dwordx4 v[212:215],v255,s[24:25] offset:1024
global_load_dwordx4 v[216:219],v255,s[24:25] offset:2048
global_load_dwordx4 v[220:223],v255,s[24:25] offset:3072
v_add_u32 v255,8192,v255
global_load_dwordx4 v[224:227],v255,s[24:25] offset:-4096
global_load_dwordx4 v[228:231],v255,s[24:25] offset:-3072
s_waitcnt vmcnt(0)
v_or3_b32 v160,v160,v164,v165
v_or3_b32 v162,v162,v166,v167
v_or3_b32 v160,v160,v168,v169
v_or3_b32 v162,v162,v170,v171
v_or3_b32 v160,v160,v172,v173
v_or3_b32 v162,v162,v174,v175
v_or3_b32 v160,v160,v176,v177
v_or3_b32 v162,v162,v178,v179
v_or3_b32 v160,v160,v180,v181
v_or3_b32 v162,v162,v182,v183
v_or3_b32 v160,v160,v184,v185
v_or3_b32 v162,v162,v186,v187
v_or3_b32 v160,v160,v188,v189
v_or3_b32 v162,v162,v190,v191
v_or3_b32 v160,v160,v192,v193
v_or3_b32 v162,v162,v194,v195
v_or3_b32 v160,v160,v196,v197
v_or3_b32 v162,v162,v198,v199
v_or3_b32 v160,v160,v200,v201
v_or3_b32 v162,v162,v202,v203
v_or3_b32 v160,v160,v204,v205
v_or3_b32 v162,v162,v206,v207
v_or3_b32 v160,v160,v208,v209
v_or3_b32 v162,v162,v210,v211
v_or3_b32 v160,v160,v212,v213
v_or3_b32 v162,v162,v214,v215
v_or3_b32 v160,v160,v216,v217
v_or3_b32 v162,v162,v218,v219
v_or3_b32 v160,v160,v220,v221
v_or3_b32 v162,v162,v222,v223
v_or3_b32 v160,v160,v224,v225
v_or3_b32 v162,v162,v226,v227
v_or3_b32 v160,v160,v228,v229
v_or3_b32 v162,v162,v230,v231
v_or3_b32 v160,v160,v161,v162
v_or_b32 v160,v160,v163
s_mov_b32 s64,0x7bff7bff
v_and_b32 v160,0x40004000,v160
v_pk_mul_f16 v160,v160,s64
v_or_b32 v7,v7,v160
s_cmp_lg_u32 s52,1
s_cbranch_scc1 N_s1_%=
s_mov_b32 s65,0x7fff7fff
v_and_b32 v112,s65,v112
v_and_b32 v113,s65,v113
v_and_b32 v114,s65,v114
v_and_b32 v115,s65,v115
v_and_b32 v116,s65,v116
v_and_b32 v117,s65,v117
v_and_b32 v118,s65,v118
v_and_b32 v119,s65,v119
v_and_b32 v120,s65,v120
v_and_b32 v121,s65,v121
v_and_b32 v122,s65,v122
v_and_b32 v123,s65,v123
v_and_b32 v124,s65,v124
v_and_b32 v125,s65,v125
v_and_b32 v126,s65,v126
v_and_b32 v127,s65,v127
v_and_b32 v128,s65,v128
v_and_b32 v129,s65,v129
v_and_b32 v130,s65,v130
v_and_b32 v131,s65,v131
v_and_b32 v132,s65,v132
v_and_b32 v133,s65,v133
v_and_b32 v134,s65,v134
v_and_b32 v135,s65,v135
v_and_b32 v136,s65,v136
v_and_b32 v137,s65,v137
v_and_b32 v138,s65,v138
v_and_b32 v139,s65,v139
v_and_b32 v140,s65,v140
v_and_b32 v141,s65,v141
v_and_b32 v142,s65,v142
v_and_b32 v143,s65,v143
v_pk_max_u16 v100,v112,v116
v_pk_max_u16 v101,v113,v117
v_pk_max_u16 v102,v114,v118
v_pk_max_u16 v103,v115,v119
v_pk_max_u16 v100,v100,v120
v_pk_max_u16 v101,v101,v121
v_pk_max_u16 v102,v102,v122
v_pk_max_u16 v103,v103,v123
v_pk_max_u16 v100,v100,v124
v_pk_max_u16 v101,v101,v125
v_pk_max_u16 v102,v102,v126
v_pk_max_u16 v103,v103,v127
v_pk_max_u16 v100,v100,v128
v_pk_max_u16 v101,v101,v129
v_pk_max_u16 v102,v102,v130
v_pk_max_u16 v103,v103,v131
v_pk_max_u16 v100,v100,v132
v_pk_max_u16 v101,v101,v133
v_pk_max_u16 v102,v102,v134
v_pk_max_u16 v103,v103,v135
v_pk_max_u16 v100,v100,v136
v_pk_max_u16 v101,v101,v137
v_pk_max_u16 v102,v102,v138
v_pk_max_u16 v103,v103,v139
v_pk_max_u16 v100,v100,v140
v_pk_max_u16 v101,v101,v141
v_pk_max_u16 v102,v102,v142
v_pk_max_u16 v103,v103,v143
v_pk_min_u16 v100,v100,v101
v_pk_min_u16 v102,v102,v103
v_pk_min_u16 v100,v100,v102
v_lshrrev_b32 v101,16,v100
v_and_b32 v100,0xffff,v100
v_min_u32 v100,v100,v101
v_mov_b32 v101,0x3a00
v_cmp_lt_u32_e64 s[92:93],v100,v101
s_nop 0
s_or_b32 s64,s92,s93
v_and_b32 v152,s65,v152
v_lshrrev_b32 v101,16,v152
v_and_b32 v152,0xffff,v152
v_max_u32 v152,v152,v101
v_mov_b32 v101,0x4580
v_cmp_le_u32_e64 s[92:93],v101,v152
s_nop 0
s_or_b32 s64,s64,s92
s_or_b32 s64,s64,s93
v_and_b32 v153,s65,v153
v_and_b32 v144,s65,v144
v_pk_max_u16 v153,v153,v144
v_and_b32 v145,s65,v145
v_pk_max_u16 v153,v153,v145
v_and_b32 v146,s65,v146
v_pk_max_u16 v153,v153,v146
v_and_b32 v147,s65,v147
v_pk_max_u16 v153,v153,v147
v_and_b32 v148,s65,v148
v_pk_max_u16 v153,v153,v148
v_and_b32 v149,s65,v149
v_pk_max_u16 v153,v153,v149
v_and_b32 v150,s65,v150
v_pk_max_u16 v153,v153,v150
v_and_b32 v151,s65,v151
v_pk_max_u16 v153,v153,v151
v_lshrrev_b32 v101,16,v153
v_and_b32 v153,0xffff,v153
v_max_u32 v153,v153,v101
v_mov_b32 v101,0x5380
v_cmp_le_u32_e64 s[92:93],v101,v153
s_nop 0
s_or_b32 s64,s64,s92
s_or_b32 s64,s64,s93
v_and_b32 v156,s65,v156
v_and_b32 v157,s65,v157
v_and_b32 v158,s65,v158
v_and_b32 v159,s65,v159
v_pk_max_u16 v156,v156,v157
v_pk_max_u16 v156,v156,v158
v_pk_max_u16 v156,v156,v159
v_lshrrev_b32 v101,16,v156
v_and_b32 v156,0xffff,v156
v_max_u32 v156,v156,v101
v_mov_b32 v101,0x4280
v_cmp_le_u32_e64 s[92:93],v101,v156
s_nop 0
s_or_b32 s64,s64,s92
s_or_b32 s64,s64,s93
s_cmp_eq_u32 s64,0
s_cbranch_scc1 N_s1_%=
v_mov_b32 v7,0x7c007c00
N_s1_%=:
v_and_b32 v1,0x7c007c00,v7
v_lshrrev_b32 v2,16,v1
v_and_b32 v1,0xffff,v1
v_max_u32 v1,v1,v2
v_cmp_eq_u32 vcc,0x7c00,v1
s_or_b32 s64,vcc_lo,vcc_hi
v_mov_b32 v1,s64
v_mov_b32 v2,8192
s_mov_b64 exec,1
ds_or_b32 v2,v1 offset:3456
s_mov_b64 exec,-1
s_waitcnt lgkmcnt(0)
s_barrier
s_setprio 3
ds_read_b32 v1,v2 offset:3456
s_waitcnt lgkmcnt(0)
s_setprio 0
v_readfirstlane_b32 s64,v1
s_cmp_lg_u32 s64,0
s_cbranch_scc1 N_fallback_%=
s_endpgm
s_nop 4
N_B_%=:
v_mov_b32 v192,0x409d3ef8
v_and_b32 v248,255,%1
v_lshlrev_b32 v198,4,v248
v_lshlrev_b32 v248,19,v248
v_cmp_eq_u32_e64 s[94:95],0,v248
v_add_u32 v199,0x8000,v198
s_mov_b32 s4,0x3f000000
s_mov_b32 s5,0x3fc00000
s_mov_b32 s6,0x40200000
s_mov_b32 s7,0x40600000
s_mov_b32 s8,0x40900000
s_mov_b32 s9,0x40b00000
s_mov_b32 s10,0x40d00000
s_mov_b32 s11,0x40f00000
s_mov_b32 s12,0x3f80222e
s_mov_b32 s13,0x13089abf
s_mov_b32 s96,0xebefe000
s_mov_b32 s15,0
N_tl_%=:
v_add_u32 v237,0x10000,v248
v_mul_f32 v194,s99,v248
v_fma_f32 v196,v237,s99,-v194
v_pk_fma_f32 v[160:161],v[196:197],s[4:5],v[194:195] op_sel_hi:[0,1,0]
v_pk_fma_f32 v[162:163],v[196:197],s[6:7],v[194:195] op_sel_hi:[0,1,0]
v_pk_fma_f32 v[164:165],v[196:197],s[8:9],v[194:195] op_sel_hi:[0,1,0]
v_pk_fma_f32 v[166:167],v[196:197],s[10:11],v[194:195] op_sel_hi:[0,1,0]
v_cndmask_b32_e64 v160,v160,0,s[94:95]
v_pk_mul_f32 v[168:169],v[160:161],v[160:161]
v_pk_mul_f32 v[170:171],v[162:163],v[162:163]
v_pk_mul_f32 v[172:173],v[164:165],v[164:165]
v_pk_mul_f32 v[174:175],v[166:167],v[166:167]
v_pk_fma_f32 v[168:169],v[168:169],s[12:13],v[192:193] op_sel_hi:[1,0,0]
v_pk_fma_f32 v[170:171],v[170:171],s[12:13],v[192:193] op_sel_hi:[1,0,0]
v_pk_fma_f32 v[172:173],v[172:173],s[12:13],v[192:193] op_sel_hi:[1,0,0]
v_pk_fma_f32 v[174:175],v[174:175],s[12:13],v[192:193] op_sel_hi:[1,0,0]
v_pk_mul_f32 v[168:169],v[168:169],v[160:161]
v_pk_mul_f32 v[170:171],v[170:171],v[162:163]
v_pk_mul_f32 v[172:173],v[172:173],v[164:165]
v_pk_mul_f32 v[174:175],v[174:175],v[166:167]
v_exp_f32 v168,v168
v_exp_f32 v169,v169
v_exp_f32 v170,v170
v_exp_f32 v171,v171
v_exp_f32 v172,v172
v_exp_f32 v173,v173
v_exp_f32 v174,v174
v_exp_f32 v175,v175
v_pk_fma_f32 v[168:169],v[168:169],s[96:97],s[96:97] op_sel_hi:[1,0,0]
v_pk_fma_f32 v[170:171],v[170:171],s[96:97],s[96:97] op_sel_hi:[1,0,0]
v_pk_fma_f32 v[172:173],v[172:173],s[96:97],s[96:97] op_sel_hi:[1,0,0]
v_pk_fma_f32 v[174:175],v[174:175],s[96:97],s[96:97] op_sel_hi:[1,0,0]
v_rcp_f32 v168,v168
v_rcp_f32 v169,v169
v_rcp_f32 v170,v170
v_rcp_f32 v171,v171
v_rcp_f32 v172,v172
v_rcp_f32 v173,v173
v_rcp_f32 v174,v174
v_rcp_f32 v175,v175
v_pk_mul_f32 v[168:169],v[168:169],v[160:161]
v_pk_mul_f32 v[170:171],v[170:171],v[162:163]
v_pk_mul_f32 v[172:173],v[172:173],v[164:165]
v_pk_mul_f32 v[174:175],v[174:175],v[166:167]
v_pk_fma_f32 v[160:161],v[160:161],s[12:13],v[168:169] op_sel:[0,1,0] op_sel_hi:[1,1,1]
v_pk_fma_f32 v[162:163],v[162:163],s[12:13],v[170:171] op_sel:[0,1,0] op_sel_hi:[1,1,1]
v_pk_fma_f32 v[164:165],v[164:165],s[12:13],v[172:173] op_sel:[0,1,0] op_sel_hi:[1,1,1]
v_pk_fma_f32 v[166:167],v[166:167],s[12:13],v[174:175] op_sel:[0,1,0] op_sel_hi:[1,1,1]
v_cvt_pk_bf16_f32 v176,v168,v169
v_cvt_pk_bf16_f32 v177,v170,v171
v_cvt_pk_bf16_f32 v178,v172,v173
v_cvt_pk_bf16_f32 v179,v174,v175
v_cvt_pk_bf16_f32 v180,v160,v161
v_cvt_pk_bf16_f32 v181,v162,v163
v_cvt_pk_bf16_f32 v182,v164,v165
v_cvt_pk_bf16_f32 v183,v166,v167
ds_write_b128 v198,v[176:179] offset:0
ds_write_b128 v199,v[180:183] offset:32768
s_mov_b64 s[94:95],0
v_add_u32 v248,0x8000000,v248
v_add_u32 v198,4096,v198
v_add_u32 v199,4096,v199
s_add_u32 s15,s15,1
s_cmp_lt_u32 s15,2
s_cbranch_scc1 N_tl_%=
v_bfe_u32 v236,v232,2,3
v_mul_u32_u24 v236,768,v236
v_and_b32 v237,3,v232
v_lshrrev_b32 v238,5,v232
v_lshl_add_u32 v237,v238,2,v237
v_lshl_add_u32 v236,v237,4,v236
v_lshlrev_b32 v237,4,v232
s_bitcmp0_b32 s52,0
s_mov_b32 s58,40896
s_cselect_b32 s58,16256,s58
s_cselect_b64 s[92:93],-1,0
s_cselect_b32 s59,64,6144
s_movk_i32 s53,1024
s_cselect_b32 s53,6144,s53
s_cselect_b32 s60,0,67584
s_cselect_b32 s97,64,0
s_cselect_b32 s69,0,0x70000000
s_mov_b32 s100,73728
s_cselect_b32 s100,110688,s100
s_cselect_b32 s64,16,0
s_movk_i32 s50,1032
s_cselect_b32 s50,1024,s50
s_cselect_b32 s51,s53,-72704
v_cndmask_b32_e64 v236,v237,v236,s[92:93]
v_cndmask_b32_e64 v5,v241,v236,vcc
v_xor_b32 v236,s64,v236
v_cndmask_b32_e64 v6,v6,v236,vcc
s_mov_b32 s46,73728
s_mov_b32 s47,0x00020000
s_waitcnt lgkmcnt(0)
s_sub_u32 s36,s20,192
s_subb_u32 s37,s21,0
s_and_b32 s37,s37,0xffff
s_bitcmp0_b32 s52,0
s_cselect_b64 s[44:45],s[24:25],s[28:29]
s_and_b32 s45,s45,0xffff
s_cbranch_vccz RB1_%=
s_add_u32 m0,s58,12320
s_nop 0
buffer_load_dwordx4 v5,s[44:47],s69 offen lds
s_add_u32 m0,m0,1024
s_add_u32 s69,s69,s53
buffer_load_dwordx4 v6,s[44:47],s69 offen lds
s_add_u32 m0,m0,1024
s_add_u32 s69,s69,s53
buffer_load_dwordx4 v5,s[44:47],s69 offen lds
s_add_u32 m0,m0,s50
s_add_u32 s69,s69,s53
buffer_load_dwordx4 v6,s[44:47],s69 offen lds
s_add_u32 m0,m0,1024
s_add_u32 s69,s69,s53
buffer_load_dwordx4 v5,s[44:47],s69 offen lds
s_add_u32 m0,m0,1024
s_add_u32 s69,s69,s53
buffer_load_dwordx4 v6,s[44:47],s69 offen lds
s_add_u32 m0,m0,s50
s_add_u32 s69,s69,s51
buffer_load_dwordx4 v5,s[44:47],s69 offen lds
s_add_u32 m0,m0,1024
s_add_u32 s69,s69,s53
buffer_load_dwordx4 v6,s[44:47],s69 offen lds
s_add_u32 m0,m0,1024
s_add_u32 s69,s69,s53
buffer_load_dwordx4 v5,s[44:47],s69 offen lds
s_add_u32 m0,m0,s50
s_add_u32 s69,s69,s53
buffer_load_dwordx4 v6,s[44:47],s69 offen lds
s_add_u32 m0,m0,1024
s_add_u32 s69,s69,s53
buffer_load_dwordx4 v5,s[44:47],s69 offen lds
s_add_u32 m0,m0,1024
s_add_u32 s69,s69,s53
buffer_load_dwordx4 v6,s[44:47],s69 offen lds
RB1_%=:
s_and_b32 s64,s52,3
s_mul_i32 s67,s64,54
s_lshl_b32 s10,s64,2
s_cmp_eq_u32 s70,31
s_cselect_b32 s66,17,99
v_add_u32 v236,s67,v232
v_add_u32 v236,108,v236
v_mul_u32_u24 v237,0xaaab,v236
v_lshrrev_b32 v237,19,v237
v_mul_u32_u24 v238,12,v237
v_sub_u32 v238,v236,v238
v_add_u32 v236,1,v237
v_bfe_u32 v236,v236,2,2
v_xor_b32 v238,v238,v236
v_mul_u32_u24 v236,192,v237
v_lshl_add_u32 v239,v238,4,v236
v_cmp_eq_u32_e64 s[92:93],s66,v237
v_mov_b32 v236,0x7fff0000
s_nop 0
v_cndmask_b32_e64 v239,v239,v236,s[92:93]
v_cndmask_b32_e64 v6,v239,v6,vcc
s_add_u32 s64,s10,s71
v_and_b32 v233,15,v232
v_lshrrev_b32 v234,4,v232
s_mul_i32 s61,s64,98304
s_add_u32 s61,s61,s68
v_and_b32 v236,1,v234
v_lshlrev_b32 v236,1,v236
v_lshrrev_b32 v237,5,v232
v_or_b32 v235,v236,v237
s_mov_b32 s54,0x44ea44ea
s_mov_b32 s55,0x877f877f
s_mov_b32 s8,0x0ff00ff0
s_mov_b32 s9,0x0ff00ff0
v_mov_b32 v7,0
v_mov_b32 v239,0x70000
v_bfe_u32 v236,v233,1,1
v_sub_u32 v236,v235,v236
v_cmp_gt_u32_e64 s[92:93],3,v236
v_lshrrev_b32 v237,2,v233
v_lshlrev_b32 v237,1,v237
v_and_b32 v238,1,v233
v_or_b32 v237,v237,v238
v_lshl_add_u32 v236,v236,3,v237
v_lshlrev_b32 v236,4,v236
v_add_u32 v236,143488,v236
v_cndmask_b32_e64 v11,v239,v236,s[92:93]
v_and_b32 v13,3,v237
v_lshlrev_b32 v236,1,v233
v_and_b32 v236,4,v236
v_lshl_add_u32 v236,v234,3,v236
v_mov_b32 v238,0x54250310
v_mul_u32_u24 v13,0x5555,v13
v_bfe_u32 v236,v238,v236,3
v_lshl_add_u32 v236,v236,3,v237
v_lshlrev_b32 v236,4,v236
v_add_u32 v12,143488,v236
v_lshrrev_b32 v237,2,v233
v_cmp_eq_u32_e64 s[92:93],v234,v237
v_lshlrev_b32 v236,4,v233
v_add_u32 v236,10112,v236
v_cndmask_b32_e64 v14,v239,v236,s[92:93]
v_and_b32 v239,7,v233
v_lshl_add_u32 v236,v239,1,v235
v_add_u32 v237,1,v236
v_bfe_u32 v237,v237,2,2
v_mul_u32_u24 v236,192,v236
v_lshl_add_u32 v236,v237,4,v236
v_add_u32 v237,1,v239
v_bfe_u32 v237,v237,1,2
v_xor_b32 v237,v237,v234
v_mul_u32_u24 v239,384,v239
v_lshl_add_u32 v237,v237,4,v239
v_sub_u32 v237,v237,v236
v_add_u32 v8,192,v237
v_add_u32 v15,73728,v236
v_and_b32 v238,1,v233
v_mul_u32_u24 v238,320,v238
v_sub_u32 v239,v239,v238
s_mov_b32 s12,0xaaaaaaaa
s_mov_b32 s13,0xaaaaaaaa
v_lshl_add_u32 v10,v234,4,v239
s_mov_b64 exec,s[8:9]
v_add_u32 v10,196608,v10
s_mov_b64 exec,-1
v_lshrrev_b32 v236,3,v233
v_bfe_u32 v237,v233,2,1
v_lshl_add_u32 v237,v236,4,v237
v_lshl_add_u32 v237,v234,1,v237
v_lshlrev_b32 v237,6,v237
v_lshl_add_u32 v237,v236,4,v237
v_and_b32 v238,1,v233
v_lshl_add_u32 v237,v238,5,v237
v_bfe_u32 v238,v233,1,1
v_lshl_add_u32 v237,v238,3,v237
v_xor_b32 v238,16,v237
v_add_u32 v1,16256,v237
v_add_u32 v2,16256,v238
v_lshrrev_b32 v236,3,v233
v_bfe_u32 v237,v233,2,1
v_lshl_add_u32 v236,v237,1,v236
v_lshrrev_b32 v237,1,v234
v_lshl_add_u32 v236,v237,2,v236
v_mul_u32_u24 v236,192,v236
v_and_b32 v238,1,v234
v_mul_u32_u24 v239,3080,v238
v_add_u32 v236,v236,v239
v_and_b32 v239,3,v233
v_lshl_add_u32 v236,v239,4,v236
v_add_u32 v3,40896,v236
v_lshlrev_b32 v238,6,v238
s_mov_b32 s86,0xbe801d5a
v_lshl_add_u32 v238,v237,4,v238
v_add_u32 v4,8192,v238
s_mov_b64 s[40:41],s[34:35]
s_and_b32 s41,s41,0xffff
s_mov_b32 s42,50331648
s_mov_b32 s43,0x00020000
s_waitcnt lgkmcnt(0)
s_barrier
s_setprio 0
s_cbranch_vccz RB2_%=
s_add_u32 m0,s58,0
s_mul_i32 s69,s59,1
s_add_u32 s69,s69,s97
buffer_load_dwordx4 v5,s[44:47],s69 offen lds
s_add_u32 m0,m0,1024
s_add_u32 s69,s69,s53
buffer_load_dwordx4 v6,s[44:47],s69 offen lds
s_add_u32 m0,m0,1024
s_add_u32 s69,s69,s53
buffer_load_dwordx4 v5,s[44:47],s69 offen lds
s_add_u32 m0,m0,s50
s_add_u32 s69,s69,s53
buffer_load_dwordx4 v6,s[44:47],s69 offen lds
s_add_u32 m0,m0,1024
s_add_u32 s69,s69,s53
buffer_load_dwordx4 v5,s[44:47],s69 offen lds
s_add_u32 m0,m0,1024
s_add_u32 s69,s69,s53
buffer_load_dwordx4 v6,s[44:47],s69 offen lds
s_add_u32 m0,m0,s50
s_add_u32 s69,s69,s53
buffer_load_dwordx4 v5,s[44:47],s69 offen lds
s_add_u32 m0,m0,1024
s_add_u32 s69,s69,s53
buffer_load_dwordx4 v6,s[44:47],s69 offen lds
s_add_u32 m0,m0,1024
s_add_u32 s69,s69,s53
buffer_load_dwordx4 v5,s[44:47],s69 offen lds
s_add_u32 m0,m0,s50
s_add_u32 s69,s69,s53
buffer_load_dwordx4 v6,s[44:47],s69 offen lds
s_add_u32 m0,m0,1024
s_add_u32 s69,s69,s53
buffer_load_dwordx4 v5,s[44:47],s69 offen lds
s_add_u32 m0,m0,1024
s_add_u32 s69,s69,s53
buffer_load_dwordx4 v6,s[44:47],s69 offen lds
RB2_%=:
v_lshlrev_b32 v253,7,v232
s_bfe_u32 s64,%0,0x20003
s_and_b32 s65,s52,3
s_lshl_b32 s65,s65,2
s_add_u32 s64,s64,s65
s_lshl_b32 s65,s64,13
v_add_u32 v252,s65,v253
s_getpc_b64 s[14:15]
N_pf_%=:
s_sub_u32 s14,s14,N_pf_%=-N_main_%=
s_subb_u32 s15,s15,0
v_min_u32 v252,N_mainend_%=-N_main_%=,v252
global_load_dword v254,v252,s[14:15]
s_getpc_b64 s[88:89]
N_itB3_%=:
s_lshl_b32 s87,s10,10
s_add_u32 s87,s87,4104
s_sub_u32 s88,s88,N_itB3_%=-N_main_%=
s_subb_u32 s89,s89,0
s_add_u32 s88,s88,s87
s_addc_u32 s89,s89,0
s_swappc_b64 s[90:91],s[88:89]
s_cbranch_vccnz RB4_%=
s_mov_b64 exec,s[62:63]
s_add_u32 m0,s57,62784
s_add_u32 s64,s56,1769472
buffer_load_dwordx4 v5,s[36:39],s64 offen lds
s_add_u32 m0,s57,64512
s_nop 0
buffer_load_dwordx4 v6,s[36:39],s64 offen lds
s_mov_b64 exec,-1
RB4_%=:
s_cbranch_vccnz RB5_%=
s_mov_b64 exec,s[62:63]
s_add_u32 m0,s57,66272
s_add_u32 s64,s56,1867776
buffer_load_dwordx4 v5,s[36:39],s64 offen lds
s_add_u32 m0,s57,68000
s_nop 0
buffer_load_dwordx4 v6,s[36:39],s64 offen lds
s_mov_b64 exec,-1
RB5_%=:
s_waitcnt vmcnt(13)
s_barrier
s_cbranch_vccnz RB6_%=
s_mov_b64 exec,s[62:63]
s_add_u32 m0,s57,0
s_add_u32 s64,s56,1966080
buffer_load_dwordx4 v5,s[36:39],s64 offen lds
s_add_u32 m0,s57,1728
s_nop 0
buffer_load_dwordx4 v6,s[36:39],s64 offen lds
s_mov_b64 exec,-1
RB6_%=:
s_sleep 4
s_cbranch_vccnz RB7_%=
s_mov_b64 exec,s[62:63]
s_add_u32 m0,s57,3488
s_add_u32 s64,s56,2064384
buffer_load_dwordx4 v5,s[36:39],s64 offen lds
s_add_u32 m0,s57,5216
s_nop 0
buffer_load_dwordx4 v6,s[36:39],s64 offen lds
s_mov_b64 exec,-1
RB7_%=:
s_sleep 4
s_cbranch_vccnz RB8_%=
s_mov_b64 exec,s[62:63]
s_add_u32 m0,s57,6976
s_add_u32 s64,s56,2162688
buffer_load_dwordx4 v5,s[36:39],s64 offen lds
s_add_u32 m0,s57,8704
s_nop 0
buffer_load_dwordx4 v6,s[36:39],s64 offen lds
s_mov_b64 exec,-1
RB8_%=:
s_sleep 4
s_cbranch_vccnz RB9_%=
s_mov_b64 exec,s[62:63]
s_add_u32 m0,s57,10464
s_add_u32 s64,s56,2260992
buffer_load_dwordx4 v5,s[36:39],s64 offen lds
s_add_u32 m0,s57,12192
s_nop 0
buffer_load_dwordx4 v6,s[36:39],s64 offen lds
s_mov_b64 exec,-1
RB9_%=:
s_cbranch_vccz RB10_%=
s_waitcnt vmcnt(1)
RB10_%=:
s_barrier
s_cbranch_vccz RB11_%=
s_add_u32 m0,s58,12320
s_mul_i32 s69,s59,3
s_add_u32 s69,s69,s97
buffer_load_dwordx4 v5,s[44:47],s69 offen lds
s_add_u32 m0,m0,1024
s_add_u32 s69,s69,s53
buffer_load_dwordx4 v6,s[44:47],s69 offen lds
RB11_%=:
s_cbranch_vccz RB12_%=
s_add_u32 m0,m0,1024
s_add_u32 s69,s69,s53
buffer_load_dwordx4 v5,s[44:47],s69 offen lds
s_add_u32 m0,m0,s50
s_add_u32 s69,s69,s53
buffer_load_dwordx4 v6,s[44:47],s69 offen lds
RB12_%=:
s_cbranch_vccnz RB13_%=
s_mov_b64 exec,s[62:63]
s_add_u32 m0,s57,13952
s_add_u32 s64,s56,2359296
buffer_load_dwordx4 v5,s[36:39],s64 offen lds
s_add_u32 m0,s57,15680
s_nop 0
buffer_load_dwordx4 v6,s[36:39],s64 offen lds
s_mov_b64 exec,-1
RB13_%=:
s_cbranch_vccz RB14_%=
s_add_u32 m0,m0,1024
s_add_u32 s69,s69,s53
buffer_load_dwordx4 v5,s[44:47],s69 offen lds
s_add_u32 m0,m0,1024
s_add_u32 s69,s69,s53
buffer_load_dwordx4 v6,s[44:47],s69 offen lds
RB14_%=:
s_cbranch_vccz RB15_%=
s_add_u32 m0,m0,s50
s_add_u32 s69,s69,s53
buffer_load_dwordx4 v5,s[44:47],s69 offen lds
s_add_u32 m0,m0,1024
s_add_u32 s69,s69,s53
buffer_load_dwordx4 v6,s[44:47],s69 offen lds
RB15_%=:
s_cbranch_vccz RB16_%=
s_add_u32 m0,m0,1024
s_add_u32 s69,s69,s53
buffer_load_dwordx4 v5,s[44:47],s69 offen lds
s_add_u32 m0,m0,s50
s_add_u32 s69,s69,s53
buffer_load_dwordx4 v6,s[44:47],s69 offen lds
RB16_%=:
s_cbranch_vccz RB17_%=
s_add_u32 m0,m0,1024
s_add_u32 s69,s69,s53
buffer_load_dwordx4 v5,s[44:47],s69 offen lds
s_add_u32 m0,m0,1024
s_add_u32 s69,s69,s53
buffer_load_dwordx4 v6,s[44:47],s69 offen lds
RB17_%=:
s_cbranch_vccnz RB18_%=
s_mov_b64 exec,s[62:63]
s_add_u32 m0,s57,17440
s_add_u32 s64,s56,2457600
buffer_load_dwordx4 v5,s[36:39],s64 offen lds
s_add_u32 m0,s57,19168
s_nop 0
buffer_load_dwordx4 v6,s[36:39],s64 offen lds
s_mov_b64 exec,-1
RB18_%=:
s_sleep 4
s_cbranch_vccnz RB19_%=
s_mov_b64 exec,s[62:63]
s_add_u32 m0,s57,20928
s_add_u32 s64,s56,2555904
buffer_load_dwordx4 v5,s[36:39],s64 offen lds
s_add_u32 m0,s57,22656
s_nop 0
buffer_load_dwordx4 v6,s[36:39],s64 offen lds
s_mov_b64 exec,-1
RB19_%=:
s_sleep 4
s_cbranch_vccnz RB20_%=
s_mov_b64 exec,s[62:63]
s_add_u32 m0,s57,24416
s_add_u32 s64,s56,2654208
buffer_load_dwordx4 v5,s[36:39],s64 offen lds
s_add_u32 m0,s57,26144
s_nop 0
buffer_load_dwordx4 v6,s[36:39],s64 offen lds
s_mov_b64 exec,-1
RB20_%=:
s_sleep 4
s_cbranch_vccnz RB21_%=
s_mov_b64 exec,s[62:63]
s_add_u32 m0,s57,27904
s_add_u32 s64,s56,2752512
buffer_load_dwordx4 v5,s[36:39],s64 offen lds
s_add_u32 m0,s57,29632
s_nop 0
buffer_load_dwordx4 v6,s[36:39],s64 offen lds
s_mov_b64 exec,-1
RB21_%=:
s_sleep 4
s_cbranch_vccnz RB22_%=
s_mov_b64 exec,s[62:63]
s_add_u32 m0,s57,31392
s_add_u32 s64,s56,2850816
buffer_load_dwordx4 v5,s[36:39],s64 offen lds
s_add_u32 m0,s57,33120
s_nop 0
buffer_load_dwordx4 v6,s[36:39],s64 offen lds
s_mov_b64 exec,-1
RB22_%=:
s_sleep 4
s_cbranch_vccnz RB23_%=
s_mov_b64 exec,s[62:63]
s_add_u32 m0,s57,34880
s_add_u32 s64,s56,2949120
buffer_load_dwordx4 v5,s[36:39],s64 offen lds
s_add_u32 m0,s57,36608
s_nop 0
buffer_load_dwordx4 v6,s[36:39],s64 offen lds
s_mov_b64 exec,-1
RB23_%=:
s_sleep 4
s_cbranch_vccnz RB24_%=
s_mov_b64 exec,s[62:63]
s_add_u32 m0,s57,38368
s_add_u32 s64,s56,3047424
buffer_load_dwordx4 v5,s[36:39],s64 offen lds
s_add_u32 m0,s57,40096
s_nop 0
buffer_load_dwordx4 v6,s[36:39],s64 offen lds
s_mov_b64 exec,-1
RB24_%=:
s_cbranch_vccz RB25_%=
s_waitcnt vmcnt(0)
RB25_%=:
s_barrier
s_cbranch_vccz RB26_%=
s_add_u32 m0,s58,0
s_mul_i32 s69,s59,5
s_add_u32 s69,s69,s97
buffer_load_dwordx4 v5,s[44:47],s69 offen lds
s_add_u32 m0,m0,1024
s_add_u32 s69,s69,s53
buffer_load_dwordx4 v6,s[44:47],s69 offen lds
RB26_%=:
s_cbranch_vccz RB27_%=
s_add_u32 m0,m0,1024
s_add_u32 s69,s69,s53
buffer_load_dwordx4 v5,s[44:47],s69 offen lds
s_add_u32 m0,m0,s50
s_add_u32 s69,s69,s53
buffer_load_dwordx4 v6,s[44:47],s69 offen lds
RB27_%=:
s_cbranch_vccz RB28_%=
s_add_u32 m0,m0,1024
s_add_u32 s69,s69,s53
buffer_load_dwordx4 v5,s[44:47],s69 offen lds
s_add_u32 m0,m0,1024
s_add_u32 s69,s69,s53
buffer_load_dwordx4 v6,s[44:47],s69 offen lds
RB28_%=:
s_cbranch_vccz RB29_%=
s_add_u32 m0,m0,s50
s_add_u32 s69,s69,s53
buffer_load_dwordx4 v5,s[44:47],s69 offen lds
s_add_u32 m0,m0,1024
s_add_u32 s69,s69,s53
buffer_load_dwordx4 v6,s[44:47],s69 offen lds
RB29_%=:
s_cbranch_vccz RB30_%=
s_add_u32 m0,m0,1024
s_add_u32 s69,s69,s53
buffer_load_dwordx4 v5,s[44:47],s69 offen lds
s_add_u32 m0,m0,s50
s_add_u32 s69,s69,s53
buffer_load_dwordx4 v6,s[44:47],s69 offen lds
RB30_%=:
s_cbranch_vccnz RB31_%=
s_mov_b64 exec,s[62:63]
s_add_u32 m0,s57,41856
s_add_u32 s64,s56,3145728
buffer_load_dwordx4 v5,s[36:39],s64 offen lds
s_add_u32 m0,s57,43584
s_nop 0
buffer_load_dwordx4 v6,s[36:39],s64 offen lds
s_mov_b64 exec,-1
RB31_%=:
s_cbranch_vccz RB32_%=
s_add_u32 m0,m0,1024
s_add_u32 s69,s69,s53
buffer_load_dwordx4 v5,s[44:47],s69 offen lds
s_add_u32 m0,m0,1024
s_add_u32 s69,s69,s53
buffer_load_dwordx4 v6,s[44:47],s69 offen lds
RB32_%=:
s_sleep 3
s_cbranch_vccnz RB33_%=
s_mov_b64 exec,s[62:63]
s_add_u32 m0,s57,45344
s_add_u32 s64,s56,3244032
buffer_load_dwordx4 v5,s[36:39],s64 offen lds
s_add_u32 m0,s57,47072
s_nop 0
buffer_load_dwordx4 v6,s[36:39],s64 offen lds
s_mov_b64 exec,-1
RB33_%=:
s_add_u32 s4,s10,16
s_cmp_ge_u32 s4,20
s_cselect_b32 s7,20,0
s_sub_u32 s4,s4,s7
s_mul_i32 s4,s4,3488
s_add_u32 s5,s10,18
s_cmp_ge_u32 s5,20
s_cselect_b32 s7,20,0
s_sub_u32 s5,s5,s7
s_mul_i32 s5,s5,3488
s_add_u32 s6,s10,0
s_cmp_ge_u32 s6,20
s_cselect_b32 s7,20,0
s_sub_u32 s6,s6,s7
s_mul_i32 s6,s6,3488
v_add_u32 v112,s4,v15
v_add_u32 v116,s5,v15
s_mov_b64 exec,s[8:9]
v_add_u32 v112,s5,v15
v_add_u32 v116,s6,v15
s_mov_b64 exec,-1
v_xor_b32 v113,16,v112
v_xor_b32 v117,16,v116
v_xor_b32 v114,32,v112
v_xor_b32 v118,32,v116
v_xor_b32 v115,48,v112
v_xor_b32 v119,48,v116
s_waitcnt vmcnt(0)
s_barrier
s_setprio 3
ds_read_b128 v[136:139],v116 offset:0
ds_read_b128 v[88:91],v11 offset:768
ds_read_b128 v[140:143],v116 offset:3488
ds_read_b128 v[128:131],v112 offset:0
ds_read_b128 v[132:135],v112 offset:3488
ds_read_b128 v[92:95],v12 offset:0
ds_read_b128 v[152:155],v117 offset:0
ds_read_b128 v[96:99],v11 offset:1920
ds_read_b128 v[156:159],v117 offset:3488
ds_read_b128 v[144:147],v113 offset:0
ds_read_b128 v[148:151],v113 offset:3488
ds_read_b128 v[100:103],v12 offset:1152
s_waitcnt lgkmcnt(10)
v_mfma_f32_16x16x32_bf16 v[160:163],v[88:91],v[136:139],0
s_waitcnt lgkmcnt(9)
v_mfma_f32_16x16x32_bf16 v[184:187],v[88:91],v[140:143],0
s_waitcnt lgkmcnt(6)
ds_read_b128 v[140:143],v116 offset:3552
ds_read_b128 v[88:91],v11 offset:5376
v_smfmac_f32_16x16x64_bf16 v[160:163],v[92:95],v[128:135],v13
s_cbranch_vccz RB34_%=
s_add_u32 m0,s58,12320
s_mul_i32 s69,s59,7
s_add_u32 s69,s69,s97
buffer_load_dwordx4 v5,s[44:47],s69 offen lds
s_add_u32 m0,m0,1024
s_add_u32 s69,s69,s53
buffer_load_dwordx4 v6,s[44:47],s69 offen lds
RB34_%=:
ds_read_b128 v[128:131],v112 offset:64
v_smfmac_f32_16x16x64_bf16 v[184:187],v[92:95],v[132:139],v13
s_waitcnt lgkmcnt(7)
ds_read_b128 v[136:139],v116 offset:64
ds_read_b128 v[132:135],v112 offset:3552
v_mfma_f32_16x16x32_bf16 v[164:167],v[96:99],v[152:155],0
s_waitcnt lgkmcnt(8)
ds_read_b128 v[92:95],v12 offset:4608
v_add_f32 v120,v160,v161
v_mul_f32 v124,v160,v160
v_fmac_f32 v124,v161,v161
v_mfma_f32_16x16x32_bf16 v[188:191],v[96:99],v[156:159],0
s_waitcnt lgkmcnt(6)
ds_read_b128 v[156:159],v117 offset:3552
ds_read_b128 v[96:99],v11 offset:6528
v_add_f32 v121,v162,v163
v_mul_f32 v125,v162,v162
v_fmac_f32 v125,v163,v163
v_smfmac_f32_16x16x64_bf16 v[164:167],v[100:103],v[144:151],v13
s_cbranch_vccz RB35_%=
s_add_u32 m0,m0,1024
s_add_u32 s69,s69,s53
buffer_load_dwordx4 v5,s[44:47],s69 offen lds
s_add_u32 m0,m0,s50
s_add_u32 s69,s69,s53
buffer_load_dwordx4 v6,s[44:47],s69 offen lds
RB35_%=:
ds_read_b128 v[144:147],v113 offset:64
v_add_f32 v122,v184,v185
v_mul_f32 v126,v184,v184
v_fmac_f32 v126,v185,v185
v_smfmac_f32_16x16x64_bf16 v[188:191],v[100:103],v[148:155],v13
s_waitcnt lgkmcnt(5)
ds_read_b128 v[152:155],v117 offset:64
ds_read_b128 v[148:151],v113 offset:3552
v_add_f32 v123,v186,v187
v_mul_f32 v127,v186,v186
v_fmac_f32 v127,v187,v187
v_mfma_f32_16x16x32_bf16 v[168:171],v[88:91],v[136:139],0
ds_read_b128 v[100:103],v12 offset:5760
v_add_f32 v120,v120,v164
v_fmac_f32 v124,v164,v164
v_add_f32 v120,v120,v165
v_fmac_f32 v124,v165,v165
v_mfma_f32_16x16x32_bf16 v[192:195],v[88:91],v[140:143],0
s_waitcnt lgkmcnt(6)
s_cbranch_vccz RB36_%=
s_add_u32 m0,m0,1024
s_add_u32 s69,s69,s53
buffer_load_dwordx4 v5,s[44:47],s69 offen lds
s_add_u32 m0,m0,1024
s_add_u32 s69,s69,s53
buffer_load_dwordx4 v6,s[44:47],s69 offen lds
RB36_%=:
ds_read_b128 v[140:143],v116 offset:3616
ds_read_b128 v[88:91],v11 offset:9984
v_add_f32 v121,v121,v166
v_fmac_f32 v125,v166,v166
v_add_f32 v121,v121,v167
v_fmac_f32 v125,v167,v167
v_smfmac_f32_16x16x64_bf16 v[168:171],v[92:95],v[128:135],v13
ds_read_b128 v[128:131],v112 offset:128
v_add_f32 v122,v122,v188
v_fmac_f32 v126,v188,v188
v_add_f32 v122,v122,v189
v_fmac_f32 v126,v189,v189
v_smfmac_f32_16x16x64_bf16 v[192:195],v[92:95],v[132:139],v13
s_waitcnt lgkmcnt(5)
s_cbranch_vccz RB37_%=
s_add_u32 m0,m0,s50
s_add_u32 s69,s69,s53
buffer_load_dwordx4 v5,s[44:47],s69 offen lds
s_add_u32 m0,m0,1024
s_add_u32 s69,s69,s53
buffer_load_dwordx4 v6,s[44:47],s69 offen lds
RB37_%=:
ds_read_b128 v[136:139],v116 offset:128
ds_read_b128 v[132:135],v112 offset:3616
v_add_f32 v123,v123,v190
v_fmac_f32 v127,v190,v190
v_add_f32 v123,v123,v191
v_fmac_f32 v127,v191,v191
v_mfma_f32_16x16x32_bf16 v[172:175],v[96:99],v[152:155],0
ds_read_b128 v[92:95],v12 offset:9216
v_add_f32 v120,v120,v168
v_fmac_f32 v124,v168,v168
v_add_f32 v120,v120,v169
v_fmac_f32 v124,v169,v169
v_mfma_f32_16x16x32_bf16 v[196:199],v[96:99],v[156:159],0
s_waitcnt lgkmcnt(6)
s_cbranch_vccz RB38_%=
s_add_u32 m0,m0,1024
s_add_u32 s69,s69,s53
buffer_load_dwordx4 v5,s[44:47],s69 offen lds
s_add_u32 m0,m0,s50
s_add_u32 s69,s69,s53
buffer_load_dwordx4 v6,s[44:47],s69 offen lds
RB38_%=:
ds_read_b128 v[156:159],v117 offset:3616
ds_read_b128 v[96:99],v11 offset:11136
v_add_f32 v121,v121,v170
v_fmac_f32 v125,v170,v170
v_add_f32 v121,v121,v171
v_fmac_f32 v125,v171,v171
v_smfmac_f32_16x16x64_bf16 v[172:175],v[100:103],v[144:151],v13
ds_read_b128 v[144:147],v113 offset:128
v_add_f32 v122,v122,v192
v_fmac_f32 v126,v192,v192
v_add_f32 v122,v122,v193
v_fmac_f32 v126,v193,v193
v_smfmac_f32_16x16x64_bf16 v[196:199],v[100:103],v[148:155],v13
s_waitcnt lgkmcnt(5)
s_cbranch_vccz RB39_%=
s_add_u32 m0,m0,1024
s_add_u32 s69,s69,s53
buffer_load_dwordx4 v5,s[44:47],s69 offen lds
s_add_u32 m0,m0,1024
s_add_u32 s69,s69,s53
buffer_load_dwordx4 v6,s[44:47],s69 offen lds
RB39_%=:
ds_read_b128 v[152:155],v117 offset:128
ds_read_b128 v[148:151],v113 offset:3616
v_add_f32 v123,v123,v194
v_fmac_f32 v127,v194,v194
v_add_f32 v123,v123,v195
v_fmac_f32 v127,v195,v195
v_mfma_f32_16x16x32_bf16 v[176:179],v[88:91],v[136:139],0
ds_read_b128 v[100:103],v12 offset:10368
v_add_f32 v120,v120,v172
v_fmac_f32 v124,v172,v172
v_add_f32 v120,v120,v173
v_fmac_f32 v124,v173,v173
v_mfma_f32_16x16x32_bf16 v[200:203],v[88:91],v[140:143],0
s_waitcnt lgkmcnt(6)
ds_read_b128 v[140:143],v118 offset:3488
ds_read_b128 v[88:91],v11 offset:3072
v_add_f32 v121,v121,v174
v_fmac_f32 v125,v174,v174
v_add_f32 v121,v121,v175
v_fmac_f32 v125,v175,v175
v_smfmac_f32_16x16x64_bf16 v[176:179],v[92:95],v[128:135],v13
ds_read_b128 v[128:131],v114 offset:0
v_add_f32 v122,v122,v196
v_fmac_f32 v126,v196,v196
v_add_f32 v122,v122,v197
v_fmac_f32 v126,v197,v197
v_smfmac_f32_16x16x64_bf16 v[200:203],v[92:95],v[132:139],v13
s_waitcnt lgkmcnt(5)
ds_read_b128 v[136:139],v118 offset:0
ds_read_b128 v[132:135],v114 offset:3488
v_add_f32 v123,v123,v198
v_fmac_f32 v127,v198,v198
v_add_f32 v123,v123,v199
v_fmac_f32 v127,v199,v199
v_mfma_f32_16x16x32_bf16 v[180:183],v[96:99],v[152:155],0
ds_read_b128 v[92:95],v12 offset:2304
v_add_f32 v120,v120,v176
v_fmac_f32 v124,v176,v176
v_add_f32 v120,v120,v177
v_fmac_f32 v124,v177,v177
v_mfma_f32_16x16x32_bf16 v[204:207],v[96:99],v[156:159],0
s_waitcnt lgkmcnt(6)
ds_read_b128 v[156:159],v119 offset:3488
ds_read_b128 v[96:99],v11 offset:4224
v_add_f32 v121,v121,v178
v_fmac_f32 v125,v178,v178
v_add_f32 v121,v121,v179
v_fmac_f32 v125,v179,v179
v_smfmac_f32_16x16x64_bf16 v[180:183],v[100:103],v[144:151],v13
ds_read_b128 v[144:147],v115 offset:0
v_add_f32 v122,v122,v200
v_fmac_f32 v126,v200,v200
v_add_f32 v122,v122,v201
v_fmac_f32 v126,v201,v201
v_smfmac_f32_16x16x64_bf16 v[204:207],v[100:103],v[148:155],v13
s_waitcnt lgkmcnt(5)
ds_read_b128 v[152:155],v119 offset:0
ds_read_b128 v[148:151],v115 offset:3488
v_add_f32 v123,v123,v202
v_fmac_f32 v127,v202,v202
v_add_f32 v123,v123,v203
v_fmac_f32 v127,v203,v203
v_mfma_f32_16x16x32_bf16 v[208:211],v[88:91],v[136:139],0
ds_read_b128 v[100:103],v12 offset:3456
v_add_f32 v120,v120,v180
v_fmac_f32 v124,v180,v180
v_add_f32 v120,v120,v181
v_fmac_f32 v124,v181,v181
v_mfma_f32_16x16x32_bf16 v[232:235],v[88:91],v[140:143],0
s_waitcnt lgkmcnt(6)
ds_read_b128 v[140:143],v118 offset:3552
ds_read_b128 v[88:91],v11 offset:7680
v_add_f32 v121,v121,v182
v_fmac_f32 v125,v182,v182
v_add_f32 v121,v121,v183
v_fmac_f32 v125,v183,v183
v_smfmac_f32_16x16x64_bf16 v[208:211],v[92:95],v[128:135],v13
ds_read_b128 v[128:131],v114 offset:64
v_add_f32 v122,v122,v204
v_fmac_f32 v126,v204,v204
v_add_f32 v122,v122,v205
v_fmac_f32 v126,v205,v205
v_smfmac_f32_16x16x64_bf16 v[232:235],v[92:95],v[132:139],v13
s_waitcnt lgkmcnt(5)
ds_read_b128 v[136:139],v118 offset:64
ds_read_b128 v[132:135],v114 offset:3552
v_add_f32 v123,v123,v206
v_fmac_f32 v127,v206,v206
v_add_f32 v123,v123,v207
v_fmac_f32 v127,v207,v207
v_mfma_f32_16x16x32_bf16 v[212:215],v[96:99],v[152:155],0
ds_read_b128 v[92:95],v12 offset:6912
v_add_f32 v120,v120,v208
v_fmac_f32 v124,v208,v208
v_add_f32 v120,v120,v209
v_fmac_f32 v124,v209,v209
v_mfma_f32_16x16x32_bf16 v[236:239],v[96:99],v[156:159],0
s_waitcnt lgkmcnt(6)
s_setprio 2
ds_read_b128 v[156:159],v119 offset:3552
ds_read_b128 v[96:99],v11 offset:8832
v_add_f32 v121,v121,v210
v_fmac_f32 v125,v210,v210
v_add_f32 v121,v121,v211
v_fmac_f32 v125,v211,v211
v_smfmac_f32_16x16x64_bf16 v[212:215],v[100:103],v[144:151],v13
ds_read_b128 v[144:147],v115 offset:64
v_add_f32 v122,v122,v232
v_fmac_f32 v126,v232,v232
v_add_f32 v122,v122,v233
v_fmac_f32 v126,v233,v233
v_smfmac_f32_16x16x64_bf16 v[236:239],v[100:103],v[148:155],v13
s_waitcnt lgkmcnt(5)
ds_read_b128 v[152:155],v119 offset:64
ds_read_b128 v[148:151],v115 offset:3552
v_add_f32 v123,v123,v234
v_fmac_f32 v127,v234,v234
v_add_f32 v123,v123,v235
v_fmac_f32 v127,v235,v235
v_mfma_f32_16x16x32_bf16 v[216:219],v[88:91],v[136:139],0
ds_read_b128 v[100:103],v12 offset:8064
v_add_f32 v120,v120,v212
v_fmac_f32 v124,v212,v212
v_add_f32 v120,v120,v213
v_fmac_f32 v124,v213,v213
v_add_f32 v121,v121,v214
v_mfma_f32_16x16x32_bf16 v[240:243],v[88:91],v[140:143],0
s_waitcnt lgkmcnt(6)
ds_read_b128 v[140:143],v118 offset:3616
ds_read_b128 v[88:91],v11 offset:12288
v_fmac_f32 v125,v214,v214
v_add_f32 v121,v121,v215
v_fmac_f32 v125,v215,v215
v_add_f32 v122,v122,v236
v_smfmac_f32_16x16x64_bf16 v[216:219],v[92:95],v[128:135],v13
ds_read_b128 v[128:131],v114 offset:128
v_fmac_f32 v126,v236,v236
v_add_f32 v122,v122,v237
v_fmac_f32 v126,v237,v237
v_add_f32 v123,v123,v238
v_fmac_f32 v127,v238,v238
v_smfmac_f32_16x16x64_bf16 v[240:243],v[92:95],v[132:139],v13
s_waitcnt lgkmcnt(5)
ds_read_b128 v[136:139],v118 offset:128
ds_read_b128 v[132:135],v114 offset:3616
v_add_f32 v123,v123,v239
v_fmac_f32 v127,v239,v239
v_add_u32 v112,v112,v8
v_add_u32 v116,v116,v8
v_mfma_f32_16x16x32_bf16 v[220:223],v[96:99],v[152:155],0
ds_read_b128 v[92:95],v12 offset:11520
ds_read_b128 v[104:107],v14 offset:0
v_add_f32 v120,v120,v216
v_fmac_f32 v124,v216,v216
v_add_f32 v120,v120,v217
v_fmac_f32 v124,v217,v217
v_mfma_f32_16x16x32_bf16 v[244:247],v[96:99],v[156:159],0
s_waitcnt lgkmcnt(7)
ds_read_b128 v[108:111],v4 offset:3584
ds_read_b128 v[156:159],v119 offset:3616
v_add_f32 v121,v121,v218
v_fmac_f32 v125,v218,v218
v_add_f32 v121,v121,v219
v_fmac_f32 v125,v219,v219
v_smfmac_f32_16x16x64_bf16 v[220:223],v[100:103],v[144:151],v13
ds_read_b128 v[96:99],v11 offset:13440
ds_read_b128 v[144:147],v115 offset:128
v_add_f32 v122,v122,v240
v_fmac_f32 v126,v240,v240
v_add_f32 v122,v122,v241
v_fmac_f32 v126,v241,v241
v_smfmac_f32_16x16x64_bf16 v[244:247],v[100:103],v[148:155],v13
s_waitcnt lgkmcnt(7)
ds_read_b128 v[152:155],v119 offset:128
ds_read_b128 v[148:151],v115 offset:3616
v_add_f32 v123,v123,v242
v_fmac_f32 v127,v242,v242
v_add_f32 v123,v123,v243
v_fmac_f32 v127,v243,v243
v_mfma_f32_16x16x32_bf16 v[224:227],v[88:91],v[136:139],0
ds_read_b128 v[100:103],v12 offset:12672
v_add_f32 v120,v120,v220
v_fmac_f32 v124,v220,v220
v_add_f32 v120,v120,v221
v_fmac_f32 v124,v221,v221
v_mfma_f32_16x16x32_bf16 v[248:251],v[88:91],v[140:143],0
s_waitcnt lgkmcnt(8)
s_setprio 1
ds_read_b128 v[140:143],v116 offset:192
ds_read_b128 v[88:91],v112 offset:3616
v_add_f32 v121,v121,v222
v_fmac_f32 v125,v222,v222
v_add_f32 v121,v121,v223
v_fmac_f32 v125,v223,v223
v_smfmac_f32_16x16x64_bf16 v[224:227],v[92:95],v[128:135],v13
ds_read_b128 v[128:131],v112 offset:3488
v_add_f32 v122,v122,v244
v_fmac_f32 v126,v244,v244
v_add_f32 v122,v122,v245
v_fmac_f32 v126,v245,v245
v_smfmac_f32_16x16x64_bf16 v[248:251],v[92:95],v[132:139],v13
s_waitcnt lgkmcnt(5)
ds_read_b128 v[136:139],v116 offset:0
ds_read_b128 v[132:135],v112 offset:3680
v_add_f32 v123,v123,v246
v_fmac_f32 v127,v246,v246
v_add_f32 v123,v123,v247
v_fmac_f32 v127,v247,v247
v_mfma_f32_16x16x32_bf16 v[228:231],v[96:99],v[152:155],0
ds_read_b128 v[92:95],v112 offset:3808
v_add_f32 v120,v120,v224
v_fmac_f32 v124,v224,v224
v_add_f32 v120,v120,v225
v_fmac_f32 v124,v225,v225
v_mfma_f32_16x16x32_bf16 v[252:255],v[96:99],v[156:159],0
s_waitcnt lgkmcnt(6)
ds_read_b128 v[156:159],v116 offset:256
ds_read_b128 v[96:99],v116 offset:128
v_add_f32 v121,v121,v226
v_fmac_f32 v125,v226,v226
v_add_f32 v121,v121,v227
v_fmac_f32 v125,v227,v227
v_smfmac_f32_16x16x64_bf16 v[228:231],v[100:103],v[144:151],v13
ds_read_b128 v[144:147],v112 offset:3552
v_add_f32 v122,v122,v248
v_fmac_f32 v126,v248,v248
v_add_f32 v122,v122,v249
v_fmac_f32 v126,v249,v249
s_nop 0
s_mov_b32 s86,0xbe801d5a
v_smfmac_f32_16x16x64_bf16 v[252:255],v[100:103],v[148:155],v13
s_setprio 0
ds_read_b128 v[152:155],v116 offset:64
ds_read_b128 v[148:151],v112 offset:3744
v_add_f32 v123,v123,v250
v_fmac_f32 v127,v250,v250
v_add_f32 v123,v123,v251
v_fmac_f32 v127,v251,v251
ds_read_b128 v[100:103],v116 offset:320
v_add_f32 v120,v120,v228
v_fmac_f32 v124,v228,v228
v_add_f32 v120,v120,v229
v_fmac_f32 v124,v229,v229
v_add_f32 v121,v121,v230
v_fmac_f32 v125,v230,v230
v_add_f32 v121,v121,v231
v_fmac_f32 v125,v231,v231
s_waitcnt lgkmcnt(0)
s_waitcnt vmcnt(0)
s_barrier
s_setprio 0
v_add_f32 v122,v122,v252
v_fmac_f32 v126,v252,v252
v_add_f32 v122,v122,v253
v_fmac_f32 v126,v253,v253
v_add_f32 v123,v123,v254
s_cbranch_vccz RB40_%=
s_add_u32 m0,s58,0
s_mul_i32 s69,s59,9
s_add_u32 s69,s69,s97
buffer_load_dwordx4 v5,s[44:47],s69 offen lds
s_add_u32 m0,m0,1024
s_add_u32 s69,s69,s53
buffer_load_dwordx4 v6,s[44:47],s69 offen lds
RB40_%=:
v_fmac_f32 v127,v254,v254
v_add_f32 v123,v123,v255
v_fmac_f32 v127,v255,v255
s_setprio 3
ds_read_b128 v[56:59],v14 offset:256
ds_read_b128 v[60:63],v4 offset:3616
s_setprio 0
v_permlane32_swap_b32 v120,v122
v_permlane32_swap_b32 v121,v123
v_permlane32_swap_b32 v124,v126
v_permlane32_swap_b32 v125,v127
v_add_f32 v120,v120,v122
v_add_f32 v121,v121,v123
v_add_f32 v124,v124,v126
v_add_f32 v125,v125,v127
s_cbranch_vccz RB41_%=
s_add_u32 m0,m0,1024
s_add_u32 s69,s69,s53
buffer_load_dwordx4 v5,s[44:47],s69 offen lds
s_add_u32 m0,m0,s50
s_add_u32 s69,s69,s53
buffer_load_dwordx4 v6,s[44:47],s69 offen lds
RB41_%=:
v_permlane16_swap_b32 v120,v121
v_permlane16_swap_b32 v124,v125
v_add_f32 v113,v120,v121
v_add_f32 v114,v124,v125
v_mul_f32 v113,0x3c2aaaab,v113
v_mul_f32 v114,0x3c2aaaab,v114
v_mul_f32 v115,0x5e000000,v114
v_fma_f32 v114,-v113,v113,v114
v_pk_maximum3_f16 v7,v7,v115,v115 op_sel:[0,1,1] op_sel_hi:[1,1,1]
v_max_f32 v114,0,v114
v_add_f32 v114,0x3727c5ac,v114
v_rsq_f32 v114,v114
s_cbranch_vccz RB42_%=
s_add_u32 m0,m0,1024
s_add_u32 s69,s69,s53
buffer_load_dwordx4 v5,s[44:47],s69 offen lds
s_add_u32 m0,m0,1024
s_add_u32 s69,s69,s53
buffer_load_dwordx4 v6,s[44:47],s69 offen lds
RB42_%=:
v_mul_f32 v120,0x8befe000,v114
v_mul_f32 v121,0x8befe000,v114
v_mul_f32 v124,-v113,v120
v_mul_f32 v125,-v113,v120
v_permlane16_swap_b32 v120,v121
s_nop 0
v_permlane16_swap_b32 v124,v125
v_mov_b32 v122,v120
v_mov_b32 v123,v121
v_mov_b32 v126,v124
v_mov_b32 v127,v125
v_permlane32_swap_b32 v120,v122
v_permlane32_swap_b32 v124,v126
s_cbranch_vccz RB43_%=
s_add_u32 m0,m0,s50
s_add_u32 s69,s69,s53
buffer_load_dwordx4 v5,s[44:47],s69 offen lds
s_add_u32 m0,m0,1024
s_add_u32 s69,s69,s53
buffer_load_dwordx4 v6,s[44:47],s69 offen lds
RB43_%=:
v_permlane32_swap_b32 v121,v123
v_permlane32_swap_b32 v125,v127
v_fma_f32 v160,v160,v120,v124
s_setprio 3
ds_read_b64_tr_b16 v[64:65],v1 offset:12832
s_setprio 0
v_fma_f32 v161,v161,v120,v124
s_setprio 3
ds_read_b64_tr_b16 v[66:67],v1 offset:14880
s_setprio 0
v_fma_f32 v162,v162,v121,v125
s_setprio 3
ds_read_b128 v[112:115],v4 offset:1152
s_setprio 0
v_fma_f32 v163,v163,v121,v125
s_setprio 3
ds_read_b64_tr_b16 v[68:69],v2 offset:12832
s_setprio 0
v_fma_f32 v164,v164,v120,v124
s_setprio 3
ds_read_b64_tr_b16 v[70:71],v2 offset:14880
s_setprio 0
v_fma_f32 v165,v165,v120,v124
s_setprio 3
ds_read_b128 v[116:119],v4 offset:1184
s_setprio 0
v_fma_f32 v166,v166,v121,v125
s_setprio 3
ds_read_b64_tr_b16 v[72:73],v1 offset:16928
s_setprio 0
v_fma_f32 v167,v167,v121,v125
s_setprio 3
ds_read_b64_tr_b16 v[74:75],v1 offset:18976
s_setprio 0
v_fma_f32 v208,v208,v120,v124
s_setprio 3
ds_read_b64_tr_b16 v[76:77],v2 offset:16928
s_setprio 0
v_fma_f32 v209,v209,v120,v124
s_cbranch_vccz RB44_%=
s_add_u32 m0,m0,1024
s_add_u32 s69,s69,s53
buffer_load_dwordx4 v5,s[44:47],s69 offen lds
s_add_u32 m0,m0,s50
s_add_u32 s69,s69,s53
buffer_load_dwordx4 v6,s[44:47],s69 offen lds
RB44_%=:
s_setprio 3
ds_read_b64_tr_b16 v[78:79],v2 offset:18976
s_setprio 0
v_fma_f32 v210,v210,v121,v125
s_setprio 3
ds_read_b64_tr_b16 v[80:81],v1 offset:21024
s_setprio 0
v_fma_f32 v211,v211,v121,v125
s_setprio 3
ds_read_b64_tr_b16 v[82:83],v1 offset:23072
s_setprio 0
v_fma_f32 v212,v212,v120,v124
s_setprio 3
ds_read_b64_tr_b16 v[84:85],v2 offset:21024
s_setprio 0
v_fma_f32 v213,v213,v120,v124
s_setprio 3
ds_read_b64_tr_b16 v[86:87],v2 offset:23072
s_setprio 0
v_fma_f32 v214,v214,v121,v125
v_fma_f32 v215,v215,v121,v125
v_cvt_pk_bf16_f32 v16,v160,v161
v_cvt_pk_bf16_f32 v20,v162,v163
v_cvt_pk_bf16_f32 v17,v164,v165
v_cvt_pk_bf16_f32 v21,v166,v167
v_cvt_pk_bf16_f32 v18,v208,v209
v_cvt_pk_bf16_f32 v22,v210,v211
v_cvt_pk_bf16_f32 v19,v212,v213
v_cvt_pk_bf16_f32 v23,v214,v215
v_fma_f32 v168,v168,v120,v124
v_fma_f32 v169,v169,v120,v124
v_mfma_f32_16x16x32_bf16 v[160:163],v[104:107],v[128:131],v[108:111]
s_cbranch_vccz RB45_%=
s_add_u32 m0,m0,1024
s_add_u32 s69,s69,s53
buffer_load_dwordx4 v5,s[44:47],s69 offen lds
s_add_u32 m0,m0,1024
s_add_u32 s69,s69,s53
buffer_load_dwordx4 v6,s[44:47],s69 offen lds
RB45_%=:
v_fma_f32 v170,v170,v121,v125
v_fma_f32 v171,v171,v121,v125
v_fma_f32 v172,v172,v120,v124
v_fma_f32 v173,v173,v120,v124
v_fma_f32 v174,v174,v121,v125
v_fma_f32 v175,v175,v121,v125
v_mfma_f32_16x16x32_bf16 v[164:167],v[104:107],v[132:135],v[108:111]
v_fma_f32 v216,v216,v120,v124
v_fma_f32 v217,v217,v120,v124
v_fma_f32 v218,v218,v121,v125
v_fma_f32 v219,v219,v121,v125
v_fma_f32 v220,v220,v120,v124
v_mfma_f32_16x16x32_bf16 v[208:211],v[104:107],v[136:139],v[108:111]
v_fma_f32 v221,v221,v120,v124
v_fma_f32 v222,v222,v121,v125
v_fma_f32 v223,v223,v121,v125
v_cvt_pk_bf16_f32 v24,v168,v169
v_cvt_pk_bf16_f32 v28,v170,v171
s_setprio 3
v_mfma_f32_16x16x32_bf16 v[212:215],v[104:107],v[140:143],v[108:111]
ds_read_b128 v[104:107],v14 offset:512
s_setprio 0
v_cvt_pk_bf16_f32 v25,v172,v173
v_cvt_pk_bf16_f32 v29,v174,v175
v_cvt_pk_bf16_f32 v26,v216,v217
v_cvt_pk_bf16_f32 v30,v218,v219
v_cvt_pk_bf16_f32 v27,v220,v221
v_cvt_pk_bf16_f32 v31,v222,v223
v_fma_f32 v176,v176,v120,v124
v_fma_f32 v177,v177,v120,v124
s_waitcnt lgkmcnt(15)
s_setprio 3
v_mfma_f32_16x16x32_bf16 v[168:171],v[56:59],v[128:131],v[60:63]
ds_read_b128 v[108:111],v4 offset:3712
s_setprio 0
v_fma_f32 v178,v178,v121,v125
v_fma_f32 v179,v179,v121,v125
v_fma_f32 v180,v180,v120,v124
v_fma_f32 v181,v181,v120,v124
v_fma_f32 v182,v182,v121,v125
v_fma_f32 v183,v183,v121,v125
v_mfma_f32_16x16x32_bf16 v[172:175],v[56:59],v[132:135],v[60:63]
v_fma_f32 v224,v224,v120,v124
v_fma_f32 v225,v225,v120,v124
v_fma_f32 v226,v226,v121,v125
v_fma_f32 v227,v227,v121,v125
v_fma_f32 v228,v228,v120,v124
v_mfma_f32_16x16x32_bf16 v[216:219],v[56:59],v[136:139],v[60:63]
v_fma_f32 v229,v229,v120,v124
v_fma_f32 v230,v230,v121,v125
v_fma_f32 v231,v231,v121,v125
v_cvt_pk_bf16_f32 v32,v176,v177
v_cvt_pk_bf16_f32 v36,v178,v179
s_setprio 3
v_mfma_f32_16x16x32_bf16 v[220:223],v[56:59],v[140:143],v[60:63]
ds_read_b128 v[56:59],v14 offset:768
s_setprio 0
v_cvt_pk_bf16_f32 v33,v180,v181
v_cvt_pk_bf16_f32 v37,v182,v183
v_cvt_pk_bf16_f32 v34,v224,v225
v_cvt_pk_bf16_f32 v38,v226,v227
v_cvt_pk_bf16_f32 v35,v228,v229
v_cvt_pk_bf16_f32 v39,v230,v231
v_fma_f32 v184,v184,v122,v126
s_waitcnt lgkmcnt(1)
s_setprio 3
v_mfma_f32_16x16x32_bf16 v[176:179],v[104:107],v[144:147],v[108:111]
ds_read_b128 v[60:63],v4 offset:3744
s_setprio 0
v_fma_f32 v185,v185,v122,v126
v_fma_f32 v186,v186,v123,v127
v_fma_f32 v187,v187,v123,v127
v_mfma_f32_16x16x32_bf16 v[128:131],v[64:67],v[16:19],v[112:115]
v_fma_f32 v188,v188,v122,v126
v_fma_f32 v189,v189,v122,v126
v_fma_f32 v190,v190,v123,v127
v_mfma_f32_16x16x32_bf16 v[180:183],v[104:107],v[148:151],v[108:111]
v_fma_f32 v191,v191,v123,v127
v_fma_f32 v232,v232,v122,v126
v_fma_f32 v233,v233,v122,v126
v_mfma_f32_16x16x32_bf16 v[136:139],v[64:67],v[20:23],v[112:115]
v_fma_f32 v234,v234,v123,v127
v_fma_f32 v235,v235,v123,v127
v_fma_f32 v236,v236,v122,v126
v_mfma_f32_16x16x32_bf16 v[224:227],v[104:107],v[152:155],v[108:111]
v_fma_f32 v237,v237,v122,v126
v_fma_f32 v238,v238,v123,v127
v_fma_f32 v239,v239,v123,v127
v_mfma_f32_16x16x32_bf16 v[140:143],v[68:71],v[20:23],v[116:119]
v_cvt_pk_bf16_f32 v40,v184,v185
v_cvt_pk_bf16_f32 v44,v186,v187
v_cvt_pk_bf16_f32 v41,v188,v189
s_setprio 3
v_mfma_f32_16x16x32_bf16 v[228:231],v[104:107],v[156:159],v[108:111]
ds_read_b128 v[104:107],v14 offset:1024
s_setprio 0
v_cvt_pk_bf16_f32 v45,v190,v191
v_cvt_pk_bf16_f32 v42,v232,v233
v_cvt_pk_bf16_f32 v46,v234,v235
s_setprio 3
v_mfma_f32_16x16x32_bf16 v[132:135],v[68:71],v[16:19],v[116:119]
ds_read_b128 v[108:111],v4 offset:3840
s_setprio 0
v_cvt_pk_bf16_f32 v43,v236,v237
v_cvt_pk_bf16_f32 v47,v238,v239
v_fma_f32 v192,v192,v122,v126
s_waitcnt lgkmcnt(2)
v_mfma_f32_16x16x32_bf16 v[184:187],v[56:59],v[144:147],v[60:63]
v_fma_f32 v193,v193,v122,v126
v_fma_f32 v194,v194,v123,v127
v_mfma_f32_16x16x32_bf16 v[128:131],v[72:75],v[24:27],v[128:131]
v_fma_f32 v195,v195,v123,v127
v_fma_f32 v196,v196,v122,v126
v_mfma_f32_16x16x32_bf16 v[188:191],v[56:59],v[148:151],v[60:63]
v_fma_f32 v197,v197,v122,v126
v_fma_f32 v198,v198,v123,v127
v_mfma_f32_16x16x32_bf16 v[136:139],v[72:75],v[28:31],v[136:139]
v_fma_f32 v199,v199,v123,v127
v_fma_f32 v240,v240,v122,v126
v_mfma_f32_16x16x32_bf16 v[232:235],v[56:59],v[152:155],v[60:63]
v_fma_f32 v241,v241,v122,v126
v_fma_f32 v242,v242,v123,v127
v_mfma_f32_16x16x32_bf16 v[140:143],v[76:79],v[28:31],v[140:143]
v_fma_f32 v243,v243,v123,v127
v_fma_f32 v244,v244,v122,v126
s_setprio 3
v_mfma_f32_16x16x32_bf16 v[236:239],v[56:59],v[156:159],v[60:63]
ds_read_b128 v[144:147],v14 offset:1280
s_setprio 0
v_fma_f32 v245,v245,v122,v126
v_fma_f32 v246,v246,v123,v127
s_setprio 3
v_mfma_f32_16x16x32_bf16 v[132:135],v[76:79],v[24:27],v[132:135]
ds_read_b128 v[148:151],v4 offset:3872
s_setprio 0
v_fma_f32 v247,v247,v123,v127
v_cvt_pk_bf16_f32 v48,v192,v193
v_cvt_pk_bf16_f32 v52,v194,v195
v_cvt_pk_bf16_f32 v49,v196,v197
v_cvt_pk_bf16_f32 v53,v198,v199
v_cvt_pk_bf16_f32 v50,v240,v241
v_cvt_pk_bf16_f32 v54,v242,v243
v_cvt_pk_bf16_f32 v51,v244,v245
v_cvt_pk_bf16_f32 v55,v246,v247
v_fma_f32 v200,v200,v122,v126
s_waitcnt lgkmcnt(2)
v_mfma_f32_16x16x32_bf16 v[192:195],v[104:107],v[88:91],v[108:111]
v_fma_f32 v201,v201,v122,v126
v_fma_f32 v202,v202,v123,v127
v_fma_f32 v203,v203,v123,v127
v_mfma_f32_16x16x32_bf16 v[128:131],v[80:83],v[32:35],v[128:131]
v_fma_f32 v204,v204,v122,v126
v_fma_f32 v205,v205,v122,v126
v_fma_f32 v206,v206,v123,v127
v_mfma_f32_16x16x32_bf16 v[196:199],v[104:107],v[92:95],v[108:111]
v_fma_f32 v207,v207,v123,v127
v_fma_f32 v248,v248,v122,v126
v_fma_f32 v249,v249,v122,v126
v_mfma_f32_16x16x32_bf16 v[136:139],v[80:83],v[36:39],v[136:139]
v_fma_f32 v250,v250,v123,v127
v_fma_f32 v251,v251,v123,v127
v_fma_f32 v252,v252,v122,v126
v_mfma_f32_16x16x32_bf16 v[240:243],v[104:107],v[96:99],v[108:111]
v_fma_f32 v253,v253,v122,v126
v_fma_f32 v254,v254,v123,v127
v_fma_f32 v255,v255,v123,v127
v_mfma_f32_16x16x32_bf16 v[140:143],v[84:87],v[36:39],v[140:143]
v_cvt_pk_bf16_f32 v56,v200,v201
v_cvt_pk_bf16_f32 v60,v202,v203
v_cvt_pk_bf16_f32 v57,v204,v205
v_mfma_f32_16x16x32_bf16 v[244:247],v[104:107],v[100:103],v[108:111]
v_cvt_pk_bf16_f32 v61,v206,v207
v_cvt_pk_bf16_f32 v58,v248,v249
v_cvt_pk_bf16_f32 v62,v250,v251
v_mfma_f32_16x16x32_bf16 v[132:135],v[84:87],v[32:35],v[132:135]
v_cvt_pk_bf16_f32 v59,v252,v253
v_cvt_pk_bf16_f32 v63,v254,v255
s_waitcnt lgkmcnt(0)
v_mfma_f32_16x16x32_bf16 v[200:203],v[144:147],v[88:91],v[148:151]
s_waitcnt vmcnt(0)
s_barrier
s_setprio 3
ds_read_b64_tr_b16 v[104:105],v3 offset:128
v_lshlrev_b32_sdwa v128,1,v128 dst_sel:DWORD dst_unused:UNUSED_PAD src0_sel:DWORD src1_sel:WORD_1
v_lshlrev_b32_sdwa v130,1,v130 dst_sel:DWORD dst_unused:UNUSED_PAD src0_sel:DWORD src1_sel:WORD_1
v_lshlrev_b32_sdwa v129,1,v129 dst_sel:DWORD dst_unused:UNUSED_PAD src0_sel:DWORD src1_sel:WORD_1
v_mfma_f32_16x16x32_bf16 v[204:207],v[144:147],v[92:95],v[148:151]
ds_read_u16 v128,v128
ds_read_b64_tr_b16 v[106:107],v3 offset:1664
v_lshlrev_b32_sdwa v131,1,v131 dst_sel:DWORD dst_unused:UNUSED_PAD src0_sel:DWORD src1_sel:WORD_1
v_fma_mixlo_f16 v120,v140,s99,0
v_fma_mixlo_f16 v122,v141,s99,0
v_mfma_f32_16x16x32_bf16 v[248:251],v[144:147],v[96:99],v[148:151]
ds_read_u16_d16_hi v130,v130
ds_read_b64_tr_b16 v[108:109],v3 offset:136
v_lshlrev_b32_sdwa v132,1,v132 dst_sel:DWORD dst_unused:UNUSED_PAD src0_sel:DWORD src1_sel:WORD_1
v_lshlrev_b32_sdwa v134,1,v134 dst_sel:DWORD dst_unused:UNUSED_PAD src0_sel:DWORD src1_sel:WORD_1
v_mfma_f32_16x16x32_bf16 v[252:255],v[144:147],v[100:103],v[148:151]
ds_read_u16 v129,v129
ds_read_b64_tr_b16 v[110:111],v3 offset:1672
v_fma_mixhi_f16 v120,v142,s99,0
v_fma_mixhi_f16 v122,v143,s99,0
v_lshlrev_b32_sdwa v133,1,v133 dst_sel:DWORD dst_unused:UNUSED_PAD src0_sel:DWORD src1_sel:WORD_1
v_lshlrev_b32_sdwa v135,1,v135 dst_sel:DWORD dst_unused:UNUSED_PAD src0_sel:DWORD src1_sel:WORD_1
v_mfma_f32_16x16x32_bf16 v[144:147],v[64:67],v[40:43],v[112:115]
ds_read_u16_d16_hi v131,v131
ds_read_b64_tr_b16 v[88:89],v3 offset:0
v_pk_fma_f16 v121,v120,v120,s54
v_pk_fma_f16 v123,v122,v122,s54
v_pk_mul_f16 v121,v120,v121
v_mfma_f32_16x16x32_bf16 v[152:155],v[64:67],v[44:47],v[112:115]
ds_read_u16 v132,v132
ds_read_b64_tr_b16 v[90:91],v3 offset:1536
v_pk_mul_f16 v123,v122,v123
v_lshlrev_b32_sdwa v136,1,v136 dst_sel:DWORD dst_unused:UNUSED_PAD src0_sel:DWORD src1_sel:WORD_1
v_mfma_f32_16x16x32_bf16 v[156:159],v[68:71],v[44:47],v[116:119]
ds_read_u16_d16_hi v134,v134
ds_read_b64_tr_b16 v[92:93],v3 offset:8
v_lshlrev_b32_sdwa v138,1,v138 dst_sel:DWORD dst_unused:UNUSED_PAD src0_sel:DWORD src1_sel:WORD_1
v_exp_f16 v120,v121
v_mfma_f32_16x16x32_bf16 v[148:151],v[68:71],v[40:43],v[116:119]
ds_read_u16 v133,v133
ds_read_b64_tr_b16 v[94:95],v3 offset:1544
v_exp_f16 v122,v123
v_lshlrev_b32_sdwa v137,1,v137 dst_sel:DWORD dst_unused:UNUSED_PAD src0_sel:DWORD src1_sel:WORD_1
v_mfma_f32_16x16x32_bf16 v[144:147],v[72:75],v[48:51],v[144:147]
ds_read_u16_d16_hi v135,v135
ds_read_b64_tr_b16 v[96:97],v3 offset:64
v_lshlrev_b32_sdwa v139,1,v139 dst_sel:DWORD dst_unused:UNUSED_PAD src0_sel:DWORD src1_sel:WORD_1
v_exp_f16_sdwa v120,v121 dst_sel:WORD_1 dst_unused:UNUSED_PRESERVE src0_sel:WORD_1
v_mfma_f32_16x16x32_bf16 v[152:155],v[72:75],v[52:55],v[152:155]
ds_read_u16 v136,v136
ds_read_b64_tr_b16 v[98:99],v3 offset:1600
v_exp_f16_sdwa v122,v123 dst_sel:WORD_1 dst_unused:UNUSED_PRESERVE src0_sel:WORD_1
v_mfma_f32_16x16x32_bf16 v[156:159],v[76:79],v[52:55],v[156:159]
ds_read_u16_d16_hi v138,v138
ds_read_b64_tr_b16 v[100:101],v3 offset:72
v_pk_fma_f16 v120,v120,s55,s55
v_pk_fma_f16 v122,v122,s55,s55
v_rcp_f16 v121,v120
v_mfma_f32_16x16x32_bf16 v[148:151],v[76:79],v[48:51],v[148:151]
ds_read_u16 v137,v137
ds_read_b64_tr_b16 v[102:103],v3 offset:1608
v_rcp_f16 v123,v122
v_mfma_f32_16x16x32_bf16 v[144:147],v[80:83],v[56:59],v[144:147]
ds_read_u16_d16_hi v139,v139
ds_read_b64_tr_b16 v[64:65],v1 offset:0
v_rcp_f16_sdwa v121,v120 dst_sel:WORD_1 dst_unused:UNUSED_PRESERVE src0_sel:WORD_1
v_mfma_f32_16x16x32_bf16 v[152:155],v[80:83],v[60:63],v[152:155]
ds_read_b64_tr_b16 v[66:67],v1 offset:2048
v_rcp_f16_sdwa v123,v122 dst_sel:WORD_1 dst_unused:UNUSED_PRESERVE src0_sel:WORD_1
v_fma_mix_f32 v140,v140,v121,0 op_sel_hi:[0,1,0]
v_mfma_f32_16x16x32_bf16 v[156:159],v[84:87],v[60:63],v[156:159]
ds_read_b128 v[112:115],v4 offset:1280
v_fma_mix_f32 v142,v142,v121,0 op_sel:[0,1,0] op_sel_hi:[0,1,0]
s_waitcnt lgkmcnt(15)
v_lshl_add_u64 v[128:129],v[128:129],0,v[130:131]
s_waitcnt lgkmcnt(11)
v_lshl_add_u64 v[130:131],v[132:133],0,v[134:135]
v_mfma_f32_16x16x32_bf16 v[148:151],v[84:87],v[56:59],v[148:151]
ds_read_b64_tr_b16 v[68:69],v2 offset:0
v_fma_mix_f32 v141,v141,v123,0 op_sel_hi:[0,1,0]
v_fma_mix_f32 v143,v143,v123,0 op_sel:[0,1,0] op_sel_hi:[0,1,0]
v_lshlrev_b32_sdwa v144,1,v144 dst_sel:DWORD dst_unused:UNUSED_PAD src0_sel:DWORD src1_sel:WORD_1
v_lshlrev_b32_sdwa v146,1,v146 dst_sel:DWORD dst_unused:UNUSED_PAD src0_sel:DWORD src1_sel:WORD_1
v_mfma_f32_16x16x32_bf16 v[160:163],v[88:91],v[128:131],v[160:163]
ds_read_u16 v144,v144
v_lshlrev_b32_sdwa v145,1,v145 dst_sel:DWORD dst_unused:UNUSED_PAD src0_sel:DWORD src1_sel:WORD_1
v_lshlrev_b32_sdwa v147,1,v147 dst_sel:DWORD dst_unused:UNUSED_PAD src0_sel:DWORD src1_sel:WORD_1
v_mfma_f32_16x16x32_bf16 v[168:171],v[92:95],v[128:131],v[168:171]
s_waitcnt lgkmcnt(10)
ds_read_b64_tr_b16 v[70:71],v2 offset:2048
ds_read_b128 v[116:119],v4 offset:1312
v_fma_mixlo_f16 v120,v156,s99,0
v_fma_mixlo_f16 v122,v157,s99,0
v_mfma_f32_16x16x32_bf16 v[176:179],v[96:99],v[128:131],v[176:179]
s_waitcnt lgkmcnt(8)
ds_read_u16_d16_hi v146,v146
v_lshlrev_b32_sdwa v148,1,v148 dst_sel:DWORD dst_unused:UNUSED_PAD src0_sel:DWORD src1_sel:WORD_1
v_lshlrev_b32_sdwa v150,1,v150 dst_sel:DWORD dst_unused:UNUSED_PAD src0_sel:DWORD src1_sel:WORD_1
v_mfma_f32_16x16x32_bf16 v[184:187],v[100:103],v[128:131],v[184:187]
s_waitcnt lgkmcnt(8)
ds_read_b64_tr_b16 v[72:73],v1 offset:4096
ds_read_b64_tr_b16 v[74:75],v1 offset:6144
v_lshl_add_u64 v[136:137],v[136:137],0,v[138:139]
v_mfma_f32_16x16x32_bf16 v[192:195],v[104:107],v[128:131],v[192:195]
ds_read_u16 v145,v145
v_cvt_pk_bf16_f32 v138,v140,v142
v_cvt_pk_bf16_f32 v139,v141,v143
v_mfma_f32_16x16x32_bf16 v[200:203],v[108:111],v[128:131],v[200:203]
ds_read_b64_tr_b16 v[76:77],v2 offset:4096
ds_read_b64_tr_b16 v[78:79],v2 offset:6144
v_fma_mixhi_f16 v120,v158,s99,0
v_fma_mixhi_f16 v122,v159,s99,0
v_mfma_f32_16x16x32_bf16 v[164:167],v[88:91],v[136:139],v[164:167]
ds_read_u16_d16_hi v147,v147
v_lshlrev_b32_sdwa v149,1,v149 dst_sel:DWORD dst_unused:UNUSED_PAD src0_sel:DWORD src1_sel:WORD_1
v_lshlrev_b32_sdwa v151,1,v151 dst_sel:DWORD dst_unused:UNUSED_PAD src0_sel:DWORD src1_sel:WORD_1
v_mfma_f32_16x16x32_bf16 v[172:175],v[92:95],v[136:139],v[172:175]
ds_read_b64_tr_b16 v[80:81],v1 offset:8192
ds_read_b64_tr_b16 v[82:83],v1 offset:10240
v_pk_fma_f16 v121,v120,v120,s54
v_pk_fma_f16 v123,v122,v122,s54
v_mfma_f32_16x16x32_bf16 v[180:183],v[96:99],v[136:139],v[180:183]
ds_read_u16 v148,v148
v_pk_mul_f16 v121,v120,v121
v_pk_mul_f16 v123,v122,v123
v_mfma_f32_16x16x32_bf16 v[188:191],v[100:103],v[136:139],v[188:191]
ds_read_b64_tr_b16 v[84:85],v2 offset:8192
v_lshlrev_b32_sdwa v152,1,v152 dst_sel:DWORD dst_unused:UNUSED_PAD src0_sel:DWORD src1_sel:WORD_1
v_lshlrev_b32_sdwa v154,1,v154 dst_sel:DWORD dst_unused:UNUSED_PAD src0_sel:DWORD src1_sel:WORD_1
v_mfma_f32_16x16x32_bf16 v[196:199],v[104:107],v[136:139],v[196:199]
ds_read_u16_d16_hi v150,v150
v_exp_f16 v120,v121
v_mfma_f32_16x16x32_bf16 v[204:207],v[108:111],v[136:139],v[204:207]
s_waitcnt lgkmcnt(15)
ds_read_b64_tr_b16 v[86:87],v2 offset:10240
v_exp_f16 v122,v123
v_mfma_f32_16x16x32_bf16 v[128:131],v[64:67],v[16:19],v[112:115]
ds_read_u16 v149,v149
v_lshlrev_b32_sdwa v153,1,v153 dst_sel:DWORD dst_unused:UNUSED_PAD src0_sel:DWORD src1_sel:WORD_1
v_lshlrev_b32_sdwa v155,1,v155 dst_sel:DWORD dst_unused:UNUSED_PAD src0_sel:DWORD src1_sel:WORD_1
v_mfma_f32_16x16x32_bf16 v[136:139],v[64:67],v[20:23],v[112:115]
s_waitcnt lgkmcnt(14)
v_exp_f16_sdwa v120,v121 dst_sel:WORD_1 dst_unused:UNUSED_PRESERVE src0_sel:WORD_1
v_mfma_f32_16x16x32_bf16 v[140:143],v[68:71],v[20:23],v[116:119]
ds_read_u16_d16_hi v151,v151
v_exp_f16_sdwa v122,v123 dst_sel:WORD_1 dst_unused:UNUSED_PRESERVE src0_sel:WORD_1
v_mfma_f32_16x16x32_bf16 v[132:135],v[68:71],v[16:19],v[116:119]
s_waitcnt lgkmcnt(12)
v_pk_fma_f16 v120,v120,s55,s55
v_pk_fma_f16 v122,v122,s55,s55
v_mfma_f32_16x16x32_bf16 v[128:131],v[72:75],v[24:27],v[128:131]
ds_read_u16 v152,v152
v_rcp_f16 v121,v120
v_mfma_f32_16x16x32_bf16 v[136:139],v[72:75],v[28:31],v[136:139]
s_waitcnt lgkmcnt(10)
v_rcp_f16 v123,v122
v_mfma_f32_16x16x32_bf16 v[140:143],v[76:79],v[28:31],v[140:143]
ds_read_u16_d16_hi v154,v154
v_rcp_f16_sdwa v121,v120 dst_sel:WORD_1 dst_unused:UNUSED_PRESERVE src0_sel:WORD_1
v_mfma_f32_16x16x32_bf16 v[132:135],v[76:79],v[24:27],v[132:135]
s_waitcnt lgkmcnt(8)
v_rcp_f16_sdwa v123,v122 dst_sel:WORD_1 dst_unused:UNUSED_PRESERVE src0_sel:WORD_1
v_mfma_f32_16x16x32_bf16 v[128:131],v[80:83],v[32:35],v[128:131]
ds_read_u16 v153,v153
v_fma_mix_f32 v156,v156,v121,0 op_sel_hi:[0,1,0]
v_fma_mix_f32 v158,v158,v121,0 op_sel:[0,1,0] op_sel_hi:[0,1,0]
v_mfma_f32_16x16x32_bf16 v[136:139],v[80:83],v[36:39],v[136:139]
s_waitcnt lgkmcnt(3)
v_lshl_add_u64 v[144:145],v[144:145],0,v[146:147]
v_lshl_add_u64 v[146:147],v[148:149],0,v[150:151]
v_mfma_f32_16x16x32_bf16 v[140:143],v[84:87],v[36:39],v[140:143]
ds_read_u16_d16_hi v155,v155
v_fma_mix_f32 v157,v157,v123,0 op_sel_hi:[0,1,0]
v_fma_mix_f32 v159,v159,v123,0 op_sel:[0,1,0] op_sel_hi:[0,1,0]
v_mfma_f32_16x16x32_bf16 v[132:135],v[84:87],v[32:35],v[132:135]
v_lshlrev_b32_sdwa v128,1,v128 dst_sel:DWORD dst_unused:UNUSED_PAD src0_sel:DWORD src1_sel:WORD_1
v_lshlrev_b32_sdwa v130,1,v130 dst_sel:DWORD dst_unused:UNUSED_PAD src0_sel:DWORD src1_sel:WORD_1
v_mfma_f32_16x16x32_bf16 v[208:211],v[88:91],v[144:147],v[208:211]
ds_read_u16 v128,v128
v_lshlrev_b32_sdwa v129,1,v129 dst_sel:DWORD dst_unused:UNUSED_PAD src0_sel:DWORD src1_sel:WORD_1
v_lshlrev_b32_sdwa v131,1,v131 dst_sel:DWORD dst_unused:UNUSED_PAD src0_sel:DWORD src1_sel:WORD_1
v_mfma_f32_16x16x32_bf16 v[216:219],v[92:95],v[144:147],v[216:219]
v_fma_mixlo_f16 v120,v140,s99,0
v_fma_mixlo_f16 v122,v141,s99,0
v_mfma_f32_16x16x32_bf16 v[224:227],v[96:99],v[144:147],v[224:227]
ds_read_u16_d16_hi v130,v130
v_lshlrev_b32_sdwa v132,1,v132 dst_sel:DWORD dst_unused:UNUSED_PAD src0_sel:DWORD src1_sel:WORD_1
v_lshlrev_b32_sdwa v134,1,v134 dst_sel:DWORD dst_unused:UNUSED_PAD src0_sel:DWORD src1_sel:WORD_1
v_mfma_f32_16x16x32_bf16 v[232:235],v[100:103],v[144:147],v[232:235]
s_waitcnt lgkmcnt(2)
v_lshl_add_u64 v[152:153],v[152:153],0,v[154:155]
v_mfma_f32_16x16x32_bf16 v[240:243],v[104:107],v[144:147],v[240:243]
ds_read_u16 v129,v129
v_cvt_pk_bf16_f32 v154,v156,v158
v_cvt_pk_bf16_f32 v155,v157,v159
v_mfma_f32_16x16x32_bf16 v[248:251],v[108:111],v[144:147],v[248:251]
v_fma_mixhi_f16 v120,v142,s99,0
v_fma_mixhi_f16 v122,v143,s99,0
v_mfma_f32_16x16x32_bf16 v[212:215],v[88:91],v[152:155],v[212:215]
ds_read_u16_d16_hi v131,v131
v_lshlrev_b32_sdwa v133,1,v133 dst_sel:DWORD dst_unused:UNUSED_PAD src0_sel:DWORD src1_sel:WORD_1
v_lshlrev_b32_sdwa v135,1,v135 dst_sel:DWORD dst_unused:UNUSED_PAD src0_sel:DWORD src1_sel:WORD_1
v_mfma_f32_16x16x32_bf16 v[220:223],v[92:95],v[152:155],v[220:223]
s_setprio 2
ds_read_b64_tr_b16 v[88:89],v3 offset:6160
v_pk_fma_f16 v121,v120,v120,s54
v_pk_fma_f16 v123,v122,v122,s54
v_mfma_f32_16x16x32_bf16 v[228:231],v[96:99],v[152:155],v[228:231]
ds_read_u16 v132,v132
v_pk_mul_f16 v121,v120,v121
v_pk_mul_f16 v123,v122,v123
v_mfma_f32_16x16x32_bf16 v[236:239],v[100:103],v[152:155],v[236:239]
ds_read_b64_tr_b16 v[90:91],v3 offset:7696
v_lshlrev_b32_sdwa v136,1,v136 dst_sel:DWORD dst_unused:UNUSED_PAD src0_sel:DWORD src1_sel:WORD_1
v_lshlrev_b32_sdwa v138,1,v138 dst_sel:DWORD dst_unused:UNUSED_PAD src0_sel:DWORD src1_sel:WORD_1
v_mfma_f32_16x16x32_bf16 v[244:247],v[104:107],v[152:155],v[244:247]
ds_read_u16_d16_hi v134,v134
v_exp_f16 v120,v121
v_mfma_f32_16x16x32_bf16 v[252:255],v[108:111],v[152:155],v[252:255]
ds_read_b64_tr_b16 v[92:93],v3 offset:6168
v_exp_f16 v122,v123
v_mfma_f32_16x16x32_bf16 v[144:147],v[64:67],v[40:43],v[112:115]
ds_read_u16 v133,v133
v_lshlrev_b32_sdwa v137,1,v137 dst_sel:DWORD dst_unused:UNUSED_PAD src0_sel:DWORD src1_sel:WORD_1
s_mov_b32 s86,0xbe801d5a
v_lshlrev_b32_sdwa v139,1,v139 dst_sel:DWORD dst_unused:UNUSED_PAD src0_sel:DWORD src1_sel:WORD_1
v_mfma_f32_16x16x32_bf16 v[152:155],v[64:67],v[44:47],v[112:115]
ds_read_b64_tr_b16 v[94:95],v3 offset:7704
v_exp_f16_sdwa v120,v121 dst_sel:WORD_1 dst_unused:UNUSED_PRESERVE src0_sel:WORD_1
v_mfma_f32_16x16x32_bf16 v[156:159],v[68:71],v[44:47],v[116:119]
ds_read_u16_d16_hi v135,v135
v_exp_f16_sdwa v122,v123 dst_sel:WORD_1 dst_unused:UNUSED_PRESERVE src0_sel:WORD_1
v_mfma_f32_16x16x32_bf16 v[148:151],v[68:71],v[40:43],v[116:119]
ds_read_b64_tr_b16 v[96:97],v3 offset:6224
v_pk_fma_f16 v120,v120,s55,s55
v_pk_fma_f16 v122,v122,s55,s55
v_mfma_f32_16x16x32_bf16 v[144:147],v[72:75],v[48:51],v[144:147]
ds_read_u16 v136,v136
v_rcp_f16 v121,v120
v_mfma_f32_16x16x32_bf16 v[152:155],v[72:75],v[52:55],v[152:155]
ds_read_b64_tr_b16 v[98:99],v3 offset:7760
ds_read_b64_tr_b16 v[100:101],v3 offset:6232
v_rcp_f16 v123,v122
v_mfma_f32_16x16x32_bf16 v[156:159],v[76:79],v[52:55],v[156:159]
ds_read_u16_d16_hi v138,v138
v_rcp_f16_sdwa v121,v120 dst_sel:WORD_1 dst_unused:UNUSED_PRESERVE src0_sel:WORD_1
v_mfma_f32_16x16x32_bf16 v[148:151],v[76:79],v[48:51],v[148:151]
ds_read_b64_tr_b16 v[102:103],v3 offset:7768
ds_read_b64_tr_b16 v[104:105],v3 offset:6288
v_rcp_f16_sdwa v123,v122 dst_sel:WORD_1 dst_unused:UNUSED_PRESERVE src0_sel:WORD_1
v_mfma_f32_16x16x32_bf16 v[144:147],v[80:83],v[56:59],v[144:147]
ds_read_u16 v137,v137
v_fma_mix_f32 v140,v140,v121,0 op_sel_hi:[0,1,0]
v_fma_mix_f32 v142,v142,v121,0 op_sel:[0,1,0] op_sel_hi:[0,1,0]
v_mfma_f32_16x16x32_bf16 v[152:155],v[80:83],v[60:63],v[152:155]
s_waitcnt lgkmcnt(8)
ds_read_b64_tr_b16 v[106:107],v3 offset:7824
ds_read_b64_tr_b16 v[108:109],v3 offset:6296
v_lshl_add_u64 v[128:129],v[128:129],0,v[130:131]
v_lshl_add_u64 v[130:131],v[132:133],0,v[134:135]
v_mfma_f32_16x16x32_bf16 v[156:159],v[84:87],v[60:63],v[156:159]
ds_read_u16_d16_hi v139,v139
v_fma_mix_f32 v141,v141,v123,0 op_sel_hi:[0,1,0]
v_fma_mix_f32 v143,v143,v123,0 op_sel:[0,1,0] op_sel_hi:[0,1,0]
v_mfma_f32_16x16x32_bf16 v[148:151],v[84:87],v[56:59],v[148:151]
ds_read_b64_tr_b16 v[110:111],v3 offset:7832
ds_read_b64_tr_b16 v[64:65],v1 offset:512
v_lshlrev_b32_sdwa v144,1,v144 dst_sel:DWORD dst_unused:UNUSED_PAD src0_sel:DWORD src1_sel:WORD_1
v_lshlrev_b32_sdwa v146,1,v146 dst_sel:DWORD dst_unused:UNUSED_PAD src0_sel:DWORD src1_sel:WORD_1
v_mfma_f32_16x16x32_bf16 v[160:163],v[88:91],v[128:131],v[160:163]
ds_read_u16 v144,v144
v_lshlrev_b32_sdwa v145,1,v145 dst_sel:DWORD dst_unused:UNUSED_PAD src0_sel:DWORD src1_sel:WORD_1
v_lshlrev_b32_sdwa v147,1,v147 dst_sel:DWORD dst_unused:UNUSED_PAD src0_sel:DWORD src1_sel:WORD_1
v_mfma_f32_16x16x32_bf16 v[168:171],v[92:95],v[128:131],v[168:171]
s_waitcnt lgkmcnt(11)
ds_read_b64_tr_b16 v[66:67],v1 offset:2560
ds_read_b128 v[112:115],v4 offset:1408
v_fma_mixlo_f16 v120,v156,s99,0
v_fma_mixlo_f16 v122,v157,s99,0
v_mfma_f32_16x16x32_bf16 v[176:179],v[96:99],v[128:131],v[176:179]
s_waitcnt lgkmcnt(10)
ds_read_u16_d16_hi v146,v146
v_lshlrev_b32_sdwa v148,1,v148 dst_sel:DWORD dst_unused:UNUSED_PAD src0_sel:DWORD src1_sel:WORD_1
v_lshlrev_b32_sdwa v150,1,v150 dst_sel:DWORD dst_unused:UNUSED_PAD src0_sel:DWORD src1_sel:WORD_1
v_mfma_f32_16x16x32_bf16 v[184:187],v[100:103],v[128:131],v[184:187]
s_waitcnt lgkmcnt(6)
ds_read_b64_tr_b16 v[68:69],v2 offset:512
ds_read_b64_tr_b16 v[70:71],v2 offset:2560
v_lshl_add_u64 v[136:137],v[136:137],0,v[138:139]
v_mfma_f32_16x16x32_bf16 v[192:195],v[104:107],v[128:131],v[192:195]
s_waitcnt lgkmcnt(7)
ds_read_u16 v145,v145
v_cvt_pk_bf16_f32 v138,v140,v142
v_cvt_pk_bf16_f32 v139,v141,v143
v_mfma_f32_16x16x32_bf16 v[200:203],v[108:111],v[128:131],v[200:203]
ds_read_b128 v[116:119],v4 offset:1440
ds_read_b64_tr_b16 v[72:73],v1 offset:4608
v_fma_mixhi_f16 v120,v158,s99,0
v_fma_mixhi_f16 v122,v159,s99,0
v_mfma_f32_16x16x32_bf16 v[164:167],v[88:91],v[136:139],v[164:167]
ds_read_u16_d16_hi v147,v147
v_lshlrev_b32_sdwa v149,1,v149 dst_sel:DWORD dst_unused:UNUSED_PAD src0_sel:DWORD src1_sel:WORD_1
v_lshlrev_b32_sdwa v151,1,v151 dst_sel:DWORD dst_unused:UNUSED_PAD src0_sel:DWORD src1_sel:WORD_1
v_mfma_f32_16x16x32_bf16 v[172:175],v[92:95],v[136:139],v[172:175]
ds_read_b64_tr_b16 v[74:75],v1 offset:6656
ds_read_b64_tr_b16 v[76:77],v2 offset:4608
v_pk_fma_f16 v121,v120,v120,s54
v_pk_fma_f16 v123,v122,v122,s54
v_mfma_f32_16x16x32_bf16 v[180:183],v[96:99],v[136:139],v[180:183]
s_setprio 1
ds_read_u16 v148,v148
v_pk_mul_f16 v121,v120,v121
v_pk_mul_f16 v123,v122,v123
v_mfma_f32_16x16x32_bf16 v[188:191],v[100:103],v[136:139],v[188:191]
ds_read_b64_tr_b16 v[78:79],v2 offset:6656
v_lshlrev_b32_sdwa v152,1,v152 dst_sel:DWORD dst_unused:UNUSED_PAD src0_sel:DWORD src1_sel:WORD_1
v_lshlrev_b32_sdwa v154,1,v154 dst_sel:DWORD dst_unused:UNUSED_PAD src0_sel:DWORD src1_sel:WORD_1
v_mfma_f32_16x16x32_bf16 v[196:199],v[104:107],v[136:139],v[196:199]
ds_read_u16_d16_hi v150,v150
v_exp_f16 v120,v121
v_mfma_f32_16x16x32_bf16 v[204:207],v[108:111],v[136:139],v[204:207]
s_waitcnt lgkmcnt(12)
ds_read_b64_tr_b16 v[80:81],v1 offset:8704
v_exp_f16 v122,v123
v_mfma_f32_16x16x32_bf16 v[128:131],v[64:67],v[16:19],v[112:115]
ds_read_u16 v149,v149
v_lshlrev_b32_sdwa v153,1,v153 dst_sel:DWORD dst_unused:UNUSED_PAD src0_sel:DWORD src1_sel:WORD_1
v_lshlrev_b32_sdwa v155,1,v155 dst_sel:DWORD dst_unused:UNUSED_PAD src0_sel:DWORD src1_sel:WORD_1
v_mfma_f32_16x16x32_bf16 v[136:139],v[64:67],v[20:23],v[112:115]
s_waitcnt lgkmcnt(9)
ds_read_b64_tr_b16 v[82:83],v1 offset:10752
v_exp_f16_sdwa v120,v121 dst_sel:WORD_1 dst_unused:UNUSED_PRESERVE src0_sel:WORD_1
v_mfma_f32_16x16x32_bf16 v[140:143],v[68:71],v[20:23],v[116:119]
ds_read_u16_d16_hi v151,v151
v_exp_f16_sdwa v122,v123 dst_sel:WORD_1 dst_unused:UNUSED_PRESERVE src0_sel:WORD_1
v_mfma_f32_16x16x32_bf16 v[132:135],v[68:71],v[16:19],v[116:119]
s_waitcnt lgkmcnt(8)
ds_read_b64_tr_b16 v[84:85],v2 offset:8704
v_pk_fma_f16 v120,v120,s55,s55
v_pk_fma_f16 v122,v122,s55,s55
v_mfma_f32_16x16x32_bf16 v[128:131],v[72:75],v[24:27],v[128:131]
ds_read_u16 v152,v152
v_rcp_f16 v121,v120
v_mfma_f32_16x16x32_bf16 v[136:139],v[72:75],v[28:31],v[136:139]
s_waitcnt lgkmcnt(7)
ds_read_b64_tr_b16 v[86:87],v2 offset:10752
v_rcp_f16 v123,v122
v_mfma_f32_16x16x32_bf16 v[140:143],v[76:79],v[28:31],v[140:143]
ds_read_u16_d16_hi v154,v154
v_rcp_f16_sdwa v121,v120 dst_sel:WORD_1 dst_unused:UNUSED_PRESERVE src0_sel:WORD_1
v_mfma_f32_16x16x32_bf16 v[132:135],v[76:79],v[24:27],v[132:135]
s_waitcnt lgkmcnt(5)
v_rcp_f16_sdwa v123,v122 dst_sel:WORD_1 dst_unused:UNUSED_PRESERVE src0_sel:WORD_1
v_mfma_f32_16x16x32_bf16 v[128:131],v[80:83],v[32:35],v[128:131]
ds_read_u16 v153,v153
v_fma_mix_f32 v156,v156,v121,0 op_sel_hi:[0,1,0]
v_fma_mix_f32 v158,v158,v121,0 op_sel:[0,1,0] op_sel_hi:[0,1,0]
v_mfma_f32_16x16x32_bf16 v[136:139],v[80:83],v[36:39],v[136:139]
s_waitcnt lgkmcnt(2)
s_setprio 0
v_lshl_add_u64 v[144:145],v[144:145],0,v[146:147]
v_lshl_add_u64 v[146:147],v[148:149],0,v[150:151]
v_mfma_f32_16x16x32_bf16 v[140:143],v[84:87],v[36:39],v[140:143]
ds_read_u16_d16_hi v155,v155
v_fma_mix_f32 v157,v157,v123,0 op_sel_hi:[0,1,0]
v_fma_mix_f32 v159,v159,v123,0 op_sel:[0,1,0] op_sel_hi:[0,1,0]
v_mfma_f32_16x16x32_bf16 v[132:135],v[84:87],v[32:35],v[132:135]
v_lshlrev_b32_sdwa v128,1,v128 dst_sel:DWORD dst_unused:UNUSED_PAD src0_sel:DWORD src1_sel:WORD_1
v_lshlrev_b32_sdwa v130,1,v130 dst_sel:DWORD dst_unused:UNUSED_PAD src0_sel:DWORD src1_sel:WORD_1
v_mfma_f32_16x16x32_bf16 v[208:211],v[88:91],v[144:147],v[208:211]
s_barrier
s_setprio 3
ds_read_u16 v128,v128
v_lshlrev_b32_sdwa v129,1,v129 dst_sel:DWORD dst_unused:UNUSED_PAD src0_sel:DWORD src1_sel:WORD_1
v_lshlrev_b32_sdwa v131,1,v131 dst_sel:DWORD dst_unused:UNUSED_PAD src0_sel:DWORD src1_sel:WORD_1
v_mfma_f32_16x16x32_bf16 v[216:219],v[92:95],v[144:147],v[216:219]
v_fma_mixlo_f16 v120,v140,s99,0
v_fma_mixlo_f16 v122,v141,s99,0
v_mfma_f32_16x16x32_bf16 v[224:227],v[96:99],v[144:147],v[224:227]
ds_read_u16_d16_hi v130,v130
v_lshlrev_b32_sdwa v132,1,v132 dst_sel:DWORD dst_unused:UNUSED_PAD src0_sel:DWORD src1_sel:WORD_1
v_lshlrev_b32_sdwa v134,1,v134 dst_sel:DWORD dst_unused:UNUSED_PAD src0_sel:DWORD src1_sel:WORD_1
v_mfma_f32_16x16x32_bf16 v[232:235],v[100:103],v[144:147],v[232:235]
s_waitcnt lgkmcnt(2)
v_lshl_add_u64 v[152:153],v[152:153],0,v[154:155]
v_mfma_f32_16x16x32_bf16 v[240:243],v[104:107],v[144:147],v[240:243]
ds_read_u16 v129,v129
v_cvt_pk_bf16_f32 v154,v156,v158
v_cvt_pk_bf16_f32 v155,v157,v159
v_mfma_f32_16x16x32_bf16 v[248:251],v[108:111],v[144:147],v[248:251]
v_fma_mixhi_f16 v120,v142,s99,0
v_fma_mixhi_f16 v122,v143,s99,0
v_mfma_f32_16x16x32_bf16 v[212:215],v[88:91],v[152:155],v[212:215]
ds_read_u16_d16_hi v131,v131
v_lshlrev_b32_sdwa v133,1,v133 dst_sel:DWORD dst_unused:UNUSED_PAD src0_sel:DWORD src1_sel:WORD_1
v_lshlrev_b32_sdwa v135,1,v135 dst_sel:DWORD dst_unused:UNUSED_PAD src0_sel:DWORD src1_sel:WORD_1
v_mfma_f32_16x16x32_bf16 v[220:223],v[92:95],v[152:155],v[220:223]
ds_read_b64_tr_b16 v[88:89],v3 offset:12320
v_pk_fma_f16 v121,v120,v120,s54
v_pk_fma_f16 v123,v122,v122,s54
v_mfma_f32_16x16x32_bf16 v[228:231],v[96:99],v[152:155],v[228:231]
ds_read_u16 v132,v132
v_pk_mul_f16 v121,v120,v121
v_pk_mul_f16 v123,v122,v123
v_mfma_f32_16x16x32_bf16 v[236:239],v[100:103],v[152:155],v[236:239]
ds_read_b64_tr_b16 v[90:91],v3 offset:13856
v_lshlrev_b32_sdwa v136,1,v136 dst_sel:DWORD dst_unused:UNUSED_PAD src0_sel:DWORD src1_sel:WORD_1
v_lshlrev_b32_sdwa v138,1,v138 dst_sel:DWORD dst_unused:UNUSED_PAD src0_sel:DWORD src1_sel:WORD_1
v_mfma_f32_16x16x32_bf16 v[244:247],v[104:107],v[152:155],v[244:247]
ds_read_u16_d16_hi v134,v134
v_exp_f16 v120,v121
v_mfma_f32_16x16x32_bf16 v[252:255],v[108:111],v[152:155],v[252:255]
ds_read_b64_tr_b16 v[92:93],v3 offset:12328
v_exp_f16 v122,v123
v_mfma_f32_16x16x32_bf16 v[144:147],v[64:67],v[40:43],v[112:115]
ds_read_u16 v133,v133
v_lshlrev_b32_sdwa v137,1,v137 dst_sel:DWORD dst_unused:UNUSED_PAD src0_sel:DWORD src1_sel:WORD_1
v_lshlrev_b32_sdwa v139,1,v139 dst_sel:DWORD dst_unused:UNUSED_PAD src0_sel:DWORD src1_sel:WORD_1
v_mfma_f32_16x16x32_bf16 v[152:155],v[64:67],v[44:47],v[112:115]
ds_read_b64_tr_b16 v[94:95],v3 offset:13864
v_exp_f16_sdwa v120,v121 dst_sel:WORD_1 dst_unused:UNUSED_PRESERVE src0_sel:WORD_1
v_mfma_f32_16x16x32_bf16 v[156:159],v[68:71],v[44:47],v[116:119]
ds_read_u16_d16_hi v135,v135
v_exp_f16_sdwa v122,v123 dst_sel:WORD_1 dst_unused:UNUSED_PRESERVE src0_sel:WORD_1
v_mfma_f32_16x16x32_bf16 v[148:151],v[68:71],v[40:43],v[116:119]
ds_read_b64_tr_b16 v[96:97],v3 offset:12384
v_pk_fma_f16 v120,v120,s55,s55
v_pk_fma_f16 v122,v122,s55,s55
v_mfma_f32_16x16x32_bf16 v[144:147],v[72:75],v[48:51],v[144:147]
ds_read_u16 v136,v136
v_rcp_f16 v121,v120
v_mfma_f32_16x16x32_bf16 v[152:155],v[72:75],v[52:55],v[152:155]
ds_read_b64_tr_b16 v[98:99],v3 offset:13920
ds_read_b64_tr_b16 v[100:101],v3 offset:12392
v_rcp_f16 v123,v122
v_mfma_f32_16x16x32_bf16 v[156:159],v[76:79],v[52:55],v[156:159]
ds_read_u16_d16_hi v138,v138
v_rcp_f16_sdwa v121,v120 dst_sel:WORD_1 dst_unused:UNUSED_PRESERVE src0_sel:WORD_1
v_mfma_f32_16x16x32_bf16 v[148:151],v[76:79],v[48:51],v[148:151]
ds_read_b64_tr_b16 v[102:103],v3 offset:13928
ds_read_b64_tr_b16 v[104:105],v3 offset:12448
v_rcp_f16_sdwa v123,v122 dst_sel:WORD_1 dst_unused:UNUSED_PRESERVE src0_sel:WORD_1
v_mfma_f32_16x16x32_bf16 v[144:147],v[80:83],v[56:59],v[144:147]
ds_read_u16 v137,v137
v_fma_mix_f32 v140,v140,v121,0 op_sel_hi:[0,1,0]
v_fma_mix_f32 v142,v142,v121,0 op_sel:[0,1,0] op_sel_hi:[0,1,0]
v_mfma_f32_16x16x32_bf16 v[152:155],v[80:83],v[60:63],v[152:155]
s_waitcnt lgkmcnt(8)
ds_read_b64_tr_b16 v[106:107],v3 offset:13984
ds_read_b64_tr_b16 v[108:109],v3 offset:12456
v_lshl_add_u64 v[128:129],v[128:129],0,v[130:131]
v_lshl_add_u64 v[130:131],v[132:133],0,v[134:135]
v_mfma_f32_16x16x32_bf16 v[156:159],v[84:87],v[60:63],v[156:159]
ds_read_u16_d16_hi v139,v139
v_fma_mix_f32 v141,v141,v123,0 op_sel_hi:[0,1,0]
v_fma_mix_f32 v143,v143,v123,0 op_sel:[0,1,0] op_sel_hi:[0,1,0]
v_mfma_f32_16x16x32_bf16 v[148:151],v[84:87],v[56:59],v[148:151]
ds_read_b64_tr_b16 v[110:111],v3 offset:13992
ds_read_b64_tr_b16 v[64:65],v1 offset:12320
v_lshlrev_b32_sdwa v144,1,v144 dst_sel:DWORD dst_unused:UNUSED_PAD src0_sel:DWORD src1_sel:WORD_1
v_lshlrev_b32_sdwa v146,1,v146 dst_sel:DWORD dst_unused:UNUSED_PAD src0_sel:DWORD src1_sel:WORD_1
v_mfma_f32_16x16x32_bf16 v[160:163],v[88:91],v[128:131],v[160:163]
ds_read_u16 v144,v144
v_lshlrev_b32_sdwa v145,1,v145 dst_sel:DWORD dst_unused:UNUSED_PAD src0_sel:DWORD src1_sel:WORD_1
v_lshlrev_b32_sdwa v147,1,v147 dst_sel:DWORD dst_unused:UNUSED_PAD src0_sel:DWORD src1_sel:WORD_1
v_mfma_f32_16x16x32_bf16 v[168:171],v[92:95],v[128:131],v[168:171]
s_waitcnt lgkmcnt(11)
ds_read_b64_tr_b16 v[66:67],v1 offset:14368
ds_read_b128 v[112:115],v4 offset:0
v_fma_mixlo_f16 v120,v156,s99,0
v_fma_mixlo_f16 v122,v157,s99,0
v_mfma_f32_16x16x32_bf16 v[176:179],v[96:99],v[128:131],v[176:179]
s_waitcnt lgkmcnt(10)
ds_read_u16_d16_hi v146,v146
v_lshlrev_b32_sdwa v148,1,v148 dst_sel:DWORD dst_unused:UNUSED_PAD src0_sel:DWORD src1_sel:WORD_1
v_lshlrev_b32_sdwa v150,1,v150 dst_sel:DWORD dst_unused:UNUSED_PAD src0_sel:DWORD src1_sel:WORD_1
v_mfma_f32_16x16x32_bf16 v[184:187],v[100:103],v[128:131],v[184:187]
s_waitcnt lgkmcnt(6)
ds_read_b64_tr_b16 v[68:69],v2 offset:12320
ds_read_b64_tr_b16 v[70:71],v2 offset:14368
v_lshl_add_u64 v[136:137],v[136:137],0,v[138:139]
v_mfma_f32_16x16x32_bf16 v[192:195],v[104:107],v[128:131],v[192:195]
s_waitcnt lgkmcnt(7)
ds_read_u16 v145,v145
v_cvt_pk_bf16_f32 v138,v140,v142
v_cvt_pk_bf16_f32 v139,v141,v143
v_mfma_f32_16x16x32_bf16 v[200:203],v[108:111],v[128:131],v[200:203]
ds_read_b128 v[116:119],v4 offset:32
ds_read_b64_tr_b16 v[72:73],v1 offset:16416
v_fma_mixhi_f16 v120,v158,s99,0
v_fma_mixhi_f16 v122,v159,s99,0
v_mfma_f32_16x16x32_bf16 v[164:167],v[88:91],v[136:139],v[164:167]
ds_read_u16_d16_hi v147,v147
v_lshlrev_b32_sdwa v149,1,v149 dst_sel:DWORD dst_unused:UNUSED_PAD src0_sel:DWORD src1_sel:WORD_1
v_lshlrev_b32_sdwa v151,1,v151 dst_sel:DWORD dst_unused:UNUSED_PAD src0_sel:DWORD src1_sel:WORD_1
v_mfma_f32_16x16x32_bf16 v[172:175],v[92:95],v[136:139],v[172:175]
ds_read_b64_tr_b16 v[74:75],v1 offset:18464
ds_read_b64_tr_b16 v[76:77],v2 offset:16416
v_pk_fma_f16 v121,v120,v120,s54
v_pk_fma_f16 v123,v122,v122,s54
v_mfma_f32_16x16x32_bf16 v[180:183],v[96:99],v[136:139],v[180:183]
ds_read_u16 v148,v148
v_pk_mul_f16 v121,v120,v121
v_pk_mul_f16 v123,v122,v123
v_mfma_f32_16x16x32_bf16 v[188:191],v[100:103],v[136:139],v[188:191]
ds_read_b64_tr_b16 v[78:79],v2 offset:18464
v_lshlrev_b32_sdwa v152,1,v152 dst_sel:DWORD dst_unused:UNUSED_PAD src0_sel:DWORD src1_sel:WORD_1
v_lshlrev_b32_sdwa v154,1,v154 dst_sel:DWORD dst_unused:UNUSED_PAD src0_sel:DWORD src1_sel:WORD_1
v_mfma_f32_16x16x32_bf16 v[196:199],v[104:107],v[136:139],v[196:199]
ds_read_u16_d16_hi v150,v150
v_exp_f16 v120,v121
v_mfma_f32_16x16x32_bf16 v[204:207],v[108:111],v[136:139],v[204:207]
s_waitcnt lgkmcnt(12)
ds_read_b64_tr_b16 v[80:81],v1 offset:20512
v_exp_f16 v122,v123
v_mfma_f32_16x16x32_bf16 v[128:131],v[64:67],v[16:19],v[112:115]
ds_read_u16 v149,v149
v_lshlrev_b32_sdwa v153,1,v153 dst_sel:DWORD dst_unused:UNUSED_PAD src0_sel:DWORD src1_sel:WORD_1
v_lshlrev_b32_sdwa v155,1,v155 dst_sel:DWORD dst_unused:UNUSED_PAD src0_sel:DWORD src1_sel:WORD_1
v_mfma_f32_16x16x32_bf16 v[136:139],v[64:67],v[20:23],v[112:115]
s_waitcnt lgkmcnt(9)
ds_read_b64_tr_b16 v[82:83],v1 offset:22560
v_exp_f16_sdwa v120,v121 dst_sel:WORD_1 dst_unused:UNUSED_PRESERVE src0_sel:WORD_1
v_mfma_f32_16x16x32_bf16 v[140:143],v[68:71],v[20:23],v[116:119]
ds_read_u16_d16_hi v151,v151
v_exp_f16_sdwa v122,v123 dst_sel:WORD_1 dst_unused:UNUSED_PRESERVE src0_sel:WORD_1
v_mfma_f32_16x16x32_bf16 v[132:135],v[68:71],v[16:19],v[116:119]
s_waitcnt lgkmcnt(8)
ds_read_b64_tr_b16 v[84:85],v2 offset:20512
v_pk_fma_f16 v120,v120,s55,s55
v_pk_fma_f16 v122,v122,s55,s55
v_mfma_f32_16x16x32_bf16 v[128:131],v[72:75],v[24:27],v[128:131]
ds_read_u16 v152,v152
v_rcp_f16 v121,v120
v_mfma_f32_16x16x32_bf16 v[136:139],v[72:75],v[28:31],v[136:139]
s_waitcnt lgkmcnt(7)
ds_read_b64_tr_b16 v[86:87],v2 offset:22560
v_rcp_f16 v123,v122
v_mfma_f32_16x16x32_bf16 v[140:143],v[76:79],v[28:31],v[140:143]
ds_read_u16_d16_hi v154,v154
v_rcp_f16_sdwa v121,v120 dst_sel:WORD_1 dst_unused:UNUSED_PRESERVE src0_sel:WORD_1
v_mfma_f32_16x16x32_bf16 v[132:135],v[76:79],v[24:27],v[132:135]
s_waitcnt lgkmcnt(5)
v_rcp_f16_sdwa v123,v122 dst_sel:WORD_1 dst_unused:UNUSED_PRESERVE src0_sel:WORD_1
v_mfma_f32_16x16x32_bf16 v[128:131],v[80:83],v[32:35],v[128:131]
ds_read_u16 v153,v153
v_fma_mix_f32 v156,v156,v121,0 op_sel_hi:[0,1,0]
v_fma_mix_f32 v158,v158,v121,0 op_sel:[0,1,0] op_sel_hi:[0,1,0]
v_mfma_f32_16x16x32_bf16 v[136:139],v[80:83],v[36:39],v[136:139]
s_waitcnt lgkmcnt(2)
v_lshl_add_u64 v[144:145],v[144:145],0,v[146:147]
v_lshl_add_u64 v[146:147],v[148:149],0,v[150:151]
v_mfma_f32_16x16x32_bf16 v[140:143],v[84:87],v[36:39],v[140:143]
ds_read_u16_d16_hi v155,v155
v_fma_mix_f32 v157,v157,v123,0 op_sel_hi:[0,1,0]
v_fma_mix_f32 v159,v159,v123,0 op_sel:[0,1,0] op_sel_hi:[0,1,0]
v_mfma_f32_16x16x32_bf16 v[132:135],v[84:87],v[32:35],v[132:135]
v_lshlrev_b32_sdwa v128,1,v128 dst_sel:DWORD dst_unused:UNUSED_PAD src0_sel:DWORD src1_sel:WORD_1
v_lshlrev_b32_sdwa v130,1,v130 dst_sel:DWORD dst_unused:UNUSED_PAD src0_sel:DWORD src1_sel:WORD_1
v_mfma_f32_16x16x32_bf16 v[208:211],v[88:91],v[144:147],v[208:211]
ds_read_u16 v128,v128
v_lshlrev_b32_sdwa v129,1,v129 dst_sel:DWORD dst_unused:UNUSED_PAD src0_sel:DWORD src1_sel:WORD_1
v_lshlrev_b32_sdwa v131,1,v131 dst_sel:DWORD dst_unused:UNUSED_PAD src0_sel:DWORD src1_sel:WORD_1
v_mfma_f32_16x16x32_bf16 v[216:219],v[92:95],v[144:147],v[216:219]
v_fma_mixlo_f16 v120,v140,s99,0
v_fma_mixlo_f16 v122,v141,s99,0
v_mfma_f32_16x16x32_bf16 v[224:227],v[96:99],v[144:147],v[224:227]
ds_read_u16_d16_hi v130,v130
v_lshlrev_b32_sdwa v132,1,v132 dst_sel:DWORD dst_unused:UNUSED_PAD src0_sel:DWORD src1_sel:WORD_1
v_lshlrev_b32_sdwa v134,1,v134 dst_sel:DWORD dst_unused:UNUSED_PAD src0_sel:DWORD src1_sel:WORD_1
v_mfma_f32_16x16x32_bf16 v[232:235],v[100:103],v[144:147],v[232:235]
s_waitcnt lgkmcnt(2)
v_lshl_add_u64 v[152:153],v[152:153],0,v[154:155]
v_mfma_f32_16x16x32_bf16 v[240:243],v[104:107],v[144:147],v[240:243]
ds_read_u16 v129,v129
v_cvt_pk_bf16_f32 v154,v156,v158
v_cvt_pk_bf16_f32 v155,v157,v159
v_mfma_f32_16x16x32_bf16 v[248:251],v[108:111],v[144:147],v[248:251]
s_setprio 2
v_fma_mixhi_f16 v120,v142,s99,0
v_fma_mixhi_f16 v122,v143,s99,0
v_mfma_f32_16x16x32_bf16 v[212:215],v[88:91],v[152:155],v[212:215]
ds_read_u16_d16_hi v131,v131
v_lshlrev_b32_sdwa v133,1,v133 dst_sel:DWORD dst_unused:UNUSED_PAD src0_sel:DWORD src1_sel:WORD_1
v_lshlrev_b32_sdwa v135,1,v135 dst_sel:DWORD dst_unused:UNUSED_PAD src0_sel:DWORD src1_sel:WORD_1
v_mfma_f32_16x16x32_bf16 v[220:223],v[92:95],v[152:155],v[220:223]
ds_read_b64_tr_b16 v[88:89],v3 offset:18480
v_pk_fma_f16 v121,v120,v120,s54
v_pk_fma_f16 v123,v122,v122,s54
v_mfma_f32_16x16x32_bf16 v[228:231],v[96:99],v[152:155],v[228:231]
ds_read_u16 v132,v132
v_pk_mul_f16 v121,v120,v121
v_pk_mul_f16 v123,v122,v123
v_mfma_f32_16x16x32_bf16 v[236:239],v[100:103],v[152:155],v[236:239]
ds_read_b64_tr_b16 v[90:91],v3 offset:20016
v_lshlrev_b32_sdwa v136,1,v136 dst_sel:DWORD dst_unused:UNUSED_PAD src0_sel:DWORD src1_sel:WORD_1
v_lshlrev_b32_sdwa v138,1,v138 dst_sel:DWORD dst_unused:UNUSED_PAD src0_sel:DWORD src1_sel:WORD_1
v_mfma_f32_16x16x32_bf16 v[244:247],v[104:107],v[152:155],v[244:247]
ds_read_u16_d16_hi v134,v134
v_exp_f16 v120,v121
v_mfma_f32_16x16x32_bf16 v[252:255],v[108:111],v[152:155],v[252:255]
ds_read_b64_tr_b16 v[92:93],v3 offset:18488
v_exp_f16 v122,v123
v_mfma_f32_16x16x32_bf16 v[144:147],v[64:67],v[40:43],v[112:115]
ds_read_u16 v133,v133
v_lshlrev_b32_sdwa v137,1,v137 dst_sel:DWORD dst_unused:UNUSED_PAD src0_sel:DWORD src1_sel:WORD_1
v_lshlrev_b32_sdwa v139,1,v139 dst_sel:DWORD dst_unused:UNUSED_PAD src0_sel:DWORD src1_sel:WORD_1
v_mfma_f32_16x16x32_bf16 v[152:155],v[64:67],v[44:47],v[112:115]
ds_read_b64_tr_b16 v[94:95],v3 offset:20024
v_exp_f16_sdwa v120,v121 dst_sel:WORD_1 dst_unused:UNUSED_PRESERVE src0_sel:WORD_1
v_mfma_f32_16x16x32_bf16 v[156:159],v[68:71],v[44:47],v[116:119]
ds_read_u16_d16_hi v135,v135
v_exp_f16_sdwa v122,v123 dst_sel:WORD_1 dst_unused:UNUSED_PRESERVE src0_sel:WORD_1
v_mfma_f32_16x16x32_bf16 v[148:151],v[68:71],v[40:43],v[116:119]
ds_read_b64_tr_b16 v[96:97],v3 offset:18544
v_pk_fma_f16 v120,v120,s55,s55
v_pk_fma_f16 v122,v122,s55,s55
v_mfma_f32_16x16x32_bf16 v[144:147],v[72:75],v[48:51],v[144:147]
ds_read_u16 v136,v136
v_rcp_f16 v121,v120
v_mfma_f32_16x16x32_bf16 v[152:155],v[72:75],v[52:55],v[152:155]
ds_read_b64_tr_b16 v[98:99],v3 offset:20080
ds_read_b64_tr_b16 v[100:101],v3 offset:18552
v_rcp_f16 v123,v122
v_mfma_f32_16x16x32_bf16 v[156:159],v[76:79],v[52:55],v[156:159]
ds_read_u16_d16_hi v138,v138
v_rcp_f16_sdwa v121,v120 dst_sel:WORD_1 dst_unused:UNUSED_PRESERVE src0_sel:WORD_1
v_mfma_f32_16x16x32_bf16 v[148:151],v[76:79],v[48:51],v[148:151]
ds_read_b64_tr_b16 v[102:103],v3 offset:20088
ds_read_b64_tr_b16 v[104:105],v3 offset:18608
v_rcp_f16_sdwa v123,v122 dst_sel:WORD_1 dst_unused:UNUSED_PRESERVE src0_sel:WORD_1
v_mfma_f32_16x16x32_bf16 v[144:147],v[80:83],v[56:59],v[144:147]
ds_read_u16 v137,v137
v_fma_mix_f32 v140,v140,v121,0 op_sel_hi:[0,1,0]
v_fma_mix_f32 v142,v142,v121,0 op_sel:[0,1,0] op_sel_hi:[0,1,0]
v_mfma_f32_16x16x32_bf16 v[152:155],v[80:83],v[60:63],v[152:155]
s_waitcnt lgkmcnt(8)
ds_read_b64_tr_b16 v[106:107],v3 offset:20144
ds_read_b64_tr_b16 v[108:109],v3 offset:18616
v_lshl_add_u64 v[128:129],v[128:129],0,v[130:131]
v_lshl_add_u64 v[130:131],v[132:133],0,v[134:135]
v_mfma_f32_16x16x32_bf16 v[156:159],v[84:87],v[60:63],v[156:159]
ds_read_u16_d16_hi v139,v139
v_fma_mix_f32 v141,v141,v123,0 op_sel_hi:[0,1,0]
v_fma_mix_f32 v143,v143,v123,0 op_sel:[0,1,0] op_sel_hi:[0,1,0]
v_mfma_f32_16x16x32_bf16 v[148:151],v[84:87],v[56:59],v[148:151]
ds_read_b64_tr_b16 v[110:111],v3 offset:20152
ds_read_b64_tr_b16 v[64:65],v1 offset:12832
v_lshlrev_b32_sdwa v144,1,v144 dst_sel:DWORD dst_unused:UNUSED_PAD src0_sel:DWORD src1_sel:WORD_1
v_lshlrev_b32_sdwa v146,1,v146 dst_sel:DWORD dst_unused:UNUSED_PAD src0_sel:DWORD src1_sel:WORD_1
v_mfma_f32_16x16x32_bf16 v[160:163],v[88:91],v[128:131],v[160:163]
ds_read_u16 v144,v144
v_lshlrev_b32_sdwa v145,1,v145 dst_sel:DWORD dst_unused:UNUSED_PAD src0_sel:DWORD src1_sel:WORD_1
v_lshlrev_b32_sdwa v147,1,v147 dst_sel:DWORD dst_unused:UNUSED_PAD src0_sel:DWORD src1_sel:WORD_1
v_mfma_f32_16x16x32_bf16 v[168:171],v[92:95],v[128:131],v[168:171]
s_waitcnt lgkmcnt(11)
ds_read_b64_tr_b16 v[66:67],v1 offset:14880
ds_read_b128 v[112:115],v4 offset:128
v_fma_mixlo_f16 v120,v156,s99,0
v_fma_mixlo_f16 v122,v157,s99,0
v_mfma_f32_16x16x32_bf16 v[176:179],v[96:99],v[128:131],v[176:179]
s_waitcnt lgkmcnt(10)
ds_read_u16_d16_hi v146,v146
v_lshlrev_b32_sdwa v148,1,v148 dst_sel:DWORD dst_unused:UNUSED_PAD src0_sel:DWORD src1_sel:WORD_1
v_lshlrev_b32_sdwa v150,1,v150 dst_sel:DWORD dst_unused:UNUSED_PAD src0_sel:DWORD src1_sel:WORD_1
v_mfma_f32_16x16x32_bf16 v[184:187],v[100:103],v[128:131],v[184:187]
s_waitcnt lgkmcnt(6)
ds_read_b64_tr_b16 v[68:69],v2 offset:12832
ds_read_b64_tr_b16 v[70:71],v2 offset:14880
v_lshl_add_u64 v[136:137],v[136:137],0,v[138:139]
v_mfma_f32_16x16x32_bf16 v[192:195],v[104:107],v[128:131],v[192:195]
s_waitcnt lgkmcnt(7)
ds_read_u16 v145,v145
v_cvt_pk_bf16_f32 v138,v140,v142
v_cvt_pk_bf16_f32 v139,v141,v143
v_mfma_f32_16x16x32_bf16 v[200:203],v[108:111],v[128:131],v[200:203]
ds_read_b128 v[116:119],v4 offset:160
ds_read_b64_tr_b16 v[72:73],v1 offset:16928
v_fma_mixhi_f16 v120,v158,s99,0
v_fma_mixhi_f16 v122,v159,s99,0
v_mfma_f32_16x16x32_bf16 v[164:167],v[88:91],v[136:139],v[164:167]
ds_read_u16_d16_hi v147,v147
v_lshlrev_b32_sdwa v149,1,v149 dst_sel:DWORD dst_unused:UNUSED_PAD src0_sel:DWORD src1_sel:WORD_1
v_lshlrev_b32_sdwa v151,1,v151 dst_sel:DWORD dst_unused:UNUSED_PAD src0_sel:DWORD src1_sel:WORD_1
v_mfma_f32_16x16x32_bf16 v[172:175],v[92:95],v[136:139],v[172:175]
s_setprio 1
ds_read_b64_tr_b16 v[74:75],v1 offset:18976
ds_read_b64_tr_b16 v[76:77],v2 offset:16928
v_pk_fma_f16 v121,v120,v120,s54
v_pk_fma_f16 v123,v122,v122,s54
v_mfma_f32_16x16x32_bf16 v[180:183],v[96:99],v[136:139],v[180:183]
ds_read_u16 v148,v148
v_pk_mul_f16 v121,v120,v121
v_pk_mul_f16 v123,v122,v123
v_mfma_f32_16x16x32_bf16 v[188:191],v[100:103],v[136:139],v[188:191]
ds_read_b64_tr_b16 v[78:79],v2 offset:18976
v_lshlrev_b32_sdwa v152,1,v152 dst_sel:DWORD dst_unused:UNUSED_PAD src0_sel:DWORD src1_sel:WORD_1
v_lshlrev_b32_sdwa v154,1,v154 dst_sel:DWORD dst_unused:UNUSED_PAD src0_sel:DWORD src1_sel:WORD_1
v_mfma_f32_16x16x32_bf16 v[196:199],v[104:107],v[136:139],v[196:199]
ds_read_u16_d16_hi v150,v150
v_exp_f16 v120,v121
v_mfma_f32_16x16x32_bf16 v[204:207],v[108:111],v[136:139],v[204:207]
s_waitcnt lgkmcnt(12)
ds_read_b64_tr_b16 v[80:81],v1 offset:21024
v_exp_f16 v122,v123
v_mfma_f32_16x16x32_bf16 v[128:131],v[64:67],v[16:19],v[112:115]
ds_read_u16 v149,v149
v_lshlrev_b32_sdwa v153,1,v153 dst_sel:DWORD dst_unused:UNUSED_PAD src0_sel:DWORD src1_sel:WORD_1
v_lshlrev_b32_sdwa v155,1,v155 dst_sel:DWORD dst_unused:UNUSED_PAD src0_sel:DWORD src1_sel:WORD_1
v_mfma_f32_16x16x32_bf16 v[136:139],v[64:67],v[20:23],v[112:115]
s_waitcnt lgkmcnt(9)
ds_read_b64_tr_b16 v[82:83],v1 offset:23072
v_exp_f16_sdwa v120,v121 dst_sel:WORD_1 dst_unused:UNUSED_PRESERVE src0_sel:WORD_1
v_mfma_f32_16x16x32_bf16 v[140:143],v[68:71],v[20:23],v[116:119]
ds_read_u16_d16_hi v151,v151
v_exp_f16_sdwa v122,v123 dst_sel:WORD_1 dst_unused:UNUSED_PRESERVE src0_sel:WORD_1
v_mfma_f32_16x16x32_bf16 v[132:135],v[68:71],v[16:19],v[116:119]
s_waitcnt lgkmcnt(8)
ds_read_b64_tr_b16 v[84:85],v2 offset:21024
v_pk_fma_f16 v120,v120,s55,s55
v_pk_fma_f16 v122,v122,s55,s55
v_mfma_f32_16x16x32_bf16 v[128:131],v[72:75],v[24:27],v[128:131]
ds_read_u16 v152,v152
v_rcp_f16 v121,v120
v_mfma_f32_16x16x32_bf16 v[136:139],v[72:75],v[28:31],v[136:139]
s_waitcnt lgkmcnt(7)
ds_read_b64_tr_b16 v[86:87],v2 offset:23072
v_rcp_f16 v123,v122
v_mfma_f32_16x16x32_bf16 v[140:143],v[76:79],v[28:31],v[140:143]
ds_read_u16_d16_hi v154,v154
v_rcp_f16_sdwa v121,v120 dst_sel:WORD_1 dst_unused:UNUSED_PRESERVE src0_sel:WORD_1
v_mfma_f32_16x16x32_bf16 v[132:135],v[76:79],v[24:27],v[132:135]
s_waitcnt lgkmcnt(5)
v_rcp_f16_sdwa v123,v122 dst_sel:WORD_1 dst_unused:UNUSED_PRESERVE src0_sel:WORD_1
v_mfma_f32_16x16x32_bf16 v[128:131],v[80:83],v[32:35],v[128:131]
ds_read_u16 v153,v153
v_fma_mix_f32 v156,v156,v121,0 op_sel_hi:[0,1,0]
v_fma_mix_f32 v158,v158,v121,0 op_sel:[0,1,0] op_sel_hi:[0,1,0]
v_mfma_f32_16x16x32_bf16 v[136:139],v[80:83],v[36:39],v[136:139]
s_waitcnt lgkmcnt(2)
s_setprio 0
v_lshl_add_u64 v[144:145],v[144:145],0,v[146:147]
v_lshl_add_u64 v[146:147],v[148:149],0,v[150:151]
v_mfma_f32_16x16x32_bf16 v[140:143],v[84:87],v[36:39],v[140:143]
ds_read_u16_d16_hi v155,v155
v_fma_mix_f32 v157,v157,v123,0 op_sel_hi:[0,1,0]
v_fma_mix_f32 v159,v159,v123,0 op_sel:[0,1,0] op_sel_hi:[0,1,0]
v_mfma_f32_16x16x32_bf16 v[132:135],v[84:87],v[32:35],v[132:135]
v_lshlrev_b32_sdwa v128,1,v128 dst_sel:DWORD dst_unused:UNUSED_PAD src0_sel:DWORD src1_sel:WORD_1
v_lshlrev_b32_sdwa v130,1,v130 dst_sel:DWORD dst_unused:UNUSED_PAD src0_sel:DWORD src1_sel:WORD_1
v_mfma_f32_16x16x32_bf16 v[208:211],v[88:91],v[144:147],v[208:211]
s_barrier
s_setprio 2
ds_read_u16 v128,v128
v_lshlrev_b32_sdwa v129,1,v129 dst_sel:DWORD dst_unused:UNUSED_PAD src0_sel:DWORD src1_sel:WORD_1
v_mfma_f32_16x16x32_bf16 v[216:219],v[92:95],v[144:147],v[216:219]
ds_read_u16_d16_hi v130,v130
v_lshlrev_b32_sdwa v131,1,v131 dst_sel:DWORD dst_unused:UNUSED_PAD src0_sel:DWORD src1_sel:WORD_1
v_mfma_f32_16x16x32_bf16 v[224:227],v[96:99],v[144:147],v[224:227]
s_cbranch_vccz RB46_%=
s_add_u32 m0,s58,12320
s_mul_i32 s69,s59,3
s_add_u32 s69,s69,s97
buffer_load_dwordx4 v5,s[44:47],s69 offen lds
s_add_u32 m0,m0,1024
s_add_u32 s69,s69,s53
buffer_load_dwordx4 v6,s[44:47],s69 offen lds
RB46_%=:
v_lshlrev_b32_sdwa v132,1,v132 dst_sel:DWORD dst_unused:UNUSED_PAD src0_sel:DWORD src1_sel:WORD_1
v_mfma_f32_16x16x32_bf16 v[232:235],v[100:103],v[144:147],v[232:235]
s_waitcnt lgkmcnt(2)
ds_read_u16 v129,v129
s_nop 0
s_mov_b32 s86,0xbe801d5a
v_lshl_add_u64 v[152:153],v[152:153],0,v[154:155]
v_lshlrev_b32_sdwa v134,1,v134 dst_sel:DWORD dst_unused:UNUSED_PAD src0_sel:DWORD src1_sel:WORD_1
v_mfma_f32_16x16x32_bf16 v[240:243],v[104:107],v[144:147],v[240:243]
ds_read_u16_d16_hi v131,v131
v_cvt_pk_bf16_f32 v154,v156,v158
v_cvt_pk_bf16_f32 v155,v157,v159
v_mfma_f32_16x16x32_bf16 v[248:251],v[108:111],v[144:147],v[248:251]
v_lshlrev_b32_sdwa v133,1,v133 dst_sel:DWORD dst_unused:UNUSED_PAD src0_sel:DWORD src1_sel:WORD_1
v_mfma_f32_16x16x32_bf16 v[212:215],v[88:91],v[152:155],v[212:215]
s_cbranch_vccz RB47_%=
s_add_u32 m0,m0,1024
s_add_u32 s69,s69,s53
buffer_load_dwordx4 v5,s[44:47],s69 offen lds
s_add_u32 m0,m0,s50
s_add_u32 s69,s69,s53
buffer_load_dwordx4 v6,s[44:47],s69 offen lds
RB47_%=:
ds_read_u16 v132,v132
v_lshlrev_b32_sdwa v135,1,v135 dst_sel:DWORD dst_unused:UNUSED_PAD src0_sel:DWORD src1_sel:WORD_1
v_mfma_f32_16x16x32_bf16 v[220:223],v[92:95],v[152:155],v[220:223]
ds_read_u16_d16_hi v134,v134
v_lshlrev_b32_sdwa v136,1,v136 dst_sel:DWORD dst_unused:UNUSED_PAD src0_sel:DWORD src1_sel:WORD_1
v_mfma_f32_16x16x32_bf16 v[228:231],v[96:99],v[152:155],v[228:231]
ds_read_b64_tr_b16 v[88:89],v3 offset:0
ds_read_b64_tr_b16 v[90:91],v3 offset:1536
v_lshlrev_b32_sdwa v138,1,v138 dst_sel:DWORD dst_unused:UNUSED_PAD src0_sel:DWORD src1_sel:WORD_1
v_mfma_f32_16x16x32_bf16 v[236:239],v[100:103],v[152:155],v[236:239]
ds_read_u16 v133,v133
v_lshlrev_b32_sdwa v137,1,v137 dst_sel:DWORD dst_unused:UNUSED_PAD src0_sel:DWORD src1_sel:WORD_1
v_mfma_f32_16x16x32_bf16 v[244:247],v[104:107],v[152:155],v[244:247]
ds_read_u16_d16_hi v135,v135
v_lshlrev_b32_sdwa v139,1,v139 dst_sel:DWORD dst_unused:UNUSED_PAD src0_sel:DWORD src1_sel:WORD_1
v_mfma_f32_16x16x32_bf16 v[252:255],v[108:111],v[152:155],v[252:255]
s_cbranch_vccz RB48_%=
s_add_u32 m0,m0,1024
s_add_u32 s69,s69,s53
buffer_load_dwordx4 v5,s[44:47],s69 offen lds
s_add_u32 m0,m0,1024
s_add_u32 s69,s69,s53
buffer_load_dwordx4 v6,s[44:47],s69 offen lds
RB48_%=:
ds_read_b64_tr_b16 v[92:93],v3 offset:8
ds_read_b64_tr_b16 v[94:95],v3 offset:1544
v_lshlrev_b32_sdwa v140,1,v140 dst_sel:DWORD dst_unused:UNUSED_PAD src0_sel:DWORD src1_sel:WORD_1
v_mfma_f32_16x16x32_bf16 v[144:147],v[64:67],v[40:43],v[112:115]
ds_read_u16 v136,v136
v_lshlrev_b32_sdwa v142,1,v142 dst_sel:DWORD dst_unused:UNUSED_PAD src0_sel:DWORD src1_sel:WORD_1
v_mfma_f32_16x16x32_bf16 v[152:155],v[64:67],v[44:47],v[112:115]
ds_read_u16_d16_hi v138,v138
v_lshlrev_b32_sdwa v141,1,v141 dst_sel:DWORD dst_unused:UNUSED_PAD src0_sel:DWORD src1_sel:WORD_1
v_mfma_f32_16x16x32_bf16 v[156:159],v[68:71],v[44:47],v[116:119]
ds_read_b64_tr_b16 v[96:97],v3 offset:64
ds_read_b64_tr_b16 v[98:99],v3 offset:1600
v_lshlrev_b32_sdwa v143,1,v143 dst_sel:DWORD dst_unused:UNUSED_PAD src0_sel:DWORD src1_sel:WORD_1
v_mfma_f32_16x16x32_bf16 v[148:151],v[68:71],v[40:43],v[116:119]
ds_read_u16 v137,v137
v_mfma_f32_16x16x32_bf16 v[144:147],v[72:75],v[48:51],v[144:147]
ds_read_u16_d16_hi v139,v139
v_mfma_f32_16x16x32_bf16 v[152:155],v[72:75],v[52:55],v[152:155]
s_cbranch_vccz RB49_%=
s_add_u32 m0,m0,s50
s_add_u32 s69,s69,s53
buffer_load_dwordx4 v5,s[44:47],s69 offen lds
s_add_u32 m0,m0,1024
s_add_u32 s69,s69,s53
buffer_load_dwordx4 v6,s[44:47],s69 offen lds
RB49_%=:
ds_read_b64_tr_b16 v[100:101],v3 offset:72
ds_read_b64_tr_b16 v[102:103],v3 offset:1608
v_mfma_f32_16x16x32_bf16 v[156:159],v[76:79],v[52:55],v[156:159]
ds_read_u16 v140,v140
v_mfma_f32_16x16x32_bf16 v[148:151],v[76:79],v[48:51],v[148:151]
s_waitcnt lgkmcnt(15)
ds_read_u16_d16_hi v142,v142
v_lshl_add_u64 v[128:129],v[128:129],0,v[130:131]
v_mfma_f32_16x16x32_bf16 v[144:147],v[80:83],v[56:59],v[144:147]
s_waitcnt lgkmcnt(12)
ds_read_b64_tr_b16 v[104:105],v3 offset:128
ds_read_b64_tr_b16 v[106:107],v3 offset:1664
v_lshl_add_u64 v[130:131],v[132:133],0,v[134:135]
v_mfma_f32_16x16x32_bf16 v[152:155],v[80:83],v[60:63],v[152:155]
ds_read_u16 v141,v141
v_mfma_f32_16x16x32_bf16 v[156:159],v[84:87],v[60:63],v[156:159]
ds_read_u16_d16_hi v143,v143
v_mfma_f32_16x16x32_bf16 v[148:151],v[84:87],v[56:59],v[148:151]
s_cbranch_vccz RB50_%=
s_add_u32 m0,m0,1024
s_add_u32 s69,s69,s53
buffer_load_dwordx4 v5,s[44:47],s69 offen lds
s_add_u32 m0,m0,s50
s_add_u32 s69,s69,s53
buffer_load_dwordx4 v6,s[44:47],s69 offen lds
RB50_%=:
ds_read_b64_tr_b16 v[108:109],v3 offset:136
ds_read_b64_tr_b16 v[110:111],v3 offset:1672
v_lshlrev_b32_sdwa v144,1,v144 dst_sel:DWORD dst_unused:UNUSED_PAD src0_sel:DWORD src1_sel:WORD_1
v_lshlrev_b32_sdwa v146,1,v146 dst_sel:DWORD dst_unused:UNUSED_PAD src0_sel:DWORD src1_sel:WORD_1
v_mfma_f32_16x16x32_bf16 v[160:163],v[88:91],v[128:131],v[160:163]
s_waitcnt lgkmcnt(15)
ds_read_u16 v144,v144
v_lshlrev_b32_sdwa v145,1,v145 dst_sel:DWORD dst_unused:UNUSED_PAD src0_sel:DWORD src1_sel:WORD_1
v_mfma_f32_16x16x32_bf16 v[168:171],v[92:95],v[128:131],v[168:171]
s_waitcnt lgkmcnt(13)
ds_read_u16_d16_hi v146,v146
v_lshlrev_b32_sdwa v147,1,v147 dst_sel:DWORD dst_unused:UNUSED_PAD src0_sel:DWORD src1_sel:WORD_1
v_mfma_f32_16x16x32_bf16 v[176:179],v[96:99],v[128:131],v[176:179]
s_waitcnt lgkmcnt(10)
ds_read_b64_tr_b16 v[64:65],v1 offset:0
ds_read_b64_tr_b16 v[66:67],v1 offset:2048
v_lshlrev_b32_sdwa v148,1,v148 dst_sel:DWORD dst_unused:UNUSED_PAD src0_sel:DWORD src1_sel:WORD_1
v_mfma_f32_16x16x32_bf16 v[184:187],v[100:103],v[128:131],v[184:187]
s_waitcnt lgkmcnt(8)
ds_read_u16 v145,v145
v_lshl_add_u64 v[136:137],v[136:137],0,v[138:139]
v_lshlrev_b32_sdwa v150,1,v150 dst_sel:DWORD dst_unused:UNUSED_PAD src0_sel:DWORD src1_sel:WORD_1
v_mfma_f32_16x16x32_bf16 v[192:195],v[104:107],v[128:131],v[192:195]
s_waitcnt lgkmcnt(5)
s_cbranch_vccz RB51_%=
s_add_u32 m0,m0,1024
s_add_u32 s69,s69,s53
buffer_load_dwordx4 v5,s[44:47],s69 offen lds
s_add_u32 m0,m0,1024
s_add_u32 s69,s69,s53
buffer_load_dwordx4 v6,s[44:47],s69 offen lds
RB51_%=:
ds_read_u16_d16_hi v147,v147
v_lshl_add_u64 v[138:139],v[140:141],0,v[142:143]
v_mfma_f32_16x16x32_bf16 v[200:203],v[108:111],v[128:131],v[200:203]
ds_read_b128 v[112:115],v4 offset:256
ds_read_b64_tr_b16 v[68:69],v2 offset:0
v_lshlrev_b32_sdwa v149,1,v149 dst_sel:DWORD dst_unused:UNUSED_PAD src0_sel:DWORD src1_sel:WORD_1
v_mfma_f32_16x16x32_bf16 v[164:167],v[88:91],v[136:139],v[164:167]
ds_read_u16 v148,v148
v_lshlrev_b32_sdwa v151,1,v151 dst_sel:DWORD dst_unused:UNUSED_PAD src0_sel:DWORD src1_sel:WORD_1
v_mfma_f32_16x16x32_bf16 v[172:175],v[92:95],v[136:139],v[172:175]
ds_read_u16_d16_hi v150,v150
ds_read_b64_tr_b16 v[70:71],v2 offset:2048
v_lshlrev_b32_sdwa v152,1,v152 dst_sel:DWORD dst_unused:UNUSED_PAD src0_sel:DWORD src1_sel:WORD_1
v_mfma_f32_16x16x32_bf16 v[180:183],v[96:99],v[136:139],v[180:183]
ds_read_b128 v[116:119],v4 offset:288
ds_read_b64_tr_b16 v[72:73],v1 offset:4096
v_lshlrev_b32_sdwa v154,1,v154 dst_sel:DWORD dst_unused:UNUSED_PAD src0_sel:DWORD src1_sel:WORD_1
v_mfma_f32_16x16x32_bf16 v[188:191],v[100:103],v[136:139],v[188:191]
ds_read_u16 v149,v149
v_lshlrev_b32_sdwa v153,1,v153 dst_sel:DWORD dst_unused:UNUSED_PAD src0_sel:DWORD src1_sel:WORD_1
v_mfma_f32_16x16x32_bf16 v[196:199],v[104:107],v[136:139],v[196:199]
ds_read_u16_d16_hi v151,v151
v_lshlrev_b32_sdwa v155,1,v155 dst_sel:DWORD dst_unused:UNUSED_PAD src0_sel:DWORD src1_sel:WORD_1
v_mfma_f32_16x16x32_bf16 v[204:207],v[108:111],v[136:139],v[204:207]
s_waitcnt lgkmcnt(8)
ds_read_b64_tr_b16 v[74:75],v1 offset:6144
ds_read_b64_tr_b16 v[76:77],v2 offset:4096
v_lshlrev_b32_sdwa v156,1,v156 dst_sel:DWORD dst_unused:UNUSED_PAD src0_sel:DWORD src1_sel:WORD_1
v_mfma_f32_16x16x32_bf16 v[128:131],v[64:67],v[16:19],v[112:115]
ds_read_u16 v152,v152
v_lshlrev_b32_sdwa v158,1,v158 dst_sel:DWORD dst_unused:UNUSED_PAD src0_sel:DWORD src1_sel:WORD_1
v_mfma_f32_16x16x32_bf16 v[136:139],v[64:67],v[20:23],v[112:115]
s_waitcnt lgkmcnt(6)
ds_read_u16_d16_hi v154,v154
ds_read_b64_tr_b16 v[78:79],v2 offset:6144
v_lshlrev_b32_sdwa v157,1,v157 dst_sel:DWORD dst_unused:UNUSED_PAD src0_sel:DWORD src1_sel:WORD_1
v_mfma_f32_16x16x32_bf16 v[140:143],v[68:71],v[20:23],v[116:119]
ds_read_b64_tr_b16 v[80:81],v1 offset:8192
ds_read_b64_tr_b16 v[82:83],v1 offset:10240
v_lshlrev_b32_sdwa v159,1,v159 dst_sel:DWORD dst_unused:UNUSED_PAD src0_sel:DWORD src1_sel:WORD_1
v_mfma_f32_16x16x32_bf16 v[132:135],v[68:71],v[16:19],v[116:119]
s_waitcnt lgkmcnt(6)
ds_read_u16 v153,v153
v_mfma_f32_16x16x32_bf16 v[128:131],v[72:75],v[24:27],v[128:131]
ds_read_u16_d16_hi v155,v155
v_mfma_f32_16x16x32_bf16 v[136:139],v[72:75],v[28:31],v[136:139]
s_waitcnt lgkmcnt(4)
ds_read_b64_tr_b16 v[84:85],v2 offset:8192
ds_read_b64_tr_b16 v[86:87],v2 offset:10240
v_mfma_f32_16x16x32_bf16 v[140:143],v[76:79],v[28:31],v[140:143]
ds_read_u16 v156,v156
v_mfma_f32_16x16x32_bf16 v[132:135],v[76:79],v[24:27],v[132:135]
s_waitcnt lgkmcnt(5)
ds_read_u16_d16_hi v158,v158
v_lshl_add_u64 v[144:145],v[144:145],0,v[146:147]
v_mfma_f32_16x16x32_bf16 v[128:131],v[80:83],v[32:35],v[128:131]
v_lshl_add_u64 v[146:147],v[148:149],0,v[150:151]
v_mfma_f32_16x16x32_bf16 v[136:139],v[80:83],v[36:39],v[136:139]
s_waitcnt lgkmcnt(2)
ds_read_u16 v157,v157
v_mfma_f32_16x16x32_bf16 v[140:143],v[84:87],v[36:39],v[140:143]
ds_read_u16_d16_hi v159,v159
v_mfma_f32_16x16x32_bf16 v[132:135],v[84:87],v[32:35],v[132:135]
s_nop 0
v_lshlrev_b32_sdwa v128,1,v128 dst_sel:DWORD dst_unused:UNUSED_PAD src0_sel:DWORD src1_sel:WORD_1
v_lshlrev_b32_sdwa v130,1,v130 dst_sel:DWORD dst_unused:UNUSED_PAD src0_sel:DWORD src1_sel:WORD_1
v_mfma_f32_16x16x32_bf16 v[208:211],v[88:91],v[144:147],v[208:211]
ds_read_u16 v128,v128
v_lshlrev_b32_sdwa v129,1,v129 dst_sel:DWORD dst_unused:UNUSED_PAD src0_sel:DWORD src1_sel:WORD_1
v_mfma_f32_16x16x32_bf16 v[216:219],v[92:95],v[144:147],v[216:219]
ds_read_u16_d16_hi v130,v130
v_lshlrev_b32_sdwa v131,1,v131 dst_sel:DWORD dst_unused:UNUSED_PAD src0_sel:DWORD src1_sel:WORD_1
v_mfma_f32_16x16x32_bf16 v[224:227],v[96:99],v[144:147],v[224:227]
v_lshlrev_b32_sdwa v132,1,v132 dst_sel:DWORD dst_unused:UNUSED_PAD src0_sel:DWORD src1_sel:WORD_1
v_mfma_f32_16x16x32_bf16 v[232:235],v[100:103],v[144:147],v[232:235]
ds_read_u16 v129,v129
v_lshl_add_u64 v[152:153],v[152:153],0,v[154:155]
v_lshlrev_b32_sdwa v134,1,v134 dst_sel:DWORD dst_unused:UNUSED_PAD src0_sel:DWORD src1_sel:WORD_1
v_mfma_f32_16x16x32_bf16 v[240:243],v[104:107],v[144:147],v[240:243]
s_waitcnt lgkmcnt(3)
ds_read_u16_d16_hi v131,v131
v_lshl_add_u64 v[154:155],v[156:157],0,v[158:159]
v_mfma_f32_16x16x32_bf16 v[248:251],v[108:111],v[144:147],v[248:251]
v_lshlrev_b32_sdwa v133,1,v133 dst_sel:DWORD dst_unused:UNUSED_PAD src0_sel:DWORD src1_sel:WORD_1
v_mfma_f32_16x16x32_bf16 v[212:215],v[88:91],v[152:155],v[212:215]
ds_read_u16 v132,v132
v_lshlrev_b32_sdwa v135,1,v135 dst_sel:DWORD dst_unused:UNUSED_PAD src0_sel:DWORD src1_sel:WORD_1
v_mfma_f32_16x16x32_bf16 v[220:223],v[92:95],v[152:155],v[220:223]
ds_read_u16_d16_hi v134,v134
v_lshlrev_b32_sdwa v136,1,v136 dst_sel:DWORD dst_unused:UNUSED_PAD src0_sel:DWORD src1_sel:WORD_1
v_mfma_f32_16x16x32_bf16 v[228:231],v[96:99],v[152:155],v[228:231]
ds_read_b64_tr_b16 v[88:89],v3 offset:6160
ds_read_b64_tr_b16 v[90:91],v3 offset:7696
v_lshlrev_b32_sdwa v138,1,v138 dst_sel:DWORD dst_unused:UNUSED_PAD src0_sel:DWORD src1_sel:WORD_1
v_mfma_f32_16x16x32_bf16 v[236:239],v[100:103],v[152:155],v[236:239]
ds_read_u16 v133,v133
v_lshlrev_b32_sdwa v137,1,v137 dst_sel:DWORD dst_unused:UNUSED_PAD src0_sel:DWORD src1_sel:WORD_1
v_mfma_f32_16x16x32_bf16 v[244:247],v[104:107],v[152:155],v[244:247]
ds_read_u16_d16_hi v135,v135
v_lshlrev_b32_sdwa v139,1,v139 dst_sel:DWORD dst_unused:UNUSED_PAD src0_sel:DWORD src1_sel:WORD_1
v_mfma_f32_16x16x32_bf16 v[252:255],v[108:111],v[152:155],v[252:255]
ds_read_b64_tr_b16 v[92:93],v3 offset:6168
ds_read_b64_tr_b16 v[94:95],v3 offset:7704
v_lshlrev_b32_sdwa v140,1,v140 dst_sel:DWORD dst_unused:UNUSED_PAD src0_sel:DWORD src1_sel:WORD_1
v_mfma_f32_16x16x32_bf16 v[144:147],v[64:67],v[40:43],v[112:115]
ds_read_u16 v136,v136
v_lshlrev_b32_sdwa v142,1,v142 dst_sel:DWORD dst_unused:UNUSED_PAD src0_sel:DWORD src1_sel:WORD_1
v_mfma_f32_16x16x32_bf16 v[152:155],v[64:67],v[44:47],v[112:115]
ds_read_u16_d16_hi v138,v138
v_lshlrev_b32_sdwa v141,1,v141 dst_sel:DWORD dst_unused:UNUSED_PAD src0_sel:DWORD src1_sel:WORD_1
v_mfma_f32_16x16x32_bf16 v[156:159],v[68:71],v[44:47],v[116:119]
ds_read_b64_tr_b16 v[96:97],v3 offset:6224
ds_read_b64_tr_b16 v[98:99],v3 offset:7760
v_lshlrev_b32_sdwa v143,1,v143 dst_sel:DWORD dst_unused:UNUSED_PAD src0_sel:DWORD src1_sel:WORD_1
v_mfma_f32_16x16x32_bf16 v[148:151],v[68:71],v[40:43],v[116:119]
ds_read_u16 v137,v137
v_mfma_f32_16x16x32_bf16 v[144:147],v[72:75],v[48:51],v[144:147]
ds_read_u16_d16_hi v139,v139
v_mfma_f32_16x16x32_bf16 v[152:155],v[72:75],v[52:55],v[152:155]
ds_read_b64_tr_b16 v[100:101],v3 offset:6232
ds_read_b64_tr_b16 v[102:103],v3 offset:7768
v_mfma_f32_16x16x32_bf16 v[156:159],v[76:79],v[52:55],v[156:159]
ds_read_u16 v140,v140
v_mfma_f32_16x16x32_bf16 v[148:151],v[76:79],v[48:51],v[148:151]
s_waitcnt lgkmcnt(15)
ds_read_u16_d16_hi v142,v142
v_lshl_add_u64 v[128:129],v[128:129],0,v[130:131]
v_mfma_f32_16x16x32_bf16 v[144:147],v[80:83],v[56:59],v[144:147]
s_waitcnt lgkmcnt(12)
ds_read_b64_tr_b16 v[104:105],v3 offset:6288
ds_read_b64_tr_b16 v[106:107],v3 offset:7824
v_lshl_add_u64 v[130:131],v[132:133],0,v[134:135]
v_mfma_f32_16x16x32_bf16 v[152:155],v[80:83],v[60:63],v[152:155]
ds_read_u16 v141,v141
v_mfma_f32_16x16x32_bf16 v[156:159],v[84:87],v[60:63],v[156:159]
ds_read_u16_d16_hi v143,v143
v_mfma_f32_16x16x32_bf16 v[148:151],v[84:87],v[56:59],v[148:151]
ds_read_b64_tr_b16 v[108:109],v3 offset:6296
ds_read_b64_tr_b16 v[110:111],v3 offset:7832
v_lshlrev_b32_sdwa v144,1,v144 dst_sel:DWORD dst_unused:UNUSED_PAD src0_sel:DWORD src1_sel:WORD_1
v_lshlrev_b32_sdwa v146,1,v146 dst_sel:DWORD dst_unused:UNUSED_PAD src0_sel:DWORD src1_sel:WORD_1
v_mfma_f32_16x16x32_bf16 v[160:163],v[88:91],v[128:131],v[160:163]
s_waitcnt lgkmcnt(15)
ds_read_u16 v144,v144
v_lshlrev_b32_sdwa v145,1,v145 dst_sel:DWORD dst_unused:UNUSED_PAD src0_sel:DWORD src1_sel:WORD_1
v_mfma_f32_16x16x32_bf16 v[168:171],v[92:95],v[128:131],v[168:171]
s_waitcnt lgkmcnt(13)
ds_read_u16_d16_hi v146,v146
v_lshlrev_b32_sdwa v147,1,v147 dst_sel:DWORD dst_unused:UNUSED_PAD src0_sel:DWORD src1_sel:WORD_1
v_mfma_f32_16x16x32_bf16 v[176:179],v[96:99],v[128:131],v[176:179]
s_waitcnt lgkmcnt(10)
ds_read_b64_tr_b16 v[64:65],v1 offset:512
ds_read_b64_tr_b16 v[66:67],v1 offset:2560
v_lshlrev_b32_sdwa v148,1,v148 dst_sel:DWORD dst_unused:UNUSED_PAD src0_sel:DWORD src1_sel:WORD_1
v_mfma_f32_16x16x32_bf16 v[184:187],v[100:103],v[128:131],v[184:187]
s_waitcnt lgkmcnt(8)
ds_read_u16 v145,v145
v_lshl_add_u64 v[136:137],v[136:137],0,v[138:139]
v_lshlrev_b32_sdwa v150,1,v150 dst_sel:DWORD dst_unused:UNUSED_PAD src0_sel:DWORD src1_sel:WORD_1
v_mfma_f32_16x16x32_bf16 v[192:195],v[104:107],v[128:131],v[192:195]
s_waitcnt lgkmcnt(5)
ds_read_u16_d16_hi v147,v147
v_lshl_add_u64 v[138:139],v[140:141],0,v[142:143]
v_mfma_f32_16x16x32_bf16 v[200:203],v[108:111],v[128:131],v[200:203]
ds_read_b128 v[112:115],v4 offset:384
ds_read_b64_tr_b16 v[68:69],v2 offset:512
v_lshlrev_b32_sdwa v149,1,v149 dst_sel:DWORD dst_unused:UNUSED_PAD src0_sel:DWORD src1_sel:WORD_1
v_mfma_f32_16x16x32_bf16 v[164:167],v[88:91],v[136:139],v[164:167]
ds_read_u16 v148,v148
v_lshlrev_b32_sdwa v151,1,v151 dst_sel:DWORD dst_unused:UNUSED_PAD src0_sel:DWORD src1_sel:WORD_1
v_mfma_f32_16x16x32_bf16 v[172:175],v[92:95],v[136:139],v[172:175]
ds_read_u16_d16_hi v150,v150
ds_read_b64_tr_b16 v[70:71],v2 offset:2560
v_lshlrev_b32_sdwa v152,1,v152 dst_sel:DWORD dst_unused:UNUSED_PAD src0_sel:DWORD src1_sel:WORD_1
v_mfma_f32_16x16x32_bf16 v[180:183],v[96:99],v[136:139],v[180:183]
ds_read_b128 v[116:119],v4 offset:416
ds_read_b64_tr_b16 v[72:73],v1 offset:4608
v_lshlrev_b32_sdwa v154,1,v154 dst_sel:DWORD dst_unused:UNUSED_PAD src0_sel:DWORD src1_sel:WORD_1
v_mfma_f32_16x16x32_bf16 v[188:191],v[100:103],v[136:139],v[188:191]
ds_read_u16 v149,v149
v_lshlrev_b32_sdwa v153,1,v153 dst_sel:DWORD dst_unused:UNUSED_PAD src0_sel:DWORD src1_sel:WORD_1
v_mfma_f32_16x16x32_bf16 v[196:199],v[104:107],v[136:139],v[196:199]
ds_read_u16_d16_hi v151,v151
v_lshlrev_b32_sdwa v155,1,v155 dst_sel:DWORD dst_unused:UNUSED_PAD src0_sel:DWORD src1_sel:WORD_1
v_mfma_f32_16x16x32_bf16 v[204:207],v[108:111],v[136:139],v[204:207]
s_waitcnt lgkmcnt(8)
ds_read_b64_tr_b16 v[74:75],v1 offset:6656
ds_read_b64_tr_b16 v[76:77],v2 offset:4608
v_lshlrev_b32_sdwa v156,1,v156 dst_sel:DWORD dst_unused:UNUSED_PAD src0_sel:DWORD src1_sel:WORD_1
v_mfma_f32_16x16x32_bf16 v[128:131],v[64:67],v[16:19],v[112:115]
ds_read_u16 v152,v152
v_lshlrev_b32_sdwa v158,1,v158 dst_sel:DWORD dst_unused:UNUSED_PAD src0_sel:DWORD src1_sel:WORD_1
v_mfma_f32_16x16x32_bf16 v[136:139],v[64:67],v[20:23],v[112:115]
s_waitcnt lgkmcnt(6)
ds_read_u16_d16_hi v154,v154
ds_read_b64_tr_b16 v[78:79],v2 offset:6656
v_lshlrev_b32_sdwa v157,1,v157 dst_sel:DWORD dst_unused:UNUSED_PAD src0_sel:DWORD src1_sel:WORD_1
v_mfma_f32_16x16x32_bf16 v[140:143],v[68:71],v[20:23],v[116:119]
ds_read_b64_tr_b16 v[80:81],v1 offset:8704
ds_read_b64_tr_b16 v[82:83],v1 offset:10752
v_lshlrev_b32_sdwa v159,1,v159 dst_sel:DWORD dst_unused:UNUSED_PAD src0_sel:DWORD src1_sel:WORD_1
v_mfma_f32_16x16x32_bf16 v[132:135],v[68:71],v[16:19],v[116:119]
s_waitcnt lgkmcnt(6)
ds_read_u16 v153,v153
v_mfma_f32_16x16x32_bf16 v[128:131],v[72:75],v[24:27],v[128:131]
ds_read_u16_d16_hi v155,v155
v_mfma_f32_16x16x32_bf16 v[136:139],v[72:75],v[28:31],v[136:139]
s_waitcnt lgkmcnt(4)
ds_read_b64_tr_b16 v[84:85],v2 offset:8704
ds_read_b64_tr_b16 v[86:87],v2 offset:10752
v_mfma_f32_16x16x32_bf16 v[140:143],v[76:79],v[28:31],v[140:143]
ds_read_u16 v156,v156
v_mfma_f32_16x16x32_bf16 v[132:135],v[76:79],v[24:27],v[132:135]
s_waitcnt lgkmcnt(5)
ds_read_u16_d16_hi v158,v158
v_lshl_add_u64 v[144:145],v[144:145],0,v[146:147]
v_mfma_f32_16x16x32_bf16 v[128:131],v[80:83],v[32:35],v[128:131]
v_lshl_add_u64 v[146:147],v[148:149],0,v[150:151]
v_mfma_f32_16x16x32_bf16 v[136:139],v[80:83],v[36:39],v[136:139]
s_waitcnt lgkmcnt(2)
ds_read_u16 v157,v157
v_mfma_f32_16x16x32_bf16 v[140:143],v[84:87],v[36:39],v[140:143]
ds_read_u16_d16_hi v159,v159
v_mfma_f32_16x16x32_bf16 v[132:135],v[84:87],v[32:35],v[132:135]
s_nop 0
v_lshlrev_b32_sdwa v128,1,v128 dst_sel:DWORD dst_unused:UNUSED_PAD src0_sel:DWORD src1_sel:WORD_1
v_lshlrev_b32_sdwa v130,1,v130 dst_sel:DWORD dst_unused:UNUSED_PAD src0_sel:DWORD src1_sel:WORD_1
v_mfma_f32_16x16x32_bf16 v[208:211],v[88:91],v[144:147],v[208:211]
s_waitcnt vmcnt(0)
s_barrier
s_setprio 3
ds_read_u16 v128,v128
v_lshlrev_b32_sdwa v129,1,v129 dst_sel:DWORD dst_unused:UNUSED_PAD src0_sel:DWORD src1_sel:WORD_1
v_lshlrev_b32_sdwa v131,1,v131 dst_sel:DWORD dst_unused:UNUSED_PAD src0_sel:DWORD src1_sel:WORD_1
v_mfma_f32_16x16x32_bf16 v[216:219],v[92:95],v[144:147],v[216:219]
v_fma_mixlo_f16 v120,v140,s99,0
v_fma_mixlo_f16 v122,v141,s99,0
v_mfma_f32_16x16x32_bf16 v[224:227],v[96:99],v[144:147],v[224:227]
s_cbranch_vccz RB52_%=
s_add_u32 m0,s58,0
s_mul_i32 s69,s59,5
s_add_u32 s69,s69,s97
buffer_load_dwordx4 v5,s[44:47],s69 offen lds
s_add_u32 m0,m0,1024
s_add_u32 s69,s69,s53
buffer_load_dwordx4 v6,s[44:47],s69 offen lds
RB52_%=:
ds_read_u16_d16_hi v130,v130
v_lshlrev_b32_sdwa v132,1,v132 dst_sel:DWORD dst_unused:UNUSED_PAD src0_sel:DWORD src1_sel:WORD_1
v_lshlrev_b32_sdwa v134,1,v134 dst_sel:DWORD dst_unused:UNUSED_PAD src0_sel:DWORD src1_sel:WORD_1
v_mfma_f32_16x16x32_bf16 v[232:235],v[100:103],v[144:147],v[232:235]
v_lshl_add_u64 v[152:153],v[152:153],0,v[154:155]
v_mfma_f32_16x16x32_bf16 v[240:243],v[104:107],v[144:147],v[240:243]
s_waitcnt lgkmcnt(2)
ds_read_u16 v129,v129
v_lshl_add_u64 v[154:155],v[156:157],0,v[158:159]
v_mfma_f32_16x16x32_bf16 v[248:251],v[108:111],v[144:147],v[248:251]
v_fma_mixhi_f16 v120,v142,s99,0
v_fma_mixhi_f16 v122,v143,s99,0
v_mfma_f32_16x16x32_bf16 v[212:215],v[88:91],v[152:155],v[212:215]
s_cbranch_vccz RB53_%=
s_add_u32 m0,m0,1024
s_add_u32 s69,s69,s53
buffer_load_dwordx4 v5,s[44:47],s69 offen lds
s_add_u32 m0,m0,s50
s_add_u32 s69,s69,s53
buffer_load_dwordx4 v6,s[44:47],s69 offen lds
RB53_%=:
ds_read_u16_d16_hi v131,v131
v_lshlrev_b32_sdwa v133,1,v133 dst_sel:DWORD dst_unused:UNUSED_PAD src0_sel:DWORD src1_sel:WORD_1
v_lshlrev_b32_sdwa v135,1,v135 dst_sel:DWORD dst_unused:UNUSED_PAD src0_sel:DWORD src1_sel:WORD_1
v_mfma_f32_16x16x32_bf16 v[220:223],v[92:95],v[152:155],v[220:223]
ds_read_b64_tr_b16 v[88:89],v3 offset:12320
v_pk_fma_f16 v121,v120,v120,s54
v_pk_fma_f16 v123,v122,v122,s54
v_mfma_f32_16x16x32_bf16 v[228:231],v[96:99],v[152:155],v[228:231]
ds_read_u16 v132,v132
v_pk_mul_f16 v121,v120,v121
v_pk_mul_f16 v123,v122,v123
v_mfma_f32_16x16x32_bf16 v[236:239],v[100:103],v[152:155],v[236:239]
ds_read_b64_tr_b16 v[90:91],v3 offset:13856
v_lshlrev_b32_sdwa v136,1,v136 dst_sel:DWORD dst_unused:UNUSED_PAD src0_sel:DWORD src1_sel:WORD_1
v_lshlrev_b32_sdwa v138,1,v138 dst_sel:DWORD dst_unused:UNUSED_PAD src0_sel:DWORD src1_sel:WORD_1
v_mfma_f32_16x16x32_bf16 v[244:247],v[104:107],v[152:155],v[244:247]
s_cbranch_vccz RB54_%=
s_add_u32 m0,m0,1024
s_add_u32 s69,s69,s53
buffer_load_dwordx4 v5,s[44:47],s69 offen lds
s_add_u32 m0,m0,1024
s_add_u32 s69,s69,s53
buffer_load_dwordx4 v6,s[44:47],s69 offen lds
RB54_%=:
ds_read_u16_d16_hi v134,v134
v_exp_f16 v120,v121
v_mfma_f32_16x16x32_bf16 v[252:255],v[108:111],v[152:155],v[252:255]
ds_read_b64_tr_b16 v[92:93],v3 offset:12328
v_exp_f16 v122,v123
v_mfma_f32_16x16x32_bf16 v[144:147],v[64:67],v[40:43],v[112:115]
ds_read_u16 v133,v133
v_lshlrev_b32_sdwa v137,1,v137 dst_sel:DWORD dst_unused:UNUSED_PAD src0_sel:DWORD src1_sel:WORD_1
v_lshlrev_b32_sdwa v139,1,v139 dst_sel:DWORD dst_unused:UNUSED_PAD src0_sel:DWORD src1_sel:WORD_1
v_mfma_f32_16x16x32_bf16 v[152:155],v[64:67],v[44:47],v[112:115]
s_cbranch_vccz RB55_%=
s_add_u32 m0,m0,s50
s_add_u32 s69,s69,s53
buffer_load_dwordx4 v5,s[44:47],s69 offen lds
s_add_u32 m0,m0,1024
s_add_u32 s69,s69,s53
buffer_load_dwordx4 v6,s[44:47],s69 offen lds
RB55_%=:
ds_read_b64_tr_b16 v[94:95],v3 offset:13864
v_exp_f16_sdwa v120,v121 dst_sel:WORD_1 dst_unused:UNUSED_PRESERVE src0_sel:WORD_1
v_mfma_f32_16x16x32_bf16 v[156:159],v[68:71],v[44:47],v[116:119]
ds_read_u16_d16_hi v135,v135
v_exp_f16_sdwa v122,v123 dst_sel:WORD_1 dst_unused:UNUSED_PRESERVE src0_sel:WORD_1
v_mfma_f32_16x16x32_bf16 v[148:151],v[68:71],v[40:43],v[116:119]
ds_read_b64_tr_b16 v[96:97],v3 offset:12384
v_pk_fma_f16 v120,v120,s55,s55
v_pk_fma_f16 v122,v122,s55,s55
v_mfma_f32_16x16x32_bf16 v[144:147],v[72:75],v[48:51],v[144:147]
ds_read_u16 v136,v136
v_rcp_f16 v121,v120
v_mfma_f32_16x16x32_bf16 v[152:155],v[72:75],v[52:55],v[152:155]
s_cbranch_vccz RB56_%=
s_add_u32 m0,m0,1024
s_add_u32 s69,s69,s53
buffer_load_dwordx4 v5,s[44:47],s69 offen lds
s_add_u32 m0,m0,s50
s_add_u32 s69,s69,s53
buffer_load_dwordx4 v6,s[44:47],s69 offen lds
RB56_%=:
ds_read_b64_tr_b16 v[98:99],v3 offset:13920
ds_read_b64_tr_b16 v[100:101],v3 offset:12392
v_rcp_f16 v123,v122
v_mfma_f32_16x16x32_bf16 v[156:159],v[76:79],v[52:55],v[156:159]
ds_read_u16_d16_hi v138,v138
v_rcp_f16_sdwa v121,v120 dst_sel:WORD_1 dst_unused:UNUSED_PRESERVE src0_sel:WORD_1
v_mfma_f32_16x16x32_bf16 v[148:151],v[76:79],v[48:51],v[148:151]
ds_read_b64_tr_b16 v[102:103],v3 offset:13928
ds_read_b64_tr_b16 v[104:105],v3 offset:12448
v_rcp_f16_sdwa v123,v122 dst_sel:WORD_1 dst_unused:UNUSED_PRESERVE src0_sel:WORD_1
v_mfma_f32_16x16x32_bf16 v[144:147],v[80:83],v[56:59],v[144:147]
ds_read_u16 v137,v137
v_fma_mix_f32 v140,v140,v121,0 op_sel_hi:[0,1,0]
v_fma_mix_f32 v142,v142,v121,0 op_sel:[0,1,0] op_sel_hi:[0,1,0]
v_mfma_f32_16x16x32_bf16 v[152:155],v[80:83],v[60:63],v[152:155]
s_waitcnt lgkmcnt(8)
s_cbranch_vccz RB57_%=
s_add_u32 m0,m0,1024
s_add_u32 s69,s69,s53
buffer_load_dwordx4 v5,s[44:47],s69 offen lds
s_add_u32 m0,m0,1024
s_add_u32 s69,s69,s53
buffer_load_dwordx4 v6,s[44:47],s69 offen lds
RB57_%=:
ds_read_b64_tr_b16 v[106:107],v3 offset:13984
ds_read_b64_tr_b16 v[108:109],v3 offset:12456
v_lshl_add_u64 v[128:129],v[128:129],0,v[130:131]
v_lshl_add_u64 v[130:131],v[132:133],0,v[134:135]
v_mfma_f32_16x16x32_bf16 v[156:159],v[84:87],v[60:63],v[156:159]
ds_read_u16_d16_hi v139,v139
v_fma_mix_f32 v141,v141,v123,0 op_sel_hi:[0,1,0]
v_fma_mix_f32 v143,v143,v123,0 op_sel:[0,1,0] op_sel_hi:[0,1,0]
v_mfma_f32_16x16x32_bf16 v[148:151],v[84:87],v[56:59],v[148:151]
ds_read_b64_tr_b16 v[110:111],v3 offset:13992
ds_read_b64_tr_b16 v[64:65],v1 offset:12320
v_lshlrev_b32_sdwa v144,1,v144 dst_sel:DWORD dst_unused:UNUSED_PAD src0_sel:DWORD src1_sel:WORD_1
v_lshlrev_b32_sdwa v146,1,v146 dst_sel:DWORD dst_unused:UNUSED_PAD src0_sel:DWORD src1_sel:WORD_1
v_mfma_f32_16x16x32_bf16 v[160:163],v[88:91],v[128:131],v[160:163]
ds_read_u16 v144,v144
v_lshlrev_b32_sdwa v145,1,v145 dst_sel:DWORD dst_unused:UNUSED_PAD src0_sel:DWORD src1_sel:WORD_1
v_lshlrev_b32_sdwa v147,1,v147 dst_sel:DWORD dst_unused:UNUSED_PAD src0_sel:DWORD src1_sel:WORD_1
v_mfma_f32_16x16x32_bf16 v[168:171],v[92:95],v[128:131],v[168:171]
s_waitcnt lgkmcnt(11)
ds_read_b64_tr_b16 v[66:67],v1 offset:14368
ds_read_b128 v[112:115],v4 offset:512
v_fma_mixlo_f16 v120,v156,s99,0
v_fma_mixlo_f16 v122,v157,s99,0
v_mfma_f32_16x16x32_bf16 v[176:179],v[96:99],v[128:131],v[176:179]
s_waitcnt lgkmcnt(10)
ds_read_u16_d16_hi v146,v146
v_lshlrev_b32_sdwa v148,1,v148 dst_sel:DWORD dst_unused:UNUSED_PAD src0_sel:DWORD src1_sel:WORD_1
v_lshlrev_b32_sdwa v150,1,v150 dst_sel:DWORD dst_unused:UNUSED_PAD src0_sel:DWORD src1_sel:WORD_1
v_mfma_f32_16x16x32_bf16 v[184:187],v[100:103],v[128:131],v[184:187]
s_waitcnt lgkmcnt(6)
ds_read_b64_tr_b16 v[68:69],v2 offset:12320
ds_read_b64_tr_b16 v[70:71],v2 offset:14368
v_lshl_add_u64 v[136:137],v[136:137],0,v[138:139]
v_mfma_f32_16x16x32_bf16 v[192:195],v[104:107],v[128:131],v[192:195]
s_waitcnt lgkmcnt(7)
ds_read_u16 v145,v145
v_cvt_pk_bf16_f32 v138,v140,v142
v_cvt_pk_bf16_f32 v139,v141,v143
v_mfma_f32_16x16x32_bf16 v[200:203],v[108:111],v[128:131],v[200:203]
ds_read_b128 v[116:119],v4 offset:544
ds_read_b64_tr_b16 v[72:73],v1 offset:16416
v_fma_mixhi_f16 v120,v158,s99,0
v_fma_mixhi_f16 v122,v159,s99,0
v_mfma_f32_16x16x32_bf16 v[164:167],v[88:91],v[136:139],v[164:167]
ds_read_u16_d16_hi v147,v147
v_lshlrev_b32_sdwa v149,1,v149 dst_sel:DWORD dst_unused:UNUSED_PAD src0_sel:DWORD src1_sel:WORD_1
v_lshlrev_b32_sdwa v151,1,v151 dst_sel:DWORD dst_unused:UNUSED_PAD src0_sel:DWORD src1_sel:WORD_1
v_mfma_f32_16x16x32_bf16 v[172:175],v[92:95],v[136:139],v[172:175]
ds_read_b64_tr_b16 v[74:75],v1 offset:18464
ds_read_b64_tr_b16 v[76:77],v2 offset:16416
v_pk_fma_f16 v121,v120,v120,s54
v_pk_fma_f16 v123,v122,v122,s54
v_mfma_f32_16x16x32_bf16 v[180:183],v[96:99],v[136:139],v[180:183]
ds_read_u16 v148,v148
v_pk_mul_f16 v121,v120,v121
v_pk_mul_f16 v123,v122,v123
v_mfma_f32_16x16x32_bf16 v[188:191],v[100:103],v[136:139],v[188:191]
ds_read_b64_tr_b16 v[78:79],v2 offset:18464
v_lshlrev_b32_sdwa v152,1,v152 dst_sel:DWORD dst_unused:UNUSED_PAD src0_sel:DWORD src1_sel:WORD_1
v_lshlrev_b32_sdwa v154,1,v154 dst_sel:DWORD dst_unused:UNUSED_PAD src0_sel:DWORD src1_sel:WORD_1
v_mfma_f32_16x16x32_bf16 v[196:199],v[104:107],v[136:139],v[196:199]
ds_read_u16_d16_hi v150,v150
v_exp_f16 v120,v121
v_mfma_f32_16x16x32_bf16 v[204:207],v[108:111],v[136:139],v[204:207]
s_waitcnt lgkmcnt(12)
ds_read_b64_tr_b16 v[80:81],v1 offset:20512
v_exp_f16 v122,v123
v_mfma_f32_16x16x32_bf16 v[128:131],v[64:67],v[16:19],v[112:115]
ds_read_u16 v149,v149
v_lshlrev_b32_sdwa v153,1,v153 dst_sel:DWORD dst_unused:UNUSED_PAD src0_sel:DWORD src1_sel:WORD_1
v_lshlrev_b32_sdwa v155,1,v155 dst_sel:DWORD dst_unused:UNUSED_PAD src0_sel:DWORD src1_sel:WORD_1
v_mfma_f32_16x16x32_bf16 v[136:139],v[64:67],v[20:23],v[112:115]
s_waitcnt lgkmcnt(9)
ds_read_b64_tr_b16 v[82:83],v1 offset:22560
v_exp_f16_sdwa v120,v121 dst_sel:WORD_1 dst_unused:UNUSED_PRESERVE src0_sel:WORD_1
v_mfma_f32_16x16x32_bf16 v[140:143],v[68:71],v[20:23],v[116:119]
ds_read_u16_d16_hi v151,v151
v_exp_f16_sdwa v122,v123 dst_sel:WORD_1 dst_unused:UNUSED_PRESERVE src0_sel:WORD_1
v_mfma_f32_16x16x32_bf16 v[132:135],v[68:71],v[16:19],v[116:119]
s_waitcnt lgkmcnt(8)
ds_read_b64_tr_b16 v[84:85],v2 offset:20512
v_pk_fma_f16 v120,v120,s55,s55
v_pk_fma_f16 v122,v122,s55,s55
v_mfma_f32_16x16x32_bf16 v[128:131],v[72:75],v[24:27],v[128:131]
ds_read_u16 v152,v152
v_rcp_f16 v121,v120
s_nop 0
s_mov_b32 s86,0xbe801d5a
v_mfma_f32_16x16x32_bf16 v[136:139],v[72:75],v[28:31],v[136:139]
s_waitcnt lgkmcnt(7)
ds_read_b64_tr_b16 v[86:87],v2 offset:22560
v_rcp_f16 v123,v122
v_mfma_f32_16x16x32_bf16 v[140:143],v[76:79],v[28:31],v[140:143]
ds_read_u16_d16_hi v154,v154
v_rcp_f16_sdwa v121,v120 dst_sel:WORD_1 dst_unused:UNUSED_PRESERVE src0_sel:WORD_1
v_mfma_f32_16x16x32_bf16 v[132:135],v[76:79],v[24:27],v[132:135]
s_waitcnt lgkmcnt(5)
v_rcp_f16_sdwa v123,v122 dst_sel:WORD_1 dst_unused:UNUSED_PRESERVE src0_sel:WORD_1
v_mfma_f32_16x16x32_bf16 v[128:131],v[80:83],v[32:35],v[128:131]
ds_read_u16 v153,v153
v_fma_mix_f32 v156,v156,v121,0 op_sel_hi:[0,1,0]
v_fma_mix_f32 v158,v158,v121,0 op_sel:[0,1,0] op_sel_hi:[0,1,0]
v_mfma_f32_16x16x32_bf16 v[136:139],v[80:83],v[36:39],v[136:139]
s_waitcnt lgkmcnt(2)
v_lshl_add_u64 v[144:145],v[144:145],0,v[146:147]
v_lshl_add_u64 v[146:147],v[148:149],0,v[150:151]
v_mfma_f32_16x16x32_bf16 v[140:143],v[84:87],v[36:39],v[140:143]
ds_read_u16_d16_hi v155,v155
v_fma_mix_f32 v157,v157,v123,0 op_sel_hi:[0,1,0]
v_fma_mix_f32 v159,v159,v123,0 op_sel:[0,1,0] op_sel_hi:[0,1,0]
v_mfma_f32_16x16x32_bf16 v[132:135],v[84:87],v[32:35],v[132:135]
v_lshlrev_b32_sdwa v128,1,v128 dst_sel:DWORD dst_unused:UNUSED_PAD src0_sel:DWORD src1_sel:WORD_1
v_lshlrev_b32_sdwa v130,1,v130 dst_sel:DWORD dst_unused:UNUSED_PAD src0_sel:DWORD src1_sel:WORD_1
v_mfma_f32_16x16x32_bf16 v[208:211],v[88:91],v[144:147],v[208:211]
ds_read_u16 v128,v128
v_lshlrev_b32_sdwa v129,1,v129 dst_sel:DWORD dst_unused:UNUSED_PAD src0_sel:DWORD src1_sel:WORD_1
v_lshlrev_b32_sdwa v131,1,v131 dst_sel:DWORD dst_unused:UNUSED_PAD src0_sel:DWORD src1_sel:WORD_1
v_mfma_f32_16x16x32_bf16 v[216:219],v[92:95],v[144:147],v[216:219]
v_fma_mixlo_f16 v120,v140,s99,0
v_fma_mixlo_f16 v122,v141,s99,0
v_mfma_f32_16x16x32_bf16 v[224:227],v[96:99],v[144:147],v[224:227]
ds_read_u16_d16_hi v130,v130
v_lshlrev_b32_sdwa v132,1,v132 dst_sel:DWORD dst_unused:UNUSED_PAD src0_sel:DWORD src1_sel:WORD_1
v_lshlrev_b32_sdwa v134,1,v134 dst_sel:DWORD dst_unused:UNUSED_PAD src0_sel:DWORD src1_sel:WORD_1
v_mfma_f32_16x16x32_bf16 v[232:235],v[100:103],v[144:147],v[232:235]
s_waitcnt lgkmcnt(2)
v_lshl_add_u64 v[152:153],v[152:153],0,v[154:155]
v_mfma_f32_16x16x32_bf16 v[240:243],v[104:107],v[144:147],v[240:243]
ds_read_u16 v129,v129
v_cvt_pk_bf16_f32 v154,v156,v158
v_cvt_pk_bf16_f32 v155,v157,v159
v_mfma_f32_16x16x32_bf16 v[248:251],v[108:111],v[144:147],v[248:251]
v_fma_mixhi_f16 v120,v142,s99,0
v_fma_mixhi_f16 v122,v143,s99,0
v_mfma_f32_16x16x32_bf16 v[212:215],v[88:91],v[152:155],v[212:215]
s_setprio 2
ds_read_u16_d16_hi v131,v131
v_lshlrev_b32_sdwa v133,1,v133 dst_sel:DWORD dst_unused:UNUSED_PAD src0_sel:DWORD src1_sel:WORD_1
v_lshlrev_b32_sdwa v135,1,v135 dst_sel:DWORD dst_unused:UNUSED_PAD src0_sel:DWORD src1_sel:WORD_1
v_mfma_f32_16x16x32_bf16 v[220:223],v[92:95],v[152:155],v[220:223]
ds_read_b64_tr_b16 v[88:89],v3 offset:18480
v_pk_fma_f16 v121,v120,v120,s54
v_pk_fma_f16 v123,v122,v122,s54
v_mfma_f32_16x16x32_bf16 v[228:231],v[96:99],v[152:155],v[228:231]
ds_read_u16 v132,v132
v_pk_mul_f16 v121,v120,v121
v_pk_mul_f16 v123,v122,v123
v_mfma_f32_16x16x32_bf16 v[236:239],v[100:103],v[152:155],v[236:239]
ds_read_b64_tr_b16 v[90:91],v3 offset:20016
v_lshlrev_b32_sdwa v136,1,v136 dst_sel:DWORD dst_unused:UNUSED_PAD src0_sel:DWORD src1_sel:WORD_1
v_lshlrev_b32_sdwa v138,1,v138 dst_sel:DWORD dst_unused:UNUSED_PAD src0_sel:DWORD src1_sel:WORD_1
v_mfma_f32_16x16x32_bf16 v[244:247],v[104:107],v[152:155],v[244:247]
ds_read_u16_d16_hi v134,v134
v_exp_f16 v120,v121
v_mfma_f32_16x16x32_bf16 v[252:255],v[108:111],v[152:155],v[252:255]
ds_read_b64_tr_b16 v[92:93],v3 offset:18488
v_exp_f16 v122,v123
v_mfma_f32_16x16x32_bf16 v[144:147],v[64:67],v[40:43],v[112:115]
ds_read_u16 v133,v133
v_lshlrev_b32_sdwa v137,1,v137 dst_sel:DWORD dst_unused:UNUSED_PAD src0_sel:DWORD src1_sel:WORD_1
v_lshlrev_b32_sdwa v139,1,v139 dst_sel:DWORD dst_unused:UNUSED_PAD src0_sel:DWORD src1_sel:WORD_1
v_mfma_f32_16x16x32_bf16 v[152:155],v[64:67],v[44:47],v[112:115]
ds_read_b64_tr_b16 v[94:95],v3 offset:20024
v_exp_f16_sdwa v120,v121 dst_sel:WORD_1 dst_unused:UNUSED_PRESERVE src0_sel:WORD_1
v_mfma_f32_16x16x32_bf16 v[156:159],v[68:71],v[44:47],v[116:119]
ds_read_u16_d16_hi v135,v135
v_exp_f16_sdwa v122,v123 dst_sel:WORD_1 dst_unused:UNUSED_PRESERVE src0_sel:WORD_1
v_mfma_f32_16x16x32_bf16 v[148:151],v[68:71],v[40:43],v[116:119]
ds_read_b64_tr_b16 v[96:97],v3 offset:18544
v_pk_fma_f16 v120,v120,s55,s55
v_pk_fma_f16 v122,v122,s55,s55
v_mfma_f32_16x16x32_bf16 v[144:147],v[72:75],v[48:51],v[144:147]
ds_read_u16 v136,v136
v_rcp_f16 v121,v120
v_mfma_f32_16x16x32_bf16 v[152:155],v[72:75],v[52:55],v[152:155]
ds_read_b64_tr_b16 v[98:99],v3 offset:20080
ds_read_b64_tr_b16 v[100:101],v3 offset:18552
v_rcp_f16 v123,v122
v_mfma_f32_16x16x32_bf16 v[156:159],v[76:79],v[52:55],v[156:159]
ds_read_u16_d16_hi v138,v138
v_rcp_f16_sdwa v121,v120 dst_sel:WORD_1 dst_unused:UNUSED_PRESERVE src0_sel:WORD_1
v_mfma_f32_16x16x32_bf16 v[148:151],v[76:79],v[48:51],v[148:151]
ds_read_b64_tr_b16 v[102:103],v3 offset:20088
ds_read_b64_tr_b16 v[104:105],v3 offset:18608
v_rcp_f16_sdwa v123,v122 dst_sel:WORD_1 dst_unused:UNUSED_PRESERVE src0_sel:WORD_1
v_mfma_f32_16x16x32_bf16 v[144:147],v[80:83],v[56:59],v[144:147]
ds_read_u16 v137,v137
v_fma_mix_f32 v140,v140,v121,0 op_sel_hi:[0,1,0]
v_fma_mix_f32 v142,v142,v121,0 op_sel:[0,1,0] op_sel_hi:[0,1,0]
v_mfma_f32_16x16x32_bf16 v[152:155],v[80:83],v[60:63],v[152:155]
s_waitcnt lgkmcnt(8)
ds_read_b64_tr_b16 v[106:107],v3 offset:20144
ds_read_b64_tr_b16 v[108:109],v3 offset:18616
v_lshl_add_u64 v[128:129],v[128:129],0,v[130:131]
v_lshl_add_u64 v[130:131],v[132:133],0,v[134:135]
v_mfma_f32_16x16x32_bf16 v[156:159],v[84:87],v[60:63],v[156:159]
ds_read_u16_d16_hi v139,v139
v_fma_mix_f32 v141,v141,v123,0 op_sel_hi:[0,1,0]
v_fma_mix_f32 v143,v143,v123,0 op_sel:[0,1,0] op_sel_hi:[0,1,0]
v_mfma_f32_16x16x32_bf16 v[148:151],v[84:87],v[56:59],v[148:151]
ds_read_b64_tr_b16 v[110:111],v3 offset:20152
ds_read_b64_tr_b16 v[64:65],v1 offset:12832
v_lshlrev_b32_sdwa v144,1,v144 dst_sel:DWORD dst_unused:UNUSED_PAD src0_sel:DWORD src1_sel:WORD_1
v_lshlrev_b32_sdwa v146,1,v146 dst_sel:DWORD dst_unused:UNUSED_PAD src0_sel:DWORD src1_sel:WORD_1
v_mfma_f32_16x16x32_bf16 v[160:163],v[88:91],v[128:131],v[160:163]
ds_read_u16 v144,v144
v_lshlrev_b32_sdwa v145,1,v145 dst_sel:DWORD dst_unused:UNUSED_PAD src0_sel:DWORD src1_sel:WORD_1
v_lshlrev_b32_sdwa v147,1,v147 dst_sel:DWORD dst_unused:UNUSED_PAD src0_sel:DWORD src1_sel:WORD_1
v_mfma_f32_16x16x32_bf16 v[168:171],v[92:95],v[128:131],v[168:171]
s_waitcnt lgkmcnt(11)
ds_read_b64_tr_b16 v[66:67],v1 offset:14880
ds_read_b128 v[112:115],v4 offset:640
v_fma_mixlo_f16 v120,v156,s99,0
v_fma_mixlo_f16 v122,v157,s99,0
v_mfma_f32_16x16x32_bf16 v[176:179],v[96:99],v[128:131],v[176:179]
s_waitcnt lgkmcnt(10)
ds_read_u16_d16_hi v146,v146
v_lshlrev_b32_sdwa v148,1,v148 dst_sel:DWORD dst_unused:UNUSED_PAD src0_sel:DWORD src1_sel:WORD_1
v_lshlrev_b32_sdwa v150,1,v150 dst_sel:DWORD dst_unused:UNUSED_PAD src0_sel:DWORD src1_sel:WORD_1
v_mfma_f32_16x16x32_bf16 v[184:187],v[100:103],v[128:131],v[184:187]
s_waitcnt lgkmcnt(6)
ds_read_b64_tr_b16 v[68:69],v2 offset:12832
ds_read_b64_tr_b16 v[70:71],v2 offset:14880
v_lshl_add_u64 v[136:137],v[136:137],0,v[138:139]
v_mfma_f32_16x16x32_bf16 v[192:195],v[104:107],v[128:131],v[192:195]
s_waitcnt lgkmcnt(7)
ds_read_u16 v145,v145
v_cvt_pk_bf16_f32 v138,v140,v142
v_cvt_pk_bf16_f32 v139,v141,v143
v_mfma_f32_16x16x32_bf16 v[200:203],v[108:111],v[128:131],v[200:203]
ds_read_b128 v[116:119],v4 offset:672
ds_read_b64_tr_b16 v[72:73],v1 offset:16928
v_fma_mixhi_f16 v120,v158,s99,0
v_fma_mixhi_f16 v122,v159,s99,0
v_mfma_f32_16x16x32_bf16 v[164:167],v[88:91],v[136:139],v[164:167]
ds_read_u16_d16_hi v147,v147
v_lshlrev_b32_sdwa v149,1,v149 dst_sel:DWORD dst_unused:UNUSED_PAD src0_sel:DWORD src1_sel:WORD_1
v_lshlrev_b32_sdwa v151,1,v151 dst_sel:DWORD dst_unused:UNUSED_PAD src0_sel:DWORD src1_sel:WORD_1
v_mfma_f32_16x16x32_bf16 v[172:175],v[92:95],v[136:139],v[172:175]
s_setprio 1
ds_read_b64_tr_b16 v[74:75],v1 offset:18976
ds_read_b64_tr_b16 v[76:77],v2 offset:16928
v_pk_fma_f16 v121,v120,v120,s54
v_pk_fma_f16 v123,v122,v122,s54
v_mfma_f32_16x16x32_bf16 v[180:183],v[96:99],v[136:139],v[180:183]
ds_read_u16 v148,v148
v_pk_mul_f16 v121,v120,v121
v_pk_mul_f16 v123,v122,v123
v_mfma_f32_16x16x32_bf16 v[188:191],v[100:103],v[136:139],v[188:191]
ds_read_b64_tr_b16 v[78:79],v2 offset:18976
v_lshlrev_b32_sdwa v152,1,v152 dst_sel:DWORD dst_unused:UNUSED_PAD src0_sel:DWORD src1_sel:WORD_1
v_lshlrev_b32_sdwa v154,1,v154 dst_sel:DWORD dst_unused:UNUSED_PAD src0_sel:DWORD src1_sel:WORD_1
v_mfma_f32_16x16x32_bf16 v[196:199],v[104:107],v[136:139],v[196:199]
ds_read_u16_d16_hi v150,v150
v_exp_f16 v120,v121
v_mfma_f32_16x16x32_bf16 v[204:207],v[108:111],v[136:139],v[204:207]
s_waitcnt lgkmcnt(12)
ds_read_b64_tr_b16 v[80:81],v1 offset:21024
v_exp_f16 v122,v123
v_mfma_f32_16x16x32_bf16 v[128:131],v[64:67],v[16:19],v[112:115]
ds_read_u16 v149,v149
v_lshlrev_b32_sdwa v153,1,v153 dst_sel:DWORD dst_unused:UNUSED_PAD src0_sel:DWORD src1_sel:WORD_1
v_lshlrev_b32_sdwa v155,1,v155 dst_sel:DWORD dst_unused:UNUSED_PAD src0_sel:DWORD src1_sel:WORD_1
v_mfma_f32_16x16x32_bf16 v[136:139],v[64:67],v[20:23],v[112:115]
s_waitcnt lgkmcnt(9)
ds_read_b64_tr_b16 v[82:83],v1 offset:23072
v_exp_f16_sdwa v120,v121 dst_sel:WORD_1 dst_unused:UNUSED_PRESERVE src0_sel:WORD_1
v_mfma_f32_16x16x32_bf16 v[140:143],v[68:71],v[20:23],v[116:119]
ds_read_u16_d16_hi v151,v151
v_exp_f16_sdwa v122,v123 dst_sel:WORD_1 dst_unused:UNUSED_PRESERVE src0_sel:WORD_1
v_mfma_f32_16x16x32_bf16 v[132:135],v[68:71],v[16:19],v[116:119]
s_waitcnt lgkmcnt(8)
ds_read_b64_tr_b16 v[84:85],v2 offset:21024
v_pk_fma_f16 v120,v120,s55,s55
v_pk_fma_f16 v122,v122,s55,s55
v_mfma_f32_16x16x32_bf16 v[128:131],v[72:75],v[24:27],v[128:131]
ds_read_u16 v152,v152
v_rcp_f16 v121,v120
v_mfma_f32_16x16x32_bf16 v[136:139],v[72:75],v[28:31],v[136:139]
s_waitcnt lgkmcnt(7)
ds_read_b64_tr_b16 v[86:87],v2 offset:23072
v_rcp_f16 v123,v122
v_mfma_f32_16x16x32_bf16 v[140:143],v[76:79],v[28:31],v[140:143]
ds_read_u16_d16_hi v154,v154
v_rcp_f16_sdwa v121,v120 dst_sel:WORD_1 dst_unused:UNUSED_PRESERVE src0_sel:WORD_1
v_mfma_f32_16x16x32_bf16 v[132:135],v[76:79],v[24:27],v[132:135]
s_waitcnt lgkmcnt(5)
v_rcp_f16_sdwa v123,v122 dst_sel:WORD_1 dst_unused:UNUSED_PRESERVE src0_sel:WORD_1
v_mfma_f32_16x16x32_bf16 v[128:131],v[80:83],v[32:35],v[128:131]
ds_read_u16 v153,v153
v_fma_mix_f32 v156,v156,v121,0 op_sel_hi:[0,1,0]
v_fma_mix_f32 v158,v158,v121,0 op_sel:[0,1,0] op_sel_hi:[0,1,0]
v_mfma_f32_16x16x32_bf16 v[136:139],v[80:83],v[36:39],v[136:139]
s_waitcnt lgkmcnt(2)
s_setprio 0
v_lshl_add_u64 v[144:145],v[144:145],0,v[146:147]
v_lshl_add_u64 v[146:147],v[148:149],0,v[150:151]
v_mfma_f32_16x16x32_bf16 v[140:143],v[84:87],v[36:39],v[140:143]
ds_read_u16_d16_hi v155,v155
v_fma_mix_f32 v157,v157,v123,0 op_sel_hi:[0,1,0]
v_fma_mix_f32 v159,v159,v123,0 op_sel:[0,1,0] op_sel_hi:[0,1,0]
v_mfma_f32_16x16x32_bf16 v[132:135],v[84:87],v[32:35],v[132:135]
v_lshlrev_b32_sdwa v128,1,v128 dst_sel:DWORD dst_unused:UNUSED_PAD src0_sel:DWORD src1_sel:WORD_1
v_lshlrev_b32_sdwa v130,1,v130 dst_sel:DWORD dst_unused:UNUSED_PAD src0_sel:DWORD src1_sel:WORD_1
v_mfma_f32_16x16x32_bf16 v[208:211],v[88:91],v[144:147],v[208:211]
s_waitcnt vmcnt(0)
s_barrier
s_setprio 2
ds_read_u16 v128,v128
v_lshlrev_b32_sdwa v129,1,v129 dst_sel:DWORD dst_unused:UNUSED_PAD src0_sel:DWORD src1_sel:WORD_1
v_mfma_f32_16x16x32_bf16 v[216:219],v[92:95],v[144:147],v[216:219]
ds_read_u16_d16_hi v130,v130
v_lshlrev_b32_sdwa v131,1,v131 dst_sel:DWORD dst_unused:UNUSED_PAD src0_sel:DWORD src1_sel:WORD_1
v_mfma_f32_16x16x32_bf16 v[224:227],v[96:99],v[144:147],v[224:227]
s_cbranch_vccz RB58_%=
s_add_u32 m0,s58,12320
s_mul_i32 s69,s59,7
s_add_u32 s69,s69,s97
buffer_load_dwordx4 v5,s[44:47],s69 offen lds
s_add_u32 m0,m0,1024
s_add_u32 s69,s69,s53
buffer_load_dwordx4 v6,s[44:47],s69 offen lds
RB58_%=:
v_lshlrev_b32_sdwa v132,1,v132 dst_sel:DWORD dst_unused:UNUSED_PAD src0_sel:DWORD src1_sel:WORD_1
v_mfma_f32_16x16x32_bf16 v[232:235],v[100:103],v[144:147],v[232:235]
s_waitcnt lgkmcnt(2)
ds_read_u16 v129,v129
v_lshl_add_u64 v[152:153],v[152:153],0,v[154:155]
v_lshlrev_b32_sdwa v134,1,v134 dst_sel:DWORD dst_unused:UNUSED_PAD src0_sel:DWORD src1_sel:WORD_1
v_mfma_f32_16x16x32_bf16 v[240:243],v[104:107],v[144:147],v[240:243]
ds_read_u16_d16_hi v131,v131
v_cvt_pk_bf16_f32 v154,v156,v158
v_cvt_pk_bf16_f32 v155,v157,v159
v_mfma_f32_16x16x32_bf16 v[248:251],v[108:111],v[144:147],v[248:251]
v_lshlrev_b32_sdwa v133,1,v133 dst_sel:DWORD dst_unused:UNUSED_PAD src0_sel:DWORD src1_sel:WORD_1
v_mfma_f32_16x16x32_bf16 v[212:215],v[88:91],v[152:155],v[212:215]
s_cbranch_vccz RB59_%=
s_add_u32 m0,m0,1024
s_add_u32 s69,s69,s53
buffer_load_dwordx4 v5,s[44:47],s69 offen lds
s_add_u32 m0,m0,s50
s_add_u32 s69,s69,s53
buffer_load_dwordx4 v6,s[44:47],s69 offen lds
RB59_%=:
ds_read_u16 v132,v132
v_lshlrev_b32_sdwa v135,1,v135 dst_sel:DWORD dst_unused:UNUSED_PAD src0_sel:DWORD src1_sel:WORD_1
v_mfma_f32_16x16x32_bf16 v[220:223],v[92:95],v[152:155],v[220:223]
ds_read_u16_d16_hi v134,v134
v_lshlrev_b32_sdwa v136,1,v136 dst_sel:DWORD dst_unused:UNUSED_PAD src0_sel:DWORD src1_sel:WORD_1
v_mfma_f32_16x16x32_bf16 v[228:231],v[96:99],v[152:155],v[228:231]
ds_read_b64_tr_b16 v[88:89],v3 offset:0
ds_read_b64_tr_b16 v[90:91],v3 offset:1536
v_lshlrev_b32_sdwa v138,1,v138 dst_sel:DWORD dst_unused:UNUSED_PAD src0_sel:DWORD src1_sel:WORD_1
v_mfma_f32_16x16x32_bf16 v[236:239],v[100:103],v[152:155],v[236:239]
ds_read_u16 v133,v133
v_lshlrev_b32_sdwa v137,1,v137 dst_sel:DWORD dst_unused:UNUSED_PAD src0_sel:DWORD src1_sel:WORD_1
v_mfma_f32_16x16x32_bf16 v[244:247],v[104:107],v[152:155],v[244:247]
ds_read_u16_d16_hi v135,v135
v_lshlrev_b32_sdwa v139,1,v139 dst_sel:DWORD dst_unused:UNUSED_PAD src0_sel:DWORD src1_sel:WORD_1
v_mfma_f32_16x16x32_bf16 v[252:255],v[108:111],v[152:155],v[252:255]
s_cbranch_vccz RB60_%=
s_add_u32 m0,m0,1024
s_add_u32 s69,s69,s53
buffer_load_dwordx4 v5,s[44:47],s69 offen lds
s_add_u32 m0,m0,1024
s_add_u32 s69,s69,s53
buffer_load_dwordx4 v6,s[44:47],s69 offen lds
RB60_%=:
ds_read_b64_tr_b16 v[92:93],v3 offset:8
ds_read_b64_tr_b16 v[94:95],v3 offset:1544
v_lshlrev_b32_sdwa v140,1,v140 dst_sel:DWORD dst_unused:UNUSED_PAD src0_sel:DWORD src1_sel:WORD_1
v_mfma_f32_16x16x32_bf16 v[144:147],v[64:67],v[40:43],v[112:115]
ds_read_u16 v136,v136
v_lshlrev_b32_sdwa v142,1,v142 dst_sel:DWORD dst_unused:UNUSED_PAD src0_sel:DWORD src1_sel:WORD_1
v_mfma_f32_16x16x32_bf16 v[152:155],v[64:67],v[44:47],v[112:115]
ds_read_u16_d16_hi v138,v138
v_lshlrev_b32_sdwa v141,1,v141 dst_sel:DWORD dst_unused:UNUSED_PAD src0_sel:DWORD src1_sel:WORD_1
v_mfma_f32_16x16x32_bf16 v[156:159],v[68:71],v[44:47],v[116:119]
ds_read_b64_tr_b16 v[96:97],v3 offset:64
ds_read_b64_tr_b16 v[98:99],v3 offset:1600
v_lshlrev_b32_sdwa v143,1,v143 dst_sel:DWORD dst_unused:UNUSED_PAD src0_sel:DWORD src1_sel:WORD_1
v_mfma_f32_16x16x32_bf16 v[148:151],v[68:71],v[40:43],v[116:119]
ds_read_u16 v137,v137
v_mfma_f32_16x16x32_bf16 v[144:147],v[72:75],v[48:51],v[144:147]
ds_read_u16_d16_hi v139,v139
v_mfma_f32_16x16x32_bf16 v[152:155],v[72:75],v[52:55],v[152:155]
s_cbranch_vccz RB61_%=
s_add_u32 m0,m0,s50
s_add_u32 s69,s69,s53
buffer_load_dwordx4 v5,s[44:47],s69 offen lds
s_add_u32 m0,m0,1024
s_add_u32 s69,s69,s53
buffer_load_dwordx4 v6,s[44:47],s69 offen lds
RB61_%=:
ds_read_b64_tr_b16 v[100:101],v3 offset:72
ds_read_b64_tr_b16 v[102:103],v3 offset:1608
v_mfma_f32_16x16x32_bf16 v[156:159],v[76:79],v[52:55],v[156:159]
ds_read_u16 v140,v140
v_mfma_f32_16x16x32_bf16 v[148:151],v[76:79],v[48:51],v[148:151]
s_waitcnt lgkmcnt(15)
ds_read_u16_d16_hi v142,v142
v_lshl_add_u64 v[128:129],v[128:129],0,v[130:131]
v_mfma_f32_16x16x32_bf16 v[144:147],v[80:83],v[56:59],v[144:147]
s_waitcnt lgkmcnt(12)
ds_read_b64_tr_b16 v[104:105],v3 offset:128
ds_read_b64_tr_b16 v[106:107],v3 offset:1664
v_lshl_add_u64 v[130:131],v[132:133],0,v[134:135]
v_mfma_f32_16x16x32_bf16 v[152:155],v[80:83],v[60:63],v[152:155]
ds_read_u16 v141,v141
v_mfma_f32_16x16x32_bf16 v[156:159],v[84:87],v[60:63],v[156:159]
ds_read_u16_d16_hi v143,v143
v_mfma_f32_16x16x32_bf16 v[148:151],v[84:87],v[56:59],v[148:151]
s_cbranch_vccz RB62_%=
s_add_u32 m0,m0,1024
s_add_u32 s69,s69,s53
buffer_load_dwordx4 v5,s[44:47],s69 offen lds
s_add_u32 m0,m0,s50
s_add_u32 s69,s69,s53
buffer_load_dwordx4 v6,s[44:47],s69 offen lds
RB62_%=:
ds_read_b64_tr_b16 v[108:109],v3 offset:136
ds_read_b64_tr_b16 v[110:111],v3 offset:1672
v_lshlrev_b32_sdwa v144,1,v144 dst_sel:DWORD dst_unused:UNUSED_PAD src0_sel:DWORD src1_sel:WORD_1
v_lshlrev_b32_sdwa v146,1,v146 dst_sel:DWORD dst_unused:UNUSED_PAD src0_sel:DWORD src1_sel:WORD_1
v_mfma_f32_16x16x32_bf16 v[160:163],v[88:91],v[128:131],v[160:163]
s_waitcnt lgkmcnt(15)
ds_read_u16 v144,v144
v_lshlrev_b32_sdwa v145,1,v145 dst_sel:DWORD dst_unused:UNUSED_PAD src0_sel:DWORD src1_sel:WORD_1
v_mfma_f32_16x16x32_bf16 v[168:171],v[92:95],v[128:131],v[168:171]
s_waitcnt lgkmcnt(13)
ds_read_u16_d16_hi v146,v146
v_lshlrev_b32_sdwa v147,1,v147 dst_sel:DWORD dst_unused:UNUSED_PAD src0_sel:DWORD src1_sel:WORD_1
v_mfma_f32_16x16x32_bf16 v[176:179],v[96:99],v[128:131],v[176:179]
s_waitcnt lgkmcnt(10)
ds_read_b64_tr_b16 v[64:65],v1 offset:0
ds_read_b64_tr_b16 v[66:67],v1 offset:2048
v_lshlrev_b32_sdwa v148,1,v148 dst_sel:DWORD dst_unused:UNUSED_PAD src0_sel:DWORD src1_sel:WORD_1
v_mfma_f32_16x16x32_bf16 v[184:187],v[100:103],v[128:131],v[184:187]
s_waitcnt lgkmcnt(8)
ds_read_u16 v145,v145
v_lshl_add_u64 v[136:137],v[136:137],0,v[138:139]
v_lshlrev_b32_sdwa v150,1,v150 dst_sel:DWORD dst_unused:UNUSED_PAD src0_sel:DWORD src1_sel:WORD_1
v_mfma_f32_16x16x32_bf16 v[192:195],v[104:107],v[128:131],v[192:195]
s_waitcnt lgkmcnt(5)
s_cbranch_vccz RB63_%=
s_add_u32 m0,m0,1024
s_add_u32 s69,s69,s53
buffer_load_dwordx4 v5,s[44:47],s69 offen lds
s_add_u32 m0,m0,1024
s_add_u32 s69,s69,s53
buffer_load_dwordx4 v6,s[44:47],s69 offen lds
RB63_%=:
ds_read_u16_d16_hi v147,v147
v_lshl_add_u64 v[138:139],v[140:141],0,v[142:143]
v_mfma_f32_16x16x32_bf16 v[200:203],v[108:111],v[128:131],v[200:203]
ds_read_b128 v[112:115],v4 offset:768
ds_read_b64_tr_b16 v[68:69],v2 offset:0
v_lshlrev_b32_sdwa v149,1,v149 dst_sel:DWORD dst_unused:UNUSED_PAD src0_sel:DWORD src1_sel:WORD_1
v_mfma_f32_16x16x32_bf16 v[164:167],v[88:91],v[136:139],v[164:167]
ds_read_u16 v148,v148
v_lshlrev_b32_sdwa v151,1,v151 dst_sel:DWORD dst_unused:UNUSED_PAD src0_sel:DWORD src1_sel:WORD_1
v_mfma_f32_16x16x32_bf16 v[172:175],v[92:95],v[136:139],v[172:175]
ds_read_u16_d16_hi v150,v150
ds_read_b64_tr_b16 v[70:71],v2 offset:2048
v_lshlrev_b32_sdwa v152,1,v152 dst_sel:DWORD dst_unused:UNUSED_PAD src0_sel:DWORD src1_sel:WORD_1
v_mfma_f32_16x16x32_bf16 v[180:183],v[96:99],v[136:139],v[180:183]
ds_read_b128 v[116:119],v4 offset:800
ds_read_b64_tr_b16 v[72:73],v1 offset:4096
v_lshlrev_b32_sdwa v154,1,v154 dst_sel:DWORD dst_unused:UNUSED_PAD src0_sel:DWORD src1_sel:WORD_1
v_mfma_f32_16x16x32_bf16 v[188:191],v[100:103],v[136:139],v[188:191]
ds_read_u16 v149,v149
v_lshlrev_b32_sdwa v153,1,v153 dst_sel:DWORD dst_unused:UNUSED_PAD src0_sel:DWORD src1_sel:WORD_1
v_mfma_f32_16x16x32_bf16 v[196:199],v[104:107],v[136:139],v[196:199]
ds_read_u16_d16_hi v151,v151
v_lshlrev_b32_sdwa v155,1,v155 dst_sel:DWORD dst_unused:UNUSED_PAD src0_sel:DWORD src1_sel:WORD_1
v_mfma_f32_16x16x32_bf16 v[204:207],v[108:111],v[136:139],v[204:207]
s_waitcnt lgkmcnt(8)
ds_read_b64_tr_b16 v[74:75],v1 offset:6144
ds_read_b64_tr_b16 v[76:77],v2 offset:4096
v_lshlrev_b32_sdwa v156,1,v156 dst_sel:DWORD dst_unused:UNUSED_PAD src0_sel:DWORD src1_sel:WORD_1
v_mfma_f32_16x16x32_bf16 v[128:131],v[64:67],v[16:19],v[112:115]
ds_read_u16 v152,v152
v_lshlrev_b32_sdwa v158,1,v158 dst_sel:DWORD dst_unused:UNUSED_PAD src0_sel:DWORD src1_sel:WORD_1
v_mfma_f32_16x16x32_bf16 v[136:139],v[64:67],v[20:23],v[112:115]
s_waitcnt lgkmcnt(6)
ds_read_u16_d16_hi v154,v154
ds_read_b64_tr_b16 v[78:79],v2 offset:6144
v_lshlrev_b32_sdwa v157,1,v157 dst_sel:DWORD dst_unused:UNUSED_PAD src0_sel:DWORD src1_sel:WORD_1
v_mfma_f32_16x16x32_bf16 v[140:143],v[68:71],v[20:23],v[116:119]
ds_read_b64_tr_b16 v[80:81],v1 offset:8192
ds_read_b64_tr_b16 v[82:83],v1 offset:10240
v_lshlrev_b32_sdwa v159,1,v159 dst_sel:DWORD dst_unused:UNUSED_PAD src0_sel:DWORD src1_sel:WORD_1
v_mfma_f32_16x16x32_bf16 v[132:135],v[68:71],v[16:19],v[116:119]
s_waitcnt lgkmcnt(6)
ds_read_u16 v153,v153
v_mfma_f32_16x16x32_bf16 v[128:131],v[72:75],v[24:27],v[128:131]
ds_read_u16_d16_hi v155,v155
v_mfma_f32_16x16x32_bf16 v[136:139],v[72:75],v[28:31],v[136:139]
s_waitcnt lgkmcnt(4)
ds_read_b64_tr_b16 v[84:85],v2 offset:8192
ds_read_b64_tr_b16 v[86:87],v2 offset:10240
v_mfma_f32_16x16x32_bf16 v[140:143],v[76:79],v[28:31],v[140:143]
ds_read_u16 v156,v156
v_mfma_f32_16x16x32_bf16 v[132:135],v[76:79],v[24:27],v[132:135]
s_waitcnt lgkmcnt(5)
ds_read_u16_d16_hi v158,v158
v_lshl_add_u64 v[144:145],v[144:145],0,v[146:147]
v_mfma_f32_16x16x32_bf16 v[128:131],v[80:83],v[32:35],v[128:131]
v_lshl_add_u64 v[146:147],v[148:149],0,v[150:151]
v_mfma_f32_16x16x32_bf16 v[136:139],v[80:83],v[36:39],v[136:139]
s_waitcnt lgkmcnt(2)
ds_read_u16 v157,v157
v_mfma_f32_16x16x32_bf16 v[140:143],v[84:87],v[36:39],v[140:143]
ds_read_u16_d16_hi v159,v159
v_mfma_f32_16x16x32_bf16 v[132:135],v[84:87],v[32:35],v[132:135]
s_nop 0
v_lshlrev_b32_sdwa v128,1,v128 dst_sel:DWORD dst_unused:UNUSED_PAD src0_sel:DWORD src1_sel:WORD_1
v_lshlrev_b32_sdwa v130,1,v130 dst_sel:DWORD dst_unused:UNUSED_PAD src0_sel:DWORD src1_sel:WORD_1
v_mfma_f32_16x16x32_bf16 v[208:211],v[88:91],v[144:147],v[208:211]
ds_read_u16 v128,v128
v_lshlrev_b32_sdwa v129,1,v129 dst_sel:DWORD dst_unused:UNUSED_PAD src0_sel:DWORD src1_sel:WORD_1
v_mfma_f32_16x16x32_bf16 v[216:219],v[92:95],v[144:147],v[216:219]
ds_read_u16_d16_hi v130,v130
v_lshlrev_b32_sdwa v131,1,v131 dst_sel:DWORD dst_unused:UNUSED_PAD src0_sel:DWORD src1_sel:WORD_1
v_mfma_f32_16x16x32_bf16 v[224:227],v[96:99],v[144:147],v[224:227]
v_lshlrev_b32_sdwa v132,1,v132 dst_sel:DWORD dst_unused:UNUSED_PAD src0_sel:DWORD src1_sel:WORD_1
v_mfma_f32_16x16x32_bf16 v[232:235],v[100:103],v[144:147],v[232:235]
ds_read_u16 v129,v129
v_lshl_add_u64 v[152:153],v[152:153],0,v[154:155]
v_lshlrev_b32_sdwa v134,1,v134 dst_sel:DWORD dst_unused:UNUSED_PAD src0_sel:DWORD src1_sel:WORD_1
v_mfma_f32_16x16x32_bf16 v[240:243],v[104:107],v[144:147],v[240:243]
s_waitcnt lgkmcnt(3)
ds_read_u16_d16_hi v131,v131
v_lshl_add_u64 v[154:155],v[156:157],0,v[158:159]
v_mfma_f32_16x16x32_bf16 v[248:251],v[108:111],v[144:147],v[248:251]
v_lshlrev_b32_sdwa v133,1,v133 dst_sel:DWORD dst_unused:UNUSED_PAD src0_sel:DWORD src1_sel:WORD_1
v_mfma_f32_16x16x32_bf16 v[212:215],v[88:91],v[152:155],v[212:215]
ds_read_u16 v132,v132
v_lshlrev_b32_sdwa v135,1,v135 dst_sel:DWORD dst_unused:UNUSED_PAD src0_sel:DWORD src1_sel:WORD_1
v_mfma_f32_16x16x32_bf16 v[220:223],v[92:95],v[152:155],v[220:223]
ds_read_u16_d16_hi v134,v134
v_lshlrev_b32_sdwa v136,1,v136 dst_sel:DWORD dst_unused:UNUSED_PAD src0_sel:DWORD src1_sel:WORD_1
v_mfma_f32_16x16x32_bf16 v[228:231],v[96:99],v[152:155],v[228:231]
ds_read_b64_tr_b16 v[88:89],v3 offset:6160
ds_read_b64_tr_b16 v[90:91],v3 offset:7696
v_lshlrev_b32_sdwa v138,1,v138 dst_sel:DWORD dst_unused:UNUSED_PAD src0_sel:DWORD src1_sel:WORD_1
v_mfma_f32_16x16x32_bf16 v[236:239],v[100:103],v[152:155],v[236:239]
ds_read_u16 v133,v133
v_lshlrev_b32_sdwa v137,1,v137 dst_sel:DWORD dst_unused:UNUSED_PAD src0_sel:DWORD src1_sel:WORD_1
v_mfma_f32_16x16x32_bf16 v[244:247],v[104:107],v[152:155],v[244:247]
ds_read_u16_d16_hi v135,v135
v_lshlrev_b32_sdwa v139,1,v139 dst_sel:DWORD dst_unused:UNUSED_PAD src0_sel:DWORD src1_sel:WORD_1
v_mfma_f32_16x16x32_bf16 v[252:255],v[108:111],v[152:155],v[252:255]
ds_read_b64_tr_b16 v[92:93],v3 offset:6168
ds_read_b64_tr_b16 v[94:95],v3 offset:7704
v_lshlrev_b32_sdwa v140,1,v140 dst_sel:DWORD dst_unused:UNUSED_PAD src0_sel:DWORD src1_sel:WORD_1
v_mfma_f32_16x16x32_bf16 v[144:147],v[64:67],v[40:43],v[112:115]
ds_read_u16 v136,v136
v_lshlrev_b32_sdwa v142,1,v142 dst_sel:DWORD dst_unused:UNUSED_PAD src0_sel:DWORD src1_sel:WORD_1
v_mfma_f32_16x16x32_bf16 v[152:155],v[64:67],v[44:47],v[112:115]
ds_read_u16_d16_hi v138,v138
v_lshlrev_b32_sdwa v141,1,v141 dst_sel:DWORD dst_unused:UNUSED_PAD src0_sel:DWORD src1_sel:WORD_1
v_mfma_f32_16x16x32_bf16 v[156:159],v[68:71],v[44:47],v[116:119]
ds_read_b64_tr_b16 v[96:97],v3 offset:6224
ds_read_b64_tr_b16 v[98:99],v3 offset:7760
v_lshlrev_b32_sdwa v143,1,v143 dst_sel:DWORD dst_unused:UNUSED_PAD src0_sel:DWORD src1_sel:WORD_1
v_mfma_f32_16x16x32_bf16 v[148:151],v[68:71],v[40:43],v[116:119]
ds_read_u16 v137,v137
v_mfma_f32_16x16x32_bf16 v[144:147],v[72:75],v[48:51],v[144:147]
ds_read_u16_d16_hi v139,v139
v_mfma_f32_16x16x32_bf16 v[152:155],v[72:75],v[52:55],v[152:155]
ds_read_b64_tr_b16 v[100:101],v3 offset:6232
ds_read_b64_tr_b16 v[102:103],v3 offset:7768
v_mfma_f32_16x16x32_bf16 v[156:159],v[76:79],v[52:55],v[156:159]
ds_read_u16 v140,v140
v_mfma_f32_16x16x32_bf16 v[148:151],v[76:79],v[48:51],v[148:151]
s_waitcnt lgkmcnt(15)
ds_read_u16_d16_hi v142,v142
v_lshl_add_u64 v[128:129],v[128:129],0,v[130:131]
v_mfma_f32_16x16x32_bf16 v[144:147],v[80:83],v[56:59],v[144:147]
s_waitcnt lgkmcnt(12)
ds_read_b64_tr_b16 v[104:105],v3 offset:6288
ds_read_b64_tr_b16 v[106:107],v3 offset:7824
v_lshl_add_u64 v[130:131],v[132:133],0,v[134:135]
v_mfma_f32_16x16x32_bf16 v[152:155],v[80:83],v[60:63],v[152:155]
ds_read_u16 v141,v141
v_mfma_f32_16x16x32_bf16 v[156:159],v[84:87],v[60:63],v[156:159]
ds_read_u16_d16_hi v143,v143
v_mfma_f32_16x16x32_bf16 v[148:151],v[84:87],v[56:59],v[148:151]
ds_read_b64_tr_b16 v[108:109],v3 offset:6296
ds_read_b64_tr_b16 v[110:111],v3 offset:7832
v_lshlrev_b32_sdwa v144,1,v144 dst_sel:DWORD dst_unused:UNUSED_PAD src0_sel:DWORD src1_sel:WORD_1
v_lshlrev_b32_sdwa v146,1,v146 dst_sel:DWORD dst_unused:UNUSED_PAD src0_sel:DWORD src1_sel:WORD_1
v_mfma_f32_16x16x32_bf16 v[160:163],v[88:91],v[128:131],v[160:163]
s_waitcnt lgkmcnt(15)
ds_read_u16 v144,v144
v_lshlrev_b32_sdwa v145,1,v145 dst_sel:DWORD dst_unused:UNUSED_PAD src0_sel:DWORD src1_sel:WORD_1
v_mfma_f32_16x16x32_bf16 v[168:171],v[92:95],v[128:131],v[168:171]
s_waitcnt lgkmcnt(13)
ds_read_u16_d16_hi v146,v146
v_lshlrev_b32_sdwa v147,1,v147 dst_sel:DWORD dst_unused:UNUSED_PAD src0_sel:DWORD src1_sel:WORD_1
v_mfma_f32_16x16x32_bf16 v[176:179],v[96:99],v[128:131],v[176:179]
s_waitcnt lgkmcnt(10)
ds_read_b64_tr_b16 v[64:65],v1 offset:512
ds_read_b64_tr_b16 v[66:67],v1 offset:2560
v_lshlrev_b32_sdwa v148,1,v148 dst_sel:DWORD dst_unused:UNUSED_PAD src0_sel:DWORD src1_sel:WORD_1
v_mfma_f32_16x16x32_bf16 v[184:187],v[100:103],v[128:131],v[184:187]
s_waitcnt lgkmcnt(8)
ds_read_u16 v145,v145
v_lshl_add_u64 v[136:137],v[136:137],0,v[138:139]
v_lshlrev_b32_sdwa v150,1,v150 dst_sel:DWORD dst_unused:UNUSED_PAD src0_sel:DWORD src1_sel:WORD_1
v_mfma_f32_16x16x32_bf16 v[192:195],v[104:107],v[128:131],v[192:195]
s_waitcnt lgkmcnt(5)
ds_read_u16_d16_hi v147,v147
v_lshl_add_u64 v[138:139],v[140:141],0,v[142:143]
v_mfma_f32_16x16x32_bf16 v[200:203],v[108:111],v[128:131],v[200:203]
ds_read_b128 v[112:115],v4 offset:896
ds_read_b64_tr_b16 v[68:69],v2 offset:512
v_lshlrev_b32_sdwa v149,1,v149 dst_sel:DWORD dst_unused:UNUSED_PAD src0_sel:DWORD src1_sel:WORD_1
v_mfma_f32_16x16x32_bf16 v[164:167],v[88:91],v[136:139],v[164:167]
ds_read_u16 v148,v148
v_lshlrev_b32_sdwa v151,1,v151 dst_sel:DWORD dst_unused:UNUSED_PAD src0_sel:DWORD src1_sel:WORD_1
v_mfma_f32_16x16x32_bf16 v[172:175],v[92:95],v[136:139],v[172:175]
ds_read_u16_d16_hi v150,v150
ds_read_b64_tr_b16 v[70:71],v2 offset:2560
v_lshlrev_b32_sdwa v152,1,v152 dst_sel:DWORD dst_unused:UNUSED_PAD src0_sel:DWORD src1_sel:WORD_1
v_mfma_f32_16x16x32_bf16 v[180:183],v[96:99],v[136:139],v[180:183]
ds_read_b128 v[116:119],v4 offset:928
ds_read_b64_tr_b16 v[72:73],v1 offset:4608
v_lshlrev_b32_sdwa v154,1,v154 dst_sel:DWORD dst_unused:UNUSED_PAD src0_sel:DWORD src1_sel:WORD_1
v_mfma_f32_16x16x32_bf16 v[188:191],v[100:103],v[136:139],v[188:191]
ds_read_u16 v149,v149
v_lshlrev_b32_sdwa v153,1,v153 dst_sel:DWORD dst_unused:UNUSED_PAD src0_sel:DWORD src1_sel:WORD_1
v_mfma_f32_16x16x32_bf16 v[196:199],v[104:107],v[136:139],v[196:199]
ds_read_u16_d16_hi v151,v151
v_lshlrev_b32_sdwa v155,1,v155 dst_sel:DWORD dst_unused:UNUSED_PAD src0_sel:DWORD src1_sel:WORD_1
v_mfma_f32_16x16x32_bf16 v[204:207],v[108:111],v[136:139],v[204:207]
s_waitcnt lgkmcnt(8)
ds_read_b64_tr_b16 v[74:75],v1 offset:6656
ds_read_b64_tr_b16 v[76:77],v2 offset:4608
v_lshlrev_b32_sdwa v156,1,v156 dst_sel:DWORD dst_unused:UNUSED_PAD src0_sel:DWORD src1_sel:WORD_1
v_mfma_f32_16x16x32_bf16 v[128:131],v[64:67],v[16:19],v[112:115]
ds_read_u16 v152,v152
s_mov_b32 s86,0xbe801d5a
v_lshlrev_b32_sdwa v158,1,v158 dst_sel:DWORD dst_unused:UNUSED_PAD src0_sel:DWORD src1_sel:WORD_1
v_mfma_f32_16x16x32_bf16 v[136:139],v[64:67],v[20:23],v[112:115]
s_waitcnt lgkmcnt(6)
ds_read_u16_d16_hi v154,v154
ds_read_b64_tr_b16 v[78:79],v2 offset:6656
v_lshlrev_b32_sdwa v157,1,v157 dst_sel:DWORD dst_unused:UNUSED_PAD src0_sel:DWORD src1_sel:WORD_1
v_mfma_f32_16x16x32_bf16 v[140:143],v[68:71],v[20:23],v[116:119]
ds_read_b64_tr_b16 v[80:81],v1 offset:8704
ds_read_b64_tr_b16 v[82:83],v1 offset:10752
v_lshlrev_b32_sdwa v159,1,v159 dst_sel:DWORD dst_unused:UNUSED_PAD src0_sel:DWORD src1_sel:WORD_1
v_mfma_f32_16x16x32_bf16 v[132:135],v[68:71],v[16:19],v[116:119]
s_waitcnt lgkmcnt(6)
ds_read_u16 v153,v153
v_mfma_f32_16x16x32_bf16 v[128:131],v[72:75],v[24:27],v[128:131]
ds_read_u16_d16_hi v155,v155
v_mfma_f32_16x16x32_bf16 v[136:139],v[72:75],v[28:31],v[136:139]
s_waitcnt lgkmcnt(4)
ds_read_b64_tr_b16 v[84:85],v2 offset:8704
ds_read_b64_tr_b16 v[86:87],v2 offset:10752
v_mfma_f32_16x16x32_bf16 v[140:143],v[76:79],v[28:31],v[140:143]
ds_read_u16 v156,v156
v_mfma_f32_16x16x32_bf16 v[132:135],v[76:79],v[24:27],v[132:135]
s_waitcnt lgkmcnt(5)
ds_read_u16_d16_hi v158,v158
v_lshl_add_u64 v[144:145],v[144:145],0,v[146:147]
v_mfma_f32_16x16x32_bf16 v[128:131],v[80:83],v[32:35],v[128:131]
v_lshl_add_u64 v[146:147],v[148:149],0,v[150:151]
v_mfma_f32_16x16x32_bf16 v[136:139],v[80:83],v[36:39],v[136:139]
s_waitcnt lgkmcnt(2)
ds_read_u16 v157,v157
v_mfma_f32_16x16x32_bf16 v[140:143],v[84:87],v[36:39],v[140:143]
ds_read_u16_d16_hi v159,v159
v_mfma_f32_16x16x32_bf16 v[132:135],v[84:87],v[32:35],v[132:135]
s_nop 0
v_lshlrev_b32_sdwa v128,1,v128 dst_sel:DWORD dst_unused:UNUSED_PAD src0_sel:DWORD src1_sel:WORD_1
v_lshlrev_b32_sdwa v130,1,v130 dst_sel:DWORD dst_unused:UNUSED_PAD src0_sel:DWORD src1_sel:WORD_1
v_mfma_f32_16x16x32_bf16 v[208:211],v[88:91],v[144:147],v[208:211]
s_waitcnt vmcnt(0)
s_barrier
s_setprio 3
ds_read_u16 v128,v128
v_lshlrev_b32_sdwa v129,1,v129 dst_sel:DWORD dst_unused:UNUSED_PAD src0_sel:DWORD src1_sel:WORD_1
v_lshlrev_b32_sdwa v131,1,v131 dst_sel:DWORD dst_unused:UNUSED_PAD src0_sel:DWORD src1_sel:WORD_1
v_mfma_f32_16x16x32_bf16 v[216:219],v[92:95],v[144:147],v[216:219]
v_fma_mixlo_f16 v120,v140,s99,0
v_fma_mixlo_f16 v122,v141,s99,0
v_mfma_f32_16x16x32_bf16 v[224:227],v[96:99],v[144:147],v[224:227]
ds_read_u16_d16_hi v130,v130
v_lshlrev_b32_sdwa v132,1,v132 dst_sel:DWORD dst_unused:UNUSED_PAD src0_sel:DWORD src1_sel:WORD_1
v_lshlrev_b32_sdwa v134,1,v134 dst_sel:DWORD dst_unused:UNUSED_PAD src0_sel:DWORD src1_sel:WORD_1
v_mfma_f32_16x16x32_bf16 v[232:235],v[100:103],v[144:147],v[232:235]
v_lshl_add_u64 v[152:153],v[152:153],0,v[154:155]
v_mfma_f32_16x16x32_bf16 v[240:243],v[104:107],v[144:147],v[240:243]
s_waitcnt lgkmcnt(2)
ds_read_u16 v129,v129
v_lshl_add_u64 v[154:155],v[156:157],0,v[158:159]
v_mfma_f32_16x16x32_bf16 v[248:251],v[108:111],v[144:147],v[248:251]
v_fma_mixhi_f16 v120,v142,s99,0
v_fma_mixhi_f16 v122,v143,s99,0
v_mfma_f32_16x16x32_bf16 v[212:215],v[88:91],v[152:155],v[212:215]
ds_read_u16_d16_hi v131,v131
v_lshlrev_b32_sdwa v133,1,v133 dst_sel:DWORD dst_unused:UNUSED_PAD src0_sel:DWORD src1_sel:WORD_1
v_lshlrev_b32_sdwa v135,1,v135 dst_sel:DWORD dst_unused:UNUSED_PAD src0_sel:DWORD src1_sel:WORD_1
v_mfma_f32_16x16x32_bf16 v[220:223],v[92:95],v[152:155],v[220:223]
ds_read_b64_tr_b16 v[88:89],v3 offset:12320
v_pk_fma_f16 v121,v120,v120,s54
v_pk_fma_f16 v123,v122,v122,s54
v_mfma_f32_16x16x32_bf16 v[228:231],v[96:99],v[152:155],v[228:231]
ds_read_u16 v132,v132
v_pk_mul_f16 v121,v120,v121
v_pk_mul_f16 v123,v122,v123
v_mfma_f32_16x16x32_bf16 v[236:239],v[100:103],v[152:155],v[236:239]
ds_read_b64_tr_b16 v[90:91],v3 offset:13856
v_lshlrev_b32_sdwa v136,1,v136 dst_sel:DWORD dst_unused:UNUSED_PAD src0_sel:DWORD src1_sel:WORD_1
v_lshlrev_b32_sdwa v138,1,v138 dst_sel:DWORD dst_unused:UNUSED_PAD src0_sel:DWORD src1_sel:WORD_1
v_mfma_f32_16x16x32_bf16 v[244:247],v[104:107],v[152:155],v[244:247]
ds_read_u16_d16_hi v134,v134
v_exp_f16 v120,v121
v_mfma_f32_16x16x32_bf16 v[252:255],v[108:111],v[152:155],v[252:255]
ds_read_b64_tr_b16 v[92:93],v3 offset:12328
v_exp_f16 v122,v123
v_mfma_f32_16x16x32_bf16 v[144:147],v[64:67],v[40:43],v[112:115]
ds_read_u16 v133,v133
v_lshlrev_b32_sdwa v137,1,v137 dst_sel:DWORD dst_unused:UNUSED_PAD src0_sel:DWORD src1_sel:WORD_1
v_lshlrev_b32_sdwa v139,1,v139 dst_sel:DWORD dst_unused:UNUSED_PAD src0_sel:DWORD src1_sel:WORD_1
v_mfma_f32_16x16x32_bf16 v[152:155],v[64:67],v[44:47],v[112:115]
ds_read_b64_tr_b16 v[94:95],v3 offset:13864
v_exp_f16_sdwa v120,v121 dst_sel:WORD_1 dst_unused:UNUSED_PRESERVE src0_sel:WORD_1
v_mfma_f32_16x16x32_bf16 v[156:159],v[68:71],v[44:47],v[116:119]
ds_read_u16_d16_hi v135,v135
v_exp_f16_sdwa v122,v123 dst_sel:WORD_1 dst_unused:UNUSED_PRESERVE src0_sel:WORD_1
v_mfma_f32_16x16x32_bf16 v[148:151],v[68:71],v[40:43],v[116:119]
ds_read_b64_tr_b16 v[96:97],v3 offset:12384
v_pk_fma_f16 v120,v120,s55,s55
v_pk_fma_f16 v122,v122,s55,s55
v_mfma_f32_16x16x32_bf16 v[144:147],v[72:75],v[48:51],v[144:147]
ds_read_u16 v136,v136
v_rcp_f16 v121,v120
v_mfma_f32_16x16x32_bf16 v[152:155],v[72:75],v[52:55],v[152:155]
ds_read_b64_tr_b16 v[98:99],v3 offset:13920
ds_read_b64_tr_b16 v[100:101],v3 offset:12392
v_rcp_f16 v123,v122
v_mfma_f32_16x16x32_bf16 v[156:159],v[76:79],v[52:55],v[156:159]
ds_read_u16_d16_hi v138,v138
v_rcp_f16_sdwa v121,v120 dst_sel:WORD_1 dst_unused:UNUSED_PRESERVE src0_sel:WORD_1
v_mfma_f32_16x16x32_bf16 v[148:151],v[76:79],v[48:51],v[148:151]
ds_read_b64_tr_b16 v[102:103],v3 offset:13928
ds_read_b64_tr_b16 v[104:105],v3 offset:12448
v_rcp_f16_sdwa v123,v122 dst_sel:WORD_1 dst_unused:UNUSED_PRESERVE src0_sel:WORD_1
v_mfma_f32_16x16x32_bf16 v[144:147],v[80:83],v[56:59],v[144:147]
ds_read_u16 v137,v137
v_fma_mix_f32 v140,v140,v121,0 op_sel_hi:[0,1,0]
v_fma_mix_f32 v142,v142,v121,0 op_sel:[0,1,0] op_sel_hi:[0,1,0]
v_mfma_f32_16x16x32_bf16 v[152:155],v[80:83],v[60:63],v[152:155]
s_waitcnt lgkmcnt(8)
ds_read_b64_tr_b16 v[106:107],v3 offset:13984
ds_read_b64_tr_b16 v[108:109],v3 offset:12456
v_lshl_add_u64 v[128:129],v[128:129],0,v[130:131]
v_lshl_add_u64 v[130:131],v[132:133],0,v[134:135]
v_mfma_f32_16x16x32_bf16 v[156:159],v[84:87],v[60:63],v[156:159]
ds_read_u16_d16_hi v139,v139
v_fma_mix_f32 v141,v141,v123,0 op_sel_hi:[0,1,0]
v_fma_mix_f32 v143,v143,v123,0 op_sel:[0,1,0] op_sel_hi:[0,1,0]
v_mfma_f32_16x16x32_bf16 v[148:151],v[84:87],v[56:59],v[148:151]
ds_read_b64_tr_b16 v[110:111],v3 offset:13992
ds_read_b64_tr_b16 v[64:65],v1 offset:12320
v_lshlrev_b32_sdwa v144,1,v144 dst_sel:DWORD dst_unused:UNUSED_PAD src0_sel:DWORD src1_sel:WORD_1
v_lshlrev_b32_sdwa v146,1,v146 dst_sel:DWORD dst_unused:UNUSED_PAD src0_sel:DWORD src1_sel:WORD_1
v_mfma_f32_16x16x32_bf16 v[160:163],v[88:91],v[128:131],v[160:163]
ds_read_u16 v144,v144
v_lshlrev_b32_sdwa v145,1,v145 dst_sel:DWORD dst_unused:UNUSED_PAD src0_sel:DWORD src1_sel:WORD_1
v_lshlrev_b32_sdwa v147,1,v147 dst_sel:DWORD dst_unused:UNUSED_PAD src0_sel:DWORD src1_sel:WORD_1
v_mfma_f32_16x16x32_bf16 v[168:171],v[92:95],v[128:131],v[168:171]
s_waitcnt lgkmcnt(11)
ds_read_b64_tr_b16 v[66:67],v1 offset:14368
ds_read_b128 v[112:115],v4 offset:1024
v_fma_mixlo_f16 v120,v156,s99,0
v_fma_mixlo_f16 v122,v157,s99,0
v_mfma_f32_16x16x32_bf16 v[176:179],v[96:99],v[128:131],v[176:179]
s_waitcnt lgkmcnt(10)
ds_read_u16_d16_hi v146,v146
v_lshlrev_b32_sdwa v148,1,v148 dst_sel:DWORD dst_unused:UNUSED_PAD src0_sel:DWORD src1_sel:WORD_1
v_lshlrev_b32_sdwa v150,1,v150 dst_sel:DWORD dst_unused:UNUSED_PAD src0_sel:DWORD src1_sel:WORD_1
v_mfma_f32_16x16x32_bf16 v[184:187],v[100:103],v[128:131],v[184:187]
s_waitcnt lgkmcnt(6)
ds_read_b64_tr_b16 v[68:69],v2 offset:12320
ds_read_b64_tr_b16 v[70:71],v2 offset:14368
v_lshl_add_u64 v[136:137],v[136:137],0,v[138:139]
v_mfma_f32_16x16x32_bf16 v[192:195],v[104:107],v[128:131],v[192:195]
s_waitcnt lgkmcnt(7)
ds_read_u16 v145,v145
v_cvt_pk_bf16_f32 v138,v140,v142
v_cvt_pk_bf16_f32 v139,v141,v143
v_mfma_f32_16x16x32_bf16 v[200:203],v[108:111],v[128:131],v[200:203]
ds_read_b128 v[116:119],v4 offset:1056
ds_read_b64_tr_b16 v[72:73],v1 offset:16416
v_fma_mixhi_f16 v120,v158,s99,0
v_fma_mixhi_f16 v122,v159,s99,0
v_mfma_f32_16x16x32_bf16 v[164:167],v[88:91],v[136:139],v[164:167]
ds_read_u16_d16_hi v147,v147
v_lshlrev_b32_sdwa v149,1,v149 dst_sel:DWORD dst_unused:UNUSED_PAD src0_sel:DWORD src1_sel:WORD_1
v_lshlrev_b32_sdwa v151,1,v151 dst_sel:DWORD dst_unused:UNUSED_PAD src0_sel:DWORD src1_sel:WORD_1
v_mfma_f32_16x16x32_bf16 v[172:175],v[92:95],v[136:139],v[172:175]
ds_read_b64_tr_b16 v[74:75],v1 offset:18464
ds_read_b64_tr_b16 v[76:77],v2 offset:16416
v_pk_fma_f16 v121,v120,v120,s54
v_pk_fma_f16 v123,v122,v122,s54
v_mfma_f32_16x16x32_bf16 v[180:183],v[96:99],v[136:139],v[180:183]
ds_read_u16 v148,v148
v_pk_mul_f16 v121,v120,v121
v_pk_mul_f16 v123,v122,v123
v_mfma_f32_16x16x32_bf16 v[188:191],v[100:103],v[136:139],v[188:191]
ds_read_b64_tr_b16 v[78:79],v2 offset:18464
v_lshlrev_b32_sdwa v152,1,v152 dst_sel:DWORD dst_unused:UNUSED_PAD src0_sel:DWORD src1_sel:WORD_1
v_lshlrev_b32_sdwa v154,1,v154 dst_sel:DWORD dst_unused:UNUSED_PAD src0_sel:DWORD src1_sel:WORD_1
v_mfma_f32_16x16x32_bf16 v[196:199],v[104:107],v[136:139],v[196:199]
ds_read_u16_d16_hi v150,v150
v_exp_f16 v120,v121
v_mfma_f32_16x16x32_bf16 v[204:207],v[108:111],v[136:139],v[204:207]
s_waitcnt lgkmcnt(12)
ds_read_b64_tr_b16 v[80:81],v1 offset:20512
v_exp_f16 v122,v123
v_mfma_f32_16x16x32_bf16 v[128:131],v[64:67],v[16:19],v[112:115]
ds_read_u16 v149,v149
v_lshlrev_b32_sdwa v153,1,v153 dst_sel:DWORD dst_unused:UNUSED_PAD src0_sel:DWORD src1_sel:WORD_1
v_lshlrev_b32_sdwa v155,1,v155 dst_sel:DWORD dst_unused:UNUSED_PAD src0_sel:DWORD src1_sel:WORD_1
v_mfma_f32_16x16x32_bf16 v[136:139],v[64:67],v[20:23],v[112:115]
s_waitcnt lgkmcnt(9)
ds_read_b64_tr_b16 v[82:83],v1 offset:22560
v_exp_f16_sdwa v120,v121 dst_sel:WORD_1 dst_unused:UNUSED_PRESERVE src0_sel:WORD_1
v_mfma_f32_16x16x32_bf16 v[140:143],v[68:71],v[20:23],v[116:119]
ds_read_u16_d16_hi v151,v151
v_exp_f16_sdwa v122,v123 dst_sel:WORD_1 dst_unused:UNUSED_PRESERVE src0_sel:WORD_1
v_mfma_f32_16x16x32_bf16 v[132:135],v[68:71],v[16:19],v[116:119]
s_waitcnt lgkmcnt(8)
ds_read_b64_tr_b16 v[84:85],v2 offset:20512
v_pk_fma_f16 v120,v120,s55,s55
v_pk_fma_f16 v122,v122,s55,s55
v_mfma_f32_16x16x32_bf16 v[128:131],v[72:75],v[24:27],v[128:131]
ds_read_u16 v152,v152
v_rcp_f16 v121,v120
v_mfma_f32_16x16x32_bf16 v[136:139],v[72:75],v[28:31],v[136:139]
s_waitcnt lgkmcnt(7)
ds_read_b64_tr_b16 v[86:87],v2 offset:22560
v_rcp_f16 v123,v122
v_mfma_f32_16x16x32_bf16 v[140:143],v[76:79],v[28:31],v[140:143]
ds_read_u16_d16_hi v154,v154
v_rcp_f16_sdwa v121,v120 dst_sel:WORD_1 dst_unused:UNUSED_PRESERVE src0_sel:WORD_1
v_mfma_f32_16x16x32_bf16 v[132:135],v[76:79],v[24:27],v[132:135]
s_waitcnt lgkmcnt(5)
v_rcp_f16_sdwa v123,v122 dst_sel:WORD_1 dst_unused:UNUSED_PRESERVE src0_sel:WORD_1
v_mfma_f32_16x16x32_bf16 v[128:131],v[80:83],v[32:35],v[128:131]
ds_read_u16 v153,v153
v_fma_mix_f32 v156,v156,v121,0 op_sel_hi:[0,1,0]
v_fma_mix_f32 v158,v158,v121,0 op_sel:[0,1,0] op_sel_hi:[0,1,0]
v_mfma_f32_16x16x32_bf16 v[136:139],v[80:83],v[36:39],v[136:139]
s_waitcnt lgkmcnt(2)
v_lshl_add_u64 v[144:145],v[144:145],0,v[146:147]
v_lshl_add_u64 v[146:147],v[148:149],0,v[150:151]
v_mfma_f32_16x16x32_bf16 v[140:143],v[84:87],v[36:39],v[140:143]
ds_read_u16_d16_hi v155,v155
v_fma_mix_f32 v157,v157,v123,0 op_sel_hi:[0,1,0]
v_fma_mix_f32 v159,v159,v123,0 op_sel:[0,1,0] op_sel_hi:[0,1,0]
v_mfma_f32_16x16x32_bf16 v[132:135],v[84:87],v[32:35],v[132:135]
v_lshlrev_b32_sdwa v128,1,v128 dst_sel:DWORD dst_unused:UNUSED_PAD src0_sel:DWORD src1_sel:WORD_1
v_lshlrev_b32_sdwa v130,1,v130 dst_sel:DWORD dst_unused:UNUSED_PAD src0_sel:DWORD src1_sel:WORD_1
v_mfma_f32_16x16x32_bf16 v[208:211],v[88:91],v[144:147],v[208:211]
ds_read_u16 v128,v128
v_lshlrev_b32_sdwa v129,1,v129 dst_sel:DWORD dst_unused:UNUSED_PAD src0_sel:DWORD src1_sel:WORD_1
v_lshlrev_b32_sdwa v131,1,v131 dst_sel:DWORD dst_unused:UNUSED_PAD src0_sel:DWORD src1_sel:WORD_1
v_mfma_f32_16x16x32_bf16 v[216:219],v[92:95],v[144:147],v[216:219]
v_fma_mixlo_f16 v120,v140,s99,0
v_fma_mixlo_f16 v122,v141,s99,0
v_mfma_f32_16x16x32_bf16 v[224:227],v[96:99],v[144:147],v[224:227]
s_setprio 2
ds_read_u16_d16_hi v130,v130
v_lshlrev_b32_sdwa v132,1,v132 dst_sel:DWORD dst_unused:UNUSED_PAD src0_sel:DWORD src1_sel:WORD_1
v_lshlrev_b32_sdwa v134,1,v134 dst_sel:DWORD dst_unused:UNUSED_PAD src0_sel:DWORD src1_sel:WORD_1
v_mfma_f32_16x16x32_bf16 v[232:235],v[100:103],v[144:147],v[232:235]
s_waitcnt lgkmcnt(2)
v_lshl_add_u64 v[152:153],v[152:153],0,v[154:155]
v_mfma_f32_16x16x32_bf16 v[240:243],v[104:107],v[144:147],v[240:243]
ds_read_u16 v129,v129
v_cvt_pk_bf16_f32 v154,v156,v158
v_cvt_pk_bf16_f32 v155,v157,v159
v_mfma_f32_16x16x32_bf16 v[248:251],v[108:111],v[144:147],v[248:251]
v_fma_mixhi_f16 v120,v142,s99,0
v_fma_mixhi_f16 v122,v143,s99,0
v_mfma_f32_16x16x32_bf16 v[212:215],v[88:91],v[152:155],v[212:215]
ds_read_u16_d16_hi v131,v131
v_lshlrev_b32_sdwa v133,1,v133 dst_sel:DWORD dst_unused:UNUSED_PAD src0_sel:DWORD src1_sel:WORD_1
v_lshlrev_b32_sdwa v135,1,v135 dst_sel:DWORD dst_unused:UNUSED_PAD src0_sel:DWORD src1_sel:WORD_1
v_mfma_f32_16x16x32_bf16 v[220:223],v[92:95],v[152:155],v[220:223]
ds_read_b64_tr_b16 v[88:89],v3 offset:18480
v_pk_fma_f16 v121,v120,v120,s54
v_pk_fma_f16 v123,v122,v122,s54
v_mfma_f32_16x16x32_bf16 v[228:231],v[96:99],v[152:155],v[228:231]
ds_read_u16 v132,v132
v_pk_mul_f16 v121,v120,v121
v_pk_mul_f16 v123,v122,v123
v_mfma_f32_16x16x32_bf16 v[236:239],v[100:103],v[152:155],v[236:239]
ds_read_b64_tr_b16 v[90:91],v3 offset:20016
v_lshlrev_b32_sdwa v136,1,v136 dst_sel:DWORD dst_unused:UNUSED_PAD src0_sel:DWORD src1_sel:WORD_1
v_lshlrev_b32_sdwa v138,1,v138 dst_sel:DWORD dst_unused:UNUSED_PAD src0_sel:DWORD src1_sel:WORD_1
v_mfma_f32_16x16x32_bf16 v[244:247],v[104:107],v[152:155],v[244:247]
ds_read_u16_d16_hi v134,v134
v_exp_f16 v120,v121
v_mfma_f32_16x16x32_bf16 v[252:255],v[108:111],v[152:155],v[252:255]
ds_read_b64_tr_b16 v[92:93],v3 offset:18488
v_exp_f16 v122,v123
v_mfma_f32_16x16x32_bf16 v[144:147],v[64:67],v[40:43],v[112:115]
ds_read_u16 v133,v133
v_lshlrev_b32_sdwa v137,1,v137 dst_sel:DWORD dst_unused:UNUSED_PAD src0_sel:DWORD src1_sel:WORD_1
v_lshlrev_b32_sdwa v139,1,v139 dst_sel:DWORD dst_unused:UNUSED_PAD src0_sel:DWORD src1_sel:WORD_1
v_mfma_f32_16x16x32_bf16 v[152:155],v[64:67],v[44:47],v[112:115]
ds_read_b64_tr_b16 v[94:95],v3 offset:20024
v_exp_f16_sdwa v120,v121 dst_sel:WORD_1 dst_unused:UNUSED_PRESERVE src0_sel:WORD_1
v_mfma_f32_16x16x32_bf16 v[156:159],v[68:71],v[44:47],v[116:119]
ds_read_u16_d16_hi v135,v135
v_exp_f16_sdwa v122,v123 dst_sel:WORD_1 dst_unused:UNUSED_PRESERVE src0_sel:WORD_1
v_mfma_f32_16x16x32_bf16 v[148:151],v[68:71],v[40:43],v[116:119]
ds_read_b64_tr_b16 v[96:97],v3 offset:18544
v_pk_fma_f16 v120,v120,s55,s55
v_pk_fma_f16 v122,v122,s55,s55
v_mfma_f32_16x16x32_bf16 v[144:147],v[72:75],v[48:51],v[144:147]
ds_read_u16 v136,v136
v_rcp_f16 v121,v120
v_mfma_f32_16x16x32_bf16 v[152:155],v[72:75],v[52:55],v[152:155]
ds_read_b64_tr_b16 v[98:99],v3 offset:20080
ds_read_b64_tr_b16 v[100:101],v3 offset:18552
v_rcp_f16 v123,v122
v_mfma_f32_16x16x32_bf16 v[156:159],v[76:79],v[52:55],v[156:159]
ds_read_u16_d16_hi v138,v138
v_rcp_f16_sdwa v121,v120 dst_sel:WORD_1 dst_unused:UNUSED_PRESERVE src0_sel:WORD_1
v_mfma_f32_16x16x32_bf16 v[148:151],v[76:79],v[48:51],v[148:151]
ds_read_b64_tr_b16 v[102:103],v3 offset:20088
ds_read_b64_tr_b16 v[104:105],v3 offset:18608
v_rcp_f16_sdwa v123,v122 dst_sel:WORD_1 dst_unused:UNUSED_PRESERVE src0_sel:WORD_1
v_mfma_f32_16x16x32_bf16 v[144:147],v[80:83],v[56:59],v[144:147]
ds_read_u16 v137,v137
v_fma_mix_f32 v140,v140,v121,0 op_sel_hi:[0,1,0]
v_fma_mix_f32 v142,v142,v121,0 op_sel:[0,1,0] op_sel_hi:[0,1,0]
v_mfma_f32_16x16x32_bf16 v[152:155],v[80:83],v[60:63],v[152:155]
s_waitcnt lgkmcnt(8)
ds_read_b64_tr_b16 v[106:107],v3 offset:20144
ds_read_b64_tr_b16 v[108:109],v3 offset:18616
v_lshl_add_u64 v[128:129],v[128:129],0,v[130:131]
v_lshl_add_u64 v[130:131],v[132:133],0,v[134:135]
v_mfma_f32_16x16x32_bf16 v[156:159],v[84:87],v[60:63],v[156:159]
ds_read_u16_d16_hi v139,v139
v_fma_mix_f32 v141,v141,v123,0 op_sel_hi:[0,1,0]
v_fma_mix_f32 v143,v143,v123,0 op_sel:[0,1,0] op_sel_hi:[0,1,0]
v_mfma_f32_16x16x32_bf16 v[148:151],v[84:87],v[56:59],v[148:151]
ds_read_b64_tr_b16 v[110:111],v3 offset:20152
ds_read_b128 v[64:67],v4 offset:1536
v_lshlrev_b32_sdwa v144,1,v144 dst_sel:DWORD dst_unused:UNUSED_PAD src0_sel:DWORD src1_sel:WORD_1
v_lshlrev_b32_sdwa v146,1,v146 dst_sel:DWORD dst_unused:UNUSED_PAD src0_sel:DWORD src1_sel:WORD_1
v_mfma_f32_16x16x32_bf16 v[160:163],v[88:91],v[128:131],v[160:163]
ds_read_u16 v144,v144
ds_read_b128 v[68:71],v4 offset:1568
v_lshlrev_b32_sdwa v145,1,v145 dst_sel:DWORD dst_unused:UNUSED_PAD src0_sel:DWORD src1_sel:WORD_1
v_lshlrev_b32_sdwa v147,1,v147 dst_sel:DWORD dst_unused:UNUSED_PAD src0_sel:DWORD src1_sel:WORD_1
v_fma_mixlo_f16 v120,v156,s99,0
v_fma_mixlo_f16 v122,v157,s99,0
v_mfma_f32_16x16x32_bf16 v[168:171],v[92:95],v[128:131],v[168:171]
s_waitcnt lgkmcnt(12)
ds_read_u16_d16_hi v146,v146
ds_read_b128 v[72:75],v4 offset:1664
v_lshlrev_b32_sdwa v148,1,v148 dst_sel:DWORD dst_unused:UNUSED_PAD src0_sel:DWORD src1_sel:WORD_1
v_lshlrev_b32_sdwa v150,1,v150 dst_sel:DWORD dst_unused:UNUSED_PAD src0_sel:DWORD src1_sel:WORD_1
s_waitcnt lgkmcnt(6)
v_lshl_add_u64 v[136:137],v[136:137],0,v[138:139]
v_cvt_pk_bf16_f32 v138,v140,v142
v_mfma_f32_16x16x32_bf16 v[176:179],v[96:99],v[128:131],v[176:179]
s_setprio 1
ds_read_u16 v145,v145
ds_read_b128 v[76:79],v4 offset:1696
v_cvt_pk_bf16_f32 v139,v141,v143
v_fma_mixhi_f16 v120,v158,s99,0
v_fma_mixhi_f16 v122,v159,s99,0
v_lshlrev_b32_sdwa v149,1,v149 dst_sel:DWORD dst_unused:UNUSED_PAD src0_sel:DWORD src1_sel:WORD_1
v_mfma_f32_16x16x32_bf16 v[184:187],v[100:103],v[128:131],v[184:187]
ds_read_u16_d16_hi v147,v147
ds_read_b128 v[80:83],v4 offset:1792
v_lshlrev_b32_sdwa v151,1,v151 dst_sel:DWORD dst_unused:UNUSED_PAD src0_sel:DWORD src1_sel:WORD_1
v_pk_fma_f16 v121,v120,v120,s54
v_pk_fma_f16 v123,v122,v122,s54
v_pk_mul_f16 v121,v120,v121
v_mfma_f32_16x16x32_bf16 v[192:195],v[104:107],v[128:131],v[192:195]
s_waitcnt lgkmcnt(9)
ds_read_u16 v148,v148
ds_read_b128 v[84:87],v4 offset:1824
v_pk_mul_f16 v123,v122,v123
v_lshlrev_b32_sdwa v152,1,v152 dst_sel:DWORD dst_unused:UNUSED_PAD src0_sel:DWORD src1_sel:WORD_1
v_lshlrev_b32_sdwa v154,1,v154 dst_sel:DWORD dst_unused:UNUSED_PAD src0_sel:DWORD src1_sel:WORD_1
v_exp_f16 v120,v121
v_mfma_f32_16x16x32_bf16 v[200:203],v[108:111],v[128:131],v[200:203]
ds_read_u16_d16_hi v150,v150
v_exp_f16 v122,v123
v_lshlrev_b32_sdwa v153,1,v153 dst_sel:DWORD dst_unused:UNUSED_PAD src0_sel:DWORD src1_sel:WORD_1
v_mfma_f32_16x16x32_bf16 v[164:167],v[88:91],v[136:139],v[164:167]
ds_read_u16 v149,v149
v_lshlrev_b32_sdwa v155,1,v155 dst_sel:DWORD dst_unused:UNUSED_PAD src0_sel:DWORD src1_sel:WORD_1
v_exp_f16_sdwa v120,v121 dst_sel:WORD_1 dst_unused:UNUSED_PRESERVE src0_sel:WORD_1
v_exp_f16_sdwa v122,v123 dst_sel:WORD_1 dst_unused:UNUSED_PRESERVE src0_sel:WORD_1
v_mfma_f32_16x16x32_bf16 v[172:175],v[92:95],v[136:139],v[172:175]
ds_read_u16_d16_hi v151,v151
v_pk_fma_f16 v120,v120,s55,s55
v_pk_fma_f16 v122,v122,s55,s55
v_rcp_f16 v121,v120
v_mfma_f32_16x16x32_bf16 v[180:183],v[96:99],v[136:139],v[180:183]
ds_read_u16 v152,v152
v_rcp_f16 v123,v122
v_rcp_f16_sdwa v121,v120 dst_sel:WORD_1 dst_unused:UNUSED_PRESERVE src0_sel:WORD_1
v_mfma_f32_16x16x32_bf16 v[188:191],v[100:103],v[136:139],v[188:191]
ds_read_u16_d16_hi v154,v154
v_rcp_f16_sdwa v123,v122 dst_sel:WORD_1 dst_unused:UNUSED_PRESERVE src0_sel:WORD_1
v_fma_mix_f32 v156,v156,v121,0 op_sel_hi:[0,1,0]
v_mfma_f32_16x16x32_bf16 v[196:199],v[104:107],v[136:139],v[196:199]
s_setprio 0
ds_read_u16 v153,v153
v_fma_mix_f32 v158,v158,v121,0 op_sel:[0,1,0] op_sel_hi:[0,1,0]
s_waitcnt lgkmcnt(9)
v_lshl_add_u64 v[144:145],v[144:145],0,v[146:147]
s_waitcnt lgkmcnt(3)
v_lshl_add_u64 v[146:147],v[148:149],0,v[150:151]
v_fma_mix_f32 v157,v157,v123,0 op_sel_hi:[0,1,0]
v_mfma_f32_16x16x32_bf16 v[204:207],v[108:111],v[136:139],v[204:207]
ds_read_u16_d16_hi v155,v155
v_fma_mix_f32 v159,v159,v123,0 op_sel:[0,1,0] op_sel_hi:[0,1,0]
s_waitcnt lgkmcnt(0)
v_lshl_add_u64 v[152:153],v[152:153],0,v[154:155]
v_cvt_pk_bf16_f32 v154,v156,v158
v_cvt_pk_bf16_f32 v155,v157,v159
v_mfma_f32_16x16x32_bf16 v[208:211],v[88:91],v[144:147],v[208:211]
s_barrier
s_setprio 0
v_xor_b32 v9,64,v10
v_mul_f32 v160,v160,v64
v_mul_f32 v161,v161,v65
v_mul_f32 v162,v162,v66
v_mul_f32 v163,v163,v67
v_mfma_f32_16x16x32_bf16 v[216:219],v[92:95],v[144:147],v[216:219]
v_mul_f32 v168,v168,v68
v_mul_f32 v169,v169,v69
v_mul_f32 v170,v170,v70
v_mul_f32 v171,v171,v71
v_cvt_pk_bf16_f32 v16,v160,v161
v_mfma_f32_16x16x32_bf16 v[224:227],v[96:99],v[144:147],v[224:227]
v_cvt_pk_bf16_f32 v17,v162,v163
v_cvt_pk_bf16_f32 v18,v168,v169
v_cvt_pk_bf16_f32 v19,v170,v171
v_mul_f32_dpp v176,v176,v72 quad_perm:[1,0,3,2] row_mask:0xf bank_mask:0xf
v_mfma_f32_16x16x32_bf16 v[232:235],v[100:103],v[144:147],v[232:235]
v_mul_f32_dpp v177,v177,v73 quad_perm:[1,0,3,2] row_mask:0xf bank_mask:0xf
v_mul_f32_dpp v178,v178,v74 quad_perm:[1,0,3,2] row_mask:0xf bank_mask:0xf
v_mul_f32_dpp v179,v179,v75 quad_perm:[1,0,3,2] row_mask:0xf bank_mask:0xf
v_mul_f32_dpp v184,v184,v76 quad_perm:[1,0,3,2] row_mask:0xf bank_mask:0xf
v_mul_f32_dpp v185,v185,v77 quad_perm:[1,0,3,2] row_mask:0xf bank_mask:0xf
v_mfma_f32_16x16x32_bf16 v[240:243],v[104:107],v[144:147],v[240:243]
v_mul_f32_dpp v186,v186,v78 quad_perm:[1,0,3,2] row_mask:0xf bank_mask:0xf
v_mul_f32_dpp v187,v187,v79 quad_perm:[1,0,3,2] row_mask:0xf bank_mask:0xf
v_cvt_pk_bf16_f32 v24,v176,v177
v_cvt_pk_bf16_f32 v25,v178,v179
v_mfma_f32_16x16x32_bf16 v[248:251],v[108:111],v[144:147],v[248:251]
v_cvt_pk_bf16_f32 v26,v184,v185
v_cvt_pk_bf16_f32 v27,v186,v187
s_mov_b64 exec,s[12:13]
v_swap_b32 v16,v24
v_swap_b32 v17,v25
v_swap_b32 v18,v26
v_swap_b32 v19,v27
s_mov_b64 exec,-1
v_mfma_f32_16x16x32_bf16 v[212:215],v[88:91],v[152:155],v[212:215]
s_add_u32 s65,s61,1572864
buffer_store_dwordx4 v[16:19],v10,s[40:43],s65 offen offset:0 sc1
v_mfma_f32_16x16x32_bf16 v[220:223],v[92:95],v[152:155],v[220:223]
v_mul_f32 v192,v192,v80
v_mul_f32 v193,v193,v81
v_mul_f32 v194,v194,v82
v_mul_f32 v195,v195,v83
v_mfma_f32_16x16x32_bf16 v[228:231],v[96:99],v[152:155],v[228:231]
v_mul_f32 v200,v200,v84
v_mul_f32 v201,v201,v85
v_mul_f32 v202,v202,v86
v_mul_f32 v203,v203,v87
v_mfma_f32_16x16x32_bf16 v[236:239],v[100:103],v[152:155],v[236:239]
v_cvt_pk_bf16_f32 v32,v192,v193
v_cvt_pk_bf16_f32 v33,v194,v195
v_cvt_pk_bf16_f32 v34,v200,v201
v_cvt_pk_bf16_f32 v35,v202,v203
v_mul_f32_dpp v164,v164,v64 quad_perm:[1,0,3,2] row_mask:0xf bank_mask:0xf
v_mfma_f32_16x16x32_bf16 v[244:247],v[104:107],v[152:155],v[244:247]
v_mul_f32_dpp v165,v165,v65 quad_perm:[1,0,3,2] row_mask:0xf bank_mask:0xf
v_mul_f32_dpp v166,v166,v66 quad_perm:[1,0,3,2] row_mask:0xf bank_mask:0xf
v_mul_f32_dpp v167,v167,v67 quad_perm:[1,0,3,2] row_mask:0xf bank_mask:0xf
v_mul_f32_dpp v172,v172,v68 quad_perm:[1,0,3,2] row_mask:0xf bank_mask:0xf
v_mfma_f32_16x16x32_bf16 v[252:255],v[108:111],v[152:155],v[252:255]
s_add_u32 s65,s61,1572864
buffer_store_dwordx4 v[24:27],v9,s[40:43],s65 offen offset:384 sc1
v_mul_f32_dpp v173,v173,v69 quad_perm:[1,0,3,2] row_mask:0xf bank_mask:0xf
v_mul_f32_dpp v174,v174,v70 quad_perm:[1,0,3,2] row_mask:0xf bank_mask:0xf
v_mul_f32_dpp v175,v175,v71 quad_perm:[1,0,3,2] row_mask:0xf bank_mask:0xf
v_cvt_pk_bf16_f32 v20,v164,v165
v_cvt_pk_bf16_f32 v21,v166,v167
v_cvt_pk_bf16_f32 v22,v172,v173
v_cvt_pk_bf16_f32 v23,v174,v175
s_mov_b64 exec,s[12:13]
v_swap_b32 v32,v20
v_swap_b32 v33,v21
v_swap_b32 v34,v22
v_swap_b32 v35,v23
s_mov_b64 exec,-1
v_mul_f32 v180,v180,v72
v_mul_f32 v181,v181,v73
v_mul_f32 v182,v182,v74
v_mul_f32 v183,v183,v75
v_mul_f32 v188,v188,v76
v_mul_f32 v189,v189,v77
s_add_u32 s65,s61,1572864
buffer_store_dwordx4 v[32:35],v10,s[40:43],s65 offen offset:128 sc1
v_mul_f32 v190,v190,v78
v_mul_f32 v191,v191,v79
v_cvt_pk_bf16_f32 v28,v180,v181
v_cvt_pk_bf16_f32 v29,v182,v183
v_cvt_pk_bf16_f32 v30,v188,v189
v_cvt_pk_bf16_f32 v31,v190,v191
v_mul_f32_dpp v196,v196,v80 quad_perm:[1,0,3,2] row_mask:0xf bank_mask:0xf
v_mul_f32_dpp v197,v197,v81 quad_perm:[1,0,3,2] row_mask:0xf bank_mask:0xf
v_mul_f32_dpp v198,v198,v82 quad_perm:[1,0,3,2] row_mask:0xf bank_mask:0xf
v_mul_f32_dpp v199,v199,v83 quad_perm:[1,0,3,2] row_mask:0xf bank_mask:0xf
v_mul_f32_dpp v204,v204,v84 quad_perm:[1,0,3,2] row_mask:0xf bank_mask:0xf
v_mul_f32_dpp v205,v205,v85 quad_perm:[1,0,3,2] row_mask:0xf bank_mask:0xf
v_mul_f32_dpp v206,v206,v86 quad_perm:[1,0,3,2] row_mask:0xf bank_mask:0xf
v_mul_f32_dpp v207,v207,v87 quad_perm:[1,0,3,2] row_mask:0xf bank_mask:0xf
v_cvt_pk_bf16_f32 v36,v196,v197
v_cvt_pk_bf16_f32 v37,v198,v199
v_cvt_pk_bf16_f32 v38,v204,v205
v_cvt_pk_bf16_f32 v39,v206,v207
s_mov_b64 exec,s[12:13]
v_swap_b32 v28,v36
v_swap_b32 v29,v37
v_swap_b32 v30,v38
v_swap_b32 v31,v39
s_mov_b64 exec,-1
s_add_u32 s65,s61,1572864
buffer_store_dwordx4 v[20:23],v9,s[40:43],s65 offen offset:512 sc1
v_mul_f32 v208,v208,v64
v_mul_f32 v209,v209,v65
v_mul_f32 v210,v210,v66
v_mul_f32 v211,v211,v67
v_mul_f32 v216,v216,v68
v_mul_f32 v217,v217,v69
v_mul_f32 v218,v218,v70
v_mul_f32 v219,v219,v71
v_cvt_pk_bf16_f32 v40,v208,v209
v_cvt_pk_bf16_f32 v41,v210,v211
v_cvt_pk_bf16_f32 v42,v216,v217
v_cvt_pk_bf16_f32 v43,v218,v219
v_mul_f32_dpp v224,v224,v72 quad_perm:[1,0,3,2] row_mask:0xf bank_mask:0xf
v_mul_f32_dpp v225,v225,v73 quad_perm:[1,0,3,2] row_mask:0xf bank_mask:0xf
v_mul_f32_dpp v226,v226,v74 quad_perm:[1,0,3,2] row_mask:0xf bank_mask:0xf
v_mul_f32_dpp v227,v227,v75 quad_perm:[1,0,3,2] row_mask:0xf bank_mask:0xf
v_mul_f32_dpp v232,v232,v76 quad_perm:[1,0,3,2] row_mask:0xf bank_mask:0xf
v_mul_f32_dpp v233,v233,v77 quad_perm:[1,0,3,2] row_mask:0xf bank_mask:0xf
v_mul_f32_dpp v234,v234,v78 quad_perm:[1,0,3,2] row_mask:0xf bank_mask:0xf
v_mul_f32_dpp v235,v235,v79 quad_perm:[1,0,3,2] row_mask:0xf bank_mask:0xf
v_cvt_pk_bf16_f32 v48,v224,v225
v_cvt_pk_bf16_f32 v49,v226,v227
v_cvt_pk_bf16_f32 v50,v232,v233
v_cvt_pk_bf16_f32 v51,v234,v235
s_add_u32 s65,s61,1572864
buffer_store_dwordx4 v[28:31],v10,s[40:43],s65 offen offset:256 sc1
s_mov_b64 exec,s[12:13]
v_swap_b32 v40,v48
v_swap_b32 v41,v49
v_swap_b32 v42,v50
v_swap_b32 v43,v51
s_mov_b64 exec,-1
v_mul_f32 v240,v240,v80
v_mul_f32 v241,v241,v81
v_mul_f32 v242,v242,v82
v_mul_f32 v243,v243,v83
v_mul_f32 v248,v248,v84
v_mul_f32 v249,v249,v85
v_mul_f32 v250,v250,v86
v_mul_f32 v251,v251,v87
v_cvt_pk_bf16_f32 v56,v240,v241
v_cvt_pk_bf16_f32 v57,v242,v243
v_cvt_pk_bf16_f32 v58,v248,v249
v_cvt_pk_bf16_f32 v59,v250,v251
v_mul_f32_dpp v212,v212,v64 quad_perm:[1,0,3,2] row_mask:0xf bank_mask:0xf
v_mul_f32_dpp v213,v213,v65 quad_perm:[1,0,3,2] row_mask:0xf bank_mask:0xf
v_mul_f32_dpp v214,v214,v66 quad_perm:[1,0,3,2] row_mask:0xf bank_mask:0xf
v_mul_f32_dpp v215,v215,v67 quad_perm:[1,0,3,2] row_mask:0xf bank_mask:0xf
v_mul_f32_dpp v220,v220,v68 quad_perm:[1,0,3,2] row_mask:0xf bank_mask:0xf
s_add_u32 s65,s61,1572864
buffer_store_dwordx4 v[36:39],v9,s[40:43],s65 offen offset:640 sc1
v_mul_f32_dpp v221,v221,v69 quad_perm:[1,0,3,2] row_mask:0xf bank_mask:0xf
v_mul_f32_dpp v222,v222,v70 quad_perm:[1,0,3,2] row_mask:0xf bank_mask:0xf
v_mul_f32_dpp v223,v223,v71 quad_perm:[1,0,3,2] row_mask:0xf bank_mask:0xf
v_cvt_pk_bf16_f32 v44,v212,v213
v_cvt_pk_bf16_f32 v45,v214,v215
v_cvt_pk_bf16_f32 v46,v220,v221
v_cvt_pk_bf16_f32 v47,v222,v223
s_mov_b64 exec,s[12:13]
v_swap_b32 v56,v44
v_swap_b32 v57,v45
v_swap_b32 v58,v46
v_swap_b32 v59,v47
s_mov_b64 exec,-1
v_mul_f32 v228,v228,v72
s_mov_b32 s86,0xbe801d5a
v_mul_f32 v229,v229,v73
v_mul_f32 v230,v230,v74
v_mul_f32 v231,v231,v75
v_mul_f32 v236,v236,v76
v_mul_f32 v237,v237,v77
v_mul_f32 v238,v238,v78
v_mul_f32 v239,v239,v79
v_cvt_pk_bf16_f32 v52,v228,v229
v_cvt_pk_bf16_f32 v53,v230,v231
s_add_u32 s65,s61,1671168
buffer_store_dwordx4 v[40:43],v10,s[40:43],s65 offen offset:0 sc1
v_cvt_pk_bf16_f32 v54,v236,v237
v_cvt_pk_bf16_f32 v55,v238,v239
v_mul_f32_dpp v244,v244,v80 quad_perm:[1,0,3,2] row_mask:0xf bank_mask:0xf
v_mul_f32_dpp v245,v245,v81 quad_perm:[1,0,3,2] row_mask:0xf bank_mask:0xf
v_mul_f32_dpp v246,v246,v82 quad_perm:[1,0,3,2] row_mask:0xf bank_mask:0xf
v_mul_f32_dpp v247,v247,v83 quad_perm:[1,0,3,2] row_mask:0xf bank_mask:0xf
v_mul_f32_dpp v252,v252,v84 quad_perm:[1,0,3,2] row_mask:0xf bank_mask:0xf
v_mul_f32_dpp v253,v253,v85 quad_perm:[1,0,3,2] row_mask:0xf bank_mask:0xf
v_mul_f32_dpp v254,v254,v86 quad_perm:[1,0,3,2] row_mask:0xf bank_mask:0xf
v_mul_f32_dpp v255,v255,v87 quad_perm:[1,0,3,2] row_mask:0xf bank_mask:0xf
v_cvt_pk_bf16_f32 v60,v244,v245
v_cvt_pk_bf16_f32 v61,v246,v247
v_cvt_pk_bf16_f32 v62,v252,v253
v_cvt_pk_bf16_f32 v63,v254,v255
s_mov_b64 exec,s[12:13]
v_swap_b32 v52,v60
v_swap_b32 v53,v61
v_swap_b32 v54,v62
v_swap_b32 v55,v63
s_mov_b64 exec,-1
s_add_u32 s4,s10,8
s_cmp_ge_u32 s4,20
s_cselect_b32 s7,20,0
s_sub_u32 s4,s4,s7
s_mul_i32 s4,s4,3488
s_add_u32 s5,s10,10
s_cmp_ge_u32 s5,20
s_cselect_b32 s7,20,0
s_sub_u32 s5,s5,s7
s_mul_i32 s5,s5,3488
s_add_u32 s6,s10,12
s_cmp_ge_u32 s6,20
s_cselect_b32 s7,20,0
s_sub_u32 s6,s6,s7
s_mul_i32 s6,s6,3488
v_add_u32 v112,s4,v15
v_add_u32 v116,s5,v15
s_mov_b64 exec,s[8:9]
v_add_u32 v112,s5,v15
v_add_u32 v116,s6,v15
s_mov_b64 exec,-1
v_xor_b32 v113,16,v112
v_xor_b32 v117,16,v116
v_xor_b32 v114,32,v112
v_xor_b32 v118,32,v116
v_xor_b32 v115,48,v112
v_xor_b32 v119,48,v116
s_barrier
s_setprio 3
ds_read_b128 v[136:139],v116 offset:0
ds_read_b128 v[88:91],v11 offset:768
ds_read_b128 v[140:143],v116 offset:3488
ds_read_b128 v[128:131],v112 offset:0
ds_read_b128 v[132:135],v112 offset:3488
ds_read_b128 v[92:95],v12 offset:0
ds_read_b128 v[152:155],v117 offset:0
ds_read_b128 v[96:99],v11 offset:1920
ds_read_b128 v[156:159],v117 offset:3488
ds_read_b128 v[144:147],v113 offset:0
ds_read_b128 v[148:151],v113 offset:3488
ds_read_b128 v[100:103],v12 offset:1152
s_waitcnt lgkmcnt(10)
v_mfma_f32_16x16x32_bf16 v[160:163],v[88:91],v[136:139],0
s_waitcnt lgkmcnt(9)
s_add_u32 s65,s61,1671168
buffer_store_dwordx4 v[48:51],v9,s[40:43],s65 offen offset:384 sc1
v_mfma_f32_16x16x32_bf16 v[184:187],v[88:91],v[140:143],0
s_waitcnt lgkmcnt(6)
ds_read_b128 v[140:143],v116 offset:3552
ds_read_b128 v[88:91],v11 offset:5376
v_smfmac_f32_16x16x64_bf16 v[160:163],v[92:95],v[128:135],v13
ds_read_b128 v[128:131],v112 offset:64
v_smfmac_f32_16x16x64_bf16 v[184:187],v[92:95],v[132:139],v13
s_waitcnt lgkmcnt(7)
ds_read_b128 v[136:139],v116 offset:64
ds_read_b128 v[132:135],v112 offset:3552
v_mfma_f32_16x16x32_bf16 v[164:167],v[96:99],v[152:155],0
s_waitcnt lgkmcnt(8)
ds_read_b128 v[92:95],v12 offset:4608
v_add_f32 v120,v160,v161
v_mul_f32 v124,v160,v160
v_fmac_f32 v124,v161,v161
v_mfma_f32_16x16x32_bf16 v[188:191],v[96:99],v[156:159],0
s_waitcnt lgkmcnt(6)
ds_read_b128 v[156:159],v117 offset:3552
ds_read_b128 v[96:99],v11 offset:6528
v_add_f32 v121,v162,v163
v_mul_f32 v125,v162,v162
v_fmac_f32 v125,v163,v163
v_smfmac_f32_16x16x64_bf16 v[164:167],v[100:103],v[144:151],v13
s_add_u32 s65,s61,1671168
buffer_store_dwordx4 v[56:59],v10,s[40:43],s65 offen offset:128 sc1
ds_read_b128 v[144:147],v113 offset:64
v_add_f32 v122,v184,v185
v_mul_f32 v126,v184,v184
v_fmac_f32 v126,v185,v185
v_smfmac_f32_16x16x64_bf16 v[188:191],v[100:103],v[148:155],v13
s_waitcnt lgkmcnt(5)
ds_read_b128 v[152:155],v117 offset:64
ds_read_b128 v[148:151],v113 offset:3552
v_add_f32 v123,v186,v187
v_mul_f32 v127,v186,v186
v_fmac_f32 v127,v187,v187
v_mfma_f32_16x16x32_bf16 v[168:171],v[88:91],v[136:139],0
ds_read_b128 v[100:103],v12 offset:5760
v_add_f32 v120,v120,v164
v_fmac_f32 v124,v164,v164
v_add_f32 v120,v120,v165
v_fmac_f32 v124,v165,v165
v_mfma_f32_16x16x32_bf16 v[192:195],v[88:91],v[140:143],0
s_waitcnt lgkmcnt(6)
ds_read_b128 v[140:143],v116 offset:3616
ds_read_b128 v[88:91],v11 offset:9984
v_add_f32 v121,v121,v166
v_fmac_f32 v125,v166,v166
v_add_f32 v121,v121,v167
v_fmac_f32 v125,v167,v167
v_smfmac_f32_16x16x64_bf16 v[168:171],v[92:95],v[128:135],v13
s_add_u32 s65,s61,1671168
buffer_store_dwordx4 v[44:47],v9,s[40:43],s65 offen offset:512 sc1
ds_read_b128 v[128:131],v112 offset:128
v_add_f32 v122,v122,v188
v_fmac_f32 v126,v188,v188
v_add_f32 v122,v122,v189
v_fmac_f32 v126,v189,v189
v_smfmac_f32_16x16x64_bf16 v[192:195],v[92:95],v[132:139],v13
s_waitcnt lgkmcnt(5)
ds_read_b128 v[136:139],v116 offset:128
ds_read_b128 v[132:135],v112 offset:3616
v_add_f32 v123,v123,v190
v_fmac_f32 v127,v190,v190
v_add_f32 v123,v123,v191
v_fmac_f32 v127,v191,v191
v_mfma_f32_16x16x32_bf16 v[172:175],v[96:99],v[152:155],0
ds_read_b128 v[92:95],v12 offset:9216
v_add_f32 v120,v120,v168
v_fmac_f32 v124,v168,v168
v_add_f32 v120,v120,v169
v_fmac_f32 v124,v169,v169
v_mfma_f32_16x16x32_bf16 v[196:199],v[96:99],v[156:159],0
s_waitcnt lgkmcnt(6)
ds_read_b128 v[156:159],v117 offset:3616
ds_read_b128 v[96:99],v11 offset:11136
v_add_f32 v121,v121,v170
v_fmac_f32 v125,v170,v170
v_add_f32 v121,v121,v171
v_fmac_f32 v125,v171,v171
v_smfmac_f32_16x16x64_bf16 v[172:175],v[100:103],v[144:151],v13
s_add_u32 s65,s61,1671168
buffer_store_dwordx4 v[52:55],v10,s[40:43],s65 offen offset:256 sc1
ds_read_b128 v[144:147],v113 offset:128
v_add_f32 v122,v122,v192
v_fmac_f32 v126,v192,v192
v_add_f32 v122,v122,v193
v_fmac_f32 v126,v193,v193
v_smfmac_f32_16x16x64_bf16 v[196:199],v[100:103],v[148:155],v13
s_waitcnt lgkmcnt(5)
ds_read_b128 v[152:155],v117 offset:128
ds_read_b128 v[148:151],v113 offset:3616
v_add_f32 v123,v123,v194
v_fmac_f32 v127,v194,v194
v_add_f32 v123,v123,v195
v_fmac_f32 v127,v195,v195
v_mfma_f32_16x16x32_bf16 v[176:179],v[88:91],v[136:139],0
ds_read_b128 v[100:103],v12 offset:10368
v_add_f32 v120,v120,v172
v_fmac_f32 v124,v172,v172
v_add_f32 v120,v120,v173
v_fmac_f32 v124,v173,v173
v_mfma_f32_16x16x32_bf16 v[200:203],v[88:91],v[140:143],0
s_waitcnt lgkmcnt(6)
ds_read_b128 v[140:143],v118 offset:3488
ds_read_b128 v[88:91],v11 offset:3072
v_add_f32 v121,v121,v174
v_fmac_f32 v125,v174,v174
v_add_f32 v121,v121,v175
v_fmac_f32 v125,v175,v175
v_smfmac_f32_16x16x64_bf16 v[176:179],v[92:95],v[128:135],v13
s_add_u32 s65,s61,1671168
buffer_store_dwordx4 v[60:63],v9,s[40:43],s65 offen offset:640 sc1
ds_read_b128 v[128:131],v114 offset:0
v_add_f32 v122,v122,v196
v_fmac_f32 v126,v196,v196
v_add_f32 v122,v122,v197
v_fmac_f32 v126,v197,v197
v_smfmac_f32_16x16x64_bf16 v[200:203],v[92:95],v[132:139],v13
s_waitcnt lgkmcnt(5)
ds_read_b128 v[136:139],v118 offset:0
ds_read_b128 v[132:135],v114 offset:3488
v_add_f32 v123,v123,v198
v_fmac_f32 v127,v198,v198
v_add_f32 v123,v123,v199
v_fmac_f32 v127,v199,v199
v_mfma_f32_16x16x32_bf16 v[180:183],v[96:99],v[152:155],0
ds_read_b128 v[92:95],v12 offset:2304
v_add_f32 v120,v120,v176
v_fmac_f32 v124,v176,v176
v_add_f32 v120,v120,v177
v_fmac_f32 v124,v177,v177
v_mfma_f32_16x16x32_bf16 v[204:207],v[96:99],v[156:159],0
s_waitcnt lgkmcnt(6)
ds_read_b128 v[156:159],v119 offset:3488
ds_read_b128 v[96:99],v11 offset:4224
v_add_f32 v121,v121,v178
v_fmac_f32 v125,v178,v178
v_add_f32 v121,v121,v179
v_fmac_f32 v125,v179,v179
v_smfmac_f32_16x16x64_bf16 v[180:183],v[100:103],v[144:151],v13
ds_read_b128 v[144:147],v115 offset:0
v_add_f32 v122,v122,v200
v_fmac_f32 v126,v200,v200
v_add_f32 v122,v122,v201
v_fmac_f32 v126,v201,v201
v_smfmac_f32_16x16x64_bf16 v[204:207],v[100:103],v[148:155],v13
s_waitcnt lgkmcnt(5)
ds_read_b128 v[152:155],v119 offset:0
ds_read_b128 v[148:151],v115 offset:3488
v_add_f32 v123,v123,v202
v_fmac_f32 v127,v202,v202
v_add_f32 v123,v123,v203
v_fmac_f32 v127,v203,v203
v_mfma_f32_16x16x32_bf16 v[208:211],v[88:91],v[136:139],0
ds_read_b128 v[100:103],v12 offset:3456
v_add_f32 v120,v120,v180
v_fmac_f32 v124,v180,v180
v_add_f32 v120,v120,v181
v_fmac_f32 v124,v181,v181
v_mfma_f32_16x16x32_bf16 v[232:235],v[88:91],v[140:143],0
s_waitcnt lgkmcnt(6)
ds_read_b128 v[140:143],v118 offset:3552
ds_read_b128 v[88:91],v11 offset:7680
v_add_f32 v121,v121,v182
v_fmac_f32 v125,v182,v182
v_add_f32 v121,v121,v183
v_fmac_f32 v125,v183,v183
v_smfmac_f32_16x16x64_bf16 v[208:211],v[92:95],v[128:135],v13
ds_read_b128 v[128:131],v114 offset:64
v_add_f32 v122,v122,v204
v_fmac_f32 v126,v204,v204
v_add_f32 v122,v122,v205
v_fmac_f32 v126,v205,v205
v_smfmac_f32_16x16x64_bf16 v[232:235],v[92:95],v[132:139],v13
s_waitcnt lgkmcnt(5)
ds_read_b128 v[136:139],v118 offset:64
ds_read_b128 v[132:135],v114 offset:3552
v_add_f32 v123,v123,v206
v_fmac_f32 v127,v206,v206
v_add_f32 v123,v123,v207
v_fmac_f32 v127,v207,v207
v_mfma_f32_16x16x32_bf16 v[212:215],v[96:99],v[152:155],0
ds_read_b128 v[92:95],v12 offset:6912
v_add_f32 v120,v120,v208
v_fmac_f32 v124,v208,v208
v_add_f32 v120,v120,v209
v_fmac_f32 v124,v209,v209
v_mfma_f32_16x16x32_bf16 v[236:239],v[96:99],v[156:159],0
s_waitcnt lgkmcnt(6)
s_setprio 2
ds_read_b128 v[156:159],v119 offset:3552
ds_read_b128 v[96:99],v11 offset:8832
v_add_f32 v121,v121,v210
v_fmac_f32 v125,v210,v210
v_add_f32 v121,v121,v211
v_fmac_f32 v125,v211,v211
v_smfmac_f32_16x16x64_bf16 v[212:215],v[100:103],v[144:151],v13
ds_read_b128 v[144:147],v115 offset:64
v_add_f32 v122,v122,v232
v_fmac_f32 v126,v232,v232
v_add_f32 v122,v122,v233
v_fmac_f32 v126,v233,v233
v_smfmac_f32_16x16x64_bf16 v[236:239],v[100:103],v[148:155],v13
s_waitcnt lgkmcnt(5)
ds_read_b128 v[152:155],v119 offset:64
ds_read_b128 v[148:151],v115 offset:3552
v_add_f32 v123,v123,v234
v_fmac_f32 v127,v234,v234
v_add_f32 v123,v123,v235
v_fmac_f32 v127,v235,v235
v_mfma_f32_16x16x32_bf16 v[216:219],v[88:91],v[136:139],0
ds_read_b128 v[100:103],v12 offset:8064
v_add_f32 v120,v120,v212
v_fmac_f32 v124,v212,v212
v_add_f32 v120,v120,v213
v_fmac_f32 v124,v213,v213
v_add_f32 v121,v121,v214
v_mfma_f32_16x16x32_bf16 v[240:243],v[88:91],v[140:143],0
s_waitcnt lgkmcnt(6)
ds_read_b128 v[140:143],v118 offset:3616
ds_read_b128 v[88:91],v11 offset:12288
v_fmac_f32 v125,v214,v214
v_add_f32 v121,v121,v215
v_fmac_f32 v125,v215,v215
v_add_f32 v122,v122,v236
v_smfmac_f32_16x16x64_bf16 v[216:219],v[92:95],v[128:135],v13
ds_read_b128 v[128:131],v114 offset:128
v_fmac_f32 v126,v236,v236
v_add_f32 v122,v122,v237
v_fmac_f32 v126,v237,v237
v_add_f32 v123,v123,v238
v_fmac_f32 v127,v238,v238
v_smfmac_f32_16x16x64_bf16 v[240:243],v[92:95],v[132:139],v13
s_waitcnt lgkmcnt(5)
ds_read_b128 v[136:139],v118 offset:128
ds_read_b128 v[132:135],v114 offset:3616
v_add_f32 v123,v123,v239
v_fmac_f32 v127,v239,v239
v_add_u32 v112,v112,v8
v_add_u32 v116,v116,v8
v_mfma_f32_16x16x32_bf16 v[220:223],v[96:99],v[152:155],0
ds_read_b128 v[92:95],v12 offset:11520
ds_read_b128 v[104:107],v14 offset:0
v_add_f32 v120,v120,v216
v_fmac_f32 v124,v216,v216
v_add_f32 v120,v120,v217
v_fmac_f32 v124,v217,v217
v_mfma_f32_16x16x32_bf16 v[244:247],v[96:99],v[156:159],0
s_waitcnt lgkmcnt(7)
ds_read_b128 v[108:111],v4 offset:3584
ds_read_b128 v[156:159],v119 offset:3616
v_add_f32 v121,v121,v218
v_fmac_f32 v125,v218,v218
v_add_f32 v121,v121,v219
v_fmac_f32 v125,v219,v219
v_smfmac_f32_16x16x64_bf16 v[220:223],v[100:103],v[144:151],v13
ds_read_b128 v[96:99],v11 offset:13440
ds_read_b128 v[144:147],v115 offset:128
v_add_f32 v122,v122,v240
v_fmac_f32 v126,v240,v240
v_add_f32 v122,v122,v241
v_fmac_f32 v126,v241,v241
v_smfmac_f32_16x16x64_bf16 v[244:247],v[100:103],v[148:155],v13
s_waitcnt lgkmcnt(7)
ds_read_b128 v[152:155],v119 offset:128
ds_read_b128 v[148:151],v115 offset:3616
v_add_f32 v123,v123,v242
v_fmac_f32 v127,v242,v242
v_add_f32 v123,v123,v243
v_fmac_f32 v127,v243,v243
v_mfma_f32_16x16x32_bf16 v[224:227],v[88:91],v[136:139],0
ds_read_b128 v[100:103],v12 offset:12672
v_add_f32 v120,v120,v220
v_fmac_f32 v124,v220,v220
v_add_f32 v120,v120,v221
v_fmac_f32 v124,v221,v221
v_mfma_f32_16x16x32_bf16 v[248:251],v[88:91],v[140:143],0
s_waitcnt lgkmcnt(8)
s_setprio 1
ds_read_b128 v[140:143],v116 offset:192
ds_read_b128 v[88:91],v112 offset:3616
v_add_f32 v121,v121,v222
v_fmac_f32 v125,v222,v222
v_add_f32 v121,v121,v223
v_fmac_f32 v125,v223,v223
v_smfmac_f32_16x16x64_bf16 v[224:227],v[92:95],v[128:135],v13
ds_read_b128 v[128:131],v112 offset:3488
v_add_f32 v122,v122,v244
v_fmac_f32 v126,v244,v244
v_add_f32 v122,v122,v245
v_fmac_f32 v126,v245,v245
v_smfmac_f32_16x16x64_bf16 v[248:251],v[92:95],v[132:139],v13
s_waitcnt lgkmcnt(5)
ds_read_b128 v[136:139],v116 offset:0
ds_read_b128 v[132:135],v112 offset:3680
v_add_f32 v123,v123,v246
v_fmac_f32 v127,v246,v246
v_add_f32 v123,v123,v247
v_fmac_f32 v127,v247,v247
v_mfma_f32_16x16x32_bf16 v[228:231],v[96:99],v[152:155],0
ds_read_b128 v[92:95],v112 offset:3808
v_add_f32 v120,v120,v224
v_fmac_f32 v124,v224,v224
v_add_f32 v120,v120,v225
v_fmac_f32 v124,v225,v225
v_mfma_f32_16x16x32_bf16 v[252:255],v[96:99],v[156:159],0
s_waitcnt lgkmcnt(6)
ds_read_b128 v[156:159],v116 offset:256
ds_read_b128 v[96:99],v116 offset:128
v_add_f32 v121,v121,v226
v_fmac_f32 v125,v226,v226
v_add_f32 v121,v121,v227
v_fmac_f32 v125,v227,v227
v_smfmac_f32_16x16x64_bf16 v[228:231],v[100:103],v[144:151],v13
ds_read_b128 v[144:147],v112 offset:3552
v_add_f32 v122,v122,v248
v_fmac_f32 v126,v248,v248
v_add_f32 v122,v122,v249
v_fmac_f32 v126,v249,v249
v_smfmac_f32_16x16x64_bf16 v[252:255],v[100:103],v[148:155],v13
s_setprio 0
ds_read_b128 v[152:155],v116 offset:64
ds_read_b128 v[148:151],v112 offset:3744
v_add_f32 v123,v123,v250
v_fmac_f32 v127,v250,v250
v_add_f32 v123,v123,v251
v_fmac_f32 v127,v251,v251
ds_read_b128 v[100:103],v116 offset:320
v_add_f32 v120,v120,v228
v_fmac_f32 v124,v228,v228
v_add_f32 v120,v120,v229
v_fmac_f32 v124,v229,v229
v_add_f32 v121,v121,v230
v_fmac_f32 v125,v230,v230
v_add_f32 v121,v121,v231
v_fmac_f32 v125,v231,v231
s_waitcnt lgkmcnt(0)
s_barrier
s_setprio 0
v_add_f32 v122,v122,v252
v_fmac_f32 v126,v252,v252
v_add_f32 v122,v122,v253
v_fmac_f32 v126,v253,v253
v_add_f32 v123,v123,v254
v_fmac_f32 v127,v254,v254
v_add_f32 v123,v123,v255
v_fmac_f32 v127,v255,v255
s_setprio 3
ds_read_b128 v[56:59],v14 offset:256
ds_read_b128 v[60:63],v4 offset:3616
s_setprio 0
v_permlane32_swap_b32 v120,v122
v_permlane32_swap_b32 v121,v123
v_permlane32_swap_b32 v124,v126
v_permlane32_swap_b32 v125,v127
v_add_f32 v120,v120,v122
v_add_f32 v121,v121,v123
v_add_f32 v124,v124,v126
v_add_f32 v125,v125,v127
v_permlane16_swap_b32 v120,v121
s_nop 0
v_permlane16_swap_b32 v124,v125
v_add_f32 v113,v120,v121
v_add_f32 v114,v124,v125
v_mul_f32 v113,0x3c2aaaab,v113
v_mul_f32 v114,0x3c2aaaab,v114
v_mul_f32 v115,0x5e000000,v114
v_fma_f32 v114,-v113,v113,v114
v_pk_maximum3_f16 v7,v7,v115,v115 op_sel:[0,1,1] op_sel_hi:[1,1,1]
v_max_f32 v114,0,v114
v_add_f32 v114,0x3727c5ac,v114
v_rsq_f32 v114,v114
s_nop 0
v_mul_f32 v120,0x8befe000,v114
v_mul_f32 v121,0x8befe000,v114
v_mul_f32 v124,-v113,v120
v_mul_f32 v125,-v113,v120
v_permlane16_swap_b32 v120,v121
s_nop 0
v_permlane16_swap_b32 v124,v125
v_mov_b32 v122,v120
v_mov_b32 v123,v121
v_mov_b32 v126,v124
v_mov_b32 v127,v125
v_permlane32_swap_b32 v120,v122
v_permlane32_swap_b32 v124,v126
v_permlane32_swap_b32 v121,v123
v_permlane32_swap_b32 v125,v127
v_add_u32 v8,94432,v1
v_add_u32 v9,94432,v2
v_fma_f32 v160,v160,v120,v124
s_setprio 3
ds_read_b64_tr_b16 v[64:65],v1 offset:512
s_setprio 0
v_fma_f32 v161,v161,v120,v124
s_setprio 3
ds_read_b64_tr_b16 v[66:67],v1 offset:2560
s_setprio 0
v_fma_f32 v162,v162,v121,v125
s_setprio 3
ds_read_b128 v[112:115],v4 offset:384
s_setprio 0
v_fma_f32 v163,v163,v121,v125
s_setprio 3
ds_read_b64_tr_b16 v[68:69],v2 offset:512
s_setprio 0
v_fma_f32 v164,v164,v120,v124
s_setprio 3
ds_read_b64_tr_b16 v[70:71],v2 offset:2560
s_setprio 0
v_fma_f32 v165,v165,v120,v124
s_setprio 3
ds_read_b128 v[116:119],v4 offset:416
s_setprio 0
v_fma_f32 v166,v166,v121,v125
s_setprio 3
ds_read_b64_tr_b16 v[72:73],v1 offset:4608
s_setprio 0
v_fma_f32 v167,v167,v121,v125
s_setprio 3
ds_read_b64_tr_b16 v[74:75],v1 offset:6656
s_setprio 0
v_fma_f32 v208,v208,v120,v124
s_setprio 3
ds_read_b64_tr_b16 v[76:77],v2 offset:4608
s_setprio 0
v_fma_f32 v209,v209,v120,v124
s_setprio 3
ds_read_b64_tr_b16 v[78:79],v2 offset:6656
s_setprio 0
v_fma_f32 v210,v210,v121,v125
s_setprio 3
ds_read_b64_tr_b16 v[80:81],v1 offset:8704
s_setprio 0
v_fma_f32 v211,v211,v121,v125
s_setprio 3
ds_read_b64_tr_b16 v[82:83],v1 offset:10752
s_setprio 0
v_fma_f32 v212,v212,v120,v124
s_setprio 3
ds_read_b64_tr_b16 v[84:85],v2 offset:8704
s_setprio 0
v_fma_f32 v213,v213,v120,v124
s_setprio 3
ds_read_b64_tr_b16 v[86:87],v2 offset:10752
s_setprio 0
v_fma_f32 v214,v214,v121,v125
v_fma_f32 v215,v215,v121,v125
v_cvt_pk_bf16_f32 v16,v160,v161
v_cvt_pk_bf16_f32 v20,v162,v163
v_cvt_pk_bf16_f32 v17,v164,v165
v_cvt_pk_bf16_f32 v21,v166,v167
v_cvt_pk_bf16_f32 v18,v208,v209
v_cvt_pk_bf16_f32 v22,v210,v211
v_cvt_pk_bf16_f32 v19,v212,v213
v_cvt_pk_bf16_f32 v23,v214,v215
v_fma_f32 v168,v168,v120,v124
v_fma_f32 v169,v169,v120,v124
v_mfma_f32_16x16x32_bf16 v[160:163],v[104:107],v[128:131],v[108:111]
v_fma_f32 v170,v170,v121,v125
v_fma_f32 v171,v171,v121,v125
v_fma_f32 v172,v172,v120,v124
v_fma_f32 v173,v173,v120,v124
v_fma_f32 v174,v174,v121,v125
v_fma_f32 v175,v175,v121,v125
v_mfma_f32_16x16x32_bf16 v[164:167],v[104:107],v[132:135],v[108:111]
v_fma_f32 v216,v216,v120,v124
v_fma_f32 v217,v217,v120,v124
v_fma_f32 v218,v218,v121,v125
v_fma_f32 v219,v219,v121,v125
v_fma_f32 v220,v220,v120,v124
v_mfma_f32_16x16x32_bf16 v[208:211],v[104:107],v[136:139],v[108:111]
v_fma_f32 v221,v221,v120,v124
v_fma_f32 v222,v222,v121,v125
v_fma_f32 v223,v223,v121,v125
v_cvt_pk_bf16_f32 v24,v168,v169
v_cvt_pk_bf16_f32 v28,v170,v171
s_setprio 3
v_mfma_f32_16x16x32_bf16 v[212:215],v[104:107],v[140:143],v[108:111]
ds_read_b128 v[104:107],v14 offset:512
s_setprio 0
v_cvt_pk_bf16_f32 v25,v172,v173
v_cvt_pk_bf16_f32 v29,v174,v175
v_cvt_pk_bf16_f32 v26,v216,v217
v_cvt_pk_bf16_f32 v30,v218,v219
v_cvt_pk_bf16_f32 v27,v220,v221
v_cvt_pk_bf16_f32 v31,v222,v223
v_fma_f32 v176,v176,v120,v124
v_fma_f32 v177,v177,v120,v124
s_waitcnt lgkmcnt(15)
s_setprio 3
v_mfma_f32_16x16x32_bf16 v[168:171],v[56:59],v[128:131],v[60:63]
ds_read_b128 v[108:111],v4 offset:3712
s_setprio 0
v_fma_f32 v178,v178,v121,v125
v_fma_f32 v179,v179,v121,v125
v_fma_f32 v180,v180,v120,v124
v_fma_f32 v181,v181,v120,v124
v_fma_f32 v182,v182,v121,v125
v_fma_f32 v183,v183,v121,v125
v_mfma_f32_16x16x32_bf16 v[172:175],v[56:59],v[132:135],v[60:63]
v_fma_f32 v224,v224,v120,v124
v_fma_f32 v225,v225,v120,v124
v_fma_f32 v226,v226,v121,v125
v_fma_f32 v227,v227,v121,v125
v_fma_f32 v228,v228,v120,v124
v_mfma_f32_16x16x32_bf16 v[216:219],v[56:59],v[136:139],v[60:63]
v_fma_f32 v229,v229,v120,v124
v_fma_f32 v230,v230,v121,v125
v_fma_f32 v231,v231,v121,v125
v_cvt_pk_bf16_f32 v32,v176,v177
v_cvt_pk_bf16_f32 v36,v178,v179
s_setprio 3
v_mfma_f32_16x16x32_bf16 v[220:223],v[56:59],v[140:143],v[60:63]
ds_read_b128 v[56:59],v14 offset:768
s_setprio 0
v_cvt_pk_bf16_f32 v33,v180,v181
v_cvt_pk_bf16_f32 v37,v182,v183
v_cvt_pk_bf16_f32 v34,v224,v225
v_cvt_pk_bf16_f32 v38,v226,v227
v_cvt_pk_bf16_f32 v35,v228,v229
v_cvt_pk_bf16_f32 v39,v230,v231
v_fma_f32 v184,v184,v122,v126
s_waitcnt lgkmcnt(1)
s_setprio 3
v_mfma_f32_16x16x32_bf16 v[176:179],v[104:107],v[144:147],v[108:111]
ds_read_b128 v[60:63],v4 offset:3744
s_setprio 0
v_fma_f32 v185,v185,v122,v126
v_fma_f32 v186,v186,v123,v127
v_fma_f32 v187,v187,v123,v127
v_mfma_f32_16x16x32_bf16 v[128:131],v[64:67],v[16:19],v[112:115]
v_fma_f32 v188,v188,v122,v126
v_fma_f32 v189,v189,v122,v126
v_fma_f32 v190,v190,v123,v127
v_mfma_f32_16x16x32_bf16 v[180:183],v[104:107],v[148:151],v[108:111]
v_fma_f32 v191,v191,v123,v127
v_fma_f32 v232,v232,v122,v126
v_fma_f32 v233,v233,v122,v126
v_mfma_f32_16x16x32_bf16 v[136:139],v[64:67],v[20:23],v[112:115]
v_fma_f32 v234,v234,v123,v127
v_fma_f32 v235,v235,v123,v127
v_fma_f32 v236,v236,v122,v126
v_mfma_f32_16x16x32_bf16 v[224:227],v[104:107],v[152:155],v[108:111]
v_fma_f32 v237,v237,v122,v126
v_fma_f32 v238,v238,v123,v127
v_fma_f32 v239,v239,v123,v127
v_mfma_f32_16x16x32_bf16 v[140:143],v[68:71],v[20:23],v[116:119]
v_cvt_pk_bf16_f32 v40,v184,v185
v_cvt_pk_bf16_f32 v44,v186,v187
v_cvt_pk_bf16_f32 v41,v188,v189
s_setprio 3
v_mfma_f32_16x16x32_bf16 v[228:231],v[104:107],v[156:159],v[108:111]
ds_read_b128 v[104:107],v14 offset:1024
s_setprio 0
v_cvt_pk_bf16_f32 v45,v190,v191
v_cvt_pk_bf16_f32 v42,v232,v233
v_cvt_pk_bf16_f32 v46,v234,v235
s_setprio 3
v_mfma_f32_16x16x32_bf16 v[132:135],v[68:71],v[16:19],v[116:119]
ds_read_b128 v[108:111],v4 offset:3840
s_setprio 0
v_cvt_pk_bf16_f32 v43,v236,v237
v_cvt_pk_bf16_f32 v47,v238,v239
v_fma_f32 v192,v192,v122,v126
s_waitcnt lgkmcnt(2)
v_mfma_f32_16x16x32_bf16 v[184:187],v[56:59],v[144:147],v[60:63]
v_fma_f32 v193,v193,v122,v126
v_fma_f32 v194,v194,v123,v127
v_mfma_f32_16x16x32_bf16 v[128:131],v[72:75],v[24:27],v[128:131]
v_fma_f32 v195,v195,v123,v127
v_fma_f32 v196,v196,v122,v126
v_mfma_f32_16x16x32_bf16 v[188:191],v[56:59],v[148:151],v[60:63]
v_fma_f32 v197,v197,v122,v126
v_fma_f32 v198,v198,v123,v127
v_mfma_f32_16x16x32_bf16 v[136:139],v[72:75],v[28:31],v[136:139]
v_fma_f32 v199,v199,v123,v127
v_fma_f32 v240,v240,v122,v126
v_mfma_f32_16x16x32_bf16 v[232:235],v[56:59],v[152:155],v[60:63]
v_fma_f32 v241,v241,v122,v126
v_fma_f32 v242,v242,v123,v127
v_mfma_f32_16x16x32_bf16 v[140:143],v[76:79],v[28:31],v[140:143]
v_fma_f32 v243,v243,v123,v127
v_fma_f32 v244,v244,v122,v126
s_setprio 3
v_mfma_f32_16x16x32_bf16 v[236:239],v[56:59],v[156:159],v[60:63]
ds_read_b128 v[144:147],v14 offset:1280
s_setprio 0
v_fma_f32 v245,v245,v122,v126
v_fma_f32 v246,v246,v123,v127
s_setprio 3
v_mfma_f32_16x16x32_bf16 v[132:135],v[76:79],v[24:27],v[132:135]
ds_read_b128 v[148:151],v4 offset:3872
s_setprio 0
v_fma_f32 v247,v247,v123,v127
v_cvt_pk_bf16_f32 v48,v192,v193
v_cvt_pk_bf16_f32 v52,v194,v195
v_cvt_pk_bf16_f32 v49,v196,v197
v_cvt_pk_bf16_f32 v53,v198,v199
v_cvt_pk_bf16_f32 v50,v240,v241
s_nop 0
s_mov_b32 s86,0xbe801d5a
v_cvt_pk_bf16_f32 v54,v242,v243
v_cvt_pk_bf16_f32 v51,v244,v245
v_cvt_pk_bf16_f32 v55,v246,v247
v_fma_f32 v200,v200,v122,v126
s_waitcnt lgkmcnt(2)
v_mfma_f32_16x16x32_bf16 v[192:195],v[104:107],v[88:91],v[108:111]
v_fma_f32 v201,v201,v122,v126
v_fma_f32 v202,v202,v123,v127
v_fma_f32 v203,v203,v123,v127
v_mfma_f32_16x16x32_bf16 v[128:131],v[80:83],v[32:35],v[128:131]
v_fma_f32 v204,v204,v122,v126
v_fma_f32 v205,v205,v122,v126
v_fma_f32 v206,v206,v123,v127
v_mfma_f32_16x16x32_bf16 v[196:199],v[104:107],v[92:95],v[108:111]
v_fma_f32 v207,v207,v123,v127
v_fma_f32 v248,v248,v122,v126
v_fma_f32 v249,v249,v122,v126
v_mfma_f32_16x16x32_bf16 v[136:139],v[80:83],v[36:39],v[136:139]
v_fma_f32 v250,v250,v123,v127
v_fma_f32 v251,v251,v123,v127
v_fma_f32 v252,v252,v122,v126
v_mfma_f32_16x16x32_bf16 v[240:243],v[104:107],v[96:99],v[108:111]
v_fma_f32 v253,v253,v122,v126
v_fma_f32 v254,v254,v123,v127
v_fma_f32 v255,v255,v123,v127
v_mfma_f32_16x16x32_bf16 v[140:143],v[84:87],v[36:39],v[140:143]
v_cvt_pk_bf16_f32 v56,v200,v201
v_cvt_pk_bf16_f32 v60,v202,v203
v_cvt_pk_bf16_f32 v57,v204,v205
v_mfma_f32_16x16x32_bf16 v[244:247],v[104:107],v[100:103],v[108:111]
v_cvt_pk_bf16_f32 v61,v206,v207
v_cvt_pk_bf16_f32 v58,v248,v249
v_cvt_pk_bf16_f32 v62,v250,v251
v_mfma_f32_16x16x32_bf16 v[132:135],v[84:87],v[32:35],v[132:135]
v_cvt_pk_bf16_f32 v59,v252,v253
v_cvt_pk_bf16_f32 v63,v254,v255
s_waitcnt lgkmcnt(0)
v_mfma_f32_16x16x32_bf16 v[200:203],v[144:147],v[88:91],v[148:151]
s_barrier
s_setprio 3
ds_read_b64_tr_b16 v[104:105],v3 offset:12448
v_lshlrev_b32_sdwa v128,1,v128 dst_sel:DWORD dst_unused:UNUSED_PAD src0_sel:DWORD src1_sel:WORD_1
v_lshlrev_b32_sdwa v130,1,v130 dst_sel:DWORD dst_unused:UNUSED_PAD src0_sel:DWORD src1_sel:WORD_1
v_lshlrev_b32_sdwa v129,1,v129 dst_sel:DWORD dst_unused:UNUSED_PAD src0_sel:DWORD src1_sel:WORD_1
v_mfma_f32_16x16x32_bf16 v[204:207],v[144:147],v[92:95],v[148:151]
ds_read_u16 v128,v128
ds_read_b64_tr_b16 v[106:107],v3 offset:13984
v_lshlrev_b32_sdwa v131,1,v131 dst_sel:DWORD dst_unused:UNUSED_PAD src0_sel:DWORD src1_sel:WORD_1
v_fma_mixlo_f16 v120,v140,s99,0
v_fma_mixlo_f16 v122,v141,s99,0
v_mfma_f32_16x16x32_bf16 v[248:251],v[144:147],v[96:99],v[148:151]
ds_read_u16_d16_hi v130,v130
ds_read_b64_tr_b16 v[108:109],v3 offset:12456
v_lshlrev_b32_sdwa v132,1,v132 dst_sel:DWORD dst_unused:UNUSED_PAD src0_sel:DWORD src1_sel:WORD_1
v_lshlrev_b32_sdwa v134,1,v134 dst_sel:DWORD dst_unused:UNUSED_PAD src0_sel:DWORD src1_sel:WORD_1
v_mfma_f32_16x16x32_bf16 v[252:255],v[144:147],v[100:103],v[148:151]
ds_read_u16 v129,v129
ds_read_b64_tr_b16 v[110:111],v3 offset:13992
v_fma_mixhi_f16 v120,v142,s99,0
v_fma_mixhi_f16 v122,v143,s99,0
v_lshlrev_b32_sdwa v133,1,v133 dst_sel:DWORD dst_unused:UNUSED_PAD src0_sel:DWORD src1_sel:WORD_1
v_lshlrev_b32_sdwa v135,1,v135 dst_sel:DWORD dst_unused:UNUSED_PAD src0_sel:DWORD src1_sel:WORD_1
v_mfma_f32_16x16x32_bf16 v[144:147],v[64:67],v[40:43],v[112:115]
ds_read_u16_d16_hi v131,v131
ds_read_b64_tr_b16 v[88:89],v3 offset:12320
v_pk_fma_f16 v121,v120,v120,s54
v_pk_fma_f16 v123,v122,v122,s54
v_pk_mul_f16 v121,v120,v121
v_mfma_f32_16x16x32_bf16 v[152:155],v[64:67],v[44:47],v[112:115]
ds_read_u16 v132,v132
ds_read_b64_tr_b16 v[90:91],v3 offset:13856
v_pk_mul_f16 v123,v122,v123
v_lshlrev_b32_sdwa v136,1,v136 dst_sel:DWORD dst_unused:UNUSED_PAD src0_sel:DWORD src1_sel:WORD_1
v_mfma_f32_16x16x32_bf16 v[156:159],v[68:71],v[44:47],v[116:119]
ds_read_u16_d16_hi v134,v134
ds_read_b64_tr_b16 v[92:93],v3 offset:12328
v_lshlrev_b32_sdwa v138,1,v138 dst_sel:DWORD dst_unused:UNUSED_PAD src0_sel:DWORD src1_sel:WORD_1
v_exp_f16 v120,v121
v_mfma_f32_16x16x32_bf16 v[148:151],v[68:71],v[40:43],v[116:119]
ds_read_u16 v133,v133
ds_read_b64_tr_b16 v[94:95],v3 offset:13864
v_exp_f16 v122,v123
v_lshlrev_b32_sdwa v137,1,v137 dst_sel:DWORD dst_unused:UNUSED_PAD src0_sel:DWORD src1_sel:WORD_1
v_mfma_f32_16x16x32_bf16 v[144:147],v[72:75],v[48:51],v[144:147]
ds_read_u16_d16_hi v135,v135
ds_read_b64_tr_b16 v[96:97],v3 offset:12384
v_lshlrev_b32_sdwa v139,1,v139 dst_sel:DWORD dst_unused:UNUSED_PAD src0_sel:DWORD src1_sel:WORD_1
v_exp_f16_sdwa v120,v121 dst_sel:WORD_1 dst_unused:UNUSED_PRESERVE src0_sel:WORD_1
v_mfma_f32_16x16x32_bf16 v[152:155],v[72:75],v[52:55],v[152:155]
ds_read_u16 v136,v136
ds_read_b64_tr_b16 v[98:99],v3 offset:13920
v_exp_f16_sdwa v122,v123 dst_sel:WORD_1 dst_unused:UNUSED_PRESERVE src0_sel:WORD_1
v_mfma_f32_16x16x32_bf16 v[156:159],v[76:79],v[52:55],v[156:159]
ds_read_u16_d16_hi v138,v138
ds_read_b64_tr_b16 v[100:101],v3 offset:12392
v_pk_fma_f16 v120,v120,s55,s55
v_pk_fma_f16 v122,v122,s55,s55
v_rcp_f16 v121,v120
v_mfma_f32_16x16x32_bf16 v[148:151],v[76:79],v[48:51],v[148:151]
ds_read_u16 v137,v137
ds_read_b64_tr_b16 v[102:103],v3 offset:13928
v_rcp_f16 v123,v122
v_mfma_f32_16x16x32_bf16 v[144:147],v[80:83],v[56:59],v[144:147]
ds_read_u16_d16_hi v139,v139
ds_read_b64_tr_b16 v[64:65],v1 offset:12320
v_rcp_f16_sdwa v121,v120 dst_sel:WORD_1 dst_unused:UNUSED_PRESERVE src0_sel:WORD_1
v_mfma_f32_16x16x32_bf16 v[152:155],v[80:83],v[60:63],v[152:155]
ds_read_b64_tr_b16 v[66:67],v1 offset:14368
v_rcp_f16_sdwa v123,v122 dst_sel:WORD_1 dst_unused:UNUSED_PRESERVE src0_sel:WORD_1
v_fma_mix_f32 v140,v140,v121,0 op_sel_hi:[0,1,0]
v_mfma_f32_16x16x32_bf16 v[156:159],v[84:87],v[60:63],v[156:159]
ds_read_b128 v[112:115],v4 offset:512
v_fma_mix_f32 v142,v142,v121,0 op_sel:[0,1,0] op_sel_hi:[0,1,0]
s_waitcnt lgkmcnt(15)
v_lshl_add_u64 v[128:129],v[128:129],0,v[130:131]
s_waitcnt lgkmcnt(11)
v_lshl_add_u64 v[130:131],v[132:133],0,v[134:135]
v_mfma_f32_16x16x32_bf16 v[148:151],v[84:87],v[56:59],v[148:151]
ds_read_b64_tr_b16 v[68:69],v2 offset:12320
v_fma_mix_f32 v141,v141,v123,0 op_sel_hi:[0,1,0]
v_fma_mix_f32 v143,v143,v123,0 op_sel:[0,1,0] op_sel_hi:[0,1,0]
v_lshlrev_b32_sdwa v144,1,v144 dst_sel:DWORD dst_unused:UNUSED_PAD src0_sel:DWORD src1_sel:WORD_1
v_lshlrev_b32_sdwa v146,1,v146 dst_sel:DWORD dst_unused:UNUSED_PAD src0_sel:DWORD src1_sel:WORD_1
v_mfma_f32_16x16x32_bf16 v[160:163],v[88:91],v[128:131],v[160:163]
ds_read_u16 v144,v144
v_lshlrev_b32_sdwa v145,1,v145 dst_sel:DWORD dst_unused:UNUSED_PAD src0_sel:DWORD src1_sel:WORD_1
v_lshlrev_b32_sdwa v147,1,v147 dst_sel:DWORD dst_unused:UNUSED_PAD src0_sel:DWORD src1_sel:WORD_1
v_mfma_f32_16x16x32_bf16 v[168:171],v[92:95],v[128:131],v[168:171]
s_waitcnt lgkmcnt(10)
ds_read_b64_tr_b16 v[70:71],v2 offset:14368
ds_read_b128 v[116:119],v4 offset:544
v_fma_mixlo_f16 v120,v156,s99,0
v_fma_mixlo_f16 v122,v157,s99,0
v_mfma_f32_16x16x32_bf16 v[176:179],v[96:99],v[128:131],v[176:179]
s_waitcnt lgkmcnt(8)
ds_read_u16_d16_hi v146,v146
v_lshlrev_b32_sdwa v148,1,v148 dst_sel:DWORD dst_unused:UNUSED_PAD src0_sel:DWORD src1_sel:WORD_1
v_lshlrev_b32_sdwa v150,1,v150 dst_sel:DWORD dst_unused:UNUSED_PAD src0_sel:DWORD src1_sel:WORD_1
v_mfma_f32_16x16x32_bf16 v[184:187],v[100:103],v[128:131],v[184:187]
s_waitcnt lgkmcnt(8)
ds_read_b64_tr_b16 v[72:73],v1 offset:16416
ds_read_b64_tr_b16 v[74:75],v1 offset:18464
v_lshl_add_u64 v[136:137],v[136:137],0,v[138:139]
v_mfma_f32_16x16x32_bf16 v[192:195],v[104:107],v[128:131],v[192:195]
ds_read_u16 v145,v145
v_cvt_pk_bf16_f32 v138,v140,v142
v_cvt_pk_bf16_f32 v139,v141,v143
v_mfma_f32_16x16x32_bf16 v[200:203],v[108:111],v[128:131],v[200:203]
ds_read_b64_tr_b16 v[76:77],v2 offset:16416
ds_read_b64_tr_b16 v[78:79],v2 offset:18464
v_fma_mixhi_f16 v120,v158,s99,0
v_fma_mixhi_f16 v122,v159,s99,0
v_mfma_f32_16x16x32_bf16 v[164:167],v[88:91],v[136:139],v[164:167]
ds_read_u16_d16_hi v147,v147
v_lshlrev_b32_sdwa v149,1,v149 dst_sel:DWORD dst_unused:UNUSED_PAD src0_sel:DWORD src1_sel:WORD_1
v_lshlrev_b32_sdwa v151,1,v151 dst_sel:DWORD dst_unused:UNUSED_PAD src0_sel:DWORD src1_sel:WORD_1
v_mfma_f32_16x16x32_bf16 v[172:175],v[92:95],v[136:139],v[172:175]
ds_read_b64_tr_b16 v[80:81],v1 offset:20512
ds_read_b64_tr_b16 v[82:83],v1 offset:22560
v_pk_fma_f16 v121,v120,v120,s54
v_pk_fma_f16 v123,v122,v122,s54
v_mfma_f32_16x16x32_bf16 v[180:183],v[96:99],v[136:139],v[180:183]
ds_read_u16 v148,v148
v_pk_mul_f16 v121,v120,v121
v_pk_mul_f16 v123,v122,v123
v_mfma_f32_16x16x32_bf16 v[188:191],v[100:103],v[136:139],v[188:191]
ds_read_b64_tr_b16 v[84:85],v2 offset:20512
v_lshlrev_b32_sdwa v152,1,v152 dst_sel:DWORD dst_unused:UNUSED_PAD src0_sel:DWORD src1_sel:WORD_1
v_lshlrev_b32_sdwa v154,1,v154 dst_sel:DWORD dst_unused:UNUSED_PAD src0_sel:DWORD src1_sel:WORD_1
v_mfma_f32_16x16x32_bf16 v[196:199],v[104:107],v[136:139],v[196:199]
ds_read_u16_d16_hi v150,v150
v_exp_f16 v120,v121
v_mfma_f32_16x16x32_bf16 v[204:207],v[108:111],v[136:139],v[204:207]
s_waitcnt lgkmcnt(15)
ds_read_b64_tr_b16 v[86:87],v2 offset:22560
v_exp_f16 v122,v123
v_mfma_f32_16x16x32_bf16 v[128:131],v[64:67],v[16:19],v[112:115]
ds_read_u16 v149,v149
v_lshlrev_b32_sdwa v153,1,v153 dst_sel:DWORD dst_unused:UNUSED_PAD src0_sel:DWORD src1_sel:WORD_1
v_lshlrev_b32_sdwa v155,1,v155 dst_sel:DWORD dst_unused:UNUSED_PAD src0_sel:DWORD src1_sel:WORD_1
v_mfma_f32_16x16x32_bf16 v[136:139],v[64:67],v[20:23],v[112:115]
s_waitcnt lgkmcnt(14)
v_exp_f16_sdwa v120,v121 dst_sel:WORD_1 dst_unused:UNUSED_PRESERVE src0_sel:WORD_1
v_mfma_f32_16x16x32_bf16 v[140:143],v[68:71],v[20:23],v[116:119]
ds_read_u16_d16_hi v151,v151
v_exp_f16_sdwa v122,v123 dst_sel:WORD_1 dst_unused:UNUSED_PRESERVE src0_sel:WORD_1
v_mfma_f32_16x16x32_bf16 v[132:135],v[68:71],v[16:19],v[116:119]
s_waitcnt lgkmcnt(12)
v_pk_fma_f16 v120,v120,s55,s55
v_pk_fma_f16 v122,v122,s55,s55
v_mfma_f32_16x16x32_bf16 v[128:131],v[72:75],v[24:27],v[128:131]
ds_read_u16 v152,v152
v_rcp_f16 v121,v120
v_mfma_f32_16x16x32_bf16 v[136:139],v[72:75],v[28:31],v[136:139]
s_waitcnt lgkmcnt(10)
v_rcp_f16 v123,v122
v_mfma_f32_16x16x32_bf16 v[140:143],v[76:79],v[28:31],v[140:143]
ds_read_u16_d16_hi v154,v154
v_rcp_f16_sdwa v121,v120 dst_sel:WORD_1 dst_unused:UNUSED_PRESERVE src0_sel:WORD_1
v_mfma_f32_16x16x32_bf16 v[132:135],v[76:79],v[24:27],v[132:135]
s_waitcnt lgkmcnt(8)
v_rcp_f16_sdwa v123,v122 dst_sel:WORD_1 dst_unused:UNUSED_PRESERVE src0_sel:WORD_1
v_mfma_f32_16x16x32_bf16 v[128:131],v[80:83],v[32:35],v[128:131]
ds_read_u16 v153,v153
v_fma_mix_f32 v156,v156,v121,0 op_sel_hi:[0,1,0]
v_fma_mix_f32 v158,v158,v121,0 op_sel:[0,1,0] op_sel_hi:[0,1,0]
v_mfma_f32_16x16x32_bf16 v[136:139],v[80:83],v[36:39],v[136:139]
s_waitcnt lgkmcnt(3)
v_lshl_add_u64 v[144:145],v[144:145],0,v[146:147]
v_lshl_add_u64 v[146:147],v[148:149],0,v[150:151]
v_mfma_f32_16x16x32_bf16 v[140:143],v[84:87],v[36:39],v[140:143]
ds_read_u16_d16_hi v155,v155
v_fma_mix_f32 v157,v157,v123,0 op_sel_hi:[0,1,0]
v_fma_mix_f32 v159,v159,v123,0 op_sel:[0,1,0] op_sel_hi:[0,1,0]
v_mfma_f32_16x16x32_bf16 v[132:135],v[84:87],v[32:35],v[132:135]
v_lshlrev_b32_sdwa v128,1,v128 dst_sel:DWORD dst_unused:UNUSED_PAD src0_sel:DWORD src1_sel:WORD_1
v_lshlrev_b32_sdwa v130,1,v130 dst_sel:DWORD dst_unused:UNUSED_PAD src0_sel:DWORD src1_sel:WORD_1
v_mfma_f32_16x16x32_bf16 v[208:211],v[88:91],v[144:147],v[208:211]
ds_read_u16 v128,v128
v_lshlrev_b32_sdwa v129,1,v129 dst_sel:DWORD dst_unused:UNUSED_PAD src0_sel:DWORD src1_sel:WORD_1
v_lshlrev_b32_sdwa v131,1,v131 dst_sel:DWORD dst_unused:UNUSED_PAD src0_sel:DWORD src1_sel:WORD_1
v_mfma_f32_16x16x32_bf16 v[216:219],v[92:95],v[144:147],v[216:219]
v_fma_mixlo_f16 v120,v140,s99,0
v_fma_mixlo_f16 v122,v141,s99,0
v_mfma_f32_16x16x32_bf16 v[224:227],v[96:99],v[144:147],v[224:227]
ds_read_u16_d16_hi v130,v130
v_lshlrev_b32_sdwa v132,1,v132 dst_sel:DWORD dst_unused:UNUSED_PAD src0_sel:DWORD src1_sel:WORD_1
v_lshlrev_b32_sdwa v134,1,v134 dst_sel:DWORD dst_unused:UNUSED_PAD src0_sel:DWORD src1_sel:WORD_1
v_mfma_f32_16x16x32_bf16 v[232:235],v[100:103],v[144:147],v[232:235]
s_waitcnt lgkmcnt(2)
v_lshl_add_u64 v[152:153],v[152:153],0,v[154:155]
v_mfma_f32_16x16x32_bf16 v[240:243],v[104:107],v[144:147],v[240:243]
ds_read_u16 v129,v129
v_cvt_pk_bf16_f32 v154,v156,v158
v_cvt_pk_bf16_f32 v155,v157,v159
v_mfma_f32_16x16x32_bf16 v[248:251],v[108:111],v[144:147],v[248:251]
v_fma_mixhi_f16 v120,v142,s99,0
v_fma_mixhi_f16 v122,v143,s99,0
v_mfma_f32_16x16x32_bf16 v[212:215],v[88:91],v[152:155],v[212:215]
ds_read_u16_d16_hi v131,v131
v_lshlrev_b32_sdwa v133,1,v133 dst_sel:DWORD dst_unused:UNUSED_PAD src0_sel:DWORD src1_sel:WORD_1
v_lshlrev_b32_sdwa v135,1,v135 dst_sel:DWORD dst_unused:UNUSED_PAD src0_sel:DWORD src1_sel:WORD_1
v_mfma_f32_16x16x32_bf16 v[220:223],v[92:95],v[152:155],v[220:223]
s_setprio 2
ds_read_b64_tr_b16 v[88:89],v3 offset:18480
v_pk_fma_f16 v121,v120,v120,s54
v_pk_fma_f16 v123,v122,v122,s54
v_mfma_f32_16x16x32_bf16 v[228:231],v[96:99],v[152:155],v[228:231]
ds_read_u16 v132,v132
v_pk_mul_f16 v121,v120,v121
v_pk_mul_f16 v123,v122,v123
v_mfma_f32_16x16x32_bf16 v[236:239],v[100:103],v[152:155],v[236:239]
ds_read_b64_tr_b16 v[90:91],v3 offset:20016
v_lshlrev_b32_sdwa v136,1,v136 dst_sel:DWORD dst_unused:UNUSED_PAD src0_sel:DWORD src1_sel:WORD_1
v_lshlrev_b32_sdwa v138,1,v138 dst_sel:DWORD dst_unused:UNUSED_PAD src0_sel:DWORD src1_sel:WORD_1
v_mfma_f32_16x16x32_bf16 v[244:247],v[104:107],v[152:155],v[244:247]
ds_read_u16_d16_hi v134,v134
v_exp_f16 v120,v121
v_mfma_f32_16x16x32_bf16 v[252:255],v[108:111],v[152:155],v[252:255]
ds_read_b64_tr_b16 v[92:93],v3 offset:18488
v_exp_f16 v122,v123
v_mfma_f32_16x16x32_bf16 v[144:147],v[64:67],v[40:43],v[112:115]
ds_read_u16 v133,v133
v_lshlrev_b32_sdwa v137,1,v137 dst_sel:DWORD dst_unused:UNUSED_PAD src0_sel:DWORD src1_sel:WORD_1
v_lshlrev_b32_sdwa v139,1,v139 dst_sel:DWORD dst_unused:UNUSED_PAD src0_sel:DWORD src1_sel:WORD_1
v_mfma_f32_16x16x32_bf16 v[152:155],v[64:67],v[44:47],v[112:115]
ds_read_b64_tr_b16 v[94:95],v3 offset:20024
v_exp_f16_sdwa v120,v121 dst_sel:WORD_1 dst_unused:UNUSED_PRESERVE src0_sel:WORD_1
v_mfma_f32_16x16x32_bf16 v[156:159],v[68:71],v[44:47],v[116:119]
ds_read_u16_d16_hi v135,v135
v_exp_f16_sdwa v122,v123 dst_sel:WORD_1 dst_unused:UNUSED_PRESERVE src0_sel:WORD_1
v_mfma_f32_16x16x32_bf16 v[148:151],v[68:71],v[40:43],v[116:119]
ds_read_b64_tr_b16 v[96:97],v3 offset:18544
v_pk_fma_f16 v120,v120,s55,s55
v_pk_fma_f16 v122,v122,s55,s55
v_mfma_f32_16x16x32_bf16 v[144:147],v[72:75],v[48:51],v[144:147]
ds_read_u16 v136,v136
v_rcp_f16 v121,v120
v_mfma_f32_16x16x32_bf16 v[152:155],v[72:75],v[52:55],v[152:155]
ds_read_b64_tr_b16 v[98:99],v3 offset:20080
ds_read_b64_tr_b16 v[100:101],v3 offset:18552
v_rcp_f16 v123,v122
v_mfma_f32_16x16x32_bf16 v[156:159],v[76:79],v[52:55],v[156:159]
ds_read_u16_d16_hi v138,v138
v_rcp_f16_sdwa v121,v120 dst_sel:WORD_1 dst_unused:UNUSED_PRESERVE src0_sel:WORD_1
v_mfma_f32_16x16x32_bf16 v[148:151],v[76:79],v[48:51],v[148:151]
ds_read_b64_tr_b16 v[102:103],v3 offset:20088
ds_read_b64_tr_b16 v[104:105],v3 offset:18608
v_rcp_f16_sdwa v123,v122 dst_sel:WORD_1 dst_unused:UNUSED_PRESERVE src0_sel:WORD_1
v_mfma_f32_16x16x32_bf16 v[144:147],v[80:83],v[56:59],v[144:147]
ds_read_u16 v137,v137
v_fma_mix_f32 v140,v140,v121,0 op_sel_hi:[0,1,0]
v_fma_mix_f32 v142,v142,v121,0 op_sel:[0,1,0] op_sel_hi:[0,1,0]
v_mfma_f32_16x16x32_bf16 v[152:155],v[80:83],v[60:63],v[152:155]
s_waitcnt lgkmcnt(8)
ds_read_b64_tr_b16 v[106:107],v3 offset:20144
ds_read_b64_tr_b16 v[108:109],v3 offset:18616
v_lshl_add_u64 v[128:129],v[128:129],0,v[130:131]
v_lshl_add_u64 v[130:131],v[132:133],0,v[134:135]
v_mfma_f32_16x16x32_bf16 v[156:159],v[84:87],v[60:63],v[156:159]
ds_read_u16_d16_hi v139,v139
v_fma_mix_f32 v141,v141,v123,0 op_sel_hi:[0,1,0]
v_fma_mix_f32 v143,v143,v123,0 op_sel:[0,1,0] op_sel_hi:[0,1,0]
v_mfma_f32_16x16x32_bf16 v[148:151],v[84:87],v[56:59],v[148:151]
ds_read_b64_tr_b16 v[110:111],v3 offset:20152
ds_read_b64_tr_b16 v[64:65],v1 offset:12832
v_lshlrev_b32_sdwa v144,1,v144 dst_sel:DWORD dst_unused:UNUSED_PAD src0_sel:DWORD src1_sel:WORD_1
v_lshlrev_b32_sdwa v146,1,v146 dst_sel:DWORD dst_unused:UNUSED_PAD src0_sel:DWORD src1_sel:WORD_1
v_mfma_f32_16x16x32_bf16 v[160:163],v[88:91],v[128:131],v[160:163]
ds_read_u16 v144,v144
v_lshlrev_b32_sdwa v145,1,v145 dst_sel:DWORD dst_unused:UNUSED_PAD src0_sel:DWORD src1_sel:WORD_1
v_lshlrev_b32_sdwa v147,1,v147 dst_sel:DWORD dst_unused:UNUSED_PAD src0_sel:DWORD src1_sel:WORD_1
v_mfma_f32_16x16x32_bf16 v[168:171],v[92:95],v[128:131],v[168:171]
s_waitcnt lgkmcnt(11)
ds_read_b64_tr_b16 v[66:67],v1 offset:14880
ds_read_b128 v[112:115],v4 offset:640
v_fma_mixlo_f16 v120,v156,s99,0
v_fma_mixlo_f16 v122,v157,s99,0
v_mfma_f32_16x16x32_bf16 v[176:179],v[96:99],v[128:131],v[176:179]
s_waitcnt lgkmcnt(10)
ds_read_u16_d16_hi v146,v146
v_lshlrev_b32_sdwa v148,1,v148 dst_sel:DWORD dst_unused:UNUSED_PAD src0_sel:DWORD src1_sel:WORD_1
v_lshlrev_b32_sdwa v150,1,v150 dst_sel:DWORD dst_unused:UNUSED_PAD src0_sel:DWORD src1_sel:WORD_1
v_mfma_f32_16x16x32_bf16 v[184:187],v[100:103],v[128:131],v[184:187]
s_waitcnt lgkmcnt(6)
ds_read_b64_tr_b16 v[68:69],v2 offset:12832
ds_read_b64_tr_b16 v[70:71],v2 offset:14880
v_lshl_add_u64 v[136:137],v[136:137],0,v[138:139]
v_mfma_f32_16x16x32_bf16 v[192:195],v[104:107],v[128:131],v[192:195]
s_waitcnt lgkmcnt(7)
ds_read_u16 v145,v145
v_cvt_pk_bf16_f32 v138,v140,v142
v_cvt_pk_bf16_f32 v139,v141,v143
v_mfma_f32_16x16x32_bf16 v[200:203],v[108:111],v[128:131],v[200:203]
ds_read_b128 v[116:119],v4 offset:672
ds_read_b64_tr_b16 v[72:73],v1 offset:16928
v_fma_mixhi_f16 v120,v158,s99,0
v_fma_mixhi_f16 v122,v159,s99,0
v_mfma_f32_16x16x32_bf16 v[164:167],v[88:91],v[136:139],v[164:167]
ds_read_u16_d16_hi v147,v147
v_lshlrev_b32_sdwa v149,1,v149 dst_sel:DWORD dst_unused:UNUSED_PAD src0_sel:DWORD src1_sel:WORD_1
v_lshlrev_b32_sdwa v151,1,v151 dst_sel:DWORD dst_unused:UNUSED_PAD src0_sel:DWORD src1_sel:WORD_1
v_mfma_f32_16x16x32_bf16 v[172:175],v[92:95],v[136:139],v[172:175]
ds_read_b64_tr_b16 v[74:75],v1 offset:18976
ds_read_b64_tr_b16 v[76:77],v2 offset:16928
v_pk_fma_f16 v121,v120,v120,s54
v_pk_fma_f16 v123,v122,v122,s54
v_mfma_f32_16x16x32_bf16 v[180:183],v[96:99],v[136:139],v[180:183]
s_setprio 1
ds_read_u16 v148,v148
v_pk_mul_f16 v121,v120,v121
v_pk_mul_f16 v123,v122,v123
v_mfma_f32_16x16x32_bf16 v[188:191],v[100:103],v[136:139],v[188:191]
ds_read_b64_tr_b16 v[78:79],v2 offset:18976
v_lshlrev_b32_sdwa v152,1,v152 dst_sel:DWORD dst_unused:UNUSED_PAD src0_sel:DWORD src1_sel:WORD_1
v_lshlrev_b32_sdwa v154,1,v154 dst_sel:DWORD dst_unused:UNUSED_PAD src0_sel:DWORD src1_sel:WORD_1
v_mfma_f32_16x16x32_bf16 v[196:199],v[104:107],v[136:139],v[196:199]
ds_read_u16_d16_hi v150,v150
v_exp_f16 v120,v121
v_mfma_f32_16x16x32_bf16 v[204:207],v[108:111],v[136:139],v[204:207]
s_waitcnt lgkmcnt(12)
ds_read_b64_tr_b16 v[80:81],v1 offset:21024
v_exp_f16 v122,v123
v_mfma_f32_16x16x32_bf16 v[128:131],v[64:67],v[16:19],v[112:115]
ds_read_u16 v149,v149
v_lshlrev_b32_sdwa v153,1,v153 dst_sel:DWORD dst_unused:UNUSED_PAD src0_sel:DWORD src1_sel:WORD_1
v_lshlrev_b32_sdwa v155,1,v155 dst_sel:DWORD dst_unused:UNUSED_PAD src0_sel:DWORD src1_sel:WORD_1
v_mfma_f32_16x16x32_bf16 v[136:139],v[64:67],v[20:23],v[112:115]
s_waitcnt lgkmcnt(9)
ds_read_b64_tr_b16 v[82:83],v1 offset:23072
v_exp_f16_sdwa v120,v121 dst_sel:WORD_1 dst_unused:UNUSED_PRESERVE src0_sel:WORD_1
v_mfma_f32_16x16x32_bf16 v[140:143],v[68:71],v[20:23],v[116:119]
ds_read_u16_d16_hi v151,v151
v_exp_f16_sdwa v122,v123 dst_sel:WORD_1 dst_unused:UNUSED_PRESERVE src0_sel:WORD_1
v_mfma_f32_16x16x32_bf16 v[132:135],v[68:71],v[16:19],v[116:119]
s_waitcnt lgkmcnt(8)
ds_read_b64_tr_b16 v[84:85],v2 offset:21024
v_pk_fma_f16 v120,v120,s55,s55
v_pk_fma_f16 v122,v122,s55,s55
v_mfma_f32_16x16x32_bf16 v[128:131],v[72:75],v[24:27],v[128:131]
ds_read_u16 v152,v152
v_rcp_f16 v121,v120
v_mfma_f32_16x16x32_bf16 v[136:139],v[72:75],v[28:31],v[136:139]
s_waitcnt lgkmcnt(7)
ds_read_b64_tr_b16 v[86:87],v2 offset:23072
v_rcp_f16 v123,v122
v_mfma_f32_16x16x32_bf16 v[140:143],v[76:79],v[28:31],v[140:143]
ds_read_u16_d16_hi v154,v154
v_rcp_f16_sdwa v121,v120 dst_sel:WORD_1 dst_unused:UNUSED_PRESERVE src0_sel:WORD_1
v_mfma_f32_16x16x32_bf16 v[132:135],v[76:79],v[24:27],v[132:135]
s_waitcnt lgkmcnt(5)
v_rcp_f16_sdwa v123,v122 dst_sel:WORD_1 dst_unused:UNUSED_PRESERVE src0_sel:WORD_1
v_mfma_f32_16x16x32_bf16 v[128:131],v[80:83],v[32:35],v[128:131]
ds_read_u16 v153,v153
v_fma_mix_f32 v156,v156,v121,0 op_sel_hi:[0,1,0]
v_fma_mix_f32 v158,v158,v121,0 op_sel:[0,1,0] op_sel_hi:[0,1,0]
v_mfma_f32_16x16x32_bf16 v[136:139],v[80:83],v[36:39],v[136:139]
s_waitcnt lgkmcnt(2)
s_setprio 0
v_lshl_add_u64 v[144:145],v[144:145],0,v[146:147]
v_lshl_add_u64 v[146:147],v[148:149],0,v[150:151]
v_mfma_f32_16x16x32_bf16 v[140:143],v[84:87],v[36:39],v[140:143]
ds_read_u16_d16_hi v155,v155
v_fma_mix_f32 v157,v157,v123,0 op_sel_hi:[0,1,0]
v_fma_mix_f32 v159,v159,v123,0 op_sel:[0,1,0] op_sel_hi:[0,1,0]
v_mfma_f32_16x16x32_bf16 v[132:135],v[84:87],v[32:35],v[132:135]
v_lshlrev_b32_sdwa v128,1,v128 dst_sel:DWORD dst_unused:UNUSED_PAD src0_sel:DWORD src1_sel:WORD_1
v_lshlrev_b32_sdwa v130,1,v130 dst_sel:DWORD dst_unused:UNUSED_PAD src0_sel:DWORD src1_sel:WORD_1
v_mfma_f32_16x16x32_bf16 v[208:211],v[88:91],v[144:147],v[208:211]
s_barrier
s_setprio 3
ds_read_u16 v128,v128
v_lshlrev_b32_sdwa v129,1,v129 dst_sel:DWORD dst_unused:UNUSED_PAD src0_sel:DWORD src1_sel:WORD_1
v_lshlrev_b32_sdwa v131,1,v131 dst_sel:DWORD dst_unused:UNUSED_PAD src0_sel:DWORD src1_sel:WORD_1
v_mfma_f32_16x16x32_bf16 v[216:219],v[92:95],v[144:147],v[216:219]
v_fma_mixlo_f16 v120,v140,s99,0
v_fma_mixlo_f16 v122,v141,s99,0
v_mfma_f32_16x16x32_bf16 v[224:227],v[96:99],v[144:147],v[224:227]
ds_read_u16_d16_hi v130,v130
v_lshlrev_b32_sdwa v132,1,v132 dst_sel:DWORD dst_unused:UNUSED_PAD src0_sel:DWORD src1_sel:WORD_1
v_lshlrev_b32_sdwa v134,1,v134 dst_sel:DWORD dst_unused:UNUSED_PAD src0_sel:DWORD src1_sel:WORD_1
v_mfma_f32_16x16x32_bf16 v[232:235],v[100:103],v[144:147],v[232:235]
s_waitcnt lgkmcnt(2)
v_lshl_add_u64 v[152:153],v[152:153],0,v[154:155]
v_mfma_f32_16x16x32_bf16 v[240:243],v[104:107],v[144:147],v[240:243]
ds_read_u16 v129,v129
v_cvt_pk_bf16_f32 v154,v156,v158
v_cvt_pk_bf16_f32 v155,v157,v159
v_mfma_f32_16x16x32_bf16 v[248:251],v[108:111],v[144:147],v[248:251]
v_fma_mixhi_f16 v120,v142,s99,0
v_fma_mixhi_f16 v122,v143,s99,0
v_mfma_f32_16x16x32_bf16 v[212:215],v[88:91],v[152:155],v[212:215]
ds_read_u16_d16_hi v131,v131
v_lshlrev_b32_sdwa v133,1,v133 dst_sel:DWORD dst_unused:UNUSED_PAD src0_sel:DWORD src1_sel:WORD_1
v_lshlrev_b32_sdwa v135,1,v135 dst_sel:DWORD dst_unused:UNUSED_PAD src0_sel:DWORD src1_sel:WORD_1
v_mfma_f32_16x16x32_bf16 v[220:223],v[92:95],v[152:155],v[220:223]
ds_read_b64_tr_b16 v[88:89],v3 offset:0
v_pk_fma_f16 v121,v120,v120,s54
v_pk_fma_f16 v123,v122,v122,s54
v_mfma_f32_16x16x32_bf16 v[228:231],v[96:99],v[152:155],v[228:231]
ds_read_u16 v132,v132
v_pk_mul_f16 v121,v120,v121
v_pk_mul_f16 v123,v122,v123
v_mfma_f32_16x16x32_bf16 v[236:239],v[100:103],v[152:155],v[236:239]
ds_read_b64_tr_b16 v[90:91],v3 offset:1536
v_lshlrev_b32_sdwa v136,1,v136 dst_sel:DWORD dst_unused:UNUSED_PAD src0_sel:DWORD src1_sel:WORD_1
v_lshlrev_b32_sdwa v138,1,v138 dst_sel:DWORD dst_unused:UNUSED_PAD src0_sel:DWORD src1_sel:WORD_1
v_mfma_f32_16x16x32_bf16 v[244:247],v[104:107],v[152:155],v[244:247]
ds_read_u16_d16_hi v134,v134
v_exp_f16 v120,v121
v_mfma_f32_16x16x32_bf16 v[252:255],v[108:111],v[152:155],v[252:255]
ds_read_b64_tr_b16 v[92:93],v3 offset:8
v_exp_f16 v122,v123
v_mfma_f32_16x16x32_bf16 v[144:147],v[64:67],v[40:43],v[112:115]
ds_read_u16 v133,v133
v_lshlrev_b32_sdwa v137,1,v137 dst_sel:DWORD dst_unused:UNUSED_PAD src0_sel:DWORD src1_sel:WORD_1
v_lshlrev_b32_sdwa v139,1,v139 dst_sel:DWORD dst_unused:UNUSED_PAD src0_sel:DWORD src1_sel:WORD_1
v_mfma_f32_16x16x32_bf16 v[152:155],v[64:67],v[44:47],v[112:115]
ds_read_b64_tr_b16 v[94:95],v3 offset:1544
v_exp_f16_sdwa v120,v121 dst_sel:WORD_1 dst_unused:UNUSED_PRESERVE src0_sel:WORD_1
v_mfma_f32_16x16x32_bf16 v[156:159],v[68:71],v[44:47],v[116:119]
ds_read_u16_d16_hi v135,v135
v_exp_f16_sdwa v122,v123 dst_sel:WORD_1 dst_unused:UNUSED_PRESERVE src0_sel:WORD_1
v_mfma_f32_16x16x32_bf16 v[148:151],v[68:71],v[40:43],v[116:119]
ds_read_b64_tr_b16 v[96:97],v3 offset:64
v_pk_fma_f16 v120,v120,s55,s55
v_pk_fma_f16 v122,v122,s55,s55
v_mfma_f32_16x16x32_bf16 v[144:147],v[72:75],v[48:51],v[144:147]
ds_read_u16 v136,v136
v_rcp_f16 v121,v120
v_mfma_f32_16x16x32_bf16 v[152:155],v[72:75],v[52:55],v[152:155]
ds_read_b64_tr_b16 v[98:99],v3 offset:1600
ds_read_b64_tr_b16 v[100:101],v3 offset:72
v_rcp_f16 v123,v122
v_mfma_f32_16x16x32_bf16 v[156:159],v[76:79],v[52:55],v[156:159]
ds_read_u16_d16_hi v138,v138
v_rcp_f16_sdwa v121,v120 dst_sel:WORD_1 dst_unused:UNUSED_PRESERVE src0_sel:WORD_1
v_mfma_f32_16x16x32_bf16 v[148:151],v[76:79],v[48:51],v[148:151]
ds_read_b64_tr_b16 v[102:103],v3 offset:1608
ds_read_b64_tr_b16 v[104:105],v3 offset:128
v_rcp_f16_sdwa v123,v122 dst_sel:WORD_1 dst_unused:UNUSED_PRESERVE src0_sel:WORD_1
v_mfma_f32_16x16x32_bf16 v[144:147],v[80:83],v[56:59],v[144:147]
ds_read_u16 v137,v137
v_fma_mix_f32 v140,v140,v121,0 op_sel_hi:[0,1,0]
v_fma_mix_f32 v142,v142,v121,0 op_sel:[0,1,0] op_sel_hi:[0,1,0]
v_mfma_f32_16x16x32_bf16 v[152:155],v[80:83],v[60:63],v[152:155]
s_waitcnt lgkmcnt(8)
ds_read_b64_tr_b16 v[106:107],v3 offset:1664
ds_read_b64_tr_b16 v[108:109],v3 offset:136
v_lshl_add_u64 v[128:129],v[128:129],0,v[130:131]
v_lshl_add_u64 v[130:131],v[132:133],0,v[134:135]
v_mfma_f32_16x16x32_bf16 v[156:159],v[84:87],v[60:63],v[156:159]
ds_read_u16_d16_hi v139,v139
v_fma_mix_f32 v141,v141,v123,0 op_sel_hi:[0,1,0]
v_fma_mix_f32 v143,v143,v123,0 op_sel:[0,1,0] op_sel_hi:[0,1,0]
v_mfma_f32_16x16x32_bf16 v[148:151],v[84:87],v[56:59],v[148:151]
ds_read_b64_tr_b16 v[110:111],v3 offset:1672
ds_read_b64_tr_b16 v[64:65],v1 offset:0
v_lshlrev_b32_sdwa v144,1,v144 dst_sel:DWORD dst_unused:UNUSED_PAD src0_sel:DWORD src1_sel:WORD_1
v_lshlrev_b32_sdwa v146,1,v146 dst_sel:DWORD dst_unused:UNUSED_PAD src0_sel:DWORD src1_sel:WORD_1
v_mfma_f32_16x16x32_bf16 v[160:163],v[88:91],v[128:131],v[160:163]
ds_read_u16 v144,v144
v_lshlrev_b32_sdwa v145,1,v145 dst_sel:DWORD dst_unused:UNUSED_PAD src0_sel:DWORD src1_sel:WORD_1
v_lshlrev_b32_sdwa v147,1,v147 dst_sel:DWORD dst_unused:UNUSED_PAD src0_sel:DWORD src1_sel:WORD_1
v_mfma_f32_16x16x32_bf16 v[168:171],v[92:95],v[128:131],v[168:171]
s_waitcnt lgkmcnt(11)
ds_read_b64_tr_b16 v[66:67],v1 offset:2048
ds_read_b128 v[112:115],v4 offset:768
v_fma_mixlo_f16 v120,v156,s99,0
v_fma_mixlo_f16 v122,v157,s99,0
v_mfma_f32_16x16x32_bf16 v[176:179],v[96:99],v[128:131],v[176:179]
s_waitcnt lgkmcnt(10)
ds_read_u16_d16_hi v146,v146
v_lshlrev_b32_sdwa v148,1,v148 dst_sel:DWORD dst_unused:UNUSED_PAD src0_sel:DWORD src1_sel:WORD_1
v_lshlrev_b32_sdwa v150,1,v150 dst_sel:DWORD dst_unused:UNUSED_PAD src0_sel:DWORD src1_sel:WORD_1
v_mfma_f32_16x16x32_bf16 v[184:187],v[100:103],v[128:131],v[184:187]
s_waitcnt lgkmcnt(6)
ds_read_b64_tr_b16 v[68:69],v2 offset:0
ds_read_b64_tr_b16 v[70:71],v2 offset:2048
v_lshl_add_u64 v[136:137],v[136:137],0,v[138:139]
v_mfma_f32_16x16x32_bf16 v[192:195],v[104:107],v[128:131],v[192:195]
s_waitcnt lgkmcnt(7)
ds_read_u16 v145,v145
v_cvt_pk_bf16_f32 v138,v140,v142
v_cvt_pk_bf16_f32 v139,v141,v143
v_mfma_f32_16x16x32_bf16 v[200:203],v[108:111],v[128:131],v[200:203]
ds_read_b128 v[116:119],v4 offset:800
ds_read_b64_tr_b16 v[72:73],v1 offset:4096
v_fma_mixhi_f16 v120,v158,s99,0
v_fma_mixhi_f16 v122,v159,s99,0
v_mfma_f32_16x16x32_bf16 v[164:167],v[88:91],v[136:139],v[164:167]
ds_read_u16_d16_hi v147,v147
v_lshlrev_b32_sdwa v149,1,v149 dst_sel:DWORD dst_unused:UNUSED_PAD src0_sel:DWORD src1_sel:WORD_1
v_lshlrev_b32_sdwa v151,1,v151 dst_sel:DWORD dst_unused:UNUSED_PAD src0_sel:DWORD src1_sel:WORD_1
v_mfma_f32_16x16x32_bf16 v[172:175],v[92:95],v[136:139],v[172:175]
ds_read_b64_tr_b16 v[74:75],v1 offset:6144
ds_read_b64_tr_b16 v[76:77],v2 offset:4096
v_pk_fma_f16 v121,v120,v120,s54
v_pk_fma_f16 v123,v122,v122,s54
v_mfma_f32_16x16x32_bf16 v[180:183],v[96:99],v[136:139],v[180:183]
s_mov_b32 s86,0xbe801d5a
ds_read_u16 v148,v148
v_pk_mul_f16 v121,v120,v121
v_pk_mul_f16 v123,v122,v123
v_mfma_f32_16x16x32_bf16 v[188:191],v[100:103],v[136:139],v[188:191]
ds_read_b64_tr_b16 v[78:79],v2 offset:6144
v_lshlrev_b32_sdwa v152,1,v152 dst_sel:DWORD dst_unused:UNUSED_PAD src0_sel:DWORD src1_sel:WORD_1
v_lshlrev_b32_sdwa v154,1,v154 dst_sel:DWORD dst_unused:UNUSED_PAD src0_sel:DWORD src1_sel:WORD_1
v_mfma_f32_16x16x32_bf16 v[196:199],v[104:107],v[136:139],v[196:199]
ds_read_u16_d16_hi v150,v150
v_exp_f16 v120,v121
v_mfma_f32_16x16x32_bf16 v[204:207],v[108:111],v[136:139],v[204:207]
s_waitcnt lgkmcnt(12)
ds_read_b64_tr_b16 v[80:81],v1 offset:8192
v_exp_f16 v122,v123
v_mfma_f32_16x16x32_bf16 v[128:131],v[64:67],v[16:19],v[112:115]
ds_read_u16 v149,v149
v_lshlrev_b32_sdwa v153,1,v153 dst_sel:DWORD dst_unused:UNUSED_PAD src0_sel:DWORD src1_sel:WORD_1
v_lshlrev_b32_sdwa v155,1,v155 dst_sel:DWORD dst_unused:UNUSED_PAD src0_sel:DWORD src1_sel:WORD_1
v_mfma_f32_16x16x32_bf16 v[136:139],v[64:67],v[20:23],v[112:115]
s_waitcnt lgkmcnt(9)
ds_read_b64_tr_b16 v[82:83],v1 offset:10240
v_exp_f16_sdwa v120,v121 dst_sel:WORD_1 dst_unused:UNUSED_PRESERVE src0_sel:WORD_1
v_mfma_f32_16x16x32_bf16 v[140:143],v[68:71],v[20:23],v[116:119]
ds_read_u16_d16_hi v151,v151
v_exp_f16_sdwa v122,v123 dst_sel:WORD_1 dst_unused:UNUSED_PRESERVE src0_sel:WORD_1
v_mfma_f32_16x16x32_bf16 v[132:135],v[68:71],v[16:19],v[116:119]
s_waitcnt lgkmcnt(8)
ds_read_b64_tr_b16 v[84:85],v2 offset:8192
v_pk_fma_f16 v120,v120,s55,s55
v_pk_fma_f16 v122,v122,s55,s55
v_mfma_f32_16x16x32_bf16 v[128:131],v[72:75],v[24:27],v[128:131]
ds_read_u16 v152,v152
v_rcp_f16 v121,v120
v_mfma_f32_16x16x32_bf16 v[136:139],v[72:75],v[28:31],v[136:139]
s_waitcnt lgkmcnt(7)
ds_read_b64_tr_b16 v[86:87],v2 offset:10240
v_rcp_f16 v123,v122
v_mfma_f32_16x16x32_bf16 v[140:143],v[76:79],v[28:31],v[140:143]
ds_read_u16_d16_hi v154,v154
v_rcp_f16_sdwa v121,v120 dst_sel:WORD_1 dst_unused:UNUSED_PRESERVE src0_sel:WORD_1
v_mfma_f32_16x16x32_bf16 v[132:135],v[76:79],v[24:27],v[132:135]
s_waitcnt lgkmcnt(5)
v_rcp_f16_sdwa v123,v122 dst_sel:WORD_1 dst_unused:UNUSED_PRESERVE src0_sel:WORD_1
v_mfma_f32_16x16x32_bf16 v[128:131],v[80:83],v[32:35],v[128:131]
ds_read_u16 v153,v153
v_fma_mix_f32 v156,v156,v121,0 op_sel_hi:[0,1,0]
v_fma_mix_f32 v158,v158,v121,0 op_sel:[0,1,0] op_sel_hi:[0,1,0]
v_mfma_f32_16x16x32_bf16 v[136:139],v[80:83],v[36:39],v[136:139]
s_waitcnt lgkmcnt(2)
v_lshl_add_u64 v[144:145],v[144:145],0,v[146:147]
v_lshl_add_u64 v[146:147],v[148:149],0,v[150:151]
v_mfma_f32_16x16x32_bf16 v[140:143],v[84:87],v[36:39],v[140:143]
ds_read_u16_d16_hi v155,v155
v_fma_mix_f32 v157,v157,v123,0 op_sel_hi:[0,1,0]
v_fma_mix_f32 v159,v159,v123,0 op_sel:[0,1,0] op_sel_hi:[0,1,0]
v_mfma_f32_16x16x32_bf16 v[132:135],v[84:87],v[32:35],v[132:135]
v_lshlrev_b32_sdwa v128,1,v128 dst_sel:DWORD dst_unused:UNUSED_PAD src0_sel:DWORD src1_sel:WORD_1
v_lshlrev_b32_sdwa v130,1,v130 dst_sel:DWORD dst_unused:UNUSED_PAD src0_sel:DWORD src1_sel:WORD_1
v_mfma_f32_16x16x32_bf16 v[208:211],v[88:91],v[144:147],v[208:211]
ds_read_u16 v128,v128
v_lshlrev_b32_sdwa v129,1,v129 dst_sel:DWORD dst_unused:UNUSED_PAD src0_sel:DWORD src1_sel:WORD_1
v_lshlrev_b32_sdwa v131,1,v131 dst_sel:DWORD dst_unused:UNUSED_PAD src0_sel:DWORD src1_sel:WORD_1
v_mfma_f32_16x16x32_bf16 v[216:219],v[92:95],v[144:147],v[216:219]
v_fma_mixlo_f16 v120,v140,s99,0
v_fma_mixlo_f16 v122,v141,s99,0
v_mfma_f32_16x16x32_bf16 v[224:227],v[96:99],v[144:147],v[224:227]
ds_read_u16_d16_hi v130,v130
v_lshlrev_b32_sdwa v132,1,v132 dst_sel:DWORD dst_unused:UNUSED_PAD src0_sel:DWORD src1_sel:WORD_1
v_lshlrev_b32_sdwa v134,1,v134 dst_sel:DWORD dst_unused:UNUSED_PAD src0_sel:DWORD src1_sel:WORD_1
v_mfma_f32_16x16x32_bf16 v[232:235],v[100:103],v[144:147],v[232:235]
s_waitcnt lgkmcnt(2)
v_lshl_add_u64 v[152:153],v[152:153],0,v[154:155]
v_mfma_f32_16x16x32_bf16 v[240:243],v[104:107],v[144:147],v[240:243]
ds_read_u16 v129,v129
v_cvt_pk_bf16_f32 v154,v156,v158
v_cvt_pk_bf16_f32 v155,v157,v159
v_mfma_f32_16x16x32_bf16 v[248:251],v[108:111],v[144:147],v[248:251]
s_setprio 2
v_fma_mixhi_f16 v120,v142,s99,0
v_fma_mixhi_f16 v122,v143,s99,0
v_mfma_f32_16x16x32_bf16 v[212:215],v[88:91],v[152:155],v[212:215]
ds_read_u16_d16_hi v131,v131
v_lshlrev_b32_sdwa v133,1,v133 dst_sel:DWORD dst_unused:UNUSED_PAD src0_sel:DWORD src1_sel:WORD_1
v_lshlrev_b32_sdwa v135,1,v135 dst_sel:DWORD dst_unused:UNUSED_PAD src0_sel:DWORD src1_sel:WORD_1
v_mfma_f32_16x16x32_bf16 v[220:223],v[92:95],v[152:155],v[220:223]
ds_read_b64_tr_b16 v[88:89],v3 offset:6160
v_pk_fma_f16 v121,v120,v120,s54
v_pk_fma_f16 v123,v122,v122,s54
v_mfma_f32_16x16x32_bf16 v[228:231],v[96:99],v[152:155],v[228:231]
ds_read_u16 v132,v132
v_pk_mul_f16 v121,v120,v121
v_pk_mul_f16 v123,v122,v123
v_mfma_f32_16x16x32_bf16 v[236:239],v[100:103],v[152:155],v[236:239]
ds_read_b64_tr_b16 v[90:91],v3 offset:7696
v_lshlrev_b32_sdwa v136,1,v136 dst_sel:DWORD dst_unused:UNUSED_PAD src0_sel:DWORD src1_sel:WORD_1
v_lshlrev_b32_sdwa v138,1,v138 dst_sel:DWORD dst_unused:UNUSED_PAD src0_sel:DWORD src1_sel:WORD_1
v_mfma_f32_16x16x32_bf16 v[244:247],v[104:107],v[152:155],v[244:247]
ds_read_u16_d16_hi v134,v134
v_exp_f16 v120,v121
v_mfma_f32_16x16x32_bf16 v[252:255],v[108:111],v[152:155],v[252:255]
ds_read_b64_tr_b16 v[92:93],v3 offset:6168
v_exp_f16 v122,v123
v_mfma_f32_16x16x32_bf16 v[144:147],v[64:67],v[40:43],v[112:115]
ds_read_u16 v133,v133
v_lshlrev_b32_sdwa v137,1,v137 dst_sel:DWORD dst_unused:UNUSED_PAD src0_sel:DWORD src1_sel:WORD_1
v_lshlrev_b32_sdwa v139,1,v139 dst_sel:DWORD dst_unused:UNUSED_PAD src0_sel:DWORD src1_sel:WORD_1
v_mfma_f32_16x16x32_bf16 v[152:155],v[64:67],v[44:47],v[112:115]
ds_read_b64_tr_b16 v[94:95],v3 offset:7704
v_exp_f16_sdwa v120,v121 dst_sel:WORD_1 dst_unused:UNUSED_PRESERVE src0_sel:WORD_1
v_mfma_f32_16x16x32_bf16 v[156:159],v[68:71],v[44:47],v[116:119]
ds_read_u16_d16_hi v135,v135
v_exp_f16_sdwa v122,v123 dst_sel:WORD_1 dst_unused:UNUSED_PRESERVE src0_sel:WORD_1
v_mfma_f32_16x16x32_bf16 v[148:151],v[68:71],v[40:43],v[116:119]
ds_read_b64_tr_b16 v[96:97],v3 offset:6224
v_pk_fma_f16 v120,v120,s55,s55
v_pk_fma_f16 v122,v122,s55,s55
v_mfma_f32_16x16x32_bf16 v[144:147],v[72:75],v[48:51],v[144:147]
ds_read_u16 v136,v136
v_rcp_f16 v121,v120
v_mfma_f32_16x16x32_bf16 v[152:155],v[72:75],v[52:55],v[152:155]
ds_read_b64_tr_b16 v[98:99],v3 offset:7760
ds_read_b64_tr_b16 v[100:101],v3 offset:6232
v_rcp_f16 v123,v122
v_mfma_f32_16x16x32_bf16 v[156:159],v[76:79],v[52:55],v[156:159]
ds_read_u16_d16_hi v138,v138
v_rcp_f16_sdwa v121,v120 dst_sel:WORD_1 dst_unused:UNUSED_PRESERVE src0_sel:WORD_1
v_mfma_f32_16x16x32_bf16 v[148:151],v[76:79],v[48:51],v[148:151]
ds_read_b64_tr_b16 v[102:103],v3 offset:7768
ds_read_b64_tr_b16 v[104:105],v3 offset:6288
v_rcp_f16_sdwa v123,v122 dst_sel:WORD_1 dst_unused:UNUSED_PRESERVE src0_sel:WORD_1
v_mfma_f32_16x16x32_bf16 v[144:147],v[80:83],v[56:59],v[144:147]
ds_read_u16 v137,v137
v_fma_mix_f32 v140,v140,v121,0 op_sel_hi:[0,1,0]
v_fma_mix_f32 v142,v142,v121,0 op_sel:[0,1,0] op_sel_hi:[0,1,0]
v_mfma_f32_16x16x32_bf16 v[152:155],v[80:83],v[60:63],v[152:155]
s_waitcnt lgkmcnt(8)
ds_read_b64_tr_b16 v[106:107],v3 offset:7824
ds_read_b64_tr_b16 v[108:109],v3 offset:6296
v_lshl_add_u64 v[128:129],v[128:129],0,v[130:131]
v_lshl_add_u64 v[130:131],v[132:133],0,v[134:135]
v_mfma_f32_16x16x32_bf16 v[156:159],v[84:87],v[60:63],v[156:159]
ds_read_u16_d16_hi v139,v139
v_fma_mix_f32 v141,v141,v123,0 op_sel_hi:[0,1,0]
v_fma_mix_f32 v143,v143,v123,0 op_sel:[0,1,0] op_sel_hi:[0,1,0]
v_mfma_f32_16x16x32_bf16 v[148:151],v[84:87],v[56:59],v[148:151]
ds_read_b64_tr_b16 v[110:111],v3 offset:7832
ds_read_b64_tr_b16 v[64:65],v1 offset:512
v_lshlrev_b32_sdwa v144,1,v144 dst_sel:DWORD dst_unused:UNUSED_PAD src0_sel:DWORD src1_sel:WORD_1
v_lshlrev_b32_sdwa v146,1,v146 dst_sel:DWORD dst_unused:UNUSED_PAD src0_sel:DWORD src1_sel:WORD_1
v_mfma_f32_16x16x32_bf16 v[160:163],v[88:91],v[128:131],v[160:163]
ds_read_u16 v144,v144
v_lshlrev_b32_sdwa v145,1,v145 dst_sel:DWORD dst_unused:UNUSED_PAD src0_sel:DWORD src1_sel:WORD_1
v_lshlrev_b32_sdwa v147,1,v147 dst_sel:DWORD dst_unused:UNUSED_PAD src0_sel:DWORD src1_sel:WORD_1
v_mfma_f32_16x16x32_bf16 v[168:171],v[92:95],v[128:131],v[168:171]
s_waitcnt lgkmcnt(11)
ds_read_b64_tr_b16 v[66:67],v1 offset:2560
ds_read_b128 v[112:115],v4 offset:896
v_fma_mixlo_f16 v120,v156,s99,0
v_fma_mixlo_f16 v122,v157,s99,0
v_mfma_f32_16x16x32_bf16 v[176:179],v[96:99],v[128:131],v[176:179]
s_waitcnt lgkmcnt(10)
ds_read_u16_d16_hi v146,v146
v_lshlrev_b32_sdwa v148,1,v148 dst_sel:DWORD dst_unused:UNUSED_PAD src0_sel:DWORD src1_sel:WORD_1
v_lshlrev_b32_sdwa v150,1,v150 dst_sel:DWORD dst_unused:UNUSED_PAD src0_sel:DWORD src1_sel:WORD_1
v_mfma_f32_16x16x32_bf16 v[184:187],v[100:103],v[128:131],v[184:187]
s_waitcnt lgkmcnt(6)
ds_read_b64_tr_b16 v[68:69],v2 offset:512
ds_read_b64_tr_b16 v[70:71],v2 offset:2560
v_lshl_add_u64 v[136:137],v[136:137],0,v[138:139]
v_mfma_f32_16x16x32_bf16 v[192:195],v[104:107],v[128:131],v[192:195]
s_waitcnt lgkmcnt(7)
ds_read_u16 v145,v145
v_cvt_pk_bf16_f32 v138,v140,v142
v_cvt_pk_bf16_f32 v139,v141,v143
v_mfma_f32_16x16x32_bf16 v[200:203],v[108:111],v[128:131],v[200:203]
ds_read_b128 v[116:119],v4 offset:928
ds_read_b64_tr_b16 v[72:73],v1 offset:4608
v_fma_mixhi_f16 v120,v158,s99,0
v_fma_mixhi_f16 v122,v159,s99,0
v_mfma_f32_16x16x32_bf16 v[164:167],v[88:91],v[136:139],v[164:167]
ds_read_u16_d16_hi v147,v147
v_lshlrev_b32_sdwa v149,1,v149 dst_sel:DWORD dst_unused:UNUSED_PAD src0_sel:DWORD src1_sel:WORD_1
v_lshlrev_b32_sdwa v151,1,v151 dst_sel:DWORD dst_unused:UNUSED_PAD src0_sel:DWORD src1_sel:WORD_1
v_mfma_f32_16x16x32_bf16 v[172:175],v[92:95],v[136:139],v[172:175]
s_setprio 1
ds_read_b64_tr_b16 v[74:75],v1 offset:6656
ds_read_b64_tr_b16 v[76:77],v2 offset:4608
v_pk_fma_f16 v121,v120,v120,s54
v_pk_fma_f16 v123,v122,v122,s54
v_mfma_f32_16x16x32_bf16 v[180:183],v[96:99],v[136:139],v[180:183]
ds_read_u16 v148,v148
v_pk_mul_f16 v121,v120,v121
v_pk_mul_f16 v123,v122,v123
v_mfma_f32_16x16x32_bf16 v[188:191],v[100:103],v[136:139],v[188:191]
ds_read_b64_tr_b16 v[78:79],v2 offset:6656
v_lshlrev_b32_sdwa v152,1,v152 dst_sel:DWORD dst_unused:UNUSED_PAD src0_sel:DWORD src1_sel:WORD_1
v_lshlrev_b32_sdwa v154,1,v154 dst_sel:DWORD dst_unused:UNUSED_PAD src0_sel:DWORD src1_sel:WORD_1
v_mfma_f32_16x16x32_bf16 v[196:199],v[104:107],v[136:139],v[196:199]
ds_read_u16_d16_hi v150,v150
v_exp_f16 v120,v121
v_mfma_f32_16x16x32_bf16 v[204:207],v[108:111],v[136:139],v[204:207]
s_waitcnt lgkmcnt(12)
ds_read_b64_tr_b16 v[80:81],v1 offset:8704
v_exp_f16 v122,v123
v_mfma_f32_16x16x32_bf16 v[128:131],v[64:67],v[16:19],v[112:115]
ds_read_u16 v149,v149
v_lshlrev_b32_sdwa v153,1,v153 dst_sel:DWORD dst_unused:UNUSED_PAD src0_sel:DWORD src1_sel:WORD_1
v_lshlrev_b32_sdwa v155,1,v155 dst_sel:DWORD dst_unused:UNUSED_PAD src0_sel:DWORD src1_sel:WORD_1
v_mfma_f32_16x16x32_bf16 v[136:139],v[64:67],v[20:23],v[112:115]
s_waitcnt lgkmcnt(9)
ds_read_b64_tr_b16 v[82:83],v1 offset:10752
v_exp_f16_sdwa v120,v121 dst_sel:WORD_1 dst_unused:UNUSED_PRESERVE src0_sel:WORD_1
v_mfma_f32_16x16x32_bf16 v[140:143],v[68:71],v[20:23],v[116:119]
ds_read_u16_d16_hi v151,v151
v_exp_f16_sdwa v122,v123 dst_sel:WORD_1 dst_unused:UNUSED_PRESERVE src0_sel:WORD_1
v_mfma_f32_16x16x32_bf16 v[132:135],v[68:71],v[16:19],v[116:119]
s_waitcnt lgkmcnt(8)
ds_read_b64_tr_b16 v[84:85],v2 offset:8704
v_pk_fma_f16 v120,v120,s55,s55
v_pk_fma_f16 v122,v122,s55,s55
v_mfma_f32_16x16x32_bf16 v[128:131],v[72:75],v[24:27],v[128:131]
ds_read_u16 v152,v152
v_rcp_f16 v121,v120
v_mfma_f32_16x16x32_bf16 v[136:139],v[72:75],v[28:31],v[136:139]
s_waitcnt lgkmcnt(7)
ds_read_b64_tr_b16 v[86:87],v2 offset:10752
v_rcp_f16 v123,v122
v_mfma_f32_16x16x32_bf16 v[140:143],v[76:79],v[28:31],v[140:143]
ds_read_u16_d16_hi v154,v154
v_rcp_f16_sdwa v121,v120 dst_sel:WORD_1 dst_unused:UNUSED_PRESERVE src0_sel:WORD_1
v_mfma_f32_16x16x32_bf16 v[132:135],v[76:79],v[24:27],v[132:135]
s_waitcnt lgkmcnt(5)
v_rcp_f16_sdwa v123,v122 dst_sel:WORD_1 dst_unused:UNUSED_PRESERVE src0_sel:WORD_1
v_mfma_f32_16x16x32_bf16 v[128:131],v[80:83],v[32:35],v[128:131]
ds_read_u16 v153,v153
v_fma_mix_f32 v156,v156,v121,0 op_sel_hi:[0,1,0]
v_fma_mix_f32 v158,v158,v121,0 op_sel:[0,1,0] op_sel_hi:[0,1,0]
v_mfma_f32_16x16x32_bf16 v[136:139],v[80:83],v[36:39],v[136:139]
s_waitcnt lgkmcnt(2)
s_setprio 0
v_lshl_add_u64 v[144:145],v[144:145],0,v[146:147]
v_lshl_add_u64 v[146:147],v[148:149],0,v[150:151]
v_mfma_f32_16x16x32_bf16 v[140:143],v[84:87],v[36:39],v[140:143]
ds_read_u16_d16_hi v155,v155
v_fma_mix_f32 v157,v157,v123,0 op_sel_hi:[0,1,0]
v_fma_mix_f32 v159,v159,v123,0 op_sel:[0,1,0] op_sel_hi:[0,1,0]
v_mfma_f32_16x16x32_bf16 v[132:135],v[84:87],v[32:35],v[132:135]
v_lshlrev_b32_sdwa v128,1,v128 dst_sel:DWORD dst_unused:UNUSED_PAD src0_sel:DWORD src1_sel:WORD_1
v_lshlrev_b32_sdwa v130,1,v130 dst_sel:DWORD dst_unused:UNUSED_PAD src0_sel:DWORD src1_sel:WORD_1
v_mfma_f32_16x16x32_bf16 v[208:211],v[88:91],v[144:147],v[208:211]
s_barrier
s_setprio 2
ds_read_u16 v128,v128
v_lshlrev_b32_sdwa v129,1,v129 dst_sel:DWORD dst_unused:UNUSED_PAD src0_sel:DWORD src1_sel:WORD_1
v_mfma_f32_16x16x32_bf16 v[216:219],v[92:95],v[144:147],v[216:219]
ds_read_u16_d16_hi v130,v130
v_lshlrev_b32_sdwa v131,1,v131 dst_sel:DWORD dst_unused:UNUSED_PAD src0_sel:DWORD src1_sel:WORD_1
v_mfma_f32_16x16x32_bf16 v[224:227],v[96:99],v[144:147],v[224:227]
v_lshlrev_b32_sdwa v132,1,v132 dst_sel:DWORD dst_unused:UNUSED_PAD src0_sel:DWORD src1_sel:WORD_1
v_mfma_f32_16x16x32_bf16 v[232:235],v[100:103],v[144:147],v[232:235]
s_waitcnt lgkmcnt(2)
ds_read_u16 v129,v129
v_lshl_add_u64 v[152:153],v[152:153],0,v[154:155]
v_lshlrev_b32_sdwa v134,1,v134 dst_sel:DWORD dst_unused:UNUSED_PAD src0_sel:DWORD src1_sel:WORD_1
v_mfma_f32_16x16x32_bf16 v[240:243],v[104:107],v[144:147],v[240:243]
ds_read_u16_d16_hi v131,v131
v_cvt_pk_bf16_f32 v154,v156,v158
v_cvt_pk_bf16_f32 v155,v157,v159
v_mfma_f32_16x16x32_bf16 v[248:251],v[108:111],v[144:147],v[248:251]
v_lshlrev_b32_sdwa v133,1,v133 dst_sel:DWORD dst_unused:UNUSED_PAD src0_sel:DWORD src1_sel:WORD_1
v_mfma_f32_16x16x32_bf16 v[212:215],v[88:91],v[152:155],v[212:215]
ds_read_u16 v132,v132
v_lshlrev_b32_sdwa v135,1,v135 dst_sel:DWORD dst_unused:UNUSED_PAD src0_sel:DWORD src1_sel:WORD_1
v_mfma_f32_16x16x32_bf16 v[220:223],v[92:95],v[152:155],v[220:223]
ds_read_u16_d16_hi v134,v134
v_lshlrev_b32_sdwa v136,1,v136 dst_sel:DWORD dst_unused:UNUSED_PAD src0_sel:DWORD src1_sel:WORD_1
v_mfma_f32_16x16x32_bf16 v[228:231],v[96:99],v[152:155],v[228:231]
ds_read_b64_tr_b16 v[88:89],v3 offset:12320
ds_read_b64_tr_b16 v[90:91],v3 offset:13856
v_lshlrev_b32_sdwa v138,1,v138 dst_sel:DWORD dst_unused:UNUSED_PAD src0_sel:DWORD src1_sel:WORD_1
v_mfma_f32_16x16x32_bf16 v[236:239],v[100:103],v[152:155],v[236:239]
ds_read_u16 v133,v133
v_lshlrev_b32_sdwa v137,1,v137 dst_sel:DWORD dst_unused:UNUSED_PAD src0_sel:DWORD src1_sel:WORD_1
v_mfma_f32_16x16x32_bf16 v[244:247],v[104:107],v[152:155],v[244:247]
ds_read_u16_d16_hi v135,v135
v_lshlrev_b32_sdwa v139,1,v139 dst_sel:DWORD dst_unused:UNUSED_PAD src0_sel:DWORD src1_sel:WORD_1
v_mfma_f32_16x16x32_bf16 v[252:255],v[108:111],v[152:155],v[252:255]
ds_read_b64_tr_b16 v[92:93],v3 offset:12328
ds_read_b64_tr_b16 v[94:95],v3 offset:13864
v_lshlrev_b32_sdwa v140,1,v140 dst_sel:DWORD dst_unused:UNUSED_PAD src0_sel:DWORD src1_sel:WORD_1
v_mfma_f32_16x16x32_bf16 v[144:147],v[64:67],v[40:43],v[112:115]
ds_read_u16 v136,v136
v_lshlrev_b32_sdwa v142,1,v142 dst_sel:DWORD dst_unused:UNUSED_PAD src0_sel:DWORD src1_sel:WORD_1
v_mfma_f32_16x16x32_bf16 v[152:155],v[64:67],v[44:47],v[112:115]
ds_read_u16_d16_hi v138,v138
v_lshlrev_b32_sdwa v141,1,v141 dst_sel:DWORD dst_unused:UNUSED_PAD src0_sel:DWORD src1_sel:WORD_1
v_mfma_f32_16x16x32_bf16 v[156:159],v[68:71],v[44:47],v[116:119]
ds_read_b64_tr_b16 v[96:97],v3 offset:12384
ds_read_b64_tr_b16 v[98:99],v3 offset:13920
v_lshlrev_b32_sdwa v143,1,v143 dst_sel:DWORD dst_unused:UNUSED_PAD src0_sel:DWORD src1_sel:WORD_1
v_mfma_f32_16x16x32_bf16 v[148:151],v[68:71],v[40:43],v[116:119]
ds_read_u16 v137,v137
v_mfma_f32_16x16x32_bf16 v[144:147],v[72:75],v[48:51],v[144:147]
ds_read_u16_d16_hi v139,v139
v_mfma_f32_16x16x32_bf16 v[152:155],v[72:75],v[52:55],v[152:155]
ds_read_b64_tr_b16 v[100:101],v3 offset:12392
ds_read_b64_tr_b16 v[102:103],v3 offset:13928
v_mfma_f32_16x16x32_bf16 v[156:159],v[76:79],v[52:55],v[156:159]
ds_read_u16 v140,v140
v_mfma_f32_16x16x32_bf16 v[148:151],v[76:79],v[48:51],v[148:151]
s_waitcnt lgkmcnt(15)
ds_read_u16_d16_hi v142,v142
v_lshl_add_u64 v[128:129],v[128:129],0,v[130:131]
v_mfma_f32_16x16x32_bf16 v[144:147],v[80:83],v[56:59],v[144:147]
s_waitcnt lgkmcnt(12)
ds_read_b64_tr_b16 v[104:105],v3 offset:12448
ds_read_b64_tr_b16 v[106:107],v3 offset:13984
v_lshl_add_u64 v[130:131],v[132:133],0,v[134:135]
v_mfma_f32_16x16x32_bf16 v[152:155],v[80:83],v[60:63],v[152:155]
ds_read_u16 v141,v141
v_mfma_f32_16x16x32_bf16 v[156:159],v[84:87],v[60:63],v[156:159]
ds_read_u16_d16_hi v143,v143
v_mfma_f32_16x16x32_bf16 v[148:151],v[84:87],v[56:59],v[148:151]
ds_read_b64_tr_b16 v[108:109],v3 offset:12456
ds_read_b64_tr_b16 v[110:111],v3 offset:13992
v_lshlrev_b32_sdwa v144,1,v144 dst_sel:DWORD dst_unused:UNUSED_PAD src0_sel:DWORD src1_sel:WORD_1
v_lshlrev_b32_sdwa v146,1,v146 dst_sel:DWORD dst_unused:UNUSED_PAD src0_sel:DWORD src1_sel:WORD_1
v_mfma_f32_16x16x32_bf16 v[160:163],v[88:91],v[128:131],v[160:163]
s_waitcnt lgkmcnt(15)
ds_read_u16 v144,v144
v_lshlrev_b32_sdwa v145,1,v145 dst_sel:DWORD dst_unused:UNUSED_PAD src0_sel:DWORD src1_sel:WORD_1
v_mfma_f32_16x16x32_bf16 v[168:171],v[92:95],v[128:131],v[168:171]
s_waitcnt lgkmcnt(13)
ds_read_u16_d16_hi v146,v146
v_lshlrev_b32_sdwa v147,1,v147 dst_sel:DWORD dst_unused:UNUSED_PAD src0_sel:DWORD src1_sel:WORD_1
v_mfma_f32_16x16x32_bf16 v[176:179],v[96:99],v[128:131],v[176:179]
s_waitcnt lgkmcnt(10)
ds_read_b64_tr_b16 v[64:65],v1 offset:12320
ds_read_b64_tr_b16 v[66:67],v1 offset:14368
v_lshlrev_b32_sdwa v148,1,v148 dst_sel:DWORD dst_unused:UNUSED_PAD src0_sel:DWORD src1_sel:WORD_1
v_mfma_f32_16x16x32_bf16 v[184:187],v[100:103],v[128:131],v[184:187]
s_waitcnt lgkmcnt(8)
ds_read_u16 v145,v145
v_lshl_add_u64 v[136:137],v[136:137],0,v[138:139]
v_lshlrev_b32_sdwa v150,1,v150 dst_sel:DWORD dst_unused:UNUSED_PAD src0_sel:DWORD src1_sel:WORD_1
v_mfma_f32_16x16x32_bf16 v[192:195],v[104:107],v[128:131],v[192:195]
s_waitcnt lgkmcnt(5)
ds_read_u16_d16_hi v147,v147
v_lshl_add_u64 v[138:139],v[140:141],0,v[142:143]
v_mfma_f32_16x16x32_bf16 v[200:203],v[108:111],v[128:131],v[200:203]
ds_read_b128 v[112:115],v4 offset:1024
ds_read_b64_tr_b16 v[68:69],v2 offset:12320
v_lshlrev_b32_sdwa v149,1,v149 dst_sel:DWORD dst_unused:UNUSED_PAD src0_sel:DWORD src1_sel:WORD_1
v_mfma_f32_16x16x32_bf16 v[164:167],v[88:91],v[136:139],v[164:167]
ds_read_u16 v148,v148
v_lshlrev_b32_sdwa v151,1,v151 dst_sel:DWORD dst_unused:UNUSED_PAD src0_sel:DWORD src1_sel:WORD_1
v_mfma_f32_16x16x32_bf16 v[172:175],v[92:95],v[136:139],v[172:175]
ds_read_u16_d16_hi v150,v150
ds_read_b64_tr_b16 v[70:71],v2 offset:14368
v_lshlrev_b32_sdwa v152,1,v152 dst_sel:DWORD dst_unused:UNUSED_PAD src0_sel:DWORD src1_sel:WORD_1
v_mfma_f32_16x16x32_bf16 v[180:183],v[96:99],v[136:139],v[180:183]
ds_read_b128 v[116:119],v4 offset:1056
ds_read_b64_tr_b16 v[72:73],v1 offset:16416
v_lshlrev_b32_sdwa v154,1,v154 dst_sel:DWORD dst_unused:UNUSED_PAD src0_sel:DWORD src1_sel:WORD_1
v_mfma_f32_16x16x32_bf16 v[188:191],v[100:103],v[136:139],v[188:191]
ds_read_u16 v149,v149
v_lshlrev_b32_sdwa v153,1,v153 dst_sel:DWORD dst_unused:UNUSED_PAD src0_sel:DWORD src1_sel:WORD_1
v_mfma_f32_16x16x32_bf16 v[196:199],v[104:107],v[136:139],v[196:199]
ds_read_u16_d16_hi v151,v151
v_lshlrev_b32_sdwa v155,1,v155 dst_sel:DWORD dst_unused:UNUSED_PAD src0_sel:DWORD src1_sel:WORD_1
v_mfma_f32_16x16x32_bf16 v[204:207],v[108:111],v[136:139],v[204:207]
s_waitcnt lgkmcnt(8)
ds_read_b64_tr_b16 v[74:75],v1 offset:18464
ds_read_b64_tr_b16 v[76:77],v2 offset:16416
v_lshlrev_b32_sdwa v156,1,v156 dst_sel:DWORD dst_unused:UNUSED_PAD src0_sel:DWORD src1_sel:WORD_1
v_mfma_f32_16x16x32_bf16 v[128:131],v[64:67],v[16:19],v[112:115]
ds_read_u16 v152,v152
v_lshlrev_b32_sdwa v158,1,v158 dst_sel:DWORD dst_unused:UNUSED_PAD src0_sel:DWORD src1_sel:WORD_1
v_mfma_f32_16x16x32_bf16 v[136:139],v[64:67],v[20:23],v[112:115]
s_waitcnt lgkmcnt(6)
ds_read_u16_d16_hi v154,v154
ds_read_b64_tr_b16 v[78:79],v2 offset:18464
v_lshlrev_b32_sdwa v157,1,v157 dst_sel:DWORD dst_unused:UNUSED_PAD src0_sel:DWORD src1_sel:WORD_1
v_mfma_f32_16x16x32_bf16 v[140:143],v[68:71],v[20:23],v[116:119]
ds_read_b64_tr_b16 v[80:81],v1 offset:20512
ds_read_b64_tr_b16 v[82:83],v1 offset:22560
v_lshlrev_b32_sdwa v159,1,v159 dst_sel:DWORD dst_unused:UNUSED_PAD src0_sel:DWORD src1_sel:WORD_1
v_mfma_f32_16x16x32_bf16 v[132:135],v[68:71],v[16:19],v[116:119]
s_waitcnt lgkmcnt(6)
ds_read_u16 v153,v153
v_mfma_f32_16x16x32_bf16 v[128:131],v[72:75],v[24:27],v[128:131]
ds_read_u16_d16_hi v155,v155
v_mfma_f32_16x16x32_bf16 v[136:139],v[72:75],v[28:31],v[136:139]
s_waitcnt lgkmcnt(4)
ds_read_b64_tr_b16 v[84:85],v2 offset:20512
ds_read_b64_tr_b16 v[86:87],v2 offset:22560
v_mfma_f32_16x16x32_bf16 v[140:143],v[76:79],v[28:31],v[140:143]
ds_read_u16 v156,v156
v_mfma_f32_16x16x32_bf16 v[132:135],v[76:79],v[24:27],v[132:135]
s_waitcnt lgkmcnt(5)
ds_read_u16_d16_hi v158,v158
v_lshl_add_u64 v[144:145],v[144:145],0,v[146:147]
v_mfma_f32_16x16x32_bf16 v[128:131],v[80:83],v[32:35],v[128:131]
v_lshl_add_u64 v[146:147],v[148:149],0,v[150:151]
v_mfma_f32_16x16x32_bf16 v[136:139],v[80:83],v[36:39],v[136:139]
s_waitcnt lgkmcnt(2)
ds_read_u16 v157,v157
v_mfma_f32_16x16x32_bf16 v[140:143],v[84:87],v[36:39],v[140:143]
ds_read_u16_d16_hi v159,v159
v_mfma_f32_16x16x32_bf16 v[132:135],v[84:87],v[32:35],v[132:135]
s_nop 0
v_lshlrev_b32_sdwa v128,1,v128 dst_sel:DWORD dst_unused:UNUSED_PAD src0_sel:DWORD src1_sel:WORD_1
v_lshlrev_b32_sdwa v130,1,v130 dst_sel:DWORD dst_unused:UNUSED_PAD src0_sel:DWORD src1_sel:WORD_1
v_mfma_f32_16x16x32_bf16 v[208:211],v[88:91],v[144:147],v[208:211]
ds_read_u16 v128,v128
v_lshlrev_b32_sdwa v129,1,v129 dst_sel:DWORD dst_unused:UNUSED_PAD src0_sel:DWORD src1_sel:WORD_1
v_mfma_f32_16x16x32_bf16 v[216:219],v[92:95],v[144:147],v[216:219]
ds_read_u16_d16_hi v130,v130
v_lshlrev_b32_sdwa v131,1,v131 dst_sel:DWORD dst_unused:UNUSED_PAD src0_sel:DWORD src1_sel:WORD_1
v_mfma_f32_16x16x32_bf16 v[224:227],v[96:99],v[144:147],v[224:227]
v_lshlrev_b32_sdwa v132,1,v132 dst_sel:DWORD dst_unused:UNUSED_PAD src0_sel:DWORD src1_sel:WORD_1
v_mfma_f32_16x16x32_bf16 v[232:235],v[100:103],v[144:147],v[232:235]
ds_read_u16 v129,v129
v_lshl_add_u64 v[152:153],v[152:153],0,v[154:155]
v_lshlrev_b32_sdwa v134,1,v134 dst_sel:DWORD dst_unused:UNUSED_PAD src0_sel:DWORD src1_sel:WORD_1
v_mfma_f32_16x16x32_bf16 v[240:243],v[104:107],v[144:147],v[240:243]
s_waitcnt lgkmcnt(3)
ds_read_u16_d16_hi v131,v131
v_lshl_add_u64 v[154:155],v[156:157],0,v[158:159]
v_mfma_f32_16x16x32_bf16 v[248:251],v[108:111],v[144:147],v[248:251]
v_lshlrev_b32_sdwa v133,1,v133 dst_sel:DWORD dst_unused:UNUSED_PAD src0_sel:DWORD src1_sel:WORD_1
v_mfma_f32_16x16x32_bf16 v[212:215],v[88:91],v[152:155],v[212:215]
ds_read_u16 v132,v132
v_lshlrev_b32_sdwa v135,1,v135 dst_sel:DWORD dst_unused:UNUSED_PAD src0_sel:DWORD src1_sel:WORD_1
v_mfma_f32_16x16x32_bf16 v[220:223],v[92:95],v[152:155],v[220:223]
ds_read_u16_d16_hi v134,v134
v_lshlrev_b32_sdwa v136,1,v136 dst_sel:DWORD dst_unused:UNUSED_PAD src0_sel:DWORD src1_sel:WORD_1
v_mfma_f32_16x16x32_bf16 v[228:231],v[96:99],v[152:155],v[228:231]
ds_read_b64_tr_b16 v[88:89],v3 offset:18480
ds_read_b64_tr_b16 v[90:91],v3 offset:20016
v_lshlrev_b32_sdwa v138,1,v138 dst_sel:DWORD dst_unused:UNUSED_PAD src0_sel:DWORD src1_sel:WORD_1
v_mfma_f32_16x16x32_bf16 v[236:239],v[100:103],v[152:155],v[236:239]
ds_read_u16 v133,v133
v_lshlrev_b32_sdwa v137,1,v137 dst_sel:DWORD dst_unused:UNUSED_PAD src0_sel:DWORD src1_sel:WORD_1
v_mfma_f32_16x16x32_bf16 v[244:247],v[104:107],v[152:155],v[244:247]
ds_read_u16_d16_hi v135,v135
v_lshlrev_b32_sdwa v139,1,v139 dst_sel:DWORD dst_unused:UNUSED_PAD src0_sel:DWORD src1_sel:WORD_1
v_mfma_f32_16x16x32_bf16 v[252:255],v[108:111],v[152:155],v[252:255]
ds_read_b64_tr_b16 v[92:93],v3 offset:18488
ds_read_b64_tr_b16 v[94:95],v3 offset:20024
v_lshlrev_b32_sdwa v140,1,v140 dst_sel:DWORD dst_unused:UNUSED_PAD src0_sel:DWORD src1_sel:WORD_1
v_mfma_f32_16x16x32_bf16 v[144:147],v[64:67],v[40:43],v[112:115]
ds_read_u16 v136,v136
v_lshlrev_b32_sdwa v142,1,v142 dst_sel:DWORD dst_unused:UNUSED_PAD src0_sel:DWORD src1_sel:WORD_1
v_mfma_f32_16x16x32_bf16 v[152:155],v[64:67],v[44:47],v[112:115]
ds_read_u16_d16_hi v138,v138
v_lshlrev_b32_sdwa v141,1,v141 dst_sel:DWORD dst_unused:UNUSED_PAD src0_sel:DWORD src1_sel:WORD_1
v_mfma_f32_16x16x32_bf16 v[156:159],v[68:71],v[44:47],v[116:119]
ds_read_b64_tr_b16 v[96:97],v3 offset:18544
ds_read_b64_tr_b16 v[98:99],v3 offset:20080
v_lshlrev_b32_sdwa v143,1,v143 dst_sel:DWORD dst_unused:UNUSED_PAD src0_sel:DWORD src1_sel:WORD_1
v_mfma_f32_16x16x32_bf16 v[148:151],v[68:71],v[40:43],v[116:119]
ds_read_u16 v137,v137
v_mfma_f32_16x16x32_bf16 v[144:147],v[72:75],v[48:51],v[144:147]
ds_read_u16_d16_hi v139,v139
v_mfma_f32_16x16x32_bf16 v[152:155],v[72:75],v[52:55],v[152:155]
ds_read_b64_tr_b16 v[100:101],v3 offset:18552
ds_read_b64_tr_b16 v[102:103],v3 offset:20088
v_mfma_f32_16x16x32_bf16 v[156:159],v[76:79],v[52:55],v[156:159]
ds_read_u16 v140,v140
v_mfma_f32_16x16x32_bf16 v[148:151],v[76:79],v[48:51],v[148:151]
s_waitcnt lgkmcnt(15)
ds_read_u16_d16_hi v142,v142
v_lshl_add_u64 v[128:129],v[128:129],0,v[130:131]
v_mfma_f32_16x16x32_bf16 v[144:147],v[80:83],v[56:59],v[144:147]
s_waitcnt lgkmcnt(12)
ds_read_b64_tr_b16 v[104:105],v3 offset:18608
ds_read_b64_tr_b16 v[106:107],v3 offset:20144
v_lshl_add_u64 v[130:131],v[132:133],0,v[134:135]
v_mfma_f32_16x16x32_bf16 v[152:155],v[80:83],v[60:63],v[152:155]
ds_read_u16 v141,v141
v_mfma_f32_16x16x32_bf16 v[156:159],v[84:87],v[60:63],v[156:159]
ds_read_u16_d16_hi v143,v143
v_mfma_f32_16x16x32_bf16 v[148:151],v[84:87],v[56:59],v[148:151]
ds_read_b64_tr_b16 v[108:109],v3 offset:18616
ds_read_b64_tr_b16 v[110:111],v3 offset:20152
v_lshlrev_b32_sdwa v144,1,v144 dst_sel:DWORD dst_unused:UNUSED_PAD src0_sel:DWORD src1_sel:WORD_1
v_lshlrev_b32_sdwa v146,1,v146 dst_sel:DWORD dst_unused:UNUSED_PAD src0_sel:DWORD src1_sel:WORD_1
v_mfma_f32_16x16x32_bf16 v[160:163],v[88:91],v[128:131],v[160:163]
s_waitcnt lgkmcnt(15)
ds_read_u16 v144,v144
v_lshlrev_b32_sdwa v145,1,v145 dst_sel:DWORD dst_unused:UNUSED_PAD src0_sel:DWORD src1_sel:WORD_1
v_mfma_f32_16x16x32_bf16 v[168:171],v[92:95],v[128:131],v[168:171]
s_waitcnt lgkmcnt(13)
ds_read_u16_d16_hi v146,v146
v_lshlrev_b32_sdwa v147,1,v147 dst_sel:DWORD dst_unused:UNUSED_PAD src0_sel:DWORD src1_sel:WORD_1
v_mfma_f32_16x16x32_bf16 v[176:179],v[96:99],v[128:131],v[176:179]
s_waitcnt lgkmcnt(10)
ds_read_b64_tr_b16 v[64:65],v1 offset:12832
ds_read_b64_tr_b16 v[66:67],v1 offset:14880
v_lshlrev_b32_sdwa v148,1,v148 dst_sel:DWORD dst_unused:UNUSED_PAD src0_sel:DWORD src1_sel:WORD_1
v_mfma_f32_16x16x32_bf16 v[184:187],v[100:103],v[128:131],v[184:187]
s_waitcnt lgkmcnt(8)
ds_read_u16 v145,v145
v_lshl_add_u64 v[136:137],v[136:137],0,v[138:139]
v_lshlrev_b32_sdwa v150,1,v150 dst_sel:DWORD dst_unused:UNUSED_PAD src0_sel:DWORD src1_sel:WORD_1
v_mfma_f32_16x16x32_bf16 v[192:195],v[104:107],v[128:131],v[192:195]
s_waitcnt lgkmcnt(5)
ds_read_u16_d16_hi v147,v147
v_lshl_add_u64 v[138:139],v[140:141],0,v[142:143]
v_mfma_f32_16x16x32_bf16 v[200:203],v[108:111],v[128:131],v[200:203]
ds_read_b128 v[112:115],v4 offset:1152
ds_read_b64_tr_b16 v[68:69],v2 offset:12832
v_lshlrev_b32_sdwa v149,1,v149 dst_sel:DWORD dst_unused:UNUSED_PAD src0_sel:DWORD src1_sel:WORD_1
v_mfma_f32_16x16x32_bf16 v[164:167],v[88:91],v[136:139],v[164:167]
ds_read_u16 v148,v148
v_lshlrev_b32_sdwa v151,1,v151 dst_sel:DWORD dst_unused:UNUSED_PAD src0_sel:DWORD src1_sel:WORD_1
v_mfma_f32_16x16x32_bf16 v[172:175],v[92:95],v[136:139],v[172:175]
ds_read_u16_d16_hi v150,v150
ds_read_b64_tr_b16 v[70:71],v2 offset:14880
v_lshlrev_b32_sdwa v152,1,v152 dst_sel:DWORD dst_unused:UNUSED_PAD src0_sel:DWORD src1_sel:WORD_1
v_mfma_f32_16x16x32_bf16 v[180:183],v[96:99],v[136:139],v[180:183]
ds_read_b128 v[116:119],v4 offset:1184
ds_read_b64_tr_b16 v[72:73],v1 offset:16928
v_lshlrev_b32_sdwa v154,1,v154 dst_sel:DWORD dst_unused:UNUSED_PAD src0_sel:DWORD src1_sel:WORD_1
v_mfma_f32_16x16x32_bf16 v[188:191],v[100:103],v[136:139],v[188:191]
ds_read_u16 v149,v149
v_lshlrev_b32_sdwa v153,1,v153 dst_sel:DWORD dst_unused:UNUSED_PAD src0_sel:DWORD src1_sel:WORD_1
v_mfma_f32_16x16x32_bf16 v[196:199],v[104:107],v[136:139],v[196:199]
ds_read_u16_d16_hi v151,v151
v_lshlrev_b32_sdwa v155,1,v155 dst_sel:DWORD dst_unused:UNUSED_PAD src0_sel:DWORD src1_sel:WORD_1
v_mfma_f32_16x16x32_bf16 v[204:207],v[108:111],v[136:139],v[204:207]
s_waitcnt lgkmcnt(8)
ds_read_b64_tr_b16 v[74:75],v1 offset:18976
ds_read_b64_tr_b16 v[76:77],v2 offset:16928
v_lshlrev_b32_sdwa v156,1,v156 dst_sel:DWORD dst_unused:UNUSED_PAD src0_sel:DWORD src1_sel:WORD_1
v_mfma_f32_16x16x32_bf16 v[128:131],v[64:67],v[16:19],v[112:115]
ds_read_u16 v152,v152
v_lshlrev_b32_sdwa v158,1,v158 dst_sel:DWORD dst_unused:UNUSED_PAD src0_sel:DWORD src1_sel:WORD_1
v_mfma_f32_16x16x32_bf16 v[136:139],v[64:67],v[20:23],v[112:115]
s_waitcnt lgkmcnt(6)
s_mov_b32 s86,0xbe801d5a
ds_read_u16_d16_hi v154,v154
ds_read_b64_tr_b16 v[78:79],v2 offset:18976
v_lshlrev_b32_sdwa v157,1,v157 dst_sel:DWORD dst_unused:UNUSED_PAD src0_sel:DWORD src1_sel:WORD_1
v_mfma_f32_16x16x32_bf16 v[140:143],v[68:71],v[20:23],v[116:119]
ds_read_b64_tr_b16 v[80:81],v1 offset:21024
ds_read_b64_tr_b16 v[82:83],v1 offset:23072
v_lshlrev_b32_sdwa v159,1,v159 dst_sel:DWORD dst_unused:UNUSED_PAD src0_sel:DWORD src1_sel:WORD_1
v_mfma_f32_16x16x32_bf16 v[132:135],v[68:71],v[16:19],v[116:119]
s_waitcnt lgkmcnt(6)
ds_read_u16 v153,v153
v_mfma_f32_16x16x32_bf16 v[128:131],v[72:75],v[24:27],v[128:131]
ds_read_u16_d16_hi v155,v155
v_mfma_f32_16x16x32_bf16 v[136:139],v[72:75],v[28:31],v[136:139]
s_waitcnt lgkmcnt(4)
ds_read_b64_tr_b16 v[84:85],v2 offset:21024
ds_read_b64_tr_b16 v[86:87],v2 offset:23072
v_mfma_f32_16x16x32_bf16 v[140:143],v[76:79],v[28:31],v[140:143]
ds_read_u16 v156,v156
v_mfma_f32_16x16x32_bf16 v[132:135],v[76:79],v[24:27],v[132:135]
s_waitcnt lgkmcnt(5)
ds_read_u16_d16_hi v158,v158
v_lshl_add_u64 v[144:145],v[144:145],0,v[146:147]
v_mfma_f32_16x16x32_bf16 v[128:131],v[80:83],v[32:35],v[128:131]
v_lshl_add_u64 v[146:147],v[148:149],0,v[150:151]
v_mfma_f32_16x16x32_bf16 v[136:139],v[80:83],v[36:39],v[136:139]
s_waitcnt lgkmcnt(2)
ds_read_u16 v157,v157
v_mfma_f32_16x16x32_bf16 v[140:143],v[84:87],v[36:39],v[140:143]
ds_read_u16_d16_hi v159,v159
v_mfma_f32_16x16x32_bf16 v[132:135],v[84:87],v[32:35],v[132:135]
s_nop 0
v_lshlrev_b32_sdwa v128,1,v128 dst_sel:DWORD dst_unused:UNUSED_PAD src0_sel:DWORD src1_sel:WORD_1
v_lshlrev_b32_sdwa v130,1,v130 dst_sel:DWORD dst_unused:UNUSED_PAD src0_sel:DWORD src1_sel:WORD_1
v_mfma_f32_16x16x32_bf16 v[208:211],v[88:91],v[144:147],v[208:211]
s_barrier
ds_read_u16 v128,v128
v_lshlrev_b32_sdwa v129,1,v129 dst_sel:DWORD dst_unused:UNUSED_PAD src0_sel:DWORD src1_sel:WORD_1
v_lshlrev_b32_sdwa v131,1,v131 dst_sel:DWORD dst_unused:UNUSED_PAD src0_sel:DWORD src1_sel:WORD_1
v_mfma_f32_16x16x32_bf16 v[216:219],v[92:95],v[144:147],v[216:219]
v_fma_mixlo_f16 v120,v141,s99,0
v_lshlrev_b32_sdwa v132,1,v132 dst_sel:DWORD dst_unused:UNUSED_PAD src0_sel:DWORD src1_sel:WORD_1
v_mfma_f32_16x16x32_bf16 v[224:227],v[96:99],v[144:147],v[224:227]
ds_read_u16_d16_hi v130,v130
v_fma_mixhi_f16 v120,v143,s99,0
v_lshlrev_b32_sdwa v134,1,v134 dst_sel:DWORD dst_unused:UNUSED_PAD src0_sel:DWORD src1_sel:WORD_1
v_mfma_f32_16x16x32_bf16 v[232:235],v[100:103],v[144:147],v[232:235]
ds_read_u16 v129,v129
v_lshl_add_u64 v[152:153],v[152:153],0,v[154:155]
v_mfma_f32_16x16x32_bf16 v[240:243],v[104:107],v[144:147],v[240:243]
s_waitcnt lgkmcnt(3)
v_lshl_add_u64 v[154:155],v[156:157],0,v[158:159]
v_mfma_f32_16x16x32_bf16 v[248:251],v[108:111],v[144:147],v[248:251]
ds_read_u16_d16_hi v131,v131
v_pk_fma_f16 v121,v120,v120,s54
v_lshlrev_b32_sdwa v133,1,v133 dst_sel:DWORD dst_unused:UNUSED_PAD src0_sel:DWORD src1_sel:WORD_1
v_mfma_f32_16x16x32_bf16 v[212:215],v[88:91],v[152:155],v[212:215]
ds_read_b64_tr_b16 v[88:89],v3 offset:32832
v_pk_mul_f16 v121,v120,v121
v_lshlrev_b32_sdwa v135,1,v135 dst_sel:DWORD dst_unused:UNUSED_PAD src0_sel:DWORD src1_sel:WORD_1
v_mfma_f32_16x16x32_bf16 v[220:223],v[92:95],v[152:155],v[220:223]
ds_read_u16 v132,v132
v_exp_f16 v120,v121
v_mfma_f32_16x16x32_bf16 v[228:231],v[96:99],v[152:155],v[228:231]
ds_read_b64_tr_b16 v[90:91],v3 offset:34368
ds_read_b64_tr_b16 v[92:93],v3 offset:32840
v_lshlrev_b32_sdwa v136,1,v136 dst_sel:DWORD dst_unused:UNUSED_PAD src0_sel:DWORD src1_sel:WORD_1
v_mfma_f32_16x16x32_bf16 v[236:239],v[100:103],v[152:155],v[236:239]
ds_read_u16_d16_hi v134,v134
v_exp_f16_sdwa v120,v121 dst_sel:WORD_1 dst_unused:UNUSED_PRESERVE src0_sel:WORD_1
v_mfma_f32_16x16x32_bf16 v[244:247],v[104:107],v[152:155],v[244:247]
ds_read_u16 v133,v133
v_lshlrev_b32_sdwa v138,1,v138 dst_sel:DWORD dst_unused:UNUSED_PAD src0_sel:DWORD src1_sel:WORD_1
v_mfma_f32_16x16x32_bf16 v[252:255],v[108:111],v[152:155],v[252:255]
ds_read_b64_tr_b16 v[94:95],v3 offset:34376
ds_read_b64_tr_b16 v[96:97],v3 offset:32896
v_pk_fma_f16 v120,v120,s55,s55
v_lshlrev_b32_sdwa v137,1,v137 dst_sel:DWORD dst_unused:UNUSED_PAD src0_sel:DWORD src1_sel:WORD_1
v_mfma_f32_16x16x32_bf16 v[144:147],v[64:67],v[40:43],v[112:115]
ds_read_u16_d16_hi v135,v135
v_rcp_f16 v121,v120
v_mfma_f32_16x16x32_bf16 v[152:155],v[64:67],v[44:47],v[112:115]
ds_read_b64_tr_b16 v[98:99],v3 offset:34432
ds_read_b64_tr_b16 v[100:101],v3 offset:32904
v_lshlrev_b32_sdwa v139,1,v139 dst_sel:DWORD dst_unused:UNUSED_PAD src0_sel:DWORD src1_sel:WORD_1
v_mfma_f32_16x16x32_bf16 v[156:159],v[68:71],v[44:47],v[116:119]
ds_read_u16 v136,v136
v_rcp_f16_sdwa v121,v120 dst_sel:WORD_1 dst_unused:UNUSED_PRESERVE src0_sel:WORD_1
v_mfma_f32_16x16x32_bf16 v[148:151],v[68:71],v[40:43],v[116:119]
ds_read_u16_d16_hi v138,v138
v_lshlrev_b32_sdwa v140,1,v140 dst_sel:DWORD dst_unused:UNUSED_PAD src0_sel:DWORD src1_sel:WORD_1
v_mfma_f32_16x16x32_bf16 v[144:147],v[72:75],v[48:51],v[144:147]
ds_read_b64_tr_b16 v[102:103],v3 offset:34440
ds_read_b64_tr_b16 v[104:105],v3 offset:32960
v_fma_mix_f32 v141,v141,v121,0 op_sel_hi:[0,1,0]
v_lshlrev_b32_sdwa v142,1,v142 dst_sel:DWORD dst_unused:UNUSED_PAD src0_sel:DWORD src1_sel:WORD_1
v_mfma_f32_16x16x32_bf16 v[152:155],v[72:75],v[52:55],v[152:155]
ds_read_u16 v137,v137
v_fma_mix_f32 v143,v143,v121,0 op_sel:[0,1,0] op_sel_hi:[0,1,0]
v_mfma_f32_16x16x32_bf16 v[156:159],v[76:79],v[52:55],v[156:159]
ds_read_b64_tr_b16 v[106:107],v3 offset:34496
ds_read_b64_tr_b16 v[108:109],v3 offset:32968
v_mfma_f32_16x16x32_bf16 v[148:151],v[76:79],v[48:51],v[148:151]
ds_read_u16_d16_hi v139,v139
v_mfma_f32_16x16x32_bf16 v[144:147],v[80:83],v[56:59],v[144:147]
ds_read_b64_tr_b16 v[110:111],v3 offset:34504
ds_read_b64_tr_b16 v[64:65],v8 offset:0
v_mfma_f32_16x16x32_bf16 v[152:155],v[80:83],v[60:63],v[152:155]
s_waitcnt lgkmcnt(12)
ds_read_u16 v140,v140
v_lshl_add_u64 v[128:129],v[128:129],0,v[130:131]
v_lshl_add_u64 v[130:131],v[132:133],0,v[134:135]
v_mfma_f32_16x16x32_bf16 v[156:159],v[84:87],v[60:63],v[156:159]
ds_read_u16_d16_hi v142,v142
v_mfma_f32_16x16x32_bf16 v[148:151],v[84:87],v[56:59],v[148:151]
ds_read_b64_tr_b16 v[66:67],v8 offset:2048
ds_read_b128 v[112:115],v4 offset:1280
v_lshlrev_b32_sdwa v144,1,v144 dst_sel:DWORD dst_unused:UNUSED_PAD src0_sel:DWORD src1_sel:WORD_1
v_lshlrev_b32_sdwa v146,1,v146 dst_sel:DWORD dst_unused:UNUSED_PAD src0_sel:DWORD src1_sel:WORD_1
v_mfma_f32_16x16x32_bf16 v[160:163],v[88:91],v[128:131],v[160:163]
ds_read_u16 v144,v144
v_lshlrev_b32_sdwa v145,1,v145 dst_sel:DWORD dst_unused:UNUSED_PAD src0_sel:DWORD src1_sel:WORD_1
v_lshlrev_b32_sdwa v147,1,v147 dst_sel:DWORD dst_unused:UNUSED_PAD src0_sel:DWORD src1_sel:WORD_1
v_mfma_f32_16x16x32_bf16 v[168:171],v[92:95],v[128:131],v[168:171]
s_waitcnt lgkmcnt(15)
ds_read_b64_tr_b16 v[68:69],v9 offset:0
ds_read_b64_tr_b16 v[70:71],v9 offset:2048
v_fma_mixlo_f16 v120,v157,s99,0
v_lshlrev_b32_sdwa v148,1,v148 dst_sel:DWORD dst_unused:UNUSED_PAD src0_sel:DWORD src1_sel:WORD_1
v_mfma_f32_16x16x32_bf16 v[176:179],v[96:99],v[128:131],v[176:179]
s_waitcnt lgkmcnt(14)
ds_read_u16_d16_hi v146,v146
v_fma_mixhi_f16 v120,v159,s99,0
v_lshlrev_b32_sdwa v150,1,v150 dst_sel:DWORD dst_unused:UNUSED_PAD src0_sel:DWORD src1_sel:WORD_1
v_mfma_f32_16x16x32_bf16 v[184:187],v[100:103],v[128:131],v[184:187]
s_waitcnt lgkmcnt(10)
ds_read_u16 v145,v145
v_lshl_add_u64 v[136:137],v[136:137],0,v[138:139]
v_mfma_f32_16x16x32_bf16 v[192:195],v[104:107],v[128:131],v[192:195]
s_waitcnt lgkmcnt(7)
ds_read_b128 v[116:119],v4 offset:1312
ds_read_b64_tr_b16 v[72:73],v8 offset:4096
v_or_b32 v138,v142,v140
v_cvt_pk_bf16_f32 v139,v141,v143
v_mfma_f32_16x16x32_bf16 v[200:203],v[108:111],v[128:131],v[200:203]
ds_read_u16_d16_hi v147,v147
v_pk_fma_f16 v121,v120,v120,s54
v_lshlrev_b32_sdwa v149,1,v149 dst_sel:DWORD dst_unused:UNUSED_PAD src0_sel:DWORD src1_sel:WORD_1
v_mfma_f32_16x16x32_bf16 v[164:167],v[88:91],v[136:139],v[164:167]
ds_read_b64_tr_b16 v[74:75],v8 offset:6144
ds_read_b64_tr_b16 v[76:77],v9 offset:4096
v_pk_mul_f16 v121,v120,v121
v_lshlrev_b32_sdwa v151,1,v151 dst_sel:DWORD dst_unused:UNUSED_PAD src0_sel:DWORD src1_sel:WORD_1
v_mfma_f32_16x16x32_bf16 v[172:175],v[92:95],v[136:139],v[172:175]
ds_read_u16 v148,v148
v_exp_f16 v120,v121
v_mfma_f32_16x16x32_bf16 v[180:183],v[96:99],v[136:139],v[180:183]
ds_read_b64_tr_b16 v[78:79],v9 offset:6144
ds_read_b64_tr_b16 v[80:81],v8 offset:8192
v_lshlrev_b32_sdwa v152,1,v152 dst_sel:DWORD dst_unused:UNUSED_PAD src0_sel:DWORD src1_sel:WORD_1
v_mfma_f32_16x16x32_bf16 v[188:191],v[100:103],v[136:139],v[188:191]
ds_read_u16_d16_hi v150,v150
v_exp_f16_sdwa v120,v121 dst_sel:WORD_1 dst_unused:UNUSED_PRESERVE src0_sel:WORD_1
v_mfma_f32_16x16x32_bf16 v[196:199],v[104:107],v[136:139],v[196:199]
ds_read_u16 v149,v149
v_lshlrev_b32_sdwa v154,1,v154 dst_sel:DWORD dst_unused:UNUSED_PAD src0_sel:DWORD src1_sel:WORD_1
v_mfma_f32_16x16x32_bf16 v[204:207],v[108:111],v[136:139],v[204:207]
s_waitcnt lgkmcnt(15)
ds_read_b64_tr_b16 v[82:83],v8 offset:10240
v_pk_fma_f16 v120,v120,s55,s55
v_lshlrev_b32_sdwa v153,1,v153 dst_sel:DWORD dst_unused:UNUSED_PAD src0_sel:DWORD src1_sel:WORD_1
v_mfma_f32_16x16x32_bf16 v[128:131],v[64:67],v[16:19],v[112:115]
ds_read_u16_d16_hi v151,v151
v_rcp_f16 v121,v120
v_mfma_f32_16x16x32_bf16 v[136:139],v[64:67],v[20:23],v[112:115]
s_waitcnt lgkmcnt(11)
ds_read_b64_tr_b16 v[84:85],v9 offset:8192
v_lshlrev_b32_sdwa v155,1,v155 dst_sel:DWORD dst_unused:UNUSED_PAD src0_sel:DWORD src1_sel:WORD_1
v_mfma_f32_16x16x32_bf16 v[140:143],v[68:71],v[20:23],v[116:119]
ds_read_u16 v152,v152
v_rcp_f16_sdwa v121,v120 dst_sel:WORD_1 dst_unused:UNUSED_PRESERVE src0_sel:WORD_1
v_mfma_f32_16x16x32_bf16 v[132:135],v[68:71],v[16:19],v[116:119]
s_waitcnt lgkmcnt(10)
ds_read_u16_d16_hi v154,v154
v_lshlrev_b32_sdwa v156,1,v156 dst_sel:DWORD dst_unused:UNUSED_PAD src0_sel:DWORD src1_sel:WORD_1
v_mfma_f32_16x16x32_bf16 v[128:131],v[72:75],v[24:27],v[128:131]
ds_read_b64_tr_b16 v[86:87],v9 offset:10240
v_fma_mix_f32 v157,v157,v121,0 op_sel_hi:[0,1,0]
v_lshlrev_b32_sdwa v158,1,v158 dst_sel:DWORD dst_unused:UNUSED_PAD src0_sel:DWORD src1_sel:WORD_1
v_mfma_f32_16x16x32_bf16 v[136:139],v[72:75],v[28:31],v[136:139]
s_waitcnt lgkmcnt(9)
ds_read_u16 v153,v153
v_fma_mix_f32 v159,v159,v121,0 op_sel:[0,1,0] op_sel_hi:[0,1,0]
v_mfma_f32_16x16x32_bf16 v[140:143],v[76:79],v[28:31],v[140:143]
v_mfma_f32_16x16x32_bf16 v[132:135],v[76:79],v[24:27],v[132:135]
s_waitcnt lgkmcnt(6)
ds_read_u16_d16_hi v155,v155
v_mfma_f32_16x16x32_bf16 v[128:131],v[80:83],v[32:35],v[128:131]
v_mfma_f32_16x16x32_bf16 v[136:139],v[80:83],v[36:39],v[136:139]
s_waitcnt lgkmcnt(2)
ds_read_u16 v156,v156
v_lshl_add_u64 v[144:145],v[144:145],0,v[146:147]
v_lshl_add_u64 v[146:147],v[148:149],0,v[150:151]
v_mfma_f32_16x16x32_bf16 v[140:143],v[84:87],v[36:39],v[140:143]
ds_read_u16_d16_hi v158,v158
v_mfma_f32_16x16x32_bf16 v[132:135],v[84:87],v[32:35],v[132:135]
v_lshlrev_b32_sdwa v128,1,v128 dst_sel:DWORD dst_unused:UNUSED_PAD src0_sel:DWORD src1_sel:WORD_1
v_lshlrev_b32_sdwa v130,1,v130 dst_sel:DWORD dst_unused:UNUSED_PAD src0_sel:DWORD src1_sel:WORD_1
v_mfma_f32_16x16x32_bf16 v[208:211],v[88:91],v[144:147],v[208:211]
ds_read_u16 v128,v128
v_lshlrev_b32_sdwa v129,1,v129 dst_sel:DWORD dst_unused:UNUSED_PAD src0_sel:DWORD src1_sel:WORD_1
v_lshlrev_b32_sdwa v131,1,v131 dst_sel:DWORD dst_unused:UNUSED_PAD src0_sel:DWORD src1_sel:WORD_1
v_mfma_f32_16x16x32_bf16 v[216:219],v[92:95],v[144:147],v[216:219]
v_fma_mixlo_f16 v120,v141,s99,0
v_lshlrev_b32_sdwa v132,1,v132 dst_sel:DWORD dst_unused:UNUSED_PAD src0_sel:DWORD src1_sel:WORD_1
v_mfma_f32_16x16x32_bf16 v[224:227],v[96:99],v[144:147],v[224:227]
ds_read_u16_d16_hi v130,v130
v_fma_mixhi_f16 v120,v143,s99,0
v_lshlrev_b32_sdwa v134,1,v134 dst_sel:DWORD dst_unused:UNUSED_PAD src0_sel:DWORD src1_sel:WORD_1
v_mfma_f32_16x16x32_bf16 v[232:235],v[100:103],v[144:147],v[232:235]
s_waitcnt lgkmcnt(4)
ds_read_u16 v129,v129
v_lshl_add_u64 v[152:153],v[152:153],0,v[154:155]
v_mfma_f32_16x16x32_bf16 v[240:243],v[104:107],v[144:147],v[240:243]
s_waitcnt lgkmcnt(3)
v_or_b32 v154,v158,v156
v_cvt_pk_bf16_f32 v155,v157,v159
v_mfma_f32_16x16x32_bf16 v[248:251],v[108:111],v[144:147],v[248:251]
ds_read_u16_d16_hi v131,v131
v_pk_fma_f16 v121,v120,v120,s54
v_lshlrev_b32_sdwa v133,1,v133 dst_sel:DWORD dst_unused:UNUSED_PAD src0_sel:DWORD src1_sel:WORD_1
v_mfma_f32_16x16x32_bf16 v[212:215],v[88:91],v[152:155],v[212:215]
ds_read_b64_tr_b16 v[88:89],v3 offset:38992
v_pk_mul_f16 v121,v120,v121
v_lshlrev_b32_sdwa v135,1,v135 dst_sel:DWORD dst_unused:UNUSED_PAD src0_sel:DWORD src1_sel:WORD_1
v_mfma_f32_16x16x32_bf16 v[220:223],v[92:95],v[152:155],v[220:223]
ds_read_u16 v132,v132
v_exp_f16 v120,v121
v_mfma_f32_16x16x32_bf16 v[228:231],v[96:99],v[152:155],v[228:231]
ds_read_b64_tr_b16 v[90:91],v3 offset:40528
ds_read_b64_tr_b16 v[92:93],v3 offset:39000
v_lshlrev_b32_sdwa v136,1,v136 dst_sel:DWORD dst_unused:UNUSED_PAD src0_sel:DWORD src1_sel:WORD_1
v_mfma_f32_16x16x32_bf16 v[236:239],v[100:103],v[152:155],v[236:239]
ds_read_u16_d16_hi v134,v134
v_exp_f16_sdwa v120,v121 dst_sel:WORD_1 dst_unused:UNUSED_PRESERVE src0_sel:WORD_1
v_mfma_f32_16x16x32_bf16 v[244:247],v[104:107],v[152:155],v[244:247]
ds_read_u16 v133,v133
v_lshlrev_b32_sdwa v138,1,v138 dst_sel:DWORD dst_unused:UNUSED_PAD src0_sel:DWORD src1_sel:WORD_1
v_mfma_f32_16x16x32_bf16 v[252:255],v[108:111],v[152:155],v[252:255]
ds_read_b64_tr_b16 v[94:95],v3 offset:40536
ds_read_b64_tr_b16 v[96:97],v3 offset:39056
v_pk_fma_f16 v120,v120,s55,s55
v_lshlrev_b32_sdwa v137,1,v137 dst_sel:DWORD dst_unused:UNUSED_PAD src0_sel:DWORD src1_sel:WORD_1
v_mfma_f32_16x16x32_bf16 v[144:147],v[64:67],v[40:43],v[112:115]
ds_read_u16_d16_hi v135,v135
v_rcp_f16 v121,v120
v_mfma_f32_16x16x32_bf16 v[152:155],v[64:67],v[44:47],v[112:115]
ds_read_b64_tr_b16 v[98:99],v3 offset:40592
ds_read_b64_tr_b16 v[100:101],v3 offset:39064
v_lshlrev_b32_sdwa v139,1,v139 dst_sel:DWORD dst_unused:UNUSED_PAD src0_sel:DWORD src1_sel:WORD_1
v_mfma_f32_16x16x32_bf16 v[156:159],v[68:71],v[44:47],v[116:119]
ds_read_u16 v136,v136
v_rcp_f16_sdwa v121,v120 dst_sel:WORD_1 dst_unused:UNUSED_PRESERVE src0_sel:WORD_1
v_mfma_f32_16x16x32_bf16 v[148:151],v[68:71],v[40:43],v[116:119]
ds_read_u16_d16_hi v138,v138
v_lshlrev_b32_sdwa v140,1,v140 dst_sel:DWORD dst_unused:UNUSED_PAD src0_sel:DWORD src1_sel:WORD_1
v_mfma_f32_16x16x32_bf16 v[144:147],v[72:75],v[48:51],v[144:147]
ds_read_b64_tr_b16 v[102:103],v3 offset:40600
ds_read_b64_tr_b16 v[104:105],v3 offset:39120
v_fma_mix_f32 v141,v141,v121,0 op_sel_hi:[0,1,0]
v_lshlrev_b32_sdwa v142,1,v142 dst_sel:DWORD dst_unused:UNUSED_PAD src0_sel:DWORD src1_sel:WORD_1
v_mfma_f32_16x16x32_bf16 v[152:155],v[72:75],v[52:55],v[152:155]
ds_read_u16 v137,v137
v_fma_mix_f32 v143,v143,v121,0 op_sel:[0,1,0] op_sel_hi:[0,1,0]
v_mfma_f32_16x16x32_bf16 v[156:159],v[76:79],v[52:55],v[156:159]
ds_read_b64_tr_b16 v[106:107],v3 offset:40656
ds_read_b64_tr_b16 v[108:109],v3 offset:39128
v_mfma_f32_16x16x32_bf16 v[148:151],v[76:79],v[48:51],v[148:151]
ds_read_u16_d16_hi v139,v139
v_mfma_f32_16x16x32_bf16 v[144:147],v[80:83],v[56:59],v[144:147]
ds_read_b64_tr_b16 v[110:111],v3 offset:40664
ds_read_b64_tr_b16 v[64:65],v8 offset:512
v_mfma_f32_16x16x32_bf16 v[152:155],v[80:83],v[60:63],v[152:155]
s_waitcnt lgkmcnt(12)
ds_read_u16 v140,v140
v_lshl_add_u64 v[128:129],v[128:129],0,v[130:131]
v_lshl_add_u64 v[130:131],v[132:133],0,v[134:135]
v_mfma_f32_16x16x32_bf16 v[156:159],v[84:87],v[60:63],v[156:159]
ds_read_u16_d16_hi v142,v142
v_mfma_f32_16x16x32_bf16 v[148:151],v[84:87],v[56:59],v[148:151]
ds_read_b64_tr_b16 v[66:67],v8 offset:2560
ds_read_b128 v[112:115],v4 offset:1408
v_lshlrev_b32_sdwa v144,1,v144 dst_sel:DWORD dst_unused:UNUSED_PAD src0_sel:DWORD src1_sel:WORD_1
v_lshlrev_b32_sdwa v146,1,v146 dst_sel:DWORD dst_unused:UNUSED_PAD src0_sel:DWORD src1_sel:WORD_1
v_mfma_f32_16x16x32_bf16 v[160:163],v[88:91],v[128:131],v[160:163]
ds_read_u16 v144,v144
v_lshlrev_b32_sdwa v145,1,v145 dst_sel:DWORD dst_unused:UNUSED_PAD src0_sel:DWORD src1_sel:WORD_1
v_lshlrev_b32_sdwa v147,1,v147 dst_sel:DWORD dst_unused:UNUSED_PAD src0_sel:DWORD src1_sel:WORD_1
v_mfma_f32_16x16x32_bf16 v[168:171],v[92:95],v[128:131],v[168:171]
s_waitcnt lgkmcnt(15)
ds_read_b64_tr_b16 v[68:69],v9 offset:512
ds_read_b64_tr_b16 v[70:71],v9 offset:2560
v_fma_mixlo_f16 v120,v157,s99,0
v_lshlrev_b32_sdwa v148,1,v148 dst_sel:DWORD dst_unused:UNUSED_PAD src0_sel:DWORD src1_sel:WORD_1
v_mfma_f32_16x16x32_bf16 v[176:179],v[96:99],v[128:131],v[176:179]
s_waitcnt lgkmcnt(14)
ds_read_u16_d16_hi v146,v146
v_fma_mixhi_f16 v120,v159,s99,0
v_lshlrev_b32_sdwa v150,1,v150 dst_sel:DWORD dst_unused:UNUSED_PAD src0_sel:DWORD src1_sel:WORD_1
v_mfma_f32_16x16x32_bf16 v[184:187],v[100:103],v[128:131],v[184:187]
s_waitcnt lgkmcnt(10)
ds_read_u16 v145,v145
v_lshl_add_u64 v[136:137],v[136:137],0,v[138:139]
v_mfma_f32_16x16x32_bf16 v[192:195],v[104:107],v[128:131],v[192:195]
s_waitcnt lgkmcnt(7)
ds_read_b128 v[116:119],v4 offset:1440
ds_read_b64_tr_b16 v[72:73],v8 offset:4608
v_or_b32 v138,v142,v140
v_cvt_pk_bf16_f32 v139,v141,v143
v_mfma_f32_16x16x32_bf16 v[200:203],v[108:111],v[128:131],v[200:203]
ds_read_u16_d16_hi v147,v147
v_pk_fma_f16 v121,v120,v120,s54
v_lshlrev_b32_sdwa v149,1,v149 dst_sel:DWORD dst_unused:UNUSED_PAD src0_sel:DWORD src1_sel:WORD_1
v_mfma_f32_16x16x32_bf16 v[164:167],v[88:91],v[136:139],v[164:167]
ds_read_b64_tr_b16 v[74:75],v8 offset:6656
ds_read_b64_tr_b16 v[76:77],v9 offset:4608
v_pk_mul_f16 v121,v120,v121
v_lshlrev_b32_sdwa v151,1,v151 dst_sel:DWORD dst_unused:UNUSED_PAD src0_sel:DWORD src1_sel:WORD_1
v_mfma_f32_16x16x32_bf16 v[172:175],v[92:95],v[136:139],v[172:175]
ds_read_u16 v148,v148
v_exp_f16 v120,v121
v_mfma_f32_16x16x32_bf16 v[180:183],v[96:99],v[136:139],v[180:183]
ds_read_b64_tr_b16 v[78:79],v9 offset:6656
ds_read_b64_tr_b16 v[80:81],v8 offset:8704
v_lshlrev_b32_sdwa v152,1,v152 dst_sel:DWORD dst_unused:UNUSED_PAD src0_sel:DWORD src1_sel:WORD_1
v_mfma_f32_16x16x32_bf16 v[188:191],v[100:103],v[136:139],v[188:191]
ds_read_u16_d16_hi v150,v150
v_exp_f16_sdwa v120,v121 dst_sel:WORD_1 dst_unused:UNUSED_PRESERVE src0_sel:WORD_1
v_mfma_f32_16x16x32_bf16 v[196:199],v[104:107],v[136:139],v[196:199]
ds_read_u16 v149,v149
v_lshlrev_b32_sdwa v154,1,v154 dst_sel:DWORD dst_unused:UNUSED_PAD src0_sel:DWORD src1_sel:WORD_1
v_mfma_f32_16x16x32_bf16 v[204:207],v[108:111],v[136:139],v[204:207]
s_waitcnt lgkmcnt(15)
ds_read_b64_tr_b16 v[82:83],v8 offset:10752
v_pk_fma_f16 v120,v120,s55,s55
v_lshlrev_b32_sdwa v153,1,v153 dst_sel:DWORD dst_unused:UNUSED_PAD src0_sel:DWORD src1_sel:WORD_1
v_mfma_f32_16x16x32_bf16 v[128:131],v[64:67],v[16:19],v[112:115]
ds_read_u16_d16_hi v151,v151
v_rcp_f16 v121,v120
v_mfma_f32_16x16x32_bf16 v[136:139],v[64:67],v[20:23],v[112:115]
s_waitcnt lgkmcnt(11)
ds_read_b64_tr_b16 v[84:85],v9 offset:8704
v_lshlrev_b32_sdwa v155,1,v155 dst_sel:DWORD dst_unused:UNUSED_PAD src0_sel:DWORD src1_sel:WORD_1
v_mfma_f32_16x16x32_bf16 v[140:143],v[68:71],v[20:23],v[116:119]
ds_read_u16 v152,v152
v_rcp_f16_sdwa v121,v120 dst_sel:WORD_1 dst_unused:UNUSED_PRESERVE src0_sel:WORD_1
v_mfma_f32_16x16x32_bf16 v[132:135],v[68:71],v[16:19],v[116:119]
s_waitcnt lgkmcnt(10)
ds_read_u16_d16_hi v154,v154
v_lshlrev_b32_sdwa v156,1,v156 dst_sel:DWORD dst_unused:UNUSED_PAD src0_sel:DWORD src1_sel:WORD_1
v_mfma_f32_16x16x32_bf16 v[128:131],v[72:75],v[24:27],v[128:131]
ds_read_b64_tr_b16 v[86:87],v9 offset:10752
v_fma_mix_f32 v157,v157,v121,0 op_sel_hi:[0,1,0]
v_lshlrev_b32_sdwa v158,1,v158 dst_sel:DWORD dst_unused:UNUSED_PAD src0_sel:DWORD src1_sel:WORD_1
v_mfma_f32_16x16x32_bf16 v[136:139],v[72:75],v[28:31],v[136:139]
s_waitcnt lgkmcnt(9)
ds_read_u16 v153,v153
v_fma_mix_f32 v159,v159,v121,0 op_sel:[0,1,0] op_sel_hi:[0,1,0]
v_mfma_f32_16x16x32_bf16 v[140:143],v[76:79],v[28:31],v[140:143]
v_mfma_f32_16x16x32_bf16 v[132:135],v[76:79],v[24:27],v[132:135]
s_waitcnt lgkmcnt(6)
ds_read_u16_d16_hi v155,v155
v_mfma_f32_16x16x32_bf16 v[128:131],v[80:83],v[32:35],v[128:131]
v_mfma_f32_16x16x32_bf16 v[136:139],v[80:83],v[36:39],v[136:139]
s_waitcnt lgkmcnt(2)
ds_read_u16 v156,v156
v_lshl_add_u64 v[144:145],v[144:145],0,v[146:147]
v_lshl_add_u64 v[146:147],v[148:149],0,v[150:151]
v_mfma_f32_16x16x32_bf16 v[140:143],v[84:87],v[36:39],v[140:143]
ds_read_u16_d16_hi v158,v158
v_mfma_f32_16x16x32_bf16 v[132:135],v[84:87],v[32:35],v[132:135]
v_lshlrev_b32_sdwa v128,1,v128 dst_sel:DWORD dst_unused:UNUSED_PAD src0_sel:DWORD src1_sel:WORD_1
v_lshlrev_b32_sdwa v130,1,v130 dst_sel:DWORD dst_unused:UNUSED_PAD src0_sel:DWORD src1_sel:WORD_1
v_mfma_f32_16x16x32_bf16 v[208:211],v[88:91],v[144:147],v[208:211]
ds_read_u16 v128,v128
v_lshlrev_b32_sdwa v129,1,v129 dst_sel:DWORD dst_unused:UNUSED_PAD src0_sel:DWORD src1_sel:WORD_1
v_lshlrev_b32_sdwa v131,1,v131 dst_sel:DWORD dst_unused:UNUSED_PAD src0_sel:DWORD src1_sel:WORD_1
v_mfma_f32_16x16x32_bf16 v[216:219],v[92:95],v[144:147],v[216:219]
v_fma_mixlo_f16 v120,v141,s99,0
v_lshlrev_b32_sdwa v132,1,v132 dst_sel:DWORD dst_unused:UNUSED_PAD src0_sel:DWORD src1_sel:WORD_1
v_mfma_f32_16x16x32_bf16 v[224:227],v[96:99],v[144:147],v[224:227]
ds_read_u16_d16_hi v130,v130
v_fma_mixhi_f16 v120,v143,s99,0
v_lshlrev_b32_sdwa v134,1,v134 dst_sel:DWORD dst_unused:UNUSED_PAD src0_sel:DWORD src1_sel:WORD_1
v_mfma_f32_16x16x32_bf16 v[232:235],v[100:103],v[144:147],v[232:235]
s_waitcnt lgkmcnt(4)
ds_read_u16 v129,v129
v_lshl_add_u64 v[152:153],v[152:153],0,v[154:155]
v_mfma_f32_16x16x32_bf16 v[240:243],v[104:107],v[144:147],v[240:243]
s_waitcnt lgkmcnt(3)
v_or_b32 v154,v158,v156
v_cvt_pk_bf16_f32 v155,v157,v159
v_mfma_f32_16x16x32_bf16 v[248:251],v[108:111],v[144:147],v[248:251]
ds_read_u16_d16_hi v131,v131
v_pk_fma_f16 v121,v120,v120,s54
v_lshlrev_b32_sdwa v133,1,v133 dst_sel:DWORD dst_unused:UNUSED_PAD src0_sel:DWORD src1_sel:WORD_1
v_mfma_f32_16x16x32_bf16 v[212:215],v[88:91],v[152:155],v[212:215]
ds_read_b64_tr_b16 v[88:89],v3 offset:45152
v_pk_mul_f16 v121,v120,v121
v_lshlrev_b32_sdwa v135,1,v135 dst_sel:DWORD dst_unused:UNUSED_PAD src0_sel:DWORD src1_sel:WORD_1
v_mfma_f32_16x16x32_bf16 v[220:223],v[92:95],v[152:155],v[220:223]
ds_read_u16 v132,v132
v_exp_f16 v120,v121
v_mfma_f32_16x16x32_bf16 v[228:231],v[96:99],v[152:155],v[228:231]
ds_read_b64_tr_b16 v[90:91],v3 offset:46688
ds_read_b64_tr_b16 v[92:93],v3 offset:45160
v_lshlrev_b32_sdwa v136,1,v136 dst_sel:DWORD dst_unused:UNUSED_PAD src0_sel:DWORD src1_sel:WORD_1
v_mfma_f32_16x16x32_bf16 v[236:239],v[100:103],v[152:155],v[236:239]
ds_read_u16_d16_hi v134,v134
v_exp_f16_sdwa v120,v121 dst_sel:WORD_1 dst_unused:UNUSED_PRESERVE src0_sel:WORD_1
v_mfma_f32_16x16x32_bf16 v[244:247],v[104:107],v[152:155],v[244:247]
ds_read_u16 v133,v133
v_lshlrev_b32_sdwa v138,1,v138 dst_sel:DWORD dst_unused:UNUSED_PAD src0_sel:DWORD src1_sel:WORD_1
v_mfma_f32_16x16x32_bf16 v[252:255],v[108:111],v[152:155],v[252:255]
ds_read_b64_tr_b16 v[94:95],v3 offset:46696
ds_read_b64_tr_b16 v[96:97],v3 offset:45216
v_pk_fma_f16 v120,v120,s55,s55
v_lshlrev_b32_sdwa v137,1,v137 dst_sel:DWORD dst_unused:UNUSED_PAD src0_sel:DWORD src1_sel:WORD_1
v_mfma_f32_16x16x32_bf16 v[144:147],v[64:67],v[40:43],v[112:115]
ds_read_u16_d16_hi v135,v135
v_rcp_f16 v121,v120
v_mfma_f32_16x16x32_bf16 v[152:155],v[64:67],v[44:47],v[112:115]
ds_read_b64_tr_b16 v[98:99],v3 offset:46752
ds_read_b64_tr_b16 v[100:101],v3 offset:45224
v_lshlrev_b32_sdwa v139,1,v139 dst_sel:DWORD dst_unused:UNUSED_PAD src0_sel:DWORD src1_sel:WORD_1
v_mfma_f32_16x16x32_bf16 v[156:159],v[68:71],v[44:47],v[116:119]
ds_read_u16 v136,v136
v_rcp_f16_sdwa v121,v120 dst_sel:WORD_1 dst_unused:UNUSED_PRESERVE src0_sel:WORD_1
v_mfma_f32_16x16x32_bf16 v[148:151],v[68:71],v[40:43],v[116:119]
ds_read_u16_d16_hi v138,v138
v_lshlrev_b32_sdwa v140,1,v140 dst_sel:DWORD dst_unused:UNUSED_PAD src0_sel:DWORD src1_sel:WORD_1
v_mfma_f32_16x16x32_bf16 v[144:147],v[72:75],v[48:51],v[144:147]
ds_read_b64_tr_b16 v[102:103],v3 offset:46760
ds_read_b64_tr_b16 v[104:105],v3 offset:45280
v_fma_mix_f32 v141,v141,v121,0 op_sel_hi:[0,1,0]
v_lshlrev_b32_sdwa v142,1,v142 dst_sel:DWORD dst_unused:UNUSED_PAD src0_sel:DWORD src1_sel:WORD_1
v_mfma_f32_16x16x32_bf16 v[152:155],v[72:75],v[52:55],v[152:155]
ds_read_u16 v137,v137
v_fma_mix_f32 v143,v143,v121,0 op_sel:[0,1,0] op_sel_hi:[0,1,0]
v_mfma_f32_16x16x32_bf16 v[156:159],v[76:79],v[52:55],v[156:159]
ds_read_b64_tr_b16 v[106:107],v3 offset:46816
ds_read_b64_tr_b16 v[108:109],v3 offset:45288
v_mfma_f32_16x16x32_bf16 v[148:151],v[76:79],v[48:51],v[148:151]
ds_read_u16_d16_hi v139,v139
v_mfma_f32_16x16x32_bf16 v[144:147],v[80:83],v[56:59],v[144:147]
ds_read_b64_tr_b16 v[110:111],v3 offset:46824
ds_read_b64_tr_b16 v[64:65],v8 offset:12320
v_mfma_f32_16x16x32_bf16 v[152:155],v[80:83],v[60:63],v[152:155]
s_waitcnt lgkmcnt(12)
ds_read_u16 v140,v140
v_lshl_add_u64 v[128:129],v[128:129],0,v[130:131]
v_lshl_add_u64 v[130:131],v[132:133],0,v[134:135]
v_mfma_f32_16x16x32_bf16 v[156:159],v[84:87],v[60:63],v[156:159]
ds_read_u16_d16_hi v142,v142
v_mfma_f32_16x16x32_bf16 v[148:151],v[84:87],v[56:59],v[148:151]
ds_read_b64_tr_b16 v[66:67],v8 offset:14368
ds_read_b128 v[112:115],v4 offset:0
v_lshlrev_b32_sdwa v144,1,v144 dst_sel:DWORD dst_unused:UNUSED_PAD src0_sel:DWORD src1_sel:WORD_1
v_lshlrev_b32_sdwa v146,1,v146 dst_sel:DWORD dst_unused:UNUSED_PAD src0_sel:DWORD src1_sel:WORD_1
v_mfma_f32_16x16x32_bf16 v[160:163],v[88:91],v[128:131],v[160:163]
ds_read_u16 v144,v144
v_lshlrev_b32_sdwa v145,1,v145 dst_sel:DWORD dst_unused:UNUSED_PAD src0_sel:DWORD src1_sel:WORD_1
v_lshlrev_b32_sdwa v147,1,v147 dst_sel:DWORD dst_unused:UNUSED_PAD src0_sel:DWORD src1_sel:WORD_1
v_mfma_f32_16x16x32_bf16 v[168:171],v[92:95],v[128:131],v[168:171]
s_waitcnt lgkmcnt(15)
ds_read_b64_tr_b16 v[68:69],v9 offset:12320
ds_read_b64_tr_b16 v[70:71],v9 offset:14368
v_fma_mixlo_f16 v120,v157,s99,0
v_lshlrev_b32_sdwa v148,1,v148 dst_sel:DWORD dst_unused:UNUSED_PAD src0_sel:DWORD src1_sel:WORD_1
v_mfma_f32_16x16x32_bf16 v[176:179],v[96:99],v[128:131],v[176:179]
s_waitcnt lgkmcnt(14)
ds_read_u16_d16_hi v146,v146
v_fma_mixhi_f16 v120,v159,s99,0
v_lshlrev_b32_sdwa v150,1,v150 dst_sel:DWORD dst_unused:UNUSED_PAD src0_sel:DWORD src1_sel:WORD_1
v_mfma_f32_16x16x32_bf16 v[184:187],v[100:103],v[128:131],v[184:187]
s_waitcnt lgkmcnt(10)
ds_read_u16 v145,v145
v_lshl_add_u64 v[136:137],v[136:137],0,v[138:139]
v_mfma_f32_16x16x32_bf16 v[192:195],v[104:107],v[128:131],v[192:195]
s_waitcnt lgkmcnt(7)
ds_read_b128 v[116:119],v4 offset:32
ds_read_b64_tr_b16 v[72:73],v8 offset:16416
v_or_b32 v138,v142,v140
v_cvt_pk_bf16_f32 v139,v141,v143
v_mfma_f32_16x16x32_bf16 v[200:203],v[108:111],v[128:131],v[200:203]
ds_read_u16_d16_hi v147,v147
v_pk_fma_f16 v121,v120,v120,s54
v_lshlrev_b32_sdwa v149,1,v149 dst_sel:DWORD dst_unused:UNUSED_PAD src0_sel:DWORD src1_sel:WORD_1
v_mfma_f32_16x16x32_bf16 v[164:167],v[88:91],v[136:139],v[164:167]
ds_read_b64_tr_b16 v[74:75],v8 offset:18464
ds_read_b64_tr_b16 v[76:77],v9 offset:16416
v_pk_mul_f16 v121,v120,v121
v_lshlrev_b32_sdwa v151,1,v151 dst_sel:DWORD dst_unused:UNUSED_PAD src0_sel:DWORD src1_sel:WORD_1
v_mfma_f32_16x16x32_bf16 v[172:175],v[92:95],v[136:139],v[172:175]
ds_read_u16 v148,v148
v_exp_f16 v120,v121
v_mfma_f32_16x16x32_bf16 v[180:183],v[96:99],v[136:139],v[180:183]
ds_read_b64_tr_b16 v[78:79],v9 offset:18464
ds_read_b64_tr_b16 v[80:81],v8 offset:20512
v_lshlrev_b32_sdwa v152,1,v152 dst_sel:DWORD dst_unused:UNUSED_PAD src0_sel:DWORD src1_sel:WORD_1
v_mfma_f32_16x16x32_bf16 v[188:191],v[100:103],v[136:139],v[188:191]
ds_read_u16_d16_hi v150,v150
v_exp_f16_sdwa v120,v121 dst_sel:WORD_1 dst_unused:UNUSED_PRESERVE src0_sel:WORD_1
v_mfma_f32_16x16x32_bf16 v[196:199],v[104:107],v[136:139],v[196:199]
ds_read_u16 v149,v149
v_lshlrev_b32_sdwa v154,1,v154 dst_sel:DWORD dst_unused:UNUSED_PAD src0_sel:DWORD src1_sel:WORD_1
v_mfma_f32_16x16x32_bf16 v[204:207],v[108:111],v[136:139],v[204:207]
s_waitcnt lgkmcnt(15)
ds_read_b64_tr_b16 v[82:83],v8 offset:22560
v_pk_fma_f16 v120,v120,s55,s55
v_lshlrev_b32_sdwa v153,1,v153 dst_sel:DWORD dst_unused:UNUSED_PAD src0_sel:DWORD src1_sel:WORD_1
v_mfma_f32_16x16x32_bf16 v[128:131],v[64:67],v[16:19],v[112:115]
ds_read_u16_d16_hi v151,v151
v_rcp_f16 v121,v120
v_mfma_f32_16x16x32_bf16 v[136:139],v[64:67],v[20:23],v[112:115]
s_waitcnt lgkmcnt(11)
ds_read_b64_tr_b16 v[84:85],v9 offset:20512
v_lshlrev_b32_sdwa v155,1,v155 dst_sel:DWORD dst_unused:UNUSED_PAD src0_sel:DWORD src1_sel:WORD_1
v_mfma_f32_16x16x32_bf16 v[140:143],v[68:71],v[20:23],v[116:119]
ds_read_u16 v152,v152
v_rcp_f16_sdwa v121,v120 dst_sel:WORD_1 dst_unused:UNUSED_PRESERVE src0_sel:WORD_1
v_mfma_f32_16x16x32_bf16 v[132:135],v[68:71],v[16:19],v[116:119]
s_waitcnt lgkmcnt(10)
ds_read_u16_d16_hi v154,v154
v_lshlrev_b32_sdwa v156,1,v156 dst_sel:DWORD dst_unused:UNUSED_PAD src0_sel:DWORD src1_sel:WORD_1
v_mfma_f32_16x16x32_bf16 v[128:131],v[72:75],v[24:27],v[128:131]
ds_read_b64_tr_b16 v[86:87],v9 offset:22560
v_fma_mix_f32 v157,v157,v121,0 op_sel_hi:[0,1,0]
v_lshlrev_b32_sdwa v158,1,v158 dst_sel:DWORD dst_unused:UNUSED_PAD src0_sel:DWORD src1_sel:WORD_1
v_mfma_f32_16x16x32_bf16 v[136:139],v[72:75],v[28:31],v[136:139]
s_waitcnt lgkmcnt(9)
s_nop 0
s_mov_b32 s86,0xbe801d5a
ds_read_u16 v153,v153
v_fma_mix_f32 v159,v159,v121,0 op_sel:[0,1,0] op_sel_hi:[0,1,0]
v_mfma_f32_16x16x32_bf16 v[140:143],v[76:79],v[28:31],v[140:143]
v_mfma_f32_16x16x32_bf16 v[132:135],v[76:79],v[24:27],v[132:135]
s_waitcnt lgkmcnt(6)
ds_read_u16_d16_hi v155,v155
v_mfma_f32_16x16x32_bf16 v[128:131],v[80:83],v[32:35],v[128:131]
v_mfma_f32_16x16x32_bf16 v[136:139],v[80:83],v[36:39],v[136:139]
s_waitcnt lgkmcnt(2)
ds_read_u16 v156,v156
v_lshl_add_u64 v[144:145],v[144:145],0,v[146:147]
v_lshl_add_u64 v[146:147],v[148:149],0,v[150:151]
v_mfma_f32_16x16x32_bf16 v[140:143],v[84:87],v[36:39],v[140:143]
ds_read_u16_d16_hi v158,v158
v_mfma_f32_16x16x32_bf16 v[132:135],v[84:87],v[32:35],v[132:135]
v_lshlrev_b32_sdwa v128,1,v128 dst_sel:DWORD dst_unused:UNUSED_PAD src0_sel:DWORD src1_sel:WORD_1
v_lshlrev_b32_sdwa v130,1,v130 dst_sel:DWORD dst_unused:UNUSED_PAD src0_sel:DWORD src1_sel:WORD_1
v_mfma_f32_16x16x32_bf16 v[208:211],v[88:91],v[144:147],v[208:211]
ds_read_u16 v128,v128
v_lshlrev_b32_sdwa v129,1,v129 dst_sel:DWORD dst_unused:UNUSED_PAD src0_sel:DWORD src1_sel:WORD_1
v_lshlrev_b32_sdwa v131,1,v131 dst_sel:DWORD dst_unused:UNUSED_PAD src0_sel:DWORD src1_sel:WORD_1
v_mfma_f32_16x16x32_bf16 v[216:219],v[92:95],v[144:147],v[216:219]
v_fma_mixlo_f16 v120,v141,s99,0
v_lshlrev_b32_sdwa v132,1,v132 dst_sel:DWORD dst_unused:UNUSED_PAD src0_sel:DWORD src1_sel:WORD_1
v_mfma_f32_16x16x32_bf16 v[224:227],v[96:99],v[144:147],v[224:227]
ds_read_u16_d16_hi v130,v130
v_fma_mixhi_f16 v120,v143,s99,0
v_lshlrev_b32_sdwa v134,1,v134 dst_sel:DWORD dst_unused:UNUSED_PAD src0_sel:DWORD src1_sel:WORD_1
v_mfma_f32_16x16x32_bf16 v[232:235],v[100:103],v[144:147],v[232:235]
s_waitcnt lgkmcnt(4)
ds_read_u16 v129,v129
v_lshl_add_u64 v[152:153],v[152:153],0,v[154:155]
v_mfma_f32_16x16x32_bf16 v[240:243],v[104:107],v[144:147],v[240:243]
s_waitcnt lgkmcnt(3)
v_or_b32 v154,v158,v156
v_cvt_pk_bf16_f32 v155,v157,v159
v_mfma_f32_16x16x32_bf16 v[248:251],v[108:111],v[144:147],v[248:251]
ds_read_u16_d16_hi v131,v131
v_pk_fma_f16 v121,v120,v120,s54
v_lshlrev_b32_sdwa v133,1,v133 dst_sel:DWORD dst_unused:UNUSED_PAD src0_sel:DWORD src1_sel:WORD_1
v_mfma_f32_16x16x32_bf16 v[212:215],v[88:91],v[152:155],v[212:215]
ds_read_b64_tr_b16 v[88:89],v3 offset:51312
v_pk_mul_f16 v121,v120,v121
v_lshlrev_b32_sdwa v135,1,v135 dst_sel:DWORD dst_unused:UNUSED_PAD src0_sel:DWORD src1_sel:WORD_1
v_mfma_f32_16x16x32_bf16 v[220:223],v[92:95],v[152:155],v[220:223]
ds_read_u16 v132,v132
v_exp_f16 v120,v121
v_mfma_f32_16x16x32_bf16 v[228:231],v[96:99],v[152:155],v[228:231]
ds_read_b64_tr_b16 v[90:91],v3 offset:52848
ds_read_b64_tr_b16 v[92:93],v3 offset:51320
v_lshlrev_b32_sdwa v136,1,v136 dst_sel:DWORD dst_unused:UNUSED_PAD src0_sel:DWORD src1_sel:WORD_1
v_mfma_f32_16x16x32_bf16 v[236:239],v[100:103],v[152:155],v[236:239]
ds_read_u16_d16_hi v134,v134
v_exp_f16_sdwa v120,v121 dst_sel:WORD_1 dst_unused:UNUSED_PRESERVE src0_sel:WORD_1
v_mfma_f32_16x16x32_bf16 v[244:247],v[104:107],v[152:155],v[244:247]
ds_read_u16 v133,v133
v_lshlrev_b32_sdwa v138,1,v138 dst_sel:DWORD dst_unused:UNUSED_PAD src0_sel:DWORD src1_sel:WORD_1
v_mfma_f32_16x16x32_bf16 v[252:255],v[108:111],v[152:155],v[252:255]
ds_read_b64_tr_b16 v[94:95],v3 offset:52856
ds_read_b64_tr_b16 v[96:97],v3 offset:51376
v_pk_fma_f16 v120,v120,s55,s55
v_lshlrev_b32_sdwa v137,1,v137 dst_sel:DWORD dst_unused:UNUSED_PAD src0_sel:DWORD src1_sel:WORD_1
v_mfma_f32_16x16x32_bf16 v[144:147],v[64:67],v[40:43],v[112:115]
ds_read_u16_d16_hi v135,v135
v_rcp_f16 v121,v120
v_mfma_f32_16x16x32_bf16 v[152:155],v[64:67],v[44:47],v[112:115]
ds_read_b64_tr_b16 v[98:99],v3 offset:52912
ds_read_b64_tr_b16 v[100:101],v3 offset:51384
v_lshlrev_b32_sdwa v139,1,v139 dst_sel:DWORD dst_unused:UNUSED_PAD src0_sel:DWORD src1_sel:WORD_1
v_mfma_f32_16x16x32_bf16 v[156:159],v[68:71],v[44:47],v[116:119]
ds_read_u16 v136,v136
v_rcp_f16_sdwa v121,v120 dst_sel:WORD_1 dst_unused:UNUSED_PRESERVE src0_sel:WORD_1
v_mfma_f32_16x16x32_bf16 v[148:151],v[68:71],v[40:43],v[116:119]
ds_read_u16_d16_hi v138,v138
v_lshlrev_b32_sdwa v140,1,v140 dst_sel:DWORD dst_unused:UNUSED_PAD src0_sel:DWORD src1_sel:WORD_1
v_mfma_f32_16x16x32_bf16 v[144:147],v[72:75],v[48:51],v[144:147]
ds_read_b64_tr_b16 v[102:103],v3 offset:52920
ds_read_b64_tr_b16 v[104:105],v3 offset:51440
v_fma_mix_f32 v141,v141,v121,0 op_sel_hi:[0,1,0]
v_lshlrev_b32_sdwa v142,1,v142 dst_sel:DWORD dst_unused:UNUSED_PAD src0_sel:DWORD src1_sel:WORD_1
v_mfma_f32_16x16x32_bf16 v[152:155],v[72:75],v[52:55],v[152:155]
ds_read_u16 v137,v137
v_fma_mix_f32 v143,v143,v121,0 op_sel:[0,1,0] op_sel_hi:[0,1,0]
v_mfma_f32_16x16x32_bf16 v[156:159],v[76:79],v[52:55],v[156:159]
ds_read_b64_tr_b16 v[106:107],v3 offset:52976
ds_read_b64_tr_b16 v[108:109],v3 offset:51448
v_mfma_f32_16x16x32_bf16 v[148:151],v[76:79],v[48:51],v[148:151]
ds_read_u16_d16_hi v139,v139
v_mfma_f32_16x16x32_bf16 v[144:147],v[80:83],v[56:59],v[144:147]
ds_read_b64_tr_b16 v[110:111],v3 offset:52984
ds_read_b64_tr_b16 v[64:65],v8 offset:12832
v_mfma_f32_16x16x32_bf16 v[152:155],v[80:83],v[60:63],v[152:155]
s_waitcnt lgkmcnt(12)
ds_read_u16 v140,v140
v_lshl_add_u64 v[128:129],v[128:129],0,v[130:131]
v_lshl_add_u64 v[130:131],v[132:133],0,v[134:135]
v_mfma_f32_16x16x32_bf16 v[156:159],v[84:87],v[60:63],v[156:159]
ds_read_u16_d16_hi v142,v142
v_mfma_f32_16x16x32_bf16 v[148:151],v[84:87],v[56:59],v[148:151]
ds_read_b64_tr_b16 v[66:67],v8 offset:14880
ds_read_b128 v[112:115],v4 offset:128
v_lshlrev_b32_sdwa v144,1,v144 dst_sel:DWORD dst_unused:UNUSED_PAD src0_sel:DWORD src1_sel:WORD_1
v_lshlrev_b32_sdwa v146,1,v146 dst_sel:DWORD dst_unused:UNUSED_PAD src0_sel:DWORD src1_sel:WORD_1
v_mfma_f32_16x16x32_bf16 v[160:163],v[88:91],v[128:131],v[160:163]
ds_read_u16 v144,v144
v_lshlrev_b32_sdwa v145,1,v145 dst_sel:DWORD dst_unused:UNUSED_PAD src0_sel:DWORD src1_sel:WORD_1
v_lshlrev_b32_sdwa v147,1,v147 dst_sel:DWORD dst_unused:UNUSED_PAD src0_sel:DWORD src1_sel:WORD_1
v_mfma_f32_16x16x32_bf16 v[168:171],v[92:95],v[128:131],v[168:171]
s_waitcnt lgkmcnt(15)
ds_read_b64_tr_b16 v[68:69],v9 offset:12832
ds_read_b64_tr_b16 v[70:71],v9 offset:14880
v_fma_mixlo_f16 v120,v157,s99,0
v_lshlrev_b32_sdwa v148,1,v148 dst_sel:DWORD dst_unused:UNUSED_PAD src0_sel:DWORD src1_sel:WORD_1
v_mfma_f32_16x16x32_bf16 v[176:179],v[96:99],v[128:131],v[176:179]
s_waitcnt lgkmcnt(14)
ds_read_u16_d16_hi v146,v146
v_fma_mixhi_f16 v120,v159,s99,0
v_lshlrev_b32_sdwa v150,1,v150 dst_sel:DWORD dst_unused:UNUSED_PAD src0_sel:DWORD src1_sel:WORD_1
v_mfma_f32_16x16x32_bf16 v[184:187],v[100:103],v[128:131],v[184:187]
s_waitcnt lgkmcnt(10)
ds_read_u16 v145,v145
v_lshl_add_u64 v[136:137],v[136:137],0,v[138:139]
v_mfma_f32_16x16x32_bf16 v[192:195],v[104:107],v[128:131],v[192:195]
s_waitcnt lgkmcnt(7)
ds_read_b128 v[116:119],v4 offset:160
ds_read_b64_tr_b16 v[72:73],v8 offset:16928
v_or_b32 v138,v142,v140
v_cvt_pk_bf16_f32 v139,v141,v143
v_mfma_f32_16x16x32_bf16 v[200:203],v[108:111],v[128:131],v[200:203]
ds_read_u16_d16_hi v147,v147
v_pk_fma_f16 v121,v120,v120,s54
v_lshlrev_b32_sdwa v149,1,v149 dst_sel:DWORD dst_unused:UNUSED_PAD src0_sel:DWORD src1_sel:WORD_1
v_mfma_f32_16x16x32_bf16 v[164:167],v[88:91],v[136:139],v[164:167]
ds_read_b64_tr_b16 v[74:75],v8 offset:18976
ds_read_b64_tr_b16 v[76:77],v9 offset:16928
v_pk_mul_f16 v121,v120,v121
v_lshlrev_b32_sdwa v151,1,v151 dst_sel:DWORD dst_unused:UNUSED_PAD src0_sel:DWORD src1_sel:WORD_1
v_mfma_f32_16x16x32_bf16 v[172:175],v[92:95],v[136:139],v[172:175]
ds_read_u16 v148,v148
v_exp_f16 v120,v121
v_mfma_f32_16x16x32_bf16 v[180:183],v[96:99],v[136:139],v[180:183]
ds_read_b64_tr_b16 v[78:79],v9 offset:18976
ds_read_b64_tr_b16 v[80:81],v8 offset:21024
v_lshlrev_b32_sdwa v152,1,v152 dst_sel:DWORD dst_unused:UNUSED_PAD src0_sel:DWORD src1_sel:WORD_1
v_mfma_f32_16x16x32_bf16 v[188:191],v[100:103],v[136:139],v[188:191]
ds_read_u16_d16_hi v150,v150
v_exp_f16_sdwa v120,v121 dst_sel:WORD_1 dst_unused:UNUSED_PRESERVE src0_sel:WORD_1
v_mfma_f32_16x16x32_bf16 v[196:199],v[104:107],v[136:139],v[196:199]
ds_read_u16 v149,v149
v_lshlrev_b32_sdwa v154,1,v154 dst_sel:DWORD dst_unused:UNUSED_PAD src0_sel:DWORD src1_sel:WORD_1
v_mfma_f32_16x16x32_bf16 v[204:207],v[108:111],v[136:139],v[204:207]
s_waitcnt lgkmcnt(15)
ds_read_b64_tr_b16 v[82:83],v8 offset:23072
v_pk_fma_f16 v120,v120,s55,s55
v_lshlrev_b32_sdwa v153,1,v153 dst_sel:DWORD dst_unused:UNUSED_PAD src0_sel:DWORD src1_sel:WORD_1
v_mfma_f32_16x16x32_bf16 v[128:131],v[64:67],v[16:19],v[112:115]
ds_read_u16_d16_hi v151,v151
v_rcp_f16 v121,v120
v_mfma_f32_16x16x32_bf16 v[136:139],v[64:67],v[20:23],v[112:115]
s_waitcnt lgkmcnt(11)
ds_read_b64_tr_b16 v[84:85],v9 offset:21024
v_lshlrev_b32_sdwa v155,1,v155 dst_sel:DWORD dst_unused:UNUSED_PAD src0_sel:DWORD src1_sel:WORD_1
v_mfma_f32_16x16x32_bf16 v[140:143],v[68:71],v[20:23],v[116:119]
ds_read_u16 v152,v152
v_rcp_f16_sdwa v121,v120 dst_sel:WORD_1 dst_unused:UNUSED_PRESERVE src0_sel:WORD_1
v_mfma_f32_16x16x32_bf16 v[132:135],v[68:71],v[16:19],v[116:119]
s_waitcnt lgkmcnt(10)
ds_read_u16_d16_hi v154,v154
v_lshlrev_b32_sdwa v156,1,v156 dst_sel:DWORD dst_unused:UNUSED_PAD src0_sel:DWORD src1_sel:WORD_1
v_mfma_f32_16x16x32_bf16 v[128:131],v[72:75],v[24:27],v[128:131]
ds_read_b64_tr_b16 v[86:87],v9 offset:23072
v_fma_mix_f32 v157,v157,v121,0 op_sel_hi:[0,1,0]
v_lshlrev_b32_sdwa v158,1,v158 dst_sel:DWORD dst_unused:UNUSED_PAD src0_sel:DWORD src1_sel:WORD_1
v_mfma_f32_16x16x32_bf16 v[136:139],v[72:75],v[28:31],v[136:139]
s_waitcnt lgkmcnt(9)
ds_read_u16 v153,v153
v_fma_mix_f32 v159,v159,v121,0 op_sel:[0,1,0] op_sel_hi:[0,1,0]
v_mfma_f32_16x16x32_bf16 v[140:143],v[76:79],v[28:31],v[140:143]
v_mfma_f32_16x16x32_bf16 v[132:135],v[76:79],v[24:27],v[132:135]
s_waitcnt lgkmcnt(6)
ds_read_u16_d16_hi v155,v155
v_mfma_f32_16x16x32_bf16 v[128:131],v[80:83],v[32:35],v[128:131]
v_mfma_f32_16x16x32_bf16 v[136:139],v[80:83],v[36:39],v[136:139]
s_waitcnt lgkmcnt(2)
ds_read_u16 v156,v156
v_lshl_add_u64 v[144:145],v[144:145],0,v[146:147]
v_lshl_add_u64 v[146:147],v[148:149],0,v[150:151]
v_mfma_f32_16x16x32_bf16 v[140:143],v[84:87],v[36:39],v[140:143]
ds_read_u16_d16_hi v158,v158
v_mfma_f32_16x16x32_bf16 v[132:135],v[84:87],v[32:35],v[132:135]
v_lshlrev_b32_sdwa v128,1,v128 dst_sel:DWORD dst_unused:UNUSED_PAD src0_sel:DWORD src1_sel:WORD_1
v_lshlrev_b32_sdwa v130,1,v130 dst_sel:DWORD dst_unused:UNUSED_PAD src0_sel:DWORD src1_sel:WORD_1
v_mfma_f32_16x16x32_bf16 v[208:211],v[88:91],v[144:147],v[208:211]
ds_read_u16 v128,v128
v_lshlrev_b32_sdwa v129,1,v129 dst_sel:DWORD dst_unused:UNUSED_PAD src0_sel:DWORD src1_sel:WORD_1
v_lshlrev_b32_sdwa v131,1,v131 dst_sel:DWORD dst_unused:UNUSED_PAD src0_sel:DWORD src1_sel:WORD_1
v_mfma_f32_16x16x32_bf16 v[216:219],v[92:95],v[144:147],v[216:219]
v_fma_mixlo_f16 v120,v141,s99,0
v_lshlrev_b32_sdwa v132,1,v132 dst_sel:DWORD dst_unused:UNUSED_PAD src0_sel:DWORD src1_sel:WORD_1
v_mfma_f32_16x16x32_bf16 v[224:227],v[96:99],v[144:147],v[224:227]
ds_read_u16_d16_hi v130,v130
v_fma_mixhi_f16 v120,v143,s99,0
v_lshlrev_b32_sdwa v134,1,v134 dst_sel:DWORD dst_unused:UNUSED_PAD src0_sel:DWORD src1_sel:WORD_1
v_mfma_f32_16x16x32_bf16 v[232:235],v[100:103],v[144:147],v[232:235]
s_waitcnt lgkmcnt(4)
ds_read_u16 v129,v129
v_lshl_add_u64 v[152:153],v[152:153],0,v[154:155]
v_mfma_f32_16x16x32_bf16 v[240:243],v[104:107],v[144:147],v[240:243]
s_waitcnt lgkmcnt(3)
v_or_b32 v154,v158,v156
v_cvt_pk_bf16_f32 v155,v157,v159
v_mfma_f32_16x16x32_bf16 v[248:251],v[108:111],v[144:147],v[248:251]
ds_read_u16_d16_hi v131,v131
v_pk_fma_f16 v121,v120,v120,s54
v_lshlrev_b32_sdwa v133,1,v133 dst_sel:DWORD dst_unused:UNUSED_PAD src0_sel:DWORD src1_sel:WORD_1
v_mfma_f32_16x16x32_bf16 v[212:215],v[88:91],v[152:155],v[212:215]
ds_read_b64_tr_b16 v[88:89],v3 offset:57472
v_pk_mul_f16 v121,v120,v121
v_lshlrev_b32_sdwa v135,1,v135 dst_sel:DWORD dst_unused:UNUSED_PAD src0_sel:DWORD src1_sel:WORD_1
v_mfma_f32_16x16x32_bf16 v[220:223],v[92:95],v[152:155],v[220:223]
ds_read_u16 v132,v132
v_exp_f16 v120,v121
v_mfma_f32_16x16x32_bf16 v[228:231],v[96:99],v[152:155],v[228:231]
ds_read_b64_tr_b16 v[90:91],v3 offset:59008
ds_read_b64_tr_b16 v[92:93],v3 offset:57480
v_lshlrev_b32_sdwa v136,1,v136 dst_sel:DWORD dst_unused:UNUSED_PAD src0_sel:DWORD src1_sel:WORD_1
v_mfma_f32_16x16x32_bf16 v[236:239],v[100:103],v[152:155],v[236:239]
ds_read_u16_d16_hi v134,v134
v_exp_f16_sdwa v120,v121 dst_sel:WORD_1 dst_unused:UNUSED_PRESERVE src0_sel:WORD_1
v_mfma_f32_16x16x32_bf16 v[244:247],v[104:107],v[152:155],v[244:247]
ds_read_u16 v133,v133
v_lshlrev_b32_sdwa v138,1,v138 dst_sel:DWORD dst_unused:UNUSED_PAD src0_sel:DWORD src1_sel:WORD_1
v_mfma_f32_16x16x32_bf16 v[252:255],v[108:111],v[152:155],v[252:255]
ds_read_b64_tr_b16 v[94:95],v3 offset:59016
ds_read_b64_tr_b16 v[96:97],v3 offset:57536
v_pk_fma_f16 v120,v120,s55,s55
v_lshlrev_b32_sdwa v137,1,v137 dst_sel:DWORD dst_unused:UNUSED_PAD src0_sel:DWORD src1_sel:WORD_1
v_mfma_f32_16x16x32_bf16 v[144:147],v[64:67],v[40:43],v[112:115]
ds_read_u16_d16_hi v135,v135
v_rcp_f16 v121,v120
v_mfma_f32_16x16x32_bf16 v[152:155],v[64:67],v[44:47],v[112:115]
ds_read_b64_tr_b16 v[98:99],v3 offset:59072
ds_read_b64_tr_b16 v[100:101],v3 offset:57544
v_lshlrev_b32_sdwa v139,1,v139 dst_sel:DWORD dst_unused:UNUSED_PAD src0_sel:DWORD src1_sel:WORD_1
v_mfma_f32_16x16x32_bf16 v[156:159],v[68:71],v[44:47],v[116:119]
ds_read_u16 v136,v136
v_rcp_f16_sdwa v121,v120 dst_sel:WORD_1 dst_unused:UNUSED_PRESERVE src0_sel:WORD_1
v_mfma_f32_16x16x32_bf16 v[148:151],v[68:71],v[40:43],v[116:119]
ds_read_u16_d16_hi v138,v138
v_lshlrev_b32_sdwa v140,1,v140 dst_sel:DWORD dst_unused:UNUSED_PAD src0_sel:DWORD src1_sel:WORD_1
v_mfma_f32_16x16x32_bf16 v[144:147],v[72:75],v[48:51],v[144:147]
ds_read_b64_tr_b16 v[102:103],v3 offset:59080
ds_read_b64_tr_b16 v[104:105],v3 offset:57600
v_fma_mix_f32 v141,v141,v121,0 op_sel_hi:[0,1,0]
v_lshlrev_b32_sdwa v142,1,v142 dst_sel:DWORD dst_unused:UNUSED_PAD src0_sel:DWORD src1_sel:WORD_1
v_mfma_f32_16x16x32_bf16 v[152:155],v[72:75],v[52:55],v[152:155]
ds_read_u16 v137,v137
v_fma_mix_f32 v143,v143,v121,0 op_sel:[0,1,0] op_sel_hi:[0,1,0]
v_mfma_f32_16x16x32_bf16 v[156:159],v[76:79],v[52:55],v[156:159]
ds_read_b64_tr_b16 v[106:107],v3 offset:59136
ds_read_b64_tr_b16 v[108:109],v3 offset:57608
v_mfma_f32_16x16x32_bf16 v[148:151],v[76:79],v[48:51],v[148:151]
ds_read_u16_d16_hi v139,v139
v_mfma_f32_16x16x32_bf16 v[144:147],v[80:83],v[56:59],v[144:147]
ds_read_b64_tr_b16 v[110:111],v3 offset:59144
ds_read_b64_tr_b16 v[64:65],v8 offset:24640
v_mfma_f32_16x16x32_bf16 v[152:155],v[80:83],v[60:63],v[152:155]
s_waitcnt lgkmcnt(12)
ds_read_u16 v140,v140
v_lshl_add_u64 v[128:129],v[128:129],0,v[130:131]
v_lshl_add_u64 v[130:131],v[132:133],0,v[134:135]
v_mfma_f32_16x16x32_bf16 v[156:159],v[84:87],v[60:63],v[156:159]
ds_read_u16_d16_hi v142,v142
v_mfma_f32_16x16x32_bf16 v[148:151],v[84:87],v[56:59],v[148:151]
ds_read_b64_tr_b16 v[66:67],v8 offset:26688
ds_read_b128 v[112:115],v4 offset:256
v_lshlrev_b32_sdwa v144,1,v144 dst_sel:DWORD dst_unused:UNUSED_PAD src0_sel:DWORD src1_sel:WORD_1
v_lshlrev_b32_sdwa v146,1,v146 dst_sel:DWORD dst_unused:UNUSED_PAD src0_sel:DWORD src1_sel:WORD_1
v_mfma_f32_16x16x32_bf16 v[160:163],v[88:91],v[128:131],v[160:163]
ds_read_u16 v144,v144
v_lshlrev_b32_sdwa v145,1,v145 dst_sel:DWORD dst_unused:UNUSED_PAD src0_sel:DWORD src1_sel:WORD_1
v_lshlrev_b32_sdwa v147,1,v147 dst_sel:DWORD dst_unused:UNUSED_PAD src0_sel:DWORD src1_sel:WORD_1
v_mfma_f32_16x16x32_bf16 v[168:171],v[92:95],v[128:131],v[168:171]
s_waitcnt lgkmcnt(15)
ds_read_b64_tr_b16 v[68:69],v9 offset:24640
ds_read_b64_tr_b16 v[70:71],v9 offset:26688
v_fma_mixlo_f16 v120,v157,s99,0
v_lshlrev_b32_sdwa v148,1,v148 dst_sel:DWORD dst_unused:UNUSED_PAD src0_sel:DWORD src1_sel:WORD_1
v_mfma_f32_16x16x32_bf16 v[176:179],v[96:99],v[128:131],v[176:179]
s_waitcnt lgkmcnt(14)
ds_read_u16_d16_hi v146,v146
v_fma_mixhi_f16 v120,v159,s99,0
v_lshlrev_b32_sdwa v150,1,v150 dst_sel:DWORD dst_unused:UNUSED_PAD src0_sel:DWORD src1_sel:WORD_1
v_mfma_f32_16x16x32_bf16 v[184:187],v[100:103],v[128:131],v[184:187]
s_waitcnt lgkmcnt(10)
ds_read_u16 v145,v145
v_lshl_add_u64 v[136:137],v[136:137],0,v[138:139]
v_mfma_f32_16x16x32_bf16 v[192:195],v[104:107],v[128:131],v[192:195]
s_waitcnt lgkmcnt(7)
ds_read_b128 v[116:119],v4 offset:288
ds_read_b64_tr_b16 v[72:73],v8 offset:28736
v_or_b32 v138,v142,v140
v_cvt_pk_bf16_f32 v139,v141,v143
v_mfma_f32_16x16x32_bf16 v[200:203],v[108:111],v[128:131],v[200:203]
ds_read_u16_d16_hi v147,v147
v_pk_fma_f16 v121,v120,v120,s54
v_lshlrev_b32_sdwa v149,1,v149 dst_sel:DWORD dst_unused:UNUSED_PAD src0_sel:DWORD src1_sel:WORD_1
v_mfma_f32_16x16x32_bf16 v[164:167],v[88:91],v[136:139],v[164:167]
ds_read_b64_tr_b16 v[74:75],v8 offset:30784
ds_read_b64_tr_b16 v[76:77],v9 offset:28736
v_pk_mul_f16 v121,v120,v121
v_lshlrev_b32_sdwa v151,1,v151 dst_sel:DWORD dst_unused:UNUSED_PAD src0_sel:DWORD src1_sel:WORD_1
v_mfma_f32_16x16x32_bf16 v[172:175],v[92:95],v[136:139],v[172:175]
ds_read_u16 v148,v148
v_exp_f16 v120,v121
v_mfma_f32_16x16x32_bf16 v[180:183],v[96:99],v[136:139],v[180:183]
ds_read_b64_tr_b16 v[78:79],v9 offset:30784
ds_read_b64_tr_b16 v[80:81],v8 offset:32832
v_lshlrev_b32_sdwa v152,1,v152 dst_sel:DWORD dst_unused:UNUSED_PAD src0_sel:DWORD src1_sel:WORD_1
v_mfma_f32_16x16x32_bf16 v[188:191],v[100:103],v[136:139],v[188:191]
ds_read_u16_d16_hi v150,v150
v_exp_f16_sdwa v120,v121 dst_sel:WORD_1 dst_unused:UNUSED_PRESERVE src0_sel:WORD_1
v_mfma_f32_16x16x32_bf16 v[196:199],v[104:107],v[136:139],v[196:199]
ds_read_u16 v149,v149
v_lshlrev_b32_sdwa v154,1,v154 dst_sel:DWORD dst_unused:UNUSED_PAD src0_sel:DWORD src1_sel:WORD_1
v_mfma_f32_16x16x32_bf16 v[204:207],v[108:111],v[136:139],v[204:207]
s_waitcnt lgkmcnt(15)
ds_read_b64_tr_b16 v[82:83],v8 offset:34880
v_pk_fma_f16 v120,v120,s55,s55
v_lshlrev_b32_sdwa v153,1,v153 dst_sel:DWORD dst_unused:UNUSED_PAD src0_sel:DWORD src1_sel:WORD_1
v_mfma_f32_16x16x32_bf16 v[128:131],v[64:67],v[16:19],v[112:115]
ds_read_u16_d16_hi v151,v151
v_rcp_f16 v121,v120
v_mfma_f32_16x16x32_bf16 v[136:139],v[64:67],v[20:23],v[112:115]
s_waitcnt lgkmcnt(11)
ds_read_b64_tr_b16 v[84:85],v9 offset:32832
v_lshlrev_b32_sdwa v155,1,v155 dst_sel:DWORD dst_unused:UNUSED_PAD src0_sel:DWORD src1_sel:WORD_1
v_mfma_f32_16x16x32_bf16 v[140:143],v[68:71],v[20:23],v[116:119]
ds_read_u16 v152,v152
v_rcp_f16_sdwa v121,v120 dst_sel:WORD_1 dst_unused:UNUSED_PRESERVE src0_sel:WORD_1
v_mfma_f32_16x16x32_bf16 v[132:135],v[68:71],v[16:19],v[116:119]
s_waitcnt lgkmcnt(10)
ds_read_u16_d16_hi v154,v154
v_lshlrev_b32_sdwa v156,1,v156 dst_sel:DWORD dst_unused:UNUSED_PAD src0_sel:DWORD src1_sel:WORD_1
v_mfma_f32_16x16x32_bf16 v[128:131],v[72:75],v[24:27],v[128:131]
ds_read_b64_tr_b16 v[86:87],v9 offset:34880
v_fma_mix_f32 v157,v157,v121,0 op_sel_hi:[0,1,0]
v_lshlrev_b32_sdwa v158,1,v158 dst_sel:DWORD dst_unused:UNUSED_PAD src0_sel:DWORD src1_sel:WORD_1
v_mfma_f32_16x16x32_bf16 v[136:139],v[72:75],v[28:31],v[136:139]
s_waitcnt lgkmcnt(9)
ds_read_u16 v153,v153
v_fma_mix_f32 v159,v159,v121,0 op_sel:[0,1,0] op_sel_hi:[0,1,0]
v_mfma_f32_16x16x32_bf16 v[140:143],v[76:79],v[28:31],v[140:143]
v_mfma_f32_16x16x32_bf16 v[132:135],v[76:79],v[24:27],v[132:135]
s_waitcnt lgkmcnt(6)
ds_read_u16_d16_hi v155,v155
v_mfma_f32_16x16x32_bf16 v[128:131],v[80:83],v[32:35],v[128:131]
v_mfma_f32_16x16x32_bf16 v[136:139],v[80:83],v[36:39],v[136:139]
s_waitcnt lgkmcnt(2)
ds_read_u16 v156,v156
v_lshl_add_u64 v[144:145],v[144:145],0,v[146:147]
v_lshl_add_u64 v[146:147],v[148:149],0,v[150:151]
v_mfma_f32_16x16x32_bf16 v[140:143],v[84:87],v[36:39],v[140:143]
ds_read_u16_d16_hi v158,v158
v_mfma_f32_16x16x32_bf16 v[132:135],v[84:87],v[32:35],v[132:135]
v_lshlrev_b32_sdwa v128,1,v128 dst_sel:DWORD dst_unused:UNUSED_PAD src0_sel:DWORD src1_sel:WORD_1
v_lshlrev_b32_sdwa v130,1,v130 dst_sel:DWORD dst_unused:UNUSED_PAD src0_sel:DWORD src1_sel:WORD_1
v_mfma_f32_16x16x32_bf16 v[208:211],v[88:91],v[144:147],v[208:211]
ds_read_u16 v128,v128
v_lshlrev_b32_sdwa v129,1,v129 dst_sel:DWORD dst_unused:UNUSED_PAD src0_sel:DWORD src1_sel:WORD_1
v_lshlrev_b32_sdwa v131,1,v131 dst_sel:DWORD dst_unused:UNUSED_PAD src0_sel:DWORD src1_sel:WORD_1
v_mfma_f32_16x16x32_bf16 v[216:219],v[92:95],v[144:147],v[216:219]
v_fma_mixlo_f16 v120,v141,s99,0
v_lshlrev_b32_sdwa v132,1,v132 dst_sel:DWORD dst_unused:UNUSED_PAD src0_sel:DWORD src1_sel:WORD_1
v_mfma_f32_16x16x32_bf16 v[224:227],v[96:99],v[144:147],v[224:227]
ds_read_u16_d16_hi v130,v130
v_fma_mixhi_f16 v120,v143,s99,0
v_lshlrev_b32_sdwa v134,1,v134 dst_sel:DWORD dst_unused:UNUSED_PAD src0_sel:DWORD src1_sel:WORD_1
v_mfma_f32_16x16x32_bf16 v[232:235],v[100:103],v[144:147],v[232:235]
s_waitcnt lgkmcnt(4)
ds_read_u16 v129,v129
v_lshl_add_u64 v[152:153],v[152:153],0,v[154:155]
v_mfma_f32_16x16x32_bf16 v[240:243],v[104:107],v[144:147],v[240:243]
s_waitcnt lgkmcnt(3)
v_or_b32 v154,v158,v156
v_cvt_pk_bf16_f32 v155,v157,v159
v_mfma_f32_16x16x32_bf16 v[248:251],v[108:111],v[144:147],v[248:251]
ds_read_u16_d16_hi v131,v131
v_pk_fma_f16 v121,v120,v120,s54
v_lshlrev_b32_sdwa v133,1,v133 dst_sel:DWORD dst_unused:UNUSED_PAD src0_sel:DWORD src1_sel:WORD_1
v_mfma_f32_16x16x32_bf16 v[212:215],v[88:91],v[152:155],v[212:215]
ds_read_b64_tr_b16 v[88:89],v3 offset:63632
v_pk_mul_f16 v121,v120,v121
v_lshlrev_b32_sdwa v135,1,v135 dst_sel:DWORD dst_unused:UNUSED_PAD src0_sel:DWORD src1_sel:WORD_1
v_mfma_f32_16x16x32_bf16 v[220:223],v[92:95],v[152:155],v[220:223]
ds_read_u16 v132,v132
v_exp_f16 v120,v121
v_mfma_f32_16x16x32_bf16 v[228:231],v[96:99],v[152:155],v[228:231]
ds_read_b64_tr_b16 v[90:91],v3 offset:65168
ds_read_b64_tr_b16 v[92:93],v3 offset:63640
v_lshlrev_b32_sdwa v136,1,v136 dst_sel:DWORD dst_unused:UNUSED_PAD src0_sel:DWORD src1_sel:WORD_1
v_mfma_f32_16x16x32_bf16 v[236:239],v[100:103],v[152:155],v[236:239]
ds_read_u16_d16_hi v134,v134
v_exp_f16_sdwa v120,v121 dst_sel:WORD_1 dst_unused:UNUSED_PRESERVE src0_sel:WORD_1
v_mfma_f32_16x16x32_bf16 v[244:247],v[104:107],v[152:155],v[244:247]
ds_read_u16 v133,v133
v_lshlrev_b32_sdwa v138,1,v138 dst_sel:DWORD dst_unused:UNUSED_PAD src0_sel:DWORD src1_sel:WORD_1
v_mfma_f32_16x16x32_bf16 v[252:255],v[108:111],v[152:155],v[252:255]
ds_read_b64_tr_b16 v[94:95],v3 offset:65176
ds_read_b64_tr_b16 v[96:97],v3 offset:63696
v_pk_fma_f16 v120,v120,s55,s55
v_lshlrev_b32_sdwa v137,1,v137 dst_sel:DWORD dst_unused:UNUSED_PAD src0_sel:DWORD src1_sel:WORD_1
v_mfma_f32_16x16x32_bf16 v[144:147],v[64:67],v[40:43],v[112:115]
ds_read_u16_d16_hi v135,v135
v_rcp_f16 v121,v120
v_mfma_f32_16x16x32_bf16 v[152:155],v[64:67],v[44:47],v[112:115]
ds_read_b64_tr_b16 v[98:99],v3 offset:65232
ds_read_b64_tr_b16 v[100:101],v3 offset:63704
v_lshlrev_b32_sdwa v139,1,v139 dst_sel:DWORD dst_unused:UNUSED_PAD src0_sel:DWORD src1_sel:WORD_1
v_mfma_f32_16x16x32_bf16 v[156:159],v[68:71],v[44:47],v[116:119]
ds_read_u16 v136,v136
v_rcp_f16_sdwa v121,v120 dst_sel:WORD_1 dst_unused:UNUSED_PRESERVE src0_sel:WORD_1
v_mfma_f32_16x16x32_bf16 v[148:151],v[68:71],v[40:43],v[116:119]
ds_read_u16_d16_hi v138,v138
v_lshlrev_b32_sdwa v140,1,v140 dst_sel:DWORD dst_unused:UNUSED_PAD src0_sel:DWORD src1_sel:WORD_1
v_mfma_f32_16x16x32_bf16 v[144:147],v[72:75],v[48:51],v[144:147]
ds_read_b64_tr_b16 v[102:103],v3 offset:65240
ds_read_b64_tr_b16 v[104:105],v3 offset:63760
v_fma_mix_f32 v141,v141,v121,0 op_sel_hi:[0,1,0]
v_lshlrev_b32_sdwa v142,1,v142 dst_sel:DWORD dst_unused:UNUSED_PAD src0_sel:DWORD src1_sel:WORD_1
v_mfma_f32_16x16x32_bf16 v[152:155],v[72:75],v[52:55],v[152:155]
ds_read_u16 v137,v137
v_fma_mix_f32 v143,v143,v121,0 op_sel:[0,1,0] op_sel_hi:[0,1,0]
v_mfma_f32_16x16x32_bf16 v[156:159],v[76:79],v[52:55],v[156:159]
ds_read_b64_tr_b16 v[106:107],v3 offset:65296
ds_read_b64_tr_b16 v[108:109],v3 offset:63768
v_mfma_f32_16x16x32_bf16 v[148:151],v[76:79],v[48:51],v[148:151]
ds_read_u16_d16_hi v139,v139
v_mfma_f32_16x16x32_bf16 v[144:147],v[80:83],v[56:59],v[144:147]
ds_read_b64_tr_b16 v[110:111],v3 offset:65304
ds_read_b128 v[64:67],v4 offset:1536
v_mfma_f32_16x16x32_bf16 v[152:155],v[80:83],v[60:63],v[152:155]
s_waitcnt lgkmcnt(12)
ds_read_u16 v140,v140
v_lshl_add_u64 v[128:129],v[128:129],0,v[130:131]
v_lshl_add_u64 v[130:131],v[132:133],0,v[134:135]
v_mfma_f32_16x16x32_bf16 v[156:159],v[84:87],v[60:63],v[156:159]
ds_read_u16_d16_hi v142,v142
v_mfma_f32_16x16x32_bf16 v[148:151],v[84:87],v[56:59],v[148:151]
ds_read_b128 v[68:71],v4 offset:1568
ds_read_b128 v[72:75],v4 offset:1664
v_lshlrev_b32_sdwa v144,1,v144 dst_sel:DWORD dst_unused:UNUSED_PAD src0_sel:DWORD src1_sel:WORD_1
v_lshlrev_b32_sdwa v146,1,v146 dst_sel:DWORD dst_unused:UNUSED_PAD src0_sel:DWORD src1_sel:WORD_1
v_mfma_f32_16x16x32_bf16 v[160:163],v[88:91],v[128:131],v[160:163]
ds_read_u16 v144,v144
ds_read_b128 v[76:79],v4 offset:1696
v_lshlrev_b32_sdwa v145,1,v145 dst_sel:DWORD dst_unused:UNUSED_PAD src0_sel:DWORD src1_sel:WORD_1
v_lshlrev_b32_sdwa v147,1,v147 dst_sel:DWORD dst_unused:UNUSED_PAD src0_sel:DWORD src1_sel:WORD_1
v_fma_mixlo_f16 v120,v157,s99,0
v_mfma_f32_16x16x32_bf16 v[168:171],v[92:95],v[128:131],v[168:171]
s_waitcnt lgkmcnt(15)
ds_read_u16_d16_hi v146,v146
ds_read_b128 v[80:83],v4 offset:1792
v_lshlrev_b32_sdwa v148,1,v148 dst_sel:DWORD dst_unused:UNUSED_PAD src0_sel:DWORD src1_sel:WORD_1
v_fma_mixhi_f16 v120,v159,s99,0
v_lshlrev_b32_sdwa v150,1,v150 dst_sel:DWORD dst_unused:UNUSED_PAD src0_sel:DWORD src1_sel:WORD_1
v_mfma_f32_16x16x32_bf16 v[176:179],v[96:99],v[128:131],v[176:179]
s_waitcnt lgkmcnt(15)
ds_read_u16 v145,v145
ds_read_b128 v[84:87],v4 offset:1824
s_waitcnt lgkmcnt(12)
v_lshl_add_u64 v[136:137],v[136:137],0,v[138:139]
s_waitcnt lgkmcnt(8)
v_or_b32 v138,v142,v140
v_cvt_pk_bf16_f32 v139,v141,v143
v_mfma_f32_16x16x32_bf16 v[184:187],v[100:103],v[128:131],v[184:187]
ds_read_u16_d16_hi v147,v147
v_pk_fma_f16 v121,v120,v120,s54
v_lshlrev_b32_sdwa v149,1,v149 dst_sel:DWORD dst_unused:UNUSED_PAD src0_sel:DWORD src1_sel:WORD_1
v_pk_mul_f16 v121,v120,v121
v_mfma_f32_16x16x32_bf16 v[192:195],v[104:107],v[128:131],v[192:195]
ds_read_u16 v148,v148
v_lshlrev_b32_sdwa v151,1,v151 dst_sel:DWORD dst_unused:UNUSED_PAD src0_sel:DWORD src1_sel:WORD_1
v_exp_f16 v120,v121
v_mfma_f32_16x16x32_bf16 v[200:203],v[108:111],v[128:131],v[200:203]
ds_read_u16_d16_hi v150,v150
v_lshlrev_b32_sdwa v152,1,v152 dst_sel:DWORD dst_unused:UNUSED_PAD src0_sel:DWORD src1_sel:WORD_1
v_exp_f16_sdwa v120,v121 dst_sel:WORD_1 dst_unused:UNUSED_PRESERVE src0_sel:WORD_1
v_mfma_f32_16x16x32_bf16 v[164:167],v[88:91],v[136:139],v[164:167]
ds_read_u16 v149,v149
v_lshlrev_b32_sdwa v154,1,v154 dst_sel:DWORD dst_unused:UNUSED_PAD src0_sel:DWORD src1_sel:WORD_1
v_pk_fma_f16 v120,v120,s55,s55
v_lshlrev_b32_sdwa v153,1,v153 dst_sel:DWORD dst_unused:UNUSED_PAD src0_sel:DWORD src1_sel:WORD_1
v_mfma_f32_16x16x32_bf16 v[172:175],v[92:95],v[136:139],v[172:175]
ds_read_u16_d16_hi v151,v151
v_rcp_f16 v121,v120
v_mfma_f32_16x16x32_bf16 v[180:183],v[96:99],v[136:139],v[180:183]
ds_read_u16 v152,v152
v_lshlrev_b32_sdwa v155,1,v155 dst_sel:DWORD dst_unused:UNUSED_PAD src0_sel:DWORD src1_sel:WORD_1
v_rcp_f16_sdwa v121,v120 dst_sel:WORD_1 dst_unused:UNUSED_PRESERVE src0_sel:WORD_1
v_mfma_f32_16x16x32_bf16 v[188:191],v[100:103],v[136:139],v[188:191]
ds_read_u16_d16_hi v154,v154
v_lshlrev_b32_sdwa v156,1,v156 dst_sel:DWORD dst_unused:UNUSED_PAD src0_sel:DWORD src1_sel:WORD_1
v_fma_mix_f32 v157,v157,v121,0 op_sel_hi:[0,1,0]
v_lshlrev_b32_sdwa v158,1,v158 dst_sel:DWORD dst_unused:UNUSED_PAD src0_sel:DWORD src1_sel:WORD_1
v_mfma_f32_16x16x32_bf16 v[196:199],v[104:107],v[136:139],v[196:199]
ds_read_u16 v153,v153
v_fma_mix_f32 v159,v159,v121,0 op_sel:[0,1,0] op_sel_hi:[0,1,0]
s_waitcnt lgkmcnt(7)
v_lshl_add_u64 v[144:145],v[144:145],0,v[146:147]
s_waitcnt lgkmcnt(3)
v_lshl_add_u64 v[146:147],v[148:149],0,v[150:151]
v_mfma_f32_16x16x32_bf16 v[204:207],v[108:111],v[136:139],v[204:207]
ds_read_u16_d16_hi v155,v155
s_waitcnt lgkmcnt(0)
v_lshl_add_u64 v[152:153],v[152:153],0,v[154:155]
ds_read_u16 v156,v156
ds_read_u16_d16_hi v158,v158
s_waitcnt lgkmcnt(0)
v_or_b32 v154,v158,v156
v_cvt_pk_bf16_f32 v155,v157,v159
v_mfma_f32_16x16x32_bf16 v[208:211],v[88:91],v[144:147],v[208:211]
v_xor_b32 v15,64,v10
v_mul_f32 v160,v160,v64
v_mul_f32 v161,v161,v65
v_mul_f32 v162,v162,v66
v_mul_f32 v163,v163,v67
v_mfma_f32_16x16x32_bf16 v[216:219],v[92:95],v[144:147],v[216:219]
v_mul_f32 v168,v168,v68
v_mul_f32 v169,v169,v69
v_mul_f32 v170,v170,v70
v_mul_f32 v171,v171,v71
v_cvt_pk_bf16_f32 v16,v160,v161
v_mfma_f32_16x16x32_bf16 v[224:227],v[96:99],v[144:147],v[224:227]
v_cvt_pk_bf16_f32 v17,v162,v163
v_cvt_pk_bf16_f32 v18,v168,v169
v_cvt_pk_bf16_f32 v19,v170,v171
v_mul_f32_dpp v176,v176,v72 quad_perm:[1,0,3,2] row_mask:0xf bank_mask:0xf
v_mfma_f32_16x16x32_bf16 v[232:235],v[100:103],v[144:147],v[232:235]
v_mul_f32_dpp v177,v177,v73 quad_perm:[1,0,3,2] row_mask:0xf bank_mask:0xf
v_mul_f32_dpp v178,v178,v74 quad_perm:[1,0,3,2] row_mask:0xf bank_mask:0xf
v_mul_f32_dpp v179,v179,v75 quad_perm:[1,0,3,2] row_mask:0xf bank_mask:0xf
v_mul_f32_dpp v184,v184,v76 quad_perm:[1,0,3,2] row_mask:0xf bank_mask:0xf
v_mul_f32_dpp v185,v185,v77 quad_perm:[1,0,3,2] row_mask:0xf bank_mask:0xf
s_nop 0
s_mov_b32 s86,0xbe801d5a
v_mfma_f32_16x16x32_bf16 v[240:243],v[104:107],v[144:147],v[240:243]
v_mul_f32_dpp v186,v186,v78 quad_perm:[1,0,3,2] row_mask:0xf bank_mask:0xf
v_mul_f32_dpp v187,v187,v79 quad_perm:[1,0,3,2] row_mask:0xf bank_mask:0xf
v_cvt_pk_bf16_f32 v24,v176,v177
v_cvt_pk_bf16_f32 v25,v178,v179
v_mfma_f32_16x16x32_bf16 v[248:251],v[108:111],v[144:147],v[248:251]
v_cvt_pk_bf16_f32 v26,v184,v185
v_cvt_pk_bf16_f32 v27,v186,v187
s_mov_b64 exec,s[12:13]
v_swap_b32 v16,v24
v_swap_b32 v17,v25
v_swap_b32 v18,v26
v_swap_b32 v19,v27
s_mov_b64 exec,-1
s_add_u32 s65,s61,4718592
buffer_store_dwordx4 v[16:19],v10,s[40:43],s65 offen offset:0 sc1
s_add_u32 s65,s61,4718592
buffer_store_dwordx4 v[24:27],v15,s[40:43],s65 offen offset:384 sc1
v_mfma_f32_16x16x32_bf16 v[212:215],v[88:91],v[152:155],v[212:215]
v_mfma_f32_16x16x32_bf16 v[220:223],v[92:95],v[152:155],v[220:223]
v_mul_f32 v192,v192,v80
v_mul_f32 v193,v193,v81
v_mul_f32 v194,v194,v82
v_mul_f32 v195,v195,v83
v_mfma_f32_16x16x32_bf16 v[228:231],v[96:99],v[152:155],v[228:231]
v_mul_f32 v200,v200,v84
v_mul_f32 v201,v201,v85
v_mul_f32 v202,v202,v86
v_mul_f32 v203,v203,v87
v_mfma_f32_16x16x32_bf16 v[236:239],v[100:103],v[152:155],v[236:239]
v_cvt_pk_bf16_f32 v32,v192,v193
v_cvt_pk_bf16_f32 v33,v194,v195
v_cvt_pk_bf16_f32 v34,v200,v201
v_cvt_pk_bf16_f32 v35,v202,v203
v_mul_f32_dpp v164,v164,v64 quad_perm:[1,0,3,2] row_mask:0xf bank_mask:0xf
v_mfma_f32_16x16x32_bf16 v[244:247],v[104:107],v[152:155],v[244:247]
v_mul_f32_dpp v165,v165,v65 quad_perm:[1,0,3,2] row_mask:0xf bank_mask:0xf
v_mul_f32_dpp v166,v166,v66 quad_perm:[1,0,3,2] row_mask:0xf bank_mask:0xf
v_mul_f32_dpp v167,v167,v67 quad_perm:[1,0,3,2] row_mask:0xf bank_mask:0xf
v_mul_f32_dpp v172,v172,v68 quad_perm:[1,0,3,2] row_mask:0xf bank_mask:0xf
v_mfma_f32_16x16x32_bf16 v[252:255],v[108:111],v[152:155],v[252:255]
v_mul_f32_dpp v173,v173,v69 quad_perm:[1,0,3,2] row_mask:0xf bank_mask:0xf
v_mul_f32_dpp v174,v174,v70 quad_perm:[1,0,3,2] row_mask:0xf bank_mask:0xf
v_mul_f32_dpp v175,v175,v71 quad_perm:[1,0,3,2] row_mask:0xf bank_mask:0xf
v_cvt_pk_bf16_f32 v20,v164,v165
v_cvt_pk_bf16_f32 v21,v166,v167
v_cvt_pk_bf16_f32 v22,v172,v173
v_cvt_pk_bf16_f32 v23,v174,v175
s_mov_b64 exec,s[12:13]
v_swap_b32 v32,v20
v_swap_b32 v33,v21
v_swap_b32 v34,v22
v_swap_b32 v35,v23
s_mov_b64 exec,-1
s_add_u32 s65,s61,4718592
buffer_store_dwordx4 v[32:35],v10,s[40:43],s65 offen offset:128 sc1
s_add_u32 s65,s61,4718592
buffer_store_dwordx4 v[20:23],v15,s[40:43],s65 offen offset:512 sc1
v_mul_f32 v180,v180,v72
v_mul_f32 v181,v181,v73
v_mul_f32 v182,v182,v74
v_mul_f32 v183,v183,v75
v_mul_f32 v188,v188,v76
v_mul_f32 v189,v189,v77
v_mul_f32 v190,v190,v78
v_mul_f32 v191,v191,v79
v_cvt_pk_bf16_f32 v28,v180,v181
v_cvt_pk_bf16_f32 v29,v182,v183
v_cvt_pk_bf16_f32 v30,v188,v189
v_cvt_pk_bf16_f32 v31,v190,v191
v_mul_f32_dpp v196,v196,v80 quad_perm:[1,0,3,2] row_mask:0xf bank_mask:0xf
v_mul_f32_dpp v197,v197,v81 quad_perm:[1,0,3,2] row_mask:0xf bank_mask:0xf
v_mul_f32_dpp v198,v198,v82 quad_perm:[1,0,3,2] row_mask:0xf bank_mask:0xf
v_mul_f32_dpp v199,v199,v83 quad_perm:[1,0,3,2] row_mask:0xf bank_mask:0xf
v_mul_f32_dpp v204,v204,v84 quad_perm:[1,0,3,2] row_mask:0xf bank_mask:0xf
v_mul_f32_dpp v205,v205,v85 quad_perm:[1,0,3,2] row_mask:0xf bank_mask:0xf
v_mul_f32_dpp v206,v206,v86 quad_perm:[1,0,3,2] row_mask:0xf bank_mask:0xf
v_mul_f32_dpp v207,v207,v87 quad_perm:[1,0,3,2] row_mask:0xf bank_mask:0xf
v_cvt_pk_bf16_f32 v36,v196,v197
v_cvt_pk_bf16_f32 v37,v198,v199
v_cvt_pk_bf16_f32 v38,v204,v205
v_cvt_pk_bf16_f32 v39,v206,v207
s_mov_b64 exec,s[12:13]
v_swap_b32 v28,v36
v_swap_b32 v29,v37
v_swap_b32 v30,v38
v_swap_b32 v31,v39
s_mov_b64 exec,-1
s_add_u32 s65,s61,4718592
buffer_store_dwordx4 v[28:31],v10,s[40:43],s65 offen offset:256 sc1
s_add_u32 s65,s61,4718592
buffer_store_dwordx4 v[36:39],v15,s[40:43],s65 offen offset:640 sc1
v_mul_f32 v208,v208,v64
v_mul_f32 v209,v209,v65
v_mul_f32 v210,v210,v66
v_mul_f32 v211,v211,v67
v_mul_f32 v216,v216,v68
v_mul_f32 v217,v217,v69
v_mul_f32 v218,v218,v70
v_mul_f32 v219,v219,v71
v_cvt_pk_bf16_f32 v40,v208,v209
v_cvt_pk_bf16_f32 v41,v210,v211
v_cvt_pk_bf16_f32 v42,v216,v217
v_cvt_pk_bf16_f32 v43,v218,v219
v_mul_f32_dpp v224,v224,v72 quad_perm:[1,0,3,2] row_mask:0xf bank_mask:0xf
v_mul_f32_dpp v225,v225,v73 quad_perm:[1,0,3,2] row_mask:0xf bank_mask:0xf
v_mul_f32_dpp v226,v226,v74 quad_perm:[1,0,3,2] row_mask:0xf bank_mask:0xf
v_mul_f32_dpp v227,v227,v75 quad_perm:[1,0,3,2] row_mask:0xf bank_mask:0xf
v_mul_f32_dpp v232,v232,v76 quad_perm:[1,0,3,2] row_mask:0xf bank_mask:0xf
v_mul_f32_dpp v233,v233,v77 quad_perm:[1,0,3,2] row_mask:0xf bank_mask:0xf
v_mul_f32_dpp v234,v234,v78 quad_perm:[1,0,3,2] row_mask:0xf bank_mask:0xf
v_mul_f32_dpp v235,v235,v79 quad_perm:[1,0,3,2] row_mask:0xf bank_mask:0xf
v_cvt_pk_bf16_f32 v48,v224,v225
v_cvt_pk_bf16_f32 v49,v226,v227
v_cvt_pk_bf16_f32 v50,v232,v233
v_cvt_pk_bf16_f32 v51,v234,v235
s_mov_b64 exec,s[12:13]
v_swap_b32 v40,v48
v_swap_b32 v41,v49
v_swap_b32 v42,v50
v_swap_b32 v43,v51
s_mov_b64 exec,-1
s_add_u32 s65,s61,4816896
buffer_store_dwordx4 v[40:43],v10,s[40:43],s65 offen offset:0 sc1
s_add_u32 s65,s61,4816896
buffer_store_dwordx4 v[48:51],v15,s[40:43],s65 offen offset:384 sc1
v_mul_f32 v240,v240,v80
v_mul_f32 v241,v241,v81
v_mul_f32 v242,v242,v82
v_mul_f32 v243,v243,v83
v_mul_f32 v248,v248,v84
v_mul_f32 v249,v249,v85
v_mul_f32 v250,v250,v86
v_mul_f32 v251,v251,v87
v_cvt_pk_bf16_f32 v56,v240,v241
v_cvt_pk_bf16_f32 v57,v242,v243
v_cvt_pk_bf16_f32 v58,v248,v249
v_cvt_pk_bf16_f32 v59,v250,v251
v_mul_f32_dpp v212,v212,v64 quad_perm:[1,0,3,2] row_mask:0xf bank_mask:0xf
v_mul_f32_dpp v213,v213,v65 quad_perm:[1,0,3,2] row_mask:0xf bank_mask:0xf
v_mul_f32_dpp v214,v214,v66 quad_perm:[1,0,3,2] row_mask:0xf bank_mask:0xf
v_mul_f32_dpp v215,v215,v67 quad_perm:[1,0,3,2] row_mask:0xf bank_mask:0xf
v_mul_f32_dpp v220,v220,v68 quad_perm:[1,0,3,2] row_mask:0xf bank_mask:0xf
v_mul_f32_dpp v221,v221,v69 quad_perm:[1,0,3,2] row_mask:0xf bank_mask:0xf
v_mul_f32_dpp v222,v222,v70 quad_perm:[1,0,3,2] row_mask:0xf bank_mask:0xf
v_mul_f32_dpp v223,v223,v71 quad_perm:[1,0,3,2] row_mask:0xf bank_mask:0xf
v_cvt_pk_bf16_f32 v44,v212,v213
v_cvt_pk_bf16_f32 v45,v214,v215
v_cvt_pk_bf16_f32 v46,v220,v221
v_cvt_pk_bf16_f32 v47,v222,v223
s_mov_b64 exec,s[12:13]
v_swap_b32 v56,v44
v_swap_b32 v57,v45
v_swap_b32 v58,v46
v_swap_b32 v59,v47
s_mov_b64 exec,-1
s_add_u32 s65,s61,4816896
buffer_store_dwordx4 v[56:59],v10,s[40:43],s65 offen offset:128 sc1
s_add_u32 s65,s61,4816896
buffer_store_dwordx4 v[44:47],v15,s[40:43],s65 offen offset:512 sc1
v_mul_f32 v228,v228,v72
v_mul_f32 v229,v229,v73
v_mul_f32 v230,v230,v74
v_mul_f32 v231,v231,v75
v_mul_f32 v236,v236,v76
v_mul_f32 v237,v237,v77
v_mul_f32 v238,v238,v78
v_mul_f32 v239,v239,v79
v_cvt_pk_bf16_f32 v52,v228,v229
v_cvt_pk_bf16_f32 v53,v230,v231
v_cvt_pk_bf16_f32 v54,v236,v237
v_cvt_pk_bf16_f32 v55,v238,v239
v_mul_f32_dpp v244,v244,v80 quad_perm:[1,0,3,2] row_mask:0xf bank_mask:0xf
v_mul_f32_dpp v245,v245,v81 quad_perm:[1,0,3,2] row_mask:0xf bank_mask:0xf
v_mul_f32_dpp v246,v246,v82 quad_perm:[1,0,3,2] row_mask:0xf bank_mask:0xf
v_mul_f32_dpp v247,v247,v83 quad_perm:[1,0,3,2] row_mask:0xf bank_mask:0xf
v_mul_f32_dpp v252,v252,v84 quad_perm:[1,0,3,2] row_mask:0xf bank_mask:0xf
v_mul_f32_dpp v253,v253,v85 quad_perm:[1,0,3,2] row_mask:0xf bank_mask:0xf
v_mul_f32_dpp v254,v254,v86 quad_perm:[1,0,3,2] row_mask:0xf bank_mask:0xf
v_mul_f32_dpp v255,v255,v87 quad_perm:[1,0,3,2] row_mask:0xf bank_mask:0xf
v_cvt_pk_bf16_f32 v60,v244,v245
v_cvt_pk_bf16_f32 v61,v246,v247
v_cvt_pk_bf16_f32 v62,v252,v253
v_cvt_pk_bf16_f32 v63,v254,v255
s_mov_b64 exec,s[12:13]
v_swap_b32 v52,v60
v_swap_b32 v53,v61
v_swap_b32 v54,v62
v_swap_b32 v55,v63
s_mov_b64 exec,-1
s_add_u32 s65,s61,4816896
buffer_store_dwordx4 v[52:55],v10,s[40:43],s65 offen offset:256 sc1
s_add_u32 s65,s61,4816896
buffer_store_dwordx4 v[60:63],v15,s[40:43],s65 offen offset:640 sc1
v_and_b32 v1,0x7c007c00,v7
v_lshrrev_b32 v2,16,v1
v_and_b32 v1,0xffff,v1
v_max_u32 v1,v1,v2
v_cmp_eq_u32 vcc,0x7c00,v1
s_or_b32 s64,vcc_lo,vcc_hi
v_mov_b32 v1,s64
v_mov_b32 v2,8192
s_mov_b64 exec,1
ds_or_b32 v2,v1 offset:3456
s_mov_b64 exec,-1
s_waitcnt lgkmcnt(0)
s_barrier
ds_read_b32 v1,v2 offset:3456
s_waitcnt lgkmcnt(0)
v_readfirstlane_b32 s64,v1
s_cmp_lg_u32 s64,0
s_cbranch_scc1 N_fallback_%=
s_endpgm
N_mainend_%=:
N_fallback_%=:
s_setprio 0
s_waitcnt vmcnt(0) lgkmcnt(0)
s_barrier
v_lshrrev_b32 v237,6,%1
v_and_b32 v232,63,%1
v_readfirstlane_b32 s52,v237
v_lshlrev_b32 v248,4,v232
v_min_u32 v244,383,%1
v_lshlrev_b32 v238,1,v244
global_load_ushort v244,v238,s[26:27]
v_min_u32 v245,95,%1
v_lshlrev_b32 v239,1,v245
global_load_ushort v245,v239,s[30:31]
global_load_ushort v246,v239,s[32:33]
s_mov_b64 s[92:93],s[22:23]
s_and_b32 s93,s93,0xffff
s_mov_b32 s94,1728
s_mov_b32 s95,0x00020000
s_lshl_b32 s71,s52,11
s_add_u32 s71,s71,62208
s_mov_b32 m0,s71
s_movk_i32 s64,1024
buffer_load_dwordx4 v248,s[92:95],0 offen lds
s_add_u32 m0,s71,1024
s_nop 0
buffer_load_dwordx4 v248,s[92:95],s64 offen lds
v_mov_b32 v250,s71
s_mov_b64 s[36:37],s[20:21]
s_mov_b64 s[40:41],s[34:35]
s_mov_b64 s[44:45],s[24:25]
s_mov_b64 s[48:49],s[28:29]
s_sub_u32 s36,s36,192
s_subb_u32 s37,s37,0
s_and_b32 s37,s37,0xffff
s_mov_b32 s38,50331840
s_mov_b32 s39,0x00020000
s_and_b32 s41,s41,0xffff
s_mov_b32 s42,50331648
s_mov_b32 s43,0x00020000
s_and_b32 s45,s45,0xffff
s_mov_b32 s46,73728
s_mov_b32 s47,0x00020000
s_and_b32 s49,s49,0xffff
s_mov_b32 s50,73728
s_mov_b32 s51,0x00020000
s_mov_b32 s60,-1
s_mov_b32 s61,0xffff
s_mov_b32 s62,-1
s_mov_b32 s63,0x3fffff
s_and_b32 s64,%0,7
s_lshr_b32 s70,%0,3
s_lshl_b32 s56,s64,6
s_mul_i32 s57,s70,3072
s_and_b32 s66,s52,3
s_mul_i32 s68,s66,864
s_mul_i32 s67,s66,54
s_mul_i32 s58,s66,1536
v_add_u32 v237,s67,v232
v_mul_u32_u24 v238,0xaaab,v237
v_lshrrev_b32 v238,19,v238
v_mul_u32_u24 v239,12,v238
v_sub_u32 v239,v237,v239
v_bfe_u32 v237,v238,2,2
v_xor_b32 v239,v239,v237
v_lshlrev_b32 v239,4,v239
v_mul_u32_u24 v237,192,v238
v_add_u32 v247,v237,v239
s_cmp_eq_u32 s70,0
s_cselect_b32 s71,0,99
s_cmp_eq_u32 s70,31
s_cselect_b32 s71,17,s71
v_cmp_eq_u32 vcc,s71,v238
v_mov_b32 v237,0x7fff0000
s_sub_u32 s67,s56,1
s_mul_i32 s59,s67,98304
s_add_u32 s59,s59,s57
v_cndmask_b32 v247,v247,v237,vcc
s_lshr_b32 s66,s52,2
s_mul_i32 s64,s66,3456
s_add_u32 s64,s64,s68
s_mul_i32 s65,s66,98304
s_add_u32 s65,s65,s59
s_add_u32 s67,s67,s66
s_cmp_lt_i32 s67,0
s_cselect_b32 s71,0x70000000,s65
s_mov_b64 exec,s[62:63]
s_add_u32 m0,s64,0
s_nop 0
buffer_load_dwordx4 v247,s[36:39],s71 offen lds
s_add_u32 m0,s64,6912
s_add_u32 s71,s65,196608
buffer_load_dwordx4 v247,s[36:39],s71 offen lds
s_add_u32 m0,s64,13824
s_add_u32 s71,s65,393216
buffer_load_dwordx4 v247,s[36:39],s71 offen lds
s_add_u32 m0,s64,20736
s_add_u32 s71,s65,589824
buffer_load_dwordx4 v247,s[36:39],s71 offen lds
s_add_u32 m0,s64,27648
s_add_u32 s71,s65,786432
buffer_load_dwordx4 v247,s[36:39],s71 offen lds
s_add_u32 m0,s64,34560
s_add_u32 s71,s65,983040
buffer_load_dwordx4 v247,s[36:39],s71 offen lds
s_add_u32 m0,s64,41472
s_add_u32 s71,s65,1179648
buffer_load_dwordx4 v247,s[36:39],s71 offen lds
s_add_u32 m0,s64,48384
s_add_u32 s71,s65,1376256
buffer_load_dwordx4 v247,s[36:39],s71 offen lds
s_add_u32 m0,s64,55296
s_add_u32 s71,s65,1572864
buffer_load_dwordx4 v247,s[36:39],s71 offen lds
s_mov_b64 exec,-1
v_lshrrev_b32 v237,2,v232
v_mul_u32_u24 v237,768,v237
v_and_b32 v238,3,v232
v_lshl_add_u32 v237,v238,4,v237
s_and_b32 s66,s52,3
s_mul_i32 s71,s66,18432
v_add_u32 v27,s71,v237
v_add_u32 v28,s58,v248
s_mov_b32 s54,62208
s_mov_b32 s53,0
s_mov_b32 s96,0x70000000
s_add_u32 s68,s68,s54
s_add_u32 s59,s59,1572864
s_lshl_b32 s66,s52,1
s_add_u32 s66,s66,s56
s_mul_i32 s69,s66,98304
s_add_u32 s69,s69,s57
s_cmp_ge_u32 s52,4
s_cbranch_scc1 L_noW_%=
s_mov_b64 exec,s[60:61]
s_add_u32 m0,s58,124416
s_movk_i32 s64,0
buffer_load_dwordx4 v27,s[44:47],s64 offen lds
s_add_u32 m0,s58,125184
s_movk_i32 s65,9216
buffer_load_dwordx4 v27,s[44:47],s65 offen lds
s_mov_b64 exec,-1
s_mov_b64 exec,s[60:61]
s_add_u32 m0,s58,142848
s_mov_b32 s64,0
buffer_load_dwordx4 v28,s[48:51],s64 offen lds
s_add_u32 m0,s58,143616
s_mov_b32 s65,768
buffer_load_dwordx4 v28,s[48:51],s65 offen lds
s_mov_b64 exec,-1
s_mov_b64 exec,s[60:61]
s_add_u32 m0,s58,130560
s_movk_i32 s64,64
buffer_load_dwordx4 v27,s[44:47],s64 offen lds
s_add_u32 m0,s58,131328
s_movk_i32 s65,9280
buffer_load_dwordx4 v27,s[44:47],s65 offen lds
s_mov_b64 exec,-1
s_mov_b64 exec,s[60:61]
s_add_u32 m0,s58,136704
s_movk_i32 s64,128
buffer_load_dwordx4 v27,s[44:47],s64 offen lds
s_add_u32 m0,s58,137472
s_movk_i32 s65,9344
buffer_load_dwordx4 v27,s[44:47],s65 offen lds
s_mov_b64 exec,-1
s_branch L_W_%=
L_noW_%=:
v_mov_b32 v27,v247
s_mul_i32 s64,s70,2304
s_and_b32 s65,s52,3
s_mul_i32 s65,s65,576
s_add_u32 s64,s64,s65
v_min_u32 v237,560,v248
buffer_load_dwordx4 v[252:255],v237,s[44:47],s64 offen
buffer_load_dwordx4 v[252:255],v237,s[48:51],s64 offen
L_W_%=:
v_and_b32 v233,15,v232
v_lshrrev_b32 v234,4,v232
v_lshrrev_b32 v235,5,v232
v_and_b32 v237,1,v234
v_lshlrev_b32 v237,1,v237
v_or_b32 v236,v237,v235
v_lshrrev_b32 v237,3,v233
v_lshlrev_b32 v237,2,v237
v_and_b32 v238,3,v233
v_or_b32 v241,v237,v238
v_lshrrev_b32 v237,2,v233
v_and_b32 v242,1,v237
v_sub_u32 v237,v236,v242
v_cmp_gt_u32 vcc,3,v237
v_mov_b32 v238,0
v_mov_b32 v239,-1
v_cndmask_b32 v237,v238,v237,vcc
v_cndmask_b32 v249,v238,v239,vcc
v_mul_u32_u24 v237,576,v237
v_lshl_add_u32 v237,v241,1,v237
v_add_u32 v250,v250,v237
v_lshrrev_b32 v237,1,v241
v_cmp_eq_u32 vcc,0,v237
s_nop 1
v_cndmask_b32 v32,v238,v249,vcc
v_cmp_eq_u32 vcc,1,v237
s_nop 1
v_cndmask_b32 v33,v238,v249,vcc
v_cmp_eq_u32 vcc,2,v237
s_nop 1
v_cndmask_b32 v34,v238,v249,vcc
v_cmp_eq_u32 vcc,3,v237
s_nop 1
v_cndmask_b32 v35,v238,v249,vcc
v_and_b32 v243,1,v241
v_lshlrev_b32 v243,4,v243
s_mul_i32 s71,s52,6912
v_add_u32 v237,0,v233
v_bfe_u32 v238,v237,2,2
v_mul_u32_u24 v239,18,v236
v_add_u32 v239,v239,v237
v_mul_u32_u24 v239,192,v239
v_add_u32 v239,s71,v239
v_xor_b32 v240,0,v238
v_lshl_add_u32 v4,v240,4,v239
v_xor_b32 v240,1,v238
v_lshl_add_u32 v5,v240,4,v239
v_xor_b32 v240,2,v238
v_lshl_add_u32 v6,v240,4,v239
v_xor_b32 v240,3,v238
v_lshl_add_u32 v7,v240,4,v239
v_add_u32 v237,1,v233
v_bfe_u32 v238,v237,2,2
v_mul_u32_u24 v239,18,v236
v_add_u32 v239,v239,v237
v_mul_u32_u24 v239,192,v239
v_add_u32 v239,s71,v239
v_xor_b32 v240,0,v238
v_lshl_add_u32 v8,v240,4,v239
v_xor_b32 v240,1,v238
v_lshl_add_u32 v9,v240,4,v239
v_xor_b32 v240,2,v238
v_lshl_add_u32 v10,v240,4,v239
v_xor_b32 v240,3,v238
v_lshl_add_u32 v11,v240,4,v239
v_add_u32 v237,2,v233
v_bfe_u32 v238,v237,2,2
v_mul_u32_u24 v239,18,v236
v_add_u32 v239,v239,v237
v_mul_u32_u24 v239,192,v239
v_add_u32 v239,s71,v239
v_xor_b32 v240,0,v238
v_lshl_add_u32 v12,v240,4,v239
v_xor_b32 v240,1,v238
v_lshl_add_u32 v13,v240,4,v239
v_xor_b32 v240,2,v238
v_lshl_add_u32 v14,v240,4,v239
v_xor_b32 v240,3,v238
v_lshl_add_u32 v15,v240,4,v239
v_lshrrev_b32 v237,2,v233
v_lshl_add_u32 v237,v235,2,v237
v_and_b32 v238,1,v234
v_lshlrev_b32 v238,5,v238
v_and_b32 v239,3,v233
v_lshl_add_u32 v238,v239,3,v238
v_add_u32 v238,124416,v238
v_lshl_add_u32 v16,v237,6,v238
v_mul_u32_u24 v237,192,v237
v_add_u32 v17,v237,v238
v_add_u32 v17,18432,v17
v_lshlrev_b32 v237,4,v235
v_add_u32 v18,161280,v237
v_add_u32 v237,s71,v248
v_add_u32 v19,3648,v237
v_and_b32 v237,1,v234
v_add_u32 v237,1,v237
v_mul_u32_u24 v237,18,v237
v_add_u32 v238,1,v233
v_add_u32 v237,v237,v238
v_mul_u32_u24 v237,192,v237
v_add_u32 v237,s71,v237
v_lshl_add_u32 v237,v235,3,v237
v_bfe_u32 v238,v238,2,2
v_xor_b32 v240,0,v238
v_lshl_add_u32 v20,v240,4,v237
v_xor_b32 v240,1,v238
v_lshl_add_u32 v21,v240,4,v237
v_xor_b32 v240,2,v238
v_lshl_add_u32 v22,v240,4,v237
v_xor_b32 v240,3,v238
v_lshl_add_u32 v23,v240,4,v237
v_add_u32 v237,0,v232
v_mul_u32_u24 v238,0xaaab,v237
v_lshrrev_b32 v238,19,v238
v_mul_u32_u24 v239,12,v238
v_sub_u32 v239,v237,v239
v_add_u32 v237,1,v238
v_bfe_u32 v237,v237,2,2
v_xor_b32 v239,v239,v237
v_lshlrev_b32 v239,4,v239
v_mul_u32_u24 v237,192,v238
v_add_u32 v24,v237,v239
v_add_u32 v237,64,v232
v_mul_u32_u24 v238,0xaaab,v237
v_lshrrev_b32 v238,19,v238
v_mul_u32_u24 v239,12,v238
v_sub_u32 v239,v237,v239
v_add_u32 v237,1,v238
v_bfe_u32 v237,v237,2,2
v_xor_b32 v239,v239,v237
v_lshlrev_b32 v239,4,v239
v_mul_u32_u24 v237,192,v238
v_add_u32 v25,v237,v239
v_add_u32 v237,128,v232
v_mul_u32_u24 v238,0xaaab,v237
v_lshrrev_b32 v238,19,v238
v_mul_u32_u24 v239,12,v238
v_sub_u32 v239,v237,v239
v_add_u32 v237,1,v238
v_bfe_u32 v237,v237,2,2
v_xor_b32 v239,v239,v237
v_lshlrev_b32 v239,4,v239
v_mul_u32_u24 v237,192,v238
v_add_u32 v26,v237,v239
s_cmp_ge_u32 s52,4
s_cbranch_scc1 L_wB_%=
s_waitcnt vmcnt(17)
s_branch L_wA_%=
L_wB_%=:
s_waitcnt vmcnt(11)
L_wA_%=:
ds_read_u16 v36,v250 offset:0
ds_read_u16 v37,v250 offset:16
ds_read_u16 v38,v250 offset:32
ds_read_u16 v39,v250 offset:48
ds_read_u16 v40,v250 offset:64
ds_read_u16 v41,v250 offset:80
ds_read_u16 v42,v250 offset:96
ds_read_u16 v43,v250 offset:112
ds_read_u16 v44,v250 offset:128
ds_read_u16 v45,v250 offset:144
ds_read_u16 v46,v250 offset:160
ds_read_u16 v47,v250 offset:176
ds_read_u16 v48,v250 offset:192
ds_read_u16 v49,v250 offset:208
ds_read_u16 v50,v250 offset:224
ds_read_u16 v51,v250 offset:240
ds_read_u16 v52,v250 offset:256
ds_read_u16 v53,v250 offset:272
ds_read_u16 v54,v250 offset:288
ds_read_u16 v55,v250 offset:304
ds_read_u16 v56,v250 offset:320
ds_read_u16 v57,v250 offset:336
ds_read_u16 v58,v250 offset:352
ds_read_u16 v59,v250 offset:368
ds_read_u16 v60,v250 offset:384
ds_read_u16 v61,v250 offset:400
ds_read_u16 v62,v250 offset:416
ds_read_u16 v63,v250 offset:432
ds_read_u16 v64,v250 offset:448
ds_read_u16 v65,v250 offset:464
ds_read_u16 v66,v250 offset:480
ds_read_u16 v67,v250 offset:496
ds_read_u16 v68,v250 offset:512
ds_read_u16 v69,v250 offset:528
ds_read_u16 v70,v250 offset:544
ds_read_u16 v71,v250 offset:560
v_lshlrev_b32 v244,16,v244
v_lshlrev_b32 v245,16,v245
v_lshlrev_b32 v246,16,v246
v_mul_f32 v244,0xbeeff558,v244
v_mul_f32 v245,0xbeeff558,v245
v_mul_f32 v246,0xc0088e99,v246
v_min_u32 v237,383,%1
v_lshlrev_b32 v237,2,v237
v_add_u32 v237,161280,v237
ds_write_b32 v237,v244 offset:0
v_min_u32 v237,95,%1
v_lshlrev_b32 v237,2,v237
v_add_u32 v237,161280,v237
ds_write_b32 v237,v245 offset:1920
ds_write_b32 v237,v246 offset:1536
s_waitcnt lgkmcnt(0)
v_lshlrev_b32 v36,v243,v36
v_lshlrev_b32 v37,v243,v37
v_lshlrev_b32 v38,v243,v38
v_lshlrev_b32 v39,v243,v39
v_lshlrev_b32 v40,v243,v40
v_lshlrev_b32 v41,v243,v41
v_lshlrev_b32 v42,v243,v42
v_lshlrev_b32 v43,v243,v43
v_lshlrev_b32 v44,v243,v44
v_lshlrev_b32 v45,v243,v45
v_lshlrev_b32 v46,v243,v46
v_lshlrev_b32 v47,v243,v47
v_lshlrev_b32 v48,v243,v48
v_lshlrev_b32 v49,v243,v49
v_lshlrev_b32 v50,v243,v50
v_lshlrev_b32 v51,v243,v51
v_lshlrev_b32 v52,v243,v52
v_lshlrev_b32 v53,v243,v53
v_lshlrev_b32 v54,v243,v54
v_lshlrev_b32 v55,v243,v55
v_lshlrev_b32 v56,v243,v56
v_lshlrev_b32 v57,v243,v57
v_lshlrev_b32 v58,v243,v58
v_lshlrev_b32 v59,v243,v59
v_lshlrev_b32 v60,v243,v60
v_lshlrev_b32 v61,v243,v61
v_lshlrev_b32 v62,v243,v62
v_lshlrev_b32 v63,v243,v63
v_lshlrev_b32 v64,v243,v64
v_lshlrev_b32 v65,v243,v65
v_lshlrev_b32 v66,v243,v66
v_lshlrev_b32 v67,v243,v67
v_lshlrev_b32 v68,v243,v68
v_lshlrev_b32 v69,v243,v69
v_lshlrev_b32 v70,v243,v70
v_lshlrev_b32 v71,v243,v71
s_cmp_ge_u32 s52,4
s_cbranch_scc1 L_fB_%=
s_waitcnt vmcnt(6)
s_branch L_fA_%=
L_fB_%=:
s_waitcnt vmcnt(2)
L_fA_%=:
s_cmp_ge_u32 s52,4
s_cbranch_scc1 L_pass1_%=
L_pass0_%=:
s_barrier
ds_read_b128 v[200:203],v4 offset:0
ds_read_b128 v[204:207],v8 offset:0
ds_read_b128 v[208:211],v12 offset:0
ds_read_b128 v[212:215],v5 offset:0
ds_read_b128 v[216:219],v9 offset:0
ds_read_b128 v[220:223],v13 offset:0
ds_read_b128 v[96:99],v18 offset:1920
ds_read_b128 v[100:103],v18 offset:1952
ds_read_b128 v[104:107],v18 offset:1984
ds_read_b128 v[108:111],v18 offset:2016
ds_read_b128 v[112:115],v18 offset:2048
ds_read_b128 v[116:119],v18 offset:2080
ds_read_b128 v[120:123],v18 offset:2112
ds_read_b128 v[124:127],v18 offset:2144
ds_read_b128 v[128:131],v18 offset:2176
ds_read_b128 v[132:135],v18 offset:2208
ds_read_b128 v[136:139],v18 offset:2240
ds_read_b128 v[140:143],v18 offset:2272
v_and_b32 v72,v32,v36
v_and_b32 v73,v33,v36
v_and_b32 v74,v34,v36
v_and_b32 v75,v35,v36
v_and_b32 v76,v32,v48
v_and_b32 v77,v33,v48
v_and_b32 v78,v34,v48
v_and_b32 v79,v35,v48
v_and_b32 v80,v32,v60
v_and_b32 v81,v33,v60
v_and_b32 v82,v34,v60
v_and_b32 v83,v35,v60
v_and_b32 v84,v32,v37
v_and_b32 v85,v33,v37
v_and_b32 v86,v34,v37
v_and_b32 v87,v35,v37
v_and_b32 v88,v32,v49
v_and_b32 v89,v33,v49
v_and_b32 v90,v34,v49
v_and_b32 v91,v35,v49
v_and_b32 v92,v32,v61
v_and_b32 v93,v33,v61
v_and_b32 v94,v34,v61
v_and_b32 v95,v35,v61
s_setprio 1
s_waitcnt lgkmcnt(15)
v_mfma_f32_16x16x32_bf16 v[144:147],v[72:75],v[200:203],0
v_mfma_f32_16x16x32_bf16 v[144:147],v[76:79],v[204:207],v[144:147]
v_mfma_f32_16x16x32_bf16 v[144:147],v[80:83],v[208:211],v[144:147]
ds_read_b128 v[200:203],v6 offset:0
ds_read_b128 v[204:207],v10 offset:0
ds_read_b128 v[208:211],v14 offset:0
s_setprio 0
v_and_b32 v72,v32,v38
v_and_b32 v73,v33,v38
v_and_b32 v74,v34,v38
v_and_b32 v75,v35,v38
v_and_b32 v76,v32,v50
v_and_b32 v77,v33,v50
v_and_b32 v78,v34,v50
v_and_b32 v79,v35,v50
v_and_b32 v80,v32,v62
v_and_b32 v81,v33,v62
v_and_b32 v82,v34,v62
v_and_b32 v83,v35,v62
v_mul_f32 v237,v144,v144
v_mul_f32 v236,v145,v145
v_mul_f32 v239,v146,v146
v_mul_f32 v238,v147,v147
s_setprio 1
s_waitcnt lgkmcnt(15)
v_mfma_f32_16x16x32_bf16 v[148:151],v[84:87],v[212:215],0
v_mfma_f32_16x16x32_bf16 v[148:151],v[88:91],v[216:219],v[148:151]
v_mfma_f32_16x16x32_bf16 v[148:151],v[92:95],v[220:223],v[148:151]
ds_read_b128 v[212:215],v7 offset:0
ds_read_b128 v[216:219],v11 offset:0
ds_read_b128 v[220:223],v15 offset:0
s_setprio 0
v_and_b32 v84,v32,v39
v_and_b32 v85,v33,v39
v_and_b32 v86,v34,v39
v_and_b32 v87,v35,v39
v_and_b32 v88,v32,v51
v_and_b32 v89,v33,v51
v_and_b32 v90,v34,v51
v_and_b32 v91,v35,v51
v_and_b32 v92,v32,v63
v_and_b32 v93,v33,v63
v_and_b32 v94,v34,v63
v_and_b32 v95,v35,v63
v_add_f32 v232,v144,v148
v_fmac_f32 v237,v148,v148
v_add_f32 v233,v145,v149
v_fmac_f32 v236,v149,v149
v_add_f32 v234,v146,v150
v_fmac_f32 v239,v150,v150
v_add_f32 v235,v147,v151
v_fmac_f32 v238,v151,v151
s_setprio 1
s_waitcnt lgkmcnt(3)
v_mfma_f32_16x16x32_bf16 v[152:155],v[72:75],v[200:203],0
v_mfma_f32_16x16x32_bf16 v[152:155],v[76:79],v[204:207],v[152:155]
v_mfma_f32_16x16x32_bf16 v[152:155],v[80:83],v[208:211],v[152:155]
ds_read_b128 v[200:203],v4 offset:64
ds_read_b128 v[204:207],v8 offset:64
ds_read_b128 v[208:211],v12 offset:64
s_setprio 0
v_and_b32 v72,v32,v40
v_and_b32 v73,v33,v40
v_and_b32 v74,v34,v40
v_and_b32 v75,v35,v40
v_and_b32 v76,v32,v52
v_and_b32 v77,v33,v52
v_and_b32 v78,v34,v52
v_and_b32 v79,v35,v52
v_and_b32 v80,v32,v64
v_and_b32 v81,v33,v64
v_and_b32 v82,v34,v64
v_and_b32 v83,v35,v64
v_add_f32 v232,v232,v152
v_fmac_f32 v237,v152,v152
v_add_f32 v233,v233,v153
v_fmac_f32 v236,v153,v153
v_add_f32 v234,v234,v154
v_fmac_f32 v239,v154,v154
v_add_f32 v235,v235,v155
v_fmac_f32 v238,v155,v155
s_setprio 1
s_waitcnt lgkmcnt(3)
v_mfma_f32_16x16x32_bf16 v[156:159],v[84:87],v[212:215],0
v_mfma_f32_16x16x32_bf16 v[156:159],v[88:91],v[216:219],v[156:159]
v_mfma_f32_16x16x32_bf16 v[156:159],v[92:95],v[220:223],v[156:159]
ds_read_b128 v[212:215],v5 offset:64
ds_read_b128 v[216:219],v9 offset:64
ds_read_b128 v[220:223],v13 offset:64
s_setprio 0
v_and_b32 v84,v32,v41
v_and_b32 v85,v33,v41
v_and_b32 v86,v34,v41
v_and_b32 v87,v35,v41
v_and_b32 v88,v32,v53
v_and_b32 v89,v33,v53
v_and_b32 v90,v34,v53
v_and_b32 v91,v35,v53
v_and_b32 v92,v32,v65
v_and_b32 v93,v33,v65
v_and_b32 v94,v34,v65
v_and_b32 v95,v35,v65
v_add_f32 v232,v232,v156
v_fmac_f32 v237,v156,v156
v_add_f32 v233,v233,v157
v_fmac_f32 v236,v157,v157
v_add_f32 v234,v234,v158
v_fmac_f32 v239,v158,v158
v_add_f32 v235,v235,v159
v_fmac_f32 v238,v159,v159
s_setprio 1
s_waitcnt lgkmcnt(3)
v_mfma_f32_16x16x32_bf16 v[160:163],v[72:75],v[200:203],0
v_mfma_f32_16x16x32_bf16 v[160:163],v[76:79],v[204:207],v[160:163]
v_mfma_f32_16x16x32_bf16 v[160:163],v[80:83],v[208:211],v[160:163]
ds_read_b128 v[200:203],v6 offset:64
ds_read_b128 v[204:207],v10 offset:64
ds_read_b128 v[208:211],v14 offset:64
s_setprio 0
v_and_b32 v72,v32,v42
v_and_b32 v73,v33,v42
v_and_b32 v74,v34,v42
v_and_b32 v75,v35,v42
v_and_b32 v76,v32,v54
v_and_b32 v77,v33,v54
v_and_b32 v78,v34,v54
v_and_b32 v79,v35,v54
v_and_b32 v80,v32,v66
v_and_b32 v81,v33,v66
v_and_b32 v82,v34,v66
v_and_b32 v83,v35,v66
v_add_f32 v232,v232,v160
v_fmac_f32 v237,v160,v160
v_add_f32 v233,v233,v161
v_fmac_f32 v236,v161,v161
v_add_f32 v234,v234,v162
v_fmac_f32 v239,v162,v162
v_add_f32 v235,v235,v163
v_fmac_f32 v238,v163,v163
s_setprio 1
s_waitcnt lgkmcnt(3)
v_mfma_f32_16x16x32_bf16 v[164:167],v[84:87],v[212:215],0
v_mfma_f32_16x16x32_bf16 v[164:167],v[88:91],v[216:219],v[164:167]
v_mfma_f32_16x16x32_bf16 v[164:167],v[92:95],v[220:223],v[164:167]
ds_read_b128 v[212:215],v7 offset:64
ds_read_b128 v[216:219],v11 offset:64
ds_read_b128 v[220:223],v15 offset:64
s_setprio 0
v_and_b32 v84,v32,v43
v_and_b32 v85,v33,v43
v_and_b32 v86,v34,v43
v_and_b32 v87,v35,v43
v_and_b32 v88,v32,v55
v_and_b32 v89,v33,v55
v_and_b32 v90,v34,v55
v_and_b32 v91,v35,v55
v_and_b32 v92,v32,v67
v_and_b32 v93,v33,v67
v_and_b32 v94,v34,v67
v_and_b32 v95,v35,v67
v_add_f32 v232,v232,v164
v_fmac_f32 v237,v164,v164
v_add_f32 v233,v233,v165
v_fmac_f32 v236,v165,v165
v_add_f32 v234,v234,v166
v_fmac_f32 v239,v166,v166
v_add_f32 v235,v235,v167
v_fmac_f32 v238,v167,v167
s_setprio 1
s_waitcnt lgkmcnt(3)
v_mfma_f32_16x16x32_bf16 v[168:171],v[72:75],v[200:203],0
v_mfma_f32_16x16x32_bf16 v[168:171],v[76:79],v[204:207],v[168:171]
v_mfma_f32_16x16x32_bf16 v[168:171],v[80:83],v[208:211],v[168:171]
ds_read_b128 v[200:203],v4 offset:128
ds_read_b128 v[204:207],v8 offset:128
ds_read_b128 v[208:211],v12 offset:128
s_setprio 0
v_and_b32 v72,v32,v44
v_and_b32 v73,v33,v44
v_and_b32 v74,v34,v44
v_and_b32 v75,v35,v44
v_and_b32 v76,v32,v56
v_and_b32 v77,v33,v56
v_and_b32 v78,v34,v56
v_and_b32 v79,v35,v56
v_and_b32 v80,v32,v68
v_and_b32 v81,v33,v68
v_and_b32 v82,v34,v68
v_and_b32 v83,v35,v68
v_add_f32 v232,v232,v168
v_fmac_f32 v237,v168,v168
v_add_f32 v233,v233,v169
v_fmac_f32 v236,v169,v169
v_add_f32 v234,v234,v170
v_fmac_f32 v239,v170,v170
v_add_f32 v235,v235,v171
v_fmac_f32 v238,v171,v171
s_setprio 1
s_waitcnt lgkmcnt(3)
v_mfma_f32_16x16x32_bf16 v[172:175],v[84:87],v[212:215],0
v_mfma_f32_16x16x32_bf16 v[172:175],v[88:91],v[216:219],v[172:175]
v_mfma_f32_16x16x32_bf16 v[172:175],v[92:95],v[220:223],v[172:175]
ds_read_b128 v[212:215],v5 offset:128
ds_read_b128 v[216:219],v9 offset:128
ds_read_b128 v[220:223],v13 offset:128
s_setprio 0
v_and_b32 v84,v32,v45
v_and_b32 v85,v33,v45
v_and_b32 v86,v34,v45
v_and_b32 v87,v35,v45
v_and_b32 v88,v32,v57
v_and_b32 v89,v33,v57
v_and_b32 v90,v34,v57
v_and_b32 v91,v35,v57
v_and_b32 v92,v32,v69
v_and_b32 v93,v33,v69
v_and_b32 v94,v34,v69
v_and_b32 v95,v35,v69
v_add_f32 v232,v232,v172
v_fmac_f32 v237,v172,v172
v_add_f32 v233,v233,v173
v_fmac_f32 v236,v173,v173
v_add_f32 v234,v234,v174
v_fmac_f32 v239,v174,v174
v_add_f32 v235,v235,v175
v_fmac_f32 v238,v175,v175
s_setprio 1
s_waitcnt lgkmcnt(3)
v_mfma_f32_16x16x32_bf16 v[176:179],v[72:75],v[200:203],0
v_mfma_f32_16x16x32_bf16 v[176:179],v[76:79],v[204:207],v[176:179]
v_mfma_f32_16x16x32_bf16 v[176:179],v[80:83],v[208:211],v[176:179]
ds_read_b128 v[200:203],v6 offset:128
ds_read_b128 v[204:207],v10 offset:128
ds_read_b128 v[208:211],v14 offset:128
s_setprio 0
v_and_b32 v72,v32,v46
v_and_b32 v73,v33,v46
v_and_b32 v74,v34,v46
v_and_b32 v75,v35,v46
v_and_b32 v76,v32,v58
v_and_b32 v77,v33,v58
v_and_b32 v78,v34,v58
v_and_b32 v79,v35,v58
v_and_b32 v80,v32,v70
v_and_b32 v81,v33,v70
v_and_b32 v82,v34,v70
v_and_b32 v83,v35,v70
v_add_f32 v232,v232,v176
v_fmac_f32 v237,v176,v176
v_add_f32 v233,v233,v177
v_fmac_f32 v236,v177,v177
v_add_f32 v234,v234,v178
v_fmac_f32 v239,v178,v178
v_add_f32 v235,v235,v179
v_fmac_f32 v238,v179,v179
s_setprio 1
s_waitcnt lgkmcnt(3)
v_mfma_f32_16x16x32_bf16 v[180:183],v[84:87],v[212:215],0
v_mfma_f32_16x16x32_bf16 v[180:183],v[88:91],v[216:219],v[180:183]
v_mfma_f32_16x16x32_bf16 v[180:183],v[92:95],v[220:223],v[180:183]
ds_read_b128 v[212:215],v7 offset:128
ds_read_b128 v[216:219],v11 offset:128
ds_read_b128 v[220:223],v15 offset:128
s_setprio 0
v_and_b32 v84,v32,v47
v_and_b32 v85,v33,v47
v_and_b32 v86,v34,v47
v_and_b32 v87,v35,v47
v_and_b32 v88,v32,v59
v_and_b32 v89,v33,v59
v_and_b32 v90,v34,v59
v_and_b32 v91,v35,v59
v_and_b32 v92,v32,v71
v_and_b32 v93,v33,v71
v_and_b32 v94,v34,v71
v_and_b32 v95,v35,v71
v_add_f32 v232,v232,v180
v_fmac_f32 v237,v180,v180
v_add_f32 v233,v233,v181
v_fmac_f32 v236,v181,v181
v_add_f32 v234,v234,v182
v_fmac_f32 v239,v182,v182
v_add_f32 v235,v235,v183
v_fmac_f32 v238,v183,v183
s_setprio 1
s_waitcnt lgkmcnt(3)
v_mfma_f32_16x16x32_bf16 v[184:187],v[72:75],v[200:203],0
v_mfma_f32_16x16x32_bf16 v[184:187],v[76:79],v[204:207],v[184:187]
v_mfma_f32_16x16x32_bf16 v[184:187],v[80:83],v[208:211],v[184:187]
s_setprio 0
s_nop 7
v_add_f32 v232,v232,v184
v_fmac_f32 v237,v184,v184
v_add_f32 v233,v233,v185
v_fmac_f32 v236,v185,v185
v_add_f32 v234,v234,v186
v_fmac_f32 v239,v186,v186
v_add_f32 v235,v235,v187
v_fmac_f32 v238,v187,v187
s_setprio 1
s_waitcnt lgkmcnt(0)
v_mfma_f32_16x16x32_bf16 v[188:191],v[84:87],v[212:215],0
v_mfma_f32_16x16x32_bf16 v[188:191],v[88:91],v[216:219],v[188:191]
v_mfma_f32_16x16x32_bf16 v[188:191],v[92:95],v[220:223],v[188:191]
s_setprio 0
ds_read_b64_tr_b16 v[200:201],v16 offset:0
ds_read_b64_tr_b16 v[202:203],v16 offset:512
ds_read_b64_tr_b16 v[204:205],v16 offset:1024
ds_read_b64_tr_b16 v[206:207],v16 offset:1536
ds_read_b64_tr_b16 v[208:209],v16 offset:2048
ds_read_b64_tr_b16 v[210:211],v16 offset:2560
ds_read_b64_tr_b16 v[212:213],v16 offset:3072
ds_read_b64_tr_b16 v[214:215],v16 offset:3584
ds_read_b64_tr_b16 v[216:217],v16 offset:4096
ds_read_b64_tr_b16 v[218:219],v16 offset:4608
ds_read_b64_tr_b16 v[220:221],v16 offset:5120
ds_read_b64_tr_b16 v[222:223],v16 offset:5632
v_add_f32 v232,v232,v188
v_fmac_f32 v237,v188,v188
v_add_f32 v233,v233,v189
v_fmac_f32 v236,v189,v189
v_add_f32 v234,v234,v190
v_fmac_f32 v239,v190,v190
v_add_f32 v235,v235,v191
v_fmac_f32 v238,v191,v191
v_add_f32 v232,v232,v233
v_add_f32 v234,v234,v235
v_add_f32 v237,v237,v236
v_add_f32 v239,v239,v238
v_add_f32 v240,v232,v234
v_add_f32 v241,v237,v239
s_nop 1
v_permlane32_swap_b32 v240,v241
v_add_f32 v240,v240,v241
v_mov_b32 v241,v240
s_nop 1
v_permlane32_swap_b32 v240,v241
v_mul_f32 v240,0x3c2aaaab,v240
v_mul_f32 v241,0x3c2aaaab,v241
v_fma_f32 v241,-v240,v240,v241
v_max_f32 v241,0,v241
v_add_f32 v241,0x3727c5ac,v241
v_rsq_f32 v241,v241
s_nop 0
v_mul_f32 v242,0xbeeff558,v241
v_mul_f32 v243,0x3eeff558,v241
v_mul_f32 v243,v240,v243
v_fma_f32 v144,v144,v242,v243
v_fma_f32 v145,v145,v242,v243
v_fma_f32 v146,v146,v242,v243
v_fma_f32 v147,v147,v242,v243
v_fma_f32 v148,v148,v242,v243
v_fma_f32 v149,v149,v242,v243
v_fma_f32 v150,v150,v242,v243
v_fma_f32 v151,v151,v242,v243
v_fma_f32 v152,v152,v242,v243
v_fma_f32 v153,v153,v242,v243
v_fma_f32 v154,v154,v242,v243
v_fma_f32 v155,v155,v242,v243
v_fma_f32 v156,v156,v242,v243
v_fma_f32 v157,v157,v242,v243
v_fma_f32 v158,v158,v242,v243
v_fma_f32 v159,v159,v242,v243
v_fma_f32 v160,v160,v242,v243
v_fma_f32 v161,v161,v242,v243
v_fma_f32 v162,v162,v242,v243
v_fma_f32 v163,v163,v242,v243
v_fma_f32 v164,v164,v242,v243
v_fma_f32 v165,v165,v242,v243
v_fma_f32 v166,v166,v242,v243
v_fma_f32 v167,v167,v242,v243
v_fma_f32 v168,v168,v242,v243
v_fma_f32 v169,v169,v242,v243
v_fma_f32 v170,v170,v242,v243
v_fma_f32 v171,v171,v242,v243
v_fma_f32 v172,v172,v242,v243
v_fma_f32 v173,v173,v242,v243
v_fma_f32 v174,v174,v242,v243
v_fma_f32 v175,v175,v242,v243
v_fma_f32 v176,v176,v242,v243
v_fma_f32 v177,v177,v242,v243
v_fma_f32 v178,v178,v242,v243
v_fma_f32 v179,v179,v242,v243
v_fma_f32 v180,v180,v242,v243
v_fma_f32 v181,v181,v242,v243
v_fma_f32 v182,v182,v242,v243
v_fma_f32 v183,v183,v242,v243
v_fma_f32 v184,v184,v242,v243
v_fma_f32 v185,v185,v242,v243
v_fma_f32 v186,v186,v242,v243
v_fma_f32 v187,v187,v242,v243
v_fma_f32 v188,v188,v242,v243
v_fma_f32 v189,v189,v242,v243
v_fma_f32 v190,v190,v242,v243
v_fma_f32 v191,v191,v242,v243
s_setprio 1
v_cvt_pk_bf16_f32 v72,v144,v145
v_cvt_pk_bf16_f32 v73,v146,v147
v_cvt_pk_bf16_f32 v74,v148,v149
v_cvt_pk_bf16_f32 v75,v150,v151
v_cvt_pk_bf16_f32 v76,v152,v153
v_cvt_pk_bf16_f32 v77,v154,v155
v_cvt_pk_bf16_f32 v78,v156,v157
v_cvt_pk_bf16_f32 v79,v158,v159
ds_read_b128 v[144:147],v18 offset:0
ds_read_b128 v[148:151],v18 offset:32
ds_read_b128 v[152:155],v18 offset:64
ds_read_b128 v[156:159],v18 offset:96
v_cvt_pk_bf16_f32 v80,v160,v161
v_cvt_pk_bf16_f32 v81,v162,v163
v_cvt_pk_bf16_f32 v82,v164,v165
v_cvt_pk_bf16_f32 v83,v166,v167
v_cvt_pk_bf16_f32 v84,v168,v169
v_cvt_pk_bf16_f32 v85,v170,v171
v_cvt_pk_bf16_f32 v86,v172,v173
v_cvt_pk_bf16_f32 v87,v174,v175
s_waitcnt lgkmcnt(0)
v_mfma_f32_32x32x16_bf16 v[144:159],v[200:203],v[72:75],v[144:159]
v_cvt_pk_bf16_f32 v88,v176,v177
v_cvt_pk_bf16_f32 v89,v178,v179
v_cvt_pk_bf16_f32 v90,v180,v181
v_cvt_pk_bf16_f32 v91,v182,v183
v_mfma_f32_32x32x16_bf16 v[144:159],v[204:207],v[76:79],v[144:159]
v_cvt_pk_bf16_f32 v92,v184,v185
v_cvt_pk_bf16_f32 v93,v186,v187
v_cvt_pk_bf16_f32 v94,v188,v189
v_cvt_pk_bf16_f32 v95,v190,v191
v_mfma_f32_32x32x16_bf16 v[144:159],v[208:211],v[80:83],v[144:159]
v_mfma_f32_32x32x16_bf16 v[144:159],v[212:215],v[84:87],v[144:159]
v_mfma_f32_32x32x16_bf16 v[144:159],v[216:219],v[88:91],v[144:159]
v_mfma_f32_32x32x16_bf16 v[144:159],v[220:223],v[92:95],v[144:159]
s_setprio 0
s_nop 7
s_nop 1
s_waitcnt vmcnt(2)
s_barrier
v_fmaak_f32 v176,v144,v144,0x409d30fc
v_fmaak_f32 v177,v145,v145,0x409d30fc
v_fmaak_f32 v178,v146,v146,0x409d30fc
v_fmaak_f32 v179,v147,v147,0x409d30fc
v_fmaak_f32 v180,v148,v148,0x409d30fc
v_fmaak_f32 v181,v149,v149,0x409d30fc
v_fmaak_f32 v182,v150,v150,0x409d30fc
v_fmaak_f32 v183,v151,v151,0x409d30fc
v_fmaak_f32 v184,v152,v152,0x409d30fc
v_fmaak_f32 v185,v153,v153,0x409d30fc
v_fmaak_f32 v186,v154,v154,0x409d30fc
v_fmaak_f32 v187,v155,v155,0x409d30fc
v_fmaak_f32 v188,v156,v156,0x409d30fc
v_fmaak_f32 v189,v157,v157,0x409d30fc
v_fmaak_f32 v190,v158,v158,0x409d30fc
v_fmaak_f32 v191,v159,v159,0x409d30fc
v_mul_f32 v176,v144,v176
v_mul_f32 v177,v145,v177
v_mul_f32 v178,v146,v178
v_mul_f32 v179,v147,v179
v_mul_f32 v180,v148,v180
v_mul_f32 v181,v149,v181
v_mul_f32 v182,v150,v182
v_mul_f32 v183,v151,v183
v_mul_f32 v184,v152,v184
v_mul_f32 v185,v153,v185
v_mul_f32 v186,v154,v186
v_mul_f32 v187,v155,v187
v_mul_f32 v188,v156,v188
v_mul_f32 v189,v157,v189
v_mul_f32 v190,v158,v190
v_mul_f32 v191,v159,v191
s_setprio 1
v_exp_f32 v176,v176
v_exp_f32 v177,v177
v_exp_f32 v178,v178
v_exp_f32 v179,v179
v_exp_f32 v180,v180
v_exp_f32 v181,v181
v_exp_f32 v182,v182
v_exp_f32 v183,v183
v_exp_f32 v184,v184
v_exp_f32 v185,v185
v_exp_f32 v186,v186
v_exp_f32 v187,v187
v_exp_f32 v188,v188
v_exp_f32 v189,v189
v_exp_f32 v190,v190
v_exp_f32 v191,v191
ds_read_b64_tr_b16 v[200:201],v16 offset:6144
ds_read_b64_tr_b16 v[202:203],v16 offset:6656
ds_read_b64_tr_b16 v[204:205],v16 offset:7168
ds_read_b64_tr_b16 v[206:207],v16 offset:7680
ds_read_b64_tr_b16 v[208:209],v16 offset:8192
ds_read_b64_tr_b16 v[210:211],v16 offset:8704
ds_read_b64_tr_b16 v[212:213],v16 offset:9216
ds_read_b64_tr_b16 v[214:215],v16 offset:9728
ds_read_b64_tr_b16 v[216:217],v16 offset:10240
ds_read_b64_tr_b16 v[218:219],v16 offset:10752
ds_read_b64_tr_b16 v[220:221],v16 offset:11264
ds_read_b64_tr_b16 v[222:223],v16 offset:11776
ds_read_b128 v[160:163],v18 offset:128
ds_read_b128 v[164:167],v18 offset:160
ds_read_b128 v[168:171],v18 offset:192
ds_read_b128 v[172:175],v18 offset:224
s_mov_b64 exec,s[60:61]
s_add_u32 m0,s58,148992
s_mov_b32 s64,6144
buffer_load_dwordx4 v28,s[48:51],s64 offen lds
s_add_u32 m0,s58,149760
s_mov_b32 s65,6912
buffer_load_dwordx4 v28,s[48:51],s65 offen lds
s_mov_b64 exec,-1
s_setprio 0
v_add_f32 v176,1.0,v176
v_add_f32 v177,1.0,v177
v_add_f32 v178,1.0,v178
v_add_f32 v179,1.0,v179
v_add_f32 v180,1.0,v180
v_add_f32 v181,1.0,v181
v_add_f32 v182,1.0,v182
v_add_f32 v183,1.0,v183
v_add_f32 v184,1.0,v184
v_add_f32 v185,1.0,v185
v_add_f32 v186,1.0,v186
v_add_f32 v187,1.0,v187
v_add_f32 v188,1.0,v188
v_add_f32 v189,1.0,v189
v_add_f32 v190,1.0,v190
v_add_f32 v191,1.0,v191
s_setprio 1
s_waitcnt lgkmcnt(0)
v_mfma_f32_32x32x16_bf16 v[160:175],v[200:203],v[72:75],v[160:175]
v_rcp_f32 v176,v176
v_rcp_f32 v177,v177
v_rcp_f32 v178,v178
v_mfma_f32_32x32x16_bf16 v[160:175],v[204:207],v[76:79],v[160:175]
v_rcp_f32 v179,v179
v_rcp_f32 v180,v180
v_rcp_f32 v181,v181
v_mfma_f32_32x32x16_bf16 v[160:175],v[208:211],v[80:83],v[160:175]
v_rcp_f32 v182,v182
v_rcp_f32 v183,v183
v_rcp_f32 v184,v184
v_mfma_f32_32x32x16_bf16 v[160:175],v[212:215],v[84:87],v[160:175]
v_rcp_f32 v185,v185
v_rcp_f32 v186,v186
v_rcp_f32 v187,v187
v_mfma_f32_32x32x16_bf16 v[160:175],v[216:219],v[88:91],v[160:175]
v_rcp_f32 v188,v188
v_rcp_f32 v189,v189
v_mfma_f32_32x32x16_bf16 v[160:175],v[220:223],v[92:95],v[160:175]
v_rcp_f32 v190,v190
v_rcp_f32 v191,v191
ds_read_b64_tr_b16 v[200:201],v17 offset:0
ds_read_b64_tr_b16 v[202:203],v17 offset:1536
ds_read_b64_tr_b16 v[204:205],v17 offset:3072
ds_read_b64_tr_b16 v[206:207],v17 offset:4608
ds_read_b64_tr_b16 v[208:209],v17 offset:64
ds_read_b64_tr_b16 v[210:211],v17 offset:1600
ds_read_b64_tr_b16 v[212:213],v17 offset:3136
ds_read_b64_tr_b16 v[214:215],v17 offset:4672
ds_read_b64_tr_b16 v[216:217],v17 offset:128
ds_read_b64_tr_b16 v[218:219],v17 offset:1664
ds_read_b64_tr_b16 v[220:221],v17 offset:3200
ds_read_b64_tr_b16 v[222:223],v17 offset:4736
s_mov_b64 exec,s[60:61]
s_add_u32 m0,s58,124416
s_movk_i32 s64,192
buffer_load_dwordx4 v27,s[44:47],s64 offen lds
s_add_u32 m0,s58,125184
s_movk_i32 s65,9408
buffer_load_dwordx4 v27,s[44:47],s65 offen lds
s_mov_b64 exec,-1
s_setprio 0
v_mul_f32 v176,v144,v176
v_mul_f32 v177,v145,v177
v_mul_f32 v178,v146,v178
v_mul_f32 v179,v147,v179
v_mul_f32 v180,v148,v180
v_mul_f32 v181,v149,v181
v_mul_f32 v182,v150,v182
v_mul_f32 v183,v151,v183
v_mul_f32 v184,v152,v184
v_mul_f32 v185,v153,v185
v_mul_f32 v186,v154,v186
v_mul_f32 v187,v155,v187
v_mul_f32 v188,v156,v188
v_mul_f32 v189,v157,v189
v_mul_f32 v190,v158,v190
v_mul_f32 v191,v159,v191
v_cvt_pk_bf16_f32 v192,v176,v177
v_cvt_pk_bf16_f32 v193,v178,v179
v_cvt_pk_bf16_f32 v194,v180,v181
v_cvt_pk_bf16_f32 v195,v182,v183
v_cvt_pk_bf16_f32 v196,v184,v185
v_cvt_pk_bf16_f32 v197,v186,v187
v_cvt_pk_bf16_f32 v198,v188,v189
v_cvt_pk_bf16_f32 v199,v190,v191
s_waitcnt lgkmcnt(0)
s_waitcnt vmcnt(2)
s_barrier
v_fmaak_f32 v176,v160,v160,0x409d30fc
v_fmaak_f32 v177,v161,v161,0x409d30fc
v_fmaak_f32 v178,v162,v162,0x409d30fc
v_fmaak_f32 v179,v163,v163,0x409d30fc
v_fmaak_f32 v180,v164,v164,0x409d30fc
v_fmaak_f32 v181,v165,v165,0x409d30fc
v_fmaak_f32 v182,v166,v166,0x409d30fc
v_fmaak_f32 v183,v167,v167,0x409d30fc
v_fmaak_f32 v184,v168,v168,0x409d30fc
v_fmaak_f32 v185,v169,v169,0x409d30fc
v_fmaak_f32 v186,v170,v170,0x409d30fc
v_fmaak_f32 v187,v171,v171,0x409d30fc
v_fmaak_f32 v188,v172,v172,0x409d30fc
v_fmaak_f32 v189,v173,v173,0x409d30fc
v_fmaak_f32 v190,v174,v174,0x409d30fc
v_fmaak_f32 v191,v175,v175,0x409d30fc
v_mul_f32 v176,v160,v176
v_mul_f32 v177,v161,v177
v_mul_f32 v178,v162,v178
v_mul_f32 v179,v163,v179
v_mul_f32 v180,v164,v180
v_mul_f32 v181,v165,v181
v_mul_f32 v182,v166,v182
v_mul_f32 v183,v167,v183
v_mul_f32 v184,v168,v184
v_mul_f32 v185,v169,v185
v_mul_f32 v186,v170,v186
v_mul_f32 v187,v171,v187
v_mul_f32 v188,v172,v188
v_mul_f32 v189,v173,v189
v_mul_f32 v190,v174,v190
v_mul_f32 v191,v175,v191
s_setprio 1
v_mfma_f32_32x32x16_bf16 v[96:111],v[200:203],v[192:195],v[96:111]
v_exp_f32 v176,v176
v_exp_f32 v177,v177
v_exp_f32 v178,v178
v_mfma_f32_32x32x16_bf16 v[96:111],v[204:207],v[196:199],v[96:111]
v_exp_f32 v179,v179
v_exp_f32 v180,v180
v_exp_f32 v181,v181
v_mfma_f32_32x32x16_bf16 v[112:127],v[208:211],v[192:195],v[112:127]
v_exp_f32 v182,v182
v_exp_f32 v183,v183
v_exp_f32 v184,v184
v_mfma_f32_32x32x16_bf16 v[112:127],v[212:215],v[196:199],v[112:127]
v_exp_f32 v185,v185
v_exp_f32 v186,v186
v_exp_f32 v187,v187
v_mfma_f32_32x32x16_bf16 v[128:143],v[216:219],v[192:195],v[128:143]
v_exp_f32 v188,v188
v_exp_f32 v189,v189
v_mfma_f32_32x32x16_bf16 v[128:143],v[220:223],v[196:199],v[128:143]
v_exp_f32 v190,v190
v_exp_f32 v191,v191
ds_read_b64_tr_b16 v[200:201],v16 offset:12288
ds_read_b64_tr_b16 v[202:203],v16 offset:12800
ds_read_b64_tr_b16 v[204:205],v16 offset:13312
ds_read_b64_tr_b16 v[206:207],v16 offset:13824
ds_read_b64_tr_b16 v[208:209],v16 offset:14336
ds_read_b64_tr_b16 v[210:211],v16 offset:14848
ds_read_b64_tr_b16 v[212:213],v16 offset:15360
ds_read_b64_tr_b16 v[214:215],v16 offset:15872
ds_read_b64_tr_b16 v[216:217],v16 offset:16384
ds_read_b64_tr_b16 v[218:219],v16 offset:16896
ds_read_b64_tr_b16 v[220:221],v16 offset:17408
ds_read_b64_tr_b16 v[222:223],v16 offset:17920
ds_read_b128 v[144:147],v18 offset:256
ds_read_b128 v[148:151],v18 offset:288
ds_read_b128 v[152:155],v18 offset:320
ds_read_b128 v[156:159],v18 offset:352
s_mov_b64 exec,s[60:61]
s_add_u32 m0,s58,155136
s_mov_b32 s64,12288
buffer_load_dwordx4 v28,s[48:51],s64 offen lds
s_add_u32 m0,s58,155904
s_mov_b32 s65,13056
buffer_load_dwordx4 v28,s[48:51],s65 offen lds
s_mov_b64 exec,-1
s_setprio 0
v_add_f32 v176,1.0,v176
v_add_f32 v177,1.0,v177
v_add_f32 v178,1.0,v178
v_add_f32 v179,1.0,v179
v_add_f32 v180,1.0,v180
v_add_f32 v181,1.0,v181
v_add_f32 v182,1.0,v182
v_add_f32 v183,1.0,v183
v_add_f32 v184,1.0,v184
v_add_f32 v185,1.0,v185
v_add_f32 v186,1.0,v186
v_add_f32 v187,1.0,v187
v_add_f32 v188,1.0,v188
v_add_f32 v189,1.0,v189
v_add_f32 v190,1.0,v190
v_add_f32 v191,1.0,v191
s_setprio 1
s_waitcnt lgkmcnt(0)
v_mfma_f32_32x32x16_bf16 v[144:159],v[200:203],v[72:75],v[144:159]
v_rcp_f32 v176,v176
v_rcp_f32 v177,v177
v_rcp_f32 v178,v178
v_mfma_f32_32x32x16_bf16 v[144:159],v[204:207],v[76:79],v[144:159]
v_rcp_f32 v179,v179
v_rcp_f32 v180,v180
v_rcp_f32 v181,v181
v_mfma_f32_32x32x16_bf16 v[144:159],v[208:211],v[80:83],v[144:159]
v_rcp_f32 v182,v182
v_rcp_f32 v183,v183
v_rcp_f32 v184,v184
v_mfma_f32_32x32x16_bf16 v[144:159],v[212:215],v[84:87],v[144:159]
v_rcp_f32 v185,v185
v_rcp_f32 v186,v186
v_rcp_f32 v187,v187
v_mfma_f32_32x32x16_bf16 v[144:159],v[216:219],v[88:91],v[144:159]
v_rcp_f32 v188,v188
v_rcp_f32 v189,v189
v_mfma_f32_32x32x16_bf16 v[144:159],v[220:223],v[92:95],v[144:159]
v_rcp_f32 v190,v190
v_rcp_f32 v191,v191
ds_read_b64_tr_b16 v[200:201],v17 offset:6144
ds_read_b64_tr_b16 v[202:203],v17 offset:7680
ds_read_b64_tr_b16 v[204:205],v17 offset:9216
ds_read_b64_tr_b16 v[206:207],v17 offset:10752
ds_read_b64_tr_b16 v[208:209],v17 offset:6208
ds_read_b64_tr_b16 v[210:211],v17 offset:7744
ds_read_b64_tr_b16 v[212:213],v17 offset:9280
ds_read_b64_tr_b16 v[214:215],v17 offset:10816
ds_read_b64_tr_b16 v[216:217],v17 offset:6272
ds_read_b64_tr_b16 v[218:219],v17 offset:7808
ds_read_b64_tr_b16 v[220:221],v17 offset:9344
ds_read_b64_tr_b16 v[222:223],v17 offset:10880
s_mov_b64 exec,s[60:61]
s_add_u32 m0,s58,130560
s_movk_i32 s64,256
buffer_load_dwordx4 v27,s[44:47],s64 offen lds
s_add_u32 m0,s58,131328
s_movk_i32 s65,9472
buffer_load_dwordx4 v27,s[44:47],s65 offen lds
s_mov_b64 exec,-1
s_setprio 0
v_mul_f32 v176,v160,v176
v_mul_f32 v177,v161,v177
v_mul_f32 v178,v162,v178
v_mul_f32 v179,v163,v179
v_mul_f32 v180,v164,v180
v_mul_f32 v181,v165,v181
v_mul_f32 v182,v166,v182
v_mul_f32 v183,v167,v183
v_mul_f32 v184,v168,v184
v_mul_f32 v185,v169,v185
v_mul_f32 v186,v170,v186
v_mul_f32 v187,v171,v187
v_mul_f32 v188,v172,v188
v_mul_f32 v189,v173,v189
v_mul_f32 v190,v174,v190
v_mul_f32 v191,v175,v191
v_cvt_pk_bf16_f32 v192,v176,v177
v_cvt_pk_bf16_f32 v193,v178,v179
v_cvt_pk_bf16_f32 v194,v180,v181
v_cvt_pk_bf16_f32 v195,v182,v183
v_cvt_pk_bf16_f32 v196,v184,v185
v_cvt_pk_bf16_f32 v197,v186,v187
v_cvt_pk_bf16_f32 v198,v188,v189
v_cvt_pk_bf16_f32 v199,v190,v191
s_waitcnt lgkmcnt(0)
s_waitcnt vmcnt(2)
s_barrier
v_fmaak_f32 v176,v144,v144,0x409d30fc
v_fmaak_f32 v177,v145,v145,0x409d30fc
v_fmaak_f32 v178,v146,v146,0x409d30fc
v_fmaak_f32 v179,v147,v147,0x409d30fc
v_fmaak_f32 v180,v148,v148,0x409d30fc
v_fmaak_f32 v181,v149,v149,0x409d30fc
v_fmaak_f32 v182,v150,v150,0x409d30fc
v_fmaak_f32 v183,v151,v151,0x409d30fc
v_fmaak_f32 v184,v152,v152,0x409d30fc
v_fmaak_f32 v185,v153,v153,0x409d30fc
v_fmaak_f32 v186,v154,v154,0x409d30fc
v_fmaak_f32 v187,v155,v155,0x409d30fc
v_fmaak_f32 v188,v156,v156,0x409d30fc
v_fmaak_f32 v189,v157,v157,0x409d30fc
v_fmaak_f32 v190,v158,v158,0x409d30fc
v_fmaak_f32 v191,v159,v159,0x409d30fc
v_mul_f32 v176,v144,v176
v_mul_f32 v177,v145,v177
v_mul_f32 v178,v146,v178
v_mul_f32 v179,v147,v179
v_mul_f32 v180,v148,v180
v_mul_f32 v181,v149,v181
v_mul_f32 v182,v150,v182
v_mul_f32 v183,v151,v183
v_mul_f32 v184,v152,v184
v_mul_f32 v185,v153,v185
v_mul_f32 v186,v154,v186
v_mul_f32 v187,v155,v187
v_mul_f32 v188,v156,v188
v_mul_f32 v189,v157,v189
v_mul_f32 v190,v158,v190
v_mul_f32 v191,v159,v191
s_setprio 1
v_mfma_f32_32x32x16_bf16 v[96:111],v[200:203],v[192:195],v[96:111]
v_exp_f32 v176,v176
v_exp_f32 v177,v177
v_exp_f32 v178,v178
v_mfma_f32_32x32x16_bf16 v[96:111],v[204:207],v[196:199],v[96:111]
v_exp_f32 v179,v179
v_exp_f32 v180,v180
v_exp_f32 v181,v181
v_mfma_f32_32x32x16_bf16 v[112:127],v[208:211],v[192:195],v[112:127]
v_exp_f32 v182,v182
v_exp_f32 v183,v183
v_exp_f32 v184,v184
v_mfma_f32_32x32x16_bf16 v[112:127],v[212:215],v[196:199],v[112:127]
v_exp_f32 v185,v185
v_exp_f32 v186,v186
v_exp_f32 v187,v187
v_mfma_f32_32x32x16_bf16 v[128:143],v[216:219],v[192:195],v[128:143]
v_exp_f32 v188,v188
v_exp_f32 v189,v189
v_mfma_f32_32x32x16_bf16 v[128:143],v[220:223],v[196:199],v[128:143]
v_exp_f32 v190,v190
v_exp_f32 v191,v191
ds_read_b64_tr_b16 v[200:201],v16 offset:0
ds_read_b64_tr_b16 v[202:203],v16 offset:512
ds_read_b64_tr_b16 v[204:205],v16 offset:1024
ds_read_b64_tr_b16 v[206:207],v16 offset:1536
ds_read_b64_tr_b16 v[208:209],v16 offset:2048
ds_read_b64_tr_b16 v[210:211],v16 offset:2560
ds_read_b64_tr_b16 v[212:213],v16 offset:3072
ds_read_b64_tr_b16 v[214:215],v16 offset:3584
ds_read_b64_tr_b16 v[216:217],v16 offset:4096
ds_read_b64_tr_b16 v[218:219],v16 offset:4608
ds_read_b64_tr_b16 v[220:221],v16 offset:5120
ds_read_b64_tr_b16 v[222:223],v16 offset:5632
ds_read_b128 v[160:163],v18 offset:384
ds_read_b128 v[164:167],v18 offset:416
ds_read_b128 v[168:171],v18 offset:448
ds_read_b128 v[172:175],v18 offset:480
s_mov_b64 exec,s[60:61]
s_add_u32 m0,s58,142848
s_mov_b32 s64,18432
buffer_load_dwordx4 v28,s[48:51],s64 offen lds
s_add_u32 m0,s58,143616
s_mov_b32 s65,19200
buffer_load_dwordx4 v28,s[48:51],s65 offen lds
s_mov_b64 exec,-1
s_setprio 0
v_add_f32 v176,1.0,v176
v_add_f32 v177,1.0,v177
v_add_f32 v178,1.0,v178
v_add_f32 v179,1.0,v179
v_add_f32 v180,1.0,v180
v_add_f32 v181,1.0,v181
v_add_f32 v182,1.0,v182
v_add_f32 v183,1.0,v183
v_add_f32 v184,1.0,v184
v_add_f32 v185,1.0,v185
v_add_f32 v186,1.0,v186
v_add_f32 v187,1.0,v187
v_add_f32 v188,1.0,v188
v_add_f32 v189,1.0,v189
v_add_f32 v190,1.0,v190
v_add_f32 v191,1.0,v191
s_setprio 1
s_waitcnt lgkmcnt(0)
v_mfma_f32_32x32x16_bf16 v[160:175],v[200:203],v[72:75],v[160:175]
v_rcp_f32 v176,v176
v_rcp_f32 v177,v177
v_rcp_f32 v178,v178
v_mfma_f32_32x32x16_bf16 v[160:175],v[204:207],v[76:79],v[160:175]
v_rcp_f32 v179,v179
v_rcp_f32 v180,v180
v_rcp_f32 v181,v181
v_mfma_f32_32x32x16_bf16 v[160:175],v[208:211],v[80:83],v[160:175]
v_rcp_f32 v182,v182
v_rcp_f32 v183,v183
v_rcp_f32 v184,v184
v_mfma_f32_32x32x16_bf16 v[160:175],v[212:215],v[84:87],v[160:175]
v_rcp_f32 v185,v185
v_rcp_f32 v186,v186
v_rcp_f32 v187,v187
v_mfma_f32_32x32x16_bf16 v[160:175],v[216:219],v[88:91],v[160:175]
v_rcp_f32 v188,v188
v_rcp_f32 v189,v189
v_mfma_f32_32x32x16_bf16 v[160:175],v[220:223],v[92:95],v[160:175]
v_rcp_f32 v190,v190
v_rcp_f32 v191,v191
ds_read_b64_tr_b16 v[200:201],v17 offset:12288
ds_read_b64_tr_b16 v[202:203],v17 offset:13824
ds_read_b64_tr_b16 v[204:205],v17 offset:15360
ds_read_b64_tr_b16 v[206:207],v17 offset:16896
ds_read_b64_tr_b16 v[208:209],v17 offset:12352
ds_read_b64_tr_b16 v[210:211],v17 offset:13888
ds_read_b64_tr_b16 v[212:213],v17 offset:15424
ds_read_b64_tr_b16 v[214:215],v17 offset:16960
ds_read_b64_tr_b16 v[216:217],v17 offset:12416
ds_read_b64_tr_b16 v[218:219],v17 offset:13952
ds_read_b64_tr_b16 v[220:221],v17 offset:15488
ds_read_b64_tr_b16 v[222:223],v17 offset:17024
s_mov_b64 exec,s[60:61]
s_add_u32 m0,s58,136704
s_movk_i32 s64,320
buffer_load_dwordx4 v27,s[44:47],s64 offen lds
s_add_u32 m0,s58,137472
s_movk_i32 s65,9536
buffer_load_dwordx4 v27,s[44:47],s65 offen lds
s_mov_b64 exec,-1
s_setprio 0
v_mul_f32 v176,v144,v176
v_mul_f32 v177,v145,v177
v_mul_f32 v178,v146,v178
v_mul_f32 v179,v147,v179
v_mul_f32 v180,v148,v180
v_mul_f32 v181,v149,v181
v_mul_f32 v182,v150,v182
v_mul_f32 v183,v151,v183
v_mul_f32 v184,v152,v184
v_mul_f32 v185,v153,v185
v_mul_f32 v186,v154,v186
v_mul_f32 v187,v155,v187
v_mul_f32 v188,v156,v188
v_mul_f32 v189,v157,v189
v_mul_f32 v190,v158,v190
v_mul_f32 v191,v159,v191
v_cvt_pk_bf16_f32 v192,v176,v177
v_cvt_pk_bf16_f32 v193,v178,v179
v_cvt_pk_bf16_f32 v194,v180,v181
v_cvt_pk_bf16_f32 v195,v182,v183
v_cvt_pk_bf16_f32 v196,v184,v185
v_cvt_pk_bf16_f32 v197,v186,v187
v_cvt_pk_bf16_f32 v198,v188,v189
v_cvt_pk_bf16_f32 v199,v190,v191
s_waitcnt lgkmcnt(0)
s_waitcnt vmcnt(2)
s_barrier
v_fmaak_f32 v176,v160,v160,0x409d30fc
v_fmaak_f32 v177,v161,v161,0x409d30fc
v_fmaak_f32 v178,v162,v162,0x409d30fc
v_fmaak_f32 v179,v163,v163,0x409d30fc
v_fmaak_f32 v180,v164,v164,0x409d30fc
v_fmaak_f32 v181,v165,v165,0x409d30fc
v_fmaak_f32 v182,v166,v166,0x409d30fc
v_fmaak_f32 v183,v167,v167,0x409d30fc
v_fmaak_f32 v184,v168,v168,0x409d30fc
v_fmaak_f32 v185,v169,v169,0x409d30fc
v_fmaak_f32 v186,v170,v170,0x409d30fc
v_fmaak_f32 v187,v171,v171,0x409d30fc
v_fmaak_f32 v188,v172,v172,0x409d30fc
v_fmaak_f32 v189,v173,v173,0x409d30fc
v_fmaak_f32 v190,v174,v174,0x409d30fc
v_fmaak_f32 v191,v175,v175,0x409d30fc
v_mul_f32 v176,v160,v176
v_mul_f32 v177,v161,v177
v_mul_f32 v178,v162,v178
v_mul_f32 v179,v163,v179
v_mul_f32 v180,v164,v180
v_mul_f32 v181,v165,v181
v_mul_f32 v182,v166,v182
v_mul_f32 v183,v167,v183
v_mul_f32 v184,v168,v184
v_mul_f32 v185,v169,v185
v_mul_f32 v186,v170,v186
v_mul_f32 v187,v171,v187
v_mul_f32 v188,v172,v188
v_mul_f32 v189,v173,v189
v_mul_f32 v190,v174,v190
v_mul_f32 v191,v175,v191
s_setprio 1
v_mfma_f32_32x32x16_bf16 v[96:111],v[200:203],v[192:195],v[96:111]
v_exp_f32 v176,v176
v_exp_f32 v177,v177
v_exp_f32 v178,v178
v_mfma_f32_32x32x16_bf16 v[96:111],v[204:207],v[196:199],v[96:111]
v_exp_f32 v179,v179
v_exp_f32 v180,v180
v_exp_f32 v181,v181
v_mfma_f32_32x32x16_bf16 v[112:127],v[208:211],v[192:195],v[112:127]
v_exp_f32 v182,v182
v_exp_f32 v183,v183
v_exp_f32 v184,v184
v_mfma_f32_32x32x16_bf16 v[112:127],v[212:215],v[196:199],v[112:127]
v_exp_f32 v185,v185
v_exp_f32 v186,v186
v_exp_f32 v187,v187
v_mfma_f32_32x32x16_bf16 v[128:143],v[216:219],v[192:195],v[128:143]
v_exp_f32 v188,v188
v_exp_f32 v189,v189
v_mfma_f32_32x32x16_bf16 v[128:143],v[220:223],v[196:199],v[128:143]
v_exp_f32 v190,v190
v_exp_f32 v191,v191
ds_read_b64_tr_b16 v[200:201],v16 offset:6144
ds_read_b64_tr_b16 v[202:203],v16 offset:6656
ds_read_b64_tr_b16 v[204:205],v16 offset:7168
ds_read_b64_tr_b16 v[206:207],v16 offset:7680
ds_read_b64_tr_b16 v[208:209],v16 offset:8192
ds_read_b64_tr_b16 v[210:211],v16 offset:8704
ds_read_b64_tr_b16 v[212:213],v16 offset:9216
ds_read_b64_tr_b16 v[214:215],v16 offset:9728
ds_read_b64_tr_b16 v[216:217],v16 offset:10240
ds_read_b64_tr_b16 v[218:219],v16 offset:10752
ds_read_b64_tr_b16 v[220:221],v16 offset:11264
ds_read_b64_tr_b16 v[222:223],v16 offset:11776
ds_read_b128 v[144:147],v18 offset:512
ds_read_b128 v[148:151],v18 offset:544
ds_read_b128 v[152:155],v18 offset:576
ds_read_b128 v[156:159],v18 offset:608
s_mov_b64 exec,s[60:61]
s_add_u32 m0,s58,148992
s_mov_b32 s64,24576
buffer_load_dwordx4 v28,s[48:51],s64 offen lds
s_add_u32 m0,s58,149760
s_mov_b32 s65,25344
buffer_load_dwordx4 v28,s[48:51],s65 offen lds
s_mov_b64 exec,-1
s_setprio 0
v_add_f32 v176,1.0,v176
v_add_f32 v177,1.0,v177
v_add_f32 v178,1.0,v178
v_add_f32 v179,1.0,v179
v_add_f32 v180,1.0,v180
v_add_f32 v181,1.0,v181
v_add_f32 v182,1.0,v182
v_add_f32 v183,1.0,v183
v_add_f32 v184,1.0,v184
v_add_f32 v185,1.0,v185
v_add_f32 v186,1.0,v186
v_add_f32 v187,1.0,v187
v_add_f32 v188,1.0,v188
v_add_f32 v189,1.0,v189
v_add_f32 v190,1.0,v190
v_add_f32 v191,1.0,v191
s_setprio 1
s_waitcnt lgkmcnt(0)
v_mfma_f32_32x32x16_bf16 v[144:159],v[200:203],v[72:75],v[144:159]
v_rcp_f32 v176,v176
v_rcp_f32 v177,v177
v_rcp_f32 v178,v178
v_mfma_f32_32x32x16_bf16 v[144:159],v[204:207],v[76:79],v[144:159]
v_rcp_f32 v179,v179
v_rcp_f32 v180,v180
v_rcp_f32 v181,v181
v_mfma_f32_32x32x16_bf16 v[144:159],v[208:211],v[80:83],v[144:159]
v_rcp_f32 v182,v182
v_rcp_f32 v183,v183
v_rcp_f32 v184,v184
v_mfma_f32_32x32x16_bf16 v[144:159],v[212:215],v[84:87],v[144:159]
v_rcp_f32 v185,v185
v_rcp_f32 v186,v186
v_rcp_f32 v187,v187
v_mfma_f32_32x32x16_bf16 v[144:159],v[216:219],v[88:91],v[144:159]
v_rcp_f32 v188,v188
v_rcp_f32 v189,v189
v_mfma_f32_32x32x16_bf16 v[144:159],v[220:223],v[92:95],v[144:159]
v_rcp_f32 v190,v190
v_rcp_f32 v191,v191
ds_read_b64_tr_b16 v[200:201],v17 offset:0
ds_read_b64_tr_b16 v[202:203],v17 offset:1536
ds_read_b64_tr_b16 v[204:205],v17 offset:3072
ds_read_b64_tr_b16 v[206:207],v17 offset:4608
ds_read_b64_tr_b16 v[208:209],v17 offset:64
ds_read_b64_tr_b16 v[210:211],v17 offset:1600
ds_read_b64_tr_b16 v[212:213],v17 offset:3136
ds_read_b64_tr_b16 v[214:215],v17 offset:4672
ds_read_b64_tr_b16 v[216:217],v17 offset:128
ds_read_b64_tr_b16 v[218:219],v17 offset:1664
ds_read_b64_tr_b16 v[220:221],v17 offset:3200
ds_read_b64_tr_b16 v[222:223],v17 offset:4736
s_mov_b64 exec,s[60:61]
s_add_u32 m0,s58,124416
s_movk_i32 s64,384
buffer_load_dwordx4 v27,s[44:47],s64 offen lds
s_add_u32 m0,s58,125184
s_movk_i32 s65,9600
buffer_load_dwordx4 v27,s[44:47],s65 offen lds
s_mov_b64 exec,-1
s_setprio 0
v_mul_f32 v176,v160,v176
v_mul_f32 v177,v161,v177
v_mul_f32 v178,v162,v178
v_mul_f32 v179,v163,v179
v_mul_f32 v180,v164,v180
v_mul_f32 v181,v165,v181
v_mul_f32 v182,v166,v182
v_mul_f32 v183,v167,v183
v_mul_f32 v184,v168,v184
v_mul_f32 v185,v169,v185
v_mul_f32 v186,v170,v186
v_mul_f32 v187,v171,v187
v_mul_f32 v188,v172,v188
v_mul_f32 v189,v173,v189
v_mul_f32 v190,v174,v190
v_mul_f32 v191,v175,v191
v_cvt_pk_bf16_f32 v192,v176,v177
v_cvt_pk_bf16_f32 v193,v178,v179
v_cvt_pk_bf16_f32 v194,v180,v181
v_cvt_pk_bf16_f32 v195,v182,v183
v_cvt_pk_bf16_f32 v196,v184,v185
v_cvt_pk_bf16_f32 v197,v186,v187
v_cvt_pk_bf16_f32 v198,v188,v189
v_cvt_pk_bf16_f32 v199,v190,v191
s_waitcnt lgkmcnt(0)
s_waitcnt vmcnt(2)
s_barrier
v_fmaak_f32 v176,v144,v144,0x409d30fc
v_fmaak_f32 v177,v145,v145,0x409d30fc
v_fmaak_f32 v178,v146,v146,0x409d30fc
v_fmaak_f32 v179,v147,v147,0x409d30fc
v_fmaak_f32 v180,v148,v148,0x409d30fc
v_fmaak_f32 v181,v149,v149,0x409d30fc
v_fmaak_f32 v182,v150,v150,0x409d30fc
v_fmaak_f32 v183,v151,v151,0x409d30fc
v_fmaak_f32 v184,v152,v152,0x409d30fc
v_fmaak_f32 v185,v153,v153,0x409d30fc
v_fmaak_f32 v186,v154,v154,0x409d30fc
v_fmaak_f32 v187,v155,v155,0x409d30fc
v_fmaak_f32 v188,v156,v156,0x409d30fc
v_fmaak_f32 v189,v157,v157,0x409d30fc
v_fmaak_f32 v190,v158,v158,0x409d30fc
v_fmaak_f32 v191,v159,v159,0x409d30fc
v_mul_f32 v176,v144,v176
v_mul_f32 v177,v145,v177
v_mul_f32 v178,v146,v178
v_mul_f32 v179,v147,v179
v_mul_f32 v180,v148,v180
v_mul_f32 v181,v149,v181
v_mul_f32 v182,v150,v182
v_mul_f32 v183,v151,v183
v_mul_f32 v184,v152,v184
v_mul_f32 v185,v153,v185
v_mul_f32 v186,v154,v186
v_mul_f32 v187,v155,v187
v_mul_f32 v188,v156,v188
v_mul_f32 v189,v157,v189
v_mul_f32 v190,v158,v190
v_mul_f32 v191,v159,v191
s_setprio 1
v_mfma_f32_32x32x16_bf16 v[96:111],v[200:203],v[192:195],v[96:111]
v_exp_f32 v176,v176
v_exp_f32 v177,v177
v_exp_f32 v178,v178
v_mfma_f32_32x32x16_bf16 v[96:111],v[204:207],v[196:199],v[96:111]
v_exp_f32 v179,v179
v_exp_f32 v180,v180
v_exp_f32 v181,v181
v_mfma_f32_32x32x16_bf16 v[112:127],v[208:211],v[192:195],v[112:127]
v_exp_f32 v182,v182
v_exp_f32 v183,v183
v_exp_f32 v184,v184
v_mfma_f32_32x32x16_bf16 v[112:127],v[212:215],v[196:199],v[112:127]
v_exp_f32 v185,v185
v_exp_f32 v186,v186
v_exp_f32 v187,v187
v_mfma_f32_32x32x16_bf16 v[128:143],v[216:219],v[192:195],v[128:143]
v_exp_f32 v188,v188
v_exp_f32 v189,v189
v_mfma_f32_32x32x16_bf16 v[128:143],v[220:223],v[196:199],v[128:143]
v_exp_f32 v190,v190
v_exp_f32 v191,v191
ds_read_b64_tr_b16 v[200:201],v16 offset:12288
ds_read_b64_tr_b16 v[202:203],v16 offset:12800
ds_read_b64_tr_b16 v[204:205],v16 offset:13312
ds_read_b64_tr_b16 v[206:207],v16 offset:13824
ds_read_b64_tr_b16 v[208:209],v16 offset:14336
ds_read_b64_tr_b16 v[210:211],v16 offset:14848
ds_read_b64_tr_b16 v[212:213],v16 offset:15360
ds_read_b64_tr_b16 v[214:215],v16 offset:15872
ds_read_b64_tr_b16 v[216:217],v16 offset:16384
ds_read_b64_tr_b16 v[218:219],v16 offset:16896
ds_read_b64_tr_b16 v[220:221],v16 offset:17408
ds_read_b64_tr_b16 v[222:223],v16 offset:17920
ds_read_b128 v[160:163],v18 offset:640
ds_read_b128 v[164:167],v18 offset:672
ds_read_b128 v[168:171],v18 offset:704
ds_read_b128 v[172:175],v18 offset:736
s_mov_b64 exec,s[60:61]
s_add_u32 m0,s58,155136
s_mov_b32 s64,30720
buffer_load_dwordx4 v28,s[48:51],s64 offen lds
s_add_u32 m0,s58,155904
s_mov_b32 s65,31488
buffer_load_dwordx4 v28,s[48:51],s65 offen lds
s_mov_b64 exec,-1
s_setprio 0
v_add_f32 v176,1.0,v176
v_add_f32 v177,1.0,v177
v_add_f32 v178,1.0,v178
v_add_f32 v179,1.0,v179
v_add_f32 v180,1.0,v180
v_add_f32 v181,1.0,v181
v_add_f32 v182,1.0,v182
v_add_f32 v183,1.0,v183
v_add_f32 v184,1.0,v184
v_add_f32 v185,1.0,v185
v_add_f32 v186,1.0,v186
v_add_f32 v187,1.0,v187
v_add_f32 v188,1.0,v188
v_add_f32 v189,1.0,v189
v_add_f32 v190,1.0,v190
v_add_f32 v191,1.0,v191
s_setprio 1
s_waitcnt lgkmcnt(0)
v_mfma_f32_32x32x16_bf16 v[160:175],v[200:203],v[72:75],v[160:175]
v_rcp_f32 v176,v176
v_rcp_f32 v177,v177
v_rcp_f32 v178,v178
v_mfma_f32_32x32x16_bf16 v[160:175],v[204:207],v[76:79],v[160:175]
v_rcp_f32 v179,v179
v_rcp_f32 v180,v180
v_rcp_f32 v181,v181
v_mfma_f32_32x32x16_bf16 v[160:175],v[208:211],v[80:83],v[160:175]
v_rcp_f32 v182,v182
v_rcp_f32 v183,v183
v_rcp_f32 v184,v184
v_mfma_f32_32x32x16_bf16 v[160:175],v[212:215],v[84:87],v[160:175]
v_rcp_f32 v185,v185
v_rcp_f32 v186,v186
v_rcp_f32 v187,v187
v_mfma_f32_32x32x16_bf16 v[160:175],v[216:219],v[88:91],v[160:175]
v_rcp_f32 v188,v188
v_rcp_f32 v189,v189
v_mfma_f32_32x32x16_bf16 v[160:175],v[220:223],v[92:95],v[160:175]
v_rcp_f32 v190,v190
v_rcp_f32 v191,v191
ds_read_b64_tr_b16 v[200:201],v17 offset:6144
ds_read_b64_tr_b16 v[202:203],v17 offset:7680
ds_read_b64_tr_b16 v[204:205],v17 offset:9216
ds_read_b64_tr_b16 v[206:207],v17 offset:10752
ds_read_b64_tr_b16 v[208:209],v17 offset:6208
ds_read_b64_tr_b16 v[210:211],v17 offset:7744
ds_read_b64_tr_b16 v[212:213],v17 offset:9280
ds_read_b64_tr_b16 v[214:215],v17 offset:10816
ds_read_b64_tr_b16 v[216:217],v17 offset:6272
ds_read_b64_tr_b16 v[218:219],v17 offset:7808
ds_read_b64_tr_b16 v[220:221],v17 offset:9344
ds_read_b64_tr_b16 v[222:223],v17 offset:10880
s_mov_b64 exec,s[60:61]
s_add_u32 m0,s58,130560
s_movk_i32 s64,448
buffer_load_dwordx4 v27,s[44:47],s64 offen lds
s_add_u32 m0,s58,131328
s_movk_i32 s65,9664
buffer_load_dwordx4 v27,s[44:47],s65 offen lds
s_mov_b64 exec,-1
s_setprio 0
v_mul_f32 v176,v144,v176
v_mul_f32 v177,v145,v177
v_mul_f32 v178,v146,v178
v_mul_f32 v179,v147,v179
v_mul_f32 v180,v148,v180
v_mul_f32 v181,v149,v181
v_mul_f32 v182,v150,v182
v_mul_f32 v183,v151,v183
v_mul_f32 v184,v152,v184
v_mul_f32 v185,v153,v185
v_mul_f32 v186,v154,v186
v_mul_f32 v187,v155,v187
v_mul_f32 v188,v156,v188
v_mul_f32 v189,v157,v189
v_mul_f32 v190,v158,v190
v_mul_f32 v191,v159,v191
v_cvt_pk_bf16_f32 v192,v176,v177
v_cvt_pk_bf16_f32 v193,v178,v179
v_cvt_pk_bf16_f32 v194,v180,v181
v_cvt_pk_bf16_f32 v195,v182,v183
v_cvt_pk_bf16_f32 v196,v184,v185
v_cvt_pk_bf16_f32 v197,v186,v187
v_cvt_pk_bf16_f32 v198,v188,v189
v_cvt_pk_bf16_f32 v199,v190,v191
s_waitcnt lgkmcnt(0)
s_waitcnt vmcnt(2)
s_barrier
v_fmaak_f32 v176,v160,v160,0x409d30fc
v_fmaak_f32 v177,v161,v161,0x409d30fc
v_fmaak_f32 v178,v162,v162,0x409d30fc
v_fmaak_f32 v179,v163,v163,0x409d30fc
v_fmaak_f32 v180,v164,v164,0x409d30fc
v_fmaak_f32 v181,v165,v165,0x409d30fc
v_fmaak_f32 v182,v166,v166,0x409d30fc
v_fmaak_f32 v183,v167,v167,0x409d30fc
v_fmaak_f32 v184,v168,v168,0x409d30fc
v_fmaak_f32 v185,v169,v169,0x409d30fc
v_fmaak_f32 v186,v170,v170,0x409d30fc
v_fmaak_f32 v187,v171,v171,0x409d30fc
v_fmaak_f32 v188,v172,v172,0x409d30fc
v_fmaak_f32 v189,v173,v173,0x409d30fc
v_fmaak_f32 v190,v174,v174,0x409d30fc
v_fmaak_f32 v191,v175,v175,0x409d30fc
v_mul_f32 v176,v160,v176
v_mul_f32 v177,v161,v177
v_mul_f32 v178,v162,v178
v_mul_f32 v179,v163,v179
v_mul_f32 v180,v164,v180
v_mul_f32 v181,v165,v181
v_mul_f32 v182,v166,v182
v_mul_f32 v183,v167,v183
v_mul_f32 v184,v168,v184
v_mul_f32 v185,v169,v185
v_mul_f32 v186,v170,v186
v_mul_f32 v187,v171,v187
v_mul_f32 v188,v172,v188
v_mul_f32 v189,v173,v189
v_mul_f32 v190,v174,v190
v_mul_f32 v191,v175,v191
s_setprio 1
v_mfma_f32_32x32x16_bf16 v[96:111],v[200:203],v[192:195],v[96:111]
v_exp_f32 v176,v176
v_exp_f32 v177,v177
v_exp_f32 v178,v178
v_mfma_f32_32x32x16_bf16 v[96:111],v[204:207],v[196:199],v[96:111]
v_exp_f32 v179,v179
v_exp_f32 v180,v180
v_exp_f32 v181,v181
v_mfma_f32_32x32x16_bf16 v[112:127],v[208:211],v[192:195],v[112:127]
v_exp_f32 v182,v182
v_exp_f32 v183,v183
v_exp_f32 v184,v184
v_mfma_f32_32x32x16_bf16 v[112:127],v[212:215],v[196:199],v[112:127]
v_exp_f32 v185,v185
v_exp_f32 v186,v186
v_exp_f32 v187,v187
v_mfma_f32_32x32x16_bf16 v[128:143],v[216:219],v[192:195],v[128:143]
v_exp_f32 v188,v188
v_exp_f32 v189,v189
v_mfma_f32_32x32x16_bf16 v[128:143],v[220:223],v[196:199],v[128:143]
v_exp_f32 v190,v190
v_exp_f32 v191,v191
ds_read_b64_tr_b16 v[200:201],v16 offset:0
ds_read_b64_tr_b16 v[202:203],v16 offset:512
ds_read_b64_tr_b16 v[204:205],v16 offset:1024
ds_read_b64_tr_b16 v[206:207],v16 offset:1536
ds_read_b64_tr_b16 v[208:209],v16 offset:2048
ds_read_b64_tr_b16 v[210:211],v16 offset:2560
ds_read_b64_tr_b16 v[212:213],v16 offset:3072
ds_read_b64_tr_b16 v[214:215],v16 offset:3584
ds_read_b64_tr_b16 v[216:217],v16 offset:4096
ds_read_b64_tr_b16 v[218:219],v16 offset:4608
ds_read_b64_tr_b16 v[220:221],v16 offset:5120
ds_read_b64_tr_b16 v[222:223],v16 offset:5632
ds_read_b128 v[144:147],v18 offset:768
ds_read_b128 v[148:151],v18 offset:800
ds_read_b128 v[152:155],v18 offset:832
ds_read_b128 v[156:159],v18 offset:864
s_mov_b64 exec,s[60:61]
s_add_u32 m0,s58,142848
s_mov_b32 s64,36864
buffer_load_dwordx4 v28,s[48:51],s64 offen lds
s_add_u32 m0,s58,143616
s_mov_b32 s65,37632
buffer_load_dwordx4 v28,s[48:51],s65 offen lds
s_mov_b64 exec,-1
s_setprio 0
v_add_f32 v176,1.0,v176
v_add_f32 v177,1.0,v177
v_add_f32 v178,1.0,v178
v_add_f32 v179,1.0,v179
v_add_f32 v180,1.0,v180
v_add_f32 v181,1.0,v181
v_add_f32 v182,1.0,v182
v_add_f32 v183,1.0,v183
v_add_f32 v184,1.0,v184
v_add_f32 v185,1.0,v185
v_add_f32 v186,1.0,v186
v_add_f32 v187,1.0,v187
v_add_f32 v188,1.0,v188
v_add_f32 v189,1.0,v189
v_add_f32 v190,1.0,v190
v_add_f32 v191,1.0,v191
s_setprio 1
s_waitcnt lgkmcnt(0)
v_mfma_f32_32x32x16_bf16 v[144:159],v[200:203],v[72:75],v[144:159]
v_rcp_f32 v176,v176
v_rcp_f32 v177,v177
v_rcp_f32 v178,v178
v_mfma_f32_32x32x16_bf16 v[144:159],v[204:207],v[76:79],v[144:159]
v_rcp_f32 v179,v179
v_rcp_f32 v180,v180
v_rcp_f32 v181,v181
v_mfma_f32_32x32x16_bf16 v[144:159],v[208:211],v[80:83],v[144:159]
v_rcp_f32 v182,v182
v_rcp_f32 v183,v183
v_rcp_f32 v184,v184
v_mfma_f32_32x32x16_bf16 v[144:159],v[212:215],v[84:87],v[144:159]
v_rcp_f32 v185,v185
v_rcp_f32 v186,v186
v_rcp_f32 v187,v187
v_mfma_f32_32x32x16_bf16 v[144:159],v[216:219],v[88:91],v[144:159]
v_rcp_f32 v188,v188
v_rcp_f32 v189,v189
v_mfma_f32_32x32x16_bf16 v[144:159],v[220:223],v[92:95],v[144:159]
v_rcp_f32 v190,v190
v_rcp_f32 v191,v191
ds_read_b64_tr_b16 v[200:201],v17 offset:12288
ds_read_b64_tr_b16 v[202:203],v17 offset:13824
ds_read_b64_tr_b16 v[204:205],v17 offset:15360
ds_read_b64_tr_b16 v[206:207],v17 offset:16896
ds_read_b64_tr_b16 v[208:209],v17 offset:12352
ds_read_b64_tr_b16 v[210:211],v17 offset:13888
ds_read_b64_tr_b16 v[212:213],v17 offset:15424
ds_read_b64_tr_b16 v[214:215],v17 offset:16960
ds_read_b64_tr_b16 v[216:217],v17 offset:12416
ds_read_b64_tr_b16 v[218:219],v17 offset:13952
ds_read_b64_tr_b16 v[220:221],v17 offset:15488
ds_read_b64_tr_b16 v[222:223],v17 offset:17024
s_mov_b64 exec,s[60:61]
s_add_u32 m0,s58,136704
s_movk_i32 s64,512
buffer_load_dwordx4 v27,s[44:47],s64 offen lds
s_add_u32 m0,s58,137472
s_movk_i32 s65,9728
buffer_load_dwordx4 v27,s[44:47],s65 offen lds
s_mov_b64 exec,-1
s_setprio 0
v_mul_f32 v176,v160,v176
v_mul_f32 v177,v161,v177
v_mul_f32 v178,v162,v178
v_mul_f32 v179,v163,v179
v_mul_f32 v180,v164,v180
v_mul_f32 v181,v165,v181
v_mul_f32 v182,v166,v182
v_mul_f32 v183,v167,v183
v_mul_f32 v184,v168,v184
v_mul_f32 v185,v169,v185
v_mul_f32 v186,v170,v186
v_mul_f32 v187,v171,v187
v_mul_f32 v188,v172,v188
v_mul_f32 v189,v173,v189
v_mul_f32 v190,v174,v190
v_mul_f32 v191,v175,v191
v_cvt_pk_bf16_f32 v192,v176,v177
v_cvt_pk_bf16_f32 v193,v178,v179
v_cvt_pk_bf16_f32 v194,v180,v181
v_cvt_pk_bf16_f32 v195,v182,v183
v_cvt_pk_bf16_f32 v196,v184,v185
v_cvt_pk_bf16_f32 v197,v186,v187
v_cvt_pk_bf16_f32 v198,v188,v189
v_cvt_pk_bf16_f32 v199,v190,v191
s_waitcnt lgkmcnt(0)
s_waitcnt vmcnt(2)
s_barrier
v_fmaak_f32 v176,v144,v144,0x409d30fc
v_fmaak_f32 v177,v145,v145,0x409d30fc
v_fmaak_f32 v178,v146,v146,0x409d30fc
v_fmaak_f32 v179,v147,v147,0x409d30fc
v_fmaak_f32 v180,v148,v148,0x409d30fc
v_fmaak_f32 v181,v149,v149,0x409d30fc
v_fmaak_f32 v182,v150,v150,0x409d30fc
v_fmaak_f32 v183,v151,v151,0x409d30fc
v_fmaak_f32 v184,v152,v152,0x409d30fc
v_fmaak_f32 v185,v153,v153,0x409d30fc
v_fmaak_f32 v186,v154,v154,0x409d30fc
v_fmaak_f32 v187,v155,v155,0x409d30fc
v_fmaak_f32 v188,v156,v156,0x409d30fc
v_fmaak_f32 v189,v157,v157,0x409d30fc
v_fmaak_f32 v190,v158,v158,0x409d30fc
v_fmaak_f32 v191,v159,v159,0x409d30fc
v_mul_f32 v176,v144,v176
v_mul_f32 v177,v145,v177
v_mul_f32 v178,v146,v178
v_mul_f32 v179,v147,v179
v_mul_f32 v180,v148,v180
v_mul_f32 v181,v149,v181
v_mul_f32 v182,v150,v182
v_mul_f32 v183,v151,v183
v_mul_f32 v184,v152,v184
v_mul_f32 v185,v153,v185
v_mul_f32 v186,v154,v186
v_mul_f32 v187,v155,v187
v_mul_f32 v188,v156,v188
v_mul_f32 v189,v157,v189
v_mul_f32 v190,v158,v190
v_mul_f32 v191,v159,v191
s_setprio 1
v_mfma_f32_32x32x16_bf16 v[96:111],v[200:203],v[192:195],v[96:111]
v_exp_f32 v176,v176
v_exp_f32 v177,v177
v_exp_f32 v178,v178
v_mfma_f32_32x32x16_bf16 v[96:111],v[204:207],v[196:199],v[96:111]
v_exp_f32 v179,v179
v_exp_f32 v180,v180
v_exp_f32 v181,v181
v_mfma_f32_32x32x16_bf16 v[112:127],v[208:211],v[192:195],v[112:127]
v_exp_f32 v182,v182
v_exp_f32 v183,v183
v_exp_f32 v184,v184
v_mfma_f32_32x32x16_bf16 v[112:127],v[212:215],v[196:199],v[112:127]
v_exp_f32 v185,v185
v_exp_f32 v186,v186
v_exp_f32 v187,v187
v_mfma_f32_32x32x16_bf16 v[128:143],v[216:219],v[192:195],v[128:143]
v_exp_f32 v188,v188
v_exp_f32 v189,v189
v_mfma_f32_32x32x16_bf16 v[128:143],v[220:223],v[196:199],v[128:143]
v_exp_f32 v190,v190
v_exp_f32 v191,v191
ds_read_b64_tr_b16 v[200:201],v16 offset:6144
ds_read_b64_tr_b16 v[202:203],v16 offset:6656
ds_read_b64_tr_b16 v[204:205],v16 offset:7168
ds_read_b64_tr_b16 v[206:207],v16 offset:7680
ds_read_b64_tr_b16 v[208:209],v16 offset:8192
ds_read_b64_tr_b16 v[210:211],v16 offset:8704
ds_read_b64_tr_b16 v[212:213],v16 offset:9216
ds_read_b64_tr_b16 v[214:215],v16 offset:9728
ds_read_b64_tr_b16 v[216:217],v16 offset:10240
ds_read_b64_tr_b16 v[218:219],v16 offset:10752
ds_read_b64_tr_b16 v[220:221],v16 offset:11264
ds_read_b64_tr_b16 v[222:223],v16 offset:11776
ds_read_b128 v[160:163],v18 offset:896
ds_read_b128 v[164:167],v18 offset:928
ds_read_b128 v[168:171],v18 offset:960
ds_read_b128 v[172:175],v18 offset:992
s_mov_b64 exec,s[60:61]
s_add_u32 m0,s58,148992
s_mov_b32 s64,43008
buffer_load_dwordx4 v28,s[48:51],s64 offen lds
s_add_u32 m0,s58,149760
s_mov_b32 s65,43776
buffer_load_dwordx4 v28,s[48:51],s65 offen lds
s_mov_b64 exec,-1
s_setprio 0
v_add_f32 v176,1.0,v176
v_add_f32 v177,1.0,v177
v_add_f32 v178,1.0,v178
v_add_f32 v179,1.0,v179
v_add_f32 v180,1.0,v180
v_add_f32 v181,1.0,v181
v_add_f32 v182,1.0,v182
v_add_f32 v183,1.0,v183
v_add_f32 v184,1.0,v184
v_add_f32 v185,1.0,v185
v_add_f32 v186,1.0,v186
v_add_f32 v187,1.0,v187
v_add_f32 v188,1.0,v188
v_add_f32 v189,1.0,v189
v_add_f32 v190,1.0,v190
v_add_f32 v191,1.0,v191
s_setprio 1
s_waitcnt lgkmcnt(0)
v_mfma_f32_32x32x16_bf16 v[160:175],v[200:203],v[72:75],v[160:175]
v_rcp_f32 v176,v176
v_rcp_f32 v177,v177
v_rcp_f32 v178,v178
v_mfma_f32_32x32x16_bf16 v[160:175],v[204:207],v[76:79],v[160:175]
v_rcp_f32 v179,v179
v_rcp_f32 v180,v180
v_rcp_f32 v181,v181
v_mfma_f32_32x32x16_bf16 v[160:175],v[208:211],v[80:83],v[160:175]
v_rcp_f32 v182,v182
v_rcp_f32 v183,v183
v_rcp_f32 v184,v184
v_mfma_f32_32x32x16_bf16 v[160:175],v[212:215],v[84:87],v[160:175]
v_rcp_f32 v185,v185
v_rcp_f32 v186,v186
v_rcp_f32 v187,v187
v_mfma_f32_32x32x16_bf16 v[160:175],v[216:219],v[88:91],v[160:175]
v_rcp_f32 v188,v188
v_rcp_f32 v189,v189
v_mfma_f32_32x32x16_bf16 v[160:175],v[220:223],v[92:95],v[160:175]
v_rcp_f32 v190,v190
v_rcp_f32 v191,v191
ds_read_b64_tr_b16 v[200:201],v17 offset:0
ds_read_b64_tr_b16 v[202:203],v17 offset:1536
ds_read_b64_tr_b16 v[204:205],v17 offset:3072
ds_read_b64_tr_b16 v[206:207],v17 offset:4608
ds_read_b64_tr_b16 v[208:209],v17 offset:64
ds_read_b64_tr_b16 v[210:211],v17 offset:1600
ds_read_b64_tr_b16 v[212:213],v17 offset:3136
ds_read_b64_tr_b16 v[214:215],v17 offset:4672
ds_read_b64_tr_b16 v[216:217],v17 offset:128
ds_read_b64_tr_b16 v[218:219],v17 offset:1664
ds_read_b64_tr_b16 v[220:221],v17 offset:3200
ds_read_b64_tr_b16 v[222:223],v17 offset:4736
s_mov_b64 exec,s[60:61]
s_add_u32 m0,s58,124416
s_movk_i32 s64,576
buffer_load_dwordx4 v27,s[44:47],s64 offen lds
s_add_u32 m0,s58,125184
s_movk_i32 s65,9792
buffer_load_dwordx4 v27,s[44:47],s65 offen lds
s_mov_b64 exec,-1
s_setprio 0
v_mul_f32 v176,v144,v176
v_mul_f32 v177,v145,v177
v_mul_f32 v178,v146,v178
v_mul_f32 v179,v147,v179
v_mul_f32 v180,v148,v180
v_mul_f32 v181,v149,v181
v_mul_f32 v182,v150,v182
v_mul_f32 v183,v151,v183
v_mul_f32 v184,v152,v184
v_mul_f32 v185,v153,v185
v_mul_f32 v186,v154,v186
v_mul_f32 v187,v155,v187
v_mul_f32 v188,v156,v188
v_mul_f32 v189,v157,v189
v_mul_f32 v190,v158,v190
v_mul_f32 v191,v159,v191
v_cvt_pk_bf16_f32 v192,v176,v177
v_cvt_pk_bf16_f32 v193,v178,v179
v_cvt_pk_bf16_f32 v194,v180,v181
v_cvt_pk_bf16_f32 v195,v182,v183
v_cvt_pk_bf16_f32 v196,v184,v185
v_cvt_pk_bf16_f32 v197,v186,v187
v_cvt_pk_bf16_f32 v198,v188,v189
v_cvt_pk_bf16_f32 v199,v190,v191
s_waitcnt lgkmcnt(0)
s_waitcnt vmcnt(2)
s_barrier
v_fmaak_f32 v176,v160,v160,0x409d30fc
v_fmaak_f32 v177,v161,v161,0x409d30fc
v_fmaak_f32 v178,v162,v162,0x409d30fc
v_fmaak_f32 v179,v163,v163,0x409d30fc
v_fmaak_f32 v180,v164,v164,0x409d30fc
v_fmaak_f32 v181,v165,v165,0x409d30fc
v_fmaak_f32 v182,v166,v166,0x409d30fc
v_fmaak_f32 v183,v167,v167,0x409d30fc
v_fmaak_f32 v184,v168,v168,0x409d30fc
v_fmaak_f32 v185,v169,v169,0x409d30fc
v_fmaak_f32 v186,v170,v170,0x409d30fc
v_fmaak_f32 v187,v171,v171,0x409d30fc
v_fmaak_f32 v188,v172,v172,0x409d30fc
v_fmaak_f32 v189,v173,v173,0x409d30fc
v_fmaak_f32 v190,v174,v174,0x409d30fc
v_fmaak_f32 v191,v175,v175,0x409d30fc
v_mul_f32 v176,v160,v176
v_mul_f32 v177,v161,v177
v_mul_f32 v178,v162,v178
v_mul_f32 v179,v163,v179
v_mul_f32 v180,v164,v180
v_mul_f32 v181,v165,v181
v_mul_f32 v182,v166,v182
v_mul_f32 v183,v167,v183
v_mul_f32 v184,v168,v184
v_mul_f32 v185,v169,v185
v_mul_f32 v186,v170,v186
v_mul_f32 v187,v171,v187
v_mul_f32 v188,v172,v188
v_mul_f32 v189,v173,v189
v_mul_f32 v190,v174,v190
v_mul_f32 v191,v175,v191
s_setprio 1
v_mfma_f32_32x32x16_bf16 v[96:111],v[200:203],v[192:195],v[96:111]
v_exp_f32 v176,v176
v_exp_f32 v177,v177
v_exp_f32 v178,v178
v_mfma_f32_32x32x16_bf16 v[96:111],v[204:207],v[196:199],v[96:111]
v_exp_f32 v179,v179
v_exp_f32 v180,v180
v_exp_f32 v181,v181
v_mfma_f32_32x32x16_bf16 v[112:127],v[208:211],v[192:195],v[112:127]
v_exp_f32 v182,v182
v_exp_f32 v183,v183
v_exp_f32 v184,v184
v_mfma_f32_32x32x16_bf16 v[112:127],v[212:215],v[196:199],v[112:127]
v_exp_f32 v185,v185
v_exp_f32 v186,v186
v_exp_f32 v187,v187
v_mfma_f32_32x32x16_bf16 v[128:143],v[216:219],v[192:195],v[128:143]
v_exp_f32 v188,v188
v_exp_f32 v189,v189
v_mfma_f32_32x32x16_bf16 v[128:143],v[220:223],v[196:199],v[128:143]
v_exp_f32 v190,v190
v_exp_f32 v191,v191
ds_read_b64_tr_b16 v[200:201],v16 offset:12288
ds_read_b64_tr_b16 v[202:203],v16 offset:12800
ds_read_b64_tr_b16 v[204:205],v16 offset:13312
ds_read_b64_tr_b16 v[206:207],v16 offset:13824
ds_read_b64_tr_b16 v[208:209],v16 offset:14336
ds_read_b64_tr_b16 v[210:211],v16 offset:14848
ds_read_b64_tr_b16 v[212:213],v16 offset:15360
ds_read_b64_tr_b16 v[214:215],v16 offset:15872
ds_read_b64_tr_b16 v[216:217],v16 offset:16384
ds_read_b64_tr_b16 v[218:219],v16 offset:16896
ds_read_b64_tr_b16 v[220:221],v16 offset:17408
ds_read_b64_tr_b16 v[222:223],v16 offset:17920
ds_read_b128 v[144:147],v18 offset:1024
ds_read_b128 v[148:151],v18 offset:1056
ds_read_b128 v[152:155],v18 offset:1088
ds_read_b128 v[156:159],v18 offset:1120
s_mov_b64 exec,s[60:61]
s_add_u32 m0,s58,155136
s_mov_b32 s64,49152
buffer_load_dwordx4 v28,s[48:51],s64 offen lds
s_add_u32 m0,s58,155904
s_mov_b32 s65,49920
buffer_load_dwordx4 v28,s[48:51],s65 offen lds
s_mov_b64 exec,-1
s_setprio 0
v_add_f32 v176,1.0,v176
v_add_f32 v177,1.0,v177
v_add_f32 v178,1.0,v178
v_add_f32 v179,1.0,v179
v_add_f32 v180,1.0,v180
v_add_f32 v181,1.0,v181
v_add_f32 v182,1.0,v182
v_add_f32 v183,1.0,v183
v_add_f32 v184,1.0,v184
v_add_f32 v185,1.0,v185
v_add_f32 v186,1.0,v186
v_add_f32 v187,1.0,v187
v_add_f32 v188,1.0,v188
v_add_f32 v189,1.0,v189
v_add_f32 v190,1.0,v190
v_add_f32 v191,1.0,v191
s_setprio 1
s_waitcnt lgkmcnt(0)
v_mfma_f32_32x32x16_bf16 v[144:159],v[200:203],v[72:75],v[144:159]
v_rcp_f32 v176,v176
v_rcp_f32 v177,v177
v_rcp_f32 v178,v178
v_mfma_f32_32x32x16_bf16 v[144:159],v[204:207],v[76:79],v[144:159]
v_rcp_f32 v179,v179
v_rcp_f32 v180,v180
v_rcp_f32 v181,v181
v_mfma_f32_32x32x16_bf16 v[144:159],v[208:211],v[80:83],v[144:159]
v_rcp_f32 v182,v182
v_rcp_f32 v183,v183
v_rcp_f32 v184,v184
v_mfma_f32_32x32x16_bf16 v[144:159],v[212:215],v[84:87],v[144:159]
v_rcp_f32 v185,v185
v_rcp_f32 v186,v186
v_rcp_f32 v187,v187
v_mfma_f32_32x32x16_bf16 v[144:159],v[216:219],v[88:91],v[144:159]
v_rcp_f32 v188,v188
v_rcp_f32 v189,v189
v_mfma_f32_32x32x16_bf16 v[144:159],v[220:223],v[92:95],v[144:159]
v_rcp_f32 v190,v190
v_rcp_f32 v191,v191
ds_read_b64_tr_b16 v[200:201],v17 offset:6144
ds_read_b64_tr_b16 v[202:203],v17 offset:7680
ds_read_b64_tr_b16 v[204:205],v17 offset:9216
ds_read_b64_tr_b16 v[206:207],v17 offset:10752
ds_read_b64_tr_b16 v[208:209],v17 offset:6208
ds_read_b64_tr_b16 v[210:211],v17 offset:7744
ds_read_b64_tr_b16 v[212:213],v17 offset:9280
ds_read_b64_tr_b16 v[214:215],v17 offset:10816
ds_read_b64_tr_b16 v[216:217],v17 offset:6272
ds_read_b64_tr_b16 v[218:219],v17 offset:7808
ds_read_b64_tr_b16 v[220:221],v17 offset:9344
ds_read_b64_tr_b16 v[222:223],v17 offset:10880
s_mov_b64 exec,s[60:61]
s_add_u32 m0,s58,130560
s_movk_i32 s64,640
buffer_load_dwordx4 v27,s[44:47],s64 offen lds
s_add_u32 m0,s58,131328
s_movk_i32 s65,9856
buffer_load_dwordx4 v27,s[44:47],s65 offen lds
s_mov_b64 exec,-1
s_setprio 0
v_mul_f32 v176,v160,v176
v_mul_f32 v177,v161,v177
v_mul_f32 v178,v162,v178
v_mul_f32 v179,v163,v179
v_mul_f32 v180,v164,v180
v_mul_f32 v181,v165,v181
v_mul_f32 v182,v166,v182
v_mul_f32 v183,v167,v183
v_mul_f32 v184,v168,v184
v_mul_f32 v185,v169,v185
v_mul_f32 v186,v170,v186
v_mul_f32 v187,v171,v187
v_mul_f32 v188,v172,v188
v_mul_f32 v189,v173,v189
v_mul_f32 v190,v174,v190
v_mul_f32 v191,v175,v191
v_cvt_pk_bf16_f32 v192,v176,v177
v_cvt_pk_bf16_f32 v193,v178,v179
v_cvt_pk_bf16_f32 v194,v180,v181
v_cvt_pk_bf16_f32 v195,v182,v183
v_cvt_pk_bf16_f32 v196,v184,v185
v_cvt_pk_bf16_f32 v197,v186,v187
v_cvt_pk_bf16_f32 v198,v188,v189
v_cvt_pk_bf16_f32 v199,v190,v191
s_waitcnt lgkmcnt(0)
s_waitcnt vmcnt(2)
s_barrier
v_fmaak_f32 v176,v144,v144,0x409d30fc
v_fmaak_f32 v177,v145,v145,0x409d30fc
v_fmaak_f32 v178,v146,v146,0x409d30fc
v_fmaak_f32 v179,v147,v147,0x409d30fc
v_fmaak_f32 v180,v148,v148,0x409d30fc
v_fmaak_f32 v181,v149,v149,0x409d30fc
v_fmaak_f32 v182,v150,v150,0x409d30fc
v_fmaak_f32 v183,v151,v151,0x409d30fc
v_fmaak_f32 v184,v152,v152,0x409d30fc
v_fmaak_f32 v185,v153,v153,0x409d30fc
v_fmaak_f32 v186,v154,v154,0x409d30fc
v_fmaak_f32 v187,v155,v155,0x409d30fc
v_fmaak_f32 v188,v156,v156,0x409d30fc
v_fmaak_f32 v189,v157,v157,0x409d30fc
v_fmaak_f32 v190,v158,v158,0x409d30fc
v_fmaak_f32 v191,v159,v159,0x409d30fc
v_mul_f32 v176,v144,v176
v_mul_f32 v177,v145,v177
v_mul_f32 v178,v146,v178
v_mul_f32 v179,v147,v179
v_mul_f32 v180,v148,v180
v_mul_f32 v181,v149,v181
v_mul_f32 v182,v150,v182
v_mul_f32 v183,v151,v183
v_mul_f32 v184,v152,v184
v_mul_f32 v185,v153,v185
v_mul_f32 v186,v154,v186
v_mul_f32 v187,v155,v187
v_mul_f32 v188,v156,v188
v_mul_f32 v189,v157,v189
v_mul_f32 v190,v158,v190
v_mul_f32 v191,v159,v191
s_setprio 1
v_mfma_f32_32x32x16_bf16 v[96:111],v[200:203],v[192:195],v[96:111]
v_exp_f32 v176,v176
v_exp_f32 v177,v177
v_exp_f32 v178,v178
v_mfma_f32_32x32x16_bf16 v[96:111],v[204:207],v[196:199],v[96:111]
v_exp_f32 v179,v179
v_exp_f32 v180,v180
v_exp_f32 v181,v181
v_mfma_f32_32x32x16_bf16 v[112:127],v[208:211],v[192:195],v[112:127]
v_exp_f32 v182,v182
v_exp_f32 v183,v183
v_exp_f32 v184,v184
v_mfma_f32_32x32x16_bf16 v[112:127],v[212:215],v[196:199],v[112:127]
v_exp_f32 v185,v185
v_exp_f32 v186,v186
v_exp_f32 v187,v187
v_mfma_f32_32x32x16_bf16 v[128:143],v[216:219],v[192:195],v[128:143]
v_exp_f32 v188,v188
v_exp_f32 v189,v189
v_mfma_f32_32x32x16_bf16 v[128:143],v[220:223],v[196:199],v[128:143]
v_exp_f32 v190,v190
v_exp_f32 v191,v191
ds_read_b64_tr_b16 v[200:201],v16 offset:0
ds_read_b64_tr_b16 v[202:203],v16 offset:512
ds_read_b64_tr_b16 v[204:205],v16 offset:1024
ds_read_b64_tr_b16 v[206:207],v16 offset:1536
ds_read_b64_tr_b16 v[208:209],v16 offset:2048
ds_read_b64_tr_b16 v[210:211],v16 offset:2560
ds_read_b64_tr_b16 v[212:213],v16 offset:3072
ds_read_b64_tr_b16 v[214:215],v16 offset:3584
ds_read_b64_tr_b16 v[216:217],v16 offset:4096
ds_read_b64_tr_b16 v[218:219],v16 offset:4608
ds_read_b64_tr_b16 v[220:221],v16 offset:5120
ds_read_b64_tr_b16 v[222:223],v16 offset:5632
ds_read_b128 v[160:163],v18 offset:1152
ds_read_b128 v[164:167],v18 offset:1184
ds_read_b128 v[168:171],v18 offset:1216
ds_read_b128 v[172:175],v18 offset:1248
s_mov_b64 exec,s[60:61]
s_add_u32 m0,s58,142848
s_mov_b32 s64,55296
buffer_load_dwordx4 v28,s[48:51],s64 offen lds
s_add_u32 m0,s58,143616
s_mov_b32 s65,56064
buffer_load_dwordx4 v28,s[48:51],s65 offen lds
s_mov_b64 exec,-1
s_setprio 0
v_add_f32 v176,1.0,v176
v_add_f32 v177,1.0,v177
v_add_f32 v178,1.0,v178
v_add_f32 v179,1.0,v179
v_add_f32 v180,1.0,v180
v_add_f32 v181,1.0,v181
v_add_f32 v182,1.0,v182
v_add_f32 v183,1.0,v183
v_add_f32 v184,1.0,v184
v_add_f32 v185,1.0,v185
v_add_f32 v186,1.0,v186
v_add_f32 v187,1.0,v187
v_add_f32 v188,1.0,v188
v_add_f32 v189,1.0,v189
v_add_f32 v190,1.0,v190
v_add_f32 v191,1.0,v191
s_setprio 1
s_waitcnt lgkmcnt(0)
v_mfma_f32_32x32x16_bf16 v[160:175],v[200:203],v[72:75],v[160:175]
v_rcp_f32 v176,v176
v_rcp_f32 v177,v177
v_rcp_f32 v178,v178
v_mfma_f32_32x32x16_bf16 v[160:175],v[204:207],v[76:79],v[160:175]
v_rcp_f32 v179,v179
v_rcp_f32 v180,v180
v_rcp_f32 v181,v181
v_mfma_f32_32x32x16_bf16 v[160:175],v[208:211],v[80:83],v[160:175]
v_rcp_f32 v182,v182
v_rcp_f32 v183,v183
v_rcp_f32 v184,v184
v_mfma_f32_32x32x16_bf16 v[160:175],v[212:215],v[84:87],v[160:175]
v_rcp_f32 v185,v185
v_rcp_f32 v186,v186
v_rcp_f32 v187,v187
v_mfma_f32_32x32x16_bf16 v[160:175],v[216:219],v[88:91],v[160:175]
v_rcp_f32 v188,v188
v_rcp_f32 v189,v189
v_mfma_f32_32x32x16_bf16 v[160:175],v[220:223],v[92:95],v[160:175]
v_rcp_f32 v190,v190
v_rcp_f32 v191,v191
ds_read_b64_tr_b16 v[200:201],v17 offset:12288
ds_read_b64_tr_b16 v[202:203],v17 offset:13824
ds_read_b64_tr_b16 v[204:205],v17 offset:15360
ds_read_b64_tr_b16 v[206:207],v17 offset:16896
ds_read_b64_tr_b16 v[208:209],v17 offset:12352
ds_read_b64_tr_b16 v[210:211],v17 offset:13888
ds_read_b64_tr_b16 v[212:213],v17 offset:15424
ds_read_b64_tr_b16 v[214:215],v17 offset:16960
ds_read_b64_tr_b16 v[216:217],v17 offset:12416
ds_read_b64_tr_b16 v[218:219],v17 offset:13952
ds_read_b64_tr_b16 v[220:221],v17 offset:15488
ds_read_b64_tr_b16 v[222:223],v17 offset:17024
s_mov_b64 exec,s[60:61]
s_add_u32 m0,s58,136704
s_movk_i32 s64,704
buffer_load_dwordx4 v27,s[44:47],s64 offen lds
s_add_u32 m0,s58,137472
s_movk_i32 s65,9920
buffer_load_dwordx4 v27,s[44:47],s65 offen lds
s_mov_b64 exec,-1
s_setprio 0
v_mul_f32 v176,v144,v176
v_mul_f32 v177,v145,v177
v_mul_f32 v178,v146,v178
v_mul_f32 v179,v147,v179
v_mul_f32 v180,v148,v180
v_mul_f32 v181,v149,v181
v_mul_f32 v182,v150,v182
v_mul_f32 v183,v151,v183
v_mul_f32 v184,v152,v184
v_mul_f32 v185,v153,v185
v_mul_f32 v186,v154,v186
v_mul_f32 v187,v155,v187
v_mul_f32 v188,v156,v188
v_mul_f32 v189,v157,v189
v_mul_f32 v190,v158,v190
v_mul_f32 v191,v159,v191
v_cvt_pk_bf16_f32 v192,v176,v177
v_cvt_pk_bf16_f32 v193,v178,v179
v_cvt_pk_bf16_f32 v194,v180,v181
v_cvt_pk_bf16_f32 v195,v182,v183
v_cvt_pk_bf16_f32 v196,v184,v185
v_cvt_pk_bf16_f32 v197,v186,v187
v_cvt_pk_bf16_f32 v198,v188,v189
v_cvt_pk_bf16_f32 v199,v190,v191
s_waitcnt lgkmcnt(0)
s_waitcnt vmcnt(2)
s_barrier
v_fmaak_f32 v176,v160,v160,0x409d30fc
v_fmaak_f32 v177,v161,v161,0x409d30fc
v_fmaak_f32 v178,v162,v162,0x409d30fc
v_fmaak_f32 v179,v163,v163,0x409d30fc
v_fmaak_f32 v180,v164,v164,0x409d30fc
v_fmaak_f32 v181,v165,v165,0x409d30fc
v_fmaak_f32 v182,v166,v166,0x409d30fc
v_fmaak_f32 v183,v167,v167,0x409d30fc
v_fmaak_f32 v184,v168,v168,0x409d30fc
v_fmaak_f32 v185,v169,v169,0x409d30fc
v_fmaak_f32 v186,v170,v170,0x409d30fc
v_fmaak_f32 v187,v171,v171,0x409d30fc
v_fmaak_f32 v188,v172,v172,0x409d30fc
v_fmaak_f32 v189,v173,v173,0x409d30fc
v_fmaak_f32 v190,v174,v174,0x409d30fc
v_fmaak_f32 v191,v175,v175,0x409d30fc
v_mul_f32 v176,v160,v176
v_mul_f32 v177,v161,v177
v_mul_f32 v178,v162,v178
v_mul_f32 v179,v163,v179
v_mul_f32 v180,v164,v180
v_mul_f32 v181,v165,v181
v_mul_f32 v182,v166,v182
v_mul_f32 v183,v167,v183
v_mul_f32 v184,v168,v184
v_mul_f32 v185,v169,v185
v_mul_f32 v186,v170,v186
v_mul_f32 v187,v171,v187
v_mul_f32 v188,v172,v188
v_mul_f32 v189,v173,v189
v_mul_f32 v190,v174,v190
v_mul_f32 v191,v175,v191
s_setprio 1
v_mfma_f32_32x32x16_bf16 v[96:111],v[200:203],v[192:195],v[96:111]
v_exp_f32 v176,v176
v_exp_f32 v177,v177
v_exp_f32 v178,v178
v_mfma_f32_32x32x16_bf16 v[96:111],v[204:207],v[196:199],v[96:111]
v_exp_f32 v179,v179
v_exp_f32 v180,v180
v_exp_f32 v181,v181
v_mfma_f32_32x32x16_bf16 v[112:127],v[208:211],v[192:195],v[112:127]
v_exp_f32 v182,v182
v_exp_f32 v183,v183
v_exp_f32 v184,v184
v_mfma_f32_32x32x16_bf16 v[112:127],v[212:215],v[196:199],v[112:127]
v_exp_f32 v185,v185
v_exp_f32 v186,v186
v_exp_f32 v187,v187
v_mfma_f32_32x32x16_bf16 v[128:143],v[216:219],v[192:195],v[128:143]
v_exp_f32 v188,v188
v_exp_f32 v189,v189
v_mfma_f32_32x32x16_bf16 v[128:143],v[220:223],v[196:199],v[128:143]
v_exp_f32 v190,v190
v_exp_f32 v191,v191
ds_read_b64_tr_b16 v[200:201],v16 offset:6144
ds_read_b64_tr_b16 v[202:203],v16 offset:6656
ds_read_b64_tr_b16 v[204:205],v16 offset:7168
ds_read_b64_tr_b16 v[206:207],v16 offset:7680
ds_read_b64_tr_b16 v[208:209],v16 offset:8192
ds_read_b64_tr_b16 v[210:211],v16 offset:8704
ds_read_b64_tr_b16 v[212:213],v16 offset:9216
ds_read_b64_tr_b16 v[214:215],v16 offset:9728
ds_read_b64_tr_b16 v[216:217],v16 offset:10240
ds_read_b64_tr_b16 v[218:219],v16 offset:10752
ds_read_b64_tr_b16 v[220:221],v16 offset:11264
ds_read_b64_tr_b16 v[222:223],v16 offset:11776
ds_read_b128 v[144:147],v18 offset:1280
ds_read_b128 v[148:151],v18 offset:1312
ds_read_b128 v[152:155],v18 offset:1344
ds_read_b128 v[156:159],v18 offset:1376
s_mov_b64 exec,s[60:61]
s_add_u32 m0,s58,148992
s_mov_b32 s64,61440
buffer_load_dwordx4 v28,s[48:51],s64 offen lds
s_add_u32 m0,s58,149760
s_mov_b32 s65,62208
buffer_load_dwordx4 v28,s[48:51],s65 offen lds
s_mov_b64 exec,-1
s_setprio 0
v_add_f32 v176,1.0,v176
v_add_f32 v177,1.0,v177
v_add_f32 v178,1.0,v178
v_add_f32 v179,1.0,v179
v_add_f32 v180,1.0,v180
v_add_f32 v181,1.0,v181
v_add_f32 v182,1.0,v182
v_add_f32 v183,1.0,v183
v_add_f32 v184,1.0,v184
v_add_f32 v185,1.0,v185
v_add_f32 v186,1.0,v186
v_add_f32 v187,1.0,v187
v_add_f32 v188,1.0,v188
v_add_f32 v189,1.0,v189
v_add_f32 v190,1.0,v190
v_add_f32 v191,1.0,v191
s_setprio 1
s_waitcnt lgkmcnt(0)
v_mfma_f32_32x32x16_bf16 v[144:159],v[200:203],v[72:75],v[144:159]
v_rcp_f32 v176,v176
v_rcp_f32 v177,v177
v_rcp_f32 v178,v178
v_mfma_f32_32x32x16_bf16 v[144:159],v[204:207],v[76:79],v[144:159]
v_rcp_f32 v179,v179
v_rcp_f32 v180,v180
v_rcp_f32 v181,v181
v_mfma_f32_32x32x16_bf16 v[144:159],v[208:211],v[80:83],v[144:159]
v_rcp_f32 v182,v182
v_rcp_f32 v183,v183
v_rcp_f32 v184,v184
v_mfma_f32_32x32x16_bf16 v[144:159],v[212:215],v[84:87],v[144:159]
v_rcp_f32 v185,v185
v_rcp_f32 v186,v186
v_rcp_f32 v187,v187
v_mfma_f32_32x32x16_bf16 v[144:159],v[216:219],v[88:91],v[144:159]
v_rcp_f32 v188,v188
v_rcp_f32 v189,v189
v_mfma_f32_32x32x16_bf16 v[144:159],v[220:223],v[92:95],v[144:159]
v_rcp_f32 v190,v190
v_rcp_f32 v191,v191
ds_read_b64_tr_b16 v[200:201],v17 offset:0
ds_read_b64_tr_b16 v[202:203],v17 offset:1536
ds_read_b64_tr_b16 v[204:205],v17 offset:3072
ds_read_b64_tr_b16 v[206:207],v17 offset:4608
ds_read_b64_tr_b16 v[208:209],v17 offset:64
ds_read_b64_tr_b16 v[210:211],v17 offset:1600
ds_read_b64_tr_b16 v[212:213],v17 offset:3136
ds_read_b64_tr_b16 v[214:215],v17 offset:4672
ds_read_b64_tr_b16 v[216:217],v17 offset:128
ds_read_b64_tr_b16 v[218:219],v17 offset:1664
ds_read_b64_tr_b16 v[220:221],v17 offset:3200
ds_read_b64_tr_b16 v[222:223],v17 offset:4736
s_mov_b64 exec,s[60:61]
s_add_u32 m0,s58,124416
s_movk_i32 s64,0
buffer_load_dwordx4 v27,s[44:47],s64 offen lds
s_add_u32 m0,s58,125184
s_movk_i32 s65,9216
buffer_load_dwordx4 v27,s[44:47],s65 offen lds
s_mov_b64 exec,-1
s_setprio 0
v_mul_f32 v176,v160,v176
v_mul_f32 v177,v161,v177
v_mul_f32 v178,v162,v178
v_mul_f32 v179,v163,v179
v_mul_f32 v180,v164,v180
v_mul_f32 v181,v165,v181
v_mul_f32 v182,v166,v182
v_mul_f32 v183,v167,v183
v_mul_f32 v184,v168,v184
v_mul_f32 v185,v169,v185
v_mul_f32 v186,v170,v186
v_mul_f32 v187,v171,v187
v_mul_f32 v188,v172,v188
v_mul_f32 v189,v173,v189
v_mul_f32 v190,v174,v190
v_mul_f32 v191,v175,v191
v_cvt_pk_bf16_f32 v192,v176,v177
v_cvt_pk_bf16_f32 v193,v178,v179
v_cvt_pk_bf16_f32 v194,v180,v181
v_cvt_pk_bf16_f32 v195,v182,v183
v_cvt_pk_bf16_f32 v196,v184,v185
v_cvt_pk_bf16_f32 v197,v186,v187
v_cvt_pk_bf16_f32 v198,v188,v189
v_cvt_pk_bf16_f32 v199,v190,v191
s_waitcnt lgkmcnt(0)
s_waitcnt vmcnt(2)
s_barrier
v_fmaak_f32 v176,v144,v144,0x409d30fc
v_fmaak_f32 v177,v145,v145,0x409d30fc
v_fmaak_f32 v178,v146,v146,0x409d30fc
v_fmaak_f32 v179,v147,v147,0x409d30fc
v_fmaak_f32 v180,v148,v148,0x409d30fc
v_fmaak_f32 v181,v149,v149,0x409d30fc
v_fmaak_f32 v182,v150,v150,0x409d30fc
v_fmaak_f32 v183,v151,v151,0x409d30fc
v_fmaak_f32 v184,v152,v152,0x409d30fc
v_fmaak_f32 v185,v153,v153,0x409d30fc
v_fmaak_f32 v186,v154,v154,0x409d30fc
v_fmaak_f32 v187,v155,v155,0x409d30fc
v_fmaak_f32 v188,v156,v156,0x409d30fc
v_fmaak_f32 v189,v157,v157,0x409d30fc
v_fmaak_f32 v190,v158,v158,0x409d30fc
v_fmaak_f32 v191,v159,v159,0x409d30fc
v_mul_f32 v176,v144,v176
v_mul_f32 v177,v145,v177
v_mul_f32 v178,v146,v178
v_mul_f32 v179,v147,v179
v_mul_f32 v180,v148,v180
v_mul_f32 v181,v149,v181
v_mul_f32 v182,v150,v182
v_mul_f32 v183,v151,v183
v_mul_f32 v184,v152,v184
v_mul_f32 v185,v153,v185
v_mul_f32 v186,v154,v186
v_mul_f32 v187,v155,v187
v_mul_f32 v188,v156,v188
v_mul_f32 v189,v157,v189
v_mul_f32 v190,v158,v190
v_mul_f32 v191,v159,v191
s_setprio 1
v_mfma_f32_32x32x16_bf16 v[96:111],v[200:203],v[192:195],v[96:111]
v_exp_f32 v176,v176
v_exp_f32 v177,v177
v_exp_f32 v178,v178
v_mfma_f32_32x32x16_bf16 v[96:111],v[204:207],v[196:199],v[96:111]
v_exp_f32 v179,v179
v_exp_f32 v180,v180
v_exp_f32 v181,v181
v_mfma_f32_32x32x16_bf16 v[112:127],v[208:211],v[192:195],v[112:127]
v_exp_f32 v182,v182
v_exp_f32 v183,v183
v_exp_f32 v184,v184
v_mfma_f32_32x32x16_bf16 v[112:127],v[212:215],v[196:199],v[112:127]
v_exp_f32 v185,v185
v_exp_f32 v186,v186
v_exp_f32 v187,v187
v_mfma_f32_32x32x16_bf16 v[128:143],v[216:219],v[192:195],v[128:143]
v_exp_f32 v188,v188
v_exp_f32 v189,v189
v_mfma_f32_32x32x16_bf16 v[128:143],v[220:223],v[196:199],v[128:143]
v_exp_f32 v190,v190
v_exp_f32 v191,v191
ds_read_b64_tr_b16 v[200:201],v16 offset:12288
ds_read_b64_tr_b16 v[202:203],v16 offset:12800
ds_read_b64_tr_b16 v[204:205],v16 offset:13312
ds_read_b64_tr_b16 v[206:207],v16 offset:13824
ds_read_b64_tr_b16 v[208:209],v16 offset:14336
ds_read_b64_tr_b16 v[210:211],v16 offset:14848
ds_read_b64_tr_b16 v[212:213],v16 offset:15360
ds_read_b64_tr_b16 v[214:215],v16 offset:15872
ds_read_b64_tr_b16 v[216:217],v16 offset:16384
ds_read_b64_tr_b16 v[218:219],v16 offset:16896
ds_read_b64_tr_b16 v[220:221],v16 offset:17408
ds_read_b64_tr_b16 v[222:223],v16 offset:17920
ds_read_b128 v[160:163],v18 offset:1408
ds_read_b128 v[164:167],v18 offset:1440
ds_read_b128 v[168:171],v18 offset:1472
ds_read_b128 v[172:175],v18 offset:1504
s_mov_b64 exec,s[60:61]
s_add_u32 m0,s58,155136
s_mov_b32 s64,67584
buffer_load_dwordx4 v28,s[48:51],s64 offen lds
s_add_u32 m0,s58,155904
s_mov_b32 s65,68352
buffer_load_dwordx4 v28,s[48:51],s65 offen lds
s_mov_b64 exec,-1
s_setprio 0
v_add_f32 v176,1.0,v176
v_add_f32 v177,1.0,v177
v_add_f32 v178,1.0,v178
v_add_f32 v179,1.0,v179
v_add_f32 v180,1.0,v180
v_add_f32 v181,1.0,v181
v_add_f32 v182,1.0,v182
v_add_f32 v183,1.0,v183
v_add_f32 v184,1.0,v184
v_add_f32 v185,1.0,v185
v_add_f32 v186,1.0,v186
v_add_f32 v187,1.0,v187
v_add_f32 v188,1.0,v188
v_add_f32 v189,1.0,v189
v_add_f32 v190,1.0,v190
v_add_f32 v191,1.0,v191
s_setprio 1
s_waitcnt lgkmcnt(0)
v_mfma_f32_32x32x16_bf16 v[160:175],v[200:203],v[72:75],v[160:175]
v_rcp_f32 v176,v176
v_rcp_f32 v177,v177
v_rcp_f32 v178,v178
v_mfma_f32_32x32x16_bf16 v[160:175],v[204:207],v[76:79],v[160:175]
v_rcp_f32 v179,v179
v_rcp_f32 v180,v180
v_rcp_f32 v181,v181
v_mfma_f32_32x32x16_bf16 v[160:175],v[208:211],v[80:83],v[160:175]
v_rcp_f32 v182,v182
v_rcp_f32 v183,v183
v_rcp_f32 v184,v184
v_mfma_f32_32x32x16_bf16 v[160:175],v[212:215],v[84:87],v[160:175]
v_rcp_f32 v185,v185
v_rcp_f32 v186,v186
v_rcp_f32 v187,v187
v_mfma_f32_32x32x16_bf16 v[160:175],v[216:219],v[88:91],v[160:175]
v_rcp_f32 v188,v188
v_rcp_f32 v189,v189
v_mfma_f32_32x32x16_bf16 v[160:175],v[220:223],v[92:95],v[160:175]
v_rcp_f32 v190,v190
v_rcp_f32 v191,v191
ds_read_b64_tr_b16 v[200:201],v17 offset:6144
ds_read_b64_tr_b16 v[202:203],v17 offset:7680
ds_read_b64_tr_b16 v[204:205],v17 offset:9216
ds_read_b64_tr_b16 v[206:207],v17 offset:10752
ds_read_b64_tr_b16 v[208:209],v17 offset:6208
ds_read_b64_tr_b16 v[210:211],v17 offset:7744
ds_read_b64_tr_b16 v[212:213],v17 offset:9280
ds_read_b64_tr_b16 v[214:215],v17 offset:10816
ds_read_b64_tr_b16 v[216:217],v17 offset:6272
ds_read_b64_tr_b16 v[218:219],v17 offset:7808
ds_read_b64_tr_b16 v[220:221],v17 offset:9344
ds_read_b64_tr_b16 v[222:223],v17 offset:10880
s_mov_b64 exec,s[60:61]
s_add_u32 m0,s58,130560
s_movk_i32 s64,64
buffer_load_dwordx4 v27,s[44:47],s64 offen lds
s_add_u32 m0,s58,131328
s_movk_i32 s65,9280
buffer_load_dwordx4 v27,s[44:47],s65 offen lds
s_mov_b64 exec,-1
s_setprio 0
v_mul_f32 v176,v144,v176
v_mul_f32 v177,v145,v177
v_mul_f32 v178,v146,v178
v_mul_f32 v179,v147,v179
v_mul_f32 v180,v148,v180
v_mul_f32 v181,v149,v181
v_mul_f32 v182,v150,v182
v_mul_f32 v183,v151,v183
v_mul_f32 v184,v152,v184
v_mul_f32 v185,v153,v185
v_mul_f32 v186,v154,v186
v_mul_f32 v187,v155,v187
v_mul_f32 v188,v156,v188
v_mul_f32 v189,v157,v189
v_mul_f32 v190,v158,v190
v_mul_f32 v191,v159,v191
v_cvt_pk_bf16_f32 v192,v176,v177
v_cvt_pk_bf16_f32 v193,v178,v179
v_cvt_pk_bf16_f32 v194,v180,v181
v_cvt_pk_bf16_f32 v195,v182,v183
v_cvt_pk_bf16_f32 v196,v184,v185
v_cvt_pk_bf16_f32 v197,v186,v187
v_cvt_pk_bf16_f32 v198,v188,v189
v_cvt_pk_bf16_f32 v199,v190,v191
s_waitcnt lgkmcnt(0)
s_waitcnt vmcnt(2)
s_barrier
v_fmaak_f32 v176,v160,v160,0x409d30fc
v_fmaak_f32 v177,v161,v161,0x409d30fc
v_fmaak_f32 v178,v162,v162,0x409d30fc
v_fmaak_f32 v179,v163,v163,0x409d30fc
v_fmaak_f32 v180,v164,v164,0x409d30fc
v_fmaak_f32 v181,v165,v165,0x409d30fc
v_fmaak_f32 v182,v166,v166,0x409d30fc
v_fmaak_f32 v183,v167,v167,0x409d30fc
v_fmaak_f32 v184,v168,v168,0x409d30fc
v_fmaak_f32 v185,v169,v169,0x409d30fc
v_fmaak_f32 v186,v170,v170,0x409d30fc
v_fmaak_f32 v187,v171,v171,0x409d30fc
v_fmaak_f32 v188,v172,v172,0x409d30fc
v_fmaak_f32 v189,v173,v173,0x409d30fc
v_fmaak_f32 v190,v174,v174,0x409d30fc
v_fmaak_f32 v191,v175,v175,0x409d30fc
v_mul_f32 v176,v160,v176
v_mul_f32 v177,v161,v177
v_mul_f32 v178,v162,v178
v_mul_f32 v179,v163,v179
v_mul_f32 v180,v164,v180
v_mul_f32 v181,v165,v181
v_mul_f32 v182,v166,v182
v_mul_f32 v183,v167,v183
v_mul_f32 v184,v168,v184
v_mul_f32 v185,v169,v185
v_mul_f32 v186,v170,v186
v_mul_f32 v187,v171,v187
v_mul_f32 v188,v172,v188
v_mul_f32 v189,v173,v189
v_mul_f32 v190,v174,v190
v_mul_f32 v191,v175,v191
s_setprio 1
v_mfma_f32_32x32x16_bf16 v[96:111],v[200:203],v[192:195],v[96:111]
v_exp_f32 v176,v176
v_exp_f32 v177,v177
v_exp_f32 v178,v178
v_mfma_f32_32x32x16_bf16 v[96:111],v[204:207],v[196:199],v[96:111]
v_exp_f32 v179,v179
v_exp_f32 v180,v180
v_exp_f32 v181,v181
v_mfma_f32_32x32x16_bf16 v[112:127],v[208:211],v[192:195],v[112:127]
v_exp_f32 v182,v182
v_exp_f32 v183,v183
v_exp_f32 v184,v184
v_mfma_f32_32x32x16_bf16 v[112:127],v[212:215],v[196:199],v[112:127]
v_exp_f32 v185,v185
v_exp_f32 v186,v186
v_exp_f32 v187,v187
v_mfma_f32_32x32x16_bf16 v[128:143],v[216:219],v[192:195],v[128:143]
v_exp_f32 v188,v188
v_exp_f32 v189,v189
v_mfma_f32_32x32x16_bf16 v[128:143],v[220:223],v[196:199],v[128:143]
v_exp_f32 v190,v190
v_exp_f32 v191,v191
ds_read_b64 v[72:73],v20 offset:0
ds_read_b64 v[74:75],v21 offset:0
ds_read_b64 v[76:77],v22 offset:0
ds_read_b64 v[78:79],v23 offset:0
ds_read_b64 v[80:81],v20 offset:64
ds_read_b64 v[82:83],v21 offset:64
ds_read_b64 v[84:85],v22 offset:64
ds_read_b64 v[86:87],v23 offset:64
ds_read_b64 v[88:89],v20 offset:128
ds_read_b64 v[90:91],v21 offset:128
ds_read_b64 v[92:93],v22 offset:128
ds_read_b64 v[94:95],v23 offset:128
s_mov_b64 exec,s[60:61]
s_add_u32 m0,s58,142848
s_mov_b32 s64,0
buffer_load_dwordx4 v28,s[48:51],s64 offen lds
s_add_u32 m0,s58,143616
s_mov_b32 s65,768
buffer_load_dwordx4 v28,s[48:51],s65 offen lds
s_mov_b64 exec,-1
s_setprio 0
v_add_f32 v176,1.0,v176
v_add_f32 v177,1.0,v177
v_add_f32 v178,1.0,v178
v_add_f32 v179,1.0,v179
v_add_f32 v180,1.0,v180
v_add_f32 v181,1.0,v181
v_add_f32 v182,1.0,v182
v_add_f32 v183,1.0,v183
v_add_f32 v184,1.0,v184
v_add_f32 v185,1.0,v185
v_add_f32 v186,1.0,v186
v_add_f32 v187,1.0,v187
v_add_f32 v188,1.0,v188
v_add_f32 v189,1.0,v189
v_add_f32 v190,1.0,v190
v_add_f32 v191,1.0,v191
s_setprio 1
s_waitcnt lgkmcnt(0)
v_rcp_f32 v176,v176
v_rcp_f32 v177,v177
v_rcp_f32 v178,v178
v_rcp_f32 v179,v179
v_rcp_f32 v180,v180
v_rcp_f32 v181,v181
v_rcp_f32 v182,v182
v_rcp_f32 v183,v183
v_rcp_f32 v184,v184
v_rcp_f32 v185,v185
v_rcp_f32 v186,v186
v_rcp_f32 v187,v187
v_rcp_f32 v188,v188
v_rcp_f32 v189,v189
v_rcp_f32 v190,v190
v_rcp_f32 v191,v191
ds_read_b64_tr_b16 v[200:201],v17 offset:12288
ds_read_b64_tr_b16 v[202:203],v17 offset:13824
ds_read_b64_tr_b16 v[204:205],v17 offset:15360
ds_read_b64_tr_b16 v[206:207],v17 offset:16896
ds_read_b64_tr_b16 v[208:209],v17 offset:12352
ds_read_b64_tr_b16 v[210:211],v17 offset:13888
ds_read_b64_tr_b16 v[212:213],v17 offset:15424
ds_read_b64_tr_b16 v[214:215],v17 offset:16960
ds_read_b64_tr_b16 v[216:217],v17 offset:12416
ds_read_b64_tr_b16 v[218:219],v17 offset:13952
ds_read_b64_tr_b16 v[220:221],v17 offset:15488
ds_read_b64_tr_b16 v[222:223],v17 offset:17024
s_mov_b64 exec,s[60:61]
s_add_u32 m0,s58,136704
s_movk_i32 s64,128
buffer_load_dwordx4 v27,s[44:47],s64 offen lds
s_add_u32 m0,s58,137472
s_movk_i32 s65,9344
buffer_load_dwordx4 v27,s[44:47],s65 offen lds
s_mov_b64 exec,-1
s_setprio 0
v_mul_f32 v176,v160,v176
v_mul_f32 v177,v161,v177
v_mul_f32 v178,v162,v178
v_mul_f32 v179,v163,v179
v_mul_f32 v180,v164,v180
v_mul_f32 v181,v165,v181
v_mul_f32 v182,v166,v182
v_mul_f32 v183,v167,v183
v_mul_f32 v184,v168,v184
v_mul_f32 v185,v169,v185
v_mul_f32 v186,v170,v186
v_mul_f32 v187,v171,v187
v_mul_f32 v188,v172,v188
v_mul_f32 v189,v173,v189
v_mul_f32 v190,v174,v190
v_mul_f32 v191,v175,v191
v_cvt_pk_bf16_f32 v192,v176,v177
v_cvt_pk_bf16_f32 v193,v178,v179
v_cvt_pk_bf16_f32 v194,v180,v181
v_cvt_pk_bf16_f32 v195,v182,v183
v_cvt_pk_bf16_f32 v196,v184,v185
v_cvt_pk_bf16_f32 v197,v186,v187
v_cvt_pk_bf16_f32 v198,v188,v189
v_cvt_pk_bf16_f32 v199,v190,v191
ds_read_b128 v[144:147],v18 offset:1536
ds_read_b128 v[148:151],v18 offset:1568
ds_read_b128 v[152:155],v18 offset:1600
ds_read_b128 v[156:159],v18 offset:1632
ds_read_b128 v[160:163],v18 offset:1664
ds_read_b128 v[164:167],v18 offset:1696
ds_read_b128 v[168:171],v18 offset:1728
ds_read_b128 v[172:175],v18 offset:1760
ds_read_b128 v[176:179],v18 offset:1792
ds_read_b128 v[180:183],v18 offset:1824
ds_read_b128 v[184:187],v18 offset:1856
ds_read_b128 v[188:191],v18 offset:1888
s_waitcnt lgkmcnt(12)
s_setprio 1
v_mfma_f32_32x32x16_bf16 v[96:111],v[200:203],v[192:195],v[96:111]
v_lshlrev_b32 v232,16,v72
v_lshlrev_b32 v233,16,v73
v_lshlrev_b32 v234,16,v74
v_lshlrev_b32 v235,16,v75
v_mfma_f32_32x32x16_bf16 v[96:111],v[204:207],v[196:199],v[96:111]
v_lshlrev_b32 v236,16,v76
v_lshlrev_b32 v237,16,v77
v_lshlrev_b32 v238,16,v78
v_lshlrev_b32 v239,16,v79
v_mfma_f32_32x32x16_bf16 v[112:127],v[208:211],v[192:195],v[112:127]
v_lshlrev_b32 v240,16,v80
v_lshlrev_b32 v241,16,v81
v_lshlrev_b32 v242,16,v82
v_lshlrev_b32 v243,16,v83
v_mfma_f32_32x32x16_bf16 v[112:127],v[212:215],v[196:199],v[112:127]
v_lshlrev_b32 v244,16,v84
v_lshlrev_b32 v245,16,v85
v_lshlrev_b32 v246,16,v86
v_lshlrev_b32 v247,16,v87
v_mfma_f32_32x32x16_bf16 v[128:143],v[216:219],v[192:195],v[128:143]
v_lshlrev_b32 v248,16,v88
v_lshlrev_b32 v249,16,v89
v_lshlrev_b32 v250,16,v90
v_lshlrev_b32 v251,16,v91
v_mfma_f32_32x32x16_bf16 v[128:143],v[220:223],v[196:199],v[128:143]
v_lshlrev_b32 v252,16,v92
v_lshlrev_b32 v253,16,v93
v_lshlrev_b32 v254,16,v94
v_lshlrev_b32 v255,16,v95
s_setprio 0
s_waitcnt lgkmcnt(0)
v_and_b32 v72,0xffff0000,v72
v_and_b32 v73,0xffff0000,v73
v_and_b32 v74,0xffff0000,v74
v_and_b32 v75,0xffff0000,v75
v_and_b32 v76,0xffff0000,v76
v_and_b32 v77,0xffff0000,v77
v_and_b32 v78,0xffff0000,v78
v_and_b32 v79,0xffff0000,v79
v_and_b32 v80,0xffff0000,v80
v_and_b32 v81,0xffff0000,v81
v_and_b32 v82,0xffff0000,v82
v_and_b32 v83,0xffff0000,v83
v_and_b32 v84,0xffff0000,v84
v_and_b32 v85,0xffff0000,v85
v_and_b32 v86,0xffff0000,v86
v_and_b32 v87,0xffff0000,v87
v_and_b32 v88,0xffff0000,v88
v_and_b32 v89,0xffff0000,v89
v_and_b32 v90,0xffff0000,v90
v_and_b32 v91,0xffff0000,v91
v_and_b32 v92,0xffff0000,v92
v_and_b32 v93,0xffff0000,v93
v_and_b32 v94,0xffff0000,v94
v_and_b32 v95,0xffff0000,v95
v_fma_f32 v232,v96,v144,v232
v_fma_f32 v72,v97,v145,v72
v_fma_f32 v233,v98,v146,v233
v_fma_f32 v73,v99,v147,v73
v_fma_f32 v234,v100,v148,v234
v_fma_f32 v74,v101,v149,v74
v_fma_f32 v235,v102,v150,v235
v_fma_f32 v75,v103,v151,v75
v_fma_f32 v236,v104,v152,v236
v_fma_f32 v76,v105,v153,v76
v_fma_f32 v237,v106,v154,v237
v_fma_f32 v77,v107,v155,v77
v_fma_f32 v238,v108,v156,v238
v_fma_f32 v78,v109,v157,v78
v_fma_f32 v239,v110,v158,v239
v_fma_f32 v79,v111,v159,v79
v_fma_f32 v240,v112,v160,v240
v_fma_f32 v80,v113,v161,v80
v_fma_f32 v241,v114,v162,v241
v_fma_f32 v81,v115,v163,v81
v_fma_f32 v242,v116,v164,v242
v_fma_f32 v82,v117,v165,v82
v_fma_f32 v243,v118,v166,v243
v_fma_f32 v83,v119,v167,v83
v_fma_f32 v244,v120,v168,v244
v_fma_f32 v84,v121,v169,v84
v_fma_f32 v245,v122,v170,v245
v_fma_f32 v85,v123,v171,v85
v_fma_f32 v246,v124,v172,v246
v_fma_f32 v86,v125,v173,v86
v_fma_f32 v247,v126,v174,v247
v_fma_f32 v87,v127,v175,v87
v_fma_f32 v248,v128,v176,v248
v_fma_f32 v88,v129,v177,v88
v_fma_f32 v249,v130,v178,v249
v_fma_f32 v89,v131,v179,v89
v_fma_f32 v250,v132,v180,v250
v_fma_f32 v90,v133,v181,v90
v_fma_f32 v251,v134,v182,v251
v_fma_f32 v91,v135,v183,v91
v_fma_f32 v252,v136,v184,v252
v_fma_f32 v92,v137,v185,v92
v_fma_f32 v253,v138,v186,v253
v_fma_f32 v93,v139,v187,v93
v_fma_f32 v254,v140,v188,v254
v_fma_f32 v94,v141,v189,v94
v_fma_f32 v255,v142,v190,v255
v_fma_f32 v95,v143,v191,v95
v_add_u32 v4,s54,v4
v_add_u32 v5,s54,v5
v_add_u32 v6,s54,v6
v_add_u32 v7,s54,v7
v_add_u32 v8,s54,v8
v_add_u32 v9,s54,v9
v_add_u32 v10,s54,v10
v_add_u32 v11,s54,v11
v_add_u32 v12,s54,v12
v_add_u32 v13,s54,v13
v_add_u32 v14,s54,v14
v_add_u32 v15,s54,v15
v_cvt_pk_bf16_f32 v200,v232,v72
v_cvt_pk_bf16_f32 v201,v233,v73
v_cvt_pk_bf16_f32 v202,v234,v74
v_cvt_pk_bf16_f32 v203,v235,v75
v_cvt_pk_bf16_f32 v204,v236,v76
v_cvt_pk_bf16_f32 v205,v237,v77
v_cvt_pk_bf16_f32 v206,v238,v78
v_cvt_pk_bf16_f32 v207,v239,v79
v_cvt_pk_bf16_f32 v208,v240,v80
v_cvt_pk_bf16_f32 v209,v241,v81
v_cvt_pk_bf16_f32 v210,v242,v82
v_cvt_pk_bf16_f32 v211,v243,v83
v_cvt_pk_bf16_f32 v212,v244,v84
v_cvt_pk_bf16_f32 v213,v245,v85
v_cvt_pk_bf16_f32 v214,v246,v86
v_cvt_pk_bf16_f32 v215,v247,v87
v_cvt_pk_bf16_f32 v216,v248,v88
v_cvt_pk_bf16_f32 v217,v249,v89
v_cvt_pk_bf16_f32 v218,v250,v90
v_cvt_pk_bf16_f32 v219,v251,v91
v_cvt_pk_bf16_f32 v220,v252,v92
v_cvt_pk_bf16_f32 v221,v253,v93
v_cvt_pk_bf16_f32 v222,v254,v94
v_cvt_pk_bf16_f32 v223,v255,v95
ds_write_b64 v20,v[200:201] offset:0
ds_write_b64 v21,v[202:203] offset:0
ds_write_b64 v22,v[204:205] offset:0
ds_write_b64 v23,v[206:207] offset:0
ds_write_b64 v20,v[208:209] offset:64
ds_write_b64 v21,v[210:211] offset:64
ds_write_b64 v22,v[212:213] offset:64
ds_write_b64 v23,v[214:215] offset:64
ds_write_b64 v20,v[216:217] offset:128
ds_write_b64 v21,v[218:219] offset:128
ds_write_b64 v22,v[220:221] offset:128
ds_write_b64 v23,v[222:223] offset:128
ds_read_b128 v[72:75],v19 offset:0
ds_read_b128 v[76:79],v19 offset:1024
ds_read_b128 v[80:83],v19 offset:2048
ds_read_b128 v[84:87],v19 offset:3456
ds_read_b128 v[88:91],v19 offset:4480
ds_read_b128 v[92:95],v19 offset:5504
v_add_u32 v20,s54,v20
v_add_u32 v21,s54,v21
v_add_u32 v22,s54,v22
v_add_u32 v23,s54,v23
s_sub_u32 s68,s68,s54
s_add_u32 s59,s59,1572864
s_add_u32 s53,s53,1
s_cmp_eq_u32 s53,3
s_cselect_b32 s59,0x70000000,s59
s_add_u32 s67,s69,98304
s_waitcnt lgkmcnt(5)
buffer_store_dwordx4 v[72:75],v24,s[40:43],s69 offen nt
s_waitcnt lgkmcnt(4)
buffer_store_dwordx4 v[76:79],v25,s[40:43],s69 offen nt
s_waitcnt lgkmcnt(3)
buffer_store_dwordx4 v[80:83],v26,s[40:43],s69 offen nt
s_waitcnt lgkmcnt(2)
buffer_store_dwordx4 v[84:87],v24,s[40:43],s67 offen nt
s_waitcnt lgkmcnt(1)
buffer_store_dwordx4 v[88:91],v25,s[40:43],s67 offen nt
s_waitcnt lgkmcnt(0)
buffer_store_dwordx4 v[92:95],v26,s[40:43],s67 offen nt
v_add_u32 v19,s54,v19
s_add_u32 s69,s69,1572864
s_sub_u32 s54,0,s54
s_cmp_lt_u32 s53,4
s_cbranch_scc1 L_pass0_%=
s_branch L_end_%=
L_pass1_%=:
s_waitcnt vmcnt(6)
s_barrier
ds_read_b128 v[200:203],v4 offset:0
ds_read_b128 v[204:207],v8 offset:0
ds_read_b128 v[208:211],v12 offset:0
ds_read_b128 v[212:215],v5 offset:0
ds_read_b128 v[216:219],v9 offset:0
ds_read_b128 v[220:223],v13 offset:0
ds_read_b128 v[96:99],v18 offset:1920
ds_read_b128 v[100:103],v18 offset:1952
ds_read_b128 v[104:107],v18 offset:1984
ds_read_b128 v[108:111],v18 offset:2016
ds_read_b128 v[112:115],v18 offset:2048
ds_read_b128 v[116:119],v18 offset:2080
ds_read_b128 v[120:123],v18 offset:2112
ds_read_b128 v[124:127],v18 offset:2144
ds_read_b128 v[128:131],v18 offset:2176
ds_read_b128 v[132:135],v18 offset:2208
ds_read_b128 v[136:139],v18 offset:2240
ds_read_b128 v[140:143],v18 offset:2272
v_and_b32 v72,v32,v36
v_and_b32 v73,v33,v36
v_and_b32 v74,v34,v36
v_and_b32 v75,v35,v36
v_and_b32 v76,v32,v48
v_and_b32 v77,v33,v48
v_and_b32 v78,v34,v48
v_and_b32 v79,v35,v48
v_and_b32 v80,v32,v60
v_and_b32 v81,v33,v60
v_and_b32 v82,v34,v60
v_and_b32 v83,v35,v60
v_and_b32 v84,v32,v37
v_and_b32 v85,v33,v37
v_and_b32 v86,v34,v37
v_and_b32 v87,v35,v37
v_and_b32 v88,v32,v49
v_and_b32 v89,v33,v49
v_and_b32 v90,v34,v49
v_and_b32 v91,v35,v49
v_and_b32 v92,v32,v61
v_and_b32 v93,v33,v61
v_and_b32 v94,v34,v61
v_and_b32 v95,v35,v61
s_setprio 1
s_waitcnt lgkmcnt(15)
v_mfma_f32_16x16x32_bf16 v[144:147],v[72:75],v[200:203],0
v_mfma_f32_16x16x32_bf16 v[144:147],v[76:79],v[204:207],v[144:147]
v_mfma_f32_16x16x32_bf16 v[144:147],v[80:83],v[208:211],v[144:147]
ds_read_b128 v[200:203],v6 offset:0
ds_read_b128 v[204:207],v10 offset:0
ds_read_b128 v[208:211],v14 offset:0
s_setprio 0
v_and_b32 v72,v32,v38
v_and_b32 v73,v33,v38
v_and_b32 v74,v34,v38
v_and_b32 v75,v35,v38
v_and_b32 v76,v32,v50
v_and_b32 v77,v33,v50
v_and_b32 v78,v34,v50
v_and_b32 v79,v35,v50
v_and_b32 v80,v32,v62
v_and_b32 v81,v33,v62
v_and_b32 v82,v34,v62
v_and_b32 v83,v35,v62
v_mul_f32 v237,v144,v144
v_mul_f32 v236,v145,v145
v_mul_f32 v239,v146,v146
v_mul_f32 v238,v147,v147
s_setprio 1
s_waitcnt lgkmcnt(15)
v_mfma_f32_16x16x32_bf16 v[148:151],v[84:87],v[212:215],0
v_mfma_f32_16x16x32_bf16 v[148:151],v[88:91],v[216:219],v[148:151]
v_mfma_f32_16x16x32_bf16 v[148:151],v[92:95],v[220:223],v[148:151]
ds_read_b128 v[212:215],v7 offset:0
ds_read_b128 v[216:219],v11 offset:0
ds_read_b128 v[220:223],v15 offset:0
s_setprio 0
v_and_b32 v84,v32,v39
v_and_b32 v85,v33,v39
v_and_b32 v86,v34,v39
v_and_b32 v87,v35,v39
v_and_b32 v88,v32,v51
v_and_b32 v89,v33,v51
v_and_b32 v90,v34,v51
v_and_b32 v91,v35,v51
v_and_b32 v92,v32,v63
v_and_b32 v93,v33,v63
v_and_b32 v94,v34,v63
v_and_b32 v95,v35,v63
v_add_f32 v232,v144,v148
v_fmac_f32 v237,v148,v148
v_add_f32 v233,v145,v149
v_fmac_f32 v236,v149,v149
v_add_f32 v234,v146,v150
v_fmac_f32 v239,v150,v150
v_add_f32 v235,v147,v151
v_fmac_f32 v238,v151,v151
s_setprio 1
s_waitcnt lgkmcnt(3)
v_mfma_f32_16x16x32_bf16 v[152:155],v[72:75],v[200:203],0
v_mfma_f32_16x16x32_bf16 v[152:155],v[76:79],v[204:207],v[152:155]
v_mfma_f32_16x16x32_bf16 v[152:155],v[80:83],v[208:211],v[152:155]
ds_read_b128 v[200:203],v4 offset:64
ds_read_b128 v[204:207],v8 offset:64
ds_read_b128 v[208:211],v12 offset:64
s_setprio 0
v_and_b32 v72,v32,v40
v_and_b32 v73,v33,v40
v_and_b32 v74,v34,v40
v_and_b32 v75,v35,v40
v_and_b32 v76,v32,v52
v_and_b32 v77,v33,v52
v_and_b32 v78,v34,v52
v_and_b32 v79,v35,v52
v_and_b32 v80,v32,v64
v_and_b32 v81,v33,v64
v_and_b32 v82,v34,v64
v_and_b32 v83,v35,v64
v_add_f32 v232,v232,v152
v_fmac_f32 v237,v152,v152
v_add_f32 v233,v233,v153
v_fmac_f32 v236,v153,v153
v_add_f32 v234,v234,v154
v_fmac_f32 v239,v154,v154
v_add_f32 v235,v235,v155
v_fmac_f32 v238,v155,v155
s_setprio 1
s_waitcnt lgkmcnt(3)
v_mfma_f32_16x16x32_bf16 v[156:159],v[84:87],v[212:215],0
v_mfma_f32_16x16x32_bf16 v[156:159],v[88:91],v[216:219],v[156:159]
v_mfma_f32_16x16x32_bf16 v[156:159],v[92:95],v[220:223],v[156:159]
ds_read_b128 v[212:215],v5 offset:64
ds_read_b128 v[216:219],v9 offset:64
ds_read_b128 v[220:223],v13 offset:64
s_setprio 0
v_and_b32 v84,v32,v41
v_and_b32 v85,v33,v41
v_and_b32 v86,v34,v41
v_and_b32 v87,v35,v41
v_and_b32 v88,v32,v53
v_and_b32 v89,v33,v53
v_and_b32 v90,v34,v53
v_and_b32 v91,v35,v53
v_and_b32 v92,v32,v65
v_and_b32 v93,v33,v65
v_and_b32 v94,v34,v65
v_and_b32 v95,v35,v65
v_add_f32 v232,v232,v156
v_fmac_f32 v237,v156,v156
v_add_f32 v233,v233,v157
v_fmac_f32 v236,v157,v157
v_add_f32 v234,v234,v158
v_fmac_f32 v239,v158,v158
v_add_f32 v235,v235,v159
v_fmac_f32 v238,v159,v159
s_setprio 1
s_waitcnt lgkmcnt(3)
v_mfma_f32_16x16x32_bf16 v[160:163],v[72:75],v[200:203],0
v_mfma_f32_16x16x32_bf16 v[160:163],v[76:79],v[204:207],v[160:163]
v_mfma_f32_16x16x32_bf16 v[160:163],v[80:83],v[208:211],v[160:163]
ds_read_b128 v[200:203],v6 offset:64
ds_read_b128 v[204:207],v10 offset:64
ds_read_b128 v[208:211],v14 offset:64
s_setprio 0
v_and_b32 v72,v32,v42
v_and_b32 v73,v33,v42
v_and_b32 v74,v34,v42
v_and_b32 v75,v35,v42
v_and_b32 v76,v32,v54
v_and_b32 v77,v33,v54
v_and_b32 v78,v34,v54
v_and_b32 v79,v35,v54
v_and_b32 v80,v32,v66
v_and_b32 v81,v33,v66
v_and_b32 v82,v34,v66
v_and_b32 v83,v35,v66
v_add_f32 v232,v232,v160
v_fmac_f32 v237,v160,v160
v_add_f32 v233,v233,v161
v_fmac_f32 v236,v161,v161
v_add_f32 v234,v234,v162
v_fmac_f32 v239,v162,v162
v_add_f32 v235,v235,v163
v_fmac_f32 v238,v163,v163
s_setprio 1
s_waitcnt lgkmcnt(3)
v_mfma_f32_16x16x32_bf16 v[164:167],v[84:87],v[212:215],0
v_mfma_f32_16x16x32_bf16 v[164:167],v[88:91],v[216:219],v[164:167]
v_mfma_f32_16x16x32_bf16 v[164:167],v[92:95],v[220:223],v[164:167]
ds_read_b128 v[212:215],v7 offset:64
ds_read_b128 v[216:219],v11 offset:64
ds_read_b128 v[220:223],v15 offset:64
s_setprio 0
v_and_b32 v84,v32,v43
v_and_b32 v85,v33,v43
v_and_b32 v86,v34,v43
v_and_b32 v87,v35,v43
v_and_b32 v88,v32,v55
v_and_b32 v89,v33,v55
v_and_b32 v90,v34,v55
v_and_b32 v91,v35,v55
v_and_b32 v92,v32,v67
v_and_b32 v93,v33,v67
v_and_b32 v94,v34,v67
v_and_b32 v95,v35,v67
v_add_f32 v232,v232,v164
v_fmac_f32 v237,v164,v164
v_add_f32 v233,v233,v165
v_fmac_f32 v236,v165,v165
v_add_f32 v234,v234,v166
v_fmac_f32 v239,v166,v166
v_add_f32 v235,v235,v167
v_fmac_f32 v238,v167,v167
s_setprio 1
s_waitcnt lgkmcnt(3)
v_mfma_f32_16x16x32_bf16 v[168:171],v[72:75],v[200:203],0
v_mfma_f32_16x16x32_bf16 v[168:171],v[76:79],v[204:207],v[168:171]
v_mfma_f32_16x16x32_bf16 v[168:171],v[80:83],v[208:211],v[168:171]
ds_read_b128 v[200:203],v4 offset:128
ds_read_b128 v[204:207],v8 offset:128
ds_read_b128 v[208:211],v12 offset:128
s_setprio 0
v_and_b32 v72,v32,v44
v_and_b32 v73,v33,v44
v_and_b32 v74,v34,v44
v_and_b32 v75,v35,v44
v_and_b32 v76,v32,v56
v_and_b32 v77,v33,v56
v_and_b32 v78,v34,v56
v_and_b32 v79,v35,v56
v_and_b32 v80,v32,v68
v_and_b32 v81,v33,v68
v_and_b32 v82,v34,v68
v_and_b32 v83,v35,v68
v_add_f32 v232,v232,v168
v_fmac_f32 v237,v168,v168
v_add_f32 v233,v233,v169
v_fmac_f32 v236,v169,v169
v_add_f32 v234,v234,v170
v_fmac_f32 v239,v170,v170
v_add_f32 v235,v235,v171
v_fmac_f32 v238,v171,v171
s_setprio 1
s_waitcnt lgkmcnt(3)
v_mfma_f32_16x16x32_bf16 v[172:175],v[84:87],v[212:215],0
v_mfma_f32_16x16x32_bf16 v[172:175],v[88:91],v[216:219],v[172:175]
v_mfma_f32_16x16x32_bf16 v[172:175],v[92:95],v[220:223],v[172:175]
ds_read_b128 v[212:215],v5 offset:128
ds_read_b128 v[216:219],v9 offset:128
ds_read_b128 v[220:223],v13 offset:128
s_setprio 0
v_and_b32 v84,v32,v45
v_and_b32 v85,v33,v45
v_and_b32 v86,v34,v45
v_and_b32 v87,v35,v45
v_and_b32 v88,v32,v57
v_and_b32 v89,v33,v57
v_and_b32 v90,v34,v57
v_and_b32 v91,v35,v57
v_and_b32 v92,v32,v69
v_and_b32 v93,v33,v69
v_and_b32 v94,v34,v69
v_and_b32 v95,v35,v69
v_add_f32 v232,v232,v172
v_fmac_f32 v237,v172,v172
v_add_f32 v233,v233,v173
v_fmac_f32 v236,v173,v173
v_add_f32 v234,v234,v174
v_fmac_f32 v239,v174,v174
v_add_f32 v235,v235,v175
v_fmac_f32 v238,v175,v175
s_setprio 1
s_waitcnt lgkmcnt(3)
v_mfma_f32_16x16x32_bf16 v[176:179],v[72:75],v[200:203],0
v_mfma_f32_16x16x32_bf16 v[176:179],v[76:79],v[204:207],v[176:179]
v_mfma_f32_16x16x32_bf16 v[176:179],v[80:83],v[208:211],v[176:179]
ds_read_b128 v[200:203],v6 offset:128
ds_read_b128 v[204:207],v10 offset:128
ds_read_b128 v[208:211],v14 offset:128
s_setprio 0
v_and_b32 v72,v32,v46
v_and_b32 v73,v33,v46
v_and_b32 v74,v34,v46
v_and_b32 v75,v35,v46
v_and_b32 v76,v32,v58
v_and_b32 v77,v33,v58
v_and_b32 v78,v34,v58
v_and_b32 v79,v35,v58
v_and_b32 v80,v32,v70
v_and_b32 v81,v33,v70
v_and_b32 v82,v34,v70
v_and_b32 v83,v35,v70
v_add_f32 v232,v232,v176
v_fmac_f32 v237,v176,v176
v_add_f32 v233,v233,v177
v_fmac_f32 v236,v177,v177
v_add_f32 v234,v234,v178
v_fmac_f32 v239,v178,v178
v_add_f32 v235,v235,v179
v_fmac_f32 v238,v179,v179
s_setprio 1
s_waitcnt lgkmcnt(3)
v_mfma_f32_16x16x32_bf16 v[180:183],v[84:87],v[212:215],0
v_mfma_f32_16x16x32_bf16 v[180:183],v[88:91],v[216:219],v[180:183]
v_mfma_f32_16x16x32_bf16 v[180:183],v[92:95],v[220:223],v[180:183]
ds_read_b128 v[212:215],v7 offset:128
ds_read_b128 v[216:219],v11 offset:128
ds_read_b128 v[220:223],v15 offset:128
s_setprio 0
v_and_b32 v84,v32,v47
v_and_b32 v85,v33,v47
v_and_b32 v86,v34,v47
v_and_b32 v87,v35,v47
v_and_b32 v88,v32,v59
v_and_b32 v89,v33,v59
v_and_b32 v90,v34,v59
v_and_b32 v91,v35,v59
v_and_b32 v92,v32,v71
v_and_b32 v93,v33,v71
v_and_b32 v94,v34,v71
v_and_b32 v95,v35,v71
v_add_f32 v232,v232,v180
v_fmac_f32 v237,v180,v180
v_add_f32 v233,v233,v181
v_fmac_f32 v236,v181,v181
v_add_f32 v234,v234,v182
v_fmac_f32 v239,v182,v182
v_add_f32 v235,v235,v183
v_fmac_f32 v238,v183,v183
s_setprio 1
s_waitcnt lgkmcnt(3)
v_mfma_f32_16x16x32_bf16 v[184:187],v[72:75],v[200:203],0
v_mfma_f32_16x16x32_bf16 v[184:187],v[76:79],v[204:207],v[184:187]
v_mfma_f32_16x16x32_bf16 v[184:187],v[80:83],v[208:211],v[184:187]
s_setprio 0
s_nop 7
v_add_f32 v232,v232,v184
v_fmac_f32 v237,v184,v184
v_add_f32 v233,v233,v185
v_fmac_f32 v236,v185,v185
v_add_f32 v234,v234,v186
v_fmac_f32 v239,v186,v186
v_add_f32 v235,v235,v187
v_fmac_f32 v238,v187,v187
s_setprio 1
s_waitcnt lgkmcnt(0)
v_mfma_f32_16x16x32_bf16 v[188:191],v[84:87],v[212:215],0
v_mfma_f32_16x16x32_bf16 v[188:191],v[88:91],v[216:219],v[188:191]
v_mfma_f32_16x16x32_bf16 v[188:191],v[92:95],v[220:223],v[188:191]
s_setprio 0
ds_read_b64_tr_b16 v[200:201],v16 offset:0
ds_read_b64_tr_b16 v[202:203],v16 offset:512
ds_read_b64_tr_b16 v[204:205],v16 offset:1024
ds_read_b64_tr_b16 v[206:207],v16 offset:1536
ds_read_b64_tr_b16 v[208:209],v16 offset:2048
ds_read_b64_tr_b16 v[210:211],v16 offset:2560
ds_read_b64_tr_b16 v[212:213],v16 offset:3072
ds_read_b64_tr_b16 v[214:215],v16 offset:3584
ds_read_b64_tr_b16 v[216:217],v16 offset:4096
ds_read_b64_tr_b16 v[218:219],v16 offset:4608
ds_read_b64_tr_b16 v[220:221],v16 offset:5120
ds_read_b64_tr_b16 v[222:223],v16 offset:5632
v_add_f32 v232,v232,v188
v_fmac_f32 v237,v188,v188
v_add_f32 v233,v233,v189
v_fmac_f32 v236,v189,v189
v_add_f32 v234,v234,v190
v_fmac_f32 v239,v190,v190
v_add_f32 v235,v235,v191
v_fmac_f32 v238,v191,v191
v_add_f32 v232,v232,v233
v_add_f32 v234,v234,v235
v_add_f32 v237,v237,v236
v_add_f32 v239,v239,v238
v_add_f32 v240,v232,v234
v_add_f32 v241,v237,v239
s_nop 1
v_permlane32_swap_b32 v240,v241
v_add_f32 v240,v240,v241
v_mov_b32 v241,v240
s_nop 1
v_permlane32_swap_b32 v240,v241
v_mul_f32 v240,0x3c2aaaab,v240
v_mul_f32 v241,0x3c2aaaab,v241
v_fma_f32 v241,-v240,v240,v241
v_max_f32 v241,0,v241
v_add_f32 v241,0x3727c5ac,v241
v_rsq_f32 v241,v241
s_nop 0
v_mul_f32 v242,0xbeeff558,v241
v_mul_f32 v243,0x3eeff558,v241
v_mul_f32 v243,v240,v243
v_fma_f32 v144,v144,v242,v243
v_fma_f32 v145,v145,v242,v243
v_fma_f32 v146,v146,v242,v243
v_fma_f32 v147,v147,v242,v243
v_fma_f32 v148,v148,v242,v243
v_fma_f32 v149,v149,v242,v243
v_fma_f32 v150,v150,v242,v243
v_fma_f32 v151,v151,v242,v243
v_fma_f32 v152,v152,v242,v243
v_fma_f32 v153,v153,v242,v243
v_fma_f32 v154,v154,v242,v243
v_fma_f32 v155,v155,v242,v243
v_fma_f32 v156,v156,v242,v243
v_fma_f32 v157,v157,v242,v243
v_fma_f32 v158,v158,v242,v243
v_fma_f32 v159,v159,v242,v243
v_fma_f32 v160,v160,v242,v243
v_fma_f32 v161,v161,v242,v243
v_fma_f32 v162,v162,v242,v243
v_fma_f32 v163,v163,v242,v243
v_fma_f32 v164,v164,v242,v243
v_fma_f32 v165,v165,v242,v243
v_fma_f32 v166,v166,v242,v243
v_fma_f32 v167,v167,v242,v243
v_fma_f32 v168,v168,v242,v243
v_fma_f32 v169,v169,v242,v243
v_fma_f32 v170,v170,v242,v243
v_fma_f32 v171,v171,v242,v243
v_fma_f32 v172,v172,v242,v243
v_fma_f32 v173,v173,v242,v243
v_fma_f32 v174,v174,v242,v243
v_fma_f32 v175,v175,v242,v243
v_fma_f32 v176,v176,v242,v243
v_fma_f32 v177,v177,v242,v243
v_fma_f32 v178,v178,v242,v243
v_fma_f32 v179,v179,v242,v243
v_fma_f32 v180,v180,v242,v243
v_fma_f32 v181,v181,v242,v243
v_fma_f32 v182,v182,v242,v243
v_fma_f32 v183,v183,v242,v243
v_fma_f32 v184,v184,v242,v243
v_fma_f32 v185,v185,v242,v243
v_fma_f32 v186,v186,v242,v243
v_fma_f32 v187,v187,v242,v243
v_fma_f32 v188,v188,v242,v243
v_fma_f32 v189,v189,v242,v243
v_fma_f32 v190,v190,v242,v243
v_fma_f32 v191,v191,v242,v243
s_setprio 1
v_cvt_pk_bf16_f32 v72,v144,v145
v_cvt_pk_bf16_f32 v73,v146,v147
v_cvt_pk_bf16_f32 v74,v148,v149
v_cvt_pk_bf16_f32 v75,v150,v151
v_cvt_pk_bf16_f32 v76,v152,v153
v_cvt_pk_bf16_f32 v77,v154,v155
v_cvt_pk_bf16_f32 v78,v156,v157
v_cvt_pk_bf16_f32 v79,v158,v159
ds_read_b128 v[144:147],v18 offset:0
ds_read_b128 v[148:151],v18 offset:32
ds_read_b128 v[152:155],v18 offset:64
ds_read_b128 v[156:159],v18 offset:96
v_cvt_pk_bf16_f32 v80,v160,v161
v_cvt_pk_bf16_f32 v81,v162,v163
v_cvt_pk_bf16_f32 v82,v164,v165
v_cvt_pk_bf16_f32 v83,v166,v167
v_cvt_pk_bf16_f32 v84,v168,v169
v_cvt_pk_bf16_f32 v85,v170,v171
v_cvt_pk_bf16_f32 v86,v172,v173
v_cvt_pk_bf16_f32 v87,v174,v175
s_waitcnt lgkmcnt(0)
v_mfma_f32_32x32x16_bf16 v[144:159],v[200:203],v[72:75],v[144:159]
v_cvt_pk_bf16_f32 v88,v176,v177
v_cvt_pk_bf16_f32 v89,v178,v179
v_cvt_pk_bf16_f32 v90,v180,v181
v_cvt_pk_bf16_f32 v91,v182,v183
v_mfma_f32_32x32x16_bf16 v[144:159],v[204:207],v[76:79],v[144:159]
v_cvt_pk_bf16_f32 v92,v184,v185
v_cvt_pk_bf16_f32 v93,v186,v187
v_cvt_pk_bf16_f32 v94,v188,v189
v_cvt_pk_bf16_f32 v95,v190,v191
v_mfma_f32_32x32x16_bf16 v[144:159],v[208:211],v[80:83],v[144:159]
v_mfma_f32_32x32x16_bf16 v[144:159],v[212:215],v[84:87],v[144:159]
v_mfma_f32_32x32x16_bf16 v[144:159],v[216:219],v[88:91],v[144:159]
v_mfma_f32_32x32x16_bf16 v[144:159],v[220:223],v[92:95],v[144:159]
s_setprio 0
s_nop 7
s_nop 1
s_waitcnt vmcnt(0)
s_barrier
v_fmaak_f32 v176,v144,v144,0x409d30fc
v_fmaak_f32 v177,v145,v145,0x409d30fc
v_fmaak_f32 v178,v146,v146,0x409d30fc
v_fmaak_f32 v179,v147,v147,0x409d30fc
v_fmaak_f32 v180,v148,v148,0x409d30fc
v_fmaak_f32 v181,v149,v149,0x409d30fc
v_fmaak_f32 v182,v150,v150,0x409d30fc
v_fmaak_f32 v183,v151,v151,0x409d30fc
v_fmaak_f32 v184,v152,v152,0x409d30fc
v_fmaak_f32 v185,v153,v153,0x409d30fc
v_fmaak_f32 v186,v154,v154,0x409d30fc
v_fmaak_f32 v187,v155,v155,0x409d30fc
v_fmaak_f32 v188,v156,v156,0x409d30fc
v_fmaak_f32 v189,v157,v157,0x409d30fc
v_fmaak_f32 v190,v158,v158,0x409d30fc
v_fmaak_f32 v191,v159,v159,0x409d30fc
v_mul_f32 v176,v144,v176
v_mul_f32 v177,v145,v177
v_mul_f32 v178,v146,v178
v_mul_f32 v179,v147,v179
v_mul_f32 v180,v148,v180
v_mul_f32 v181,v149,v181
v_mul_f32 v182,v150,v182
v_mul_f32 v183,v151,v183
v_mul_f32 v184,v152,v184
v_mul_f32 v185,v153,v185
v_mul_f32 v186,v154,v186
v_mul_f32 v187,v155,v187
v_mul_f32 v188,v156,v188
v_mul_f32 v189,v157,v189
v_mul_f32 v190,v158,v190
v_mul_f32 v191,v159,v191
s_setprio 1
v_exp_f32 v176,v176
v_exp_f32 v177,v177
v_exp_f32 v178,v178
v_exp_f32 v179,v179
v_exp_f32 v180,v180
v_exp_f32 v181,v181
v_exp_f32 v182,v182
v_exp_f32 v183,v183
v_exp_f32 v184,v184
v_exp_f32 v185,v185
v_exp_f32 v186,v186
v_exp_f32 v187,v187
v_exp_f32 v188,v188
v_exp_f32 v189,v189
v_exp_f32 v190,v190
v_exp_f32 v191,v191
ds_read_b64_tr_b16 v[200:201],v16 offset:6144
ds_read_b64_tr_b16 v[202:203],v16 offset:6656
ds_read_b64_tr_b16 v[204:205],v16 offset:7168
ds_read_b64_tr_b16 v[206:207],v16 offset:7680
ds_read_b64_tr_b16 v[208:209],v16 offset:8192
ds_read_b64_tr_b16 v[210:211],v16 offset:8704
ds_read_b64_tr_b16 v[212:213],v16 offset:9216
ds_read_b64_tr_b16 v[214:215],v16 offset:9728
ds_read_b64_tr_b16 v[216:217],v16 offset:10240
ds_read_b64_tr_b16 v[218:219],v16 offset:10752
ds_read_b64_tr_b16 v[220:221],v16 offset:11264
ds_read_b64_tr_b16 v[222:223],v16 offset:11776
ds_read_b128 v[160:163],v18 offset:128
ds_read_b128 v[164:167],v18 offset:160
ds_read_b128 v[168:171],v18 offset:192
ds_read_b128 v[172:175],v18 offset:224
s_mov_b64 exec,s[62:63]
s_add_u32 m0,s68,0
s_add_u32 s64,s59,0
buffer_load_dwordx4 v27,s[36:39],s64 offen lds
s_mov_b64 exec,-1
s_setprio 0
v_add_f32 v176,1.0,v176
v_add_f32 v177,1.0,v177
v_add_f32 v178,1.0,v178
v_add_f32 v179,1.0,v179
v_add_f32 v180,1.0,v180
v_add_f32 v181,1.0,v181
v_add_f32 v182,1.0,v182
v_add_f32 v183,1.0,v183
v_add_f32 v184,1.0,v184
v_add_f32 v185,1.0,v185
v_add_f32 v186,1.0,v186
v_add_f32 v187,1.0,v187
v_add_f32 v188,1.0,v188
v_add_f32 v189,1.0,v189
v_add_f32 v190,1.0,v190
v_add_f32 v191,1.0,v191
s_setprio 1
s_waitcnt lgkmcnt(0)
v_mfma_f32_32x32x16_bf16 v[160:175],v[200:203],v[72:75],v[160:175]
v_rcp_f32 v176,v176
v_rcp_f32 v177,v177
v_rcp_f32 v178,v178
v_mfma_f32_32x32x16_bf16 v[160:175],v[204:207],v[76:79],v[160:175]
v_rcp_f32 v179,v179
v_rcp_f32 v180,v180
v_rcp_f32 v181,v181
v_mfma_f32_32x32x16_bf16 v[160:175],v[208:211],v[80:83],v[160:175]
v_rcp_f32 v182,v182
v_rcp_f32 v183,v183
v_rcp_f32 v184,v184
v_mfma_f32_32x32x16_bf16 v[160:175],v[212:215],v[84:87],v[160:175]
v_rcp_f32 v185,v185
v_rcp_f32 v186,v186
v_rcp_f32 v187,v187
v_mfma_f32_32x32x16_bf16 v[160:175],v[216:219],v[88:91],v[160:175]
v_rcp_f32 v188,v188
v_rcp_f32 v189,v189
v_mfma_f32_32x32x16_bf16 v[160:175],v[220:223],v[92:95],v[160:175]
v_rcp_f32 v190,v190
v_rcp_f32 v191,v191
ds_read_b64_tr_b16 v[200:201],v17 offset:0
ds_read_b64_tr_b16 v[202:203],v17 offset:1536
ds_read_b64_tr_b16 v[204:205],v17 offset:3072
ds_read_b64_tr_b16 v[206:207],v17 offset:4608
ds_read_b64_tr_b16 v[208:209],v17 offset:64
ds_read_b64_tr_b16 v[210:211],v17 offset:1600
ds_read_b64_tr_b16 v[212:213],v17 offset:3136
ds_read_b64_tr_b16 v[214:215],v17 offset:4672
ds_read_b64_tr_b16 v[216:217],v17 offset:128
ds_read_b64_tr_b16 v[218:219],v17 offset:1664
ds_read_b64_tr_b16 v[220:221],v17 offset:3200
ds_read_b64_tr_b16 v[222:223],v17 offset:4736
s_mov_b64 exec,s[62:63]
s_add_u32 m0,s68,3456
s_add_u32 s64,s59,98304
buffer_load_dwordx4 v27,s[36:39],s64 offen lds
s_mov_b64 exec,-1
s_setprio 0
v_mul_f32 v176,v144,v176
v_mul_f32 v177,v145,v177
v_mul_f32 v178,v146,v178
v_mul_f32 v179,v147,v179
v_mul_f32 v180,v148,v180
v_mul_f32 v181,v149,v181
v_mul_f32 v182,v150,v182
v_mul_f32 v183,v151,v183
v_mul_f32 v184,v152,v184
v_mul_f32 v185,v153,v185
v_mul_f32 v186,v154,v186
v_mul_f32 v187,v155,v187
v_mul_f32 v188,v156,v188
v_mul_f32 v189,v157,v189
v_mul_f32 v190,v158,v190
v_mul_f32 v191,v159,v191
v_cvt_pk_bf16_f32 v192,v176,v177
v_cvt_pk_bf16_f32 v193,v178,v179
v_cvt_pk_bf16_f32 v194,v180,v181
v_cvt_pk_bf16_f32 v195,v182,v183
v_cvt_pk_bf16_f32 v196,v184,v185
v_cvt_pk_bf16_f32 v197,v186,v187
v_cvt_pk_bf16_f32 v198,v188,v189
v_cvt_pk_bf16_f32 v199,v190,v191
s_waitcnt lgkmcnt(0)
s_barrier
v_fmaak_f32 v176,v160,v160,0x409d30fc
v_fmaak_f32 v177,v161,v161,0x409d30fc
v_fmaak_f32 v178,v162,v162,0x409d30fc
v_fmaak_f32 v179,v163,v163,0x409d30fc
v_fmaak_f32 v180,v164,v164,0x409d30fc
v_fmaak_f32 v181,v165,v165,0x409d30fc
v_fmaak_f32 v182,v166,v166,0x409d30fc
v_fmaak_f32 v183,v167,v167,0x409d30fc
v_fmaak_f32 v184,v168,v168,0x409d30fc
v_fmaak_f32 v185,v169,v169,0x409d30fc
v_fmaak_f32 v186,v170,v170,0x409d30fc
v_fmaak_f32 v187,v171,v171,0x409d30fc
v_fmaak_f32 v188,v172,v172,0x409d30fc
v_fmaak_f32 v189,v173,v173,0x409d30fc
v_fmaak_f32 v190,v174,v174,0x409d30fc
v_fmaak_f32 v191,v175,v175,0x409d30fc
v_mul_f32 v176,v160,v176
v_mul_f32 v177,v161,v177
v_mul_f32 v178,v162,v178
v_mul_f32 v179,v163,v179
v_mul_f32 v180,v164,v180
v_mul_f32 v181,v165,v181
v_mul_f32 v182,v166,v182
v_mul_f32 v183,v167,v183
v_mul_f32 v184,v168,v184
v_mul_f32 v185,v169,v185
v_mul_f32 v186,v170,v186
v_mul_f32 v187,v171,v187
v_mul_f32 v188,v172,v188
v_mul_f32 v189,v173,v189
v_mul_f32 v190,v174,v190
v_mul_f32 v191,v175,v191
s_setprio 1
v_mfma_f32_32x32x16_bf16 v[96:111],v[200:203],v[192:195],v[96:111]
v_exp_f32 v176,v176
v_exp_f32 v177,v177
v_exp_f32 v178,v178
v_mfma_f32_32x32x16_bf16 v[96:111],v[204:207],v[196:199],v[96:111]
v_exp_f32 v179,v179
v_exp_f32 v180,v180
v_exp_f32 v181,v181
v_mfma_f32_32x32x16_bf16 v[112:127],v[208:211],v[192:195],v[112:127]
v_exp_f32 v182,v182
v_exp_f32 v183,v183
v_exp_f32 v184,v184
v_mfma_f32_32x32x16_bf16 v[112:127],v[212:215],v[196:199],v[112:127]
v_exp_f32 v185,v185
v_exp_f32 v186,v186
v_exp_f32 v187,v187
v_mfma_f32_32x32x16_bf16 v[128:143],v[216:219],v[192:195],v[128:143]
v_exp_f32 v188,v188
v_exp_f32 v189,v189
v_mfma_f32_32x32x16_bf16 v[128:143],v[220:223],v[196:199],v[128:143]
v_exp_f32 v190,v190
v_exp_f32 v191,v191
ds_read_b64_tr_b16 v[200:201],v16 offset:12288
ds_read_b64_tr_b16 v[202:203],v16 offset:12800
ds_read_b64_tr_b16 v[204:205],v16 offset:13312
ds_read_b64_tr_b16 v[206:207],v16 offset:13824
ds_read_b64_tr_b16 v[208:209],v16 offset:14336
ds_read_b64_tr_b16 v[210:211],v16 offset:14848
ds_read_b64_tr_b16 v[212:213],v16 offset:15360
ds_read_b64_tr_b16 v[214:215],v16 offset:15872
ds_read_b64_tr_b16 v[216:217],v16 offset:16384
ds_read_b64_tr_b16 v[218:219],v16 offset:16896
ds_read_b64_tr_b16 v[220:221],v16 offset:17408
ds_read_b64_tr_b16 v[222:223],v16 offset:17920
ds_read_b128 v[144:147],v18 offset:256
ds_read_b128 v[148:151],v18 offset:288
ds_read_b128 v[152:155],v18 offset:320
ds_read_b128 v[156:159],v18 offset:352
s_mov_b64 exec,s[62:63]
s_add_u32 m0,s68,6912
s_add_u32 s64,s59,196608
buffer_load_dwordx4 v27,s[36:39],s64 offen lds
s_mov_b64 exec,-1
s_setprio 0
v_add_f32 v176,1.0,v176
v_add_f32 v177,1.0,v177
v_add_f32 v178,1.0,v178
v_add_f32 v179,1.0,v179
v_add_f32 v180,1.0,v180
v_add_f32 v181,1.0,v181
v_add_f32 v182,1.0,v182
v_add_f32 v183,1.0,v183
v_add_f32 v184,1.0,v184
v_add_f32 v185,1.0,v185
v_add_f32 v186,1.0,v186
v_add_f32 v187,1.0,v187
v_add_f32 v188,1.0,v188
v_add_f32 v189,1.0,v189
v_add_f32 v190,1.0,v190
v_add_f32 v191,1.0,v191
s_setprio 1
s_waitcnt lgkmcnt(0)
v_mfma_f32_32x32x16_bf16 v[144:159],v[200:203],v[72:75],v[144:159]
v_rcp_f32 v176,v176
v_rcp_f32 v177,v177
v_rcp_f32 v178,v178
v_mfma_f32_32x32x16_bf16 v[144:159],v[204:207],v[76:79],v[144:159]
v_rcp_f32 v179,v179
v_rcp_f32 v180,v180
v_rcp_f32 v181,v181
v_mfma_f32_32x32x16_bf16 v[144:159],v[208:211],v[80:83],v[144:159]
v_rcp_f32 v182,v182
v_rcp_f32 v183,v183
v_rcp_f32 v184,v184
v_mfma_f32_32x32x16_bf16 v[144:159],v[212:215],v[84:87],v[144:159]
v_rcp_f32 v185,v185
v_rcp_f32 v186,v186
v_rcp_f32 v187,v187
v_mfma_f32_32x32x16_bf16 v[144:159],v[216:219],v[88:91],v[144:159]
v_rcp_f32 v188,v188
v_rcp_f32 v189,v189
v_mfma_f32_32x32x16_bf16 v[144:159],v[220:223],v[92:95],v[144:159]
v_rcp_f32 v190,v190
v_rcp_f32 v191,v191
ds_read_b64_tr_b16 v[200:201],v17 offset:6144
ds_read_b64_tr_b16 v[202:203],v17 offset:7680
ds_read_b64_tr_b16 v[204:205],v17 offset:9216
ds_read_b64_tr_b16 v[206:207],v17 offset:10752
ds_read_b64_tr_b16 v[208:209],v17 offset:6208
ds_read_b64_tr_b16 v[210:211],v17 offset:7744
ds_read_b64_tr_b16 v[212:213],v17 offset:9280
ds_read_b64_tr_b16 v[214:215],v17 offset:10816
ds_read_b64_tr_b16 v[216:217],v17 offset:6272
ds_read_b64_tr_b16 v[218:219],v17 offset:7808
ds_read_b64_tr_b16 v[220:221],v17 offset:9344
ds_read_b64_tr_b16 v[222:223],v17 offset:10880
s_mov_b64 exec,s[62:63]
s_add_u32 m0,s68,10368
s_add_u32 s64,s59,294912
buffer_load_dwordx4 v27,s[36:39],s64 offen lds
s_mov_b64 exec,-1
s_setprio 0
v_mul_f32 v176,v160,v176
v_mul_f32 v177,v161,v177
v_mul_f32 v178,v162,v178
v_mul_f32 v179,v163,v179
v_mul_f32 v180,v164,v180
v_mul_f32 v181,v165,v181
v_mul_f32 v182,v166,v182
v_mul_f32 v183,v167,v183
v_mul_f32 v184,v168,v184
v_mul_f32 v185,v169,v185
v_mul_f32 v186,v170,v186
v_mul_f32 v187,v171,v187
v_mul_f32 v188,v172,v188
v_mul_f32 v189,v173,v189
v_mul_f32 v190,v174,v190
v_mul_f32 v191,v175,v191
v_cvt_pk_bf16_f32 v192,v176,v177
v_cvt_pk_bf16_f32 v193,v178,v179
v_cvt_pk_bf16_f32 v194,v180,v181
v_cvt_pk_bf16_f32 v195,v182,v183
v_cvt_pk_bf16_f32 v196,v184,v185
v_cvt_pk_bf16_f32 v197,v186,v187
v_cvt_pk_bf16_f32 v198,v188,v189
v_cvt_pk_bf16_f32 v199,v190,v191
s_waitcnt lgkmcnt(0)
s_barrier
v_fmaak_f32 v176,v144,v144,0x409d30fc
v_fmaak_f32 v177,v145,v145,0x409d30fc
v_fmaak_f32 v178,v146,v146,0x409d30fc
v_fmaak_f32 v179,v147,v147,0x409d30fc
v_fmaak_f32 v180,v148,v148,0x409d30fc
v_fmaak_f32 v181,v149,v149,0x409d30fc
v_fmaak_f32 v182,v150,v150,0x409d30fc
v_fmaak_f32 v183,v151,v151,0x409d30fc
v_fmaak_f32 v184,v152,v152,0x409d30fc
v_fmaak_f32 v185,v153,v153,0x409d30fc
v_fmaak_f32 v186,v154,v154,0x409d30fc
v_fmaak_f32 v187,v155,v155,0x409d30fc
v_fmaak_f32 v188,v156,v156,0x409d30fc
v_fmaak_f32 v189,v157,v157,0x409d30fc
v_fmaak_f32 v190,v158,v158,0x409d30fc
v_fmaak_f32 v191,v159,v159,0x409d30fc
v_mul_f32 v176,v144,v176
v_mul_f32 v177,v145,v177
v_mul_f32 v178,v146,v178
v_mul_f32 v179,v147,v179
v_mul_f32 v180,v148,v180
v_mul_f32 v181,v149,v181
v_mul_f32 v182,v150,v182
v_mul_f32 v183,v151,v183
v_mul_f32 v184,v152,v184
v_mul_f32 v185,v153,v185
v_mul_f32 v186,v154,v186
v_mul_f32 v187,v155,v187
v_mul_f32 v188,v156,v188
v_mul_f32 v189,v157,v189
v_mul_f32 v190,v158,v190
v_mul_f32 v191,v159,v191
s_setprio 1
v_mfma_f32_32x32x16_bf16 v[96:111],v[200:203],v[192:195],v[96:111]
v_exp_f32 v176,v176
v_exp_f32 v177,v177
v_exp_f32 v178,v178
v_mfma_f32_32x32x16_bf16 v[96:111],v[204:207],v[196:199],v[96:111]
v_exp_f32 v179,v179
v_exp_f32 v180,v180
v_exp_f32 v181,v181
v_mfma_f32_32x32x16_bf16 v[112:127],v[208:211],v[192:195],v[112:127]
v_exp_f32 v182,v182
v_exp_f32 v183,v183
v_exp_f32 v184,v184
v_mfma_f32_32x32x16_bf16 v[112:127],v[212:215],v[196:199],v[112:127]
v_exp_f32 v185,v185
v_exp_f32 v186,v186
v_exp_f32 v187,v187
v_mfma_f32_32x32x16_bf16 v[128:143],v[216:219],v[192:195],v[128:143]
v_exp_f32 v188,v188
v_exp_f32 v189,v189
v_mfma_f32_32x32x16_bf16 v[128:143],v[220:223],v[196:199],v[128:143]
v_exp_f32 v190,v190
v_exp_f32 v191,v191
ds_read_b64_tr_b16 v[200:201],v16 offset:0
ds_read_b64_tr_b16 v[202:203],v16 offset:512
ds_read_b64_tr_b16 v[204:205],v16 offset:1024
ds_read_b64_tr_b16 v[206:207],v16 offset:1536
ds_read_b64_tr_b16 v[208:209],v16 offset:2048
ds_read_b64_tr_b16 v[210:211],v16 offset:2560
ds_read_b64_tr_b16 v[212:213],v16 offset:3072
ds_read_b64_tr_b16 v[214:215],v16 offset:3584
ds_read_b64_tr_b16 v[216:217],v16 offset:4096
ds_read_b64_tr_b16 v[218:219],v16 offset:4608
ds_read_b64_tr_b16 v[220:221],v16 offset:5120
ds_read_b64_tr_b16 v[222:223],v16 offset:5632
ds_read_b128 v[160:163],v18 offset:384
ds_read_b128 v[164:167],v18 offset:416
ds_read_b128 v[168:171],v18 offset:448
ds_read_b128 v[172:175],v18 offset:480
s_mov_b64 exec,s[62:63]
s_add_u32 m0,s68,13824
s_add_u32 s64,s59,393216
buffer_load_dwordx4 v27,s[36:39],s64 offen lds
s_mov_b64 exec,-1
s_setprio 0
v_add_f32 v176,1.0,v176
v_add_f32 v177,1.0,v177
v_add_f32 v178,1.0,v178
v_add_f32 v179,1.0,v179
v_add_f32 v180,1.0,v180
v_add_f32 v181,1.0,v181
v_add_f32 v182,1.0,v182
v_add_f32 v183,1.0,v183
v_add_f32 v184,1.0,v184
v_add_f32 v185,1.0,v185
v_add_f32 v186,1.0,v186
v_add_f32 v187,1.0,v187
v_add_f32 v188,1.0,v188
v_add_f32 v189,1.0,v189
v_add_f32 v190,1.0,v190
v_add_f32 v191,1.0,v191
s_setprio 1
s_waitcnt lgkmcnt(0)
v_mfma_f32_32x32x16_bf16 v[160:175],v[200:203],v[72:75],v[160:175]
v_rcp_f32 v176,v176
v_rcp_f32 v177,v177
v_rcp_f32 v178,v178
v_mfma_f32_32x32x16_bf16 v[160:175],v[204:207],v[76:79],v[160:175]
v_rcp_f32 v179,v179
v_rcp_f32 v180,v180
v_rcp_f32 v181,v181
v_mfma_f32_32x32x16_bf16 v[160:175],v[208:211],v[80:83],v[160:175]
v_rcp_f32 v182,v182
v_rcp_f32 v183,v183
v_rcp_f32 v184,v184
v_mfma_f32_32x32x16_bf16 v[160:175],v[212:215],v[84:87],v[160:175]
v_rcp_f32 v185,v185
v_rcp_f32 v186,v186
v_rcp_f32 v187,v187
v_mfma_f32_32x32x16_bf16 v[160:175],v[216:219],v[88:91],v[160:175]
v_rcp_f32 v188,v188
v_rcp_f32 v189,v189
v_mfma_f32_32x32x16_bf16 v[160:175],v[220:223],v[92:95],v[160:175]
v_rcp_f32 v190,v190
v_rcp_f32 v191,v191
ds_read_b64_tr_b16 v[200:201],v17 offset:12288
ds_read_b64_tr_b16 v[202:203],v17 offset:13824
ds_read_b64_tr_b16 v[204:205],v17 offset:15360
ds_read_b64_tr_b16 v[206:207],v17 offset:16896
ds_read_b64_tr_b16 v[208:209],v17 offset:12352
ds_read_b64_tr_b16 v[210:211],v17 offset:13888
ds_read_b64_tr_b16 v[212:213],v17 offset:15424
ds_read_b64_tr_b16 v[214:215],v17 offset:16960
ds_read_b64_tr_b16 v[216:217],v17 offset:12416
ds_read_b64_tr_b16 v[218:219],v17 offset:13952
ds_read_b64_tr_b16 v[220:221],v17 offset:15488
ds_read_b64_tr_b16 v[222:223],v17 offset:17024
s_mov_b64 exec,s[62:63]
s_add_u32 m0,s68,17280
s_add_u32 s64,s59,491520
buffer_load_dwordx4 v27,s[36:39],s64 offen lds
s_mov_b64 exec,-1
s_setprio 0
v_mul_f32 v176,v144,v176
v_mul_f32 v177,v145,v177
v_mul_f32 v178,v146,v178
v_mul_f32 v179,v147,v179
v_mul_f32 v180,v148,v180
v_mul_f32 v181,v149,v181
v_mul_f32 v182,v150,v182
v_mul_f32 v183,v151,v183
v_mul_f32 v184,v152,v184
v_mul_f32 v185,v153,v185
v_mul_f32 v186,v154,v186
v_mul_f32 v187,v155,v187
v_mul_f32 v188,v156,v188
v_mul_f32 v189,v157,v189
v_mul_f32 v190,v158,v190
v_mul_f32 v191,v159,v191
v_cvt_pk_bf16_f32 v192,v176,v177
v_cvt_pk_bf16_f32 v193,v178,v179
v_cvt_pk_bf16_f32 v194,v180,v181
v_cvt_pk_bf16_f32 v195,v182,v183
v_cvt_pk_bf16_f32 v196,v184,v185
v_cvt_pk_bf16_f32 v197,v186,v187
v_cvt_pk_bf16_f32 v198,v188,v189
v_cvt_pk_bf16_f32 v199,v190,v191
s_waitcnt lgkmcnt(0)
s_barrier
v_fmaak_f32 v176,v160,v160,0x409d30fc
v_fmaak_f32 v177,v161,v161,0x409d30fc
v_fmaak_f32 v178,v162,v162,0x409d30fc
v_fmaak_f32 v179,v163,v163,0x409d30fc
v_fmaak_f32 v180,v164,v164,0x409d30fc
v_fmaak_f32 v181,v165,v165,0x409d30fc
v_fmaak_f32 v182,v166,v166,0x409d30fc
v_fmaak_f32 v183,v167,v167,0x409d30fc
v_fmaak_f32 v184,v168,v168,0x409d30fc
v_fmaak_f32 v185,v169,v169,0x409d30fc
v_fmaak_f32 v186,v170,v170,0x409d30fc
v_fmaak_f32 v187,v171,v171,0x409d30fc
v_fmaak_f32 v188,v172,v172,0x409d30fc
v_fmaak_f32 v189,v173,v173,0x409d30fc
v_fmaak_f32 v190,v174,v174,0x409d30fc
v_fmaak_f32 v191,v175,v175,0x409d30fc
v_mul_f32 v176,v160,v176
v_mul_f32 v177,v161,v177
v_mul_f32 v178,v162,v178
v_mul_f32 v179,v163,v179
v_mul_f32 v180,v164,v180
v_mul_f32 v181,v165,v181
v_mul_f32 v182,v166,v182
v_mul_f32 v183,v167,v183
v_mul_f32 v184,v168,v184
v_mul_f32 v185,v169,v185
v_mul_f32 v186,v170,v186
v_mul_f32 v187,v171,v187
v_mul_f32 v188,v172,v188
v_mul_f32 v189,v173,v189
v_mul_f32 v190,v174,v190
v_mul_f32 v191,v175,v191
s_setprio 1
v_mfma_f32_32x32x16_bf16 v[96:111],v[200:203],v[192:195],v[96:111]
v_exp_f32 v176,v176
v_exp_f32 v177,v177
v_exp_f32 v178,v178
v_mfma_f32_32x32x16_bf16 v[96:111],v[204:207],v[196:199],v[96:111]
v_exp_f32 v179,v179
v_exp_f32 v180,v180
v_exp_f32 v181,v181
v_mfma_f32_32x32x16_bf16 v[112:127],v[208:211],v[192:195],v[112:127]
v_exp_f32 v182,v182
v_exp_f32 v183,v183
v_exp_f32 v184,v184
v_mfma_f32_32x32x16_bf16 v[112:127],v[212:215],v[196:199],v[112:127]
v_exp_f32 v185,v185
v_exp_f32 v186,v186
v_exp_f32 v187,v187
v_mfma_f32_32x32x16_bf16 v[128:143],v[216:219],v[192:195],v[128:143]
v_exp_f32 v188,v188
v_exp_f32 v189,v189
v_mfma_f32_32x32x16_bf16 v[128:143],v[220:223],v[196:199],v[128:143]
v_exp_f32 v190,v190
v_exp_f32 v191,v191
ds_read_b64_tr_b16 v[200:201],v16 offset:6144
ds_read_b64_tr_b16 v[202:203],v16 offset:6656
ds_read_b64_tr_b16 v[204:205],v16 offset:7168
ds_read_b64_tr_b16 v[206:207],v16 offset:7680
ds_read_b64_tr_b16 v[208:209],v16 offset:8192
ds_read_b64_tr_b16 v[210:211],v16 offset:8704
ds_read_b64_tr_b16 v[212:213],v16 offset:9216
ds_read_b64_tr_b16 v[214:215],v16 offset:9728
ds_read_b64_tr_b16 v[216:217],v16 offset:10240
ds_read_b64_tr_b16 v[218:219],v16 offset:10752
ds_read_b64_tr_b16 v[220:221],v16 offset:11264
ds_read_b64_tr_b16 v[222:223],v16 offset:11776
ds_read_b128 v[144:147],v18 offset:512
ds_read_b128 v[148:151],v18 offset:544
ds_read_b128 v[152:155],v18 offset:576
ds_read_b128 v[156:159],v18 offset:608
s_mov_b64 exec,s[62:63]
s_add_u32 m0,s68,20736
s_add_u32 s64,s59,589824
buffer_load_dwordx4 v27,s[36:39],s64 offen lds
s_mov_b64 exec,-1
s_setprio 0
v_add_f32 v176,1.0,v176
v_add_f32 v177,1.0,v177
v_add_f32 v178,1.0,v178
v_add_f32 v179,1.0,v179
v_add_f32 v180,1.0,v180
v_add_f32 v181,1.0,v181
v_add_f32 v182,1.0,v182
v_add_f32 v183,1.0,v183
v_add_f32 v184,1.0,v184
v_add_f32 v185,1.0,v185
v_add_f32 v186,1.0,v186
v_add_f32 v187,1.0,v187
v_add_f32 v188,1.0,v188
v_add_f32 v189,1.0,v189
v_add_f32 v190,1.0,v190
v_add_f32 v191,1.0,v191
s_setprio 1
s_waitcnt lgkmcnt(0)
v_mfma_f32_32x32x16_bf16 v[144:159],v[200:203],v[72:75],v[144:159]
v_rcp_f32 v176,v176
v_rcp_f32 v177,v177
v_rcp_f32 v178,v178
v_mfma_f32_32x32x16_bf16 v[144:159],v[204:207],v[76:79],v[144:159]
v_rcp_f32 v179,v179
v_rcp_f32 v180,v180
v_rcp_f32 v181,v181
v_mfma_f32_32x32x16_bf16 v[144:159],v[208:211],v[80:83],v[144:159]
v_rcp_f32 v182,v182
v_rcp_f32 v183,v183
v_rcp_f32 v184,v184
v_mfma_f32_32x32x16_bf16 v[144:159],v[212:215],v[84:87],v[144:159]
v_rcp_f32 v185,v185
v_rcp_f32 v186,v186
v_rcp_f32 v187,v187
v_mfma_f32_32x32x16_bf16 v[144:159],v[216:219],v[88:91],v[144:159]
v_rcp_f32 v188,v188
v_rcp_f32 v189,v189
v_mfma_f32_32x32x16_bf16 v[144:159],v[220:223],v[92:95],v[144:159]
v_rcp_f32 v190,v190
v_rcp_f32 v191,v191
ds_read_b64_tr_b16 v[200:201],v17 offset:0
ds_read_b64_tr_b16 v[202:203],v17 offset:1536
ds_read_b64_tr_b16 v[204:205],v17 offset:3072
ds_read_b64_tr_b16 v[206:207],v17 offset:4608
ds_read_b64_tr_b16 v[208:209],v17 offset:64
ds_read_b64_tr_b16 v[210:211],v17 offset:1600
ds_read_b64_tr_b16 v[212:213],v17 offset:3136
ds_read_b64_tr_b16 v[214:215],v17 offset:4672
ds_read_b64_tr_b16 v[216:217],v17 offset:128
ds_read_b64_tr_b16 v[218:219],v17 offset:1664
ds_read_b64_tr_b16 v[220:221],v17 offset:3200
ds_read_b64_tr_b16 v[222:223],v17 offset:4736
s_mov_b64 exec,s[62:63]
s_add_u32 m0,s68,24192
s_add_u32 s64,s59,688128
buffer_load_dwordx4 v27,s[36:39],s64 offen lds
s_mov_b64 exec,-1
s_setprio 0
v_mul_f32 v176,v160,v176
v_mul_f32 v177,v161,v177
v_mul_f32 v178,v162,v178
v_mul_f32 v179,v163,v179
v_mul_f32 v180,v164,v180
v_mul_f32 v181,v165,v181
v_mul_f32 v182,v166,v182
v_mul_f32 v183,v167,v183
v_mul_f32 v184,v168,v184
v_mul_f32 v185,v169,v185
v_mul_f32 v186,v170,v186
v_mul_f32 v187,v171,v187
v_mul_f32 v188,v172,v188
v_mul_f32 v189,v173,v189
v_mul_f32 v190,v174,v190
v_mul_f32 v191,v175,v191
v_cvt_pk_bf16_f32 v192,v176,v177
v_cvt_pk_bf16_f32 v193,v178,v179
v_cvt_pk_bf16_f32 v194,v180,v181
v_cvt_pk_bf16_f32 v195,v182,v183
v_cvt_pk_bf16_f32 v196,v184,v185
v_cvt_pk_bf16_f32 v197,v186,v187
v_cvt_pk_bf16_f32 v198,v188,v189
v_cvt_pk_bf16_f32 v199,v190,v191
s_waitcnt lgkmcnt(0)
s_barrier
v_fmaak_f32 v176,v144,v144,0x409d30fc
v_fmaak_f32 v177,v145,v145,0x409d30fc
v_fmaak_f32 v178,v146,v146,0x409d30fc
v_fmaak_f32 v179,v147,v147,0x409d30fc
v_fmaak_f32 v180,v148,v148,0x409d30fc
v_fmaak_f32 v181,v149,v149,0x409d30fc
v_fmaak_f32 v182,v150,v150,0x409d30fc
v_fmaak_f32 v183,v151,v151,0x409d30fc
v_fmaak_f32 v184,v152,v152,0x409d30fc
v_fmaak_f32 v185,v153,v153,0x409d30fc
v_fmaak_f32 v186,v154,v154,0x409d30fc
v_fmaak_f32 v187,v155,v155,0x409d30fc
v_fmaak_f32 v188,v156,v156,0x409d30fc
v_fmaak_f32 v189,v157,v157,0x409d30fc
v_fmaak_f32 v190,v158,v158,0x409d30fc
v_fmaak_f32 v191,v159,v159,0x409d30fc
v_mul_f32 v176,v144,v176
v_mul_f32 v177,v145,v177
v_mul_f32 v178,v146,v178
v_mul_f32 v179,v147,v179
v_mul_f32 v180,v148,v180
v_mul_f32 v181,v149,v181
v_mul_f32 v182,v150,v182
v_mul_f32 v183,v151,v183
v_mul_f32 v184,v152,v184
v_mul_f32 v185,v153,v185
v_mul_f32 v186,v154,v186
v_mul_f32 v187,v155,v187
v_mul_f32 v188,v156,v188
v_mul_f32 v189,v157,v189
v_mul_f32 v190,v158,v190
v_mul_f32 v191,v159,v191
s_setprio 1
v_mfma_f32_32x32x16_bf16 v[96:111],v[200:203],v[192:195],v[96:111]
v_exp_f32 v176,v176
v_exp_f32 v177,v177
v_exp_f32 v178,v178
v_mfma_f32_32x32x16_bf16 v[96:111],v[204:207],v[196:199],v[96:111]
v_exp_f32 v179,v179
v_exp_f32 v180,v180
v_exp_f32 v181,v181
v_mfma_f32_32x32x16_bf16 v[112:127],v[208:211],v[192:195],v[112:127]
v_exp_f32 v182,v182
v_exp_f32 v183,v183
v_exp_f32 v184,v184
v_mfma_f32_32x32x16_bf16 v[112:127],v[212:215],v[196:199],v[112:127]
v_exp_f32 v185,v185
v_exp_f32 v186,v186
v_exp_f32 v187,v187
v_mfma_f32_32x32x16_bf16 v[128:143],v[216:219],v[192:195],v[128:143]
v_exp_f32 v188,v188
v_exp_f32 v189,v189
v_mfma_f32_32x32x16_bf16 v[128:143],v[220:223],v[196:199],v[128:143]
v_exp_f32 v190,v190
v_exp_f32 v191,v191
ds_read_b64_tr_b16 v[200:201],v16 offset:12288
ds_read_b64_tr_b16 v[202:203],v16 offset:12800
ds_read_b64_tr_b16 v[204:205],v16 offset:13312
ds_read_b64_tr_b16 v[206:207],v16 offset:13824
ds_read_b64_tr_b16 v[208:209],v16 offset:14336
ds_read_b64_tr_b16 v[210:211],v16 offset:14848
ds_read_b64_tr_b16 v[212:213],v16 offset:15360
ds_read_b64_tr_b16 v[214:215],v16 offset:15872
ds_read_b64_tr_b16 v[216:217],v16 offset:16384
ds_read_b64_tr_b16 v[218:219],v16 offset:16896
ds_read_b64_tr_b16 v[220:221],v16 offset:17408
ds_read_b64_tr_b16 v[222:223],v16 offset:17920
ds_read_b128 v[160:163],v18 offset:640
ds_read_b128 v[164:167],v18 offset:672
ds_read_b128 v[168:171],v18 offset:704
ds_read_b128 v[172:175],v18 offset:736
s_mov_b64 exec,s[62:63]
s_add_u32 m0,s68,27648
s_add_u32 s64,s59,786432
buffer_load_dwordx4 v27,s[36:39],s64 offen lds
s_mov_b64 exec,-1
s_setprio 0
v_add_f32 v176,1.0,v176
v_add_f32 v177,1.0,v177
v_add_f32 v178,1.0,v178
v_add_f32 v179,1.0,v179
v_add_f32 v180,1.0,v180
v_add_f32 v181,1.0,v181
v_add_f32 v182,1.0,v182
v_add_f32 v183,1.0,v183
v_add_f32 v184,1.0,v184
v_add_f32 v185,1.0,v185
v_add_f32 v186,1.0,v186
v_add_f32 v187,1.0,v187
v_add_f32 v188,1.0,v188
v_add_f32 v189,1.0,v189
v_add_f32 v190,1.0,v190
v_add_f32 v191,1.0,v191
s_setprio 1
s_waitcnt lgkmcnt(0)
v_mfma_f32_32x32x16_bf16 v[160:175],v[200:203],v[72:75],v[160:175]
v_rcp_f32 v176,v176
v_rcp_f32 v177,v177
v_rcp_f32 v178,v178
v_mfma_f32_32x32x16_bf16 v[160:175],v[204:207],v[76:79],v[160:175]
v_rcp_f32 v179,v179
v_rcp_f32 v180,v180
v_rcp_f32 v181,v181
v_mfma_f32_32x32x16_bf16 v[160:175],v[208:211],v[80:83],v[160:175]
v_rcp_f32 v182,v182
v_rcp_f32 v183,v183
v_rcp_f32 v184,v184
v_mfma_f32_32x32x16_bf16 v[160:175],v[212:215],v[84:87],v[160:175]
v_rcp_f32 v185,v185
v_rcp_f32 v186,v186
v_rcp_f32 v187,v187
v_mfma_f32_32x32x16_bf16 v[160:175],v[216:219],v[88:91],v[160:175]
v_rcp_f32 v188,v188
v_rcp_f32 v189,v189
v_mfma_f32_32x32x16_bf16 v[160:175],v[220:223],v[92:95],v[160:175]
v_rcp_f32 v190,v190
v_rcp_f32 v191,v191
ds_read_b64_tr_b16 v[200:201],v17 offset:6144
ds_read_b64_tr_b16 v[202:203],v17 offset:7680
ds_read_b64_tr_b16 v[204:205],v17 offset:9216
ds_read_b64_tr_b16 v[206:207],v17 offset:10752
ds_read_b64_tr_b16 v[208:209],v17 offset:6208
ds_read_b64_tr_b16 v[210:211],v17 offset:7744
ds_read_b64_tr_b16 v[212:213],v17 offset:9280
ds_read_b64_tr_b16 v[214:215],v17 offset:10816
ds_read_b64_tr_b16 v[216:217],v17 offset:6272
ds_read_b64_tr_b16 v[218:219],v17 offset:7808
ds_read_b64_tr_b16 v[220:221],v17 offset:9344
ds_read_b64_tr_b16 v[222:223],v17 offset:10880
s_mov_b64 exec,s[62:63]
s_add_u32 m0,s68,31104
s_add_u32 s64,s59,884736
buffer_load_dwordx4 v27,s[36:39],s64 offen lds
s_mov_b64 exec,-1
s_setprio 0
v_mul_f32 v176,v144,v176
v_mul_f32 v177,v145,v177
v_mul_f32 v178,v146,v178
v_mul_f32 v179,v147,v179
v_mul_f32 v180,v148,v180
v_mul_f32 v181,v149,v181
v_mul_f32 v182,v150,v182
v_mul_f32 v183,v151,v183
v_mul_f32 v184,v152,v184
v_mul_f32 v185,v153,v185
v_mul_f32 v186,v154,v186
v_mul_f32 v187,v155,v187
v_mul_f32 v188,v156,v188
v_mul_f32 v189,v157,v189
v_mul_f32 v190,v158,v190
v_mul_f32 v191,v159,v191
v_cvt_pk_bf16_f32 v192,v176,v177
v_cvt_pk_bf16_f32 v193,v178,v179
v_cvt_pk_bf16_f32 v194,v180,v181
v_cvt_pk_bf16_f32 v195,v182,v183
v_cvt_pk_bf16_f32 v196,v184,v185
v_cvt_pk_bf16_f32 v197,v186,v187
v_cvt_pk_bf16_f32 v198,v188,v189
v_cvt_pk_bf16_f32 v199,v190,v191
s_waitcnt lgkmcnt(0)
s_barrier
v_fmaak_f32 v176,v160,v160,0x409d30fc
v_fmaak_f32 v177,v161,v161,0x409d30fc
v_fmaak_f32 v178,v162,v162,0x409d30fc
v_fmaak_f32 v179,v163,v163,0x409d30fc
v_fmaak_f32 v180,v164,v164,0x409d30fc
v_fmaak_f32 v181,v165,v165,0x409d30fc
v_fmaak_f32 v182,v166,v166,0x409d30fc
v_fmaak_f32 v183,v167,v167,0x409d30fc
v_fmaak_f32 v184,v168,v168,0x409d30fc
v_fmaak_f32 v185,v169,v169,0x409d30fc
v_fmaak_f32 v186,v170,v170,0x409d30fc
v_fmaak_f32 v187,v171,v171,0x409d30fc
v_fmaak_f32 v188,v172,v172,0x409d30fc
v_fmaak_f32 v189,v173,v173,0x409d30fc
v_fmaak_f32 v190,v174,v174,0x409d30fc
v_fmaak_f32 v191,v175,v175,0x409d30fc
v_mul_f32 v176,v160,v176
v_mul_f32 v177,v161,v177
v_mul_f32 v178,v162,v178
v_mul_f32 v179,v163,v179
v_mul_f32 v180,v164,v180
v_mul_f32 v181,v165,v181
v_mul_f32 v182,v166,v182
v_mul_f32 v183,v167,v183
v_mul_f32 v184,v168,v184
v_mul_f32 v185,v169,v185
v_mul_f32 v186,v170,v186
v_mul_f32 v187,v171,v187
v_mul_f32 v188,v172,v188
v_mul_f32 v189,v173,v189
v_mul_f32 v190,v174,v190
v_mul_f32 v191,v175,v191
s_setprio 1
v_mfma_f32_32x32x16_bf16 v[96:111],v[200:203],v[192:195],v[96:111]
v_exp_f32 v176,v176
v_exp_f32 v177,v177
v_exp_f32 v178,v178
v_mfma_f32_32x32x16_bf16 v[96:111],v[204:207],v[196:199],v[96:111]
v_exp_f32 v179,v179
v_exp_f32 v180,v180
v_exp_f32 v181,v181
v_mfma_f32_32x32x16_bf16 v[112:127],v[208:211],v[192:195],v[112:127]
v_exp_f32 v182,v182
v_exp_f32 v183,v183
v_exp_f32 v184,v184
v_mfma_f32_32x32x16_bf16 v[112:127],v[212:215],v[196:199],v[112:127]
v_exp_f32 v185,v185
v_exp_f32 v186,v186
v_exp_f32 v187,v187
v_mfma_f32_32x32x16_bf16 v[128:143],v[216:219],v[192:195],v[128:143]
v_exp_f32 v188,v188
v_exp_f32 v189,v189
v_mfma_f32_32x32x16_bf16 v[128:143],v[220:223],v[196:199],v[128:143]
v_exp_f32 v190,v190
v_exp_f32 v191,v191
ds_read_b64_tr_b16 v[200:201],v16 offset:0
ds_read_b64_tr_b16 v[202:203],v16 offset:512
ds_read_b64_tr_b16 v[204:205],v16 offset:1024
ds_read_b64_tr_b16 v[206:207],v16 offset:1536
ds_read_b64_tr_b16 v[208:209],v16 offset:2048
ds_read_b64_tr_b16 v[210:211],v16 offset:2560
ds_read_b64_tr_b16 v[212:213],v16 offset:3072
ds_read_b64_tr_b16 v[214:215],v16 offset:3584
ds_read_b64_tr_b16 v[216:217],v16 offset:4096
ds_read_b64_tr_b16 v[218:219],v16 offset:4608
ds_read_b64_tr_b16 v[220:221],v16 offset:5120
ds_read_b64_tr_b16 v[222:223],v16 offset:5632
ds_read_b128 v[144:147],v18 offset:768
ds_read_b128 v[148:151],v18 offset:800
ds_read_b128 v[152:155],v18 offset:832
ds_read_b128 v[156:159],v18 offset:864
s_mov_b64 exec,s[62:63]
s_add_u32 m0,s68,34560
s_add_u32 s64,s59,983040
buffer_load_dwordx4 v27,s[36:39],s64 offen lds
s_mov_b64 exec,-1
s_setprio 0
v_add_f32 v176,1.0,v176
v_add_f32 v177,1.0,v177
v_add_f32 v178,1.0,v178
v_add_f32 v179,1.0,v179
v_add_f32 v180,1.0,v180
v_add_f32 v181,1.0,v181
v_add_f32 v182,1.0,v182
v_add_f32 v183,1.0,v183
v_add_f32 v184,1.0,v184
v_add_f32 v185,1.0,v185
v_add_f32 v186,1.0,v186
v_add_f32 v187,1.0,v187
v_add_f32 v188,1.0,v188
v_add_f32 v189,1.0,v189
v_add_f32 v190,1.0,v190
v_add_f32 v191,1.0,v191
s_setprio 1
s_waitcnt lgkmcnt(0)
v_mfma_f32_32x32x16_bf16 v[144:159],v[200:203],v[72:75],v[144:159]
v_rcp_f32 v176,v176
v_rcp_f32 v177,v177
v_rcp_f32 v178,v178
v_mfma_f32_32x32x16_bf16 v[144:159],v[204:207],v[76:79],v[144:159]
v_rcp_f32 v179,v179
v_rcp_f32 v180,v180
v_rcp_f32 v181,v181
v_mfma_f32_32x32x16_bf16 v[144:159],v[208:211],v[80:83],v[144:159]
v_rcp_f32 v182,v182
v_rcp_f32 v183,v183
v_rcp_f32 v184,v184
v_mfma_f32_32x32x16_bf16 v[144:159],v[212:215],v[84:87],v[144:159]
v_rcp_f32 v185,v185
v_rcp_f32 v186,v186
v_rcp_f32 v187,v187
v_mfma_f32_32x32x16_bf16 v[144:159],v[216:219],v[88:91],v[144:159]
v_rcp_f32 v188,v188
v_rcp_f32 v189,v189
v_mfma_f32_32x32x16_bf16 v[144:159],v[220:223],v[92:95],v[144:159]
v_rcp_f32 v190,v190
v_rcp_f32 v191,v191
ds_read_b64_tr_b16 v[200:201],v17 offset:12288
ds_read_b64_tr_b16 v[202:203],v17 offset:13824
ds_read_b64_tr_b16 v[204:205],v17 offset:15360
ds_read_b64_tr_b16 v[206:207],v17 offset:16896
ds_read_b64_tr_b16 v[208:209],v17 offset:12352
ds_read_b64_tr_b16 v[210:211],v17 offset:13888
ds_read_b64_tr_b16 v[212:213],v17 offset:15424
ds_read_b64_tr_b16 v[214:215],v17 offset:16960
ds_read_b64_tr_b16 v[216:217],v17 offset:12416
ds_read_b64_tr_b16 v[218:219],v17 offset:13952
ds_read_b64_tr_b16 v[220:221],v17 offset:15488
ds_read_b64_tr_b16 v[222:223],v17 offset:17024
s_mov_b64 exec,s[62:63]
s_add_u32 m0,s68,38016
s_add_u32 s64,s59,1081344
buffer_load_dwordx4 v27,s[36:39],s64 offen lds
s_mov_b64 exec,-1
s_setprio 0
v_mul_f32 v176,v160,v176
v_mul_f32 v177,v161,v177
v_mul_f32 v178,v162,v178
v_mul_f32 v179,v163,v179
v_mul_f32 v180,v164,v180
v_mul_f32 v181,v165,v181
v_mul_f32 v182,v166,v182
v_mul_f32 v183,v167,v183
v_mul_f32 v184,v168,v184
v_mul_f32 v185,v169,v185
v_mul_f32 v186,v170,v186
v_mul_f32 v187,v171,v187
v_mul_f32 v188,v172,v188
v_mul_f32 v189,v173,v189
v_mul_f32 v190,v174,v190
v_mul_f32 v191,v175,v191
v_cvt_pk_bf16_f32 v192,v176,v177
v_cvt_pk_bf16_f32 v193,v178,v179
v_cvt_pk_bf16_f32 v194,v180,v181
v_cvt_pk_bf16_f32 v195,v182,v183
v_cvt_pk_bf16_f32 v196,v184,v185
v_cvt_pk_bf16_f32 v197,v186,v187
v_cvt_pk_bf16_f32 v198,v188,v189
v_cvt_pk_bf16_f32 v199,v190,v191
s_waitcnt lgkmcnt(0)
s_barrier
v_fmaak_f32 v176,v144,v144,0x409d30fc
v_fmaak_f32 v177,v145,v145,0x409d30fc
v_fmaak_f32 v178,v146,v146,0x409d30fc
v_fmaak_f32 v179,v147,v147,0x409d30fc
v_fmaak_f32 v180,v148,v148,0x409d30fc
v_fmaak_f32 v181,v149,v149,0x409d30fc
v_fmaak_f32 v182,v150,v150,0x409d30fc
v_fmaak_f32 v183,v151,v151,0x409d30fc
v_fmaak_f32 v184,v152,v152,0x409d30fc
v_fmaak_f32 v185,v153,v153,0x409d30fc
v_fmaak_f32 v186,v154,v154,0x409d30fc
v_fmaak_f32 v187,v155,v155,0x409d30fc
v_fmaak_f32 v188,v156,v156,0x409d30fc
v_fmaak_f32 v189,v157,v157,0x409d30fc
v_fmaak_f32 v190,v158,v158,0x409d30fc
v_fmaak_f32 v191,v159,v159,0x409d30fc
v_mul_f32 v176,v144,v176
v_mul_f32 v177,v145,v177
v_mul_f32 v178,v146,v178
v_mul_f32 v179,v147,v179
v_mul_f32 v180,v148,v180
v_mul_f32 v181,v149,v181
v_mul_f32 v182,v150,v182
v_mul_f32 v183,v151,v183
v_mul_f32 v184,v152,v184
v_mul_f32 v185,v153,v185
v_mul_f32 v186,v154,v186
v_mul_f32 v187,v155,v187
v_mul_f32 v188,v156,v188
v_mul_f32 v189,v157,v189
v_mul_f32 v190,v158,v190
v_mul_f32 v191,v159,v191
s_setprio 1
v_mfma_f32_32x32x16_bf16 v[96:111],v[200:203],v[192:195],v[96:111]
v_exp_f32 v176,v176
v_exp_f32 v177,v177
v_exp_f32 v178,v178
v_mfma_f32_32x32x16_bf16 v[96:111],v[204:207],v[196:199],v[96:111]
v_exp_f32 v179,v179
v_exp_f32 v180,v180
v_exp_f32 v181,v181
v_mfma_f32_32x32x16_bf16 v[112:127],v[208:211],v[192:195],v[112:127]
v_exp_f32 v182,v182
v_exp_f32 v183,v183
v_exp_f32 v184,v184
v_mfma_f32_32x32x16_bf16 v[112:127],v[212:215],v[196:199],v[112:127]
v_exp_f32 v185,v185
v_exp_f32 v186,v186
v_exp_f32 v187,v187
v_mfma_f32_32x32x16_bf16 v[128:143],v[216:219],v[192:195],v[128:143]
v_exp_f32 v188,v188
v_exp_f32 v189,v189
v_mfma_f32_32x32x16_bf16 v[128:143],v[220:223],v[196:199],v[128:143]
v_exp_f32 v190,v190
v_exp_f32 v191,v191
ds_read_b64_tr_b16 v[200:201],v16 offset:6144
ds_read_b64_tr_b16 v[202:203],v16 offset:6656
ds_read_b64_tr_b16 v[204:205],v16 offset:7168
ds_read_b64_tr_b16 v[206:207],v16 offset:7680
ds_read_b64_tr_b16 v[208:209],v16 offset:8192
ds_read_b64_tr_b16 v[210:211],v16 offset:8704
ds_read_b64_tr_b16 v[212:213],v16 offset:9216
ds_read_b64_tr_b16 v[214:215],v16 offset:9728
ds_read_b64_tr_b16 v[216:217],v16 offset:10240
ds_read_b64_tr_b16 v[218:219],v16 offset:10752
ds_read_b64_tr_b16 v[220:221],v16 offset:11264
ds_read_b64_tr_b16 v[222:223],v16 offset:11776
ds_read_b128 v[160:163],v18 offset:896
ds_read_b128 v[164:167],v18 offset:928
ds_read_b128 v[168:171],v18 offset:960
ds_read_b128 v[172:175],v18 offset:992
s_mov_b64 exec,s[62:63]
s_add_u32 m0,s68,41472
s_add_u32 s64,s59,1179648
buffer_load_dwordx4 v27,s[36:39],s64 offen lds
s_mov_b64 exec,-1
s_setprio 0
v_add_f32 v176,1.0,v176
v_add_f32 v177,1.0,v177
v_add_f32 v178,1.0,v178
v_add_f32 v179,1.0,v179
v_add_f32 v180,1.0,v180
v_add_f32 v181,1.0,v181
v_add_f32 v182,1.0,v182
v_add_f32 v183,1.0,v183
v_add_f32 v184,1.0,v184
v_add_f32 v185,1.0,v185
v_add_f32 v186,1.0,v186
v_add_f32 v187,1.0,v187
v_add_f32 v188,1.0,v188
v_add_f32 v189,1.0,v189
v_add_f32 v190,1.0,v190
v_add_f32 v191,1.0,v191
s_setprio 1
s_waitcnt lgkmcnt(0)
v_mfma_f32_32x32x16_bf16 v[160:175],v[200:203],v[72:75],v[160:175]
v_rcp_f32 v176,v176
v_rcp_f32 v177,v177
v_rcp_f32 v178,v178
v_mfma_f32_32x32x16_bf16 v[160:175],v[204:207],v[76:79],v[160:175]
v_rcp_f32 v179,v179
v_rcp_f32 v180,v180
v_rcp_f32 v181,v181
v_mfma_f32_32x32x16_bf16 v[160:175],v[208:211],v[80:83],v[160:175]
v_rcp_f32 v182,v182
v_rcp_f32 v183,v183
v_rcp_f32 v184,v184
v_mfma_f32_32x32x16_bf16 v[160:175],v[212:215],v[84:87],v[160:175]
v_rcp_f32 v185,v185
v_rcp_f32 v186,v186
v_rcp_f32 v187,v187
v_mfma_f32_32x32x16_bf16 v[160:175],v[216:219],v[88:91],v[160:175]
v_rcp_f32 v188,v188
v_rcp_f32 v189,v189
v_mfma_f32_32x32x16_bf16 v[160:175],v[220:223],v[92:95],v[160:175]
v_rcp_f32 v190,v190
v_rcp_f32 v191,v191
ds_read_b64_tr_b16 v[200:201],v17 offset:0
ds_read_b64_tr_b16 v[202:203],v17 offset:1536
ds_read_b64_tr_b16 v[204:205],v17 offset:3072
ds_read_b64_tr_b16 v[206:207],v17 offset:4608
ds_read_b64_tr_b16 v[208:209],v17 offset:64
ds_read_b64_tr_b16 v[210:211],v17 offset:1600
ds_read_b64_tr_b16 v[212:213],v17 offset:3136
ds_read_b64_tr_b16 v[214:215],v17 offset:4672
ds_read_b64_tr_b16 v[216:217],v17 offset:128
ds_read_b64_tr_b16 v[218:219],v17 offset:1664
ds_read_b64_tr_b16 v[220:221],v17 offset:3200
ds_read_b64_tr_b16 v[222:223],v17 offset:4736
s_mov_b64 exec,s[62:63]
s_add_u32 m0,s68,44928
s_add_u32 s64,s59,1277952
buffer_load_dwordx4 v27,s[36:39],s64 offen lds
s_mov_b64 exec,-1
s_setprio 0
v_mul_f32 v176,v144,v176
v_mul_f32 v177,v145,v177
v_mul_f32 v178,v146,v178
v_mul_f32 v179,v147,v179
v_mul_f32 v180,v148,v180
v_mul_f32 v181,v149,v181
v_mul_f32 v182,v150,v182
v_mul_f32 v183,v151,v183
v_mul_f32 v184,v152,v184
v_mul_f32 v185,v153,v185
v_mul_f32 v186,v154,v186
v_mul_f32 v187,v155,v187
v_mul_f32 v188,v156,v188
v_mul_f32 v189,v157,v189
v_mul_f32 v190,v158,v190
v_mul_f32 v191,v159,v191
v_cvt_pk_bf16_f32 v192,v176,v177
v_cvt_pk_bf16_f32 v193,v178,v179
v_cvt_pk_bf16_f32 v194,v180,v181
v_cvt_pk_bf16_f32 v195,v182,v183
v_cvt_pk_bf16_f32 v196,v184,v185
v_cvt_pk_bf16_f32 v197,v186,v187
v_cvt_pk_bf16_f32 v198,v188,v189
v_cvt_pk_bf16_f32 v199,v190,v191
s_waitcnt lgkmcnt(0)
s_barrier
v_fmaak_f32 v176,v160,v160,0x409d30fc
v_fmaak_f32 v177,v161,v161,0x409d30fc
v_fmaak_f32 v178,v162,v162,0x409d30fc
v_fmaak_f32 v179,v163,v163,0x409d30fc
v_fmaak_f32 v180,v164,v164,0x409d30fc
v_fmaak_f32 v181,v165,v165,0x409d30fc
v_fmaak_f32 v182,v166,v166,0x409d30fc
v_fmaak_f32 v183,v167,v167,0x409d30fc
v_fmaak_f32 v184,v168,v168,0x409d30fc
v_fmaak_f32 v185,v169,v169,0x409d30fc
v_fmaak_f32 v186,v170,v170,0x409d30fc
v_fmaak_f32 v187,v171,v171,0x409d30fc
v_fmaak_f32 v188,v172,v172,0x409d30fc
v_fmaak_f32 v189,v173,v173,0x409d30fc
v_fmaak_f32 v190,v174,v174,0x409d30fc
v_fmaak_f32 v191,v175,v175,0x409d30fc
v_mul_f32 v176,v160,v176
v_mul_f32 v177,v161,v177
v_mul_f32 v178,v162,v178
v_mul_f32 v179,v163,v179
v_mul_f32 v180,v164,v180
v_mul_f32 v181,v165,v181
v_mul_f32 v182,v166,v182
v_mul_f32 v183,v167,v183
v_mul_f32 v184,v168,v184
v_mul_f32 v185,v169,v185
v_mul_f32 v186,v170,v186
v_mul_f32 v187,v171,v187
v_mul_f32 v188,v172,v188
v_mul_f32 v189,v173,v189
v_mul_f32 v190,v174,v190
v_mul_f32 v191,v175,v191
s_setprio 1
v_mfma_f32_32x32x16_bf16 v[96:111],v[200:203],v[192:195],v[96:111]
v_exp_f32 v176,v176
v_exp_f32 v177,v177
v_exp_f32 v178,v178
v_mfma_f32_32x32x16_bf16 v[96:111],v[204:207],v[196:199],v[96:111]
v_exp_f32 v179,v179
v_exp_f32 v180,v180
v_exp_f32 v181,v181
v_mfma_f32_32x32x16_bf16 v[112:127],v[208:211],v[192:195],v[112:127]
v_exp_f32 v182,v182
v_exp_f32 v183,v183
v_exp_f32 v184,v184
v_mfma_f32_32x32x16_bf16 v[112:127],v[212:215],v[196:199],v[112:127]
v_exp_f32 v185,v185
v_exp_f32 v186,v186
v_exp_f32 v187,v187
v_mfma_f32_32x32x16_bf16 v[128:143],v[216:219],v[192:195],v[128:143]
v_exp_f32 v188,v188
v_exp_f32 v189,v189
v_mfma_f32_32x32x16_bf16 v[128:143],v[220:223],v[196:199],v[128:143]
v_exp_f32 v190,v190
v_exp_f32 v191,v191
ds_read_b64_tr_b16 v[200:201],v16 offset:12288
ds_read_b64_tr_b16 v[202:203],v16 offset:12800
ds_read_b64_tr_b16 v[204:205],v16 offset:13312
ds_read_b64_tr_b16 v[206:207],v16 offset:13824
ds_read_b64_tr_b16 v[208:209],v16 offset:14336
ds_read_b64_tr_b16 v[210:211],v16 offset:14848
ds_read_b64_tr_b16 v[212:213],v16 offset:15360
ds_read_b64_tr_b16 v[214:215],v16 offset:15872
ds_read_b64_tr_b16 v[216:217],v16 offset:16384
ds_read_b64_tr_b16 v[218:219],v16 offset:16896
ds_read_b64_tr_b16 v[220:221],v16 offset:17408
ds_read_b64_tr_b16 v[222:223],v16 offset:17920
ds_read_b128 v[144:147],v18 offset:1024
ds_read_b128 v[148:151],v18 offset:1056
ds_read_b128 v[152:155],v18 offset:1088
ds_read_b128 v[156:159],v18 offset:1120
s_mov_b64 exec,s[62:63]
s_add_u32 m0,s68,48384
s_add_u32 s64,s59,1376256
buffer_load_dwordx4 v27,s[36:39],s64 offen lds
s_mov_b64 exec,-1
s_setprio 0
v_add_f32 v176,1.0,v176
v_add_f32 v177,1.0,v177
v_add_f32 v178,1.0,v178
v_add_f32 v179,1.0,v179
v_add_f32 v180,1.0,v180
v_add_f32 v181,1.0,v181
v_add_f32 v182,1.0,v182
v_add_f32 v183,1.0,v183
v_add_f32 v184,1.0,v184
v_add_f32 v185,1.0,v185
v_add_f32 v186,1.0,v186
v_add_f32 v187,1.0,v187
v_add_f32 v188,1.0,v188
v_add_f32 v189,1.0,v189
v_add_f32 v190,1.0,v190
v_add_f32 v191,1.0,v191
s_setprio 1
s_waitcnt lgkmcnt(0)
v_mfma_f32_32x32x16_bf16 v[144:159],v[200:203],v[72:75],v[144:159]
v_rcp_f32 v176,v176
v_rcp_f32 v177,v177
v_rcp_f32 v178,v178
v_mfma_f32_32x32x16_bf16 v[144:159],v[204:207],v[76:79],v[144:159]
v_rcp_f32 v179,v179
v_rcp_f32 v180,v180
v_rcp_f32 v181,v181
v_mfma_f32_32x32x16_bf16 v[144:159],v[208:211],v[80:83],v[144:159]
v_rcp_f32 v182,v182
v_rcp_f32 v183,v183
v_rcp_f32 v184,v184
v_mfma_f32_32x32x16_bf16 v[144:159],v[212:215],v[84:87],v[144:159]
v_rcp_f32 v185,v185
v_rcp_f32 v186,v186
v_rcp_f32 v187,v187
v_mfma_f32_32x32x16_bf16 v[144:159],v[216:219],v[88:91],v[144:159]
v_rcp_f32 v188,v188
v_rcp_f32 v189,v189
v_mfma_f32_32x32x16_bf16 v[144:159],v[220:223],v[92:95],v[144:159]
v_rcp_f32 v190,v190
v_rcp_f32 v191,v191
ds_read_b64_tr_b16 v[200:201],v17 offset:6144
ds_read_b64_tr_b16 v[202:203],v17 offset:7680
ds_read_b64_tr_b16 v[204:205],v17 offset:9216
ds_read_b64_tr_b16 v[206:207],v17 offset:10752
ds_read_b64_tr_b16 v[208:209],v17 offset:6208
ds_read_b64_tr_b16 v[210:211],v17 offset:7744
ds_read_b64_tr_b16 v[212:213],v17 offset:9280
ds_read_b64_tr_b16 v[214:215],v17 offset:10816
ds_read_b64_tr_b16 v[216:217],v17 offset:6272
ds_read_b64_tr_b16 v[218:219],v17 offset:7808
ds_read_b64_tr_b16 v[220:221],v17 offset:9344
ds_read_b64_tr_b16 v[222:223],v17 offset:10880
s_mov_b64 exec,s[62:63]
s_add_u32 m0,s68,51840
s_add_u32 s64,s59,1474560
buffer_load_dwordx4 v27,s[36:39],s64 offen lds
s_mov_b64 exec,-1
s_setprio 0
v_mul_f32 v176,v160,v176
v_mul_f32 v177,v161,v177
v_mul_f32 v178,v162,v178
v_mul_f32 v179,v163,v179
v_mul_f32 v180,v164,v180
v_mul_f32 v181,v165,v181
v_mul_f32 v182,v166,v182
v_mul_f32 v183,v167,v183
v_mul_f32 v184,v168,v184
v_mul_f32 v185,v169,v185
v_mul_f32 v186,v170,v186
v_mul_f32 v187,v171,v187
v_mul_f32 v188,v172,v188
v_mul_f32 v189,v173,v189
v_mul_f32 v190,v174,v190
v_mul_f32 v191,v175,v191
v_cvt_pk_bf16_f32 v192,v176,v177
v_cvt_pk_bf16_f32 v193,v178,v179
v_cvt_pk_bf16_f32 v194,v180,v181
v_cvt_pk_bf16_f32 v195,v182,v183
v_cvt_pk_bf16_f32 v196,v184,v185
v_cvt_pk_bf16_f32 v197,v186,v187
v_cvt_pk_bf16_f32 v198,v188,v189
v_cvt_pk_bf16_f32 v199,v190,v191
s_waitcnt lgkmcnt(0)
s_barrier
v_fmaak_f32 v176,v144,v144,0x409d30fc
v_fmaak_f32 v177,v145,v145,0x409d30fc
v_fmaak_f32 v178,v146,v146,0x409d30fc
v_fmaak_f32 v179,v147,v147,0x409d30fc
v_fmaak_f32 v180,v148,v148,0x409d30fc
v_fmaak_f32 v181,v149,v149,0x409d30fc
v_fmaak_f32 v182,v150,v150,0x409d30fc
v_fmaak_f32 v183,v151,v151,0x409d30fc
v_fmaak_f32 v184,v152,v152,0x409d30fc
v_fmaak_f32 v185,v153,v153,0x409d30fc
v_fmaak_f32 v186,v154,v154,0x409d30fc
v_fmaak_f32 v187,v155,v155,0x409d30fc
v_fmaak_f32 v188,v156,v156,0x409d30fc
v_fmaak_f32 v189,v157,v157,0x409d30fc
v_fmaak_f32 v190,v158,v158,0x409d30fc
v_fmaak_f32 v191,v159,v159,0x409d30fc
v_mul_f32 v176,v144,v176
v_mul_f32 v177,v145,v177
v_mul_f32 v178,v146,v178
v_mul_f32 v179,v147,v179
v_mul_f32 v180,v148,v180
v_mul_f32 v181,v149,v181
v_mul_f32 v182,v150,v182
v_mul_f32 v183,v151,v183
v_mul_f32 v184,v152,v184
v_mul_f32 v185,v153,v185
v_mul_f32 v186,v154,v186
v_mul_f32 v187,v155,v187
v_mul_f32 v188,v156,v188
v_mul_f32 v189,v157,v189
v_mul_f32 v190,v158,v190
v_mul_f32 v191,v159,v191
s_setprio 1
v_mfma_f32_32x32x16_bf16 v[96:111],v[200:203],v[192:195],v[96:111]
v_exp_f32 v176,v176
v_exp_f32 v177,v177
v_exp_f32 v178,v178
v_mfma_f32_32x32x16_bf16 v[96:111],v[204:207],v[196:199],v[96:111]
v_exp_f32 v179,v179
v_exp_f32 v180,v180
v_exp_f32 v181,v181
v_mfma_f32_32x32x16_bf16 v[112:127],v[208:211],v[192:195],v[112:127]
v_exp_f32 v182,v182
v_exp_f32 v183,v183
v_exp_f32 v184,v184
v_mfma_f32_32x32x16_bf16 v[112:127],v[212:215],v[196:199],v[112:127]
v_exp_f32 v185,v185
v_exp_f32 v186,v186
v_exp_f32 v187,v187
v_mfma_f32_32x32x16_bf16 v[128:143],v[216:219],v[192:195],v[128:143]
v_exp_f32 v188,v188
v_exp_f32 v189,v189
v_mfma_f32_32x32x16_bf16 v[128:143],v[220:223],v[196:199],v[128:143]
v_exp_f32 v190,v190
v_exp_f32 v191,v191
ds_read_b64_tr_b16 v[200:201],v16 offset:0
ds_read_b64_tr_b16 v[202:203],v16 offset:512
ds_read_b64_tr_b16 v[204:205],v16 offset:1024
ds_read_b64_tr_b16 v[206:207],v16 offset:1536
ds_read_b64_tr_b16 v[208:209],v16 offset:2048
ds_read_b64_tr_b16 v[210:211],v16 offset:2560
ds_read_b64_tr_b16 v[212:213],v16 offset:3072
ds_read_b64_tr_b16 v[214:215],v16 offset:3584
ds_read_b64_tr_b16 v[216:217],v16 offset:4096
ds_read_b64_tr_b16 v[218:219],v16 offset:4608
ds_read_b64_tr_b16 v[220:221],v16 offset:5120
ds_read_b64_tr_b16 v[222:223],v16 offset:5632
ds_read_b128 v[160:163],v18 offset:1152
ds_read_b128 v[164:167],v18 offset:1184
ds_read_b128 v[168:171],v18 offset:1216
ds_read_b128 v[172:175],v18 offset:1248
s_mov_b64 exec,s[62:63]
s_add_u32 m0,s68,55296
s_add_u32 s64,s59,1572864
buffer_load_dwordx4 v27,s[36:39],s64 offen lds
s_mov_b64 exec,-1
s_setprio 0
v_add_f32 v176,1.0,v176
v_add_f32 v177,1.0,v177
v_add_f32 v178,1.0,v178
v_add_f32 v179,1.0,v179
v_add_f32 v180,1.0,v180
v_add_f32 v181,1.0,v181
v_add_f32 v182,1.0,v182
v_add_f32 v183,1.0,v183
v_add_f32 v184,1.0,v184
v_add_f32 v185,1.0,v185
v_add_f32 v186,1.0,v186
v_add_f32 v187,1.0,v187
v_add_f32 v188,1.0,v188
v_add_f32 v189,1.0,v189
v_add_f32 v190,1.0,v190
v_add_f32 v191,1.0,v191
s_setprio 1
s_waitcnt lgkmcnt(0)
v_mfma_f32_32x32x16_bf16 v[160:175],v[200:203],v[72:75],v[160:175]
v_rcp_f32 v176,v176
v_rcp_f32 v177,v177
v_rcp_f32 v178,v178
v_mfma_f32_32x32x16_bf16 v[160:175],v[204:207],v[76:79],v[160:175]
v_rcp_f32 v179,v179
v_rcp_f32 v180,v180
v_rcp_f32 v181,v181
v_mfma_f32_32x32x16_bf16 v[160:175],v[208:211],v[80:83],v[160:175]
v_rcp_f32 v182,v182
v_rcp_f32 v183,v183
v_rcp_f32 v184,v184
v_mfma_f32_32x32x16_bf16 v[160:175],v[212:215],v[84:87],v[160:175]
v_rcp_f32 v185,v185
v_rcp_f32 v186,v186
v_rcp_f32 v187,v187
v_mfma_f32_32x32x16_bf16 v[160:175],v[216:219],v[88:91],v[160:175]
v_rcp_f32 v188,v188
v_rcp_f32 v189,v189
v_mfma_f32_32x32x16_bf16 v[160:175],v[220:223],v[92:95],v[160:175]
v_rcp_f32 v190,v190
v_rcp_f32 v191,v191
ds_read_b64_tr_b16 v[200:201],v17 offset:12288
ds_read_b64_tr_b16 v[202:203],v17 offset:13824
ds_read_b64_tr_b16 v[204:205],v17 offset:15360
ds_read_b64_tr_b16 v[206:207],v17 offset:16896
ds_read_b64_tr_b16 v[208:209],v17 offset:12352
ds_read_b64_tr_b16 v[210:211],v17 offset:13888
ds_read_b64_tr_b16 v[212:213],v17 offset:15424
ds_read_b64_tr_b16 v[214:215],v17 offset:16960
ds_read_b64_tr_b16 v[216:217],v17 offset:12416
ds_read_b64_tr_b16 v[218:219],v17 offset:13952
ds_read_b64_tr_b16 v[220:221],v17 offset:15488
ds_read_b64_tr_b16 v[222:223],v17 offset:17024
s_mov_b64 exec,s[62:63]
s_add_u32 m0,s68,58752
s_add_u32 s64,s59,1671168
buffer_load_dwordx4 v27,s[36:39],s64 offen lds
s_mov_b64 exec,-1
s_setprio 0
v_mul_f32 v176,v144,v176
v_mul_f32 v177,v145,v177
v_mul_f32 v178,v146,v178
v_mul_f32 v179,v147,v179
v_mul_f32 v180,v148,v180
v_mul_f32 v181,v149,v181
v_mul_f32 v182,v150,v182
v_mul_f32 v183,v151,v183
v_mul_f32 v184,v152,v184
v_mul_f32 v185,v153,v185
v_mul_f32 v186,v154,v186
v_mul_f32 v187,v155,v187
v_mul_f32 v188,v156,v188
v_mul_f32 v189,v157,v189
v_mul_f32 v190,v158,v190
v_mul_f32 v191,v159,v191
v_cvt_pk_bf16_f32 v192,v176,v177
v_cvt_pk_bf16_f32 v193,v178,v179
v_cvt_pk_bf16_f32 v194,v180,v181
v_cvt_pk_bf16_f32 v195,v182,v183
v_cvt_pk_bf16_f32 v196,v184,v185
v_cvt_pk_bf16_f32 v197,v186,v187
v_cvt_pk_bf16_f32 v198,v188,v189
v_cvt_pk_bf16_f32 v199,v190,v191
s_waitcnt lgkmcnt(0)
s_barrier
v_fmaak_f32 v176,v160,v160,0x409d30fc
v_fmaak_f32 v177,v161,v161,0x409d30fc
v_fmaak_f32 v178,v162,v162,0x409d30fc
v_fmaak_f32 v179,v163,v163,0x409d30fc
v_fmaak_f32 v180,v164,v164,0x409d30fc
v_fmaak_f32 v181,v165,v165,0x409d30fc
v_fmaak_f32 v182,v166,v166,0x409d30fc
v_fmaak_f32 v183,v167,v167,0x409d30fc
v_fmaak_f32 v184,v168,v168,0x409d30fc
v_fmaak_f32 v185,v169,v169,0x409d30fc
v_fmaak_f32 v186,v170,v170,0x409d30fc
v_fmaak_f32 v187,v171,v171,0x409d30fc
v_fmaak_f32 v188,v172,v172,0x409d30fc
v_fmaak_f32 v189,v173,v173,0x409d30fc
v_fmaak_f32 v190,v174,v174,0x409d30fc
v_fmaak_f32 v191,v175,v175,0x409d30fc
v_mul_f32 v176,v160,v176
v_mul_f32 v177,v161,v177
v_mul_f32 v178,v162,v178
v_mul_f32 v179,v163,v179
v_mul_f32 v180,v164,v180
v_mul_f32 v181,v165,v181
v_mul_f32 v182,v166,v182
v_mul_f32 v183,v167,v183
v_mul_f32 v184,v168,v184
v_mul_f32 v185,v169,v185
v_mul_f32 v186,v170,v186
v_mul_f32 v187,v171,v187
v_mul_f32 v188,v172,v188
v_mul_f32 v189,v173,v189
v_mul_f32 v190,v174,v190
v_mul_f32 v191,v175,v191
s_setprio 1
v_mfma_f32_32x32x16_bf16 v[96:111],v[200:203],v[192:195],v[96:111]
v_exp_f32 v176,v176
v_exp_f32 v177,v177
v_exp_f32 v178,v178
v_mfma_f32_32x32x16_bf16 v[96:111],v[204:207],v[196:199],v[96:111]
v_exp_f32 v179,v179
v_exp_f32 v180,v180
v_exp_f32 v181,v181
v_mfma_f32_32x32x16_bf16 v[112:127],v[208:211],v[192:195],v[112:127]
v_exp_f32 v182,v182
v_exp_f32 v183,v183
v_exp_f32 v184,v184
v_mfma_f32_32x32x16_bf16 v[112:127],v[212:215],v[196:199],v[112:127]
v_exp_f32 v185,v185
v_exp_f32 v186,v186
v_exp_f32 v187,v187
v_mfma_f32_32x32x16_bf16 v[128:143],v[216:219],v[192:195],v[128:143]
v_exp_f32 v188,v188
v_exp_f32 v189,v189
v_mfma_f32_32x32x16_bf16 v[128:143],v[220:223],v[196:199],v[128:143]
v_exp_f32 v190,v190
v_exp_f32 v191,v191
ds_read_b64_tr_b16 v[200:201],v16 offset:6144
ds_read_b64_tr_b16 v[202:203],v16 offset:6656
ds_read_b64_tr_b16 v[204:205],v16 offset:7168
ds_read_b64_tr_b16 v[206:207],v16 offset:7680
ds_read_b64_tr_b16 v[208:209],v16 offset:8192
ds_read_b64_tr_b16 v[210:211],v16 offset:8704
ds_read_b64_tr_b16 v[212:213],v16 offset:9216
ds_read_b64_tr_b16 v[214:215],v16 offset:9728
ds_read_b64_tr_b16 v[216:217],v16 offset:10240
ds_read_b64_tr_b16 v[218:219],v16 offset:10752
ds_read_b64_tr_b16 v[220:221],v16 offset:11264
ds_read_b64_tr_b16 v[222:223],v16 offset:11776
ds_read_b128 v[144:147],v18 offset:1280
ds_read_b128 v[148:151],v18 offset:1312
ds_read_b128 v[152:155],v18 offset:1344
ds_read_b128 v[156:159],v18 offset:1376
s_setprio 0
v_add_f32 v176,1.0,v176
v_add_f32 v177,1.0,v177
v_add_f32 v178,1.0,v178
v_add_f32 v179,1.0,v179
v_add_f32 v180,1.0,v180
v_add_f32 v181,1.0,v181
v_add_f32 v182,1.0,v182
v_add_f32 v183,1.0,v183
v_add_f32 v184,1.0,v184
v_add_f32 v185,1.0,v185
v_add_f32 v186,1.0,v186
v_add_f32 v187,1.0,v187
v_add_f32 v188,1.0,v188
v_add_f32 v189,1.0,v189
v_add_f32 v190,1.0,v190
v_add_f32 v191,1.0,v191
s_setprio 1
s_waitcnt lgkmcnt(0)
v_mfma_f32_32x32x16_bf16 v[144:159],v[200:203],v[72:75],v[144:159]
v_rcp_f32 v176,v176
v_rcp_f32 v177,v177
v_rcp_f32 v178,v178
v_mfma_f32_32x32x16_bf16 v[144:159],v[204:207],v[76:79],v[144:159]
v_rcp_f32 v179,v179
v_rcp_f32 v180,v180
v_rcp_f32 v181,v181
v_mfma_f32_32x32x16_bf16 v[144:159],v[208:211],v[80:83],v[144:159]
v_rcp_f32 v182,v182
v_rcp_f32 v183,v183
v_rcp_f32 v184,v184
v_mfma_f32_32x32x16_bf16 v[144:159],v[212:215],v[84:87],v[144:159]
v_rcp_f32 v185,v185
v_rcp_f32 v186,v186
v_rcp_f32 v187,v187
v_mfma_f32_32x32x16_bf16 v[144:159],v[216:219],v[88:91],v[144:159]
v_rcp_f32 v188,v188
v_rcp_f32 v189,v189
v_mfma_f32_32x32x16_bf16 v[144:159],v[220:223],v[92:95],v[144:159]
v_rcp_f32 v190,v190
v_rcp_f32 v191,v191
ds_read_b64_tr_b16 v[200:201],v17 offset:0
ds_read_b64_tr_b16 v[202:203],v17 offset:1536
ds_read_b64_tr_b16 v[204:205],v17 offset:3072
ds_read_b64_tr_b16 v[206:207],v17 offset:4608
ds_read_b64_tr_b16 v[208:209],v17 offset:64
ds_read_b64_tr_b16 v[210:211],v17 offset:1600
ds_read_b64_tr_b16 v[212:213],v17 offset:3136
ds_read_b64_tr_b16 v[214:215],v17 offset:4672
ds_read_b64_tr_b16 v[216:217],v17 offset:128
ds_read_b64_tr_b16 v[218:219],v17 offset:1664
ds_read_b64_tr_b16 v[220:221],v17 offset:3200
ds_read_b64_tr_b16 v[222:223],v17 offset:4736
s_setprio 0
v_mul_f32 v176,v160,v176
v_mul_f32 v177,v161,v177
v_mul_f32 v178,v162,v178
v_mul_f32 v179,v163,v179
v_mul_f32 v180,v164,v180
v_mul_f32 v181,v165,v181
v_mul_f32 v182,v166,v182
v_mul_f32 v183,v167,v183
v_mul_f32 v184,v168,v184
v_mul_f32 v185,v169,v185
v_mul_f32 v186,v170,v186
v_mul_f32 v187,v171,v187
v_mul_f32 v188,v172,v188
v_mul_f32 v189,v173,v189
v_mul_f32 v190,v174,v190
v_mul_f32 v191,v175,v191
v_cvt_pk_bf16_f32 v192,v176,v177
v_cvt_pk_bf16_f32 v193,v178,v179
v_cvt_pk_bf16_f32 v194,v180,v181
v_cvt_pk_bf16_f32 v195,v182,v183
v_cvt_pk_bf16_f32 v196,v184,v185
v_cvt_pk_bf16_f32 v197,v186,v187
v_cvt_pk_bf16_f32 v198,v188,v189
v_cvt_pk_bf16_f32 v199,v190,v191
s_waitcnt lgkmcnt(0)
s_barrier
v_fmaak_f32 v176,v144,v144,0x409d30fc
v_fmaak_f32 v177,v145,v145,0x409d30fc
v_fmaak_f32 v178,v146,v146,0x409d30fc
v_fmaak_f32 v179,v147,v147,0x409d30fc
v_fmaak_f32 v180,v148,v148,0x409d30fc
v_fmaak_f32 v181,v149,v149,0x409d30fc
v_fmaak_f32 v182,v150,v150,0x409d30fc
v_fmaak_f32 v183,v151,v151,0x409d30fc
v_fmaak_f32 v184,v152,v152,0x409d30fc
v_fmaak_f32 v185,v153,v153,0x409d30fc
v_fmaak_f32 v186,v154,v154,0x409d30fc
v_fmaak_f32 v187,v155,v155,0x409d30fc
v_fmaak_f32 v188,v156,v156,0x409d30fc
v_fmaak_f32 v189,v157,v157,0x409d30fc
v_fmaak_f32 v190,v158,v158,0x409d30fc
v_fmaak_f32 v191,v159,v159,0x409d30fc
v_mul_f32 v176,v144,v176
v_mul_f32 v177,v145,v177
v_mul_f32 v178,v146,v178
v_mul_f32 v179,v147,v179
v_mul_f32 v180,v148,v180
v_mul_f32 v181,v149,v181
v_mul_f32 v182,v150,v182
v_mul_f32 v183,v151,v183
v_mul_f32 v184,v152,v184
v_mul_f32 v185,v153,v185
v_mul_f32 v186,v154,v186
v_mul_f32 v187,v155,v187
v_mul_f32 v188,v156,v188
v_mul_f32 v189,v157,v189
v_mul_f32 v190,v158,v190
v_mul_f32 v191,v159,v191
s_setprio 1
v_mfma_f32_32x32x16_bf16 v[96:111],v[200:203],v[192:195],v[96:111]
v_exp_f32 v176,v176
v_exp_f32 v177,v177
v_exp_f32 v178,v178
v_mfma_f32_32x32x16_bf16 v[96:111],v[204:207],v[196:199],v[96:111]
v_exp_f32 v179,v179
v_exp_f32 v180,v180
v_exp_f32 v181,v181
v_mfma_f32_32x32x16_bf16 v[112:127],v[208:211],v[192:195],v[112:127]
v_exp_f32 v182,v182
v_exp_f32 v183,v183
v_exp_f32 v184,v184
v_mfma_f32_32x32x16_bf16 v[112:127],v[212:215],v[196:199],v[112:127]
v_exp_f32 v185,v185
v_exp_f32 v186,v186
v_exp_f32 v187,v187
v_mfma_f32_32x32x16_bf16 v[128:143],v[216:219],v[192:195],v[128:143]
v_exp_f32 v188,v188
v_exp_f32 v189,v189
v_mfma_f32_32x32x16_bf16 v[128:143],v[220:223],v[196:199],v[128:143]
v_exp_f32 v190,v190
v_exp_f32 v191,v191
ds_read_b64_tr_b16 v[200:201],v16 offset:12288
ds_read_b64_tr_b16 v[202:203],v16 offset:12800
ds_read_b64_tr_b16 v[204:205],v16 offset:13312
ds_read_b64_tr_b16 v[206:207],v16 offset:13824
ds_read_b64_tr_b16 v[208:209],v16 offset:14336
ds_read_b64_tr_b16 v[210:211],v16 offset:14848
ds_read_b64_tr_b16 v[212:213],v16 offset:15360
ds_read_b64_tr_b16 v[214:215],v16 offset:15872
ds_read_b64_tr_b16 v[216:217],v16 offset:16384
ds_read_b64_tr_b16 v[218:219],v16 offset:16896
ds_read_b64_tr_b16 v[220:221],v16 offset:17408
ds_read_b64_tr_b16 v[222:223],v16 offset:17920
ds_read_b128 v[160:163],v18 offset:1408
ds_read_b128 v[164:167],v18 offset:1440
ds_read_b128 v[168:171],v18 offset:1472
ds_read_b128 v[172:175],v18 offset:1504
s_setprio 0
v_add_f32 v176,1.0,v176
v_add_f32 v177,1.0,v177
v_add_f32 v178,1.0,v178
v_add_f32 v179,1.0,v179
v_add_f32 v180,1.0,v180
v_add_f32 v181,1.0,v181
v_add_f32 v182,1.0,v182
v_add_f32 v183,1.0,v183
v_add_f32 v184,1.0,v184
v_add_f32 v185,1.0,v185
v_add_f32 v186,1.0,v186
v_add_f32 v187,1.0,v187
v_add_f32 v188,1.0,v188
v_add_f32 v189,1.0,v189
v_add_f32 v190,1.0,v190
v_add_f32 v191,1.0,v191
s_setprio 1
s_waitcnt lgkmcnt(0)
v_mfma_f32_32x32x16_bf16 v[160:175],v[200:203],v[72:75],v[160:175]
v_rcp_f32 v176,v176
v_rcp_f32 v177,v177
v_rcp_f32 v178,v178
v_mfma_f32_32x32x16_bf16 v[160:175],v[204:207],v[76:79],v[160:175]
v_rcp_f32 v179,v179
v_rcp_f32 v180,v180
v_rcp_f32 v181,v181
v_mfma_f32_32x32x16_bf16 v[160:175],v[208:211],v[80:83],v[160:175]
v_rcp_f32 v182,v182
v_rcp_f32 v183,v183
v_rcp_f32 v184,v184
v_mfma_f32_32x32x16_bf16 v[160:175],v[212:215],v[84:87],v[160:175]
v_rcp_f32 v185,v185
v_rcp_f32 v186,v186
v_rcp_f32 v187,v187
v_mfma_f32_32x32x16_bf16 v[160:175],v[216:219],v[88:91],v[160:175]
v_rcp_f32 v188,v188
v_rcp_f32 v189,v189
v_mfma_f32_32x32x16_bf16 v[160:175],v[220:223],v[92:95],v[160:175]
v_rcp_f32 v190,v190
v_rcp_f32 v191,v191
ds_read_b64_tr_b16 v[200:201],v17 offset:6144
ds_read_b64_tr_b16 v[202:203],v17 offset:7680
ds_read_b64_tr_b16 v[204:205],v17 offset:9216
ds_read_b64_tr_b16 v[206:207],v17 offset:10752
ds_read_b64_tr_b16 v[208:209],v17 offset:6208
ds_read_b64_tr_b16 v[210:211],v17 offset:7744
ds_read_b64_tr_b16 v[212:213],v17 offset:9280
ds_read_b64_tr_b16 v[214:215],v17 offset:10816
ds_read_b64_tr_b16 v[216:217],v17 offset:6272
ds_read_b64_tr_b16 v[218:219],v17 offset:7808
ds_read_b64_tr_b16 v[220:221],v17 offset:9344
ds_read_b64_tr_b16 v[222:223],v17 offset:10880
s_setprio 0
v_mul_f32 v176,v144,v176
v_mul_f32 v177,v145,v177
v_mul_f32 v178,v146,v178
v_mul_f32 v179,v147,v179
v_mul_f32 v180,v148,v180
v_mul_f32 v181,v149,v181
v_mul_f32 v182,v150,v182
v_mul_f32 v183,v151,v183
v_mul_f32 v184,v152,v184
v_mul_f32 v185,v153,v185
v_mul_f32 v186,v154,v186
v_mul_f32 v187,v155,v187
v_mul_f32 v188,v156,v188
v_mul_f32 v189,v157,v189
v_mul_f32 v190,v158,v190
v_mul_f32 v191,v159,v191
v_cvt_pk_bf16_f32 v192,v176,v177
v_cvt_pk_bf16_f32 v193,v178,v179
v_cvt_pk_bf16_f32 v194,v180,v181
v_cvt_pk_bf16_f32 v195,v182,v183
v_cvt_pk_bf16_f32 v196,v184,v185
v_cvt_pk_bf16_f32 v197,v186,v187
v_cvt_pk_bf16_f32 v198,v188,v189
v_cvt_pk_bf16_f32 v199,v190,v191
s_waitcnt lgkmcnt(0)
s_barrier
v_fmaak_f32 v176,v160,v160,0x409d30fc
v_fmaak_f32 v177,v161,v161,0x409d30fc
v_fmaak_f32 v178,v162,v162,0x409d30fc
v_fmaak_f32 v179,v163,v163,0x409d30fc
v_fmaak_f32 v180,v164,v164,0x409d30fc
v_fmaak_f32 v181,v165,v165,0x409d30fc
v_fmaak_f32 v182,v166,v166,0x409d30fc
v_fmaak_f32 v183,v167,v167,0x409d30fc
v_fmaak_f32 v184,v168,v168,0x409d30fc
v_fmaak_f32 v185,v169,v169,0x409d30fc
v_fmaak_f32 v186,v170,v170,0x409d30fc
v_fmaak_f32 v187,v171,v171,0x409d30fc
v_fmaak_f32 v188,v172,v172,0x409d30fc
v_fmaak_f32 v189,v173,v173,0x409d30fc
v_fmaak_f32 v190,v174,v174,0x409d30fc
v_fmaak_f32 v191,v175,v175,0x409d30fc
v_mul_f32 v176,v160,v176
v_mul_f32 v177,v161,v177
v_mul_f32 v178,v162,v178
v_mul_f32 v179,v163,v179
v_mul_f32 v180,v164,v180
v_mul_f32 v181,v165,v181
v_mul_f32 v182,v166,v182
v_mul_f32 v183,v167,v183
v_mul_f32 v184,v168,v184
v_mul_f32 v185,v169,v185
v_mul_f32 v186,v170,v186
v_mul_f32 v187,v171,v187
v_mul_f32 v188,v172,v188
v_mul_f32 v189,v173,v189
v_mul_f32 v190,v174,v190
v_mul_f32 v191,v175,v191
s_setprio 1
v_mfma_f32_32x32x16_bf16 v[96:111],v[200:203],v[192:195],v[96:111]
v_exp_f32 v176,v176
v_exp_f32 v177,v177
v_exp_f32 v178,v178
v_mfma_f32_32x32x16_bf16 v[96:111],v[204:207],v[196:199],v[96:111]
v_exp_f32 v179,v179
v_exp_f32 v180,v180
v_exp_f32 v181,v181
v_mfma_f32_32x32x16_bf16 v[112:127],v[208:211],v[192:195],v[112:127]
v_exp_f32 v182,v182
v_exp_f32 v183,v183
v_exp_f32 v184,v184
v_mfma_f32_32x32x16_bf16 v[112:127],v[212:215],v[196:199],v[112:127]
v_exp_f32 v185,v185
v_exp_f32 v186,v186
v_exp_f32 v187,v187
v_mfma_f32_32x32x16_bf16 v[128:143],v[216:219],v[192:195],v[128:143]
v_exp_f32 v188,v188
v_exp_f32 v189,v189
v_mfma_f32_32x32x16_bf16 v[128:143],v[220:223],v[196:199],v[128:143]
v_exp_f32 v190,v190
v_exp_f32 v191,v191
ds_read_b64 v[72:73],v20 offset:0
ds_read_b64 v[74:75],v21 offset:0
ds_read_b64 v[76:77],v22 offset:0
ds_read_b64 v[78:79],v23 offset:0
ds_read_b64 v[80:81],v20 offset:64
ds_read_b64 v[82:83],v21 offset:64
ds_read_b64 v[84:85],v22 offset:64
ds_read_b64 v[86:87],v23 offset:64
ds_read_b64 v[88:89],v20 offset:128
ds_read_b64 v[90:91],v21 offset:128
ds_read_b64 v[92:93],v22 offset:128
ds_read_b64 v[94:95],v23 offset:128
s_setprio 0
v_add_f32 v176,1.0,v176
v_add_f32 v177,1.0,v177
v_add_f32 v178,1.0,v178
v_add_f32 v179,1.0,v179
v_add_f32 v180,1.0,v180
v_add_f32 v181,1.0,v181
v_add_f32 v182,1.0,v182
v_add_f32 v183,1.0,v183
v_add_f32 v184,1.0,v184
v_add_f32 v185,1.0,v185
v_add_f32 v186,1.0,v186
v_add_f32 v187,1.0,v187
v_add_f32 v188,1.0,v188
v_add_f32 v189,1.0,v189
v_add_f32 v190,1.0,v190
v_add_f32 v191,1.0,v191
s_setprio 1
s_waitcnt lgkmcnt(0)
v_rcp_f32 v176,v176
v_rcp_f32 v177,v177
v_rcp_f32 v178,v178
v_rcp_f32 v179,v179
v_rcp_f32 v180,v180
v_rcp_f32 v181,v181
v_rcp_f32 v182,v182
v_rcp_f32 v183,v183
v_rcp_f32 v184,v184
v_rcp_f32 v185,v185
v_rcp_f32 v186,v186
v_rcp_f32 v187,v187
v_rcp_f32 v188,v188
v_rcp_f32 v189,v189
v_rcp_f32 v190,v190
v_rcp_f32 v191,v191
ds_read_b64_tr_b16 v[200:201],v17 offset:12288
ds_read_b64_tr_b16 v[202:203],v17 offset:13824
ds_read_b64_tr_b16 v[204:205],v17 offset:15360
ds_read_b64_tr_b16 v[206:207],v17 offset:16896
ds_read_b64_tr_b16 v[208:209],v17 offset:12352
ds_read_b64_tr_b16 v[210:211],v17 offset:13888
ds_read_b64_tr_b16 v[212:213],v17 offset:15424
ds_read_b64_tr_b16 v[214:215],v17 offset:16960
ds_read_b64_tr_b16 v[216:217],v17 offset:12416
ds_read_b64_tr_b16 v[218:219],v17 offset:13952
ds_read_b64_tr_b16 v[220:221],v17 offset:15488
ds_read_b64_tr_b16 v[222:223],v17 offset:17024
s_setprio 0
v_mul_f32 v176,v160,v176
v_mul_f32 v177,v161,v177
v_mul_f32 v178,v162,v178
v_mul_f32 v179,v163,v179
v_mul_f32 v180,v164,v180
v_mul_f32 v181,v165,v181
v_mul_f32 v182,v166,v182
v_mul_f32 v183,v167,v183
v_mul_f32 v184,v168,v184
v_mul_f32 v185,v169,v185
v_mul_f32 v186,v170,v186
v_mul_f32 v187,v171,v187
v_mul_f32 v188,v172,v188
v_mul_f32 v189,v173,v189
v_mul_f32 v190,v174,v190
v_mul_f32 v191,v175,v191
v_cvt_pk_bf16_f32 v192,v176,v177
v_cvt_pk_bf16_f32 v193,v178,v179
v_cvt_pk_bf16_f32 v194,v180,v181
v_cvt_pk_bf16_f32 v195,v182,v183
v_cvt_pk_bf16_f32 v196,v184,v185
v_cvt_pk_bf16_f32 v197,v186,v187
v_cvt_pk_bf16_f32 v198,v188,v189
v_cvt_pk_bf16_f32 v199,v190,v191
ds_read_b128 v[144:147],v18 offset:1536
ds_read_b128 v[148:151],v18 offset:1568
ds_read_b128 v[152:155],v18 offset:1600
ds_read_b128 v[156:159],v18 offset:1632
ds_read_b128 v[160:163],v18 offset:1664
ds_read_b128 v[164:167],v18 offset:1696
ds_read_b128 v[168:171],v18 offset:1728
ds_read_b128 v[172:175],v18 offset:1760
ds_read_b128 v[176:179],v18 offset:1792
ds_read_b128 v[180:183],v18 offset:1824
ds_read_b128 v[184:187],v18 offset:1856
ds_read_b128 v[188:191],v18 offset:1888
s_waitcnt lgkmcnt(12)
s_setprio 1
v_mfma_f32_32x32x16_bf16 v[96:111],v[200:203],v[192:195],v[96:111]
v_lshlrev_b32 v232,16,v72
v_lshlrev_b32 v233,16,v73
v_lshlrev_b32 v234,16,v74
v_lshlrev_b32 v235,16,v75
v_mfma_f32_32x32x16_bf16 v[96:111],v[204:207],v[196:199],v[96:111]
v_lshlrev_b32 v236,16,v76
v_lshlrev_b32 v237,16,v77
v_lshlrev_b32 v238,16,v78
v_lshlrev_b32 v239,16,v79
v_mfma_f32_32x32x16_bf16 v[112:127],v[208:211],v[192:195],v[112:127]
v_lshlrev_b32 v240,16,v80
v_lshlrev_b32 v241,16,v81
v_lshlrev_b32 v242,16,v82
v_lshlrev_b32 v243,16,v83
v_mfma_f32_32x32x16_bf16 v[112:127],v[212:215],v[196:199],v[112:127]
v_lshlrev_b32 v244,16,v84
v_lshlrev_b32 v245,16,v85
v_lshlrev_b32 v246,16,v86
v_lshlrev_b32 v247,16,v87
v_mfma_f32_32x32x16_bf16 v[128:143],v[216:219],v[192:195],v[128:143]
v_lshlrev_b32 v248,16,v88
v_lshlrev_b32 v249,16,v89
v_lshlrev_b32 v250,16,v90
v_lshlrev_b32 v251,16,v91
v_mfma_f32_32x32x16_bf16 v[128:143],v[220:223],v[196:199],v[128:143]
v_lshlrev_b32 v252,16,v92
v_lshlrev_b32 v253,16,v93
v_lshlrev_b32 v254,16,v94
v_lshlrev_b32 v255,16,v95
s_setprio 0
s_waitcnt lgkmcnt(0)
v_and_b32 v72,0xffff0000,v72
v_and_b32 v73,0xffff0000,v73
v_and_b32 v74,0xffff0000,v74
v_and_b32 v75,0xffff0000,v75
v_and_b32 v76,0xffff0000,v76
v_and_b32 v77,0xffff0000,v77
v_and_b32 v78,0xffff0000,v78
v_and_b32 v79,0xffff0000,v79
v_and_b32 v80,0xffff0000,v80
v_and_b32 v81,0xffff0000,v81
v_and_b32 v82,0xffff0000,v82
v_and_b32 v83,0xffff0000,v83
v_and_b32 v84,0xffff0000,v84
v_and_b32 v85,0xffff0000,v85
v_and_b32 v86,0xffff0000,v86
v_and_b32 v87,0xffff0000,v87
v_and_b32 v88,0xffff0000,v88
v_and_b32 v89,0xffff0000,v89
v_and_b32 v90,0xffff0000,v90
v_and_b32 v91,0xffff0000,v91
v_and_b32 v92,0xffff0000,v92
v_and_b32 v93,0xffff0000,v93
v_and_b32 v94,0xffff0000,v94
v_and_b32 v95,0xffff0000,v95
v_fma_f32 v232,v96,v144,v232
v_fma_f32 v72,v97,v145,v72
v_fma_f32 v233,v98,v146,v233
v_fma_f32 v73,v99,v147,v73
v_fma_f32 v234,v100,v148,v234
v_fma_f32 v74,v101,v149,v74
v_fma_f32 v235,v102,v150,v235
v_fma_f32 v75,v103,v151,v75
v_fma_f32 v236,v104,v152,v236
v_fma_f32 v76,v105,v153,v76
v_fma_f32 v237,v106,v154,v237
v_fma_f32 v77,v107,v155,v77
v_fma_f32 v238,v108,v156,v238
v_fma_f32 v78,v109,v157,v78
v_fma_f32 v239,v110,v158,v239
v_fma_f32 v79,v111,v159,v79
v_fma_f32 v240,v112,v160,v240
v_fma_f32 v80,v113,v161,v80
v_fma_f32 v241,v114,v162,v241
v_fma_f32 v81,v115,v163,v81
v_fma_f32 v242,v116,v164,v242
v_fma_f32 v82,v117,v165,v82
v_fma_f32 v243,v118,v166,v243
v_fma_f32 v83,v119,v167,v83
v_fma_f32 v244,v120,v168,v244
v_fma_f32 v84,v121,v169,v84
v_fma_f32 v245,v122,v170,v245
v_fma_f32 v85,v123,v171,v85
v_fma_f32 v246,v124,v172,v246
v_fma_f32 v86,v125,v173,v86
v_fma_f32 v247,v126,v174,v247
v_fma_f32 v87,v127,v175,v87
v_fma_f32 v248,v128,v176,v248
v_fma_f32 v88,v129,v177,v88
v_fma_f32 v249,v130,v178,v249
v_fma_f32 v89,v131,v179,v89
v_fma_f32 v250,v132,v180,v250
v_fma_f32 v90,v133,v181,v90
v_fma_f32 v251,v134,v182,v251
v_fma_f32 v91,v135,v183,v91
v_fma_f32 v252,v136,v184,v252
v_fma_f32 v92,v137,v185,v92
v_fma_f32 v253,v138,v186,v253
v_fma_f32 v93,v139,v187,v93
v_fma_f32 v254,v140,v188,v254
v_fma_f32 v94,v141,v189,v94
v_fma_f32 v255,v142,v190,v255
v_fma_f32 v95,v143,v191,v95
v_add_u32 v4,s54,v4
v_add_u32 v5,s54,v5
v_add_u32 v6,s54,v6
v_add_u32 v7,s54,v7
v_add_u32 v8,s54,v8
v_add_u32 v9,s54,v9
v_add_u32 v10,s54,v10
v_add_u32 v11,s54,v11
v_add_u32 v12,s54,v12
v_add_u32 v13,s54,v13
v_add_u32 v14,s54,v14
v_add_u32 v15,s54,v15
v_cvt_pk_bf16_f32 v200,v232,v72
v_cvt_pk_bf16_f32 v201,v233,v73
v_cvt_pk_bf16_f32 v202,v234,v74
v_cvt_pk_bf16_f32 v203,v235,v75
v_cvt_pk_bf16_f32 v204,v236,v76
v_cvt_pk_bf16_f32 v205,v237,v77
v_cvt_pk_bf16_f32 v206,v238,v78
v_cvt_pk_bf16_f32 v207,v239,v79
v_cvt_pk_bf16_f32 v208,v240,v80
v_cvt_pk_bf16_f32 v209,v241,v81
v_cvt_pk_bf16_f32 v210,v242,v82
v_cvt_pk_bf16_f32 v211,v243,v83
v_cvt_pk_bf16_f32 v212,v244,v84
v_cvt_pk_bf16_f32 v213,v245,v85
v_cvt_pk_bf16_f32 v214,v246,v86
v_cvt_pk_bf16_f32 v215,v247,v87
v_cvt_pk_bf16_f32 v216,v248,v88
v_cvt_pk_bf16_f32 v217,v249,v89
v_cvt_pk_bf16_f32 v218,v250,v90
v_cvt_pk_bf16_f32 v219,v251,v91
v_cvt_pk_bf16_f32 v220,v252,v92
v_cvt_pk_bf16_f32 v221,v253,v93
v_cvt_pk_bf16_f32 v222,v254,v94
v_cvt_pk_bf16_f32 v223,v255,v95
ds_write_b64 v20,v[200:201] offset:0
ds_write_b64 v21,v[202:203] offset:0
ds_write_b64 v22,v[204:205] offset:0
ds_write_b64 v23,v[206:207] offset:0
ds_write_b64 v20,v[208:209] offset:64
ds_write_b64 v21,v[210:211] offset:64
ds_write_b64 v22,v[212:213] offset:64
ds_write_b64 v23,v[214:215] offset:64
ds_write_b64 v20,v[216:217] offset:128
ds_write_b64 v21,v[218:219] offset:128
ds_write_b64 v22,v[220:221] offset:128
ds_write_b64 v23,v[222:223] offset:128
ds_read_b128 v[72:75],v19 offset:0
ds_read_b128 v[76:79],v19 offset:1024
ds_read_b128 v[80:83],v19 offset:2048
ds_read_b128 v[84:87],v19 offset:3456
ds_read_b128 v[88:91],v19 offset:4480
ds_read_b128 v[92:95],v19 offset:5504
v_add_u32 v20,s54,v20
v_add_u32 v21,s54,v21
v_add_u32 v22,s54,v22
v_add_u32 v23,s54,v23
s_sub_u32 s68,s68,s54
s_add_u32 s59,s59,1572864
s_add_u32 s53,s53,1
s_cmp_eq_u32 s53,3
s_cselect_b32 s59,0x70000000,s59
s_add_u32 s67,s69,98304
s_waitcnt lgkmcnt(5)
buffer_store_dwordx4 v[72:75],v24,s[40:43],s69 offen nt
s_waitcnt lgkmcnt(4)
buffer_store_dwordx4 v[76:79],v25,s[40:43],s69 offen nt
s_waitcnt lgkmcnt(3)
buffer_store_dwordx4 v[80:83],v26,s[40:43],s69 offen nt
s_waitcnt lgkmcnt(2)
buffer_store_dwordx4 v[84:87],v24,s[40:43],s67 offen nt
s_waitcnt lgkmcnt(1)
buffer_store_dwordx4 v[88:91],v25,s[40:43],s67 offen nt
s_waitcnt lgkmcnt(0)
buffer_store_dwordx4 v[92:95],v26,s[40:43],s67 offen nt
v_add_u32 v19,s54,v19
s_add_u32 s69,s69,1572864
s_sub_u32 s54,0,s54
s_cmp_lt_u32 s53,4
s_cbranch_scc1 L_pass1_%=
L_end_%=:
s_waitcnt vmcnt(0) lgkmcnt(0)
s_endpgm
.p2align 12
)ASM"
    :: "s"(blockIdx.x), "v"(tid), "s"((unsigned long long)__builtin_amdgcn_kernarg_segment_ptr())
    : "v1", "v2", "v3", "v4", "v5", "v6", "v7", "v8", "v9", "v10", "v11", "v12", "v13", "v14", "v15", "v16", "v17", "v18", "v19", "v20", "v21", "v22", "v23", "v24", "v25", "v26", "v27", "v28", "v29", "v30", "v31", "v32", "v33", "v34", "v35", "v36", "v37", "v38", "v39", "v40", "v41", "v42", "v43", "v44", "v45", "v46", "v47", "v48", "v49", "v50", "v51", "v52", "v53", "v54", "v55", "v56", "v57", "v58", "v59", "v60", "v61", "v62", "v63", "v64", "v65", "v66", "v67", "v68", "v69", "v70", "v71", "v72", "v73", "v74", "v75", "v76", "v77", "v78", "v79", "v80", "v81", "v82", "v83", "v84", "v85", "v86", "v87", "v88", "v89", "v90", "v91", "v92", "v93", "v94", "v95", "v96", "v97", "v98", "v99", "v100", "v101", "v102", "v103", "v104", "v105", "v106", "v107", "v108", "v109", "v110", "v111", "v112", "v113", "v114", "v115", "v116", "v117", "v118", "v119", "v120", "v121", "v122", "v123", "v124", "v125", "v126", "v127", "v128", "v129", "v130", "v131", "v132", "v133", "v134", "v135", "v136", "v137", "v138", "v139", "v140", "v141", "v142", "v143", "v144", "v145", "v146", "v147", "v148", "v149", "v150", "v151", "v152", "v153", "v154", "v155", "v156", "v157", "v158", "v159", "v160", "v161", "v162", "v163", "v164", "v165", "v166", "v167", "v168", "v169", "v170", "v171", "v172", "v173", "v174", "v175", "v176", "v177", "v178", "v179", "v180", "v181", "v182", "v183", "v184", "v185", "v186", "v187", "v188", "v189", "v190", "v191", "v192", "v193", "v194", "v195", "v196", "v197", "v198", "v199", "v200", "v201", "v202", "v203", "v204", "v205", "v206", "v207", "v208", "v209", "v210", "v211", "v212", "v213", "v214", "v215", "v216", "v217", "v218", "v219", "v220", "v221", "v222", "v223", "v224", "v225", "v226", "v227", "v228", "v229", "v230", "v231", "v232", "v233", "v234", "v235", "v236", "v237", "v238", "v239", "v240", "v241", "v242", "v243", "v244", "v245", "v246", "v247", "v248", "v249", "v250", "v251", "v252", "v253", "v254", "v255", "s4", "s5", "s6", "s7", "s8", "s9", "s10", "s11", "s12", "s13", "s14", "s15", "s16", "s17", "s18", "s19", "s20", "s21", "s22", "s23", "s24", "s25", "s26", "s27", "s28", "s29", "s30", "s31", "s32", "s33", "s34", "s35", "s36", "s37", "s38", "s39", "s40", "s41", "s42", "s43", "s44", "s45", "s46", "s47", "s48", "s49", "s50", "s51", "s52", "s53", "s54", "s55", "s56", "s57", "s58", "s59", "s60", "s61", "s62", "s63", "s64", "s65", "s66", "s67", "s68", "s69", "s70", "s71", "s72", "s73", "s74", "s75", "s76", "s77", "s78", "s79", "s80", "s81", "s82", "s83", "s84", "s85", "s86", "s87", "s88", "s89", "s90", "s91", "s92", "s93", "s94", "s95", "s96", "s97", "s98", "s99", "s100", "s101", "vcc", "scc", "memory");
}

extern "C" void kernel_launch(void* const* d_in, const int* in_sizes, int n_in,
                              void* d_out, int out_size, void* d_ws, size_t ws_size,
                              hipStream_t stream) {
    (void)hipFuncSetAttribute((const void*)convnext_kernel, hipFuncAttributeMaxDynamicSharedMemorySize, LDS_BYTES);
    convnext_kernel<<<256, 512, LDS_BYTES, stream>>>(
        (const unsigned short*)d_in[0], (const unsigned short*)d_in[1], (const unsigned short*)d_in[2], (const unsigned short*)d_in[3],
        (const unsigned short*)d_in[4], (const unsigned short*)d_in[5], (const unsigned short*)d_in[6], (unsigned short*)d_out);
}
```

```cpp
#include <hip/hip_runtime.h>
#include <stdint.h>

constexpr int LDS_BYTES = 163584;

__global__ __launch_bounds__(512, 2) void convnext_kernel(
    const unsigned short* __restrict__ x, const unsigned short* __restrict__ dw, const unsigned short* __restrict__ w1,
    const unsigned short* __restrict__ b1, const unsigned short* __restrict__ w2, const unsigned short* __restrict__ b2,
    const unsigned short* __restrict__ gamma, unsigned short* __restrict__ out) {
    const unsigned tid = threadIdx.x;
    asm volatile(
R"ASM(
N_main_%=:
s_load_dwordx16 s[20:35],%2,0
v_lshrrev_b32 v236,6,%1
v_and_b32 v232,63,%1
v_readfirstlane_b32 s52,v236
s_getpc_b64 s[14:15]
N_ep_%=:
v_lshlrev_b32 v252,7,v232
v_lshrrev_b32 v253,8,%1
v_mul_u32_u24 v253,N_B_%=-N_ep_%=,v253
v_add_u32 v252,v252,v253
global_load_dword v254,v252,s[14:15]
v_mov_b32 v228,0
v_mov_b32 v229,0
v_mov_b32 v230,0
v_mov_b32 v231,0
s_and_b32 s64,%0,7
s_lshr_b32 s70,%0,3
s_lshl_b32 s71,s64,6
s_mul_i32 s68,s70,3072
s_and_b32 s64,s52,3
s_mul_i32 s67,s64,54
s_cmp_eq_u32 s70,0
s_cselect_b32 s66,0,99
s_cmp_eq_u32 s70,31
s_cselect_b32 s66,17,s66
v_add_u32 v236,s67,v232
s_mul_i32 s57,s64,864
s_add_u32 s57,s57,73728
v_mul_u32_u24 v237,0xaaab,v236
s_sub_u32 s65,s71,1
v_lshrrev_b32 v237,19,v237
s_mul_i32 s56,s65,98304
v_mul_u32_u24 v238,12,v237
s_add_u32 s56,s56,s68
v_sub_u32 v238,v236,v238
s_cmp_lt_i32 s65,0
s_cselect_b32 s101,0x70000000,s56
v_add_u32 v236,1,v237
s_mov_b32 s62,-1
s_mov_b32 s63,0x3fffff
v_bfe_u32 v236,v236,2,2
s_mov_b32 s38,50331840
s_mov_b32 s39,0x00020000
v_xor_b32 v238,v238,v236
s_mov_b32 s96,0x8befe000
s_mov_b32 s99,0x72800000
v_mul_u32_u24 v236,192,v237
s_mov_b32 s98,0xffff0000
s_bitcmp1_b32 s52,1
s_cselect_b64 vcc,-1,0
v_lshl_add_u32 v241,v238,4,v236
v_cmp_eq_u32_e64 s[92:93],s66,v237
v_mov_b32 v236,0x7fff0000
s_nop 0
v_cndmask_b32_e64 v241,v241,v236,s[92:93]
s_cmp_ge_u32 s52,4
s_cbranch_scc1 N_B_%=
s_setprio 3
s_waitcnt lgkmcnt(0)
s_sub_u32 s36,s20,192
s_subb_u32 s37,s21,0
s_and_b32 s37,s37,0xffff
s_bitcmp0_b32 s52,0
s_cselect_b64 s[44:45],s[24:25],s[28:29]
s_and_b32 s45,s45,0xffff
s_mov_b32 s40,s34
s_and_b32 s41,s35,0xffff
s_mov_b64 exec,s[62:63]
s_add_u32 m0,s57,0
s_nop 0
buffer_load_dwordx4 v241,s[36:39],s101 offen lds
s_add_u32 m0,s57,3488
s_add_u32 s101,s56,98304
buffer_load_dwordx4 v241,s[36:39],s101 offen lds
s_add_u32 m0,s57,6976
s_add_u32 s101,s56,196608
buffer_load_dwordx4 v241,s[36:39],s101 offen lds
s_add_u32 m0,s57,10464
s_add_u32 s101,s56,294912
buffer_load_dwordx4 v241,s[36:39],s101 offen lds
s_mov_b64 exec,-1
s_and_b32 s66,s52,3
s_cmp_eq_u32 s66,3
s_cselect_b32 s14,1,s66
v_min_u32 v236,47,v232
s_mul_i32 s64,s14,192
s_lshl_b32 s65,s14,7
v_lshl_add_u32 v200,v236,2,s64
s_add_u32 s65,s65,144256
v_and_b32 v199,255,%1
v_min_u32 v237,191,v199
v_lshlrev_b32 v201,2,v237
v_min_u32 v249,95,v199
v_lshlrev_b32 v202,1,v249
s_lshl_b32 s64,s66,3
s_lshr_b32 s67,0x30011100,s64
s_and_b32 s67,s67,0xff
s_lshl_b32 s67,s67,3
s_add_u32 s67,s67,143488
v_lshrrev_b32 v238,2,v236
v_and_b32 v239,3,v236
v_mul_u32_u24 v238,1152,v238
v_lshl_add_u32 v238,v239,5,v238
s_and_b32 s66,s66,1
s_lshl_b32 s64,s66,3
s_cmp_eq_u32 s66,0
s_movk_i32 s66,128
s_cselect_b32 s66,640,s66
v_add_u32 v203,s65,v238
v_add_u32 v209,s67,v238
v_lshl_add_u32 v204,v239,2,v203
v_add_u32 v215,s66,v209
v_xor_b32 v237,s64,v209
v_lshrrev_b32 v239,1,v239
v_xor_b32 v238,s64,v215
ds_write_b128 v203,v[228:231] offset:0
ds_write_b128 v203,v[228:231] offset:16
ds_write2_b64 v209,v[228:229],v[228:229] offset0:0 offset1:2
ds_write2_b64 v215,v[228:229],v[228:229] offset0:0 offset1:2
ds_write2_b64 v237,v[228:229],v[228:229] offset0:0 offset1:2
ds_write2_b64 v238,v[228:229],v[228:229] offset0:0 offset1:2
v_lshl_add_u32 v209,v239,2,v209
v_lshl_add_u32 v215,v239,2,v215
ds_write_b32 v228,v228 offset:11648
global_load_dword v242,v200,s[22:23] offset:0
global_load_dword v243,v200,s[22:23] offset:576
global_load_dword v244,v200,s[22:23] offset:1152
global_load_ushort v247,v202,s[32:33]
global_load_dword v245,v201,s[26:27]
global_load_ushort v246,v202,s[30:31]
s_mov_b64 exec,s[62:63]
s_add_u32 m0,s57,13952
s_add_u32 s101,s56,393216
buffer_load_dwordx4 v241,s[36:39],s101 offen lds
s_add_u32 m0,s57,17440
s_add_u32 s101,s56,491520
buffer_load_dwordx4 v241,s[36:39],s101 offen lds
s_add_u32 m0,s57,20928
s_add_u32 s101,s56,589824
buffer_load_dwordx4 v241,s[36:39],s101 offen lds
s_add_u32 m0,s57,24416
s_add_u32 s101,s56,688128
buffer_load_dwordx4 v241,s[36:39],s101 offen lds
s_add_u32 m0,s57,27904
s_add_u32 s101,s56,786432
buffer_load_dwordx4 v241,s[36:39],s101 offen lds
s_add_u32 m0,s57,31392
s_add_u32 s101,s56,884736
buffer_load_dwordx4 v241,s[36:39],s101 offen lds
s_mov_b64 exec,-1
s_and_b32 s64,s52,3
s_lshl_b32 s10,s64,2
s_add_u32 s64,s10,s71
s_mul_i32 s61,s64,98304
s_add_u32 s61,s61,s68
s_mov_b32 s54,0x44ea44ea
s_mov_b32 s55,0x877f877f
s_mov_b32 s8,0x0ff00ff0
s_mov_b32 s9,0x0ff00ff0
s_and_b32 s64,s52,3
s_mul_i32 s67,s64,54
s_cmp_eq_u32 s70,31
s_cselect_b32 s66,17,99
s_mov_b32 s12,0xaaaaaaaa
s_mov_b32 s13,0xaaaaaaaa
s_mov_b32 s42,50331648
s_mov_b32 s43,0x00020000
s_bitcmp0_b32 s52,0
s_mov_b32 s58,40896
s_cselect_b32 s58,16256,s58
s_cselect_b32 s59,64,6144
s_movk_i32 s53,1024
s_cselect_b32 s53,6144,s53
s_cselect_b32 s60,0,67584
s_cselect_b32 s97,64,0
s_cselect_b32 s69,0,0x70000000
s_mov_b32 s100,73728
s_cselect_b32 s100,110688,s100
s_cselect_b32 s64,16,0
s_movk_i32 s50,1032
s_cselect_b32 s50,1024,s50
s_cselect_b32 s51,s53,-72704
s_mov_b32 s46,73728
s_mov_b32 s47,0x00020000
v_mov_b32 v1,0
v_lshlrev_b32 v233,4,v232
v_lshlrev_b32 v234,3,%1
v_add_u32 v233,157312,v233
v_add_u32 v234,157312,v234
v_lshrrev_b32 v236,5,v249
v_bfe_u32 v237,v249,2,1
v_and_b32 v238,3,v249
v_lshlrev_b32 v206,7,v236
v_lshl_add_u32 v206,v237,5,v206
v_lshl_add_u32 v206,v238,2,v206
v_bfe_u32 v239,v249,3,1
v_lshl_add_u32 v206,v239,6,v206
v_bfe_u32 v239,v249,4,1
v_lshl_add_u32 v206,v239,4,v206
v_add_u32 v206,9728,v206
v_lshl_add_u32 v207,v236,1,v237
v_lshlrev_b32 v207,4,v207
v_bfe_u32 v239,v249,3,2
v_lshl_add_u32 v207,v239,2,v207
v_add_u32 v207,v207,v238
v_lshlrev_b32 v207,4,v207
v_add_u32 v207,10112,v207
v_lshlrev_b32 v208,4,v238
v_cmp_eq_u32_e64 s[48:49],1,v237
v_lshlrev_b32 v205,1,v201
v_add_u32 v205,8192,v205
v_mov_b32 v221,0
v_mov_b32 v222,0x3f800000
s_waitcnt vmcnt(6)
v_and_b32 v212,0xffff,v242
v_and_b32 v242,s98,v242
ds_write2_b32 v209,v212,v242 offset0:0 offset1:4
v_and_b32 v213,0xffff,v243
v_and_b32 v243,s98,v243
ds_write2_b32 v215,v213,v243 offset0:0 offset1:4
v_and_b32 v214,0xffff,v244
v_and_b32 v244,s98,v244
ds_write2_b32 v204,v214,v244 offset0:0 offset1:4
v_lshlrev_b32 v247,16,v247
v_lshlrev_b32 v246,16,v246
v_mul_f32 v247,0x6c800000,v247
v_mul_f32 v246,0x12800000,v246
v_rcp_f32 v224,v247
v_lshlrev_b32 v210,16,v245
v_and_b32 v211,s98,v245
v_med3_f32 v224,v224,v222,-v222
v_pk_mul_f32 v[210:211],v[210:211],s[96:97] op_sel_hi:[1,0]
v_cvt_pk_bf16_f32 v220,v224,v228
ds_write_b64 v205,v[210:211]
ds_write_b32 v206,v246 offset:2048
v_lshlrev_b32 v222,16,v220
v_rcp_f32 v222,v222
v_lshlrev_b64 v[220:221],v208,v[220:221]
v_cndmask_b32_e64 v224,v220,0,s[48:49]
v_cndmask_b32_e64 v226,0,v220,s[48:49]
v_cndmask_b32_e64 v225,v221,0,s[48:49]
v_cndmask_b32_e64 v227,0,v221,s[48:49]
ds_write_b128 v207,v[224:227]
ds_write_b32 v206,v222
s_waitcnt lgkmcnt(0)
s_lshl_b32 s4,s52,1
s_and_b32 s5,s52,1
s_sub_u32 s4,s4,s5
s_sub_u32 s6,s4,s10
s_mul_i32 s4,s4,3488
s_mul_i32 s6,s6,98304
s_add_u32 s49,s61,s6
v_mov_b32 v124,s4
s_mov_b64 exec,s[8:9]
v_add_u32 v124,6976,v124
s_mov_b64 exec,-1
s_waitcnt vmcnt(0)
s_barrier
s_setprio 3
ds_read_b128 v[12:15],v233 offset:2048
ds_read_b128 v[8:11],v233 offset:1024
ds_read_b128 v[4:7],v233 offset:0
ds_read_b64 v[2:3],v234 offset:3072
s_waitcnt lgkmcnt(2)
v_xad_u32 v125,v15,16,v124
v_xad_u32 v126,v15,32,v124
v_xad_u32 v127,v15,48,v124
v_add_u32 v124,v15,v124
ds_read_b128 v[88:91],v11 offset:768
ds_read_b128 v[92:95],v12 offset:0
ds_read_b128 v[96:99],v11 offset:1920
ds_read_b128 v[100:103],v12 offset:1152
ds_read_b128 v[48:51],v124 offset:6976
ds_read_b128 v[40:43],v124 offset:0
ds_read_b128 v[44:47],v124 offset:3488
ds_read_b128 v[60:63],v125 offset:6976
ds_read_b128 v[52:55],v125 offset:0
ds_read_b128 v[56:59],v125 offset:3488
s_waitcnt lgkmcnt(5)
v_mfma_f32_16x16x32_bf16 v[160:163],v[88:91],v[48:51],0
s_waitcnt lgkmcnt(3)
ds_read_b128 v[48:51],v124 offset:7040
ds_read_b128 v[88:91],v11 offset:5376
v_smfmac_f32_16x16x64_bf16 v[160:163],v[92:95],v[40:47],v13
s_waitcnt lgkmcnt(4)
ds_read_b128 v[40:43],v124 offset:64
ds_read_b128 v[44:47],v124 offset:3552
ds_read_b128 v[92:95],v12 offset:4608
v_mfma_f32_16x16x32_bf16 v[164:167],v[96:99],v[60:63],0
s_waitcnt lgkmcnt(5)
ds_read_b128 v[60:63],v125 offset:7040
ds_read_b128 v[96:99],v11 offset:6528
v_smfmac_f32_16x16x64_bf16 v[164:167],v[100:103],v[52:59],v13
s_waitcnt lgkmcnt(5)
ds_read_b128 v[52:55],v125 offset:64
ds_read_b128 v[56:59],v125 offset:3552
ds_read_b128 v[100:103],v12 offset:5760
v_mfma_f32_16x16x32_bf16 v[176:179],v[88:91],v[48:51],0
s_waitcnt lgkmcnt(5)
ds_read_b128 v[48:51],v124 offset:7104
ds_read_b128 v[88:91],v11 offset:9984
v_add_f32 v120,v160,v161
v_mul_f32 v122,v160,v160
v_fmac_f32 v122,v161,v161
v_smfmac_f32_16x16x64_bf16 v[176:179],v[92:95],v[40:47],v13
s_waitcnt lgkmcnt(5)
ds_read_b128 v[40:43],v124 offset:128
ds_read_b128 v[44:47],v124 offset:3616
ds_read_b128 v[92:95],v12 offset:9216
v_add_f32 v121,v162,v163
v_mul_f32 v123,v162,v162
v_fmac_f32 v123,v163,v163
v_mfma_f32_16x16x32_bf16 v[180:183],v[96:99],v[60:63],0
s_waitcnt lgkmcnt(5)
ds_read_b128 v[60:63],v125 offset:7104
ds_read_b128 v[96:99],v11 offset:11136
v_add_f32 v120,v120,v164
v_fmac_f32 v122,v164,v164
v_add_f32 v120,v120,v165
v_fmac_f32 v122,v165,v165
v_smfmac_f32_16x16x64_bf16 v[180:183],v[100:103],v[52:59],v13
s_waitcnt lgkmcnt(5)
ds_read_b128 v[52:55],v125 offset:128
ds_read_b128 v[56:59],v125 offset:3616
ds_read_b128 v[100:103],v12 offset:10368
v_add_f32 v121,v121,v166
v_fmac_f32 v123,v166,v166
v_add_f32 v121,v121,v167
v_fmac_f32 v123,v167,v167
v_mfma_f32_16x16x32_bf16 v[192:195],v[88:91],v[48:51],0
s_waitcnt lgkmcnt(5)
ds_read_b128 v[48:51],v126 offset:6976
ds_read_b128 v[88:91],v11 offset:3072
v_add_f32 v120,v120,v176
v_fmac_f32 v122,v176,v176
v_add_f32 v120,v120,v177
v_fmac_f32 v122,v177,v177
v_smfmac_f32_16x16x64_bf16 v[192:195],v[92:95],v[40:47],v13
s_waitcnt lgkmcnt(5)
ds_read_b128 v[40:43],v126 offset:0
ds_read_b128 v[44:47],v126 offset:3488
ds_read_b128 v[92:95],v12 offset:2304
v_add_f32 v121,v121,v178
v_fmac_f32 v123,v178,v178
v_add_f32 v121,v121,v179
v_fmac_f32 v123,v179,v179
v_mfma_f32_16x16x32_bf16 v[196:199],v[96:99],v[60:63],0
s_waitcnt lgkmcnt(5)
ds_read_b128 v[60:63],v127 offset:6976
ds_read_b128 v[96:99],v11 offset:4224
v_add_f32 v120,v120,v180
v_fmac_f32 v122,v180,v180
v_add_f32 v120,v120,v181
v_fmac_f32 v122,v181,v181
v_smfmac_f32_16x16x64_bf16 v[196:199],v[100:103],v[52:59],v13
s_waitcnt lgkmcnt(5)
ds_read_b128 v[52:55],v127 offset:0
ds_read_b128 v[56:59],v127 offset:3488
ds_read_b128 v[100:103],v12 offset:3456
v_add_f32 v121,v121,v182
v_fmac_f32 v123,v182,v182
v_add_f32 v121,v121,v183
v_fmac_f32 v123,v183,v183
v_mfma_f32_16x16x32_bf16 v[168:171],v[88:91],v[48:51],0
s_waitcnt lgkmcnt(5)
ds_read_b128 v[48:51],v126 offset:7040
ds_read_b128 v[88:91],v11 offset:7680
v_add_f32 v120,v120,v192
v_fmac_f32 v122,v192,v192
v_add_f32 v120,v120,v193
v_fmac_f32 v122,v193,v193
v_smfmac_f32_16x16x64_bf16 v[168:171],v[92:95],v[40:47],v13
s_waitcnt lgkmcnt(5)
ds_read_b128 v[40:43],v126 offset:64
ds_read_b128 v[44:47],v126 offset:3552
ds_read_b128 v[92:95],v12 offset:6912
v_add_f32 v121,v121,v194
v_fmac_f32 v123,v194,v194
v_add_f32 v121,v121,v195
v_fmac_f32 v123,v195,v195
v_mfma_f32_16x16x32_bf16 v[172:175],v[96:99],v[60:63],0
s_waitcnt lgkmcnt(5)
ds_read_b128 v[60:63],v127 offset:7040
ds_read_b128 v[96:99],v11 offset:8832
v_add_f32 v120,v120,v196
v_fmac_f32 v122,v196,v196
v_add_f32 v120,v120,v197
v_fmac_f32 v122,v197,v197
v_add_f32 v121,v121,v198
v_smfmac_f32_16x16x64_bf16 v[172:175],v[100:103],v[52:59],v13
s_waitcnt lgkmcnt(5)
ds_read_b128 v[52:55],v127 offset:64
ds_read_b128 v[56:59],v127 offset:3552
ds_read_b128 v[100:103],v12 offset:8064
v_fmac_f32 v123,v198,v198
v_add_f32 v121,v121,v199
v_fmac_f32 v123,v199,v199
v_add_u32 v124,v124,v8
v_mfma_f32_16x16x32_bf16 v[184:187],v[88:91],v[48:51],0
s_waitcnt lgkmcnt(5)
ds_read_b128 v[48:51],v126 offset:7104
ds_read_b128 v[88:91],v11 offset:12288
v_add_f32 v120,v120,v168
v_fmac_f32 v122,v168,v168
v_add_f32 v120,v120,v169
v_fmac_f32 v122,v169,v169
v_smfmac_f32_16x16x64_bf16 v[184:187],v[92:95],v[40:47],v13
s_waitcnt lgkmcnt(5)
ds_read_b128 v[40:43],v126 offset:128
ds_read_b128 v[44:47],v126 offset:3616
ds_read_b128 v[92:95],v12 offset:11520
v_add_f32 v121,v121,v170
v_fmac_f32 v123,v170,v170
v_add_f32 v121,v121,v171
v_fmac_f32 v123,v171,v171
v_mfma_f32_16x16x32_bf16 v[188:191],v[96:99],v[60:63],0
s_waitcnt lgkmcnt(5)
ds_read_b128 v[60:63],v127 offset:7104
ds_read_b128 v[96:99],v11 offset:13440
v_add_f32 v120,v120,v172
v_fmac_f32 v122,v172,v172
v_add_f32 v120,v120,v173
v_fmac_f32 v122,v173,v173
v_smfmac_f32_16x16x64_bf16 v[188:191],v[100:103],v[52:59],v13
s_waitcnt lgkmcnt(5)
ds_read_b128 v[52:55],v127 offset:128
ds_read_b128 v[56:59],v127 offset:3616
ds_read_b128 v[100:103],v12 offset:12672
v_add_f32 v121,v121,v174
v_fmac_f32 v123,v174,v174
v_add_f32 v121,v121,v175
v_fmac_f32 v123,v175,v175
v_mfma_f32_16x16x32_bf16 v[200:203],v[88:91],v[48:51],0
s_waitcnt lgkmcnt(5)
ds_read_b128 v[144:147],v124 offset:3488
ds_read_b128 v[148:151],v124 offset:3680
ds_read_b128 v[88:91],v124 offset:3552
v_add_f32 v120,v120,v184
v_fmac_f32 v122,v184,v184
v_add_f32 v120,v120,v185
v_fmac_f32 v122,v185,v185
v_smfmac_f32_16x16x64_bf16 v[200:203],v[92:95],v[40:47],v13
s_waitcnt lgkmcnt(6)
ds_read_b128 v[64:67],v14 offset:512
ds_read_b128 v[68:71],v7 offset:3712
ds_read_b128 v[92:95],v124 offset:3744
v_add_f32 v121,v121,v186
v_fmac_f32 v123,v186,v186
v_add_f32 v121,v121,v187
v_fmac_f32 v123,v187,v187
v_mfma_f32_16x16x32_bf16 v[204:207],v[96:99],v[60:63],0
s_waitcnt lgkmcnt(6)
ds_read_b128 v[104:107],v14 offset:0
ds_read_b128 v[108:111],v7 offset:3584
ds_read_b128 v[80:83],v14 offset:1024
v_add_f32 v120,v120,v188
v_fmac_f32 v122,v188,v188
v_add_f32 v120,v120,v189
v_fmac_f32 v122,v189,v189
v_smfmac_f32_16x16x64_bf16 v[204:207],v[100:103],v[52:59],v13
ds_read_b128 v[84:87],v7 offset:3840
ds_read_b128 v[96:99],v124 offset:3616
ds_read_b128 v[72:75],v14 offset:768
v_add_f32 v121,v121,v190
v_fmac_f32 v123,v190,v190
v_add_f32 v121,v121,v191
v_fmac_f32 v123,v191,v191
ds_read_b128 v[76:79],v7 offset:3744
ds_read_b128 v[100:103],v124 offset:3808
ds_read_b128 v[152:155],v14 offset:256
ds_read_b128 v[156:159],v7 offset:3616
ds_read_b128 v[128:131],v14 offset:1280
ds_read_b128 v[132:135],v7 offset:3872
v_add_f32 v120,v120,v200
v_fmac_f32 v122,v200,v200
v_add_f32 v120,v120,v201
v_fmac_f32 v122,v201,v201
v_add_f32 v121,v121,v202
v_fmac_f32 v123,v202,v202
v_add_f32 v121,v121,v203
v_fmac_f32 v123,v203,v203
v_add_f32 v120,v120,v204
v_fmac_f32 v122,v204,v204
v_add_f32 v120,v120,v205
v_fmac_f32 v122,v205,v205
v_add_f32 v121,v121,v206
v_fmac_f32 v123,v206,v206
v_add_f32 v121,v121,v207
v_fmac_f32 v123,v207,v207
v_sub_u32 v124,v124,v8
v_permlane32_swap_b32 v120,v121
v_permlane32_swap_b32 v122,v123
v_add_f32 v120,v120,v121
v_add_f32 v122,v122,v123
s_nop 1
v_permlane16_swap_b32 v120,v122
v_add_f32 v61,v120,v122
v_add_f32 v62,v120,v122
s_nop 1
v_permlane16_swap_b32 v61,v62
v_mul_f32 v61,0x3c2aaaab,v61
v_mul_f32 v62,0x3c2aaaab,v62
v_mul_f32 v60,0x5e000000,v62
v_fma_f32 v62,-v61,v61,v62
v_pk_maximum3_f16 v1,v1,v60,v60 op_sel:[0,1,1] op_sel_hi:[1,1,1]
v_max_f32 v62,0,v62
v_add_f32 v62,0x3727c5ac,v62
v_rsq_f32 v62,v62
s_nop 0
v_mul_f32 v120,0x8befe000,v62
v_mul_f32 v121,0x8befe000,v62
v_mul_f32 v122,-v61,v120
v_mul_f32 v123,-v61,v120
v_permlane32_swap_b32 v120,v121
s_nop 0
v_permlane32_swap_b32 v122,v123
v_pk_fma_f32 v[160:161],v[160:161],v[120:121],v[122:123] op_sel:[0,0,0] op_sel_hi:[1,0,0]
v_pk_fma_f32 v[162:163],v[162:163],v[120:121],v[122:123] op_sel:[0,1,1] op_sel_hi:[1,1,1]
v_pk_fma_f32 v[164:165],v[164:165],v[120:121],v[122:123] op_sel:[0,0,0] op_sel_hi:[1,0,0]
v_pk_fma_f32 v[166:167],v[166:167],v[120:121],v[122:123] op_sel:[0,1,1] op_sel_hi:[1,1,1]
v_pk_fma_f32 v[168:169],v[168:169],v[120:121],v[122:123] op_sel:[0,0,0] op_sel_hi:[1,0,0]
v_pk_fma_f32 v[170:171],v[170:171],v[120:121],v[122:123] op_sel:[0,1,1] op_sel_hi:[1,1,1]
v_pk_fma_f32 v[172:173],v[172:173],v[120:121],v[122:123] op_sel:[0,0,0] op_sel_hi:[1,0,0]
v_pk_fma_f32 v[174:175],v[174:175],v[120:121],v[122:123] op_sel:[0,1,1] op_sel_hi:[1,1,1]
v_pk_fma_f32 v[176:177],v[176:177],v[120:121],v[122:123] op_sel:[0,0,0] op_sel_hi:[1,0,0]
v_pk_fma_f32 v[178:179],v[178:179],v[120:121],v[122:123] op_sel:[0,1,1] op_sel_hi:[1,1,1]
v_pk_fma_f32 v[180:181],v[180:181],v[120:121],v[122:123] op_sel:[0,0,0] op_sel_hi:[1,0,0]
v_pk_fma_f32 v[182:183],v[182:183],v[120:121],v[122:123] op_sel:[0,1,1] op_sel_hi:[1,1,1]
v_pk_fma_f32 v[184:185],v[184:185],v[120:121],v[122:123] op_sel:[0,0,0] op_sel_hi:[1,0,0]
v_pk_fma_f32 v[186:187],v[186:187],v[120:121],v[122:123] op_sel:[0,1,1] op_sel_hi:[1,1,1]
v_pk_fma_f32 v[188:189],v[188:189],v[120:121],v[122:123] op_sel:[0,0,0] op_sel_hi:[1,0,0]
v_pk_fma_f32 v[190:191],v[190:191],v[120:121],v[122:123] op_sel:[0,1,1] op_sel_hi:[1,1,1]
v_pk_fma_f32 v[192:193],v[192:193],v[120:121],v[122:123] op_sel:[0,0,0] op_sel_hi:[1,0,0]
v_pk_fma_f32 v[194:195],v[194:195],v[120:121],v[122:123] op_sel:[0,1,1] op_sel_hi:[1,1,1]
v_pk_fma_f32 v[196:197],v[196:197],v[120:121],v[122:123] op_sel:[0,0,0] op_sel_hi:[1,0,0]
v_pk_fma_f32 v[198:199],v[198:199],v[120:121],v[122:123] op_sel:[0,1,1] op_sel_hi:[1,1,1]
v_pk_fma_f32 v[200:201],v[200:201],v[120:121],v[122:123] op_sel:[0,0,0] op_sel_hi:[1,0,0]
v_pk_fma_f32 v[202:203],v[202:203],v[120:121],v[122:123] op_sel:[0,1,1] op_sel_hi:[1,1,1]
v_pk_fma_f32 v[204:205],v[204:205],v[120:121],v[122:123] op_sel:[0,0,0] op_sel_hi:[1,0,0]
v_pk_fma_f32 v[206:207],v[206:207],v[120:121],v[122:123] op_sel:[0,1,1] op_sel_hi:[1,1,1]
v_cvt_pk_bf16_f32 v16,v160,v161
v_cvt_pk_bf16_f32 v20,v162,v163
v_cvt_pk_bf16_f32 v17,v164,v165
v_cvt_pk_bf16_f32 v21,v166,v167
v_cvt_pk_bf16_f32 v18,v168,v169
v_cvt_pk_bf16_f32 v22,v170,v171
v_cvt_pk_bf16_f32 v19,v172,v173
v_cvt_pk_bf16_f32 v23,v174,v175
v_cvt_pk_bf16_f32 v24,v176,v177
s_waitcnt lgkmcnt(10)
v_mfma_f32_16x16x32_bf16 v[160:163],v[104:107],v[144:147],v[108:111]
v_cvt_pk_bf16_f32 v28,v178,v179
v_cvt_pk_bf16_f32 v25,v180,v181
v_mfma_f32_16x16x32_bf16 v[164:167],v[104:107],v[148:151],v[108:111]
s_waitcnt lgkmcnt(2)
v_cvt_pk_bf16_f32 v29,v182,v183
v_cvt_pk_bf16_f32 v26,v184,v185
v_mfma_f32_16x16x32_bf16 v[168:171],v[152:155],v[144:147],v[156:159]
v_cvt_pk_bf16_f32 v30,v186,v187
v_cvt_pk_bf16_f32 v27,v188,v189
v_mfma_f32_16x16x32_bf16 v[172:175],v[152:155],v[148:151],v[156:159]
v_cvt_pk_bf16_f32 v31,v190,v191
v_cvt_pk_bf16_f32 v32,v192,v193
v_mfma_f32_16x16x32_bf16 v[176:179],v[64:67],v[88:91],v[68:71]
v_cvt_pk_bf16_f32 v36,v194,v195
v_cvt_pk_bf16_f32 v33,v196,v197
v_mfma_f32_16x16x32_bf16 v[180:183],v[64:67],v[92:95],v[68:71]
v_cvt_pk_bf16_f32 v37,v198,v199
v_cvt_pk_bf16_f32 v34,v200,v201
v_mfma_f32_16x16x32_bf16 v[184:187],v[72:75],v[88:91],v[76:79]
v_cvt_pk_bf16_f32 v38,v202,v203
v_cvt_pk_bf16_f32 v35,v204,v205
v_mfma_f32_16x16x32_bf16 v[188:191],v[72:75],v[92:95],v[76:79]
v_cvt_pk_bf16_f32 v39,v206,v207
v_mfma_f32_16x16x32_bf16 v[192:195],v[80:83],v[96:99],v[84:87]
v_mfma_f32_16x16x32_bf16 v[196:199],v[80:83],v[100:103],v[84:87]
s_waitcnt lgkmcnt(0)
v_mfma_f32_16x16x32_bf16 v[200:203],v[128:131],v[96:99],v[132:135]
v_mfma_f32_16x16x32_bf16 v[204:207],v[128:131],v[100:103],v[132:135]
ds_read_b128 v[88:91],v11 offset:768
ds_read_b128 v[92:95],v12 offset:0
ds_read_b128 v[96:99],v11 offset:1920
ds_read_b128 v[100:103],v12 offset:1152
s_waitcnt lgkmcnt(0)
s_barrier
ds_read_b128 v[48:51],v124 offset:34880
ds_read_b128 v[40:43],v124 offset:27904
ds_read_b128 v[44:47],v124 offset:31392
ds_read_b128 v[60:63],v125 offset:34880
ds_read_b128 v[52:55],v125 offset:27904
ds_read_b128 v[56:59],v125 offset:31392
ds_read_b64_tr_b16 v[64:65],v4 offset:12832
ds_read_b64_tr_b16 v[66:67],v4 offset:14880
ds_read_b128 v[112:115],v7 offset:128
ds_read_b64_tr_b16 v[68:69],v5 offset:12832
ds_read_b64_tr_b16 v[70:71],v5 offset:14880
ds_read_b128 v[116:119],v7 offset:160
ds_read_b64_tr_b16 v[72:73],v4 offset:16928
ds_read_b64_tr_b16 v[74:75],v4 offset:18976
ds_read_b64_tr_b16 v[76:77],v5 offset:16928
ds_read_b64_tr_b16 v[78:79],v5 offset:18976
ds_read_b64_tr_b16 v[80:81],v4 offset:21024
ds_read_b64_tr_b16 v[82:83],v4 offset:23072
ds_read_b64_tr_b16 v[84:85],v5 offset:21024
ds_read_b64_tr_b16 v[86:87],v5 offset:23072
s_waitcnt lgkmcnt(15)
v_mfma_f32_16x16x32_bf16 v[208:211],v[88:91],v[48:51],0
ds_read_b128 v[48:51],v124 offset:34944
ds_read_b128 v[88:91],v11 offset:5376
v_smfmac_f32_16x16x64_bf16 v[208:211],v[92:95],v[40:47],v13
ds_read_b128 v[40:43],v124 offset:27968
ds_read_b128 v[44:47],v124 offset:31456
ds_read_b128 v[92:95],v12 offset:4608
v_mfma_f32_16x16x32_bf16 v[212:215],v[96:99],v[60:63],0
s_waitcnt lgkmcnt(15)
ds_read_b128 v[60:63],v125 offset:34944
ds_read_b128 v[96:99],v11 offset:6528
v_smfmac_f32_16x16x64_bf16 v[212:215],v[100:103],v[52:59],v13
s_waitcnt lgkmcnt(5)
ds_read_b128 v[52:55],v125 offset:27968
ds_read_b128 v[56:59],v125 offset:31456
ds_read_b128 v[100:103],v12 offset:5760
v_mfma_f32_16x16x32_bf16 v[224:227],v[88:91],v[48:51],0
s_waitcnt lgkmcnt(5)
ds_read_b128 v[48:51],v124 offset:35008
ds_read_b128 v[88:91],v11 offset:9984
v_add_f32 v120,v208,v209
v_mul_f32 v122,v208,v208
v_smfmac_f32_16x16x64_bf16 v[224:227],v[92:95],v[40:47],v13
ds_read_b128 v[40:43],v124 offset:28032
ds_read_b128 v[44:47],v124 offset:31520
ds_read_b128 v[92:95],v12 offset:9216
v_fmac_f32 v122,v209,v209
v_mfma_f32_16x16x32_bf16 v[128:131],v[64:67],v[16:19],v[112:115]
v_add_f32 v121,v210,v211
v_mul_f32 v123,v210,v210
v_mfma_f32_16x16x32_bf16 v[136:139],v[64:67],v[20:23],v[112:115]
s_waitcnt lgkmcnt(8)
v_fmac_f32 v123,v211,v211
v_mfma_f32_16x16x32_bf16 v[228:231],v[96:99],v[60:63],0
s_waitcnt lgkmcnt(5)
ds_read_b128 v[60:63],v125 offset:35008
ds_read_b128 v[96:99],v11 offset:11136
v_add_f32 v120,v120,v212
v_fmac_f32 v122,v212,v212
v_smfmac_f32_16x16x64_bf16 v[228:231],v[100:103],v[52:59],v13
ds_read_b128 v[52:55],v125 offset:28032
ds_read_b128 v[56:59],v125 offset:31520
ds_read_b128 v[100:103],v12 offset:10368
v_add_f32 v120,v120,v213
s_mov_b32 s86,0xbe801d5a
v_fmac_f32 v122,v213,v213
v_mfma_f32_16x16x32_bf16 v[140:143],v[68:71],v[20:23],v[116:119]
v_add_f32 v121,v121,v214
v_fmac_f32 v123,v214,v214
v_mfma_f32_16x16x32_bf16 v[132:135],v[68:71],v[16:19],v[116:119]
s_waitcnt lgkmcnt(8)
v_add_f32 v121,v121,v215
v_fmac_f32 v123,v215,v215
v_mfma_f32_16x16x32_bf16 v[240:243],v[88:91],v[48:51],0
s_waitcnt lgkmcnt(5)
ds_read_b128 v[48:51],v126 offset:34880
ds_read_b128 v[88:91],v11 offset:3072
v_add_f32 v120,v120,v224
v_fmac_f32 v122,v224,v224
v_add_f32 v120,v120,v225
v_smfmac_f32_16x16x64_bf16 v[240:243],v[92:95],v[40:47],v13
ds_read_b128 v[40:43],v126 offset:27904
ds_read_b128 v[44:47],v126 offset:31392
ds_read_b128 v[92:95],v12 offset:2304
v_fmac_f32 v122,v225,v225
v_add_f32 v121,v121,v226
v_fmac_f32 v123,v226,v226
v_mfma_f32_16x16x32_bf16 v[128:131],v[72:75],v[24:27],v[128:131]
s_waitcnt lgkmcnt(8)
v_add_f32 v121,v121,v227
v_fmac_f32 v123,v227,v227
v_mfma_f32_16x16x32_bf16 v[244:247],v[96:99],v[60:63],0
s_waitcnt lgkmcnt(5)
ds_read_b128 v[60:63],v127 offset:34880
ds_read_b128 v[96:99],v11 offset:4224
v_add_f32 v120,v120,v228
v_fmac_f32 v122,v228,v228
v_add_f32 v120,v120,v229
v_smfmac_f32_16x16x64_bf16 v[244:247],v[100:103],v[52:59],v13
ds_read_b128 v[52:55],v127 offset:27904
ds_read_b128 v[56:59],v127 offset:31392
ds_read_b128 v[100:103],v12 offset:3456
v_fmac_f32 v122,v229,v229
v_add_f32 v121,v121,v230
v_fmac_f32 v123,v230,v230
v_mfma_f32_16x16x32_bf16 v[136:139],v[72:75],v[28:31],v[136:139]
s_waitcnt lgkmcnt(8)
v_add_f32 v121,v121,v231
v_fmac_f32 v123,v231,v231
v_mfma_f32_16x16x32_bf16 v[216:219],v[88:91],v[48:51],0
s_waitcnt lgkmcnt(5)
ds_read_b128 v[48:51],v126 offset:34944
ds_read_b128 v[88:91],v11 offset:7680
v_add_f32 v120,v120,v240
v_fmac_f32 v122,v240,v240
v_add_f32 v120,v120,v241
v_smfmac_f32_16x16x64_bf16 v[216:219],v[92:95],v[40:47],v13
ds_read_b128 v[40:43],v126 offset:27968
ds_read_b128 v[44:47],v126 offset:31456
ds_read_b128 v[92:95],v12 offset:6912
v_fmac_f32 v122,v241,v241
v_add_f32 v121,v121,v242
v_fmac_f32 v123,v242,v242
v_mfma_f32_16x16x32_bf16 v[140:143],v[76:79],v[28:31],v[140:143]
s_waitcnt lgkmcnt(8)
v_add_f32 v121,v121,v243
v_fmac_f32 v123,v243,v243
v_mfma_f32_16x16x32_bf16 v[220:223],v[96:99],v[60:63],0
s_waitcnt lgkmcnt(5)
ds_read_b128 v[60:63],v127 offset:34944
ds_read_b128 v[96:99],v11 offset:8832
v_add_f32 v120,v120,v244
v_fmac_f32 v122,v244,v244
v_add_f32 v120,v120,v245
v_fmac_f32 v122,v245,v245
v_smfmac_f32_16x16x64_bf16 v[220:223],v[100:103],v[52:59],v13
ds_read_b128 v[52:55],v127 offset:27968
ds_read_b128 v[56:59],v127 offset:31456
ds_read_b128 v[100:103],v12 offset:8064
v_add_f32 v121,v121,v246
v_fmac_f32 v123,v246,v246
v_add_f32 v121,v121,v247
v_mfma_f32_16x16x32_bf16 v[132:135],v[76:79],v[24:27],v[132:135]
s_waitcnt lgkmcnt(8)
v_fmac_f32 v123,v247,v247
v_add_u32 v124,v124,v8
v_mfma_f32_16x16x32_bf16 v[232:235],v[88:91],v[48:51],0
s_waitcnt lgkmcnt(5)
ds_read_b128 v[48:51],v126 offset:35008
ds_read_b128 v[88:91],v11 offset:12288
v_add_f32 v120,v120,v216
v_fmac_f32 v122,v216,v216
v_add_f32 v120,v120,v217
v_smfmac_f32_16x16x64_bf16 v[232:235],v[92:95],v[40:47],v13
ds_read_b128 v[40:43],v126 offset:28032
ds_read_b128 v[44:47],v126 offset:31520
ds_read_b128 v[92:95],v12 offset:11520
v_fmac_f32 v122,v217,v217
v_add_f32 v121,v121,v218
v_fmac_f32 v123,v218,v218
v_mfma_f32_16x16x32_bf16 v[128:131],v[80:83],v[32:35],v[128:131]
s_waitcnt lgkmcnt(8)
v_add_f32 v121,v121,v219
v_fmac_f32 v123,v219,v219
v_mfma_f32_16x16x32_bf16 v[236:239],v[96:99],v[60:63],0
s_waitcnt lgkmcnt(5)
ds_read_b128 v[60:63],v127 offset:35008
ds_read_b128 v[96:99],v11 offset:13440
v_add_f32 v120,v120,v220
v_fmac_f32 v122,v220,v220
v_add_f32 v120,v120,v221
v_smfmac_f32_16x16x64_bf16 v[236:239],v[100:103],v[52:59],v13
ds_read_b128 v[52:55],v127 offset:28032
ds_read_b128 v[56:59],v127 offset:31520
ds_read_b128 v[100:103],v12 offset:12672
v_fmac_f32 v122,v221,v221
v_add_f32 v121,v121,v222
v_fmac_f32 v123,v222,v222
v_mfma_f32_16x16x32_bf16 v[136:139],v[80:83],v[36:39],v[136:139]
s_waitcnt lgkmcnt(8)
ds_read_b128 v[144:147],v124 offset:31392
ds_read_b128 v[148:151],v124 offset:31584
v_add_f32 v121,v121,v223
v_fmac_f32 v123,v223,v223
v_mfma_f32_16x16x32_bf16 v[248:251],v[88:91],v[48:51],0
s_waitcnt lgkmcnt(7)
ds_read_b128 v[88:91],v124 offset:31456
v_add_f32 v120,v120,v232
v_fmac_f32 v122,v232,v232
v_add_f32 v120,v120,v233
v_smfmac_f32_16x16x64_bf16 v[248:251],v[92:95],v[40:47],v13
ds_read_b128 v[92:95],v124 offset:31648
ds_read_b128 v[104:107],v14 offset:0
ds_read_b128 v[108:111],v7 offset:3584
v_fmac_f32 v122,v233,v233
v_add_f32 v121,v121,v234
v_fmac_f32 v123,v234,v234
v_mfma_f32_16x16x32_bf16 v[140:143],v[84:87],v[36:39],v[140:143]
s_waitcnt lgkmcnt(9)
v_add_f32 v121,v121,v235
v_fmac_f32 v123,v235,v235
v_mfma_f32_16x16x32_bf16 v[252:255],v[96:99],v[60:63],0
s_waitcnt lgkmcnt(6)
ds_read_b128 v[96:99],v124 offset:31520
v_add_f32 v120,v120,v236
v_fmac_f32 v122,v236,v236
v_add_f32 v120,v120,v237
v_smfmac_f32_16x16x64_bf16 v[252:255],v[100:103],v[52:59],v13
ds_read_b128 v[100:103],v124 offset:31712
ds_read_b128 v[152:155],v14 offset:256
ds_read_b128 v[156:159],v7 offset:3616
v_fmac_f32 v122,v237,v237
v_add_f32 v121,v121,v238
v_fmac_f32 v123,v238,v238
v_mfma_f32_16x16x32_bf16 v[132:135],v[84:87],v[32:35],v[132:135]
v_add_f32 v121,v121,v239
v_fmac_f32 v123,v239,v239
v_add_f32 v120,v120,v248
v_fmac_f32 v122,v248,v248
v_add_f32 v120,v120,v249
v_fmac_f32 v122,v249,v249
v_add_f32 v121,v121,v250
v_fmac_f32 v123,v250,v250
v_add_f32 v121,v121,v251
v_fmac_f32 v123,v251,v251
s_waitcnt lgkmcnt(2)
s_barrier
v_add_f32 v120,v120,v252
v_fmac_f32 v122,v252,v252
v_add_f32 v120,v120,v253
v_fmac_f32 v122,v253,v253
v_add_f32 v121,v121,v254
v_fmac_f32 v123,v254,v254
v_add_f32 v121,v121,v255
v_fmac_f32 v123,v255,v255
s_nop 0
v_permlane32_swap_b32 v120,v121
v_permlane32_swap_b32 v122,v123
v_add_f32 v120,v120,v121
v_add_f32 v122,v122,v123
s_nop 1
v_permlane16_swap_b32 v120,v122
v_add_f32 v61,v120,v122
v_add_f32 v62,v120,v122
s_nop 1
v_permlane16_swap_b32 v61,v62
v_mul_f32 v61,0x3c2aaaab,v61
v_mul_f32 v62,0x3c2aaaab,v62
v_mul_f32 v60,0x5e000000,v62
v_fma_f32 v62,-v61,v61,v62
v_pk_maximum3_f16 v1,v1,v60,v60 op_sel:[0,1,1] op_sel_hi:[1,1,1]
v_max_f32 v62,0,v62
v_add_f32 v62,0x3727c5ac,v62
v_rsq_f32 v62,v62
s_nop 0
v_mul_f32 v120,0x8befe000,v62
v_mul_f32 v121,0x8befe000,v62
v_mul_f32 v122,-v61,v120
v_mul_f32 v123,-v61,v120
v_permlane32_swap_b32 v120,v121
s_nop 0
v_permlane32_swap_b32 v122,v123
v_pk_fma_f32 v[208:209],v[208:209],v[120:121],v[122:123] op_sel:[0,0,0] op_sel_hi:[1,0,0]
v_pk_fma_f32 v[210:211],v[210:211],v[120:121],v[122:123] op_sel:[0,1,1] op_sel_hi:[1,1,1]
v_pk_fma_f32 v[212:213],v[212:213],v[120:121],v[122:123] op_sel:[0,0,0] op_sel_hi:[1,0,0]
v_pk_fma_f32 v[214:215],v[214:215],v[120:121],v[122:123] op_sel:[0,1,1] op_sel_hi:[1,1,1]
v_pk_fma_f32 v[216:217],v[216:217],v[120:121],v[122:123] op_sel:[0,0,0] op_sel_hi:[1,0,0]
v_pk_fma_f32 v[218:219],v[218:219],v[120:121],v[122:123] op_sel:[0,1,1] op_sel_hi:[1,1,1]
v_pk_fma_f32 v[220:221],v[220:221],v[120:121],v[122:123] op_sel:[0,0,0] op_sel_hi:[1,0,0]
v_pk_fma_f32 v[222:223],v[222:223],v[120:121],v[122:123] op_sel:[0,1,1] op_sel_hi:[1,1,1]
v_pk_fma_f32 v[224:225],v[224:225],v[120:121],v[122:123] op_sel:[0,0,0] op_sel_hi:[1,0,0]
v_pk_fma_f32 v[226:227],v[226:227],v[120:121],v[122:123] op_sel:[0,1,1] op_sel_hi:[1,1,1]
v_pk_fma_f32 v[228:229],v[228:229],v[120:121],v[122:123] op_sel:[0,0,0] op_sel_hi:[1,0,0]
v_pk_fma_f32 v[230:231],v[230:231],v[120:121],v[122:123] op_sel:[0,1,1] op_sel_hi:[1,1,1]
v_pk_fma_f32 v[232:233],v[232:233],v[120:121],v[122:123] op_sel:[0,0,0] op_sel_hi:[1,0,0]
v_pk_fma_f32 v[234:235],v[234:235],v[120:121],v[122:123] op_sel:[0,1,1] op_sel_hi:[1,1,1]
v_pk_fma_f32 v[236:237],v[236:237],v[120:121],v[122:123] op_sel:[0,0,0] op_sel_hi:[1,0,0]
v_pk_fma_f32 v[238:239],v[238:239],v[120:121],v[122:123] op_sel:[0,1,1] op_sel_hi:[1,1,1]
v_pk_fma_f32 v[240:241],v[240:241],v[120:121],v[122:123] op_sel:[0,0,0] op_sel_hi:[1,0,0]
v_pk_fma_f32 v[242:243],v[242:243],v[120:121],v[122:123] op_sel:[0,1,1] op_sel_hi:[1,1,1]
v_pk_fma_f32 v[244:245],v[244:245],v[120:121],v[122:123] op_sel:[0,0,0] op_sel_hi:[1,0,0]
v_pk_fma_f32 v[246:247],v[246:247],v[120:121],v[122:123] op_sel:[0,1,1] op_sel_hi:[1,1,1]
v_pk_fma_f32 v[248:249],v[248:249],v[120:121],v[122:123] op_sel:[0,0,0] op_sel_hi:[1,0,0]
v_pk_fma_f32 v[250:251],v[250:251],v[120:121],v[122:123] op_sel:[0,1,1] op_sel_hi:[1,1,1]
v_pk_fma_f32 v[252:253],v[252:253],v[120:121],v[122:123] op_sel:[0,0,0] op_sel_hi:[1,0,0]
v_pk_fma_f32 v[254:255],v[254:255],v[120:121],v[122:123] op_sel:[0,1,1] op_sel_hi:[1,1,1]
v_cvt_pk_bf16_f32 v40,v208,v209
v_cvt_pk_bf16_f32 v44,v210,v211
v_cvt_pk_bf16_f32 v41,v212,v213
v_cvt_pk_bf16_f32 v45,v214,v215
v_cvt_pk_bf16_f32 v42,v216,v217
v_cvt_pk_bf16_f32 v46,v218,v219
v_cvt_pk_bf16_f32 v43,v220,v221
v_cvt_pk_bf16_f32 v47,v222,v223
v_cvt_pk_bf16_f32 v48,v224,v225
v_mfma_f32_16x16x32_bf16 v[208:211],v[104:107],v[144:147],v[108:111]
v_cvt_pk_bf16_f32 v52,v226,v227
v_cvt_pk_bf16_f32 v49,v228,v229
v_mfma_f32_16x16x32_bf16 v[212:215],v[104:107],v[148:151],v[108:111]
s_waitcnt lgkmcnt(0)
ds_read_b128 v[104:107],v14 offset:512
ds_read_b128 v[108:111],v7 offset:3712
v_cvt_pk_bf16_f32 v53,v230,v231
v_cvt_pk_bf16_f32 v50,v232,v233
v_mfma_f32_16x16x32_bf16 v[216:219],v[152:155],v[144:147],v[156:159]
v_cvt_pk_bf16_f32 v54,v234,v235
v_cvt_pk_bf16_f32 v51,v236,v237
v_mfma_f32_16x16x32_bf16 v[220:223],v[152:155],v[148:151],v[156:159]
ds_read_b128 v[152:155],v14 offset:768
ds_read_b128 v[156:159],v7 offset:3744
v_cvt_pk_bf16_f32 v55,v238,v239
v_cvt_pk_bf16_f32 v56,v240,v241
s_waitcnt lgkmcnt(2)
v_mfma_f32_16x16x32_bf16 v[224:227],v[104:107],v[88:91],v[108:111]
v_cvt_pk_bf16_f32 v60,v242,v243
v_cvt_pk_bf16_f32 v57,v244,v245
v_mfma_f32_16x16x32_bf16 v[228:231],v[104:107],v[92:95],v[108:111]
s_waitcnt lgkmcnt(0)
ds_read_b128 v[104:107],v14 offset:1024
ds_read_b128 v[108:111],v7 offset:3840
v_cvt_pk_bf16_f32 v61,v246,v247
v_cvt_pk_bf16_f32 v58,v248,v249
v_mfma_f32_16x16x32_bf16 v[232:235],v[152:155],v[88:91],v[156:159]
v_cvt_pk_bf16_f32 v62,v250,v251
v_cvt_pk_bf16_f32 v59,v252,v253
v_mfma_f32_16x16x32_bf16 v[236:239],v[152:155],v[92:95],v[156:159]
ds_read_b128 v[152:155],v14 offset:1280
ds_read_b128 v[156:159],v7 offset:3872
v_cvt_pk_bf16_f32 v63,v254,v255
s_waitcnt lgkmcnt(2)
v_mfma_f32_16x16x32_bf16 v[240:243],v[104:107],v[96:99],v[108:111]
s_barrier
s_setprio 2
ds_read_b64_tr_b16 v[88:89],v6 offset:0
v_lshlrev_b32_sdwa v128,1,v128 dst_sel:DWORD dst_unused:UNUSED_PAD src0_sel:DWORD src1_sel:WORD_1
v_lshlrev_b32_sdwa v130,1,v130 dst_sel:DWORD dst_unused:UNUSED_PAD src0_sel:DWORD src1_sel:WORD_1
v_mfma_f32_16x16x32_bf16 v[244:247],v[104:107],v[100:103],v[108:111]
s_waitcnt lgkmcnt(1)
ds_read_u16 v128,v128
ds_read_b64_tr_b16 v[90:91],v6 offset:1536
v_lshlrev_b32_sdwa v129,1,v129 dst_sel:DWORD dst_unused:UNUSED_PAD src0_sel:DWORD src1_sel:WORD_1
v_lshlrev_b32_sdwa v131,1,v131 dst_sel:DWORD dst_unused:UNUSED_PAD src0_sel:DWORD src1_sel:WORD_1
v_mfma_f32_16x16x32_bf16 v[248:251],v[152:155],v[96:99],v[156:159]
ds_read_u16_d16_hi v130,v130
ds_read_b64_tr_b16 v[92:93],v6 offset:8
v_fma_mixlo_f16 v120,v141,s99,0
v_lshlrev_b32_sdwa v132,1,v132 dst_sel:DWORD dst_unused:UNUSED_PAD src0_sel:DWORD src1_sel:WORD_1
v_mfma_f32_16x16x32_bf16 v[252:255],v[152:155],v[100:103],v[156:159]
ds_read_u16 v129,v129
ds_read_b64_tr_b16 v[94:95],v6 offset:1544
v_fma_mixhi_f16 v120,v143,s99,0
v_lshlrev_b32_sdwa v134,1,v134 dst_sel:DWORD dst_unused:UNUSED_PAD src0_sel:DWORD src1_sel:WORD_1
v_pk_fma_f16 v121,v120,v120,s54
v_mfma_f32_16x16x32_bf16 v[144:147],v[64:67],v[40:43],v[112:115]
ds_read_u16_d16_hi v131,v131
ds_read_b64_tr_b16 v[104:105],v6 offset:128
v_lshlrev_b32_sdwa v133,1,v133 dst_sel:DWORD dst_unused:UNUSED_PAD src0_sel:DWORD src1_sel:WORD_1
v_pk_mul_f16 v121,v120,v121
v_mfma_f32_16x16x32_bf16 v[152:155],v[64:67],v[44:47],v[112:115]
ds_read_u16 v132,v132
ds_read_b64_tr_b16 v[106:107],v6 offset:1664
v_lshlrev_b32_sdwa v135,1,v135 dst_sel:DWORD dst_unused:UNUSED_PAD src0_sel:DWORD src1_sel:WORD_1
v_exp_f16 v120,v121
v_mfma_f32_16x16x32_bf16 v[156:159],v[68:71],v[44:47],v[116:119]
ds_read_u16_d16_hi v134,v134
ds_read_b64_tr_b16 v[108:109],v6 offset:136
v_lshlrev_b32_sdwa v136,1,v136 dst_sel:DWORD dst_unused:UNUSED_PAD src0_sel:DWORD src1_sel:WORD_1
v_mfma_f32_16x16x32_bf16 v[148:151],v[68:71],v[40:43],v[116:119]
ds_read_u16 v133,v133
ds_read_b64_tr_b16 v[110:111],v6 offset:1672
v_exp_f16_sdwa v120,v121 dst_sel:WORD_1 dst_unused:UNUSED_PRESERVE src0_sel:WORD_1
v_mfma_f32_16x16x32_bf16 v[144:147],v[72:75],v[48:51],v[144:147]
ds_read_u16_d16_hi v135,v135
ds_read_b64_tr_b16 v[96:97],v6 offset:64
v_lshlrev_b32_sdwa v138,1,v138 dst_sel:DWORD dst_unused:UNUSED_PAD src0_sel:DWORD src1_sel:WORD_1
v_mfma_f32_16x16x32_bf16 v[152:155],v[72:75],v[52:55],v[152:155]
ds_read_u16 v136,v136
ds_read_b64_tr_b16 v[98:99],v6 offset:1600
v_pk_fma_f16 v120,v120,s55,s55
v_lshlrev_b32_sdwa v137,1,v137 dst_sel:DWORD dst_unused:UNUSED_PAD src0_sel:DWORD src1_sel:WORD_1
v_mfma_f32_16x16x32_bf16 v[156:159],v[76:79],v[52:55],v[156:159]
ds_read_u16_d16_hi v138,v138
ds_read_b64_tr_b16 v[100:101],v6 offset:72
v_rcp_f16 v121,v120
v_mfma_f32_16x16x32_bf16 v[148:151],v[76:79],v[48:51],v[148:151]
ds_read_u16 v137,v137
ds_read_b64_tr_b16 v[102:103],v6 offset:1608
v_lshlrev_b32_sdwa v139,1,v139 dst_sel:DWORD dst_unused:UNUSED_PAD src0_sel:DWORD src1_sel:WORD_1
v_rcp_f16_sdwa v121,v120 dst_sel:WORD_1 dst_unused:UNUSED_PRESERVE src0_sel:WORD_1
v_mfma_f32_16x16x32_bf16 v[144:147],v[80:83],v[56:59],v[144:147]
ds_read_u16_d16_hi v139,v139
ds_read_b64_tr_b16 v[64:65],v4 offset:0
v_lshlrev_b32_sdwa v140,1,v140 dst_sel:DWORD dst_unused:UNUSED_PAD src0_sel:DWORD src1_sel:WORD_1
v_mfma_f32_16x16x32_bf16 v[152:155],v[80:83],v[60:63],v[152:155]
ds_read_u16 v140,v140
ds_read_b64_tr_b16 v[66:67],v4 offset:2048
v_fma_mix_f32 v141,v141,v121,0 op_sel_hi:[0,1,0]
v_lshlrev_b32_sdwa v142,1,v142 dst_sel:DWORD dst_unused:UNUSED_PAD src0_sel:DWORD src1_sel:WORD_1
v_mfma_f32_16x16x32_bf16 v[156:159],v[84:87],v[60:63],v[156:159]
ds_read_u16_d16_hi v142,v142
ds_read_b128 v[112:115],v7 offset:256
v_fma_mix_f32 v143,v143,v121,0 op_sel:[0,1,0] op_sel_hi:[0,1,0]
s_waitcnt lgkmcnt(15)
v_lshl_add_u64 v[128:129],v[128:129],0,v[130:131]
v_mfma_f32_16x16x32_bf16 v[148:151],v[84:87],v[56:59],v[148:151]
ds_read_b64_tr_b16 v[68:69],v5 offset:0
s_waitcnt lgkmcnt(14)
v_lshl_add_u64 v[130:131],v[132:133],0,v[134:135]
v_lshlrev_b32_sdwa v144,1,v144 dst_sel:DWORD dst_unused:UNUSED_PAD src0_sel:DWORD src1_sel:WORD_1
v_lshlrev_b32_sdwa v146,1,v146 dst_sel:DWORD dst_unused:UNUSED_PAD src0_sel:DWORD src1_sel:WORD_1
v_mfma_f32_16x16x32_bf16 v[160:163],v[88:91],v[128:131],v[160:163]
ds_read_u16 v144,v144
v_lshlrev_b32_sdwa v145,1,v145 dst_sel:DWORD dst_unused:UNUSED_PAD src0_sel:DWORD src1_sel:WORD_1
v_lshlrev_b32_sdwa v147,1,v147 dst_sel:DWORD dst_unused:UNUSED_PAD src0_sel:DWORD src1_sel:WORD_1
v_mfma_f32_16x16x32_bf16 v[168:171],v[92:95],v[128:131],v[168:171]
s_waitcnt lgkmcnt(12)
ds_read_b64_tr_b16 v[70:71],v5 offset:2048
ds_read_b128 v[116:119],v7 offset:288
v_fma_mixlo_f16 v120,v157,s99,0
v_lshlrev_b32_sdwa v148,1,v148 dst_sel:DWORD dst_unused:UNUSED_PAD src0_sel:DWORD src1_sel:WORD_1
v_mfma_f32_16x16x32_bf16 v[176:179],v[96:99],v[128:131],v[176:179]
s_waitcnt lgkmcnt(10)
ds_read_u16_d16_hi v146,v146
v_fma_mixhi_f16 v120,v159,s99,0
v_lshlrev_b32_sdwa v150,1,v150 dst_sel:DWORD dst_unused:UNUSED_PAD src0_sel:DWORD src1_sel:WORD_1
v_mfma_f32_16x16x32_bf16 v[184:187],v[100:103],v[128:131],v[184:187]
s_waitcnt lgkmcnt(10)
ds_read_u16 v145,v145
v_lshl_add_u64 v[136:137],v[136:137],0,v[138:139]
v_mfma_f32_16x16x32_bf16 v[192:195],v[104:107],v[128:131],v[192:195]
s_waitcnt lgkmcnt(7)
ds_read_b64_tr_b16 v[72:73],v4 offset:4096
ds_read_b64_tr_b16 v[74:75],v4 offset:6144
v_or_b32 v138,v142,v140
v_cvt_pk_bf16_f32 v139,v141,v143
v_mfma_f32_16x16x32_bf16 v[200:203],v[108:111],v[128:131],v[200:203]
ds_read_u16_d16_hi v147,v147
v_pk_fma_f16 v121,v120,v120,s54
v_lshlrev_b32_sdwa v149,1,v149 dst_sel:DWORD dst_unused:UNUSED_PAD src0_sel:DWORD src1_sel:WORD_1
v_mfma_f32_16x16x32_bf16 v[164:167],v[88:91],v[136:139],v[164:167]
ds_read_b64_tr_b16 v[76:77],v5 offset:4096
ds_read_b64_tr_b16 v[78:79],v5 offset:6144
v_pk_mul_f16 v121,v120,v121
v_lshlrev_b32_sdwa v151,1,v151 dst_sel:DWORD dst_unused:UNUSED_PAD src0_sel:DWORD src1_sel:WORD_1
v_mfma_f32_16x16x32_bf16 v[172:175],v[92:95],v[136:139],v[172:175]
ds_read_u16 v148,v148
v_exp_f16 v120,v121
v_mfma_f32_16x16x32_bf16 v[180:183],v[96:99],v[136:139],v[180:183]
ds_read_b64_tr_b16 v[80:81],v4 offset:8192
ds_read_b64_tr_b16 v[82:83],v4 offset:10240
v_lshlrev_b32_sdwa v152,1,v152 dst_sel:DWORD dst_unused:UNUSED_PAD src0_sel:DWORD src1_sel:WORD_1
v_mfma_f32_16x16x32_bf16 v[188:191],v[100:103],v[136:139],v[188:191]
ds_read_u16_d16_hi v150,v150
v_exp_f16_sdwa v120,v121 dst_sel:WORD_1 dst_unused:UNUSED_PRESERVE src0_sel:WORD_1
v_mfma_f32_16x16x32_bf16 v[196:199],v[104:107],v[136:139],v[196:199]
ds_read_u16 v149,v149
v_lshlrev_b32_sdwa v154,1,v154 dst_sel:DWORD dst_unused:UNUSED_PAD src0_sel:DWORD src1_sel:WORD_1
v_mfma_f32_16x16x32_bf16 v[204:207],v[108:111],v[136:139],v[204:207]
s_waitcnt lgkmcnt(15)
ds_read_b64_tr_b16 v[84:85],v5 offset:8192
v_pk_fma_f16 v120,v120,s55,s55
v_lshlrev_b32_sdwa v153,1,v153 dst_sel:DWORD dst_unused:UNUSED_PAD src0_sel:DWORD src1_sel:WORD_1
v_mfma_f32_16x16x32_bf16 v[128:131],v[64:67],v[16:19],v[112:115]
ds_read_u16_d16_hi v151,v151
v_rcp_f16 v121,v120
v_mfma_f32_16x16x32_bf16 v[136:139],v[64:67],v[20:23],v[112:115]
s_waitcnt lgkmcnt(14)
ds_read_b64_tr_b16 v[86:87],v5 offset:10240
v_lshlrev_b32_sdwa v155,1,v155 dst_sel:DWORD dst_unused:UNUSED_PAD src0_sel:DWORD src1_sel:WORD_1
v_mfma_f32_16x16x32_bf16 v[140:143],v[68:71],v[20:23],v[116:119]
ds_read_u16 v152,v152
v_rcp_f16_sdwa v121,v120 dst_sel:WORD_1 dst_unused:UNUSED_PRESERVE src0_sel:WORD_1
v_mfma_f32_16x16x32_bf16 v[132:135],v[68:71],v[16:19],v[116:119]
s_waitcnt lgkmcnt(12)
ds_read_u16_d16_hi v154,v154
v_lshlrev_b32_sdwa v156,1,v156 dst_sel:DWORD dst_unused:UNUSED_PAD src0_sel:DWORD src1_sel:WORD_1
v_mfma_f32_16x16x32_bf16 v[128:131],v[72:75],v[24:27],v[128:131]
v_fma_mix_f32 v157,v157,v121,0 op_sel_hi:[0,1,0]
v_lshlrev_b32_sdwa v158,1,v158 dst_sel:DWORD dst_unused:UNUSED_PAD src0_sel:DWORD src1_sel:WORD_1
v_mfma_f32_16x16x32_bf16 v[136:139],v[72:75],v[28:31],v[136:139]
s_waitcnt lgkmcnt(10)
ds_read_u16 v153,v153
v_fma_mix_f32 v159,v159,v121,0 op_sel:[0,1,0] op_sel_hi:[0,1,0]
v_mfma_f32_16x16x32_bf16 v[140:143],v[76:79],v[28:31],v[140:143]
v_mfma_f32_16x16x32_bf16 v[132:135],v[76:79],v[24:27],v[132:135]
s_waitcnt lgkmcnt(8)
ds_read_u16_d16_hi v155,v155
v_mfma_f32_16x16x32_bf16 v[128:131],v[80:83],v[32:35],v[128:131]
v_mfma_f32_16x16x32_bf16 v[136:139],v[80:83],v[36:39],v[136:139]
s_waitcnt lgkmcnt(4)
ds_read_u16 v156,v156
v_lshl_add_u64 v[144:145],v[144:145],0,v[146:147]
v_lshl_add_u64 v[146:147],v[148:149],0,v[150:151]
v_mfma_f32_16x16x32_bf16 v[140:143],v[84:87],v[36:39],v[140:143]
ds_read_u16_d16_hi v158,v158
v_mfma_f32_16x16x32_bf16 v[132:135],v[84:87],v[32:35],v[132:135]
v_lshlrev_b32_sdwa v128,1,v128 dst_sel:DWORD dst_unused:UNUSED_PAD src0_sel:DWORD src1_sel:WORD_1
v_lshlrev_b32_sdwa v130,1,v130 dst_sel:DWORD dst_unused:UNUSED_PAD src0_sel:DWORD src1_sel:WORD_1
v_mfma_f32_16x16x32_bf16 v[208:211],v[88:91],v[144:147],v[208:211]
ds_read_u16 v128,v128
v_lshlrev_b32_sdwa v129,1,v129 dst_sel:DWORD dst_unused:UNUSED_PAD src0_sel:DWORD src1_sel:WORD_1
v_lshlrev_b32_sdwa v131,1,v131 dst_sel:DWORD dst_unused:UNUSED_PAD src0_sel:DWORD src1_sel:WORD_1
v_mfma_f32_16x16x32_bf16 v[216:219],v[92:95],v[144:147],v[216:219]
v_fma_mixlo_f16 v120,v141,s99,0
v_lshlrev_b32_sdwa v132,1,v132 dst_sel:DWORD dst_unused:UNUSED_PAD src0_sel:DWORD src1_sel:WORD_1
v_mfma_f32_16x16x32_bf16 v[224:227],v[96:99],v[144:147],v[224:227]
ds_read_u16_d16_hi v130,v130
v_fma_mixhi_f16 v120,v143,s99,0
v_lshlrev_b32_sdwa v134,1,v134 dst_sel:DWORD dst_unused:UNUSED_PAD src0_sel:DWORD src1_sel:WORD_1
v_mfma_f32_16x16x32_bf16 v[232:235],v[100:103],v[144:147],v[232:235]
s_waitcnt lgkmcnt(4)
ds_read_u16 v129,v129
v_lshl_add_u64 v[152:153],v[152:153],0,v[154:155]
v_mfma_f32_16x16x32_bf16 v[240:243],v[104:107],v[144:147],v[240:243]
s_waitcnt lgkmcnt(3)
v_or_b32 v154,v158,v156
v_cvt_pk_bf16_f32 v155,v157,v159
v_mfma_f32_16x16x32_bf16 v[248:251],v[108:111],v[144:147],v[248:251]
ds_read_u16_d16_hi v131,v131
v_pk_fma_f16 v121,v120,v120,s54
v_lshlrev_b32_sdwa v133,1,v133 dst_sel:DWORD dst_unused:UNUSED_PAD src0_sel:DWORD src1_sel:WORD_1
v_mfma_f32_16x16x32_bf16 v[212:215],v[88:91],v[152:155],v[212:215]
ds_read_b64_tr_b16 v[88:89],v6 offset:6160
v_pk_mul_f16 v121,v120,v121
v_lshlrev_b32_sdwa v135,1,v135 dst_sel:DWORD dst_unused:UNUSED_PAD src0_sel:DWORD src1_sel:WORD_1
v_mfma_f32_16x16x32_bf16 v[220:223],v[92:95],v[152:155],v[220:223]
ds_read_u16 v132,v132
v_exp_f16 v120,v121
v_mfma_f32_16x16x32_bf16 v[228:231],v[96:99],v[152:155],v[228:231]
ds_read_b64_tr_b16 v[90:91],v6 offset:7696
ds_read_b64_tr_b16 v[92:93],v6 offset:6168
v_lshlrev_b32_sdwa v136,1,v136 dst_sel:DWORD dst_unused:UNUSED_PAD src0_sel:DWORD src1_sel:WORD_1
v_mfma_f32_16x16x32_bf16 v[236:239],v[100:103],v[152:155],v[236:239]
ds_read_u16_d16_hi v134,v134
v_exp_f16_sdwa v120,v121 dst_sel:WORD_1 dst_unused:UNUSED_PRESERVE src0_sel:WORD_1
v_mfma_f32_16x16x32_bf16 v[244:247],v[104:107],v[152:155],v[244:247]
ds_read_u16 v133,v133
v_lshlrev_b32_sdwa v138,1,v138 dst_sel:DWORD dst_unused:UNUSED_PAD src0_sel:DWORD src1_sel:WORD_1
v_mfma_f32_16x16x32_bf16 v[252:255],v[108:111],v[152:155],v[252:255]
ds_read_b64_tr_b16 v[94:95],v6 offset:7704
ds_read_b64_tr_b16 v[96:97],v6 offset:6224
v_pk_fma_f16 v120,v120,s55,s55
v_lshlrev_b32_sdwa v137,1,v137 dst_sel:DWORD dst_unused:UNUSED_PAD src0_sel:DWORD src1_sel:WORD_1
v_mfma_f32_16x16x32_bf16 v[144:147],v[64:67],v[40:43],v[112:115]
ds_read_u16_d16_hi v135,v135
v_rcp_f16 v121,v120
v_mfma_f32_16x16x32_bf16 v[152:155],v[64:67],v[44:47],v[112:115]
ds_read_b64_tr_b16 v[98:99],v6 offset:7760
ds_read_b64_tr_b16 v[100:101],v6 offset:6232
v_lshlrev_b32_sdwa v139,1,v139 dst_sel:DWORD dst_unused:UNUSED_PAD src0_sel:DWORD src1_sel:WORD_1
v_mfma_f32_16x16x32_bf16 v[156:159],v[68:71],v[44:47],v[116:119]
ds_read_u16 v136,v136
v_rcp_f16_sdwa v121,v120 dst_sel:WORD_1 dst_unused:UNUSED_PRESERVE src0_sel:WORD_1
v_mfma_f32_16x16x32_bf16 v[148:151],v[68:71],v[40:43],v[116:119]
ds_read_u16_d16_hi v138,v138
v_lshlrev_b32_sdwa v140,1,v140 dst_sel:DWORD dst_unused:UNUSED_PAD src0_sel:DWORD src1_sel:WORD_1
v_mfma_f32_16x16x32_bf16 v[144:147],v[72:75],v[48:51],v[144:147]
ds_read_b64_tr_b16 v[102:103],v6 offset:7768
ds_read_b64_tr_b16 v[104:105],v6 offset:6288
v_fma_mix_f32 v141,v141,v121,0 op_sel_hi:[0,1,0]
v_lshlrev_b32_sdwa v142,1,v142 dst_sel:DWORD dst_unused:UNUSED_PAD src0_sel:DWORD src1_sel:WORD_1
v_mfma_f32_16x16x32_bf16 v[152:155],v[72:75],v[52:55],v[152:155]
ds_read_u16 v137,v137
v_fma_mix_f32 v143,v143,v121,0 op_sel:[0,1,0] op_sel_hi:[0,1,0]
v_mfma_f32_16x16x32_bf16 v[156:159],v[76:79],v[52:55],v[156:159]
ds_read_b64_tr_b16 v[106:107],v6 offset:7824
ds_read_b64_tr_b16 v[108:109],v6 offset:6296
v_mfma_f32_16x16x32_bf16 v[148:151],v[76:79],v[48:51],v[148:151]
ds_read_u16_d16_hi v139,v139
v_mfma_f32_16x16x32_bf16 v[144:147],v[80:83],v[56:59],v[144:147]
ds_read_b64_tr_b16 v[110:111],v6 offset:7832
ds_read_b64_tr_b16 v[64:65],v4 offset:512
v_mfma_f32_16x16x32_bf16 v[152:155],v[80:83],v[60:63],v[152:155]
s_waitcnt lgkmcnt(12)
ds_read_u16 v140,v140
v_lshl_add_u64 v[128:129],v[128:129],0,v[130:131]
v_lshl_add_u64 v[130:131],v[132:133],0,v[134:135]
v_mfma_f32_16x16x32_bf16 v[156:159],v[84:87],v[60:63],v[156:159]
ds_read_u16_d16_hi v142,v142
v_mfma_f32_16x16x32_bf16 v[148:151],v[84:87],v[56:59],v[148:151]
ds_read_b64_tr_b16 v[66:67],v4 offset:2560
ds_read_b128 v[112:115],v7 offset:384
v_lshlrev_b32_sdwa v144,1,v144 dst_sel:DWORD dst_unused:UNUSED_PAD src0_sel:DWORD src1_sel:WORD_1
v_lshlrev_b32_sdwa v146,1,v146 dst_sel:DWORD dst_unused:UNUSED_PAD src0_sel:DWORD src1_sel:WORD_1
v_mfma_f32_16x16x32_bf16 v[160:163],v[88:91],v[128:131],v[160:163]
ds_read_u16 v144,v144
v_lshlrev_b32_sdwa v145,1,v145 dst_sel:DWORD dst_unused:UNUSED_PAD src0_sel:DWORD src1_sel:WORD_1
v_lshlrev_b32_sdwa v147,1,v147 dst_sel:DWORD dst_unused:UNUSED_PAD src0_sel:DWORD src1_sel:WORD_1
v_mfma_f32_16x16x32_bf16 v[168:171],v[92:95],v[128:131],v[168:171]
s_waitcnt lgkmcnt(15)
ds_read_b64_tr_b16 v[68:69],v5 offset:512
ds_read_b64_tr_b16 v[70:71],v5 offset:2560
v_fma_mixlo_f16 v120,v157,s99,0
v_lshlrev_b32_sdwa v148,1,v148 dst_sel:DWORD dst_unused:UNUSED_PAD src0_sel:DWORD src1_sel:WORD_1
v_mfma_f32_16x16x32_bf16 v[176:179],v[96:99],v[128:131],v[176:179]
s_waitcnt lgkmcnt(14)
ds_read_u16_d16_hi v146,v146
v_fma_mixhi_f16 v120,v159,s99,0
v_lshlrev_b32_sdwa v150,1,v150 dst_sel:DWORD dst_unused:UNUSED_PAD src0_sel:DWORD src1_sel:WORD_1
v_mfma_f32_16x16x32_bf16 v[184:187],v[100:103],v[128:131],v[184:187]
s_waitcnt lgkmcnt(10)
ds_read_u16 v145,v145
v_lshl_add_u64 v[136:137],v[136:137],0,v[138:139]
v_mfma_f32_16x16x32_bf16 v[192:195],v[104:107],v[128:131],v[192:195]
s_waitcnt lgkmcnt(7)
ds_read_b128 v[116:119],v7 offset:416
ds_read_b64_tr_b16 v[72:73],v4 offset:4608
v_or_b32 v138,v142,v140
v_cvt_pk_bf16_f32 v139,v141,v143
v_mfma_f32_16x16x32_bf16 v[200:203],v[108:111],v[128:131],v[200:203]
ds_read_u16_d16_hi v147,v147
v_pk_fma_f16 v121,v120,v120,s54
v_lshlrev_b32_sdwa v149,1,v149 dst_sel:DWORD dst_unused:UNUSED_PAD src0_sel:DWORD src1_sel:WORD_1
v_mfma_f32_16x16x32_bf16 v[164:167],v[88:91],v[136:139],v[164:167]
ds_read_b64_tr_b16 v[74:75],v4 offset:6656
ds_read_b64_tr_b16 v[76:77],v5 offset:4608
v_pk_mul_f16 v121,v120,v121
v_lshlrev_b32_sdwa v151,1,v151 dst_sel:DWORD dst_unused:UNUSED_PAD src0_sel:DWORD src1_sel:WORD_1
v_mfma_f32_16x16x32_bf16 v[172:175],v[92:95],v[136:139],v[172:175]
ds_read_u16 v148,v148
v_exp_f16 v120,v121
v_mfma_f32_16x16x32_bf16 v[180:183],v[96:99],v[136:139],v[180:183]
ds_read_b64_tr_b16 v[78:79],v5 offset:6656
ds_read_b64_tr_b16 v[80:81],v4 offset:8704
v_lshlrev_b32_sdwa v152,1,v152 dst_sel:DWORD dst_unused:UNUSED_PAD src0_sel:DWORD src1_sel:WORD_1
v_mfma_f32_16x16x32_bf16 v[188:191],v[100:103],v[136:139],v[188:191]
ds_read_u16_d16_hi v150,v150
v_exp_f16_sdwa v120,v121 dst_sel:WORD_1 dst_unused:UNUSED_PRESERVE src0_sel:WORD_1
v_mfma_f32_16x16x32_bf16 v[196:199],v[104:107],v[136:139],v[196:199]
ds_read_u16 v149,v149
v_lshlrev_b32_sdwa v154,1,v154 dst_sel:DWORD dst_unused:UNUSED_PAD src0_sel:DWORD src1_sel:WORD_1
v_mfma_f32_16x16x32_bf16 v[204:207],v[108:111],v[136:139],v[204:207]
s_waitcnt lgkmcnt(15)
ds_read_b64_tr_b16 v[82:83],v4 offset:10752
v_pk_fma_f16 v120,v120,s55,s55
v_lshlrev_b32_sdwa v153,1,v153 dst_sel:DWORD dst_unused:UNUSED_PAD src0_sel:DWORD src1_sel:WORD_1
v_mfma_f32_16x16x32_bf16 v[128:131],v[64:67],v[16:19],v[112:115]
ds_read_u16_d16_hi v151,v151
v_rcp_f16 v121,v120
v_mfma_f32_16x16x32_bf16 v[136:139],v[64:67],v[20:23],v[112:115]
s_waitcnt lgkmcnt(11)
ds_read_b64_tr_b16 v[84:85],v5 offset:8704
v_lshlrev_b32_sdwa v155,1,v155 dst_sel:DWORD dst_unused:UNUSED_PAD src0_sel:DWORD src1_sel:WORD_1
v_mfma_f32_16x16x32_bf16 v[140:143],v[68:71],v[20:23],v[116:119]
ds_read_u16 v152,v152
v_rcp_f16_sdwa v121,v120 dst_sel:WORD_1 dst_unused:UNUSED_PRESERVE src0_sel:WORD_1
v_mfma_f32_16x16x32_bf16 v[132:135],v[68:71],v[16:19],v[116:119]
s_waitcnt lgkmcnt(10)
ds_read_u16_d16_hi v154,v154
v_lshlrev_b32_sdwa v156,1,v156 dst_sel:DWORD dst_unused:UNUSED_PAD src0_sel:DWORD src1_sel:WORD_1
v_mfma_f32_16x16x32_bf16 v[128:131],v[72:75],v[24:27],v[128:131]
ds_read_b64_tr_b16 v[86:87],v5 offset:10752
v_fma_mix_f32 v157,v157,v121,0 op_sel_hi:[0,1,0]
v_lshlrev_b32_sdwa v158,1,v158 dst_sel:DWORD dst_unused:UNUSED_PAD src0_sel:DWORD src1_sel:WORD_1
v_mfma_f32_16x16x32_bf16 v[136:139],v[72:75],v[28:31],v[136:139]
s_waitcnt lgkmcnt(9)
ds_read_u16 v153,v153
s_mov_b32 s86,0xbe801d5a
v_fma_mix_f32 v159,v159,v121,0 op_sel:[0,1,0] op_sel_hi:[0,1,0]
v_mfma_f32_16x16x32_bf16 v[140:143],v[76:79],v[28:31],v[140:143]
v_mfma_f32_16x16x32_bf16 v[132:135],v[76:79],v[24:27],v[132:135]
s_waitcnt lgkmcnt(6)
ds_read_u16_d16_hi v155,v155
v_mfma_f32_16x16x32_bf16 v[128:131],v[80:83],v[32:35],v[128:131]
v_mfma_f32_16x16x32_bf16 v[136:139],v[80:83],v[36:39],v[136:139]
s_waitcnt lgkmcnt(2)
ds_read_u16 v156,v156
v_lshl_add_u64 v[144:145],v[144:145],0,v[146:147]
v_lshl_add_u64 v[146:147],v[148:149],0,v[150:151]
v_mfma_f32_16x16x32_bf16 v[140:143],v[84:87],v[36:39],v[140:143]
ds_read_u16_d16_hi v158,v158
v_mfma_f32_16x16x32_bf16 v[132:135],v[84:87],v[32:35],v[132:135]
v_lshlrev_b32_sdwa v128,1,v128 dst_sel:DWORD dst_unused:UNUSED_PAD src0_sel:DWORD src1_sel:WORD_1
v_lshlrev_b32_sdwa v130,1,v130 dst_sel:DWORD dst_unused:UNUSED_PAD src0_sel:DWORD src1_sel:WORD_1
v_mfma_f32_16x16x32_bf16 v[208:211],v[88:91],v[144:147],v[208:211]
s_barrier
ds_read_u16 v128,v128
v_lshlrev_b32_sdwa v129,1,v129 dst_sel:DWORD dst_unused:UNUSED_PAD src0_sel:DWORD src1_sel:WORD_1
v_lshlrev_b32_sdwa v131,1,v131 dst_sel:DWORD dst_unused:UNUSED_PAD src0_sel:DWORD src1_sel:WORD_1
v_mfma_f32_16x16x32_bf16 v[216:219],v[92:95],v[144:147],v[216:219]
v_fma_mixlo_f16 v120,v141,s99,0
v_lshlrev_b32_sdwa v132,1,v132 dst_sel:DWORD dst_unused:UNUSED_PAD src0_sel:DWORD src1_sel:WORD_1
v_mfma_f32_16x16x32_bf16 v[224:227],v[96:99],v[144:147],v[224:227]
ds_read_u16_d16_hi v130,v130
v_fma_mixhi_f16 v120,v143,s99,0
v_lshlrev_b32_sdwa v134,1,v134 dst_sel:DWORD dst_unused:UNUSED_PAD src0_sel:DWORD src1_sel:WORD_1
v_mfma_f32_16x16x32_bf16 v[232:235],v[100:103],v[144:147],v[232:235]
s_waitcnt lgkmcnt(4)
ds_read_u16 v129,v129
v_lshl_add_u64 v[152:153],v[152:153],0,v[154:155]
v_mfma_f32_16x16x32_bf16 v[240:243],v[104:107],v[144:147],v[240:243]
s_waitcnt lgkmcnt(3)
v_or_b32 v154,v158,v156
v_cvt_pk_bf16_f32 v155,v157,v159
v_mfma_f32_16x16x32_bf16 v[248:251],v[108:111],v[144:147],v[248:251]
ds_read_u16_d16_hi v131,v131
v_pk_fma_f16 v121,v120,v120,s54
v_lshlrev_b32_sdwa v133,1,v133 dst_sel:DWORD dst_unused:UNUSED_PAD src0_sel:DWORD src1_sel:WORD_1
v_mfma_f32_16x16x32_bf16 v[212:215],v[88:91],v[152:155],v[212:215]
ds_read_b64_tr_b16 v[88:89],v6 offset:12320
v_pk_mul_f16 v121,v120,v121
v_lshlrev_b32_sdwa v135,1,v135 dst_sel:DWORD dst_unused:UNUSED_PAD src0_sel:DWORD src1_sel:WORD_1
v_mfma_f32_16x16x32_bf16 v[220:223],v[92:95],v[152:155],v[220:223]
ds_read_u16 v132,v132
v_exp_f16 v120,v121
v_mfma_f32_16x16x32_bf16 v[228:231],v[96:99],v[152:155],v[228:231]
ds_read_b64_tr_b16 v[90:91],v6 offset:13856
ds_read_b64_tr_b16 v[92:93],v6 offset:12328
v_lshlrev_b32_sdwa v136,1,v136 dst_sel:DWORD dst_unused:UNUSED_PAD src0_sel:DWORD src1_sel:WORD_1
v_mfma_f32_16x16x32_bf16 v[236:239],v[100:103],v[152:155],v[236:239]
ds_read_u16_d16_hi v134,v134
v_exp_f16_sdwa v120,v121 dst_sel:WORD_1 dst_unused:UNUSED_PRESERVE src0_sel:WORD_1
v_mfma_f32_16x16x32_bf16 v[244:247],v[104:107],v[152:155],v[244:247]
ds_read_u16 v133,v133
v_lshlrev_b32_sdwa v138,1,v138 dst_sel:DWORD dst_unused:UNUSED_PAD src0_sel:DWORD src1_sel:WORD_1
v_mfma_f32_16x16x32_bf16 v[252:255],v[108:111],v[152:155],v[252:255]
ds_read_b64_tr_b16 v[94:95],v6 offset:13864
ds_read_b64_tr_b16 v[96:97],v6 offset:12384
v_pk_fma_f16 v120,v120,s55,s55
v_lshlrev_b32_sdwa v137,1,v137 dst_sel:DWORD dst_unused:UNUSED_PAD src0_sel:DWORD src1_sel:WORD_1
v_mfma_f32_16x16x32_bf16 v[144:147],v[64:67],v[40:43],v[112:115]
ds_read_u16_d16_hi v135,v135
v_rcp_f16 v121,v120
v_mfma_f32_16x16x32_bf16 v[152:155],v[64:67],v[44:47],v[112:115]
ds_read_b64_tr_b16 v[98:99],v6 offset:13920
ds_read_b64_tr_b16 v[100:101],v6 offset:12392
v_lshlrev_b32_sdwa v139,1,v139 dst_sel:DWORD dst_unused:UNUSED_PAD src0_sel:DWORD src1_sel:WORD_1
v_mfma_f32_16x16x32_bf16 v[156:159],v[68:71],v[44:47],v[116:119]
ds_read_u16 v136,v136
v_rcp_f16_sdwa v121,v120 dst_sel:WORD_1 dst_unused:UNUSED_PRESERVE src0_sel:WORD_1
v_mfma_f32_16x16x32_bf16 v[148:151],v[68:71],v[40:43],v[116:119]
ds_read_u16_d16_hi v138,v138
v_lshlrev_b32_sdwa v140,1,v140 dst_sel:DWORD dst_unused:UNUSED_PAD src0_sel:DWORD src1_sel:WORD_1
v_mfma_f32_16x16x32_bf16 v[144:147],v[72:75],v[48:51],v[144:147]
ds_read_b64_tr_b16 v[102:103],v6 offset:13928
ds_read_b64_tr_b16 v[104:105],v6 offset:12448
v_fma_mix_f32 v141,v141,v121,0 op_sel_hi:[0,1,0]
v_lshlrev_b32_sdwa v142,1,v142 dst_sel:DWORD dst_unused:UNUSED_PAD src0_sel:DWORD src1_sel:WORD_1
v_mfma_f32_16x16x32_bf16 v[152:155],v[72:75],v[52:55],v[152:155]
ds_read_u16 v137,v137
v_fma_mix_f32 v143,v143,v121,0 op_sel:[0,1,0] op_sel_hi:[0,1,0]
v_mfma_f32_16x16x32_bf16 v[156:159],v[76:79],v[52:55],v[156:159]
ds_read_b64_tr_b16 v[106:107],v6 offset:13984
ds_read_b64_tr_b16 v[108:109],v6 offset:12456
v_mfma_f32_16x16x32_bf16 v[148:151],v[76:79],v[48:51],v[148:151]
ds_read_u16_d16_hi v139,v139
v_mfma_f32_16x16x32_bf16 v[144:147],v[80:83],v[56:59],v[144:147]
ds_read_b64_tr_b16 v[110:111],v6 offset:13992
ds_read_b64_tr_b16 v[64:65],v4 offset:12320
v_mfma_f32_16x16x32_bf16 v[152:155],v[80:83],v[60:63],v[152:155]
s_waitcnt lgkmcnt(12)
ds_read_u16 v140,v140
v_lshl_add_u64 v[128:129],v[128:129],0,v[130:131]
v_lshl_add_u64 v[130:131],v[132:133],0,v[134:135]
v_mfma_f32_16x16x32_bf16 v[156:159],v[84:87],v[60:63],v[156:159]
ds_read_u16_d16_hi v142,v142
v_mfma_f32_16x16x32_bf16 v[148:151],v[84:87],v[56:59],v[148:151]
ds_read_b64_tr_b16 v[66:67],v4 offset:14368
ds_read_b128 v[112:115],v7 offset:512
v_lshlrev_b32_sdwa v144,1,v144 dst_sel:DWORD dst_unused:UNUSED_PAD src0_sel:DWORD src1_sel:WORD_1
v_lshlrev_b32_sdwa v146,1,v146 dst_sel:DWORD dst_unused:UNUSED_PAD src0_sel:DWORD src1_sel:WORD_1
v_mfma_f32_16x16x32_bf16 v[160:163],v[88:91],v[128:131],v[160:163]
ds_read_u16 v144,v144
v_lshlrev_b32_sdwa v145,1,v145 dst_sel:DWORD dst_unused:UNUSED_PAD src0_sel:DWORD src1_sel:WORD_1
v_lshlrev_b32_sdwa v147,1,v147 dst_sel:DWORD dst_unused:UNUSED_PAD src0_sel:DWORD src1_sel:WORD_1
v_mfma_f32_16x16x32_bf16 v[168:171],v[92:95],v[128:131],v[168:171]
s_waitcnt lgkmcnt(15)
ds_read_b64_tr_b16 v[68:69],v5 offset:12320
ds_read_b64_tr_b16 v[70:71],v5 offset:14368
v_fma_mixlo_f16 v120,v157,s99,0
v_lshlrev_b32_sdwa v148,1,v148 dst_sel:DWORD dst_unused:UNUSED_PAD src0_sel:DWORD src1_sel:WORD_1
v_mfma_f32_16x16x32_bf16 v[176:179],v[96:99],v[128:131],v[176:179]
s_waitcnt lgkmcnt(14)
ds_read_u16_d16_hi v146,v146
v_fma_mixhi_f16 v120,v159,s99,0
v_lshlrev_b32_sdwa v150,1,v150 dst_sel:DWORD dst_unused:UNUSED_PAD src0_sel:DWORD src1_sel:WORD_1
v_mfma_f32_16x16x32_bf16 v[184:187],v[100:103],v[128:131],v[184:187]
s_waitcnt lgkmcnt(10)
ds_read_u16 v145,v145
v_lshl_add_u64 v[136:137],v[136:137],0,v[138:139]
v_mfma_f32_16x16x32_bf16 v[192:195],v[104:107],v[128:131],v[192:195]
s_waitcnt lgkmcnt(7)
ds_read_b128 v[116:119],v7 offset:544
ds_read_b64_tr_b16 v[72:73],v4 offset:16416
v_or_b32 v138,v142,v140
v_cvt_pk_bf16_f32 v139,v141,v143
v_mfma_f32_16x16x32_bf16 v[200:203],v[108:111],v[128:131],v[200:203]
ds_read_u16_d16_hi v147,v147
v_pk_fma_f16 v121,v120,v120,s54
v_lshlrev_b32_sdwa v149,1,v149 dst_sel:DWORD dst_unused:UNUSED_PAD src0_sel:DWORD src1_sel:WORD_1
v_mfma_f32_16x16x32_bf16 v[164:167],v[88:91],v[136:139],v[164:167]
ds_read_b64_tr_b16 v[74:75],v4 offset:18464
ds_read_b64_tr_b16 v[76:77],v5 offset:16416
v_pk_mul_f16 v121,v120,v121
v_lshlrev_b32_sdwa v151,1,v151 dst_sel:DWORD dst_unused:UNUSED_PAD src0_sel:DWORD src1_sel:WORD_1
v_mfma_f32_16x16x32_bf16 v[172:175],v[92:95],v[136:139],v[172:175]
ds_read_u16 v148,v148
v_exp_f16 v120,v121
v_mfma_f32_16x16x32_bf16 v[180:183],v[96:99],v[136:139],v[180:183]
ds_read_b64_tr_b16 v[78:79],v5 offset:18464
ds_read_b64_tr_b16 v[80:81],v4 offset:20512
v_lshlrev_b32_sdwa v152,1,v152 dst_sel:DWORD dst_unused:UNUSED_PAD src0_sel:DWORD src1_sel:WORD_1
v_mfma_f32_16x16x32_bf16 v[188:191],v[100:103],v[136:139],v[188:191]
ds_read_u16_d16_hi v150,v150
v_exp_f16_sdwa v120,v121 dst_sel:WORD_1 dst_unused:UNUSED_PRESERVE src0_sel:WORD_1
v_mfma_f32_16x16x32_bf16 v[196:199],v[104:107],v[136:139],v[196:199]
ds_read_u16 v149,v149
v_lshlrev_b32_sdwa v154,1,v154 dst_sel:DWORD dst_unused:UNUSED_PAD src0_sel:DWORD src1_sel:WORD_1
v_mfma_f32_16x16x32_bf16 v[204:207],v[108:111],v[136:139],v[204:207]
s_waitcnt lgkmcnt(15)
ds_read_b64_tr_b16 v[82:83],v4 offset:22560
v_pk_fma_f16 v120,v120,s55,s55
v_lshlrev_b32_sdwa v153,1,v153 dst_sel:DWORD dst_unused:UNUSED_PAD src0_sel:DWORD src1_sel:WORD_1
v_mfma_f32_16x16x32_bf16 v[128:131],v[64:67],v[16:19],v[112:115]
ds_read_u16_d16_hi v151,v151
v_rcp_f16 v121,v120
v_mfma_f32_16x16x32_bf16 v[136:139],v[64:67],v[20:23],v[112:115]
s_waitcnt lgkmcnt(11)
ds_read_b64_tr_b16 v[84:85],v5 offset:20512
v_lshlrev_b32_sdwa v155,1,v155 dst_sel:DWORD dst_unused:UNUSED_PAD src0_sel:DWORD src1_sel:WORD_1
v_mfma_f32_16x16x32_bf16 v[140:143],v[68:71],v[20:23],v[116:119]
ds_read_u16 v152,v152
v_rcp_f16_sdwa v121,v120 dst_sel:WORD_1 dst_unused:UNUSED_PRESERVE src0_sel:WORD_1
v_mfma_f32_16x16x32_bf16 v[132:135],v[68:71],v[16:19],v[116:119]
s_waitcnt lgkmcnt(10)
ds_read_u16_d16_hi v154,v154
v_lshlrev_b32_sdwa v156,1,v156 dst_sel:DWORD dst_unused:UNUSED_PAD src0_sel:DWORD src1_sel:WORD_1
v_mfma_f32_16x16x32_bf16 v[128:131],v[72:75],v[24:27],v[128:131]
ds_read_b64_tr_b16 v[86:87],v5 offset:22560
v_fma_mix_f32 v157,v157,v121,0 op_sel_hi:[0,1,0]
v_lshlrev_b32_sdwa v158,1,v158 dst_sel:DWORD dst_unused:UNUSED_PAD src0_sel:DWORD src1_sel:WORD_1
v_mfma_f32_16x16x32_bf16 v[136:139],v[72:75],v[28:31],v[136:139]
s_waitcnt lgkmcnt(9)
ds_read_u16 v153,v153
v_fma_mix_f32 v159,v159,v121,0 op_sel:[0,1,0] op_sel_hi:[0,1,0]
v_mfma_f32_16x16x32_bf16 v[140:143],v[76:79],v[28:31],v[140:143]
v_mfma_f32_16x16x32_bf16 v[132:135],v[76:79],v[24:27],v[132:135]
s_waitcnt lgkmcnt(6)
ds_read_u16_d16_hi v155,v155
v_mfma_f32_16x16x32_bf16 v[128:131],v[80:83],v[32:35],v[128:131]
v_mfma_f32_16x16x32_bf16 v[136:139],v[80:83],v[36:39],v[136:139]
s_waitcnt lgkmcnt(2)
ds_read_u16 v156,v156
v_lshl_add_u64 v[144:145],v[144:145],0,v[146:147]
v_lshl_add_u64 v[146:147],v[148:149],0,v[150:151]
v_mfma_f32_16x16x32_bf16 v[140:143],v[84:87],v[36:39],v[140:143]
ds_read_u16_d16_hi v158,v158
v_mfma_f32_16x16x32_bf16 v[132:135],v[84:87],v[32:35],v[132:135]
v_lshlrev_b32_sdwa v128,1,v128 dst_sel:DWORD dst_unused:UNUSED_PAD src0_sel:DWORD src1_sel:WORD_1
v_lshlrev_b32_sdwa v130,1,v130 dst_sel:DWORD dst_unused:UNUSED_PAD src0_sel:DWORD src1_sel:WORD_1
v_mfma_f32_16x16x32_bf16 v[208:211],v[88:91],v[144:147],v[208:211]
ds_read_u16 v128,v128
v_lshlrev_b32_sdwa v129,1,v129 dst_sel:DWORD dst_unused:UNUSED_PAD src0_sel:DWORD src1_sel:WORD_1
v_lshlrev_b32_sdwa v131,1,v131 dst_sel:DWORD dst_unused:UNUSED_PAD src0_sel:DWORD src1_sel:WORD_1
v_mfma_f32_16x16x32_bf16 v[216:219],v[92:95],v[144:147],v[216:219]
v_fma_mixlo_f16 v120,v141,s99,0
v_lshlrev_b32_sdwa v132,1,v132 dst_sel:DWORD dst_unused:UNUSED_PAD src0_sel:DWORD src1_sel:WORD_1
v_mfma_f32_16x16x32_bf16 v[224:227],v[96:99],v[144:147],v[224:227]
ds_read_u16_d16_hi v130,v130
v_fma_mixhi_f16 v120,v143,s99,0
v_lshlrev_b32_sdwa v134,1,v134 dst_sel:DWORD dst_unused:UNUSED_PAD src0_sel:DWORD src1_sel:WORD_1
v_mfma_f32_16x16x32_bf16 v[232:235],v[100:103],v[144:147],v[232:235]
s_waitcnt lgkmcnt(4)
ds_read_u16 v129,v129
v_lshl_add_u64 v[152:153],v[152:153],0,v[154:155]
v_mfma_f32_16x16x32_bf16 v[240:243],v[104:107],v[144:147],v[240:243]
s_waitcnt lgkmcnt(3)
v_or_b32 v154,v158,v156
v_cvt_pk_bf16_f32 v155,v157,v159
v_mfma_f32_16x16x32_bf16 v[248:251],v[108:111],v[144:147],v[248:251]
ds_read_u16_d16_hi v131,v131
v_pk_fma_f16 v121,v120,v120,s54
v_lshlrev_b32_sdwa v133,1,v133 dst_sel:DWORD dst_unused:UNUSED_PAD src0_sel:DWORD src1_sel:WORD_1
v_mfma_f32_16x16x32_bf16 v[212:215],v[88:91],v[152:155],v[212:215]
ds_read_b64_tr_b16 v[88:89],v6 offset:18480
v_pk_mul_f16 v121,v120,v121
v_lshlrev_b32_sdwa v135,1,v135 dst_sel:DWORD dst_unused:UNUSED_PAD src0_sel:DWORD src1_sel:WORD_1
v_mfma_f32_16x16x32_bf16 v[220:223],v[92:95],v[152:155],v[220:223]
ds_read_u16 v132,v132
v_exp_f16 v120,v121
v_mfma_f32_16x16x32_bf16 v[228:231],v[96:99],v[152:155],v[228:231]
ds_read_b64_tr_b16 v[90:91],v6 offset:20016
ds_read_b64_tr_b16 v[92:93],v6 offset:18488
v_lshlrev_b32_sdwa v136,1,v136 dst_sel:DWORD dst_unused:UNUSED_PAD src0_sel:DWORD src1_sel:WORD_1
v_mfma_f32_16x16x32_bf16 v[236:239],v[100:103],v[152:155],v[236:239]
ds_read_u16_d16_hi v134,v134
v_exp_f16_sdwa v120,v121 dst_sel:WORD_1 dst_unused:UNUSED_PRESERVE src0_sel:WORD_1
v_mfma_f32_16x16x32_bf16 v[244:247],v[104:107],v[152:155],v[244:247]
ds_read_u16 v133,v133
v_lshlrev_b32_sdwa v138,1,v138 dst_sel:DWORD dst_unused:UNUSED_PAD src0_sel:DWORD src1_sel:WORD_1
v_mfma_f32_16x16x32_bf16 v[252:255],v[108:111],v[152:155],v[252:255]
ds_read_b64_tr_b16 v[94:95],v6 offset:20024
ds_read_b64_tr_b16 v[96:97],v6 offset:18544
v_pk_fma_f16 v120,v120,s55,s55
v_lshlrev_b32_sdwa v137,1,v137 dst_sel:DWORD dst_unused:UNUSED_PAD src0_sel:DWORD src1_sel:WORD_1
v_mfma_f32_16x16x32_bf16 v[144:147],v[64:67],v[40:43],v[112:115]
ds_read_u16_d16_hi v135,v135
v_rcp_f16 v121,v120
v_mfma_f32_16x16x32_bf16 v[152:155],v[64:67],v[44:47],v[112:115]
ds_read_b64_tr_b16 v[98:99],v6 offset:20080
ds_read_b64_tr_b16 v[100:101],v6 offset:18552
v_lshlrev_b32_sdwa v139,1,v139 dst_sel:DWORD dst_unused:UNUSED_PAD src0_sel:DWORD src1_sel:WORD_1
v_mfma_f32_16x16x32_bf16 v[156:159],v[68:71],v[44:47],v[116:119]
ds_read_u16 v136,v136
v_rcp_f16_sdwa v121,v120 dst_sel:WORD_1 dst_unused:UNUSED_PRESERVE src0_sel:WORD_1
v_mfma_f32_16x16x32_bf16 v[148:151],v[68:71],v[40:43],v[116:119]
ds_read_u16_d16_hi v138,v138
v_lshlrev_b32_sdwa v140,1,v140 dst_sel:DWORD dst_unused:UNUSED_PAD src0_sel:DWORD src1_sel:WORD_1
v_mfma_f32_16x16x32_bf16 v[144:147],v[72:75],v[48:51],v[144:147]
ds_read_b64_tr_b16 v[102:103],v6 offset:20088
ds_read_b64_tr_b16 v[104:105],v6 offset:18608
v_fma_mix_f32 v141,v141,v121,0 op_sel_hi:[0,1,0]
v_lshlrev_b32_sdwa v142,1,v142 dst_sel:DWORD dst_unused:UNUSED_PAD src0_sel:DWORD src1_sel:WORD_1
v_mfma_f32_16x16x32_bf16 v[152:155],v[72:75],v[52:55],v[152:155]
ds_read_u16 v137,v137
v_fma_mix_f32 v143,v143,v121,0 op_sel:[0,1,0] op_sel_hi:[0,1,0]
v_mfma_f32_16x16x32_bf16 v[156:159],v[76:79],v[52:55],v[156:159]
ds_read_b64_tr_b16 v[106:107],v6 offset:20144
ds_read_b64_tr_b16 v[108:109],v6 offset:18616
v_mfma_f32_16x16x32_bf16 v[148:151],v[76:79],v[48:51],v[148:151]
ds_read_u16_d16_hi v139,v139
v_mfma_f32_16x16x32_bf16 v[144:147],v[80:83],v[56:59],v[144:147]
ds_read_b64_tr_b16 v[110:111],v6 offset:20152
ds_read_b64_tr_b16 v[64:65],v4 offset:12832
v_mfma_f32_16x16x32_bf16 v[152:155],v[80:83],v[60:63],v[152:155]
s_waitcnt lgkmcnt(12)
ds_read_u16 v140,v140
v_lshl_add_u64 v[128:129],v[128:129],0,v[130:131]
v_lshl_add_u64 v[130:131],v[132:133],0,v[134:135]
v_mfma_f32_16x16x32_bf16 v[156:159],v[84:87],v[60:63],v[156:159]
ds_read_u16_d16_hi v142,v142
v_mfma_f32_16x16x32_bf16 v[148:151],v[84:87],v[56:59],v[148:151]
ds_read_b64_tr_b16 v[66:67],v4 offset:14880
ds_read_b128 v[112:115],v7 offset:640
v_lshlrev_b32_sdwa v144,1,v144 dst_sel:DWORD dst_unused:UNUSED_PAD src0_sel:DWORD src1_sel:WORD_1
v_lshlrev_b32_sdwa v146,1,v146 dst_sel:DWORD dst_unused:UNUSED_PAD src0_sel:DWORD src1_sel:WORD_1
v_mfma_f32_16x16x32_bf16 v[160:163],v[88:91],v[128:131],v[160:163]
ds_read_u16 v144,v144
v_lshlrev_b32_sdwa v145,1,v145 dst_sel:DWORD dst_unused:UNUSED_PAD src0_sel:DWORD src1_sel:WORD_1
v_lshlrev_b32_sdwa v147,1,v147 dst_sel:DWORD dst_unused:UNUSED_PAD src0_sel:DWORD src1_sel:WORD_1
v_mfma_f32_16x16x32_bf16 v[168:171],v[92:95],v[128:131],v[168:171]
s_waitcnt lgkmcnt(15)
ds_read_b64_tr_b16 v[68:69],v5 offset:12832
ds_read_b64_tr_b16 v[70:71],v5 offset:14880
v_fma_mixlo_f16 v120,v157,s99,0
v_lshlrev_b32_sdwa v148,1,v148 dst_sel:DWORD dst_unused:UNUSED_PAD src0_sel:DWORD src1_sel:WORD_1
v_mfma_f32_16x16x32_bf16 v[176:179],v[96:99],v[128:131],v[176:179]
s_waitcnt lgkmcnt(14)
ds_read_u16_d16_hi v146,v146
v_fma_mixhi_f16 v120,v159,s99,0
v_lshlrev_b32_sdwa v150,1,v150 dst_sel:DWORD dst_unused:UNUSED_PAD src0_sel:DWORD src1_sel:WORD_1
v_mfma_f32_16x16x32_bf16 v[184:187],v[100:103],v[128:131],v[184:187]
s_waitcnt lgkmcnt(10)
ds_read_u16 v145,v145
v_lshl_add_u64 v[136:137],v[136:137],0,v[138:139]
v_mfma_f32_16x16x32_bf16 v[192:195],v[104:107],v[128:131],v[192:195]
s_waitcnt lgkmcnt(7)
ds_read_b128 v[116:119],v7 offset:672
ds_read_b64_tr_b16 v[72:73],v4 offset:16928
v_or_b32 v138,v142,v140
v_cvt_pk_bf16_f32 v139,v141,v143
v_mfma_f32_16x16x32_bf16 v[200:203],v[108:111],v[128:131],v[200:203]
ds_read_u16_d16_hi v147,v147
v_pk_fma_f16 v121,v120,v120,s54
v_lshlrev_b32_sdwa v149,1,v149 dst_sel:DWORD dst_unused:UNUSED_PAD src0_sel:DWORD src1_sel:WORD_1
v_mfma_f32_16x16x32_bf16 v[164:167],v[88:91],v[136:139],v[164:167]
ds_read_b64_tr_b16 v[74:75],v4 offset:18976
ds_read_b64_tr_b16 v[76:77],v5 offset:16928
v_pk_mul_f16 v121,v120,v121
v_lshlrev_b32_sdwa v151,1,v151 dst_sel:DWORD dst_unused:UNUSED_PAD src0_sel:DWORD src1_sel:WORD_1
v_mfma_f32_16x16x32_bf16 v[172:175],v[92:95],v[136:139],v[172:175]
ds_read_u16 v148,v148
v_exp_f16 v120,v121
v_mfma_f32_16x16x32_bf16 v[180:183],v[96:99],v[136:139],v[180:183]
ds_read_b64_tr_b16 v[78:79],v5 offset:18976
ds_read_b64_tr_b16 v[80:81],v4 offset:21024
v_lshlrev_b32_sdwa v152,1,v152 dst_sel:DWORD dst_unused:UNUSED_PAD src0_sel:DWORD src1_sel:WORD_1
v_mfma_f32_16x16x32_bf16 v[188:191],v[100:103],v[136:139],v[188:191]
ds_read_u16_d16_hi v150,v150
v_exp_f16_sdwa v120,v121 dst_sel:WORD_1 dst_unused:UNUSED_PRESERVE src0_sel:WORD_1
v_mfma_f32_16x16x32_bf16 v[196:199],v[104:107],v[136:139],v[196:199]
ds_read_u16 v149,v149
v_lshlrev_b32_sdwa v154,1,v154 dst_sel:DWORD dst_unused:UNUSED_PAD src0_sel:DWORD src1_sel:WORD_1
v_mfma_f32_16x16x32_bf16 v[204:207],v[108:111],v[136:139],v[204:207]
s_waitcnt lgkmcnt(15)
ds_read_b64_tr_b16 v[82:83],v4 offset:23072
v_pk_fma_f16 v120,v120,s55,s55
v_lshlrev_b32_sdwa v153,1,v153 dst_sel:DWORD dst_unused:UNUSED_PAD src0_sel:DWORD src1_sel:WORD_1
v_mfma_f32_16x16x32_bf16 v[128:131],v[64:67],v[16:19],v[112:115]
ds_read_u16_d16_hi v151,v151
v_rcp_f16 v121,v120
v_mfma_f32_16x16x32_bf16 v[136:139],v[64:67],v[20:23],v[112:115]
s_waitcnt lgkmcnt(11)
ds_read_b64_tr_b16 v[84:85],v5 offset:21024
v_lshlrev_b32_sdwa v155,1,v155 dst_sel:DWORD dst_unused:UNUSED_PAD src0_sel:DWORD src1_sel:WORD_1
v_mfma_f32_16x16x32_bf16 v[140:143],v[68:71],v[20:23],v[116:119]
ds_read_u16 v152,v152
v_rcp_f16_sdwa v121,v120 dst_sel:WORD_1 dst_unused:UNUSED_PRESERVE src0_sel:WORD_1
v_mfma_f32_16x16x32_bf16 v[132:135],v[68:71],v[16:19],v[116:119]
s_waitcnt lgkmcnt(10)
ds_read_u16_d16_hi v154,v154
v_lshlrev_b32_sdwa v156,1,v156 dst_sel:DWORD dst_unused:UNUSED_PAD src0_sel:DWORD src1_sel:WORD_1
v_mfma_f32_16x16x32_bf16 v[128:131],v[72:75],v[24:27],v[128:131]
ds_read_b64_tr_b16 v[86:87],v5 offset:23072
v_fma_mix_f32 v157,v157,v121,0 op_sel_hi:[0,1,0]
v_lshlrev_b32_sdwa v158,1,v158 dst_sel:DWORD dst_unused:UNUSED_PAD src0_sel:DWORD src1_sel:WORD_1
v_mfma_f32_16x16x32_bf16 v[136:139],v[72:75],v[28:31],v[136:139]
s_waitcnt lgkmcnt(9)
ds_read_u16 v153,v153
v_fma_mix_f32 v159,v159,v121,0 op_sel:[0,1,0] op_sel_hi:[0,1,0]
v_mfma_f32_16x16x32_bf16 v[140:143],v[76:79],v[28:31],v[140:143]
v_mfma_f32_16x16x32_bf16 v[132:135],v[76:79],v[24:27],v[132:135]
s_waitcnt lgkmcnt(6)
ds_read_u16_d16_hi v155,v155
v_mfma_f32_16x16x32_bf16 v[128:131],v[80:83],v[32:35],v[128:131]
v_mfma_f32_16x16x32_bf16 v[136:139],v[80:83],v[36:39],v[136:139]
s_waitcnt lgkmcnt(2)
ds_read_u16 v156,v156
v_lshl_add_u64 v[144:145],v[144:145],0,v[146:147]
v_lshl_add_u64 v[146:147],v[148:149],0,v[150:151]
v_mfma_f32_16x16x32_bf16 v[140:143],v[84:87],v[36:39],v[140:143]
ds_read_u16_d16_hi v158,v158
v_mfma_f32_16x16x32_bf16 v[132:135],v[84:87],v[32:35],v[132:135]
v_lshlrev_b32_sdwa v128,1,v128 dst_sel:DWORD dst_unused:UNUSED_PAD src0_sel:DWORD src1_sel:WORD_1
v_lshlrev_b32_sdwa v130,1,v130 dst_sel:DWORD dst_unused:UNUSED_PAD src0_sel:DWORD src1_sel:WORD_1
v_mfma_f32_16x16x32_bf16 v[208:211],v[88:91],v[144:147],v[208:211]
s_barrier
s_setprio 3
ds_read_u16 v128,v128
v_lshlrev_b32_sdwa v129,1,v129 dst_sel:DWORD dst_unused:UNUSED_PAD src0_sel:DWORD src1_sel:WORD_1
v_lshlrev_b32_sdwa v131,1,v131 dst_sel:DWORD dst_unused:UNUSED_PAD src0_sel:DWORD src1_sel:WORD_1
v_mfma_f32_16x16x32_bf16 v[216:219],v[92:95],v[144:147],v[216:219]
s_cbranch_vccnz RA1_%=
s_mov_b64 exec,s[62:63]
s_add_u32 m0,s57,48832
s_add_u32 s64,s56,3342336
buffer_load_dwordx4 v2,s[36:39],s64 offen lds
s_add_u32 m0,s57,50560
s_nop 0
buffer_load_dwordx4 v3,s[36:39],s64 offen lds
s_mov_b64 exec,-1
RA1_%=:
v_fma_mixlo_f16 v120,v140,s99,0
v_fma_mixlo_f16 v122,v141,s99,0
v_mfma_f32_16x16x32_bf16 v[224:227],v[96:99],v[144:147],v[224:227]
ds_read_u16_d16_hi v130,v130
v_lshlrev_b32_sdwa v132,1,v132 dst_sel:DWORD dst_unused:UNUSED_PAD src0_sel:DWORD src1_sel:WORD_1
v_lshlrev_b32_sdwa v134,1,v134 dst_sel:DWORD dst_unused:UNUSED_PAD src0_sel:DWORD src1_sel:WORD_1
v_mfma_f32_16x16x32_bf16 v[232:235],v[100:103],v[144:147],v[232:235]
s_waitcnt lgkmcnt(4)
v_lshl_add_u64 v[152:153],v[152:153],0,v[154:155]
v_mfma_f32_16x16x32_bf16 v[240:243],v[104:107],v[144:147],v[240:243]
s_waitcnt lgkmcnt(2)
ds_read_u16 v129,v129
v_or_b32 v154,v158,v156
v_cvt_pk_bf16_f32 v155,v157,v159
v_mfma_f32_16x16x32_bf16 v[248:251],v[108:111],v[144:147],v[248:251]
v_fma_mixhi_f16 v120,v142,s99,0
v_fma_mixhi_f16 v122,v143,s99,0
v_mfma_f32_16x16x32_bf16 v[212:215],v[88:91],v[152:155],v[212:215]
ds_read_u16_d16_hi v131,v131
v_lshlrev_b32_sdwa v133,1,v133 dst_sel:DWORD dst_unused:UNUSED_PAD src0_sel:DWORD src1_sel:WORD_1
v_lshlrev_b32_sdwa v135,1,v135 dst_sel:DWORD dst_unused:UNUSED_PAD src0_sel:DWORD src1_sel:WORD_1
v_mfma_f32_16x16x32_bf16 v[220:223],v[92:95],v[152:155],v[220:223]
ds_read_b64_tr_b16 v[88:89],v6 offset:0
v_pk_fma_f16 v121,v120,v120,s54
v_pk_fma_f16 v123,v122,v122,s54
v_mfma_f32_16x16x32_bf16 v[228:231],v[96:99],v[152:155],v[228:231]
s_cbranch_vccnz RA2_%=
s_mov_b64 exec,s[62:63]
s_add_u32 m0,s57,52320
s_add_u32 s64,s56,3440640
buffer_load_dwordx4 v2,s[36:39],s64 offen lds
s_add_u32 m0,s57,54048
s_nop 0
buffer_load_dwordx4 v3,s[36:39],s64 offen lds
s_mov_b64 exec,-1
RA2_%=:
ds_read_u16 v132,v132
v_pk_mul_f16 v121,v120,v121
v_pk_mul_f16 v123,v122,v123
v_mfma_f32_16x16x32_bf16 v[236:239],v[100:103],v[152:155],v[236:239]
ds_read_b64_tr_b16 v[90:91],v6 offset:1536
v_lshlrev_b32_sdwa v136,1,v136 dst_sel:DWORD dst_unused:UNUSED_PAD src0_sel:DWORD src1_sel:WORD_1
v_lshlrev_b32_sdwa v138,1,v138 dst_sel:DWORD dst_unused:UNUSED_PAD src0_sel:DWORD src1_sel:WORD_1
v_mfma_f32_16x16x32_bf16 v[244:247],v[104:107],v[152:155],v[244:247]
ds_read_u16_d16_hi v134,v134
v_exp_f16 v120,v121
v_mfma_f32_16x16x32_bf16 v[252:255],v[108:111],v[152:155],v[252:255]
ds_read_b64_tr_b16 v[92:93],v6 offset:8
v_exp_f16 v122,v123
v_mfma_f32_16x16x32_bf16 v[144:147],v[64:67],v[40:43],v[112:115]
ds_read_u16 v133,v133
v_lshlrev_b32_sdwa v137,1,v137 dst_sel:DWORD dst_unused:UNUSED_PAD src0_sel:DWORD src1_sel:WORD_1
v_lshlrev_b32_sdwa v139,1,v139 dst_sel:DWORD dst_unused:UNUSED_PAD src0_sel:DWORD src1_sel:WORD_1
v_mfma_f32_16x16x32_bf16 v[152:155],v[64:67],v[44:47],v[112:115]
ds_read_b64_tr_b16 v[94:95],v6 offset:1544
v_exp_f16_sdwa v120,v121 dst_sel:WORD_1 dst_unused:UNUSED_PRESERVE src0_sel:WORD_1
v_mfma_f32_16x16x32_bf16 v[156:159],v[68:71],v[44:47],v[116:119]
ds_read_u16_d16_hi v135,v135
v_exp_f16_sdwa v122,v123 dst_sel:WORD_1 dst_unused:UNUSED_PRESERVE src0_sel:WORD_1
v_mfma_f32_16x16x32_bf16 v[148:151],v[68:71],v[40:43],v[116:119]
ds_read_b64_tr_b16 v[96:97],v6 offset:64
v_pk_fma_f16 v120,v120,s55,s55
v_pk_fma_f16 v122,v122,s55,s55
v_mfma_f32_16x16x32_bf16 v[144:147],v[72:75],v[48:51],v[144:147]
ds_read_u16 v136,v136
v_rcp_f16 v121,v120
v_mfma_f32_16x16x32_bf16 v[152:155],v[72:75],v[52:55],v[152:155]
ds_read_b64_tr_b16 v[98:99],v6 offset:1600
ds_read_b64_tr_b16 v[100:101],v6 offset:72
v_rcp_f16 v123,v122
v_mfma_f32_16x16x32_bf16 v[156:159],v[76:79],v[52:55],v[156:159]
ds_read_u16_d16_hi v138,v138
v_rcp_f16_sdwa v121,v120 dst_sel:WORD_1 dst_unused:UNUSED_PRESERVE src0_sel:WORD_1
v_mfma_f32_16x16x32_bf16 v[148:151],v[76:79],v[48:51],v[148:151]
ds_read_b64_tr_b16 v[102:103],v6 offset:1608
ds_read_b64_tr_b16 v[104:105],v6 offset:128
v_rcp_f16_sdwa v123,v122 dst_sel:WORD_1 dst_unused:UNUSED_PRESERVE src0_sel:WORD_1
v_mfma_f32_16x16x32_bf16 v[144:147],v[80:83],v[56:59],v[144:147]
ds_read_u16 v137,v137
v_fma_mix_f32 v140,v140,v121,0 op_sel_hi:[0,1,0]
v_fma_mix_f32 v142,v142,v121,0 op_sel:[0,1,0] op_sel_hi:[0,1,0]
v_mfma_f32_16x16x32_bf16 v[152:155],v[80:83],v[60:63],v[152:155]
s_waitcnt lgkmcnt(8)
ds_read_b64_tr_b16 v[106:107],v6 offset:1664
ds_read_b64_tr_b16 v[108:109],v6 offset:136
v_lshl_add_u64 v[128:129],v[128:129],0,v[130:131]
v_lshl_add_u64 v[130:131],v[132:133],0,v[134:135]
v_mfma_f32_16x16x32_bf16 v[156:159],v[84:87],v[60:63],v[156:159]
ds_read_u16_d16_hi v139,v139
v_fma_mix_f32 v141,v141,v123,0 op_sel_hi:[0,1,0]
v_fma_mix_f32 v143,v143,v123,0 op_sel:[0,1,0] op_sel_hi:[0,1,0]
v_mfma_f32_16x16x32_bf16 v[148:151],v[84:87],v[56:59],v[148:151]
ds_read_b64_tr_b16 v[110:111],v6 offset:1672
ds_read_b64_tr_b16 v[64:65],v4 offset:0
v_lshlrev_b32_sdwa v144,1,v144 dst_sel:DWORD dst_unused:UNUSED_PAD src0_sel:DWORD src1_sel:WORD_1
v_lshlrev_b32_sdwa v146,1,v146 dst_sel:DWORD dst_unused:UNUSED_PAD src0_sel:DWORD src1_sel:WORD_1
v_mfma_f32_16x16x32_bf16 v[160:163],v[88:91],v[128:131],v[160:163]
ds_read_u16 v144,v144
v_lshlrev_b32_sdwa v145,1,v145 dst_sel:DWORD dst_unused:UNUSED_PAD src0_sel:DWORD src1_sel:WORD_1
v_lshlrev_b32_sdwa v147,1,v147 dst_sel:DWORD dst_unused:UNUSED_PAD src0_sel:DWORD src1_sel:WORD_1
v_mfma_f32_16x16x32_bf16 v[168:171],v[92:95],v[128:131],v[168:171]
s_waitcnt lgkmcnt(11)
ds_read_b64_tr_b16 v[66:67],v4 offset:2048
ds_read_b128 v[112:115],v7 offset:768
v_fma_mixlo_f16 v120,v156,s99,0
v_fma_mixlo_f16 v122,v157,s99,0
v_mfma_f32_16x16x32_bf16 v[176:179],v[96:99],v[128:131],v[176:179]
s_waitcnt lgkmcnt(10)
ds_read_u16_d16_hi v146,v146
v_lshlrev_b32_sdwa v148,1,v148 dst_sel:DWORD dst_unused:UNUSED_PAD src0_sel:DWORD src1_sel:WORD_1
v_lshlrev_b32_sdwa v150,1,v150 dst_sel:DWORD dst_unused:UNUSED_PAD src0_sel:DWORD src1_sel:WORD_1
v_mfma_f32_16x16x32_bf16 v[184:187],v[100:103],v[128:131],v[184:187]
s_waitcnt lgkmcnt(6)
ds_read_b64_tr_b16 v[68:69],v5 offset:0
ds_read_b64_tr_b16 v[70:71],v5 offset:2048
v_lshl_add_u64 v[136:137],v[136:137],0,v[138:139]
v_mfma_f32_16x16x32_bf16 v[192:195],v[104:107],v[128:131],v[192:195]
s_waitcnt lgkmcnt(7)
ds_read_u16 v145,v145
v_cvt_pk_bf16_f32 v138,v140,v142
v_cvt_pk_bf16_f32 v139,v141,v143
v_mfma_f32_16x16x32_bf16 v[200:203],v[108:111],v[128:131],v[200:203]
ds_read_b128 v[116:119],v7 offset:800
ds_read_b64_tr_b16 v[72:73],v4 offset:4096
v_fma_mixhi_f16 v120,v158,s99,0
v_fma_mixhi_f16 v122,v159,s99,0
v_mfma_f32_16x16x32_bf16 v[164:167],v[88:91],v[136:139],v[164:167]
ds_read_u16_d16_hi v147,v147
v_lshlrev_b32_sdwa v149,1,v149 dst_sel:DWORD dst_unused:UNUSED_PAD src0_sel:DWORD src1_sel:WORD_1
v_lshlrev_b32_sdwa v151,1,v151 dst_sel:DWORD dst_unused:UNUSED_PAD src0_sel:DWORD src1_sel:WORD_1
v_mfma_f32_16x16x32_bf16 v[172:175],v[92:95],v[136:139],v[172:175]
ds_read_b64_tr_b16 v[74:75],v4 offset:6144
ds_read_b64_tr_b16 v[76:77],v5 offset:4096
v_pk_fma_f16 v121,v120,v120,s54
v_pk_fma_f16 v123,v122,v122,s54
v_mfma_f32_16x16x32_bf16 v[180:183],v[96:99],v[136:139],v[180:183]
ds_read_u16 v148,v148
v_pk_mul_f16 v121,v120,v121
v_pk_mul_f16 v123,v122,v123
v_mfma_f32_16x16x32_bf16 v[188:191],v[100:103],v[136:139],v[188:191]
ds_read_b64_tr_b16 v[78:79],v5 offset:6144
v_lshlrev_b32_sdwa v152,1,v152 dst_sel:DWORD dst_unused:UNUSED_PAD src0_sel:DWORD src1_sel:WORD_1
v_lshlrev_b32_sdwa v154,1,v154 dst_sel:DWORD dst_unused:UNUSED_PAD src0_sel:DWORD src1_sel:WORD_1
v_mfma_f32_16x16x32_bf16 v[196:199],v[104:107],v[136:139],v[196:199]
ds_read_u16_d16_hi v150,v150
v_exp_f16 v120,v121
v_mfma_f32_16x16x32_bf16 v[204:207],v[108:111],v[136:139],v[204:207]
s_waitcnt lgkmcnt(12)
ds_read_b64_tr_b16 v[80:81],v4 offset:8192
v_exp_f16 v122,v123
v_mfma_f32_16x16x32_bf16 v[128:131],v[64:67],v[16:19],v[112:115]
ds_read_u16 v149,v149
v_lshlrev_b32_sdwa v153,1,v153 dst_sel:DWORD dst_unused:UNUSED_PAD src0_sel:DWORD src1_sel:WORD_1
v_lshlrev_b32_sdwa v155,1,v155 dst_sel:DWORD dst_unused:UNUSED_PAD src0_sel:DWORD src1_sel:WORD_1
v_mfma_f32_16x16x32_bf16 v[136:139],v[64:67],v[20:23],v[112:115]
s_waitcnt lgkmcnt(9)
ds_read_b64_tr_b16 v[82:83],v4 offset:10240
v_exp_f16_sdwa v120,v121 dst_sel:WORD_1 dst_unused:UNUSED_PRESERVE src0_sel:WORD_1
v_mfma_f32_16x16x32_bf16 v[140:143],v[68:71],v[20:23],v[116:119]
ds_read_u16_d16_hi v151,v151
v_exp_f16_sdwa v122,v123 dst_sel:WORD_1 dst_unused:UNUSED_PRESERVE src0_sel:WORD_1
v_mfma_f32_16x16x32_bf16 v[132:135],v[68:71],v[16:19],v[116:119]
s_waitcnt lgkmcnt(8)
s_mov_b32 s86,0xbe801d5a
ds_read_b64_tr_b16 v[84:85],v5 offset:8192
v_pk_fma_f16 v120,v120,s55,s55
v_pk_fma_f16 v122,v122,s55,s55
v_mfma_f32_16x16x32_bf16 v[128:131],v[72:75],v[24:27],v[128:131]
ds_read_u16 v152,v152
v_rcp_f16 v121,v120
v_mfma_f32_16x16x32_bf16 v[136:139],v[72:75],v[28:31],v[136:139]
s_waitcnt lgkmcnt(7)
ds_read_b64_tr_b16 v[86:87],v5 offset:10240
v_rcp_f16 v123,v122
v_mfma_f32_16x16x32_bf16 v[140:143],v[76:79],v[28:31],v[140:143]
ds_read_u16_d16_hi v154,v154
v_rcp_f16_sdwa v121,v120 dst_sel:WORD_1 dst_unused:UNUSED_PRESERVE src0_sel:WORD_1
v_mfma_f32_16x16x32_bf16 v[132:135],v[76:79],v[24:27],v[132:135]
s_waitcnt lgkmcnt(5)
v_rcp_f16_sdwa v123,v122 dst_sel:WORD_1 dst_unused:UNUSED_PRESERVE src0_sel:WORD_1
v_mfma_f32_16x16x32_bf16 v[128:131],v[80:83],v[32:35],v[128:131]
ds_read_u16 v153,v153
v_fma_mix_f32 v156,v156,v121,0 op_sel_hi:[0,1,0]
v_fma_mix_f32 v158,v158,v121,0 op_sel:[0,1,0] op_sel_hi:[0,1,0]
v_mfma_f32_16x16x32_bf16 v[136:139],v[80:83],v[36:39],v[136:139]
s_waitcnt lgkmcnt(2)
v_lshl_add_u64 v[144:145],v[144:145],0,v[146:147]
v_lshl_add_u64 v[146:147],v[148:149],0,v[150:151]
v_mfma_f32_16x16x32_bf16 v[140:143],v[84:87],v[36:39],v[140:143]
ds_read_u16_d16_hi v155,v155
v_fma_mix_f32 v157,v157,v123,0 op_sel_hi:[0,1,0]
v_fma_mix_f32 v159,v159,v123,0 op_sel:[0,1,0] op_sel_hi:[0,1,0]
v_mfma_f32_16x16x32_bf16 v[132:135],v[84:87],v[32:35],v[132:135]
v_lshlrev_b32_sdwa v128,1,v128 dst_sel:DWORD dst_unused:UNUSED_PAD src0_sel:DWORD src1_sel:WORD_1
v_lshlrev_b32_sdwa v130,1,v130 dst_sel:DWORD dst_unused:UNUSED_PAD src0_sel:DWORD src1_sel:WORD_1
v_mfma_f32_16x16x32_bf16 v[208:211],v[88:91],v[144:147],v[208:211]
ds_read_u16 v128,v128
v_lshlrev_b32_sdwa v129,1,v129 dst_sel:DWORD dst_unused:UNUSED_PAD src0_sel:DWORD src1_sel:WORD_1
v_lshlrev_b32_sdwa v131,1,v131 dst_sel:DWORD dst_unused:UNUSED_PAD src0_sel:DWORD src1_sel:WORD_1
v_mfma_f32_16x16x32_bf16 v[216:219],v[92:95],v[144:147],v[216:219]
v_fma_mixlo_f16 v120,v140,s99,0
v_fma_mixlo_f16 v122,v141,s99,0
v_mfma_f32_16x16x32_bf16 v[224:227],v[96:99],v[144:147],v[224:227]
ds_read_u16_d16_hi v130,v130
v_lshlrev_b32_sdwa v132,1,v132 dst_sel:DWORD dst_unused:UNUSED_PAD src0_sel:DWORD src1_sel:WORD_1
v_lshlrev_b32_sdwa v134,1,v134 dst_sel:DWORD dst_unused:UNUSED_PAD src0_sel:DWORD src1_sel:WORD_1
v_mfma_f32_16x16x32_bf16 v[232:235],v[100:103],v[144:147],v[232:235]
s_waitcnt lgkmcnt(2)
v_lshl_add_u64 v[152:153],v[152:153],0,v[154:155]
v_mfma_f32_16x16x32_bf16 v[240:243],v[104:107],v[144:147],v[240:243]
ds_read_u16 v129,v129
v_cvt_pk_bf16_f32 v154,v156,v158
v_cvt_pk_bf16_f32 v155,v157,v159
v_mfma_f32_16x16x32_bf16 v[248:251],v[108:111],v[144:147],v[248:251]
s_setprio 2
v_fma_mixhi_f16 v120,v142,s99,0
v_fma_mixhi_f16 v122,v143,s99,0
v_mfma_f32_16x16x32_bf16 v[212:215],v[88:91],v[152:155],v[212:215]
ds_read_u16_d16_hi v131,v131
v_lshlrev_b32_sdwa v133,1,v133 dst_sel:DWORD dst_unused:UNUSED_PAD src0_sel:DWORD src1_sel:WORD_1
v_lshlrev_b32_sdwa v135,1,v135 dst_sel:DWORD dst_unused:UNUSED_PAD src0_sel:DWORD src1_sel:WORD_1
v_mfma_f32_16x16x32_bf16 v[220:223],v[92:95],v[152:155],v[220:223]
ds_read_b64_tr_b16 v[88:89],v6 offset:6160
v_pk_fma_f16 v121,v120,v120,s54
v_pk_fma_f16 v123,v122,v122,s54
v_mfma_f32_16x16x32_bf16 v[228:231],v[96:99],v[152:155],v[228:231]
ds_read_u16 v132,v132
v_pk_mul_f16 v121,v120,v121
v_pk_mul_f16 v123,v122,v123
v_mfma_f32_16x16x32_bf16 v[236:239],v[100:103],v[152:155],v[236:239]
ds_read_b64_tr_b16 v[90:91],v6 offset:7696
v_lshlrev_b32_sdwa v136,1,v136 dst_sel:DWORD dst_unused:UNUSED_PAD src0_sel:DWORD src1_sel:WORD_1
v_lshlrev_b32_sdwa v138,1,v138 dst_sel:DWORD dst_unused:UNUSED_PAD src0_sel:DWORD src1_sel:WORD_1
v_mfma_f32_16x16x32_bf16 v[244:247],v[104:107],v[152:155],v[244:247]
ds_read_u16_d16_hi v134,v134
v_exp_f16 v120,v121
v_mfma_f32_16x16x32_bf16 v[252:255],v[108:111],v[152:155],v[252:255]
ds_read_b64_tr_b16 v[92:93],v6 offset:6168
v_exp_f16 v122,v123
v_mfma_f32_16x16x32_bf16 v[144:147],v[64:67],v[40:43],v[112:115]
ds_read_u16 v133,v133
v_lshlrev_b32_sdwa v137,1,v137 dst_sel:DWORD dst_unused:UNUSED_PAD src0_sel:DWORD src1_sel:WORD_1
v_lshlrev_b32_sdwa v139,1,v139 dst_sel:DWORD dst_unused:UNUSED_PAD src0_sel:DWORD src1_sel:WORD_1
v_mfma_f32_16x16x32_bf16 v[152:155],v[64:67],v[44:47],v[112:115]
ds_read_b64_tr_b16 v[94:95],v6 offset:7704
v_exp_f16_sdwa v120,v121 dst_sel:WORD_1 dst_unused:UNUSED_PRESERVE src0_sel:WORD_1
v_mfma_f32_16x16x32_bf16 v[156:159],v[68:71],v[44:47],v[116:119]
ds_read_u16_d16_hi v135,v135
v_exp_f16_sdwa v122,v123 dst_sel:WORD_1 dst_unused:UNUSED_PRESERVE src0_sel:WORD_1
v_mfma_f32_16x16x32_bf16 v[148:151],v[68:71],v[40:43],v[116:119]
ds_read_b64_tr_b16 v[96:97],v6 offset:6224
v_pk_fma_f16 v120,v120,s55,s55
v_pk_fma_f16 v122,v122,s55,s55
v_mfma_f32_16x16x32_bf16 v[144:147],v[72:75],v[48:51],v[144:147]
ds_read_u16 v136,v136
v_rcp_f16 v121,v120
v_mfma_f32_16x16x32_bf16 v[152:155],v[72:75],v[52:55],v[152:155]
ds_read_b64_tr_b16 v[98:99],v6 offset:7760
ds_read_b64_tr_b16 v[100:101],v6 offset:6232
v_rcp_f16 v123,v122
v_mfma_f32_16x16x32_bf16 v[156:159],v[76:79],v[52:55],v[156:159]
ds_read_u16_d16_hi v138,v138
v_rcp_f16_sdwa v121,v120 dst_sel:WORD_1 dst_unused:UNUSED_PRESERVE src0_sel:WORD_1
v_mfma_f32_16x16x32_bf16 v[148:151],v[76:79],v[48:51],v[148:151]
ds_read_b64_tr_b16 v[102:103],v6 offset:7768
ds_read_b64_tr_b16 v[104:105],v6 offset:6288
v_rcp_f16_sdwa v123,v122 dst_sel:WORD_1 dst_unused:UNUSED_PRESERVE src0_sel:WORD_1
v_mfma_f32_16x16x32_bf16 v[144:147],v[80:83],v[56:59],v[144:147]
ds_read_u16 v137,v137
v_fma_mix_f32 v140,v140,v121,0 op_sel_hi:[0,1,0]
v_fma_mix_f32 v142,v142,v121,0 op_sel:[0,1,0] op_sel_hi:[0,1,0]
v_mfma_f32_16x16x32_bf16 v[152:155],v[80:83],v[60:63],v[152:155]
s_waitcnt lgkmcnt(8)
ds_read_b64_tr_b16 v[106:107],v6 offset:7824
ds_read_b64_tr_b16 v[108:109],v6 offset:6296
v_lshl_add_u64 v[128:129],v[128:129],0,v[130:131]
v_lshl_add_u64 v[130:131],v[132:133],0,v[134:135]
v_mfma_f32_16x16x32_bf16 v[156:159],v[84:87],v[60:63],v[156:159]
ds_read_u16_d16_hi v139,v139
v_fma_mix_f32 v141,v141,v123,0 op_sel_hi:[0,1,0]
v_fma_mix_f32 v143,v143,v123,0 op_sel:[0,1,0] op_sel_hi:[0,1,0]
v_mfma_f32_16x16x32_bf16 v[148:151],v[84:87],v[56:59],v[148:151]
ds_read_b64_tr_b16 v[110:111],v6 offset:7832
ds_read_b64_tr_b16 v[64:65],v4 offset:512
v_lshlrev_b32_sdwa v144,1,v144 dst_sel:DWORD dst_unused:UNUSED_PAD src0_sel:DWORD src1_sel:WORD_1
v_lshlrev_b32_sdwa v146,1,v146 dst_sel:DWORD dst_unused:UNUSED_PAD src0_sel:DWORD src1_sel:WORD_1
v_mfma_f32_16x16x32_bf16 v[160:163],v[88:91],v[128:131],v[160:163]
ds_read_u16 v144,v144
v_lshlrev_b32_sdwa v145,1,v145 dst_sel:DWORD dst_unused:UNUSED_PAD src0_sel:DWORD src1_sel:WORD_1
v_lshlrev_b32_sdwa v147,1,v147 dst_sel:DWORD dst_unused:UNUSED_PAD src0_sel:DWORD src1_sel:WORD_1
v_mfma_f32_16x16x32_bf16 v[168:171],v[92:95],v[128:131],v[168:171]
s_waitcnt lgkmcnt(11)
ds_read_b64_tr_b16 v[66:67],v4 offset:2560
ds_read_b128 v[112:115],v7 offset:896
v_fma_mixlo_f16 v120,v156,s99,0
v_fma_mixlo_f16 v122,v157,s99,0
v_mfma_f32_16x16x32_bf16 v[176:179],v[96:99],v[128:131],v[176:179]
s_waitcnt lgkmcnt(10)
ds_read_u16_d16_hi v146,v146
v_lshlrev_b32_sdwa v148,1,v148 dst_sel:DWORD dst_unused:UNUSED_PAD src0_sel:DWORD src1_sel:WORD_1
v_lshlrev_b32_sdwa v150,1,v150 dst_sel:DWORD dst_unused:UNUSED_PAD src0_sel:DWORD src1_sel:WORD_1
v_mfma_f32_16x16x32_bf16 v[184:187],v[100:103],v[128:131],v[184:187]
s_waitcnt lgkmcnt(6)
ds_read_b64_tr_b16 v[68:69],v5 offset:512
ds_read_b64_tr_b16 v[70:71],v5 offset:2560
v_lshl_add_u64 v[136:137],v[136:137],0,v[138:139]
v_mfma_f32_16x16x32_bf16 v[192:195],v[104:107],v[128:131],v[192:195]
s_waitcnt lgkmcnt(7)
ds_read_u16 v145,v145
v_cvt_pk_bf16_f32 v138,v140,v142
v_cvt_pk_bf16_f32 v139,v141,v143
v_mfma_f32_16x16x32_bf16 v[200:203],v[108:111],v[128:131],v[200:203]
ds_read_b128 v[116:119],v7 offset:928
ds_read_b64_tr_b16 v[72:73],v4 offset:4608
v_fma_mixhi_f16 v120,v158,s99,0
v_fma_mixhi_f16 v122,v159,s99,0
v_mfma_f32_16x16x32_bf16 v[164:167],v[88:91],v[136:139],v[164:167]
ds_read_u16_d16_hi v147,v147
v_lshlrev_b32_sdwa v149,1,v149 dst_sel:DWORD dst_unused:UNUSED_PAD src0_sel:DWORD src1_sel:WORD_1
v_lshlrev_b32_sdwa v151,1,v151 dst_sel:DWORD dst_unused:UNUSED_PAD src0_sel:DWORD src1_sel:WORD_1
v_mfma_f32_16x16x32_bf16 v[172:175],v[92:95],v[136:139],v[172:175]
s_setprio 1
ds_read_b64_tr_b16 v[74:75],v4 offset:6656
ds_read_b64_tr_b16 v[76:77],v5 offset:4608
v_pk_fma_f16 v121,v120,v120,s54
v_pk_fma_f16 v123,v122,v122,s54
v_mfma_f32_16x16x32_bf16 v[180:183],v[96:99],v[136:139],v[180:183]
ds_read_u16 v148,v148
v_pk_mul_f16 v121,v120,v121
v_pk_mul_f16 v123,v122,v123
v_mfma_f32_16x16x32_bf16 v[188:191],v[100:103],v[136:139],v[188:191]
ds_read_b64_tr_b16 v[78:79],v5 offset:6656
v_lshlrev_b32_sdwa v152,1,v152 dst_sel:DWORD dst_unused:UNUSED_PAD src0_sel:DWORD src1_sel:WORD_1
v_lshlrev_b32_sdwa v154,1,v154 dst_sel:DWORD dst_unused:UNUSED_PAD src0_sel:DWORD src1_sel:WORD_1
v_mfma_f32_16x16x32_bf16 v[196:199],v[104:107],v[136:139],v[196:199]
ds_read_u16_d16_hi v150,v150
v_exp_f16 v120,v121
v_mfma_f32_16x16x32_bf16 v[204:207],v[108:111],v[136:139],v[204:207]
s_waitcnt lgkmcnt(12)
ds_read_b64_tr_b16 v[80:81],v4 offset:8704
v_exp_f16 v122,v123
v_mfma_f32_16x16x32_bf16 v[128:131],v[64:67],v[16:19],v[112:115]
ds_read_u16 v149,v149
v_lshlrev_b32_sdwa v153,1,v153 dst_sel:DWORD dst_unused:UNUSED_PAD src0_sel:DWORD src1_sel:WORD_1
v_lshlrev_b32_sdwa v155,1,v155 dst_sel:DWORD dst_unused:UNUSED_PAD src0_sel:DWORD src1_sel:WORD_1
v_mfma_f32_16x16x32_bf16 v[136:139],v[64:67],v[20:23],v[112:115]
s_waitcnt lgkmcnt(9)
ds_read_b64_tr_b16 v[82:83],v4 offset:10752
v_exp_f16_sdwa v120,v121 dst_sel:WORD_1 dst_unused:UNUSED_PRESERVE src0_sel:WORD_1
v_mfma_f32_16x16x32_bf16 v[140:143],v[68:71],v[20:23],v[116:119]
ds_read_u16_d16_hi v151,v151
v_exp_f16_sdwa v122,v123 dst_sel:WORD_1 dst_unused:UNUSED_PRESERVE src0_sel:WORD_1
v_mfma_f32_16x16x32_bf16 v[132:135],v[68:71],v[16:19],v[116:119]
s_waitcnt lgkmcnt(8)
ds_read_b64_tr_b16 v[84:85],v5 offset:8704
v_pk_fma_f16 v120,v120,s55,s55
v_pk_fma_f16 v122,v122,s55,s55
v_mfma_f32_16x16x32_bf16 v[128:131],v[72:75],v[24:27],v[128:131]
ds_read_u16 v152,v152
v_rcp_f16 v121,v120
v_mfma_f32_16x16x32_bf16 v[136:139],v[72:75],v[28:31],v[136:139]
s_waitcnt lgkmcnt(7)
ds_read_b64_tr_b16 v[86:87],v5 offset:10752
v_rcp_f16 v123,v122
v_mfma_f32_16x16x32_bf16 v[140:143],v[76:79],v[28:31],v[140:143]
ds_read_u16_d16_hi v154,v154
v_rcp_f16_sdwa v121,v120 dst_sel:WORD_1 dst_unused:UNUSED_PRESERVE src0_sel:WORD_1
v_mfma_f32_16x16x32_bf16 v[132:135],v[76:79],v[24:27],v[132:135]
s_waitcnt lgkmcnt(5)
v_rcp_f16_sdwa v123,v122 dst_sel:WORD_1 dst_unused:UNUSED_PRESERVE src0_sel:WORD_1
v_mfma_f32_16x16x32_bf16 v[128:131],v[80:83],v[32:35],v[128:131]
ds_read_u16 v153,v153
v_fma_mix_f32 v156,v156,v121,0 op_sel_hi:[0,1,0]
v_fma_mix_f32 v158,v158,v121,0 op_sel:[0,1,0] op_sel_hi:[0,1,0]
v_mfma_f32_16x16x32_bf16 v[136:139],v[80:83],v[36:39],v[136:139]
s_waitcnt lgkmcnt(2)
s_setprio 0
v_lshl_add_u64 v[144:145],v[144:145],0,v[146:147]
v_lshl_add_u64 v[146:147],v[148:149],0,v[150:151]
v_mfma_f32_16x16x32_bf16 v[140:143],v[84:87],v[36:39],v[140:143]
ds_read_u16_d16_hi v155,v155
v_fma_mix_f32 v157,v157,v123,0 op_sel_hi:[0,1,0]
v_fma_mix_f32 v159,v159,v123,0 op_sel:[0,1,0] op_sel_hi:[0,1,0]
v_mfma_f32_16x16x32_bf16 v[132:135],v[84:87],v[32:35],v[132:135]
v_lshlrev_b32_sdwa v128,1,v128 dst_sel:DWORD dst_unused:UNUSED_PAD src0_sel:DWORD src1_sel:WORD_1
v_lshlrev_b32_sdwa v130,1,v130 dst_sel:DWORD dst_unused:UNUSED_PAD src0_sel:DWORD src1_sel:WORD_1
v_mfma_f32_16x16x32_bf16 v[208:211],v[88:91],v[144:147],v[208:211]
s_barrier
s_setprio 2
ds_read_u16 v128,v128
v_lshlrev_b32_sdwa v129,1,v129 dst_sel:DWORD dst_unused:UNUSED_PAD src0_sel:DWORD src1_sel:WORD_1
v_mfma_f32_16x16x32_bf16 v[216:219],v[92:95],v[144:147],v[216:219]
s_cbranch_vccnz RA3_%=
s_mov_b64 exec,s[62:63]
s_add_u32 m0,s57,55808
s_add_u32 s64,s56,3538944
buffer_load_dwordx4 v2,s[36:39],s64 offen lds
s_add_u32 m0,s57,57536
s_nop 0
buffer_load_dwordx4 v3,s[36:39],s64 offen lds
s_mov_b64 exec,-1
RA3_%=:
ds_read_u16_d16_hi v130,v130
v_lshlrev_b32_sdwa v131,1,v131 dst_sel:DWORD dst_unused:UNUSED_PAD src0_sel:DWORD src1_sel:WORD_1
v_mfma_f32_16x16x32_bf16 v[224:227],v[96:99],v[144:147],v[224:227]
v_lshlrev_b32_sdwa v132,1,v132 dst_sel:DWORD dst_unused:UNUSED_PAD src0_sel:DWORD src1_sel:WORD_1
v_mfma_f32_16x16x32_bf16 v[232:235],v[100:103],v[144:147],v[232:235]
s_waitcnt lgkmcnt(2)
ds_read_u16 v129,v129
v_lshl_add_u64 v[152:153],v[152:153],0,v[154:155]
v_lshlrev_b32_sdwa v134,1,v134 dst_sel:DWORD dst_unused:UNUSED_PAD src0_sel:DWORD src1_sel:WORD_1
v_mfma_f32_16x16x32_bf16 v[240:243],v[104:107],v[144:147],v[240:243]
ds_read_u16_d16_hi v131,v131
v_cvt_pk_bf16_f32 v154,v156,v158
v_cvt_pk_bf16_f32 v155,v157,v159
v_mfma_f32_16x16x32_bf16 v[248:251],v[108:111],v[144:147],v[248:251]
v_lshlrev_b32_sdwa v133,1,v133 dst_sel:DWORD dst_unused:UNUSED_PAD src0_sel:DWORD src1_sel:WORD_1
v_mfma_f32_16x16x32_bf16 v[212:215],v[88:91],v[152:155],v[212:215]
ds_read_u16 v132,v132
v_lshlrev_b32_sdwa v135,1,v135 dst_sel:DWORD dst_unused:UNUSED_PAD src0_sel:DWORD src1_sel:WORD_1
v_mfma_f32_16x16x32_bf16 v[220:223],v[92:95],v[152:155],v[220:223]
ds_read_u16_d16_hi v134,v134
v_lshlrev_b32_sdwa v136,1,v136 dst_sel:DWORD dst_unused:UNUSED_PAD src0_sel:DWORD src1_sel:WORD_1
v_mfma_f32_16x16x32_bf16 v[228:231],v[96:99],v[152:155],v[228:231]
ds_read_b64_tr_b16 v[88:89],v6 offset:12320
ds_read_b64_tr_b16 v[90:91],v6 offset:13856
v_lshlrev_b32_sdwa v138,1,v138 dst_sel:DWORD dst_unused:UNUSED_PAD src0_sel:DWORD src1_sel:WORD_1
v_mfma_f32_16x16x32_bf16 v[236:239],v[100:103],v[152:155],v[236:239]
ds_read_u16 v133,v133
v_lshlrev_b32_sdwa v137,1,v137 dst_sel:DWORD dst_unused:UNUSED_PAD src0_sel:DWORD src1_sel:WORD_1
v_mfma_f32_16x16x32_bf16 v[244:247],v[104:107],v[152:155],v[244:247]
ds_read_u16_d16_hi v135,v135
v_lshlrev_b32_sdwa v139,1,v139 dst_sel:DWORD dst_unused:UNUSED_PAD src0_sel:DWORD src1_sel:WORD_1
v_mfma_f32_16x16x32_bf16 v[252:255],v[108:111],v[152:155],v[252:255]
ds_read_b64_tr_b16 v[92:93],v6 offset:12328
ds_read_b64_tr_b16 v[94:95],v6 offset:13864
v_lshlrev_b32_sdwa v140,1,v140 dst_sel:DWORD dst_unused:UNUSED_PAD src0_sel:DWORD src1_sel:WORD_1
v_mfma_f32_16x16x32_bf16 v[144:147],v[64:67],v[40:43],v[112:115]
ds_read_u16 v136,v136
v_lshlrev_b32_sdwa v142,1,v142 dst_sel:DWORD dst_unused:UNUSED_PAD src0_sel:DWORD src1_sel:WORD_1
v_mfma_f32_16x16x32_bf16 v[152:155],v[64:67],v[44:47],v[112:115]
ds_read_u16_d16_hi v138,v138
v_lshlrev_b32_sdwa v141,1,v141 dst_sel:DWORD dst_unused:UNUSED_PAD src0_sel:DWORD src1_sel:WORD_1
v_mfma_f32_16x16x32_bf16 v[156:159],v[68:71],v[44:47],v[116:119]
ds_read_b64_tr_b16 v[96:97],v6 offset:12384
ds_read_b64_tr_b16 v[98:99],v6 offset:13920
v_lshlrev_b32_sdwa v143,1,v143 dst_sel:DWORD dst_unused:UNUSED_PAD src0_sel:DWORD src1_sel:WORD_1
v_mfma_f32_16x16x32_bf16 v[148:151],v[68:71],v[40:43],v[116:119]
ds_read_u16 v137,v137
v_mfma_f32_16x16x32_bf16 v[144:147],v[72:75],v[48:51],v[144:147]
ds_read_u16_d16_hi v139,v139
v_mfma_f32_16x16x32_bf16 v[152:155],v[72:75],v[52:55],v[152:155]
ds_read_b64_tr_b16 v[100:101],v6 offset:12392
ds_read_b64_tr_b16 v[102:103],v6 offset:13928
v_mfma_f32_16x16x32_bf16 v[156:159],v[76:79],v[52:55],v[156:159]
ds_read_u16 v140,v140
v_mfma_f32_16x16x32_bf16 v[148:151],v[76:79],v[48:51],v[148:151]
s_waitcnt lgkmcnt(15)
ds_read_u16_d16_hi v142,v142
v_lshl_add_u64 v[128:129],v[128:129],0,v[130:131]
v_mfma_f32_16x16x32_bf16 v[144:147],v[80:83],v[56:59],v[144:147]
s_waitcnt lgkmcnt(12)
ds_read_b64_tr_b16 v[104:105],v6 offset:12448
ds_read_b64_tr_b16 v[106:107],v6 offset:13984
v_lshl_add_u64 v[130:131],v[132:133],0,v[134:135]
v_mfma_f32_16x16x32_bf16 v[152:155],v[80:83],v[60:63],v[152:155]
ds_read_u16 v141,v141
v_mfma_f32_16x16x32_bf16 v[156:159],v[84:87],v[60:63],v[156:159]
ds_read_u16_d16_hi v143,v143
v_mfma_f32_16x16x32_bf16 v[148:151],v[84:87],v[56:59],v[148:151]
ds_read_b64_tr_b16 v[108:109],v6 offset:12456
ds_read_b64_tr_b16 v[110:111],v6 offset:13992
v_lshlrev_b32_sdwa v144,1,v144 dst_sel:DWORD dst_unused:UNUSED_PAD src0_sel:DWORD src1_sel:WORD_1
v_lshlrev_b32_sdwa v146,1,v146 dst_sel:DWORD dst_unused:UNUSED_PAD src0_sel:DWORD src1_sel:WORD_1
v_mfma_f32_16x16x32_bf16 v[160:163],v[88:91],v[128:131],v[160:163]
s_waitcnt lgkmcnt(15)
s_cbranch_vccnz RA4_%=
s_mov_b64 exec,s[62:63]
s_add_u32 m0,s57,59296
s_add_u32 s64,s56,3637248
buffer_load_dwordx4 v2,s[36:39],s64 offen lds
s_add_u32 m0,s57,61024
s_nop 0
buffer_load_dwordx4 v3,s[36:39],s64 offen lds
s_mov_b64 exec,-1
RA4_%=:
ds_read_u16 v144,v144
v_lshlrev_b32_sdwa v145,1,v145 dst_sel:DWORD dst_unused:UNUSED_PAD src0_sel:DWORD src1_sel:WORD_1
v_mfma_f32_16x16x32_bf16 v[168:171],v[92:95],v[128:131],v[168:171]
s_waitcnt lgkmcnt(13)
ds_read_u16_d16_hi v146,v146
v_lshlrev_b32_sdwa v147,1,v147 dst_sel:DWORD dst_unused:UNUSED_PAD src0_sel:DWORD src1_sel:WORD_1
v_mfma_f32_16x16x32_bf16 v[176:179],v[96:99],v[128:131],v[176:179]
s_waitcnt lgkmcnt(10)
ds_read_b64_tr_b16 v[64:65],v4 offset:12320
ds_read_b64_tr_b16 v[66:67],v4 offset:14368
v_lshlrev_b32_sdwa v148,1,v148 dst_sel:DWORD dst_unused:UNUSED_PAD src0_sel:DWORD src1_sel:WORD_1
v_mfma_f32_16x16x32_bf16 v[184:187],v[100:103],v[128:131],v[184:187]
s_waitcnt lgkmcnt(8)
ds_read_u16 v145,v145
v_lshl_add_u64 v[136:137],v[136:137],0,v[138:139]
v_lshlrev_b32_sdwa v150,1,v150 dst_sel:DWORD dst_unused:UNUSED_PAD src0_sel:DWORD src1_sel:WORD_1
v_mfma_f32_16x16x32_bf16 v[192:195],v[104:107],v[128:131],v[192:195]
s_waitcnt lgkmcnt(5)
ds_read_u16_d16_hi v147,v147
v_lshl_add_u64 v[138:139],v[140:141],0,v[142:143]
v_mfma_f32_16x16x32_bf16 v[200:203],v[108:111],v[128:131],v[200:203]
ds_read_b128 v[112:115],v7 offset:1024
ds_read_b64_tr_b16 v[68:69],v5 offset:12320
v_lshlrev_b32_sdwa v149,1,v149 dst_sel:DWORD dst_unused:UNUSED_PAD src0_sel:DWORD src1_sel:WORD_1
v_mfma_f32_16x16x32_bf16 v[164:167],v[88:91],v[136:139],v[164:167]
ds_read_u16 v148,v148
v_lshlrev_b32_sdwa v151,1,v151 dst_sel:DWORD dst_unused:UNUSED_PAD src0_sel:DWORD src1_sel:WORD_1
v_mfma_f32_16x16x32_bf16 v[172:175],v[92:95],v[136:139],v[172:175]
ds_read_u16_d16_hi v150,v150
ds_read_b64_tr_b16 v[70:71],v5 offset:14368
v_lshlrev_b32_sdwa v152,1,v152 dst_sel:DWORD dst_unused:UNUSED_PAD src0_sel:DWORD src1_sel:WORD_1
v_mfma_f32_16x16x32_bf16 v[180:183],v[96:99],v[136:139],v[180:183]
ds_read_b128 v[116:119],v7 offset:1056
ds_read_b64_tr_b16 v[72:73],v4 offset:16416
v_lshlrev_b32_sdwa v154,1,v154 dst_sel:DWORD dst_unused:UNUSED_PAD src0_sel:DWORD src1_sel:WORD_1
v_mfma_f32_16x16x32_bf16 v[188:191],v[100:103],v[136:139],v[188:191]
ds_read_u16 v149,v149
v_lshlrev_b32_sdwa v153,1,v153 dst_sel:DWORD dst_unused:UNUSED_PAD src0_sel:DWORD src1_sel:WORD_1
v_mfma_f32_16x16x32_bf16 v[196:199],v[104:107],v[136:139],v[196:199]
ds_read_u16_d16_hi v151,v151
v_lshlrev_b32_sdwa v155,1,v155 dst_sel:DWORD dst_unused:UNUSED_PAD src0_sel:DWORD src1_sel:WORD_1
v_mfma_f32_16x16x32_bf16 v[204:207],v[108:111],v[136:139],v[204:207]
s_waitcnt lgkmcnt(8)
ds_read_b64_tr_b16 v[74:75],v4 offset:18464
ds_read_b64_tr_b16 v[76:77],v5 offset:16416
v_lshlrev_b32_sdwa v156,1,v156 dst_sel:DWORD dst_unused:UNUSED_PAD src0_sel:DWORD src1_sel:WORD_1
v_mfma_f32_16x16x32_bf16 v[128:131],v[64:67],v[16:19],v[112:115]
ds_read_u16 v152,v152
v_lshlrev_b32_sdwa v158,1,v158 dst_sel:DWORD dst_unused:UNUSED_PAD src0_sel:DWORD src1_sel:WORD_1
v_mfma_f32_16x16x32_bf16 v[136:139],v[64:67],v[20:23],v[112:115]
s_waitcnt lgkmcnt(6)
ds_read_u16_d16_hi v154,v154
ds_read_b64_tr_b16 v[78:79],v5 offset:18464
v_lshlrev_b32_sdwa v157,1,v157 dst_sel:DWORD dst_unused:UNUSED_PAD src0_sel:DWORD src1_sel:WORD_1
v_mfma_f32_16x16x32_bf16 v[140:143],v[68:71],v[20:23],v[116:119]
ds_read_b64_tr_b16 v[80:81],v4 offset:20512
ds_read_b64_tr_b16 v[82:83],v4 offset:22560
v_lshlrev_b32_sdwa v159,1,v159 dst_sel:DWORD dst_unused:UNUSED_PAD src0_sel:DWORD src1_sel:WORD_1
v_mfma_f32_16x16x32_bf16 v[132:135],v[68:71],v[16:19],v[116:119]
s_waitcnt lgkmcnt(6)
ds_read_u16 v153,v153
v_mfma_f32_16x16x32_bf16 v[128:131],v[72:75],v[24:27],v[128:131]
ds_read_u16_d16_hi v155,v155
v_mfma_f32_16x16x32_bf16 v[136:139],v[72:75],v[28:31],v[136:139]
s_waitcnt lgkmcnt(4)
ds_read_b64_tr_b16 v[84:85],v5 offset:20512
ds_read_b64_tr_b16 v[86:87],v5 offset:22560
v_mfma_f32_16x16x32_bf16 v[140:143],v[76:79],v[28:31],v[140:143]
ds_read_u16 v156,v156
v_mfma_f32_16x16x32_bf16 v[132:135],v[76:79],v[24:27],v[132:135]
s_waitcnt lgkmcnt(5)
ds_read_u16_d16_hi v158,v158
v_lshl_add_u64 v[144:145],v[144:145],0,v[146:147]
v_mfma_f32_16x16x32_bf16 v[128:131],v[80:83],v[32:35],v[128:131]
v_lshl_add_u64 v[146:147],v[148:149],0,v[150:151]
v_mfma_f32_16x16x32_bf16 v[136:139],v[80:83],v[36:39],v[136:139]
s_waitcnt lgkmcnt(2)
ds_read_u16 v157,v157
v_mfma_f32_16x16x32_bf16 v[140:143],v[84:87],v[36:39],v[140:143]
ds_read_u16_d16_hi v159,v159
v_mfma_f32_16x16x32_bf16 v[132:135],v[84:87],v[32:35],v[132:135]
s_nop 0
v_lshlrev_b32_sdwa v128,1,v128 dst_sel:DWORD dst_unused:UNUSED_PAD src0_sel:DWORD src1_sel:WORD_1
v_lshlrev_b32_sdwa v130,1,v130 dst_sel:DWORD dst_unused:UNUSED_PAD src0_sel:DWORD src1_sel:WORD_1
v_mfma_f32_16x16x32_bf16 v[208:211],v[88:91],v[144:147],v[208:211]
s_cbranch_vccnz RA5_%=
s_mov_b64 exec,s[62:63]
s_add_u32 m0,s57,62784
s_add_u32 s64,s56,3735552
buffer_load_dwordx4 v2,s[36:39],s64 offen lds
s_add_u32 m0,s57,64512
s_nop 0
buffer_load_dwordx4 v3,s[36:39],s64 offen lds
s_mov_b64 exec,-1
RA5_%=:
ds_read_u16 v128,v128
v_lshlrev_b32_sdwa v129,1,v129 dst_sel:DWORD dst_unused:UNUSED_PAD src0_sel:DWORD src1_sel:WORD_1
v_mfma_f32_16x16x32_bf16 v[216:219],v[92:95],v[144:147],v[216:219]
ds_read_u16_d16_hi v130,v130
v_lshlrev_b32_sdwa v131,1,v131 dst_sel:DWORD dst_unused:UNUSED_PAD src0_sel:DWORD src1_sel:WORD_1
v_mfma_f32_16x16x32_bf16 v[224:227],v[96:99],v[144:147],v[224:227]
v_lshlrev_b32_sdwa v132,1,v132 dst_sel:DWORD dst_unused:UNUSED_PAD src0_sel:DWORD src1_sel:WORD_1
v_mfma_f32_16x16x32_bf16 v[232:235],v[100:103],v[144:147],v[232:235]
ds_read_u16 v129,v129
v_lshl_add_u64 v[152:153],v[152:153],0,v[154:155]
v_lshlrev_b32_sdwa v134,1,v134 dst_sel:DWORD dst_unused:UNUSED_PAD src0_sel:DWORD src1_sel:WORD_1
v_mfma_f32_16x16x32_bf16 v[240:243],v[104:107],v[144:147],v[240:243]
s_waitcnt lgkmcnt(3)
ds_read_u16_d16_hi v131,v131
v_lshl_add_u64 v[154:155],v[156:157],0,v[158:159]
v_mfma_f32_16x16x32_bf16 v[248:251],v[108:111],v[144:147],v[248:251]
v_lshlrev_b32_sdwa v133,1,v133 dst_sel:DWORD dst_unused:UNUSED_PAD src0_sel:DWORD src1_sel:WORD_1
v_mfma_f32_16x16x32_bf16 v[212:215],v[88:91],v[152:155],v[212:215]
ds_read_u16 v132,v132
v_lshlrev_b32_sdwa v135,1,v135 dst_sel:DWORD dst_unused:UNUSED_PAD src0_sel:DWORD src1_sel:WORD_1
v_mfma_f32_16x16x32_bf16 v[220:223],v[92:95],v[152:155],v[220:223]
ds_read_u16_d16_hi v134,v134
v_lshlrev_b32_sdwa v136,1,v136 dst_sel:DWORD dst_unused:UNUSED_PAD src0_sel:DWORD src1_sel:WORD_1
v_mfma_f32_16x16x32_bf16 v[228:231],v[96:99],v[152:155],v[228:231]
ds_read_b64_tr_b16 v[88:89],v6 offset:18480
ds_read_b64_tr_b16 v[90:91],v6 offset:20016
v_lshlrev_b32_sdwa v138,1,v138 dst_sel:DWORD dst_unused:UNUSED_PAD src0_sel:DWORD src1_sel:WORD_1
v_mfma_f32_16x16x32_bf16 v[236:239],v[100:103],v[152:155],v[236:239]
ds_read_u16 v133,v133
v_lshlrev_b32_sdwa v137,1,v137 dst_sel:DWORD dst_unused:UNUSED_PAD src0_sel:DWORD src1_sel:WORD_1
v_mfma_f32_16x16x32_bf16 v[244:247],v[104:107],v[152:155],v[244:247]
ds_read_u16_d16_hi v135,v135
v_lshlrev_b32_sdwa v139,1,v139 dst_sel:DWORD dst_unused:UNUSED_PAD src0_sel:DWORD src1_sel:WORD_1
v_mfma_f32_16x16x32_bf16 v[252:255],v[108:111],v[152:155],v[252:255]
ds_read_b64_tr_b16 v[92:93],v6 offset:18488
ds_read_b64_tr_b16 v[94:95],v6 offset:20024
v_lshlrev_b32_sdwa v140,1,v140 dst_sel:DWORD dst_unused:UNUSED_PAD src0_sel:DWORD src1_sel:WORD_1
v_mfma_f32_16x16x32_bf16 v[144:147],v[64:67],v[40:43],v[112:115]
ds_read_u16 v136,v136
v_lshlrev_b32_sdwa v142,1,v142 dst_sel:DWORD dst_unused:UNUSED_PAD src0_sel:DWORD src1_sel:WORD_1
v_mfma_f32_16x16x32_bf16 v[152:155],v[64:67],v[44:47],v[112:115]
ds_read_u16_d16_hi v138,v138
v_lshlrev_b32_sdwa v141,1,v141 dst_sel:DWORD dst_unused:UNUSED_PAD src0_sel:DWORD src1_sel:WORD_1
v_mfma_f32_16x16x32_bf16 v[156:159],v[68:71],v[44:47],v[116:119]
ds_read_b64_tr_b16 v[96:97],v6 offset:18544
ds_read_b64_tr_b16 v[98:99],v6 offset:20080
v_lshlrev_b32_sdwa v143,1,v143 dst_sel:DWORD dst_unused:UNUSED_PAD src0_sel:DWORD src1_sel:WORD_1
v_mfma_f32_16x16x32_bf16 v[148:151],v[68:71],v[40:43],v[116:119]
ds_read_u16 v137,v137
v_mfma_f32_16x16x32_bf16 v[144:147],v[72:75],v[48:51],v[144:147]
ds_read_u16_d16_hi v139,v139
v_mfma_f32_16x16x32_bf16 v[152:155],v[72:75],v[52:55],v[152:155]
ds_read_b64_tr_b16 v[100:101],v6 offset:18552
ds_read_b64_tr_b16 v[102:103],v6 offset:20088
v_mfma_f32_16x16x32_bf16 v[156:159],v[76:79],v[52:55],v[156:159]
ds_read_u16 v140,v140
v_mfma_f32_16x16x32_bf16 v[148:151],v[76:79],v[48:51],v[148:151]
s_waitcnt lgkmcnt(15)
ds_read_u16_d16_hi v142,v142
v_lshl_add_u64 v[128:129],v[128:129],0,v[130:131]
v_mfma_f32_16x16x32_bf16 v[144:147],v[80:83],v[56:59],v[144:147]
s_waitcnt lgkmcnt(12)
ds_read_b64_tr_b16 v[104:105],v6 offset:18608
ds_read_b64_tr_b16 v[106:107],v6 offset:20144
v_lshl_add_u64 v[130:131],v[132:133],0,v[134:135]
v_mfma_f32_16x16x32_bf16 v[152:155],v[80:83],v[60:63],v[152:155]
ds_read_u16 v141,v141
v_mfma_f32_16x16x32_bf16 v[156:159],v[84:87],v[60:63],v[156:159]
ds_read_u16_d16_hi v143,v143
v_mfma_f32_16x16x32_bf16 v[148:151],v[84:87],v[56:59],v[148:151]
ds_read_b64_tr_b16 v[108:109],v6 offset:18616
ds_read_b64_tr_b16 v[110:111],v6 offset:20152
v_lshlrev_b32_sdwa v144,1,v144 dst_sel:DWORD dst_unused:UNUSED_PAD src0_sel:DWORD src1_sel:WORD_1
v_lshlrev_b32_sdwa v146,1,v146 dst_sel:DWORD dst_unused:UNUSED_PAD src0_sel:DWORD src1_sel:WORD_1
v_mfma_f32_16x16x32_bf16 v[160:163],v[88:91],v[128:131],v[160:163]
s_waitcnt lgkmcnt(15)
s_cbranch_vccnz RA6_%=
s_mov_b64 exec,s[62:63]
s_add_u32 m0,s57,66272
s_add_u32 s64,s56,3833856
buffer_load_dwordx4 v2,s[36:39],s64 offen lds
s_add_u32 m0,s57,68000
s_nop 0
buffer_load_dwordx4 v3,s[36:39],s64 offen lds
s_mov_b64 exec,-1
RA6_%=:
ds_read_u16 v144,v144
v_lshlrev_b32_sdwa v145,1,v145 dst_sel:DWORD dst_unused:UNUSED_PAD src0_sel:DWORD src1_sel:WORD_1
v_mfma_f32_16x16x32_bf16 v[168:171],v[92:95],v[128:131],v[168:171]
s_waitcnt lgkmcnt(13)
ds_read_u16_d16_hi v146,v146
v_lshlrev_b32_sdwa v147,1,v147 dst_sel:DWORD dst_unused:UNUSED_PAD src0_sel:DWORD src1_sel:WORD_1
v_mfma_f32_16x16x32_bf16 v[176:179],v[96:99],v[128:131],v[176:179]
s_waitcnt lgkmcnt(10)
ds_read_b64_tr_b16 v[64:65],v4 offset:12832
ds_read_b64_tr_b16 v[66:67],v4 offset:14880
v_lshlrev_b32_sdwa v148,1,v148 dst_sel:DWORD dst_unused:UNUSED_PAD src0_sel:DWORD src1_sel:WORD_1
v_mfma_f32_16x16x32_bf16 v[184:187],v[100:103],v[128:131],v[184:187]
s_waitcnt lgkmcnt(8)
ds_read_u16 v145,v145
v_lshl_add_u64 v[136:137],v[136:137],0,v[138:139]
v_lshlrev_b32_sdwa v150,1,v150 dst_sel:DWORD dst_unused:UNUSED_PAD src0_sel:DWORD src1_sel:WORD_1
v_mfma_f32_16x16x32_bf16 v[192:195],v[104:107],v[128:131],v[192:195]
s_waitcnt lgkmcnt(5)
ds_read_u16_d16_hi v147,v147
v_lshl_add_u64 v[138:139],v[140:141],0,v[142:143]
v_mfma_f32_16x16x32_bf16 v[200:203],v[108:111],v[128:131],v[200:203]
ds_read_b128 v[112:115],v7 offset:1152
ds_read_b64_tr_b16 v[68:69],v5 offset:12832
v_lshlrev_b32_sdwa v149,1,v149 dst_sel:DWORD dst_unused:UNUSED_PAD src0_sel:DWORD src1_sel:WORD_1
v_mfma_f32_16x16x32_bf16 v[164:167],v[88:91],v[136:139],v[164:167]
ds_read_u16 v148,v148
v_lshlrev_b32_sdwa v151,1,v151 dst_sel:DWORD dst_unused:UNUSED_PAD src0_sel:DWORD src1_sel:WORD_1
v_mfma_f32_16x16x32_bf16 v[172:175],v[92:95],v[136:139],v[172:175]
ds_read_u16_d16_hi v150,v150
ds_read_b64_tr_b16 v[70:71],v5 offset:14880
v_lshlrev_b32_sdwa v152,1,v152 dst_sel:DWORD dst_unused:UNUSED_PAD src0_sel:DWORD src1_sel:WORD_1
v_mfma_f32_16x16x32_bf16 v[180:183],v[96:99],v[136:139],v[180:183]
ds_read_b128 v[116:119],v7 offset:1184
ds_read_b64_tr_b16 v[72:73],v4 offset:16928
v_lshlrev_b32_sdwa v154,1,v154 dst_sel:DWORD dst_unused:UNUSED_PAD src0_sel:DWORD src1_sel:WORD_1
v_mfma_f32_16x16x32_bf16 v[188:191],v[100:103],v[136:139],v[188:191]
ds_read_u16 v149,v149
v_lshlrev_b32_sdwa v153,1,v153 dst_sel:DWORD dst_unused:UNUSED_PAD src0_sel:DWORD src1_sel:WORD_1
v_mfma_f32_16x16x32_bf16 v[196:199],v[104:107],v[136:139],v[196:199]
ds_read_u16_d16_hi v151,v151
v_lshlrev_b32_sdwa v155,1,v155 dst_sel:DWORD dst_unused:UNUSED_PAD src0_sel:DWORD src1_sel:WORD_1
v_mfma_f32_16x16x32_bf16 v[204:207],v[108:111],v[136:139],v[204:207]
s_waitcnt lgkmcnt(8)
ds_read_b64_tr_b16 v[74:75],v4 offset:18976
ds_read_b64_tr_b16 v[76:77],v5 offset:16928
v_lshlrev_b32_sdwa v156,1,v156 dst_sel:DWORD dst_unused:UNUSED_PAD src0_sel:DWORD src1_sel:WORD_1
v_mfma_f32_16x16x32_bf16 v[128:131],v[64:67],v[16:19],v[112:115]
s_mov_b32 s86,0xbe801d5a
ds_read_u16 v152,v152
v_lshlrev_b32_sdwa v158,1,v158 dst_sel:DWORD dst_unused:UNUSED_PAD src0_sel:DWORD src1_sel:WORD_1
v_mfma_f32_16x16x32_bf16 v[136:139],v[64:67],v[20:23],v[112:115]
s_waitcnt lgkmcnt(6)
ds_read_u16_d16_hi v154,v154
ds_read_b64_tr_b16 v[78:79],v5 offset:18976
v_lshlrev_b32_sdwa v157,1,v157 dst_sel:DWORD dst_unused:UNUSED_PAD src0_sel:DWORD src1_sel:WORD_1
v_mfma_f32_16x16x32_bf16 v[140:143],v[68:71],v[20:23],v[116:119]
ds_read_b64_tr_b16 v[80:81],v4 offset:21024
ds_read_b64_tr_b16 v[82:83],v4 offset:23072
v_lshlrev_b32_sdwa v159,1,v159 dst_sel:DWORD dst_unused:UNUSED_PAD src0_sel:DWORD src1_sel:WORD_1
v_mfma_f32_16x16x32_bf16 v[132:135],v[68:71],v[16:19],v[116:119]
s_waitcnt lgkmcnt(6)
ds_read_u16 v153,v153
v_mfma_f32_16x16x32_bf16 v[128:131],v[72:75],v[24:27],v[128:131]
ds_read_u16_d16_hi v155,v155
v_mfma_f32_16x16x32_bf16 v[136:139],v[72:75],v[28:31],v[136:139]
s_waitcnt lgkmcnt(4)
ds_read_b64_tr_b16 v[84:85],v5 offset:21024
ds_read_b64_tr_b16 v[86:87],v5 offset:23072
v_mfma_f32_16x16x32_bf16 v[140:143],v[76:79],v[28:31],v[140:143]
ds_read_u16 v156,v156
v_mfma_f32_16x16x32_bf16 v[132:135],v[76:79],v[24:27],v[132:135]
s_waitcnt lgkmcnt(5)
ds_read_u16_d16_hi v158,v158
v_lshl_add_u64 v[144:145],v[144:145],0,v[146:147]
v_mfma_f32_16x16x32_bf16 v[128:131],v[80:83],v[32:35],v[128:131]
v_lshl_add_u64 v[146:147],v[148:149],0,v[150:151]
v_mfma_f32_16x16x32_bf16 v[136:139],v[80:83],v[36:39],v[136:139]
s_waitcnt lgkmcnt(2)
ds_read_u16 v157,v157
v_mfma_f32_16x16x32_bf16 v[140:143],v[84:87],v[36:39],v[140:143]
ds_read_u16_d16_hi v159,v159
v_mfma_f32_16x16x32_bf16 v[132:135],v[84:87],v[32:35],v[132:135]
s_cbranch_vccnz RA7_%=
s_mov_b64 exec,s[62:63]
s_add_u32 m0,s57,0
s_add_u32 s64,s56,3932160
buffer_load_dwordx4 v2,s[36:39],s64 offen lds
s_add_u32 m0,s57,1728
s_nop 0
buffer_load_dwordx4 v3,s[36:39],s64 offen lds
s_mov_b64 exec,-1
RA7_%=:
v_lshlrev_b32_sdwa v128,1,v128 dst_sel:DWORD dst_unused:UNUSED_PAD src0_sel:DWORD src1_sel:WORD_1
v_lshlrev_b32_sdwa v130,1,v130 dst_sel:DWORD dst_unused:UNUSED_PAD src0_sel:DWORD src1_sel:WORD_1
v_mfma_f32_16x16x32_bf16 v[208:211],v[88:91],v[144:147],v[208:211]
s_barrier
s_setprio 3
ds_read_u16 v128,v128
v_lshlrev_b32_sdwa v129,1,v129 dst_sel:DWORD dst_unused:UNUSED_PAD src0_sel:DWORD src1_sel:WORD_1
v_lshlrev_b32_sdwa v131,1,v131 dst_sel:DWORD dst_unused:UNUSED_PAD src0_sel:DWORD src1_sel:WORD_1
v_mfma_f32_16x16x32_bf16 v[216:219],v[92:95],v[144:147],v[216:219]
v_fma_mixlo_f16 v120,v140,s99,0
v_fma_mixlo_f16 v122,v141,s99,0
v_mfma_f32_16x16x32_bf16 v[224:227],v[96:99],v[144:147],v[224:227]
s_cbranch_vccz RA8_%=
s_add_u32 m0,s58,12320
s_mov_b32 s69,s60
buffer_load_dwordx4 v2,s[44:47],s69 offen lds
s_add_u32 m0,m0,1024
s_add_u32 s69,s69,s53
buffer_load_dwordx4 v3,s[44:47],s69 offen lds
RA8_%=:
ds_read_u16_d16_hi v130,v130
v_lshlrev_b32_sdwa v132,1,v132 dst_sel:DWORD dst_unused:UNUSED_PAD src0_sel:DWORD src1_sel:WORD_1
v_lshlrev_b32_sdwa v134,1,v134 dst_sel:DWORD dst_unused:UNUSED_PAD src0_sel:DWORD src1_sel:WORD_1
v_mfma_f32_16x16x32_bf16 v[232:235],v[100:103],v[144:147],v[232:235]
v_lshl_add_u64 v[152:153],v[152:153],0,v[154:155]
v_mfma_f32_16x16x32_bf16 v[240:243],v[104:107],v[144:147],v[240:243]
s_waitcnt lgkmcnt(2)
ds_read_u16 v129,v129
v_lshl_add_u64 v[154:155],v[156:157],0,v[158:159]
v_mfma_f32_16x16x32_bf16 v[248:251],v[108:111],v[144:147],v[248:251]
v_fma_mixhi_f16 v120,v142,s99,0
v_fma_mixhi_f16 v122,v143,s99,0
v_mfma_f32_16x16x32_bf16 v[212:215],v[88:91],v[152:155],v[212:215]
s_cbranch_vccz RA9_%=
s_add_u32 m0,m0,1024
s_add_u32 s69,s69,s53
buffer_load_dwordx4 v2,s[44:47],s69 offen lds
s_add_u32 m0,m0,s50
s_add_u32 s69,s69,s53
buffer_load_dwordx4 v3,s[44:47],s69 offen lds
RA9_%=:
ds_read_u16_d16_hi v131,v131
v_lshlrev_b32_sdwa v133,1,v133 dst_sel:DWORD dst_unused:UNUSED_PAD src0_sel:DWORD src1_sel:WORD_1
v_lshlrev_b32_sdwa v135,1,v135 dst_sel:DWORD dst_unused:UNUSED_PAD src0_sel:DWORD src1_sel:WORD_1
v_mfma_f32_16x16x32_bf16 v[220:223],v[92:95],v[152:155],v[220:223]
ds_read_b64_tr_b16 v[88:89],v6 offset:0
v_pk_fma_f16 v121,v120,v120,s54
v_pk_fma_f16 v123,v122,v122,s54
v_mfma_f32_16x16x32_bf16 v[228:231],v[96:99],v[152:155],v[228:231]
ds_read_u16 v132,v132
v_pk_mul_f16 v121,v120,v121
v_pk_mul_f16 v123,v122,v123
v_mfma_f32_16x16x32_bf16 v[236:239],v[100:103],v[152:155],v[236:239]
ds_read_b64_tr_b16 v[90:91],v6 offset:1536
v_lshlrev_b32_sdwa v136,1,v136 dst_sel:DWORD dst_unused:UNUSED_PAD src0_sel:DWORD src1_sel:WORD_1
v_lshlrev_b32_sdwa v138,1,v138 dst_sel:DWORD dst_unused:UNUSED_PAD src0_sel:DWORD src1_sel:WORD_1
v_mfma_f32_16x16x32_bf16 v[244:247],v[104:107],v[152:155],v[244:247]
s_cbranch_vccz RA10_%=
s_add_u32 m0,m0,1024
s_add_u32 s69,s69,s53
buffer_load_dwordx4 v2,s[44:47],s69 offen lds
s_add_u32 m0,m0,1024
s_add_u32 s69,s69,s53
buffer_load_dwordx4 v3,s[44:47],s69 offen lds
RA10_%=:
ds_read_u16_d16_hi v134,v134
v_exp_f16 v120,v121
v_mfma_f32_16x16x32_bf16 v[252:255],v[108:111],v[152:155],v[252:255]
ds_read_b64_tr_b16 v[92:93],v6 offset:8
v_exp_f16 v122,v123
v_mfma_f32_16x16x32_bf16 v[144:147],v[64:67],v[40:43],v[112:115]
ds_read_u16 v133,v133
v_lshlrev_b32_sdwa v137,1,v137 dst_sel:DWORD dst_unused:UNUSED_PAD src0_sel:DWORD src1_sel:WORD_1
v_lshlrev_b32_sdwa v139,1,v139 dst_sel:DWORD dst_unused:UNUSED_PAD src0_sel:DWORD src1_sel:WORD_1
v_mfma_f32_16x16x32_bf16 v[152:155],v[64:67],v[44:47],v[112:115]
s_cbranch_vccz RA11_%=
s_add_u32 m0,m0,s50
s_add_u32 s69,s69,s51
buffer_load_dwordx4 v2,s[44:47],s69 offen lds
s_add_u32 m0,m0,1024
s_add_u32 s69,s69,s53
buffer_load_dwordx4 v3,s[44:47],s69 offen lds
RA11_%=:
ds_read_b64_tr_b16 v[94:95],v6 offset:1544
v_exp_f16_sdwa v120,v121 dst_sel:WORD_1 dst_unused:UNUSED_PRESERVE src0_sel:WORD_1
v_mfma_f32_16x16x32_bf16 v[156:159],v[68:71],v[44:47],v[116:119]
ds_read_u16_d16_hi v135,v135
v_exp_f16_sdwa v122,v123 dst_sel:WORD_1 dst_unused:UNUSED_PRESERVE src0_sel:WORD_1
v_mfma_f32_16x16x32_bf16 v[148:151],v[68:71],v[40:43],v[116:119]
ds_read_b64_tr_b16 v[96:97],v6 offset:64
v_pk_fma_f16 v120,v120,s55,s55
v_pk_fma_f16 v122,v122,s55,s55
v_mfma_f32_16x16x32_bf16 v[144:147],v[72:75],v[48:51],v[144:147]
s_cbranch_vccnz RA12_%=
s_mov_b64 exec,s[62:63]
s_add_u32 m0,s57,3488
s_add_u32 s64,s56,4030464
buffer_load_dwordx4 v2,s[36:39],s64 offen lds
s_add_u32 m0,s57,5216
s_nop 0
buffer_load_dwordx4 v3,s[36:39],s64 offen lds
s_mov_b64 exec,-1
RA12_%=:
ds_read_u16 v136,v136
v_rcp_f16 v121,v120
v_mfma_f32_16x16x32_bf16 v[152:155],v[72:75],v[52:55],v[152:155]
s_cbranch_vccz RA13_%=
s_add_u32 m0,m0,1024
s_add_u32 s69,s69,s53
buffer_load_dwordx4 v2,s[44:47],s69 offen lds
s_add_u32 m0,m0,s50
s_add_u32 s69,s69,s53
buffer_load_dwordx4 v3,s[44:47],s69 offen lds
RA13_%=:
ds_read_b64_tr_b16 v[98:99],v6 offset:1600
ds_read_b64_tr_b16 v[100:101],v6 offset:72
v_rcp_f16 v123,v122
v_mfma_f32_16x16x32_bf16 v[156:159],v[76:79],v[52:55],v[156:159]
ds_read_u16_d16_hi v138,v138
v_rcp_f16_sdwa v121,v120 dst_sel:WORD_1 dst_unused:UNUSED_PRESERVE src0_sel:WORD_1
v_mfma_f32_16x16x32_bf16 v[148:151],v[76:79],v[48:51],v[148:151]
ds_read_b64_tr_b16 v[102:103],v6 offset:1608
ds_read_b64_tr_b16 v[104:105],v6 offset:128
v_rcp_f16_sdwa v123,v122 dst_sel:WORD_1 dst_unused:UNUSED_PRESERVE src0_sel:WORD_1
v_mfma_f32_16x16x32_bf16 v[144:147],v[80:83],v[56:59],v[144:147]
ds_read_u16 v137,v137
v_fma_mix_f32 v140,v140,v121,0 op_sel_hi:[0,1,0]
v_fma_mix_f32 v142,v142,v121,0 op_sel:[0,1,0] op_sel_hi:[0,1,0]
v_mfma_f32_16x16x32_bf16 v[152:155],v[80:83],v[60:63],v[152:155]
s_waitcnt lgkmcnt(8)
s_cbranch_vccz RA14_%=
s_add_u32 m0,m0,1024
s_add_u32 s69,s69,s53
buffer_load_dwordx4 v2,s[44:47],s69 offen lds
s_add_u32 m0,m0,1024
s_add_u32 s69,s69,s53
buffer_load_dwordx4 v3,s[44:47],s69 offen lds
RA14_%=:
ds_read_b64_tr_b16 v[106:107],v6 offset:1664
ds_read_b64_tr_b16 v[108:109],v6 offset:136
v_lshl_add_u64 v[128:129],v[128:129],0,v[130:131]
v_lshl_add_u64 v[130:131],v[132:133],0,v[134:135]
v_mfma_f32_16x16x32_bf16 v[156:159],v[84:87],v[60:63],v[156:159]
ds_read_u16_d16_hi v139,v139
v_fma_mix_f32 v141,v141,v123,0 op_sel_hi:[0,1,0]
v_fma_mix_f32 v143,v143,v123,0 op_sel:[0,1,0] op_sel_hi:[0,1,0]
v_mfma_f32_16x16x32_bf16 v[148:151],v[84:87],v[56:59],v[148:151]
ds_read_b64_tr_b16 v[110:111],v6 offset:1672
ds_read_b64_tr_b16 v[64:65],v4 offset:0
v_lshlrev_b32_sdwa v144,1,v144 dst_sel:DWORD dst_unused:UNUSED_PAD src0_sel:DWORD src1_sel:WORD_1
v_lshlrev_b32_sdwa v146,1,v146 dst_sel:DWORD dst_unused:UNUSED_PAD src0_sel:DWORD src1_sel:WORD_1
v_mfma_f32_16x16x32_bf16 v[160:163],v[88:91],v[128:131],v[160:163]
ds_read_u16 v144,v144
v_lshlrev_b32_sdwa v145,1,v145 dst_sel:DWORD dst_unused:UNUSED_PAD src0_sel:DWORD src1_sel:WORD_1
v_lshlrev_b32_sdwa v147,1,v147 dst_sel:DWORD dst_unused:UNUSED_PAD src0_sel:DWORD src1_sel:WORD_1
v_mfma_f32_16x16x32_bf16 v[168:171],v[92:95],v[128:131],v[168:171]
s_waitcnt lgkmcnt(11)
ds_read_b64_tr_b16 v[66:67],v4 offset:2048
ds_read_b128 v[112:115],v7 offset:1280
v_fma_mixlo_f16 v120,v156,s99,0
v_fma_mixlo_f16 v122,v157,s99,0
v_mfma_f32_16x16x32_bf16 v[176:179],v[96:99],v[128:131],v[176:179]
s_waitcnt lgkmcnt(10)
ds_read_u16_d16_hi v146,v146
v_lshlrev_b32_sdwa v148,1,v148 dst_sel:DWORD dst_unused:UNUSED_PAD src0_sel:DWORD src1_sel:WORD_1
v_lshlrev_b32_sdwa v150,1,v150 dst_sel:DWORD dst_unused:UNUSED_PAD src0_sel:DWORD src1_sel:WORD_1
v_mfma_f32_16x16x32_bf16 v[184:187],v[100:103],v[128:131],v[184:187]
s_waitcnt lgkmcnt(6)
ds_read_b64_tr_b16 v[68:69],v5 offset:0
ds_read_b64_tr_b16 v[70:71],v5 offset:2048
v_lshl_add_u64 v[136:137],v[136:137],0,v[138:139]
v_mfma_f32_16x16x32_bf16 v[192:195],v[104:107],v[128:131],v[192:195]
s_waitcnt lgkmcnt(7)
ds_read_u16 v145,v145
v_cvt_pk_bf16_f32 v138,v140,v142
v_cvt_pk_bf16_f32 v139,v141,v143
v_mfma_f32_16x16x32_bf16 v[200:203],v[108:111],v[128:131],v[200:203]
ds_read_b128 v[116:119],v7 offset:1312
ds_read_b64_tr_b16 v[72:73],v4 offset:4096
v_fma_mixhi_f16 v120,v158,s99,0
v_fma_mixhi_f16 v122,v159,s99,0
v_mfma_f32_16x16x32_bf16 v[164:167],v[88:91],v[136:139],v[164:167]
ds_read_u16_d16_hi v147,v147
v_lshlrev_b32_sdwa v149,1,v149 dst_sel:DWORD dst_unused:UNUSED_PAD src0_sel:DWORD src1_sel:WORD_1
v_lshlrev_b32_sdwa v151,1,v151 dst_sel:DWORD dst_unused:UNUSED_PAD src0_sel:DWORD src1_sel:WORD_1
v_mfma_f32_16x16x32_bf16 v[172:175],v[92:95],v[136:139],v[172:175]
ds_read_b64_tr_b16 v[74:75],v4 offset:6144
ds_read_b64_tr_b16 v[76:77],v5 offset:4096
v_pk_fma_f16 v121,v120,v120,s54
v_pk_fma_f16 v123,v122,v122,s54
v_mfma_f32_16x16x32_bf16 v[180:183],v[96:99],v[136:139],v[180:183]
ds_read_u16 v148,v148
v_pk_mul_f16 v121,v120,v121
v_pk_mul_f16 v123,v122,v123
v_mfma_f32_16x16x32_bf16 v[188:191],v[100:103],v[136:139],v[188:191]
s_cbranch_vccnz RA15_%=
s_mov_b64 exec,s[62:63]
s_add_u32 m0,s57,6976
s_add_u32 s64,s56,4128768
buffer_load_dwordx4 v2,s[36:39],s64 offen lds
s_add_u32 m0,s57,8704
s_nop 0
buffer_load_dwordx4 v3,s[36:39],s64 offen lds
s_mov_b64 exec,-1
RA15_%=:
ds_read_b64_tr_b16 v[78:79],v5 offset:6144
v_lshlrev_b32_sdwa v152,1,v152 dst_sel:DWORD dst_unused:UNUSED_PAD src0_sel:DWORD src1_sel:WORD_1
v_lshlrev_b32_sdwa v154,1,v154 dst_sel:DWORD dst_unused:UNUSED_PAD src0_sel:DWORD src1_sel:WORD_1
v_mfma_f32_16x16x32_bf16 v[196:199],v[104:107],v[136:139],v[196:199]
ds_read_u16_d16_hi v150,v150
v_exp_f16 v120,v121
v_mfma_f32_16x16x32_bf16 v[204:207],v[108:111],v[136:139],v[204:207]
s_waitcnt lgkmcnt(12)
ds_read_b64_tr_b16 v[80:81],v4 offset:8192
v_exp_f16 v122,v123
v_mfma_f32_16x16x32_bf16 v[128:131],v[64:67],v[16:19],v[112:115]
ds_read_u16 v149,v149
v_lshlrev_b32_sdwa v153,1,v153 dst_sel:DWORD dst_unused:UNUSED_PAD src0_sel:DWORD src1_sel:WORD_1
v_lshlrev_b32_sdwa v155,1,v155 dst_sel:DWORD dst_unused:UNUSED_PAD src0_sel:DWORD src1_sel:WORD_1
v_mfma_f32_16x16x32_bf16 v[136:139],v[64:67],v[20:23],v[112:115]
s_waitcnt lgkmcnt(9)
ds_read_b64_tr_b16 v[82:83],v4 offset:10240
v_exp_f16_sdwa v120,v121 dst_sel:WORD_1 dst_unused:UNUSED_PRESERVE src0_sel:WORD_1
v_mfma_f32_16x16x32_bf16 v[140:143],v[68:71],v[20:23],v[116:119]
ds_read_u16_d16_hi v151,v151
v_exp_f16_sdwa v122,v123 dst_sel:WORD_1 dst_unused:UNUSED_PRESERVE src0_sel:WORD_1
v_mfma_f32_16x16x32_bf16 v[132:135],v[68:71],v[16:19],v[116:119]
s_waitcnt lgkmcnt(8)
ds_read_b64_tr_b16 v[84:85],v5 offset:8192
v_pk_fma_f16 v120,v120,s55,s55
v_pk_fma_f16 v122,v122,s55,s55
v_mfma_f32_16x16x32_bf16 v[128:131],v[72:75],v[24:27],v[128:131]
ds_read_u16 v152,v152
v_rcp_f16 v121,v120
v_mfma_f32_16x16x32_bf16 v[136:139],v[72:75],v[28:31],v[136:139]
s_waitcnt lgkmcnt(7)
ds_read_b64_tr_b16 v[86:87],v5 offset:10240
v_rcp_f16 v123,v122
v_mfma_f32_16x16x32_bf16 v[140:143],v[76:79],v[28:31],v[140:143]
ds_read_u16_d16_hi v154,v154
v_rcp_f16_sdwa v121,v120 dst_sel:WORD_1 dst_unused:UNUSED_PRESERVE src0_sel:WORD_1
v_mfma_f32_16x16x32_bf16 v[132:135],v[76:79],v[24:27],v[132:135]
s_waitcnt lgkmcnt(5)
v_rcp_f16_sdwa v123,v122 dst_sel:WORD_1 dst_unused:UNUSED_PRESERVE src0_sel:WORD_1
v_mfma_f32_16x16x32_bf16 v[128:131],v[80:83],v[32:35],v[128:131]
ds_read_u16 v153,v153
v_fma_mix_f32 v156,v156,v121,0 op_sel_hi:[0,1,0]
v_fma_mix_f32 v158,v158,v121,0 op_sel:[0,1,0] op_sel_hi:[0,1,0]
v_mfma_f32_16x16x32_bf16 v[136:139],v[80:83],v[36:39],v[136:139]
s_waitcnt lgkmcnt(2)
v_lshl_add_u64 v[144:145],v[144:145],0,v[146:147]
v_lshl_add_u64 v[146:147],v[148:149],0,v[150:151]
v_mfma_f32_16x16x32_bf16 v[140:143],v[84:87],v[36:39],v[140:143]
ds_read_u16_d16_hi v155,v155
v_fma_mix_f32 v157,v157,v123,0 op_sel_hi:[0,1,0]
v_fma_mix_f32 v159,v159,v123,0 op_sel:[0,1,0] op_sel_hi:[0,1,0]
v_mfma_f32_16x16x32_bf16 v[132:135],v[84:87],v[32:35],v[132:135]
v_lshlrev_b32_sdwa v128,1,v128 dst_sel:DWORD dst_unused:UNUSED_PAD src0_sel:DWORD src1_sel:WORD_1
v_lshlrev_b32_sdwa v130,1,v130 dst_sel:DWORD dst_unused:UNUSED_PAD src0_sel:DWORD src1_sel:WORD_1
v_mfma_f32_16x16x32_bf16 v[208:211],v[88:91],v[144:147],v[208:211]
ds_read_u16 v128,v128
v_lshlrev_b32_sdwa v129,1,v129 dst_sel:DWORD dst_unused:UNUSED_PAD src0_sel:DWORD src1_sel:WORD_1
v_lshlrev_b32_sdwa v131,1,v131 dst_sel:DWORD dst_unused:UNUSED_PAD src0_sel:DWORD src1_sel:WORD_1
v_mfma_f32_16x16x32_bf16 v[216:219],v[92:95],v[144:147],v[216:219]
v_fma_mixlo_f16 v120,v140,s99,0
v_fma_mixlo_f16 v122,v141,s99,0
v_mfma_f32_16x16x32_bf16 v[224:227],v[96:99],v[144:147],v[224:227]
s_cbranch_vccnz RA16_%=
s_mov_b64 exec,s[62:63]
s_add_u32 m0,s57,10464
s_add_u32 s64,s56,4227072
buffer_load_dwordx4 v2,s[36:39],s64 offen lds
s_add_u32 m0,s57,12192
s_nop 0
buffer_load_dwordx4 v3,s[36:39],s64 offen lds
s_mov_b64 exec,-1
RA16_%=:
ds_read_u16_d16_hi v130,v130
v_lshlrev_b32_sdwa v132,1,v132 dst_sel:DWORD dst_unused:UNUSED_PAD src0_sel:DWORD src1_sel:WORD_1
v_lshlrev_b32_sdwa v134,1,v134 dst_sel:DWORD dst_unused:UNUSED_PAD src0_sel:DWORD src1_sel:WORD_1
v_mfma_f32_16x16x32_bf16 v[232:235],v[100:103],v[144:147],v[232:235]
s_waitcnt lgkmcnt(2)
v_lshl_add_u64 v[152:153],v[152:153],0,v[154:155]
v_mfma_f32_16x16x32_bf16 v[240:243],v[104:107],v[144:147],v[240:243]
ds_read_u16 v129,v129
v_cvt_pk_bf16_f32 v154,v156,v158
v_cvt_pk_bf16_f32 v155,v157,v159
v_mfma_f32_16x16x32_bf16 v[248:251],v[108:111],v[144:147],v[248:251]
v_fma_mixhi_f16 v120,v142,s99,0
v_fma_mixhi_f16 v122,v143,s99,0
v_mfma_f32_16x16x32_bf16 v[212:215],v[88:91],v[152:155],v[212:215]
s_setprio 2
ds_read_u16_d16_hi v131,v131
v_lshlrev_b32_sdwa v133,1,v133 dst_sel:DWORD dst_unused:UNUSED_PAD src0_sel:DWORD src1_sel:WORD_1
v_lshlrev_b32_sdwa v135,1,v135 dst_sel:DWORD dst_unused:UNUSED_PAD src0_sel:DWORD src1_sel:WORD_1
v_mfma_f32_16x16x32_bf16 v[220:223],v[92:95],v[152:155],v[220:223]
ds_read_b64_tr_b16 v[88:89],v6 offset:6160
v_pk_fma_f16 v121,v120,v120,s54
v_pk_fma_f16 v123,v122,v122,s54
v_mfma_f32_16x16x32_bf16 v[228:231],v[96:99],v[152:155],v[228:231]
ds_read_u16 v132,v132
v_pk_mul_f16 v121,v120,v121
v_pk_mul_f16 v123,v122,v123
v_mfma_f32_16x16x32_bf16 v[236:239],v[100:103],v[152:155],v[236:239]
ds_read_b64_tr_b16 v[90:91],v6 offset:7696
v_lshlrev_b32_sdwa v136,1,v136 dst_sel:DWORD dst_unused:UNUSED_PAD src0_sel:DWORD src1_sel:WORD_1
v_lshlrev_b32_sdwa v138,1,v138 dst_sel:DWORD dst_unused:UNUSED_PAD src0_sel:DWORD src1_sel:WORD_1
v_mfma_f32_16x16x32_bf16 v[244:247],v[104:107],v[152:155],v[244:247]
ds_read_u16_d16_hi v134,v134
v_exp_f16 v120,v121
v_mfma_f32_16x16x32_bf16 v[252:255],v[108:111],v[152:155],v[252:255]
ds_read_b64_tr_b16 v[92:93],v6 offset:6168
v_exp_f16 v122,v123
v_mfma_f32_16x16x32_bf16 v[144:147],v[64:67],v[40:43],v[112:115]
ds_read_u16 v133,v133
v_lshlrev_b32_sdwa v137,1,v137 dst_sel:DWORD dst_unused:UNUSED_PAD src0_sel:DWORD src1_sel:WORD_1
v_lshlrev_b32_sdwa v139,1,v139 dst_sel:DWORD dst_unused:UNUSED_PAD src0_sel:DWORD src1_sel:WORD_1
v_mfma_f32_16x16x32_bf16 v[152:155],v[64:67],v[44:47],v[112:115]
ds_read_b64_tr_b16 v[94:95],v6 offset:7704
v_exp_f16_sdwa v120,v121 dst_sel:WORD_1 dst_unused:UNUSED_PRESERVE src0_sel:WORD_1
v_mfma_f32_16x16x32_bf16 v[156:159],v[68:71],v[44:47],v[116:119]
ds_read_u16_d16_hi v135,v135
v_exp_f16_sdwa v122,v123 dst_sel:WORD_1 dst_unused:UNUSED_PRESERVE src0_sel:WORD_1
v_mfma_f32_16x16x32_bf16 v[148:151],v[68:71],v[40:43],v[116:119]
ds_read_b64_tr_b16 v[96:97],v6 offset:6224
v_pk_fma_f16 v120,v120,s55,s55
v_pk_fma_f16 v122,v122,s55,s55
v_mfma_f32_16x16x32_bf16 v[144:147],v[72:75],v[48:51],v[144:147]
ds_read_u16 v136,v136
v_rcp_f16 v121,v120
v_mfma_f32_16x16x32_bf16 v[152:155],v[72:75],v[52:55],v[152:155]
ds_read_b64_tr_b16 v[98:99],v6 offset:7760
ds_read_b64_tr_b16 v[100:101],v6 offset:6232
v_rcp_f16 v123,v122
v_mfma_f32_16x16x32_bf16 v[156:159],v[76:79],v[52:55],v[156:159]
ds_read_u16_d16_hi v138,v138
v_rcp_f16_sdwa v121,v120 dst_sel:WORD_1 dst_unused:UNUSED_PRESERVE src0_sel:WORD_1
v_mfma_f32_16x16x32_bf16 v[148:151],v[76:79],v[48:51],v[148:151]
s_cbranch_vccnz RA17_%=
s_mov_b64 exec,s[62:63]
s_add_u32 m0,s57,13952
s_add_u32 s64,s56,4325376
buffer_load_dwordx4 v2,s[36:39],s64 offen lds
s_add_u32 m0,s57,15680
s_nop 0
buffer_load_dwordx4 v3,s[36:39],s64 offen lds
s_mov_b64 exec,-1
RA17_%=:
ds_read_b64_tr_b16 v[102:103],v6 offset:7768
ds_read_b64_tr_b16 v[104:105],v6 offset:6288
v_rcp_f16_sdwa v123,v122 dst_sel:WORD_1 dst_unused:UNUSED_PRESERVE src0_sel:WORD_1
v_mfma_f32_16x16x32_bf16 v[144:147],v[80:83],v[56:59],v[144:147]
ds_read_u16 v137,v137
v_fma_mix_f32 v140,v140,v121,0 op_sel_hi:[0,1,0]
v_fma_mix_f32 v142,v142,v121,0 op_sel:[0,1,0] op_sel_hi:[0,1,0]
v_mfma_f32_16x16x32_bf16 v[152:155],v[80:83],v[60:63],v[152:155]
s_waitcnt lgkmcnt(8)
ds_read_b64_tr_b16 v[106:107],v6 offset:7824
ds_read_b64_tr_b16 v[108:109],v6 offset:6296
v_lshl_add_u64 v[128:129],v[128:129],0,v[130:131]
v_lshl_add_u64 v[130:131],v[132:133],0,v[134:135]
v_mfma_f32_16x16x32_bf16 v[156:159],v[84:87],v[60:63],v[156:159]
ds_read_u16_d16_hi v139,v139
v_fma_mix_f32 v141,v141,v123,0 op_sel_hi:[0,1,0]
v_fma_mix_f32 v143,v143,v123,0 op_sel:[0,1,0] op_sel_hi:[0,1,0]
v_mfma_f32_16x16x32_bf16 v[148:151],v[84:87],v[56:59],v[148:151]
ds_read_b64_tr_b16 v[110:111],v6 offset:7832
ds_read_b64_tr_b16 v[64:65],v4 offset:512
v_lshlrev_b32_sdwa v144,1,v144 dst_sel:DWORD dst_unused:UNUSED_PAD src0_sel:DWORD src1_sel:WORD_1
v_lshlrev_b32_sdwa v146,1,v146 dst_sel:DWORD dst_unused:UNUSED_PAD src0_sel:DWORD src1_sel:WORD_1
v_mfma_f32_16x16x32_bf16 v[160:163],v[88:91],v[128:131],v[160:163]
ds_read_u16 v144,v144
v_lshlrev_b32_sdwa v145,1,v145 dst_sel:DWORD dst_unused:UNUSED_PAD src0_sel:DWORD src1_sel:WORD_1
v_lshlrev_b32_sdwa v147,1,v147 dst_sel:DWORD dst_unused:UNUSED_PAD src0_sel:DWORD src1_sel:WORD_1
v_mfma_f32_16x16x32_bf16 v[168:171],v[92:95],v[128:131],v[168:171]
s_waitcnt lgkmcnt(11)
ds_read_b64_tr_b16 v[66:67],v4 offset:2560
ds_read_b128 v[112:115],v7 offset:1408
v_fma_mixlo_f16 v120,v156,s99,0
v_fma_mixlo_f16 v122,v157,s99,0
v_mfma_f32_16x16x32_bf16 v[176:179],v[96:99],v[128:131],v[176:179]
s_waitcnt lgkmcnt(10)
ds_read_u16_d16_hi v146,v146
v_lshlrev_b32_sdwa v148,1,v148 dst_sel:DWORD dst_unused:UNUSED_PAD src0_sel:DWORD src1_sel:WORD_1
v_lshlrev_b32_sdwa v150,1,v150 dst_sel:DWORD dst_unused:UNUSED_PAD src0_sel:DWORD src1_sel:WORD_1
v_mfma_f32_16x16x32_bf16 v[184:187],v[100:103],v[128:131],v[184:187]
s_waitcnt lgkmcnt(6)
ds_read_b64_tr_b16 v[68:69],v5 offset:512
ds_read_b64_tr_b16 v[70:71],v5 offset:2560
v_lshl_add_u64 v[136:137],v[136:137],0,v[138:139]
v_mfma_f32_16x16x32_bf16 v[192:195],v[104:107],v[128:131],v[192:195]
s_waitcnt lgkmcnt(7)
ds_read_u16 v145,v145
v_cvt_pk_bf16_f32 v138,v140,v142
v_cvt_pk_bf16_f32 v139,v141,v143
v_mfma_f32_16x16x32_bf16 v[200:203],v[108:111],v[128:131],v[200:203]
ds_read_b128 v[116:119],v7 offset:1440
ds_read_b64_tr_b16 v[72:73],v4 offset:4608
v_fma_mixhi_f16 v120,v158,s99,0
v_fma_mixhi_f16 v122,v159,s99,0
v_mfma_f32_16x16x32_bf16 v[164:167],v[88:91],v[136:139],v[164:167]
ds_read_u16_d16_hi v147,v147
v_lshlrev_b32_sdwa v149,1,v149 dst_sel:DWORD dst_unused:UNUSED_PAD src0_sel:DWORD src1_sel:WORD_1
v_lshlrev_b32_sdwa v151,1,v151 dst_sel:DWORD dst_unused:UNUSED_PAD src0_sel:DWORD src1_sel:WORD_1
v_mfma_f32_16x16x32_bf16 v[172:175],v[92:95],v[136:139],v[172:175]
s_setprio 1
ds_read_b64_tr_b16 v[74:75],v4 offset:6656
ds_read_b64_tr_b16 v[76:77],v5 offset:4608
v_pk_fma_f16 v121,v120,v120,s54
v_pk_fma_f16 v123,v122,v122,s54
v_mfma_f32_16x16x32_bf16 v[180:183],v[96:99],v[136:139],v[180:183]
ds_read_u16 v148,v148
v_pk_mul_f16 v121,v120,v121
v_pk_mul_f16 v123,v122,v123
v_mfma_f32_16x16x32_bf16 v[188:191],v[100:103],v[136:139],v[188:191]
ds_read_b64_tr_b16 v[78:79],v5 offset:6656
v_lshlrev_b32_sdwa v152,1,v152 dst_sel:DWORD dst_unused:UNUSED_PAD src0_sel:DWORD src1_sel:WORD_1
v_lshlrev_b32_sdwa v154,1,v154 dst_sel:DWORD dst_unused:UNUSED_PAD src0_sel:DWORD src1_sel:WORD_1
v_mfma_f32_16x16x32_bf16 v[196:199],v[104:107],v[136:139],v[196:199]
ds_read_u16_d16_hi v150,v150
v_exp_f16 v120,v121
v_mfma_f32_16x16x32_bf16 v[204:207],v[108:111],v[136:139],v[204:207]
s_waitcnt lgkmcnt(12)
ds_read_b64_tr_b16 v[80:81],v4 offset:8704
v_exp_f16 v122,v123
v_mfma_f32_16x16x32_bf16 v[128:131],v[64:67],v[16:19],v[112:115]
s_cbranch_vccnz RA18_%=
s_mov_b64 exec,s[62:63]
s_add_u32 m0,s57,17440
s_add_u32 s64,s56,4423680
buffer_load_dwordx4 v2,s[36:39],s64 offen lds
s_add_u32 m0,s57,19168
s_nop 0
buffer_load_dwordx4 v3,s[36:39],s64 offen lds
s_mov_b64 exec,-1
RA18_%=:
ds_read_u16 v149,v149
v_lshlrev_b32_sdwa v153,1,v153 dst_sel:DWORD dst_unused:UNUSED_PAD src0_sel:DWORD src1_sel:WORD_1
v_lshlrev_b32_sdwa v155,1,v155 dst_sel:DWORD dst_unused:UNUSED_PAD src0_sel:DWORD src1_sel:WORD_1
v_mfma_f32_16x16x32_bf16 v[136:139],v[64:67],v[20:23],v[112:115]
s_waitcnt lgkmcnt(9)
ds_read_b64_tr_b16 v[82:83],v4 offset:10752
v_exp_f16_sdwa v120,v121 dst_sel:WORD_1 dst_unused:UNUSED_PRESERVE src0_sel:WORD_1
v_mfma_f32_16x16x32_bf16 v[140:143],v[68:71],v[20:23],v[116:119]
ds_read_u16_d16_hi v151,v151
v_exp_f16_sdwa v122,v123 dst_sel:WORD_1 dst_unused:UNUSED_PRESERVE src0_sel:WORD_1
v_mfma_f32_16x16x32_bf16 v[132:135],v[68:71],v[16:19],v[116:119]
s_waitcnt lgkmcnt(8)
ds_read_b64_tr_b16 v[84:85],v5 offset:8704
v_pk_fma_f16 v120,v120,s55,s55
v_pk_fma_f16 v122,v122,s55,s55
v_mfma_f32_16x16x32_bf16 v[128:131],v[72:75],v[24:27],v[128:131]
ds_read_u16 v152,v152
v_rcp_f16 v121,v120
v_mfma_f32_16x16x32_bf16 v[136:139],v[72:75],v[28:31],v[136:139]
s_waitcnt lgkmcnt(7)
ds_read_b64_tr_b16 v[86:87],v5 offset:10752
v_rcp_f16 v123,v122
v_mfma_f32_16x16x32_bf16 v[140:143],v[76:79],v[28:31],v[140:143]
ds_read_u16_d16_hi v154,v154
v_rcp_f16_sdwa v121,v120 dst_sel:WORD_1 dst_unused:UNUSED_PRESERVE src0_sel:WORD_1
v_mfma_f32_16x16x32_bf16 v[132:135],v[76:79],v[24:27],v[132:135]
s_waitcnt lgkmcnt(5)
v_rcp_f16_sdwa v123,v122 dst_sel:WORD_1 dst_unused:UNUSED_PRESERVE src0_sel:WORD_1
v_mfma_f32_16x16x32_bf16 v[128:131],v[80:83],v[32:35],v[128:131]
ds_read_u16 v153,v153
v_fma_mix_f32 v156,v156,v121,0 op_sel_hi:[0,1,0]
v_fma_mix_f32 v158,v158,v121,0 op_sel:[0,1,0] op_sel_hi:[0,1,0]
v_mfma_f32_16x16x32_bf16 v[136:139],v[80:83],v[36:39],v[136:139]
s_waitcnt lgkmcnt(2)
s_setprio 0
v_lshl_add_u64 v[144:145],v[144:145],0,v[146:147]
v_lshl_add_u64 v[146:147],v[148:149],0,v[150:151]
v_mfma_f32_16x16x32_bf16 v[140:143],v[84:87],v[36:39],v[140:143]
ds_read_u16_d16_hi v155,v155
v_fma_mix_f32 v157,v157,v123,0 op_sel_hi:[0,1,0]
v_fma_mix_f32 v159,v159,v123,0 op_sel:[0,1,0] op_sel_hi:[0,1,0]
v_mfma_f32_16x16x32_bf16 v[132:135],v[84:87],v[32:35],v[132:135]
v_lshlrev_b32_sdwa v128,1,v128 dst_sel:DWORD dst_unused:UNUSED_PAD src0_sel:DWORD src1_sel:WORD_1
v_lshlrev_b32_sdwa v130,1,v130 dst_sel:DWORD dst_unused:UNUSED_PAD src0_sel:DWORD src1_sel:WORD_1
v_mfma_f32_16x16x32_bf16 v[208:211],v[88:91],v[144:147],v[208:211]
s_cbranch_vccz RA19_%=
s_waitcnt vmcnt(0)
RA19_%=:
s_barrier
s_setprio 3
ds_read_u16 v128,v128
v_lshlrev_b32_sdwa v129,1,v129 dst_sel:DWORD dst_unused:UNUSED_PAD src0_sel:DWORD src1_sel:WORD_1
v_lshlrev_b32_sdwa v131,1,v131 dst_sel:DWORD dst_unused:UNUSED_PAD src0_sel:DWORD src1_sel:WORD_1
v_mfma_f32_16x16x32_bf16 v[216:219],v[92:95],v[144:147],v[216:219]
v_fma_mixlo_f16 v120,v140,s99,0
v_fma_mixlo_f16 v122,v141,s99,0
v_mfma_f32_16x16x32_bf16 v[224:227],v[96:99],v[144:147],v[224:227]
s_cbranch_vccz RA20_%=
s_add_u32 m0,s58,0
s_mul_i32 s69,s59,1
s_add_u32 s69,s69,s97
buffer_load_dwordx4 v2,s[44:47],s69 offen lds
s_add_u32 m0,m0,1024
s_add_u32 s69,s69,s53
buffer_load_dwordx4 v3,s[44:47],s69 offen lds
RA20_%=:
ds_read_u16_d16_hi v130,v130
v_lshlrev_b32_sdwa v132,1,v132 dst_sel:DWORD dst_unused:UNUSED_PAD src0_sel:DWORD src1_sel:WORD_1
v_lshlrev_b32_sdwa v134,1,v134 dst_sel:DWORD dst_unused:UNUSED_PAD src0_sel:DWORD src1_sel:WORD_1
v_mfma_f32_16x16x32_bf16 v[232:235],v[100:103],v[144:147],v[232:235]
s_waitcnt lgkmcnt(2)
v_lshl_add_u64 v[152:153],v[152:153],0,v[154:155]
v_mfma_f32_16x16x32_bf16 v[240:243],v[104:107],v[144:147],v[240:243]
ds_read_u16 v129,v129
v_cvt_pk_bf16_f32 v154,v156,v158
v_cvt_pk_bf16_f32 v155,v157,v159
v_mfma_f32_16x16x32_bf16 v[248:251],v[108:111],v[144:147],v[248:251]
s_cbranch_vccnz RA21_%=
s_mov_b64 exec,s[62:63]
s_add_u32 m0,s57,20928
s_add_u32 s64,s56,4521984
buffer_load_dwordx4 v2,s[36:39],s64 offen lds
s_add_u32 m0,s57,22656
s_nop 0
buffer_load_dwordx4 v3,s[36:39],s64 offen lds
s_mov_b64 exec,-1
RA21_%=:
v_fma_mixhi_f16 v120,v142,s99,0
v_fma_mixhi_f16 v122,v143,s99,0
v_mfma_f32_16x16x32_bf16 v[212:215],v[88:91],v[152:155],v[212:215]
s_cbranch_vccz RA22_%=
s_add_u32 m0,m0,1024
s_add_u32 s69,s69,s53
buffer_load_dwordx4 v2,s[44:47],s69 offen lds
s_add_u32 m0,m0,s50
s_add_u32 s69,s69,s53
buffer_load_dwordx4 v3,s[44:47],s69 offen lds
RA22_%=:
ds_read_u16_d16_hi v131,v131
v_lshlrev_b32_sdwa v133,1,v133 dst_sel:DWORD dst_unused:UNUSED_PAD src0_sel:DWORD src1_sel:WORD_1
v_lshlrev_b32_sdwa v135,1,v135 dst_sel:DWORD dst_unused:UNUSED_PAD src0_sel:DWORD src1_sel:WORD_1
v_mfma_f32_16x16x32_bf16 v[220:223],v[92:95],v[152:155],v[220:223]
ds_read_b64_tr_b16 v[88:89],v6 offset:12320
v_pk_fma_f16 v121,v120,v120,s54
s_mov_b32 s86,0xbe801d5a
v_pk_fma_f16 v123,v122,v122,s54
v_mfma_f32_16x16x32_bf16 v[228:231],v[96:99],v[152:155],v[228:231]
ds_read_u16 v132,v132
v_pk_mul_f16 v121,v120,v121
v_pk_mul_f16 v123,v122,v123
v_mfma_f32_16x16x32_bf16 v[236:239],v[100:103],v[152:155],v[236:239]
s_cbranch_vccz RA23_%=
s_add_u32 m0,m0,1024
s_add_u32 s69,s69,s53
buffer_load_dwordx4 v2,s[44:47],s69 offen lds
s_add_u32 m0,m0,1024
s_add_u32 s69,s69,s53
buffer_load_dwordx4 v3,s[44:47],s69 offen lds
RA23_%=:
ds_read_b64_tr_b16 v[90:91],v6 offset:13856
v_lshlrev_b32_sdwa v136,1,v136 dst_sel:DWORD dst_unused:UNUSED_PAD src0_sel:DWORD src1_sel:WORD_1
v_lshlrev_b32_sdwa v138,1,v138 dst_sel:DWORD dst_unused:UNUSED_PAD src0_sel:DWORD src1_sel:WORD_1
v_mfma_f32_16x16x32_bf16 v[244:247],v[104:107],v[152:155],v[244:247]
ds_read_u16_d16_hi v134,v134
v_exp_f16 v120,v121
v_mfma_f32_16x16x32_bf16 v[252:255],v[108:111],v[152:155],v[252:255]
ds_read_b64_tr_b16 v[92:93],v6 offset:12328
v_exp_f16 v122,v123
v_mfma_f32_16x16x32_bf16 v[144:147],v[64:67],v[40:43],v[112:115]
ds_read_u16 v133,v133
v_lshlrev_b32_sdwa v137,1,v137 dst_sel:DWORD dst_unused:UNUSED_PAD src0_sel:DWORD src1_sel:WORD_1
v_lshlrev_b32_sdwa v139,1,v139 dst_sel:DWORD dst_unused:UNUSED_PAD src0_sel:DWORD src1_sel:WORD_1
v_mfma_f32_16x16x32_bf16 v[152:155],v[64:67],v[44:47],v[112:115]
s_cbranch_vccz RA24_%=
s_add_u32 m0,m0,s50
s_add_u32 s69,s69,s53
buffer_load_dwordx4 v2,s[44:47],s69 offen lds
s_add_u32 m0,m0,1024
s_add_u32 s69,s69,s53
buffer_load_dwordx4 v3,s[44:47],s69 offen lds
RA24_%=:
ds_read_b64_tr_b16 v[94:95],v6 offset:13864
v_exp_f16_sdwa v120,v121 dst_sel:WORD_1 dst_unused:UNUSED_PRESERVE src0_sel:WORD_1
v_mfma_f32_16x16x32_bf16 v[156:159],v[68:71],v[44:47],v[116:119]
ds_read_u16_d16_hi v135,v135
v_exp_f16_sdwa v122,v123 dst_sel:WORD_1 dst_unused:UNUSED_PRESERVE src0_sel:WORD_1
v_mfma_f32_16x16x32_bf16 v[148:151],v[68:71],v[40:43],v[116:119]
ds_read_b64_tr_b16 v[96:97],v6 offset:12384
v_pk_fma_f16 v120,v120,s55,s55
v_pk_fma_f16 v122,v122,s55,s55
v_mfma_f32_16x16x32_bf16 v[144:147],v[72:75],v[48:51],v[144:147]
ds_read_u16 v136,v136
v_rcp_f16 v121,v120
v_mfma_f32_16x16x32_bf16 v[152:155],v[72:75],v[52:55],v[152:155]
s_cbranch_vccz RA25_%=
s_add_u32 m0,m0,1024
s_add_u32 s69,s69,s53
buffer_load_dwordx4 v2,s[44:47],s69 offen lds
s_add_u32 m0,m0,s50
s_add_u32 s69,s69,s53
buffer_load_dwordx4 v3,s[44:47],s69 offen lds
RA25_%=:
ds_read_b64_tr_b16 v[98:99],v6 offset:13920
ds_read_b64_tr_b16 v[100:101],v6 offset:12392
v_rcp_f16 v123,v122
v_mfma_f32_16x16x32_bf16 v[156:159],v[76:79],v[52:55],v[156:159]
ds_read_u16_d16_hi v138,v138
v_rcp_f16_sdwa v121,v120 dst_sel:WORD_1 dst_unused:UNUSED_PRESERVE src0_sel:WORD_1
v_mfma_f32_16x16x32_bf16 v[148:151],v[76:79],v[48:51],v[148:151]
ds_read_b64_tr_b16 v[102:103],v6 offset:13928
ds_read_b64_tr_b16 v[104:105],v6 offset:12448
v_rcp_f16_sdwa v123,v122 dst_sel:WORD_1 dst_unused:UNUSED_PRESERVE src0_sel:WORD_1
v_mfma_f32_16x16x32_bf16 v[144:147],v[80:83],v[56:59],v[144:147]
ds_read_u16 v137,v137
v_fma_mix_f32 v140,v140,v121,0 op_sel_hi:[0,1,0]
v_fma_mix_f32 v142,v142,v121,0 op_sel:[0,1,0] op_sel_hi:[0,1,0]
v_mfma_f32_16x16x32_bf16 v[152:155],v[80:83],v[60:63],v[152:155]
s_waitcnt lgkmcnt(8)
s_cbranch_vccz RA26_%=
s_add_u32 m0,m0,1024
s_add_u32 s69,s69,s53
buffer_load_dwordx4 v2,s[44:47],s69 offen lds
s_add_u32 m0,m0,1024
s_add_u32 s69,s69,s53
buffer_load_dwordx4 v3,s[44:47],s69 offen lds
RA26_%=:
ds_read_b64_tr_b16 v[106:107],v6 offset:13984
ds_read_b64_tr_b16 v[108:109],v6 offset:12456
v_lshl_add_u64 v[128:129],v[128:129],0,v[130:131]
v_lshl_add_u64 v[130:131],v[132:133],0,v[134:135]
v_mfma_f32_16x16x32_bf16 v[156:159],v[84:87],v[60:63],v[156:159]
s_cbranch_vccnz RA27_%=
s_mov_b64 exec,s[62:63]
s_add_u32 m0,s57,24416
s_add_u32 s64,s56,4620288
buffer_load_dwordx4 v2,s[36:39],s64 offen lds
s_add_u32 m0,s57,26144
s_nop 0
buffer_load_dwordx4 v3,s[36:39],s64 offen lds
s_mov_b64 exec,-1
RA27_%=:
ds_read_u16_d16_hi v139,v139
v_fma_mix_f32 v141,v141,v123,0 op_sel_hi:[0,1,0]
v_fma_mix_f32 v143,v143,v123,0 op_sel:[0,1,0] op_sel_hi:[0,1,0]
v_mfma_f32_16x16x32_bf16 v[148:151],v[84:87],v[56:59],v[148:151]
ds_read_b64_tr_b16 v[110:111],v6 offset:13992
ds_read_b64_tr_b16 v[64:65],v4 offset:12320
v_lshlrev_b32_sdwa v144,1,v144 dst_sel:DWORD dst_unused:UNUSED_PAD src0_sel:DWORD src1_sel:WORD_1
v_lshlrev_b32_sdwa v146,1,v146 dst_sel:DWORD dst_unused:UNUSED_PAD src0_sel:DWORD src1_sel:WORD_1
v_mfma_f32_16x16x32_bf16 v[160:163],v[88:91],v[128:131],v[160:163]
ds_read_u16 v144,v144
v_lshlrev_b32_sdwa v145,1,v145 dst_sel:DWORD dst_unused:UNUSED_PAD src0_sel:DWORD src1_sel:WORD_1
v_lshlrev_b32_sdwa v147,1,v147 dst_sel:DWORD dst_unused:UNUSED_PAD src0_sel:DWORD src1_sel:WORD_1
v_mfma_f32_16x16x32_bf16 v[168:171],v[92:95],v[128:131],v[168:171]
s_waitcnt lgkmcnt(11)
ds_read_b64_tr_b16 v[66:67],v4 offset:14368
ds_read_b128 v[112:115],v7 offset:0
v_fma_mixlo_f16 v120,v156,s99,0
v_fma_mixlo_f16 v122,v157,s99,0
v_mfma_f32_16x16x32_bf16 v[176:179],v[96:99],v[128:131],v[176:179]
s_waitcnt lgkmcnt(10)
ds_read_u16_d16_hi v146,v146
v_lshlrev_b32_sdwa v148,1,v148 dst_sel:DWORD dst_unused:UNUSED_PAD src0_sel:DWORD src1_sel:WORD_1
v_lshlrev_b32_sdwa v150,1,v150 dst_sel:DWORD dst_unused:UNUSED_PAD src0_sel:DWORD src1_sel:WORD_1
v_mfma_f32_16x16x32_bf16 v[184:187],v[100:103],v[128:131],v[184:187]
s_waitcnt lgkmcnt(6)
ds_read_b64_tr_b16 v[68:69],v5 offset:12320
ds_read_b64_tr_b16 v[70:71],v5 offset:14368
v_lshl_add_u64 v[136:137],v[136:137],0,v[138:139]
v_mfma_f32_16x16x32_bf16 v[192:195],v[104:107],v[128:131],v[192:195]
s_waitcnt lgkmcnt(7)
ds_read_u16 v145,v145
v_cvt_pk_bf16_f32 v138,v140,v142
v_cvt_pk_bf16_f32 v139,v141,v143
v_mfma_f32_16x16x32_bf16 v[200:203],v[108:111],v[128:131],v[200:203]
ds_read_b128 v[116:119],v7 offset:32
ds_read_b64_tr_b16 v[72:73],v4 offset:16416
v_fma_mixhi_f16 v120,v158,s99,0
v_fma_mixhi_f16 v122,v159,s99,0
v_mfma_f32_16x16x32_bf16 v[164:167],v[88:91],v[136:139],v[164:167]
ds_read_u16_d16_hi v147,v147
v_lshlrev_b32_sdwa v149,1,v149 dst_sel:DWORD dst_unused:UNUSED_PAD src0_sel:DWORD src1_sel:WORD_1
v_lshlrev_b32_sdwa v151,1,v151 dst_sel:DWORD dst_unused:UNUSED_PAD src0_sel:DWORD src1_sel:WORD_1
v_mfma_f32_16x16x32_bf16 v[172:175],v[92:95],v[136:139],v[172:175]
ds_read_b64_tr_b16 v[74:75],v4 offset:18464
ds_read_b64_tr_b16 v[76:77],v5 offset:16416
v_pk_fma_f16 v121,v120,v120,s54
v_pk_fma_f16 v123,v122,v122,s54
v_mfma_f32_16x16x32_bf16 v[180:183],v[96:99],v[136:139],v[180:183]
ds_read_u16 v148,v148
v_pk_mul_f16 v121,v120,v121
v_pk_mul_f16 v123,v122,v123
v_mfma_f32_16x16x32_bf16 v[188:191],v[100:103],v[136:139],v[188:191]
ds_read_b64_tr_b16 v[78:79],v5 offset:18464
v_lshlrev_b32_sdwa v152,1,v152 dst_sel:DWORD dst_unused:UNUSED_PAD src0_sel:DWORD src1_sel:WORD_1
v_lshlrev_b32_sdwa v154,1,v154 dst_sel:DWORD dst_unused:UNUSED_PAD src0_sel:DWORD src1_sel:WORD_1
v_mfma_f32_16x16x32_bf16 v[196:199],v[104:107],v[136:139],v[196:199]
ds_read_u16_d16_hi v150,v150
v_exp_f16 v120,v121
v_mfma_f32_16x16x32_bf16 v[204:207],v[108:111],v[136:139],v[204:207]
s_waitcnt lgkmcnt(12)
ds_read_b64_tr_b16 v[80:81],v4 offset:20512
v_exp_f16 v122,v123
v_mfma_f32_16x16x32_bf16 v[128:131],v[64:67],v[16:19],v[112:115]
ds_read_u16 v149,v149
v_lshlrev_b32_sdwa v153,1,v153 dst_sel:DWORD dst_unused:UNUSED_PAD src0_sel:DWORD src1_sel:WORD_1
v_lshlrev_b32_sdwa v155,1,v155 dst_sel:DWORD dst_unused:UNUSED_PAD src0_sel:DWORD src1_sel:WORD_1
v_mfma_f32_16x16x32_bf16 v[136:139],v[64:67],v[20:23],v[112:115]
s_waitcnt lgkmcnt(9)
ds_read_b64_tr_b16 v[82:83],v4 offset:22560
v_exp_f16_sdwa v120,v121 dst_sel:WORD_1 dst_unused:UNUSED_PRESERVE src0_sel:WORD_1
v_mfma_f32_16x16x32_bf16 v[140:143],v[68:71],v[20:23],v[116:119]
s_cbranch_vccnz RA28_%=
s_mov_b64 exec,s[62:63]
s_add_u32 m0,s57,27904
s_add_u32 s64,s56,4718592
buffer_load_dwordx4 v2,s[36:39],s64 offen lds
s_add_u32 m0,s57,29632
s_nop 0
buffer_load_dwordx4 v3,s[36:39],s64 offen lds
s_mov_b64 exec,-1
RA28_%=:
ds_read_u16_d16_hi v151,v151
v_exp_f16_sdwa v122,v123 dst_sel:WORD_1 dst_unused:UNUSED_PRESERVE src0_sel:WORD_1
v_mfma_f32_16x16x32_bf16 v[132:135],v[68:71],v[16:19],v[116:119]
s_waitcnt lgkmcnt(8)
ds_read_b64_tr_b16 v[84:85],v5 offset:20512
v_pk_fma_f16 v120,v120,s55,s55
v_pk_fma_f16 v122,v122,s55,s55
v_mfma_f32_16x16x32_bf16 v[128:131],v[72:75],v[24:27],v[128:131]
ds_read_u16 v152,v152
v_rcp_f16 v121,v120
v_mfma_f32_16x16x32_bf16 v[136:139],v[72:75],v[28:31],v[136:139]
s_waitcnt lgkmcnt(7)
ds_read_b64_tr_b16 v[86:87],v5 offset:22560
v_rcp_f16 v123,v122
v_mfma_f32_16x16x32_bf16 v[140:143],v[76:79],v[28:31],v[140:143]
ds_read_u16_d16_hi v154,v154
v_rcp_f16_sdwa v121,v120 dst_sel:WORD_1 dst_unused:UNUSED_PRESERVE src0_sel:WORD_1
v_mfma_f32_16x16x32_bf16 v[132:135],v[76:79],v[24:27],v[132:135]
s_waitcnt lgkmcnt(5)
v_rcp_f16_sdwa v123,v122 dst_sel:WORD_1 dst_unused:UNUSED_PRESERVE src0_sel:WORD_1
v_mfma_f32_16x16x32_bf16 v[128:131],v[80:83],v[32:35],v[128:131]
ds_read_u16 v153,v153
v_fma_mix_f32 v156,v156,v121,0 op_sel_hi:[0,1,0]
v_fma_mix_f32 v158,v158,v121,0 op_sel:[0,1,0] op_sel_hi:[0,1,0]
v_mfma_f32_16x16x32_bf16 v[136:139],v[80:83],v[36:39],v[136:139]
s_waitcnt lgkmcnt(2)
v_lshl_add_u64 v[144:145],v[144:145],0,v[146:147]
v_lshl_add_u64 v[146:147],v[148:149],0,v[150:151]
v_mfma_f32_16x16x32_bf16 v[140:143],v[84:87],v[36:39],v[140:143]
ds_read_u16_d16_hi v155,v155
v_fma_mix_f32 v157,v157,v123,0 op_sel_hi:[0,1,0]
v_fma_mix_f32 v159,v159,v123,0 op_sel:[0,1,0] op_sel_hi:[0,1,0]
v_mfma_f32_16x16x32_bf16 v[132:135],v[84:87],v[32:35],v[132:135]
v_lshlrev_b32_sdwa v128,1,v128 dst_sel:DWORD dst_unused:UNUSED_PAD src0_sel:DWORD src1_sel:WORD_1
v_lshlrev_b32_sdwa v130,1,v130 dst_sel:DWORD dst_unused:UNUSED_PAD src0_sel:DWORD src1_sel:WORD_1
v_mfma_f32_16x16x32_bf16 v[208:211],v[88:91],v[144:147],v[208:211]
ds_read_u16 v128,v128
v_lshlrev_b32_sdwa v129,1,v129 dst_sel:DWORD dst_unused:UNUSED_PAD src0_sel:DWORD src1_sel:WORD_1
v_lshlrev_b32_sdwa v131,1,v131 dst_sel:DWORD dst_unused:UNUSED_PAD src0_sel:DWORD src1_sel:WORD_1
v_mfma_f32_16x16x32_bf16 v[216:219],v[92:95],v[144:147],v[216:219]
v_fma_mixlo_f16 v120,v140,s99,0
v_fma_mixlo_f16 v122,v141,s99,0
v_mfma_f32_16x16x32_bf16 v[224:227],v[96:99],v[144:147],v[224:227]
s_setprio 2
ds_read_u16_d16_hi v130,v130
v_lshlrev_b32_sdwa v132,1,v132 dst_sel:DWORD dst_unused:UNUSED_PAD src0_sel:DWORD src1_sel:WORD_1
v_lshlrev_b32_sdwa v134,1,v134 dst_sel:DWORD dst_unused:UNUSED_PAD src0_sel:DWORD src1_sel:WORD_1
v_mfma_f32_16x16x32_bf16 v[232:235],v[100:103],v[144:147],v[232:235]
s_waitcnt lgkmcnt(2)
v_lshl_add_u64 v[152:153],v[152:153],0,v[154:155]
v_mfma_f32_16x16x32_bf16 v[240:243],v[104:107],v[144:147],v[240:243]
ds_read_u16 v129,v129
v_cvt_pk_bf16_f32 v154,v156,v158
v_cvt_pk_bf16_f32 v155,v157,v159
v_mfma_f32_16x16x32_bf16 v[248:251],v[108:111],v[144:147],v[248:251]
v_fma_mixhi_f16 v120,v142,s99,0
v_fma_mixhi_f16 v122,v143,s99,0
v_mfma_f32_16x16x32_bf16 v[212:215],v[88:91],v[152:155],v[212:215]
ds_read_u16_d16_hi v131,v131
v_lshlrev_b32_sdwa v133,1,v133 dst_sel:DWORD dst_unused:UNUSED_PAD src0_sel:DWORD src1_sel:WORD_1
v_lshlrev_b32_sdwa v135,1,v135 dst_sel:DWORD dst_unused:UNUSED_PAD src0_sel:DWORD src1_sel:WORD_1
v_mfma_f32_16x16x32_bf16 v[220:223],v[92:95],v[152:155],v[220:223]
s_cbranch_vccnz RA29_%=
s_mov_b64 exec,s[62:63]
s_add_u32 m0,s57,31392
s_add_u32 s64,s56,4816896
buffer_load_dwordx4 v2,s[36:39],s64 offen lds
s_add_u32 m0,s57,33120
s_nop 0
buffer_load_dwordx4 v3,s[36:39],s64 offen lds
s_mov_b64 exec,-1
RA29_%=:
ds_read_b64_tr_b16 v[88:89],v6 offset:18480
v_pk_fma_f16 v121,v120,v120,s54
v_pk_fma_f16 v123,v122,v122,s54
v_mfma_f32_16x16x32_bf16 v[228:231],v[96:99],v[152:155],v[228:231]
ds_read_u16 v132,v132
v_pk_mul_f16 v121,v120,v121
v_pk_mul_f16 v123,v122,v123
v_mfma_f32_16x16x32_bf16 v[236:239],v[100:103],v[152:155],v[236:239]
ds_read_b64_tr_b16 v[90:91],v6 offset:20016
v_lshlrev_b32_sdwa v136,1,v136 dst_sel:DWORD dst_unused:UNUSED_PAD src0_sel:DWORD src1_sel:WORD_1
v_lshlrev_b32_sdwa v138,1,v138 dst_sel:DWORD dst_unused:UNUSED_PAD src0_sel:DWORD src1_sel:WORD_1
v_mfma_f32_16x16x32_bf16 v[244:247],v[104:107],v[152:155],v[244:247]
ds_read_u16_d16_hi v134,v134
v_exp_f16 v120,v121
v_mfma_f32_16x16x32_bf16 v[252:255],v[108:111],v[152:155],v[252:255]
ds_read_b64_tr_b16 v[92:93],v6 offset:18488
v_exp_f16 v122,v123
v_mfma_f32_16x16x32_bf16 v[144:147],v[64:67],v[40:43],v[112:115]
ds_read_u16 v133,v133
v_lshlrev_b32_sdwa v137,1,v137 dst_sel:DWORD dst_unused:UNUSED_PAD src0_sel:DWORD src1_sel:WORD_1
v_lshlrev_b32_sdwa v139,1,v139 dst_sel:DWORD dst_unused:UNUSED_PAD src0_sel:DWORD src1_sel:WORD_1
v_mfma_f32_16x16x32_bf16 v[152:155],v[64:67],v[44:47],v[112:115]
ds_read_b64_tr_b16 v[94:95],v6 offset:20024
v_exp_f16_sdwa v120,v121 dst_sel:WORD_1 dst_unused:UNUSED_PRESERVE src0_sel:WORD_1
v_mfma_f32_16x16x32_bf16 v[156:159],v[68:71],v[44:47],v[116:119]
ds_read_u16_d16_hi v135,v135
v_exp_f16_sdwa v122,v123 dst_sel:WORD_1 dst_unused:UNUSED_PRESERVE src0_sel:WORD_1
v_mfma_f32_16x16x32_bf16 v[148:151],v[68:71],v[40:43],v[116:119]
ds_read_b64_tr_b16 v[96:97],v6 offset:18544
v_pk_fma_f16 v120,v120,s55,s55
v_pk_fma_f16 v122,v122,s55,s55
v_mfma_f32_16x16x32_bf16 v[144:147],v[72:75],v[48:51],v[144:147]
ds_read_u16 v136,v136
v_rcp_f16 v121,v120
v_mfma_f32_16x16x32_bf16 v[152:155],v[72:75],v[52:55],v[152:155]
ds_read_b64_tr_b16 v[98:99],v6 offset:20080
ds_read_b64_tr_b16 v[100:101],v6 offset:18552
v_rcp_f16 v123,v122
v_mfma_f32_16x16x32_bf16 v[156:159],v[76:79],v[52:55],v[156:159]
ds_read_u16_d16_hi v138,v138
v_rcp_f16_sdwa v121,v120 dst_sel:WORD_1 dst_unused:UNUSED_PRESERVE src0_sel:WORD_1
v_mfma_f32_16x16x32_bf16 v[148:151],v[76:79],v[48:51],v[148:151]
ds_read_b64_tr_b16 v[102:103],v6 offset:20088
ds_read_b64_tr_b16 v[104:105],v6 offset:18608
v_rcp_f16_sdwa v123,v122 dst_sel:WORD_1 dst_unused:UNUSED_PRESERVE src0_sel:WORD_1
v_mfma_f32_16x16x32_bf16 v[144:147],v[80:83],v[56:59],v[144:147]
ds_read_u16 v137,v137
v_fma_mix_f32 v140,v140,v121,0 op_sel_hi:[0,1,0]
v_fma_mix_f32 v142,v142,v121,0 op_sel:[0,1,0] op_sel_hi:[0,1,0]
v_mfma_f32_16x16x32_bf16 v[152:155],v[80:83],v[60:63],v[152:155]
s_waitcnt lgkmcnt(8)
ds_read_b64_tr_b16 v[106:107],v6 offset:20144
ds_read_b64_tr_b16 v[108:109],v6 offset:18616
v_lshl_add_u64 v[128:129],v[128:129],0,v[130:131]
v_lshl_add_u64 v[130:131],v[132:133],0,v[134:135]
v_mfma_f32_16x16x32_bf16 v[156:159],v[84:87],v[60:63],v[156:159]
ds_read_u16_d16_hi v139,v139
v_fma_mix_f32 v141,v141,v123,0 op_sel_hi:[0,1,0]
v_fma_mix_f32 v143,v143,v123,0 op_sel:[0,1,0] op_sel_hi:[0,1,0]
v_mfma_f32_16x16x32_bf16 v[148:151],v[84:87],v[56:59],v[148:151]
ds_read_b64_tr_b16 v[110:111],v6 offset:20152
ds_read_b128 v[64:67],v7 offset:1536
v_lshlrev_b32_sdwa v144,1,v144 dst_sel:DWORD dst_unused:UNUSED_PAD src0_sel:DWORD src1_sel:WORD_1
v_lshlrev_b32_sdwa v146,1,v146 dst_sel:DWORD dst_unused:UNUSED_PAD src0_sel:DWORD src1_sel:WORD_1
v_mfma_f32_16x16x32_bf16 v[160:163],v[88:91],v[128:131],v[160:163]
ds_read_u16 v144,v144
ds_read_b128 v[68:71],v7 offset:1568
v_lshlrev_b32_sdwa v145,1,v145 dst_sel:DWORD dst_unused:UNUSED_PAD src0_sel:DWORD src1_sel:WORD_1
v_lshlrev_b32_sdwa v147,1,v147 dst_sel:DWORD dst_unused:UNUSED_PAD src0_sel:DWORD src1_sel:WORD_1
v_fma_mixlo_f16 v120,v156,s99,0
v_fma_mixlo_f16 v122,v157,s99,0
v_mfma_f32_16x16x32_bf16 v[168:171],v[92:95],v[128:131],v[168:171]
s_waitcnt lgkmcnt(12)
ds_read_u16_d16_hi v146,v146
ds_read_b128 v[72:75],v7 offset:1664
v_lshlrev_b32_sdwa v148,1,v148 dst_sel:DWORD dst_unused:UNUSED_PAD src0_sel:DWORD src1_sel:WORD_1
v_lshlrev_b32_sdwa v150,1,v150 dst_sel:DWORD dst_unused:UNUSED_PAD src0_sel:DWORD src1_sel:WORD_1
s_waitcnt lgkmcnt(6)
v_lshl_add_u64 v[136:137],v[136:137],0,v[138:139]
v_cvt_pk_bf16_f32 v138,v140,v142
v_mfma_f32_16x16x32_bf16 v[176:179],v[96:99],v[128:131],v[176:179]
s_setprio 1
ds_read_u16 v145,v145
ds_read_b128 v[76:79],v7 offset:1696
v_cvt_pk_bf16_f32 v139,v141,v143
v_fma_mixhi_f16 v120,v158,s99,0
v_fma_mixhi_f16 v122,v159,s99,0
v_lshlrev_b32_sdwa v149,1,v149 dst_sel:DWORD dst_unused:UNUSED_PAD src0_sel:DWORD src1_sel:WORD_1
v_mfma_f32_16x16x32_bf16 v[184:187],v[100:103],v[128:131],v[184:187]
ds_read_u16_d16_hi v147,v147
ds_read_b128 v[80:83],v7 offset:1792
v_lshlrev_b32_sdwa v151,1,v151 dst_sel:DWORD dst_unused:UNUSED_PAD src0_sel:DWORD src1_sel:WORD_1
v_pk_fma_f16 v121,v120,v120,s54
v_pk_fma_f16 v123,v122,v122,s54
v_pk_mul_f16 v121,v120,v121
v_mfma_f32_16x16x32_bf16 v[192:195],v[104:107],v[128:131],v[192:195]
s_waitcnt lgkmcnt(9)
ds_read_u16 v148,v148
ds_read_b128 v[84:87],v7 offset:1824
v_pk_mul_f16 v123,v122,v123
v_lshlrev_b32_sdwa v152,1,v152 dst_sel:DWORD dst_unused:UNUSED_PAD src0_sel:DWORD src1_sel:WORD_1
v_lshlrev_b32_sdwa v154,1,v154 dst_sel:DWORD dst_unused:UNUSED_PAD src0_sel:DWORD src1_sel:WORD_1
v_exp_f16 v120,v121
v_mfma_f32_16x16x32_bf16 v[200:203],v[108:111],v[128:131],v[200:203]
ds_read_u16_d16_hi v150,v150
v_exp_f16 v122,v123
v_lshlrev_b32_sdwa v153,1,v153 dst_sel:DWORD dst_unused:UNUSED_PAD src0_sel:DWORD src1_sel:WORD_1
v_mfma_f32_16x16x32_bf16 v[164:167],v[88:91],v[136:139],v[164:167]
ds_read_u16 v149,v149
v_lshlrev_b32_sdwa v155,1,v155 dst_sel:DWORD dst_unused:UNUSED_PAD src0_sel:DWORD src1_sel:WORD_1
v_exp_f16_sdwa v120,v121 dst_sel:WORD_1 dst_unused:UNUSED_PRESERVE src0_sel:WORD_1
v_exp_f16_sdwa v122,v123 dst_sel:WORD_1 dst_unused:UNUSED_PRESERVE src0_sel:WORD_1
v_mfma_f32_16x16x32_bf16 v[172:175],v[92:95],v[136:139],v[172:175]
ds_read_u16_d16_hi v151,v151
v_pk_fma_f16 v120,v120,s55,s55
v_pk_fma_f16 v122,v122,s55,s55
v_rcp_f16 v121,v120
v_mfma_f32_16x16x32_bf16 v[180:183],v[96:99],v[136:139],v[180:183]
ds_read_u16 v152,v152
v_rcp_f16 v123,v122
v_rcp_f16_sdwa v121,v120 dst_sel:WORD_1 dst_unused:UNUSED_PRESERVE src0_sel:WORD_1
v_mfma_f32_16x16x32_bf16 v[188:191],v[100:103],v[136:139],v[188:191]
ds_read_u16_d16_hi v154,v154
v_rcp_f16_sdwa v123,v122 dst_sel:WORD_1 dst_unused:UNUSED_PRESERVE src0_sel:WORD_1
v_fma_mix_f32 v156,v156,v121,0 op_sel_hi:[0,1,0]
v_mfma_f32_16x16x32_bf16 v[196:199],v[104:107],v[136:139],v[196:199]
s_setprio 0
ds_read_u16 v153,v153
v_fma_mix_f32 v158,v158,v121,0 op_sel:[0,1,0] op_sel_hi:[0,1,0]
s_waitcnt lgkmcnt(9)
v_lshl_add_u64 v[144:145],v[144:145],0,v[146:147]
s_waitcnt lgkmcnt(3)
v_lshl_add_u64 v[146:147],v[148:149],0,v[150:151]
v_fma_mix_f32 v157,v157,v123,0 op_sel_hi:[0,1,0]
v_mfma_f32_16x16x32_bf16 v[204:207],v[108:111],v[136:139],v[204:207]
ds_read_u16_d16_hi v155,v155
v_fma_mix_f32 v159,v159,v123,0 op_sel:[0,1,0] op_sel_hi:[0,1,0]
s_waitcnt lgkmcnt(0)
v_lshl_add_u64 v[152:153],v[152:153],0,v[154:155]
v_cvt_pk_bf16_f32 v154,v156,v158
v_cvt_pk_bf16_f32 v155,v157,v159
v_mfma_f32_16x16x32_bf16 v[208:211],v[88:91],v[144:147],v[208:211]
s_cbranch_vccz RA30_%=
s_waitcnt vmcnt(0)
RA30_%=:
s_barrier
s_setprio 0
v_xor_b32 v9,64,v10
v_mul_f32 v160,v160,v64
v_mul_f32 v161,v161,v65
v_mul_f32 v162,v162,v66
v_mul_f32 v163,v163,v67
v_mfma_f32_16x16x32_bf16 v[216:219],v[92:95],v[144:147],v[216:219]
v_mul_f32 v168,v168,v68
v_mul_f32 v169,v169,v69
v_mul_f32 v170,v170,v70
v_mul_f32 v171,v171,v71
v_cvt_pk_bf16_f32 v16,v160,v161
v_mfma_f32_16x16x32_bf16 v[224:227],v[96:99],v[144:147],v[224:227]
v_cvt_pk_bf16_f32 v17,v162,v163
v_cvt_pk_bf16_f32 v18,v168,v169
v_cvt_pk_bf16_f32 v19,v170,v171
v_mul_f32_dpp v176,v176,v72 quad_perm:[1,0,3,2] row_mask:0xf bank_mask:0xf
v_mfma_f32_16x16x32_bf16 v[232:235],v[100:103],v[144:147],v[232:235]
v_mul_f32_dpp v177,v177,v73 quad_perm:[1,0,3,2] row_mask:0xf bank_mask:0xf
v_mul_f32_dpp v178,v178,v74 quad_perm:[1,0,3,2] row_mask:0xf bank_mask:0xf
v_mul_f32_dpp v179,v179,v75 quad_perm:[1,0,3,2] row_mask:0xf bank_mask:0xf
v_mul_f32_dpp v184,v184,v76 quad_perm:[1,0,3,2] row_mask:0xf bank_mask:0xf
v_mul_f32_dpp v185,v185,v77 quad_perm:[1,0,3,2] row_mask:0xf bank_mask:0xf
v_mfma_f32_16x16x32_bf16 v[240:243],v[104:107],v[144:147],v[240:243]
v_mul_f32_dpp v186,v186,v78 quad_perm:[1,0,3,2] row_mask:0xf bank_mask:0xf
v_mul_f32_dpp v187,v187,v79 quad_perm:[1,0,3,2] row_mask:0xf bank_mask:0xf
v_cvt_pk_bf16_f32 v24,v176,v177
v_cvt_pk_bf16_f32 v25,v178,v179
v_mfma_f32_16x16x32_bf16 v[248:251],v[108:111],v[144:147],v[248:251]
v_cvt_pk_bf16_f32 v26,v184,v185
v_cvt_pk_bf16_f32 v27,v186,v187
s_mov_b64 exec,s[12:13]
v_swap_b32 v16,v24
v_swap_b32 v17,v25
v_swap_b32 v18,v26
v_swap_b32 v19,v27
s_mov_b64 exec,-1
v_mfma_f32_16x16x32_bf16 v[212:215],v[88:91],v[152:155],v[212:215]
s_add_u32 s65,s49,0
buffer_store_dwordx4 v[16:19],v10,s[40:43],s65 offen offset:0 sc1
v_mfma_f32_16x16x32_bf16 v[220:223],v[92:95],v[152:155],v[220:223]
v_mul_f32 v192,v192,v80
v_mul_f32 v193,v193,v81
v_mul_f32 v194,v194,v82
v_mul_f32 v195,v195,v83
v_mfma_f32_16x16x32_bf16 v[228:231],v[96:99],v[152:155],v[228:231]
v_mul_f32 v200,v200,v84
v_mul_f32 v201,v201,v85
v_mul_f32 v202,v202,v86
v_mul_f32 v203,v203,v87
v_mfma_f32_16x16x32_bf16 v[236:239],v[100:103],v[152:155],v[236:239]
v_cvt_pk_bf16_f32 v32,v192,v193
v_cvt_pk_bf16_f32 v33,v194,v195
v_cvt_pk_bf16_f32 v34,v200,v201
v_cvt_pk_bf16_f32 v35,v202,v203
v_mul_f32_dpp v164,v164,v64 quad_perm:[1,0,3,2] row_mask:0xf bank_mask:0xf
v_mfma_f32_16x16x32_bf16 v[244:247],v[104:107],v[152:155],v[244:247]
v_mul_f32_dpp v165,v165,v65 quad_perm:[1,0,3,2] row_mask:0xf bank_mask:0xf
v_mul_f32_dpp v166,v166,v66 quad_perm:[1,0,3,2] row_mask:0xf bank_mask:0xf
v_mul_f32_dpp v167,v167,v67 quad_perm:[1,0,3,2] row_mask:0xf bank_mask:0xf
v_mul_f32_dpp v172,v172,v68 quad_perm:[1,0,3,2] row_mask:0xf bank_mask:0xf
v_mfma_f32_16x16x32_bf16 v[252:255],v[108:111],v[152:155],v[252:255]
s_add_u32 s65,s49,0
buffer_store_dwordx4 v[24:27],v9,s[40:43],s65 offen offset:384 sc1
v_mul_f32_dpp v173,v173,v69 quad_perm:[1,0,3,2] row_mask:0xf bank_mask:0xf
v_mul_f32_dpp v174,v174,v70 quad_perm:[1,0,3,2] row_mask:0xf bank_mask:0xf
v_mul_f32_dpp v175,v175,v71 quad_perm:[1,0,3,2] row_mask:0xf bank_mask:0xf
v_cvt_pk_bf16_f32 v20,v164,v165
v_cvt_pk_bf16_f32 v21,v166,v167
v_cvt_pk_bf16_f32 v22,v172,v173
v_cvt_pk_bf16_f32 v23,v174,v175
s_mov_b64 exec,s[12:13]
v_swap_b32 v32,v20
v_swap_b32 v33,v21
v_swap_b32 v34,v22
v_swap_b32 v35,v23
s_mov_b64 exec,-1
v_mul_f32 v180,v180,v72
v_mul_f32 v181,v181,v73
v_mul_f32 v182,v182,v74
v_mul_f32 v183,v183,v75
v_mul_f32 v188,v188,v76
v_mul_f32 v189,v189,v77
s_add_u32 s65,s49,0
buffer_store_dwordx4 v[32:35],v10,s[40:43],s65 offen offset:128 sc1
v_mul_f32 v190,v190,v78
v_mul_f32 v191,v191,v79
v_cvt_pk_bf16_f32 v28,v180,v181
v_cvt_pk_bf16_f32 v29,v182,v183
v_cvt_pk_bf16_f32 v30,v188,v189
v_cvt_pk_bf16_f32 v31,v190,v191
v_mul_f32_dpp v196,v196,v80 quad_perm:[1,0,3,2] row_mask:0xf bank_mask:0xf
v_mul_f32_dpp v197,v197,v81 quad_perm:[1,0,3,2] row_mask:0xf bank_mask:0xf
v_mul_f32_dpp v198,v198,v82 quad_perm:[1,0,3,2] row_mask:0xf bank_mask:0xf
v_mul_f32_dpp v199,v199,v83 quad_perm:[1,0,3,2] row_mask:0xf bank_mask:0xf
v_mul_f32_dpp v204,v204,v84 quad_perm:[1,0,3,2] row_mask:0xf bank_mask:0xf
v_mul_f32_dpp v205,v205,v85 quad_perm:[1,0,3,2] row_mask:0xf bank_mask:0xf
v_mul_f32_dpp v206,v206,v86 quad_perm:[1,0,3,2] row_mask:0xf bank_mask:0xf
v_mul_f32_dpp v207,v207,v87 quad_perm:[1,0,3,2] row_mask:0xf bank_mask:0xf
v_cvt_pk_bf16_f32 v36,v196,v197
v_cvt_pk_bf16_f32 v37,v198,v199
v_cvt_pk_bf16_f32 v38,v204,v205
v_cvt_pk_bf16_f32 v39,v206,v207
s_mov_b64 exec,s[12:13]
v_swap_b32 v28,v36
v_swap_b32 v29,v37
v_swap_b32 v30,v38
v_swap_b32 v31,v39
s_mov_b64 exec,-1
s_add_u32 s65,s49,0
buffer_store_dwordx4 v[20:23],v9,s[40:43],s65 offen offset:512 sc1
v_mul_f32 v208,v208,v64
v_mul_f32 v209,v209,v65
v_mul_f32 v210,v210,v66
v_mul_f32 v211,v211,v67
v_mul_f32 v216,v216,v68
v_mul_f32 v217,v217,v69
v_mul_f32 v218,v218,v70
v_mul_f32 v219,v219,v71
v_cvt_pk_bf16_f32 v40,v208,v209
v_cvt_pk_bf16_f32 v41,v210,v211
v_cvt_pk_bf16_f32 v42,v216,v217
v_cvt_pk_bf16_f32 v43,v218,v219
v_mul_f32_dpp v224,v224,v72 quad_perm:[1,0,3,2] row_mask:0xf bank_mask:0xf
v_mul_f32_dpp v225,v225,v73 quad_perm:[1,0,3,2] row_mask:0xf bank_mask:0xf
v_mul_f32_dpp v226,v226,v74 quad_perm:[1,0,3,2] row_mask:0xf bank_mask:0xf
v_mul_f32_dpp v227,v227,v75 quad_perm:[1,0,3,2] row_mask:0xf bank_mask:0xf
v_mul_f32_dpp v232,v232,v76 quad_perm:[1,0,3,2] row_mask:0xf bank_mask:0xf
v_mul_f32_dpp v233,v233,v77 quad_perm:[1,0,3,2] row_mask:0xf bank_mask:0xf
v_mul_f32_dpp v234,v234,v78 quad_perm:[1,0,3,2] row_mask:0xf bank_mask:0xf
v_mul_f32_dpp v235,v235,v79 quad_perm:[1,0,3,2] row_mask:0xf bank_mask:0xf
v_cvt_pk_bf16_f32 v48,v224,v225
v_cvt_pk_bf16_f32 v49,v226,v227
v_cvt_pk_bf16_f32 v50,v232,v233
v_cvt_pk_bf16_f32 v51,v234,v235
s_add_u32 s65,s49,0
buffer_store_dwordx4 v[28:31],v10,s[40:43],s65 offen offset:256 sc1
s_mov_b64 exec,s[12:13]
v_swap_b32 v40,v48
v_swap_b32 v41,v49
v_swap_b32 v42,v50
v_swap_b32 v43,v51
s_mov_b64 exec,-1
v_mul_f32 v240,v240,v80
v_mul_f32 v241,v241,v81
v_mul_f32 v242,v242,v82
v_mul_f32 v243,v243,v83
v_mul_f32 v248,v248,v84
v_mul_f32 v249,v249,v85
v_mul_f32 v250,v250,v86
v_mul_f32 v251,v251,v87
v_cvt_pk_bf16_f32 v56,v240,v241
v_cvt_pk_bf16_f32 v57,v242,v243
v_cvt_pk_bf16_f32 v58,v248,v249
v_cvt_pk_bf16_f32 v59,v250,v251
v_mul_f32_dpp v212,v212,v64 quad_perm:[1,0,3,2] row_mask:0xf bank_mask:0xf
v_mul_f32_dpp v213,v213,v65 quad_perm:[1,0,3,2] row_mask:0xf bank_mask:0xf
v_mul_f32_dpp v214,v214,v66 quad_perm:[1,0,3,2] row_mask:0xf bank_mask:0xf
v_mul_f32_dpp v215,v215,v67 quad_perm:[1,0,3,2] row_mask:0xf bank_mask:0xf
v_mul_f32_dpp v220,v220,v68 quad_perm:[1,0,3,2] row_mask:0xf bank_mask:0xf
s_add_u32 s65,s49,0
buffer_store_dwordx4 v[36:39],v9,s[40:43],s65 offen offset:640 sc1
v_mul_f32_dpp v221,v221,v69 quad_perm:[1,0,3,2] row_mask:0xf bank_mask:0xf
v_mul_f32_dpp v222,v222,v70 quad_perm:[1,0,3,2] row_mask:0xf bank_mask:0xf
v_mul_f32_dpp v223,v223,v71 quad_perm:[1,0,3,2] row_mask:0xf bank_mask:0xf
v_cvt_pk_bf16_f32 v44,v212,v213
v_cvt_pk_bf16_f32 v45,v214,v215
v_cvt_pk_bf16_f32 v46,v220,v221
v_cvt_pk_bf16_f32 v47,v222,v223
s_mov_b64 exec,s[12:13]
v_swap_b32 v56,v44
v_swap_b32 v57,v45
v_swap_b32 v58,v46
v_swap_b32 v59,v47
s_mov_b64 exec,-1
v_mul_f32 v228,v228,v72
v_mul_f32 v229,v229,v73
v_mul_f32 v230,v230,v74
v_mul_f32 v231,v231,v75
v_mul_f32 v236,v236,v76
v_mul_f32 v237,v237,v77
v_mul_f32 v238,v238,v78
v_mul_f32 v239,v239,v79
v_cvt_pk_bf16_f32 v52,v228,v229
v_cvt_pk_bf16_f32 v53,v230,v231
s_add_u32 s65,s49,786432
buffer_store_dwordx4 v[40:43],v10,s[40:43],s65 offen offset:0 sc1
v_cvt_pk_bf16_f32 v54,v236,v237
v_cvt_pk_bf16_f32 v55,v238,v239
v_mul_f32_dpp v244,v244,v80 quad_perm:[1,0,3,2] row_mask:0xf bank_mask:0xf
v_mul_f32_dpp v245,v245,v81 quad_perm:[1,0,3,2] row_mask:0xf bank_mask:0xf
v_mul_f32_dpp v246,v246,v82 quad_perm:[1,0,3,2] row_mask:0xf bank_mask:0xf
v_mul_f32_dpp v247,v247,v83 quad_perm:[1,0,3,2] row_mask:0xf bank_mask:0xf
v_mul_f32_dpp v252,v252,v84 quad_perm:[1,0,3,2] row_mask:0xf bank_mask:0xf
v_mul_f32_dpp v253,v253,v85 quad_perm:[1,0,3,2] row_mask:0xf bank_mask:0xf
v_mul_f32_dpp v254,v254,v86 quad_perm:[1,0,3,2] row_mask:0xf bank_mask:0xf
v_mul_f32_dpp v255,v255,v87 quad_perm:[1,0,3,2] row_mask:0xf bank_mask:0xf
v_cvt_pk_bf16_f32 v60,v244,v245
v_cvt_pk_bf16_f32 v61,v246,v247
v_cvt_pk_bf16_f32 v62,v252,v253
s_nop 0
s_mov_b32 s86,0xbe801d5a
v_cvt_pk_bf16_f32 v63,v254,v255
s_mov_b64 exec,s[12:13]
v_swap_b32 v52,v60
v_swap_b32 v53,v61
v_swap_b32 v54,v62
v_swap_b32 v55,v63
s_mov_b64 exec,-1
s_cbranch_vccnz RA32_%=
v_and_b32 v112,63,%1
s_bfe_u32 s64,%0,0x20003
s_lshl_b32 s64,s64,13
s_bitcmp1_b32 s52,0
s_mov_b32 s66,28672
s_cselect_b32 s66,69632,s66
s_add_u32 s64,s64,s66
v_lshl_add_u32 v112,v112,7,s64
s_getpc_b64 s[14:15]
N_pgA31_%=:
s_sub_u32 s14,s14,N_pgA31_%=-N_main_%=
s_subb_u32 s15,s15,0
s_mov_b32 m0,157312
v_min_u32 v112,N_mainend_%=-N_main_%=,v112
global_load_lds_dword v112,s[14:15]
RA32_%=:
s_add_u32 s4,s10,12
s_cmp_ge_u32 s4,20
s_cselect_b32 s7,20,0
s_sub_u32 s4,s4,s7
s_mul_i32 s4,s4,3488
s_add_u32 s5,s10,14
s_cmp_ge_u32 s5,20
s_cselect_b32 s7,20,0
s_sub_u32 s5,s5,s7
s_mul_i32 s5,s5,3488
s_add_u32 s6,s10,16
s_cmp_ge_u32 s6,20
s_cselect_b32 s7,20,0
s_sub_u32 s6,s6,s7
s_mul_i32 s6,s6,3488
v_add_u32 v112,s4,v15
v_add_u32 v116,s5,v15
s_mov_b64 exec,s[8:9]
v_add_u32 v112,s5,v15
v_add_u32 v116,s6,v15
s_mov_b64 exec,-1
v_xor_b32 v113,16,v112
v_xor_b32 v117,16,v116
v_xor_b32 v114,32,v112
v_xor_b32 v118,32,v116
v_xor_b32 v115,48,v112
v_xor_b32 v119,48,v116
s_waitcnt vmcnt(8)
s_barrier
s_setprio 3
ds_read_b128 v[136:139],v116 offset:0
ds_read_b128 v[88:91],v11 offset:768
ds_read_b128 v[140:143],v116 offset:3488
ds_read_b128 v[128:131],v112 offset:0
ds_read_b128 v[132:135],v112 offset:3488
ds_read_b128 v[92:95],v12 offset:0
ds_read_b128 v[152:155],v117 offset:0
ds_read_b128 v[96:99],v11 offset:1920
ds_read_b128 v[156:159],v117 offset:3488
ds_read_b128 v[144:147],v113 offset:0
ds_read_b128 v[148:151],v113 offset:3488
ds_read_b128 v[100:103],v12 offset:1152
s_waitcnt lgkmcnt(10)
v_mfma_f32_16x16x32_bf16 v[160:163],v[88:91],v[136:139],0
s_waitcnt lgkmcnt(9)
s_add_u32 s65,s49,786432
buffer_store_dwordx4 v[48:51],v9,s[40:43],s65 offen offset:384 sc1
v_mfma_f32_16x16x32_bf16 v[184:187],v[88:91],v[140:143],0
s_waitcnt lgkmcnt(6)
ds_read_b128 v[140:143],v116 offset:3552
ds_read_b128 v[88:91],v11 offset:5376
v_smfmac_f32_16x16x64_bf16 v[160:163],v[92:95],v[128:135],v13
ds_read_b128 v[128:131],v112 offset:64
v_smfmac_f32_16x16x64_bf16 v[184:187],v[92:95],v[132:139],v13
s_waitcnt lgkmcnt(7)
ds_read_b128 v[136:139],v116 offset:64
ds_read_b128 v[132:135],v112 offset:3552
v_mfma_f32_16x16x32_bf16 v[164:167],v[96:99],v[152:155],0
s_waitcnt lgkmcnt(8)
ds_read_b128 v[92:95],v12 offset:4608
v_add_f32 v120,v160,v161
v_mul_f32 v124,v160,v160
v_fmac_f32 v124,v161,v161
v_mfma_f32_16x16x32_bf16 v[188:191],v[96:99],v[156:159],0
s_waitcnt lgkmcnt(6)
s_add_u32 s65,s49,786432
buffer_store_dwordx4 v[56:59],v10,s[40:43],s65 offen offset:128 sc1
ds_read_b128 v[156:159],v117 offset:3552
ds_read_b128 v[96:99],v11 offset:6528
v_add_f32 v121,v162,v163
v_mul_f32 v125,v162,v162
v_fmac_f32 v125,v163,v163
v_smfmac_f32_16x16x64_bf16 v[164:167],v[100:103],v[144:151],v13
ds_read_b128 v[144:147],v113 offset:64
v_add_f32 v122,v184,v185
v_mul_f32 v126,v184,v184
v_fmac_f32 v126,v185,v185
v_smfmac_f32_16x16x64_bf16 v[188:191],v[100:103],v[148:155],v13
s_waitcnt lgkmcnt(5)
ds_read_b128 v[152:155],v117 offset:64
ds_read_b128 v[148:151],v113 offset:3552
v_add_f32 v123,v186,v187
v_mul_f32 v127,v186,v186
v_fmac_f32 v127,v187,v187
v_mfma_f32_16x16x32_bf16 v[168:171],v[88:91],v[136:139],0
ds_read_b128 v[100:103],v12 offset:5760
v_add_f32 v120,v120,v164
v_fmac_f32 v124,v164,v164
v_add_f32 v120,v120,v165
v_fmac_f32 v124,v165,v165
v_mfma_f32_16x16x32_bf16 v[192:195],v[88:91],v[140:143],0
s_waitcnt lgkmcnt(6)
s_add_u32 s65,s49,786432
buffer_store_dwordx4 v[44:47],v9,s[40:43],s65 offen offset:512 sc1
ds_read_b128 v[140:143],v116 offset:3616
ds_read_b128 v[88:91],v11 offset:9984
v_add_f32 v121,v121,v166
v_fmac_f32 v125,v166,v166
v_add_f32 v121,v121,v167
v_fmac_f32 v125,v167,v167
v_smfmac_f32_16x16x64_bf16 v[168:171],v[92:95],v[128:135],v13
ds_read_b128 v[128:131],v112 offset:128
v_add_f32 v122,v122,v188
v_fmac_f32 v126,v188,v188
v_add_f32 v122,v122,v189
v_fmac_f32 v126,v189,v189
v_smfmac_f32_16x16x64_bf16 v[192:195],v[92:95],v[132:139],v13
s_waitcnt lgkmcnt(5)
ds_read_b128 v[136:139],v116 offset:128
ds_read_b128 v[132:135],v112 offset:3616
v_add_f32 v123,v123,v190
v_fmac_f32 v127,v190,v190
v_add_f32 v123,v123,v191
v_fmac_f32 v127,v191,v191
v_mfma_f32_16x16x32_bf16 v[172:175],v[96:99],v[152:155],0
ds_read_b128 v[92:95],v12 offset:9216
v_add_f32 v120,v120,v168
v_fmac_f32 v124,v168,v168
v_add_f32 v120,v120,v169
v_fmac_f32 v124,v169,v169
v_mfma_f32_16x16x32_bf16 v[196:199],v[96:99],v[156:159],0
s_waitcnt lgkmcnt(6)
s_add_u32 s65,s49,786432
buffer_store_dwordx4 v[52:55],v10,s[40:43],s65 offen offset:256 sc1
ds_read_b128 v[156:159],v117 offset:3616
ds_read_b128 v[96:99],v11 offset:11136
v_add_f32 v121,v121,v170
v_fmac_f32 v125,v170,v170
v_add_f32 v121,v121,v171
v_fmac_f32 v125,v171,v171
v_smfmac_f32_16x16x64_bf16 v[172:175],v[100:103],v[144:151],v13
ds_read_b128 v[144:147],v113 offset:128
v_add_f32 v122,v122,v192
v_fmac_f32 v126,v192,v192
v_add_f32 v122,v122,v193
v_fmac_f32 v126,v193,v193
v_smfmac_f32_16x16x64_bf16 v[196:199],v[100:103],v[148:155],v13
s_waitcnt lgkmcnt(5)
ds_read_b128 v[152:155],v117 offset:128
ds_read_b128 v[148:151],v113 offset:3616
v_add_f32 v123,v123,v194
v_fmac_f32 v127,v194,v194
v_add_f32 v123,v123,v195
v_fmac_f32 v127,v195,v195
v_mfma_f32_16x16x32_bf16 v[176:179],v[88:91],v[136:139],0
ds_read_b128 v[100:103],v12 offset:10368
v_add_f32 v120,v120,v172
v_fmac_f32 v124,v172,v172
v_add_f32 v120,v120,v173
v_fmac_f32 v124,v173,v173
v_mfma_f32_16x16x32_bf16 v[200:203],v[88:91],v[140:143],0
s_waitcnt lgkmcnt(6)
s_add_u32 s65,s49,786432
buffer_store_dwordx4 v[60:63],v9,s[40:43],s65 offen offset:640 sc1
ds_read_b128 v[140:143],v118 offset:3488
ds_read_b128 v[88:91],v11 offset:3072
v_add_f32 v121,v121,v174
v_fmac_f32 v125,v174,v174
v_add_f32 v121,v121,v175
v_fmac_f32 v125,v175,v175
v_smfmac_f32_16x16x64_bf16 v[176:179],v[92:95],v[128:135],v13
ds_read_b128 v[128:131],v114 offset:0
v_add_f32 v122,v122,v196
v_fmac_f32 v126,v196,v196
v_add_f32 v122,v122,v197
v_fmac_f32 v126,v197,v197
v_smfmac_f32_16x16x64_bf16 v[200:203],v[92:95],v[132:139],v13
s_waitcnt lgkmcnt(5)
ds_read_b128 v[136:139],v118 offset:0
ds_read_b128 v[132:135],v114 offset:3488
v_add_f32 v123,v123,v198
v_fmac_f32 v127,v198,v198
v_add_f32 v123,v123,v199
v_fmac_f32 v127,v199,v199
v_mfma_f32_16x16x32_bf16 v[180:183],v[96:99],v[152:155],0
ds_read_b128 v[92:95],v12 offset:2304
v_add_f32 v120,v120,v176
v_fmac_f32 v124,v176,v176
v_add_f32 v120,v120,v177
v_fmac_f32 v124,v177,v177
v_mfma_f32_16x16x32_bf16 v[204:207],v[96:99],v[156:159],0
s_waitcnt lgkmcnt(6)
ds_read_b128 v[156:159],v119 offset:3488
ds_read_b128 v[96:99],v11 offset:4224
v_add_f32 v121,v121,v178
v_fmac_f32 v125,v178,v178
v_add_f32 v121,v121,v179
v_fmac_f32 v125,v179,v179
v_smfmac_f32_16x16x64_bf16 v[180:183],v[100:103],v[144:151],v13
ds_read_b128 v[144:147],v115 offset:0
v_add_f32 v122,v122,v200
v_fmac_f32 v126,v200,v200
v_add_f32 v122,v122,v201
v_fmac_f32 v126,v201,v201
v_smfmac_f32_16x16x64_bf16 v[204:207],v[100:103],v[148:155],v13
s_waitcnt lgkmcnt(5)
ds_read_b128 v[152:155],v119 offset:0
ds_read_b128 v[148:151],v115 offset:3488
v_add_f32 v123,v123,v202
v_fmac_f32 v127,v202,v202
v_add_f32 v123,v123,v203
v_fmac_f32 v127,v203,v203
v_mfma_f32_16x16x32_bf16 v[208:211],v[88:91],v[136:139],0
ds_read_b128 v[100:103],v12 offset:3456
v_add_f32 v120,v120,v180
v_fmac_f32 v124,v180,v180
v_add_f32 v120,v120,v181
v_fmac_f32 v124,v181,v181
v_mfma_f32_16x16x32_bf16 v[232:235],v[88:91],v[140:143],0
s_waitcnt lgkmcnt(6)
ds_read_b128 v[140:143],v118 offset:3552
ds_read_b128 v[88:91],v11 offset:7680
v_add_f32 v121,v121,v182
v_fmac_f32 v125,v182,v182
v_add_f32 v121,v121,v183
v_fmac_f32 v125,v183,v183
v_smfmac_f32_16x16x64_bf16 v[208:211],v[92:95],v[128:135],v13
ds_read_b128 v[128:131],v114 offset:64
v_add_f32 v122,v122,v204
v_fmac_f32 v126,v204,v204
v_add_f32 v122,v122,v205
v_fmac_f32 v126,v205,v205
v_smfmac_f32_16x16x64_bf16 v[232:235],v[92:95],v[132:139],v13
s_waitcnt lgkmcnt(5)
ds_read_b128 v[136:139],v118 offset:64
ds_read_b128 v[132:135],v114 offset:3552
v_add_f32 v123,v123,v206
v_fmac_f32 v127,v206,v206
v_add_f32 v123,v123,v207
v_fmac_f32 v127,v207,v207
v_mfma_f32_16x16x32_bf16 v[212:215],v[96:99],v[152:155],0
ds_read_b128 v[92:95],v12 offset:6912
v_add_f32 v120,v120,v208
v_fmac_f32 v124,v208,v208
v_add_f32 v120,v120,v209
v_fmac_f32 v124,v209,v209
v_mfma_f32_16x16x32_bf16 v[236:239],v[96:99],v[156:159],0
s_waitcnt lgkmcnt(6)
s_setprio 2
ds_read_b128 v[156:159],v119 offset:3552
ds_read_b128 v[96:99],v11 offset:8832
v_add_f32 v121,v121,v210
v_fmac_f32 v125,v210,v210
v_add_f32 v121,v121,v211
v_fmac_f32 v125,v211,v211
v_smfmac_f32_16x16x64_bf16 v[212:215],v[100:103],v[144:151],v13
ds_read_b128 v[144:147],v115 offset:64
v_add_f32 v122,v122,v232
v_fmac_f32 v126,v232,v232
v_add_f32 v122,v122,v233
v_fmac_f32 v126,v233,v233
v_smfmac_f32_16x16x64_bf16 v[236:239],v[100:103],v[148:155],v13
s_waitcnt lgkmcnt(5)
ds_read_b128 v[152:155],v119 offset:64
ds_read_b128 v[148:151],v115 offset:3552
v_add_f32 v123,v123,v234
v_fmac_f32 v127,v234,v234
v_add_f32 v123,v123,v235
v_fmac_f32 v127,v235,v235
v_mfma_f32_16x16x32_bf16 v[216:219],v[88:91],v[136:139],0
ds_read_b128 v[100:103],v12 offset:8064
v_add_f32 v120,v120,v212
v_fmac_f32 v124,v212,v212
v_add_f32 v120,v120,v213
v_fmac_f32 v124,v213,v213
v_add_f32 v121,v121,v214
v_mfma_f32_16x16x32_bf16 v[240:243],v[88:91],v[140:143],0
s_waitcnt lgkmcnt(6)
ds_read_b128 v[140:143],v118 offset:3616
ds_read_b128 v[88:91],v11 offset:12288
v_fmac_f32 v125,v214,v214
v_add_f32 v121,v121,v215
v_fmac_f32 v125,v215,v215
v_add_f32 v122,v122,v236
v_smfmac_f32_16x16x64_bf16 v[216:219],v[92:95],v[128:135],v13
ds_read_b128 v[128:131],v114 offset:128
v_fmac_f32 v126,v236,v236
v_add_f32 v122,v122,v237
v_fmac_f32 v126,v237,v237
v_add_f32 v123,v123,v238
v_fmac_f32 v127,v238,v238
v_smfmac_f32_16x16x64_bf16 v[240:243],v[92:95],v[132:139],v13
s_waitcnt lgkmcnt(5)
ds_read_b128 v[136:139],v118 offset:128
ds_read_b128 v[132:135],v114 offset:3616
v_add_f32 v123,v123,v239
v_fmac_f32 v127,v239,v239
v_add_u32 v112,v112,v8
v_add_u32 v116,v116,v8
v_mfma_f32_16x16x32_bf16 v[220:223],v[96:99],v[152:155],0
ds_read_b128 v[92:95],v12 offset:11520
ds_read_b128 v[104:107],v14 offset:0
v_add_f32 v120,v120,v216
v_fmac_f32 v124,v216,v216
v_add_f32 v120,v120,v217
v_fmac_f32 v124,v217,v217
v_mfma_f32_16x16x32_bf16 v[244:247],v[96:99],v[156:159],0
s_waitcnt lgkmcnt(7)
ds_read_b128 v[108:111],v7 offset:3584
ds_read_b128 v[156:159],v119 offset:3616
v_add_f32 v121,v121,v218
v_fmac_f32 v125,v218,v218
v_add_f32 v121,v121,v219
v_fmac_f32 v125,v219,v219
v_smfmac_f32_16x16x64_bf16 v[220:223],v[100:103],v[144:151],v13
ds_read_b128 v[96:99],v11 offset:13440
ds_read_b128 v[144:147],v115 offset:128
v_add_f32 v122,v122,v240
v_fmac_f32 v126,v240,v240
v_add_f32 v122,v122,v241
v_fmac_f32 v126,v241,v241
v_smfmac_f32_16x16x64_bf16 v[244:247],v[100:103],v[148:155],v13
s_waitcnt lgkmcnt(7)
ds_read_b128 v[152:155],v119 offset:128
ds_read_b128 v[148:151],v115 offset:3616
v_add_f32 v123,v123,v242
v_fmac_f32 v127,v242,v242
v_add_f32 v123,v123,v243
v_fmac_f32 v127,v243,v243
v_mfma_f32_16x16x32_bf16 v[224:227],v[88:91],v[136:139],0
ds_read_b128 v[100:103],v12 offset:12672
v_add_f32 v120,v120,v220
v_fmac_f32 v124,v220,v220
v_add_f32 v120,v120,v221
v_fmac_f32 v124,v221,v221
v_mfma_f32_16x16x32_bf16 v[248:251],v[88:91],v[140:143],0
s_waitcnt lgkmcnt(8)
s_setprio 1
ds_read_b128 v[140:143],v116 offset:192
ds_read_b128 v[88:91],v112 offset:3616
v_add_f32 v121,v121,v222
v_fmac_f32 v125,v222,v222
v_add_f32 v121,v121,v223
v_fmac_f32 v125,v223,v223
v_smfmac_f32_16x16x64_bf16 v[224:227],v[92:95],v[128:135],v13
ds_read_b128 v[128:131],v112 offset:3488
v_add_f32 v122,v122,v244
v_fmac_f32 v126,v244,v244
v_add_f32 v122,v122,v245
v_fmac_f32 v126,v245,v245
v_smfmac_f32_16x16x64_bf16 v[248:251],v[92:95],v[132:139],v13
s_waitcnt lgkmcnt(5)
ds_read_b128 v[136:139],v116 offset:0
ds_read_b128 v[132:135],v112 offset:3680
v_add_f32 v123,v123,v246
v_fmac_f32 v127,v246,v246
v_add_f32 v123,v123,v247
v_fmac_f32 v127,v247,v247
v_mfma_f32_16x16x32_bf16 v[228:231],v[96:99],v[152:155],0
ds_read_b128 v[92:95],v112 offset:3808
v_add_f32 v120,v120,v224
v_fmac_f32 v124,v224,v224
v_add_f32 v120,v120,v225
v_fmac_f32 v124,v225,v225
v_mfma_f32_16x16x32_bf16 v[252:255],v[96:99],v[156:159],0
s_waitcnt lgkmcnt(6)
s_cbranch_vccnz RA33_%=
s_mov_b64 exec,s[62:63]
s_add_u32 m0,s57,34880
s_add_u32 s64,s56,4915200
buffer_load_dwordx4 v2,s[36:39],s64 offen lds
s_add_u32 m0,s57,36608
s_nop 0
buffer_load_dwordx4 v3,s[36:39],s64 offen lds
s_mov_b64 exec,-1
RA33_%=:
ds_read_b128 v[156:159],v116 offset:256
ds_read_b128 v[96:99],v116 offset:128
v_add_f32 v121,v121,v226
v_fmac_f32 v125,v226,v226
v_add_f32 v121,v121,v227
v_fmac_f32 v125,v227,v227
v_smfmac_f32_16x16x64_bf16 v[228:231],v[100:103],v[144:151],v13
ds_read_b128 v[144:147],v112 offset:3552
v_add_f32 v122,v122,v248
v_fmac_f32 v126,v248,v248
v_add_f32 v122,v122,v249
v_fmac_f32 v126,v249,v249
v_smfmac_f32_16x16x64_bf16 v[252:255],v[100:103],v[148:155],v13
s_setprio 0
ds_read_b128 v[152:155],v116 offset:64
ds_read_b128 v[148:151],v112 offset:3744
v_add_f32 v123,v123,v250
v_fmac_f32 v127,v250,v250
v_add_f32 v123,v123,v251
v_fmac_f32 v127,v251,v251
ds_read_b128 v[100:103],v116 offset:320
v_add_f32 v120,v120,v228
v_fmac_f32 v124,v228,v228
v_add_f32 v120,v120,v229
v_fmac_f32 v124,v229,v229
v_add_f32 v121,v121,v230
v_fmac_f32 v125,v230,v230
v_add_f32 v121,v121,v231
v_fmac_f32 v125,v231,v231
s_waitcnt lgkmcnt(0)
s_barrier
s_setprio 0
v_add_f32 v122,v122,v252
s_cbranch_vccnz RA34_%=
s_mov_b64 exec,s[62:63]
s_add_u32 m0,s57,38368
s_add_u32 s64,s56,5013504
buffer_load_dwordx4 v2,s[36:39],s64 offen lds
s_add_u32 m0,s57,40096
s_nop 0
buffer_load_dwordx4 v3,s[36:39],s64 offen lds
s_mov_b64 exec,-1
RA34_%=:
v_fmac_f32 v126,v252,v252
v_add_f32 v122,v122,v253
s_cbranch_vccnz RA35_%=
s_mov_b64 exec,s[62:63]
s_add_u32 m0,s57,41856
s_add_u32 s64,s56,5111808
buffer_load_dwordx4 v2,s[36:39],s64 offen lds
s_add_u32 m0,s57,43584
s_nop 0
buffer_load_dwordx4 v3,s[36:39],s64 offen lds
s_mov_b64 exec,-1
RA35_%=:
v_fmac_f32 v126,v253,v253
v_add_f32 v123,v123,v254
v_fmac_f32 v127,v254,v254
v_add_f32 v123,v123,v255
v_fmac_f32 v127,v255,v255
s_setprio 3
ds_read_b128 v[56:59],v14 offset:256
ds_read_b128 v[60:63],v7 offset:3616
s_setprio 0
v_permlane32_swap_b32 v120,v122
v_permlane32_swap_b32 v121,v123
v_permlane32_swap_b32 v124,v126
v_permlane32_swap_b32 v125,v127
v_add_f32 v120,v120,v122
v_add_f32 v121,v121,v123
v_add_f32 v124,v124,v126
v_add_f32 v125,v125,v127
v_permlane16_swap_b32 v120,v121
s_nop 0
v_permlane16_swap_b32 v124,v125
v_add_f32 v113,v120,v121
v_add_f32 v114,v124,v125
v_mul_f32 v113,0x3c2aaaab,v113
v_mul_f32 v114,0x3c2aaaab,v114
v_mul_f32 v115,0x5e000000,v114
v_fma_f32 v114,-v113,v113,v114
v_pk_maximum3_f16 v1,v1,v115,v115 op_sel:[0,1,1] op_sel_hi:[1,1,1]
v_max_f32 v114,0,v114
v_add_f32 v114,0x3727c5ac,v114
v_rsq_f32 v114,v114
s_nop 0
v_mul_f32 v120,0x8befe000,v114
v_mul_f32 v121,0x8befe000,v114
v_mul_f32 v124,-v113,v120
v_mul_f32 v125,-v113,v120
v_permlane16_swap_b32 v120,v121
s_nop 0
v_permlane16_swap_b32 v124,v125
v_mov_b32 v122,v120
v_mov_b32 v123,v121
v_mov_b32 v126,v124
v_mov_b32 v127,v125
v_permlane32_swap_b32 v120,v122
v_permlane32_swap_b32 v124,v126
s_cbranch_vccnz RA36_%=
s_mov_b64 exec,s[62:63]
s_add_u32 m0,s57,45344
s_add_u32 s64,s56,5210112
buffer_load_dwordx4 v2,s[36:39],s64 offen lds
s_add_u32 m0,s57,47072
s_nop 0
buffer_load_dwordx4 v3,s[36:39],s64 offen lds
s_mov_b64 exec,-1
RA36_%=:
v_permlane32_swap_b32 v121,v123
v_permlane32_swap_b32 v125,v127
v_add_u32 v8,94432,v4
v_add_u32 v9,94432,v5
v_fma_f32 v160,v160,v120,v124
s_setprio 3
ds_read_b64_tr_b16 v[64:65],v4 offset:512
s_setprio 0
v_fma_f32 v161,v161,v120,v124
s_setprio 3
ds_read_b64_tr_b16 v[66:67],v4 offset:2560
s_setprio 0
v_fma_f32 v162,v162,v121,v125
s_setprio 3
ds_read_b128 v[112:115],v7 offset:896
s_setprio 0
v_fma_f32 v163,v163,v121,v125
s_setprio 3
ds_read_b64_tr_b16 v[68:69],v5 offset:512
s_setprio 0
v_fma_f32 v164,v164,v120,v124
s_setprio 3
ds_read_b64_tr_b16 v[70:71],v5 offset:2560
s_setprio 0
v_fma_f32 v165,v165,v120,v124
s_setprio 3
ds_read_b128 v[116:119],v7 offset:928
s_setprio 0
v_fma_f32 v166,v166,v121,v125
s_setprio 3
ds_read_b64_tr_b16 v[72:73],v4 offset:4608
s_setprio 0
v_fma_f32 v167,v167,v121,v125
s_setprio 3
ds_read_b64_tr_b16 v[74:75],v4 offset:6656
s_setprio 0
v_fma_f32 v208,v208,v120,v124
s_setprio 3
ds_read_b64_tr_b16 v[76:77],v5 offset:4608
s_setprio 0
v_fma_f32 v209,v209,v120,v124
s_setprio 3
ds_read_b64_tr_b16 v[78:79],v5 offset:6656
s_setprio 0
v_fma_f32 v210,v210,v121,v125
s_setprio 3
ds_read_b64_tr_b16 v[80:81],v4 offset:8704
s_setprio 0
v_fma_f32 v211,v211,v121,v125
s_setprio 3
ds_read_b64_tr_b16 v[82:83],v4 offset:10752
s_setprio 0
v_fma_f32 v212,v212,v120,v124
s_setprio 3
ds_read_b64_tr_b16 v[84:85],v5 offset:8704
s_setprio 0
v_fma_f32 v213,v213,v120,v124
s_setprio 3
ds_read_b64_tr_b16 v[86:87],v5 offset:10752
s_setprio 0
v_fma_f32 v214,v214,v121,v125
v_fma_f32 v215,v215,v121,v125
v_cvt_pk_bf16_f32 v16,v160,v161
v_cvt_pk_bf16_f32 v20,v162,v163
v_cvt_pk_bf16_f32 v17,v164,v165
v_cvt_pk_bf16_f32 v21,v166,v167
v_cvt_pk_bf16_f32 v18,v208,v209
v_cvt_pk_bf16_f32 v22,v210,v211
v_cvt_pk_bf16_f32 v19,v212,v213
v_cvt_pk_bf16_f32 v23,v214,v215
v_fma_f32 v168,v168,v120,v124
v_fma_f32 v169,v169,v120,v124
v_mfma_f32_16x16x32_bf16 v[160:163],v[104:107],v[128:131],v[108:111]
v_fma_f32 v170,v170,v121,v125
v_fma_f32 v171,v171,v121,v125
v_fma_f32 v172,v172,v120,v124
v_fma_f32 v173,v173,v120,v124
v_fma_f32 v174,v174,v121,v125
v_fma_f32 v175,v175,v121,v125
v_mfma_f32_16x16x32_bf16 v[164:167],v[104:107],v[132:135],v[108:111]
v_fma_f32 v216,v216,v120,v124
v_fma_f32 v217,v217,v120,v124
v_fma_f32 v218,v218,v121,v125
v_fma_f32 v219,v219,v121,v125
v_fma_f32 v220,v220,v120,v124
v_mfma_f32_16x16x32_bf16 v[208:211],v[104:107],v[136:139],v[108:111]
s_cbranch_vccnz RA37_%=
s_mov_b64 exec,s[62:63]
s_add_u32 m0,s57,48832
s_add_u32 s64,s56,5308416
buffer_load_dwordx4 v2,s[36:39],s64 offen lds
s_add_u32 m0,s57,50560
s_nop 0
buffer_load_dwordx4 v3,s[36:39],s64 offen lds
s_mov_b64 exec,-1
RA37_%=:
v_fma_f32 v221,v221,v120,v124
v_fma_f32 v222,v222,v121,v125
v_fma_f32 v223,v223,v121,v125
v_cvt_pk_bf16_f32 v24,v168,v169
v_cvt_pk_bf16_f32 v28,v170,v171
s_setprio 3
v_mfma_f32_16x16x32_bf16 v[212:215],v[104:107],v[140:143],v[108:111]
ds_read_b128 v[104:107],v14 offset:512
s_setprio 0
v_cvt_pk_bf16_f32 v25,v172,v173
v_cvt_pk_bf16_f32 v29,v174,v175
v_cvt_pk_bf16_f32 v26,v216,v217
v_cvt_pk_bf16_f32 v30,v218,v219
v_cvt_pk_bf16_f32 v27,v220,v221
v_cvt_pk_bf16_f32 v31,v222,v223
v_fma_f32 v176,v176,v120,v124
v_fma_f32 v177,v177,v120,v124
s_waitcnt lgkmcnt(15)
s_setprio 3
v_mfma_f32_16x16x32_bf16 v[168:171],v[56:59],v[128:131],v[60:63]
ds_read_b128 v[108:111],v7 offset:3712
s_setprio 0
v_fma_f32 v178,v178,v121,v125
v_fma_f32 v179,v179,v121,v125
v_fma_f32 v180,v180,v120,v124
v_fma_f32 v181,v181,v120,v124
v_fma_f32 v182,v182,v121,v125
v_fma_f32 v183,v183,v121,v125
v_mfma_f32_16x16x32_bf16 v[172:175],v[56:59],v[132:135],v[60:63]
v_fma_f32 v224,v224,v120,v124
v_fma_f32 v225,v225,v120,v124
v_fma_f32 v226,v226,v121,v125
v_fma_f32 v227,v227,v121,v125
v_fma_f32 v228,v228,v120,v124
v_mfma_f32_16x16x32_bf16 v[216:219],v[56:59],v[136:139],v[60:63]
v_fma_f32 v229,v229,v120,v124
v_fma_f32 v230,v230,v121,v125
v_fma_f32 v231,v231,v121,v125
v_cvt_pk_bf16_f32 v32,v176,v177
v_cvt_pk_bf16_f32 v36,v178,v179
s_setprio 3
v_mfma_f32_16x16x32_bf16 v[220:223],v[56:59],v[140:143],v[60:63]
ds_read_b128 v[56:59],v14 offset:768
s_setprio 0
v_cvt_pk_bf16_f32 v33,v180,v181
v_cvt_pk_bf16_f32 v37,v182,v183
v_cvt_pk_bf16_f32 v34,v224,v225
v_cvt_pk_bf16_f32 v38,v226,v227
v_cvt_pk_bf16_f32 v35,v228,v229
v_cvt_pk_bf16_f32 v39,v230,v231
v_fma_f32 v184,v184,v122,v126
s_waitcnt lgkmcnt(1)
v_mfma_f32_16x16x32_bf16 v[176:179],v[104:107],v[144:147],v[108:111]
s_cbranch_vccnz RA38_%=
s_mov_b64 exec,s[62:63]
s_add_u32 m0,s57,52320
s_add_u32 s64,s56,5406720
buffer_load_dwordx4 v2,s[36:39],s64 offen lds
s_add_u32 m0,s57,54048
s_nop 0
buffer_load_dwordx4 v3,s[36:39],s64 offen lds
s_mov_b64 exec,-1
RA38_%=:
s_setprio 3
ds_read_b128 v[60:63],v7 offset:3744
s_setprio 0
v_fma_f32 v185,v185,v122,v126
v_fma_f32 v186,v186,v123,v127
v_fma_f32 v187,v187,v123,v127
v_mfma_f32_16x16x32_bf16 v[128:131],v[64:67],v[16:19],v[112:115]
v_fma_f32 v188,v188,v122,v126
v_fma_f32 v189,v189,v122,v126
v_fma_f32 v190,v190,v123,v127
v_mfma_f32_16x16x32_bf16 v[180:183],v[104:107],v[148:151],v[108:111]
v_fma_f32 v191,v191,v123,v127
v_fma_f32 v232,v232,v122,v126
v_fma_f32 v233,v233,v122,v126
v_mfma_f32_16x16x32_bf16 v[136:139],v[64:67],v[20:23],v[112:115]
v_fma_f32 v234,v234,v123,v127
v_fma_f32 v235,v235,v123,v127
v_fma_f32 v236,v236,v122,v126
v_mfma_f32_16x16x32_bf16 v[224:227],v[104:107],v[152:155],v[108:111]
v_fma_f32 v237,v237,v122,v126
v_fma_f32 v238,v238,v123,v127
v_fma_f32 v239,v239,v123,v127
v_mfma_f32_16x16x32_bf16 v[140:143],v[68:71],v[20:23],v[116:119]
v_cvt_pk_bf16_f32 v40,v184,v185
v_cvt_pk_bf16_f32 v44,v186,v187
v_cvt_pk_bf16_f32 v41,v188,v189
s_setprio 3
v_mfma_f32_16x16x32_bf16 v[228:231],v[104:107],v[156:159],v[108:111]
ds_read_b128 v[104:107],v14 offset:1024
s_setprio 0
v_cvt_pk_bf16_f32 v45,v190,v191
v_cvt_pk_bf16_f32 v42,v232,v233
v_cvt_pk_bf16_f32 v46,v234,v235
s_setprio 3
v_mfma_f32_16x16x32_bf16 v[132:135],v[68:71],v[16:19],v[116:119]
ds_read_b128 v[108:111],v7 offset:3840
s_setprio 0
v_cvt_pk_bf16_f32 v43,v236,v237
v_cvt_pk_bf16_f32 v47,v238,v239
s_nop 0
s_mov_b32 s86,0xbe801d5a
v_fma_f32 v192,v192,v122,v126
s_waitcnt lgkmcnt(2)
v_mfma_f32_16x16x32_bf16 v[184:187],v[56:59],v[144:147],v[60:63]
v_fma_f32 v193,v193,v122,v126
v_fma_f32 v194,v194,v123,v127
v_mfma_f32_16x16x32_bf16 v[128:131],v[72:75],v[24:27],v[128:131]
v_fma_f32 v195,v195,v123,v127
v_fma_f32 v196,v196,v122,v126
v_mfma_f32_16x16x32_bf16 v[188:191],v[56:59],v[148:151],v[60:63]
v_fma_f32 v197,v197,v122,v126
v_fma_f32 v198,v198,v123,v127
v_mfma_f32_16x16x32_bf16 v[136:139],v[72:75],v[28:31],v[136:139]
s_cbranch_vccnz RA39_%=
s_mov_b64 exec,s[62:63]
s_add_u32 m0,s57,55808
s_add_u32 s64,s56,5505024
buffer_load_dwordx4 v2,s[36:39],s64 offen lds
s_add_u32 m0,s57,57536
s_nop 0
buffer_load_dwordx4 v3,s[36:39],s64 offen lds
s_mov_b64 exec,-1
RA39_%=:
v_fma_f32 v199,v199,v123,v127
v_fma_f32 v240,v240,v122,v126
v_mfma_f32_16x16x32_bf16 v[232:235],v[56:59],v[152:155],v[60:63]
v_fma_f32 v241,v241,v122,v126
v_fma_f32 v242,v242,v123,v127
v_mfma_f32_16x16x32_bf16 v[140:143],v[76:79],v[28:31],v[140:143]
v_fma_f32 v243,v243,v123,v127
v_fma_f32 v244,v244,v122,v126
s_setprio 3
v_mfma_f32_16x16x32_bf16 v[236:239],v[56:59],v[156:159],v[60:63]
ds_read_b128 v[144:147],v14 offset:1280
s_setprio 0
v_fma_f32 v245,v245,v122,v126
v_fma_f32 v246,v246,v123,v127
s_setprio 3
v_mfma_f32_16x16x32_bf16 v[132:135],v[76:79],v[24:27],v[132:135]
ds_read_b128 v[148:151],v7 offset:3872
s_setprio 0
v_fma_f32 v247,v247,v123,v127
v_cvt_pk_bf16_f32 v48,v192,v193
v_cvt_pk_bf16_f32 v52,v194,v195
v_cvt_pk_bf16_f32 v49,v196,v197
v_cvt_pk_bf16_f32 v53,v198,v199
v_cvt_pk_bf16_f32 v50,v240,v241
v_cvt_pk_bf16_f32 v54,v242,v243
v_cvt_pk_bf16_f32 v51,v244,v245
v_cvt_pk_bf16_f32 v55,v246,v247
v_fma_f32 v200,v200,v122,v126
s_waitcnt lgkmcnt(2)
v_mfma_f32_16x16x32_bf16 v[192:195],v[104:107],v[88:91],v[108:111]
v_fma_f32 v201,v201,v122,v126
v_fma_f32 v202,v202,v123,v127
v_fma_f32 v203,v203,v123,v127
v_mfma_f32_16x16x32_bf16 v[128:131],v[80:83],v[32:35],v[128:131]
v_fma_f32 v204,v204,v122,v126
v_fma_f32 v205,v205,v122,v126
v_fma_f32 v206,v206,v123,v127
v_mfma_f32_16x16x32_bf16 v[196:199],v[104:107],v[92:95],v[108:111]
v_fma_f32 v207,v207,v123,v127
v_fma_f32 v248,v248,v122,v126
v_fma_f32 v249,v249,v122,v126
v_mfma_f32_16x16x32_bf16 v[136:139],v[80:83],v[36:39],v[136:139]
v_fma_f32 v250,v250,v123,v127
v_fma_f32 v251,v251,v123,v127
v_fma_f32 v252,v252,v122,v126
v_mfma_f32_16x16x32_bf16 v[240:243],v[104:107],v[96:99],v[108:111]
s_cbranch_vccnz RA40_%=
s_mov_b64 exec,s[62:63]
s_add_u32 m0,s57,59296
s_add_u32 s64,s56,5603328
buffer_load_dwordx4 v2,s[36:39],s64 offen lds
s_add_u32 m0,s57,61024
s_nop 0
buffer_load_dwordx4 v3,s[36:39],s64 offen lds
s_mov_b64 exec,-1
RA40_%=:
v_fma_f32 v253,v253,v122,v126
v_fma_f32 v254,v254,v123,v127
v_fma_f32 v255,v255,v123,v127
v_mfma_f32_16x16x32_bf16 v[140:143],v[84:87],v[36:39],v[140:143]
v_cvt_pk_bf16_f32 v56,v200,v201
v_cvt_pk_bf16_f32 v60,v202,v203
v_cvt_pk_bf16_f32 v57,v204,v205
v_mfma_f32_16x16x32_bf16 v[244:247],v[104:107],v[100:103],v[108:111]
v_cvt_pk_bf16_f32 v61,v206,v207
v_cvt_pk_bf16_f32 v58,v248,v249
v_cvt_pk_bf16_f32 v62,v250,v251
v_mfma_f32_16x16x32_bf16 v[132:135],v[84:87],v[32:35],v[132:135]
v_cvt_pk_bf16_f32 v59,v252,v253
v_cvt_pk_bf16_f32 v63,v254,v255
s_waitcnt lgkmcnt(0)
v_mfma_f32_16x16x32_bf16 v[200:203],v[144:147],v[88:91],v[148:151]
s_barrier
s_setprio 3
ds_read_b64_tr_b16 v[104:105],v6 offset:12448
v_lshlrev_b32_sdwa v128,1,v128 dst_sel:DWORD dst_unused:UNUSED_PAD src0_sel:DWORD src1_sel:WORD_1
v_lshlrev_b32_sdwa v130,1,v130 dst_sel:DWORD dst_unused:UNUSED_PAD src0_sel:DWORD src1_sel:WORD_1
v_lshlrev_b32_sdwa v129,1,v129 dst_sel:DWORD dst_unused:UNUSED_PAD src0_sel:DWORD src1_sel:WORD_1
v_mfma_f32_16x16x32_bf16 v[204:207],v[144:147],v[92:95],v[148:151]
s_cbranch_vccz RA41_%=
s_add_u32 m0,s58,0
s_mul_i32 s69,s59,9
s_add_u32 s69,s69,s97
buffer_load_dwordx4 v2,s[44:47],s69 offen lds
s_add_u32 m0,m0,1024
s_add_u32 s69,s69,s53
buffer_load_dwordx4 v3,s[44:47],s69 offen lds
RA41_%=:
ds_read_u16 v128,v128
ds_read_b64_tr_b16 v[106:107],v6 offset:13984
v_lshlrev_b32_sdwa v131,1,v131 dst_sel:DWORD dst_unused:UNUSED_PAD src0_sel:DWORD src1_sel:WORD_1
v_fma_mixlo_f16 v120,v140,s99,0
v_fma_mixlo_f16 v122,v141,s99,0
v_mfma_f32_16x16x32_bf16 v[248:251],v[144:147],v[96:99],v[148:151]
ds_read_u16_d16_hi v130,v130
ds_read_b64_tr_b16 v[108:109],v6 offset:12456
v_lshlrev_b32_sdwa v132,1,v132 dst_sel:DWORD dst_unused:UNUSED_PAD src0_sel:DWORD src1_sel:WORD_1
v_lshlrev_b32_sdwa v134,1,v134 dst_sel:DWORD dst_unused:UNUSED_PAD src0_sel:DWORD src1_sel:WORD_1
v_mfma_f32_16x16x32_bf16 v[252:255],v[144:147],v[100:103],v[148:151]
ds_read_u16 v129,v129
ds_read_b64_tr_b16 v[110:111],v6 offset:13992
v_fma_mixhi_f16 v120,v142,s99,0
v_fma_mixhi_f16 v122,v143,s99,0
v_lshlrev_b32_sdwa v133,1,v133 dst_sel:DWORD dst_unused:UNUSED_PAD src0_sel:DWORD src1_sel:WORD_1
v_lshlrev_b32_sdwa v135,1,v135 dst_sel:DWORD dst_unused:UNUSED_PAD src0_sel:DWORD src1_sel:WORD_1
v_mfma_f32_16x16x32_bf16 v[144:147],v[64:67],v[40:43],v[112:115]
s_cbranch_vccz RA42_%=
s_add_u32 m0,m0,1024
s_add_u32 s69,s69,s53
buffer_load_dwordx4 v2,s[44:47],s69 offen lds
s_add_u32 m0,m0,s50
s_add_u32 s69,s69,s53
buffer_load_dwordx4 v3,s[44:47],s69 offen lds
RA42_%=:
ds_read_u16_d16_hi v131,v131
ds_read_b64_tr_b16 v[88:89],v6 offset:12320
v_pk_fma_f16 v121,v120,v120,s54
v_pk_fma_f16 v123,v122,v122,s54
v_pk_mul_f16 v121,v120,v121
v_mfma_f32_16x16x32_bf16 v[152:155],v[64:67],v[44:47],v[112:115]
ds_read_u16 v132,v132
ds_read_b64_tr_b16 v[90:91],v6 offset:13856
v_pk_mul_f16 v123,v122,v123
v_lshlrev_b32_sdwa v136,1,v136 dst_sel:DWORD dst_unused:UNUSED_PAD src0_sel:DWORD src1_sel:WORD_1
v_mfma_f32_16x16x32_bf16 v[156:159],v[68:71],v[44:47],v[116:119]
s_cbranch_vccnz RA43_%=
s_mov_b64 exec,s[62:63]
s_add_u32 m0,s57,62784
s_add_u32 s64,s56,5701632
buffer_load_dwordx4 v2,s[36:39],s64 offen lds
s_add_u32 m0,s57,64512
s_nop 0
buffer_load_dwordx4 v3,s[36:39],s64 offen lds
s_mov_b64 exec,-1
RA43_%=:
ds_read_u16_d16_hi v134,v134
ds_read_b64_tr_b16 v[92:93],v6 offset:12328
v_lshlrev_b32_sdwa v138,1,v138 dst_sel:DWORD dst_unused:UNUSED_PAD src0_sel:DWORD src1_sel:WORD_1
v_exp_f16 v120,v121
v_mfma_f32_16x16x32_bf16 v[148:151],v[68:71],v[40:43],v[116:119]
ds_read_u16 v133,v133
ds_read_b64_tr_b16 v[94:95],v6 offset:13864
v_exp_f16 v122,v123
v_lshlrev_b32_sdwa v137,1,v137 dst_sel:DWORD dst_unused:UNUSED_PAD src0_sel:DWORD src1_sel:WORD_1
v_mfma_f32_16x16x32_bf16 v[144:147],v[72:75],v[48:51],v[144:147]
s_cbranch_vccz RA44_%=
s_add_u32 m0,m0,1024
s_add_u32 s69,s69,s53
buffer_load_dwordx4 v2,s[44:47],s69 offen lds
s_add_u32 m0,m0,1024
s_add_u32 s69,s69,s53
buffer_load_dwordx4 v3,s[44:47],s69 offen lds
RA44_%=:
ds_read_u16_d16_hi v135,v135
ds_read_b64_tr_b16 v[96:97],v6 offset:12384
v_lshlrev_b32_sdwa v139,1,v139 dst_sel:DWORD dst_unused:UNUSED_PAD src0_sel:DWORD src1_sel:WORD_1
v_exp_f16_sdwa v120,v121 dst_sel:WORD_1 dst_unused:UNUSED_PRESERVE src0_sel:WORD_1
v_mfma_f32_16x16x32_bf16 v[152:155],v[72:75],v[52:55],v[152:155]
ds_read_u16 v136,v136
ds_read_b64_tr_b16 v[98:99],v6 offset:13920
v_exp_f16_sdwa v122,v123 dst_sel:WORD_1 dst_unused:UNUSED_PRESERVE src0_sel:WORD_1
v_mfma_f32_16x16x32_bf16 v[156:159],v[76:79],v[52:55],v[156:159]
ds_read_u16_d16_hi v138,v138
ds_read_b64_tr_b16 v[100:101],v6 offset:12392
v_pk_fma_f16 v120,v120,s55,s55
v_pk_fma_f16 v122,v122,s55,s55
v_rcp_f16 v121,v120
v_mfma_f32_16x16x32_bf16 v[148:151],v[76:79],v[48:51],v[148:151]
s_cbranch_vccz RA45_%=
s_add_u32 m0,m0,s50
s_add_u32 s69,s69,s53
buffer_load_dwordx4 v2,s[44:47],s69 offen lds
s_add_u32 m0,m0,1024
s_add_u32 s69,s69,s53
buffer_load_dwordx4 v3,s[44:47],s69 offen lds
RA45_%=:
ds_read_u16 v137,v137
ds_read_b64_tr_b16 v[102:103],v6 offset:13928
v_rcp_f16 v123,v122
v_mfma_f32_16x16x32_bf16 v[144:147],v[80:83],v[56:59],v[144:147]
ds_read_u16_d16_hi v139,v139
ds_read_b64_tr_b16 v[64:65],v4 offset:12320
v_rcp_f16_sdwa v121,v120 dst_sel:WORD_1 dst_unused:UNUSED_PRESERVE src0_sel:WORD_1
v_mfma_f32_16x16x32_bf16 v[152:155],v[80:83],v[60:63],v[152:155]
ds_read_b64_tr_b16 v[66:67],v4 offset:14368
v_rcp_f16_sdwa v123,v122 dst_sel:WORD_1 dst_unused:UNUSED_PRESERVE src0_sel:WORD_1
v_fma_mix_f32 v140,v140,v121,0 op_sel_hi:[0,1,0]
v_mfma_f32_16x16x32_bf16 v[156:159],v[84:87],v[60:63],v[156:159]
s_cbranch_vccz RA46_%=
s_add_u32 m0,m0,1024
s_add_u32 s69,s69,s53
buffer_load_dwordx4 v2,s[44:47],s69 offen lds
s_add_u32 m0,m0,s50
s_add_u32 s69,s69,s53
buffer_load_dwordx4 v3,s[44:47],s69 offen lds
RA46_%=:
ds_read_b128 v[112:115],v7 offset:1024
v_fma_mix_f32 v142,v142,v121,0 op_sel:[0,1,0] op_sel_hi:[0,1,0]
s_waitcnt lgkmcnt(15)
v_lshl_add_u64 v[128:129],v[128:129],0,v[130:131]
s_waitcnt lgkmcnt(11)
v_lshl_add_u64 v[130:131],v[132:133],0,v[134:135]
v_mfma_f32_16x16x32_bf16 v[148:151],v[84:87],v[56:59],v[148:151]
ds_read_b64_tr_b16 v[68:69],v5 offset:12320
v_fma_mix_f32 v141,v141,v123,0 op_sel_hi:[0,1,0]
v_fma_mix_f32 v143,v143,v123,0 op_sel:[0,1,0] op_sel_hi:[0,1,0]
v_lshlrev_b32_sdwa v144,1,v144 dst_sel:DWORD dst_unused:UNUSED_PAD src0_sel:DWORD src1_sel:WORD_1
v_lshlrev_b32_sdwa v146,1,v146 dst_sel:DWORD dst_unused:UNUSED_PAD src0_sel:DWORD src1_sel:WORD_1
v_mfma_f32_16x16x32_bf16 v[160:163],v[88:91],v[128:131],v[160:163]
s_cbranch_vccnz RA47_%=
s_mov_b64 exec,s[62:63]
s_add_u32 m0,s57,66272
s_add_u32 s64,s56,5799936
buffer_load_dwordx4 v2,s[36:39],s64 offen lds
s_add_u32 m0,s57,68000
s_nop 0
buffer_load_dwordx4 v3,s[36:39],s64 offen lds
s_mov_b64 exec,-1
RA47_%=:
ds_read_u16 v144,v144
v_lshlrev_b32_sdwa v145,1,v145 dst_sel:DWORD dst_unused:UNUSED_PAD src0_sel:DWORD src1_sel:WORD_1
v_lshlrev_b32_sdwa v147,1,v147 dst_sel:DWORD dst_unused:UNUSED_PAD src0_sel:DWORD src1_sel:WORD_1
v_mfma_f32_16x16x32_bf16 v[168:171],v[92:95],v[128:131],v[168:171]
s_waitcnt lgkmcnt(10)
s_cbranch_vccz RA48_%=
s_add_u32 m0,m0,1024
s_add_u32 s69,s69,s53
buffer_load_dwordx4 v2,s[44:47],s69 offen lds
s_add_u32 m0,m0,1024
s_add_u32 s69,s69,s53
buffer_load_dwordx4 v3,s[44:47],s69 offen lds
RA48_%=:
ds_read_b64_tr_b16 v[70:71],v5 offset:14368
ds_read_b128 v[116:119],v7 offset:1056
v_fma_mixlo_f16 v120,v156,s99,0
v_fma_mixlo_f16 v122,v157,s99,0
v_mfma_f32_16x16x32_bf16 v[176:179],v[96:99],v[128:131],v[176:179]
s_waitcnt lgkmcnt(8)
ds_read_u16_d16_hi v146,v146
v_lshlrev_b32_sdwa v148,1,v148 dst_sel:DWORD dst_unused:UNUSED_PAD src0_sel:DWORD src1_sel:WORD_1
v_lshlrev_b32_sdwa v150,1,v150 dst_sel:DWORD dst_unused:UNUSED_PAD src0_sel:DWORD src1_sel:WORD_1
v_mfma_f32_16x16x32_bf16 v[184:187],v[100:103],v[128:131],v[184:187]
s_waitcnt lgkmcnt(8)
ds_read_b64_tr_b16 v[72:73],v4 offset:16416
ds_read_b64_tr_b16 v[74:75],v4 offset:18464
v_lshl_add_u64 v[136:137],v[136:137],0,v[138:139]
v_mfma_f32_16x16x32_bf16 v[192:195],v[104:107],v[128:131],v[192:195]
ds_read_u16 v145,v145
v_cvt_pk_bf16_f32 v138,v140,v142
v_cvt_pk_bf16_f32 v139,v141,v143
v_mfma_f32_16x16x32_bf16 v[200:203],v[108:111],v[128:131],v[200:203]
ds_read_b64_tr_b16 v[76:77],v5 offset:16416
ds_read_b64_tr_b16 v[78:79],v5 offset:18464
v_fma_mixhi_f16 v120,v158,s99,0
v_fma_mixhi_f16 v122,v159,s99,0
v_mfma_f32_16x16x32_bf16 v[164:167],v[88:91],v[136:139],v[164:167]
ds_read_u16_d16_hi v147,v147
v_lshlrev_b32_sdwa v149,1,v149 dst_sel:DWORD dst_unused:UNUSED_PAD src0_sel:DWORD src1_sel:WORD_1
v_lshlrev_b32_sdwa v151,1,v151 dst_sel:DWORD dst_unused:UNUSED_PAD src0_sel:DWORD src1_sel:WORD_1
v_mfma_f32_16x16x32_bf16 v[172:175],v[92:95],v[136:139],v[172:175]
ds_read_b64_tr_b16 v[80:81],v4 offset:20512
ds_read_b64_tr_b16 v[82:83],v4 offset:22560
v_pk_fma_f16 v121,v120,v120,s54
v_pk_fma_f16 v123,v122,v122,s54
v_mfma_f32_16x16x32_bf16 v[180:183],v[96:99],v[136:139],v[180:183]
ds_read_u16 v148,v148
v_pk_mul_f16 v121,v120,v121
v_pk_mul_f16 v123,v122,v123
v_mfma_f32_16x16x32_bf16 v[188:191],v[100:103],v[136:139],v[188:191]
ds_read_b64_tr_b16 v[84:85],v5 offset:20512
v_lshlrev_b32_sdwa v152,1,v152 dst_sel:DWORD dst_unused:UNUSED_PAD src0_sel:DWORD src1_sel:WORD_1
v_lshlrev_b32_sdwa v154,1,v154 dst_sel:DWORD dst_unused:UNUSED_PAD src0_sel:DWORD src1_sel:WORD_1
v_mfma_f32_16x16x32_bf16 v[196:199],v[104:107],v[136:139],v[196:199]
ds_read_u16_d16_hi v150,v150
v_exp_f16 v120,v121
v_mfma_f32_16x16x32_bf16 v[204:207],v[108:111],v[136:139],v[204:207]
s_waitcnt lgkmcnt(15)
ds_read_b64_tr_b16 v[86:87],v5 offset:22560
v_exp_f16 v122,v123
v_mfma_f32_16x16x32_bf16 v[128:131],v[64:67],v[16:19],v[112:115]
s_cbranch_vccnz RA49_%=
s_mov_b64 exec,s[62:63]
s_add_u32 m0,s57,0
s_add_u32 s64,s56,5898240
buffer_load_dwordx4 v2,s[36:39],s64 offen lds
s_add_u32 m0,s57,1728
s_nop 0
buffer_load_dwordx4 v3,s[36:39],s64 offen lds
s_mov_b64 exec,-1
RA49_%=:
ds_read_u16 v149,v149
v_lshlrev_b32_sdwa v153,1,v153 dst_sel:DWORD dst_unused:UNUSED_PAD src0_sel:DWORD src1_sel:WORD_1
v_lshlrev_b32_sdwa v155,1,v155 dst_sel:DWORD dst_unused:UNUSED_PAD src0_sel:DWORD src1_sel:WORD_1
v_mfma_f32_16x16x32_bf16 v[136:139],v[64:67],v[20:23],v[112:115]
s_waitcnt lgkmcnt(14)
v_exp_f16_sdwa v120,v121 dst_sel:WORD_1 dst_unused:UNUSED_PRESERVE src0_sel:WORD_1
v_mfma_f32_16x16x32_bf16 v[140:143],v[68:71],v[20:23],v[116:119]
ds_read_u16_d16_hi v151,v151
v_exp_f16_sdwa v122,v123 dst_sel:WORD_1 dst_unused:UNUSED_PRESERVE src0_sel:WORD_1
v_mfma_f32_16x16x32_bf16 v[132:135],v[68:71],v[16:19],v[116:119]
s_waitcnt lgkmcnt(12)
v_pk_fma_f16 v120,v120,s55,s55
v_pk_fma_f16 v122,v122,s55,s55
v_mfma_f32_16x16x32_bf16 v[128:131],v[72:75],v[24:27],v[128:131]
ds_read_u16 v152,v152
v_rcp_f16 v121,v120
v_mfma_f32_16x16x32_bf16 v[136:139],v[72:75],v[28:31],v[136:139]
s_waitcnt lgkmcnt(10)
v_rcp_f16 v123,v122
v_mfma_f32_16x16x32_bf16 v[140:143],v[76:79],v[28:31],v[140:143]
ds_read_u16_d16_hi v154,v154
v_rcp_f16_sdwa v121,v120 dst_sel:WORD_1 dst_unused:UNUSED_PRESERVE src0_sel:WORD_1
v_mfma_f32_16x16x32_bf16 v[132:135],v[76:79],v[24:27],v[132:135]
s_waitcnt lgkmcnt(8)
v_rcp_f16_sdwa v123,v122 dst_sel:WORD_1 dst_unused:UNUSED_PRESERVE src0_sel:WORD_1
v_mfma_f32_16x16x32_bf16 v[128:131],v[80:83],v[32:35],v[128:131]
ds_read_u16 v153,v153
v_fma_mix_f32 v156,v156,v121,0 op_sel_hi:[0,1,0]
v_fma_mix_f32 v158,v158,v121,0 op_sel:[0,1,0] op_sel_hi:[0,1,0]
v_mfma_f32_16x16x32_bf16 v[136:139],v[80:83],v[36:39],v[136:139]
s_waitcnt lgkmcnt(3)
v_lshl_add_u64 v[144:145],v[144:145],0,v[146:147]
v_lshl_add_u64 v[146:147],v[148:149],0,v[150:151]
v_mfma_f32_16x16x32_bf16 v[140:143],v[84:87],v[36:39],v[140:143]
ds_read_u16_d16_hi v155,v155
v_fma_mix_f32 v157,v157,v123,0 op_sel_hi:[0,1,0]
v_fma_mix_f32 v159,v159,v123,0 op_sel:[0,1,0] op_sel_hi:[0,1,0]
v_mfma_f32_16x16x32_bf16 v[132:135],v[84:87],v[32:35],v[132:135]
v_lshlrev_b32_sdwa v128,1,v128 dst_sel:DWORD dst_unused:UNUSED_PAD src0_sel:DWORD src1_sel:WORD_1
v_lshlrev_b32_sdwa v130,1,v130 dst_sel:DWORD dst_unused:UNUSED_PAD src0_sel:DWORD src1_sel:WORD_1
v_mfma_f32_16x16x32_bf16 v[208:211],v[88:91],v[144:147],v[208:211]
s_cbranch_vccnz RA50_%=
s_mov_b64 exec,s[62:63]
s_add_u32 m0,s57,3488
s_add_u32 s64,s56,5996544
buffer_load_dwordx4 v2,s[36:39],s64 offen lds
s_add_u32 m0,s57,5216
s_nop 0
buffer_load_dwordx4 v3,s[36:39],s64 offen lds
s_mov_b64 exec,-1
RA50_%=:
ds_read_u16 v128,v128
v_lshlrev_b32_sdwa v129,1,v129 dst_sel:DWORD dst_unused:UNUSED_PAD src0_sel:DWORD src1_sel:WORD_1
v_lshlrev_b32_sdwa v131,1,v131 dst_sel:DWORD dst_unused:UNUSED_PAD src0_sel:DWORD src1_sel:WORD_1
v_mfma_f32_16x16x32_bf16 v[216:219],v[92:95],v[144:147],v[216:219]
v_fma_mixlo_f16 v120,v140,s99,0
v_fma_mixlo_f16 v122,v141,s99,0
v_mfma_f32_16x16x32_bf16 v[224:227],v[96:99],v[144:147],v[224:227]
ds_read_u16_d16_hi v130,v130
v_lshlrev_b32_sdwa v132,1,v132 dst_sel:DWORD dst_unused:UNUSED_PAD src0_sel:DWORD src1_sel:WORD_1
v_lshlrev_b32_sdwa v134,1,v134 dst_sel:DWORD dst_unused:UNUSED_PAD src0_sel:DWORD src1_sel:WORD_1
v_mfma_f32_16x16x32_bf16 v[232:235],v[100:103],v[144:147],v[232:235]
s_waitcnt lgkmcnt(2)
v_lshl_add_u64 v[152:153],v[152:153],0,v[154:155]
v_mfma_f32_16x16x32_bf16 v[240:243],v[104:107],v[144:147],v[240:243]
ds_read_u16 v129,v129
v_cvt_pk_bf16_f32 v154,v156,v158
v_cvt_pk_bf16_f32 v155,v157,v159
v_mfma_f32_16x16x32_bf16 v[248:251],v[108:111],v[144:147],v[248:251]
v_fma_mixhi_f16 v120,v142,s99,0
v_fma_mixhi_f16 v122,v143,s99,0
v_mfma_f32_16x16x32_bf16 v[212:215],v[88:91],v[152:155],v[212:215]
ds_read_u16_d16_hi v131,v131
v_lshlrev_b32_sdwa v133,1,v133 dst_sel:DWORD dst_unused:UNUSED_PAD src0_sel:DWORD src1_sel:WORD_1
v_lshlrev_b32_sdwa v135,1,v135 dst_sel:DWORD dst_unused:UNUSED_PAD src0_sel:DWORD src1_sel:WORD_1
v_mfma_f32_16x16x32_bf16 v[220:223],v[92:95],v[152:155],v[220:223]
s_setprio 2
ds_read_b64_tr_b16 v[88:89],v6 offset:18480
v_pk_fma_f16 v121,v120,v120,s54
v_pk_fma_f16 v123,v122,v122,s54
v_mfma_f32_16x16x32_bf16 v[228:231],v[96:99],v[152:155],v[228:231]
ds_read_u16 v132,v132
v_pk_mul_f16 v121,v120,v121
v_pk_mul_f16 v123,v122,v123
v_mfma_f32_16x16x32_bf16 v[236:239],v[100:103],v[152:155],v[236:239]
ds_read_b64_tr_b16 v[90:91],v6 offset:20016
v_lshlrev_b32_sdwa v136,1,v136 dst_sel:DWORD dst_unused:UNUSED_PAD src0_sel:DWORD src1_sel:WORD_1
v_lshlrev_b32_sdwa v138,1,v138 dst_sel:DWORD dst_unused:UNUSED_PAD src0_sel:DWORD src1_sel:WORD_1
v_mfma_f32_16x16x32_bf16 v[244:247],v[104:107],v[152:155],v[244:247]
ds_read_u16_d16_hi v134,v134
v_exp_f16 v120,v121
v_mfma_f32_16x16x32_bf16 v[252:255],v[108:111],v[152:155],v[252:255]
ds_read_b64_tr_b16 v[92:93],v6 offset:18488
v_exp_f16 v122,v123
v_mfma_f32_16x16x32_bf16 v[144:147],v[64:67],v[40:43],v[112:115]
ds_read_u16 v133,v133
v_lshlrev_b32_sdwa v137,1,v137 dst_sel:DWORD dst_unused:UNUSED_PAD src0_sel:DWORD src1_sel:WORD_1
v_lshlrev_b32_sdwa v139,1,v139 dst_sel:DWORD dst_unused:UNUSED_PAD src0_sel:DWORD src1_sel:WORD_1
v_mfma_f32_16x16x32_bf16 v[152:155],v[64:67],v[44:47],v[112:115]
s_cbranch_vccnz RA51_%=
s_mov_b64 exec,s[62:63]
s_add_u32 m0,s57,6976
s_add_u32 s64,s56,6094848
buffer_load_dwordx4 v2,s[36:39],s64 offen lds
s_add_u32 m0,s57,8704
s_nop 0
buffer_load_dwordx4 v3,s[36:39],s64 offen lds
s_mov_b64 exec,-1
RA51_%=:
ds_read_b64_tr_b16 v[94:95],v6 offset:20024
v_exp_f16_sdwa v120,v121 dst_sel:WORD_1 dst_unused:UNUSED_PRESERVE src0_sel:WORD_1
v_mfma_f32_16x16x32_bf16 v[156:159],v[68:71],v[44:47],v[116:119]
ds_read_u16_d16_hi v135,v135
v_exp_f16_sdwa v122,v123 dst_sel:WORD_1 dst_unused:UNUSED_PRESERVE src0_sel:WORD_1
v_mfma_f32_16x16x32_bf16 v[148:151],v[68:71],v[40:43],v[116:119]
ds_read_b64_tr_b16 v[96:97],v6 offset:18544
v_pk_fma_f16 v120,v120,s55,s55
v_pk_fma_f16 v122,v122,s55,s55
v_mfma_f32_16x16x32_bf16 v[144:147],v[72:75],v[48:51],v[144:147]
ds_read_u16 v136,v136
v_rcp_f16 v121,v120
v_mfma_f32_16x16x32_bf16 v[152:155],v[72:75],v[52:55],v[152:155]
ds_read_b64_tr_b16 v[98:99],v6 offset:20080
ds_read_b64_tr_b16 v[100:101],v6 offset:18552
v_rcp_f16 v123,v122
v_mfma_f32_16x16x32_bf16 v[156:159],v[76:79],v[52:55],v[156:159]
ds_read_u16_d16_hi v138,v138
v_rcp_f16_sdwa v121,v120 dst_sel:WORD_1 dst_unused:UNUSED_PRESERVE src0_sel:WORD_1
v_mfma_f32_16x16x32_bf16 v[148:151],v[76:79],v[48:51],v[148:151]
ds_read_b64_tr_b16 v[102:103],v6 offset:20088
ds_read_b64_tr_b16 v[104:105],v6 offset:18608
v_rcp_f16_sdwa v123,v122 dst_sel:WORD_1 dst_unused:UNUSED_PRESERVE src0_sel:WORD_1
v_mfma_f32_16x16x32_bf16 v[144:147],v[80:83],v[56:59],v[144:147]
ds_read_u16 v137,v137
v_fma_mix_f32 v140,v140,v121,0 op_sel_hi:[0,1,0]
v_fma_mix_f32 v142,v142,v121,0 op_sel:[0,1,0] op_sel_hi:[0,1,0]
v_mfma_f32_16x16x32_bf16 v[152:155],v[80:83],v[60:63],v[152:155]
s_waitcnt lgkmcnt(8)
ds_read_b64_tr_b16 v[106:107],v6 offset:20144
ds_read_b64_tr_b16 v[108:109],v6 offset:18616
v_lshl_add_u64 v[128:129],v[128:129],0,v[130:131]
v_lshl_add_u64 v[130:131],v[132:133],0,v[134:135]
v_mfma_f32_16x16x32_bf16 v[156:159],v[84:87],v[60:63],v[156:159]
ds_read_u16_d16_hi v139,v139
v_fma_mix_f32 v141,v141,v123,0 op_sel_hi:[0,1,0]
v_fma_mix_f32 v143,v143,v123,0 op_sel:[0,1,0] op_sel_hi:[0,1,0]
v_mfma_f32_16x16x32_bf16 v[148:151],v[84:87],v[56:59],v[148:151]
ds_read_b64_tr_b16 v[110:111],v6 offset:20152
ds_read_b64_tr_b16 v[64:65],v4 offset:12832
v_lshlrev_b32_sdwa v144,1,v144 dst_sel:DWORD dst_unused:UNUSED_PAD src0_sel:DWORD src1_sel:WORD_1
v_lshlrev_b32_sdwa v146,1,v146 dst_sel:DWORD dst_unused:UNUSED_PAD src0_sel:DWORD src1_sel:WORD_1
v_mfma_f32_16x16x32_bf16 v[160:163],v[88:91],v[128:131],v[160:163]
ds_read_u16 v144,v144
v_lshlrev_b32_sdwa v145,1,v145 dst_sel:DWORD dst_unused:UNUSED_PAD src0_sel:DWORD src1_sel:WORD_1
v_lshlrev_b32_sdwa v147,1,v147 dst_sel:DWORD dst_unused:UNUSED_PAD src0_sel:DWORD src1_sel:WORD_1
v_mfma_f32_16x16x32_bf16 v[168:171],v[92:95],v[128:131],v[168:171]
s_waitcnt lgkmcnt(11)
s_cbranch_vccnz RA52_%=
s_mov_b64 exec,s[62:63]
s_add_u32 m0,s57,10464
s_add_u32 s64,s56,6193152
buffer_load_dwordx4 v2,s[36:39],s64 offen lds
s_add_u32 m0,s57,12192
s_nop 0
buffer_load_dwordx4 v3,s[36:39],s64 offen lds
s_mov_b64 exec,-1
RA52_%=:
ds_read_b64_tr_b16 v[66:67],v4 offset:14880
ds_read_b128 v[112:115],v7 offset:1152
v_fma_mixlo_f16 v120,v156,s99,0
v_fma_mixlo_f16 v122,v157,s99,0
v_mfma_f32_16x16x32_bf16 v[176:179],v[96:99],v[128:131],v[176:179]
s_waitcnt lgkmcnt(10)
ds_read_u16_d16_hi v146,v146
v_lshlrev_b32_sdwa v148,1,v148 dst_sel:DWORD dst_unused:UNUSED_PAD src0_sel:DWORD src1_sel:WORD_1
v_lshlrev_b32_sdwa v150,1,v150 dst_sel:DWORD dst_unused:UNUSED_PAD src0_sel:DWORD src1_sel:WORD_1
v_mfma_f32_16x16x32_bf16 v[184:187],v[100:103],v[128:131],v[184:187]
s_waitcnt lgkmcnt(6)
ds_read_b64_tr_b16 v[68:69],v5 offset:12832
ds_read_b64_tr_b16 v[70:71],v5 offset:14880
v_lshl_add_u64 v[136:137],v[136:137],0,v[138:139]
v_mfma_f32_16x16x32_bf16 v[192:195],v[104:107],v[128:131],v[192:195]
s_waitcnt lgkmcnt(7)
ds_read_u16 v145,v145
v_cvt_pk_bf16_f32 v138,v140,v142
v_cvt_pk_bf16_f32 v139,v141,v143
v_mfma_f32_16x16x32_bf16 v[200:203],v[108:111],v[128:131],v[200:203]
ds_read_b128 v[116:119],v7 offset:1184
ds_read_b64_tr_b16 v[72:73],v4 offset:16928
v_fma_mixhi_f16 v120,v158,s99,0
v_fma_mixhi_f16 v122,v159,s99,0
v_mfma_f32_16x16x32_bf16 v[164:167],v[88:91],v[136:139],v[164:167]
ds_read_u16_d16_hi v147,v147
v_lshlrev_b32_sdwa v149,1,v149 dst_sel:DWORD dst_unused:UNUSED_PAD src0_sel:DWORD src1_sel:WORD_1
v_lshlrev_b32_sdwa v151,1,v151 dst_sel:DWORD dst_unused:UNUSED_PAD src0_sel:DWORD src1_sel:WORD_1
v_mfma_f32_16x16x32_bf16 v[172:175],v[92:95],v[136:139],v[172:175]
ds_read_b64_tr_b16 v[74:75],v4 offset:18976
ds_read_b64_tr_b16 v[76:77],v5 offset:16928
v_pk_fma_f16 v121,v120,v120,s54
v_pk_fma_f16 v123,v122,v122,s54
v_mfma_f32_16x16x32_bf16 v[180:183],v[96:99],v[136:139],v[180:183]
s_setprio 1
ds_read_u16 v148,v148
v_pk_mul_f16 v121,v120,v121
v_pk_mul_f16 v123,v122,v123
v_mfma_f32_16x16x32_bf16 v[188:191],v[100:103],v[136:139],v[188:191]
ds_read_b64_tr_b16 v[78:79],v5 offset:18976
v_lshlrev_b32_sdwa v152,1,v152 dst_sel:DWORD dst_unused:UNUSED_PAD src0_sel:DWORD src1_sel:WORD_1
v_lshlrev_b32_sdwa v154,1,v154 dst_sel:DWORD dst_unused:UNUSED_PAD src0_sel:DWORD src1_sel:WORD_1
v_mfma_f32_16x16x32_bf16 v[196:199],v[104:107],v[136:139],v[196:199]
ds_read_u16_d16_hi v150,v150
v_exp_f16 v120,v121
v_mfma_f32_16x16x32_bf16 v[204:207],v[108:111],v[136:139],v[204:207]
s_waitcnt lgkmcnt(12)
ds_read_b64_tr_b16 v[80:81],v4 offset:21024
v_exp_f16 v122,v123
v_mfma_f32_16x16x32_bf16 v[128:131],v[64:67],v[16:19],v[112:115]
ds_read_u16 v149,v149
v_lshlrev_b32_sdwa v153,1,v153 dst_sel:DWORD dst_unused:UNUSED_PAD src0_sel:DWORD src1_sel:WORD_1
v_lshlrev_b32_sdwa v155,1,v155 dst_sel:DWORD dst_unused:UNUSED_PAD src0_sel:DWORD src1_sel:WORD_1
v_mfma_f32_16x16x32_bf16 v[136:139],v[64:67],v[20:23],v[112:115]
s_waitcnt lgkmcnt(9)
s_cbranch_vccnz RA53_%=
s_mov_b64 exec,s[62:63]
s_add_u32 m0,s57,13952
s_add_u32 s64,s56,6291456
buffer_load_dwordx4 v2,s[36:39],s64 offen lds
s_add_u32 m0,s57,15680
s_nop 0
buffer_load_dwordx4 v3,s[36:39],s64 offen lds
s_mov_b64 exec,-1
RA53_%=:
ds_read_b64_tr_b16 v[82:83],v4 offset:23072
v_exp_f16_sdwa v120,v121 dst_sel:WORD_1 dst_unused:UNUSED_PRESERVE src0_sel:WORD_1
v_mfma_f32_16x16x32_bf16 v[140:143],v[68:71],v[20:23],v[116:119]
ds_read_u16_d16_hi v151,v151
v_exp_f16_sdwa v122,v123 dst_sel:WORD_1 dst_unused:UNUSED_PRESERVE src0_sel:WORD_1
v_mfma_f32_16x16x32_bf16 v[132:135],v[68:71],v[16:19],v[116:119]
s_waitcnt lgkmcnt(8)
ds_read_b64_tr_b16 v[84:85],v5 offset:21024
v_pk_fma_f16 v120,v120,s55,s55
v_pk_fma_f16 v122,v122,s55,s55
v_mfma_f32_16x16x32_bf16 v[128:131],v[72:75],v[24:27],v[128:131]
ds_read_u16 v152,v152
v_rcp_f16 v121,v120
v_mfma_f32_16x16x32_bf16 v[136:139],v[72:75],v[28:31],v[136:139]
s_waitcnt lgkmcnt(7)
ds_read_b64_tr_b16 v[86:87],v5 offset:23072
v_rcp_f16 v123,v122
v_mfma_f32_16x16x32_bf16 v[140:143],v[76:79],v[28:31],v[140:143]
ds_read_u16_d16_hi v154,v154
v_rcp_f16_sdwa v121,v120 dst_sel:WORD_1 dst_unused:UNUSED_PRESERVE src0_sel:WORD_1
v_mfma_f32_16x16x32_bf16 v[132:135],v[76:79],v[24:27],v[132:135]
s_waitcnt lgkmcnt(5)
v_rcp_f16_sdwa v123,v122 dst_sel:WORD_1 dst_unused:UNUSED_PRESERVE src0_sel:WORD_1
v_mfma_f32_16x16x32_bf16 v[128:131],v[80:83],v[32:35],v[128:131]
ds_read_u16 v153,v153
v_fma_mix_f32 v156,v156,v121,0 op_sel_hi:[0,1,0]
v_fma_mix_f32 v158,v158,v121,0 op_sel:[0,1,0] op_sel_hi:[0,1,0]
v_mfma_f32_16x16x32_bf16 v[136:139],v[80:83],v[36:39],v[136:139]
s_waitcnt lgkmcnt(2)
s_setprio 0
v_lshl_add_u64 v[144:145],v[144:145],0,v[146:147]
v_lshl_add_u64 v[146:147],v[148:149],0,v[150:151]
v_mfma_f32_16x16x32_bf16 v[140:143],v[84:87],v[36:39],v[140:143]
ds_read_u16_d16_hi v155,v155
v_fma_mix_f32 v157,v157,v123,0 op_sel_hi:[0,1,0]
v_fma_mix_f32 v159,v159,v123,0 op_sel:[0,1,0] op_sel_hi:[0,1,0]
v_mfma_f32_16x16x32_bf16 v[132:135],v[84:87],v[32:35],v[132:135]
s_nop 0
s_mov_b32 s86,0xbe801d5a
v_lshlrev_b32_sdwa v128,1,v128 dst_sel:DWORD dst_unused:UNUSED_PAD src0_sel:DWORD src1_sel:WORD_1
v_lshlrev_b32_sdwa v130,1,v130 dst_sel:DWORD dst_unused:UNUSED_PAD src0_sel:DWORD src1_sel:WORD_1
v_mfma_f32_16x16x32_bf16 v[208:211],v[88:91],v[144:147],v[208:211]
s_cbranch_vccz RA54_%=
s_waitcnt vmcnt(0)
RA54_%=:
s_barrier
s_setprio 2
ds_read_u16 v128,v128
v_lshlrev_b32_sdwa v129,1,v129 dst_sel:DWORD dst_unused:UNUSED_PAD src0_sel:DWORD src1_sel:WORD_1
v_mfma_f32_16x16x32_bf16 v[216:219],v[92:95],v[144:147],v[216:219]
s_cbranch_vccnz RA55_%=
s_mov_b64 exec,s[62:63]
s_add_u32 m0,s57,17440
s_add_u32 s64,s56,6389760
buffer_load_dwordx4 v2,s[36:39],s64 offen lds
s_add_u32 m0,s57,19168
s_nop 0
buffer_load_dwordx4 v3,s[36:39],s64 offen lds
s_mov_b64 exec,-1
RA55_%=:
ds_read_u16_d16_hi v130,v130
v_lshlrev_b32_sdwa v131,1,v131 dst_sel:DWORD dst_unused:UNUSED_PAD src0_sel:DWORD src1_sel:WORD_1
v_mfma_f32_16x16x32_bf16 v[224:227],v[96:99],v[144:147],v[224:227]
s_cbranch_vccz RA56_%=
s_add_u32 m0,s58,12320
s_mov_b32 s69,s60
buffer_load_dwordx4 v2,s[44:47],s69 offen lds
s_add_u32 m0,m0,1024
s_add_u32 s69,s69,s53
buffer_load_dwordx4 v3,s[44:47],s69 offen lds
RA56_%=:
v_lshlrev_b32_sdwa v132,1,v132 dst_sel:DWORD dst_unused:UNUSED_PAD src0_sel:DWORD src1_sel:WORD_1
v_mfma_f32_16x16x32_bf16 v[232:235],v[100:103],v[144:147],v[232:235]
s_waitcnt lgkmcnt(2)
ds_read_u16 v129,v129
v_lshl_add_u64 v[152:153],v[152:153],0,v[154:155]
v_lshlrev_b32_sdwa v134,1,v134 dst_sel:DWORD dst_unused:UNUSED_PAD src0_sel:DWORD src1_sel:WORD_1
v_mfma_f32_16x16x32_bf16 v[240:243],v[104:107],v[144:147],v[240:243]
ds_read_u16_d16_hi v131,v131
v_cvt_pk_bf16_f32 v154,v156,v158
v_cvt_pk_bf16_f32 v155,v157,v159
v_mfma_f32_16x16x32_bf16 v[248:251],v[108:111],v[144:147],v[248:251]
v_lshlrev_b32_sdwa v133,1,v133 dst_sel:DWORD dst_unused:UNUSED_PAD src0_sel:DWORD src1_sel:WORD_1
v_mfma_f32_16x16x32_bf16 v[212:215],v[88:91],v[152:155],v[212:215]
s_cbranch_vccz RA57_%=
s_add_u32 m0,m0,1024
s_add_u32 s69,s69,s53
buffer_load_dwordx4 v2,s[44:47],s69 offen lds
s_add_u32 m0,m0,s50
s_add_u32 s69,s69,s53
buffer_load_dwordx4 v3,s[44:47],s69 offen lds
RA57_%=:
ds_read_u16 v132,v132
v_lshlrev_b32_sdwa v135,1,v135 dst_sel:DWORD dst_unused:UNUSED_PAD src0_sel:DWORD src1_sel:WORD_1
v_mfma_f32_16x16x32_bf16 v[220:223],v[92:95],v[152:155],v[220:223]
ds_read_u16_d16_hi v134,v134
v_lshlrev_b32_sdwa v136,1,v136 dst_sel:DWORD dst_unused:UNUSED_PAD src0_sel:DWORD src1_sel:WORD_1
v_mfma_f32_16x16x32_bf16 v[228:231],v[96:99],v[152:155],v[228:231]
ds_read_b64_tr_b16 v[88:89],v6 offset:0
ds_read_b64_tr_b16 v[90:91],v6 offset:1536
v_lshlrev_b32_sdwa v138,1,v138 dst_sel:DWORD dst_unused:UNUSED_PAD src0_sel:DWORD src1_sel:WORD_1
v_mfma_f32_16x16x32_bf16 v[236:239],v[100:103],v[152:155],v[236:239]
ds_read_u16 v133,v133
v_lshlrev_b32_sdwa v137,1,v137 dst_sel:DWORD dst_unused:UNUSED_PAD src0_sel:DWORD src1_sel:WORD_1
v_mfma_f32_16x16x32_bf16 v[244:247],v[104:107],v[152:155],v[244:247]
ds_read_u16_d16_hi v135,v135
v_lshlrev_b32_sdwa v139,1,v139 dst_sel:DWORD dst_unused:UNUSED_PAD src0_sel:DWORD src1_sel:WORD_1
v_mfma_f32_16x16x32_bf16 v[252:255],v[108:111],v[152:155],v[252:255]
s_cbranch_vccz RA58_%=
s_add_u32 m0,m0,1024
s_add_u32 s69,s69,s53
buffer_load_dwordx4 v2,s[44:47],s69 offen lds
s_add_u32 m0,m0,1024
s_add_u32 s69,s69,s53
buffer_load_dwordx4 v3,s[44:47],s69 offen lds
RA58_%=:
ds_read_b64_tr_b16 v[92:93],v6 offset:8
ds_read_b64_tr_b16 v[94:95],v6 offset:1544
v_lshlrev_b32_sdwa v140,1,v140 dst_sel:DWORD dst_unused:UNUSED_PAD src0_sel:DWORD src1_sel:WORD_1
v_mfma_f32_16x16x32_bf16 v[144:147],v[64:67],v[40:43],v[112:115]
ds_read_u16 v136,v136
v_lshlrev_b32_sdwa v142,1,v142 dst_sel:DWORD dst_unused:UNUSED_PAD src0_sel:DWORD src1_sel:WORD_1
v_mfma_f32_16x16x32_bf16 v[152:155],v[64:67],v[44:47],v[112:115]
ds_read_u16_d16_hi v138,v138
v_lshlrev_b32_sdwa v141,1,v141 dst_sel:DWORD dst_unused:UNUSED_PAD src0_sel:DWORD src1_sel:WORD_1
v_mfma_f32_16x16x32_bf16 v[156:159],v[68:71],v[44:47],v[116:119]
ds_read_b64_tr_b16 v[96:97],v6 offset:64
ds_read_b64_tr_b16 v[98:99],v6 offset:1600
v_lshlrev_b32_sdwa v143,1,v143 dst_sel:DWORD dst_unused:UNUSED_PAD src0_sel:DWORD src1_sel:WORD_1
v_mfma_f32_16x16x32_bf16 v[148:151],v[68:71],v[40:43],v[116:119]
ds_read_u16 v137,v137
v_mfma_f32_16x16x32_bf16 v[144:147],v[72:75],v[48:51],v[144:147]
ds_read_u16_d16_hi v139,v139
v_mfma_f32_16x16x32_bf16 v[152:155],v[72:75],v[52:55],v[152:155]
s_cbranch_vccz RA59_%=
s_add_u32 m0,m0,s50
s_add_u32 s69,s69,s51
buffer_load_dwordx4 v2,s[44:47],s69 offen lds
s_add_u32 m0,m0,1024
s_add_u32 s69,s69,s53
buffer_load_dwordx4 v3,s[44:47],s69 offen lds
RA59_%=:
ds_read_b64_tr_b16 v[100:101],v6 offset:72
ds_read_b64_tr_b16 v[102:103],v6 offset:1608
v_mfma_f32_16x16x32_bf16 v[156:159],v[76:79],v[52:55],v[156:159]
ds_read_u16 v140,v140
v_mfma_f32_16x16x32_bf16 v[148:151],v[76:79],v[48:51],v[148:151]
s_waitcnt lgkmcnt(15)
ds_read_u16_d16_hi v142,v142
v_lshl_add_u64 v[128:129],v[128:129],0,v[130:131]
v_mfma_f32_16x16x32_bf16 v[144:147],v[80:83],v[56:59],v[144:147]
s_waitcnt lgkmcnt(12)
ds_read_b64_tr_b16 v[104:105],v6 offset:128
ds_read_b64_tr_b16 v[106:107],v6 offset:1664
v_lshl_add_u64 v[130:131],v[132:133],0,v[134:135]
v_mfma_f32_16x16x32_bf16 v[152:155],v[80:83],v[60:63],v[152:155]
ds_read_u16 v141,v141
v_mfma_f32_16x16x32_bf16 v[156:159],v[84:87],v[60:63],v[156:159]
ds_read_u16_d16_hi v143,v143
v_mfma_f32_16x16x32_bf16 v[148:151],v[84:87],v[56:59],v[148:151]
s_cbranch_vccz RA60_%=
s_add_u32 m0,m0,1024
s_add_u32 s69,s69,s53
buffer_load_dwordx4 v2,s[44:47],s69 offen lds
s_add_u32 m0,m0,s50
s_add_u32 s69,s69,s53
buffer_load_dwordx4 v3,s[44:47],s69 offen lds
RA60_%=:
ds_read_b64_tr_b16 v[108:109],v6 offset:136
ds_read_b64_tr_b16 v[110:111],v6 offset:1672
v_lshlrev_b32_sdwa v144,1,v144 dst_sel:DWORD dst_unused:UNUSED_PAD src0_sel:DWORD src1_sel:WORD_1
v_lshlrev_b32_sdwa v146,1,v146 dst_sel:DWORD dst_unused:UNUSED_PAD src0_sel:DWORD src1_sel:WORD_1
v_mfma_f32_16x16x32_bf16 v[160:163],v[88:91],v[128:131],v[160:163]
s_waitcnt lgkmcnt(15)
ds_read_u16 v144,v144
v_lshlrev_b32_sdwa v145,1,v145 dst_sel:DWORD dst_unused:UNUSED_PAD src0_sel:DWORD src1_sel:WORD_1
v_mfma_f32_16x16x32_bf16 v[168:171],v[92:95],v[128:131],v[168:171]
s_waitcnt lgkmcnt(13)
ds_read_u16_d16_hi v146,v146
v_lshlrev_b32_sdwa v147,1,v147 dst_sel:DWORD dst_unused:UNUSED_PAD src0_sel:DWORD src1_sel:WORD_1
v_mfma_f32_16x16x32_bf16 v[176:179],v[96:99],v[128:131],v[176:179]
s_waitcnt lgkmcnt(10)
ds_read_b64_tr_b16 v[64:65],v4 offset:0
ds_read_b64_tr_b16 v[66:67],v4 offset:2048
v_lshlrev_b32_sdwa v148,1,v148 dst_sel:DWORD dst_unused:UNUSED_PAD src0_sel:DWORD src1_sel:WORD_1
v_mfma_f32_16x16x32_bf16 v[184:187],v[100:103],v[128:131],v[184:187]
s_waitcnt lgkmcnt(8)
ds_read_u16 v145,v145
v_lshl_add_u64 v[136:137],v[136:137],0,v[138:139]
v_lshlrev_b32_sdwa v150,1,v150 dst_sel:DWORD dst_unused:UNUSED_PAD src0_sel:DWORD src1_sel:WORD_1
v_mfma_f32_16x16x32_bf16 v[192:195],v[104:107],v[128:131],v[192:195]
s_waitcnt lgkmcnt(5)
s_cbranch_vccz RA61_%=
s_add_u32 m0,m0,1024
s_add_u32 s69,s69,s53
buffer_load_dwordx4 v2,s[44:47],s69 offen lds
s_add_u32 m0,m0,1024
s_add_u32 s69,s69,s53
buffer_load_dwordx4 v3,s[44:47],s69 offen lds
RA61_%=:
ds_read_u16_d16_hi v147,v147
v_lshl_add_u64 v[138:139],v[140:141],0,v[142:143]
v_mfma_f32_16x16x32_bf16 v[200:203],v[108:111],v[128:131],v[200:203]
ds_read_b128 v[112:115],v7 offset:1280
ds_read_b64_tr_b16 v[68:69],v5 offset:0
v_lshlrev_b32_sdwa v149,1,v149 dst_sel:DWORD dst_unused:UNUSED_PAD src0_sel:DWORD src1_sel:WORD_1
v_mfma_f32_16x16x32_bf16 v[164:167],v[88:91],v[136:139],v[164:167]
ds_read_u16 v148,v148
v_lshlrev_b32_sdwa v151,1,v151 dst_sel:DWORD dst_unused:UNUSED_PAD src0_sel:DWORD src1_sel:WORD_1
v_mfma_f32_16x16x32_bf16 v[172:175],v[92:95],v[136:139],v[172:175]
ds_read_u16_d16_hi v150,v150
ds_read_b64_tr_b16 v[70:71],v5 offset:2048
v_lshlrev_b32_sdwa v152,1,v152 dst_sel:DWORD dst_unused:UNUSED_PAD src0_sel:DWORD src1_sel:WORD_1
v_mfma_f32_16x16x32_bf16 v[180:183],v[96:99],v[136:139],v[180:183]
ds_read_b128 v[116:119],v7 offset:1312
ds_read_b64_tr_b16 v[72:73],v4 offset:4096
v_lshlrev_b32_sdwa v154,1,v154 dst_sel:DWORD dst_unused:UNUSED_PAD src0_sel:DWORD src1_sel:WORD_1
v_mfma_f32_16x16x32_bf16 v[188:191],v[100:103],v[136:139],v[188:191]
ds_read_u16 v149,v149
v_lshlrev_b32_sdwa v153,1,v153 dst_sel:DWORD dst_unused:UNUSED_PAD src0_sel:DWORD src1_sel:WORD_1
v_mfma_f32_16x16x32_bf16 v[196:199],v[104:107],v[136:139],v[196:199]
ds_read_u16_d16_hi v151,v151
v_lshlrev_b32_sdwa v155,1,v155 dst_sel:DWORD dst_unused:UNUSED_PAD src0_sel:DWORD src1_sel:WORD_1
v_mfma_f32_16x16x32_bf16 v[204:207],v[108:111],v[136:139],v[204:207]
s_waitcnt lgkmcnt(8)
ds_read_b64_tr_b16 v[74:75],v4 offset:6144
ds_read_b64_tr_b16 v[76:77],v5 offset:4096
v_lshlrev_b32_sdwa v156,1,v156 dst_sel:DWORD dst_unused:UNUSED_PAD src0_sel:DWORD src1_sel:WORD_1
v_mfma_f32_16x16x32_bf16 v[128:131],v[64:67],v[16:19],v[112:115]
ds_read_u16 v152,v152
v_lshlrev_b32_sdwa v158,1,v158 dst_sel:DWORD dst_unused:UNUSED_PAD src0_sel:DWORD src1_sel:WORD_1
v_mfma_f32_16x16x32_bf16 v[136:139],v[64:67],v[20:23],v[112:115]
s_waitcnt lgkmcnt(6)
ds_read_u16_d16_hi v154,v154
ds_read_b64_tr_b16 v[78:79],v5 offset:6144
v_lshlrev_b32_sdwa v157,1,v157 dst_sel:DWORD dst_unused:UNUSED_PAD src0_sel:DWORD src1_sel:WORD_1
v_mfma_f32_16x16x32_bf16 v[140:143],v[68:71],v[20:23],v[116:119]
ds_read_b64_tr_b16 v[80:81],v4 offset:8192
ds_read_b64_tr_b16 v[82:83],v4 offset:10240
v_lshlrev_b32_sdwa v159,1,v159 dst_sel:DWORD dst_unused:UNUSED_PAD src0_sel:DWORD src1_sel:WORD_1
v_mfma_f32_16x16x32_bf16 v[132:135],v[68:71],v[16:19],v[116:119]
s_waitcnt lgkmcnt(6)
ds_read_u16 v153,v153
v_mfma_f32_16x16x32_bf16 v[128:131],v[72:75],v[24:27],v[128:131]
ds_read_u16_d16_hi v155,v155
v_mfma_f32_16x16x32_bf16 v[136:139],v[72:75],v[28:31],v[136:139]
s_waitcnt lgkmcnt(4)
ds_read_b64_tr_b16 v[84:85],v5 offset:8192
ds_read_b64_tr_b16 v[86:87],v5 offset:10240
v_mfma_f32_16x16x32_bf16 v[140:143],v[76:79],v[28:31],v[140:143]
ds_read_u16 v156,v156
v_mfma_f32_16x16x32_bf16 v[132:135],v[76:79],v[24:27],v[132:135]
s_waitcnt lgkmcnt(5)
ds_read_u16_d16_hi v158,v158
v_lshl_add_u64 v[144:145],v[144:145],0,v[146:147]
v_mfma_f32_16x16x32_bf16 v[128:131],v[80:83],v[32:35],v[128:131]
v_lshl_add_u64 v[146:147],v[148:149],0,v[150:151]
v_mfma_f32_16x16x32_bf16 v[136:139],v[80:83],v[36:39],v[136:139]
s_waitcnt lgkmcnt(2)
ds_read_u16 v157,v157
v_mfma_f32_16x16x32_bf16 v[140:143],v[84:87],v[36:39],v[140:143]
ds_read_u16_d16_hi v159,v159
v_mfma_f32_16x16x32_bf16 v[132:135],v[84:87],v[32:35],v[132:135]
s_nop 0
v_lshlrev_b32_sdwa v128,1,v128 dst_sel:DWORD dst_unused:UNUSED_PAD src0_sel:DWORD src1_sel:WORD_1
v_lshlrev_b32_sdwa v130,1,v130 dst_sel:DWORD dst_unused:UNUSED_PAD src0_sel:DWORD src1_sel:WORD_1
v_mfma_f32_16x16x32_bf16 v[208:211],v[88:91],v[144:147],v[208:211]
ds_read_u16 v128,v128
v_lshlrev_b32_sdwa v129,1,v129 dst_sel:DWORD dst_unused:UNUSED_PAD src0_sel:DWORD src1_sel:WORD_1
v_mfma_f32_16x16x32_bf16 v[216:219],v[92:95],v[144:147],v[216:219]
ds_read_u16_d16_hi v130,v130
v_lshlrev_b32_sdwa v131,1,v131 dst_sel:DWORD dst_unused:UNUSED_PAD src0_sel:DWORD src1_sel:WORD_1
v_mfma_f32_16x16x32_bf16 v[224:227],v[96:99],v[144:147],v[224:227]
v_lshlrev_b32_sdwa v132,1,v132 dst_sel:DWORD dst_unused:UNUSED_PAD src0_sel:DWORD src1_sel:WORD_1
v_mfma_f32_16x16x32_bf16 v[232:235],v[100:103],v[144:147],v[232:235]
ds_read_u16 v129,v129
v_lshl_add_u64 v[152:153],v[152:153],0,v[154:155]
v_lshlrev_b32_sdwa v134,1,v134 dst_sel:DWORD dst_unused:UNUSED_PAD src0_sel:DWORD src1_sel:WORD_1
v_mfma_f32_16x16x32_bf16 v[240:243],v[104:107],v[144:147],v[240:243]
s_waitcnt lgkmcnt(3)
ds_read_u16_d16_hi v131,v131
v_lshl_add_u64 v[154:155],v[156:157],0,v[158:159]
v_mfma_f32_16x16x32_bf16 v[248:251],v[108:111],v[144:147],v[248:251]
v_lshlrev_b32_sdwa v133,1,v133 dst_sel:DWORD dst_unused:UNUSED_PAD src0_sel:DWORD src1_sel:WORD_1
v_mfma_f32_16x16x32_bf16 v[212:215],v[88:91],v[152:155],v[212:215]
ds_read_u16 v132,v132
v_lshlrev_b32_sdwa v135,1,v135 dst_sel:DWORD dst_unused:UNUSED_PAD src0_sel:DWORD src1_sel:WORD_1
v_mfma_f32_16x16x32_bf16 v[220:223],v[92:95],v[152:155],v[220:223]
ds_read_u16_d16_hi v134,v134
v_lshlrev_b32_sdwa v136,1,v136 dst_sel:DWORD dst_unused:UNUSED_PAD src0_sel:DWORD src1_sel:WORD_1
v_mfma_f32_16x16x32_bf16 v[228:231],v[96:99],v[152:155],v[228:231]
ds_read_b64_tr_b16 v[88:89],v6 offset:6160
ds_read_b64_tr_b16 v[90:91],v6 offset:7696
v_lshlrev_b32_sdwa v138,1,v138 dst_sel:DWORD dst_unused:UNUSED_PAD src0_sel:DWORD src1_sel:WORD_1
v_mfma_f32_16x16x32_bf16 v[236:239],v[100:103],v[152:155],v[236:239]
ds_read_u16 v133,v133
v_lshlrev_b32_sdwa v137,1,v137 dst_sel:DWORD dst_unused:UNUSED_PAD src0_sel:DWORD src1_sel:WORD_1
v_mfma_f32_16x16x32_bf16 v[244:247],v[104:107],v[152:155],v[244:247]
ds_read_u16_d16_hi v135,v135
v_lshlrev_b32_sdwa v139,1,v139 dst_sel:DWORD dst_unused:UNUSED_PAD src0_sel:DWORD src1_sel:WORD_1
v_mfma_f32_16x16x32_bf16 v[252:255],v[108:111],v[152:155],v[252:255]
ds_read_b64_tr_b16 v[92:93],v6 offset:6168
ds_read_b64_tr_b16 v[94:95],v6 offset:7704
v_lshlrev_b32_sdwa v140,1,v140 dst_sel:DWORD dst_unused:UNUSED_PAD src0_sel:DWORD src1_sel:WORD_1
v_mfma_f32_16x16x32_bf16 v[144:147],v[64:67],v[40:43],v[112:115]
ds_read_u16 v136,v136
v_lshlrev_b32_sdwa v142,1,v142 dst_sel:DWORD dst_unused:UNUSED_PAD src0_sel:DWORD src1_sel:WORD_1
v_mfma_f32_16x16x32_bf16 v[152:155],v[64:67],v[44:47],v[112:115]
ds_read_u16_d16_hi v138,v138
v_lshlrev_b32_sdwa v141,1,v141 dst_sel:DWORD dst_unused:UNUSED_PAD src0_sel:DWORD src1_sel:WORD_1
v_mfma_f32_16x16x32_bf16 v[156:159],v[68:71],v[44:47],v[116:119]
ds_read_b64_tr_b16 v[96:97],v6 offset:6224
ds_read_b64_tr_b16 v[98:99],v6 offset:7760
v_lshlrev_b32_sdwa v143,1,v143 dst_sel:DWORD dst_unused:UNUSED_PAD src0_sel:DWORD src1_sel:WORD_1
v_mfma_f32_16x16x32_bf16 v[148:151],v[68:71],v[40:43],v[116:119]
ds_read_u16 v137,v137
v_mfma_f32_16x16x32_bf16 v[144:147],v[72:75],v[48:51],v[144:147]
ds_read_u16_d16_hi v139,v139
v_mfma_f32_16x16x32_bf16 v[152:155],v[72:75],v[52:55],v[152:155]
ds_read_b64_tr_b16 v[100:101],v6 offset:6232
ds_read_b64_tr_b16 v[102:103],v6 offset:7768
v_mfma_f32_16x16x32_bf16 v[156:159],v[76:79],v[52:55],v[156:159]
ds_read_u16 v140,v140
v_mfma_f32_16x16x32_bf16 v[148:151],v[76:79],v[48:51],v[148:151]
s_waitcnt lgkmcnt(15)
ds_read_u16_d16_hi v142,v142
v_lshl_add_u64 v[128:129],v[128:129],0,v[130:131]
v_mfma_f32_16x16x32_bf16 v[144:147],v[80:83],v[56:59],v[144:147]
s_waitcnt lgkmcnt(12)
ds_read_b64_tr_b16 v[104:105],v6 offset:6288
ds_read_b64_tr_b16 v[106:107],v6 offset:7824
v_lshl_add_u64 v[130:131],v[132:133],0,v[134:135]
v_mfma_f32_16x16x32_bf16 v[152:155],v[80:83],v[60:63],v[152:155]
ds_read_u16 v141,v141
v_mfma_f32_16x16x32_bf16 v[156:159],v[84:87],v[60:63],v[156:159]
ds_read_u16_d16_hi v143,v143
v_mfma_f32_16x16x32_bf16 v[148:151],v[84:87],v[56:59],v[148:151]
ds_read_b64_tr_b16 v[108:109],v6 offset:6296
ds_read_b64_tr_b16 v[110:111],v6 offset:7832
v_lshlrev_b32_sdwa v144,1,v144 dst_sel:DWORD dst_unused:UNUSED_PAD src0_sel:DWORD src1_sel:WORD_1
v_lshlrev_b32_sdwa v146,1,v146 dst_sel:DWORD dst_unused:UNUSED_PAD src0_sel:DWORD src1_sel:WORD_1
v_mfma_f32_16x16x32_bf16 v[160:163],v[88:91],v[128:131],v[160:163]
s_waitcnt lgkmcnt(15)
ds_read_u16 v144,v144
v_lshlrev_b32_sdwa v145,1,v145 dst_sel:DWORD dst_unused:UNUSED_PAD src0_sel:DWORD src1_sel:WORD_1
v_mfma_f32_16x16x32_bf16 v[168:171],v[92:95],v[128:131],v[168:171]
s_waitcnt lgkmcnt(13)
ds_read_u16_d16_hi v146,v146
v_lshlrev_b32_sdwa v147,1,v147 dst_sel:DWORD dst_unused:UNUSED_PAD src0_sel:DWORD src1_sel:WORD_1
v_mfma_f32_16x16x32_bf16 v[176:179],v[96:99],v[128:131],v[176:179]
s_waitcnt lgkmcnt(10)
ds_read_b64_tr_b16 v[64:65],v4 offset:512
ds_read_b64_tr_b16 v[66:67],v4 offset:2560
v_lshlrev_b32_sdwa v148,1,v148 dst_sel:DWORD dst_unused:UNUSED_PAD src0_sel:DWORD src1_sel:WORD_1
v_mfma_f32_16x16x32_bf16 v[184:187],v[100:103],v[128:131],v[184:187]
s_waitcnt lgkmcnt(8)
ds_read_u16 v145,v145
v_lshl_add_u64 v[136:137],v[136:137],0,v[138:139]
v_lshlrev_b32_sdwa v150,1,v150 dst_sel:DWORD dst_unused:UNUSED_PAD src0_sel:DWORD src1_sel:WORD_1
v_mfma_f32_16x16x32_bf16 v[192:195],v[104:107],v[128:131],v[192:195]
s_waitcnt lgkmcnt(5)
ds_read_u16_d16_hi v147,v147
v_lshl_add_u64 v[138:139],v[140:141],0,v[142:143]
v_mfma_f32_16x16x32_bf16 v[200:203],v[108:111],v[128:131],v[200:203]
ds_read_b128 v[112:115],v7 offset:1408
ds_read_b64_tr_b16 v[68:69],v5 offset:512
v_lshlrev_b32_sdwa v149,1,v149 dst_sel:DWORD dst_unused:UNUSED_PAD src0_sel:DWORD src1_sel:WORD_1
v_mfma_f32_16x16x32_bf16 v[164:167],v[88:91],v[136:139],v[164:167]
ds_read_u16 v148,v148
v_lshlrev_b32_sdwa v151,1,v151 dst_sel:DWORD dst_unused:UNUSED_PAD src0_sel:DWORD src1_sel:WORD_1
v_mfma_f32_16x16x32_bf16 v[172:175],v[92:95],v[136:139],v[172:175]
ds_read_u16_d16_hi v150,v150
ds_read_b64_tr_b16 v[70:71],v5 offset:2560
v_lshlrev_b32_sdwa v152,1,v152 dst_sel:DWORD dst_unused:UNUSED_PAD src0_sel:DWORD src1_sel:WORD_1
v_mfma_f32_16x16x32_bf16 v[180:183],v[96:99],v[136:139],v[180:183]
ds_read_b128 v[116:119],v7 offset:1440
ds_read_b64_tr_b16 v[72:73],v4 offset:4608
v_lshlrev_b32_sdwa v154,1,v154 dst_sel:DWORD dst_unused:UNUSED_PAD src0_sel:DWORD src1_sel:WORD_1
v_mfma_f32_16x16x32_bf16 v[188:191],v[100:103],v[136:139],v[188:191]
ds_read_u16 v149,v149
v_lshlrev_b32_sdwa v153,1,v153 dst_sel:DWORD dst_unused:UNUSED_PAD src0_sel:DWORD src1_sel:WORD_1
v_mfma_f32_16x16x32_bf16 v[196:199],v[104:107],v[136:139],v[196:199]
ds_read_u16_d16_hi v151,v151
v_lshlrev_b32_sdwa v155,1,v155 dst_sel:DWORD dst_unused:UNUSED_PAD src0_sel:DWORD src1_sel:WORD_1
v_mfma_f32_16x16x32_bf16 v[204:207],v[108:111],v[136:139],v[204:207]
s_waitcnt lgkmcnt(8)
ds_read_b64_tr_b16 v[74:75],v4 offset:6656
ds_read_b64_tr_b16 v[76:77],v5 offset:4608
v_lshlrev_b32_sdwa v156,1,v156 dst_sel:DWORD dst_unused:UNUSED_PAD src0_sel:DWORD src1_sel:WORD_1
v_mfma_f32_16x16x32_bf16 v[128:131],v[64:67],v[16:19],v[112:115]
ds_read_u16 v152,v152
v_lshlrev_b32_sdwa v158,1,v158 dst_sel:DWORD dst_unused:UNUSED_PAD src0_sel:DWORD src1_sel:WORD_1
v_mfma_f32_16x16x32_bf16 v[136:139],v[64:67],v[20:23],v[112:115]
s_waitcnt lgkmcnt(6)
ds_read_u16_d16_hi v154,v154
ds_read_b64_tr_b16 v[78:79],v5 offset:6656
v_lshlrev_b32_sdwa v157,1,v157 dst_sel:DWORD dst_unused:UNUSED_PAD src0_sel:DWORD src1_sel:WORD_1
v_mfma_f32_16x16x32_bf16 v[140:143],v[68:71],v[20:23],v[116:119]
ds_read_b64_tr_b16 v[80:81],v4 offset:8704
ds_read_b64_tr_b16 v[82:83],v4 offset:10752
v_lshlrev_b32_sdwa v159,1,v159 dst_sel:DWORD dst_unused:UNUSED_PAD src0_sel:DWORD src1_sel:WORD_1
v_mfma_f32_16x16x32_bf16 v[132:135],v[68:71],v[16:19],v[116:119]
s_waitcnt lgkmcnt(6)
ds_read_u16 v153,v153
v_mfma_f32_16x16x32_bf16 v[128:131],v[72:75],v[24:27],v[128:131]
ds_read_u16_d16_hi v155,v155
v_mfma_f32_16x16x32_bf16 v[136:139],v[72:75],v[28:31],v[136:139]
s_waitcnt lgkmcnt(4)
ds_read_b64_tr_b16 v[84:85],v5 offset:8704
ds_read_b64_tr_b16 v[86:87],v5 offset:10752
v_mfma_f32_16x16x32_bf16 v[140:143],v[76:79],v[28:31],v[140:143]
ds_read_u16 v156,v156
v_mfma_f32_16x16x32_bf16 v[132:135],v[76:79],v[24:27],v[132:135]
s_waitcnt lgkmcnt(5)
ds_read_u16_d16_hi v158,v158
v_lshl_add_u64 v[144:145],v[144:145],0,v[146:147]
v_mfma_f32_16x16x32_bf16 v[128:131],v[80:83],v[32:35],v[128:131]
v_lshl_add_u64 v[146:147],v[148:149],0,v[150:151]
v_mfma_f32_16x16x32_bf16 v[136:139],v[80:83],v[36:39],v[136:139]
s_waitcnt lgkmcnt(2)
ds_read_u16 v157,v157
v_mfma_f32_16x16x32_bf16 v[140:143],v[84:87],v[36:39],v[140:143]
ds_read_u16_d16_hi v159,v159
v_mfma_f32_16x16x32_bf16 v[132:135],v[84:87],v[32:35],v[132:135]
s_nop 0
v_lshlrev_b32_sdwa v128,1,v128 dst_sel:DWORD dst_unused:UNUSED_PAD src0_sel:DWORD src1_sel:WORD_1
v_lshlrev_b32_sdwa v130,1,v130 dst_sel:DWORD dst_unused:UNUSED_PAD src0_sel:DWORD src1_sel:WORD_1
v_mfma_f32_16x16x32_bf16 v[208:211],v[88:91],v[144:147],v[208:211]
s_waitcnt vmcnt(0)
s_barrier
s_setprio 3
ds_read_u16 v128,v128
v_lshlrev_b32_sdwa v129,1,v129 dst_sel:DWORD dst_unused:UNUSED_PAD src0_sel:DWORD src1_sel:WORD_1
v_lshlrev_b32_sdwa v131,1,v131 dst_sel:DWORD dst_unused:UNUSED_PAD src0_sel:DWORD src1_sel:WORD_1
v_mfma_f32_16x16x32_bf16 v[216:219],v[92:95],v[144:147],v[216:219]
v_fma_mixlo_f16 v120,v140,s99,0
v_fma_mixlo_f16 v122,v141,s99,0
v_mfma_f32_16x16x32_bf16 v[224:227],v[96:99],v[144:147],v[224:227]
s_cbranch_vccz RA62_%=
s_add_u32 m0,s58,0
s_mul_i32 s69,s59,1
s_add_u32 s69,s69,s97
buffer_load_dwordx4 v2,s[44:47],s69 offen lds
s_add_u32 m0,m0,1024
s_add_u32 s69,s69,s53
buffer_load_dwordx4 v3,s[44:47],s69 offen lds
RA62_%=:
ds_read_u16_d16_hi v130,v130
v_lshlrev_b32_sdwa v132,1,v132 dst_sel:DWORD dst_unused:UNUSED_PAD src0_sel:DWORD src1_sel:WORD_1
v_lshlrev_b32_sdwa v134,1,v134 dst_sel:DWORD dst_unused:UNUSED_PAD src0_sel:DWORD src1_sel:WORD_1
v_mfma_f32_16x16x32_bf16 v[232:235],v[100:103],v[144:147],v[232:235]
v_lshl_add_u64 v[152:153],v[152:153],0,v[154:155]
v_mfma_f32_16x16x32_bf16 v[240:243],v[104:107],v[144:147],v[240:243]
s_waitcnt lgkmcnt(2)
ds_read_u16 v129,v129
v_lshl_add_u64 v[154:155],v[156:157],0,v[158:159]
v_mfma_f32_16x16x32_bf16 v[248:251],v[108:111],v[144:147],v[248:251]
v_fma_mixhi_f16 v120,v142,s99,0
v_fma_mixhi_f16 v122,v143,s99,0
v_mfma_f32_16x16x32_bf16 v[212:215],v[88:91],v[152:155],v[212:215]
s_cbranch_vccz RA63_%=
s_add_u32 m0,m0,1024
s_add_u32 s69,s69,s53
buffer_load_dwordx4 v2,s[44:47],s69 offen lds
s_add_u32 m0,m0,s50
s_add_u32 s69,s69,s53
buffer_load_dwordx4 v3,s[44:47],s69 offen lds
RA63_%=:
ds_read_u16_d16_hi v131,v131
v_lshlrev_b32_sdwa v133,1,v133 dst_sel:DWORD dst_unused:UNUSED_PAD src0_sel:DWORD src1_sel:WORD_1
v_lshlrev_b32_sdwa v135,1,v135 dst_sel:DWORD dst_unused:UNUSED_PAD src0_sel:DWORD src1_sel:WORD_1
v_mfma_f32_16x16x32_bf16 v[220:223],v[92:95],v[152:155],v[220:223]
ds_read_b64_tr_b16 v[88:89],v6 offset:12320
v_pk_fma_f16 v121,v120,v120,s54
v_pk_fma_f16 v123,v122,v122,s54
v_mfma_f32_16x16x32_bf16 v[228:231],v[96:99],v[152:155],v[228:231]
ds_read_u16 v132,v132
v_pk_mul_f16 v121,v120,v121
v_pk_mul_f16 v123,v122,v123
v_mfma_f32_16x16x32_bf16 v[236:239],v[100:103],v[152:155],v[236:239]
ds_read_b64_tr_b16 v[90:91],v6 offset:13856
v_lshlrev_b32_sdwa v136,1,v136 dst_sel:DWORD dst_unused:UNUSED_PAD src0_sel:DWORD src1_sel:WORD_1
v_lshlrev_b32_sdwa v138,1,v138 dst_sel:DWORD dst_unused:UNUSED_PAD src0_sel:DWORD src1_sel:WORD_1
v_mfma_f32_16x16x32_bf16 v[244:247],v[104:107],v[152:155],v[244:247]
s_cbranch_vccz RA64_%=
s_add_u32 m0,m0,1024
s_add_u32 s69,s69,s53
buffer_load_dwordx4 v2,s[44:47],s69 offen lds
s_add_u32 m0,m0,1024
s_add_u32 s69,s69,s53
buffer_load_dwordx4 v3,s[44:47],s69 offen lds
RA64_%=:
ds_read_u16_d16_hi v134,v134
v_exp_f16 v120,v121
v_mfma_f32_16x16x32_bf16 v[252:255],v[108:111],v[152:155],v[252:255]
ds_read_b64_tr_b16 v[92:93],v6 offset:12328
v_exp_f16 v122,v123
v_mfma_f32_16x16x32_bf16 v[144:147],v[64:67],v[40:43],v[112:115]
ds_read_u16 v133,v133
v_lshlrev_b32_sdwa v137,1,v137 dst_sel:DWORD dst_unused:UNUSED_PAD src0_sel:DWORD src1_sel:WORD_1
v_lshlrev_b32_sdwa v139,1,v139 dst_sel:DWORD dst_unused:UNUSED_PAD src0_sel:DWORD src1_sel:WORD_1
v_mfma_f32_16x16x32_bf16 v[152:155],v[64:67],v[44:47],v[112:115]
s_cbranch_vccz RA65_%=
s_add_u32 m0,m0,s50
s_add_u32 s69,s69,s53
buffer_load_dwordx4 v2,s[44:47],s69 offen lds
s_add_u32 m0,m0,1024
s_add_u32 s69,s69,s53
buffer_load_dwordx4 v3,s[44:47],s69 offen lds
RA65_%=:
ds_read_b64_tr_b16 v[94:95],v6 offset:13864
v_exp_f16_sdwa v120,v121 dst_sel:WORD_1 dst_unused:UNUSED_PRESERVE src0_sel:WORD_1
v_mfma_f32_16x16x32_bf16 v[156:159],v[68:71],v[44:47],v[116:119]
ds_read_u16_d16_hi v135,v135
v_exp_f16_sdwa v122,v123 dst_sel:WORD_1 dst_unused:UNUSED_PRESERVE src0_sel:WORD_1
v_mfma_f32_16x16x32_bf16 v[148:151],v[68:71],v[40:43],v[116:119]
ds_read_b64_tr_b16 v[96:97],v6 offset:12384
v_pk_fma_f16 v120,v120,s55,s55
v_pk_fma_f16 v122,v122,s55,s55
v_mfma_f32_16x16x32_bf16 v[144:147],v[72:75],v[48:51],v[144:147]
ds_read_u16 v136,v136
v_rcp_f16 v121,v120
v_mfma_f32_16x16x32_bf16 v[152:155],v[72:75],v[52:55],v[152:155]
s_cbranch_vccz RA66_%=
s_add_u32 m0,m0,1024
s_add_u32 s69,s69,s53
buffer_load_dwordx4 v2,s[44:47],s69 offen lds
s_add_u32 m0,m0,s50
s_add_u32 s69,s69,s53
buffer_load_dwordx4 v3,s[44:47],s69 offen lds
RA66_%=:
ds_read_b64_tr_b16 v[98:99],v6 offset:13920
ds_read_b64_tr_b16 v[100:101],v6 offset:12392
v_rcp_f16 v123,v122
v_mfma_f32_16x16x32_bf16 v[156:159],v[76:79],v[52:55],v[156:159]
ds_read_u16_d16_hi v138,v138
v_rcp_f16_sdwa v121,v120 dst_sel:WORD_1 dst_unused:UNUSED_PRESERVE src0_sel:WORD_1
v_mfma_f32_16x16x32_bf16 v[148:151],v[76:79],v[48:51],v[148:151]
ds_read_b64_tr_b16 v[102:103],v6 offset:13928
ds_read_b64_tr_b16 v[104:105],v6 offset:12448
v_rcp_f16_sdwa v123,v122 dst_sel:WORD_1 dst_unused:UNUSED_PRESERVE src0_sel:WORD_1
v_mfma_f32_16x16x32_bf16 v[144:147],v[80:83],v[56:59],v[144:147]
ds_read_u16 v137,v137
v_fma_mix_f32 v140,v140,v121,0 op_sel_hi:[0,1,0]
v_fma_mix_f32 v142,v142,v121,0 op_sel:[0,1,0] op_sel_hi:[0,1,0]
v_mfma_f32_16x16x32_bf16 v[152:155],v[80:83],v[60:63],v[152:155]
s_waitcnt lgkmcnt(8)
s_cbranch_vccz RA67_%=
s_add_u32 m0,m0,1024
s_add_u32 s69,s69,s53
buffer_load_dwordx4 v2,s[44:47],s69 offen lds
s_add_u32 m0,m0,1024
s_add_u32 s69,s69,s53
buffer_load_dwordx4 v3,s[44:47],s69 offen lds
RA67_%=:
ds_read_b64_tr_b16 v[106:107],v6 offset:13984
ds_read_b64_tr_b16 v[108:109],v6 offset:12456
v_lshl_add_u64 v[128:129],v[128:129],0,v[130:131]
v_lshl_add_u64 v[130:131],v[132:133],0,v[134:135]
v_mfma_f32_16x16x32_bf16 v[156:159],v[84:87],v[60:63],v[156:159]
ds_read_u16_d16_hi v139,v139
v_fma_mix_f32 v141,v141,v123,0 op_sel_hi:[0,1,0]
v_fma_mix_f32 v143,v143,v123,0 op_sel:[0,1,0] op_sel_hi:[0,1,0]
v_mfma_f32_16x16x32_bf16 v[148:151],v[84:87],v[56:59],v[148:151]
ds_read_b64_tr_b16 v[110:111],v6 offset:13992
ds_read_b64_tr_b16 v[64:65],v4 offset:12320
v_lshlrev_b32_sdwa v144,1,v144 dst_sel:DWORD dst_unused:UNUSED_PAD src0_sel:DWORD src1_sel:WORD_1
v_lshlrev_b32_sdwa v146,1,v146 dst_sel:DWORD dst_unused:UNUSED_PAD src0_sel:DWORD src1_sel:WORD_1
v_mfma_f32_16x16x32_bf16 v[160:163],v[88:91],v[128:131],v[160:163]
ds_read_u16 v144,v144
v_lshlrev_b32_sdwa v145,1,v145 dst_sel:DWORD dst_unused:UNUSED_PAD src0_sel:DWORD src1_sel:WORD_1
v_lshlrev_b32_sdwa v147,1,v147 dst_sel:DWORD dst_unused:UNUSED_PAD src0_sel:DWORD src1_sel:WORD_1
v_mfma_f32_16x16x32_bf16 v[168:171],v[92:95],v[128:131],v[168:171]
s_waitcnt lgkmcnt(11)
ds_read_b64_tr_b16 v[66:67],v4 offset:14368
ds_read_b128 v[112:115],v7 offset:0
v_fma_mixlo_f16 v120,v156,s99,0
v_fma_mixlo_f16 v122,v157,s99,0
v_mfma_f32_16x16x32_bf16 v[176:179],v[96:99],v[128:131],v[176:179]
s_waitcnt lgkmcnt(10)
ds_read_u16_d16_hi v146,v146
v_lshlrev_b32_sdwa v148,1,v148 dst_sel:DWORD dst_unused:UNUSED_PAD src0_sel:DWORD src1_sel:WORD_1
v_lshlrev_b32_sdwa v150,1,v150 dst_sel:DWORD dst_unused:UNUSED_PAD src0_sel:DWORD src1_sel:WORD_1
v_mfma_f32_16x16x32_bf16 v[184:187],v[100:103],v[128:131],v[184:187]
s_waitcnt lgkmcnt(6)
ds_read_b64_tr_b16 v[68:69],v5 offset:12320
ds_read_b64_tr_b16 v[70:71],v5 offset:14368
v_lshl_add_u64 v[136:137],v[136:137],0,v[138:139]
v_mfma_f32_16x16x32_bf16 v[192:195],v[104:107],v[128:131],v[192:195]
s_waitcnt lgkmcnt(7)
ds_read_u16 v145,v145
v_cvt_pk_bf16_f32 v138,v140,v142
v_cvt_pk_bf16_f32 v139,v141,v143
v_mfma_f32_16x16x32_bf16 v[200:203],v[108:111],v[128:131],v[200:203]
ds_read_b128 v[116:119],v7 offset:32
ds_read_b64_tr_b16 v[72:73],v4 offset:16416
v_fma_mixhi_f16 v120,v158,s99,0
v_fma_mixhi_f16 v122,v159,s99,0
v_mfma_f32_16x16x32_bf16 v[164:167],v[88:91],v[136:139],v[164:167]
ds_read_u16_d16_hi v147,v147
v_lshlrev_b32_sdwa v149,1,v149 dst_sel:DWORD dst_unused:UNUSED_PAD src0_sel:DWORD src1_sel:WORD_1
v_lshlrev_b32_sdwa v151,1,v151 dst_sel:DWORD dst_unused:UNUSED_PAD src0_sel:DWORD src1_sel:WORD_1
v_mfma_f32_16x16x32_bf16 v[172:175],v[92:95],v[136:139],v[172:175]
ds_read_b64_tr_b16 v[74:75],v4 offset:18464
ds_read_b64_tr_b16 v[76:77],v5 offset:16416
v_pk_fma_f16 v121,v120,v120,s54
v_pk_fma_f16 v123,v122,v122,s54
v_mfma_f32_16x16x32_bf16 v[180:183],v[96:99],v[136:139],v[180:183]
ds_read_u16 v148,v148
v_pk_mul_f16 v121,v120,v121
v_pk_mul_f16 v123,v122,v123
s_nop 0
s_mov_b32 s86,0xbe801d5a
v_mfma_f32_16x16x32_bf16 v[188:191],v[100:103],v[136:139],v[188:191]
ds_read_b64_tr_b16 v[78:79],v5 offset:18464
v_lshlrev_b32_sdwa v152,1,v152 dst_sel:DWORD dst_unused:UNUSED_PAD src0_sel:DWORD src1_sel:WORD_1
v_lshlrev_b32_sdwa v154,1,v154 dst_sel:DWORD dst_unused:UNUSED_PAD src0_sel:DWORD src1_sel:WORD_1
v_mfma_f32_16x16x32_bf16 v[196:199],v[104:107],v[136:139],v[196:199]
ds_read_u16_d16_hi v150,v150
v_exp_f16 v120,v121
v_mfma_f32_16x16x32_bf16 v[204:207],v[108:111],v[136:139],v[204:207]
s_waitcnt lgkmcnt(12)
ds_read_b64_tr_b16 v[80:81],v4 offset:20512
v_exp_f16 v122,v123
v_mfma_f32_16x16x32_bf16 v[128:131],v[64:67],v[16:19],v[112:115]
ds_read_u16 v149,v149
v_lshlrev_b32_sdwa v153,1,v153 dst_sel:DWORD dst_unused:UNUSED_PAD src0_sel:DWORD src1_sel:WORD_1
v_lshlrev_b32_sdwa v155,1,v155 dst_sel:DWORD dst_unused:UNUSED_PAD src0_sel:DWORD src1_sel:WORD_1
v_mfma_f32_16x16x32_bf16 v[136:139],v[64:67],v[20:23],v[112:115]
s_waitcnt lgkmcnt(9)
ds_read_b64_tr_b16 v[82:83],v4 offset:22560
v_exp_f16_sdwa v120,v121 dst_sel:WORD_1 dst_unused:UNUSED_PRESERVE src0_sel:WORD_1
v_mfma_f32_16x16x32_bf16 v[140:143],v[68:71],v[20:23],v[116:119]
ds_read_u16_d16_hi v151,v151
v_exp_f16_sdwa v122,v123 dst_sel:WORD_1 dst_unused:UNUSED_PRESERVE src0_sel:WORD_1
v_mfma_f32_16x16x32_bf16 v[132:135],v[68:71],v[16:19],v[116:119]
s_waitcnt lgkmcnt(8)
ds_read_b64_tr_b16 v[84:85],v5 offset:20512
v_pk_fma_f16 v120,v120,s55,s55
v_pk_fma_f16 v122,v122,s55,s55
v_mfma_f32_16x16x32_bf16 v[128:131],v[72:75],v[24:27],v[128:131]
ds_read_u16 v152,v152
v_rcp_f16 v121,v120
v_mfma_f32_16x16x32_bf16 v[136:139],v[72:75],v[28:31],v[136:139]
s_waitcnt lgkmcnt(7)
ds_read_b64_tr_b16 v[86:87],v5 offset:22560
v_rcp_f16 v123,v122
v_mfma_f32_16x16x32_bf16 v[140:143],v[76:79],v[28:31],v[140:143]
ds_read_u16_d16_hi v154,v154
v_rcp_f16_sdwa v121,v120 dst_sel:WORD_1 dst_unused:UNUSED_PRESERVE src0_sel:WORD_1
v_mfma_f32_16x16x32_bf16 v[132:135],v[76:79],v[24:27],v[132:135]
s_waitcnt lgkmcnt(5)
v_rcp_f16_sdwa v123,v122 dst_sel:WORD_1 dst_unused:UNUSED_PRESERVE src0_sel:WORD_1
v_mfma_f32_16x16x32_bf16 v[128:131],v[80:83],v[32:35],v[128:131]
ds_read_u16 v153,v153
v_fma_mix_f32 v156,v156,v121,0 op_sel_hi:[0,1,0]
v_fma_mix_f32 v158,v158,v121,0 op_sel:[0,1,0] op_sel_hi:[0,1,0]
v_mfma_f32_16x16x32_bf16 v[136:139],v[80:83],v[36:39],v[136:139]
s_waitcnt lgkmcnt(2)
v_lshl_add_u64 v[144:145],v[144:145],0,v[146:147]
v_lshl_add_u64 v[146:147],v[148:149],0,v[150:151]
v_mfma_f32_16x16x32_bf16 v[140:143],v[84:87],v[36:39],v[140:143]
ds_read_u16_d16_hi v155,v155
v_fma_mix_f32 v157,v157,v123,0 op_sel_hi:[0,1,0]
v_fma_mix_f32 v159,v159,v123,0 op_sel:[0,1,0] op_sel_hi:[0,1,0]
v_mfma_f32_16x16x32_bf16 v[132:135],v[84:87],v[32:35],v[132:135]
v_lshlrev_b32_sdwa v128,1,v128 dst_sel:DWORD dst_unused:UNUSED_PAD src0_sel:DWORD src1_sel:WORD_1
v_lshlrev_b32_sdwa v130,1,v130 dst_sel:DWORD dst_unused:UNUSED_PAD src0_sel:DWORD src1_sel:WORD_1
v_mfma_f32_16x16x32_bf16 v[208:211],v[88:91],v[144:147],v[208:211]
ds_read_u16 v128,v128
v_lshlrev_b32_sdwa v129,1,v129 dst_sel:DWORD dst_unused:UNUSED_PAD src0_sel:DWORD src1_sel:WORD_1
v_lshlrev_b32_sdwa v131,1,v131 dst_sel:DWORD dst_unused:UNUSED_PAD src0_sel:DWORD src1_sel:WORD_1
v_mfma_f32_16x16x32_bf16 v[216:219],v[92:95],v[144:147],v[216:219]
v_fma_mixlo_f16 v120,v140,s99,0
v_fma_mixlo_f16 v122,v141,s99,0
v_mfma_f32_16x16x32_bf16 v[224:227],v[96:99],v[144:147],v[224:227]
ds_read_u16_d16_hi v130,v130
v_lshlrev_b32_sdwa v132,1,v132 dst_sel:DWORD dst_unused:UNUSED_PAD src0_sel:DWORD src1_sel:WORD_1
v_lshlrev_b32_sdwa v134,1,v134 dst_sel:DWORD dst_unused:UNUSED_PAD src0_sel:DWORD src1_sel:WORD_1
v_mfma_f32_16x16x32_bf16 v[232:235],v[100:103],v[144:147],v[232:235]
s_waitcnt lgkmcnt(2)
v_lshl_add_u64 v[152:153],v[152:153],0,v[154:155]
v_mfma_f32_16x16x32_bf16 v[240:243],v[104:107],v[144:147],v[240:243]
ds_read_u16 v129,v129
v_cvt_pk_bf16_f32 v154,v156,v158
v_cvt_pk_bf16_f32 v155,v157,v159
v_mfma_f32_16x16x32_bf16 v[248:251],v[108:111],v[144:147],v[248:251]
v_fma_mixhi_f16 v120,v142,s99,0
v_fma_mixhi_f16 v122,v143,s99,0
v_mfma_f32_16x16x32_bf16 v[212:215],v[88:91],v[152:155],v[212:215]
s_setprio 2
ds_read_u16_d16_hi v131,v131
v_lshlrev_b32_sdwa v133,1,v133 dst_sel:DWORD dst_unused:UNUSED_PAD src0_sel:DWORD src1_sel:WORD_1
v_lshlrev_b32_sdwa v135,1,v135 dst_sel:DWORD dst_unused:UNUSED_PAD src0_sel:DWORD src1_sel:WORD_1
v_mfma_f32_16x16x32_bf16 v[220:223],v[92:95],v[152:155],v[220:223]
ds_read_b64_tr_b16 v[88:89],v6 offset:18480
v_pk_fma_f16 v121,v120,v120,s54
v_pk_fma_f16 v123,v122,v122,s54
v_mfma_f32_16x16x32_bf16 v[228:231],v[96:99],v[152:155],v[228:231]
ds_read_u16 v132,v132
v_pk_mul_f16 v121,v120,v121
v_pk_mul_f16 v123,v122,v123
v_mfma_f32_16x16x32_bf16 v[236:239],v[100:103],v[152:155],v[236:239]
ds_read_b64_tr_b16 v[90:91],v6 offset:20016
v_lshlrev_b32_sdwa v136,1,v136 dst_sel:DWORD dst_unused:UNUSED_PAD src0_sel:DWORD src1_sel:WORD_1
v_lshlrev_b32_sdwa v138,1,v138 dst_sel:DWORD dst_unused:UNUSED_PAD src0_sel:DWORD src1_sel:WORD_1
v_mfma_f32_16x16x32_bf16 v[244:247],v[104:107],v[152:155],v[244:247]
ds_read_u16_d16_hi v134,v134
v_exp_f16 v120,v121
v_mfma_f32_16x16x32_bf16 v[252:255],v[108:111],v[152:155],v[252:255]
ds_read_b64_tr_b16 v[92:93],v6 offset:18488
v_exp_f16 v122,v123
v_mfma_f32_16x16x32_bf16 v[144:147],v[64:67],v[40:43],v[112:115]
ds_read_u16 v133,v133
v_lshlrev_b32_sdwa v137,1,v137 dst_sel:DWORD dst_unused:UNUSED_PAD src0_sel:DWORD src1_sel:WORD_1
v_lshlrev_b32_sdwa v139,1,v139 dst_sel:DWORD dst_unused:UNUSED_PAD src0_sel:DWORD src1_sel:WORD_1
v_mfma_f32_16x16x32_bf16 v[152:155],v[64:67],v[44:47],v[112:115]
ds_read_b64_tr_b16 v[94:95],v6 offset:20024
v_exp_f16_sdwa v120,v121 dst_sel:WORD_1 dst_unused:UNUSED_PRESERVE src0_sel:WORD_1
v_mfma_f32_16x16x32_bf16 v[156:159],v[68:71],v[44:47],v[116:119]
ds_read_u16_d16_hi v135,v135
v_exp_f16_sdwa v122,v123 dst_sel:WORD_1 dst_unused:UNUSED_PRESERVE src0_sel:WORD_1
v_mfma_f32_16x16x32_bf16 v[148:151],v[68:71],v[40:43],v[116:119]
ds_read_b64_tr_b16 v[96:97],v6 offset:18544
v_pk_fma_f16 v120,v120,s55,s55
v_pk_fma_f16 v122,v122,s55,s55
v_mfma_f32_16x16x32_bf16 v[144:147],v[72:75],v[48:51],v[144:147]
ds_read_u16 v136,v136
v_rcp_f16 v121,v120
v_mfma_f32_16x16x32_bf16 v[152:155],v[72:75],v[52:55],v[152:155]
ds_read_b64_tr_b16 v[98:99],v6 offset:20080
ds_read_b64_tr_b16 v[100:101],v6 offset:18552
v_rcp_f16 v123,v122
v_mfma_f32_16x16x32_bf16 v[156:159],v[76:79],v[52:55],v[156:159]
ds_read_u16_d16_hi v138,v138
v_rcp_f16_sdwa v121,v120 dst_sel:WORD_1 dst_unused:UNUSED_PRESERVE src0_sel:WORD_1
v_mfma_f32_16x16x32_bf16 v[148:151],v[76:79],v[48:51],v[148:151]
ds_read_b64_tr_b16 v[102:103],v6 offset:20088
ds_read_b64_tr_b16 v[104:105],v6 offset:18608
v_rcp_f16_sdwa v123,v122 dst_sel:WORD_1 dst_unused:UNUSED_PRESERVE src0_sel:WORD_1
v_mfma_f32_16x16x32_bf16 v[144:147],v[80:83],v[56:59],v[144:147]
ds_read_u16 v137,v137
v_fma_mix_f32 v140,v140,v121,0 op_sel_hi:[0,1,0]
v_fma_mix_f32 v142,v142,v121,0 op_sel:[0,1,0] op_sel_hi:[0,1,0]
v_mfma_f32_16x16x32_bf16 v[152:155],v[80:83],v[60:63],v[152:155]
s_waitcnt lgkmcnt(8)
ds_read_b64_tr_b16 v[106:107],v6 offset:20144
ds_read_b64_tr_b16 v[108:109],v6 offset:18616
v_lshl_add_u64 v[128:129],v[128:129],0,v[130:131]
v_lshl_add_u64 v[130:131],v[132:133],0,v[134:135]
v_mfma_f32_16x16x32_bf16 v[156:159],v[84:87],v[60:63],v[156:159]
ds_read_u16_d16_hi v139,v139
v_fma_mix_f32 v141,v141,v123,0 op_sel_hi:[0,1,0]
v_fma_mix_f32 v143,v143,v123,0 op_sel:[0,1,0] op_sel_hi:[0,1,0]
v_mfma_f32_16x16x32_bf16 v[148:151],v[84:87],v[56:59],v[148:151]
ds_read_b64_tr_b16 v[110:111],v6 offset:20152
ds_read_b64_tr_b16 v[64:65],v4 offset:12832
v_lshlrev_b32_sdwa v144,1,v144 dst_sel:DWORD dst_unused:UNUSED_PAD src0_sel:DWORD src1_sel:WORD_1
v_lshlrev_b32_sdwa v146,1,v146 dst_sel:DWORD dst_unused:UNUSED_PAD src0_sel:DWORD src1_sel:WORD_1
v_mfma_f32_16x16x32_bf16 v[160:163],v[88:91],v[128:131],v[160:163]
ds_read_u16 v144,v144
v_lshlrev_b32_sdwa v145,1,v145 dst_sel:DWORD dst_unused:UNUSED_PAD src0_sel:DWORD src1_sel:WORD_1
v_lshlrev_b32_sdwa v147,1,v147 dst_sel:DWORD dst_unused:UNUSED_PAD src0_sel:DWORD src1_sel:WORD_1
v_mfma_f32_16x16x32_bf16 v[168:171],v[92:95],v[128:131],v[168:171]
s_waitcnt lgkmcnt(11)
ds_read_b64_tr_b16 v[66:67],v4 offset:14880
ds_read_b128 v[112:115],v7 offset:128
v_fma_mixlo_f16 v120,v156,s99,0
v_fma_mixlo_f16 v122,v157,s99,0
v_mfma_f32_16x16x32_bf16 v[176:179],v[96:99],v[128:131],v[176:179]
s_waitcnt lgkmcnt(10)
ds_read_u16_d16_hi v146,v146
v_lshlrev_b32_sdwa v148,1,v148 dst_sel:DWORD dst_unused:UNUSED_PAD src0_sel:DWORD src1_sel:WORD_1
v_lshlrev_b32_sdwa v150,1,v150 dst_sel:DWORD dst_unused:UNUSED_PAD src0_sel:DWORD src1_sel:WORD_1
v_mfma_f32_16x16x32_bf16 v[184:187],v[100:103],v[128:131],v[184:187]
s_waitcnt lgkmcnt(6)
ds_read_b64_tr_b16 v[68:69],v5 offset:12832
ds_read_b64_tr_b16 v[70:71],v5 offset:14880
v_lshl_add_u64 v[136:137],v[136:137],0,v[138:139]
v_mfma_f32_16x16x32_bf16 v[192:195],v[104:107],v[128:131],v[192:195]
s_waitcnt lgkmcnt(7)
ds_read_u16 v145,v145
v_cvt_pk_bf16_f32 v138,v140,v142
v_cvt_pk_bf16_f32 v139,v141,v143
v_mfma_f32_16x16x32_bf16 v[200:203],v[108:111],v[128:131],v[200:203]
ds_read_b128 v[116:119],v7 offset:160
ds_read_b64_tr_b16 v[72:73],v4 offset:16928
v_fma_mixhi_f16 v120,v158,s99,0
v_fma_mixhi_f16 v122,v159,s99,0
v_mfma_f32_16x16x32_bf16 v[164:167],v[88:91],v[136:139],v[164:167]
ds_read_u16_d16_hi v147,v147
v_lshlrev_b32_sdwa v149,1,v149 dst_sel:DWORD dst_unused:UNUSED_PAD src0_sel:DWORD src1_sel:WORD_1
v_lshlrev_b32_sdwa v151,1,v151 dst_sel:DWORD dst_unused:UNUSED_PAD src0_sel:DWORD src1_sel:WORD_1
v_mfma_f32_16x16x32_bf16 v[172:175],v[92:95],v[136:139],v[172:175]
s_setprio 1
ds_read_b64_tr_b16 v[74:75],v4 offset:18976
ds_read_b64_tr_b16 v[76:77],v5 offset:16928
v_pk_fma_f16 v121,v120,v120,s54
v_pk_fma_f16 v123,v122,v122,s54
v_mfma_f32_16x16x32_bf16 v[180:183],v[96:99],v[136:139],v[180:183]
ds_read_u16 v148,v148
v_pk_mul_f16 v121,v120,v121
v_pk_mul_f16 v123,v122,v123
v_mfma_f32_16x16x32_bf16 v[188:191],v[100:103],v[136:139],v[188:191]
ds_read_b64_tr_b16 v[78:79],v5 offset:18976
v_lshlrev_b32_sdwa v152,1,v152 dst_sel:DWORD dst_unused:UNUSED_PAD src0_sel:DWORD src1_sel:WORD_1
v_lshlrev_b32_sdwa v154,1,v154 dst_sel:DWORD dst_unused:UNUSED_PAD src0_sel:DWORD src1_sel:WORD_1
v_mfma_f32_16x16x32_bf16 v[196:199],v[104:107],v[136:139],v[196:199]
ds_read_u16_d16_hi v150,v150
v_exp_f16 v120,v121
v_mfma_f32_16x16x32_bf16 v[204:207],v[108:111],v[136:139],v[204:207]
s_waitcnt lgkmcnt(12)
ds_read_b64_tr_b16 v[80:81],v4 offset:21024
v_exp_f16 v122,v123
v_mfma_f32_16x16x32_bf16 v[128:131],v[64:67],v[16:19],v[112:115]
ds_read_u16 v149,v149
v_lshlrev_b32_sdwa v153,1,v153 dst_sel:DWORD dst_unused:UNUSED_PAD src0_sel:DWORD src1_sel:WORD_1
v_lshlrev_b32_sdwa v155,1,v155 dst_sel:DWORD dst_unused:UNUSED_PAD src0_sel:DWORD src1_sel:WORD_1
v_mfma_f32_16x16x32_bf16 v[136:139],v[64:67],v[20:23],v[112:115]
s_waitcnt lgkmcnt(9)
ds_read_b64_tr_b16 v[82:83],v4 offset:23072
v_exp_f16_sdwa v120,v121 dst_sel:WORD_1 dst_unused:UNUSED_PRESERVE src0_sel:WORD_1
v_mfma_f32_16x16x32_bf16 v[140:143],v[68:71],v[20:23],v[116:119]
ds_read_u16_d16_hi v151,v151
v_exp_f16_sdwa v122,v123 dst_sel:WORD_1 dst_unused:UNUSED_PRESERVE src0_sel:WORD_1
v_mfma_f32_16x16x32_bf16 v[132:135],v[68:71],v[16:19],v[116:119]
s_waitcnt lgkmcnt(8)
ds_read_b64_tr_b16 v[84:85],v5 offset:21024
v_pk_fma_f16 v120,v120,s55,s55
v_pk_fma_f16 v122,v122,s55,s55
v_mfma_f32_16x16x32_bf16 v[128:131],v[72:75],v[24:27],v[128:131]
ds_read_u16 v152,v152
v_rcp_f16 v121,v120
v_mfma_f32_16x16x32_bf16 v[136:139],v[72:75],v[28:31],v[136:139]
s_waitcnt lgkmcnt(7)
ds_read_b64_tr_b16 v[86:87],v5 offset:23072
v_rcp_f16 v123,v122
v_mfma_f32_16x16x32_bf16 v[140:143],v[76:79],v[28:31],v[140:143]
ds_read_u16_d16_hi v154,v154
v_rcp_f16_sdwa v121,v120 dst_sel:WORD_1 dst_unused:UNUSED_PRESERVE src0_sel:WORD_1
v_mfma_f32_16x16x32_bf16 v[132:135],v[76:79],v[24:27],v[132:135]
s_waitcnt lgkmcnt(5)
v_rcp_f16_sdwa v123,v122 dst_sel:WORD_1 dst_unused:UNUSED_PRESERVE src0_sel:WORD_1
v_mfma_f32_16x16x32_bf16 v[128:131],v[80:83],v[32:35],v[128:131]
ds_read_u16 v153,v153
v_fma_mix_f32 v156,v156,v121,0 op_sel_hi:[0,1,0]
v_fma_mix_f32 v158,v158,v121,0 op_sel:[0,1,0] op_sel_hi:[0,1,0]
v_mfma_f32_16x16x32_bf16 v[136:139],v[80:83],v[36:39],v[136:139]
s_waitcnt lgkmcnt(2)
s_setprio 0
v_lshl_add_u64 v[144:145],v[144:145],0,v[146:147]
v_lshl_add_u64 v[146:147],v[148:149],0,v[150:151]
v_mfma_f32_16x16x32_bf16 v[140:143],v[84:87],v[36:39],v[140:143]
ds_read_u16_d16_hi v155,v155
v_fma_mix_f32 v157,v157,v123,0 op_sel_hi:[0,1,0]
v_fma_mix_f32 v159,v159,v123,0 op_sel:[0,1,0] op_sel_hi:[0,1,0]
v_mfma_f32_16x16x32_bf16 v[132:135],v[84:87],v[32:35],v[132:135]
v_lshlrev_b32_sdwa v128,1,v128 dst_sel:DWORD dst_unused:UNUSED_PAD src0_sel:DWORD src1_sel:WORD_1
v_lshlrev_b32_sdwa v130,1,v130 dst_sel:DWORD dst_unused:UNUSED_PAD src0_sel:DWORD src1_sel:WORD_1
v_mfma_f32_16x16x32_bf16 v[208:211],v[88:91],v[144:147],v[208:211]
s_waitcnt vmcnt(0)
s_barrier
s_setprio 2
ds_read_u16 v128,v128
v_lshlrev_b32_sdwa v129,1,v129 dst_sel:DWORD dst_unused:UNUSED_PAD src0_sel:DWORD src1_sel:WORD_1
v_mfma_f32_16x16x32_bf16 v[216:219],v[92:95],v[144:147],v[216:219]
ds_read_u16_d16_hi v130,v130
v_lshlrev_b32_sdwa v131,1,v131 dst_sel:DWORD dst_unused:UNUSED_PAD src0_sel:DWORD src1_sel:WORD_1
v_mfma_f32_16x16x32_bf16 v[224:227],v[96:99],v[144:147],v[224:227]
s_cbranch_vccz RA68_%=
s_add_u32 m0,s58,12320
s_mul_i32 s69,s59,3
s_add_u32 s69,s69,s97
buffer_load_dwordx4 v2,s[44:47],s69 offen lds
s_add_u32 m0,m0,1024
s_add_u32 s69,s69,s53
buffer_load_dwordx4 v3,s[44:47],s69 offen lds
RA68_%=:
v_lshlrev_b32_sdwa v132,1,v132 dst_sel:DWORD dst_unused:UNUSED_PAD src0_sel:DWORD src1_sel:WORD_1
v_mfma_f32_16x16x32_bf16 v[232:235],v[100:103],v[144:147],v[232:235]
s_waitcnt lgkmcnt(2)
ds_read_u16 v129,v129
v_lshl_add_u64 v[152:153],v[152:153],0,v[154:155]
v_lshlrev_b32_sdwa v134,1,v134 dst_sel:DWORD dst_unused:UNUSED_PAD src0_sel:DWORD src1_sel:WORD_1
v_mfma_f32_16x16x32_bf16 v[240:243],v[104:107],v[144:147],v[240:243]
ds_read_u16_d16_hi v131,v131
v_cvt_pk_bf16_f32 v154,v156,v158
v_cvt_pk_bf16_f32 v155,v157,v159
v_mfma_f32_16x16x32_bf16 v[248:251],v[108:111],v[144:147],v[248:251]
v_lshlrev_b32_sdwa v133,1,v133 dst_sel:DWORD dst_unused:UNUSED_PAD src0_sel:DWORD src1_sel:WORD_1
v_mfma_f32_16x16x32_bf16 v[212:215],v[88:91],v[152:155],v[212:215]
s_cbranch_vccz RA69_%=
s_add_u32 m0,m0,1024
s_add_u32 s69,s69,s53
buffer_load_dwordx4 v2,s[44:47],s69 offen lds
s_add_u32 m0,m0,s50
s_add_u32 s69,s69,s53
buffer_load_dwordx4 v3,s[44:47],s69 offen lds
RA69_%=:
ds_read_u16 v132,v132
v_lshlrev_b32_sdwa v135,1,v135 dst_sel:DWORD dst_unused:UNUSED_PAD src0_sel:DWORD src1_sel:WORD_1
v_mfma_f32_16x16x32_bf16 v[220:223],v[92:95],v[152:155],v[220:223]
ds_read_u16_d16_hi v134,v134
v_lshlrev_b32_sdwa v136,1,v136 dst_sel:DWORD dst_unused:UNUSED_PAD src0_sel:DWORD src1_sel:WORD_1
v_mfma_f32_16x16x32_bf16 v[228:231],v[96:99],v[152:155],v[228:231]
ds_read_b64_tr_b16 v[88:89],v6 offset:0
ds_read_b64_tr_b16 v[90:91],v6 offset:1536
v_lshlrev_b32_sdwa v138,1,v138 dst_sel:DWORD dst_unused:UNUSED_PAD src0_sel:DWORD src1_sel:WORD_1
v_mfma_f32_16x16x32_bf16 v[236:239],v[100:103],v[152:155],v[236:239]
ds_read_u16 v133,v133
v_lshlrev_b32_sdwa v137,1,v137 dst_sel:DWORD dst_unused:UNUSED_PAD src0_sel:DWORD src1_sel:WORD_1
v_mfma_f32_16x16x32_bf16 v[244:247],v[104:107],v[152:155],v[244:247]
ds_read_u16_d16_hi v135,v135
v_lshlrev_b32_sdwa v139,1,v139 dst_sel:DWORD dst_unused:UNUSED_PAD src0_sel:DWORD src1_sel:WORD_1
v_mfma_f32_16x16x32_bf16 v[252:255],v[108:111],v[152:155],v[252:255]
s_cbranch_vccz RA70_%=
s_add_u32 m0,m0,1024
s_add_u32 s69,s69,s53
buffer_load_dwordx4 v2,s[44:47],s69 offen lds
s_add_u32 m0,m0,1024
s_add_u32 s69,s69,s53
buffer_load_dwordx4 v3,s[44:47],s69 offen lds
RA70_%=:
ds_read_b64_tr_b16 v[92:93],v6 offset:8
ds_read_b64_tr_b16 v[94:95],v6 offset:1544
v_lshlrev_b32_sdwa v140,1,v140 dst_sel:DWORD dst_unused:UNUSED_PAD src0_sel:DWORD src1_sel:WORD_1
v_mfma_f32_16x16x32_bf16 v[144:147],v[64:67],v[40:43],v[112:115]
ds_read_u16 v136,v136
v_lshlrev_b32_sdwa v142,1,v142 dst_sel:DWORD dst_unused:UNUSED_PAD src0_sel:DWORD src1_sel:WORD_1
v_mfma_f32_16x16x32_bf16 v[152:155],v[64:67],v[44:47],v[112:115]
ds_read_u16_d16_hi v138,v138
v_lshlrev_b32_sdwa v141,1,v141 dst_sel:DWORD dst_unused:UNUSED_PAD src0_sel:DWORD src1_sel:WORD_1
v_mfma_f32_16x16x32_bf16 v[156:159],v[68:71],v[44:47],v[116:119]
ds_read_b64_tr_b16 v[96:97],v6 offset:64
ds_read_b64_tr_b16 v[98:99],v6 offset:1600
v_lshlrev_b32_sdwa v143,1,v143 dst_sel:DWORD dst_unused:UNUSED_PAD src0_sel:DWORD src1_sel:WORD_1
v_mfma_f32_16x16x32_bf16 v[148:151],v[68:71],v[40:43],v[116:119]
ds_read_u16 v137,v137
v_mfma_f32_16x16x32_bf16 v[144:147],v[72:75],v[48:51],v[144:147]
ds_read_u16_d16_hi v139,v139
v_mfma_f32_16x16x32_bf16 v[152:155],v[72:75],v[52:55],v[152:155]
s_cbranch_vccz RA71_%=
s_add_u32 m0,m0,s50
s_add_u32 s69,s69,s53
buffer_load_dwordx4 v2,s[44:47],s69 offen lds
s_add_u32 m0,m0,1024
s_add_u32 s69,s69,s53
buffer_load_dwordx4 v3,s[44:47],s69 offen lds
RA71_%=:
ds_read_b64_tr_b16 v[100:101],v6 offset:72
ds_read_b64_tr_b16 v[102:103],v6 offset:1608
v_mfma_f32_16x16x32_bf16 v[156:159],v[76:79],v[52:55],v[156:159]
ds_read_u16 v140,v140
v_mfma_f32_16x16x32_bf16 v[148:151],v[76:79],v[48:51],v[148:151]
s_waitcnt lgkmcnt(15)
ds_read_u16_d16_hi v142,v142
v_lshl_add_u64 v[128:129],v[128:129],0,v[130:131]
v_mfma_f32_16x16x32_bf16 v[144:147],v[80:83],v[56:59],v[144:147]
s_waitcnt lgkmcnt(12)
ds_read_b64_tr_b16 v[104:105],v6 offset:128
ds_read_b64_tr_b16 v[106:107],v6 offset:1664
v_lshl_add_u64 v[130:131],v[132:133],0,v[134:135]
v_mfma_f32_16x16x32_bf16 v[152:155],v[80:83],v[60:63],v[152:155]
ds_read_u16 v141,v141
v_mfma_f32_16x16x32_bf16 v[156:159],v[84:87],v[60:63],v[156:159]
ds_read_u16_d16_hi v143,v143
v_mfma_f32_16x16x32_bf16 v[148:151],v[84:87],v[56:59],v[148:151]
s_cbranch_vccz RA72_%=
s_add_u32 m0,m0,1024
s_add_u32 s69,s69,s53
buffer_load_dwordx4 v2,s[44:47],s69 offen lds
s_add_u32 m0,m0,s50
s_add_u32 s69,s69,s53
buffer_load_dwordx4 v3,s[44:47],s69 offen lds
RA72_%=:
ds_read_b64_tr_b16 v[108:109],v6 offset:136
ds_read_b64_tr_b16 v[110:111],v6 offset:1672
v_lshlrev_b32_sdwa v144,1,v144 dst_sel:DWORD dst_unused:UNUSED_PAD src0_sel:DWORD src1_sel:WORD_1
v_lshlrev_b32_sdwa v146,1,v146 dst_sel:DWORD dst_unused:UNUSED_PAD src0_sel:DWORD src1_sel:WORD_1
v_mfma_f32_16x16x32_bf16 v[160:163],v[88:91],v[128:131],v[160:163]
s_waitcnt lgkmcnt(15)
ds_read_u16 v144,v144
v_lshlrev_b32_sdwa v145,1,v145 dst_sel:DWORD dst_unused:UNUSED_PAD src0_sel:DWORD src1_sel:WORD_1
v_mfma_f32_16x16x32_bf16 v[168:171],v[92:95],v[128:131],v[168:171]
s_waitcnt lgkmcnt(13)
ds_read_u16_d16_hi v146,v146
v_lshlrev_b32_sdwa v147,1,v147 dst_sel:DWORD dst_unused:UNUSED_PAD src0_sel:DWORD src1_sel:WORD_1
v_mfma_f32_16x16x32_bf16 v[176:179],v[96:99],v[128:131],v[176:179]
s_waitcnt lgkmcnt(10)
ds_read_b64_tr_b16 v[64:65],v4 offset:0
ds_read_b64_tr_b16 v[66:67],v4 offset:2048
v_lshlrev_b32_sdwa v148,1,v148 dst_sel:DWORD dst_unused:UNUSED_PAD src0_sel:DWORD src1_sel:WORD_1
v_mfma_f32_16x16x32_bf16 v[184:187],v[100:103],v[128:131],v[184:187]
s_waitcnt lgkmcnt(8)
ds_read_u16 v145,v145
v_lshl_add_u64 v[136:137],v[136:137],0,v[138:139]
v_lshlrev_b32_sdwa v150,1,v150 dst_sel:DWORD dst_unused:UNUSED_PAD src0_sel:DWORD src1_sel:WORD_1
v_mfma_f32_16x16x32_bf16 v[192:195],v[104:107],v[128:131],v[192:195]
s_waitcnt lgkmcnt(5)
s_cbranch_vccz RA73_%=
s_add_u32 m0,m0,1024
s_add_u32 s69,s69,s53
buffer_load_dwordx4 v2,s[44:47],s69 offen lds
s_add_u32 m0,m0,1024
s_add_u32 s69,s69,s53
buffer_load_dwordx4 v3,s[44:47],s69 offen lds
RA73_%=:
ds_read_u16_d16_hi v147,v147
v_lshl_add_u64 v[138:139],v[140:141],0,v[142:143]
v_mfma_f32_16x16x32_bf16 v[200:203],v[108:111],v[128:131],v[200:203]
ds_read_b128 v[112:115],v7 offset:256
ds_read_b64_tr_b16 v[68:69],v5 offset:0
v_lshlrev_b32_sdwa v149,1,v149 dst_sel:DWORD dst_unused:UNUSED_PAD src0_sel:DWORD src1_sel:WORD_1
v_mfma_f32_16x16x32_bf16 v[164:167],v[88:91],v[136:139],v[164:167]
ds_read_u16 v148,v148
v_lshlrev_b32_sdwa v151,1,v151 dst_sel:DWORD dst_unused:UNUSED_PAD src0_sel:DWORD src1_sel:WORD_1
v_mfma_f32_16x16x32_bf16 v[172:175],v[92:95],v[136:139],v[172:175]
ds_read_u16_d16_hi v150,v150
ds_read_b64_tr_b16 v[70:71],v5 offset:2048
v_lshlrev_b32_sdwa v152,1,v152 dst_sel:DWORD dst_unused:UNUSED_PAD src0_sel:DWORD src1_sel:WORD_1
v_mfma_f32_16x16x32_bf16 v[180:183],v[96:99],v[136:139],v[180:183]
ds_read_b128 v[116:119],v7 offset:288
ds_read_b64_tr_b16 v[72:73],v4 offset:4096
v_lshlrev_b32_sdwa v154,1,v154 dst_sel:DWORD dst_unused:UNUSED_PAD src0_sel:DWORD src1_sel:WORD_1
v_mfma_f32_16x16x32_bf16 v[188:191],v[100:103],v[136:139],v[188:191]
s_cbranch_vccz RA74_%=
s_add_u32 m0,s100,0
s_mul_i32 s69,s59,9
s_add_u32 s69,s69,s97
buffer_load_dwordx4 v2,s[44:47],s69 offen lds
s_add_u32 m0,m0,1024
s_add_u32 s69,s69,s53
buffer_load_dwordx4 v3,s[44:47],s69 offen lds
RA74_%=:
ds_read_u16 v149,v149
v_lshlrev_b32_sdwa v153,1,v153 dst_sel:DWORD dst_unused:UNUSED_PAD src0_sel:DWORD src1_sel:WORD_1
v_mfma_f32_16x16x32_bf16 v[196:199],v[104:107],v[136:139],v[196:199]
ds_read_u16_d16_hi v151,v151
v_lshlrev_b32_sdwa v155,1,v155 dst_sel:DWORD dst_unused:UNUSED_PAD src0_sel:DWORD src1_sel:WORD_1
v_mfma_f32_16x16x32_bf16 v[204:207],v[108:111],v[136:139],v[204:207]
s_waitcnt lgkmcnt(8)
ds_read_b64_tr_b16 v[74:75],v4 offset:6144
ds_read_b64_tr_b16 v[76:77],v5 offset:4096
v_lshlrev_b32_sdwa v156,1,v156 dst_sel:DWORD dst_unused:UNUSED_PAD src0_sel:DWORD src1_sel:WORD_1
v_mfma_f32_16x16x32_bf16 v[128:131],v[64:67],v[16:19],v[112:115]
ds_read_u16 v152,v152
v_lshlrev_b32_sdwa v158,1,v158 dst_sel:DWORD dst_unused:UNUSED_PAD src0_sel:DWORD src1_sel:WORD_1
v_mfma_f32_16x16x32_bf16 v[136:139],v[64:67],v[20:23],v[112:115]
s_waitcnt lgkmcnt(6)
ds_read_u16_d16_hi v154,v154
ds_read_b64_tr_b16 v[78:79],v5 offset:6144
v_lshlrev_b32_sdwa v157,1,v157 dst_sel:DWORD dst_unused:UNUSED_PAD src0_sel:DWORD src1_sel:WORD_1
v_mfma_f32_16x16x32_bf16 v[140:143],v[68:71],v[20:23],v[116:119]
s_cbranch_vccz RA75_%=
s_add_u32 m0,m0,1024
s_add_u32 s69,s69,s53
buffer_load_dwordx4 v2,s[44:47],s69 offen lds
s_add_u32 m0,m0,s50
s_add_u32 s69,s69,s53
buffer_load_dwordx4 v3,s[44:47],s69 offen lds
RA75_%=:
ds_read_b64_tr_b16 v[80:81],v4 offset:8192
ds_read_b64_tr_b16 v[82:83],v4 offset:10240
v_lshlrev_b32_sdwa v159,1,v159 dst_sel:DWORD dst_unused:UNUSED_PAD src0_sel:DWORD src1_sel:WORD_1
v_mfma_f32_16x16x32_bf16 v[132:135],v[68:71],v[16:19],v[116:119]
s_waitcnt lgkmcnt(6)
ds_read_u16 v153,v153
v_mfma_f32_16x16x32_bf16 v[128:131],v[72:75],v[24:27],v[128:131]
ds_read_u16_d16_hi v155,v155
v_mfma_f32_16x16x32_bf16 v[136:139],v[72:75],v[28:31],v[136:139]
s_waitcnt lgkmcnt(4)
ds_read_b64_tr_b16 v[84:85],v5 offset:8192
ds_read_b64_tr_b16 v[86:87],v5 offset:10240
v_mfma_f32_16x16x32_bf16 v[140:143],v[76:79],v[28:31],v[140:143]
ds_read_u16 v156,v156
v_mfma_f32_16x16x32_bf16 v[132:135],v[76:79],v[24:27],v[132:135]
s_waitcnt lgkmcnt(5)
ds_read_u16_d16_hi v158,v158
v_lshl_add_u64 v[144:145],v[144:145],0,v[146:147]
v_mfma_f32_16x16x32_bf16 v[128:131],v[80:83],v[32:35],v[128:131]
s_cbranch_vccz RA76_%=
s_add_u32 m0,m0,1024
s_add_u32 s69,s69,s53
buffer_load_dwordx4 v2,s[44:47],s69 offen lds
s_add_u32 m0,m0,1024
s_add_u32 s69,s69,s53
buffer_load_dwordx4 v3,s[44:47],s69 offen lds
RA76_%=:
v_lshl_add_u64 v[146:147],v[148:149],0,v[150:151]
v_mfma_f32_16x16x32_bf16 v[136:139],v[80:83],v[36:39],v[136:139]
s_waitcnt lgkmcnt(2)
ds_read_u16 v157,v157
v_mfma_f32_16x16x32_bf16 v[140:143],v[84:87],v[36:39],v[140:143]
ds_read_u16_d16_hi v159,v159
v_mfma_f32_16x16x32_bf16 v[132:135],v[84:87],v[32:35],v[132:135]
v_lshlrev_b32_sdwa v128,1,v128 dst_sel:DWORD dst_unused:UNUSED_PAD src0_sel:DWORD src1_sel:WORD_1
v_lshlrev_b32_sdwa v130,1,v130 dst_sel:DWORD dst_unused:UNUSED_PAD src0_sel:DWORD src1_sel:WORD_1
v_mfma_f32_16x16x32_bf16 v[208:211],v[88:91],v[144:147],v[208:211]
ds_read_u16 v128,v128
v_lshlrev_b32_sdwa v129,1,v129 dst_sel:DWORD dst_unused:UNUSED_PAD src0_sel:DWORD src1_sel:WORD_1
v_mfma_f32_16x16x32_bf16 v[216:219],v[92:95],v[144:147],v[216:219]
s_cbranch_vccz RA77_%=
s_add_u32 m0,m0,s50
s_add_u32 s69,s69,s53
buffer_load_dwordx4 v2,s[44:47],s69 offen lds
s_add_u32 m0,m0,1024
s_add_u32 s69,s69,s53
buffer_load_dwordx4 v3,s[44:47],s69 offen lds
RA77_%=:
ds_read_u16_d16_hi v130,v130
v_lshlrev_b32_sdwa v131,1,v131 dst_sel:DWORD dst_unused:UNUSED_PAD src0_sel:DWORD src1_sel:WORD_1
v_mfma_f32_16x16x32_bf16 v[224:227],v[96:99],v[144:147],v[224:227]
v_lshlrev_b32_sdwa v132,1,v132 dst_sel:DWORD dst_unused:UNUSED_PAD src0_sel:DWORD src1_sel:WORD_1
v_mfma_f32_16x16x32_bf16 v[232:235],v[100:103],v[144:147],v[232:235]
ds_read_u16 v129,v129
v_lshl_add_u64 v[152:153],v[152:153],0,v[154:155]
v_lshlrev_b32_sdwa v134,1,v134 dst_sel:DWORD dst_unused:UNUSED_PAD src0_sel:DWORD src1_sel:WORD_1
v_mfma_f32_16x16x32_bf16 v[240:243],v[104:107],v[144:147],v[240:243]
s_waitcnt lgkmcnt(3)
ds_read_u16_d16_hi v131,v131
v_lshl_add_u64 v[154:155],v[156:157],0,v[158:159]
v_mfma_f32_16x16x32_bf16 v[248:251],v[108:111],v[144:147],v[248:251]
v_lshlrev_b32_sdwa v133,1,v133 dst_sel:DWORD dst_unused:UNUSED_PAD src0_sel:DWORD src1_sel:WORD_1
v_mfma_f32_16x16x32_bf16 v[212:215],v[88:91],v[152:155],v[212:215]
s_cbranch_vccz RA78_%=
s_add_u32 m0,m0,1024
s_add_u32 s69,s69,s53
buffer_load_dwordx4 v2,s[44:47],s69 offen lds
s_add_u32 m0,m0,s50
s_add_u32 s69,s69,s53
buffer_load_dwordx4 v3,s[44:47],s69 offen lds
RA78_%=:
ds_read_u16 v132,v132
v_lshlrev_b32_sdwa v135,1,v135 dst_sel:DWORD dst_unused:UNUSED_PAD src0_sel:DWORD src1_sel:WORD_1
v_mfma_f32_16x16x32_bf16 v[220:223],v[92:95],v[152:155],v[220:223]
ds_read_u16_d16_hi v134,v134
v_lshlrev_b32_sdwa v136,1,v136 dst_sel:DWORD dst_unused:UNUSED_PAD src0_sel:DWORD src1_sel:WORD_1
v_mfma_f32_16x16x32_bf16 v[228:231],v[96:99],v[152:155],v[228:231]
ds_read_b64_tr_b16 v[88:89],v6 offset:6160
ds_read_b64_tr_b16 v[90:91],v6 offset:7696
v_lshlrev_b32_sdwa v138,1,v138 dst_sel:DWORD dst_unused:UNUSED_PAD src0_sel:DWORD src1_sel:WORD_1
v_mfma_f32_16x16x32_bf16 v[236:239],v[100:103],v[152:155],v[236:239]
ds_read_u16 v133,v133
v_lshlrev_b32_sdwa v137,1,v137 dst_sel:DWORD dst_unused:UNUSED_PAD src0_sel:DWORD src1_sel:WORD_1
v_mfma_f32_16x16x32_bf16 v[244:247],v[104:107],v[152:155],v[244:247]
ds_read_u16_d16_hi v135,v135
v_lshlrev_b32_sdwa v139,1,v139 dst_sel:DWORD dst_unused:UNUSED_PAD src0_sel:DWORD src1_sel:WORD_1
v_mfma_f32_16x16x32_bf16 v[252:255],v[108:111],v[152:155],v[252:255]
s_cbranch_vccz RA79_%=
s_add_u32 m0,m0,1024
s_add_u32 s69,s69,s53
buffer_load_dwordx4 v2,s[44:47],s69 offen lds
s_add_u32 m0,m0,1024
s_add_u32 s69,s69,s53
buffer_load_dwordx4 v3,s[44:47],s69 offen lds
RA79_%=:
ds_read_b64_tr_b16 v[92:93],v6 offset:6168
ds_read_b64_tr_b16 v[94:95],v6 offset:7704
v_lshlrev_b32_sdwa v140,1,v140 dst_sel:DWORD dst_unused:UNUSED_PAD src0_sel:DWORD src1_sel:WORD_1
v_mfma_f32_16x16x32_bf16 v[144:147],v[64:67],v[40:43],v[112:115]
ds_read_u16 v136,v136
v_lshlrev_b32_sdwa v142,1,v142 dst_sel:DWORD dst_unused:UNUSED_PAD src0_sel:DWORD src1_sel:WORD_1
v_mfma_f32_16x16x32_bf16 v[152:155],v[64:67],v[44:47],v[112:115]
ds_read_u16_d16_hi v138,v138
v_lshlrev_b32_sdwa v141,1,v141 dst_sel:DWORD dst_unused:UNUSED_PAD src0_sel:DWORD src1_sel:WORD_1
v_mfma_f32_16x16x32_bf16 v[156:159],v[68:71],v[44:47],v[116:119]
ds_read_b64_tr_b16 v[96:97],v6 offset:6224
ds_read_b64_tr_b16 v[98:99],v6 offset:7760
v_lshlrev_b32_sdwa v143,1,v143 dst_sel:DWORD dst_unused:UNUSED_PAD src0_sel:DWORD src1_sel:WORD_1
v_mfma_f32_16x16x32_bf16 v[148:151],v[68:71],v[40:43],v[116:119]
ds_read_u16 v137,v137
v_mfma_f32_16x16x32_bf16 v[144:147],v[72:75],v[48:51],v[144:147]
ds_read_u16_d16_hi v139,v139
v_mfma_f32_16x16x32_bf16 v[152:155],v[72:75],v[52:55],v[152:155]
ds_read_b64_tr_b16 v[100:101],v6 offset:6232
ds_read_b64_tr_b16 v[102:103],v6 offset:7768
v_mfma_f32_16x16x32_bf16 v[156:159],v[76:79],v[52:55],v[156:159]
ds_read_u16 v140,v140
v_mfma_f32_16x16x32_bf16 v[148:151],v[76:79],v[48:51],v[148:151]
s_waitcnt lgkmcnt(15)
ds_read_u16_d16_hi v142,v142
v_lshl_add_u64 v[128:129],v[128:129],0,v[130:131]
v_mfma_f32_16x16x32_bf16 v[144:147],v[80:83],v[56:59],v[144:147]
s_waitcnt lgkmcnt(12)
ds_read_b64_tr_b16 v[104:105],v6 offset:6288
s_mov_b32 s86,0xbe801d5a
ds_read_b64_tr_b16 v[106:107],v6 offset:7824
v_lshl_add_u64 v[130:131],v[132:133],0,v[134:135]
v_mfma_f32_16x16x32_bf16 v[152:155],v[80:83],v[60:63],v[152:155]
ds_read_u16 v141,v141
v_mfma_f32_16x16x32_bf16 v[156:159],v[84:87],v[60:63],v[156:159]
ds_read_u16_d16_hi v143,v143
v_mfma_f32_16x16x32_bf16 v[148:151],v[84:87],v[56:59],v[148:151]
ds_read_b64_tr_b16 v[108:109],v6 offset:6296
ds_read_b64_tr_b16 v[110:111],v6 offset:7832
v_lshlrev_b32_sdwa v144,1,v144 dst_sel:DWORD dst_unused:UNUSED_PAD src0_sel:DWORD src1_sel:WORD_1
v_lshlrev_b32_sdwa v146,1,v146 dst_sel:DWORD dst_unused:UNUSED_PAD src0_sel:DWORD src1_sel:WORD_1
v_mfma_f32_16x16x32_bf16 v[160:163],v[88:91],v[128:131],v[160:163]
s_waitcnt lgkmcnt(15)
ds_read_u16 v144,v144
v_lshlrev_b32_sdwa v145,1,v145 dst_sel:DWORD dst_unused:UNUSED_PAD src0_sel:DWORD src1_sel:WORD_1
v_mfma_f32_16x16x32_bf16 v[168:171],v[92:95],v[128:131],v[168:171]
s_waitcnt lgkmcnt(13)
ds_read_u16_d16_hi v146,v146
v_lshlrev_b32_sdwa v147,1,v147 dst_sel:DWORD dst_unused:UNUSED_PAD src0_sel:DWORD src1_sel:WORD_1
v_mfma_f32_16x16x32_bf16 v[176:179],v[96:99],v[128:131],v[176:179]
s_waitcnt lgkmcnt(10)
ds_read_b64_tr_b16 v[64:65],v4 offset:512
ds_read_b64_tr_b16 v[66:67],v4 offset:2560
v_lshlrev_b32_sdwa v148,1,v148 dst_sel:DWORD dst_unused:UNUSED_PAD src0_sel:DWORD src1_sel:WORD_1
v_mfma_f32_16x16x32_bf16 v[184:187],v[100:103],v[128:131],v[184:187]
s_waitcnt lgkmcnt(8)
ds_read_u16 v145,v145
v_lshl_add_u64 v[136:137],v[136:137],0,v[138:139]
v_lshlrev_b32_sdwa v150,1,v150 dst_sel:DWORD dst_unused:UNUSED_PAD src0_sel:DWORD src1_sel:WORD_1
v_mfma_f32_16x16x32_bf16 v[192:195],v[104:107],v[128:131],v[192:195]
s_waitcnt lgkmcnt(5)
ds_read_u16_d16_hi v147,v147
v_lshl_add_u64 v[138:139],v[140:141],0,v[142:143]
v_mfma_f32_16x16x32_bf16 v[200:203],v[108:111],v[128:131],v[200:203]
ds_read_b128 v[112:115],v7 offset:384
ds_read_b64_tr_b16 v[68:69],v5 offset:512
v_lshlrev_b32_sdwa v149,1,v149 dst_sel:DWORD dst_unused:UNUSED_PAD src0_sel:DWORD src1_sel:WORD_1
v_mfma_f32_16x16x32_bf16 v[164:167],v[88:91],v[136:139],v[164:167]
ds_read_u16 v148,v148
v_lshlrev_b32_sdwa v151,1,v151 dst_sel:DWORD dst_unused:UNUSED_PAD src0_sel:DWORD src1_sel:WORD_1
v_mfma_f32_16x16x32_bf16 v[172:175],v[92:95],v[136:139],v[172:175]
ds_read_u16_d16_hi v150,v150
ds_read_b64_tr_b16 v[70:71],v5 offset:2560
v_lshlrev_b32_sdwa v152,1,v152 dst_sel:DWORD dst_unused:UNUSED_PAD src0_sel:DWORD src1_sel:WORD_1
v_mfma_f32_16x16x32_bf16 v[180:183],v[96:99],v[136:139],v[180:183]
ds_read_b128 v[116:119],v7 offset:416
ds_read_b64_tr_b16 v[72:73],v4 offset:4608
v_lshlrev_b32_sdwa v154,1,v154 dst_sel:DWORD dst_unused:UNUSED_PAD src0_sel:DWORD src1_sel:WORD_1
v_mfma_f32_16x16x32_bf16 v[188:191],v[100:103],v[136:139],v[188:191]
ds_read_u16 v149,v149
v_lshlrev_b32_sdwa v153,1,v153 dst_sel:DWORD dst_unused:UNUSED_PAD src0_sel:DWORD src1_sel:WORD_1
v_mfma_f32_16x16x32_bf16 v[196:199],v[104:107],v[136:139],v[196:199]
ds_read_u16_d16_hi v151,v151
v_lshlrev_b32_sdwa v155,1,v155 dst_sel:DWORD dst_unused:UNUSED_PAD src0_sel:DWORD src1_sel:WORD_1
v_mfma_f32_16x16x32_bf16 v[204:207],v[108:111],v[136:139],v[204:207]
s_waitcnt lgkmcnt(8)
ds_read_b64_tr_b16 v[74:75],v4 offset:6656
ds_read_b64_tr_b16 v[76:77],v5 offset:4608
v_lshlrev_b32_sdwa v156,1,v156 dst_sel:DWORD dst_unused:UNUSED_PAD src0_sel:DWORD src1_sel:WORD_1
v_mfma_f32_16x16x32_bf16 v[128:131],v[64:67],v[16:19],v[112:115]
ds_read_u16 v152,v152
v_lshlrev_b32_sdwa v158,1,v158 dst_sel:DWORD dst_unused:UNUSED_PAD src0_sel:DWORD src1_sel:WORD_1
v_mfma_f32_16x16x32_bf16 v[136:139],v[64:67],v[20:23],v[112:115]
s_waitcnt lgkmcnt(6)
ds_read_u16_d16_hi v154,v154
ds_read_b64_tr_b16 v[78:79],v5 offset:6656
v_lshlrev_b32_sdwa v157,1,v157 dst_sel:DWORD dst_unused:UNUSED_PAD src0_sel:DWORD src1_sel:WORD_1
v_mfma_f32_16x16x32_bf16 v[140:143],v[68:71],v[20:23],v[116:119]
ds_read_b64_tr_b16 v[80:81],v4 offset:8704
ds_read_b64_tr_b16 v[82:83],v4 offset:10752
v_lshlrev_b32_sdwa v159,1,v159 dst_sel:DWORD dst_unused:UNUSED_PAD src0_sel:DWORD src1_sel:WORD_1
v_mfma_f32_16x16x32_bf16 v[132:135],v[68:71],v[16:19],v[116:119]
s_waitcnt lgkmcnt(6)
ds_read_u16 v153,v153
v_mfma_f32_16x16x32_bf16 v[128:131],v[72:75],v[24:27],v[128:131]
ds_read_u16_d16_hi v155,v155
v_mfma_f32_16x16x32_bf16 v[136:139],v[72:75],v[28:31],v[136:139]
s_waitcnt lgkmcnt(4)
ds_read_b64_tr_b16 v[84:85],v5 offset:8704
ds_read_b64_tr_b16 v[86:87],v5 offset:10752
v_mfma_f32_16x16x32_bf16 v[140:143],v[76:79],v[28:31],v[140:143]
ds_read_u16 v156,v156
v_mfma_f32_16x16x32_bf16 v[132:135],v[76:79],v[24:27],v[132:135]
s_waitcnt lgkmcnt(5)
ds_read_u16_d16_hi v158,v158
v_lshl_add_u64 v[144:145],v[144:145],0,v[146:147]
v_mfma_f32_16x16x32_bf16 v[128:131],v[80:83],v[32:35],v[128:131]
v_lshl_add_u64 v[146:147],v[148:149],0,v[150:151]
v_mfma_f32_16x16x32_bf16 v[136:139],v[80:83],v[36:39],v[136:139]
s_waitcnt lgkmcnt(2)
ds_read_u16 v157,v157
v_mfma_f32_16x16x32_bf16 v[140:143],v[84:87],v[36:39],v[140:143]
ds_read_u16_d16_hi v159,v159
v_mfma_f32_16x16x32_bf16 v[132:135],v[84:87],v[32:35],v[132:135]
s_nop 0
v_lshlrev_b32_sdwa v128,1,v128 dst_sel:DWORD dst_unused:UNUSED_PAD src0_sel:DWORD src1_sel:WORD_1
v_lshlrev_b32_sdwa v130,1,v130 dst_sel:DWORD dst_unused:UNUSED_PAD src0_sel:DWORD src1_sel:WORD_1
v_mfma_f32_16x16x32_bf16 v[208:211],v[88:91],v[144:147],v[208:211]
s_waitcnt vmcnt(12)
s_barrier
s_setprio 3
ds_read_u16 v128,v128
v_lshlrev_b32_sdwa v129,1,v129 dst_sel:DWORD dst_unused:UNUSED_PAD src0_sel:DWORD src1_sel:WORD_1
v_lshlrev_b32_sdwa v131,1,v131 dst_sel:DWORD dst_unused:UNUSED_PAD src0_sel:DWORD src1_sel:WORD_1
v_mfma_f32_16x16x32_bf16 v[216:219],v[92:95],v[144:147],v[216:219]
v_fma_mixlo_f16 v120,v140,s99,0
v_fma_mixlo_f16 v122,v141,s99,0
v_mfma_f32_16x16x32_bf16 v[224:227],v[96:99],v[144:147],v[224:227]
s_cbranch_vccz RA80_%=
s_add_u32 m0,s58,0
s_mul_i32 s69,s59,5
s_add_u32 s69,s69,s97
buffer_load_dwordx4 v2,s[44:47],s69 offen lds
s_add_u32 m0,m0,1024
s_add_u32 s69,s69,s53
buffer_load_dwordx4 v3,s[44:47],s69 offen lds
RA80_%=:
ds_read_u16_d16_hi v130,v130
v_lshlrev_b32_sdwa v132,1,v132 dst_sel:DWORD dst_unused:UNUSED_PAD src0_sel:DWORD src1_sel:WORD_1
v_lshlrev_b32_sdwa v134,1,v134 dst_sel:DWORD dst_unused:UNUSED_PAD src0_sel:DWORD src1_sel:WORD_1
v_mfma_f32_16x16x32_bf16 v[232:235],v[100:103],v[144:147],v[232:235]
v_lshl_add_u64 v[152:153],v[152:153],0,v[154:155]
v_mfma_f32_16x16x32_bf16 v[240:243],v[104:107],v[144:147],v[240:243]
s_waitcnt lgkmcnt(2)
ds_read_u16 v129,v129
v_lshl_add_u64 v[154:155],v[156:157],0,v[158:159]
v_mfma_f32_16x16x32_bf16 v[248:251],v[108:111],v[144:147],v[248:251]
v_fma_mixhi_f16 v120,v142,s99,0
v_fma_mixhi_f16 v122,v143,s99,0
v_mfma_f32_16x16x32_bf16 v[212:215],v[88:91],v[152:155],v[212:215]
s_cbranch_vccz RA81_%=
s_add_u32 m0,m0,1024
s_add_u32 s69,s69,s53
buffer_load_dwordx4 v2,s[44:47],s69 offen lds
s_add_u32 m0,m0,s50
s_add_u32 s69,s69,s53
buffer_load_dwordx4 v3,s[44:47],s69 offen lds
RA81_%=:
ds_read_u16_d16_hi v131,v131
v_lshlrev_b32_sdwa v133,1,v133 dst_sel:DWORD dst_unused:UNUSED_PAD src0_sel:DWORD src1_sel:WORD_1
v_lshlrev_b32_sdwa v135,1,v135 dst_sel:DWORD dst_unused:UNUSED_PAD src0_sel:DWORD src1_sel:WORD_1
v_mfma_f32_16x16x32_bf16 v[220:223],v[92:95],v[152:155],v[220:223]
ds_read_b64_tr_b16 v[88:89],v6 offset:12320
v_pk_fma_f16 v121,v120,v120,s54
v_pk_fma_f16 v123,v122,v122,s54
v_mfma_f32_16x16x32_bf16 v[228:231],v[96:99],v[152:155],v[228:231]
ds_read_u16 v132,v132
v_pk_mul_f16 v121,v120,v121
v_pk_mul_f16 v123,v122,v123
v_mfma_f32_16x16x32_bf16 v[236:239],v[100:103],v[152:155],v[236:239]
ds_read_b64_tr_b16 v[90:91],v6 offset:13856
v_lshlrev_b32_sdwa v136,1,v136 dst_sel:DWORD dst_unused:UNUSED_PAD src0_sel:DWORD src1_sel:WORD_1
v_lshlrev_b32_sdwa v138,1,v138 dst_sel:DWORD dst_unused:UNUSED_PAD src0_sel:DWORD src1_sel:WORD_1
v_mfma_f32_16x16x32_bf16 v[244:247],v[104:107],v[152:155],v[244:247]
s_cbranch_vccz RA82_%=
s_add_u32 m0,m0,1024
s_add_u32 s69,s69,s53
buffer_load_dwordx4 v2,s[44:47],s69 offen lds
s_add_u32 m0,m0,1024
s_add_u32 s69,s69,s53
buffer_load_dwordx4 v3,s[44:47],s69 offen lds
RA82_%=:
ds_read_u16_d16_hi v134,v134
v_exp_f16 v120,v121
v_mfma_f32_16x16x32_bf16 v[252:255],v[108:111],v[152:155],v[252:255]
ds_read_b64_tr_b16 v[92:93],v6 offset:12328
v_exp_f16 v122,v123
v_mfma_f32_16x16x32_bf16 v[144:147],v[64:67],v[40:43],v[112:115]
ds_read_u16 v133,v133
v_lshlrev_b32_sdwa v137,1,v137 dst_sel:DWORD dst_unused:UNUSED_PAD src0_sel:DWORD src1_sel:WORD_1
v_lshlrev_b32_sdwa v139,1,v139 dst_sel:DWORD dst_unused:UNUSED_PAD src0_sel:DWORD src1_sel:WORD_1
v_mfma_f32_16x16x32_bf16 v[152:155],v[64:67],v[44:47],v[112:115]
s_cbranch_vccz RA83_%=
s_add_u32 m0,m0,s50
s_add_u32 s69,s69,s53
buffer_load_dwordx4 v2,s[44:47],s69 offen lds
s_add_u32 m0,m0,1024
s_add_u32 s69,s69,s53
buffer_load_dwordx4 v3,s[44:47],s69 offen lds
RA83_%=:
ds_read_b64_tr_b16 v[94:95],v6 offset:13864
v_exp_f16_sdwa v120,v121 dst_sel:WORD_1 dst_unused:UNUSED_PRESERVE src0_sel:WORD_1
v_mfma_f32_16x16x32_bf16 v[156:159],v[68:71],v[44:47],v[116:119]
ds_read_u16_d16_hi v135,v135
v_exp_f16_sdwa v122,v123 dst_sel:WORD_1 dst_unused:UNUSED_PRESERVE src0_sel:WORD_1
v_mfma_f32_16x16x32_bf16 v[148:151],v[68:71],v[40:43],v[116:119]
ds_read_b64_tr_b16 v[96:97],v6 offset:12384
v_pk_fma_f16 v120,v120,s55,s55
v_pk_fma_f16 v122,v122,s55,s55
v_mfma_f32_16x16x32_bf16 v[144:147],v[72:75],v[48:51],v[144:147]
ds_read_u16 v136,v136
v_rcp_f16 v121,v120
v_mfma_f32_16x16x32_bf16 v[152:155],v[72:75],v[52:55],v[152:155]
s_cbranch_vccz RA84_%=
s_add_u32 m0,m0,1024
s_add_u32 s69,s69,s53
buffer_load_dwordx4 v2,s[44:47],s69 offen lds
s_add_u32 m0,m0,s50
s_add_u32 s69,s69,s53
buffer_load_dwordx4 v3,s[44:47],s69 offen lds
RA84_%=:
ds_read_b64_tr_b16 v[98:99],v6 offset:13920
ds_read_b64_tr_b16 v[100:101],v6 offset:12392
v_rcp_f16 v123,v122
v_mfma_f32_16x16x32_bf16 v[156:159],v[76:79],v[52:55],v[156:159]
ds_read_u16_d16_hi v138,v138
v_rcp_f16_sdwa v121,v120 dst_sel:WORD_1 dst_unused:UNUSED_PRESERVE src0_sel:WORD_1
v_mfma_f32_16x16x32_bf16 v[148:151],v[76:79],v[48:51],v[148:151]
ds_read_b64_tr_b16 v[102:103],v6 offset:13928
ds_read_b64_tr_b16 v[104:105],v6 offset:12448
v_rcp_f16_sdwa v123,v122 dst_sel:WORD_1 dst_unused:UNUSED_PRESERVE src0_sel:WORD_1
v_mfma_f32_16x16x32_bf16 v[144:147],v[80:83],v[56:59],v[144:147]
ds_read_u16 v137,v137
v_fma_mix_f32 v140,v140,v121,0 op_sel_hi:[0,1,0]
v_fma_mix_f32 v142,v142,v121,0 op_sel:[0,1,0] op_sel_hi:[0,1,0]
v_mfma_f32_16x16x32_bf16 v[152:155],v[80:83],v[60:63],v[152:155]
s_waitcnt lgkmcnt(8)
s_cbranch_vccz RA85_%=
s_add_u32 m0,m0,1024
s_add_u32 s69,s69,s53
buffer_load_dwordx4 v2,s[44:47],s69 offen lds
s_add_u32 m0,m0,1024
s_add_u32 s69,s69,s53
buffer_load_dwordx4 v3,s[44:47],s69 offen lds
RA85_%=:
ds_read_b64_tr_b16 v[106:107],v6 offset:13984
ds_read_b64_tr_b16 v[108:109],v6 offset:12456
v_lshl_add_u64 v[128:129],v[128:129],0,v[130:131]
v_lshl_add_u64 v[130:131],v[132:133],0,v[134:135]
v_mfma_f32_16x16x32_bf16 v[156:159],v[84:87],v[60:63],v[156:159]
ds_read_u16_d16_hi v139,v139
v_fma_mix_f32 v141,v141,v123,0 op_sel_hi:[0,1,0]
v_fma_mix_f32 v143,v143,v123,0 op_sel:[0,1,0] op_sel_hi:[0,1,0]
v_mfma_f32_16x16x32_bf16 v[148:151],v[84:87],v[56:59],v[148:151]
ds_read_b64_tr_b16 v[110:111],v6 offset:13992
ds_read_b64_tr_b16 v[64:65],v4 offset:12320
v_lshlrev_b32_sdwa v144,1,v144 dst_sel:DWORD dst_unused:UNUSED_PAD src0_sel:DWORD src1_sel:WORD_1
v_lshlrev_b32_sdwa v146,1,v146 dst_sel:DWORD dst_unused:UNUSED_PAD src0_sel:DWORD src1_sel:WORD_1
v_mfma_f32_16x16x32_bf16 v[160:163],v[88:91],v[128:131],v[160:163]
ds_read_u16 v144,v144
v_lshlrev_b32_sdwa v145,1,v145 dst_sel:DWORD dst_unused:UNUSED_PAD src0_sel:DWORD src1_sel:WORD_1
v_lshlrev_b32_sdwa v147,1,v147 dst_sel:DWORD dst_unused:UNUSED_PAD src0_sel:DWORD src1_sel:WORD_1
v_mfma_f32_16x16x32_bf16 v[168:171],v[92:95],v[128:131],v[168:171]
s_waitcnt lgkmcnt(11)
s_cbranch_vccz RA86_%=
s_add_u32 m0,s100,12320
s_mov_b32 s69,s60
buffer_load_dwordx4 v2,s[44:47],s69 offen lds
s_add_u32 m0,m0,1024
s_add_u32 s69,s69,s53
buffer_load_dwordx4 v3,s[44:47],s69 offen lds
RA86_%=:
ds_read_b64_tr_b16 v[66:67],v4 offset:14368
ds_read_b128 v[112:115],v7 offset:512
v_fma_mixlo_f16 v120,v156,s99,0
v_fma_mixlo_f16 v122,v157,s99,0
v_mfma_f32_16x16x32_bf16 v[176:179],v[96:99],v[128:131],v[176:179]
s_waitcnt lgkmcnt(10)
ds_read_u16_d16_hi v146,v146
v_lshlrev_b32_sdwa v148,1,v148 dst_sel:DWORD dst_unused:UNUSED_PAD src0_sel:DWORD src1_sel:WORD_1
v_lshlrev_b32_sdwa v150,1,v150 dst_sel:DWORD dst_unused:UNUSED_PAD src0_sel:DWORD src1_sel:WORD_1
v_mfma_f32_16x16x32_bf16 v[184:187],v[100:103],v[128:131],v[184:187]
s_waitcnt lgkmcnt(6)
ds_read_b64_tr_b16 v[68:69],v5 offset:12320
ds_read_b64_tr_b16 v[70:71],v5 offset:14368
v_lshl_add_u64 v[136:137],v[136:137],0,v[138:139]
v_mfma_f32_16x16x32_bf16 v[192:195],v[104:107],v[128:131],v[192:195]
s_waitcnt lgkmcnt(7)
ds_read_u16 v145,v145
v_cvt_pk_bf16_f32 v138,v140,v142
v_cvt_pk_bf16_f32 v139,v141,v143
v_mfma_f32_16x16x32_bf16 v[200:203],v[108:111],v[128:131],v[200:203]
s_cbranch_vccz RA87_%=
s_add_u32 m0,m0,1024
s_add_u32 s69,s69,s53
buffer_load_dwordx4 v2,s[44:47],s69 offen lds
s_add_u32 m0,m0,s50
s_add_u32 s69,s69,s53
buffer_load_dwordx4 v3,s[44:47],s69 offen lds
RA87_%=:
ds_read_b128 v[116:119],v7 offset:544
ds_read_b64_tr_b16 v[72:73],v4 offset:16416
v_fma_mixhi_f16 v120,v158,s99,0
v_fma_mixhi_f16 v122,v159,s99,0
v_mfma_f32_16x16x32_bf16 v[164:167],v[88:91],v[136:139],v[164:167]
ds_read_u16_d16_hi v147,v147
v_lshlrev_b32_sdwa v149,1,v149 dst_sel:DWORD dst_unused:UNUSED_PAD src0_sel:DWORD src1_sel:WORD_1
v_lshlrev_b32_sdwa v151,1,v151 dst_sel:DWORD dst_unused:UNUSED_PAD src0_sel:DWORD src1_sel:WORD_1
v_mfma_f32_16x16x32_bf16 v[172:175],v[92:95],v[136:139],v[172:175]
ds_read_b64_tr_b16 v[74:75],v4 offset:18464
ds_read_b64_tr_b16 v[76:77],v5 offset:16416
v_pk_fma_f16 v121,v120,v120,s54
v_pk_fma_f16 v123,v122,v122,s54
v_mfma_f32_16x16x32_bf16 v[180:183],v[96:99],v[136:139],v[180:183]
s_cbranch_vccz RA88_%=
s_add_u32 m0,m0,1024
s_add_u32 s69,s69,s53
buffer_load_dwordx4 v2,s[44:47],s69 offen lds
s_add_u32 m0,m0,1024
s_add_u32 s69,s69,s53
buffer_load_dwordx4 v3,s[44:47],s69 offen lds
RA88_%=:
ds_read_u16 v148,v148
v_pk_mul_f16 v121,v120,v121
v_pk_mul_f16 v123,v122,v123
v_mfma_f32_16x16x32_bf16 v[188:191],v[100:103],v[136:139],v[188:191]
ds_read_b64_tr_b16 v[78:79],v5 offset:18464
v_lshlrev_b32_sdwa v152,1,v152 dst_sel:DWORD dst_unused:UNUSED_PAD src0_sel:DWORD src1_sel:WORD_1
v_lshlrev_b32_sdwa v154,1,v154 dst_sel:DWORD dst_unused:UNUSED_PAD src0_sel:DWORD src1_sel:WORD_1
v_mfma_f32_16x16x32_bf16 v[196:199],v[104:107],v[136:139],v[196:199]
ds_read_u16_d16_hi v150,v150
v_exp_f16 v120,v121
v_mfma_f32_16x16x32_bf16 v[204:207],v[108:111],v[136:139],v[204:207]
s_waitcnt lgkmcnt(12)
ds_read_b64_tr_b16 v[80:81],v4 offset:20512
v_exp_f16 v122,v123
v_mfma_f32_16x16x32_bf16 v[128:131],v[64:67],v[16:19],v[112:115]
s_cbranch_vccz RA89_%=
s_add_u32 m0,m0,s50
s_add_u32 s69,s69,s51
buffer_load_dwordx4 v2,s[44:47],s69 offen lds
s_add_u32 m0,m0,1024
s_add_u32 s69,s69,s53
buffer_load_dwordx4 v3,s[44:47],s69 offen lds
RA89_%=:
ds_read_u16 v149,v149
v_lshlrev_b32_sdwa v153,1,v153 dst_sel:DWORD dst_unused:UNUSED_PAD src0_sel:DWORD src1_sel:WORD_1
v_lshlrev_b32_sdwa v155,1,v155 dst_sel:DWORD dst_unused:UNUSED_PAD src0_sel:DWORD src1_sel:WORD_1
v_mfma_f32_16x16x32_bf16 v[136:139],v[64:67],v[20:23],v[112:115]
s_waitcnt lgkmcnt(9)
ds_read_b64_tr_b16 v[82:83],v4 offset:22560
v_exp_f16_sdwa v120,v121 dst_sel:WORD_1 dst_unused:UNUSED_PRESERVE src0_sel:WORD_1
v_mfma_f32_16x16x32_bf16 v[140:143],v[68:71],v[20:23],v[116:119]
ds_read_u16_d16_hi v151,v151
v_exp_f16_sdwa v122,v123 dst_sel:WORD_1 dst_unused:UNUSED_PRESERVE src0_sel:WORD_1
v_mfma_f32_16x16x32_bf16 v[132:135],v[68:71],v[16:19],v[116:119]
s_waitcnt lgkmcnt(8)
ds_read_b64_tr_b16 v[84:85],v5 offset:20512
v_pk_fma_f16 v120,v120,s55,s55
v_pk_fma_f16 v122,v122,s55,s55
v_mfma_f32_16x16x32_bf16 v[128:131],v[72:75],v[24:27],v[128:131]
s_cbranch_vccz RA90_%=
s_add_u32 m0,m0,1024
s_add_u32 s69,s69,s53
buffer_load_dwordx4 v2,s[44:47],s69 offen lds
s_add_u32 m0,m0,s50
s_add_u32 s69,s69,s53
buffer_load_dwordx4 v3,s[44:47],s69 offen lds
RA90_%=:
ds_read_u16 v152,v152
v_rcp_f16 v121,v120
v_mfma_f32_16x16x32_bf16 v[136:139],v[72:75],v[28:31],v[136:139]
s_waitcnt lgkmcnt(7)
ds_read_b64_tr_b16 v[86:87],v5 offset:22560
v_rcp_f16 v123,v122
v_mfma_f32_16x16x32_bf16 v[140:143],v[76:79],v[28:31],v[140:143]
ds_read_u16_d16_hi v154,v154
v_rcp_f16_sdwa v121,v120 dst_sel:WORD_1 dst_unused:UNUSED_PRESERVE src0_sel:WORD_1
v_mfma_f32_16x16x32_bf16 v[132:135],v[76:79],v[24:27],v[132:135]
s_waitcnt lgkmcnt(5)
v_rcp_f16_sdwa v123,v122 dst_sel:WORD_1 dst_unused:UNUSED_PRESERVE src0_sel:WORD_1
v_mfma_f32_16x16x32_bf16 v[128:131],v[80:83],v[32:35],v[128:131]
s_cbranch_vccz RA91_%=
s_add_u32 m0,m0,1024
s_add_u32 s69,s69,s53
buffer_load_dwordx4 v2,s[44:47],s69 offen lds
s_add_u32 m0,m0,1024
s_add_u32 s69,s69,s53
buffer_load_dwordx4 v3,s[44:47],s69 offen lds
RA91_%=:
ds_read_u16 v153,v153
v_fma_mix_f32 v156,v156,v121,0 op_sel_hi:[0,1,0]
v_fma_mix_f32 v158,v158,v121,0 op_sel:[0,1,0] op_sel_hi:[0,1,0]
v_mfma_f32_16x16x32_bf16 v[136:139],v[80:83],v[36:39],v[136:139]
s_waitcnt lgkmcnt(2)
v_lshl_add_u64 v[144:145],v[144:145],0,v[146:147]
v_lshl_add_u64 v[146:147],v[148:149],0,v[150:151]
v_mfma_f32_16x16x32_bf16 v[140:143],v[84:87],v[36:39],v[140:143]
ds_read_u16_d16_hi v155,v155
v_fma_mix_f32 v157,v157,v123,0 op_sel_hi:[0,1,0]
v_fma_mix_f32 v159,v159,v123,0 op_sel:[0,1,0] op_sel_hi:[0,1,0]
v_mfma_f32_16x16x32_bf16 v[132:135],v[84:87],v[32:35],v[132:135]
v_lshlrev_b32_sdwa v128,1,v128 dst_sel:DWORD dst_unused:UNUSED_PAD src0_sel:DWORD src1_sel:WORD_1
v_lshlrev_b32_sdwa v130,1,v130 dst_sel:DWORD dst_unused:UNUSED_PAD src0_sel:DWORD src1_sel:WORD_1
v_mfma_f32_16x16x32_bf16 v[208:211],v[88:91],v[144:147],v[208:211]
ds_read_u16 v128,v128
v_lshlrev_b32_sdwa v129,1,v129 dst_sel:DWORD dst_unused:UNUSED_PAD src0_sel:DWORD src1_sel:WORD_1
v_lshlrev_b32_sdwa v131,1,v131 dst_sel:DWORD dst_unused:UNUSED_PAD src0_sel:DWORD src1_sel:WORD_1
v_mfma_f32_16x16x32_bf16 v[216:219],v[92:95],v[144:147],v[216:219]
v_fma_mixlo_f16 v120,v140,s99,0
v_fma_mixlo_f16 v122,v141,s99,0
v_mfma_f32_16x16x32_bf16 v[224:227],v[96:99],v[144:147],v[224:227]
ds_read_u16_d16_hi v130,v130
v_lshlrev_b32_sdwa v132,1,v132 dst_sel:DWORD dst_unused:UNUSED_PAD src0_sel:DWORD src1_sel:WORD_1
v_lshlrev_b32_sdwa v134,1,v134 dst_sel:DWORD dst_unused:UNUSED_PAD src0_sel:DWORD src1_sel:WORD_1
v_mfma_f32_16x16x32_bf16 v[232:235],v[100:103],v[144:147],v[232:235]
s_waitcnt lgkmcnt(2)
v_lshl_add_u64 v[152:153],v[152:153],0,v[154:155]
v_mfma_f32_16x16x32_bf16 v[240:243],v[104:107],v[144:147],v[240:243]
ds_read_u16 v129,v129
v_cvt_pk_bf16_f32 v154,v156,v158
v_cvt_pk_bf16_f32 v155,v157,v159
v_mfma_f32_16x16x32_bf16 v[248:251],v[108:111],v[144:147],v[248:251]
v_fma_mixhi_f16 v120,v142,s99,0
v_fma_mixhi_f16 v122,v143,s99,0
v_mfma_f32_16x16x32_bf16 v[212:215],v[88:91],v[152:155],v[212:215]
s_setprio 2
ds_read_u16_d16_hi v131,v131
v_lshlrev_b32_sdwa v133,1,v133 dst_sel:DWORD dst_unused:UNUSED_PAD src0_sel:DWORD src1_sel:WORD_1
v_lshlrev_b32_sdwa v135,1,v135 dst_sel:DWORD dst_unused:UNUSED_PAD src0_sel:DWORD src1_sel:WORD_1
v_mfma_f32_16x16x32_bf16 v[220:223],v[92:95],v[152:155],v[220:223]
ds_read_b64_tr_b16 v[88:89],v6 offset:18480
v_pk_fma_f16 v121,v120,v120,s54
v_pk_fma_f16 v123,v122,v122,s54
v_mfma_f32_16x16x32_bf16 v[228:231],v[96:99],v[152:155],v[228:231]
ds_read_u16 v132,v132
v_pk_mul_f16 v121,v120,v121
v_pk_mul_f16 v123,v122,v123
v_mfma_f32_16x16x32_bf16 v[236:239],v[100:103],v[152:155],v[236:239]
ds_read_b64_tr_b16 v[90:91],v6 offset:20016
v_lshlrev_b32_sdwa v136,1,v136 dst_sel:DWORD dst_unused:UNUSED_PAD src0_sel:DWORD src1_sel:WORD_1
v_lshlrev_b32_sdwa v138,1,v138 dst_sel:DWORD dst_unused:UNUSED_PAD src0_sel:DWORD src1_sel:WORD_1
v_mfma_f32_16x16x32_bf16 v[244:247],v[104:107],v[152:155],v[244:247]
ds_read_u16_d16_hi v134,v134
v_exp_f16 v120,v121
v_mfma_f32_16x16x32_bf16 v[252:255],v[108:111],v[152:155],v[252:255]
ds_read_b64_tr_b16 v[92:93],v6 offset:18488
v_exp_f16 v122,v123
v_mfma_f32_16x16x32_bf16 v[144:147],v[64:67],v[40:43],v[112:115]
ds_read_u16 v133,v133
v_lshlrev_b32_sdwa v137,1,v137 dst_sel:DWORD dst_unused:UNUSED_PAD src0_sel:DWORD src1_sel:WORD_1
v_lshlrev_b32_sdwa v139,1,v139 dst_sel:DWORD dst_unused:UNUSED_PAD src0_sel:DWORD src1_sel:WORD_1
v_mfma_f32_16x16x32_bf16 v[152:155],v[64:67],v[44:47],v[112:115]
ds_read_b64_tr_b16 v[94:95],v6 offset:20024
v_exp_f16_sdwa v120,v121 dst_sel:WORD_1 dst_unused:UNUSED_PRESERVE src0_sel:WORD_1
v_mfma_f32_16x16x32_bf16 v[156:159],v[68:71],v[44:47],v[116:119]
ds_read_u16_d16_hi v135,v135
v_exp_f16_sdwa v122,v123 dst_sel:WORD_1 dst_unused:UNUSED_PRESERVE src0_sel:WORD_1
v_mfma_f32_16x16x32_bf16 v[148:151],v[68:71],v[40:43],v[116:119]
ds_read_b64_tr_b16 v[96:97],v6 offset:18544
v_pk_fma_f16 v120,v120,s55,s55
v_pk_fma_f16 v122,v122,s55,s55
v_mfma_f32_16x16x32_bf16 v[144:147],v[72:75],v[48:51],v[144:147]
ds_read_u16 v136,v136
v_rcp_f16 v121,v120
v_mfma_f32_16x16x32_bf16 v[152:155],v[72:75],v[52:55],v[152:155]
ds_read_b64_tr_b16 v[98:99],v6 offset:20080
ds_read_b64_tr_b16 v[100:101],v6 offset:18552
v_rcp_f16 v123,v122
v_mfma_f32_16x16x32_bf16 v[156:159],v[76:79],v[52:55],v[156:159]
ds_read_u16_d16_hi v138,v138
v_rcp_f16_sdwa v121,v120 dst_sel:WORD_1 dst_unused:UNUSED_PRESERVE src0_sel:WORD_1
v_mfma_f32_16x16x32_bf16 v[148:151],v[76:79],v[48:51],v[148:151]
ds_read_b64_tr_b16 v[102:103],v6 offset:20088
ds_read_b64_tr_b16 v[104:105],v6 offset:18608
v_rcp_f16_sdwa v123,v122 dst_sel:WORD_1 dst_unused:UNUSED_PRESERVE src0_sel:WORD_1
v_mfma_f32_16x16x32_bf16 v[144:147],v[80:83],v[56:59],v[144:147]
ds_read_u16 v137,v137
v_fma_mix_f32 v140,v140,v121,0 op_sel_hi:[0,1,0]
v_fma_mix_f32 v142,v142,v121,0 op_sel:[0,1,0] op_sel_hi:[0,1,0]
v_mfma_f32_16x16x32_bf16 v[152:155],v[80:83],v[60:63],v[152:155]
s_waitcnt lgkmcnt(8)
ds_read_b64_tr_b16 v[106:107],v6 offset:20144
ds_read_b64_tr_b16 v[108:109],v6 offset:18616
v_lshl_add_u64 v[128:129],v[128:129],0,v[130:131]
v_lshl_add_u64 v[130:131],v[132:133],0,v[134:135]
v_mfma_f32_16x16x32_bf16 v[156:159],v[84:87],v[60:63],v[156:159]
ds_read_u16_d16_hi v139,v139
v_fma_mix_f32 v141,v141,v123,0 op_sel_hi:[0,1,0]
v_fma_mix_f32 v143,v143,v123,0 op_sel:[0,1,0] op_sel_hi:[0,1,0]
v_mfma_f32_16x16x32_bf16 v[148:151],v[84:87],v[56:59],v[148:151]
ds_read_b64_tr_b16 v[110:111],v6 offset:20152
ds_read_b64_tr_b16 v[64:65],v4 offset:12832
v_lshlrev_b32_sdwa v144,1,v144 dst_sel:DWORD dst_unused:UNUSED_PAD src0_sel:DWORD src1_sel:WORD_1
v_lshlrev_b32_sdwa v146,1,v146 dst_sel:DWORD dst_unused:UNUSED_PAD src0_sel:DWORD src1_sel:WORD_1
v_mfma_f32_16x16x32_bf16 v[160:163],v[88:91],v[128:131],v[160:163]
ds_read_u16 v144,v144
v_lshlrev_b32_sdwa v145,1,v145 dst_sel:DWORD dst_unused:UNUSED_PAD src0_sel:DWORD src1_sel:WORD_1
v_lshlrev_b32_sdwa v147,1,v147 dst_sel:DWORD dst_unused:UNUSED_PAD src0_sel:DWORD src1_sel:WORD_1
v_mfma_f32_16x16x32_bf16 v[168:171],v[92:95],v[128:131],v[168:171]
s_waitcnt lgkmcnt(11)
ds_read_b64_tr_b16 v[66:67],v4 offset:14880
ds_read_b128 v[112:115],v7 offset:640
v_fma_mixlo_f16 v120,v156,s99,0
v_fma_mixlo_f16 v122,v157,s99,0
v_mfma_f32_16x16x32_bf16 v[176:179],v[96:99],v[128:131],v[176:179]
s_waitcnt lgkmcnt(10)
ds_read_u16_d16_hi v146,v146
v_lshlrev_b32_sdwa v148,1,v148 dst_sel:DWORD dst_unused:UNUSED_PAD src0_sel:DWORD src1_sel:WORD_1
v_lshlrev_b32_sdwa v150,1,v150 dst_sel:DWORD dst_unused:UNUSED_PAD src0_sel:DWORD src1_sel:WORD_1
v_mfma_f32_16x16x32_bf16 v[184:187],v[100:103],v[128:131],v[184:187]
s_waitcnt lgkmcnt(6)
ds_read_b64_tr_b16 v[68:69],v5 offset:12832
ds_read_b64_tr_b16 v[70:71],v5 offset:14880
v_lshl_add_u64 v[136:137],v[136:137],0,v[138:139]
v_mfma_f32_16x16x32_bf16 v[192:195],v[104:107],v[128:131],v[192:195]
s_waitcnt lgkmcnt(7)
ds_read_u16 v145,v145
v_cvt_pk_bf16_f32 v138,v140,v142
v_cvt_pk_bf16_f32 v139,v141,v143
v_mfma_f32_16x16x32_bf16 v[200:203],v[108:111],v[128:131],v[200:203]
ds_read_b128 v[116:119],v7 offset:672
ds_read_b64_tr_b16 v[72:73],v4 offset:16928
v_fma_mixhi_f16 v120,v158,s99,0
v_fma_mixhi_f16 v122,v159,s99,0
v_mfma_f32_16x16x32_bf16 v[164:167],v[88:91],v[136:139],v[164:167]
ds_read_u16_d16_hi v147,v147
v_lshlrev_b32_sdwa v149,1,v149 dst_sel:DWORD dst_unused:UNUSED_PAD src0_sel:DWORD src1_sel:WORD_1
v_lshlrev_b32_sdwa v151,1,v151 dst_sel:DWORD dst_unused:UNUSED_PAD src0_sel:DWORD src1_sel:WORD_1
v_mfma_f32_16x16x32_bf16 v[172:175],v[92:95],v[136:139],v[172:175]
s_setprio 1
ds_read_b64_tr_b16 v[74:75],v4 offset:18976
ds_read_b64_tr_b16 v[76:77],v5 offset:16928
v_pk_fma_f16 v121,v120,v120,s54
v_pk_fma_f16 v123,v122,v122,s54
v_mfma_f32_16x16x32_bf16 v[180:183],v[96:99],v[136:139],v[180:183]
ds_read_u16 v148,v148
v_pk_mul_f16 v121,v120,v121
v_pk_mul_f16 v123,v122,v123
v_mfma_f32_16x16x32_bf16 v[188:191],v[100:103],v[136:139],v[188:191]
ds_read_b64_tr_b16 v[78:79],v5 offset:18976
v_lshlrev_b32_sdwa v152,1,v152 dst_sel:DWORD dst_unused:UNUSED_PAD src0_sel:DWORD src1_sel:WORD_1
v_lshlrev_b32_sdwa v154,1,v154 dst_sel:DWORD dst_unused:UNUSED_PAD src0_sel:DWORD src1_sel:WORD_1
v_mfma_f32_16x16x32_bf16 v[196:199],v[104:107],v[136:139],v[196:199]
ds_read_u16_d16_hi v150,v150
v_exp_f16 v120,v121
v_mfma_f32_16x16x32_bf16 v[204:207],v[108:111],v[136:139],v[204:207]
s_waitcnt lgkmcnt(12)
ds_read_b64_tr_b16 v[80:81],v4 offset:21024
v_exp_f16 v122,v123
v_mfma_f32_16x16x32_bf16 v[128:131],v[64:67],v[16:19],v[112:115]
ds_read_u16 v149,v149
v_lshlrev_b32_sdwa v153,1,v153 dst_sel:DWORD dst_unused:UNUSED_PAD src0_sel:DWORD src1_sel:WORD_1
v_lshlrev_b32_sdwa v155,1,v155 dst_sel:DWORD dst_unused:UNUSED_PAD src0_sel:DWORD src1_sel:WORD_1
v_mfma_f32_16x16x32_bf16 v[136:139],v[64:67],v[20:23],v[112:115]
s_waitcnt lgkmcnt(9)
ds_read_b64_tr_b16 v[82:83],v4 offset:23072
v_exp_f16_sdwa v120,v121 dst_sel:WORD_1 dst_unused:UNUSED_PRESERVE src0_sel:WORD_1
v_mfma_f32_16x16x32_bf16 v[140:143],v[68:71],v[20:23],v[116:119]
ds_read_u16_d16_hi v151,v151
v_exp_f16_sdwa v122,v123 dst_sel:WORD_1 dst_unused:UNUSED_PRESERVE src0_sel:WORD_1
v_mfma_f32_16x16x32_bf16 v[132:135],v[68:71],v[16:19],v[116:119]
s_waitcnt lgkmcnt(8)
ds_read_b64_tr_b16 v[84:85],v5 offset:21024
v_pk_fma_f16 v120,v120,s55,s55
v_pk_fma_f16 v122,v122,s55,s55
v_mfma_f32_16x16x32_bf16 v[128:131],v[72:75],v[24:27],v[128:131]
ds_read_u16 v152,v152
v_rcp_f16 v121,v120
v_mfma_f32_16x16x32_bf16 v[136:139],v[72:75],v[28:31],v[136:139]
s_waitcnt lgkmcnt(7)
ds_read_b64_tr_b16 v[86:87],v5 offset:23072
v_rcp_f16 v123,v122
v_mfma_f32_16x16x32_bf16 v[140:143],v[76:79],v[28:31],v[140:143]
ds_read_u16_d16_hi v154,v154
v_rcp_f16_sdwa v121,v120 dst_sel:WORD_1 dst_unused:UNUSED_PRESERVE src0_sel:WORD_1
v_mfma_f32_16x16x32_bf16 v[132:135],v[76:79],v[24:27],v[132:135]
s_waitcnt lgkmcnt(5)
v_rcp_f16_sdwa v123,v122 dst_sel:WORD_1 dst_unused:UNUSED_PRESERVE src0_sel:WORD_1
v_mfma_f32_16x16x32_bf16 v[128:131],v[80:83],v[32:35],v[128:131]
ds_read_u16 v153,v153
v_fma_mix_f32 v156,v156,v121,0 op_sel_hi:[0,1,0]
v_fma_mix_f32 v158,v158,v121,0 op_sel:[0,1,0] op_sel_hi:[0,1,0]
v_mfma_f32_16x16x32_bf16 v[136:139],v[80:83],v[36:39],v[136:139]
s_waitcnt lgkmcnt(2)
s_setprio 0
v_lshl_add_u64 v[144:145],v[144:145],0,v[146:147]
v_lshl_add_u64 v[146:147],v[148:149],0,v[150:151]
v_mfma_f32_16x16x32_bf16 v[140:143],v[84:87],v[36:39],v[140:143]
ds_read_u16_d16_hi v155,v155
v_fma_mix_f32 v157,v157,v123,0 op_sel_hi:[0,1,0]
v_fma_mix_f32 v159,v159,v123,0 op_sel:[0,1,0] op_sel_hi:[0,1,0]
v_mfma_f32_16x16x32_bf16 v[132:135],v[84:87],v[32:35],v[132:135]
v_lshlrev_b32_sdwa v128,1,v128 dst_sel:DWORD dst_unused:UNUSED_PAD src0_sel:DWORD src1_sel:WORD_1
s_mov_b32 s86,0xbe801d5a
v_lshlrev_b32_sdwa v130,1,v130 dst_sel:DWORD dst_unused:UNUSED_PAD src0_sel:DWORD src1_sel:WORD_1
v_mfma_f32_16x16x32_bf16 v[208:211],v[88:91],v[144:147],v[208:211]
s_waitcnt vmcnt(12)
s_barrier
s_setprio 3
ds_read_u16 v128,v128
v_lshlrev_b32_sdwa v129,1,v129 dst_sel:DWORD dst_unused:UNUSED_PAD src0_sel:DWORD src1_sel:WORD_1
v_lshlrev_b32_sdwa v131,1,v131 dst_sel:DWORD dst_unused:UNUSED_PAD src0_sel:DWORD src1_sel:WORD_1
v_mfma_f32_16x16x32_bf16 v[216:219],v[92:95],v[144:147],v[216:219]
v_fma_mixlo_f16 v120,v140,s99,0
v_fma_mixlo_f16 v122,v141,s99,0
v_mfma_f32_16x16x32_bf16 v[224:227],v[96:99],v[144:147],v[224:227]
s_cbranch_vccz RA92_%=
s_add_u32 m0,s58,12320
s_mul_i32 s69,s59,7
s_add_u32 s69,s69,s97
buffer_load_dwordx4 v2,s[44:47],s69 offen lds
s_add_u32 m0,m0,1024
s_add_u32 s69,s69,s53
buffer_load_dwordx4 v3,s[44:47],s69 offen lds
RA92_%=:
ds_read_u16_d16_hi v130,v130
v_lshlrev_b32_sdwa v132,1,v132 dst_sel:DWORD dst_unused:UNUSED_PAD src0_sel:DWORD src1_sel:WORD_1
v_lshlrev_b32_sdwa v134,1,v134 dst_sel:DWORD dst_unused:UNUSED_PAD src0_sel:DWORD src1_sel:WORD_1
v_mfma_f32_16x16x32_bf16 v[232:235],v[100:103],v[144:147],v[232:235]
s_waitcnt lgkmcnt(2)
v_lshl_add_u64 v[152:153],v[152:153],0,v[154:155]
v_mfma_f32_16x16x32_bf16 v[240:243],v[104:107],v[144:147],v[240:243]
ds_read_u16 v129,v129
v_cvt_pk_bf16_f32 v154,v156,v158
v_cvt_pk_bf16_f32 v155,v157,v159
v_mfma_f32_16x16x32_bf16 v[248:251],v[108:111],v[144:147],v[248:251]
v_fma_mixhi_f16 v120,v142,s99,0
v_fma_mixhi_f16 v122,v143,s99,0
v_mfma_f32_16x16x32_bf16 v[212:215],v[88:91],v[152:155],v[212:215]
s_cbranch_vccz RA93_%=
s_add_u32 m0,m0,1024
s_add_u32 s69,s69,s53
buffer_load_dwordx4 v2,s[44:47],s69 offen lds
s_add_u32 m0,m0,s50
s_add_u32 s69,s69,s53
buffer_load_dwordx4 v3,s[44:47],s69 offen lds
RA93_%=:
ds_read_u16_d16_hi v131,v131
v_lshlrev_b32_sdwa v133,1,v133 dst_sel:DWORD dst_unused:UNUSED_PAD src0_sel:DWORD src1_sel:WORD_1
v_lshlrev_b32_sdwa v135,1,v135 dst_sel:DWORD dst_unused:UNUSED_PAD src0_sel:DWORD src1_sel:WORD_1
v_mfma_f32_16x16x32_bf16 v[220:223],v[92:95],v[152:155],v[220:223]
ds_read_b64_tr_b16 v[88:89],v6 offset:0
v_pk_fma_f16 v121,v120,v120,s54
v_pk_fma_f16 v123,v122,v122,s54
v_mfma_f32_16x16x32_bf16 v[228:231],v[96:99],v[152:155],v[228:231]
ds_read_u16 v132,v132
v_pk_mul_f16 v121,v120,v121
v_pk_mul_f16 v123,v122,v123
v_mfma_f32_16x16x32_bf16 v[236:239],v[100:103],v[152:155],v[236:239]
s_cbranch_vccz RA94_%=
s_add_u32 m0,m0,1024
s_add_u32 s69,s69,s53
buffer_load_dwordx4 v2,s[44:47],s69 offen lds
s_add_u32 m0,m0,1024
s_add_u32 s69,s69,s53
buffer_load_dwordx4 v3,s[44:47],s69 offen lds
RA94_%=:
ds_read_b64_tr_b16 v[90:91],v6 offset:1536
v_lshlrev_b32_sdwa v136,1,v136 dst_sel:DWORD dst_unused:UNUSED_PAD src0_sel:DWORD src1_sel:WORD_1
v_lshlrev_b32_sdwa v138,1,v138 dst_sel:DWORD dst_unused:UNUSED_PAD src0_sel:DWORD src1_sel:WORD_1
v_mfma_f32_16x16x32_bf16 v[244:247],v[104:107],v[152:155],v[244:247]
ds_read_u16_d16_hi v134,v134
v_exp_f16 v120,v121
v_mfma_f32_16x16x32_bf16 v[252:255],v[108:111],v[152:155],v[252:255]
ds_read_b64_tr_b16 v[92:93],v6 offset:8
v_exp_f16 v122,v123
v_mfma_f32_16x16x32_bf16 v[144:147],v[64:67],v[40:43],v[112:115]
ds_read_u16 v133,v133
v_lshlrev_b32_sdwa v137,1,v137 dst_sel:DWORD dst_unused:UNUSED_PAD src0_sel:DWORD src1_sel:WORD_1
v_lshlrev_b32_sdwa v139,1,v139 dst_sel:DWORD dst_unused:UNUSED_PAD src0_sel:DWORD src1_sel:WORD_1
v_mfma_f32_16x16x32_bf16 v[152:155],v[64:67],v[44:47],v[112:115]
s_cbranch_vccz RA95_%=
s_add_u32 m0,m0,s50
s_add_u32 s69,s69,s53
buffer_load_dwordx4 v2,s[44:47],s69 offen lds
s_add_u32 m0,m0,1024
s_add_u32 s69,s69,s53
buffer_load_dwordx4 v3,s[44:47],s69 offen lds
RA95_%=:
ds_read_b64_tr_b16 v[94:95],v6 offset:1544
v_exp_f16_sdwa v120,v121 dst_sel:WORD_1 dst_unused:UNUSED_PRESERVE src0_sel:WORD_1
v_mfma_f32_16x16x32_bf16 v[156:159],v[68:71],v[44:47],v[116:119]
ds_read_u16_d16_hi v135,v135
v_exp_f16_sdwa v122,v123 dst_sel:WORD_1 dst_unused:UNUSED_PRESERVE src0_sel:WORD_1
v_mfma_f32_16x16x32_bf16 v[148:151],v[68:71],v[40:43],v[116:119]
ds_read_b64_tr_b16 v[96:97],v6 offset:64
v_pk_fma_f16 v120,v120,s55,s55
v_pk_fma_f16 v122,v122,s55,s55
v_mfma_f32_16x16x32_bf16 v[144:147],v[72:75],v[48:51],v[144:147]
ds_read_u16 v136,v136
v_rcp_f16 v121,v120
v_mfma_f32_16x16x32_bf16 v[152:155],v[72:75],v[52:55],v[152:155]
s_cbranch_vccz RA96_%=
s_add_u32 m0,m0,1024
s_add_u32 s69,s69,s53
buffer_load_dwordx4 v2,s[44:47],s69 offen lds
s_add_u32 m0,m0,s50
s_add_u32 s69,s69,s53
buffer_load_dwordx4 v3,s[44:47],s69 offen lds
RA96_%=:
ds_read_b64_tr_b16 v[98:99],v6 offset:1600
ds_read_b64_tr_b16 v[100:101],v6 offset:72
v_rcp_f16 v123,v122
v_mfma_f32_16x16x32_bf16 v[156:159],v[76:79],v[52:55],v[156:159]
ds_read_u16_d16_hi v138,v138
v_rcp_f16_sdwa v121,v120 dst_sel:WORD_1 dst_unused:UNUSED_PRESERVE src0_sel:WORD_1
v_mfma_f32_16x16x32_bf16 v[148:151],v[76:79],v[48:51],v[148:151]
ds_read_b64_tr_b16 v[102:103],v6 offset:1608
ds_read_b64_tr_b16 v[104:105],v6 offset:128
v_rcp_f16_sdwa v123,v122 dst_sel:WORD_1 dst_unused:UNUSED_PRESERVE src0_sel:WORD_1
v_mfma_f32_16x16x32_bf16 v[144:147],v[80:83],v[56:59],v[144:147]
ds_read_u16 v137,v137
v_fma_mix_f32 v140,v140,v121,0 op_sel_hi:[0,1,0]
v_fma_mix_f32 v142,v142,v121,0 op_sel:[0,1,0] op_sel_hi:[0,1,0]
v_mfma_f32_16x16x32_bf16 v[152:155],v[80:83],v[60:63],v[152:155]
s_waitcnt lgkmcnt(8)
s_cbranch_vccz RA97_%=
s_add_u32 m0,m0,1024
s_add_u32 s69,s69,s53
buffer_load_dwordx4 v2,s[44:47],s69 offen lds
s_add_u32 m0,m0,1024
s_add_u32 s69,s69,s53
buffer_load_dwordx4 v3,s[44:47],s69 offen lds
RA97_%=:
ds_read_b64_tr_b16 v[106:107],v6 offset:1664
ds_read_b64_tr_b16 v[108:109],v6 offset:136
v_lshl_add_u64 v[128:129],v[128:129],0,v[130:131]
v_lshl_add_u64 v[130:131],v[132:133],0,v[134:135]
v_mfma_f32_16x16x32_bf16 v[156:159],v[84:87],v[60:63],v[156:159]
ds_read_u16_d16_hi v139,v139
v_fma_mix_f32 v141,v141,v123,0 op_sel_hi:[0,1,0]
v_fma_mix_f32 v143,v143,v123,0 op_sel:[0,1,0] op_sel_hi:[0,1,0]
v_mfma_f32_16x16x32_bf16 v[148:151],v[84:87],v[56:59],v[148:151]
ds_read_b64_tr_b16 v[110:111],v6 offset:1672
ds_read_b64_tr_b16 v[64:65],v4 offset:0
v_lshlrev_b32_sdwa v144,1,v144 dst_sel:DWORD dst_unused:UNUSED_PAD src0_sel:DWORD src1_sel:WORD_1
v_lshlrev_b32_sdwa v146,1,v146 dst_sel:DWORD dst_unused:UNUSED_PAD src0_sel:DWORD src1_sel:WORD_1
v_mfma_f32_16x16x32_bf16 v[160:163],v[88:91],v[128:131],v[160:163]
s_cbranch_vccz RA98_%=
s_add_u32 m0,s100,24640
s_mul_i32 s69,s59,1
s_add_u32 s69,s69,s97
buffer_load_dwordx4 v2,s[44:47],s69 offen lds
s_add_u32 m0,m0,1024
s_add_u32 s69,s69,s53
buffer_load_dwordx4 v3,s[44:47],s69 offen lds
RA98_%=:
ds_read_u16 v144,v144
v_lshlrev_b32_sdwa v145,1,v145 dst_sel:DWORD dst_unused:UNUSED_PAD src0_sel:DWORD src1_sel:WORD_1
v_lshlrev_b32_sdwa v147,1,v147 dst_sel:DWORD dst_unused:UNUSED_PAD src0_sel:DWORD src1_sel:WORD_1
v_mfma_f32_16x16x32_bf16 v[168:171],v[92:95],v[128:131],v[168:171]
s_waitcnt lgkmcnt(11)
ds_read_b64_tr_b16 v[66:67],v4 offset:2048
ds_read_b128 v[112:115],v7 offset:768
v_fma_mixlo_f16 v120,v156,s99,0
v_fma_mixlo_f16 v122,v157,s99,0
v_mfma_f32_16x16x32_bf16 v[176:179],v[96:99],v[128:131],v[176:179]
s_waitcnt lgkmcnt(10)
ds_read_u16_d16_hi v146,v146
v_lshlrev_b32_sdwa v148,1,v148 dst_sel:DWORD dst_unused:UNUSED_PAD src0_sel:DWORD src1_sel:WORD_1
v_lshlrev_b32_sdwa v150,1,v150 dst_sel:DWORD dst_unused:UNUSED_PAD src0_sel:DWORD src1_sel:WORD_1
v_mfma_f32_16x16x32_bf16 v[184:187],v[100:103],v[128:131],v[184:187]
s_waitcnt lgkmcnt(6)
ds_read_b64_tr_b16 v[68:69],v5 offset:0
ds_read_b64_tr_b16 v[70:71],v5 offset:2048
v_lshl_add_u64 v[136:137],v[136:137],0,v[138:139]
v_mfma_f32_16x16x32_bf16 v[192:195],v[104:107],v[128:131],v[192:195]
s_waitcnt lgkmcnt(7)
s_cbranch_vccz RA99_%=
s_add_u32 m0,m0,1024
s_add_u32 s69,s69,s53
buffer_load_dwordx4 v2,s[44:47],s69 offen lds
s_add_u32 m0,m0,s50
s_add_u32 s69,s69,s53
buffer_load_dwordx4 v3,s[44:47],s69 offen lds
RA99_%=:
ds_read_u16 v145,v145
v_cvt_pk_bf16_f32 v138,v140,v142
v_cvt_pk_bf16_f32 v139,v141,v143
v_mfma_f32_16x16x32_bf16 v[200:203],v[108:111],v[128:131],v[200:203]
ds_read_b128 v[116:119],v7 offset:800
ds_read_b64_tr_b16 v[72:73],v4 offset:4096
v_fma_mixhi_f16 v120,v158,s99,0
v_fma_mixhi_f16 v122,v159,s99,0
v_mfma_f32_16x16x32_bf16 v[164:167],v[88:91],v[136:139],v[164:167]
ds_read_u16_d16_hi v147,v147
v_lshlrev_b32_sdwa v149,1,v149 dst_sel:DWORD dst_unused:UNUSED_PAD src0_sel:DWORD src1_sel:WORD_1
v_lshlrev_b32_sdwa v151,1,v151 dst_sel:DWORD dst_unused:UNUSED_PAD src0_sel:DWORD src1_sel:WORD_1
v_mfma_f32_16x16x32_bf16 v[172:175],v[92:95],v[136:139],v[172:175]
ds_read_b64_tr_b16 v[74:75],v4 offset:6144
ds_read_b64_tr_b16 v[76:77],v5 offset:4096
v_pk_fma_f16 v121,v120,v120,s54
v_pk_fma_f16 v123,v122,v122,s54
v_mfma_f32_16x16x32_bf16 v[180:183],v[96:99],v[136:139],v[180:183]
s_cbranch_vccz RA100_%=
s_add_u32 m0,m0,1024
s_add_u32 s69,s69,s53
buffer_load_dwordx4 v2,s[44:47],s69 offen lds
s_add_u32 m0,m0,1024
s_add_u32 s69,s69,s53
buffer_load_dwordx4 v3,s[44:47],s69 offen lds
RA100_%=:
ds_read_u16 v148,v148
v_pk_mul_f16 v121,v120,v121
v_pk_mul_f16 v123,v122,v123
v_mfma_f32_16x16x32_bf16 v[188:191],v[100:103],v[136:139],v[188:191]
ds_read_b64_tr_b16 v[78:79],v5 offset:6144
v_lshlrev_b32_sdwa v152,1,v152 dst_sel:DWORD dst_unused:UNUSED_PAD src0_sel:DWORD src1_sel:WORD_1
v_lshlrev_b32_sdwa v154,1,v154 dst_sel:DWORD dst_unused:UNUSED_PAD src0_sel:DWORD src1_sel:WORD_1
v_mfma_f32_16x16x32_bf16 v[196:199],v[104:107],v[136:139],v[196:199]
ds_read_u16_d16_hi v150,v150
v_exp_f16 v120,v121
v_mfma_f32_16x16x32_bf16 v[204:207],v[108:111],v[136:139],v[204:207]
s_waitcnt lgkmcnt(12)
ds_read_b64_tr_b16 v[80:81],v4 offset:8192
v_exp_f16 v122,v123
v_mfma_f32_16x16x32_bf16 v[128:131],v[64:67],v[16:19],v[112:115]
s_cbranch_vccz RA101_%=
s_add_u32 m0,m0,s50
s_add_u32 s69,s69,s53
buffer_load_dwordx4 v2,s[44:47],s69 offen lds
s_add_u32 m0,m0,1024
s_add_u32 s69,s69,s53
buffer_load_dwordx4 v3,s[44:47],s69 offen lds
RA101_%=:
ds_read_u16 v149,v149
v_lshlrev_b32_sdwa v153,1,v153 dst_sel:DWORD dst_unused:UNUSED_PAD src0_sel:DWORD src1_sel:WORD_1
v_lshlrev_b32_sdwa v155,1,v155 dst_sel:DWORD dst_unused:UNUSED_PAD src0_sel:DWORD src1_sel:WORD_1
v_mfma_f32_16x16x32_bf16 v[136:139],v[64:67],v[20:23],v[112:115]
s_waitcnt lgkmcnt(9)
ds_read_b64_tr_b16 v[82:83],v4 offset:10240
v_exp_f16_sdwa v120,v121 dst_sel:WORD_1 dst_unused:UNUSED_PRESERVE src0_sel:WORD_1
v_mfma_f32_16x16x32_bf16 v[140:143],v[68:71],v[20:23],v[116:119]
ds_read_u16_d16_hi v151,v151
v_exp_f16_sdwa v122,v123 dst_sel:WORD_1 dst_unused:UNUSED_PRESERVE src0_sel:WORD_1
v_mfma_f32_16x16x32_bf16 v[132:135],v[68:71],v[16:19],v[116:119]
s_waitcnt lgkmcnt(8)
ds_read_b64_tr_b16 v[84:85],v5 offset:8192
v_pk_fma_f16 v120,v120,s55,s55
v_pk_fma_f16 v122,v122,s55,s55
v_mfma_f32_16x16x32_bf16 v[128:131],v[72:75],v[24:27],v[128:131]
s_cbranch_vccz RA102_%=
s_add_u32 m0,m0,1024
s_add_u32 s69,s69,s53
buffer_load_dwordx4 v2,s[44:47],s69 offen lds
s_add_u32 m0,m0,s50
s_add_u32 s69,s69,s53
buffer_load_dwordx4 v3,s[44:47],s69 offen lds
RA102_%=:
ds_read_u16 v152,v152
v_rcp_f16 v121,v120
v_mfma_f32_16x16x32_bf16 v[136:139],v[72:75],v[28:31],v[136:139]
s_waitcnt lgkmcnt(7)
ds_read_b64_tr_b16 v[86:87],v5 offset:10240
v_rcp_f16 v123,v122
v_mfma_f32_16x16x32_bf16 v[140:143],v[76:79],v[28:31],v[140:143]
ds_read_u16_d16_hi v154,v154
v_rcp_f16_sdwa v121,v120 dst_sel:WORD_1 dst_unused:UNUSED_PRESERVE src0_sel:WORD_1
v_mfma_f32_16x16x32_bf16 v[132:135],v[76:79],v[24:27],v[132:135]
s_waitcnt lgkmcnt(5)
s_cbranch_vccz RA103_%=
s_add_u32 m0,m0,1024
s_add_u32 s69,s69,s53
buffer_load_dwordx4 v2,s[44:47],s69 offen lds
s_add_u32 m0,m0,1024
s_add_u32 s69,s69,s53
buffer_load_dwordx4 v3,s[44:47],s69 offen lds
RA103_%=:
v_rcp_f16_sdwa v123,v122 dst_sel:WORD_1 dst_unused:UNUSED_PRESERVE src0_sel:WORD_1
v_mfma_f32_16x16x32_bf16 v[128:131],v[80:83],v[32:35],v[128:131]
ds_read_u16 v153,v153
v_fma_mix_f32 v156,v156,v121,0 op_sel_hi:[0,1,0]
v_fma_mix_f32 v158,v158,v121,0 op_sel:[0,1,0] op_sel_hi:[0,1,0]
v_mfma_f32_16x16x32_bf16 v[136:139],v[80:83],v[36:39],v[136:139]
s_waitcnt lgkmcnt(2)
v_lshl_add_u64 v[144:145],v[144:145],0,v[146:147]
v_lshl_add_u64 v[146:147],v[148:149],0,v[150:151]
v_mfma_f32_16x16x32_bf16 v[140:143],v[84:87],v[36:39],v[140:143]
ds_read_u16_d16_hi v155,v155
v_fma_mix_f32 v157,v157,v123,0 op_sel_hi:[0,1,0]
v_fma_mix_f32 v159,v159,v123,0 op_sel:[0,1,0] op_sel_hi:[0,1,0]
v_mfma_f32_16x16x32_bf16 v[132:135],v[84:87],v[32:35],v[132:135]
v_lshlrev_b32_sdwa v128,1,v128 dst_sel:DWORD dst_unused:UNUSED_PAD src0_sel:DWORD src1_sel:WORD_1
v_lshlrev_b32_sdwa v130,1,v130 dst_sel:DWORD dst_unused:UNUSED_PAD src0_sel:DWORD src1_sel:WORD_1
v_mfma_f32_16x16x32_bf16 v[208:211],v[88:91],v[144:147],v[208:211]
ds_read_u16 v128,v128
v_lshlrev_b32_sdwa v129,1,v129 dst_sel:DWORD dst_unused:UNUSED_PAD src0_sel:DWORD src1_sel:WORD_1
v_lshlrev_b32_sdwa v131,1,v131 dst_sel:DWORD dst_unused:UNUSED_PAD src0_sel:DWORD src1_sel:WORD_1
v_mfma_f32_16x16x32_bf16 v[216:219],v[92:95],v[144:147],v[216:219]
v_fma_mixlo_f16 v120,v140,s99,0
v_fma_mixlo_f16 v122,v141,s99,0
v_mfma_f32_16x16x32_bf16 v[224:227],v[96:99],v[144:147],v[224:227]
s_setprio 2
ds_read_u16_d16_hi v130,v130
v_lshlrev_b32_sdwa v132,1,v132 dst_sel:DWORD dst_unused:UNUSED_PAD src0_sel:DWORD src1_sel:WORD_1
v_lshlrev_b32_sdwa v134,1,v134 dst_sel:DWORD dst_unused:UNUSED_PAD src0_sel:DWORD src1_sel:WORD_1
v_mfma_f32_16x16x32_bf16 v[232:235],v[100:103],v[144:147],v[232:235]
s_waitcnt lgkmcnt(2)
v_lshl_add_u64 v[152:153],v[152:153],0,v[154:155]
v_mfma_f32_16x16x32_bf16 v[240:243],v[104:107],v[144:147],v[240:243]
ds_read_u16 v129,v129
v_cvt_pk_bf16_f32 v154,v156,v158
v_cvt_pk_bf16_f32 v155,v157,v159
v_mfma_f32_16x16x32_bf16 v[248:251],v[108:111],v[144:147],v[248:251]
v_fma_mixhi_f16 v120,v142,s99,0
v_fma_mixhi_f16 v122,v143,s99,0
v_mfma_f32_16x16x32_bf16 v[212:215],v[88:91],v[152:155],v[212:215]
ds_read_u16_d16_hi v131,v131
v_lshlrev_b32_sdwa v133,1,v133 dst_sel:DWORD dst_unused:UNUSED_PAD src0_sel:DWORD src1_sel:WORD_1
v_lshlrev_b32_sdwa v135,1,v135 dst_sel:DWORD dst_unused:UNUSED_PAD src0_sel:DWORD src1_sel:WORD_1
v_mfma_f32_16x16x32_bf16 v[220:223],v[92:95],v[152:155],v[220:223]
ds_read_b64_tr_b16 v[88:89],v6 offset:6160
v_pk_fma_f16 v121,v120,v120,s54
v_pk_fma_f16 v123,v122,v122,s54
v_mfma_f32_16x16x32_bf16 v[228:231],v[96:99],v[152:155],v[228:231]
ds_read_u16 v132,v132
v_pk_mul_f16 v121,v120,v121
v_pk_mul_f16 v123,v122,v123
v_mfma_f32_16x16x32_bf16 v[236:239],v[100:103],v[152:155],v[236:239]
ds_read_b64_tr_b16 v[90:91],v6 offset:7696
v_lshlrev_b32_sdwa v136,1,v136 dst_sel:DWORD dst_unused:UNUSED_PAD src0_sel:DWORD src1_sel:WORD_1
v_lshlrev_b32_sdwa v138,1,v138 dst_sel:DWORD dst_unused:UNUSED_PAD src0_sel:DWORD src1_sel:WORD_1
v_mfma_f32_16x16x32_bf16 v[244:247],v[104:107],v[152:155],v[244:247]
ds_read_u16_d16_hi v134,v134
v_exp_f16 v120,v121
v_mfma_f32_16x16x32_bf16 v[252:255],v[108:111],v[152:155],v[252:255]
ds_read_b64_tr_b16 v[92:93],v6 offset:6168
v_exp_f16 v122,v123
v_mfma_f32_16x16x32_bf16 v[144:147],v[64:67],v[40:43],v[112:115]
ds_read_u16 v133,v133
v_lshlrev_b32_sdwa v137,1,v137 dst_sel:DWORD dst_unused:UNUSED_PAD src0_sel:DWORD src1_sel:WORD_1
v_lshlrev_b32_sdwa v139,1,v139 dst_sel:DWORD dst_unused:UNUSED_PAD src0_sel:DWORD src1_sel:WORD_1
v_mfma_f32_16x16x32_bf16 v[152:155],v[64:67],v[44:47],v[112:115]
ds_read_b64_tr_b16 v[94:95],v6 offset:7704
v_exp_f16_sdwa v120,v121 dst_sel:WORD_1 dst_unused:UNUSED_PRESERVE src0_sel:WORD_1
v_mfma_f32_16x16x32_bf16 v[156:159],v[68:71],v[44:47],v[116:119]
ds_read_u16_d16_hi v135,v135
v_exp_f16_sdwa v122,v123 dst_sel:WORD_1 dst_unused:UNUSED_PRESERVE src0_sel:WORD_1
v_mfma_f32_16x16x32_bf16 v[148:151],v[68:71],v[40:43],v[116:119]
ds_read_b64_tr_b16 v[96:97],v6 offset:6224
v_pk_fma_f16 v120,v120,s55,s55
v_pk_fma_f16 v122,v122,s55,s55
v_mfma_f32_16x16x32_bf16 v[144:147],v[72:75],v[48:51],v[144:147]
ds_read_u16 v136,v136
v_rcp_f16 v121,v120
v_mfma_f32_16x16x32_bf16 v[152:155],v[72:75],v[52:55],v[152:155]
ds_read_b64_tr_b16 v[98:99],v6 offset:7760
ds_read_b64_tr_b16 v[100:101],v6 offset:6232
v_rcp_f16 v123,v122
v_mfma_f32_16x16x32_bf16 v[156:159],v[76:79],v[52:55],v[156:159]
ds_read_u16_d16_hi v138,v138
v_rcp_f16_sdwa v121,v120 dst_sel:WORD_1 dst_unused:UNUSED_PRESERVE src0_sel:WORD_1
v_mfma_f32_16x16x32_bf16 v[148:151],v[76:79],v[48:51],v[148:151]
ds_read_b64_tr_b16 v[102:103],v6 offset:7768
ds_read_b64_tr_b16 v[104:105],v6 offset:6288
v_rcp_f16_sdwa v123,v122 dst_sel:WORD_1 dst_unused:UNUSED_PRESERVE src0_sel:WORD_1
v_mfma_f32_16x16x32_bf16 v[144:147],v[80:83],v[56:59],v[144:147]
ds_read_u16 v137,v137
v_fma_mix_f32 v140,v140,v121,0 op_sel_hi:[0,1,0]
v_fma_mix_f32 v142,v142,v121,0 op_sel:[0,1,0] op_sel_hi:[0,1,0]
v_mfma_f32_16x16x32_bf16 v[152:155],v[80:83],v[60:63],v[152:155]
s_waitcnt lgkmcnt(8)
ds_read_b64_tr_b16 v[106:107],v6 offset:7824
ds_read_b64_tr_b16 v[108:109],v6 offset:6296
v_lshl_add_u64 v[128:129],v[128:129],0,v[130:131]
v_lshl_add_u64 v[130:131],v[132:133],0,v[134:135]
v_mfma_f32_16x16x32_bf16 v[156:159],v[84:87],v[60:63],v[156:159]
ds_read_u16_d16_hi v139,v139
v_fma_mix_f32 v141,v141,v123,0 op_sel_hi:[0,1,0]
v_fma_mix_f32 v143,v143,v123,0 op_sel:[0,1,0] op_sel_hi:[0,1,0]
v_mfma_f32_16x16x32_bf16 v[148:151],v[84:87],v[56:59],v[148:151]
ds_read_b64_tr_b16 v[110:111],v6 offset:7832
ds_read_b128 v[64:67],v7 offset:1536
v_lshlrev_b32_sdwa v144,1,v144 dst_sel:DWORD dst_unused:UNUSED_PAD src0_sel:DWORD src1_sel:WORD_1
v_lshlrev_b32_sdwa v146,1,v146 dst_sel:DWORD dst_unused:UNUSED_PAD src0_sel:DWORD src1_sel:WORD_1
v_mfma_f32_16x16x32_bf16 v[160:163],v[88:91],v[128:131],v[160:163]
ds_read_u16 v144,v144
ds_read_b128 v[68:71],v7 offset:1568
v_lshlrev_b32_sdwa v145,1,v145 dst_sel:DWORD dst_unused:UNUSED_PAD src0_sel:DWORD src1_sel:WORD_1
v_lshlrev_b32_sdwa v147,1,v147 dst_sel:DWORD dst_unused:UNUSED_PAD src0_sel:DWORD src1_sel:WORD_1
v_fma_mixlo_f16 v120,v156,s99,0
v_fma_mixlo_f16 v122,v157,s99,0
v_mfma_f32_16x16x32_bf16 v[168:171],v[92:95],v[128:131],v[168:171]
s_waitcnt lgkmcnt(12)
ds_read_u16_d16_hi v146,v146
ds_read_b128 v[72:75],v7 offset:1664
v_lshlrev_b32_sdwa v148,1,v148 dst_sel:DWORD dst_unused:UNUSED_PAD src0_sel:DWORD src1_sel:WORD_1
v_lshlrev_b32_sdwa v150,1,v150 dst_sel:DWORD dst_unused:UNUSED_PAD src0_sel:DWORD src1_sel:WORD_1
s_waitcnt lgkmcnt(6)
v_lshl_add_u64 v[136:137],v[136:137],0,v[138:139]
v_cvt_pk_bf16_f32 v138,v140,v142
v_mfma_f32_16x16x32_bf16 v[176:179],v[96:99],v[128:131],v[176:179]
s_setprio 1
ds_read_u16 v145,v145
ds_read_b128 v[76:79],v7 offset:1696
v_cvt_pk_bf16_f32 v139,v141,v143
v_fma_mixhi_f16 v120,v158,s99,0
v_fma_mixhi_f16 v122,v159,s99,0
v_lshlrev_b32_sdwa v149,1,v149 dst_sel:DWORD dst_unused:UNUSED_PAD src0_sel:DWORD src1_sel:WORD_1
v_mfma_f32_16x16x32_bf16 v[184:187],v[100:103],v[128:131],v[184:187]
ds_read_u16_d16_hi v147,v147
ds_read_b128 v[80:83],v7 offset:1792
v_lshlrev_b32_sdwa v151,1,v151 dst_sel:DWORD dst_unused:UNUSED_PAD src0_sel:DWORD src1_sel:WORD_1
v_pk_fma_f16 v121,v120,v120,s54
v_pk_fma_f16 v123,v122,v122,s54
v_pk_mul_f16 v121,v120,v121
v_mfma_f32_16x16x32_bf16 v[192:195],v[104:107],v[128:131],v[192:195]
s_waitcnt lgkmcnt(9)
ds_read_u16 v148,v148
ds_read_b128 v[84:87],v7 offset:1824
v_pk_mul_f16 v123,v122,v123
v_lshlrev_b32_sdwa v152,1,v152 dst_sel:DWORD dst_unused:UNUSED_PAD src0_sel:DWORD src1_sel:WORD_1
v_lshlrev_b32_sdwa v154,1,v154 dst_sel:DWORD dst_unused:UNUSED_PAD src0_sel:DWORD src1_sel:WORD_1
v_exp_f16 v120,v121
v_mfma_f32_16x16x32_bf16 v[200:203],v[108:111],v[128:131],v[200:203]
ds_read_u16_d16_hi v150,v150
v_exp_f16 v122,v123
v_lshlrev_b32_sdwa v153,1,v153 dst_sel:DWORD dst_unused:UNUSED_PAD src0_sel:DWORD src1_sel:WORD_1
v_mfma_f32_16x16x32_bf16 v[164:167],v[88:91],v[136:139],v[164:167]
ds_read_u16 v149,v149
v_lshlrev_b32_sdwa v155,1,v155 dst_sel:DWORD dst_unused:UNUSED_PAD src0_sel:DWORD src1_sel:WORD_1
v_exp_f16_sdwa v120,v121 dst_sel:WORD_1 dst_unused:UNUSED_PRESERVE src0_sel:WORD_1
v_exp_f16_sdwa v122,v123 dst_sel:WORD_1 dst_unused:UNUSED_PRESERVE src0_sel:WORD_1
v_mfma_f32_16x16x32_bf16 v[172:175],v[92:95],v[136:139],v[172:175]
ds_read_u16_d16_hi v151,v151
v_pk_fma_f16 v120,v120,s55,s55
v_pk_fma_f16 v122,v122,s55,s55
v_rcp_f16 v121,v120
v_mfma_f32_16x16x32_bf16 v[180:183],v[96:99],v[136:139],v[180:183]
ds_read_u16 v152,v152
v_rcp_f16 v123,v122
v_rcp_f16_sdwa v121,v120 dst_sel:WORD_1 dst_unused:UNUSED_PRESERVE src0_sel:WORD_1
v_mfma_f32_16x16x32_bf16 v[188:191],v[100:103],v[136:139],v[188:191]
ds_read_u16_d16_hi v154,v154
v_rcp_f16_sdwa v123,v122 dst_sel:WORD_1 dst_unused:UNUSED_PRESERVE src0_sel:WORD_1
v_fma_mix_f32 v156,v156,v121,0 op_sel_hi:[0,1,0]
v_mfma_f32_16x16x32_bf16 v[196:199],v[104:107],v[136:139],v[196:199]
s_setprio 0
ds_read_u16 v153,v153
v_fma_mix_f32 v158,v158,v121,0 op_sel:[0,1,0] op_sel_hi:[0,1,0]
s_waitcnt lgkmcnt(9)
v_lshl_add_u64 v[144:145],v[144:145],0,v[146:147]
s_waitcnt lgkmcnt(3)
v_lshl_add_u64 v[146:147],v[148:149],0,v[150:151]
v_fma_mix_f32 v157,v157,v123,0 op_sel_hi:[0,1,0]
v_mfma_f32_16x16x32_bf16 v[204:207],v[108:111],v[136:139],v[204:207]
ds_read_u16_d16_hi v155,v155
v_fma_mix_f32 v159,v159,v123,0 op_sel:[0,1,0] op_sel_hi:[0,1,0]
s_waitcnt lgkmcnt(0)
v_lshl_add_u64 v[152:153],v[152:153],0,v[154:155]
v_cvt_pk_bf16_f32 v154,v156,v158
v_cvt_pk_bf16_f32 v155,v157,v159
v_mfma_f32_16x16x32_bf16 v[208:211],v[88:91],v[144:147],v[208:211]
s_waitcnt vmcnt(12)
s_barrier
s_setprio 0
v_xor_b32 v15,64,v10
v_mul_f32 v160,v160,v64
v_mul_f32 v161,v161,v65
v_mul_f32 v162,v162,v66
v_mul_f32 v163,v163,v67
v_mfma_f32_16x16x32_bf16 v[216:219],v[92:95],v[144:147],v[216:219]
v_mul_f32 v168,v168,v68
v_mul_f32 v169,v169,v69
v_mul_f32 v170,v170,v70
v_mul_f32 v171,v171,v71
v_cvt_pk_bf16_f32 v16,v160,v161
v_mfma_f32_16x16x32_bf16 v[224:227],v[96:99],v[144:147],v[224:227]
v_cvt_pk_bf16_f32 v17,v162,v163
v_cvt_pk_bf16_f32 v18,v168,v169
v_cvt_pk_bf16_f32 v19,v170,v171
v_mul_f32_dpp v176,v176,v72 quad_perm:[1,0,3,2] row_mask:0xf bank_mask:0xf
v_mfma_f32_16x16x32_bf16 v[232:235],v[100:103],v[144:147],v[232:235]
v_mul_f32_dpp v177,v177,v73 quad_perm:[1,0,3,2] row_mask:0xf bank_mask:0xf
v_mul_f32_dpp v178,v178,v74 quad_perm:[1,0,3,2] row_mask:0xf bank_mask:0xf
v_mul_f32_dpp v179,v179,v75 quad_perm:[1,0,3,2] row_mask:0xf bank_mask:0xf
v_mul_f32_dpp v184,v184,v76 quad_perm:[1,0,3,2] row_mask:0xf bank_mask:0xf
v_mul_f32_dpp v185,v185,v77 quad_perm:[1,0,3,2] row_mask:0xf bank_mask:0xf
v_mfma_f32_16x16x32_bf16 v[240:243],v[104:107],v[144:147],v[240:243]
v_mul_f32_dpp v186,v186,v78 quad_perm:[1,0,3,2] row_mask:0xf bank_mask:0xf
v_mul_f32_dpp v187,v187,v79 quad_perm:[1,0,3,2] row_mask:0xf bank_mask:0xf
v_cvt_pk_bf16_f32 v24,v176,v177
v_cvt_pk_bf16_f32 v25,v178,v179
v_mfma_f32_16x16x32_bf16 v[248:251],v[108:111],v[144:147],v[248:251]
v_cvt_pk_bf16_f32 v26,v184,v185
v_cvt_pk_bf16_f32 v27,v186,v187
s_mov_b64 exec,s[12:13]
v_swap_b32 v16,v24
v_swap_b32 v17,v25
v_swap_b32 v18,v26
v_swap_b32 v19,v27
s_mov_b64 exec,-1
v_mfma_f32_16x16x32_bf16 v[212:215],v[88:91],v[152:155],v[212:215]
s_add_u32 s65,s61,3145728
buffer_store_dwordx4 v[16:19],v10,s[40:43],s65 offen offset:0 sc1
v_mfma_f32_16x16x32_bf16 v[220:223],v[92:95],v[152:155],v[220:223]
v_mul_f32 v192,v192,v80
v_mul_f32 v193,v193,v81
v_mul_f32 v194,v194,v82
v_mul_f32 v195,v195,v83
v_mfma_f32_16x16x32_bf16 v[228:231],v[96:99],v[152:155],v[228:231]
v_mul_f32 v200,v200,v84
v_mul_f32 v201,v201,v85
v_mul_f32 v202,v202,v86
v_mul_f32 v203,v203,v87
v_mfma_f32_16x16x32_bf16 v[236:239],v[100:103],v[152:155],v[236:239]
v_cvt_pk_bf16_f32 v32,v192,v193
v_cvt_pk_bf16_f32 v33,v194,v195
v_cvt_pk_bf16_f32 v34,v200,v201
v_cvt_pk_bf16_f32 v35,v202,v203
v_mul_f32_dpp v164,v164,v64 quad_perm:[1,0,3,2] row_mask:0xf bank_mask:0xf
v_mfma_f32_16x16x32_bf16 v[244:247],v[104:107],v[152:155],v[244:247]
v_mul_f32_dpp v165,v165,v65 quad_perm:[1,0,3,2] row_mask:0xf bank_mask:0xf
v_mul_f32_dpp v166,v166,v66 quad_perm:[1,0,3,2] row_mask:0xf bank_mask:0xf
v_mul_f32_dpp v167,v167,v67 quad_perm:[1,0,3,2] row_mask:0xf bank_mask:0xf
v_mul_f32_dpp v172,v172,v68 quad_perm:[1,0,3,2] row_mask:0xf bank_mask:0xf
v_mfma_f32_16x16x32_bf16 v[252:255],v[108:111],v[152:155],v[252:255]
v_mul_f32_dpp v173,v173,v69 quad_perm:[1,0,3,2] row_mask:0xf bank_mask:0xf
v_mul_f32_dpp v174,v174,v70 quad_perm:[1,0,3,2] row_mask:0xf bank_mask:0xf
v_mul_f32_dpp v175,v175,v71 quad_perm:[1,0,3,2] row_mask:0xf bank_mask:0xf
v_cvt_pk_bf16_f32 v20,v164,v165
v_cvt_pk_bf16_f32 v21,v166,v167
v_cvt_pk_bf16_f32 v22,v172,v173
v_cvt_pk_bf16_f32 v23,v174,v175
s_mov_b64 exec,s[12:13]
v_swap_b32 v32,v20
v_swap_b32 v33,v21
v_swap_b32 v34,v22
v_swap_b32 v35,v23
s_mov_b64 exec,-1
s_add_u32 s65,s61,3145728
buffer_store_dwordx4 v[24:27],v15,s[40:43],s65 offen offset:384 sc1
v_mul_f32 v180,v180,v72
v_mul_f32 v181,v181,v73
v_mul_f32 v182,v182,v74
v_mul_f32 v183,v183,v75
v_mul_f32 v188,v188,v76
v_mul_f32 v189,v189,v77
v_mul_f32 v190,v190,v78
v_mul_f32 v191,v191,v79
v_cvt_pk_bf16_f32 v28,v180,v181
v_cvt_pk_bf16_f32 v29,v182,v183
v_cvt_pk_bf16_f32 v30,v188,v189
v_cvt_pk_bf16_f32 v31,v190,v191
v_mul_f32_dpp v196,v196,v80 quad_perm:[1,0,3,2] row_mask:0xf bank_mask:0xf
v_mul_f32_dpp v197,v197,v81 quad_perm:[1,0,3,2] row_mask:0xf bank_mask:0xf
v_mul_f32_dpp v198,v198,v82 quad_perm:[1,0,3,2] row_mask:0xf bank_mask:0xf
v_mul_f32_dpp v199,v199,v83 quad_perm:[1,0,3,2] row_mask:0xf bank_mask:0xf
v_mul_f32_dpp v204,v204,v84 quad_perm:[1,0,3,2] row_mask:0xf bank_mask:0xf
v_mul_f32_dpp v205,v205,v85 quad_perm:[1,0,3,2] row_mask:0xf bank_mask:0xf
v_mul_f32_dpp v206,v206,v86 quad_perm:[1,0,3,2] row_mask:0xf bank_mask:0xf
v_mul_f32_dpp v207,v207,v87 quad_perm:[1,0,3,2] row_mask:0xf bank_mask:0xf
v_cvt_pk_bf16_f32 v36,v196,v197
v_cvt_pk_bf16_f32 v37,v198,v199
v_cvt_pk_bf16_f32 v38,v204,v205
v_cvt_pk_bf16_f32 v39,v206,v207
s_mov_b64 exec,s[12:13]
v_swap_b32 v28,v36
v_swap_b32 v29,v37
v_swap_b32 v30,v38
v_swap_b32 v31,v39
s_mov_b64 exec,-1
s_add_u32 s65,s61,3145728
buffer_store_dwordx4 v[32:35],v10,s[40:43],s65 offen offset:128 sc1
v_mul_f32 v208,v208,v64
v_mul_f32 v209,v209,v65
v_mul_f32 v210,v210,v66
v_mul_f32 v211,v211,v67
v_mul_f32 v216,v216,v68
v_mul_f32 v217,v217,v69
v_mul_f32 v218,v218,v70
v_mul_f32 v219,v219,v71
v_cvt_pk_bf16_f32 v40,v208,v209
v_cvt_pk_bf16_f32 v41,v210,v211
v_cvt_pk_bf16_f32 v42,v216,v217
v_cvt_pk_bf16_f32 v43,v218,v219
v_mul_f32_dpp v224,v224,v72 quad_perm:[1,0,3,2] row_mask:0xf bank_mask:0xf
v_mul_f32_dpp v225,v225,v73 quad_perm:[1,0,3,2] row_mask:0xf bank_mask:0xf
v_mul_f32_dpp v226,v226,v74 quad_perm:[1,0,3,2] row_mask:0xf bank_mask:0xf
v_mul_f32_dpp v227,v227,v75 quad_perm:[1,0,3,2] row_mask:0xf bank_mask:0xf
v_mul_f32_dpp v232,v232,v76 quad_perm:[1,0,3,2] row_mask:0xf bank_mask:0xf
v_mul_f32_dpp v233,v233,v77 quad_perm:[1,0,3,2] row_mask:0xf bank_mask:0xf
v_mul_f32_dpp v234,v234,v78 quad_perm:[1,0,3,2] row_mask:0xf bank_mask:0xf
v_mul_f32_dpp v235,v235,v79 quad_perm:[1,0,3,2] row_mask:0xf bank_mask:0xf
v_cvt_pk_bf16_f32 v48,v224,v225
v_cvt_pk_bf16_f32 v49,v226,v227
v_cvt_pk_bf16_f32 v50,v232,v233
s_mov_b32 s86,0xbe801d5a
v_cvt_pk_bf16_f32 v51,v234,v235
s_mov_b64 exec,s[12:13]
v_swap_b32 v40,v48
v_swap_b32 v41,v49
v_swap_b32 v42,v50
v_swap_b32 v43,v51
s_mov_b64 exec,-1
v_mul_f32 v240,v240,v80
v_mul_f32 v241,v241,v81
v_mul_f32 v242,v242,v82
v_mul_f32 v243,v243,v83
v_mul_f32 v248,v248,v84
s_add_u32 s65,s61,3145728
buffer_store_dwordx4 v[20:23],v15,s[40:43],s65 offen offset:512 sc1
v_mul_f32 v249,v249,v85
v_mul_f32 v250,v250,v86
v_mul_f32 v251,v251,v87
v_cvt_pk_bf16_f32 v56,v240,v241
v_cvt_pk_bf16_f32 v57,v242,v243
v_cvt_pk_bf16_f32 v58,v248,v249
v_cvt_pk_bf16_f32 v59,v250,v251
v_mul_f32_dpp v212,v212,v64 quad_perm:[1,0,3,2] row_mask:0xf bank_mask:0xf
v_mul_f32_dpp v213,v213,v65 quad_perm:[1,0,3,2] row_mask:0xf bank_mask:0xf
v_mul_f32_dpp v214,v214,v66 quad_perm:[1,0,3,2] row_mask:0xf bank_mask:0xf
v_mul_f32_dpp v215,v215,v67 quad_perm:[1,0,3,2] row_mask:0xf bank_mask:0xf
v_mul_f32_dpp v220,v220,v68 quad_perm:[1,0,3,2] row_mask:0xf bank_mask:0xf
v_mul_f32_dpp v221,v221,v69 quad_perm:[1,0,3,2] row_mask:0xf bank_mask:0xf
v_mul_f32_dpp v222,v222,v70 quad_perm:[1,0,3,2] row_mask:0xf bank_mask:0xf
v_mul_f32_dpp v223,v223,v71 quad_perm:[1,0,3,2] row_mask:0xf bank_mask:0xf
v_cvt_pk_bf16_f32 v44,v212,v213
v_cvt_pk_bf16_f32 v45,v214,v215
v_cvt_pk_bf16_f32 v46,v220,v221
v_cvt_pk_bf16_f32 v47,v222,v223
s_mov_b64 exec,s[12:13]
v_swap_b32 v56,v44
v_swap_b32 v57,v45
v_swap_b32 v58,v46
v_swap_b32 v59,v47
s_mov_b64 exec,-1
v_mul_f32 v228,v228,v72
v_mul_f32 v229,v229,v73
v_mul_f32 v230,v230,v74
v_mul_f32 v231,v231,v75
v_mul_f32 v236,v236,v76
v_mul_f32 v237,v237,v77
v_mul_f32 v238,v238,v78
v_mul_f32 v239,v239,v79
v_cvt_pk_bf16_f32 v52,v228,v229
v_cvt_pk_bf16_f32 v53,v230,v231
s_add_u32 s65,s61,3145728
buffer_store_dwordx4 v[28:31],v10,s[40:43],s65 offen offset:256 sc1
v_cvt_pk_bf16_f32 v54,v236,v237
v_cvt_pk_bf16_f32 v55,v238,v239
v_mul_f32_dpp v244,v244,v80 quad_perm:[1,0,3,2] row_mask:0xf bank_mask:0xf
v_mul_f32_dpp v245,v245,v81 quad_perm:[1,0,3,2] row_mask:0xf bank_mask:0xf
v_mul_f32_dpp v246,v246,v82 quad_perm:[1,0,3,2] row_mask:0xf bank_mask:0xf
v_mul_f32_dpp v247,v247,v83 quad_perm:[1,0,3,2] row_mask:0xf bank_mask:0xf
v_mul_f32_dpp v252,v252,v84 quad_perm:[1,0,3,2] row_mask:0xf bank_mask:0xf
v_mul_f32_dpp v253,v253,v85 quad_perm:[1,0,3,2] row_mask:0xf bank_mask:0xf
v_mul_f32_dpp v254,v254,v86 quad_perm:[1,0,3,2] row_mask:0xf bank_mask:0xf
v_mul_f32_dpp v255,v255,v87 quad_perm:[1,0,3,2] row_mask:0xf bank_mask:0xf
v_cvt_pk_bf16_f32 v60,v244,v245
v_cvt_pk_bf16_f32 v61,v246,v247
v_cvt_pk_bf16_f32 v62,v252,v253
v_cvt_pk_bf16_f32 v63,v254,v255
s_mov_b64 exec,s[12:13]
v_swap_b32 v52,v60
v_swap_b32 v53,v61
v_swap_b32 v54,v62
v_swap_b32 v55,v63
s_mov_b64 exec,-1
s_waitcnt vmcnt(5)
s_barrier
s_add_u32 s65,s61,3145728
buffer_store_dwordx4 v[36:39],v15,s[40:43],s65 offen offset:640 sc1
s_sleep 2
s_add_u32 s65,s61,3244032
buffer_store_dwordx4 v[40:43],v10,s[40:43],s65 offen offset:0 sc1
s_sleep 2
s_add_u32 s65,s61,3244032
buffer_store_dwordx4 v[48:51],v15,s[40:43],s65 offen offset:384 sc1
s_sleep 2
s_add_u32 s65,s61,3244032
buffer_store_dwordx4 v[56:59],v10,s[40:43],s65 offen offset:128 sc1
s_sleep 2
s_add_u32 s65,s61,3244032
buffer_store_dwordx4 v[44:47],v15,s[40:43],s65 offen offset:512 sc1
s_sleep 2
s_add_u32 s65,s61,3244032
buffer_store_dwordx4 v[52:55],v10,s[40:43],s65 offen offset:256 sc1
s_sleep 2
s_add_u32 s65,s61,3244032
buffer_store_dwordx4 v[60:63],v15,s[40:43],s65 offen offset:640 sc1
v_and_b32 v232,63,%1
s_getpc_b64 s[88:89]
N_itA104_%=:
s_lshl_b32 s87,s10,10
s_add_u32 s87,s87,86024
s_sub_u32 s88,s88,N_itA104_%=-N_main_%=
s_subb_u32 s89,s89,0
s_add_u32 s88,s88,s87
s_addc_u32 s89,s89,0
s_swappc_b64 s[90:91],s[88:89]
s_cmp_lg_u32 s52,1
s_cbranch_scc1 N_s0_%=
v_min_u32 v236,47,v232
v_lshlrev_b32 v152,4,v236
v_lshlrev_b32 v153,2,v236
v_min_u32 v236,59,v232
v_mul_u32_u24 v237,0xaaab,v236
v_lshrrev_b32 v237,19,v237
v_mul_u32_u24 v237,14592,v237
v_lshl_add_u32 v148,v236,4,v237
s_add_u32 s64,s24,0
s_addc_u32 s65,s25,0
global_load_dwordx4 v[112:115],v152,s[64:65]
s_add_u32 s64,s24,9216
s_addc_u32 s65,s25,0
global_load_dwordx4 v[116:119],v152,s[64:65]
s_add_u32 s64,s24,18432
s_addc_u32 s65,s25,0
global_load_dwordx4 v[120:123],v152,s[64:65]
s_add_u32 s64,s24,27648
s_addc_u32 s65,s25,0
global_load_dwordx4 v[124:127],v152,s[64:65]
s_add_u32 s64,s24,36864
s_addc_u32 s65,s25,0
global_load_dwordx4 v[128:131],v152,s[64:65]
s_add_u32 s64,s24,46080
s_addc_u32 s65,s25,0
global_load_dwordx4 v[132:135],v152,s[64:65]
s_add_u32 s64,s24,55296
s_addc_u32 s65,s25,0
global_load_dwordx4 v[136:139],v152,s[64:65]
s_add_u32 s64,s24,64512
s_addc_u32 s65,s25,0
global_load_dwordx4 v[140:143],v152,s[64:65]
global_load_dwordx4 v[144:147],v148,s[28:29]
global_load_dwordx4 v[148:151],v148,s[28:29] offset:3648
global_load_dwordx4 v[156:159],v152,s[26:27]
global_load_dword v152,v153,s[32:33]
global_load_dword v153,v153,s[30:31]
N_s0_%=:
s_and_b32 s64,s52,3
s_mul_i32 s64,s64,18432
s_add_u32 s64,s64,4096
v_lshl_add_u32 v255,v232,4,s64
global_load_dwordx4 v[160:163],v255,s[24:25] offset:-4096
global_load_dwordx4 v[164:167],v255,s[24:25] offset:-3072
global_load_dwordx4 v[168:171],v255,s[24:25] offset:-2048
global_load_dwordx4 v[172:175],v255,s[24:25] offset:-1024
global_load_dwordx4 v[176:179],v255,s[24:25] offset:0
global_load_dwordx4 v[180:183],v255,s[24:25] offset:1024
global_load_dwordx4 v[184:187],v255,s[24:25] offset:2048
global_load_dwordx4 v[188:191],v255,s[24:25] offset:3072
v_add_u32 v255,8192,v255
global_load_dwordx4 v[192:195],v255,s[24:25] offset:-4096
global_load_dwordx4 v[196:199],v255,s[24:25] offset:-3072
global_load_dwordx4 v[200:203],v255,s[24:25] offset:-2048
global_load_dwordx4 v[204:207],v255,s[24:25] offset:-1024
global_load_dwordx4 v[208:211],v255,s[24:25] offset:0
global_load_dwordx4 v[212:215],v255,s[24:25] offset:1024
global_load_dwordx4 v[216:219],v255,s[24:25] offset:2048
global_load_dwordx4 v[220:223],v255,s[24:25] offset:3072
v_add_u32 v255,8192,v255
global_load_dwordx4 v[224:227],v255,s[24:25] offset:-4096
global_load_dwordx4 v[228:231],v255,s[24:25] offset:-3072
s_waitcnt vmcnt(0)
v_or3_b32 v160,v160,v164,v165
v_or3_b32 v162,v162,v166,v167
v_or3_b32 v160,v160,v168,v169
v_or3_b32 v162,v162,v170,v171
v_or3_b32 v160,v160,v172,v173
v_or3_b32 v162,v162,v174,v175
v_or3_b32 v160,v160,v176,v177
v_or3_b32 v162,v162,v178,v179
v_or3_b32 v160,v160,v180,v181
v_or3_b32 v162,v162,v182,v183
v_or3_b32 v160,v160,v184,v185
v_or3_b32 v162,v162,v186,v187
v_or3_b32 v160,v160,v188,v189
v_or3_b32 v162,v162,v190,v191
v_or3_b32 v160,v160,v192,v193
v_or3_b32 v162,v162,v194,v195
v_or3_b32 v160,v160,v196,v197
v_or3_b32 v162,v162,v198,v199
v_or3_b32 v160,v160,v200,v201
v_or3_b32 v162,v162,v202,v203
v_or3_b32 v160,v160,v204,v205
v_or3_b32 v162,v162,v206,v207
v_or3_b32 v160,v160,v208,v209
v_or3_b32 v162,v162,v210,v211
v_or3_b32 v160,v160,v212,v213
v_or3_b32 v162,v162,v214,v215
v_or3_b32 v160,v160,v216,v217
v_or3_b32 v162,v162,v218,v219
v_or3_b32 v160,v160,v220,v221
v_or3_b32 v162,v162,v222,v223
v_or3_b32 v160,v160,v224,v225
v_or3_b32 v162,v162,v226,v227
v_or3_b32 v160,v160,v228,v229
v_or3_b32 v162,v162,v230,v231
v_or3_b32 v160,v160,v161,v162
v_or_b32 v160,v160,v163
s_mov_b32 s64,0x7bff7bff
v_and_b32 v160,0x40004000,v160
v_pk_mul_f16 v160,v160,s64
v_or_b32 v1,v1,v160
s_cmp_lg_u32 s52,1
s_cbranch_scc1 N_s1_%=
s_mov_b32 s65,0x7fff7fff
v_and_b32 v112,s65,v112
v_and_b32 v113,s65,v113
v_and_b32 v114,s65,v114
v_and_b32 v115,s65,v115
v_and_b32 v116,s65,v116
v_and_b32 v117,s65,v117
v_and_b32 v118,s65,v118
v_and_b32 v119,s65,v119
v_and_b32 v120,s65,v120
v_and_b32 v121,s65,v121
v_and_b32 v122,s65,v122
v_and_b32 v123,s65,v123
v_and_b32 v124,s65,v124
v_and_b32 v125,s65,v125
v_and_b32 v126,s65,v126
v_and_b32 v127,s65,v127
v_and_b32 v128,s65,v128
v_and_b32 v129,s65,v129
v_and_b32 v130,s65,v130
v_and_b32 v131,s65,v131
v_and_b32 v132,s65,v132
v_and_b32 v133,s65,v133
v_and_b32 v134,s65,v134
v_and_b32 v135,s65,v135
v_and_b32 v136,s65,v136
v_and_b32 v137,s65,v137
v_and_b32 v138,s65,v138
v_and_b32 v139,s65,v139
v_and_b32 v140,s65,v140
v_and_b32 v141,s65,v141
v_and_b32 v142,s65,v142
v_and_b32 v143,s65,v143
v_pk_max_u16 v100,v112,v116
v_pk_max_u16 v101,v113,v117
v_pk_max_u16 v102,v114,v118
v_pk_max_u16 v103,v115,v119
v_pk_max_u16 v100,v100,v120
v_pk_max_u16 v101,v101,v121
v_pk_max_u16 v102,v102,v122
v_pk_max_u16 v103,v103,v123
v_pk_max_u16 v100,v100,v124
v_pk_max_u16 v101,v101,v125
v_pk_max_u16 v102,v102,v126
v_pk_max_u16 v103,v103,v127
v_pk_max_u16 v100,v100,v128
v_pk_max_u16 v101,v101,v129
v_pk_max_u16 v102,v102,v130
v_pk_max_u16 v103,v103,v131
v_pk_max_u16 v100,v100,v132
v_pk_max_u16 v101,v101,v133
v_pk_max_u16 v102,v102,v134
v_pk_max_u16 v103,v103,v135
v_pk_max_u16 v100,v100,v136
v_pk_max_u16 v101,v101,v137
v_pk_max_u16 v102,v102,v138
v_pk_max_u16 v103,v103,v139
v_pk_max_u16 v100,v100,v140
v_pk_max_u16 v101,v101,v141
v_pk_max_u16 v102,v102,v142
v_pk_max_u16 v103,v103,v143
v_pk_min_u16 v100,v100,v101
v_pk_min_u16 v102,v102,v103
v_pk_min_u16 v100,v100,v102
v_lshrrev_b32 v101,16,v100
v_and_b32 v100,0xffff,v100
v_min_u32 v100,v100,v101
v_mov_b32 v101,0x3a00
v_cmp_lt_u32_e64 s[92:93],v100,v101
s_nop 0
s_or_b32 s64,s92,s93
v_and_b32 v152,s65,v152
v_lshrrev_b32 v101,16,v152
v_and_b32 v152,0xffff,v152
v_max_u32 v152,v152,v101
v_mov_b32 v101,0x4580
v_cmp_le_u32_e64 s[92:93],v101,v152
s_nop 0
s_or_b32 s64,s64,s92
s_or_b32 s64,s64,s93
v_and_b32 v153,s65,v153
v_and_b32 v144,s65,v144
v_pk_max_u16 v153,v153,v144
v_and_b32 v145,s65,v145
v_pk_max_u16 v153,v153,v145
v_and_b32 v146,s65,v146
v_pk_max_u16 v153,v153,v146
v_and_b32 v147,s65,v147
v_pk_max_u16 v153,v153,v147
v_and_b32 v148,s65,v148
v_pk_max_u16 v153,v153,v148
v_and_b32 v149,s65,v149
v_pk_max_u16 v153,v153,v149
v_and_b32 v150,s65,v150
v_pk_max_u16 v153,v153,v150
v_and_b32 v151,s65,v151
v_pk_max_u16 v153,v153,v151
v_lshrrev_b32 v101,16,v153
v_and_b32 v153,0xffff,v153
v_max_u32 v153,v153,v101
v_mov_b32 v101,0x5380
v_cmp_le_u32_e64 s[92:93],v101,v153
s_nop 0
s_or_b32 s64,s64,s92
s_or_b32 s64,s64,s93
v_and_b32 v156,s65,v156
v_and_b32 v157,s65,v157
v_and_b32 v158,s65,v158
v_and_b32 v159,s65,v159
v_pk_max_u16 v156,v156,v157
v_pk_max_u16 v156,v156,v158
v_pk_max_u16 v156,v156,v159
v_lshrrev_b32 v101,16,v156
v_and_b32 v156,0xffff,v156
v_max_u32 v156,v156,v101
v_mov_b32 v101,0x4280
v_cmp_le_u32_e64 s[92:93],v101,v156
s_nop 0
s_or_b32 s64,s64,s92
s_or_b32 s64,s64,s93
s_cmp_eq_u32 s64,0
s_cbranch_scc1 N_s1_%=
v_mov_b32 v1,0x7c007c00
N_s1_%=:
v_and_b32 v1,0x7c007c00,v1
v_lshrrev_b32 v2,16,v1
v_and_b32 v1,0xffff,v1
v_max_u32 v1,v1,v2
v_cmp_eq_u32 vcc,0x7c00,v1
s_or_b32 s64,vcc_lo,vcc_hi
v_mov_b32 v1,s64
v_mov_b32 v2,8192
s_mov_b64 exec,1
ds_or_b32 v2,v1 offset:3456
s_mov_b64 exec,-1
s_waitcnt lgkmcnt(0)
s_barrier
s_setprio 3
ds_read_b32 v1,v2 offset:3456
s_waitcnt lgkmcnt(0)
s_setprio 0
v_readfirstlane_b32 s64,v1
s_cmp_lg_u32 s64,0
s_cbranch_scc1 N_fallback_%=
s_endpgm
s_nop 4
N_B_%=:
v_mov_b32 v192,0x409d3ef8
v_and_b32 v248,255,%1
v_lshlrev_b32 v198,4,v248
v_lshlrev_b32 v248,19,v248
v_cmp_eq_u32_e64 s[94:95],0,v248
v_add_u32 v199,0x8000,v198
s_mov_b32 s4,0x3f000000
s_mov_b32 s5,0x3fc00000
s_mov_b32 s6,0x40200000
s_mov_b32 s7,0x40600000
s_mov_b32 s8,0x40900000
s_mov_b32 s9,0x40b00000
s_mov_b32 s10,0x40d00000
s_mov_b32 s11,0x40f00000
s_mov_b32 s12,0x3f80222e
s_mov_b32 s13,0x13089abf
s_mov_b32 s96,0xebefe000
s_mov_b32 s15,0
N_tl_%=:
v_add_u32 v237,0x10000,v248
v_mul_f32 v194,s99,v248
v_fma_f32 v196,v237,s99,-v194
v_pk_fma_f32 v[160:161],v[196:197],s[4:5],v[194:195] op_sel_hi:[0,1,0]
v_pk_fma_f32 v[162:163],v[196:197],s[6:7],v[194:195] op_sel_hi:[0,1,0]
v_pk_fma_f32 v[164:165],v[196:197],s[8:9],v[194:195] op_sel_hi:[0,1,0]
v_pk_fma_f32 v[166:167],v[196:197],s[10:11],v[194:195] op_sel_hi:[0,1,0]
v_cndmask_b32_e64 v160,v160,0,s[94:95]
v_pk_mul_f32 v[168:169],v[160:161],v[160:161]
v_pk_mul_f32 v[170:171],v[162:163],v[162:163]
v_pk_mul_f32 v[172:173],v[164:165],v[164:165]
v_pk_mul_f32 v[174:175],v[166:167],v[166:167]
v_pk_fma_f32 v[168:169],v[168:169],s[12:13],v[192:193] op_sel_hi:[1,0,0]
v_pk_fma_f32 v[170:171],v[170:171],s[12:13],v[192:193] op_sel_hi:[1,0,0]
v_pk_fma_f32 v[172:173],v[172:173],s[12:13],v[192:193] op_sel_hi:[1,0,0]
v_pk_fma_f32 v[174:175],v[174:175],s[12:13],v[192:193] op_sel_hi:[1,0,0]
v_pk_mul_f32 v[168:169],v[168:169],v[160:161]
v_pk_mul_f32 v[170:171],v[170:171],v[162:163]
v_pk_mul_f32 v[172:173],v[172:173],v[164:165]
v_pk_mul_f32 v[174:175],v[174:175],v[166:167]
v_exp_f32 v168,v168
v_exp_f32 v169,v169
v_exp_f32 v170,v170
v_exp_f32 v171,v171
v_exp_f32 v172,v172
v_exp_f32 v173,v173
v_exp_f32 v174,v174
v_exp_f32 v175,v175
v_pk_fma_f32 v[168:169],v[168:169],s[96:97],s[96:97] op_sel_hi:[1,0,0]
v_pk_fma_f32 v[170:171],v[170:171],s[96:97],s[96:97] op_sel_hi:[1,0,0]
v_pk_fma_f32 v[172:173],v[172:173],s[96:97],s[96:97] op_sel_hi:[1,0,0]
v_pk_fma_f32 v[174:175],v[174:175],s[96:97],s[96:97] op_sel_hi:[1,0,0]
v_rcp_f32 v168,v168
v_rcp_f32 v169,v169
v_rcp_f32 v170,v170
v_rcp_f32 v171,v171
v_rcp_f32 v172,v172
v_rcp_f32 v173,v173
v_rcp_f32 v174,v174
v_rcp_f32 v175,v175
v_pk_mul_f32 v[168:169],v[168:169],v[160:161]
v_pk_mul_f32 v[170:171],v[170:171],v[162:163]
v_pk_mul_f32 v[172:173],v[172:173],v[164:165]
v_pk_mul_f32 v[174:175],v[174:175],v[166:167]
v_pk_fma_f32 v[160:161],v[160:161],s[12:13],v[168:169] op_sel:[0,1,0] op_sel_hi:[1,1,1]
v_pk_fma_f32 v[162:163],v[162:163],s[12:13],v[170:171] op_sel:[0,1,0] op_sel_hi:[1,1,1]
v_pk_fma_f32 v[164:165],v[164:165],s[12:13],v[172:173] op_sel:[0,1,0] op_sel_hi:[1,1,1]
v_pk_fma_f32 v[166:167],v[166:167],s[12:13],v[174:175] op_sel:[0,1,0] op_sel_hi:[1,1,1]
v_cvt_pk_bf16_f32 v176,v168,v169
v_cvt_pk_bf16_f32 v177,v170,v171
v_cvt_pk_bf16_f32 v178,v172,v173
v_cvt_pk_bf16_f32 v179,v174,v175
v_cvt_pk_bf16_f32 v180,v160,v161
v_cvt_pk_bf16_f32 v181,v162,v163
v_cvt_pk_bf16_f32 v182,v164,v165
v_cvt_pk_bf16_f32 v183,v166,v167
ds_write_b128 v198,v[176:179] offset:0
ds_write_b128 v199,v[180:183] offset:32768
s_mov_b64 s[94:95],0
v_add_u32 v248,0x8000000,v248
v_add_u32 v198,4096,v198
v_add_u32 v199,4096,v199
s_add_u32 s15,s15,1
s_cmp_lt_u32 s15,2
s_cbranch_scc1 N_tl_%=
s_and_b32 s64,s52,3
s_lshl_b32 s10,s64,2
s_mov_b32 s8,0x0ff00ff0
s_mov_b32 s9,0x0ff00ff0
s_and_b32 s64,s52,3
s_mov_b32 s12,0xaaaaaaaa
s_mov_b32 s13,0xaaaaaaaa
s_and_b32 s64,s52,3
s_lshl_b32 s10,s64,2
s_add_u32 s64,s10,s71
v_and_b32 v233,15,v232
v_lshrrev_b32 v234,4,v232
s_mul_i32 s61,s64,98304
s_add_u32 s61,s61,s68
v_and_b32 v236,1,v234
v_lshlrev_b32 v236,1,v236
v_lshrrev_b32 v237,5,v232
v_or_b32 v235,v236,v237
s_mov_b32 s54,0x44ea44ea
s_mov_b32 s55,0x877f877f
s_mov_b32 s8,0x0ff00ff0
s_mov_b32 s9,0x0ff00ff0
v_mov_b32 v1,0
v_mov_b32 v239,0x70000
v_bfe_u32 v236,v233,1,1
v_sub_u32 v236,v235,v236
v_cmp_gt_u32_e64 s[92:93],3,v236
v_lshrrev_b32 v237,2,v233
v_lshlrev_b32 v237,1,v237
v_and_b32 v238,1,v233
v_or_b32 v237,v237,v238
v_lshl_add_u32 v236,v236,3,v237
v_lshlrev_b32 v236,4,v236
v_add_u32 v236,143488,v236
v_cndmask_b32_e64 v11,v239,v236,s[92:93]
v_and_b32 v13,3,v237
v_lshlrev_b32 v236,1,v233
v_and_b32 v236,4,v236
v_lshl_add_u32 v236,v234,3,v236
v_mov_b32 v238,0x54250310
v_mul_u32_u24 v13,0x5555,v13
v_bfe_u32 v236,v238,v236,3
v_lshl_add_u32 v236,v236,3,v237
v_lshlrev_b32 v236,4,v236
v_add_u32 v12,143488,v236
v_lshrrev_b32 v237,2,v233
v_cmp_eq_u32_e64 s[92:93],v234,v237
v_lshlrev_b32 v236,4,v233
v_add_u32 v236,10112,v236
v_cndmask_b32_e64 v14,v239,v236,s[92:93]
v_and_b32 v239,7,v233
v_lshl_add_u32 v236,v239,1,v235
v_add_u32 v237,1,v236
v_bfe_u32 v237,v237,2,2
v_mul_u32_u24 v236,192,v236
v_lshl_add_u32 v236,v237,4,v236
v_add_u32 v237,1,v239
v_bfe_u32 v237,v237,1,2
v_xor_b32 v237,v237,v234
v_mul_u32_u24 v239,384,v239
v_lshl_add_u32 v237,v237,4,v239
v_sub_u32 v237,v237,v236
v_add_u32 v8,192,v237
v_add_u32 v15,73728,v236
v_and_b32 v238,1,v234
v_lshrrev_b32 v237,1,v234
v_lshlrev_b32 v238,6,v238
v_lshl_add_u32 v238,v237,4,v238
v_add_u32 v7,8192,v238
s_and_b32 s64,s52,3
s_mul_i32 s67,s64,54
s_cmp_eq_u32 s70,31
s_cselect_b32 s66,17,99
v_add_u32 v236,s67,v232
v_add_u32 v236,108,v236
v_mul_u32_u24 v237,0xaaab,v236
v_lshrrev_b32 v237,19,v237
v_mul_u32_u24 v238,12,v237
v_sub_u32 v238,v236,v238
v_add_u32 v236,1,v237
v_bfe_u32 v236,v236,2,2
v_xor_b32 v238,v238,v236
v_mul_u32_u24 v236,192,v237
v_lshl_add_u32 v239,v238,4,v236
v_cmp_eq_u32_e64 s[92:93],s66,v237
v_mov_b32 v236,0x7fff0000
s_nop 0
v_cndmask_b32_e64 v239,v239,v236,s[92:93]
v_cndmask_b32_e64 v3,v239,v3,vcc
v_and_b32 v239,7,v233
v_mul_u32_u24 v239,384,v239
v_and_b32 v238,1,v233
v_mul_u32_u24 v238,320,v238
v_sub_u32 v239,v239,v238
s_mov_b32 s12,0xaaaaaaaa
s_mov_b32 s13,0xaaaaaaaa
v_lshl_add_u32 v10,v234,4,v239
s_mov_b64 exec,s[8:9]
v_add_u32 v10,196608,v10
s_mov_b64 exec,-1
v_lshrrev_b32 v236,3,v233
v_bfe_u32 v237,v233,2,1
v_lshl_add_u32 v237,v236,4,v237
v_lshl_add_u32 v237,v234,1,v237
v_lshlrev_b32 v237,6,v237
v_lshl_add_u32 v237,v236,4,v237
v_and_b32 v238,1,v233
v_lshl_add_u32 v237,v238,5,v237
v_bfe_u32 v238,v233,1,1
v_lshl_add_u32 v237,v238,3,v237
v_xor_b32 v238,16,v237
v_add_u32 v4,16256,v237
v_add_u32 v5,16256,v238
v_lshrrev_b32 v236,3,v233
v_bfe_u32 v237,v233,2,1
v_lshl_add_u32 v236,v237,1,v236
v_lshrrev_b32 v237,1,v234
v_lshl_add_u32 v236,v237,2,v236
v_mul_u32_u24 v236,192,v236
v_and_b32 v238,1,v234
v_mul_u32_u24 v239,3080,v238
v_add_u32 v236,v236,v239
v_and_b32 v239,3,v233
v_lshl_add_u32 v236,v239,4,v236
v_add_u32 v6,40896,v236
s_mov_b32 s42,50331648
s_mov_b32 s43,0x00020000
v_bfe_u32 v236,v232,2,3
v_mul_u32_u24 v236,768,v236
v_and_b32 v237,3,v232
v_lshrrev_b32 v238,5,v232
v_lshl_add_u32 v237,v238,2,v237
v_lshl_add_u32 v236,v237,4,v236
v_lshlrev_b32 v237,4,v232
s_bitcmp0_b32 s52,0
s_mov_b32 s58,40896
s_cselect_b32 s58,16256,s58
s_cselect_b64 s[92:93],-1,0
s_cselect_b32 s59,64,6144
s_movk_i32 s53,1024
s_cselect_b32 s53,6144,s53
s_cselect_b32 s60,0,67584
s_cselect_b32 s97,64,0
s_cselect_b32 s69,0,0x70000000
s_mov_b32 s100,73728
s_cselect_b32 s100,110688,s100
s_cselect_b32 s64,16,0
s_movk_i32 s50,1032
s_cselect_b32 s50,1024,s50
s_cselect_b32 s51,s53,-72704
v_cndmask_b32_e64 v236,v237,v236,s[92:93]
v_cndmask_b32_e64 v2,v241,v236,vcc
v_xor_b32 v236,s64,v236
v_cndmask_b32_e64 v3,v3,v236,vcc
s_mov_b32 s46,73728
s_mov_b32 s47,0x00020000
v_lshlrev_b32 v236,4,v232
v_and_b32 v237,255,%1
v_add_u32 v236,157312,v236
v_lshlrev_b32 v237,3,v237
v_add_u32 v237,157312,v237
ds_write_b128 v236,v[4:7] offset:0
ds_write_b128 v236,v[8:11] offset:1024
ds_write_b128 v236,v[12:15] offset:2048
ds_write_b64 v237,v[2:3] offset:3072
s_waitcnt lgkmcnt(0)
s_sub_u32 s36,s20,192
s_subb_u32 s37,s21,0
s_and_b32 s37,s37,0xffff
s_bitcmp0_b32 s52,0
s_cselect_b64 s[44:45],s[24:25],s[28:29]
s_and_b32 s45,s45,0xffff
s_mov_b32 s40,s34
s_and_b32 s41,s35,0xffff
s_cbranch_vccz RB1_%=
s_add_u32 m0,s58,12320
s_nop 0
buffer_load_dwordx4 v2,s[44:47],s69 offen lds
s_add_u32 m0,m0,1024
s_add_u32 s69,s69,s53
buffer_load_dwordx4 v3,s[44:47],s69 offen lds
s_add_u32 m0,m0,1024
s_add_u32 s69,s69,s53
buffer_load_dwordx4 v2,s[44:47],s69 offen lds
s_add_u32 m0,m0,s50
s_add_u32 s69,s69,s53
buffer_load_dwordx4 v3,s[44:47],s69 offen lds
s_add_u32 m0,m0,1024
s_add_u32 s69,s69,s53
buffer_load_dwordx4 v2,s[44:47],s69 offen lds
s_add_u32 m0,m0,1024
s_add_u32 s69,s69,s53
buffer_load_dwordx4 v3,s[44:47],s69 offen lds
s_add_u32 m0,m0,s50
s_add_u32 s69,s69,s51
buffer_load_dwordx4 v2,s[44:47],s69 offen lds
s_add_u32 m0,m0,1024
s_add_u32 s69,s69,s53
buffer_load_dwordx4 v3,s[44:47],s69 offen lds
s_add_u32 m0,m0,1024
s_add_u32 s69,s69,s53
buffer_load_dwordx4 v2,s[44:47],s69 offen lds
s_add_u32 m0,m0,s50
s_add_u32 s69,s69,s53
buffer_load_dwordx4 v3,s[44:47],s69 offen lds
s_add_u32 m0,m0,1024
s_add_u32 s69,s69,s53
buffer_load_dwordx4 v2,s[44:47],s69 offen lds
s_add_u32 m0,m0,1024
s_add_u32 s69,s69,s53
s_mov_b32 s86,0xbe801d5a
buffer_load_dwordx4 v3,s[44:47],s69 offen lds
RB1_%=:
s_mov_b64 exec,s[62:63]
s_add_u32 m0,s57,34880
s_add_u32 s101,s56,983040
buffer_load_dwordx4 v241,s[36:39],s101 offen lds
s_add_u32 m0,s57,38368
s_add_u32 s101,s56,1081344
buffer_load_dwordx4 v241,s[36:39],s101 offen lds
s_add_u32 m0,s57,41856
s_add_u32 s101,s56,1179648
buffer_load_dwordx4 v241,s[36:39],s101 offen lds
s_add_u32 m0,s57,45344
s_add_u32 s101,s56,1277952
buffer_load_dwordx4 v241,s[36:39],s101 offen lds
s_add_u32 m0,s57,48832
s_add_u32 s101,s56,1376256
buffer_load_dwordx4 v241,s[36:39],s101 offen lds
s_add_u32 m0,s57,52320
s_add_u32 s101,s56,1474560
buffer_load_dwordx4 v241,s[36:39],s101 offen lds
s_add_u32 m0,s57,55808
s_add_u32 s101,s56,1572864
buffer_load_dwordx4 v241,s[36:39],s101 offen lds
s_add_u32 m0,s57,59296
s_add_u32 s101,s56,1671168
buffer_load_dwordx4 v241,s[36:39],s101 offen lds
s_mov_b64 exec,-1
s_waitcnt lgkmcnt(0)
s_barrier
s_setprio 0
s_cbranch_vccz RB2_%=
s_add_u32 m0,s58,0
s_mul_i32 s69,s59,1
s_add_u32 s69,s69,s97
buffer_load_dwordx4 v2,s[44:47],s69 offen lds
s_add_u32 m0,m0,1024
s_add_u32 s69,s69,s53
buffer_load_dwordx4 v3,s[44:47],s69 offen lds
s_add_u32 m0,m0,1024
s_add_u32 s69,s69,s53
buffer_load_dwordx4 v2,s[44:47],s69 offen lds
s_add_u32 m0,m0,s50
s_add_u32 s69,s69,s53
buffer_load_dwordx4 v3,s[44:47],s69 offen lds
s_add_u32 m0,m0,1024
s_add_u32 s69,s69,s53
buffer_load_dwordx4 v2,s[44:47],s69 offen lds
s_add_u32 m0,m0,1024
s_add_u32 s69,s69,s53
buffer_load_dwordx4 v3,s[44:47],s69 offen lds
s_add_u32 m0,m0,s50
s_add_u32 s69,s69,s53
buffer_load_dwordx4 v2,s[44:47],s69 offen lds
s_add_u32 m0,m0,1024
s_add_u32 s69,s69,s53
buffer_load_dwordx4 v3,s[44:47],s69 offen lds
s_add_u32 m0,m0,1024
s_add_u32 s69,s69,s53
buffer_load_dwordx4 v2,s[44:47],s69 offen lds
s_add_u32 m0,m0,s50
s_add_u32 s69,s69,s53
buffer_load_dwordx4 v3,s[44:47],s69 offen lds
s_add_u32 m0,m0,1024
s_add_u32 s69,s69,s53
buffer_load_dwordx4 v2,s[44:47],s69 offen lds
s_add_u32 m0,m0,1024
s_add_u32 s69,s69,s53
buffer_load_dwordx4 v3,s[44:47],s69 offen lds
RB2_%=:
s_waitcnt lgkmcnt(0)
s_cbranch_vccnz RB3_%=
s_waitcnt vmcnt(0)
RB3_%=:
s_cbranch_vccz RB4_%=
s_waitcnt vmcnt(12)
RB4_%=:
s_barrier
v_lshlrev_b32 v253,7,v232
s_bfe_u32 s64,%0,0x20003
s_and_b32 s65,s52,3
s_lshl_b32 s65,s65,2
s_add_u32 s64,s64,s65
s_lshl_b32 s65,s64,13
v_add_u32 v252,s65,v253
s_getpc_b64 s[14:15]
N_pf_%=:
s_sub_u32 s14,s14,N_pf_%=-N_main_%=
s_subb_u32 s15,s15,0
v_min_u32 v252,N_mainend_%=-N_main_%=,v252
global_load_dword v254,v252,s[14:15]
s_getpc_b64 s[88:89]
N_itB5_%=:
s_lshl_b32 s87,s10,10
s_add_u32 s87,s87,4104
s_sub_u32 s88,s88,N_itB5_%=-N_main_%=
s_subb_u32 s89,s89,0
s_add_u32 s88,s88,s87
s_addc_u32 s89,s89,0
s_swappc_b64 s[90:91],s[88:89]
s_cbranch_vccnz RB6_%=
s_mov_b64 exec,s[62:63]
s_add_u32 m0,s57,62784
s_add_u32 s64,s56,1769472
buffer_load_dwordx4 v2,s[36:39],s64 offen lds
s_add_u32 m0,s57,64512
s_nop 0
buffer_load_dwordx4 v3,s[36:39],s64 offen lds
s_mov_b64 exec,-1
RB6_%=:
s_cbranch_vccnz RB7_%=
s_mov_b64 exec,s[62:63]
s_add_u32 m0,s57,0
s_add_u32 s64,s56,1966080
buffer_load_dwordx4 v2,s[36:39],s64 offen lds
s_add_u32 m0,s57,1728
s_nop 0
buffer_load_dwordx4 v3,s[36:39],s64 offen lds
s_mov_b64 exec,-1
RB7_%=:
s_cbranch_vccnz RB8_%=
s_mov_b64 exec,s[62:63]
s_add_u32 m0,s57,3488
s_add_u32 s64,s56,2064384
buffer_load_dwordx4 v2,s[36:39],s64 offen lds
s_add_u32 m0,s57,5216
s_nop 0
buffer_load_dwordx4 v3,s[36:39],s64 offen lds
s_mov_b64 exec,-1
RB8_%=:
s_cbranch_vccnz RB9_%=
s_mov_b64 exec,s[62:63]
s_add_u32 m0,s57,6976
s_add_u32 s64,s56,2162688
buffer_load_dwordx4 v2,s[36:39],s64 offen lds
s_add_u32 m0,s57,8704
s_nop 0
buffer_load_dwordx4 v3,s[36:39],s64 offen lds
s_mov_b64 exec,-1
RB9_%=:
s_cbranch_vccnz RB10_%=
s_mov_b64 exec,s[62:63]
s_add_u32 m0,s57,10464
s_add_u32 s64,s56,2260992
buffer_load_dwordx4 v2,s[36:39],s64 offen lds
s_add_u32 m0,s57,12192
s_nop 0
buffer_load_dwordx4 v3,s[36:39],s64 offen lds
s_mov_b64 exec,-1
RB10_%=:
s_cbranch_vccnz RB11_%=
s_mov_b64 exec,s[62:63]
s_add_u32 m0,s57,66272
s_add_u32 s64,s56,1867776
buffer_load_dwordx4 v2,s[36:39],s64 offen lds
s_add_u32 m0,s57,68000
s_nop 0
buffer_load_dwordx4 v3,s[36:39],s64 offen lds
s_mov_b64 exec,-1
RB11_%=:
s_barrier
s_cbranch_vccnz RB12_%=
s_mov_b64 exec,s[62:63]
s_add_u32 m0,s57,13952
s_add_u32 s64,s56,2359296
buffer_load_dwordx4 v2,s[36:39],s64 offen lds
s_add_u32 m0,s57,15680
s_nop 0
buffer_load_dwordx4 v3,s[36:39],s64 offen lds
s_mov_b64 exec,-1
RB12_%=:
s_cbranch_vccz RB13_%=
s_add_u32 m0,s58,12320
s_mul_i32 s69,s59,3
s_add_u32 s69,s69,s97
buffer_load_dwordx4 v2,s[44:47],s69 offen lds
s_add_u32 m0,m0,1024
s_add_u32 s69,s69,s53
buffer_load_dwordx4 v3,s[44:47],s69 offen lds
RB13_%=:
s_cbranch_vccz RB14_%=
s_add_u32 m0,m0,1024
s_add_u32 s69,s69,s53
buffer_load_dwordx4 v2,s[44:47],s69 offen lds
s_add_u32 m0,m0,s50
s_add_u32 s69,s69,s53
buffer_load_dwordx4 v3,s[44:47],s69 offen lds
RB14_%=:
s_cbranch_vccz RB15_%=
s_add_u32 m0,m0,1024
s_add_u32 s69,s69,s53
buffer_load_dwordx4 v2,s[44:47],s69 offen lds
s_add_u32 m0,m0,1024
s_add_u32 s69,s69,s53
buffer_load_dwordx4 v3,s[44:47],s69 offen lds
RB15_%=:
s_cbranch_vccz RB16_%=
s_add_u32 m0,m0,s50
s_add_u32 s69,s69,s53
buffer_load_dwordx4 v2,s[44:47],s69 offen lds
s_add_u32 m0,m0,1024
s_add_u32 s69,s69,s53
buffer_load_dwordx4 v3,s[44:47],s69 offen lds
RB16_%=:
s_cbranch_vccz RB17_%=
s_add_u32 m0,m0,1024
s_add_u32 s69,s69,s53
buffer_load_dwordx4 v2,s[44:47],s69 offen lds
s_add_u32 m0,m0,s50
s_add_u32 s69,s69,s53
buffer_load_dwordx4 v3,s[44:47],s69 offen lds
RB17_%=:
s_cbranch_vccz RB18_%=
s_add_u32 m0,m0,1024
s_add_u32 s69,s69,s53
buffer_load_dwordx4 v2,s[44:47],s69 offen lds
s_add_u32 m0,m0,1024
s_add_u32 s69,s69,s53
buffer_load_dwordx4 v3,s[44:47],s69 offen lds
RB18_%=:
s_cbranch_vccnz RB19_%=
s_mov_b64 exec,s[62:63]
s_add_u32 m0,s57,17440
s_add_u32 s64,s56,2457600
buffer_load_dwordx4 v2,s[36:39],s64 offen lds
s_add_u32 m0,s57,19168
s_nop 0
buffer_load_dwordx4 v3,s[36:39],s64 offen lds
s_mov_b64 exec,-1
RB19_%=:
s_cbranch_vccz RB20_%=
s_waitcnt vmcnt(13)
RB20_%=:
s_barrier
s_sleep 4
s_cbranch_vccnz RB21_%=
s_mov_b64 exec,s[62:63]
s_add_u32 m0,s57,20928
s_add_u32 s64,s56,2555904
buffer_load_dwordx4 v2,s[36:39],s64 offen lds
s_add_u32 m0,s57,22656
s_nop 0
buffer_load_dwordx4 v3,s[36:39],s64 offen lds
s_mov_b64 exec,-1
RB21_%=:
s_sleep 5
s_cbranch_vccnz RB22_%=
s_mov_b64 exec,s[62:63]
s_add_u32 m0,s57,24416
s_add_u32 s64,s56,2654208
buffer_load_dwordx4 v2,s[36:39],s64 offen lds
s_add_u32 m0,s57,26144
s_nop 0
buffer_load_dwordx4 v3,s[36:39],s64 offen lds
s_mov_b64 exec,-1
RB22_%=:
s_sleep 5
s_cbranch_vccnz RB23_%=
s_mov_b64 exec,s[62:63]
s_add_u32 m0,s57,27904
s_add_u32 s64,s56,2752512
buffer_load_dwordx4 v2,s[36:39],s64 offen lds
s_add_u32 m0,s57,29632
s_nop 0
buffer_load_dwordx4 v3,s[36:39],s64 offen lds
s_mov_b64 exec,-1
RB23_%=:
s_sleep 5
s_cbranch_vccnz RB24_%=
s_mov_b64 exec,s[62:63]
s_add_u32 m0,s57,31392
s_add_u32 s64,s56,2850816
buffer_load_dwordx4 v2,s[36:39],s64 offen lds
s_add_u32 m0,s57,33120
s_nop 0
buffer_load_dwordx4 v3,s[36:39],s64 offen lds
s_mov_b64 exec,-1
RB24_%=:
s_sleep 5
s_cbranch_vccnz RB25_%=
s_mov_b64 exec,s[62:63]
s_add_u32 m0,s57,34880
s_add_u32 s64,s56,2949120
buffer_load_dwordx4 v2,s[36:39],s64 offen lds
s_add_u32 m0,s57,36608
s_nop 0
buffer_load_dwordx4 v3,s[36:39],s64 offen lds
s_mov_b64 exec,-1
RB25_%=:
s_cbranch_vccz RB26_%=
s_waitcnt vmcnt(0)
RB26_%=:
s_barrier
s_cbranch_vccz RB27_%=
s_add_u32 m0,s58,0
s_mul_i32 s69,s59,5
s_add_u32 s69,s69,s97
buffer_load_dwordx4 v2,s[44:47],s69 offen lds
s_add_u32 m0,m0,1024
s_add_u32 s69,s69,s53
buffer_load_dwordx4 v3,s[44:47],s69 offen lds
RB27_%=:
s_cbranch_vccnz RB28_%=
s_mov_b64 exec,s[62:63]
s_add_u32 m0,s57,38368
s_add_u32 s64,s56,3047424
buffer_load_dwordx4 v2,s[36:39],s64 offen lds
s_add_u32 m0,s57,40096
s_nop 0
buffer_load_dwordx4 v3,s[36:39],s64 offen lds
s_mov_b64 exec,-1
RB28_%=:
s_cbranch_vccz RB29_%=
s_add_u32 m0,m0,1024
s_add_u32 s69,s69,s53
buffer_load_dwordx4 v2,s[44:47],s69 offen lds
s_add_u32 m0,m0,s50
s_add_u32 s69,s69,s53
buffer_load_dwordx4 v3,s[44:47],s69 offen lds
RB29_%=:
s_cbranch_vccz RB30_%=
s_add_u32 m0,m0,1024
s_add_u32 s69,s69,s53
buffer_load_dwordx4 v2,s[44:47],s69 offen lds
s_add_u32 m0,m0,1024
s_add_u32 s69,s69,s53
buffer_load_dwordx4 v3,s[44:47],s69 offen lds
RB30_%=:
s_cbranch_vccz RB31_%=
s_add_u32 m0,m0,s50
s_add_u32 s69,s69,s53
buffer_load_dwordx4 v2,s[44:47],s69 offen lds
s_add_u32 m0,m0,1024
s_add_u32 s69,s69,s53
buffer_load_dwordx4 v3,s[44:47],s69 offen lds
RB31_%=:
s_cbranch_vccz RB32_%=
s_add_u32 m0,m0,1024
s_add_u32 s69,s69,s53
buffer_load_dwordx4 v2,s[44:47],s69 offen lds
s_add_u32 m0,m0,s50
s_add_u32 s69,s69,s53
buffer_load_dwordx4 v3,s[44:47],s69 offen lds
RB32_%=:
s_cbranch_vccz RB33_%=
s_add_u32 m0,m0,1024
s_add_u32 s69,s69,s53
buffer_load_dwordx4 v2,s[44:47],s69 offen lds
s_add_u32 m0,m0,1024
s_add_u32 s69,s69,s53
buffer_load_dwordx4 v3,s[44:47],s69 offen lds
RB33_%=:
s_cbranch_vccnz RB34_%=
s_mov_b64 exec,s[62:63]
s_add_u32 m0,s57,41856
s_add_u32 s64,s56,3145728
buffer_load_dwordx4 v2,s[36:39],s64 offen lds
s_add_u32 m0,s57,43584
s_nop 0
buffer_load_dwordx4 v3,s[36:39],s64 offen lds
s_mov_b64 exec,-1
RB34_%=:
s_sleep 5
s_cbranch_vccnz RB35_%=
s_mov_b64 exec,s[62:63]
s_add_u32 m0,s57,45344
s_add_u32 s64,s56,3244032
buffer_load_dwordx4 v2,s[36:39],s64 offen lds
s_add_u32 m0,s57,47072
s_nop 0
buffer_load_dwordx4 v3,s[36:39],s64 offen lds
s_mov_b64 exec,-1
RB35_%=:
s_add_u32 s4,s10,16
s_cmp_ge_u32 s4,20
s_cselect_b32 s7,20,0
s_sub_u32 s4,s4,s7
s_mul_i32 s4,s4,3488
s_add_u32 s5,s10,18
s_cmp_ge_u32 s5,20
s_cselect_b32 s7,20,0
s_sub_u32 s5,s5,s7
s_mul_i32 s5,s5,3488
s_add_u32 s6,s10,0
s_cmp_ge_u32 s6,20
s_cselect_b32 s7,20,0
s_sub_u32 s6,s6,s7
s_mul_i32 s6,s6,3488
v_add_u32 v112,s4,v15
v_add_u32 v116,s5,v15
s_mov_b64 exec,s[8:9]
v_add_u32 v112,s5,v15
v_add_u32 v116,s6,v15
s_mov_b64 exec,-1
v_xor_b32 v113,16,v112
v_xor_b32 v117,16,v116
v_xor_b32 v114,32,v112
v_xor_b32 v118,32,v116
v_xor_b32 v115,48,v112
v_xor_b32 v119,48,v116
s_waitcnt vmcnt(0)
s_barrier
s_setprio 3
ds_read_b128 v[136:139],v116 offset:0
ds_read_b128 v[88:91],v11 offset:768
ds_read_b128 v[140:143],v116 offset:3488
ds_read_b128 v[128:131],v112 offset:0
ds_read_b128 v[132:135],v112 offset:3488
ds_read_b128 v[92:95],v12 offset:0
ds_read_b128 v[152:155],v117 offset:0
ds_read_b128 v[96:99],v11 offset:1920
ds_read_b128 v[156:159],v117 offset:3488
ds_read_b128 v[144:147],v113 offset:0
ds_read_b128 v[148:151],v113 offset:3488
ds_read_b128 v[100:103],v12 offset:1152
s_waitcnt lgkmcnt(10)
v_mfma_f32_16x16x32_bf16 v[160:163],v[88:91],v[136:139],0
s_waitcnt lgkmcnt(9)
v_mfma_f32_16x16x32_bf16 v[184:187],v[88:91],v[140:143],0
s_waitcnt lgkmcnt(6)
ds_read_b128 v[140:143],v116 offset:3552
ds_read_b128 v[88:91],v11 offset:5376
v_smfmac_f32_16x16x64_bf16 v[160:163],v[92:95],v[128:135],v13
s_cbranch_vccz RB36_%=
s_add_u32 m0,s58,12320
s_mul_i32 s69,s59,7
s_add_u32 s69,s69,s97
buffer_load_dwordx4 v2,s[44:47],s69 offen lds
s_add_u32 m0,m0,1024
s_add_u32 s69,s69,s53
buffer_load_dwordx4 v3,s[44:47],s69 offen lds
RB36_%=:
ds_read_b128 v[128:131],v112 offset:64
v_smfmac_f32_16x16x64_bf16 v[184:187],v[92:95],v[132:139],v13
s_waitcnt lgkmcnt(7)
ds_read_b128 v[136:139],v116 offset:64
ds_read_b128 v[132:135],v112 offset:3552
v_mfma_f32_16x16x32_bf16 v[164:167],v[96:99],v[152:155],0
s_waitcnt lgkmcnt(8)
ds_read_b128 v[92:95],v12 offset:4608
v_add_f32 v120,v160,v161
v_mul_f32 v124,v160,v160
v_fmac_f32 v124,v161,v161
v_mfma_f32_16x16x32_bf16 v[188:191],v[96:99],v[156:159],0
s_waitcnt lgkmcnt(6)
ds_read_b128 v[156:159],v117 offset:3552
ds_read_b128 v[96:99],v11 offset:6528
v_add_f32 v121,v162,v163
v_mul_f32 v125,v162,v162
v_fmac_f32 v125,v163,v163
v_smfmac_f32_16x16x64_bf16 v[164:167],v[100:103],v[144:151],v13
s_cbranch_vccz RB37_%=
s_add_u32 m0,m0,1024
s_add_u32 s69,s69,s53
buffer_load_dwordx4 v2,s[44:47],s69 offen lds
s_add_u32 m0,m0,s50
s_add_u32 s69,s69,s53
buffer_load_dwordx4 v3,s[44:47],s69 offen lds
RB37_%=:
ds_read_b128 v[144:147],v113 offset:64
v_add_f32 v122,v184,v185
v_mul_f32 v126,v184,v184
v_fmac_f32 v126,v185,v185
v_smfmac_f32_16x16x64_bf16 v[188:191],v[100:103],v[148:155],v13
s_waitcnt lgkmcnt(5)
ds_read_b128 v[152:155],v117 offset:64
ds_read_b128 v[148:151],v113 offset:3552
v_add_f32 v123,v186,v187
v_mul_f32 v127,v186,v186
v_fmac_f32 v127,v187,v187
v_mfma_f32_16x16x32_bf16 v[168:171],v[88:91],v[136:139],0
ds_read_b128 v[100:103],v12 offset:5760
v_add_f32 v120,v120,v164
v_fmac_f32 v124,v164,v164
v_add_f32 v120,v120,v165
v_fmac_f32 v124,v165,v165
v_mfma_f32_16x16x32_bf16 v[192:195],v[88:91],v[140:143],0
s_waitcnt lgkmcnt(6)
s_cbranch_vccz RB38_%=
s_add_u32 m0,m0,1024
s_add_u32 s69,s69,s53
buffer_load_dwordx4 v2,s[44:47],s69 offen lds
s_add_u32 m0,m0,1024
s_add_u32 s69,s69,s53
buffer_load_dwordx4 v3,s[44:47],s69 offen lds
RB38_%=:
ds_read_b128 v[140:143],v116 offset:3616
ds_read_b128 v[88:91],v11 offset:9984
v_add_f32 v121,v121,v166
v_fmac_f32 v125,v166,v166
v_add_f32 v121,v121,v167
v_fmac_f32 v125,v167,v167
v_smfmac_f32_16x16x64_bf16 v[168:171],v[92:95],v[128:135],v13
ds_read_b128 v[128:131],v112 offset:128
v_add_f32 v122,v122,v188
v_fmac_f32 v126,v188,v188
v_add_f32 v122,v122,v189
v_fmac_f32 v126,v189,v189
v_smfmac_f32_16x16x64_bf16 v[192:195],v[92:95],v[132:139],v13
s_waitcnt lgkmcnt(5)
s_cbranch_vccz RB39_%=
s_add_u32 m0,m0,s50
s_add_u32 s69,s69,s53
buffer_load_dwordx4 v2,s[44:47],s69 offen lds
s_add_u32 m0,m0,1024
s_add_u32 s69,s69,s53
buffer_load_dwordx4 v3,s[44:47],s69 offen lds
RB39_%=:
ds_read_b128 v[136:139],v116 offset:128
ds_read_b128 v[132:135],v112 offset:3616
v_add_f32 v123,v123,v190
v_fmac_f32 v127,v190,v190
v_add_f32 v123,v123,v191
v_fmac_f32 v127,v191,v191
v_mfma_f32_16x16x32_bf16 v[172:175],v[96:99],v[152:155],0
ds_read_b128 v[92:95],v12 offset:9216
v_add_f32 v120,v120,v168
v_fmac_f32 v124,v168,v168
v_add_f32 v120,v120,v169
v_fmac_f32 v124,v169,v169
v_mfma_f32_16x16x32_bf16 v[196:199],v[96:99],v[156:159],0
s_waitcnt lgkmcnt(6)
s_cbranch_vccz RB40_%=
s_add_u32 m0,m0,1024
s_add_u32 s69,s69,s53
buffer_load_dwordx4 v2,s[44:47],s69 offen lds
s_add_u32 m0,m0,s50
s_add_u32 s69,s69,s53
buffer_load_dwordx4 v3,s[44:47],s69 offen lds
RB40_%=:
ds_read_b128 v[156:159],v117 offset:3616
ds_read_b128 v[96:99],v11 offset:11136
v_add_f32 v121,v121,v170
v_fmac_f32 v125,v170,v170
v_add_f32 v121,v121,v171
v_fmac_f32 v125,v171,v171
v_smfmac_f32_16x16x64_bf16 v[172:175],v[100:103],v[144:151],v13
ds_read_b128 v[144:147],v113 offset:128
v_add_f32 v122,v122,v192
v_fmac_f32 v126,v192,v192
v_add_f32 v122,v122,v193
v_fmac_f32 v126,v193,v193
v_smfmac_f32_16x16x64_bf16 v[196:199],v[100:103],v[148:155],v13
s_waitcnt lgkmcnt(5)
s_cbranch_vccz RB41_%=
s_add_u32 m0,m0,1024
s_add_u32 s69,s69,s53
buffer_load_dwordx4 v2,s[44:47],s69 offen lds
s_add_u32 m0,m0,1024
s_add_u32 s69,s69,s53
buffer_load_dwordx4 v3,s[44:47],s69 offen lds
RB41_%=:
ds_read_b128 v[152:155],v117 offset:128
ds_read_b128 v[148:151],v113 offset:3616
v_add_f32 v123,v123,v194
v_fmac_f32 v127,v194,v194
v_add_f32 v123,v123,v195
v_fmac_f32 v127,v195,v195
v_mfma_f32_16x16x32_bf16 v[176:179],v[88:91],v[136:139],0
ds_read_b128 v[100:103],v12 offset:10368
v_add_f32 v120,v120,v172
v_fmac_f32 v124,v172,v172
v_add_f32 v120,v120,v173
v_fmac_f32 v124,v173,v173
v_mfma_f32_16x16x32_bf16 v[200:203],v[88:91],v[140:143],0
s_waitcnt lgkmcnt(6)
ds_read_b128 v[140:143],v118 offset:3488
ds_read_b128 v[88:91],v11 offset:3072
v_add_f32 v121,v121,v174
v_fmac_f32 v125,v174,v174
v_add_f32 v121,v121,v175
v_fmac_f32 v125,v175,v175
v_smfmac_f32_16x16x64_bf16 v[176:179],v[92:95],v[128:135],v13
ds_read_b128 v[128:131],v114 offset:0
v_add_f32 v122,v122,v196
v_fmac_f32 v126,v196,v196
v_add_f32 v122,v122,v197
v_fmac_f32 v126,v197,v197
v_smfmac_f32_16x16x64_bf16 v[200:203],v[92:95],v[132:139],v13
s_waitcnt lgkmcnt(5)
ds_read_b128 v[136:139],v118 offset:0
ds_read_b128 v[132:135],v114 offset:3488
v_add_f32 v123,v123,v198
v_fmac_f32 v127,v198,v198
v_add_f32 v123,v123,v199
v_fmac_f32 v127,v199,v199
v_mfma_f32_16x16x32_bf16 v[180:183],v[96:99],v[152:155],0
ds_read_b128 v[92:95],v12 offset:2304
v_add_f32 v120,v120,v176
v_fmac_f32 v124,v176,v176
v_add_f32 v120,v120,v177
v_fmac_f32 v124,v177,v177
v_mfma_f32_16x16x32_bf16 v[204:207],v[96:99],v[156:159],0
s_waitcnt lgkmcnt(6)
ds_read_b128 v[156:159],v119 offset:3488
ds_read_b128 v[96:99],v11 offset:4224
v_add_f32 v121,v121,v178
v_fmac_f32 v125,v178,v178
v_add_f32 v121,v121,v179
v_fmac_f32 v125,v179,v179
v_smfmac_f32_16x16x64_bf16 v[180:183],v[100:103],v[144:151],v13
ds_read_b128 v[144:147],v115 offset:0
v_add_f32 v122,v122,v200
v_fmac_f32 v126,v200,v200
v_add_f32 v122,v122,v201
v_fmac_f32 v126,v201,v201
v_smfmac_f32_16x16x64_bf16 v[204:207],v[100:103],v[148:155],v13
s_waitcnt lgkmcnt(5)
ds_read_b128 v[152:155],v119 offset:0
ds_read_b128 v[148:151],v115 offset:3488
v_add_f32 v123,v123,v202
v_fmac_f32 v127,v202,v202
v_add_f32 v123,v123,v203
v_fmac_f32 v127,v203,v203
v_mfma_f32_16x16x32_bf16 v[208:211],v[88:91],v[136:139],0
ds_read_b128 v[100:103],v12 offset:3456
v_add_f32 v120,v120,v180
v_fmac_f32 v124,v180,v180
v_add_f32 v120,v120,v181
v_fmac_f32 v124,v181,v181
v_mfma_f32_16x16x32_bf16 v[232:235],v[88:91],v[140:143],0
s_waitcnt lgkmcnt(6)
ds_read_b128 v[140:143],v118 offset:3552
ds_read_b128 v[88:91],v11 offset:7680
v_add_f32 v121,v121,v182
v_fmac_f32 v125,v182,v182
v_add_f32 v121,v121,v183
v_fmac_f32 v125,v183,v183
v_smfmac_f32_16x16x64_bf16 v[208:211],v[92:95],v[128:135],v13
ds_read_b128 v[128:131],v114 offset:64
v_add_f32 v122,v122,v204
v_fmac_f32 v126,v204,v204
v_add_f32 v122,v122,v205
v_fmac_f32 v126,v205,v205
v_smfmac_f32_16x16x64_bf16 v[232:235],v[92:95],v[132:139],v13
s_waitcnt lgkmcnt(5)
ds_read_b128 v[136:139],v118 offset:64
ds_read_b128 v[132:135],v114 offset:3552
v_add_f32 v123,v123,v206
v_fmac_f32 v127,v206,v206
v_add_f32 v123,v123,v207
v_fmac_f32 v127,v207,v207
v_mfma_f32_16x16x32_bf16 v[212:215],v[96:99],v[152:155],0
ds_read_b128 v[92:95],v12 offset:6912
v_add_f32 v120,v120,v208
v_fmac_f32 v124,v208,v208
v_add_f32 v120,v120,v209
v_fmac_f32 v124,v209,v209
v_mfma_f32_16x16x32_bf16 v[236:239],v[96:99],v[156:159],0
s_waitcnt lgkmcnt(6)
s_setprio 2
ds_read_b128 v[156:159],v119 offset:3552
ds_read_b128 v[96:99],v11 offset:8832
v_add_f32 v121,v121,v210
v_fmac_f32 v125,v210,v210
v_add_f32 v121,v121,v211
v_fmac_f32 v125,v211,v211
v_smfmac_f32_16x16x64_bf16 v[212:215],v[100:103],v[144:151],v13
ds_read_b128 v[144:147],v115 offset:64
v_add_f32 v122,v122,v232
v_fmac_f32 v126,v232,v232
v_add_f32 v122,v122,v233
v_fmac_f32 v126,v233,v233
v_smfmac_f32_16x16x64_bf16 v[236:239],v[100:103],v[148:155],v13
s_waitcnt lgkmcnt(5)
ds_read_b128 v[152:155],v119 offset:64
ds_read_b128 v[148:151],v115 offset:3552
v_add_f32 v123,v123,v234
v_fmac_f32 v127,v234,v234
v_add_f32 v123,v123,v235
v_fmac_f32 v127,v235,v235
v_mfma_f32_16x16x32_bf16 v[216:219],v[88:91],v[136:139],0
ds_read_b128 v[100:103],v12 offset:8064
v_add_f32 v120,v120,v212
v_fmac_f32 v124,v212,v212
v_add_f32 v120,v120,v213
v_fmac_f32 v124,v213,v213
v_add_f32 v121,v121,v214
v_mfma_f32_16x16x32_bf16 v[240:243],v[88:91],v[140:143],0
s_waitcnt lgkmcnt(6)
ds_read_b128 v[140:143],v118 offset:3616
ds_read_b128 v[88:91],v11 offset:12288
v_fmac_f32 v125,v214,v214
v_add_f32 v121,v121,v215
v_fmac_f32 v125,v215,v215
v_add_f32 v122,v122,v236
v_smfmac_f32_16x16x64_bf16 v[216:219],v[92:95],v[128:135],v13
ds_read_b128 v[128:131],v114 offset:128
v_fmac_f32 v126,v236,v236
v_add_f32 v122,v122,v237
v_fmac_f32 v126,v237,v237
v_add_f32 v123,v123,v238
v_fmac_f32 v127,v238,v238
v_smfmac_f32_16x16x64_bf16 v[240:243],v[92:95],v[132:139],v13
s_waitcnt lgkmcnt(5)
ds_read_b128 v[136:139],v118 offset:128
ds_read_b128 v[132:135],v114 offset:3616
v_add_f32 v123,v123,v239
v_fmac_f32 v127,v239,v239
v_add_u32 v112,v112,v8
v_add_u32 v116,v116,v8
v_mfma_f32_16x16x32_bf16 v[220:223],v[96:99],v[152:155],0
ds_read_b128 v[92:95],v12 offset:11520
ds_read_b128 v[104:107],v14 offset:0
v_add_f32 v120,v120,v216
v_fmac_f32 v124,v216,v216
v_add_f32 v120,v120,v217
v_fmac_f32 v124,v217,v217
v_mfma_f32_16x16x32_bf16 v[244:247],v[96:99],v[156:159],0
s_waitcnt lgkmcnt(7)
ds_read_b128 v[108:111],v7 offset:3584
ds_read_b128 v[156:159],v119 offset:3616
v_add_f32 v121,v121,v218
v_fmac_f32 v125,v218,v218
v_add_f32 v121,v121,v219
v_fmac_f32 v125,v219,v219
v_smfmac_f32_16x16x64_bf16 v[220:223],v[100:103],v[144:151],v13
ds_read_b128 v[96:99],v11 offset:13440
ds_read_b128 v[144:147],v115 offset:128
v_add_f32 v122,v122,v240
v_fmac_f32 v126,v240,v240
v_add_f32 v122,v122,v241
v_fmac_f32 v126,v241,v241
v_smfmac_f32_16x16x64_bf16 v[244:247],v[100:103],v[148:155],v13
s_waitcnt lgkmcnt(7)
ds_read_b128 v[152:155],v119 offset:128
ds_read_b128 v[148:151],v115 offset:3616
v_add_f32 v123,v123,v242
v_fmac_f32 v127,v242,v242
v_add_f32 v123,v123,v243
v_fmac_f32 v127,v243,v243
v_mfma_f32_16x16x32_bf16 v[224:227],v[88:91],v[136:139],0
ds_read_b128 v[100:103],v12 offset:12672
v_add_f32 v120,v120,v220
v_fmac_f32 v124,v220,v220
v_add_f32 v120,v120,v221
v_fmac_f32 v124,v221,v221
v_mfma_f32_16x16x32_bf16 v[248:251],v[88:91],v[140:143],0
s_waitcnt lgkmcnt(8)
s_setprio 1
ds_read_b128 v[140:143],v116 offset:192
ds_read_b128 v[88:91],v112 offset:3616
v_add_f32 v121,v121,v222
v_fmac_f32 v125,v222,v222
v_add_f32 v121,v121,v223
v_fmac_f32 v125,v223,v223
v_smfmac_f32_16x16x64_bf16 v[224:227],v[92:95],v[128:135],v13
ds_read_b128 v[128:131],v112 offset:3488
v_add_f32 v122,v122,v244
s_mov_b32 s86,0xbe801d5a
v_fmac_f32 v126,v244,v244
v_add_f32 v122,v122,v245
v_fmac_f32 v126,v245,v245
v_smfmac_f32_16x16x64_bf16 v[248:251],v[92:95],v[132:139],v13
s_waitcnt lgkmcnt(5)
ds_read_b128 v[136:139],v116 offset:0
ds_read_b128 v[132:135],v112 offset:3680
v_add_f32 v123,v123,v246
v_fmac_f32 v127,v246,v246
v_add_f32 v123,v123,v247
v_fmac_f32 v127,v247,v247
v_mfma_f32_16x16x32_bf16 v[228:231],v[96:99],v[152:155],0
ds_read_b128 v[92:95],v112 offset:3808
v_add_f32 v120,v120,v224
v_fmac_f32 v124,v224,v224
v_add_f32 v120,v120,v225
v_fmac_f32 v124,v225,v225
v_mfma_f32_16x16x32_bf16 v[252:255],v[96:99],v[156:159],0
s_waitcnt lgkmcnt(6)
ds_read_b128 v[156:159],v116 offset:256
ds_read_b128 v[96:99],v116 offset:128
v_add_f32 v121,v121,v226
v_fmac_f32 v125,v226,v226
v_add_f32 v121,v121,v227
v_fmac_f32 v125,v227,v227
v_smfmac_f32_16x16x64_bf16 v[228:231],v[100:103],v[144:151],v13
ds_read_b128 v[144:147],v112 offset:3552
v_add_f32 v122,v122,v248
v_fmac_f32 v126,v248,v248
v_add_f32 v122,v122,v249
v_fmac_f32 v126,v249,v249
v_smfmac_f32_16x16x64_bf16 v[252:255],v[100:103],v[148:155],v13
s_setprio 0
ds_read_b128 v[152:155],v116 offset:64
ds_read_b128 v[148:151],v112 offset:3744
v_add_f32 v123,v123,v250
v_fmac_f32 v127,v250,v250
v_add_f32 v123,v123,v251
v_fmac_f32 v127,v251,v251
ds_read_b128 v[100:103],v116 offset:320
v_add_f32 v120,v120,v228
v_fmac_f32 v124,v228,v228
v_add_f32 v120,v120,v229
v_fmac_f32 v124,v229,v229
v_add_f32 v121,v121,v230
v_fmac_f32 v125,v230,v230
v_add_f32 v121,v121,v231
v_fmac_f32 v125,v231,v231
s_waitcnt lgkmcnt(0)
s_waitcnt vmcnt(0)
s_barrier
s_setprio 0
v_add_f32 v122,v122,v252
v_fmac_f32 v126,v252,v252
v_add_f32 v122,v122,v253
v_fmac_f32 v126,v253,v253
v_add_f32 v123,v123,v254
s_cbranch_vccz RB42_%=
s_add_u32 m0,s58,0
s_mul_i32 s69,s59,9
s_add_u32 s69,s69,s97
buffer_load_dwordx4 v2,s[44:47],s69 offen lds
s_add_u32 m0,m0,1024
s_add_u32 s69,s69,s53
buffer_load_dwordx4 v3,s[44:47],s69 offen lds
RB42_%=:
v_fmac_f32 v127,v254,v254
v_add_f32 v123,v123,v255
v_fmac_f32 v127,v255,v255
s_setprio 3
ds_read_b128 v[56:59],v14 offset:256
ds_read_b128 v[60:63],v7 offset:3616
s_setprio 0
v_permlane32_swap_b32 v120,v122
v_permlane32_swap_b32 v121,v123
v_permlane32_swap_b32 v124,v126
v_permlane32_swap_b32 v125,v127
v_add_f32 v120,v120,v122
v_add_f32 v121,v121,v123
v_add_f32 v124,v124,v126
v_add_f32 v125,v125,v127
s_cbranch_vccz RB43_%=
s_add_u32 m0,m0,1024
s_add_u32 s69,s69,s53
buffer_load_dwordx4 v2,s[44:47],s69 offen lds
s_add_u32 m0,m0,s50
s_add_u32 s69,s69,s53
buffer_load_dwordx4 v3,s[44:47],s69 offen lds
RB43_%=:
v_permlane16_swap_b32 v120,v121
v_permlane16_swap_b32 v124,v125
v_add_f32 v113,v120,v121
v_add_f32 v114,v124,v125
v_mul_f32 v113,0x3c2aaaab,v113
v_mul_f32 v114,0x3c2aaaab,v114
v_mul_f32 v115,0x5e000000,v114
v_fma_f32 v114,-v113,v113,v114
v_pk_maximum3_f16 v1,v1,v115,v115 op_sel:[0,1,1] op_sel_hi:[1,1,1]
v_max_f32 v114,0,v114
v_add_f32 v114,0x3727c5ac,v114
v_rsq_f32 v114,v114
s_cbranch_vccz RB44_%=
s_add_u32 m0,m0,1024
s_add_u32 s69,s69,s53
buffer_load_dwordx4 v2,s[44:47],s69 offen lds
s_add_u32 m0,m0,1024
s_add_u32 s69,s69,s53
buffer_load_dwordx4 v3,s[44:47],s69 offen lds
RB44_%=:
v_mul_f32 v120,0x8befe000,v114
v_mul_f32 v121,0x8befe000,v114
v_mul_f32 v124,-v113,v120
v_mul_f32 v125,-v113,v120
v_permlane16_swap_b32 v120,v121
s_nop 0
v_permlane16_swap_b32 v124,v125
v_mov_b32 v122,v120
v_mov_b32 v123,v121
v_mov_b32 v126,v124
v_mov_b32 v127,v125
v_permlane32_swap_b32 v120,v122
v_permlane32_swap_b32 v124,v126
s_cbranch_vccz RB45_%=
s_add_u32 m0,m0,s50
s_add_u32 s69,s69,s53
buffer_load_dwordx4 v2,s[44:47],s69 offen lds
s_add_u32 m0,m0,1024
s_add_u32 s69,s69,s53
buffer_load_dwordx4 v3,s[44:47],s69 offen lds
RB45_%=:
v_permlane32_swap_b32 v121,v123
v_permlane32_swap_b32 v125,v127
v_fma_f32 v160,v160,v120,v124
s_setprio 3
ds_read_b64_tr_b16 v[64:65],v4 offset:12832
s_setprio 0
v_fma_f32 v161,v161,v120,v124
s_setprio 3
ds_read_b64_tr_b16 v[66:67],v4 offset:14880
s_setprio 0
v_fma_f32 v162,v162,v121,v125
s_setprio 3
ds_read_b128 v[112:115],v7 offset:1152
s_setprio 0
v_fma_f32 v163,v163,v121,v125
s_setprio 3
ds_read_b64_tr_b16 v[68:69],v5 offset:12832
s_setprio 0
v_fma_f32 v164,v164,v120,v124
s_setprio 3
ds_read_b64_tr_b16 v[70:71],v5 offset:14880
s_setprio 0
v_fma_f32 v165,v165,v120,v124
s_setprio 3
ds_read_b128 v[116:119],v7 offset:1184
s_setprio 0
v_fma_f32 v166,v166,v121,v125
s_setprio 3
ds_read_b64_tr_b16 v[72:73],v4 offset:16928
s_setprio 0
v_fma_f32 v167,v167,v121,v125
s_setprio 3
ds_read_b64_tr_b16 v[74:75],v4 offset:18976
s_setprio 0
v_fma_f32 v208,v208,v120,v124
s_setprio 3
ds_read_b64_tr_b16 v[76:77],v5 offset:16928
s_setprio 0
v_fma_f32 v209,v209,v120,v124
s_cbranch_vccz RB46_%=
s_add_u32 m0,m0,1024
s_add_u32 s69,s69,s53
buffer_load_dwordx4 v2,s[44:47],s69 offen lds
s_add_u32 m0,m0,s50
s_add_u32 s69,s69,s53
buffer_load_dwordx4 v3,s[44:47],s69 offen lds
RB46_%=:
s_setprio 3
ds_read_b64_tr_b16 v[78:79],v5 offset:18976
s_setprio 0
v_fma_f32 v210,v210,v121,v125
s_setprio 3
ds_read_b64_tr_b16 v[80:81],v4 offset:21024
s_setprio 0
v_fma_f32 v211,v211,v121,v125
s_setprio 3
ds_read_b64_tr_b16 v[82:83],v4 offset:23072
s_setprio 0
v_fma_f32 v212,v212,v120,v124
s_setprio 3
ds_read_b64_tr_b16 v[84:85],v5 offset:21024
s_setprio 0
v_fma_f32 v213,v213,v120,v124
s_setprio 3
ds_read_b64_tr_b16 v[86:87],v5 offset:23072
s_setprio 0
v_fma_f32 v214,v214,v121,v125
v_fma_f32 v215,v215,v121,v125
v_cvt_pk_bf16_f32 v16,v160,v161
v_cvt_pk_bf16_f32 v20,v162,v163
v_cvt_pk_bf16_f32 v17,v164,v165
v_cvt_pk_bf16_f32 v21,v166,v167
v_cvt_pk_bf16_f32 v18,v208,v209
v_cvt_pk_bf16_f32 v22,v210,v211
v_cvt_pk_bf16_f32 v19,v212,v213
v_cvt_pk_bf16_f32 v23,v214,v215
v_fma_f32 v168,v168,v120,v124
v_fma_f32 v169,v169,v120,v124
v_mfma_f32_16x16x32_bf16 v[160:163],v[104:107],v[128:131],v[108:111]
s_cbranch_vccz RB47_%=
s_add_u32 m0,m0,1024
s_add_u32 s69,s69,s53
buffer_load_dwordx4 v2,s[44:47],s69 offen lds
s_add_u32 m0,m0,1024
s_add_u32 s69,s69,s53
buffer_load_dwordx4 v3,s[44:47],s69 offen lds
RB47_%=:
v_fma_f32 v170,v170,v121,v125
v_fma_f32 v171,v171,v121,v125
v_fma_f32 v172,v172,v120,v124
v_fma_f32 v173,v173,v120,v124
v_fma_f32 v174,v174,v121,v125
v_fma_f32 v175,v175,v121,v125
v_mfma_f32_16x16x32_bf16 v[164:167],v[104:107],v[132:135],v[108:111]
v_fma_f32 v216,v216,v120,v124
v_fma_f32 v217,v217,v120,v124
v_fma_f32 v218,v218,v121,v125
v_fma_f32 v219,v219,v121,v125
v_fma_f32 v220,v220,v120,v124
v_mfma_f32_16x16x32_bf16 v[208:211],v[104:107],v[136:139],v[108:111]
v_fma_f32 v221,v221,v120,v124
v_fma_f32 v222,v222,v121,v125
v_fma_f32 v223,v223,v121,v125
v_cvt_pk_bf16_f32 v24,v168,v169
v_cvt_pk_bf16_f32 v28,v170,v171
s_setprio 3
v_mfma_f32_16x16x32_bf16 v[212:215],v[104:107],v[140:143],v[108:111]
ds_read_b128 v[104:107],v14 offset:512
s_setprio 0
v_cvt_pk_bf16_f32 v25,v172,v173
v_cvt_pk_bf16_f32 v29,v174,v175
v_cvt_pk_bf16_f32 v26,v216,v217
v_cvt_pk_bf16_f32 v30,v218,v219
v_cvt_pk_bf16_f32 v27,v220,v221
v_cvt_pk_bf16_f32 v31,v222,v223
v_fma_f32 v176,v176,v120,v124
v_fma_f32 v177,v177,v120,v124
s_waitcnt lgkmcnt(15)
s_setprio 3
v_mfma_f32_16x16x32_bf16 v[168:171],v[56:59],v[128:131],v[60:63]
ds_read_b128 v[108:111],v7 offset:3712
s_setprio 0
v_fma_f32 v178,v178,v121,v125
v_fma_f32 v179,v179,v121,v125
v_fma_f32 v180,v180,v120,v124
v_fma_f32 v181,v181,v120,v124
v_fma_f32 v182,v182,v121,v125
v_fma_f32 v183,v183,v121,v125
v_mfma_f32_16x16x32_bf16 v[172:175],v[56:59],v[132:135],v[60:63]
v_fma_f32 v224,v224,v120,v124
v_fma_f32 v225,v225,v120,v124
v_fma_f32 v226,v226,v121,v125
v_fma_f32 v227,v227,v121,v125
v_fma_f32 v228,v228,v120,v124
v_mfma_f32_16x16x32_bf16 v[216:219],v[56:59],v[136:139],v[60:63]
v_fma_f32 v229,v229,v120,v124
v_fma_f32 v230,v230,v121,v125
v_fma_f32 v231,v231,v121,v125
v_cvt_pk_bf16_f32 v32,v176,v177
v_cvt_pk_bf16_f32 v36,v178,v179
s_setprio 3
v_mfma_f32_16x16x32_bf16 v[220:223],v[56:59],v[140:143],v[60:63]
ds_read_b128 v[56:59],v14 offset:768
s_setprio 0
v_cvt_pk_bf16_f32 v33,v180,v181
v_cvt_pk_bf16_f32 v37,v182,v183
v_cvt_pk_bf16_f32 v34,v224,v225
v_cvt_pk_bf16_f32 v38,v226,v227
v_cvt_pk_bf16_f32 v35,v228,v229
v_cvt_pk_bf16_f32 v39,v230,v231
v_fma_f32 v184,v184,v122,v126
s_waitcnt lgkmcnt(1)
s_setprio 3
v_mfma_f32_16x16x32_bf16 v[176:179],v[104:107],v[144:147],v[108:111]
ds_read_b128 v[60:63],v7 offset:3744
s_setprio 0
v_fma_f32 v185,v185,v122,v126
v_fma_f32 v186,v186,v123,v127
v_fma_f32 v187,v187,v123,v127
v_mfma_f32_16x16x32_bf16 v[128:131],v[64:67],v[16:19],v[112:115]
v_fma_f32 v188,v188,v122,v126
v_fma_f32 v189,v189,v122,v126
v_fma_f32 v190,v190,v123,v127
v_mfma_f32_16x16x32_bf16 v[180:183],v[104:107],v[148:151],v[108:111]
v_fma_f32 v191,v191,v123,v127
v_fma_f32 v232,v232,v122,v126
v_fma_f32 v233,v233,v122,v126
v_mfma_f32_16x16x32_bf16 v[136:139],v[64:67],v[20:23],v[112:115]
v_fma_f32 v234,v234,v123,v127
v_fma_f32 v235,v235,v123,v127
v_fma_f32 v236,v236,v122,v126
v_mfma_f32_16x16x32_bf16 v[224:227],v[104:107],v[152:155],v[108:111]
v_fma_f32 v237,v237,v122,v126
v_fma_f32 v238,v238,v123,v127
v_fma_f32 v239,v239,v123,v127
v_mfma_f32_16x16x32_bf16 v[140:143],v[68:71],v[20:23],v[116:119]
v_cvt_pk_bf16_f32 v40,v184,v185
v_cvt_pk_bf16_f32 v44,v186,v187
v_cvt_pk_bf16_f32 v41,v188,v189
s_setprio 3
v_mfma_f32_16x16x32_bf16 v[228:231],v[104:107],v[156:159],v[108:111]
ds_read_b128 v[104:107],v14 offset:1024
s_setprio 0
v_cvt_pk_bf16_f32 v45,v190,v191
v_cvt_pk_bf16_f32 v42,v232,v233
v_cvt_pk_bf16_f32 v46,v234,v235
s_setprio 3
v_mfma_f32_16x16x32_bf16 v[132:135],v[68:71],v[16:19],v[116:119]
ds_read_b128 v[108:111],v7 offset:3840
s_setprio 0
v_cvt_pk_bf16_f32 v43,v236,v237
v_cvt_pk_bf16_f32 v47,v238,v239
v_fma_f32 v192,v192,v122,v126
s_waitcnt lgkmcnt(2)
v_mfma_f32_16x16x32_bf16 v[184:187],v[56:59],v[144:147],v[60:63]
v_fma_f32 v193,v193,v122,v126
v_fma_f32 v194,v194,v123,v127
v_mfma_f32_16x16x32_bf16 v[128:131],v[72:75],v[24:27],v[128:131]
v_fma_f32 v195,v195,v123,v127
v_fma_f32 v196,v196,v122,v126
v_mfma_f32_16x16x32_bf16 v[188:191],v[56:59],v[148:151],v[60:63]
v_fma_f32 v197,v197,v122,v126
v_fma_f32 v198,v198,v123,v127
v_mfma_f32_16x16x32_bf16 v[136:139],v[72:75],v[28:31],v[136:139]
v_fma_f32 v199,v199,v123,v127
v_fma_f32 v240,v240,v122,v126
v_mfma_f32_16x16x32_bf16 v[232:235],v[56:59],v[152:155],v[60:63]
v_fma_f32 v241,v241,v122,v126
v_fma_f32 v242,v242,v123,v127
v_mfma_f32_16x16x32_bf16 v[140:143],v[76:79],v[28:31],v[140:143]
v_fma_f32 v243,v243,v123,v127
v_fma_f32 v244,v244,v122,v126
s_setprio 3
v_mfma_f32_16x16x32_bf16 v[236:239],v[56:59],v[156:159],v[60:63]
ds_read_b128 v[144:147],v14 offset:1280
s_setprio 0
v_fma_f32 v245,v245,v122,v126
v_fma_f32 v246,v246,v123,v127
s_setprio 3
v_mfma_f32_16x16x32_bf16 v[132:135],v[76:79],v[24:27],v[132:135]
ds_read_b128 v[148:151],v7 offset:3872
s_setprio 0
v_fma_f32 v247,v247,v123,v127
v_cvt_pk_bf16_f32 v48,v192,v193
v_cvt_pk_bf16_f32 v52,v194,v195
v_cvt_pk_bf16_f32 v49,v196,v197
v_cvt_pk_bf16_f32 v53,v198,v199
v_cvt_pk_bf16_f32 v50,v240,v241
v_cvt_pk_bf16_f32 v54,v242,v243
v_cvt_pk_bf16_f32 v51,v244,v245
v_cvt_pk_bf16_f32 v55,v246,v247
v_fma_f32 v200,v200,v122,v126
s_waitcnt lgkmcnt(2)
v_mfma_f32_16x16x32_bf16 v[192:195],v[104:107],v[88:91],v[108:111]
v_fma_f32 v201,v201,v122,v126
v_fma_f32 v202,v202,v123,v127
v_fma_f32 v203,v203,v123,v127
v_mfma_f32_16x16x32_bf16 v[128:131],v[80:83],v[32:35],v[128:131]
v_fma_f32 v204,v204,v122,v126
v_fma_f32 v205,v205,v122,v126
v_fma_f32 v206,v206,v123,v127
v_mfma_f32_16x16x32_bf16 v[196:199],v[104:107],v[92:95],v[108:111]
v_fma_f32 v207,v207,v123,v127
v_fma_f32 v248,v248,v122,v126
v_fma_f32 v249,v249,v122,v126
v_mfma_f32_16x16x32_bf16 v[136:139],v[80:83],v[36:39],v[136:139]
v_fma_f32 v250,v250,v123,v127
v_fma_f32 v251,v251,v123,v127
v_fma_f32 v252,v252,v122,v126
v_mfma_f32_16x16x32_bf16 v[240:243],v[104:107],v[96:99],v[108:111]
v_fma_f32 v253,v253,v122,v126
v_fma_f32 v254,v254,v123,v127
v_fma_f32 v255,v255,v123,v127
v_mfma_f32_16x16x32_bf16 v[140:143],v[84:87],v[36:39],v[140:143]
v_cvt_pk_bf16_f32 v56,v200,v201
v_cvt_pk_bf16_f32 v60,v202,v203
v_cvt_pk_bf16_f32 v57,v204,v205
v_mfma_f32_16x16x32_bf16 v[244:247],v[104:107],v[100:103],v[108:111]
v_cvt_pk_bf16_f32 v61,v206,v207
v_cvt_pk_bf16_f32 v58,v248,v249
v_cvt_pk_bf16_f32 v62,v250,v251
v_mfma_f32_16x16x32_bf16 v[132:135],v[84:87],v[32:35],v[132:135]
v_cvt_pk_bf16_f32 v59,v252,v253
v_cvt_pk_bf16_f32 v63,v254,v255
s_waitcnt lgkmcnt(0)
v_mfma_f32_16x16x32_bf16 v[200:203],v[144:147],v[88:91],v[148:151]
s_waitcnt vmcnt(0)
s_barrier
s_setprio 3
ds_read_b64_tr_b16 v[104:105],v6 offset:128
v_lshlrev_b32_sdwa v128,1,v128 dst_sel:DWORD dst_unused:UNUSED_PAD src0_sel:DWORD src1_sel:WORD_1
v_lshlrev_b32_sdwa v130,1,v130 dst_sel:DWORD dst_unused:UNUSED_PAD src0_sel:DWORD src1_sel:WORD_1
v_lshlrev_b32_sdwa v129,1,v129 dst_sel:DWORD dst_unused:UNUSED_PAD src0_sel:DWORD src1_sel:WORD_1
v_mfma_f32_16x16x32_bf16 v[204:207],v[144:147],v[92:95],v[148:151]
ds_read_u16 v128,v128
ds_read_b64_tr_b16 v[106:107],v6 offset:1664
v_lshlrev_b32_sdwa v131,1,v131 dst_sel:DWORD dst_unused:UNUSED_PAD src0_sel:DWORD src1_sel:WORD_1
v_fma_mixlo_f16 v120,v140,s99,0
v_fma_mixlo_f16 v122,v141,s99,0
v_mfma_f32_16x16x32_bf16 v[248:251],v[144:147],v[96:99],v[148:151]
ds_read_u16_d16_hi v130,v130
ds_read_b64_tr_b16 v[108:109],v6 offset:136
v_lshlrev_b32_sdwa v132,1,v132 dst_sel:DWORD dst_unused:UNUSED_PAD src0_sel:DWORD src1_sel:WORD_1
v_lshlrev_b32_sdwa v134,1,v134 dst_sel:DWORD dst_unused:UNUSED_PAD src0_sel:DWORD src1_sel:WORD_1
v_mfma_f32_16x16x32_bf16 v[252:255],v[144:147],v[100:103],v[148:151]
ds_read_u16 v129,v129
ds_read_b64_tr_b16 v[110:111],v6 offset:1672
v_fma_mixhi_f16 v120,v142,s99,0
v_fma_mixhi_f16 v122,v143,s99,0
v_lshlrev_b32_sdwa v133,1,v133 dst_sel:DWORD dst_unused:UNUSED_PAD src0_sel:DWORD src1_sel:WORD_1
v_lshlrev_b32_sdwa v135,1,v135 dst_sel:DWORD dst_unused:UNUSED_PAD src0_sel:DWORD src1_sel:WORD_1
v_mfma_f32_16x16x32_bf16 v[144:147],v[64:67],v[40:43],v[112:115]
ds_read_u16_d16_hi v131,v131
ds_read_b64_tr_b16 v[88:89],v6 offset:0
v_pk_fma_f16 v121,v120,v120,s54
v_pk_fma_f16 v123,v122,v122,s54
v_pk_mul_f16 v121,v120,v121
v_mfma_f32_16x16x32_bf16 v[152:155],v[64:67],v[44:47],v[112:115]
ds_read_u16 v132,v132
ds_read_b64_tr_b16 v[90:91],v6 offset:1536
v_pk_mul_f16 v123,v122,v123
v_lshlrev_b32_sdwa v136,1,v136 dst_sel:DWORD dst_unused:UNUSED_PAD src0_sel:DWORD src1_sel:WORD_1
v_mfma_f32_16x16x32_bf16 v[156:159],v[68:71],v[44:47],v[116:119]
ds_read_u16_d16_hi v134,v134
ds_read_b64_tr_b16 v[92:93],v6 offset:8
v_lshlrev_b32_sdwa v138,1,v138 dst_sel:DWORD dst_unused:UNUSED_PAD src0_sel:DWORD src1_sel:WORD_1
v_exp_f16 v120,v121
v_mfma_f32_16x16x32_bf16 v[148:151],v[68:71],v[40:43],v[116:119]
ds_read_u16 v133,v133
ds_read_b64_tr_b16 v[94:95],v6 offset:1544
v_exp_f16 v122,v123
v_lshlrev_b32_sdwa v137,1,v137 dst_sel:DWORD dst_unused:UNUSED_PAD src0_sel:DWORD src1_sel:WORD_1
v_mfma_f32_16x16x32_bf16 v[144:147],v[72:75],v[48:51],v[144:147]
ds_read_u16_d16_hi v135,v135
ds_read_b64_tr_b16 v[96:97],v6 offset:64
v_lshlrev_b32_sdwa v139,1,v139 dst_sel:DWORD dst_unused:UNUSED_PAD src0_sel:DWORD src1_sel:WORD_1
v_exp_f16_sdwa v120,v121 dst_sel:WORD_1 dst_unused:UNUSED_PRESERVE src0_sel:WORD_1
v_mfma_f32_16x16x32_bf16 v[152:155],v[72:75],v[52:55],v[152:155]
ds_read_u16 v136,v136
ds_read_b64_tr_b16 v[98:99],v6 offset:1600
v_exp_f16_sdwa v122,v123 dst_sel:WORD_1 dst_unused:UNUSED_PRESERVE src0_sel:WORD_1
v_mfma_f32_16x16x32_bf16 v[156:159],v[76:79],v[52:55],v[156:159]
ds_read_u16_d16_hi v138,v138
ds_read_b64_tr_b16 v[100:101],v6 offset:72
v_pk_fma_f16 v120,v120,s55,s55
v_pk_fma_f16 v122,v122,s55,s55
v_rcp_f16 v121,v120
v_mfma_f32_16x16x32_bf16 v[148:151],v[76:79],v[48:51],v[148:151]
ds_read_u16 v137,v137
ds_read_b64_tr_b16 v[102:103],v6 offset:1608
v_rcp_f16 v123,v122
v_mfma_f32_16x16x32_bf16 v[144:147],v[80:83],v[56:59],v[144:147]
ds_read_u16_d16_hi v139,v139
ds_read_b64_tr_b16 v[64:65],v4 offset:0
v_rcp_f16_sdwa v121,v120 dst_sel:WORD_1 dst_unused:UNUSED_PRESERVE src0_sel:WORD_1
v_mfma_f32_16x16x32_bf16 v[152:155],v[80:83],v[60:63],v[152:155]
ds_read_b64_tr_b16 v[66:67],v4 offset:2048
v_rcp_f16_sdwa v123,v122 dst_sel:WORD_1 dst_unused:UNUSED_PRESERVE src0_sel:WORD_1
v_fma_mix_f32 v140,v140,v121,0 op_sel_hi:[0,1,0]
v_mfma_f32_16x16x32_bf16 v[156:159],v[84:87],v[60:63],v[156:159]
ds_read_b128 v[112:115],v7 offset:1280
v_fma_mix_f32 v142,v142,v121,0 op_sel:[0,1,0] op_sel_hi:[0,1,0]
s_waitcnt lgkmcnt(15)
v_lshl_add_u64 v[128:129],v[128:129],0,v[130:131]
s_waitcnt lgkmcnt(11)
v_lshl_add_u64 v[130:131],v[132:133],0,v[134:135]
v_mfma_f32_16x16x32_bf16 v[148:151],v[84:87],v[56:59],v[148:151]
ds_read_b64_tr_b16 v[68:69],v5 offset:0
v_fma_mix_f32 v141,v141,v123,0 op_sel_hi:[0,1,0]
v_fma_mix_f32 v143,v143,v123,0 op_sel:[0,1,0] op_sel_hi:[0,1,0]
v_lshlrev_b32_sdwa v144,1,v144 dst_sel:DWORD dst_unused:UNUSED_PAD src0_sel:DWORD src1_sel:WORD_1
v_lshlrev_b32_sdwa v146,1,v146 dst_sel:DWORD dst_unused:UNUSED_PAD src0_sel:DWORD src1_sel:WORD_1
v_mfma_f32_16x16x32_bf16 v[160:163],v[88:91],v[128:131],v[160:163]
ds_read_u16 v144,v144
v_lshlrev_b32_sdwa v145,1,v145 dst_sel:DWORD dst_unused:UNUSED_PAD src0_sel:DWORD src1_sel:WORD_1
v_lshlrev_b32_sdwa v147,1,v147 dst_sel:DWORD dst_unused:UNUSED_PAD src0_sel:DWORD src1_sel:WORD_1
v_mfma_f32_16x16x32_bf16 v[168:171],v[92:95],v[128:131],v[168:171]
s_waitcnt lgkmcnt(10)
ds_read_b64_tr_b16 v[70:71],v5 offset:2048
ds_read_b128 v[116:119],v7 offset:1312
v_fma_mixlo_f16 v120,v156,s99,0
v_fma_mixlo_f16 v122,v157,s99,0
v_mfma_f32_16x16x32_bf16 v[176:179],v[96:99],v[128:131],v[176:179]
s_waitcnt lgkmcnt(8)
ds_read_u16_d16_hi v146,v146
v_lshlrev_b32_sdwa v148,1,v148 dst_sel:DWORD dst_unused:UNUSED_PAD src0_sel:DWORD src1_sel:WORD_1
v_lshlrev_b32_sdwa v150,1,v150 dst_sel:DWORD dst_unused:UNUSED_PAD src0_sel:DWORD src1_sel:WORD_1
v_mfma_f32_16x16x32_bf16 v[184:187],v[100:103],v[128:131],v[184:187]
s_waitcnt lgkmcnt(8)
ds_read_b64_tr_b16 v[72:73],v4 offset:4096
ds_read_b64_tr_b16 v[74:75],v4 offset:6144
v_lshl_add_u64 v[136:137],v[136:137],0,v[138:139]
v_mfma_f32_16x16x32_bf16 v[192:195],v[104:107],v[128:131],v[192:195]
ds_read_u16 v145,v145
v_cvt_pk_bf16_f32 v138,v140,v142
v_cvt_pk_bf16_f32 v139,v141,v143
v_mfma_f32_16x16x32_bf16 v[200:203],v[108:111],v[128:131],v[200:203]
ds_read_b64_tr_b16 v[76:77],v5 offset:4096
ds_read_b64_tr_b16 v[78:79],v5 offset:6144
v_fma_mixhi_f16 v120,v158,s99,0
v_fma_mixhi_f16 v122,v159,s99,0
v_mfma_f32_16x16x32_bf16 v[164:167],v[88:91],v[136:139],v[164:167]
ds_read_u16_d16_hi v147,v147
v_lshlrev_b32_sdwa v149,1,v149 dst_sel:DWORD dst_unused:UNUSED_PAD src0_sel:DWORD src1_sel:WORD_1
v_lshlrev_b32_sdwa v151,1,v151 dst_sel:DWORD dst_unused:UNUSED_PAD src0_sel:DWORD src1_sel:WORD_1
v_mfma_f32_16x16x32_bf16 v[172:175],v[92:95],v[136:139],v[172:175]
ds_read_b64_tr_b16 v[80:81],v4 offset:8192
ds_read_b64_tr_b16 v[82:83],v4 offset:10240
v_pk_fma_f16 v121,v120,v120,s54
v_pk_fma_f16 v123,v122,v122,s54
v_mfma_f32_16x16x32_bf16 v[180:183],v[96:99],v[136:139],v[180:183]
ds_read_u16 v148,v148
v_pk_mul_f16 v121,v120,v121
v_pk_mul_f16 v123,v122,v123
v_mfma_f32_16x16x32_bf16 v[188:191],v[100:103],v[136:139],v[188:191]
ds_read_b64_tr_b16 v[84:85],v5 offset:8192
v_lshlrev_b32_sdwa v152,1,v152 dst_sel:DWORD dst_unused:UNUSED_PAD src0_sel:DWORD src1_sel:WORD_1
v_lshlrev_b32_sdwa v154,1,v154 dst_sel:DWORD dst_unused:UNUSED_PAD src0_sel:DWORD src1_sel:WORD_1
v_mfma_f32_16x16x32_bf16 v[196:199],v[104:107],v[136:139],v[196:199]
ds_read_u16_d16_hi v150,v150
v_exp_f16 v120,v121
v_mfma_f32_16x16x32_bf16 v[204:207],v[108:111],v[136:139],v[204:207]
s_waitcnt lgkmcnt(15)
ds_read_b64_tr_b16 v[86:87],v5 offset:10240
v_exp_f16 v122,v123
v_mfma_f32_16x16x32_bf16 v[128:131],v[64:67],v[16:19],v[112:115]
ds_read_u16 v149,v149
v_lshlrev_b32_sdwa v153,1,v153 dst_sel:DWORD dst_unused:UNUSED_PAD src0_sel:DWORD src1_sel:WORD_1
v_lshlrev_b32_sdwa v155,1,v155 dst_sel:DWORD dst_unused:UNUSED_PAD src0_sel:DWORD src1_sel:WORD_1
v_mfma_f32_16x16x32_bf16 v[136:139],v[64:67],v[20:23],v[112:115]
s_waitcnt lgkmcnt(14)
v_exp_f16_sdwa v120,v121 dst_sel:WORD_1 dst_unused:UNUSED_PRESERVE src0_sel:WORD_1
v_mfma_f32_16x16x32_bf16 v[140:143],v[68:71],v[20:23],v[116:119]
ds_read_u16_d16_hi v151,v151
v_exp_f16_sdwa v122,v123 dst_sel:WORD_1 dst_unused:UNUSED_PRESERVE src0_sel:WORD_1
v_mfma_f32_16x16x32_bf16 v[132:135],v[68:71],v[16:19],v[116:119]
s_waitcnt lgkmcnt(12)
v_pk_fma_f16 v120,v120,s55,s55
v_pk_fma_f16 v122,v122,s55,s55
v_mfma_f32_16x16x32_bf16 v[128:131],v[72:75],v[24:27],v[128:131]
ds_read_u16 v152,v152
v_rcp_f16 v121,v120
v_mfma_f32_16x16x32_bf16 v[136:139],v[72:75],v[28:31],v[136:139]
s_waitcnt lgkmcnt(10)
v_rcp_f16 v123,v122
v_mfma_f32_16x16x32_bf16 v[140:143],v[76:79],v[28:31],v[140:143]
ds_read_u16_d16_hi v154,v154
v_rcp_f16_sdwa v121,v120 dst_sel:WORD_1 dst_unused:UNUSED_PRESERVE src0_sel:WORD_1
v_mfma_f32_16x16x32_bf16 v[132:135],v[76:79],v[24:27],v[132:135]
s_waitcnt lgkmcnt(8)
v_rcp_f16_sdwa v123,v122 dst_sel:WORD_1 dst_unused:UNUSED_PRESERVE src0_sel:WORD_1
v_mfma_f32_16x16x32_bf16 v[128:131],v[80:83],v[32:35],v[128:131]
ds_read_u16 v153,v153
v_fma_mix_f32 v156,v156,v121,0 op_sel_hi:[0,1,0]
v_fma_mix_f32 v158,v158,v121,0 op_sel:[0,1,0] op_sel_hi:[0,1,0]
v_mfma_f32_16x16x32_bf16 v[136:139],v[80:83],v[36:39],v[136:139]
s_waitcnt lgkmcnt(3)
v_lshl_add_u64 v[144:145],v[144:145],0,v[146:147]
v_lshl_add_u64 v[146:147],v[148:149],0,v[150:151]
v_mfma_f32_16x16x32_bf16 v[140:143],v[84:87],v[36:39],v[140:143]
ds_read_u16_d16_hi v155,v155
v_fma_mix_f32 v157,v157,v123,0 op_sel_hi:[0,1,0]
v_fma_mix_f32 v159,v159,v123,0 op_sel:[0,1,0] op_sel_hi:[0,1,0]
v_mfma_f32_16x16x32_bf16 v[132:135],v[84:87],v[32:35],v[132:135]
v_lshlrev_b32_sdwa v128,1,v128 dst_sel:DWORD dst_unused:UNUSED_PAD src0_sel:DWORD src1_sel:WORD_1
v_lshlrev_b32_sdwa v130,1,v130 dst_sel:DWORD dst_unused:UNUSED_PAD src0_sel:DWORD src1_sel:WORD_1
v_mfma_f32_16x16x32_bf16 v[208:211],v[88:91],v[144:147],v[208:211]
ds_read_u16 v128,v128
v_lshlrev_b32_sdwa v129,1,v129 dst_sel:DWORD dst_unused:UNUSED_PAD src0_sel:DWORD src1_sel:WORD_1
v_lshlrev_b32_sdwa v131,1,v131 dst_sel:DWORD dst_unused:UNUSED_PAD src0_sel:DWORD src1_sel:WORD_1
v_mfma_f32_16x16x32_bf16 v[216:219],v[92:95],v[144:147],v[216:219]
v_fma_mixlo_f16 v120,v140,s99,0
v_fma_mixlo_f16 v122,v141,s99,0
v_mfma_f32_16x16x32_bf16 v[224:227],v[96:99],v[144:147],v[224:227]
ds_read_u16_d16_hi v130,v130
v_lshlrev_b32_sdwa v132,1,v132 dst_sel:DWORD dst_unused:UNUSED_PAD src0_sel:DWORD src1_sel:WORD_1
v_lshlrev_b32_sdwa v134,1,v134 dst_sel:DWORD dst_unused:UNUSED_PAD src0_sel:DWORD src1_sel:WORD_1
v_mfma_f32_16x16x32_bf16 v[232:235],v[100:103],v[144:147],v[232:235]
s_waitcnt lgkmcnt(2)
v_lshl_add_u64 v[152:153],v[152:153],0,v[154:155]
v_mfma_f32_16x16x32_bf16 v[240:243],v[104:107],v[144:147],v[240:243]
ds_read_u16 v129,v129
v_cvt_pk_bf16_f32 v154,v156,v158
v_cvt_pk_bf16_f32 v155,v157,v159
v_mfma_f32_16x16x32_bf16 v[248:251],v[108:111],v[144:147],v[248:251]
v_fma_mixhi_f16 v120,v142,s99,0
v_fma_mixhi_f16 v122,v143,s99,0
v_mfma_f32_16x16x32_bf16 v[212:215],v[88:91],v[152:155],v[212:215]
ds_read_u16_d16_hi v131,v131
v_lshlrev_b32_sdwa v133,1,v133 dst_sel:DWORD dst_unused:UNUSED_PAD src0_sel:DWORD src1_sel:WORD_1
v_lshlrev_b32_sdwa v135,1,v135 dst_sel:DWORD dst_unused:UNUSED_PAD src0_sel:DWORD src1_sel:WORD_1
s_mov_b32 s86,0xbe801d5a
v_mfma_f32_16x16x32_bf16 v[220:223],v[92:95],v[152:155],v[220:223]
s_setprio 2
ds_read_b64_tr_b16 v[88:89],v6 offset:6160
v_pk_fma_f16 v121,v120,v120,s54
v_pk_fma_f16 v123,v122,v122,s54
v_mfma_f32_16x16x32_bf16 v[228:231],v[96:99],v[152:155],v[228:231]
ds_read_u16 v132,v132
v_pk_mul_f16 v121,v120,v121
v_pk_mul_f16 v123,v122,v123
v_mfma_f32_16x16x32_bf16 v[236:239],v[100:103],v[152:155],v[236:239]
ds_read_b64_tr_b16 v[90:91],v6 offset:7696
v_lshlrev_b32_sdwa v136,1,v136 dst_sel:DWORD dst_unused:UNUSED_PAD src0_sel:DWORD src1_sel:WORD_1
v_lshlrev_b32_sdwa v138,1,v138 dst_sel:DWORD dst_unused:UNUSED_PAD src0_sel:DWORD src1_sel:WORD_1
v_mfma_f32_16x16x32_bf16 v[244:247],v[104:107],v[152:155],v[244:247]
ds_read_u16_d16_hi v134,v134
v_exp_f16 v120,v121
v_mfma_f32_16x16x32_bf16 v[252:255],v[108:111],v[152:155],v[252:255]
ds_read_b64_tr_b16 v[92:93],v6 offset:6168
v_exp_f16 v122,v123
v_mfma_f32_16x16x32_bf16 v[144:147],v[64:67],v[40:43],v[112:115]
ds_read_u16 v133,v133
v_lshlrev_b32_sdwa v137,1,v137 dst_sel:DWORD dst_unused:UNUSED_PAD src0_sel:DWORD src1_sel:WORD_1
v_lshlrev_b32_sdwa v139,1,v139 dst_sel:DWORD dst_unused:UNUSED_PAD src0_sel:DWORD src1_sel:WORD_1
v_mfma_f32_16x16x32_bf16 v[152:155],v[64:67],v[44:47],v[112:115]
ds_read_b64_tr_b16 v[94:95],v6 offset:7704
v_exp_f16_sdwa v120,v121 dst_sel:WORD_1 dst_unused:UNUSED_PRESERVE src0_sel:WORD_1
v_mfma_f32_16x16x32_bf16 v[156:159],v[68:71],v[44:47],v[116:119]
ds_read_u16_d16_hi v135,v135
v_exp_f16_sdwa v122,v123 dst_sel:WORD_1 dst_unused:UNUSED_PRESERVE src0_sel:WORD_1
v_mfma_f32_16x16x32_bf16 v[148:151],v[68:71],v[40:43],v[116:119]
ds_read_b64_tr_b16 v[96:97],v6 offset:6224
v_pk_fma_f16 v120,v120,s55,s55
v_pk_fma_f16 v122,v122,s55,s55
v_mfma_f32_16x16x32_bf16 v[144:147],v[72:75],v[48:51],v[144:147]
ds_read_u16 v136,v136
v_rcp_f16 v121,v120
v_mfma_f32_16x16x32_bf16 v[152:155],v[72:75],v[52:55],v[152:155]
ds_read_b64_tr_b16 v[98:99],v6 offset:7760
ds_read_b64_tr_b16 v[100:101],v6 offset:6232
v_rcp_f16 v123,v122
v_mfma_f32_16x16x32_bf16 v[156:159],v[76:79],v[52:55],v[156:159]
ds_read_u16_d16_hi v138,v138
v_rcp_f16_sdwa v121,v120 dst_sel:WORD_1 dst_unused:UNUSED_PRESERVE src0_sel:WORD_1
v_mfma_f32_16x16x32_bf16 v[148:151],v[76:79],v[48:51],v[148:151]
ds_read_b64_tr_b16 v[102:103],v6 offset:7768
ds_read_b64_tr_b16 v[104:105],v6 offset:6288
v_rcp_f16_sdwa v123,v122 dst_sel:WORD_1 dst_unused:UNUSED_PRESERVE src0_sel:WORD_1
v_mfma_f32_16x16x32_bf16 v[144:147],v[80:83],v[56:59],v[144:147]
ds_read_u16 v137,v137
v_fma_mix_f32 v140,v140,v121,0 op_sel_hi:[0,1,0]
v_fma_mix_f32 v142,v142,v121,0 op_sel:[0,1,0] op_sel_hi:[0,1,0]
v_mfma_f32_16x16x32_bf16 v[152:155],v[80:83],v[60:63],v[152:155]
s_waitcnt lgkmcnt(8)
ds_read_b64_tr_b16 v[106:107],v6 offset:7824
ds_read_b64_tr_b16 v[108:109],v6 offset:6296
v_lshl_add_u64 v[128:129],v[128:129],0,v[130:131]
v_lshl_add_u64 v[130:131],v[132:133],0,v[134:135]
v_mfma_f32_16x16x32_bf16 v[156:159],v[84:87],v[60:63],v[156:159]
ds_read_u16_d16_hi v139,v139
v_fma_mix_f32 v141,v141,v123,0 op_sel_hi:[0,1,0]
v_fma_mix_f32 v143,v143,v123,0 op_sel:[0,1,0] op_sel_hi:[0,1,0]
v_mfma_f32_16x16x32_bf16 v[148:151],v[84:87],v[56:59],v[148:151]
ds_read_b64_tr_b16 v[110:111],v6 offset:7832
ds_read_b64_tr_b16 v[64:65],v4 offset:512
v_lshlrev_b32_sdwa v144,1,v144 dst_sel:DWORD dst_unused:UNUSED_PAD src0_sel:DWORD src1_sel:WORD_1
v_lshlrev_b32_sdwa v146,1,v146 dst_sel:DWORD dst_unused:UNUSED_PAD src0_sel:DWORD src1_sel:WORD_1
v_mfma_f32_16x16x32_bf16 v[160:163],v[88:91],v[128:131],v[160:163]
ds_read_u16 v144,v144
v_lshlrev_b32_sdwa v145,1,v145 dst_sel:DWORD dst_unused:UNUSED_PAD src0_sel:DWORD src1_sel:WORD_1
v_lshlrev_b32_sdwa v147,1,v147 dst_sel:DWORD dst_unused:UNUSED_PAD src0_sel:DWORD src1_sel:WORD_1
v_mfma_f32_16x16x32_bf16 v[168:171],v[92:95],v[128:131],v[168:171]
s_waitcnt lgkmcnt(11)
ds_read_b64_tr_b16 v[66:67],v4 offset:2560
ds_read_b128 v[112:115],v7 offset:1408
v_fma_mixlo_f16 v120,v156,s99,0
v_fma_mixlo_f16 v122,v157,s99,0
v_mfma_f32_16x16x32_bf16 v[176:179],v[96:99],v[128:131],v[176:179]
s_waitcnt lgkmcnt(10)
ds_read_u16_d16_hi v146,v146
v_lshlrev_b32_sdwa v148,1,v148 dst_sel:DWORD dst_unused:UNUSED_PAD src0_sel:DWORD src1_sel:WORD_1
v_lshlrev_b32_sdwa v150,1,v150 dst_sel:DWORD dst_unused:UNUSED_PAD src0_sel:DWORD src1_sel:WORD_1
v_mfma_f32_16x16x32_bf16 v[184:187],v[100:103],v[128:131],v[184:187]
s_waitcnt lgkmcnt(6)
ds_read_b64_tr_b16 v[68:69],v5 offset:512
ds_read_b64_tr_b16 v[70:71],v5 offset:2560
v_lshl_add_u64 v[136:137],v[136:137],0,v[138:139]
v_mfma_f32_16x16x32_bf16 v[192:195],v[104:107],v[128:131],v[192:195]
s_waitcnt lgkmcnt(7)
ds_read_u16 v145,v145
v_cvt_pk_bf16_f32 v138,v140,v142
v_cvt_pk_bf16_f32 v139,v141,v143
v_mfma_f32_16x16x32_bf16 v[200:203],v[108:111],v[128:131],v[200:203]
ds_read_b128 v[116:119],v7 offset:1440
ds_read_b64_tr_b16 v[72:73],v4 offset:4608
v_fma_mixhi_f16 v120,v158,s99,0
v_fma_mixhi_f16 v122,v159,s99,0
v_mfma_f32_16x16x32_bf16 v[164:167],v[88:91],v[136:139],v[164:167]
ds_read_u16_d16_hi v147,v147
v_lshlrev_b32_sdwa v149,1,v149 dst_sel:DWORD dst_unused:UNUSED_PAD src0_sel:DWORD src1_sel:WORD_1
v_lshlrev_b32_sdwa v151,1,v151 dst_sel:DWORD dst_unused:UNUSED_PAD src0_sel:DWORD src1_sel:WORD_1
v_mfma_f32_16x16x32_bf16 v[172:175],v[92:95],v[136:139],v[172:175]
ds_read_b64_tr_b16 v[74:75],v4 offset:6656
ds_read_b64_tr_b16 v[76:77],v5 offset:4608
v_pk_fma_f16 v121,v120,v120,s54
v_pk_fma_f16 v123,v122,v122,s54
v_mfma_f32_16x16x32_bf16 v[180:183],v[96:99],v[136:139],v[180:183]
s_setprio 1
ds_read_u16 v148,v148
v_pk_mul_f16 v121,v120,v121
v_pk_mul_f16 v123,v122,v123
v_mfma_f32_16x16x32_bf16 v[188:191],v[100:103],v[136:139],v[188:191]
ds_read_b64_tr_b16 v[78:79],v5 offset:6656
v_lshlrev_b32_sdwa v152,1,v152 dst_sel:DWORD dst_unused:UNUSED_PAD src0_sel:DWORD src1_sel:WORD_1
v_lshlrev_b32_sdwa v154,1,v154 dst_sel:DWORD dst_unused:UNUSED_PAD src0_sel:DWORD src1_sel:WORD_1
v_mfma_f32_16x16x32_bf16 v[196:199],v[104:107],v[136:139],v[196:199]
ds_read_u16_d16_hi v150,v150
v_exp_f16 v120,v121
v_mfma_f32_16x16x32_bf16 v[204:207],v[108:111],v[136:139],v[204:207]
s_waitcnt lgkmcnt(12)
ds_read_b64_tr_b16 v[80:81],v4 offset:8704
v_exp_f16 v122,v123
v_mfma_f32_16x16x32_bf16 v[128:131],v[64:67],v[16:19],v[112:115]
ds_read_u16 v149,v149
v_lshlrev_b32_sdwa v153,1,v153 dst_sel:DWORD dst_unused:UNUSED_PAD src0_sel:DWORD src1_sel:WORD_1
v_lshlrev_b32_sdwa v155,1,v155 dst_sel:DWORD dst_unused:UNUSED_PAD src0_sel:DWORD src1_sel:WORD_1
v_mfma_f32_16x16x32_bf16 v[136:139],v[64:67],v[20:23],v[112:115]
s_waitcnt lgkmcnt(9)
ds_read_b64_tr_b16 v[82:83],v4 offset:10752
v_exp_f16_sdwa v120,v121 dst_sel:WORD_1 dst_unused:UNUSED_PRESERVE src0_sel:WORD_1
v_mfma_f32_16x16x32_bf16 v[140:143],v[68:71],v[20:23],v[116:119]
ds_read_u16_d16_hi v151,v151
v_exp_f16_sdwa v122,v123 dst_sel:WORD_1 dst_unused:UNUSED_PRESERVE src0_sel:WORD_1
v_mfma_f32_16x16x32_bf16 v[132:135],v[68:71],v[16:19],v[116:119]
s_waitcnt lgkmcnt(8)
ds_read_b64_tr_b16 v[84:85],v5 offset:8704
v_pk_fma_f16 v120,v120,s55,s55
v_pk_fma_f16 v122,v122,s55,s55
v_mfma_f32_16x16x32_bf16 v[128:131],v[72:75],v[24:27],v[128:131]
ds_read_u16 v152,v152
v_rcp_f16 v121,v120
v_mfma_f32_16x16x32_bf16 v[136:139],v[72:75],v[28:31],v[136:139]
s_waitcnt lgkmcnt(7)
ds_read_b64_tr_b16 v[86:87],v5 offset:10752
v_rcp_f16 v123,v122
v_mfma_f32_16x16x32_bf16 v[140:143],v[76:79],v[28:31],v[140:143]
ds_read_u16_d16_hi v154,v154
v_rcp_f16_sdwa v121,v120 dst_sel:WORD_1 dst_unused:UNUSED_PRESERVE src0_sel:WORD_1
v_mfma_f32_16x16x32_bf16 v[132:135],v[76:79],v[24:27],v[132:135]
s_waitcnt lgkmcnt(5)
v_rcp_f16_sdwa v123,v122 dst_sel:WORD_1 dst_unused:UNUSED_PRESERVE src0_sel:WORD_1
v_mfma_f32_16x16x32_bf16 v[128:131],v[80:83],v[32:35],v[128:131]
ds_read_u16 v153,v153
v_fma_mix_f32 v156,v156,v121,0 op_sel_hi:[0,1,0]
v_fma_mix_f32 v158,v158,v121,0 op_sel:[0,1,0] op_sel_hi:[0,1,0]
v_mfma_f32_16x16x32_bf16 v[136:139],v[80:83],v[36:39],v[136:139]
s_waitcnt lgkmcnt(2)
s_setprio 0
v_lshl_add_u64 v[144:145],v[144:145],0,v[146:147]
v_lshl_add_u64 v[146:147],v[148:149],0,v[150:151]
v_mfma_f32_16x16x32_bf16 v[140:143],v[84:87],v[36:39],v[140:143]
ds_read_u16_d16_hi v155,v155
v_fma_mix_f32 v157,v157,v123,0 op_sel_hi:[0,1,0]
v_fma_mix_f32 v159,v159,v123,0 op_sel:[0,1,0] op_sel_hi:[0,1,0]
v_mfma_f32_16x16x32_bf16 v[132:135],v[84:87],v[32:35],v[132:135]
v_lshlrev_b32_sdwa v128,1,v128 dst_sel:DWORD dst_unused:UNUSED_PAD src0_sel:DWORD src1_sel:WORD_1
v_lshlrev_b32_sdwa v130,1,v130 dst_sel:DWORD dst_unused:UNUSED_PAD src0_sel:DWORD src1_sel:WORD_1
v_mfma_f32_16x16x32_bf16 v[208:211],v[88:91],v[144:147],v[208:211]
s_barrier
s_setprio 3
ds_read_u16 v128,v128
v_lshlrev_b32_sdwa v129,1,v129 dst_sel:DWORD dst_unused:UNUSED_PAD src0_sel:DWORD src1_sel:WORD_1
v_lshlrev_b32_sdwa v131,1,v131 dst_sel:DWORD dst_unused:UNUSED_PAD src0_sel:DWORD src1_sel:WORD_1
v_mfma_f32_16x16x32_bf16 v[216:219],v[92:95],v[144:147],v[216:219]
v_fma_mixlo_f16 v120,v140,s99,0
v_fma_mixlo_f16 v122,v141,s99,0
v_mfma_f32_16x16x32_bf16 v[224:227],v[96:99],v[144:147],v[224:227]
ds_read_u16_d16_hi v130,v130
v_lshlrev_b32_sdwa v132,1,v132 dst_sel:DWORD dst_unused:UNUSED_PAD src0_sel:DWORD src1_sel:WORD_1
v_lshlrev_b32_sdwa v134,1,v134 dst_sel:DWORD dst_unused:UNUSED_PAD src0_sel:DWORD src1_sel:WORD_1
v_mfma_f32_16x16x32_bf16 v[232:235],v[100:103],v[144:147],v[232:235]
s_waitcnt lgkmcnt(2)
v_lshl_add_u64 v[152:153],v[152:153],0,v[154:155]
v_mfma_f32_16x16x32_bf16 v[240:243],v[104:107],v[144:147],v[240:243]
ds_read_u16 v129,v129
v_cvt_pk_bf16_f32 v154,v156,v158
v_cvt_pk_bf16_f32 v155,v157,v159
v_mfma_f32_16x16x32_bf16 v[248:251],v[108:111],v[144:147],v[248:251]
v_fma_mixhi_f16 v120,v142,s99,0
v_fma_mixhi_f16 v122,v143,s99,0
v_mfma_f32_16x16x32_bf16 v[212:215],v[88:91],v[152:155],v[212:215]
ds_read_u16_d16_hi v131,v131
v_lshlrev_b32_sdwa v133,1,v133 dst_sel:DWORD dst_unused:UNUSED_PAD src0_sel:DWORD src1_sel:WORD_1
v_lshlrev_b32_sdwa v135,1,v135 dst_sel:DWORD dst_unused:UNUSED_PAD src0_sel:DWORD src1_sel:WORD_1
v_mfma_f32_16x16x32_bf16 v[220:223],v[92:95],v[152:155],v[220:223]
ds_read_b64_tr_b16 v[88:89],v6 offset:12320
v_pk_fma_f16 v121,v120,v120,s54
v_pk_fma_f16 v123,v122,v122,s54
v_mfma_f32_16x16x32_bf16 v[228:231],v[96:99],v[152:155],v[228:231]
ds_read_u16 v132,v132
v_pk_mul_f16 v121,v120,v121
v_pk_mul_f16 v123,v122,v123
v_mfma_f32_16x16x32_bf16 v[236:239],v[100:103],v[152:155],v[236:239]
ds_read_b64_tr_b16 v[90:91],v6 offset:13856
v_lshlrev_b32_sdwa v136,1,v136 dst_sel:DWORD dst_unused:UNUSED_PAD src0_sel:DWORD src1_sel:WORD_1
v_lshlrev_b32_sdwa v138,1,v138 dst_sel:DWORD dst_unused:UNUSED_PAD src0_sel:DWORD src1_sel:WORD_1
v_mfma_f32_16x16x32_bf16 v[244:247],v[104:107],v[152:155],v[244:247]
ds_read_u16_d16_hi v134,v134
v_exp_f16 v120,v121
v_mfma_f32_16x16x32_bf16 v[252:255],v[108:111],v[152:155],v[252:255]
ds_read_b64_tr_b16 v[92:93],v6 offset:12328
v_exp_f16 v122,v123
v_mfma_f32_16x16x32_bf16 v[144:147],v[64:67],v[40:43],v[112:115]
ds_read_u16 v133,v133
v_lshlrev_b32_sdwa v137,1,v137 dst_sel:DWORD dst_unused:UNUSED_PAD src0_sel:DWORD src1_sel:WORD_1
v_lshlrev_b32_sdwa v139,1,v139 dst_sel:DWORD dst_unused:UNUSED_PAD src0_sel:DWORD src1_sel:WORD_1
v_mfma_f32_16x16x32_bf16 v[152:155],v[64:67],v[44:47],v[112:115]
ds_read_b64_tr_b16 v[94:95],v6 offset:13864
v_exp_f16_sdwa v120,v121 dst_sel:WORD_1 dst_unused:UNUSED_PRESERVE src0_sel:WORD_1
v_mfma_f32_16x16x32_bf16 v[156:159],v[68:71],v[44:47],v[116:119]
ds_read_u16_d16_hi v135,v135
v_exp_f16_sdwa v122,v123 dst_sel:WORD_1 dst_unused:UNUSED_PRESERVE src0_sel:WORD_1
v_mfma_f32_16x16x32_bf16 v[148:151],v[68:71],v[40:43],v[116:119]
ds_read_b64_tr_b16 v[96:97],v6 offset:12384
v_pk_fma_f16 v120,v120,s55,s55
v_pk_fma_f16 v122,v122,s55,s55
v_mfma_f32_16x16x32_bf16 v[144:147],v[72:75],v[48:51],v[144:147]
ds_read_u16 v136,v136
v_rcp_f16 v121,v120
v_mfma_f32_16x16x32_bf16 v[152:155],v[72:75],v[52:55],v[152:155]
ds_read_b64_tr_b16 v[98:99],v6 offset:13920
ds_read_b64_tr_b16 v[100:101],v6 offset:12392
v_rcp_f16 v123,v122
v_mfma_f32_16x16x32_bf16 v[156:159],v[76:79],v[52:55],v[156:159]
ds_read_u16_d16_hi v138,v138
v_rcp_f16_sdwa v121,v120 dst_sel:WORD_1 dst_unused:UNUSED_PRESERVE src0_sel:WORD_1
v_mfma_f32_16x16x32_bf16 v[148:151],v[76:79],v[48:51],v[148:151]
ds_read_b64_tr_b16 v[102:103],v6 offset:13928
ds_read_b64_tr_b16 v[104:105],v6 offset:12448
v_rcp_f16_sdwa v123,v122 dst_sel:WORD_1 dst_unused:UNUSED_PRESERVE src0_sel:WORD_1
v_mfma_f32_16x16x32_bf16 v[144:147],v[80:83],v[56:59],v[144:147]
ds_read_u16 v137,v137
v_fma_mix_f32 v140,v140,v121,0 op_sel_hi:[0,1,0]
v_fma_mix_f32 v142,v142,v121,0 op_sel:[0,1,0] op_sel_hi:[0,1,0]
v_mfma_f32_16x16x32_bf16 v[152:155],v[80:83],v[60:63],v[152:155]
s_waitcnt lgkmcnt(8)
ds_read_b64_tr_b16 v[106:107],v6 offset:13984
ds_read_b64_tr_b16 v[108:109],v6 offset:12456
v_lshl_add_u64 v[128:129],v[128:129],0,v[130:131]
v_lshl_add_u64 v[130:131],v[132:133],0,v[134:135]
v_mfma_f32_16x16x32_bf16 v[156:159],v[84:87],v[60:63],v[156:159]
ds_read_u16_d16_hi v139,v139
v_fma_mix_f32 v141,v141,v123,0 op_sel_hi:[0,1,0]
v_fma_mix_f32 v143,v143,v123,0 op_sel:[0,1,0] op_sel_hi:[0,1,0]
v_mfma_f32_16x16x32_bf16 v[148:151],v[84:87],v[56:59],v[148:151]
ds_read_b64_tr_b16 v[110:111],v6 offset:13992
ds_read_b64_tr_b16 v[64:65],v4 offset:12320
v_lshlrev_b32_sdwa v144,1,v144 dst_sel:DWORD dst_unused:UNUSED_PAD src0_sel:DWORD src1_sel:WORD_1
v_lshlrev_b32_sdwa v146,1,v146 dst_sel:DWORD dst_unused:UNUSED_PAD src0_sel:DWORD src1_sel:WORD_1
v_mfma_f32_16x16x32_bf16 v[160:163],v[88:91],v[128:131],v[160:163]
ds_read_u16 v144,v144
v_lshlrev_b32_sdwa v145,1,v145 dst_sel:DWORD dst_unused:UNUSED_PAD src0_sel:DWORD src1_sel:WORD_1
v_lshlrev_b32_sdwa v147,1,v147 dst_sel:DWORD dst_unused:UNUSED_PAD src0_sel:DWORD src1_sel:WORD_1
v_mfma_f32_16x16x32_bf16 v[168:171],v[92:95],v[128:131],v[168:171]
s_waitcnt lgkmcnt(11)
ds_read_b64_tr_b16 v[66:67],v4 offset:14368
ds_read_b128 v[112:115],v7 offset:0
v_fma_mixlo_f16 v120,v156,s99,0
v_fma_mixlo_f16 v122,v157,s99,0
v_mfma_f32_16x16x32_bf16 v[176:179],v[96:99],v[128:131],v[176:179]
s_waitcnt lgkmcnt(10)
ds_read_u16_d16_hi v146,v146
v_lshlrev_b32_sdwa v148,1,v148 dst_sel:DWORD dst_unused:UNUSED_PAD src0_sel:DWORD src1_sel:WORD_1
v_lshlrev_b32_sdwa v150,1,v150 dst_sel:DWORD dst_unused:UNUSED_PAD src0_sel:DWORD src1_sel:WORD_1
v_mfma_f32_16x16x32_bf16 v[184:187],v[100:103],v[128:131],v[184:187]
s_waitcnt lgkmcnt(6)
ds_read_b64_tr_b16 v[68:69],v5 offset:12320
ds_read_b64_tr_b16 v[70:71],v5 offset:14368
v_lshl_add_u64 v[136:137],v[136:137],0,v[138:139]
v_mfma_f32_16x16x32_bf16 v[192:195],v[104:107],v[128:131],v[192:195]
s_waitcnt lgkmcnt(7)
ds_read_u16 v145,v145
v_cvt_pk_bf16_f32 v138,v140,v142
v_cvt_pk_bf16_f32 v139,v141,v143
v_mfma_f32_16x16x32_bf16 v[200:203],v[108:111],v[128:131],v[200:203]
ds_read_b128 v[116:119],v7 offset:32
ds_read_b64_tr_b16 v[72:73],v4 offset:16416
v_fma_mixhi_f16 v120,v158,s99,0
v_fma_mixhi_f16 v122,v159,s99,0
v_mfma_f32_16x16x32_bf16 v[164:167],v[88:91],v[136:139],v[164:167]
ds_read_u16_d16_hi v147,v147
v_lshlrev_b32_sdwa v149,1,v149 dst_sel:DWORD dst_unused:UNUSED_PAD src0_sel:DWORD src1_sel:WORD_1
v_lshlrev_b32_sdwa v151,1,v151 dst_sel:DWORD dst_unused:UNUSED_PAD src0_sel:DWORD src1_sel:WORD_1
v_mfma_f32_16x16x32_bf16 v[172:175],v[92:95],v[136:139],v[172:175]
ds_read_b64_tr_b16 v[74:75],v4 offset:18464
ds_read_b64_tr_b16 v[76:77],v5 offset:16416
v_pk_fma_f16 v121,v120,v120,s54
v_pk_fma_f16 v123,v122,v122,s54
v_mfma_f32_16x16x32_bf16 v[180:183],v[96:99],v[136:139],v[180:183]
ds_read_u16 v148,v148
v_pk_mul_f16 v121,v120,v121
v_pk_mul_f16 v123,v122,v123
v_mfma_f32_16x16x32_bf16 v[188:191],v[100:103],v[136:139],v[188:191]
ds_read_b64_tr_b16 v[78:79],v5 offset:18464
v_lshlrev_b32_sdwa v152,1,v152 dst_sel:DWORD dst_unused:UNUSED_PAD src0_sel:DWORD src1_sel:WORD_1
v_lshlrev_b32_sdwa v154,1,v154 dst_sel:DWORD dst_unused:UNUSED_PAD src0_sel:DWORD src1_sel:WORD_1
v_mfma_f32_16x16x32_bf16 v[196:199],v[104:107],v[136:139],v[196:199]
ds_read_u16_d16_hi v150,v150
v_exp_f16 v120,v121
v_mfma_f32_16x16x32_bf16 v[204:207],v[108:111],v[136:139],v[204:207]
s_waitcnt lgkmcnt(12)
ds_read_b64_tr_b16 v[80:81],v4 offset:20512
v_exp_f16 v122,v123
v_mfma_f32_16x16x32_bf16 v[128:131],v[64:67],v[16:19],v[112:115]
ds_read_u16 v149,v149
v_lshlrev_b32_sdwa v153,1,v153 dst_sel:DWORD dst_unused:UNUSED_PAD src0_sel:DWORD src1_sel:WORD_1
v_lshlrev_b32_sdwa v155,1,v155 dst_sel:DWORD dst_unused:UNUSED_PAD src0_sel:DWORD src1_sel:WORD_1
v_mfma_f32_16x16x32_bf16 v[136:139],v[64:67],v[20:23],v[112:115]
s_waitcnt lgkmcnt(9)
ds_read_b64_tr_b16 v[82:83],v4 offset:22560
v_exp_f16_sdwa v120,v121 dst_sel:WORD_1 dst_unused:UNUSED_PRESERVE src0_sel:WORD_1
v_mfma_f32_16x16x32_bf16 v[140:143],v[68:71],v[20:23],v[116:119]
ds_read_u16_d16_hi v151,v151
v_exp_f16_sdwa v122,v123 dst_sel:WORD_1 dst_unused:UNUSED_PRESERVE src0_sel:WORD_1
v_mfma_f32_16x16x32_bf16 v[132:135],v[68:71],v[16:19],v[116:119]
s_waitcnt lgkmcnt(8)
ds_read_b64_tr_b16 v[84:85],v5 offset:20512
v_pk_fma_f16 v120,v120,s55,s55
v_pk_fma_f16 v122,v122,s55,s55
v_mfma_f32_16x16x32_bf16 v[128:131],v[72:75],v[24:27],v[128:131]
ds_read_u16 v152,v152
v_rcp_f16 v121,v120
v_mfma_f32_16x16x32_bf16 v[136:139],v[72:75],v[28:31],v[136:139]
s_waitcnt lgkmcnt(7)
ds_read_b64_tr_b16 v[86:87],v5 offset:22560
v_rcp_f16 v123,v122
v_mfma_f32_16x16x32_bf16 v[140:143],v[76:79],v[28:31],v[140:143]
ds_read_u16_d16_hi v154,v154
v_rcp_f16_sdwa v121,v120 dst_sel:WORD_1 dst_unused:UNUSED_PRESERVE src0_sel:WORD_1
v_mfma_f32_16x16x32_bf16 v[132:135],v[76:79],v[24:27],v[132:135]
s_waitcnt lgkmcnt(5)
v_rcp_f16_sdwa v123,v122 dst_sel:WORD_1 dst_unused:UNUSED_PRESERVE src0_sel:WORD_1
v_mfma_f32_16x16x32_bf16 v[128:131],v[80:83],v[32:35],v[128:131]
ds_read_u16 v153,v153
v_fma_mix_f32 v156,v156,v121,0 op_sel_hi:[0,1,0]
v_fma_mix_f32 v158,v158,v121,0 op_sel:[0,1,0] op_sel_hi:[0,1,0]
v_mfma_f32_16x16x32_bf16 v[136:139],v[80:83],v[36:39],v[136:139]
s_waitcnt lgkmcnt(2)
v_lshl_add_u64 v[144:145],v[144:145],0,v[146:147]
v_lshl_add_u64 v[146:147],v[148:149],0,v[150:151]
v_mfma_f32_16x16x32_bf16 v[140:143],v[84:87],v[36:39],v[140:143]
ds_read_u16_d16_hi v155,v155
v_fma_mix_f32 v157,v157,v123,0 op_sel_hi:[0,1,0]
v_fma_mix_f32 v159,v159,v123,0 op_sel:[0,1,0] op_sel_hi:[0,1,0]
v_mfma_f32_16x16x32_bf16 v[132:135],v[84:87],v[32:35],v[132:135]
v_lshlrev_b32_sdwa v128,1,v128 dst_sel:DWORD dst_unused:UNUSED_PAD src0_sel:DWORD src1_sel:WORD_1
v_lshlrev_b32_sdwa v130,1,v130 dst_sel:DWORD dst_unused:UNUSED_PAD src0_sel:DWORD src1_sel:WORD_1
v_mfma_f32_16x16x32_bf16 v[208:211],v[88:91],v[144:147],v[208:211]
ds_read_u16 v128,v128
v_lshlrev_b32_sdwa v129,1,v129 dst_sel:DWORD dst_unused:UNUSED_PAD src0_sel:DWORD src1_sel:WORD_1
v_lshlrev_b32_sdwa v131,1,v131 dst_sel:DWORD dst_unused:UNUSED_PAD src0_sel:DWORD src1_sel:WORD_1
v_mfma_f32_16x16x32_bf16 v[216:219],v[92:95],v[144:147],v[216:219]
v_fma_mixlo_f16 v120,v140,s99,0
v_fma_mixlo_f16 v122,v141,s99,0
v_mfma_f32_16x16x32_bf16 v[224:227],v[96:99],v[144:147],v[224:227]
ds_read_u16_d16_hi v130,v130
v_lshlrev_b32_sdwa v132,1,v132 dst_sel:DWORD dst_unused:UNUSED_PAD src0_sel:DWORD src1_sel:WORD_1
v_lshlrev_b32_sdwa v134,1,v134 dst_sel:DWORD dst_unused:UNUSED_PAD src0_sel:DWORD src1_sel:WORD_1
v_mfma_f32_16x16x32_bf16 v[232:235],v[100:103],v[144:147],v[232:235]
s_waitcnt lgkmcnt(2)
v_lshl_add_u64 v[152:153],v[152:153],0,v[154:155]
v_mfma_f32_16x16x32_bf16 v[240:243],v[104:107],v[144:147],v[240:243]
ds_read_u16 v129,v129
v_cvt_pk_bf16_f32 v154,v156,v158
v_cvt_pk_bf16_f32 v155,v157,v159
v_mfma_f32_16x16x32_bf16 v[248:251],v[108:111],v[144:147],v[248:251]
s_setprio 2
v_fma_mixhi_f16 v120,v142,s99,0
v_fma_mixhi_f16 v122,v143,s99,0
v_mfma_f32_16x16x32_bf16 v[212:215],v[88:91],v[152:155],v[212:215]
ds_read_u16_d16_hi v131,v131
v_lshlrev_b32_sdwa v133,1,v133 dst_sel:DWORD dst_unused:UNUSED_PAD src0_sel:DWORD src1_sel:WORD_1
v_lshlrev_b32_sdwa v135,1,v135 dst_sel:DWORD dst_unused:UNUSED_PAD src0_sel:DWORD src1_sel:WORD_1
v_mfma_f32_16x16x32_bf16 v[220:223],v[92:95],v[152:155],v[220:223]
ds_read_b64_tr_b16 v[88:89],v6 offset:18480
v_pk_fma_f16 v121,v120,v120,s54
v_pk_fma_f16 v123,v122,v122,s54
v_mfma_f32_16x16x32_bf16 v[228:231],v[96:99],v[152:155],v[228:231]
ds_read_u16 v132,v132
v_pk_mul_f16 v121,v120,v121
v_pk_mul_f16 v123,v122,v123
v_mfma_f32_16x16x32_bf16 v[236:239],v[100:103],v[152:155],v[236:239]
ds_read_b64_tr_b16 v[90:91],v6 offset:20016
v_lshlrev_b32_sdwa v136,1,v136 dst_sel:DWORD dst_unused:UNUSED_PAD src0_sel:DWORD src1_sel:WORD_1
v_lshlrev_b32_sdwa v138,1,v138 dst_sel:DWORD dst_unused:UNUSED_PAD src0_sel:DWORD src1_sel:WORD_1
v_mfma_f32_16x16x32_bf16 v[244:247],v[104:107],v[152:155],v[244:247]
ds_read_u16_d16_hi v134,v134
v_exp_f16 v120,v121
v_mfma_f32_16x16x32_bf16 v[252:255],v[108:111],v[152:155],v[252:255]
ds_read_b64_tr_b16 v[92:93],v6 offset:18488
v_exp_f16 v122,v123
v_mfma_f32_16x16x32_bf16 v[144:147],v[64:67],v[40:43],v[112:115]
ds_read_u16 v133,v133
v_lshlrev_b32_sdwa v137,1,v137 dst_sel:DWORD dst_unused:UNUSED_PAD src0_sel:DWORD src1_sel:WORD_1
v_lshlrev_b32_sdwa v139,1,v139 dst_sel:DWORD dst_unused:UNUSED_PAD src0_sel:DWORD src1_sel:WORD_1
v_mfma_f32_16x16x32_bf16 v[152:155],v[64:67],v[44:47],v[112:115]
ds_read_b64_tr_b16 v[94:95],v6 offset:20024
v_exp_f16_sdwa v120,v121 dst_sel:WORD_1 dst_unused:UNUSED_PRESERVE src0_sel:WORD_1
v_mfma_f32_16x16x32_bf16 v[156:159],v[68:71],v[44:47],v[116:119]
ds_read_u16_d16_hi v135,v135
v_exp_f16_sdwa v122,v123 dst_sel:WORD_1 dst_unused:UNUSED_PRESERVE src0_sel:WORD_1
v_mfma_f32_16x16x32_bf16 v[148:151],v[68:71],v[40:43],v[116:119]
ds_read_b64_tr_b16 v[96:97],v6 offset:18544
v_pk_fma_f16 v120,v120,s55,s55
v_pk_fma_f16 v122,v122,s55,s55
v_mfma_f32_16x16x32_bf16 v[144:147],v[72:75],v[48:51],v[144:147]
ds_read_u16 v136,v136
v_rcp_f16 v121,v120
v_mfma_f32_16x16x32_bf16 v[152:155],v[72:75],v[52:55],v[152:155]
ds_read_b64_tr_b16 v[98:99],v6 offset:20080
ds_read_b64_tr_b16 v[100:101],v6 offset:18552
v_rcp_f16 v123,v122
v_mfma_f32_16x16x32_bf16 v[156:159],v[76:79],v[52:55],v[156:159]
ds_read_u16_d16_hi v138,v138
v_rcp_f16_sdwa v121,v120 dst_sel:WORD_1 dst_unused:UNUSED_PRESERVE src0_sel:WORD_1
v_mfma_f32_16x16x32_bf16 v[148:151],v[76:79],v[48:51],v[148:151]
ds_read_b64_tr_b16 v[102:103],v6 offset:20088
ds_read_b64_tr_b16 v[104:105],v6 offset:18608
v_rcp_f16_sdwa v123,v122 dst_sel:WORD_1 dst_unused:UNUSED_PRESERVE src0_sel:WORD_1
v_mfma_f32_16x16x32_bf16 v[144:147],v[80:83],v[56:59],v[144:147]
ds_read_u16 v137,v137
v_fma_mix_f32 v140,v140,v121,0 op_sel_hi:[0,1,0]
v_fma_mix_f32 v142,v142,v121,0 op_sel:[0,1,0] op_sel_hi:[0,1,0]
v_mfma_f32_16x16x32_bf16 v[152:155],v[80:83],v[60:63],v[152:155]
s_waitcnt lgkmcnt(8)
ds_read_b64_tr_b16 v[106:107],v6 offset:20144
ds_read_b64_tr_b16 v[108:109],v6 offset:18616
v_lshl_add_u64 v[128:129],v[128:129],0,v[130:131]
v_lshl_add_u64 v[130:131],v[132:133],0,v[134:135]
v_mfma_f32_16x16x32_bf16 v[156:159],v[84:87],v[60:63],v[156:159]
ds_read_u16_d16_hi v139,v139
v_fma_mix_f32 v141,v141,v123,0 op_sel_hi:[0,1,0]
v_fma_mix_f32 v143,v143,v123,0 op_sel:[0,1,0] op_sel_hi:[0,1,0]
v_mfma_f32_16x16x32_bf16 v[148:151],v[84:87],v[56:59],v[148:151]
ds_read_b64_tr_b16 v[110:111],v6 offset:20152
ds_read_b64_tr_b16 v[64:65],v4 offset:12832
v_lshlrev_b32_sdwa v144,1,v144 dst_sel:DWORD dst_unused:UNUSED_PAD src0_sel:DWORD src1_sel:WORD_1
v_lshlrev_b32_sdwa v146,1,v146 dst_sel:DWORD dst_unused:UNUSED_PAD src0_sel:DWORD src1_sel:WORD_1
v_mfma_f32_16x16x32_bf16 v[160:163],v[88:91],v[128:131],v[160:163]
ds_read_u16 v144,v144
v_lshlrev_b32_sdwa v145,1,v145 dst_sel:DWORD dst_unused:UNUSED_PAD src0_sel:DWORD src1_sel:WORD_1
v_lshlrev_b32_sdwa v147,1,v147 dst_sel:DWORD dst_unused:UNUSED_PAD src0_sel:DWORD src1_sel:WORD_1
v_mfma_f32_16x16x32_bf16 v[168:171],v[92:95],v[128:131],v[168:171]
s_waitcnt lgkmcnt(11)
ds_read_b64_tr_b16 v[66:67],v4 offset:14880
ds_read_b128 v[112:115],v7 offset:128
v_fma_mixlo_f16 v120,v156,s99,0
v_fma_mixlo_f16 v122,v157,s99,0
v_mfma_f32_16x16x32_bf16 v[176:179],v[96:99],v[128:131],v[176:179]
s_waitcnt lgkmcnt(10)
ds_read_u16_d16_hi v146,v146
v_lshlrev_b32_sdwa v148,1,v148 dst_sel:DWORD dst_unused:UNUSED_PAD src0_sel:DWORD src1_sel:WORD_1
v_lshlrev_b32_sdwa v150,1,v150 dst_sel:DWORD dst_unused:UNUSED_PAD src0_sel:DWORD src1_sel:WORD_1
v_mfma_f32_16x16x32_bf16 v[184:187],v[100:103],v[128:131],v[184:187]
s_waitcnt lgkmcnt(6)
ds_read_b64_tr_b16 v[68:69],v5 offset:12832
ds_read_b64_tr_b16 v[70:71],v5 offset:14880
v_lshl_add_u64 v[136:137],v[136:137],0,v[138:139]
v_mfma_f32_16x16x32_bf16 v[192:195],v[104:107],v[128:131],v[192:195]
s_waitcnt lgkmcnt(7)
ds_read_u16 v145,v145
v_cvt_pk_bf16_f32 v138,v140,v142
v_cvt_pk_bf16_f32 v139,v141,v143
v_mfma_f32_16x16x32_bf16 v[200:203],v[108:111],v[128:131],v[200:203]
ds_read_b128 v[116:119],v7 offset:160
ds_read_b64_tr_b16 v[72:73],v4 offset:16928
v_fma_mixhi_f16 v120,v158,s99,0
v_fma_mixhi_f16 v122,v159,s99,0
v_mfma_f32_16x16x32_bf16 v[164:167],v[88:91],v[136:139],v[164:167]
ds_read_u16_d16_hi v147,v147
v_lshlrev_b32_sdwa v149,1,v149 dst_sel:DWORD dst_unused:UNUSED_PAD src0_sel:DWORD src1_sel:WORD_1
v_lshlrev_b32_sdwa v151,1,v151 dst_sel:DWORD dst_unused:UNUSED_PAD src0_sel:DWORD src1_sel:WORD_1
v_mfma_f32_16x16x32_bf16 v[172:175],v[92:95],v[136:139],v[172:175]
s_setprio 1
ds_read_b64_tr_b16 v[74:75],v4 offset:18976
ds_read_b64_tr_b16 v[76:77],v5 offset:16928
v_pk_fma_f16 v121,v120,v120,s54
v_pk_fma_f16 v123,v122,v122,s54
v_mfma_f32_16x16x32_bf16 v[180:183],v[96:99],v[136:139],v[180:183]
ds_read_u16 v148,v148
v_pk_mul_f16 v121,v120,v121
v_pk_mul_f16 v123,v122,v123
v_mfma_f32_16x16x32_bf16 v[188:191],v[100:103],v[136:139],v[188:191]
ds_read_b64_tr_b16 v[78:79],v5 offset:18976
v_lshlrev_b32_sdwa v152,1,v152 dst_sel:DWORD dst_unused:UNUSED_PAD src0_sel:DWORD src1_sel:WORD_1
v_lshlrev_b32_sdwa v154,1,v154 dst_sel:DWORD dst_unused:UNUSED_PAD src0_sel:DWORD src1_sel:WORD_1
v_mfma_f32_16x16x32_bf16 v[196:199],v[104:107],v[136:139],v[196:199]
ds_read_u16_d16_hi v150,v150
v_exp_f16 v120,v121
v_mfma_f32_16x16x32_bf16 v[204:207],v[108:111],v[136:139],v[204:207]
s_waitcnt lgkmcnt(12)
ds_read_b64_tr_b16 v[80:81],v4 offset:21024
v_exp_f16 v122,v123
v_mfma_f32_16x16x32_bf16 v[128:131],v[64:67],v[16:19],v[112:115]
ds_read_u16 v149,v149
v_lshlrev_b32_sdwa v153,1,v153 dst_sel:DWORD dst_unused:UNUSED_PAD src0_sel:DWORD src1_sel:WORD_1
v_lshlrev_b32_sdwa v155,1,v155 dst_sel:DWORD dst_unused:UNUSED_PAD src0_sel:DWORD src1_sel:WORD_1
v_mfma_f32_16x16x32_bf16 v[136:139],v[64:67],v[20:23],v[112:115]
s_waitcnt lgkmcnt(9)
ds_read_b64_tr_b16 v[82:83],v4 offset:23072
v_exp_f16_sdwa v120,v121 dst_sel:WORD_1 dst_unused:UNUSED_PRESERVE src0_sel:WORD_1
v_mfma_f32_16x16x32_bf16 v[140:143],v[68:71],v[20:23],v[116:119]
ds_read_u16_d16_hi v151,v151
v_exp_f16_sdwa v122,v123 dst_sel:WORD_1 dst_unused:UNUSED_PRESERVE src0_sel:WORD_1
v_mfma_f32_16x16x32_bf16 v[132:135],v[68:71],v[16:19],v[116:119]
s_waitcnt lgkmcnt(8)
ds_read_b64_tr_b16 v[84:85],v5 offset:21024
v_pk_fma_f16 v120,v120,s55,s55
v_pk_fma_f16 v122,v122,s55,s55
v_mfma_f32_16x16x32_bf16 v[128:131],v[72:75],v[24:27],v[128:131]
ds_read_u16 v152,v152
v_rcp_f16 v121,v120
v_mfma_f32_16x16x32_bf16 v[136:139],v[72:75],v[28:31],v[136:139]
s_waitcnt lgkmcnt(7)
ds_read_b64_tr_b16 v[86:87],v5 offset:23072
v_rcp_f16 v123,v122
v_mfma_f32_16x16x32_bf16 v[140:143],v[76:79],v[28:31],v[140:143]
ds_read_u16_d16_hi v154,v154
v_rcp_f16_sdwa v121,v120 dst_sel:WORD_1 dst_unused:UNUSED_PRESERVE src0_sel:WORD_1
v_mfma_f32_16x16x32_bf16 v[132:135],v[76:79],v[24:27],v[132:135]
s_waitcnt lgkmcnt(5)
v_rcp_f16_sdwa v123,v122 dst_sel:WORD_1 dst_unused:UNUSED_PRESERVE src0_sel:WORD_1
v_mfma_f32_16x16x32_bf16 v[128:131],v[80:83],v[32:35],v[128:131]
ds_read_u16 v153,v153
v_fma_mix_f32 v156,v156,v121,0 op_sel_hi:[0,1,0]
v_fma_mix_f32 v158,v158,v121,0 op_sel:[0,1,0] op_sel_hi:[0,1,0]
v_mfma_f32_16x16x32_bf16 v[136:139],v[80:83],v[36:39],v[136:139]
s_waitcnt lgkmcnt(2)
s_setprio 0
v_lshl_add_u64 v[144:145],v[144:145],0,v[146:147]
v_lshl_add_u64 v[146:147],v[148:149],0,v[150:151]
v_mfma_f32_16x16x32_bf16 v[140:143],v[84:87],v[36:39],v[140:143]
ds_read_u16_d16_hi v155,v155
v_fma_mix_f32 v157,v157,v123,0 op_sel_hi:[0,1,0]
v_fma_mix_f32 v159,v159,v123,0 op_sel:[0,1,0] op_sel_hi:[0,1,0]
s_nop 0
s_mov_b32 s86,0xbe801d5a
v_mfma_f32_16x16x32_bf16 v[132:135],v[84:87],v[32:35],v[132:135]
v_lshlrev_b32_sdwa v128,1,v128 dst_sel:DWORD dst_unused:UNUSED_PAD src0_sel:DWORD src1_sel:WORD_1
v_lshlrev_b32_sdwa v130,1,v130 dst_sel:DWORD dst_unused:UNUSED_PAD src0_sel:DWORD src1_sel:WORD_1
v_mfma_f32_16x16x32_bf16 v[208:211],v[88:91],v[144:147],v[208:211]
s_barrier
s_setprio 2
ds_read_u16 v128,v128
v_lshlrev_b32_sdwa v129,1,v129 dst_sel:DWORD dst_unused:UNUSED_PAD src0_sel:DWORD src1_sel:WORD_1
v_mfma_f32_16x16x32_bf16 v[216:219],v[92:95],v[144:147],v[216:219]
ds_read_u16_d16_hi v130,v130
v_lshlrev_b32_sdwa v131,1,v131 dst_sel:DWORD dst_unused:UNUSED_PAD src0_sel:DWORD src1_sel:WORD_1
v_mfma_f32_16x16x32_bf16 v[224:227],v[96:99],v[144:147],v[224:227]
s_cbranch_vccz RB48_%=
s_add_u32 m0,s58,12320
s_mul_i32 s69,s59,3
s_add_u32 s69,s69,s97
buffer_load_dwordx4 v2,s[44:47],s69 offen lds
s_add_u32 m0,m0,1024
s_add_u32 s69,s69,s53
buffer_load_dwordx4 v3,s[44:47],s69 offen lds
RB48_%=:
v_lshlrev_b32_sdwa v132,1,v132 dst_sel:DWORD dst_unused:UNUSED_PAD src0_sel:DWORD src1_sel:WORD_1
v_mfma_f32_16x16x32_bf16 v[232:235],v[100:103],v[144:147],v[232:235]
s_waitcnt lgkmcnt(2)
ds_read_u16 v129,v129
v_lshl_add_u64 v[152:153],v[152:153],0,v[154:155]
v_lshlrev_b32_sdwa v134,1,v134 dst_sel:DWORD dst_unused:UNUSED_PAD src0_sel:DWORD src1_sel:WORD_1
v_mfma_f32_16x16x32_bf16 v[240:243],v[104:107],v[144:147],v[240:243]
ds_read_u16_d16_hi v131,v131
v_cvt_pk_bf16_f32 v154,v156,v158
v_cvt_pk_bf16_f32 v155,v157,v159
v_mfma_f32_16x16x32_bf16 v[248:251],v[108:111],v[144:147],v[248:251]
v_lshlrev_b32_sdwa v133,1,v133 dst_sel:DWORD dst_unused:UNUSED_PAD src0_sel:DWORD src1_sel:WORD_1
v_mfma_f32_16x16x32_bf16 v[212:215],v[88:91],v[152:155],v[212:215]
s_cbranch_vccz RB49_%=
s_add_u32 m0,m0,1024
s_add_u32 s69,s69,s53
buffer_load_dwordx4 v2,s[44:47],s69 offen lds
s_add_u32 m0,m0,s50
s_add_u32 s69,s69,s53
buffer_load_dwordx4 v3,s[44:47],s69 offen lds
RB49_%=:
ds_read_u16 v132,v132
v_lshlrev_b32_sdwa v135,1,v135 dst_sel:DWORD dst_unused:UNUSED_PAD src0_sel:DWORD src1_sel:WORD_1
v_mfma_f32_16x16x32_bf16 v[220:223],v[92:95],v[152:155],v[220:223]
ds_read_u16_d16_hi v134,v134
v_lshlrev_b32_sdwa v136,1,v136 dst_sel:DWORD dst_unused:UNUSED_PAD src0_sel:DWORD src1_sel:WORD_1
v_mfma_f32_16x16x32_bf16 v[228:231],v[96:99],v[152:155],v[228:231]
ds_read_b64_tr_b16 v[88:89],v6 offset:0
ds_read_b64_tr_b16 v[90:91],v6 offset:1536
v_lshlrev_b32_sdwa v138,1,v138 dst_sel:DWORD dst_unused:UNUSED_PAD src0_sel:DWORD src1_sel:WORD_1
v_mfma_f32_16x16x32_bf16 v[236:239],v[100:103],v[152:155],v[236:239]
ds_read_u16 v133,v133
v_lshlrev_b32_sdwa v137,1,v137 dst_sel:DWORD dst_unused:UNUSED_PAD src0_sel:DWORD src1_sel:WORD_1
v_mfma_f32_16x16x32_bf16 v[244:247],v[104:107],v[152:155],v[244:247]
ds_read_u16_d16_hi v135,v135
v_lshlrev_b32_sdwa v139,1,v139 dst_sel:DWORD dst_unused:UNUSED_PAD src0_sel:DWORD src1_sel:WORD_1
v_mfma_f32_16x16x32_bf16 v[252:255],v[108:111],v[152:155],v[252:255]
s_cbranch_vccz RB50_%=
s_add_u32 m0,m0,1024
s_add_u32 s69,s69,s53
buffer_load_dwordx4 v2,s[44:47],s69 offen lds
s_add_u32 m0,m0,1024
s_add_u32 s69,s69,s53
buffer_load_dwordx4 v3,s[44:47],s69 offen lds
RB50_%=:
ds_read_b64_tr_b16 v[92:93],v6 offset:8
ds_read_b64_tr_b16 v[94:95],v6 offset:1544
v_lshlrev_b32_sdwa v140,1,v140 dst_sel:DWORD dst_unused:UNUSED_PAD src0_sel:DWORD src1_sel:WORD_1
v_mfma_f32_16x16x32_bf16 v[144:147],v[64:67],v[40:43],v[112:115]
ds_read_u16 v136,v136
v_lshlrev_b32_sdwa v142,1,v142 dst_sel:DWORD dst_unused:UNUSED_PAD src0_sel:DWORD src1_sel:WORD_1
v_mfma_f32_16x16x32_bf16 v[152:155],v[64:67],v[44:47],v[112:115]
ds_read_u16_d16_hi v138,v138
v_lshlrev_b32_sdwa v141,1,v141 dst_sel:DWORD dst_unused:UNUSED_PAD src0_sel:DWORD src1_sel:WORD_1
v_mfma_f32_16x16x32_bf16 v[156:159],v[68:71],v[44:47],v[116:119]
ds_read_b64_tr_b16 v[96:97],v6 offset:64
ds_read_b64_tr_b16 v[98:99],v6 offset:1600
v_lshlrev_b32_sdwa v143,1,v143 dst_sel:DWORD dst_unused:UNUSED_PAD src0_sel:DWORD src1_sel:WORD_1
v_mfma_f32_16x16x32_bf16 v[148:151],v[68:71],v[40:43],v[116:119]
ds_read_u16 v137,v137
v_mfma_f32_16x16x32_bf16 v[144:147],v[72:75],v[48:51],v[144:147]
ds_read_u16_d16_hi v139,v139
v_mfma_f32_16x16x32_bf16 v[152:155],v[72:75],v[52:55],v[152:155]
s_cbranch_vccz RB51_%=
s_add_u32 m0,m0,s50
s_add_u32 s69,s69,s53
buffer_load_dwordx4 v2,s[44:47],s69 offen lds
s_add_u32 m0,m0,1024
s_add_u32 s69,s69,s53
buffer_load_dwordx4 v3,s[44:47],s69 offen lds
RB51_%=:
ds_read_b64_tr_b16 v[100:101],v6 offset:72
ds_read_b64_tr_b16 v[102:103],v6 offset:1608
v_mfma_f32_16x16x32_bf16 v[156:159],v[76:79],v[52:55],v[156:159]
ds_read_u16 v140,v140
v_mfma_f32_16x16x32_bf16 v[148:151],v[76:79],v[48:51],v[148:151]
s_waitcnt lgkmcnt(15)
ds_read_u16_d16_hi v142,v142
v_lshl_add_u64 v[128:129],v[128:129],0,v[130:131]
v_mfma_f32_16x16x32_bf16 v[144:147],v[80:83],v[56:59],v[144:147]
s_waitcnt lgkmcnt(12)
ds_read_b64_tr_b16 v[104:105],v6 offset:128
ds_read_b64_tr_b16 v[106:107],v6 offset:1664
v_lshl_add_u64 v[130:131],v[132:133],0,v[134:135]
v_mfma_f32_16x16x32_bf16 v[152:155],v[80:83],v[60:63],v[152:155]
ds_read_u16 v141,v141
v_mfma_f32_16x16x32_bf16 v[156:159],v[84:87],v[60:63],v[156:159]
ds_read_u16_d16_hi v143,v143
v_mfma_f32_16x16x32_bf16 v[148:151],v[84:87],v[56:59],v[148:151]
s_cbranch_vccz RB52_%=
s_add_u32 m0,m0,1024
s_add_u32 s69,s69,s53
buffer_load_dwordx4 v2,s[44:47],s69 offen lds
s_add_u32 m0,m0,s50
s_add_u32 s69,s69,s53
buffer_load_dwordx4 v3,s[44:47],s69 offen lds
RB52_%=:
ds_read_b64_tr_b16 v[108:109],v6 offset:136
ds_read_b64_tr_b16 v[110:111],v6 offset:1672
v_lshlrev_b32_sdwa v144,1,v144 dst_sel:DWORD dst_unused:UNUSED_PAD src0_sel:DWORD src1_sel:WORD_1
v_lshlrev_b32_sdwa v146,1,v146 dst_sel:DWORD dst_unused:UNUSED_PAD src0_sel:DWORD src1_sel:WORD_1
v_mfma_f32_16x16x32_bf16 v[160:163],v[88:91],v[128:131],v[160:163]
s_waitcnt lgkmcnt(15)
ds_read_u16 v144,v144
v_lshlrev_b32_sdwa v145,1,v145 dst_sel:DWORD dst_unused:UNUSED_PAD src0_sel:DWORD src1_sel:WORD_1
v_mfma_f32_16x16x32_bf16 v[168:171],v[92:95],v[128:131],v[168:171]
s_waitcnt lgkmcnt(13)
ds_read_u16_d16_hi v146,v146
v_lshlrev_b32_sdwa v147,1,v147 dst_sel:DWORD dst_unused:UNUSED_PAD src0_sel:DWORD src1_sel:WORD_1
v_mfma_f32_16x16x32_bf16 v[176:179],v[96:99],v[128:131],v[176:179]
s_waitcnt lgkmcnt(10)
ds_read_b64_tr_b16 v[64:65],v4 offset:0
ds_read_b64_tr_b16 v[66:67],v4 offset:2048
v_lshlrev_b32_sdwa v148,1,v148 dst_sel:DWORD dst_unused:UNUSED_PAD src0_sel:DWORD src1_sel:WORD_1
v_mfma_f32_16x16x32_bf16 v[184:187],v[100:103],v[128:131],v[184:187]
s_waitcnt lgkmcnt(8)
ds_read_u16 v145,v145
v_lshl_add_u64 v[136:137],v[136:137],0,v[138:139]
v_lshlrev_b32_sdwa v150,1,v150 dst_sel:DWORD dst_unused:UNUSED_PAD src0_sel:DWORD src1_sel:WORD_1
v_mfma_f32_16x16x32_bf16 v[192:195],v[104:107],v[128:131],v[192:195]
s_waitcnt lgkmcnt(5)
s_cbranch_vccz RB53_%=
s_add_u32 m0,m0,1024
s_add_u32 s69,s69,s53
buffer_load_dwordx4 v2,s[44:47],s69 offen lds
s_add_u32 m0,m0,1024
s_add_u32 s69,s69,s53
buffer_load_dwordx4 v3,s[44:47],s69 offen lds
RB53_%=:
ds_read_u16_d16_hi v147,v147
v_lshl_add_u64 v[138:139],v[140:141],0,v[142:143]
v_mfma_f32_16x16x32_bf16 v[200:203],v[108:111],v[128:131],v[200:203]
ds_read_b128 v[112:115],v7 offset:256
ds_read_b64_tr_b16 v[68:69],v5 offset:0
v_lshlrev_b32_sdwa v149,1,v149 dst_sel:DWORD dst_unused:UNUSED_PAD src0_sel:DWORD src1_sel:WORD_1
v_mfma_f32_16x16x32_bf16 v[164:167],v[88:91],v[136:139],v[164:167]
ds_read_u16 v148,v148
v_lshlrev_b32_sdwa v151,1,v151 dst_sel:DWORD dst_unused:UNUSED_PAD src0_sel:DWORD src1_sel:WORD_1
v_mfma_f32_16x16x32_bf16 v[172:175],v[92:95],v[136:139],v[172:175]
ds_read_u16_d16_hi v150,v150
ds_read_b64_tr_b16 v[70:71],v5 offset:2048
v_lshlrev_b32_sdwa v152,1,v152 dst_sel:DWORD dst_unused:UNUSED_PAD src0_sel:DWORD src1_sel:WORD_1
v_mfma_f32_16x16x32_bf16 v[180:183],v[96:99],v[136:139],v[180:183]
ds_read_b128 v[116:119],v7 offset:288
ds_read_b64_tr_b16 v[72:73],v4 offset:4096
v_lshlrev_b32_sdwa v154,1,v154 dst_sel:DWORD dst_unused:UNUSED_PAD src0_sel:DWORD src1_sel:WORD_1
v_mfma_f32_16x16x32_bf16 v[188:191],v[100:103],v[136:139],v[188:191]
ds_read_u16 v149,v149
v_lshlrev_b32_sdwa v153,1,v153 dst_sel:DWORD dst_unused:UNUSED_PAD src0_sel:DWORD src1_sel:WORD_1
v_mfma_f32_16x16x32_bf16 v[196:199],v[104:107],v[136:139],v[196:199]
ds_read_u16_d16_hi v151,v151
v_lshlrev_b32_sdwa v155,1,v155 dst_sel:DWORD dst_unused:UNUSED_PAD src0_sel:DWORD src1_sel:WORD_1
v_mfma_f32_16x16x32_bf16 v[204:207],v[108:111],v[136:139],v[204:207]
s_waitcnt lgkmcnt(8)
ds_read_b64_tr_b16 v[74:75],v4 offset:6144
ds_read_b64_tr_b16 v[76:77],v5 offset:4096
v_lshlrev_b32_sdwa v156,1,v156 dst_sel:DWORD dst_unused:UNUSED_PAD src0_sel:DWORD src1_sel:WORD_1
v_mfma_f32_16x16x32_bf16 v[128:131],v[64:67],v[16:19],v[112:115]
ds_read_u16 v152,v152
v_lshlrev_b32_sdwa v158,1,v158 dst_sel:DWORD dst_unused:UNUSED_PAD src0_sel:DWORD src1_sel:WORD_1
v_mfma_f32_16x16x32_bf16 v[136:139],v[64:67],v[20:23],v[112:115]
s_waitcnt lgkmcnt(6)
ds_read_u16_d16_hi v154,v154
ds_read_b64_tr_b16 v[78:79],v5 offset:6144
v_lshlrev_b32_sdwa v157,1,v157 dst_sel:DWORD dst_unused:UNUSED_PAD src0_sel:DWORD src1_sel:WORD_1
v_mfma_f32_16x16x32_bf16 v[140:143],v[68:71],v[20:23],v[116:119]
ds_read_b64_tr_b16 v[80:81],v4 offset:8192
ds_read_b64_tr_b16 v[82:83],v4 offset:10240
v_lshlrev_b32_sdwa v159,1,v159 dst_sel:DWORD dst_unused:UNUSED_PAD src0_sel:DWORD src1_sel:WORD_1
v_mfma_f32_16x16x32_bf16 v[132:135],v[68:71],v[16:19],v[116:119]
s_waitcnt lgkmcnt(6)
ds_read_u16 v153,v153
v_mfma_f32_16x16x32_bf16 v[128:131],v[72:75],v[24:27],v[128:131]
ds_read_u16_d16_hi v155,v155
v_mfma_f32_16x16x32_bf16 v[136:139],v[72:75],v[28:31],v[136:139]
s_waitcnt lgkmcnt(4)
ds_read_b64_tr_b16 v[84:85],v5 offset:8192
ds_read_b64_tr_b16 v[86:87],v5 offset:10240
v_mfma_f32_16x16x32_bf16 v[140:143],v[76:79],v[28:31],v[140:143]
ds_read_u16 v156,v156
v_mfma_f32_16x16x32_bf16 v[132:135],v[76:79],v[24:27],v[132:135]
s_waitcnt lgkmcnt(5)
ds_read_u16_d16_hi v158,v158
v_lshl_add_u64 v[144:145],v[144:145],0,v[146:147]
v_mfma_f32_16x16x32_bf16 v[128:131],v[80:83],v[32:35],v[128:131]
v_lshl_add_u64 v[146:147],v[148:149],0,v[150:151]
v_mfma_f32_16x16x32_bf16 v[136:139],v[80:83],v[36:39],v[136:139]
s_waitcnt lgkmcnt(2)
ds_read_u16 v157,v157
v_mfma_f32_16x16x32_bf16 v[140:143],v[84:87],v[36:39],v[140:143]
ds_read_u16_d16_hi v159,v159
v_mfma_f32_16x16x32_bf16 v[132:135],v[84:87],v[32:35],v[132:135]
s_nop 0
v_lshlrev_b32_sdwa v128,1,v128 dst_sel:DWORD dst_unused:UNUSED_PAD src0_sel:DWORD src1_sel:WORD_1
v_lshlrev_b32_sdwa v130,1,v130 dst_sel:DWORD dst_unused:UNUSED_PAD src0_sel:DWORD src1_sel:WORD_1
v_mfma_f32_16x16x32_bf16 v[208:211],v[88:91],v[144:147],v[208:211]
ds_read_u16 v128,v128
v_lshlrev_b32_sdwa v129,1,v129 dst_sel:DWORD dst_unused:UNUSED_PAD src0_sel:DWORD src1_sel:WORD_1
v_mfma_f32_16x16x32_bf16 v[216:219],v[92:95],v[144:147],v[216:219]
ds_read_u16_d16_hi v130,v130
v_lshlrev_b32_sdwa v131,1,v131 dst_sel:DWORD dst_unused:UNUSED_PAD src0_sel:DWORD src1_sel:WORD_1
v_mfma_f32_16x16x32_bf16 v[224:227],v[96:99],v[144:147],v[224:227]
v_lshlrev_b32_sdwa v132,1,v132 dst_sel:DWORD dst_unused:UNUSED_PAD src0_sel:DWORD src1_sel:WORD_1
v_mfma_f32_16x16x32_bf16 v[232:235],v[100:103],v[144:147],v[232:235]
ds_read_u16 v129,v129
v_lshl_add_u64 v[152:153],v[152:153],0,v[154:155]
v_lshlrev_b32_sdwa v134,1,v134 dst_sel:DWORD dst_unused:UNUSED_PAD src0_sel:DWORD src1_sel:WORD_1
v_mfma_f32_16x16x32_bf16 v[240:243],v[104:107],v[144:147],v[240:243]
s_waitcnt lgkmcnt(3)
ds_read_u16_d16_hi v131,v131
v_lshl_add_u64 v[154:155],v[156:157],0,v[158:159]
v_mfma_f32_16x16x32_bf16 v[248:251],v[108:111],v[144:147],v[248:251]
v_lshlrev_b32_sdwa v133,1,v133 dst_sel:DWORD dst_unused:UNUSED_PAD src0_sel:DWORD src1_sel:WORD_1
v_mfma_f32_16x16x32_bf16 v[212:215],v[88:91],v[152:155],v[212:215]
ds_read_u16 v132,v132
v_lshlrev_b32_sdwa v135,1,v135 dst_sel:DWORD dst_unused:UNUSED_PAD src0_sel:DWORD src1_sel:WORD_1
v_mfma_f32_16x16x32_bf16 v[220:223],v[92:95],v[152:155],v[220:223]
ds_read_u16_d16_hi v134,v134
v_lshlrev_b32_sdwa v136,1,v136 dst_sel:DWORD dst_unused:UNUSED_PAD src0_sel:DWORD src1_sel:WORD_1
v_mfma_f32_16x16x32_bf16 v[228:231],v[96:99],v[152:155],v[228:231]
ds_read_b64_tr_b16 v[88:89],v6 offset:6160
ds_read_b64_tr_b16 v[90:91],v6 offset:7696
v_lshlrev_b32_sdwa v138,1,v138 dst_sel:DWORD dst_unused:UNUSED_PAD src0_sel:DWORD src1_sel:WORD_1
v_mfma_f32_16x16x32_bf16 v[236:239],v[100:103],v[152:155],v[236:239]
ds_read_u16 v133,v133
v_lshlrev_b32_sdwa v137,1,v137 dst_sel:DWORD dst_unused:UNUSED_PAD src0_sel:DWORD src1_sel:WORD_1
v_mfma_f32_16x16x32_bf16 v[244:247],v[104:107],v[152:155],v[244:247]
ds_read_u16_d16_hi v135,v135
v_lshlrev_b32_sdwa v139,1,v139 dst_sel:DWORD dst_unused:UNUSED_PAD src0_sel:DWORD src1_sel:WORD_1
v_mfma_f32_16x16x32_bf16 v[252:255],v[108:111],v[152:155],v[252:255]
ds_read_b64_tr_b16 v[92:93],v6 offset:6168
ds_read_b64_tr_b16 v[94:95],v6 offset:7704
v_lshlrev_b32_sdwa v140,1,v140 dst_sel:DWORD dst_unused:UNUSED_PAD src0_sel:DWORD src1_sel:WORD_1
v_mfma_f32_16x16x32_bf16 v[144:147],v[64:67],v[40:43],v[112:115]
ds_read_u16 v136,v136
v_lshlrev_b32_sdwa v142,1,v142 dst_sel:DWORD dst_unused:UNUSED_PAD src0_sel:DWORD src1_sel:WORD_1
v_mfma_f32_16x16x32_bf16 v[152:155],v[64:67],v[44:47],v[112:115]
ds_read_u16_d16_hi v138,v138
v_lshlrev_b32_sdwa v141,1,v141 dst_sel:DWORD dst_unused:UNUSED_PAD src0_sel:DWORD src1_sel:WORD_1
v_mfma_f32_16x16x32_bf16 v[156:159],v[68:71],v[44:47],v[116:119]
ds_read_b64_tr_b16 v[96:97],v6 offset:6224
ds_read_b64_tr_b16 v[98:99],v6 offset:7760
v_lshlrev_b32_sdwa v143,1,v143 dst_sel:DWORD dst_unused:UNUSED_PAD src0_sel:DWORD src1_sel:WORD_1
v_mfma_f32_16x16x32_bf16 v[148:151],v[68:71],v[40:43],v[116:119]
ds_read_u16 v137,v137
v_mfma_f32_16x16x32_bf16 v[144:147],v[72:75],v[48:51],v[144:147]
ds_read_u16_d16_hi v139,v139
v_mfma_f32_16x16x32_bf16 v[152:155],v[72:75],v[52:55],v[152:155]
ds_read_b64_tr_b16 v[100:101],v6 offset:6232
ds_read_b64_tr_b16 v[102:103],v6 offset:7768
v_mfma_f32_16x16x32_bf16 v[156:159],v[76:79],v[52:55],v[156:159]
ds_read_u16 v140,v140
v_mfma_f32_16x16x32_bf16 v[148:151],v[76:79],v[48:51],v[148:151]
s_waitcnt lgkmcnt(15)
ds_read_u16_d16_hi v142,v142
v_lshl_add_u64 v[128:129],v[128:129],0,v[130:131]
v_mfma_f32_16x16x32_bf16 v[144:147],v[80:83],v[56:59],v[144:147]
s_waitcnt lgkmcnt(12)
ds_read_b64_tr_b16 v[104:105],v6 offset:6288
ds_read_b64_tr_b16 v[106:107],v6 offset:7824
v_lshl_add_u64 v[130:131],v[132:133],0,v[134:135]
v_mfma_f32_16x16x32_bf16 v[152:155],v[80:83],v[60:63],v[152:155]
ds_read_u16 v141,v141
v_mfma_f32_16x16x32_bf16 v[156:159],v[84:87],v[60:63],v[156:159]
ds_read_u16_d16_hi v143,v143
v_mfma_f32_16x16x32_bf16 v[148:151],v[84:87],v[56:59],v[148:151]
ds_read_b64_tr_b16 v[108:109],v6 offset:6296
ds_read_b64_tr_b16 v[110:111],v6 offset:7832
v_lshlrev_b32_sdwa v144,1,v144 dst_sel:DWORD dst_unused:UNUSED_PAD src0_sel:DWORD src1_sel:WORD_1
v_lshlrev_b32_sdwa v146,1,v146 dst_sel:DWORD dst_unused:UNUSED_PAD src0_sel:DWORD src1_sel:WORD_1
v_mfma_f32_16x16x32_bf16 v[160:163],v[88:91],v[128:131],v[160:163]
s_waitcnt lgkmcnt(15)
ds_read_u16 v144,v144
v_lshlrev_b32_sdwa v145,1,v145 dst_sel:DWORD dst_unused:UNUSED_PAD src0_sel:DWORD src1_sel:WORD_1
v_mfma_f32_16x16x32_bf16 v[168:171],v[92:95],v[128:131],v[168:171]
s_waitcnt lgkmcnt(13)
ds_read_u16_d16_hi v146,v146
v_lshlrev_b32_sdwa v147,1,v147 dst_sel:DWORD dst_unused:UNUSED_PAD src0_sel:DWORD src1_sel:WORD_1
v_mfma_f32_16x16x32_bf16 v[176:179],v[96:99],v[128:131],v[176:179]
s_waitcnt lgkmcnt(10)
ds_read_b64_tr_b16 v[64:65],v4 offset:512
ds_read_b64_tr_b16 v[66:67],v4 offset:2560
v_lshlrev_b32_sdwa v148,1,v148 dst_sel:DWORD dst_unused:UNUSED_PAD src0_sel:DWORD src1_sel:WORD_1
v_mfma_f32_16x16x32_bf16 v[184:187],v[100:103],v[128:131],v[184:187]
s_waitcnt lgkmcnt(8)
ds_read_u16 v145,v145
v_lshl_add_u64 v[136:137],v[136:137],0,v[138:139]
v_lshlrev_b32_sdwa v150,1,v150 dst_sel:DWORD dst_unused:UNUSED_PAD src0_sel:DWORD src1_sel:WORD_1
v_mfma_f32_16x16x32_bf16 v[192:195],v[104:107],v[128:131],v[192:195]
s_waitcnt lgkmcnt(5)
ds_read_u16_d16_hi v147,v147
v_lshl_add_u64 v[138:139],v[140:141],0,v[142:143]
v_mfma_f32_16x16x32_bf16 v[200:203],v[108:111],v[128:131],v[200:203]
ds_read_b128 v[112:115],v7 offset:384
ds_read_b64_tr_b16 v[68:69],v5 offset:512
v_lshlrev_b32_sdwa v149,1,v149 dst_sel:DWORD dst_unused:UNUSED_PAD src0_sel:DWORD src1_sel:WORD_1
v_mfma_f32_16x16x32_bf16 v[164:167],v[88:91],v[136:139],v[164:167]
ds_read_u16 v148,v148
v_lshlrev_b32_sdwa v151,1,v151 dst_sel:DWORD dst_unused:UNUSED_PAD src0_sel:DWORD src1_sel:WORD_1
v_mfma_f32_16x16x32_bf16 v[172:175],v[92:95],v[136:139],v[172:175]
ds_read_u16_d16_hi v150,v150
ds_read_b64_tr_b16 v[70:71],v5 offset:2560
v_lshlrev_b32_sdwa v152,1,v152 dst_sel:DWORD dst_unused:UNUSED_PAD src0_sel:DWORD src1_sel:WORD_1
v_mfma_f32_16x16x32_bf16 v[180:183],v[96:99],v[136:139],v[180:183]
ds_read_b128 v[116:119],v7 offset:416
ds_read_b64_tr_b16 v[72:73],v4 offset:4608
v_lshlrev_b32_sdwa v154,1,v154 dst_sel:DWORD dst_unused:UNUSED_PAD src0_sel:DWORD src1_sel:WORD_1
v_mfma_f32_16x16x32_bf16 v[188:191],v[100:103],v[136:139],v[188:191]
ds_read_u16 v149,v149
v_lshlrev_b32_sdwa v153,1,v153 dst_sel:DWORD dst_unused:UNUSED_PAD src0_sel:DWORD src1_sel:WORD_1
v_mfma_f32_16x16x32_bf16 v[196:199],v[104:107],v[136:139],v[196:199]
ds_read_u16_d16_hi v151,v151
v_lshlrev_b32_sdwa v155,1,v155 dst_sel:DWORD dst_unused:UNUSED_PAD src0_sel:DWORD src1_sel:WORD_1
v_mfma_f32_16x16x32_bf16 v[204:207],v[108:111],v[136:139],v[204:207]
s_waitcnt lgkmcnt(8)
ds_read_b64_tr_b16 v[74:75],v4 offset:6656
ds_read_b64_tr_b16 v[76:77],v5 offset:4608
v_lshlrev_b32_sdwa v156,1,v156 dst_sel:DWORD dst_unused:UNUSED_PAD src0_sel:DWORD src1_sel:WORD_1
v_mfma_f32_16x16x32_bf16 v[128:131],v[64:67],v[16:19],v[112:115]
ds_read_u16 v152,v152
v_lshlrev_b32_sdwa v158,1,v158 dst_sel:DWORD dst_unused:UNUSED_PAD src0_sel:DWORD src1_sel:WORD_1
v_mfma_f32_16x16x32_bf16 v[136:139],v[64:67],v[20:23],v[112:115]
s_waitcnt lgkmcnt(6)
ds_read_u16_d16_hi v154,v154
ds_read_b64_tr_b16 v[78:79],v5 offset:6656
v_lshlrev_b32_sdwa v157,1,v157 dst_sel:DWORD dst_unused:UNUSED_PAD src0_sel:DWORD src1_sel:WORD_1
v_mfma_f32_16x16x32_bf16 v[140:143],v[68:71],v[20:23],v[116:119]
ds_read_b64_tr_b16 v[80:81],v4 offset:8704
ds_read_b64_tr_b16 v[82:83],v4 offset:10752
v_lshlrev_b32_sdwa v159,1,v159 dst_sel:DWORD dst_unused:UNUSED_PAD src0_sel:DWORD src1_sel:WORD_1
v_mfma_f32_16x16x32_bf16 v[132:135],v[68:71],v[16:19],v[116:119]
s_waitcnt lgkmcnt(6)
ds_read_u16 v153,v153
v_mfma_f32_16x16x32_bf16 v[128:131],v[72:75],v[24:27],v[128:131]
ds_read_u16_d16_hi v155,v155
v_mfma_f32_16x16x32_bf16 v[136:139],v[72:75],v[28:31],v[136:139]
s_waitcnt lgkmcnt(4)
ds_read_b64_tr_b16 v[84:85],v5 offset:8704
ds_read_b64_tr_b16 v[86:87],v5 offset:10752
v_mfma_f32_16x16x32_bf16 v[140:143],v[76:79],v[28:31],v[140:143]
ds_read_u16 v156,v156
v_mfma_f32_16x16x32_bf16 v[132:135],v[76:79],v[24:27],v[132:135]
s_waitcnt lgkmcnt(5)
ds_read_u16_d16_hi v158,v158
v_lshl_add_u64 v[144:145],v[144:145],0,v[146:147]
v_mfma_f32_16x16x32_bf16 v[128:131],v[80:83],v[32:35],v[128:131]
v_lshl_add_u64 v[146:147],v[148:149],0,v[150:151]
v_mfma_f32_16x16x32_bf16 v[136:139],v[80:83],v[36:39],v[136:139]
s_waitcnt lgkmcnt(2)
ds_read_u16 v157,v157
v_mfma_f32_16x16x32_bf16 v[140:143],v[84:87],v[36:39],v[140:143]
ds_read_u16_d16_hi v159,v159
v_mfma_f32_16x16x32_bf16 v[132:135],v[84:87],v[32:35],v[132:135]
s_nop 0
v_lshlrev_b32_sdwa v128,1,v128 dst_sel:DWORD dst_unused:UNUSED_PAD src0_sel:DWORD src1_sel:WORD_1
v_lshlrev_b32_sdwa v130,1,v130 dst_sel:DWORD dst_unused:UNUSED_PAD src0_sel:DWORD src1_sel:WORD_1
v_mfma_f32_16x16x32_bf16 v[208:211],v[88:91],v[144:147],v[208:211]
s_waitcnt vmcnt(0)
s_barrier
s_setprio 3
ds_read_u16 v128,v128
v_lshlrev_b32_sdwa v129,1,v129 dst_sel:DWORD dst_unused:UNUSED_PAD src0_sel:DWORD src1_sel:WORD_1
v_lshlrev_b32_sdwa v131,1,v131 dst_sel:DWORD dst_unused:UNUSED_PAD src0_sel:DWORD src1_sel:WORD_1
v_mfma_f32_16x16x32_bf16 v[216:219],v[92:95],v[144:147],v[216:219]
v_fma_mixlo_f16 v120,v140,s99,0
v_fma_mixlo_f16 v122,v141,s99,0
v_mfma_f32_16x16x32_bf16 v[224:227],v[96:99],v[144:147],v[224:227]
s_cbranch_vccz RB54_%=
s_add_u32 m0,s58,0
s_mul_i32 s69,s59,5
s_add_u32 s69,s69,s97
buffer_load_dwordx4 v2,s[44:47],s69 offen lds
s_add_u32 m0,m0,1024
s_add_u32 s69,s69,s53
buffer_load_dwordx4 v3,s[44:47],s69 offen lds
RB54_%=:
ds_read_u16_d16_hi v130,v130
v_lshlrev_b32_sdwa v132,1,v132 dst_sel:DWORD dst_unused:UNUSED_PAD src0_sel:DWORD src1_sel:WORD_1
v_lshlrev_b32_sdwa v134,1,v134 dst_sel:DWORD dst_unused:UNUSED_PAD src0_sel:DWORD src1_sel:WORD_1
v_mfma_f32_16x16x32_bf16 v[232:235],v[100:103],v[144:147],v[232:235]
v_lshl_add_u64 v[152:153],v[152:153],0,v[154:155]
v_mfma_f32_16x16x32_bf16 v[240:243],v[104:107],v[144:147],v[240:243]
s_waitcnt lgkmcnt(2)
ds_read_u16 v129,v129
v_lshl_add_u64 v[154:155],v[156:157],0,v[158:159]
v_mfma_f32_16x16x32_bf16 v[248:251],v[108:111],v[144:147],v[248:251]
v_fma_mixhi_f16 v120,v142,s99,0
v_fma_mixhi_f16 v122,v143,s99,0
v_mfma_f32_16x16x32_bf16 v[212:215],v[88:91],v[152:155],v[212:215]
s_cbranch_vccz RB55_%=
s_add_u32 m0,m0,1024
s_add_u32 s69,s69,s53
buffer_load_dwordx4 v2,s[44:47],s69 offen lds
s_add_u32 m0,m0,s50
s_add_u32 s69,s69,s53
buffer_load_dwordx4 v3,s[44:47],s69 offen lds
RB55_%=:
ds_read_u16_d16_hi v131,v131
v_lshlrev_b32_sdwa v133,1,v133 dst_sel:DWORD dst_unused:UNUSED_PAD src0_sel:DWORD src1_sel:WORD_1
v_lshlrev_b32_sdwa v135,1,v135 dst_sel:DWORD dst_unused:UNUSED_PAD src0_sel:DWORD src1_sel:WORD_1
v_mfma_f32_16x16x32_bf16 v[220:223],v[92:95],v[152:155],v[220:223]
ds_read_b64_tr_b16 v[88:89],v6 offset:12320
v_pk_fma_f16 v121,v120,v120,s54
v_pk_fma_f16 v123,v122,v122,s54
v_mfma_f32_16x16x32_bf16 v[228:231],v[96:99],v[152:155],v[228:231]
ds_read_u16 v132,v132
v_pk_mul_f16 v121,v120,v121
v_pk_mul_f16 v123,v122,v123
v_mfma_f32_16x16x32_bf16 v[236:239],v[100:103],v[152:155],v[236:239]
ds_read_b64_tr_b16 v[90:91],v6 offset:13856
v_lshlrev_b32_sdwa v136,1,v136 dst_sel:DWORD dst_unused:UNUSED_PAD src0_sel:DWORD src1_sel:WORD_1
v_lshlrev_b32_sdwa v138,1,v138 dst_sel:DWORD dst_unused:UNUSED_PAD src0_sel:DWORD src1_sel:WORD_1
v_mfma_f32_16x16x32_bf16 v[244:247],v[104:107],v[152:155],v[244:247]
s_cbranch_vccz RB56_%=
s_add_u32 m0,m0,1024
s_add_u32 s69,s69,s53
buffer_load_dwordx4 v2,s[44:47],s69 offen lds
s_add_u32 m0,m0,1024
s_add_u32 s69,s69,s53
buffer_load_dwordx4 v3,s[44:47],s69 offen lds
RB56_%=:
ds_read_u16_d16_hi v134,v134
v_exp_f16 v120,v121
v_mfma_f32_16x16x32_bf16 v[252:255],v[108:111],v[152:155],v[252:255]
ds_read_b64_tr_b16 v[92:93],v6 offset:12328
v_exp_f16 v122,v123
v_mfma_f32_16x16x32_bf16 v[144:147],v[64:67],v[40:43],v[112:115]
ds_read_u16 v133,v133
v_lshlrev_b32_sdwa v137,1,v137 dst_sel:DWORD dst_unused:UNUSED_PAD src0_sel:DWORD src1_sel:WORD_1
v_lshlrev_b32_sdwa v139,1,v139 dst_sel:DWORD dst_unused:UNUSED_PAD src0_sel:DWORD src1_sel:WORD_1
v_mfma_f32_16x16x32_bf16 v[152:155],v[64:67],v[44:47],v[112:115]
s_cbranch_vccz RB57_%=
s_add_u32 m0,m0,s50
s_add_u32 s69,s69,s53
buffer_load_dwordx4 v2,s[44:47],s69 offen lds
s_add_u32 m0,m0,1024
s_add_u32 s69,s69,s53
buffer_load_dwordx4 v3,s[44:47],s69 offen lds
RB57_%=:
ds_read_b64_tr_b16 v[94:95],v6 offset:13864
v_exp_f16_sdwa v120,v121 dst_sel:WORD_1 dst_unused:UNUSED_PRESERVE src0_sel:WORD_1
v_mfma_f32_16x16x32_bf16 v[156:159],v[68:71],v[44:47],v[116:119]
ds_read_u16_d16_hi v135,v135
v_exp_f16_sdwa v122,v123 dst_sel:WORD_1 dst_unused:UNUSED_PRESERVE src0_sel:WORD_1
v_mfma_f32_16x16x32_bf16 v[148:151],v[68:71],v[40:43],v[116:119]
ds_read_b64_tr_b16 v[96:97],v6 offset:12384
v_pk_fma_f16 v120,v120,s55,s55
v_pk_fma_f16 v122,v122,s55,s55
v_mfma_f32_16x16x32_bf16 v[144:147],v[72:75],v[48:51],v[144:147]
ds_read_u16 v136,v136
v_rcp_f16 v121,v120
v_mfma_f32_16x16x32_bf16 v[152:155],v[72:75],v[52:55],v[152:155]
s_cbranch_vccz RB58_%=
s_add_u32 m0,m0,1024
s_add_u32 s69,s69,s53
buffer_load_dwordx4 v2,s[44:47],s69 offen lds
s_add_u32 m0,m0,s50
s_add_u32 s69,s69,s53
buffer_load_dwordx4 v3,s[44:47],s69 offen lds
RB58_%=:
ds_read_b64_tr_b16 v[98:99],v6 offset:13920
ds_read_b64_tr_b16 v[100:101],v6 offset:12392
v_rcp_f16 v123,v122
v_mfma_f32_16x16x32_bf16 v[156:159],v[76:79],v[52:55],v[156:159]
ds_read_u16_d16_hi v138,v138
v_rcp_f16_sdwa v121,v120 dst_sel:WORD_1 dst_unused:UNUSED_PRESERVE src0_sel:WORD_1
v_mfma_f32_16x16x32_bf16 v[148:151],v[76:79],v[48:51],v[148:151]
ds_read_b64_tr_b16 v[102:103],v6 offset:13928
ds_read_b64_tr_b16 v[104:105],v6 offset:12448
v_rcp_f16_sdwa v123,v122 dst_sel:WORD_1 dst_unused:UNUSED_PRESERVE src0_sel:WORD_1
v_mfma_f32_16x16x32_bf16 v[144:147],v[80:83],v[56:59],v[144:147]
ds_read_u16 v137,v137
v_fma_mix_f32 v140,v140,v121,0 op_sel_hi:[0,1,0]
v_fma_mix_f32 v142,v142,v121,0 op_sel:[0,1,0] op_sel_hi:[0,1,0]
v_mfma_f32_16x16x32_bf16 v[152:155],v[80:83],v[60:63],v[152:155]
s_waitcnt lgkmcnt(8)
s_cbranch_vccz RB59_%=
s_add_u32 m0,m0,1024
s_add_u32 s69,s69,s53
buffer_load_dwordx4 v2,s[44:47],s69 offen lds
s_add_u32 m0,m0,1024
s_add_u32 s69,s69,s53
buffer_load_dwordx4 v3,s[44:47],s69 offen lds
RB59_%=:
ds_read_b64_tr_b16 v[106:107],v6 offset:13984
ds_read_b64_tr_b16 v[108:109],v6 offset:12456
v_lshl_add_u64 v[128:129],v[128:129],0,v[130:131]
v_lshl_add_u64 v[130:131],v[132:133],0,v[134:135]
v_mfma_f32_16x16x32_bf16 v[156:159],v[84:87],v[60:63],v[156:159]
ds_read_u16_d16_hi v139,v139
v_fma_mix_f32 v141,v141,v123,0 op_sel_hi:[0,1,0]
v_fma_mix_f32 v143,v143,v123,0 op_sel:[0,1,0] op_sel_hi:[0,1,0]
v_mfma_f32_16x16x32_bf16 v[148:151],v[84:87],v[56:59],v[148:151]
ds_read_b64_tr_b16 v[110:111],v6 offset:13992
ds_read_b64_tr_b16 v[64:65],v4 offset:12320
v_lshlrev_b32_sdwa v144,1,v144 dst_sel:DWORD dst_unused:UNUSED_PAD src0_sel:DWORD src1_sel:WORD_1
v_lshlrev_b32_sdwa v146,1,v146 dst_sel:DWORD dst_unused:UNUSED_PAD src0_sel:DWORD src1_sel:WORD_1
v_mfma_f32_16x16x32_bf16 v[160:163],v[88:91],v[128:131],v[160:163]
ds_read_u16 v144,v144
v_lshlrev_b32_sdwa v145,1,v145 dst_sel:DWORD dst_unused:UNUSED_PAD src0_sel:DWORD src1_sel:WORD_1
v_lshlrev_b32_sdwa v147,1,v147 dst_sel:DWORD dst_unused:UNUSED_PAD src0_sel:DWORD src1_sel:WORD_1
v_mfma_f32_16x16x32_bf16 v[168:171],v[92:95],v[128:131],v[168:171]
s_waitcnt lgkmcnt(11)
ds_read_b64_tr_b16 v[66:67],v4 offset:14368
ds_read_b128 v[112:115],v7 offset:512
v_fma_mixlo_f16 v120,v156,s99,0
v_fma_mixlo_f16 v122,v157,s99,0
v_mfma_f32_16x16x32_bf16 v[176:179],v[96:99],v[128:131],v[176:179]
s_waitcnt lgkmcnt(10)
ds_read_u16_d16_hi v146,v146
v_lshlrev_b32_sdwa v148,1,v148 dst_sel:DWORD dst_unused:UNUSED_PAD src0_sel:DWORD src1_sel:WORD_1
v_lshlrev_b32_sdwa v150,1,v150 dst_sel:DWORD dst_unused:UNUSED_PAD src0_sel:DWORD src1_sel:WORD_1
v_mfma_f32_16x16x32_bf16 v[184:187],v[100:103],v[128:131],v[184:187]
s_waitcnt lgkmcnt(6)
ds_read_b64_tr_b16 v[68:69],v5 offset:12320
ds_read_b64_tr_b16 v[70:71],v5 offset:14368
v_lshl_add_u64 v[136:137],v[136:137],0,v[138:139]
v_mfma_f32_16x16x32_bf16 v[192:195],v[104:107],v[128:131],v[192:195]
s_waitcnt lgkmcnt(7)
ds_read_u16 v145,v145
v_cvt_pk_bf16_f32 v138,v140,v142
v_cvt_pk_bf16_f32 v139,v141,v143
v_mfma_f32_16x16x32_bf16 v[200:203],v[108:111],v[128:131],v[200:203]
ds_read_b128 v[116:119],v7 offset:544
ds_read_b64_tr_b16 v[72:73],v4 offset:16416
v_fma_mixhi_f16 v120,v158,s99,0
v_fma_mixhi_f16 v122,v159,s99,0
v_mfma_f32_16x16x32_bf16 v[164:167],v[88:91],v[136:139],v[164:167]
ds_read_u16_d16_hi v147,v147
v_lshlrev_b32_sdwa v149,1,v149 dst_sel:DWORD dst_unused:UNUSED_PAD src0_sel:DWORD src1_sel:WORD_1
v_lshlrev_b32_sdwa v151,1,v151 dst_sel:DWORD dst_unused:UNUSED_PAD src0_sel:DWORD src1_sel:WORD_1
v_mfma_f32_16x16x32_bf16 v[172:175],v[92:95],v[136:139],v[172:175]
ds_read_b64_tr_b16 v[74:75],v4 offset:18464
ds_read_b64_tr_b16 v[76:77],v5 offset:16416
v_pk_fma_f16 v121,v120,v120,s54
v_pk_fma_f16 v123,v122,v122,s54
v_mfma_f32_16x16x32_bf16 v[180:183],v[96:99],v[136:139],v[180:183]
ds_read_u16 v148,v148
v_pk_mul_f16 v121,v120,v121
v_pk_mul_f16 v123,v122,v123
v_mfma_f32_16x16x32_bf16 v[188:191],v[100:103],v[136:139],v[188:191]
ds_read_b64_tr_b16 v[78:79],v5 offset:18464
v_lshlrev_b32_sdwa v152,1,v152 dst_sel:DWORD dst_unused:UNUSED_PAD src0_sel:DWORD src1_sel:WORD_1
v_lshlrev_b32_sdwa v154,1,v154 dst_sel:DWORD dst_unused:UNUSED_PAD src0_sel:DWORD src1_sel:WORD_1
v_mfma_f32_16x16x32_bf16 v[196:199],v[104:107],v[136:139],v[196:199]
ds_read_u16_d16_hi v150,v150
v_exp_f16 v120,v121
s_nop 0
s_mov_b32 s86,0xbe801d5a
v_mfma_f32_16x16x32_bf16 v[204:207],v[108:111],v[136:139],v[204:207]
s_waitcnt lgkmcnt(12)
ds_read_b64_tr_b16 v[80:81],v4 offset:20512
v_exp_f16 v122,v123
v_mfma_f32_16x16x32_bf16 v[128:131],v[64:67],v[16:19],v[112:115]
ds_read_u16 v149,v149
v_lshlrev_b32_sdwa v153,1,v153 dst_sel:DWORD dst_unused:UNUSED_PAD src0_sel:DWORD src1_sel:WORD_1
v_lshlrev_b32_sdwa v155,1,v155 dst_sel:DWORD dst_unused:UNUSED_PAD src0_sel:DWORD src1_sel:WORD_1
v_mfma_f32_16x16x32_bf16 v[136:139],v[64:67],v[20:23],v[112:115]
s_waitcnt lgkmcnt(9)
ds_read_b64_tr_b16 v[82:83],v4 offset:22560
v_exp_f16_sdwa v120,v121 dst_sel:WORD_1 dst_unused:UNUSED_PRESERVE src0_sel:WORD_1
v_mfma_f32_16x16x32_bf16 v[140:143],v[68:71],v[20:23],v[116:119]
ds_read_u16_d16_hi v151,v151
v_exp_f16_sdwa v122,v123 dst_sel:WORD_1 dst_unused:UNUSED_PRESERVE src0_sel:WORD_1
v_mfma_f32_16x16x32_bf16 v[132:135],v[68:71],v[16:19],v[116:119]
s_waitcnt lgkmcnt(8)
ds_read_b64_tr_b16 v[84:85],v5 offset:20512
v_pk_fma_f16 v120,v120,s55,s55
v_pk_fma_f16 v122,v122,s55,s55
v_mfma_f32_16x16x32_bf16 v[128:131],v[72:75],v[24:27],v[128:131]
ds_read_u16 v152,v152
v_rcp_f16 v121,v120
v_mfma_f32_16x16x32_bf16 v[136:139],v[72:75],v[28:31],v[136:139]
s_waitcnt lgkmcnt(7)
ds_read_b64_tr_b16 v[86:87],v5 offset:22560
v_rcp_f16 v123,v122
v_mfma_f32_16x16x32_bf16 v[140:143],v[76:79],v[28:31],v[140:143]
ds_read_u16_d16_hi v154,v154
v_rcp_f16_sdwa v121,v120 dst_sel:WORD_1 dst_unused:UNUSED_PRESERVE src0_sel:WORD_1
v_mfma_f32_16x16x32_bf16 v[132:135],v[76:79],v[24:27],v[132:135]
s_waitcnt lgkmcnt(5)
v_rcp_f16_sdwa v123,v122 dst_sel:WORD_1 dst_unused:UNUSED_PRESERVE src0_sel:WORD_1
v_mfma_f32_16x16x32_bf16 v[128:131],v[80:83],v[32:35],v[128:131]
ds_read_u16 v153,v153
v_fma_mix_f32 v156,v156,v121,0 op_sel_hi:[0,1,0]
v_fma_mix_f32 v158,v158,v121,0 op_sel:[0,1,0] op_sel_hi:[0,1,0]
v_mfma_f32_16x16x32_bf16 v[136:139],v[80:83],v[36:39],v[136:139]
s_waitcnt lgkmcnt(2)
v_lshl_add_u64 v[144:145],v[144:145],0,v[146:147]
v_lshl_add_u64 v[146:147],v[148:149],0,v[150:151]
v_mfma_f32_16x16x32_bf16 v[140:143],v[84:87],v[36:39],v[140:143]
ds_read_u16_d16_hi v155,v155
v_fma_mix_f32 v157,v157,v123,0 op_sel_hi:[0,1,0]
v_fma_mix_f32 v159,v159,v123,0 op_sel:[0,1,0] op_sel_hi:[0,1,0]
v_mfma_f32_16x16x32_bf16 v[132:135],v[84:87],v[32:35],v[132:135]
v_lshlrev_b32_sdwa v128,1,v128 dst_sel:DWORD dst_unused:UNUSED_PAD src0_sel:DWORD src1_sel:WORD_1
v_lshlrev_b32_sdwa v130,1,v130 dst_sel:DWORD dst_unused:UNUSED_PAD src0_sel:DWORD src1_sel:WORD_1
v_mfma_f32_16x16x32_bf16 v[208:211],v[88:91],v[144:147],v[208:211]
ds_read_u16 v128,v128
v_lshlrev_b32_sdwa v129,1,v129 dst_sel:DWORD dst_unused:UNUSED_PAD src0_sel:DWORD src1_sel:WORD_1
v_lshlrev_b32_sdwa v131,1,v131 dst_sel:DWORD dst_unused:UNUSED_PAD src0_sel:DWORD src1_sel:WORD_1
v_mfma_f32_16x16x32_bf16 v[216:219],v[92:95],v[144:147],v[216:219]
v_fma_mixlo_f16 v120,v140,s99,0
v_fma_mixlo_f16 v122,v141,s99,0
v_mfma_f32_16x16x32_bf16 v[224:227],v[96:99],v[144:147],v[224:227]
ds_read_u16_d16_hi v130,v130
v_lshlrev_b32_sdwa v132,1,v132 dst_sel:DWORD dst_unused:UNUSED_PAD src0_sel:DWORD src1_sel:WORD_1
v_lshlrev_b32_sdwa v134,1,v134 dst_sel:DWORD dst_unused:UNUSED_PAD src0_sel:DWORD src1_sel:WORD_1
v_mfma_f32_16x16x32_bf16 v[232:235],v[100:103],v[144:147],v[232:235]
s_waitcnt lgkmcnt(2)
v_lshl_add_u64 v[152:153],v[152:153],0,v[154:155]
v_mfma_f32_16x16x32_bf16 v[240:243],v[104:107],v[144:147],v[240:243]
ds_read_u16 v129,v129
v_cvt_pk_bf16_f32 v154,v156,v158
v_cvt_pk_bf16_f32 v155,v157,v159
v_mfma_f32_16x16x32_bf16 v[248:251],v[108:111],v[144:147],v[248:251]
v_fma_mixhi_f16 v120,v142,s99,0
v_fma_mixhi_f16 v122,v143,s99,0
v_mfma_f32_16x16x32_bf16 v[212:215],v[88:91],v[152:155],v[212:215]
s_setprio 2
ds_read_u16_d16_hi v131,v131
v_lshlrev_b32_sdwa v133,1,v133 dst_sel:DWORD dst_unused:UNUSED_PAD src0_sel:DWORD src1_sel:WORD_1
v_lshlrev_b32_sdwa v135,1,v135 dst_sel:DWORD dst_unused:UNUSED_PAD src0_sel:DWORD src1_sel:WORD_1
v_mfma_f32_16x16x32_bf16 v[220:223],v[92:95],v[152:155],v[220:223]
ds_read_b64_tr_b16 v[88:89],v6 offset:18480
v_pk_fma_f16 v121,v120,v120,s54
v_pk_fma_f16 v123,v122,v122,s54
v_mfma_f32_16x16x32_bf16 v[228:231],v[96:99],v[152:155],v[228:231]
ds_read_u16 v132,v132
v_pk_mul_f16 v121,v120,v121
v_pk_mul_f16 v123,v122,v123
v_mfma_f32_16x16x32_bf16 v[236:239],v[100:103],v[152:155],v[236:239]
ds_read_b64_tr_b16 v[90:91],v6 offset:20016
v_lshlrev_b32_sdwa v136,1,v136 dst_sel:DWORD dst_unused:UNUSED_PAD src0_sel:DWORD src1_sel:WORD_1
v_lshlrev_b32_sdwa v138,1,v138 dst_sel:DWORD dst_unused:UNUSED_PAD src0_sel:DWORD src1_sel:WORD_1
v_mfma_f32_16x16x32_bf16 v[244:247],v[104:107],v[152:155],v[244:247]
ds_read_u16_d16_hi v134,v134
v_exp_f16 v120,v121
v_mfma_f32_16x16x32_bf16 v[252:255],v[108:111],v[152:155],v[252:255]
ds_read_b64_tr_b16 v[92:93],v6 offset:18488
v_exp_f16 v122,v123
v_mfma_f32_16x16x32_bf16 v[144:147],v[64:67],v[40:43],v[112:115]
ds_read_u16 v133,v133
v_lshlrev_b32_sdwa v137,1,v137 dst_sel:DWORD dst_unused:UNUSED_PAD src0_sel:DWORD src1_sel:WORD_1
v_lshlrev_b32_sdwa v139,1,v139 dst_sel:DWORD dst_unused:UNUSED_PAD src0_sel:DWORD src1_sel:WORD_1
v_mfma_f32_16x16x32_bf16 v[152:155],v[64:67],v[44:47],v[112:115]
ds_read_b64_tr_b16 v[94:95],v6 offset:20024
v_exp_f16_sdwa v120,v121 dst_sel:WORD_1 dst_unused:UNUSED_PRESERVE src0_sel:WORD_1
v_mfma_f32_16x16x32_bf16 v[156:159],v[68:71],v[44:47],v[116:119]
ds_read_u16_d16_hi v135,v135
v_exp_f16_sdwa v122,v123 dst_sel:WORD_1 dst_unused:UNUSED_PRESERVE src0_sel:WORD_1
v_mfma_f32_16x16x32_bf16 v[148:151],v[68:71],v[40:43],v[116:119]
ds_read_b64_tr_b16 v[96:97],v6 offset:18544
v_pk_fma_f16 v120,v120,s55,s55
v_pk_fma_f16 v122,v122,s55,s55
v_mfma_f32_16x16x32_bf16 v[144:147],v[72:75],v[48:51],v[144:147]
ds_read_u16 v136,v136
v_rcp_f16 v121,v120
v_mfma_f32_16x16x32_bf16 v[152:155],v[72:75],v[52:55],v[152:155]
ds_read_b64_tr_b16 v[98:99],v6 offset:20080
ds_read_b64_tr_b16 v[100:101],v6 offset:18552
v_rcp_f16 v123,v122
v_mfma_f32_16x16x32_bf16 v[156:159],v[76:79],v[52:55],v[156:159]
ds_read_u16_d16_hi v138,v138
v_rcp_f16_sdwa v121,v120 dst_sel:WORD_1 dst_unused:UNUSED_PRESERVE src0_sel:WORD_1
v_mfma_f32_16x16x32_bf16 v[148:151],v[76:79],v[48:51],v[148:151]
ds_read_b64_tr_b16 v[102:103],v6 offset:20088
ds_read_b64_tr_b16 v[104:105],v6 offset:18608
v_rcp_f16_sdwa v123,v122 dst_sel:WORD_1 dst_unused:UNUSED_PRESERVE src0_sel:WORD_1
v_mfma_f32_16x16x32_bf16 v[144:147],v[80:83],v[56:59],v[144:147]
ds_read_u16 v137,v137
v_fma_mix_f32 v140,v140,v121,0 op_sel_hi:[0,1,0]
v_fma_mix_f32 v142,v142,v121,0 op_sel:[0,1,0] op_sel_hi:[0,1,0]
v_mfma_f32_16x16x32_bf16 v[152:155],v[80:83],v[60:63],v[152:155]
s_waitcnt lgkmcnt(8)
ds_read_b64_tr_b16 v[106:107],v6 offset:20144
ds_read_b64_tr_b16 v[108:109],v6 offset:18616
v_lshl_add_u64 v[128:129],v[128:129],0,v[130:131]
v_lshl_add_u64 v[130:131],v[132:133],0,v[134:135]
v_mfma_f32_16x16x32_bf16 v[156:159],v[84:87],v[60:63],v[156:159]
ds_read_u16_d16_hi v139,v139
v_fma_mix_f32 v141,v141,v123,0 op_sel_hi:[0,1,0]
v_fma_mix_f32 v143,v143,v123,0 op_sel:[0,1,0] op_sel_hi:[0,1,0]
v_mfma_f32_16x16x32_bf16 v[148:151],v[84:87],v[56:59],v[148:151]
ds_read_b64_tr_b16 v[110:111],v6 offset:20152
ds_read_b64_tr_b16 v[64:65],v4 offset:12832
v_lshlrev_b32_sdwa v144,1,v144 dst_sel:DWORD dst_unused:UNUSED_PAD src0_sel:DWORD src1_sel:WORD_1
v_lshlrev_b32_sdwa v146,1,v146 dst_sel:DWORD dst_unused:UNUSED_PAD src0_sel:DWORD src1_sel:WORD_1
v_mfma_f32_16x16x32_bf16 v[160:163],v[88:91],v[128:131],v[160:163]
ds_read_u16 v144,v144
v_lshlrev_b32_sdwa v145,1,v145 dst_sel:DWORD dst_unused:UNUSED_PAD src0_sel:DWORD src1_sel:WORD_1
v_lshlrev_b32_sdwa v147,1,v147 dst_sel:DWORD dst_unused:UNUSED_PAD src0_sel:DWORD src1_sel:WORD_1
v_mfma_f32_16x16x32_bf16 v[168:171],v[92:95],v[128:131],v[168:171]
s_waitcnt lgkmcnt(11)
ds_read_b64_tr_b16 v[66:67],v4 offset:14880
ds_read_b128 v[112:115],v7 offset:640
v_fma_mixlo_f16 v120,v156,s99,0
v_fma_mixlo_f16 v122,v157,s99,0
v_mfma_f32_16x16x32_bf16 v[176:179],v[96:99],v[128:131],v[176:179]
s_waitcnt lgkmcnt(10)
ds_read_u16_d16_hi v146,v146
v_lshlrev_b32_sdwa v148,1,v148 dst_sel:DWORD dst_unused:UNUSED_PAD src0_sel:DWORD src1_sel:WORD_1
v_lshlrev_b32_sdwa v150,1,v150 dst_sel:DWORD dst_unused:UNUSED_PAD src0_sel:DWORD src1_sel:WORD_1
v_mfma_f32_16x16x32_bf16 v[184:187],v[100:103],v[128:131],v[184:187]
s_waitcnt lgkmcnt(6)
ds_read_b64_tr_b16 v[68:69],v5 offset:12832
ds_read_b64_tr_b16 v[70:71],v5 offset:14880
v_lshl_add_u64 v[136:137],v[136:137],0,v[138:139]
v_mfma_f32_16x16x32_bf16 v[192:195],v[104:107],v[128:131],v[192:195]
s_waitcnt lgkmcnt(7)
ds_read_u16 v145,v145
v_cvt_pk_bf16_f32 v138,v140,v142
v_cvt_pk_bf16_f32 v139,v141,v143
v_mfma_f32_16x16x32_bf16 v[200:203],v[108:111],v[128:131],v[200:203]
ds_read_b128 v[116:119],v7 offset:672
ds_read_b64_tr_b16 v[72:73],v4 offset:16928
v_fma_mixhi_f16 v120,v158,s99,0
v_fma_mixhi_f16 v122,v159,s99,0
v_mfma_f32_16x16x32_bf16 v[164:167],v[88:91],v[136:139],v[164:167]
ds_read_u16_d16_hi v147,v147
v_lshlrev_b32_sdwa v149,1,v149 dst_sel:DWORD dst_unused:UNUSED_PAD src0_sel:DWORD src1_sel:WORD_1
v_lshlrev_b32_sdwa v151,1,v151 dst_sel:DWORD dst_unused:UNUSED_PAD src0_sel:DWORD src1_sel:WORD_1
v_mfma_f32_16x16x32_bf16 v[172:175],v[92:95],v[136:139],v[172:175]
s_setprio 1
ds_read_b64_tr_b16 v[74:75],v4 offset:18976
ds_read_b64_tr_b16 v[76:77],v5 offset:16928
v_pk_fma_f16 v121,v120,v120,s54
v_pk_fma_f16 v123,v122,v122,s54
v_mfma_f32_16x16x32_bf16 v[180:183],v[96:99],v[136:139],v[180:183]
ds_read_u16 v148,v148
v_pk_mul_f16 v121,v120,v121
v_pk_mul_f16 v123,v122,v123
v_mfma_f32_16x16x32_bf16 v[188:191],v[100:103],v[136:139],v[188:191]
ds_read_b64_tr_b16 v[78:79],v5 offset:18976
v_lshlrev_b32_sdwa v152,1,v152 dst_sel:DWORD dst_unused:UNUSED_PAD src0_sel:DWORD src1_sel:WORD_1
v_lshlrev_b32_sdwa v154,1,v154 dst_sel:DWORD dst_unused:UNUSED_PAD src0_sel:DWORD src1_sel:WORD_1
v_mfma_f32_16x16x32_bf16 v[196:199],v[104:107],v[136:139],v[196:199]
ds_read_u16_d16_hi v150,v150
v_exp_f16 v120,v121
v_mfma_f32_16x16x32_bf16 v[204:207],v[108:111],v[136:139],v[204:207]
s_waitcnt lgkmcnt(12)
ds_read_b64_tr_b16 v[80:81],v4 offset:21024
v_exp_f16 v122,v123
v_mfma_f32_16x16x32_bf16 v[128:131],v[64:67],v[16:19],v[112:115]
ds_read_u16 v149,v149
v_lshlrev_b32_sdwa v153,1,v153 dst_sel:DWORD dst_unused:UNUSED_PAD src0_sel:DWORD src1_sel:WORD_1
v_lshlrev_b32_sdwa v155,1,v155 dst_sel:DWORD dst_unused:UNUSED_PAD src0_sel:DWORD src1_sel:WORD_1
v_mfma_f32_16x16x32_bf16 v[136:139],v[64:67],v[20:23],v[112:115]
s_waitcnt lgkmcnt(9)
ds_read_b64_tr_b16 v[82:83],v4 offset:23072
v_exp_f16_sdwa v120,v121 dst_sel:WORD_1 dst_unused:UNUSED_PRESERVE src0_sel:WORD_1
v_mfma_f32_16x16x32_bf16 v[140:143],v[68:71],v[20:23],v[116:119]
ds_read_u16_d16_hi v151,v151
v_exp_f16_sdwa v122,v123 dst_sel:WORD_1 dst_unused:UNUSED_PRESERVE src0_sel:WORD_1
v_mfma_f32_16x16x32_bf16 v[132:135],v[68:71],v[16:19],v[116:119]
s_waitcnt lgkmcnt(8)
ds_read_b64_tr_b16 v[84:85],v5 offset:21024
v_pk_fma_f16 v120,v120,s55,s55
v_pk_fma_f16 v122,v122,s55,s55
v_mfma_f32_16x16x32_bf16 v[128:131],v[72:75],v[24:27],v[128:131]
ds_read_u16 v152,v152
v_rcp_f16 v121,v120
v_mfma_f32_16x16x32_bf16 v[136:139],v[72:75],v[28:31],v[136:139]
s_waitcnt lgkmcnt(7)
ds_read_b64_tr_b16 v[86:87],v5 offset:23072
v_rcp_f16 v123,v122
v_mfma_f32_16x16x32_bf16 v[140:143],v[76:79],v[28:31],v[140:143]
ds_read_u16_d16_hi v154,v154
v_rcp_f16_sdwa v121,v120 dst_sel:WORD_1 dst_unused:UNUSED_PRESERVE src0_sel:WORD_1
v_mfma_f32_16x16x32_bf16 v[132:135],v[76:79],v[24:27],v[132:135]
s_waitcnt lgkmcnt(5)
v_rcp_f16_sdwa v123,v122 dst_sel:WORD_1 dst_unused:UNUSED_PRESERVE src0_sel:WORD_1
v_mfma_f32_16x16x32_bf16 v[128:131],v[80:83],v[32:35],v[128:131]
ds_read_u16 v153,v153
v_fma_mix_f32 v156,v156,v121,0 op_sel_hi:[0,1,0]
v_fma_mix_f32 v158,v158,v121,0 op_sel:[0,1,0] op_sel_hi:[0,1,0]
v_mfma_f32_16x16x32_bf16 v[136:139],v[80:83],v[36:39],v[136:139]
s_waitcnt lgkmcnt(2)
s_setprio 0
v_lshl_add_u64 v[144:145],v[144:145],0,v[146:147]
v_lshl_add_u64 v[146:147],v[148:149],0,v[150:151]
v_mfma_f32_16x16x32_bf16 v[140:143],v[84:87],v[36:39],v[140:143]
ds_read_u16_d16_hi v155,v155
v_fma_mix_f32 v157,v157,v123,0 op_sel_hi:[0,1,0]
v_fma_mix_f32 v159,v159,v123,0 op_sel:[0,1,0] op_sel_hi:[0,1,0]
v_mfma_f32_16x16x32_bf16 v[132:135],v[84:87],v[32:35],v[132:135]
v_lshlrev_b32_sdwa v128,1,v128 dst_sel:DWORD dst_unused:UNUSED_PAD src0_sel:DWORD src1_sel:WORD_1
v_lshlrev_b32_sdwa v130,1,v130 dst_sel:DWORD dst_unused:UNUSED_PAD src0_sel:DWORD src1_sel:WORD_1
v_mfma_f32_16x16x32_bf16 v[208:211],v[88:91],v[144:147],v[208:211]
s_waitcnt vmcnt(0)
s_barrier
s_setprio 2
ds_read_u16 v128,v128
v_lshlrev_b32_sdwa v129,1,v129 dst_sel:DWORD dst_unused:UNUSED_PAD src0_sel:DWORD src1_sel:WORD_1
v_mfma_f32_16x16x32_bf16 v[216:219],v[92:95],v[144:147],v[216:219]
ds_read_u16_d16_hi v130,v130
v_lshlrev_b32_sdwa v131,1,v131 dst_sel:DWORD dst_unused:UNUSED_PAD src0_sel:DWORD src1_sel:WORD_1
v_mfma_f32_16x16x32_bf16 v[224:227],v[96:99],v[144:147],v[224:227]
s_cbranch_vccz RB60_%=
s_add_u32 m0,s58,12320
s_mul_i32 s69,s59,7
s_add_u32 s69,s69,s97
buffer_load_dwordx4 v2,s[44:47],s69 offen lds
s_add_u32 m0,m0,1024
s_add_u32 s69,s69,s53
buffer_load_dwordx4 v3,s[44:47],s69 offen lds
RB60_%=:
v_lshlrev_b32_sdwa v132,1,v132 dst_sel:DWORD dst_unused:UNUSED_PAD src0_sel:DWORD src1_sel:WORD_1
v_mfma_f32_16x16x32_bf16 v[232:235],v[100:103],v[144:147],v[232:235]
s_waitcnt lgkmcnt(2)
ds_read_u16 v129,v129
v_lshl_add_u64 v[152:153],v[152:153],0,v[154:155]
v_lshlrev_b32_sdwa v134,1,v134 dst_sel:DWORD dst_unused:UNUSED_PAD src0_sel:DWORD src1_sel:WORD_1
v_mfma_f32_16x16x32_bf16 v[240:243],v[104:107],v[144:147],v[240:243]
ds_read_u16_d16_hi v131,v131
v_cvt_pk_bf16_f32 v154,v156,v158
v_cvt_pk_bf16_f32 v155,v157,v159
v_mfma_f32_16x16x32_bf16 v[248:251],v[108:111],v[144:147],v[248:251]
v_lshlrev_b32_sdwa v133,1,v133 dst_sel:DWORD dst_unused:UNUSED_PAD src0_sel:DWORD src1_sel:WORD_1
v_mfma_f32_16x16x32_bf16 v[212:215],v[88:91],v[152:155],v[212:215]
s_cbranch_vccz RB61_%=
s_add_u32 m0,m0,1024
s_add_u32 s69,s69,s53
buffer_load_dwordx4 v2,s[44:47],s69 offen lds
s_add_u32 m0,m0,s50
s_add_u32 s69,s69,s53
buffer_load_dwordx4 v3,s[44:47],s69 offen lds
RB61_%=:
ds_read_u16 v132,v132
v_lshlrev_b32_sdwa v135,1,v135 dst_sel:DWORD dst_unused:UNUSED_PAD src0_sel:DWORD src1_sel:WORD_1
v_mfma_f32_16x16x32_bf16 v[220:223],v[92:95],v[152:155],v[220:223]
ds_read_u16_d16_hi v134,v134
v_lshlrev_b32_sdwa v136,1,v136 dst_sel:DWORD dst_unused:UNUSED_PAD src0_sel:DWORD src1_sel:WORD_1
v_mfma_f32_16x16x32_bf16 v[228:231],v[96:99],v[152:155],v[228:231]
ds_read_b64_tr_b16 v[88:89],v6 offset:0
ds_read_b64_tr_b16 v[90:91],v6 offset:1536
v_lshlrev_b32_sdwa v138,1,v138 dst_sel:DWORD dst_unused:UNUSED_PAD src0_sel:DWORD src1_sel:WORD_1
v_mfma_f32_16x16x32_bf16 v[236:239],v[100:103],v[152:155],v[236:239]
ds_read_u16 v133,v133
v_lshlrev_b32_sdwa v137,1,v137 dst_sel:DWORD dst_unused:UNUSED_PAD src0_sel:DWORD src1_sel:WORD_1
v_mfma_f32_16x16x32_bf16 v[244:247],v[104:107],v[152:155],v[244:247]
ds_read_u16_d16_hi v135,v135
v_lshlrev_b32_sdwa v139,1,v139 dst_sel:DWORD dst_unused:UNUSED_PAD src0_sel:DWORD src1_sel:WORD_1
v_mfma_f32_16x16x32_bf16 v[252:255],v[108:111],v[152:155],v[252:255]
s_cbranch_vccz RB62_%=
s_add_u32 m0,m0,1024
s_add_u32 s69,s69,s53
buffer_load_dwordx4 v2,s[44:47],s69 offen lds
s_add_u32 m0,m0,1024
s_add_u32 s69,s69,s53
buffer_load_dwordx4 v3,s[44:47],s69 offen lds
RB62_%=:
ds_read_b64_tr_b16 v[92:93],v6 offset:8
ds_read_b64_tr_b16 v[94:95],v6 offset:1544
v_lshlrev_b32_sdwa v140,1,v140 dst_sel:DWORD dst_unused:UNUSED_PAD src0_sel:DWORD src1_sel:WORD_1
v_mfma_f32_16x16x32_bf16 v[144:147],v[64:67],v[40:43],v[112:115]
ds_read_u16 v136,v136
v_lshlrev_b32_sdwa v142,1,v142 dst_sel:DWORD dst_unused:UNUSED_PAD src0_sel:DWORD src1_sel:WORD_1
v_mfma_f32_16x16x32_bf16 v[152:155],v[64:67],v[44:47],v[112:115]
ds_read_u16_d16_hi v138,v138
v_lshlrev_b32_sdwa v141,1,v141 dst_sel:DWORD dst_unused:UNUSED_PAD src0_sel:DWORD src1_sel:WORD_1
v_mfma_f32_16x16x32_bf16 v[156:159],v[68:71],v[44:47],v[116:119]
ds_read_b64_tr_b16 v[96:97],v6 offset:64
ds_read_b64_tr_b16 v[98:99],v6 offset:1600
v_lshlrev_b32_sdwa v143,1,v143 dst_sel:DWORD dst_unused:UNUSED_PAD src0_sel:DWORD src1_sel:WORD_1
v_mfma_f32_16x16x32_bf16 v[148:151],v[68:71],v[40:43],v[116:119]
ds_read_u16 v137,v137
v_mfma_f32_16x16x32_bf16 v[144:147],v[72:75],v[48:51],v[144:147]
ds_read_u16_d16_hi v139,v139
v_mfma_f32_16x16x32_bf16 v[152:155],v[72:75],v[52:55],v[152:155]
s_cbranch_vccz RB63_%=
s_add_u32 m0,m0,s50
s_add_u32 s69,s69,s53
buffer_load_dwordx4 v2,s[44:47],s69 offen lds
s_add_u32 m0,m0,1024
s_add_u32 s69,s69,s53
buffer_load_dwordx4 v3,s[44:47],s69 offen lds
RB63_%=:
ds_read_b64_tr_b16 v[100:101],v6 offset:72
ds_read_b64_tr_b16 v[102:103],v6 offset:1608
v_mfma_f32_16x16x32_bf16 v[156:159],v[76:79],v[52:55],v[156:159]
ds_read_u16 v140,v140
v_mfma_f32_16x16x32_bf16 v[148:151],v[76:79],v[48:51],v[148:151]
s_waitcnt lgkmcnt(15)
ds_read_u16_d16_hi v142,v142
v_lshl_add_u64 v[128:129],v[128:129],0,v[130:131]
v_mfma_f32_16x16x32_bf16 v[144:147],v[80:83],v[56:59],v[144:147]
s_waitcnt lgkmcnt(12)
ds_read_b64_tr_b16 v[104:105],v6 offset:128
ds_read_b64_tr_b16 v[106:107],v6 offset:1664
v_lshl_add_u64 v[130:131],v[132:133],0,v[134:135]
v_mfma_f32_16x16x32_bf16 v[152:155],v[80:83],v[60:63],v[152:155]
ds_read_u16 v141,v141
v_mfma_f32_16x16x32_bf16 v[156:159],v[84:87],v[60:63],v[156:159]
ds_read_u16_d16_hi v143,v143
v_mfma_f32_16x16x32_bf16 v[148:151],v[84:87],v[56:59],v[148:151]
s_cbranch_vccz RB64_%=
s_add_u32 m0,m0,1024
s_add_u32 s69,s69,s53
buffer_load_dwordx4 v2,s[44:47],s69 offen lds
s_add_u32 m0,m0,s50
s_add_u32 s69,s69,s53
buffer_load_dwordx4 v3,s[44:47],s69 offen lds
RB64_%=:
ds_read_b64_tr_b16 v[108:109],v6 offset:136
ds_read_b64_tr_b16 v[110:111],v6 offset:1672
v_lshlrev_b32_sdwa v144,1,v144 dst_sel:DWORD dst_unused:UNUSED_PAD src0_sel:DWORD src1_sel:WORD_1
v_lshlrev_b32_sdwa v146,1,v146 dst_sel:DWORD dst_unused:UNUSED_PAD src0_sel:DWORD src1_sel:WORD_1
v_mfma_f32_16x16x32_bf16 v[160:163],v[88:91],v[128:131],v[160:163]
s_waitcnt lgkmcnt(15)
ds_read_u16 v144,v144
v_lshlrev_b32_sdwa v145,1,v145 dst_sel:DWORD dst_unused:UNUSED_PAD src0_sel:DWORD src1_sel:WORD_1
v_mfma_f32_16x16x32_bf16 v[168:171],v[92:95],v[128:131],v[168:171]
s_waitcnt lgkmcnt(13)
ds_read_u16_d16_hi v146,v146
v_lshlrev_b32_sdwa v147,1,v147 dst_sel:DWORD dst_unused:UNUSED_PAD src0_sel:DWORD src1_sel:WORD_1
v_mfma_f32_16x16x32_bf16 v[176:179],v[96:99],v[128:131],v[176:179]
s_waitcnt lgkmcnt(10)
ds_read_b64_tr_b16 v[64:65],v4 offset:0
ds_read_b64_tr_b16 v[66:67],v4 offset:2048
v_lshlrev_b32_sdwa v148,1,v148 dst_sel:DWORD dst_unused:UNUSED_PAD src0_sel:DWORD src1_sel:WORD_1
v_mfma_f32_16x16x32_bf16 v[184:187],v[100:103],v[128:131],v[184:187]
s_waitcnt lgkmcnt(8)
ds_read_u16 v145,v145
v_lshl_add_u64 v[136:137],v[136:137],0,v[138:139]
v_lshlrev_b32_sdwa v150,1,v150 dst_sel:DWORD dst_unused:UNUSED_PAD src0_sel:DWORD src1_sel:WORD_1
v_mfma_f32_16x16x32_bf16 v[192:195],v[104:107],v[128:131],v[192:195]
s_waitcnt lgkmcnt(5)
s_cbranch_vccz RB65_%=
s_add_u32 m0,m0,1024
s_add_u32 s69,s69,s53
buffer_load_dwordx4 v2,s[44:47],s69 offen lds
s_add_u32 m0,m0,1024
s_add_u32 s69,s69,s53
buffer_load_dwordx4 v3,s[44:47],s69 offen lds
RB65_%=:
ds_read_u16_d16_hi v147,v147
v_lshl_add_u64 v[138:139],v[140:141],0,v[142:143]
v_mfma_f32_16x16x32_bf16 v[200:203],v[108:111],v[128:131],v[200:203]
ds_read_b128 v[112:115],v7 offset:768
ds_read_b64_tr_b16 v[68:69],v5 offset:0
v_lshlrev_b32_sdwa v149,1,v149 dst_sel:DWORD dst_unused:UNUSED_PAD src0_sel:DWORD src1_sel:WORD_1
v_mfma_f32_16x16x32_bf16 v[164:167],v[88:91],v[136:139],v[164:167]
ds_read_u16 v148,v148
v_lshlrev_b32_sdwa v151,1,v151 dst_sel:DWORD dst_unused:UNUSED_PAD src0_sel:DWORD src1_sel:WORD_1
v_mfma_f32_16x16x32_bf16 v[172:175],v[92:95],v[136:139],v[172:175]
ds_read_u16_d16_hi v150,v150
ds_read_b64_tr_b16 v[70:71],v5 offset:2048
v_lshlrev_b32_sdwa v152,1,v152 dst_sel:DWORD dst_unused:UNUSED_PAD src0_sel:DWORD src1_sel:WORD_1
v_mfma_f32_16x16x32_bf16 v[180:183],v[96:99],v[136:139],v[180:183]
ds_read_b128 v[116:119],v7 offset:800
ds_read_b64_tr_b16 v[72:73],v4 offset:4096
v_lshlrev_b32_sdwa v154,1,v154 dst_sel:DWORD dst_unused:UNUSED_PAD src0_sel:DWORD src1_sel:WORD_1
v_mfma_f32_16x16x32_bf16 v[188:191],v[100:103],v[136:139],v[188:191]
ds_read_u16 v149,v149
v_lshlrev_b32_sdwa v153,1,v153 dst_sel:DWORD dst_unused:UNUSED_PAD src0_sel:DWORD src1_sel:WORD_1
v_mfma_f32_16x16x32_bf16 v[196:199],v[104:107],v[136:139],v[196:199]
ds_read_u16_d16_hi v151,v151
v_lshlrev_b32_sdwa v155,1,v155 dst_sel:DWORD dst_unused:UNUSED_PAD src0_sel:DWORD src1_sel:WORD_1
v_mfma_f32_16x16x32_bf16 v[204:207],v[108:111],v[136:139],v[204:207]
s_waitcnt lgkmcnt(8)
ds_read_b64_tr_b16 v[74:75],v4 offset:6144
ds_read_b64_tr_b16 v[76:77],v5 offset:4096
v_lshlrev_b32_sdwa v156,1,v156 dst_sel:DWORD dst_unused:UNUSED_PAD src0_sel:DWORD src1_sel:WORD_1
v_mfma_f32_16x16x32_bf16 v[128:131],v[64:67],v[16:19],v[112:115]
ds_read_u16 v152,v152
v_lshlrev_b32_sdwa v158,1,v158 dst_sel:DWORD dst_unused:UNUSED_PAD src0_sel:DWORD src1_sel:WORD_1
v_mfma_f32_16x16x32_bf16 v[136:139],v[64:67],v[20:23],v[112:115]
s_waitcnt lgkmcnt(6)
ds_read_u16_d16_hi v154,v154
ds_read_b64_tr_b16 v[78:79],v5 offset:6144
v_lshlrev_b32_sdwa v157,1,v157 dst_sel:DWORD dst_unused:UNUSED_PAD src0_sel:DWORD src1_sel:WORD_1
v_mfma_f32_16x16x32_bf16 v[140:143],v[68:71],v[20:23],v[116:119]
ds_read_b64_tr_b16 v[80:81],v4 offset:8192
ds_read_b64_tr_b16 v[82:83],v4 offset:10240
v_lshlrev_b32_sdwa v159,1,v159 dst_sel:DWORD dst_unused:UNUSED_PAD src0_sel:DWORD src1_sel:WORD_1
v_mfma_f32_16x16x32_bf16 v[132:135],v[68:71],v[16:19],v[116:119]
s_waitcnt lgkmcnt(6)
ds_read_u16 v153,v153
v_mfma_f32_16x16x32_bf16 v[128:131],v[72:75],v[24:27],v[128:131]
ds_read_u16_d16_hi v155,v155
v_mfma_f32_16x16x32_bf16 v[136:139],v[72:75],v[28:31],v[136:139]
s_waitcnt lgkmcnt(4)
ds_read_b64_tr_b16 v[84:85],v5 offset:8192
ds_read_b64_tr_b16 v[86:87],v5 offset:10240
v_mfma_f32_16x16x32_bf16 v[140:143],v[76:79],v[28:31],v[140:143]
ds_read_u16 v156,v156
v_mfma_f32_16x16x32_bf16 v[132:135],v[76:79],v[24:27],v[132:135]
s_waitcnt lgkmcnt(5)
ds_read_u16_d16_hi v158,v158
v_lshl_add_u64 v[144:145],v[144:145],0,v[146:147]
v_mfma_f32_16x16x32_bf16 v[128:131],v[80:83],v[32:35],v[128:131]
v_lshl_add_u64 v[146:147],v[148:149],0,v[150:151]
v_mfma_f32_16x16x32_bf16 v[136:139],v[80:83],v[36:39],v[136:139]
s_waitcnt lgkmcnt(2)
ds_read_u16 v157,v157
v_mfma_f32_16x16x32_bf16 v[140:143],v[84:87],v[36:39],v[140:143]
ds_read_u16_d16_hi v159,v159
v_mfma_f32_16x16x32_bf16 v[132:135],v[84:87],v[32:35],v[132:135]
s_nop 0
v_lshlrev_b32_sdwa v128,1,v128 dst_sel:DWORD dst_unused:UNUSED_PAD src0_sel:DWORD src1_sel:WORD_1
v_lshlrev_b32_sdwa v130,1,v130 dst_sel:DWORD dst_unused:UNUSED_PAD src0_sel:DWORD src1_sel:WORD_1
v_mfma_f32_16x16x32_bf16 v[208:211],v[88:91],v[144:147],v[208:211]
ds_read_u16 v128,v128
v_lshlrev_b32_sdwa v129,1,v129 dst_sel:DWORD dst_unused:UNUSED_PAD src0_sel:DWORD src1_sel:WORD_1
v_mfma_f32_16x16x32_bf16 v[216:219],v[92:95],v[144:147],v[216:219]
ds_read_u16_d16_hi v130,v130
v_lshlrev_b32_sdwa v131,1,v131 dst_sel:DWORD dst_unused:UNUSED_PAD src0_sel:DWORD src1_sel:WORD_1
v_mfma_f32_16x16x32_bf16 v[224:227],v[96:99],v[144:147],v[224:227]
v_lshlrev_b32_sdwa v132,1,v132 dst_sel:DWORD dst_unused:UNUSED_PAD src0_sel:DWORD src1_sel:WORD_1
v_mfma_f32_16x16x32_bf16 v[232:235],v[100:103],v[144:147],v[232:235]
ds_read_u16 v129,v129
v_lshl_add_u64 v[152:153],v[152:153],0,v[154:155]
v_lshlrev_b32_sdwa v134,1,v134 dst_sel:DWORD dst_unused:UNUSED_PAD src0_sel:DWORD src1_sel:WORD_1
v_mfma_f32_16x16x32_bf16 v[240:243],v[104:107],v[144:147],v[240:243]
s_waitcnt lgkmcnt(3)
ds_read_u16_d16_hi v131,v131
v_lshl_add_u64 v[154:155],v[156:157],0,v[158:159]
v_mfma_f32_16x16x32_bf16 v[248:251],v[108:111],v[144:147],v[248:251]
v_lshlrev_b32_sdwa v133,1,v133 dst_sel:DWORD dst_unused:UNUSED_PAD src0_sel:DWORD src1_sel:WORD_1
v_mfma_f32_16x16x32_bf16 v[212:215],v[88:91],v[152:155],v[212:215]
ds_read_u16 v132,v132
v_lshlrev_b32_sdwa v135,1,v135 dst_sel:DWORD dst_unused:UNUSED_PAD src0_sel:DWORD src1_sel:WORD_1
v_mfma_f32_16x16x32_bf16 v[220:223],v[92:95],v[152:155],v[220:223]
ds_read_u16_d16_hi v134,v134
v_lshlrev_b32_sdwa v136,1,v136 dst_sel:DWORD dst_unused:UNUSED_PAD src0_sel:DWORD src1_sel:WORD_1
v_mfma_f32_16x16x32_bf16 v[228:231],v[96:99],v[152:155],v[228:231]
ds_read_b64_tr_b16 v[88:89],v6 offset:6160
ds_read_b64_tr_b16 v[90:91],v6 offset:7696
v_lshlrev_b32_sdwa v138,1,v138 dst_sel:DWORD dst_unused:UNUSED_PAD src0_sel:DWORD src1_sel:WORD_1
v_mfma_f32_16x16x32_bf16 v[236:239],v[100:103],v[152:155],v[236:239]
ds_read_u16 v133,v133
v_lshlrev_b32_sdwa v137,1,v137 dst_sel:DWORD dst_unused:UNUSED_PAD src0_sel:DWORD src1_sel:WORD_1
v_mfma_f32_16x16x32_bf16 v[244:247],v[104:107],v[152:155],v[244:247]
ds_read_u16_d16_hi v135,v135
v_lshlrev_b32_sdwa v139,1,v139 dst_sel:DWORD dst_unused:UNUSED_PAD src0_sel:DWORD src1_sel:WORD_1
v_mfma_f32_16x16x32_bf16 v[252:255],v[108:111],v[152:155],v[252:255]
ds_read_b64_tr_b16 v[92:93],v6 offset:6168
ds_read_b64_tr_b16 v[94:95],v6 offset:7704
v_lshlrev_b32_sdwa v140,1,v140 dst_sel:DWORD dst_unused:UNUSED_PAD src0_sel:DWORD src1_sel:WORD_1
v_mfma_f32_16x16x32_bf16 v[144:147],v[64:67],v[40:43],v[112:115]
ds_read_u16 v136,v136
v_lshlrev_b32_sdwa v142,1,v142 dst_sel:DWORD dst_unused:UNUSED_PAD src0_sel:DWORD src1_sel:WORD_1
v_mfma_f32_16x16x32_bf16 v[152:155],v[64:67],v[44:47],v[112:115]
ds_read_u16_d16_hi v138,v138
v_lshlrev_b32_sdwa v141,1,v141 dst_sel:DWORD dst_unused:UNUSED_PAD src0_sel:DWORD src1_sel:WORD_1
v_mfma_f32_16x16x32_bf16 v[156:159],v[68:71],v[44:47],v[116:119]
ds_read_b64_tr_b16 v[96:97],v6 offset:6224
ds_read_b64_tr_b16 v[98:99],v6 offset:7760
v_lshlrev_b32_sdwa v143,1,v143 dst_sel:DWORD dst_unused:UNUSED_PAD src0_sel:DWORD src1_sel:WORD_1
v_mfma_f32_16x16x32_bf16 v[148:151],v[68:71],v[40:43],v[116:119]
ds_read_u16 v137,v137
v_mfma_f32_16x16x32_bf16 v[144:147],v[72:75],v[48:51],v[144:147]
ds_read_u16_d16_hi v139,v139
v_mfma_f32_16x16x32_bf16 v[152:155],v[72:75],v[52:55],v[152:155]
ds_read_b64_tr_b16 v[100:101],v6 offset:6232
ds_read_b64_tr_b16 v[102:103],v6 offset:7768
v_mfma_f32_16x16x32_bf16 v[156:159],v[76:79],v[52:55],v[156:159]
ds_read_u16 v140,v140
v_mfma_f32_16x16x32_bf16 v[148:151],v[76:79],v[48:51],v[148:151]
s_waitcnt lgkmcnt(15)
ds_read_u16_d16_hi v142,v142
v_lshl_add_u64 v[128:129],v[128:129],0,v[130:131]
v_mfma_f32_16x16x32_bf16 v[144:147],v[80:83],v[56:59],v[144:147]
s_waitcnt lgkmcnt(12)
ds_read_b64_tr_b16 v[104:105],v6 offset:6288
ds_read_b64_tr_b16 v[106:107],v6 offset:7824
v_lshl_add_u64 v[130:131],v[132:133],0,v[134:135]
v_mfma_f32_16x16x32_bf16 v[152:155],v[80:83],v[60:63],v[152:155]
ds_read_u16 v141,v141
v_mfma_f32_16x16x32_bf16 v[156:159],v[84:87],v[60:63],v[156:159]
ds_read_u16_d16_hi v143,v143
v_mfma_f32_16x16x32_bf16 v[148:151],v[84:87],v[56:59],v[148:151]
ds_read_b64_tr_b16 v[108:109],v6 offset:6296
ds_read_b64_tr_b16 v[110:111],v6 offset:7832
v_lshlrev_b32_sdwa v144,1,v144 dst_sel:DWORD dst_unused:UNUSED_PAD src0_sel:DWORD src1_sel:WORD_1
v_lshlrev_b32_sdwa v146,1,v146 dst_sel:DWORD dst_unused:UNUSED_PAD src0_sel:DWORD src1_sel:WORD_1
v_mfma_f32_16x16x32_bf16 v[160:163],v[88:91],v[128:131],v[160:163]
s_waitcnt lgkmcnt(15)
ds_read_u16 v144,v144
v_lshlrev_b32_sdwa v145,1,v145 dst_sel:DWORD dst_unused:UNUSED_PAD src0_sel:DWORD src1_sel:WORD_1
v_mfma_f32_16x16x32_bf16 v[168:171],v[92:95],v[128:131],v[168:171]
s_waitcnt lgkmcnt(13)
ds_read_u16_d16_hi v146,v146
v_lshlrev_b32_sdwa v147,1,v147 dst_sel:DWORD dst_unused:UNUSED_PAD src0_sel:DWORD src1_sel:WORD_1
v_mfma_f32_16x16x32_bf16 v[176:179],v[96:99],v[128:131],v[176:179]
s_waitcnt lgkmcnt(10)
ds_read_b64_tr_b16 v[64:65],v4 offset:512
ds_read_b64_tr_b16 v[66:67],v4 offset:2560
v_lshlrev_b32_sdwa v148,1,v148 dst_sel:DWORD dst_unused:UNUSED_PAD src0_sel:DWORD src1_sel:WORD_1
v_mfma_f32_16x16x32_bf16 v[184:187],v[100:103],v[128:131],v[184:187]
s_waitcnt lgkmcnt(8)
ds_read_u16 v145,v145
v_lshl_add_u64 v[136:137],v[136:137],0,v[138:139]
v_lshlrev_b32_sdwa v150,1,v150 dst_sel:DWORD dst_unused:UNUSED_PAD src0_sel:DWORD src1_sel:WORD_1
v_mfma_f32_16x16x32_bf16 v[192:195],v[104:107],v[128:131],v[192:195]
s_waitcnt lgkmcnt(5)
ds_read_u16_d16_hi v147,v147
v_lshl_add_u64 v[138:139],v[140:141],0,v[142:143]
v_mfma_f32_16x16x32_bf16 v[200:203],v[108:111],v[128:131],v[200:203]
ds_read_b128 v[112:115],v7 offset:896
ds_read_b64_tr_b16 v[68:69],v5 offset:512
v_lshlrev_b32_sdwa v149,1,v149 dst_sel:DWORD dst_unused:UNUSED_PAD src0_sel:DWORD src1_sel:WORD_1
v_mfma_f32_16x16x32_bf16 v[164:167],v[88:91],v[136:139],v[164:167]
ds_read_u16 v148,v148
v_lshlrev_b32_sdwa v151,1,v151 dst_sel:DWORD dst_unused:UNUSED_PAD src0_sel:DWORD src1_sel:WORD_1
s_mov_b32 s86,0xbe801d5a
v_mfma_f32_16x16x32_bf16 v[172:175],v[92:95],v[136:139],v[172:175]
ds_read_u16_d16_hi v150,v150
ds_read_b64_tr_b16 v[70:71],v5 offset:2560
v_lshlrev_b32_sdwa v152,1,v152 dst_sel:DWORD dst_unused:UNUSED_PAD src0_sel:DWORD src1_sel:WORD_1
v_mfma_f32_16x16x32_bf16 v[180:183],v[96:99],v[136:139],v[180:183]
ds_read_b128 v[116:119],v7 offset:928
ds_read_b64_tr_b16 v[72:73],v4 offset:4608
v_lshlrev_b32_sdwa v154,1,v154 dst_sel:DWORD dst_unused:UNUSED_PAD src0_sel:DWORD src1_sel:WORD_1
v_mfma_f32_16x16x32_bf16 v[188:191],v[100:103],v[136:139],v[188:191]
ds_read_u16 v149,v149
v_lshlrev_b32_sdwa v153,1,v153 dst_sel:DWORD dst_unused:UNUSED_PAD src0_sel:DWORD src1_sel:WORD_1
v_mfma_f32_16x16x32_bf16 v[196:199],v[104:107],v[136:139],v[196:199]
ds_read_u16_d16_hi v151,v151
v_lshlrev_b32_sdwa v155,1,v155 dst_sel:DWORD dst_unused:UNUSED_PAD src0_sel:DWORD src1_sel:WORD_1
v_mfma_f32_16x16x32_bf16 v[204:207],v[108:111],v[136:139],v[204:207]
s_waitcnt lgkmcnt(8)
ds_read_b64_tr_b16 v[74:75],v4 offset:6656
ds_read_b64_tr_b16 v[76:77],v5 offset:4608
v_lshlrev_b32_sdwa v156,1,v156 dst_sel:DWORD dst_unused:UNUSED_PAD src0_sel:DWORD src1_sel:WORD_1
v_mfma_f32_16x16x32_bf16 v[128:131],v[64:67],v[16:19],v[112:115]
ds_read_u16 v152,v152
v_lshlrev_b32_sdwa v158,1,v158 dst_sel:DWORD dst_unused:UNUSED_PAD src0_sel:DWORD src1_sel:WORD_1
v_mfma_f32_16x16x32_bf16 v[136:139],v[64:67],v[20:23],v[112:115]
s_waitcnt lgkmcnt(6)
ds_read_u16_d16_hi v154,v154
ds_read_b64_tr_b16 v[78:79],v5 offset:6656
v_lshlrev_b32_sdwa v157,1,v157 dst_sel:DWORD dst_unused:UNUSED_PAD src0_sel:DWORD src1_sel:WORD_1
v_mfma_f32_16x16x32_bf16 v[140:143],v[68:71],v[20:23],v[116:119]
ds_read_b64_tr_b16 v[80:81],v4 offset:8704
ds_read_b64_tr_b16 v[82:83],v4 offset:10752
v_lshlrev_b32_sdwa v159,1,v159 dst_sel:DWORD dst_unused:UNUSED_PAD src0_sel:DWORD src1_sel:WORD_1
v_mfma_f32_16x16x32_bf16 v[132:135],v[68:71],v[16:19],v[116:119]
s_waitcnt lgkmcnt(6)
ds_read_u16 v153,v153
v_mfma_f32_16x16x32_bf16 v[128:131],v[72:75],v[24:27],v[128:131]
ds_read_u16_d16_hi v155,v155
v_mfma_f32_16x16x32_bf16 v[136:139],v[72:75],v[28:31],v[136:139]
s_waitcnt lgkmcnt(4)
ds_read_b64_tr_b16 v[84:85],v5 offset:8704
ds_read_b64_tr_b16 v[86:87],v5 offset:10752
v_mfma_f32_16x16x32_bf16 v[140:143],v[76:79],v[28:31],v[140:143]
ds_read_u16 v156,v156
v_mfma_f32_16x16x32_bf16 v[132:135],v[76:79],v[24:27],v[132:135]
s_waitcnt lgkmcnt(5)
ds_read_u16_d16_hi v158,v158
v_lshl_add_u64 v[144:145],v[144:145],0,v[146:147]
v_mfma_f32_16x16x32_bf16 v[128:131],v[80:83],v[32:35],v[128:131]
v_lshl_add_u64 v[146:147],v[148:149],0,v[150:151]
v_mfma_f32_16x16x32_bf16 v[136:139],v[80:83],v[36:39],v[136:139]
s_waitcnt lgkmcnt(2)
ds_read_u16 v157,v157
v_mfma_f32_16x16x32_bf16 v[140:143],v[84:87],v[36:39],v[140:143]
ds_read_u16_d16_hi v159,v159
v_mfma_f32_16x16x32_bf16 v[132:135],v[84:87],v[32:35],v[132:135]
s_nop 0
v_lshlrev_b32_sdwa v128,1,v128 dst_sel:DWORD dst_unused:UNUSED_PAD src0_sel:DWORD src1_sel:WORD_1
v_lshlrev_b32_sdwa v130,1,v130 dst_sel:DWORD dst_unused:UNUSED_PAD src0_sel:DWORD src1_sel:WORD_1
v_mfma_f32_16x16x32_bf16 v[208:211],v[88:91],v[144:147],v[208:211]
s_waitcnt vmcnt(0)
s_barrier
s_setprio 3
ds_read_u16 v128,v128
v_lshlrev_b32_sdwa v129,1,v129 dst_sel:DWORD dst_unused:UNUSED_PAD src0_sel:DWORD src1_sel:WORD_1
v_lshlrev_b32_sdwa v131,1,v131 dst_sel:DWORD dst_unused:UNUSED_PAD src0_sel:DWORD src1_sel:WORD_1
v_mfma_f32_16x16x32_bf16 v[216:219],v[92:95],v[144:147],v[216:219]
v_fma_mixlo_f16 v120,v140,s99,0
v_fma_mixlo_f16 v122,v141,s99,0
v_mfma_f32_16x16x32_bf16 v[224:227],v[96:99],v[144:147],v[224:227]
ds_read_u16_d16_hi v130,v130
v_lshlrev_b32_sdwa v132,1,v132 dst_sel:DWORD dst_unused:UNUSED_PAD src0_sel:DWORD src1_sel:WORD_1
v_lshlrev_b32_sdwa v134,1,v134 dst_sel:DWORD dst_unused:UNUSED_PAD src0_sel:DWORD src1_sel:WORD_1
v_mfma_f32_16x16x32_bf16 v[232:235],v[100:103],v[144:147],v[232:235]
v_lshl_add_u64 v[152:153],v[152:153],0,v[154:155]
v_mfma_f32_16x16x32_bf16 v[240:243],v[104:107],v[144:147],v[240:243]
s_waitcnt lgkmcnt(2)
ds_read_u16 v129,v129
v_lshl_add_u64 v[154:155],v[156:157],0,v[158:159]
v_mfma_f32_16x16x32_bf16 v[248:251],v[108:111],v[144:147],v[248:251]
v_fma_mixhi_f16 v120,v142,s99,0
v_fma_mixhi_f16 v122,v143,s99,0
v_mfma_f32_16x16x32_bf16 v[212:215],v[88:91],v[152:155],v[212:215]
ds_read_u16_d16_hi v131,v131
v_lshlrev_b32_sdwa v133,1,v133 dst_sel:DWORD dst_unused:UNUSED_PAD src0_sel:DWORD src1_sel:WORD_1
v_lshlrev_b32_sdwa v135,1,v135 dst_sel:DWORD dst_unused:UNUSED_PAD src0_sel:DWORD src1_sel:WORD_1
v_mfma_f32_16x16x32_bf16 v[220:223],v[92:95],v[152:155],v[220:223]
ds_read_b64_tr_b16 v[88:89],v6 offset:12320
v_pk_fma_f16 v121,v120,v120,s54
v_pk_fma_f16 v123,v122,v122,s54
v_mfma_f32_16x16x32_bf16 v[228:231],v[96:99],v[152:155],v[228:231]
ds_read_u16 v132,v132
v_pk_mul_f16 v121,v120,v121
v_pk_mul_f16 v123,v122,v123
v_mfma_f32_16x16x32_bf16 v[236:239],v[100:103],v[152:155],v[236:239]
ds_read_b64_tr_b16 v[90:91],v6 offset:13856
v_lshlrev_b32_sdwa v136,1,v136 dst_sel:DWORD dst_unused:UNUSED_PAD src0_sel:DWORD src1_sel:WORD_1
v_lshlrev_b32_sdwa v138,1,v138 dst_sel:DWORD dst_unused:UNUSED_PAD src0_sel:DWORD src1_sel:WORD_1
v_mfma_f32_16x16x32_bf16 v[244:247],v[104:107],v[152:155],v[244:247]
ds_read_u16_d16_hi v134,v134
v_exp_f16 v120,v121
v_mfma_f32_16x16x32_bf16 v[252:255],v[108:111],v[152:155],v[252:255]
ds_read_b64_tr_b16 v[92:93],v6 offset:12328
v_exp_f16 v122,v123
v_mfma_f32_16x16x32_bf16 v[144:147],v[64:67],v[40:43],v[112:115]
ds_read_u16 v133,v133
v_lshlrev_b32_sdwa v137,1,v137 dst_sel:DWORD dst_unused:UNUSED_PAD src0_sel:DWORD src1_sel:WORD_1
v_lshlrev_b32_sdwa v139,1,v139 dst_sel:DWORD dst_unused:UNUSED_PAD src0_sel:DWORD src1_sel:WORD_1
v_mfma_f32_16x16x32_bf16 v[152:155],v[64:67],v[44:47],v[112:115]
ds_read_b64_tr_b16 v[94:95],v6 offset:13864
v_exp_f16_sdwa v120,v121 dst_sel:WORD_1 dst_unused:UNUSED_PRESERVE src0_sel:WORD_1
v_mfma_f32_16x16x32_bf16 v[156:159],v[68:71],v[44:47],v[116:119]
ds_read_u16_d16_hi v135,v135
v_exp_f16_sdwa v122,v123 dst_sel:WORD_1 dst_unused:UNUSED_PRESERVE src0_sel:WORD_1
v_mfma_f32_16x16x32_bf16 v[148:151],v[68:71],v[40:43],v[116:119]
ds_read_b64_tr_b16 v[96:97],v6 offset:12384
v_pk_fma_f16 v120,v120,s55,s55
v_pk_fma_f16 v122,v122,s55,s55
v_mfma_f32_16x16x32_bf16 v[144:147],v[72:75],v[48:51],v[144:147]
ds_read_u16 v136,v136
v_rcp_f16 v121,v120
v_mfma_f32_16x16x32_bf16 v[152:155],v[72:75],v[52:55],v[152:155]
ds_read_b64_tr_b16 v[98:99],v6 offset:13920
ds_read_b64_tr_b16 v[100:101],v6 offset:12392
v_rcp_f16 v123,v122
v_mfma_f32_16x16x32_bf16 v[156:159],v[76:79],v[52:55],v[156:159]
ds_read_u16_d16_hi v138,v138
v_rcp_f16_sdwa v121,v120 dst_sel:WORD_1 dst_unused:UNUSED_PRESERVE src0_sel:WORD_1
v_mfma_f32_16x16x32_bf16 v[148:151],v[76:79],v[48:51],v[148:151]
ds_read_b64_tr_b16 v[102:103],v6 offset:13928
ds_read_b64_tr_b16 v[104:105],v6 offset:12448
v_rcp_f16_sdwa v123,v122 dst_sel:WORD_1 dst_unused:UNUSED_PRESERVE src0_sel:WORD_1
v_mfma_f32_16x16x32_bf16 v[144:147],v[80:83],v[56:59],v[144:147]
ds_read_u16 v137,v137
v_fma_mix_f32 v140,v140,v121,0 op_sel_hi:[0,1,0]
v_fma_mix_f32 v142,v142,v121,0 op_sel:[0,1,0] op_sel_hi:[0,1,0]
v_mfma_f32_16x16x32_bf16 v[152:155],v[80:83],v[60:63],v[152:155]
s_waitcnt lgkmcnt(8)
ds_read_b64_tr_b16 v[106:107],v6 offset:13984
ds_read_b64_tr_b16 v[108:109],v6 offset:12456
v_lshl_add_u64 v[128:129],v[128:129],0,v[130:131]
v_lshl_add_u64 v[130:131],v[132:133],0,v[134:135]
v_mfma_f32_16x16x32_bf16 v[156:159],v[84:87],v[60:63],v[156:159]
ds_read_u16_d16_hi v139,v139
v_fma_mix_f32 v141,v141,v123,0 op_sel_hi:[0,1,0]
v_fma_mix_f32 v143,v143,v123,0 op_sel:[0,1,0] op_sel_hi:[0,1,0]
v_mfma_f32_16x16x32_bf16 v[148:151],v[84:87],v[56:59],v[148:151]
ds_read_b64_tr_b16 v[110:111],v6 offset:13992
ds_read_b64_tr_b16 v[64:65],v4 offset:12320
v_lshlrev_b32_sdwa v144,1,v144 dst_sel:DWORD dst_unused:UNUSED_PAD src0_sel:DWORD src1_sel:WORD_1
v_lshlrev_b32_sdwa v146,1,v146 dst_sel:DWORD dst_unused:UNUSED_PAD src0_sel:DWORD src1_sel:WORD_1
v_mfma_f32_16x16x32_bf16 v[160:163],v[88:91],v[128:131],v[160:163]
ds_read_u16 v144,v144
v_lshlrev_b32_sdwa v145,1,v145 dst_sel:DWORD dst_unused:UNUSED_PAD src0_sel:DWORD src1_sel:WORD_1
v_lshlrev_b32_sdwa v147,1,v147 dst_sel:DWORD dst_unused:UNUSED_PAD src0_sel:DWORD src1_sel:WORD_1
v_mfma_f32_16x16x32_bf16 v[168:171],v[92:95],v[128:131],v[168:171]
s_waitcnt lgkmcnt(11)
ds_read_b64_tr_b16 v[66:67],v4 offset:14368
ds_read_b128 v[112:115],v7 offset:1024
v_fma_mixlo_f16 v120,v156,s99,0
v_fma_mixlo_f16 v122,v157,s99,0
v_mfma_f32_16x16x32_bf16 v[176:179],v[96:99],v[128:131],v[176:179]
s_waitcnt lgkmcnt(10)
ds_read_u16_d16_hi v146,v146
v_lshlrev_b32_sdwa v148,1,v148 dst_sel:DWORD dst_unused:UNUSED_PAD src0_sel:DWORD src1_sel:WORD_1
v_lshlrev_b32_sdwa v150,1,v150 dst_sel:DWORD dst_unused:UNUSED_PAD src0_sel:DWORD src1_sel:WORD_1
v_mfma_f32_16x16x32_bf16 v[184:187],v[100:103],v[128:131],v[184:187]
s_waitcnt lgkmcnt(6)
ds_read_b64_tr_b16 v[68:69],v5 offset:12320
ds_read_b64_tr_b16 v[70:71],v5 offset:14368
v_lshl_add_u64 v[136:137],v[136:137],0,v[138:139]
v_mfma_f32_16x16x32_bf16 v[192:195],v[104:107],v[128:131],v[192:195]
s_waitcnt lgkmcnt(7)
ds_read_u16 v145,v145
v_cvt_pk_bf16_f32 v138,v140,v142
v_cvt_pk_bf16_f32 v139,v141,v143
v_mfma_f32_16x16x32_bf16 v[200:203],v[108:111],v[128:131],v[200:203]
ds_read_b128 v[116:119],v7 offset:1056
ds_read_b64_tr_b16 v[72:73],v4 offset:16416
v_fma_mixhi_f16 v120,v158,s99,0
v_fma_mixhi_f16 v122,v159,s99,0
v_mfma_f32_16x16x32_bf16 v[164:167],v[88:91],v[136:139],v[164:167]
ds_read_u16_d16_hi v147,v147
v_lshlrev_b32_sdwa v149,1,v149 dst_sel:DWORD dst_unused:UNUSED_PAD src0_sel:DWORD src1_sel:WORD_1
v_lshlrev_b32_sdwa v151,1,v151 dst_sel:DWORD dst_unused:UNUSED_PAD src0_sel:DWORD src1_sel:WORD_1
v_mfma_f32_16x16x32_bf16 v[172:175],v[92:95],v[136:139],v[172:175]
ds_read_b64_tr_b16 v[74:75],v4 offset:18464
ds_read_b64_tr_b16 v[76:77],v5 offset:16416
v_pk_fma_f16 v121,v120,v120,s54
v_pk_fma_f16 v123,v122,v122,s54
v_mfma_f32_16x16x32_bf16 v[180:183],v[96:99],v[136:139],v[180:183]
ds_read_u16 v148,v148
v_pk_mul_f16 v121,v120,v121
v_pk_mul_f16 v123,v122,v123
v_mfma_f32_16x16x32_bf16 v[188:191],v[100:103],v[136:139],v[188:191]
ds_read_b64_tr_b16 v[78:79],v5 offset:18464
v_lshlrev_b32_sdwa v152,1,v152 dst_sel:DWORD dst_unused:UNUSED_PAD src0_sel:DWORD src1_sel:WORD_1
v_lshlrev_b32_sdwa v154,1,v154 dst_sel:DWORD dst_unused:UNUSED_PAD src0_sel:DWORD src1_sel:WORD_1
v_mfma_f32_16x16x32_bf16 v[196:199],v[104:107],v[136:139],v[196:199]
ds_read_u16_d16_hi v150,v150
v_exp_f16 v120,v121
v_mfma_f32_16x16x32_bf16 v[204:207],v[108:111],v[136:139],v[204:207]
s_waitcnt lgkmcnt(12)
ds_read_b64_tr_b16 v[80:81],v4 offset:20512
v_exp_f16 v122,v123
v_mfma_f32_16x16x32_bf16 v[128:131],v[64:67],v[16:19],v[112:115]
ds_read_u16 v149,v149
v_lshlrev_b32_sdwa v153,1,v153 dst_sel:DWORD dst_unused:UNUSED_PAD src0_sel:DWORD src1_sel:WORD_1
v_lshlrev_b32_sdwa v155,1,v155 dst_sel:DWORD dst_unused:UNUSED_PAD src0_sel:DWORD src1_sel:WORD_1
v_mfma_f32_16x16x32_bf16 v[136:139],v[64:67],v[20:23],v[112:115]
s_waitcnt lgkmcnt(9)
ds_read_b64_tr_b16 v[82:83],v4 offset:22560
v_exp_f16_sdwa v120,v121 dst_sel:WORD_1 dst_unused:UNUSED_PRESERVE src0_sel:WORD_1
v_mfma_f32_16x16x32_bf16 v[140:143],v[68:71],v[20:23],v[116:119]
ds_read_u16_d16_hi v151,v151
v_exp_f16_sdwa v122,v123 dst_sel:WORD_1 dst_unused:UNUSED_PRESERVE src0_sel:WORD_1
v_mfma_f32_16x16x32_bf16 v[132:135],v[68:71],v[16:19],v[116:119]
s_waitcnt lgkmcnt(8)
ds_read_b64_tr_b16 v[84:85],v5 offset:20512
v_pk_fma_f16 v120,v120,s55,s55
v_pk_fma_f16 v122,v122,s55,s55
v_mfma_f32_16x16x32_bf16 v[128:131],v[72:75],v[24:27],v[128:131]
ds_read_u16 v152,v152
v_rcp_f16 v121,v120
v_mfma_f32_16x16x32_bf16 v[136:139],v[72:75],v[28:31],v[136:139]
s_waitcnt lgkmcnt(7)
ds_read_b64_tr_b16 v[86:87],v5 offset:22560
v_rcp_f16 v123,v122
v_mfma_f32_16x16x32_bf16 v[140:143],v[76:79],v[28:31],v[140:143]
ds_read_u16_d16_hi v154,v154
v_rcp_f16_sdwa v121,v120 dst_sel:WORD_1 dst_unused:UNUSED_PRESERVE src0_sel:WORD_1
v_mfma_f32_16x16x32_bf16 v[132:135],v[76:79],v[24:27],v[132:135]
s_waitcnt lgkmcnt(5)
v_rcp_f16_sdwa v123,v122 dst_sel:WORD_1 dst_unused:UNUSED_PRESERVE src0_sel:WORD_1
v_mfma_f32_16x16x32_bf16 v[128:131],v[80:83],v[32:35],v[128:131]
ds_read_u16 v153,v153
v_fma_mix_f32 v156,v156,v121,0 op_sel_hi:[0,1,0]
v_fma_mix_f32 v158,v158,v121,0 op_sel:[0,1,0] op_sel_hi:[0,1,0]
v_mfma_f32_16x16x32_bf16 v[136:139],v[80:83],v[36:39],v[136:139]
s_waitcnt lgkmcnt(2)
v_lshl_add_u64 v[144:145],v[144:145],0,v[146:147]
v_lshl_add_u64 v[146:147],v[148:149],0,v[150:151]
v_mfma_f32_16x16x32_bf16 v[140:143],v[84:87],v[36:39],v[140:143]
ds_read_u16_d16_hi v155,v155
v_fma_mix_f32 v157,v157,v123,0 op_sel_hi:[0,1,0]
v_fma_mix_f32 v159,v159,v123,0 op_sel:[0,1,0] op_sel_hi:[0,1,0]
v_mfma_f32_16x16x32_bf16 v[132:135],v[84:87],v[32:35],v[132:135]
v_lshlrev_b32_sdwa v128,1,v128 dst_sel:DWORD dst_unused:UNUSED_PAD src0_sel:DWORD src1_sel:WORD_1
v_lshlrev_b32_sdwa v130,1,v130 dst_sel:DWORD dst_unused:UNUSED_PAD src0_sel:DWORD src1_sel:WORD_1
v_mfma_f32_16x16x32_bf16 v[208:211],v[88:91],v[144:147],v[208:211]
ds_read_u16 v128,v128
v_lshlrev_b32_sdwa v129,1,v129 dst_sel:DWORD dst_unused:UNUSED_PAD src0_sel:DWORD src1_sel:WORD_1
v_lshlrev_b32_sdwa v131,1,v131 dst_sel:DWORD dst_unused:UNUSED_PAD src0_sel:DWORD src1_sel:WORD_1
v_mfma_f32_16x16x32_bf16 v[216:219],v[92:95],v[144:147],v[216:219]
v_fma_mixlo_f16 v120,v140,s99,0
v_fma_mixlo_f16 v122,v141,s99,0
v_mfma_f32_16x16x32_bf16 v[224:227],v[96:99],v[144:147],v[224:227]
s_setprio 2
ds_read_u16_d16_hi v130,v130
v_lshlrev_b32_sdwa v132,1,v132 dst_sel:DWORD dst_unused:UNUSED_PAD src0_sel:DWORD src1_sel:WORD_1
v_lshlrev_b32_sdwa v134,1,v134 dst_sel:DWORD dst_unused:UNUSED_PAD src0_sel:DWORD src1_sel:WORD_1
v_mfma_f32_16x16x32_bf16 v[232:235],v[100:103],v[144:147],v[232:235]
s_waitcnt lgkmcnt(2)
v_lshl_add_u64 v[152:153],v[152:153],0,v[154:155]
v_mfma_f32_16x16x32_bf16 v[240:243],v[104:107],v[144:147],v[240:243]
ds_read_u16 v129,v129
v_cvt_pk_bf16_f32 v154,v156,v158
v_cvt_pk_bf16_f32 v155,v157,v159
v_mfma_f32_16x16x32_bf16 v[248:251],v[108:111],v[144:147],v[248:251]
v_fma_mixhi_f16 v120,v142,s99,0
v_fma_mixhi_f16 v122,v143,s99,0
v_mfma_f32_16x16x32_bf16 v[212:215],v[88:91],v[152:155],v[212:215]
ds_read_u16_d16_hi v131,v131
v_lshlrev_b32_sdwa v133,1,v133 dst_sel:DWORD dst_unused:UNUSED_PAD src0_sel:DWORD src1_sel:WORD_1
v_lshlrev_b32_sdwa v135,1,v135 dst_sel:DWORD dst_unused:UNUSED_PAD src0_sel:DWORD src1_sel:WORD_1
v_mfma_f32_16x16x32_bf16 v[220:223],v[92:95],v[152:155],v[220:223]
ds_read_b64_tr_b16 v[88:89],v6 offset:18480
v_pk_fma_f16 v121,v120,v120,s54
v_pk_fma_f16 v123,v122,v122,s54
v_mfma_f32_16x16x32_bf16 v[228:231],v[96:99],v[152:155],v[228:231]
ds_read_u16 v132,v132
v_pk_mul_f16 v121,v120,v121
v_pk_mul_f16 v123,v122,v123
v_mfma_f32_16x16x32_bf16 v[236:239],v[100:103],v[152:155],v[236:239]
ds_read_b64_tr_b16 v[90:91],v6 offset:20016
v_lshlrev_b32_sdwa v136,1,v136 dst_sel:DWORD dst_unused:UNUSED_PAD src0_sel:DWORD src1_sel:WORD_1
v_lshlrev_b32_sdwa v138,1,v138 dst_sel:DWORD dst_unused:UNUSED_PAD src0_sel:DWORD src1_sel:WORD_1
v_mfma_f32_16x16x32_bf16 v[244:247],v[104:107],v[152:155],v[244:247]
ds_read_u16_d16_hi v134,v134
v_exp_f16 v120,v121
v_mfma_f32_16x16x32_bf16 v[252:255],v[108:111],v[152:155],v[252:255]
ds_read_b64_tr_b16 v[92:93],v6 offset:18488
v_exp_f16 v122,v123
v_mfma_f32_16x16x32_bf16 v[144:147],v[64:67],v[40:43],v[112:115]
ds_read_u16 v133,v133
v_lshlrev_b32_sdwa v137,1,v137 dst_sel:DWORD dst_unused:UNUSED_PAD src0_sel:DWORD src1_sel:WORD_1
v_lshlrev_b32_sdwa v139,1,v139 dst_sel:DWORD dst_unused:UNUSED_PAD src0_sel:DWORD src1_sel:WORD_1
v_mfma_f32_16x16x32_bf16 v[152:155],v[64:67],v[44:47],v[112:115]
ds_read_b64_tr_b16 v[94:95],v6 offset:20024
v_exp_f16_sdwa v120,v121 dst_sel:WORD_1 dst_unused:UNUSED_PRESERVE src0_sel:WORD_1
v_mfma_f32_16x16x32_bf16 v[156:159],v[68:71],v[44:47],v[116:119]
ds_read_u16_d16_hi v135,v135
v_exp_f16_sdwa v122,v123 dst_sel:WORD_1 dst_unused:UNUSED_PRESERVE src0_sel:WORD_1
v_mfma_f32_16x16x32_bf16 v[148:151],v[68:71],v[40:43],v[116:119]
ds_read_b64_tr_b16 v[96:97],v6 offset:18544
v_pk_fma_f16 v120,v120,s55,s55
v_pk_fma_f16 v122,v122,s55,s55
v_mfma_f32_16x16x32_bf16 v[144:147],v[72:75],v[48:51],v[144:147]
ds_read_u16 v136,v136
v_rcp_f16 v121,v120
v_mfma_f32_16x16x32_bf16 v[152:155],v[72:75],v[52:55],v[152:155]
ds_read_b64_tr_b16 v[98:99],v6 offset:20080
ds_read_b64_tr_b16 v[100:101],v6 offset:18552
v_rcp_f16 v123,v122
v_mfma_f32_16x16x32_bf16 v[156:159],v[76:79],v[52:55],v[156:159]
ds_read_u16_d16_hi v138,v138
v_rcp_f16_sdwa v121,v120 dst_sel:WORD_1 dst_unused:UNUSED_PRESERVE src0_sel:WORD_1
v_mfma_f32_16x16x32_bf16 v[148:151],v[76:79],v[48:51],v[148:151]
ds_read_b64_tr_b16 v[102:103],v6 offset:20088
ds_read_b64_tr_b16 v[104:105],v6 offset:18608
v_rcp_f16_sdwa v123,v122 dst_sel:WORD_1 dst_unused:UNUSED_PRESERVE src0_sel:WORD_1
v_mfma_f32_16x16x32_bf16 v[144:147],v[80:83],v[56:59],v[144:147]
ds_read_u16 v137,v137
v_fma_mix_f32 v140,v140,v121,0 op_sel_hi:[0,1,0]
v_fma_mix_f32 v142,v142,v121,0 op_sel:[0,1,0] op_sel_hi:[0,1,0]
v_mfma_f32_16x16x32_bf16 v[152:155],v[80:83],v[60:63],v[152:155]
s_waitcnt lgkmcnt(8)
ds_read_b64_tr_b16 v[106:107],v6 offset:20144
ds_read_b64_tr_b16 v[108:109],v6 offset:18616
v_lshl_add_u64 v[128:129],v[128:129],0,v[130:131]
v_lshl_add_u64 v[130:131],v[132:133],0,v[134:135]
v_mfma_f32_16x16x32_bf16 v[156:159],v[84:87],v[60:63],v[156:159]
ds_read_u16_d16_hi v139,v139
v_fma_mix_f32 v141,v141,v123,0 op_sel_hi:[0,1,0]
v_fma_mix_f32 v143,v143,v123,0 op_sel:[0,1,0] op_sel_hi:[0,1,0]
v_mfma_f32_16x16x32_bf16 v[148:151],v[84:87],v[56:59],v[148:151]
ds_read_b64_tr_b16 v[110:111],v6 offset:20152
ds_read_b128 v[64:67],v7 offset:1536
v_lshlrev_b32_sdwa v144,1,v144 dst_sel:DWORD dst_unused:UNUSED_PAD src0_sel:DWORD src1_sel:WORD_1
v_lshlrev_b32_sdwa v146,1,v146 dst_sel:DWORD dst_unused:UNUSED_PAD src0_sel:DWORD src1_sel:WORD_1
v_mfma_f32_16x16x32_bf16 v[160:163],v[88:91],v[128:131],v[160:163]
ds_read_u16 v144,v144
ds_read_b128 v[68:71],v7 offset:1568
v_lshlrev_b32_sdwa v145,1,v145 dst_sel:DWORD dst_unused:UNUSED_PAD src0_sel:DWORD src1_sel:WORD_1
v_lshlrev_b32_sdwa v147,1,v147 dst_sel:DWORD dst_unused:UNUSED_PAD src0_sel:DWORD src1_sel:WORD_1
v_fma_mixlo_f16 v120,v156,s99,0
v_fma_mixlo_f16 v122,v157,s99,0
v_mfma_f32_16x16x32_bf16 v[168:171],v[92:95],v[128:131],v[168:171]
s_waitcnt lgkmcnt(12)
ds_read_u16_d16_hi v146,v146
ds_read_b128 v[72:75],v7 offset:1664
v_lshlrev_b32_sdwa v148,1,v148 dst_sel:DWORD dst_unused:UNUSED_PAD src0_sel:DWORD src1_sel:WORD_1
v_lshlrev_b32_sdwa v150,1,v150 dst_sel:DWORD dst_unused:UNUSED_PAD src0_sel:DWORD src1_sel:WORD_1
s_waitcnt lgkmcnt(6)
v_lshl_add_u64 v[136:137],v[136:137],0,v[138:139]
v_cvt_pk_bf16_f32 v138,v140,v142
v_mfma_f32_16x16x32_bf16 v[176:179],v[96:99],v[128:131],v[176:179]
s_setprio 1
ds_read_u16 v145,v145
ds_read_b128 v[76:79],v7 offset:1696
v_cvt_pk_bf16_f32 v139,v141,v143
v_fma_mixhi_f16 v120,v158,s99,0
v_fma_mixhi_f16 v122,v159,s99,0
v_lshlrev_b32_sdwa v149,1,v149 dst_sel:DWORD dst_unused:UNUSED_PAD src0_sel:DWORD src1_sel:WORD_1
v_mfma_f32_16x16x32_bf16 v[184:187],v[100:103],v[128:131],v[184:187]
ds_read_u16_d16_hi v147,v147
ds_read_b128 v[80:83],v7 offset:1792
v_lshlrev_b32_sdwa v151,1,v151 dst_sel:DWORD dst_unused:UNUSED_PAD src0_sel:DWORD src1_sel:WORD_1
v_pk_fma_f16 v121,v120,v120,s54
v_pk_fma_f16 v123,v122,v122,s54
v_pk_mul_f16 v121,v120,v121
v_mfma_f32_16x16x32_bf16 v[192:195],v[104:107],v[128:131],v[192:195]
s_waitcnt lgkmcnt(9)
ds_read_u16 v148,v148
ds_read_b128 v[84:87],v7 offset:1824
v_pk_mul_f16 v123,v122,v123
v_lshlrev_b32_sdwa v152,1,v152 dst_sel:DWORD dst_unused:UNUSED_PAD src0_sel:DWORD src1_sel:WORD_1
v_lshlrev_b32_sdwa v154,1,v154 dst_sel:DWORD dst_unused:UNUSED_PAD src0_sel:DWORD src1_sel:WORD_1
v_exp_f16 v120,v121
v_mfma_f32_16x16x32_bf16 v[200:203],v[108:111],v[128:131],v[200:203]
ds_read_u16_d16_hi v150,v150
v_exp_f16 v122,v123
v_lshlrev_b32_sdwa v153,1,v153 dst_sel:DWORD dst_unused:UNUSED_PAD src0_sel:DWORD src1_sel:WORD_1
v_mfma_f32_16x16x32_bf16 v[164:167],v[88:91],v[136:139],v[164:167]
ds_read_u16 v149,v149
v_lshlrev_b32_sdwa v155,1,v155 dst_sel:DWORD dst_unused:UNUSED_PAD src0_sel:DWORD src1_sel:WORD_1
v_exp_f16_sdwa v120,v121 dst_sel:WORD_1 dst_unused:UNUSED_PRESERVE src0_sel:WORD_1
v_exp_f16_sdwa v122,v123 dst_sel:WORD_1 dst_unused:UNUSED_PRESERVE src0_sel:WORD_1
v_mfma_f32_16x16x32_bf16 v[172:175],v[92:95],v[136:139],v[172:175]
ds_read_u16_d16_hi v151,v151
v_pk_fma_f16 v120,v120,s55,s55
v_pk_fma_f16 v122,v122,s55,s55
v_rcp_f16 v121,v120
v_mfma_f32_16x16x32_bf16 v[180:183],v[96:99],v[136:139],v[180:183]
ds_read_u16 v152,v152
v_rcp_f16 v123,v122
v_rcp_f16_sdwa v121,v120 dst_sel:WORD_1 dst_unused:UNUSED_PRESERVE src0_sel:WORD_1
v_mfma_f32_16x16x32_bf16 v[188:191],v[100:103],v[136:139],v[188:191]
ds_read_u16_d16_hi v154,v154
v_rcp_f16_sdwa v123,v122 dst_sel:WORD_1 dst_unused:UNUSED_PRESERVE src0_sel:WORD_1
v_fma_mix_f32 v156,v156,v121,0 op_sel_hi:[0,1,0]
v_mfma_f32_16x16x32_bf16 v[196:199],v[104:107],v[136:139],v[196:199]
s_setprio 0
ds_read_u16 v153,v153
v_fma_mix_f32 v158,v158,v121,0 op_sel:[0,1,0] op_sel_hi:[0,1,0]
s_waitcnt lgkmcnt(9)
v_lshl_add_u64 v[144:145],v[144:145],0,v[146:147]
s_waitcnt lgkmcnt(3)
v_lshl_add_u64 v[146:147],v[148:149],0,v[150:151]
v_fma_mix_f32 v157,v157,v123,0 op_sel_hi:[0,1,0]
v_mfma_f32_16x16x32_bf16 v[204:207],v[108:111],v[136:139],v[204:207]
ds_read_u16_d16_hi v155,v155
v_fma_mix_f32 v159,v159,v123,0 op_sel:[0,1,0] op_sel_hi:[0,1,0]
s_waitcnt lgkmcnt(0)
v_lshl_add_u64 v[152:153],v[152:153],0,v[154:155]
v_cvt_pk_bf16_f32 v154,v156,v158
v_cvt_pk_bf16_f32 v155,v157,v159
v_mfma_f32_16x16x32_bf16 v[208:211],v[88:91],v[144:147],v[208:211]
s_barrier
s_setprio 0
v_xor_b32 v9,64,v10
v_mul_f32 v160,v160,v64
v_mul_f32 v161,v161,v65
v_mul_f32 v162,v162,v66
v_mul_f32 v163,v163,v67
v_mfma_f32_16x16x32_bf16 v[216:219],v[92:95],v[144:147],v[216:219]
v_mul_f32 v168,v168,v68
v_mul_f32 v169,v169,v69
v_mul_f32 v170,v170,v70
v_mul_f32 v171,v171,v71
v_cvt_pk_bf16_f32 v16,v160,v161
v_mfma_f32_16x16x32_bf16 v[224:227],v[96:99],v[144:147],v[224:227]
v_cvt_pk_bf16_f32 v17,v162,v163
v_cvt_pk_bf16_f32 v18,v168,v169
v_cvt_pk_bf16_f32 v19,v170,v171
v_mul_f32_dpp v176,v176,v72 quad_perm:[1,0,3,2] row_mask:0xf bank_mask:0xf
v_mfma_f32_16x16x32_bf16 v[232:235],v[100:103],v[144:147],v[232:235]
v_mul_f32_dpp v177,v177,v73 quad_perm:[1,0,3,2] row_mask:0xf bank_mask:0xf
v_mul_f32_dpp v178,v178,v74 quad_perm:[1,0,3,2] row_mask:0xf bank_mask:0xf
v_mul_f32_dpp v179,v179,v75 quad_perm:[1,0,3,2] row_mask:0xf bank_mask:0xf
v_mul_f32_dpp v184,v184,v76 quad_perm:[1,0,3,2] row_mask:0xf bank_mask:0xf
v_mul_f32_dpp v185,v185,v77 quad_perm:[1,0,3,2] row_mask:0xf bank_mask:0xf
v_mfma_f32_16x16x32_bf16 v[240:243],v[104:107],v[144:147],v[240:243]
v_mul_f32_dpp v186,v186,v78 quad_perm:[1,0,3,2] row_mask:0xf bank_mask:0xf
v_mul_f32_dpp v187,v187,v79 quad_perm:[1,0,3,2] row_mask:0xf bank_mask:0xf
v_cvt_pk_bf16_f32 v24,v176,v177
v_cvt_pk_bf16_f32 v25,v178,v179
v_mfma_f32_16x16x32_bf16 v[248:251],v[108:111],v[144:147],v[248:251]
v_cvt_pk_bf16_f32 v26,v184,v185
v_cvt_pk_bf16_f32 v27,v186,v187
s_mov_b64 exec,s[12:13]
v_swap_b32 v16,v24
v_swap_b32 v17,v25
v_swap_b32 v18,v26
v_swap_b32 v19,v27
s_mov_b64 exec,-1
v_mfma_f32_16x16x32_bf16 v[212:215],v[88:91],v[152:155],v[212:215]
s_add_u32 s65,s61,1572864
buffer_store_dwordx4 v[16:19],v10,s[40:43],s65 offen offset:0 sc1
v_mfma_f32_16x16x32_bf16 v[220:223],v[92:95],v[152:155],v[220:223]
v_mul_f32 v192,v192,v80
v_mul_f32 v193,v193,v81
v_mul_f32 v194,v194,v82
v_mul_f32 v195,v195,v83
v_mfma_f32_16x16x32_bf16 v[228:231],v[96:99],v[152:155],v[228:231]
v_mul_f32 v200,v200,v84
v_mul_f32 v201,v201,v85
v_mul_f32 v202,v202,v86
v_mul_f32 v203,v203,v87
v_mfma_f32_16x16x32_bf16 v[236:239],v[100:103],v[152:155],v[236:239]
v_cvt_pk_bf16_f32 v32,v192,v193
v_cvt_pk_bf16_f32 v33,v194,v195
v_cvt_pk_bf16_f32 v34,v200,v201
v_cvt_pk_bf16_f32 v35,v202,v203
v_mul_f32_dpp v164,v164,v64 quad_perm:[1,0,3,2] row_mask:0xf bank_mask:0xf
v_mfma_f32_16x16x32_bf16 v[244:247],v[104:107],v[152:155],v[244:247]
v_mul_f32_dpp v165,v165,v65 quad_perm:[1,0,3,2] row_mask:0xf bank_mask:0xf
v_mul_f32_dpp v166,v166,v66 quad_perm:[1,0,3,2] row_mask:0xf bank_mask:0xf
v_mul_f32_dpp v167,v167,v67 quad_perm:[1,0,3,2] row_mask:0xf bank_mask:0xf
v_mul_f32_dpp v172,v172,v68 quad_perm:[1,0,3,2] row_mask:0xf bank_mask:0xf
v_mfma_f32_16x16x32_bf16 v[252:255],v[108:111],v[152:155],v[252:255]
s_add_u32 s65,s61,1572864
buffer_store_dwordx4 v[24:27],v9,s[40:43],s65 offen offset:384 sc1
v_mul_f32_dpp v173,v173,v69 quad_perm:[1,0,3,2] row_mask:0xf bank_mask:0xf
v_mul_f32_dpp v174,v174,v70 quad_perm:[1,0,3,2] row_mask:0xf bank_mask:0xf
v_mul_f32_dpp v175,v175,v71 quad_perm:[1,0,3,2] row_mask:0xf bank_mask:0xf
v_cvt_pk_bf16_f32 v20,v164,v165
v_cvt_pk_bf16_f32 v21,v166,v167
v_cvt_pk_bf16_f32 v22,v172,v173
v_cvt_pk_bf16_f32 v23,v174,v175
s_mov_b64 exec,s[12:13]
v_swap_b32 v32,v20
v_swap_b32 v33,v21
v_swap_b32 v34,v22
v_swap_b32 v35,v23
s_mov_b64 exec,-1
v_mul_f32 v180,v180,v72
v_mul_f32 v181,v181,v73
v_mul_f32 v182,v182,v74
v_mul_f32 v183,v183,v75
v_mul_f32 v188,v188,v76
v_mul_f32 v189,v189,v77
s_add_u32 s65,s61,1572864
buffer_store_dwordx4 v[32:35],v10,s[40:43],s65 offen offset:128 sc1
v_mul_f32 v190,v190,v78
v_mul_f32 v191,v191,v79
v_cvt_pk_bf16_f32 v28,v180,v181
v_cvt_pk_bf16_f32 v29,v182,v183
v_cvt_pk_bf16_f32 v30,v188,v189
v_cvt_pk_bf16_f32 v31,v190,v191
v_mul_f32_dpp v196,v196,v80 quad_perm:[1,0,3,2] row_mask:0xf bank_mask:0xf
v_mul_f32_dpp v197,v197,v81 quad_perm:[1,0,3,2] row_mask:0xf bank_mask:0xf
v_mul_f32_dpp v198,v198,v82 quad_perm:[1,0,3,2] row_mask:0xf bank_mask:0xf
v_mul_f32_dpp v199,v199,v83 quad_perm:[1,0,3,2] row_mask:0xf bank_mask:0xf
v_mul_f32_dpp v204,v204,v84 quad_perm:[1,0,3,2] row_mask:0xf bank_mask:0xf
v_mul_f32_dpp v205,v205,v85 quad_perm:[1,0,3,2] row_mask:0xf bank_mask:0xf
v_mul_f32_dpp v206,v206,v86 quad_perm:[1,0,3,2] row_mask:0xf bank_mask:0xf
v_mul_f32_dpp v207,v207,v87 quad_perm:[1,0,3,2] row_mask:0xf bank_mask:0xf
v_cvt_pk_bf16_f32 v36,v196,v197
v_cvt_pk_bf16_f32 v37,v198,v199
v_cvt_pk_bf16_f32 v38,v204,v205
v_cvt_pk_bf16_f32 v39,v206,v207
s_mov_b64 exec,s[12:13]
v_swap_b32 v28,v36
v_swap_b32 v29,v37
v_swap_b32 v30,v38
v_swap_b32 v31,v39
s_mov_b64 exec,-1
s_add_u32 s65,s61,1572864
buffer_store_dwordx4 v[20:23],v9,s[40:43],s65 offen offset:512 sc1
v_mul_f32 v208,v208,v64
v_mul_f32 v209,v209,v65
v_mul_f32 v210,v210,v66
v_mul_f32 v211,v211,v67
v_mul_f32 v216,v216,v68
v_mul_f32 v217,v217,v69
v_mul_f32 v218,v218,v70
v_mul_f32 v219,v219,v71
v_cvt_pk_bf16_f32 v40,v208,v209
v_cvt_pk_bf16_f32 v41,v210,v211
v_cvt_pk_bf16_f32 v42,v216,v217
v_cvt_pk_bf16_f32 v43,v218,v219
v_mul_f32_dpp v224,v224,v72 quad_perm:[1,0,3,2] row_mask:0xf bank_mask:0xf
v_mul_f32_dpp v225,v225,v73 quad_perm:[1,0,3,2] row_mask:0xf bank_mask:0xf
v_mul_f32_dpp v226,v226,v74 quad_perm:[1,0,3,2] row_mask:0xf bank_mask:0xf
v_mul_f32_dpp v227,v227,v75 quad_perm:[1,0,3,2] row_mask:0xf bank_mask:0xf
v_mul_f32_dpp v232,v232,v76 quad_perm:[1,0,3,2] row_mask:0xf bank_mask:0xf
v_mul_f32_dpp v233,v233,v77 quad_perm:[1,0,3,2] row_mask:0xf bank_mask:0xf
v_mul_f32_dpp v234,v234,v78 quad_perm:[1,0,3,2] row_mask:0xf bank_mask:0xf
v_mul_f32_dpp v235,v235,v79 quad_perm:[1,0,3,2] row_mask:0xf bank_mask:0xf
v_cvt_pk_bf16_f32 v48,v224,v225
v_cvt_pk_bf16_f32 v49,v226,v227
v_cvt_pk_bf16_f32 v50,v232,v233
v_cvt_pk_bf16_f32 v51,v234,v235
s_add_u32 s65,s61,1572864
buffer_store_dwordx4 v[28:31],v10,s[40:43],s65 offen offset:256 sc1
s_mov_b64 exec,s[12:13]
v_swap_b32 v40,v48
v_swap_b32 v41,v49
v_swap_b32 v42,v50
v_swap_b32 v43,v51
s_mov_b64 exec,-1
v_mul_f32 v240,v240,v80
v_mul_f32 v241,v241,v81
v_mul_f32 v242,v242,v82
v_mul_f32 v243,v243,v83
v_mul_f32 v248,v248,v84
v_mul_f32 v249,v249,v85
v_mul_f32 v250,v250,v86
v_mul_f32 v251,v251,v87
v_cvt_pk_bf16_f32 v56,v240,v241
s_mov_b32 s86,0xbe801d5a
v_cvt_pk_bf16_f32 v57,v242,v243
v_cvt_pk_bf16_f32 v58,v248,v249
v_cvt_pk_bf16_f32 v59,v250,v251
v_mul_f32_dpp v212,v212,v64 quad_perm:[1,0,3,2] row_mask:0xf bank_mask:0xf
v_mul_f32_dpp v213,v213,v65 quad_perm:[1,0,3,2] row_mask:0xf bank_mask:0xf
v_mul_f32_dpp v214,v214,v66 quad_perm:[1,0,3,2] row_mask:0xf bank_mask:0xf
v_mul_f32_dpp v215,v215,v67 quad_perm:[1,0,3,2] row_mask:0xf bank_mask:0xf
v_mul_f32_dpp v220,v220,v68 quad_perm:[1,0,3,2] row_mask:0xf bank_mask:0xf
s_add_u32 s65,s61,1572864
buffer_store_dwordx4 v[36:39],v9,s[40:43],s65 offen offset:640 sc1
v_mul_f32_dpp v221,v221,v69 quad_perm:[1,0,3,2] row_mask:0xf bank_mask:0xf
v_mul_f32_dpp v222,v222,v70 quad_perm:[1,0,3,2] row_mask:0xf bank_mask:0xf
v_mul_f32_dpp v223,v223,v71 quad_perm:[1,0,3,2] row_mask:0xf bank_mask:0xf
v_cvt_pk_bf16_f32 v44,v212,v213
v_cvt_pk_bf16_f32 v45,v214,v215
v_cvt_pk_bf16_f32 v46,v220,v221
v_cvt_pk_bf16_f32 v47,v222,v223
s_mov_b64 exec,s[12:13]
v_swap_b32 v56,v44
v_swap_b32 v57,v45
v_swap_b32 v58,v46
v_swap_b32 v59,v47
s_mov_b64 exec,-1
v_mul_f32 v228,v228,v72
v_mul_f32 v229,v229,v73
v_mul_f32 v230,v230,v74
v_mul_f32 v231,v231,v75
v_mul_f32 v236,v236,v76
v_mul_f32 v237,v237,v77
v_mul_f32 v238,v238,v78
v_mul_f32 v239,v239,v79
v_cvt_pk_bf16_f32 v52,v228,v229
v_cvt_pk_bf16_f32 v53,v230,v231
s_add_u32 s65,s61,1671168
buffer_store_dwordx4 v[40:43],v10,s[40:43],s65 offen offset:0 sc1
v_cvt_pk_bf16_f32 v54,v236,v237
v_cvt_pk_bf16_f32 v55,v238,v239
v_mul_f32_dpp v244,v244,v80 quad_perm:[1,0,3,2] row_mask:0xf bank_mask:0xf
v_mul_f32_dpp v245,v245,v81 quad_perm:[1,0,3,2] row_mask:0xf bank_mask:0xf
v_mul_f32_dpp v246,v246,v82 quad_perm:[1,0,3,2] row_mask:0xf bank_mask:0xf
v_mul_f32_dpp v247,v247,v83 quad_perm:[1,0,3,2] row_mask:0xf bank_mask:0xf
v_mul_f32_dpp v252,v252,v84 quad_perm:[1,0,3,2] row_mask:0xf bank_mask:0xf
v_mul_f32_dpp v253,v253,v85 quad_perm:[1,0,3,2] row_mask:0xf bank_mask:0xf
v_mul_f32_dpp v254,v254,v86 quad_perm:[1,0,3,2] row_mask:0xf bank_mask:0xf
v_mul_f32_dpp v255,v255,v87 quad_perm:[1,0,3,2] row_mask:0xf bank_mask:0xf
v_cvt_pk_bf16_f32 v60,v244,v245
v_cvt_pk_bf16_f32 v61,v246,v247
v_cvt_pk_bf16_f32 v62,v252,v253
v_cvt_pk_bf16_f32 v63,v254,v255
s_mov_b64 exec,s[12:13]
v_swap_b32 v52,v60
v_swap_b32 v53,v61
v_swap_b32 v54,v62
v_swap_b32 v55,v63
s_mov_b64 exec,-1
s_add_u32 s4,s10,8
s_cmp_ge_u32 s4,20
s_cselect_b32 s7,20,0
s_sub_u32 s4,s4,s7
s_mul_i32 s4,s4,3488
s_add_u32 s5,s10,10
s_cmp_ge_u32 s5,20
s_cselect_b32 s7,20,0
s_sub_u32 s5,s5,s7
s_mul_i32 s5,s5,3488
s_add_u32 s6,s10,12
s_cmp_ge_u32 s6,20
s_cselect_b32 s7,20,0
s_sub_u32 s6,s6,s7
s_mul_i32 s6,s6,3488
v_add_u32 v112,s4,v15
v_add_u32 v116,s5,v15
s_mov_b64 exec,s[8:9]
v_add_u32 v112,s5,v15
v_add_u32 v116,s6,v15
s_mov_b64 exec,-1
v_xor_b32 v113,16,v112
v_xor_b32 v117,16,v116
v_xor_b32 v114,32,v112
v_xor_b32 v118,32,v116
v_xor_b32 v115,48,v112
v_xor_b32 v119,48,v116
s_barrier
s_setprio 3
ds_read_b128 v[136:139],v116 offset:0
ds_read_b128 v[88:91],v11 offset:768
ds_read_b128 v[140:143],v116 offset:3488
ds_read_b128 v[128:131],v112 offset:0
ds_read_b128 v[132:135],v112 offset:3488
ds_read_b128 v[92:95],v12 offset:0
ds_read_b128 v[152:155],v117 offset:0
ds_read_b128 v[96:99],v11 offset:1920
ds_read_b128 v[156:159],v117 offset:3488
ds_read_b128 v[144:147],v113 offset:0
ds_read_b128 v[148:151],v113 offset:3488
ds_read_b128 v[100:103],v12 offset:1152
s_waitcnt lgkmcnt(10)
v_mfma_f32_16x16x32_bf16 v[160:163],v[88:91],v[136:139],0
s_waitcnt lgkmcnt(9)
s_add_u32 s65,s61,1671168
buffer_store_dwordx4 v[48:51],v9,s[40:43],s65 offen offset:384 sc1
v_mfma_f32_16x16x32_bf16 v[184:187],v[88:91],v[140:143],0
s_waitcnt lgkmcnt(6)
ds_read_b128 v[140:143],v116 offset:3552
ds_read_b128 v[88:91],v11 offset:5376
v_smfmac_f32_16x16x64_bf16 v[160:163],v[92:95],v[128:135],v13
ds_read_b128 v[128:131],v112 offset:64
v_smfmac_f32_16x16x64_bf16 v[184:187],v[92:95],v[132:139],v13
s_waitcnt lgkmcnt(7)
ds_read_b128 v[136:139],v116 offset:64
ds_read_b128 v[132:135],v112 offset:3552
v_mfma_f32_16x16x32_bf16 v[164:167],v[96:99],v[152:155],0
s_waitcnt lgkmcnt(8)
ds_read_b128 v[92:95],v12 offset:4608
v_add_f32 v120,v160,v161
v_mul_f32 v124,v160,v160
v_fmac_f32 v124,v161,v161
v_mfma_f32_16x16x32_bf16 v[188:191],v[96:99],v[156:159],0
s_waitcnt lgkmcnt(6)
ds_read_b128 v[156:159],v117 offset:3552
ds_read_b128 v[96:99],v11 offset:6528
v_add_f32 v121,v162,v163
v_mul_f32 v125,v162,v162
v_fmac_f32 v125,v163,v163
v_smfmac_f32_16x16x64_bf16 v[164:167],v[100:103],v[144:151],v13
s_add_u32 s65,s61,1671168
buffer_store_dwordx4 v[56:59],v10,s[40:43],s65 offen offset:128 sc1
ds_read_b128 v[144:147],v113 offset:64
v_add_f32 v122,v184,v185
v_mul_f32 v126,v184,v184
v_fmac_f32 v126,v185,v185
v_smfmac_f32_16x16x64_bf16 v[188:191],v[100:103],v[148:155],v13
s_waitcnt lgkmcnt(5)
ds_read_b128 v[152:155],v117 offset:64
ds_read_b128 v[148:151],v113 offset:3552
v_add_f32 v123,v186,v187
v_mul_f32 v127,v186,v186
v_fmac_f32 v127,v187,v187
v_mfma_f32_16x16x32_bf16 v[168:171],v[88:91],v[136:139],0
ds_read_b128 v[100:103],v12 offset:5760
v_add_f32 v120,v120,v164
v_fmac_f32 v124,v164,v164
v_add_f32 v120,v120,v165
v_fmac_f32 v124,v165,v165
v_mfma_f32_16x16x32_bf16 v[192:195],v[88:91],v[140:143],0
s_waitcnt lgkmcnt(6)
ds_read_b128 v[140:143],v116 offset:3616
ds_read_b128 v[88:91],v11 offset:9984
v_add_f32 v121,v121,v166
v_fmac_f32 v125,v166,v166
v_add_f32 v121,v121,v167
v_fmac_f32 v125,v167,v167
v_smfmac_f32_16x16x64_bf16 v[168:171],v[92:95],v[128:135],v13
s_add_u32 s65,s61,1671168
buffer_store_dwordx4 v[44:47],v9,s[40:43],s65 offen offset:512 sc1
ds_read_b128 v[128:131],v112 offset:128
v_add_f32 v122,v122,v188
v_fmac_f32 v126,v188,v188
v_add_f32 v122,v122,v189
v_fmac_f32 v126,v189,v189
v_smfmac_f32_16x16x64_bf16 v[192:195],v[92:95],v[132:139],v13
s_waitcnt lgkmcnt(5)
ds_read_b128 v[136:139],v116 offset:128
ds_read_b128 v[132:135],v112 offset:3616
v_add_f32 v123,v123,v190
v_fmac_f32 v127,v190,v190
v_add_f32 v123,v123,v191
v_fmac_f32 v127,v191,v191
v_mfma_f32_16x16x32_bf16 v[172:175],v[96:99],v[152:155],0
ds_read_b128 v[92:95],v12 offset:9216
v_add_f32 v120,v120,v168
v_fmac_f32 v124,v168,v168
v_add_f32 v120,v120,v169
v_fmac_f32 v124,v169,v169
v_mfma_f32_16x16x32_bf16 v[196:199],v[96:99],v[156:159],0
s_waitcnt lgkmcnt(6)
ds_read_b128 v[156:159],v117 offset:3616
ds_read_b128 v[96:99],v11 offset:11136
v_add_f32 v121,v121,v170
v_fmac_f32 v125,v170,v170
v_add_f32 v121,v121,v171
v_fmac_f32 v125,v171,v171
v_smfmac_f32_16x16x64_bf16 v[172:175],v[100:103],v[144:151],v13
s_add_u32 s65,s61,1671168
buffer_store_dwordx4 v[52:55],v10,s[40:43],s65 offen offset:256 sc1
ds_read_b128 v[144:147],v113 offset:128
v_add_f32 v122,v122,v192
v_fmac_f32 v126,v192,v192
v_add_f32 v122,v122,v193
v_fmac_f32 v126,v193,v193
v_smfmac_f32_16x16x64_bf16 v[196:199],v[100:103],v[148:155],v13
s_waitcnt lgkmcnt(5)
ds_read_b128 v[152:155],v117 offset:128
ds_read_b128 v[148:151],v113 offset:3616
v_add_f32 v123,v123,v194
v_fmac_f32 v127,v194,v194
v_add_f32 v123,v123,v195
v_fmac_f32 v127,v195,v195
v_mfma_f32_16x16x32_bf16 v[176:179],v[88:91],v[136:139],0
ds_read_b128 v[100:103],v12 offset:10368
v_add_f32 v120,v120,v172
v_fmac_f32 v124,v172,v172
v_add_f32 v120,v120,v173
v_fmac_f32 v124,v173,v173
v_mfma_f32_16x16x32_bf16 v[200:203],v[88:91],v[140:143],0
s_waitcnt lgkmcnt(6)
ds_read_b128 v[140:143],v118 offset:3488
ds_read_b128 v[88:91],v11 offset:3072
v_add_f32 v121,v121,v174
v_fmac_f32 v125,v174,v174
v_add_f32 v121,v121,v175
v_fmac_f32 v125,v175,v175
v_smfmac_f32_16x16x64_bf16 v[176:179],v[92:95],v[128:135],v13
s_add_u32 s65,s61,1671168
buffer_store_dwordx4 v[60:63],v9,s[40:43],s65 offen offset:640 sc1
ds_read_b128 v[128:131],v114 offset:0
v_add_f32 v122,v122,v196
v_fmac_f32 v126,v196,v196
v_add_f32 v122,v122,v197
v_fmac_f32 v126,v197,v197
v_smfmac_f32_16x16x64_bf16 v[200:203],v[92:95],v[132:139],v13
s_waitcnt lgkmcnt(5)
ds_read_b128 v[136:139],v118 offset:0
ds_read_b128 v[132:135],v114 offset:3488
v_add_f32 v123,v123,v198
v_fmac_f32 v127,v198,v198
v_add_f32 v123,v123,v199
v_fmac_f32 v127,v199,v199
v_mfma_f32_16x16x32_bf16 v[180:183],v[96:99],v[152:155],0
ds_read_b128 v[92:95],v12 offset:2304
v_add_f32 v120,v120,v176
v_fmac_f32 v124,v176,v176
v_add_f32 v120,v120,v177
v_fmac_f32 v124,v177,v177
v_mfma_f32_16x16x32_bf16 v[204:207],v[96:99],v[156:159],0
s_waitcnt lgkmcnt(6)
ds_read_b128 v[156:159],v119 offset:3488
ds_read_b128 v[96:99],v11 offset:4224
v_add_f32 v121,v121,v178
v_fmac_f32 v125,v178,v178
v_add_f32 v121,v121,v179
v_fmac_f32 v125,v179,v179
v_smfmac_f32_16x16x64_bf16 v[180:183],v[100:103],v[144:151],v13
ds_read_b128 v[144:147],v115 offset:0
v_add_f32 v122,v122,v200
v_fmac_f32 v126,v200,v200
v_add_f32 v122,v122,v201
v_fmac_f32 v126,v201,v201
v_smfmac_f32_16x16x64_bf16 v[204:207],v[100:103],v[148:155],v13
s_waitcnt lgkmcnt(5)
ds_read_b128 v[152:155],v119 offset:0
ds_read_b128 v[148:151],v115 offset:3488
v_add_f32 v123,v123,v202
v_fmac_f32 v127,v202,v202
v_add_f32 v123,v123,v203
v_fmac_f32 v127,v203,v203
v_mfma_f32_16x16x32_bf16 v[208:211],v[88:91],v[136:139],0
ds_read_b128 v[100:103],v12 offset:3456
v_add_f32 v120,v120,v180
v_fmac_f32 v124,v180,v180
v_add_f32 v120,v120,v181
v_fmac_f32 v124,v181,v181
v_mfma_f32_16x16x32_bf16 v[232:235],v[88:91],v[140:143],0
s_waitcnt lgkmcnt(6)
ds_read_b128 v[140:143],v118 offset:3552
ds_read_b128 v[88:91],v11 offset:7680
v_add_f32 v121,v121,v182
v_fmac_f32 v125,v182,v182
v_add_f32 v121,v121,v183
v_fmac_f32 v125,v183,v183
v_smfmac_f32_16x16x64_bf16 v[208:211],v[92:95],v[128:135],v13
ds_read_b128 v[128:131],v114 offset:64
v_add_f32 v122,v122,v204
v_fmac_f32 v126,v204,v204
v_add_f32 v122,v122,v205
v_fmac_f32 v126,v205,v205
v_smfmac_f32_16x16x64_bf16 v[232:235],v[92:95],v[132:139],v13
s_waitcnt lgkmcnt(5)
ds_read_b128 v[136:139],v118 offset:64
ds_read_b128 v[132:135],v114 offset:3552
v_add_f32 v123,v123,v206
v_fmac_f32 v127,v206,v206
v_add_f32 v123,v123,v207
v_fmac_f32 v127,v207,v207
v_mfma_f32_16x16x32_bf16 v[212:215],v[96:99],v[152:155],0
ds_read_b128 v[92:95],v12 offset:6912
v_add_f32 v120,v120,v208
v_fmac_f32 v124,v208,v208
v_add_f32 v120,v120,v209
v_fmac_f32 v124,v209,v209
v_mfma_f32_16x16x32_bf16 v[236:239],v[96:99],v[156:159],0
s_waitcnt lgkmcnt(6)
s_setprio 2
ds_read_b128 v[156:159],v119 offset:3552
ds_read_b128 v[96:99],v11 offset:8832
v_add_f32 v121,v121,v210
v_fmac_f32 v125,v210,v210
v_add_f32 v121,v121,v211
v_fmac_f32 v125,v211,v211
v_smfmac_f32_16x16x64_bf16 v[212:215],v[100:103],v[144:151],v13
ds_read_b128 v[144:147],v115 offset:64
v_add_f32 v122,v122,v232
v_fmac_f32 v126,v232,v232
v_add_f32 v122,v122,v233
v_fmac_f32 v126,v233,v233
v_smfmac_f32_16x16x64_bf16 v[236:239],v[100:103],v[148:155],v13
s_waitcnt lgkmcnt(5)
ds_read_b128 v[152:155],v119 offset:64
ds_read_b128 v[148:151],v115 offset:3552
v_add_f32 v123,v123,v234
v_fmac_f32 v127,v234,v234
v_add_f32 v123,v123,v235
v_fmac_f32 v127,v235,v235
v_mfma_f32_16x16x32_bf16 v[216:219],v[88:91],v[136:139],0
ds_read_b128 v[100:103],v12 offset:8064
v_add_f32 v120,v120,v212
v_fmac_f32 v124,v212,v212
v_add_f32 v120,v120,v213
v_fmac_f32 v124,v213,v213
v_add_f32 v121,v121,v214
v_mfma_f32_16x16x32_bf16 v[240:243],v[88:91],v[140:143],0
s_waitcnt lgkmcnt(6)
ds_read_b128 v[140:143],v118 offset:3616
ds_read_b128 v[88:91],v11 offset:12288
v_fmac_f32 v125,v214,v214
v_add_f32 v121,v121,v215
v_fmac_f32 v125,v215,v215
v_add_f32 v122,v122,v236
v_smfmac_f32_16x16x64_bf16 v[216:219],v[92:95],v[128:135],v13
ds_read_b128 v[128:131],v114 offset:128
v_fmac_f32 v126,v236,v236
v_add_f32 v122,v122,v237
v_fmac_f32 v126,v237,v237
v_add_f32 v123,v123,v238
v_fmac_f32 v127,v238,v238
v_smfmac_f32_16x16x64_bf16 v[240:243],v[92:95],v[132:139],v13
s_waitcnt lgkmcnt(5)
ds_read_b128 v[136:139],v118 offset:128
ds_read_b128 v[132:135],v114 offset:3616
v_add_f32 v123,v123,v239
v_fmac_f32 v127,v239,v239
v_add_u32 v112,v112,v8
v_add_u32 v116,v116,v8
v_mfma_f32_16x16x32_bf16 v[220:223],v[96:99],v[152:155],0
ds_read_b128 v[92:95],v12 offset:11520
ds_read_b128 v[104:107],v14 offset:0
v_add_f32 v120,v120,v216
v_fmac_f32 v124,v216,v216
v_add_f32 v120,v120,v217
v_fmac_f32 v124,v217,v217
v_mfma_f32_16x16x32_bf16 v[244:247],v[96:99],v[156:159],0
s_waitcnt lgkmcnt(7)
ds_read_b128 v[108:111],v7 offset:3584
ds_read_b128 v[156:159],v119 offset:3616
v_add_f32 v121,v121,v218
v_fmac_f32 v125,v218,v218
v_add_f32 v121,v121,v219
v_fmac_f32 v125,v219,v219
v_smfmac_f32_16x16x64_bf16 v[220:223],v[100:103],v[144:151],v13
ds_read_b128 v[96:99],v11 offset:13440
ds_read_b128 v[144:147],v115 offset:128
v_add_f32 v122,v122,v240
v_fmac_f32 v126,v240,v240
v_add_f32 v122,v122,v241
v_fmac_f32 v126,v241,v241
v_smfmac_f32_16x16x64_bf16 v[244:247],v[100:103],v[148:155],v13
s_waitcnt lgkmcnt(7)
ds_read_b128 v[152:155],v119 offset:128
ds_read_b128 v[148:151],v115 offset:3616
v_add_f32 v123,v123,v242
v_fmac_f32 v127,v242,v242
v_add_f32 v123,v123,v243
v_fmac_f32 v127,v243,v243
v_mfma_f32_16x16x32_bf16 v[224:227],v[88:91],v[136:139],0
ds_read_b128 v[100:103],v12 offset:12672
v_add_f32 v120,v120,v220
v_fmac_f32 v124,v220,v220
v_add_f32 v120,v120,v221
v_fmac_f32 v124,v221,v221
v_mfma_f32_16x16x32_bf16 v[248:251],v[88:91],v[140:143],0
s_waitcnt lgkmcnt(8)
s_setprio 1
ds_read_b128 v[140:143],v116 offset:192
ds_read_b128 v[88:91],v112 offset:3616
v_add_f32 v121,v121,v222
v_fmac_f32 v125,v222,v222
v_add_f32 v121,v121,v223
v_fmac_f32 v125,v223,v223
v_smfmac_f32_16x16x64_bf16 v[224:227],v[92:95],v[128:135],v13
ds_read_b128 v[128:131],v112 offset:3488
v_add_f32 v122,v122,v244
v_fmac_f32 v126,v244,v244
v_add_f32 v122,v122,v245
v_fmac_f32 v126,v245,v245
v_smfmac_f32_16x16x64_bf16 v[248:251],v[92:95],v[132:139],v13
s_waitcnt lgkmcnt(5)
ds_read_b128 v[136:139],v116 offset:0
ds_read_b128 v[132:135],v112 offset:3680
v_add_f32 v123,v123,v246
v_fmac_f32 v127,v246,v246
v_add_f32 v123,v123,v247
v_fmac_f32 v127,v247,v247
v_mfma_f32_16x16x32_bf16 v[228:231],v[96:99],v[152:155],0
ds_read_b128 v[92:95],v112 offset:3808
v_add_f32 v120,v120,v224
v_fmac_f32 v124,v224,v224
v_add_f32 v120,v120,v225
v_fmac_f32 v124,v225,v225
v_mfma_f32_16x16x32_bf16 v[252:255],v[96:99],v[156:159],0
s_waitcnt lgkmcnt(6)
ds_read_b128 v[156:159],v116 offset:256
ds_read_b128 v[96:99],v116 offset:128
v_add_f32 v121,v121,v226
v_fmac_f32 v125,v226,v226
v_add_f32 v121,v121,v227
v_fmac_f32 v125,v227,v227
v_smfmac_f32_16x16x64_bf16 v[228:231],v[100:103],v[144:151],v13
ds_read_b128 v[144:147],v112 offset:3552
v_add_f32 v122,v122,v248
v_fmac_f32 v126,v248,v248
v_add_f32 v122,v122,v249
v_fmac_f32 v126,v249,v249
v_smfmac_f32_16x16x64_bf16 v[252:255],v[100:103],v[148:155],v13
s_setprio 0
ds_read_b128 v[152:155],v116 offset:64
ds_read_b128 v[148:151],v112 offset:3744
v_add_f32 v123,v123,v250
v_fmac_f32 v127,v250,v250
v_add_f32 v123,v123,v251
v_fmac_f32 v127,v251,v251
ds_read_b128 v[100:103],v116 offset:320
v_add_f32 v120,v120,v228
v_fmac_f32 v124,v228,v228
v_add_f32 v120,v120,v229
v_fmac_f32 v124,v229,v229
v_add_f32 v121,v121,v230
v_fmac_f32 v125,v230,v230
v_add_f32 v121,v121,v231
v_fmac_f32 v125,v231,v231
s_waitcnt lgkmcnt(0)
s_barrier
s_setprio 0
v_add_f32 v122,v122,v252
v_fmac_f32 v126,v252,v252
v_add_f32 v122,v122,v253
v_fmac_f32 v126,v253,v253
v_add_f32 v123,v123,v254
v_fmac_f32 v127,v254,v254
v_add_f32 v123,v123,v255
v_fmac_f32 v127,v255,v255
s_setprio 3
ds_read_b128 v[56:59],v14 offset:256
ds_read_b128 v[60:63],v7 offset:3616
s_setprio 0
v_permlane32_swap_b32 v120,v122
v_permlane32_swap_b32 v121,v123
v_permlane32_swap_b32 v124,v126
v_permlane32_swap_b32 v125,v127
v_add_f32 v120,v120,v122
v_add_f32 v121,v121,v123
v_add_f32 v124,v124,v126
v_add_f32 v125,v125,v127
v_permlane16_swap_b32 v120,v121
s_nop 0
v_permlane16_swap_b32 v124,v125
v_add_f32 v113,v120,v121
v_add_f32 v114,v124,v125
v_mul_f32 v113,0x3c2aaaab,v113
v_mul_f32 v114,0x3c2aaaab,v114
v_mul_f32 v115,0x5e000000,v114
v_fma_f32 v114,-v113,v113,v114
v_pk_maximum3_f16 v1,v1,v115,v115 op_sel:[0,1,1] op_sel_hi:[1,1,1]
v_max_f32 v114,0,v114
v_add_f32 v114,0x3727c5ac,v114
v_rsq_f32 v114,v114
s_nop 0
v_mul_f32 v120,0x8befe000,v114
v_mul_f32 v121,0x8befe000,v114
v_mul_f32 v124,-v113,v120
v_mul_f32 v125,-v113,v120
v_permlane16_swap_b32 v120,v121
s_nop 0
v_permlane16_swap_b32 v124,v125
v_mov_b32 v122,v120
v_mov_b32 v123,v121
v_mov_b32 v126,v124
v_mov_b32 v127,v125
v_permlane32_swap_b32 v120,v122
v_permlane32_swap_b32 v124,v126
v_permlane32_swap_b32 v121,v123
v_permlane32_swap_b32 v125,v127
v_add_u32 v8,94432,v4
v_add_u32 v9,94432,v5
v_fma_f32 v160,v160,v120,v124
s_setprio 3
ds_read_b64_tr_b16 v[64:65],v4 offset:512
s_setprio 0
v_fma_f32 v161,v161,v120,v124
s_setprio 3
ds_read_b64_tr_b16 v[66:67],v4 offset:2560
s_setprio 0
v_fma_f32 v162,v162,v121,v125
s_setprio 3
ds_read_b128 v[112:115],v7 offset:384
s_setprio 0
v_fma_f32 v163,v163,v121,v125
s_setprio 3
ds_read_b64_tr_b16 v[68:69],v5 offset:512
s_setprio 0
v_fma_f32 v164,v164,v120,v124
s_setprio 3
ds_read_b64_tr_b16 v[70:71],v5 offset:2560
s_setprio 0
v_fma_f32 v165,v165,v120,v124
s_setprio 3
ds_read_b128 v[116:119],v7 offset:416
s_setprio 0
v_fma_f32 v166,v166,v121,v125
s_setprio 3
ds_read_b64_tr_b16 v[72:73],v4 offset:4608
s_setprio 0
v_fma_f32 v167,v167,v121,v125
s_setprio 3
ds_read_b64_tr_b16 v[74:75],v4 offset:6656
s_setprio 0
v_fma_f32 v208,v208,v120,v124
s_setprio 3
ds_read_b64_tr_b16 v[76:77],v5 offset:4608
s_setprio 0
v_fma_f32 v209,v209,v120,v124
s_setprio 3
ds_read_b64_tr_b16 v[78:79],v5 offset:6656
s_setprio 0
v_fma_f32 v210,v210,v121,v125
s_setprio 3
ds_read_b64_tr_b16 v[80:81],v4 offset:8704
s_setprio 0
v_fma_f32 v211,v211,v121,v125
s_setprio 3
ds_read_b64_tr_b16 v[82:83],v4 offset:10752
s_setprio 0
v_fma_f32 v212,v212,v120,v124
s_setprio 3
ds_read_b64_tr_b16 v[84:85],v5 offset:8704
s_setprio 0
v_fma_f32 v213,v213,v120,v124
s_setprio 3
ds_read_b64_tr_b16 v[86:87],v5 offset:10752
s_setprio 0
v_fma_f32 v214,v214,v121,v125
v_fma_f32 v215,v215,v121,v125
v_cvt_pk_bf16_f32 v16,v160,v161
v_cvt_pk_bf16_f32 v20,v162,v163
v_cvt_pk_bf16_f32 v17,v164,v165
v_cvt_pk_bf16_f32 v21,v166,v167
v_cvt_pk_bf16_f32 v18,v208,v209
v_cvt_pk_bf16_f32 v22,v210,v211
v_cvt_pk_bf16_f32 v19,v212,v213
v_cvt_pk_bf16_f32 v23,v214,v215
v_fma_f32 v168,v168,v120,v124
v_fma_f32 v169,v169,v120,v124
v_mfma_f32_16x16x32_bf16 v[160:163],v[104:107],v[128:131],v[108:111]
v_fma_f32 v170,v170,v121,v125
v_fma_f32 v171,v171,v121,v125
v_fma_f32 v172,v172,v120,v124
v_fma_f32 v173,v173,v120,v124
v_fma_f32 v174,v174,v121,v125
v_fma_f32 v175,v175,v121,v125
v_mfma_f32_16x16x32_bf16 v[164:167],v[104:107],v[132:135],v[108:111]
v_fma_f32 v216,v216,v120,v124
v_fma_f32 v217,v217,v120,v124
v_fma_f32 v218,v218,v121,v125
v_fma_f32 v219,v219,v121,v125
v_fma_f32 v220,v220,v120,v124
v_mfma_f32_16x16x32_bf16 v[208:211],v[104:107],v[136:139],v[108:111]
v_fma_f32 v221,v221,v120,v124
v_fma_f32 v222,v222,v121,v125
v_fma_f32 v223,v223,v121,v125
v_cvt_pk_bf16_f32 v24,v168,v169
v_cvt_pk_bf16_f32 v28,v170,v171
s_setprio 3
v_mfma_f32_16x16x32_bf16 v[212:215],v[104:107],v[140:143],v[108:111]
ds_read_b128 v[104:107],v14 offset:512
s_setprio 0
v_cvt_pk_bf16_f32 v25,v172,v173
v_cvt_pk_bf16_f32 v29,v174,v175
v_cvt_pk_bf16_f32 v26,v216,v217
v_cvt_pk_bf16_f32 v30,v218,v219
v_cvt_pk_bf16_f32 v27,v220,v221
v_cvt_pk_bf16_f32 v31,v222,v223
v_fma_f32 v176,v176,v120,v124
v_fma_f32 v177,v177,v120,v124
s_waitcnt lgkmcnt(15)
s_setprio 3
v_mfma_f32_16x16x32_bf16 v[168:171],v[56:59],v[128:131],v[60:63]
ds_read_b128 v[108:111],v7 offset:3712
s_setprio 0
v_fma_f32 v178,v178,v121,v125
v_fma_f32 v179,v179,v121,v125
v_fma_f32 v180,v180,v120,v124
v_fma_f32 v181,v181,v120,v124
v_fma_f32 v182,v182,v121,v125
v_fma_f32 v183,v183,v121,v125
v_mfma_f32_16x16x32_bf16 v[172:175],v[56:59],v[132:135],v[60:63]
v_fma_f32 v224,v224,v120,v124
v_fma_f32 v225,v225,v120,v124
v_fma_f32 v226,v226,v121,v125
v_fma_f32 v227,v227,v121,v125
v_fma_f32 v228,v228,v120,v124
v_mfma_f32_16x16x32_bf16 v[216:219],v[56:59],v[136:139],v[60:63]
v_fma_f32 v229,v229,v120,v124
v_fma_f32 v230,v230,v121,v125
v_fma_f32 v231,v231,v121,v125
v_cvt_pk_bf16_f32 v32,v176,v177
v_cvt_pk_bf16_f32 v36,v178,v179
s_setprio 3
v_mfma_f32_16x16x32_bf16 v[220:223],v[56:59],v[140:143],v[60:63]
ds_read_b128 v[56:59],v14 offset:768
s_setprio 0
v_cvt_pk_bf16_f32 v33,v180,v181
v_cvt_pk_bf16_f32 v37,v182,v183
v_cvt_pk_bf16_f32 v34,v224,v225
v_cvt_pk_bf16_f32 v38,v226,v227
v_cvt_pk_bf16_f32 v35,v228,v229
v_cvt_pk_bf16_f32 v39,v230,v231
v_fma_f32 v184,v184,v122,v126
s_waitcnt lgkmcnt(1)
s_setprio 3
v_mfma_f32_16x16x32_bf16 v[176:179],v[104:107],v[144:147],v[108:111]
ds_read_b128 v[60:63],v7 offset:3744
s_setprio 0
v_fma_f32 v185,v185,v122,v126
v_fma_f32 v186,v186,v123,v127
v_fma_f32 v187,v187,v123,v127
v_mfma_f32_16x16x32_bf16 v[128:131],v[64:67],v[16:19],v[112:115]
v_fma_f32 v188,v188,v122,v126
v_fma_f32 v189,v189,v122,v126
v_fma_f32 v190,v190,v123,v127
v_mfma_f32_16x16x32_bf16 v[180:183],v[104:107],v[148:151],v[108:111]
v_fma_f32 v191,v191,v123,v127
v_fma_f32 v232,v232,v122,v126
v_fma_f32 v233,v233,v122,v126
v_mfma_f32_16x16x32_bf16 v[136:139],v[64:67],v[20:23],v[112:115]
v_fma_f32 v234,v234,v123,v127
v_fma_f32 v235,v235,v123,v127
v_fma_f32 v236,v236,v122,v126
v_mfma_f32_16x16x32_bf16 v[224:227],v[104:107],v[152:155],v[108:111]
v_fma_f32 v237,v237,v122,v126
v_fma_f32 v238,v238,v123,v127
v_fma_f32 v239,v239,v123,v127
v_mfma_f32_16x16x32_bf16 v[140:143],v[68:71],v[20:23],v[116:119]
v_cvt_pk_bf16_f32 v40,v184,v185
v_cvt_pk_bf16_f32 v44,v186,v187
v_cvt_pk_bf16_f32 v41,v188,v189
s_setprio 3
v_mfma_f32_16x16x32_bf16 v[228:231],v[104:107],v[156:159],v[108:111]
ds_read_b128 v[104:107],v14 offset:1024
s_setprio 0
v_cvt_pk_bf16_f32 v45,v190,v191
v_cvt_pk_bf16_f32 v42,v232,v233
v_cvt_pk_bf16_f32 v46,v234,v235
s_setprio 3
v_mfma_f32_16x16x32_bf16 v[132:135],v[68:71],v[16:19],v[116:119]
ds_read_b128 v[108:111],v7 offset:3840
s_setprio 0
v_cvt_pk_bf16_f32 v43,v236,v237
v_cvt_pk_bf16_f32 v47,v238,v239
v_fma_f32 v192,v192,v122,v126
s_waitcnt lgkmcnt(2)
v_mfma_f32_16x16x32_bf16 v[184:187],v[56:59],v[144:147],v[60:63]
v_fma_f32 v193,v193,v122,v126
v_fma_f32 v194,v194,v123,v127
v_mfma_f32_16x16x32_bf16 v[128:131],v[72:75],v[24:27],v[128:131]
v_fma_f32 v195,v195,v123,v127
v_fma_f32 v196,v196,v122,v126
v_mfma_f32_16x16x32_bf16 v[188:191],v[56:59],v[148:151],v[60:63]
v_fma_f32 v197,v197,v122,v126
v_fma_f32 v198,v198,v123,v127
v_mfma_f32_16x16x32_bf16 v[136:139],v[72:75],v[28:31],v[136:139]
v_fma_f32 v199,v199,v123,v127
v_fma_f32 v240,v240,v122,v126
s_mov_b32 s86,0xbe801d5a
v_mfma_f32_16x16x32_bf16 v[232:235],v[56:59],v[152:155],v[60:63]
v_fma_f32 v241,v241,v122,v126
v_fma_f32 v242,v242,v123,v127
v_mfma_f32_16x16x32_bf16 v[140:143],v[76:79],v[28:31],v[140:143]
v_fma_f32 v243,v243,v123,v127
v_fma_f32 v244,v244,v122,v126
s_setprio 3
v_mfma_f32_16x16x32_bf16 v[236:239],v[56:59],v[156:159],v[60:63]
ds_read_b128 v[144:147],v14 offset:1280
s_setprio 0
v_fma_f32 v245,v245,v122,v126
v_fma_f32 v246,v246,v123,v127
s_setprio 3
v_mfma_f32_16x16x32_bf16 v[132:135],v[76:79],v[24:27],v[132:135]
ds_read_b128 v[148:151],v7 offset:3872
s_setprio 0
v_fma_f32 v247,v247,v123,v127
v_cvt_pk_bf16_f32 v48,v192,v193
v_cvt_pk_bf16_f32 v52,v194,v195
v_cvt_pk_bf16_f32 v49,v196,v197
v_cvt_pk_bf16_f32 v53,v198,v199
v_cvt_pk_bf16_f32 v50,v240,v241
v_cvt_pk_bf16_f32 v54,v242,v243
v_cvt_pk_bf16_f32 v51,v244,v245
v_cvt_pk_bf16_f32 v55,v246,v247
v_fma_f32 v200,v200,v122,v126
s_waitcnt lgkmcnt(2)
v_mfma_f32_16x16x32_bf16 v[192:195],v[104:107],v[88:91],v[108:111]
v_fma_f32 v201,v201,v122,v126
v_fma_f32 v202,v202,v123,v127
v_fma_f32 v203,v203,v123,v127
v_mfma_f32_16x16x32_bf16 v[128:131],v[80:83],v[32:35],v[128:131]
v_fma_f32 v204,v204,v122,v126
v_fma_f32 v205,v205,v122,v126
v_fma_f32 v206,v206,v123,v127
v_mfma_f32_16x16x32_bf16 v[196:199],v[104:107],v[92:95],v[108:111]
v_fma_f32 v207,v207,v123,v127
v_fma_f32 v248,v248,v122,v126
v_fma_f32 v249,v249,v122,v126
v_mfma_f32_16x16x32_bf16 v[136:139],v[80:83],v[36:39],v[136:139]
v_fma_f32 v250,v250,v123,v127
v_fma_f32 v251,v251,v123,v127
v_fma_f32 v252,v252,v122,v126
v_mfma_f32_16x16x32_bf16 v[240:243],v[104:107],v[96:99],v[108:111]
v_fma_f32 v253,v253,v122,v126
v_fma_f32 v254,v254,v123,v127
v_fma_f32 v255,v255,v123,v127
v_mfma_f32_16x16x32_bf16 v[140:143],v[84:87],v[36:39],v[140:143]
v_cvt_pk_bf16_f32 v56,v200,v201
v_cvt_pk_bf16_f32 v60,v202,v203
v_cvt_pk_bf16_f32 v57,v204,v205
v_mfma_f32_16x16x32_bf16 v[244:247],v[104:107],v[100:103],v[108:111]
v_cvt_pk_bf16_f32 v61,v206,v207
v_cvt_pk_bf16_f32 v58,v248,v249
v_cvt_pk_bf16_f32 v62,v250,v251
v_mfma_f32_16x16x32_bf16 v[132:135],v[84:87],v[32:35],v[132:135]
v_cvt_pk_bf16_f32 v59,v252,v253
v_cvt_pk_bf16_f32 v63,v254,v255
s_waitcnt lgkmcnt(0)
v_mfma_f32_16x16x32_bf16 v[200:203],v[144:147],v[88:91],v[148:151]
s_barrier
s_setprio 3
ds_read_b64_tr_b16 v[104:105],v6 offset:12448
v_lshlrev_b32_sdwa v128,1,v128 dst_sel:DWORD dst_unused:UNUSED_PAD src0_sel:DWORD src1_sel:WORD_1
v_lshlrev_b32_sdwa v130,1,v130 dst_sel:DWORD dst_unused:UNUSED_PAD src0_sel:DWORD src1_sel:WORD_1
v_lshlrev_b32_sdwa v129,1,v129 dst_sel:DWORD dst_unused:UNUSED_PAD src0_sel:DWORD src1_sel:WORD_1
v_mfma_f32_16x16x32_bf16 v[204:207],v[144:147],v[92:95],v[148:151]
ds_read_u16 v128,v128
ds_read_b64_tr_b16 v[106:107],v6 offset:13984
v_lshlrev_b32_sdwa v131,1,v131 dst_sel:DWORD dst_unused:UNUSED_PAD src0_sel:DWORD src1_sel:WORD_1
v_fma_mixlo_f16 v120,v140,s99,0
v_fma_mixlo_f16 v122,v141,s99,0
v_mfma_f32_16x16x32_bf16 v[248:251],v[144:147],v[96:99],v[148:151]
ds_read_u16_d16_hi v130,v130
ds_read_b64_tr_b16 v[108:109],v6 offset:12456
v_lshlrev_b32_sdwa v132,1,v132 dst_sel:DWORD dst_unused:UNUSED_PAD src0_sel:DWORD src1_sel:WORD_1
v_lshlrev_b32_sdwa v134,1,v134 dst_sel:DWORD dst_unused:UNUSED_PAD src0_sel:DWORD src1_sel:WORD_1
v_mfma_f32_16x16x32_bf16 v[252:255],v[144:147],v[100:103],v[148:151]
ds_read_u16 v129,v129
ds_read_b64_tr_b16 v[110:111],v6 offset:13992
v_fma_mixhi_f16 v120,v142,s99,0
v_fma_mixhi_f16 v122,v143,s99,0
v_lshlrev_b32_sdwa v133,1,v133 dst_sel:DWORD dst_unused:UNUSED_PAD src0_sel:DWORD src1_sel:WORD_1
v_lshlrev_b32_sdwa v135,1,v135 dst_sel:DWORD dst_unused:UNUSED_PAD src0_sel:DWORD src1_sel:WORD_1
v_mfma_f32_16x16x32_bf16 v[144:147],v[64:67],v[40:43],v[112:115]
ds_read_u16_d16_hi v131,v131
ds_read_b64_tr_b16 v[88:89],v6 offset:12320
v_pk_fma_f16 v121,v120,v120,s54
v_pk_fma_f16 v123,v122,v122,s54
v_pk_mul_f16 v121,v120,v121
v_mfma_f32_16x16x32_bf16 v[152:155],v[64:67],v[44:47],v[112:115]
ds_read_u16 v132,v132
ds_read_b64_tr_b16 v[90:91],v6 offset:13856
v_pk_mul_f16 v123,v122,v123
v_lshlrev_b32_sdwa v136,1,v136 dst_sel:DWORD dst_unused:UNUSED_PAD src0_sel:DWORD src1_sel:WORD_1
v_mfma_f32_16x16x32_bf16 v[156:159],v[68:71],v[44:47],v[116:119]
ds_read_u16_d16_hi v134,v134
ds_read_b64_tr_b16 v[92:93],v6 offset:12328
v_lshlrev_b32_sdwa v138,1,v138 dst_sel:DWORD dst_unused:UNUSED_PAD src0_sel:DWORD src1_sel:WORD_1
v_exp_f16 v120,v121
v_mfma_f32_16x16x32_bf16 v[148:151],v[68:71],v[40:43],v[116:119]
ds_read_u16 v133,v133
ds_read_b64_tr_b16 v[94:95],v6 offset:13864
v_exp_f16 v122,v123
v_lshlrev_b32_sdwa v137,1,v137 dst_sel:DWORD dst_unused:UNUSED_PAD src0_sel:DWORD src1_sel:WORD_1
v_mfma_f32_16x16x32_bf16 v[144:147],v[72:75],v[48:51],v[144:147]
ds_read_u16_d16_hi v135,v135
ds_read_b64_tr_b16 v[96:97],v6 offset:12384
v_lshlrev_b32_sdwa v139,1,v139 dst_sel:DWORD dst_unused:UNUSED_PAD src0_sel:DWORD src1_sel:WORD_1
v_exp_f16_sdwa v120,v121 dst_sel:WORD_1 dst_unused:UNUSED_PRESERVE src0_sel:WORD_1
v_mfma_f32_16x16x32_bf16 v[152:155],v[72:75],v[52:55],v[152:155]
ds_read_u16 v136,v136
ds_read_b64_tr_b16 v[98:99],v6 offset:13920
v_exp_f16_sdwa v122,v123 dst_sel:WORD_1 dst_unused:UNUSED_PRESERVE src0_sel:WORD_1
v_mfma_f32_16x16x32_bf16 v[156:159],v[76:79],v[52:55],v[156:159]
ds_read_u16_d16_hi v138,v138
ds_read_b64_tr_b16 v[100:101],v6 offset:12392
v_pk_fma_f16 v120,v120,s55,s55
v_pk_fma_f16 v122,v122,s55,s55
v_rcp_f16 v121,v120
v_mfma_f32_16x16x32_bf16 v[148:151],v[76:79],v[48:51],v[148:151]
ds_read_u16 v137,v137
ds_read_b64_tr_b16 v[102:103],v6 offset:13928
v_rcp_f16 v123,v122
v_mfma_f32_16x16x32_bf16 v[144:147],v[80:83],v[56:59],v[144:147]
ds_read_u16_d16_hi v139,v139
ds_read_b64_tr_b16 v[64:65],v4 offset:12320
v_rcp_f16_sdwa v121,v120 dst_sel:WORD_1 dst_unused:UNUSED_PRESERVE src0_sel:WORD_1
v_mfma_f32_16x16x32_bf16 v[152:155],v[80:83],v[60:63],v[152:155]
ds_read_b64_tr_b16 v[66:67],v4 offset:14368
v_rcp_f16_sdwa v123,v122 dst_sel:WORD_1 dst_unused:UNUSED_PRESERVE src0_sel:WORD_1
v_fma_mix_f32 v140,v140,v121,0 op_sel_hi:[0,1,0]
v_mfma_f32_16x16x32_bf16 v[156:159],v[84:87],v[60:63],v[156:159]
ds_read_b128 v[112:115],v7 offset:512
v_fma_mix_f32 v142,v142,v121,0 op_sel:[0,1,0] op_sel_hi:[0,1,0]
s_waitcnt lgkmcnt(15)
v_lshl_add_u64 v[128:129],v[128:129],0,v[130:131]
s_waitcnt lgkmcnt(11)
v_lshl_add_u64 v[130:131],v[132:133],0,v[134:135]
v_mfma_f32_16x16x32_bf16 v[148:151],v[84:87],v[56:59],v[148:151]
ds_read_b64_tr_b16 v[68:69],v5 offset:12320
v_fma_mix_f32 v141,v141,v123,0 op_sel_hi:[0,1,0]
v_fma_mix_f32 v143,v143,v123,0 op_sel:[0,1,0] op_sel_hi:[0,1,0]
v_lshlrev_b32_sdwa v144,1,v144 dst_sel:DWORD dst_unused:UNUSED_PAD src0_sel:DWORD src1_sel:WORD_1
v_lshlrev_b32_sdwa v146,1,v146 dst_sel:DWORD dst_unused:UNUSED_PAD src0_sel:DWORD src1_sel:WORD_1
v_mfma_f32_16x16x32_bf16 v[160:163],v[88:91],v[128:131],v[160:163]
ds_read_u16 v144,v144
v_lshlrev_b32_sdwa v145,1,v145 dst_sel:DWORD dst_unused:UNUSED_PAD src0_sel:DWORD src1_sel:WORD_1
v_lshlrev_b32_sdwa v147,1,v147 dst_sel:DWORD dst_unused:UNUSED_PAD src0_sel:DWORD src1_sel:WORD_1
v_mfma_f32_16x16x32_bf16 v[168:171],v[92:95],v[128:131],v[168:171]
s_waitcnt lgkmcnt(10)
ds_read_b64_tr_b16 v[70:71],v5 offset:14368
ds_read_b128 v[116:119],v7 offset:544
v_fma_mixlo_f16 v120,v156,s99,0
v_fma_mixlo_f16 v122,v157,s99,0
v_mfma_f32_16x16x32_bf16 v[176:179],v[96:99],v[128:131],v[176:179]
s_waitcnt lgkmcnt(8)
ds_read_u16_d16_hi v146,v146
v_lshlrev_b32_sdwa v148,1,v148 dst_sel:DWORD dst_unused:UNUSED_PAD src0_sel:DWORD src1_sel:WORD_1
v_lshlrev_b32_sdwa v150,1,v150 dst_sel:DWORD dst_unused:UNUSED_PAD src0_sel:DWORD src1_sel:WORD_1
v_mfma_f32_16x16x32_bf16 v[184:187],v[100:103],v[128:131],v[184:187]
s_waitcnt lgkmcnt(8)
ds_read_b64_tr_b16 v[72:73],v4 offset:16416
ds_read_b64_tr_b16 v[74:75],v4 offset:18464
v_lshl_add_u64 v[136:137],v[136:137],0,v[138:139]
v_mfma_f32_16x16x32_bf16 v[192:195],v[104:107],v[128:131],v[192:195]
ds_read_u16 v145,v145
v_cvt_pk_bf16_f32 v138,v140,v142
v_cvt_pk_bf16_f32 v139,v141,v143
v_mfma_f32_16x16x32_bf16 v[200:203],v[108:111],v[128:131],v[200:203]
ds_read_b64_tr_b16 v[76:77],v5 offset:16416
ds_read_b64_tr_b16 v[78:79],v5 offset:18464
v_fma_mixhi_f16 v120,v158,s99,0
v_fma_mixhi_f16 v122,v159,s99,0
v_mfma_f32_16x16x32_bf16 v[164:167],v[88:91],v[136:139],v[164:167]
ds_read_u16_d16_hi v147,v147
v_lshlrev_b32_sdwa v149,1,v149 dst_sel:DWORD dst_unused:UNUSED_PAD src0_sel:DWORD src1_sel:WORD_1
v_lshlrev_b32_sdwa v151,1,v151 dst_sel:DWORD dst_unused:UNUSED_PAD src0_sel:DWORD src1_sel:WORD_1
v_mfma_f32_16x16x32_bf16 v[172:175],v[92:95],v[136:139],v[172:175]
ds_read_b64_tr_b16 v[80:81],v4 offset:20512
ds_read_b64_tr_b16 v[82:83],v4 offset:22560
v_pk_fma_f16 v121,v120,v120,s54
v_pk_fma_f16 v123,v122,v122,s54
v_mfma_f32_16x16x32_bf16 v[180:183],v[96:99],v[136:139],v[180:183]
ds_read_u16 v148,v148
v_pk_mul_f16 v121,v120,v121
v_pk_mul_f16 v123,v122,v123
v_mfma_f32_16x16x32_bf16 v[188:191],v[100:103],v[136:139],v[188:191]
ds_read_b64_tr_b16 v[84:85],v5 offset:20512
v_lshlrev_b32_sdwa v152,1,v152 dst_sel:DWORD dst_unused:UNUSED_PAD src0_sel:DWORD src1_sel:WORD_1
v_lshlrev_b32_sdwa v154,1,v154 dst_sel:DWORD dst_unused:UNUSED_PAD src0_sel:DWORD src1_sel:WORD_1
v_mfma_f32_16x16x32_bf16 v[196:199],v[104:107],v[136:139],v[196:199]
ds_read_u16_d16_hi v150,v150
v_exp_f16 v120,v121
v_mfma_f32_16x16x32_bf16 v[204:207],v[108:111],v[136:139],v[204:207]
s_waitcnt lgkmcnt(15)
ds_read_b64_tr_b16 v[86:87],v5 offset:22560
v_exp_f16 v122,v123
v_mfma_f32_16x16x32_bf16 v[128:131],v[64:67],v[16:19],v[112:115]
ds_read_u16 v149,v149
v_lshlrev_b32_sdwa v153,1,v153 dst_sel:DWORD dst_unused:UNUSED_PAD src0_sel:DWORD src1_sel:WORD_1
v_lshlrev_b32_sdwa v155,1,v155 dst_sel:DWORD dst_unused:UNUSED_PAD src0_sel:DWORD src1_sel:WORD_1
v_mfma_f32_16x16x32_bf16 v[136:139],v[64:67],v[20:23],v[112:115]
s_waitcnt lgkmcnt(14)
v_exp_f16_sdwa v120,v121 dst_sel:WORD_1 dst_unused:UNUSED_PRESERVE src0_sel:WORD_1
v_mfma_f32_16x16x32_bf16 v[140:143],v[68:71],v[20:23],v[116:119]
ds_read_u16_d16_hi v151,v151
v_exp_f16_sdwa v122,v123 dst_sel:WORD_1 dst_unused:UNUSED_PRESERVE src0_sel:WORD_1
v_mfma_f32_16x16x32_bf16 v[132:135],v[68:71],v[16:19],v[116:119]
s_waitcnt lgkmcnt(12)
v_pk_fma_f16 v120,v120,s55,s55
v_pk_fma_f16 v122,v122,s55,s55
v_mfma_f32_16x16x32_bf16 v[128:131],v[72:75],v[24:27],v[128:131]
ds_read_u16 v152,v152
v_rcp_f16 v121,v120
v_mfma_f32_16x16x32_bf16 v[136:139],v[72:75],v[28:31],v[136:139]
s_waitcnt lgkmcnt(10)
v_rcp_f16 v123,v122
v_mfma_f32_16x16x32_bf16 v[140:143],v[76:79],v[28:31],v[140:143]
ds_read_u16_d16_hi v154,v154
v_rcp_f16_sdwa v121,v120 dst_sel:WORD_1 dst_unused:UNUSED_PRESERVE src0_sel:WORD_1
v_mfma_f32_16x16x32_bf16 v[132:135],v[76:79],v[24:27],v[132:135]
s_waitcnt lgkmcnt(8)
v_rcp_f16_sdwa v123,v122 dst_sel:WORD_1 dst_unused:UNUSED_PRESERVE src0_sel:WORD_1
v_mfma_f32_16x16x32_bf16 v[128:131],v[80:83],v[32:35],v[128:131]
ds_read_u16 v153,v153
v_fma_mix_f32 v156,v156,v121,0 op_sel_hi:[0,1,0]
v_fma_mix_f32 v158,v158,v121,0 op_sel:[0,1,0] op_sel_hi:[0,1,0]
v_mfma_f32_16x16x32_bf16 v[136:139],v[80:83],v[36:39],v[136:139]
s_waitcnt lgkmcnt(3)
v_lshl_add_u64 v[144:145],v[144:145],0,v[146:147]
v_lshl_add_u64 v[146:147],v[148:149],0,v[150:151]
v_mfma_f32_16x16x32_bf16 v[140:143],v[84:87],v[36:39],v[140:143]
ds_read_u16_d16_hi v155,v155
v_fma_mix_f32 v157,v157,v123,0 op_sel_hi:[0,1,0]
v_fma_mix_f32 v159,v159,v123,0 op_sel:[0,1,0] op_sel_hi:[0,1,0]
v_mfma_f32_16x16x32_bf16 v[132:135],v[84:87],v[32:35],v[132:135]
v_lshlrev_b32_sdwa v128,1,v128 dst_sel:DWORD dst_unused:UNUSED_PAD src0_sel:DWORD src1_sel:WORD_1
v_lshlrev_b32_sdwa v130,1,v130 dst_sel:DWORD dst_unused:UNUSED_PAD src0_sel:DWORD src1_sel:WORD_1
v_mfma_f32_16x16x32_bf16 v[208:211],v[88:91],v[144:147],v[208:211]
ds_read_u16 v128,v128
v_lshlrev_b32_sdwa v129,1,v129 dst_sel:DWORD dst_unused:UNUSED_PAD src0_sel:DWORD src1_sel:WORD_1
v_lshlrev_b32_sdwa v131,1,v131 dst_sel:DWORD dst_unused:UNUSED_PAD src0_sel:DWORD src1_sel:WORD_1
v_mfma_f32_16x16x32_bf16 v[216:219],v[92:95],v[144:147],v[216:219]
v_fma_mixlo_f16 v120,v140,s99,0
v_fma_mixlo_f16 v122,v141,s99,0
v_mfma_f32_16x16x32_bf16 v[224:227],v[96:99],v[144:147],v[224:227]
ds_read_u16_d16_hi v130,v130
v_lshlrev_b32_sdwa v132,1,v132 dst_sel:DWORD dst_unused:UNUSED_PAD src0_sel:DWORD src1_sel:WORD_1
v_lshlrev_b32_sdwa v134,1,v134 dst_sel:DWORD dst_unused:UNUSED_PAD src0_sel:DWORD src1_sel:WORD_1
v_mfma_f32_16x16x32_bf16 v[232:235],v[100:103],v[144:147],v[232:235]
s_waitcnt lgkmcnt(2)
v_lshl_add_u64 v[152:153],v[152:153],0,v[154:155]
v_mfma_f32_16x16x32_bf16 v[240:243],v[104:107],v[144:147],v[240:243]
ds_read_u16 v129,v129
v_cvt_pk_bf16_f32 v154,v156,v158
v_cvt_pk_bf16_f32 v155,v157,v159
v_mfma_f32_16x16x32_bf16 v[248:251],v[108:111],v[144:147],v[248:251]
v_fma_mixhi_f16 v120,v142,s99,0
v_fma_mixhi_f16 v122,v143,s99,0
v_mfma_f32_16x16x32_bf16 v[212:215],v[88:91],v[152:155],v[212:215]
ds_read_u16_d16_hi v131,v131
v_lshlrev_b32_sdwa v133,1,v133 dst_sel:DWORD dst_unused:UNUSED_PAD src0_sel:DWORD src1_sel:WORD_1
v_lshlrev_b32_sdwa v135,1,v135 dst_sel:DWORD dst_unused:UNUSED_PAD src0_sel:DWORD src1_sel:WORD_1
v_mfma_f32_16x16x32_bf16 v[220:223],v[92:95],v[152:155],v[220:223]
s_setprio 2
ds_read_b64_tr_b16 v[88:89],v6 offset:18480
v_pk_fma_f16 v121,v120,v120,s54
v_pk_fma_f16 v123,v122,v122,s54
v_mfma_f32_16x16x32_bf16 v[228:231],v[96:99],v[152:155],v[228:231]
ds_read_u16 v132,v132
v_pk_mul_f16 v121,v120,v121
v_pk_mul_f16 v123,v122,v123
v_mfma_f32_16x16x32_bf16 v[236:239],v[100:103],v[152:155],v[236:239]
ds_read_b64_tr_b16 v[90:91],v6 offset:20016
v_lshlrev_b32_sdwa v136,1,v136 dst_sel:DWORD dst_unused:UNUSED_PAD src0_sel:DWORD src1_sel:WORD_1
v_lshlrev_b32_sdwa v138,1,v138 dst_sel:DWORD dst_unused:UNUSED_PAD src0_sel:DWORD src1_sel:WORD_1
v_mfma_f32_16x16x32_bf16 v[244:247],v[104:107],v[152:155],v[244:247]
ds_read_u16_d16_hi v134,v134
v_exp_f16 v120,v121
v_mfma_f32_16x16x32_bf16 v[252:255],v[108:111],v[152:155],v[252:255]
ds_read_b64_tr_b16 v[92:93],v6 offset:18488
v_exp_f16 v122,v123
v_mfma_f32_16x16x32_bf16 v[144:147],v[64:67],v[40:43],v[112:115]
ds_read_u16 v133,v133
v_lshlrev_b32_sdwa v137,1,v137 dst_sel:DWORD dst_unused:UNUSED_PAD src0_sel:DWORD src1_sel:WORD_1
v_lshlrev_b32_sdwa v139,1,v139 dst_sel:DWORD dst_unused:UNUSED_PAD src0_sel:DWORD src1_sel:WORD_1
v_mfma_f32_16x16x32_bf16 v[152:155],v[64:67],v[44:47],v[112:115]
ds_read_b64_tr_b16 v[94:95],v6 offset:20024
v_exp_f16_sdwa v120,v121 dst_sel:WORD_1 dst_unused:UNUSED_PRESERVE src0_sel:WORD_1
v_mfma_f32_16x16x32_bf16 v[156:159],v[68:71],v[44:47],v[116:119]
ds_read_u16_d16_hi v135,v135
v_exp_f16_sdwa v122,v123 dst_sel:WORD_1 dst_unused:UNUSED_PRESERVE src0_sel:WORD_1
v_mfma_f32_16x16x32_bf16 v[148:151],v[68:71],v[40:43],v[116:119]
ds_read_b64_tr_b16 v[96:97],v6 offset:18544
v_pk_fma_f16 v120,v120,s55,s55
v_pk_fma_f16 v122,v122,s55,s55
v_mfma_f32_16x16x32_bf16 v[144:147],v[72:75],v[48:51],v[144:147]
ds_read_u16 v136,v136
v_rcp_f16 v121,v120
v_mfma_f32_16x16x32_bf16 v[152:155],v[72:75],v[52:55],v[152:155]
ds_read_b64_tr_b16 v[98:99],v6 offset:20080
ds_read_b64_tr_b16 v[100:101],v6 offset:18552
v_rcp_f16 v123,v122
v_mfma_f32_16x16x32_bf16 v[156:159],v[76:79],v[52:55],v[156:159]
ds_read_u16_d16_hi v138,v138
v_rcp_f16_sdwa v121,v120 dst_sel:WORD_1 dst_unused:UNUSED_PRESERVE src0_sel:WORD_1
v_mfma_f32_16x16x32_bf16 v[148:151],v[76:79],v[48:51],v[148:151]
ds_read_b64_tr_b16 v[102:103],v6 offset:20088
ds_read_b64_tr_b16 v[104:105],v6 offset:18608
v_rcp_f16_sdwa v123,v122 dst_sel:WORD_1 dst_unused:UNUSED_PRESERVE src0_sel:WORD_1
v_mfma_f32_16x16x32_bf16 v[144:147],v[80:83],v[56:59],v[144:147]
ds_read_u16 v137,v137
v_fma_mix_f32 v140,v140,v121,0 op_sel_hi:[0,1,0]
v_fma_mix_f32 v142,v142,v121,0 op_sel:[0,1,0] op_sel_hi:[0,1,0]
v_mfma_f32_16x16x32_bf16 v[152:155],v[80:83],v[60:63],v[152:155]
s_waitcnt lgkmcnt(8)
ds_read_b64_tr_b16 v[106:107],v6 offset:20144
ds_read_b64_tr_b16 v[108:109],v6 offset:18616
v_lshl_add_u64 v[128:129],v[128:129],0,v[130:131]
v_lshl_add_u64 v[130:131],v[132:133],0,v[134:135]
v_mfma_f32_16x16x32_bf16 v[156:159],v[84:87],v[60:63],v[156:159]
ds_read_u16_d16_hi v139,v139
v_fma_mix_f32 v141,v141,v123,0 op_sel_hi:[0,1,0]
v_fma_mix_f32 v143,v143,v123,0 op_sel:[0,1,0] op_sel_hi:[0,1,0]
v_mfma_f32_16x16x32_bf16 v[148:151],v[84:87],v[56:59],v[148:151]
ds_read_b64_tr_b16 v[110:111],v6 offset:20152
ds_read_b64_tr_b16 v[64:65],v4 offset:12832
v_lshlrev_b32_sdwa v144,1,v144 dst_sel:DWORD dst_unused:UNUSED_PAD src0_sel:DWORD src1_sel:WORD_1
v_lshlrev_b32_sdwa v146,1,v146 dst_sel:DWORD dst_unused:UNUSED_PAD src0_sel:DWORD src1_sel:WORD_1
v_mfma_f32_16x16x32_bf16 v[160:163],v[88:91],v[128:131],v[160:163]
ds_read_u16 v144,v144
v_lshlrev_b32_sdwa v145,1,v145 dst_sel:DWORD dst_unused:UNUSED_PAD src0_sel:DWORD src1_sel:WORD_1
v_lshlrev_b32_sdwa v147,1,v147 dst_sel:DWORD dst_unused:UNUSED_PAD src0_sel:DWORD src1_sel:WORD_1
v_mfma_f32_16x16x32_bf16 v[168:171],v[92:95],v[128:131],v[168:171]
s_waitcnt lgkmcnt(11)
ds_read_b64_tr_b16 v[66:67],v4 offset:14880
ds_read_b128 v[112:115],v7 offset:640
v_fma_mixlo_f16 v120,v156,s99,0
v_fma_mixlo_f16 v122,v157,s99,0
v_mfma_f32_16x16x32_bf16 v[176:179],v[96:99],v[128:131],v[176:179]
s_waitcnt lgkmcnt(10)
ds_read_u16_d16_hi v146,v146
v_lshlrev_b32_sdwa v148,1,v148 dst_sel:DWORD dst_unused:UNUSED_PAD src0_sel:DWORD src1_sel:WORD_1
v_lshlrev_b32_sdwa v150,1,v150 dst_sel:DWORD dst_unused:UNUSED_PAD src0_sel:DWORD src1_sel:WORD_1
v_mfma_f32_16x16x32_bf16 v[184:187],v[100:103],v[128:131],v[184:187]
s_waitcnt lgkmcnt(6)
ds_read_b64_tr_b16 v[68:69],v5 offset:12832
ds_read_b64_tr_b16 v[70:71],v5 offset:14880
v_lshl_add_u64 v[136:137],v[136:137],0,v[138:139]
v_mfma_f32_16x16x32_bf16 v[192:195],v[104:107],v[128:131],v[192:195]
s_waitcnt lgkmcnt(7)
ds_read_u16 v145,v145
v_cvt_pk_bf16_f32 v138,v140,v142
v_cvt_pk_bf16_f32 v139,v141,v143
v_mfma_f32_16x16x32_bf16 v[200:203],v[108:111],v[128:131],v[200:203]
ds_read_b128 v[116:119],v7 offset:672
ds_read_b64_tr_b16 v[72:73],v4 offset:16928
v_fma_mixhi_f16 v120,v158,s99,0
v_fma_mixhi_f16 v122,v159,s99,0
v_mfma_f32_16x16x32_bf16 v[164:167],v[88:91],v[136:139],v[164:167]
ds_read_u16_d16_hi v147,v147
v_lshlrev_b32_sdwa v149,1,v149 dst_sel:DWORD dst_unused:UNUSED_PAD src0_sel:DWORD src1_sel:WORD_1
v_lshlrev_b32_sdwa v151,1,v151 dst_sel:DWORD dst_unused:UNUSED_PAD src0_sel:DWORD src1_sel:WORD_1
v_mfma_f32_16x16x32_bf16 v[172:175],v[92:95],v[136:139],v[172:175]
ds_read_b64_tr_b16 v[74:75],v4 offset:18976
ds_read_b64_tr_b16 v[76:77],v5 offset:16928
v_pk_fma_f16 v121,v120,v120,s54
v_pk_fma_f16 v123,v122,v122,s54
v_mfma_f32_16x16x32_bf16 v[180:183],v[96:99],v[136:139],v[180:183]
s_setprio 1
ds_read_u16 v148,v148
v_pk_mul_f16 v121,v120,v121
v_pk_mul_f16 v123,v122,v123
v_mfma_f32_16x16x32_bf16 v[188:191],v[100:103],v[136:139],v[188:191]
ds_read_b64_tr_b16 v[78:79],v5 offset:18976
v_lshlrev_b32_sdwa v152,1,v152 dst_sel:DWORD dst_unused:UNUSED_PAD src0_sel:DWORD src1_sel:WORD_1
v_lshlrev_b32_sdwa v154,1,v154 dst_sel:DWORD dst_unused:UNUSED_PAD src0_sel:DWORD src1_sel:WORD_1
v_mfma_f32_16x16x32_bf16 v[196:199],v[104:107],v[136:139],v[196:199]
ds_read_u16_d16_hi v150,v150
v_exp_f16 v120,v121
v_mfma_f32_16x16x32_bf16 v[204:207],v[108:111],v[136:139],v[204:207]
s_waitcnt lgkmcnt(12)
ds_read_b64_tr_b16 v[80:81],v4 offset:21024
v_exp_f16 v122,v123
v_mfma_f32_16x16x32_bf16 v[128:131],v[64:67],v[16:19],v[112:115]
ds_read_u16 v149,v149
v_lshlrev_b32_sdwa v153,1,v153 dst_sel:DWORD dst_unused:UNUSED_PAD src0_sel:DWORD src1_sel:WORD_1
v_lshlrev_b32_sdwa v155,1,v155 dst_sel:DWORD dst_unused:UNUSED_PAD src0_sel:DWORD src1_sel:WORD_1
v_mfma_f32_16x16x32_bf16 v[136:139],v[64:67],v[20:23],v[112:115]
s_waitcnt lgkmcnt(9)
ds_read_b64_tr_b16 v[82:83],v4 offset:23072
v_exp_f16_sdwa v120,v121 dst_sel:WORD_1 dst_unused:UNUSED_PRESERVE src0_sel:WORD_1
v_mfma_f32_16x16x32_bf16 v[140:143],v[68:71],v[20:23],v[116:119]
ds_read_u16_d16_hi v151,v151
v_exp_f16_sdwa v122,v123 dst_sel:WORD_1 dst_unused:UNUSED_PRESERVE src0_sel:WORD_1
v_mfma_f32_16x16x32_bf16 v[132:135],v[68:71],v[16:19],v[116:119]
s_waitcnt lgkmcnt(8)
ds_read_b64_tr_b16 v[84:85],v5 offset:21024
v_pk_fma_f16 v120,v120,s55,s55
v_pk_fma_f16 v122,v122,s55,s55
v_mfma_f32_16x16x32_bf16 v[128:131],v[72:75],v[24:27],v[128:131]
ds_read_u16 v152,v152
v_rcp_f16 v121,v120
v_mfma_f32_16x16x32_bf16 v[136:139],v[72:75],v[28:31],v[136:139]
s_waitcnt lgkmcnt(7)
ds_read_b64_tr_b16 v[86:87],v5 offset:23072
v_rcp_f16 v123,v122
v_mfma_f32_16x16x32_bf16 v[140:143],v[76:79],v[28:31],v[140:143]
ds_read_u16_d16_hi v154,v154
v_rcp_f16_sdwa v121,v120 dst_sel:WORD_1 dst_unused:UNUSED_PRESERVE src0_sel:WORD_1
v_mfma_f32_16x16x32_bf16 v[132:135],v[76:79],v[24:27],v[132:135]
s_waitcnt lgkmcnt(5)
v_rcp_f16_sdwa v123,v122 dst_sel:WORD_1 dst_unused:UNUSED_PRESERVE src0_sel:WORD_1
v_mfma_f32_16x16x32_bf16 v[128:131],v[80:83],v[32:35],v[128:131]
ds_read_u16 v153,v153
v_fma_mix_f32 v156,v156,v121,0 op_sel_hi:[0,1,0]
v_fma_mix_f32 v158,v158,v121,0 op_sel:[0,1,0] op_sel_hi:[0,1,0]
v_mfma_f32_16x16x32_bf16 v[136:139],v[80:83],v[36:39],v[136:139]
s_waitcnt lgkmcnt(2)
s_setprio 0
v_lshl_add_u64 v[144:145],v[144:145],0,v[146:147]
v_lshl_add_u64 v[146:147],v[148:149],0,v[150:151]
v_mfma_f32_16x16x32_bf16 v[140:143],v[84:87],v[36:39],v[140:143]
ds_read_u16_d16_hi v155,v155
v_fma_mix_f32 v157,v157,v123,0 op_sel_hi:[0,1,0]
v_fma_mix_f32 v159,v159,v123,0 op_sel:[0,1,0] op_sel_hi:[0,1,0]
v_mfma_f32_16x16x32_bf16 v[132:135],v[84:87],v[32:35],v[132:135]
v_lshlrev_b32_sdwa v128,1,v128 dst_sel:DWORD dst_unused:UNUSED_PAD src0_sel:DWORD src1_sel:WORD_1
v_lshlrev_b32_sdwa v130,1,v130 dst_sel:DWORD dst_unused:UNUSED_PAD src0_sel:DWORD src1_sel:WORD_1
v_mfma_f32_16x16x32_bf16 v[208:211],v[88:91],v[144:147],v[208:211]
s_barrier
s_setprio 3
ds_read_u16 v128,v128
v_lshlrev_b32_sdwa v129,1,v129 dst_sel:DWORD dst_unused:UNUSED_PAD src0_sel:DWORD src1_sel:WORD_1
v_lshlrev_b32_sdwa v131,1,v131 dst_sel:DWORD dst_unused:UNUSED_PAD src0_sel:DWORD src1_sel:WORD_1
v_mfma_f32_16x16x32_bf16 v[216:219],v[92:95],v[144:147],v[216:219]
v_fma_mixlo_f16 v120,v140,s99,0
v_fma_mixlo_f16 v122,v141,s99,0
v_mfma_f32_16x16x32_bf16 v[224:227],v[96:99],v[144:147],v[224:227]
ds_read_u16_d16_hi v130,v130
v_lshlrev_b32_sdwa v132,1,v132 dst_sel:DWORD dst_unused:UNUSED_PAD src0_sel:DWORD src1_sel:WORD_1
v_lshlrev_b32_sdwa v134,1,v134 dst_sel:DWORD dst_unused:UNUSED_PAD src0_sel:DWORD src1_sel:WORD_1
v_mfma_f32_16x16x32_bf16 v[232:235],v[100:103],v[144:147],v[232:235]
s_waitcnt lgkmcnt(2)
v_lshl_add_u64 v[152:153],v[152:153],0,v[154:155]
v_mfma_f32_16x16x32_bf16 v[240:243],v[104:107],v[144:147],v[240:243]
ds_read_u16 v129,v129
v_cvt_pk_bf16_f32 v154,v156,v158
v_cvt_pk_bf16_f32 v155,v157,v159
v_mfma_f32_16x16x32_bf16 v[248:251],v[108:111],v[144:147],v[248:251]
v_fma_mixhi_f16 v120,v142,s99,0
v_fma_mixhi_f16 v122,v143,s99,0
v_mfma_f32_16x16x32_bf16 v[212:215],v[88:91],v[152:155],v[212:215]
ds_read_u16_d16_hi v131,v131
v_lshlrev_b32_sdwa v133,1,v133 dst_sel:DWORD dst_unused:UNUSED_PAD src0_sel:DWORD src1_sel:WORD_1
v_lshlrev_b32_sdwa v135,1,v135 dst_sel:DWORD dst_unused:UNUSED_PAD src0_sel:DWORD src1_sel:WORD_1
v_mfma_f32_16x16x32_bf16 v[220:223],v[92:95],v[152:155],v[220:223]
ds_read_b64_tr_b16 v[88:89],v6 offset:0
v_pk_fma_f16 v121,v120,v120,s54
v_pk_fma_f16 v123,v122,v122,s54
v_mfma_f32_16x16x32_bf16 v[228:231],v[96:99],v[152:155],v[228:231]
ds_read_u16 v132,v132
v_pk_mul_f16 v121,v120,v121
v_pk_mul_f16 v123,v122,v123
v_mfma_f32_16x16x32_bf16 v[236:239],v[100:103],v[152:155],v[236:239]
ds_read_b64_tr_b16 v[90:91],v6 offset:1536
v_lshlrev_b32_sdwa v136,1,v136 dst_sel:DWORD dst_unused:UNUSED_PAD src0_sel:DWORD src1_sel:WORD_1
v_lshlrev_b32_sdwa v138,1,v138 dst_sel:DWORD dst_unused:UNUSED_PAD src0_sel:DWORD src1_sel:WORD_1
v_mfma_f32_16x16x32_bf16 v[244:247],v[104:107],v[152:155],v[244:247]
ds_read_u16_d16_hi v134,v134
v_exp_f16 v120,v121
v_mfma_f32_16x16x32_bf16 v[252:255],v[108:111],v[152:155],v[252:255]
ds_read_b64_tr_b16 v[92:93],v6 offset:8
v_exp_f16 v122,v123
v_mfma_f32_16x16x32_bf16 v[144:147],v[64:67],v[40:43],v[112:115]
ds_read_u16 v133,v133
v_lshlrev_b32_sdwa v137,1,v137 dst_sel:DWORD dst_unused:UNUSED_PAD src0_sel:DWORD src1_sel:WORD_1
v_lshlrev_b32_sdwa v139,1,v139 dst_sel:DWORD dst_unused:UNUSED_PAD src0_sel:DWORD src1_sel:WORD_1
v_mfma_f32_16x16x32_bf16 v[152:155],v[64:67],v[44:47],v[112:115]
ds_read_b64_tr_b16 v[94:95],v6 offset:1544
v_exp_f16_sdwa v120,v121 dst_sel:WORD_1 dst_unused:UNUSED_PRESERVE src0_sel:WORD_1
v_mfma_f32_16x16x32_bf16 v[156:159],v[68:71],v[44:47],v[116:119]
ds_read_u16_d16_hi v135,v135
v_exp_f16_sdwa v122,v123 dst_sel:WORD_1 dst_unused:UNUSED_PRESERVE src0_sel:WORD_1
v_mfma_f32_16x16x32_bf16 v[148:151],v[68:71],v[40:43],v[116:119]
ds_read_b64_tr_b16 v[96:97],v6 offset:64
v_pk_fma_f16 v120,v120,s55,s55
v_pk_fma_f16 v122,v122,s55,s55
v_mfma_f32_16x16x32_bf16 v[144:147],v[72:75],v[48:51],v[144:147]
ds_read_u16 v136,v136
v_rcp_f16 v121,v120
v_mfma_f32_16x16x32_bf16 v[152:155],v[72:75],v[52:55],v[152:155]
ds_read_b64_tr_b16 v[98:99],v6 offset:1600
ds_read_b64_tr_b16 v[100:101],v6 offset:72
v_rcp_f16 v123,v122
v_mfma_f32_16x16x32_bf16 v[156:159],v[76:79],v[52:55],v[156:159]
ds_read_u16_d16_hi v138,v138
v_rcp_f16_sdwa v121,v120 dst_sel:WORD_1 dst_unused:UNUSED_PRESERVE src0_sel:WORD_1
v_mfma_f32_16x16x32_bf16 v[148:151],v[76:79],v[48:51],v[148:151]
ds_read_b64_tr_b16 v[102:103],v6 offset:1608
ds_read_b64_tr_b16 v[104:105],v6 offset:128
v_rcp_f16_sdwa v123,v122 dst_sel:WORD_1 dst_unused:UNUSED_PRESERVE src0_sel:WORD_1
v_mfma_f32_16x16x32_bf16 v[144:147],v[80:83],v[56:59],v[144:147]
ds_read_u16 v137,v137
v_fma_mix_f32 v140,v140,v121,0 op_sel_hi:[0,1,0]
v_fma_mix_f32 v142,v142,v121,0 op_sel:[0,1,0] op_sel_hi:[0,1,0]
v_mfma_f32_16x16x32_bf16 v[152:155],v[80:83],v[60:63],v[152:155]
s_waitcnt lgkmcnt(8)
ds_read_b64_tr_b16 v[106:107],v6 offset:1664
ds_read_b64_tr_b16 v[108:109],v6 offset:136
v_lshl_add_u64 v[128:129],v[128:129],0,v[130:131]
v_lshl_add_u64 v[130:131],v[132:133],0,v[134:135]
v_mfma_f32_16x16x32_bf16 v[156:159],v[84:87],v[60:63],v[156:159]
ds_read_u16_d16_hi v139,v139
v_fma_mix_f32 v141,v141,v123,0 op_sel_hi:[0,1,0]
v_fma_mix_f32 v143,v143,v123,0 op_sel:[0,1,0] op_sel_hi:[0,1,0]
v_mfma_f32_16x16x32_bf16 v[148:151],v[84:87],v[56:59],v[148:151]
ds_read_b64_tr_b16 v[110:111],v6 offset:1672
ds_read_b64_tr_b16 v[64:65],v4 offset:0
v_lshlrev_b32_sdwa v144,1,v144 dst_sel:DWORD dst_unused:UNUSED_PAD src0_sel:DWORD src1_sel:WORD_1
v_lshlrev_b32_sdwa v146,1,v146 dst_sel:DWORD dst_unused:UNUSED_PAD src0_sel:DWORD src1_sel:WORD_1
v_mfma_f32_16x16x32_bf16 v[160:163],v[88:91],v[128:131],v[160:163]
ds_read_u16 v144,v144
v_lshlrev_b32_sdwa v145,1,v145 dst_sel:DWORD dst_unused:UNUSED_PAD src0_sel:DWORD src1_sel:WORD_1
v_lshlrev_b32_sdwa v147,1,v147 dst_sel:DWORD dst_unused:UNUSED_PAD src0_sel:DWORD src1_sel:WORD_1
v_mfma_f32_16x16x32_bf16 v[168:171],v[92:95],v[128:131],v[168:171]
s_waitcnt lgkmcnt(11)
ds_read_b64_tr_b16 v[66:67],v4 offset:2048
ds_read_b128 v[112:115],v7 offset:768
v_fma_mixlo_f16 v120,v156,s99,0
v_fma_mixlo_f16 v122,v157,s99,0
v_mfma_f32_16x16x32_bf16 v[176:179],v[96:99],v[128:131],v[176:179]
s_waitcnt lgkmcnt(10)
ds_read_u16_d16_hi v146,v146
v_lshlrev_b32_sdwa v148,1,v148 dst_sel:DWORD dst_unused:UNUSED_PAD src0_sel:DWORD src1_sel:WORD_1
v_lshlrev_b32_sdwa v150,1,v150 dst_sel:DWORD dst_unused:UNUSED_PAD src0_sel:DWORD src1_sel:WORD_1
v_mfma_f32_16x16x32_bf16 v[184:187],v[100:103],v[128:131],v[184:187]
s_waitcnt lgkmcnt(6)
ds_read_b64_tr_b16 v[68:69],v5 offset:0
ds_read_b64_tr_b16 v[70:71],v5 offset:2048
s_nop 0
s_mov_b32 s86,0xbe801d5a
v_lshl_add_u64 v[136:137],v[136:137],0,v[138:139]
v_mfma_f32_16x16x32_bf16 v[192:195],v[104:107],v[128:131],v[192:195]
s_waitcnt lgkmcnt(7)
ds_read_u16 v145,v145
v_cvt_pk_bf16_f32 v138,v140,v142
v_cvt_pk_bf16_f32 v139,v141,v143
v_mfma_f32_16x16x32_bf16 v[200:203],v[108:111],v[128:131],v[200:203]
ds_read_b128 v[116:119],v7 offset:800
ds_read_b64_tr_b16 v[72:73],v4 offset:4096
v_fma_mixhi_f16 v120,v158,s99,0
v_fma_mixhi_f16 v122,v159,s99,0
v_mfma_f32_16x16x32_bf16 v[164:167],v[88:91],v[136:139],v[164:167]
ds_read_u16_d16_hi v147,v147
v_lshlrev_b32_sdwa v149,1,v149 dst_sel:DWORD dst_unused:UNUSED_PAD src0_sel:DWORD src1_sel:WORD_1
v_lshlrev_b32_sdwa v151,1,v151 dst_sel:DWORD dst_unused:UNUSED_PAD src0_sel:DWORD src1_sel:WORD_1
v_mfma_f32_16x16x32_bf16 v[172:175],v[92:95],v[136:139],v[172:175]
ds_read_b64_tr_b16 v[74:75],v4 offset:6144
ds_read_b64_tr_b16 v[76:77],v5 offset:4096
v_pk_fma_f16 v121,v120,v120,s54
v_pk_fma_f16 v123,v122,v122,s54
v_mfma_f32_16x16x32_bf16 v[180:183],v[96:99],v[136:139],v[180:183]
ds_read_u16 v148,v148
v_pk_mul_f16 v121,v120,v121
v_pk_mul_f16 v123,v122,v123
v_mfma_f32_16x16x32_bf16 v[188:191],v[100:103],v[136:139],v[188:191]
ds_read_b64_tr_b16 v[78:79],v5 offset:6144
v_lshlrev_b32_sdwa v152,1,v152 dst_sel:DWORD dst_unused:UNUSED_PAD src0_sel:DWORD src1_sel:WORD_1
v_lshlrev_b32_sdwa v154,1,v154 dst_sel:DWORD dst_unused:UNUSED_PAD src0_sel:DWORD src1_sel:WORD_1
v_mfma_f32_16x16x32_bf16 v[196:199],v[104:107],v[136:139],v[196:199]
ds_read_u16_d16_hi v150,v150
v_exp_f16 v120,v121
v_mfma_f32_16x16x32_bf16 v[204:207],v[108:111],v[136:139],v[204:207]
s_waitcnt lgkmcnt(12)
ds_read_b64_tr_b16 v[80:81],v4 offset:8192
v_exp_f16 v122,v123
v_mfma_f32_16x16x32_bf16 v[128:131],v[64:67],v[16:19],v[112:115]
ds_read_u16 v149,v149
v_lshlrev_b32_sdwa v153,1,v153 dst_sel:DWORD dst_unused:UNUSED_PAD src0_sel:DWORD src1_sel:WORD_1
v_lshlrev_b32_sdwa v155,1,v155 dst_sel:DWORD dst_unused:UNUSED_PAD src0_sel:DWORD src1_sel:WORD_1
v_mfma_f32_16x16x32_bf16 v[136:139],v[64:67],v[20:23],v[112:115]
s_waitcnt lgkmcnt(9)
ds_read_b64_tr_b16 v[82:83],v4 offset:10240
v_exp_f16_sdwa v120,v121 dst_sel:WORD_1 dst_unused:UNUSED_PRESERVE src0_sel:WORD_1
v_mfma_f32_16x16x32_bf16 v[140:143],v[68:71],v[20:23],v[116:119]
ds_read_u16_d16_hi v151,v151
v_exp_f16_sdwa v122,v123 dst_sel:WORD_1 dst_unused:UNUSED_PRESERVE src0_sel:WORD_1
v_mfma_f32_16x16x32_bf16 v[132:135],v[68:71],v[16:19],v[116:119]
s_waitcnt lgkmcnt(8)
ds_read_b64_tr_b16 v[84:85],v5 offset:8192
v_pk_fma_f16 v120,v120,s55,s55
v_pk_fma_f16 v122,v122,s55,s55
v_mfma_f32_16x16x32_bf16 v[128:131],v[72:75],v[24:27],v[128:131]
ds_read_u16 v152,v152
v_rcp_f16 v121,v120
v_mfma_f32_16x16x32_bf16 v[136:139],v[72:75],v[28:31],v[136:139]
s_waitcnt lgkmcnt(7)
ds_read_b64_tr_b16 v[86:87],v5 offset:10240
v_rcp_f16 v123,v122
v_mfma_f32_16x16x32_bf16 v[140:143],v[76:79],v[28:31],v[140:143]
ds_read_u16_d16_hi v154,v154
v_rcp_f16_sdwa v121,v120 dst_sel:WORD_1 dst_unused:UNUSED_PRESERVE src0_sel:WORD_1
v_mfma_f32_16x16x32_bf16 v[132:135],v[76:79],v[24:27],v[132:135]
s_waitcnt lgkmcnt(5)
v_rcp_f16_sdwa v123,v122 dst_sel:WORD_1 dst_unused:UNUSED_PRESERVE src0_sel:WORD_1
v_mfma_f32_16x16x32_bf16 v[128:131],v[80:83],v[32:35],v[128:131]
ds_read_u16 v153,v153
v_fma_mix_f32 v156,v156,v121,0 op_sel_hi:[0,1,0]
v_fma_mix_f32 v158,v158,v121,0 op_sel:[0,1,0] op_sel_hi:[0,1,0]
v_mfma_f32_16x16x32_bf16 v[136:139],v[80:83],v[36:39],v[136:139]
s_waitcnt lgkmcnt(2)
v_lshl_add_u64 v[144:145],v[144:145],0,v[146:147]
v_lshl_add_u64 v[146:147],v[148:149],0,v[150:151]
v_mfma_f32_16x16x32_bf16 v[140:143],v[84:87],v[36:39],v[140:143]
ds_read_u16_d16_hi v155,v155
v_fma_mix_f32 v157,v157,v123,0 op_sel_hi:[0,1,0]
v_fma_mix_f32 v159,v159,v123,0 op_sel:[0,1,0] op_sel_hi:[0,1,0]
v_mfma_f32_16x16x32_bf16 v[132:135],v[84:87],v[32:35],v[132:135]
v_lshlrev_b32_sdwa v128,1,v128 dst_sel:DWORD dst_unused:UNUSED_PAD src0_sel:DWORD src1_sel:WORD_1
v_lshlrev_b32_sdwa v130,1,v130 dst_sel:DWORD dst_unused:UNUSED_PAD src0_sel:DWORD src1_sel:WORD_1
v_mfma_f32_16x16x32_bf16 v[208:211],v[88:91],v[144:147],v[208:211]
ds_read_u16 v128,v128
v_lshlrev_b32_sdwa v129,1,v129 dst_sel:DWORD dst_unused:UNUSED_PAD src0_sel:DWORD src1_sel:WORD_1
v_lshlrev_b32_sdwa v131,1,v131 dst_sel:DWORD dst_unused:UNUSED_PAD src0_sel:DWORD src1_sel:WORD_1
v_mfma_f32_16x16x32_bf16 v[216:219],v[92:95],v[144:147],v[216:219]
v_fma_mixlo_f16 v120,v140,s99,0
v_fma_mixlo_f16 v122,v141,s99,0
v_mfma_f32_16x16x32_bf16 v[224:227],v[96:99],v[144:147],v[224:227]
ds_read_u16_d16_hi v130,v130
v_lshlrev_b32_sdwa v132,1,v132 dst_sel:DWORD dst_unused:UNUSED_PAD src0_sel:DWORD src1_sel:WORD_1
v_lshlrev_b32_sdwa v134,1,v134 dst_sel:DWORD dst_unused:UNUSED_PAD src0_sel:DWORD src1_sel:WORD_1
v_mfma_f32_16x16x32_bf16 v[232:235],v[100:103],v[144:147],v[232:235]
s_waitcnt lgkmcnt(2)
v_lshl_add_u64 v[152:153],v[152:153],0,v[154:155]
v_mfma_f32_16x16x32_bf16 v[240:243],v[104:107],v[144:147],v[240:243]
ds_read_u16 v129,v129
v_cvt_pk_bf16_f32 v154,v156,v158
v_cvt_pk_bf16_f32 v155,v157,v159
v_mfma_f32_16x16x32_bf16 v[248:251],v[108:111],v[144:147],v[248:251]
s_setprio 2
v_fma_mixhi_f16 v120,v142,s99,0
v_fma_mixhi_f16 v122,v143,s99,0
v_mfma_f32_16x16x32_bf16 v[212:215],v[88:91],v[152:155],v[212:215]
ds_read_u16_d16_hi v131,v131
v_lshlrev_b32_sdwa v133,1,v133 dst_sel:DWORD dst_unused:UNUSED_PAD src0_sel:DWORD src1_sel:WORD_1
v_lshlrev_b32_sdwa v135,1,v135 dst_sel:DWORD dst_unused:UNUSED_PAD src0_sel:DWORD src1_sel:WORD_1
v_mfma_f32_16x16x32_bf16 v[220:223],v[92:95],v[152:155],v[220:223]
ds_read_b64_tr_b16 v[88:89],v6 offset:6160
v_pk_fma_f16 v121,v120,v120,s54
v_pk_fma_f16 v123,v122,v122,s54
v_mfma_f32_16x16x32_bf16 v[228:231],v[96:99],v[152:155],v[228:231]
ds_read_u16 v132,v132
v_pk_mul_f16 v121,v120,v121
v_pk_mul_f16 v123,v122,v123
v_mfma_f32_16x16x32_bf16 v[236:239],v[100:103],v[152:155],v[236:239]
ds_read_b64_tr_b16 v[90:91],v6 offset:7696
v_lshlrev_b32_sdwa v136,1,v136 dst_sel:DWORD dst_unused:UNUSED_PAD src0_sel:DWORD src1_sel:WORD_1
v_lshlrev_b32_sdwa v138,1,v138 dst_sel:DWORD dst_unused:UNUSED_PAD src0_sel:DWORD src1_sel:WORD_1
v_mfma_f32_16x16x32_bf16 v[244:247],v[104:107],v[152:155],v[244:247]
ds_read_u16_d16_hi v134,v134
v_exp_f16 v120,v121
v_mfma_f32_16x16x32_bf16 v[252:255],v[108:111],v[152:155],v[252:255]
ds_read_b64_tr_b16 v[92:93],v6 offset:6168
v_exp_f16 v122,v123
v_mfma_f32_16x16x32_bf16 v[144:147],v[64:67],v[40:43],v[112:115]
ds_read_u16 v133,v133
v_lshlrev_b32_sdwa v137,1,v137 dst_sel:DWORD dst_unused:UNUSED_PAD src0_sel:DWORD src1_sel:WORD_1
v_lshlrev_b32_sdwa v139,1,v139 dst_sel:DWORD dst_unused:UNUSED_PAD src0_sel:DWORD src1_sel:WORD_1
v_mfma_f32_16x16x32_bf16 v[152:155],v[64:67],v[44:47],v[112:115]
ds_read_b64_tr_b16 v[94:95],v6 offset:7704
v_exp_f16_sdwa v120,v121 dst_sel:WORD_1 dst_unused:UNUSED_PRESERVE src0_sel:WORD_1
v_mfma_f32_16x16x32_bf16 v[156:159],v[68:71],v[44:47],v[116:119]
ds_read_u16_d16_hi v135,v135
v_exp_f16_sdwa v122,v123 dst_sel:WORD_1 dst_unused:UNUSED_PRESERVE src0_sel:WORD_1
v_mfma_f32_16x16x32_bf16 v[148:151],v[68:71],v[40:43],v[116:119]
ds_read_b64_tr_b16 v[96:97],v6 offset:6224
v_pk_fma_f16 v120,v120,s55,s55
v_pk_fma_f16 v122,v122,s55,s55
v_mfma_f32_16x16x32_bf16 v[144:147],v[72:75],v[48:51],v[144:147]
ds_read_u16 v136,v136
v_rcp_f16 v121,v120
v_mfma_f32_16x16x32_bf16 v[152:155],v[72:75],v[52:55],v[152:155]
ds_read_b64_tr_b16 v[98:99],v6 offset:7760
ds_read_b64_tr_b16 v[100:101],v6 offset:6232
v_rcp_f16 v123,v122
v_mfma_f32_16x16x32_bf16 v[156:159],v[76:79],v[52:55],v[156:159]
ds_read_u16_d16_hi v138,v138
v_rcp_f16_sdwa v121,v120 dst_sel:WORD_1 dst_unused:UNUSED_PRESERVE src0_sel:WORD_1
v_mfma_f32_16x16x32_bf16 v[148:151],v[76:79],v[48:51],v[148:151]
ds_read_b64_tr_b16 v[102:103],v6 offset:7768
ds_read_b64_tr_b16 v[104:105],v6 offset:6288
v_rcp_f16_sdwa v123,v122 dst_sel:WORD_1 dst_unused:UNUSED_PRESERVE src0_sel:WORD_1
v_mfma_f32_16x16x32_bf16 v[144:147],v[80:83],v[56:59],v[144:147]
ds_read_u16 v137,v137
v_fma_mix_f32 v140,v140,v121,0 op_sel_hi:[0,1,0]
v_fma_mix_f32 v142,v142,v121,0 op_sel:[0,1,0] op_sel_hi:[0,1,0]
v_mfma_f32_16x16x32_bf16 v[152:155],v[80:83],v[60:63],v[152:155]
s_waitcnt lgkmcnt(8)
ds_read_b64_tr_b16 v[106:107],v6 offset:7824
ds_read_b64_tr_b16 v[108:109],v6 offset:6296
v_lshl_add_u64 v[128:129],v[128:129],0,v[130:131]
v_lshl_add_u64 v[130:131],v[132:133],0,v[134:135]
v_mfma_f32_16x16x32_bf16 v[156:159],v[84:87],v[60:63],v[156:159]
ds_read_u16_d16_hi v139,v139
v_fma_mix_f32 v141,v141,v123,0 op_sel_hi:[0,1,0]
v_fma_mix_f32 v143,v143,v123,0 op_sel:[0,1,0] op_sel_hi:[0,1,0]
v_mfma_f32_16x16x32_bf16 v[148:151],v[84:87],v[56:59],v[148:151]
ds_read_b64_tr_b16 v[110:111],v6 offset:7832
ds_read_b64_tr_b16 v[64:65],v4 offset:512
v_lshlrev_b32_sdwa v144,1,v144 dst_sel:DWORD dst_unused:UNUSED_PAD src0_sel:DWORD src1_sel:WORD_1
v_lshlrev_b32_sdwa v146,1,v146 dst_sel:DWORD dst_unused:UNUSED_PAD src0_sel:DWORD src1_sel:WORD_1
v_mfma_f32_16x16x32_bf16 v[160:163],v[88:91],v[128:131],v[160:163]
ds_read_u16 v144,v144
v_lshlrev_b32_sdwa v145,1,v145 dst_sel:DWORD dst_unused:UNUSED_PAD src0_sel:DWORD src1_sel:WORD_1
v_lshlrev_b32_sdwa v147,1,v147 dst_sel:DWORD dst_unused:UNUSED_PAD src0_sel:DWORD src1_sel:WORD_1
v_mfma_f32_16x16x32_bf16 v[168:171],v[92:95],v[128:131],v[168:171]
s_waitcnt lgkmcnt(11)
ds_read_b64_tr_b16 v[66:67],v4 offset:2560
ds_read_b128 v[112:115],v7 offset:896
v_fma_mixlo_f16 v120,v156,s99,0
v_fma_mixlo_f16 v122,v157,s99,0
v_mfma_f32_16x16x32_bf16 v[176:179],v[96:99],v[128:131],v[176:179]
s_waitcnt lgkmcnt(10)
ds_read_u16_d16_hi v146,v146
v_lshlrev_b32_sdwa v148,1,v148 dst_sel:DWORD dst_unused:UNUSED_PAD src0_sel:DWORD src1_sel:WORD_1
v_lshlrev_b32_sdwa v150,1,v150 dst_sel:DWORD dst_unused:UNUSED_PAD src0_sel:DWORD src1_sel:WORD_1
v_mfma_f32_16x16x32_bf16 v[184:187],v[100:103],v[128:131],v[184:187]
s_waitcnt lgkmcnt(6)
ds_read_b64_tr_b16 v[68:69],v5 offset:512
ds_read_b64_tr_b16 v[70:71],v5 offset:2560
v_lshl_add_u64 v[136:137],v[136:137],0,v[138:139]
v_mfma_f32_16x16x32_bf16 v[192:195],v[104:107],v[128:131],v[192:195]
s_waitcnt lgkmcnt(7)
ds_read_u16 v145,v145
v_cvt_pk_bf16_f32 v138,v140,v142
v_cvt_pk_bf16_f32 v139,v141,v143
v_mfma_f32_16x16x32_bf16 v[200:203],v[108:111],v[128:131],v[200:203]
ds_read_b128 v[116:119],v7 offset:928
ds_read_b64_tr_b16 v[72:73],v4 offset:4608
v_fma_mixhi_f16 v120,v158,s99,0
v_fma_mixhi_f16 v122,v159,s99,0
v_mfma_f32_16x16x32_bf16 v[164:167],v[88:91],v[136:139],v[164:167]
ds_read_u16_d16_hi v147,v147
v_lshlrev_b32_sdwa v149,1,v149 dst_sel:DWORD dst_unused:UNUSED_PAD src0_sel:DWORD src1_sel:WORD_1
v_lshlrev_b32_sdwa v151,1,v151 dst_sel:DWORD dst_unused:UNUSED_PAD src0_sel:DWORD src1_sel:WORD_1
v_mfma_f32_16x16x32_bf16 v[172:175],v[92:95],v[136:139],v[172:175]
s_setprio 1
ds_read_b64_tr_b16 v[74:75],v4 offset:6656
ds_read_b64_tr_b16 v[76:77],v5 offset:4608
v_pk_fma_f16 v121,v120,v120,s54
v_pk_fma_f16 v123,v122,v122,s54
v_mfma_f32_16x16x32_bf16 v[180:183],v[96:99],v[136:139],v[180:183]
ds_read_u16 v148,v148
v_pk_mul_f16 v121,v120,v121
v_pk_mul_f16 v123,v122,v123
v_mfma_f32_16x16x32_bf16 v[188:191],v[100:103],v[136:139],v[188:191]
ds_read_b64_tr_b16 v[78:79],v5 offset:6656
v_lshlrev_b32_sdwa v152,1,v152 dst_sel:DWORD dst_unused:UNUSED_PAD src0_sel:DWORD src1_sel:WORD_1
v_lshlrev_b32_sdwa v154,1,v154 dst_sel:DWORD dst_unused:UNUSED_PAD src0_sel:DWORD src1_sel:WORD_1
v_mfma_f32_16x16x32_bf16 v[196:199],v[104:107],v[136:139],v[196:199]
ds_read_u16_d16_hi v150,v150
v_exp_f16 v120,v121
v_mfma_f32_16x16x32_bf16 v[204:207],v[108:111],v[136:139],v[204:207]
s_waitcnt lgkmcnt(12)
ds_read_b64_tr_b16 v[80:81],v4 offset:8704
v_exp_f16 v122,v123
v_mfma_f32_16x16x32_bf16 v[128:131],v[64:67],v[16:19],v[112:115]
ds_read_u16 v149,v149
v_lshlrev_b32_sdwa v153,1,v153 dst_sel:DWORD dst_unused:UNUSED_PAD src0_sel:DWORD src1_sel:WORD_1
v_lshlrev_b32_sdwa v155,1,v155 dst_sel:DWORD dst_unused:UNUSED_PAD src0_sel:DWORD src1_sel:WORD_1
v_mfma_f32_16x16x32_bf16 v[136:139],v[64:67],v[20:23],v[112:115]
s_waitcnt lgkmcnt(9)
ds_read_b64_tr_b16 v[82:83],v4 offset:10752
v_exp_f16_sdwa v120,v121 dst_sel:WORD_1 dst_unused:UNUSED_PRESERVE src0_sel:WORD_1
v_mfma_f32_16x16x32_bf16 v[140:143],v[68:71],v[20:23],v[116:119]
ds_read_u16_d16_hi v151,v151
v_exp_f16_sdwa v122,v123 dst_sel:WORD_1 dst_unused:UNUSED_PRESERVE src0_sel:WORD_1
v_mfma_f32_16x16x32_bf16 v[132:135],v[68:71],v[16:19],v[116:119]
s_waitcnt lgkmcnt(8)
ds_read_b64_tr_b16 v[84:85],v5 offset:8704
v_pk_fma_f16 v120,v120,s55,s55
v_pk_fma_f16 v122,v122,s55,s55
v_mfma_f32_16x16x32_bf16 v[128:131],v[72:75],v[24:27],v[128:131]
ds_read_u16 v152,v152
v_rcp_f16 v121,v120
v_mfma_f32_16x16x32_bf16 v[136:139],v[72:75],v[28:31],v[136:139]
s_waitcnt lgkmcnt(7)
ds_read_b64_tr_b16 v[86:87],v5 offset:10752
v_rcp_f16 v123,v122
v_mfma_f32_16x16x32_bf16 v[140:143],v[76:79],v[28:31],v[140:143]
ds_read_u16_d16_hi v154,v154
v_rcp_f16_sdwa v121,v120 dst_sel:WORD_1 dst_unused:UNUSED_PRESERVE src0_sel:WORD_1
v_mfma_f32_16x16x32_bf16 v[132:135],v[76:79],v[24:27],v[132:135]
s_waitcnt lgkmcnt(5)
v_rcp_f16_sdwa v123,v122 dst_sel:WORD_1 dst_unused:UNUSED_PRESERVE src0_sel:WORD_1
v_mfma_f32_16x16x32_bf16 v[128:131],v[80:83],v[32:35],v[128:131]
ds_read_u16 v153,v153
v_fma_mix_f32 v156,v156,v121,0 op_sel_hi:[0,1,0]
v_fma_mix_f32 v158,v158,v121,0 op_sel:[0,1,0] op_sel_hi:[0,1,0]
v_mfma_f32_16x16x32_bf16 v[136:139],v[80:83],v[36:39],v[136:139]
s_waitcnt lgkmcnt(2)
s_setprio 0
v_lshl_add_u64 v[144:145],v[144:145],0,v[146:147]
v_lshl_add_u64 v[146:147],v[148:149],0,v[150:151]
v_mfma_f32_16x16x32_bf16 v[140:143],v[84:87],v[36:39],v[140:143]
ds_read_u16_d16_hi v155,v155
v_fma_mix_f32 v157,v157,v123,0 op_sel_hi:[0,1,0]
v_fma_mix_f32 v159,v159,v123,0 op_sel:[0,1,0] op_sel_hi:[0,1,0]
v_mfma_f32_16x16x32_bf16 v[132:135],v[84:87],v[32:35],v[132:135]
v_lshlrev_b32_sdwa v128,1,v128 dst_sel:DWORD dst_unused:UNUSED_PAD src0_sel:DWORD src1_sel:WORD_1
v_lshlrev_b32_sdwa v130,1,v130 dst_sel:DWORD dst_unused:UNUSED_PAD src0_sel:DWORD src1_sel:WORD_1
v_mfma_f32_16x16x32_bf16 v[208:211],v[88:91],v[144:147],v[208:211]
s_barrier
s_setprio 2
ds_read_u16 v128,v128
v_lshlrev_b32_sdwa v129,1,v129 dst_sel:DWORD dst_unused:UNUSED_PAD src0_sel:DWORD src1_sel:WORD_1
v_mfma_f32_16x16x32_bf16 v[216:219],v[92:95],v[144:147],v[216:219]
ds_read_u16_d16_hi v130,v130
v_lshlrev_b32_sdwa v131,1,v131 dst_sel:DWORD dst_unused:UNUSED_PAD src0_sel:DWORD src1_sel:WORD_1
v_mfma_f32_16x16x32_bf16 v[224:227],v[96:99],v[144:147],v[224:227]
v_lshlrev_b32_sdwa v132,1,v132 dst_sel:DWORD dst_unused:UNUSED_PAD src0_sel:DWORD src1_sel:WORD_1
v_mfma_f32_16x16x32_bf16 v[232:235],v[100:103],v[144:147],v[232:235]
s_waitcnt lgkmcnt(2)
ds_read_u16 v129,v129
v_lshl_add_u64 v[152:153],v[152:153],0,v[154:155]
v_lshlrev_b32_sdwa v134,1,v134 dst_sel:DWORD dst_unused:UNUSED_PAD src0_sel:DWORD src1_sel:WORD_1
v_mfma_f32_16x16x32_bf16 v[240:243],v[104:107],v[144:147],v[240:243]
ds_read_u16_d16_hi v131,v131
v_cvt_pk_bf16_f32 v154,v156,v158
v_cvt_pk_bf16_f32 v155,v157,v159
v_mfma_f32_16x16x32_bf16 v[248:251],v[108:111],v[144:147],v[248:251]
v_lshlrev_b32_sdwa v133,1,v133 dst_sel:DWORD dst_unused:UNUSED_PAD src0_sel:DWORD src1_sel:WORD_1
v_mfma_f32_16x16x32_bf16 v[212:215],v[88:91],v[152:155],v[212:215]
ds_read_u16 v132,v132
v_lshlrev_b32_sdwa v135,1,v135 dst_sel:DWORD dst_unused:UNUSED_PAD src0_sel:DWORD src1_sel:WORD_1
v_mfma_f32_16x16x32_bf16 v[220:223],v[92:95],v[152:155],v[220:223]
ds_read_u16_d16_hi v134,v134
v_lshlrev_b32_sdwa v136,1,v136 dst_sel:DWORD dst_unused:UNUSED_PAD src0_sel:DWORD src1_sel:WORD_1
v_mfma_f32_16x16x32_bf16 v[228:231],v[96:99],v[152:155],v[228:231]
ds_read_b64_tr_b16 v[88:89],v6 offset:12320
ds_read_b64_tr_b16 v[90:91],v6 offset:13856
v_lshlrev_b32_sdwa v138,1,v138 dst_sel:DWORD dst_unused:UNUSED_PAD src0_sel:DWORD src1_sel:WORD_1
v_mfma_f32_16x16x32_bf16 v[236:239],v[100:103],v[152:155],v[236:239]
ds_read_u16 v133,v133
v_lshlrev_b32_sdwa v137,1,v137 dst_sel:DWORD dst_unused:UNUSED_PAD src0_sel:DWORD src1_sel:WORD_1
v_mfma_f32_16x16x32_bf16 v[244:247],v[104:107],v[152:155],v[244:247]
ds_read_u16_d16_hi v135,v135
v_lshlrev_b32_sdwa v139,1,v139 dst_sel:DWORD dst_unused:UNUSED_PAD src0_sel:DWORD src1_sel:WORD_1
v_mfma_f32_16x16x32_bf16 v[252:255],v[108:111],v[152:155],v[252:255]
ds_read_b64_tr_b16 v[92:93],v6 offset:12328
ds_read_b64_tr_b16 v[94:95],v6 offset:13864
v_lshlrev_b32_sdwa v140,1,v140 dst_sel:DWORD dst_unused:UNUSED_PAD src0_sel:DWORD src1_sel:WORD_1
v_mfma_f32_16x16x32_bf16 v[144:147],v[64:67],v[40:43],v[112:115]
ds_read_u16 v136,v136
v_lshlrev_b32_sdwa v142,1,v142 dst_sel:DWORD dst_unused:UNUSED_PAD src0_sel:DWORD src1_sel:WORD_1
v_mfma_f32_16x16x32_bf16 v[152:155],v[64:67],v[44:47],v[112:115]
ds_read_u16_d16_hi v138,v138
v_lshlrev_b32_sdwa v141,1,v141 dst_sel:DWORD dst_unused:UNUSED_PAD src0_sel:DWORD src1_sel:WORD_1
v_mfma_f32_16x16x32_bf16 v[156:159],v[68:71],v[44:47],v[116:119]
ds_read_b64_tr_b16 v[96:97],v6 offset:12384
ds_read_b64_tr_b16 v[98:99],v6 offset:13920
v_lshlrev_b32_sdwa v143,1,v143 dst_sel:DWORD dst_unused:UNUSED_PAD src0_sel:DWORD src1_sel:WORD_1
v_mfma_f32_16x16x32_bf16 v[148:151],v[68:71],v[40:43],v[116:119]
ds_read_u16 v137,v137
v_mfma_f32_16x16x32_bf16 v[144:147],v[72:75],v[48:51],v[144:147]
ds_read_u16_d16_hi v139,v139
v_mfma_f32_16x16x32_bf16 v[152:155],v[72:75],v[52:55],v[152:155]
ds_read_b64_tr_b16 v[100:101],v6 offset:12392
ds_read_b64_tr_b16 v[102:103],v6 offset:13928
v_mfma_f32_16x16x32_bf16 v[156:159],v[76:79],v[52:55],v[156:159]
ds_read_u16 v140,v140
v_mfma_f32_16x16x32_bf16 v[148:151],v[76:79],v[48:51],v[148:151]
s_waitcnt lgkmcnt(15)
ds_read_u16_d16_hi v142,v142
v_lshl_add_u64 v[128:129],v[128:129],0,v[130:131]
v_mfma_f32_16x16x32_bf16 v[144:147],v[80:83],v[56:59],v[144:147]
s_waitcnt lgkmcnt(12)
ds_read_b64_tr_b16 v[104:105],v6 offset:12448
ds_read_b64_tr_b16 v[106:107],v6 offset:13984
v_lshl_add_u64 v[130:131],v[132:133],0,v[134:135]
v_mfma_f32_16x16x32_bf16 v[152:155],v[80:83],v[60:63],v[152:155]
ds_read_u16 v141,v141
v_mfma_f32_16x16x32_bf16 v[156:159],v[84:87],v[60:63],v[156:159]
ds_read_u16_d16_hi v143,v143
v_mfma_f32_16x16x32_bf16 v[148:151],v[84:87],v[56:59],v[148:151]
ds_read_b64_tr_b16 v[108:109],v6 offset:12456
ds_read_b64_tr_b16 v[110:111],v6 offset:13992
v_lshlrev_b32_sdwa v144,1,v144 dst_sel:DWORD dst_unused:UNUSED_PAD src0_sel:DWORD src1_sel:WORD_1
v_lshlrev_b32_sdwa v146,1,v146 dst_sel:DWORD dst_unused:UNUSED_PAD src0_sel:DWORD src1_sel:WORD_1
v_mfma_f32_16x16x32_bf16 v[160:163],v[88:91],v[128:131],v[160:163]
s_waitcnt lgkmcnt(15)
ds_read_u16 v144,v144
v_lshlrev_b32_sdwa v145,1,v145 dst_sel:DWORD dst_unused:UNUSED_PAD src0_sel:DWORD src1_sel:WORD_1
v_mfma_f32_16x16x32_bf16 v[168:171],v[92:95],v[128:131],v[168:171]
s_waitcnt lgkmcnt(13)
ds_read_u16_d16_hi v146,v146
v_lshlrev_b32_sdwa v147,1,v147 dst_sel:DWORD dst_unused:UNUSED_PAD src0_sel:DWORD src1_sel:WORD_1
v_mfma_f32_16x16x32_bf16 v[176:179],v[96:99],v[128:131],v[176:179]
s_waitcnt lgkmcnt(10)
ds_read_b64_tr_b16 v[64:65],v4 offset:12320
ds_read_b64_tr_b16 v[66:67],v4 offset:14368
v_lshlrev_b32_sdwa v148,1,v148 dst_sel:DWORD dst_unused:UNUSED_PAD src0_sel:DWORD src1_sel:WORD_1
v_mfma_f32_16x16x32_bf16 v[184:187],v[100:103],v[128:131],v[184:187]
s_waitcnt lgkmcnt(8)
ds_read_u16 v145,v145
v_lshl_add_u64 v[136:137],v[136:137],0,v[138:139]
v_lshlrev_b32_sdwa v150,1,v150 dst_sel:DWORD dst_unused:UNUSED_PAD src0_sel:DWORD src1_sel:WORD_1
v_mfma_f32_16x16x32_bf16 v[192:195],v[104:107],v[128:131],v[192:195]
s_waitcnt lgkmcnt(5)
ds_read_u16_d16_hi v147,v147
v_lshl_add_u64 v[138:139],v[140:141],0,v[142:143]
v_mfma_f32_16x16x32_bf16 v[200:203],v[108:111],v[128:131],v[200:203]
ds_read_b128 v[112:115],v7 offset:1024
ds_read_b64_tr_b16 v[68:69],v5 offset:12320
v_lshlrev_b32_sdwa v149,1,v149 dst_sel:DWORD dst_unused:UNUSED_PAD src0_sel:DWORD src1_sel:WORD_1
v_mfma_f32_16x16x32_bf16 v[164:167],v[88:91],v[136:139],v[164:167]
ds_read_u16 v148,v148
v_lshlrev_b32_sdwa v151,1,v151 dst_sel:DWORD dst_unused:UNUSED_PAD src0_sel:DWORD src1_sel:WORD_1
v_mfma_f32_16x16x32_bf16 v[172:175],v[92:95],v[136:139],v[172:175]
ds_read_u16_d16_hi v150,v150
ds_read_b64_tr_b16 v[70:71],v5 offset:14368
v_lshlrev_b32_sdwa v152,1,v152 dst_sel:DWORD dst_unused:UNUSED_PAD src0_sel:DWORD src1_sel:WORD_1
v_mfma_f32_16x16x32_bf16 v[180:183],v[96:99],v[136:139],v[180:183]
ds_read_b128 v[116:119],v7 offset:1056
ds_read_b64_tr_b16 v[72:73],v4 offset:16416
v_lshlrev_b32_sdwa v154,1,v154 dst_sel:DWORD dst_unused:UNUSED_PAD src0_sel:DWORD src1_sel:WORD_1
v_mfma_f32_16x16x32_bf16 v[188:191],v[100:103],v[136:139],v[188:191]
ds_read_u16 v149,v149
v_lshlrev_b32_sdwa v153,1,v153 dst_sel:DWORD dst_unused:UNUSED_PAD src0_sel:DWORD src1_sel:WORD_1
v_mfma_f32_16x16x32_bf16 v[196:199],v[104:107],v[136:139],v[196:199]
ds_read_u16_d16_hi v151,v151
v_lshlrev_b32_sdwa v155,1,v155 dst_sel:DWORD dst_unused:UNUSED_PAD src0_sel:DWORD src1_sel:WORD_1
v_mfma_f32_16x16x32_bf16 v[204:207],v[108:111],v[136:139],v[204:207]
s_waitcnt lgkmcnt(8)
ds_read_b64_tr_b16 v[74:75],v4 offset:18464
ds_read_b64_tr_b16 v[76:77],v5 offset:16416
v_lshlrev_b32_sdwa v156,1,v156 dst_sel:DWORD dst_unused:UNUSED_PAD src0_sel:DWORD src1_sel:WORD_1
v_mfma_f32_16x16x32_bf16 v[128:131],v[64:67],v[16:19],v[112:115]
ds_read_u16 v152,v152
v_lshlrev_b32_sdwa v158,1,v158 dst_sel:DWORD dst_unused:UNUSED_PAD src0_sel:DWORD src1_sel:WORD_1
v_mfma_f32_16x16x32_bf16 v[136:139],v[64:67],v[20:23],v[112:115]
s_waitcnt lgkmcnt(6)
ds_read_u16_d16_hi v154,v154
ds_read_b64_tr_b16 v[78:79],v5 offset:18464
v_lshlrev_b32_sdwa v157,1,v157 dst_sel:DWORD dst_unused:UNUSED_PAD src0_sel:DWORD src1_sel:WORD_1
v_mfma_f32_16x16x32_bf16 v[140:143],v[68:71],v[20:23],v[116:119]
ds_read_b64_tr_b16 v[80:81],v4 offset:20512
ds_read_b64_tr_b16 v[82:83],v4 offset:22560
v_lshlrev_b32_sdwa v159,1,v159 dst_sel:DWORD dst_unused:UNUSED_PAD src0_sel:DWORD src1_sel:WORD_1
v_mfma_f32_16x16x32_bf16 v[132:135],v[68:71],v[16:19],v[116:119]
s_waitcnt lgkmcnt(6)
ds_read_u16 v153,v153
v_mfma_f32_16x16x32_bf16 v[128:131],v[72:75],v[24:27],v[128:131]
ds_read_u16_d16_hi v155,v155
v_mfma_f32_16x16x32_bf16 v[136:139],v[72:75],v[28:31],v[136:139]
s_waitcnt lgkmcnt(4)
ds_read_b64_tr_b16 v[84:85],v5 offset:20512
ds_read_b64_tr_b16 v[86:87],v5 offset:22560
v_mfma_f32_16x16x32_bf16 v[140:143],v[76:79],v[28:31],v[140:143]
ds_read_u16 v156,v156
v_mfma_f32_16x16x32_bf16 v[132:135],v[76:79],v[24:27],v[132:135]
s_waitcnt lgkmcnt(5)
ds_read_u16_d16_hi v158,v158
v_lshl_add_u64 v[144:145],v[144:145],0,v[146:147]
v_mfma_f32_16x16x32_bf16 v[128:131],v[80:83],v[32:35],v[128:131]
v_lshl_add_u64 v[146:147],v[148:149],0,v[150:151]
v_mfma_f32_16x16x32_bf16 v[136:139],v[80:83],v[36:39],v[136:139]
s_waitcnt lgkmcnt(2)
ds_read_u16 v157,v157
v_mfma_f32_16x16x32_bf16 v[140:143],v[84:87],v[36:39],v[140:143]
ds_read_u16_d16_hi v159,v159
v_mfma_f32_16x16x32_bf16 v[132:135],v[84:87],v[32:35],v[132:135]
s_nop 0
v_lshlrev_b32_sdwa v128,1,v128 dst_sel:DWORD dst_unused:UNUSED_PAD src0_sel:DWORD src1_sel:WORD_1
v_lshlrev_b32_sdwa v130,1,v130 dst_sel:DWORD dst_unused:UNUSED_PAD src0_sel:DWORD src1_sel:WORD_1
v_mfma_f32_16x16x32_bf16 v[208:211],v[88:91],v[144:147],v[208:211]
ds_read_u16 v128,v128
v_lshlrev_b32_sdwa v129,1,v129 dst_sel:DWORD dst_unused:UNUSED_PAD src0_sel:DWORD src1_sel:WORD_1
v_mfma_f32_16x16x32_bf16 v[216:219],v[92:95],v[144:147],v[216:219]
ds_read_u16_d16_hi v130,v130
v_lshlrev_b32_sdwa v131,1,v131 dst_sel:DWORD dst_unused:UNUSED_PAD src0_sel:DWORD src1_sel:WORD_1
v_mfma_f32_16x16x32_bf16 v[224:227],v[96:99],v[144:147],v[224:227]
v_lshlrev_b32_sdwa v132,1,v132 dst_sel:DWORD dst_unused:UNUSED_PAD src0_sel:DWORD src1_sel:WORD_1
v_mfma_f32_16x16x32_bf16 v[232:235],v[100:103],v[144:147],v[232:235]
ds_read_u16 v129,v129
v_lshl_add_u64 v[152:153],v[152:153],0,v[154:155]
v_lshlrev_b32_sdwa v134,1,v134 dst_sel:DWORD dst_unused:UNUSED_PAD src0_sel:DWORD src1_sel:WORD_1
v_mfma_f32_16x16x32_bf16 v[240:243],v[104:107],v[144:147],v[240:243]
s_waitcnt lgkmcnt(3)
ds_read_u16_d16_hi v131,v131
v_lshl_add_u64 v[154:155],v[156:157],0,v[158:159]
v_mfma_f32_16x16x32_bf16 v[248:251],v[108:111],v[144:147],v[248:251]
v_lshlrev_b32_sdwa v133,1,v133 dst_sel:DWORD dst_unused:UNUSED_PAD src0_sel:DWORD src1_sel:WORD_1
v_mfma_f32_16x16x32_bf16 v[212:215],v[88:91],v[152:155],v[212:215]
ds_read_u16 v132,v132
v_lshlrev_b32_sdwa v135,1,v135 dst_sel:DWORD dst_unused:UNUSED_PAD src0_sel:DWORD src1_sel:WORD_1
v_mfma_f32_16x16x32_bf16 v[220:223],v[92:95],v[152:155],v[220:223]
ds_read_u16_d16_hi v134,v134
v_lshlrev_b32_sdwa v136,1,v136 dst_sel:DWORD dst_unused:UNUSED_PAD src0_sel:DWORD src1_sel:WORD_1
v_mfma_f32_16x16x32_bf16 v[228:231],v[96:99],v[152:155],v[228:231]
ds_read_b64_tr_b16 v[88:89],v6 offset:18480
ds_read_b64_tr_b16 v[90:91],v6 offset:20016
v_lshlrev_b32_sdwa v138,1,v138 dst_sel:DWORD dst_unused:UNUSED_PAD src0_sel:DWORD src1_sel:WORD_1
v_mfma_f32_16x16x32_bf16 v[236:239],v[100:103],v[152:155],v[236:239]
ds_read_u16 v133,v133
v_lshlrev_b32_sdwa v137,1,v137 dst_sel:DWORD dst_unused:UNUSED_PAD src0_sel:DWORD src1_sel:WORD_1
v_mfma_f32_16x16x32_bf16 v[244:247],v[104:107],v[152:155],v[244:247]
ds_read_u16_d16_hi v135,v135
v_lshlrev_b32_sdwa v139,1,v139 dst_sel:DWORD dst_unused:UNUSED_PAD src0_sel:DWORD src1_sel:WORD_1
v_mfma_f32_16x16x32_bf16 v[252:255],v[108:111],v[152:155],v[252:255]
ds_read_b64_tr_b16 v[92:93],v6 offset:18488
ds_read_b64_tr_b16 v[94:95],v6 offset:20024
v_lshlrev_b32_sdwa v140,1,v140 dst_sel:DWORD dst_unused:UNUSED_PAD src0_sel:DWORD src1_sel:WORD_1
v_mfma_f32_16x16x32_bf16 v[144:147],v[64:67],v[40:43],v[112:115]
ds_read_u16 v136,v136
v_lshlrev_b32_sdwa v142,1,v142 dst_sel:DWORD dst_unused:UNUSED_PAD src0_sel:DWORD src1_sel:WORD_1
v_mfma_f32_16x16x32_bf16 v[152:155],v[64:67],v[44:47],v[112:115]
ds_read_u16_d16_hi v138,v138
v_lshlrev_b32_sdwa v141,1,v141 dst_sel:DWORD dst_unused:UNUSED_PAD src0_sel:DWORD src1_sel:WORD_1
v_mfma_f32_16x16x32_bf16 v[156:159],v[68:71],v[44:47],v[116:119]
ds_read_b64_tr_b16 v[96:97],v6 offset:18544
ds_read_b64_tr_b16 v[98:99],v6 offset:20080
v_lshlrev_b32_sdwa v143,1,v143 dst_sel:DWORD dst_unused:UNUSED_PAD src0_sel:DWORD src1_sel:WORD_1
v_mfma_f32_16x16x32_bf16 v[148:151],v[68:71],v[40:43],v[116:119]
ds_read_u16 v137,v137
v_mfma_f32_16x16x32_bf16 v[144:147],v[72:75],v[48:51],v[144:147]
ds_read_u16_d16_hi v139,v139
v_mfma_f32_16x16x32_bf16 v[152:155],v[72:75],v[52:55],v[152:155]
ds_read_b64_tr_b16 v[100:101],v6 offset:18552
ds_read_b64_tr_b16 v[102:103],v6 offset:20088
v_mfma_f32_16x16x32_bf16 v[156:159],v[76:79],v[52:55],v[156:159]
ds_read_u16 v140,v140
v_mfma_f32_16x16x32_bf16 v[148:151],v[76:79],v[48:51],v[148:151]
s_waitcnt lgkmcnt(15)
ds_read_u16_d16_hi v142,v142
v_lshl_add_u64 v[128:129],v[128:129],0,v[130:131]
v_mfma_f32_16x16x32_bf16 v[144:147],v[80:83],v[56:59],v[144:147]
s_waitcnt lgkmcnt(12)
ds_read_b64_tr_b16 v[104:105],v6 offset:18608
ds_read_b64_tr_b16 v[106:107],v6 offset:20144
v_lshl_add_u64 v[130:131],v[132:133],0,v[134:135]
v_mfma_f32_16x16x32_bf16 v[152:155],v[80:83],v[60:63],v[152:155]
ds_read_u16 v141,v141
v_mfma_f32_16x16x32_bf16 v[156:159],v[84:87],v[60:63],v[156:159]
ds_read_u16_d16_hi v143,v143
v_mfma_f32_16x16x32_bf16 v[148:151],v[84:87],v[56:59],v[148:151]
ds_read_b64_tr_b16 v[108:109],v6 offset:18616
ds_read_b64_tr_b16 v[110:111],v6 offset:20152
v_lshlrev_b32_sdwa v144,1,v144 dst_sel:DWORD dst_unused:UNUSED_PAD src0_sel:DWORD src1_sel:WORD_1
v_lshlrev_b32_sdwa v146,1,v146 dst_sel:DWORD dst_unused:UNUSED_PAD src0_sel:DWORD src1_sel:WORD_1
v_mfma_f32_16x16x32_bf16 v[160:163],v[88:91],v[128:131],v[160:163]
s_waitcnt lgkmcnt(15)
ds_read_u16 v144,v144
v_lshlrev_b32_sdwa v145,1,v145 dst_sel:DWORD dst_unused:UNUSED_PAD src0_sel:DWORD src1_sel:WORD_1
v_mfma_f32_16x16x32_bf16 v[168:171],v[92:95],v[128:131],v[168:171]
s_waitcnt lgkmcnt(13)
ds_read_u16_d16_hi v146,v146
v_lshlrev_b32_sdwa v147,1,v147 dst_sel:DWORD dst_unused:UNUSED_PAD src0_sel:DWORD src1_sel:WORD_1
v_mfma_f32_16x16x32_bf16 v[176:179],v[96:99],v[128:131],v[176:179]
s_waitcnt lgkmcnt(10)
ds_read_b64_tr_b16 v[64:65],v4 offset:12832
ds_read_b64_tr_b16 v[66:67],v4 offset:14880
v_lshlrev_b32_sdwa v148,1,v148 dst_sel:DWORD dst_unused:UNUSED_PAD src0_sel:DWORD src1_sel:WORD_1
v_mfma_f32_16x16x32_bf16 v[184:187],v[100:103],v[128:131],v[184:187]
s_waitcnt lgkmcnt(8)
ds_read_u16 v145,v145
v_lshl_add_u64 v[136:137],v[136:137],0,v[138:139]
v_lshlrev_b32_sdwa v150,1,v150 dst_sel:DWORD dst_unused:UNUSED_PAD src0_sel:DWORD src1_sel:WORD_1
v_mfma_f32_16x16x32_bf16 v[192:195],v[104:107],v[128:131],v[192:195]
s_waitcnt lgkmcnt(5)
ds_read_u16_d16_hi v147,v147
v_lshl_add_u64 v[138:139],v[140:141],0,v[142:143]
v_mfma_f32_16x16x32_bf16 v[200:203],v[108:111],v[128:131],v[200:203]
ds_read_b128 v[112:115],v7 offset:1152
ds_read_b64_tr_b16 v[68:69],v5 offset:12832
v_lshlrev_b32_sdwa v149,1,v149 dst_sel:DWORD dst_unused:UNUSED_PAD src0_sel:DWORD src1_sel:WORD_1
v_mfma_f32_16x16x32_bf16 v[164:167],v[88:91],v[136:139],v[164:167]
ds_read_u16 v148,v148
v_lshlrev_b32_sdwa v151,1,v151 dst_sel:DWORD dst_unused:UNUSED_PAD src0_sel:DWORD src1_sel:WORD_1
v_mfma_f32_16x16x32_bf16 v[172:175],v[92:95],v[136:139],v[172:175]
ds_read_u16_d16_hi v150,v150
s_nop 0
s_mov_b32 s86,0xbe801d5a
ds_read_b64_tr_b16 v[70:71],v5 offset:14880
v_lshlrev_b32_sdwa v152,1,v152 dst_sel:DWORD dst_unused:UNUSED_PAD src0_sel:DWORD src1_sel:WORD_1
v_mfma_f32_16x16x32_bf16 v[180:183],v[96:99],v[136:139],v[180:183]
ds_read_b128 v[116:119],v7 offset:1184
ds_read_b64_tr_b16 v[72:73],v4 offset:16928
v_lshlrev_b32_sdwa v154,1,v154 dst_sel:DWORD dst_unused:UNUSED_PAD src0_sel:DWORD src1_sel:WORD_1
v_mfma_f32_16x16x32_bf16 v[188:191],v[100:103],v[136:139],v[188:191]
ds_read_u16 v149,v149
v_lshlrev_b32_sdwa v153,1,v153 dst_sel:DWORD dst_unused:UNUSED_PAD src0_sel:DWORD src1_sel:WORD_1
v_mfma_f32_16x16x32_bf16 v[196:199],v[104:107],v[136:139],v[196:199]
ds_read_u16_d16_hi v151,v151
v_lshlrev_b32_sdwa v155,1,v155 dst_sel:DWORD dst_unused:UNUSED_PAD src0_sel:DWORD src1_sel:WORD_1
v_mfma_f32_16x16x32_bf16 v[204:207],v[108:111],v[136:139],v[204:207]
s_waitcnt lgkmcnt(8)
ds_read_b64_tr_b16 v[74:75],v4 offset:18976
ds_read_b64_tr_b16 v[76:77],v5 offset:16928
v_lshlrev_b32_sdwa v156,1,v156 dst_sel:DWORD dst_unused:UNUSED_PAD src0_sel:DWORD src1_sel:WORD_1
v_mfma_f32_16x16x32_bf16 v[128:131],v[64:67],v[16:19],v[112:115]
ds_read_u16 v152,v152
v_lshlrev_b32_sdwa v158,1,v158 dst_sel:DWORD dst_unused:UNUSED_PAD src0_sel:DWORD src1_sel:WORD_1
v_mfma_f32_16x16x32_bf16 v[136:139],v[64:67],v[20:23],v[112:115]
s_waitcnt lgkmcnt(6)
ds_read_u16_d16_hi v154,v154
ds_read_b64_tr_b16 v[78:79],v5 offset:18976
v_lshlrev_b32_sdwa v157,1,v157 dst_sel:DWORD dst_unused:UNUSED_PAD src0_sel:DWORD src1_sel:WORD_1
v_mfma_f32_16x16x32_bf16 v[140:143],v[68:71],v[20:23],v[116:119]
ds_read_b64_tr_b16 v[80:81],v4 offset:21024
ds_read_b64_tr_b16 v[82:83],v4 offset:23072
v_lshlrev_b32_sdwa v159,1,v159 dst_sel:DWORD dst_unused:UNUSED_PAD src0_sel:DWORD src1_sel:WORD_1
v_mfma_f32_16x16x32_bf16 v[132:135],v[68:71],v[16:19],v[116:119]
s_waitcnt lgkmcnt(6)
ds_read_u16 v153,v153
v_mfma_f32_16x16x32_bf16 v[128:131],v[72:75],v[24:27],v[128:131]
ds_read_u16_d16_hi v155,v155
v_mfma_f32_16x16x32_bf16 v[136:139],v[72:75],v[28:31],v[136:139]
s_waitcnt lgkmcnt(4)
ds_read_b64_tr_b16 v[84:85],v5 offset:21024
ds_read_b64_tr_b16 v[86:87],v5 offset:23072
v_mfma_f32_16x16x32_bf16 v[140:143],v[76:79],v[28:31],v[140:143]
ds_read_u16 v156,v156
v_mfma_f32_16x16x32_bf16 v[132:135],v[76:79],v[24:27],v[132:135]
s_waitcnt lgkmcnt(5)
ds_read_u16_d16_hi v158,v158
v_lshl_add_u64 v[144:145],v[144:145],0,v[146:147]
v_mfma_f32_16x16x32_bf16 v[128:131],v[80:83],v[32:35],v[128:131]
v_lshl_add_u64 v[146:147],v[148:149],0,v[150:151]
v_mfma_f32_16x16x32_bf16 v[136:139],v[80:83],v[36:39],v[136:139]
s_waitcnt lgkmcnt(2)
ds_read_u16 v157,v157
v_mfma_f32_16x16x32_bf16 v[140:143],v[84:87],v[36:39],v[140:143]
ds_read_u16_d16_hi v159,v159
v_mfma_f32_16x16x32_bf16 v[132:135],v[84:87],v[32:35],v[132:135]
s_nop 0
v_lshlrev_b32_sdwa v128,1,v128 dst_sel:DWORD dst_unused:UNUSED_PAD src0_sel:DWORD src1_sel:WORD_1
v_lshlrev_b32_sdwa v130,1,v130 dst_sel:DWORD dst_unused:UNUSED_PAD src0_sel:DWORD src1_sel:WORD_1
v_mfma_f32_16x16x32_bf16 v[208:211],v[88:91],v[144:147],v[208:211]
s_barrier
ds_read_u16 v128,v128
v_lshlrev_b32_sdwa v129,1,v129 dst_sel:DWORD dst_unused:UNUSED_PAD src0_sel:DWORD src1_sel:WORD_1
v_lshlrev_b32_sdwa v131,1,v131 dst_sel:DWORD dst_unused:UNUSED_PAD src0_sel:DWORD src1_sel:WORD_1
v_mfma_f32_16x16x32_bf16 v[216:219],v[92:95],v[144:147],v[216:219]
v_fma_mixlo_f16 v120,v141,s99,0
v_lshlrev_b32_sdwa v132,1,v132 dst_sel:DWORD dst_unused:UNUSED_PAD src0_sel:DWORD src1_sel:WORD_1
v_mfma_f32_16x16x32_bf16 v[224:227],v[96:99],v[144:147],v[224:227]
ds_read_u16_d16_hi v130,v130
v_fma_mixhi_f16 v120,v143,s99,0
v_lshlrev_b32_sdwa v134,1,v134 dst_sel:DWORD dst_unused:UNUSED_PAD src0_sel:DWORD src1_sel:WORD_1
v_mfma_f32_16x16x32_bf16 v[232:235],v[100:103],v[144:147],v[232:235]
ds_read_u16 v129,v129
v_lshl_add_u64 v[152:153],v[152:153],0,v[154:155]
v_mfma_f32_16x16x32_bf16 v[240:243],v[104:107],v[144:147],v[240:243]
s_waitcnt lgkmcnt(3)
v_lshl_add_u64 v[154:155],v[156:157],0,v[158:159]
v_mfma_f32_16x16x32_bf16 v[248:251],v[108:111],v[144:147],v[248:251]
ds_read_u16_d16_hi v131,v131
v_pk_fma_f16 v121,v120,v120,s54
v_lshlrev_b32_sdwa v133,1,v133 dst_sel:DWORD dst_unused:UNUSED_PAD src0_sel:DWORD src1_sel:WORD_1
v_mfma_f32_16x16x32_bf16 v[212:215],v[88:91],v[152:155],v[212:215]
ds_read_b64_tr_b16 v[88:89],v6 offset:32832
v_pk_mul_f16 v121,v120,v121
v_lshlrev_b32_sdwa v135,1,v135 dst_sel:DWORD dst_unused:UNUSED_PAD src0_sel:DWORD src1_sel:WORD_1
v_mfma_f32_16x16x32_bf16 v[220:223],v[92:95],v[152:155],v[220:223]
ds_read_u16 v132,v132
v_exp_f16 v120,v121
v_mfma_f32_16x16x32_bf16 v[228:231],v[96:99],v[152:155],v[228:231]
ds_read_b64_tr_b16 v[90:91],v6 offset:34368
ds_read_b64_tr_b16 v[92:93],v6 offset:32840
v_lshlrev_b32_sdwa v136,1,v136 dst_sel:DWORD dst_unused:UNUSED_PAD src0_sel:DWORD src1_sel:WORD_1
v_mfma_f32_16x16x32_bf16 v[236:239],v[100:103],v[152:155],v[236:239]
ds_read_u16_d16_hi v134,v134
v_exp_f16_sdwa v120,v121 dst_sel:WORD_1 dst_unused:UNUSED_PRESERVE src0_sel:WORD_1
v_mfma_f32_16x16x32_bf16 v[244:247],v[104:107],v[152:155],v[244:247]
ds_read_u16 v133,v133
v_lshlrev_b32_sdwa v138,1,v138 dst_sel:DWORD dst_unused:UNUSED_PAD src0_sel:DWORD src1_sel:WORD_1
v_mfma_f32_16x16x32_bf16 v[252:255],v[108:111],v[152:155],v[252:255]
ds_read_b64_tr_b16 v[94:95],v6 offset:34376
ds_read_b64_tr_b16 v[96:97],v6 offset:32896
v_pk_fma_f16 v120,v120,s55,s55
v_lshlrev_b32_sdwa v137,1,v137 dst_sel:DWORD dst_unused:UNUSED_PAD src0_sel:DWORD src1_sel:WORD_1
v_mfma_f32_16x16x32_bf16 v[144:147],v[64:67],v[40:43],v[112:115]
ds_read_u16_d16_hi v135,v135
v_rcp_f16 v121,v120
v_mfma_f32_16x16x32_bf16 v[152:155],v[64:67],v[44:47],v[112:115]
ds_read_b64_tr_b16 v[98:99],v6 offset:34432
ds_read_b64_tr_b16 v[100:101],v6 offset:32904
v_lshlrev_b32_sdwa v139,1,v139 dst_sel:DWORD dst_unused:UNUSED_PAD src0_sel:DWORD src1_sel:WORD_1
v_mfma_f32_16x16x32_bf16 v[156:159],v[68:71],v[44:47],v[116:119]
ds_read_u16 v136,v136
v_rcp_f16_sdwa v121,v120 dst_sel:WORD_1 dst_unused:UNUSED_PRESERVE src0_sel:WORD_1
v_mfma_f32_16x16x32_bf16 v[148:151],v[68:71],v[40:43],v[116:119]
ds_read_u16_d16_hi v138,v138
v_lshlrev_b32_sdwa v140,1,v140 dst_sel:DWORD dst_unused:UNUSED_PAD src0_sel:DWORD src1_sel:WORD_1
v_mfma_f32_16x16x32_bf16 v[144:147],v[72:75],v[48:51],v[144:147]
ds_read_b64_tr_b16 v[102:103],v6 offset:34440
ds_read_b64_tr_b16 v[104:105],v6 offset:32960
v_fma_mix_f32 v141,v141,v121,0 op_sel_hi:[0,1,0]
v_lshlrev_b32_sdwa v142,1,v142 dst_sel:DWORD dst_unused:UNUSED_PAD src0_sel:DWORD src1_sel:WORD_1
v_mfma_f32_16x16x32_bf16 v[152:155],v[72:75],v[52:55],v[152:155]
ds_read_u16 v137,v137
v_fma_mix_f32 v143,v143,v121,0 op_sel:[0,1,0] op_sel_hi:[0,1,0]
v_mfma_f32_16x16x32_bf16 v[156:159],v[76:79],v[52:55],v[156:159]
ds_read_b64_tr_b16 v[106:107],v6 offset:34496
ds_read_b64_tr_b16 v[108:109],v6 offset:32968
v_mfma_f32_16x16x32_bf16 v[148:151],v[76:79],v[48:51],v[148:151]
ds_read_u16_d16_hi v139,v139
v_mfma_f32_16x16x32_bf16 v[144:147],v[80:83],v[56:59],v[144:147]
ds_read_b64_tr_b16 v[110:111],v6 offset:34504
ds_read_b64_tr_b16 v[64:65],v8 offset:0
v_mfma_f32_16x16x32_bf16 v[152:155],v[80:83],v[60:63],v[152:155]
s_waitcnt lgkmcnt(12)
ds_read_u16 v140,v140
v_lshl_add_u64 v[128:129],v[128:129],0,v[130:131]
v_lshl_add_u64 v[130:131],v[132:133],0,v[134:135]
v_mfma_f32_16x16x32_bf16 v[156:159],v[84:87],v[60:63],v[156:159]
ds_read_u16_d16_hi v142,v142
v_mfma_f32_16x16x32_bf16 v[148:151],v[84:87],v[56:59],v[148:151]
ds_read_b64_tr_b16 v[66:67],v8 offset:2048
ds_read_b128 v[112:115],v7 offset:1280
v_lshlrev_b32_sdwa v144,1,v144 dst_sel:DWORD dst_unused:UNUSED_PAD src0_sel:DWORD src1_sel:WORD_1
v_lshlrev_b32_sdwa v146,1,v146 dst_sel:DWORD dst_unused:UNUSED_PAD src0_sel:DWORD src1_sel:WORD_1
v_mfma_f32_16x16x32_bf16 v[160:163],v[88:91],v[128:131],v[160:163]
ds_read_u16 v144,v144
v_lshlrev_b32_sdwa v145,1,v145 dst_sel:DWORD dst_unused:UNUSED_PAD src0_sel:DWORD src1_sel:WORD_1
v_lshlrev_b32_sdwa v147,1,v147 dst_sel:DWORD dst_unused:UNUSED_PAD src0_sel:DWORD src1_sel:WORD_1
v_mfma_f32_16x16x32_bf16 v[168:171],v[92:95],v[128:131],v[168:171]
s_waitcnt lgkmcnt(15)
ds_read_b64_tr_b16 v[68:69],v9 offset:0
ds_read_b64_tr_b16 v[70:71],v9 offset:2048
v_fma_mixlo_f16 v120,v157,s99,0
v_lshlrev_b32_sdwa v148,1,v148 dst_sel:DWORD dst_unused:UNUSED_PAD src0_sel:DWORD src1_sel:WORD_1
v_mfma_f32_16x16x32_bf16 v[176:179],v[96:99],v[128:131],v[176:179]
s_waitcnt lgkmcnt(14)
ds_read_u16_d16_hi v146,v146
v_fma_mixhi_f16 v120,v159,s99,0
v_lshlrev_b32_sdwa v150,1,v150 dst_sel:DWORD dst_unused:UNUSED_PAD src0_sel:DWORD src1_sel:WORD_1
v_mfma_f32_16x16x32_bf16 v[184:187],v[100:103],v[128:131],v[184:187]
s_waitcnt lgkmcnt(10)
ds_read_u16 v145,v145
v_lshl_add_u64 v[136:137],v[136:137],0,v[138:139]
v_mfma_f32_16x16x32_bf16 v[192:195],v[104:107],v[128:131],v[192:195]
s_waitcnt lgkmcnt(7)
ds_read_b128 v[116:119],v7 offset:1312
ds_read_b64_tr_b16 v[72:73],v8 offset:4096
v_or_b32 v138,v142,v140
v_cvt_pk_bf16_f32 v139,v141,v143
v_mfma_f32_16x16x32_bf16 v[200:203],v[108:111],v[128:131],v[200:203]
ds_read_u16_d16_hi v147,v147
v_pk_fma_f16 v121,v120,v120,s54
v_lshlrev_b32_sdwa v149,1,v149 dst_sel:DWORD dst_unused:UNUSED_PAD src0_sel:DWORD src1_sel:WORD_1
v_mfma_f32_16x16x32_bf16 v[164:167],v[88:91],v[136:139],v[164:167]
ds_read_b64_tr_b16 v[74:75],v8 offset:6144
ds_read_b64_tr_b16 v[76:77],v9 offset:4096
v_pk_mul_f16 v121,v120,v121
v_lshlrev_b32_sdwa v151,1,v151 dst_sel:DWORD dst_unused:UNUSED_PAD src0_sel:DWORD src1_sel:WORD_1
v_mfma_f32_16x16x32_bf16 v[172:175],v[92:95],v[136:139],v[172:175]
ds_read_u16 v148,v148
v_exp_f16 v120,v121
v_mfma_f32_16x16x32_bf16 v[180:183],v[96:99],v[136:139],v[180:183]
ds_read_b64_tr_b16 v[78:79],v9 offset:6144
ds_read_b64_tr_b16 v[80:81],v8 offset:8192
v_lshlrev_b32_sdwa v152,1,v152 dst_sel:DWORD dst_unused:UNUSED_PAD src0_sel:DWORD src1_sel:WORD_1
v_mfma_f32_16x16x32_bf16 v[188:191],v[100:103],v[136:139],v[188:191]
ds_read_u16_d16_hi v150,v150
v_exp_f16_sdwa v120,v121 dst_sel:WORD_1 dst_unused:UNUSED_PRESERVE src0_sel:WORD_1
v_mfma_f32_16x16x32_bf16 v[196:199],v[104:107],v[136:139],v[196:199]
ds_read_u16 v149,v149
v_lshlrev_b32_sdwa v154,1,v154 dst_sel:DWORD dst_unused:UNUSED_PAD src0_sel:DWORD src1_sel:WORD_1
v_mfma_f32_16x16x32_bf16 v[204:207],v[108:111],v[136:139],v[204:207]
s_waitcnt lgkmcnt(15)
ds_read_b64_tr_b16 v[82:83],v8 offset:10240
v_pk_fma_f16 v120,v120,s55,s55
v_lshlrev_b32_sdwa v153,1,v153 dst_sel:DWORD dst_unused:UNUSED_PAD src0_sel:DWORD src1_sel:WORD_1
v_mfma_f32_16x16x32_bf16 v[128:131],v[64:67],v[16:19],v[112:115]
ds_read_u16_d16_hi v151,v151
v_rcp_f16 v121,v120
v_mfma_f32_16x16x32_bf16 v[136:139],v[64:67],v[20:23],v[112:115]
s_waitcnt lgkmcnt(11)
ds_read_b64_tr_b16 v[84:85],v9 offset:8192
v_lshlrev_b32_sdwa v155,1,v155 dst_sel:DWORD dst_unused:UNUSED_PAD src0_sel:DWORD src1_sel:WORD_1
v_mfma_f32_16x16x32_bf16 v[140:143],v[68:71],v[20:23],v[116:119]
ds_read_u16 v152,v152
v_rcp_f16_sdwa v121,v120 dst_sel:WORD_1 dst_unused:UNUSED_PRESERVE src0_sel:WORD_1
v_mfma_f32_16x16x32_bf16 v[132:135],v[68:71],v[16:19],v[116:119]
s_waitcnt lgkmcnt(10)
ds_read_u16_d16_hi v154,v154
v_lshlrev_b32_sdwa v156,1,v156 dst_sel:DWORD dst_unused:UNUSED_PAD src0_sel:DWORD src1_sel:WORD_1
v_mfma_f32_16x16x32_bf16 v[128:131],v[72:75],v[24:27],v[128:131]
ds_read_b64_tr_b16 v[86:87],v9 offset:10240
v_fma_mix_f32 v157,v157,v121,0 op_sel_hi:[0,1,0]
v_lshlrev_b32_sdwa v158,1,v158 dst_sel:DWORD dst_unused:UNUSED_PAD src0_sel:DWORD src1_sel:WORD_1
v_mfma_f32_16x16x32_bf16 v[136:139],v[72:75],v[28:31],v[136:139]
s_waitcnt lgkmcnt(9)
ds_read_u16 v153,v153
v_fma_mix_f32 v159,v159,v121,0 op_sel:[0,1,0] op_sel_hi:[0,1,0]
v_mfma_f32_16x16x32_bf16 v[140:143],v[76:79],v[28:31],v[140:143]
v_mfma_f32_16x16x32_bf16 v[132:135],v[76:79],v[24:27],v[132:135]
s_waitcnt lgkmcnt(6)
ds_read_u16_d16_hi v155,v155
v_mfma_f32_16x16x32_bf16 v[128:131],v[80:83],v[32:35],v[128:131]
v_mfma_f32_16x16x32_bf16 v[136:139],v[80:83],v[36:39],v[136:139]
s_waitcnt lgkmcnt(2)
ds_read_u16 v156,v156
v_lshl_add_u64 v[144:145],v[144:145],0,v[146:147]
v_lshl_add_u64 v[146:147],v[148:149],0,v[150:151]
v_mfma_f32_16x16x32_bf16 v[140:143],v[84:87],v[36:39],v[140:143]
ds_read_u16_d16_hi v158,v158
v_mfma_f32_16x16x32_bf16 v[132:135],v[84:87],v[32:35],v[132:135]
v_lshlrev_b32_sdwa v128,1,v128 dst_sel:DWORD dst_unused:UNUSED_PAD src0_sel:DWORD src1_sel:WORD_1
v_lshlrev_b32_sdwa v130,1,v130 dst_sel:DWORD dst_unused:UNUSED_PAD src0_sel:DWORD src1_sel:WORD_1
v_mfma_f32_16x16x32_bf16 v[208:211],v[88:91],v[144:147],v[208:211]
ds_read_u16 v128,v128
v_lshlrev_b32_sdwa v129,1,v129 dst_sel:DWORD dst_unused:UNUSED_PAD src0_sel:DWORD src1_sel:WORD_1
v_lshlrev_b32_sdwa v131,1,v131 dst_sel:DWORD dst_unused:UNUSED_PAD src0_sel:DWORD src1_sel:WORD_1
v_mfma_f32_16x16x32_bf16 v[216:219],v[92:95],v[144:147],v[216:219]
v_fma_mixlo_f16 v120,v141,s99,0
v_lshlrev_b32_sdwa v132,1,v132 dst_sel:DWORD dst_unused:UNUSED_PAD src0_sel:DWORD src1_sel:WORD_1
v_mfma_f32_16x16x32_bf16 v[224:227],v[96:99],v[144:147],v[224:227]
ds_read_u16_d16_hi v130,v130
v_fma_mixhi_f16 v120,v143,s99,0
v_lshlrev_b32_sdwa v134,1,v134 dst_sel:DWORD dst_unused:UNUSED_PAD src0_sel:DWORD src1_sel:WORD_1
v_mfma_f32_16x16x32_bf16 v[232:235],v[100:103],v[144:147],v[232:235]
s_waitcnt lgkmcnt(4)
ds_read_u16 v129,v129
v_lshl_add_u64 v[152:153],v[152:153],0,v[154:155]
v_mfma_f32_16x16x32_bf16 v[240:243],v[104:107],v[144:147],v[240:243]
s_waitcnt lgkmcnt(3)
v_or_b32 v154,v158,v156
v_cvt_pk_bf16_f32 v155,v157,v159
v_mfma_f32_16x16x32_bf16 v[248:251],v[108:111],v[144:147],v[248:251]
ds_read_u16_d16_hi v131,v131
v_pk_fma_f16 v121,v120,v120,s54
v_lshlrev_b32_sdwa v133,1,v133 dst_sel:DWORD dst_unused:UNUSED_PAD src0_sel:DWORD src1_sel:WORD_1
v_mfma_f32_16x16x32_bf16 v[212:215],v[88:91],v[152:155],v[212:215]
ds_read_b64_tr_b16 v[88:89],v6 offset:38992
v_pk_mul_f16 v121,v120,v121
v_lshlrev_b32_sdwa v135,1,v135 dst_sel:DWORD dst_unused:UNUSED_PAD src0_sel:DWORD src1_sel:WORD_1
v_mfma_f32_16x16x32_bf16 v[220:223],v[92:95],v[152:155],v[220:223]
ds_read_u16 v132,v132
v_exp_f16 v120,v121
v_mfma_f32_16x16x32_bf16 v[228:231],v[96:99],v[152:155],v[228:231]
ds_read_b64_tr_b16 v[90:91],v6 offset:40528
ds_read_b64_tr_b16 v[92:93],v6 offset:39000
v_lshlrev_b32_sdwa v136,1,v136 dst_sel:DWORD dst_unused:UNUSED_PAD src0_sel:DWORD src1_sel:WORD_1
v_mfma_f32_16x16x32_bf16 v[236:239],v[100:103],v[152:155],v[236:239]
ds_read_u16_d16_hi v134,v134
v_exp_f16_sdwa v120,v121 dst_sel:WORD_1 dst_unused:UNUSED_PRESERVE src0_sel:WORD_1
v_mfma_f32_16x16x32_bf16 v[244:247],v[104:107],v[152:155],v[244:247]
ds_read_u16 v133,v133
v_lshlrev_b32_sdwa v138,1,v138 dst_sel:DWORD dst_unused:UNUSED_PAD src0_sel:DWORD src1_sel:WORD_1
v_mfma_f32_16x16x32_bf16 v[252:255],v[108:111],v[152:155],v[252:255]
ds_read_b64_tr_b16 v[94:95],v6 offset:40536
ds_read_b64_tr_b16 v[96:97],v6 offset:39056
v_pk_fma_f16 v120,v120,s55,s55
v_lshlrev_b32_sdwa v137,1,v137 dst_sel:DWORD dst_unused:UNUSED_PAD src0_sel:DWORD src1_sel:WORD_1
v_mfma_f32_16x16x32_bf16 v[144:147],v[64:67],v[40:43],v[112:115]
ds_read_u16_d16_hi v135,v135
v_rcp_f16 v121,v120
v_mfma_f32_16x16x32_bf16 v[152:155],v[64:67],v[44:47],v[112:115]
ds_read_b64_tr_b16 v[98:99],v6 offset:40592
ds_read_b64_tr_b16 v[100:101],v6 offset:39064
v_lshlrev_b32_sdwa v139,1,v139 dst_sel:DWORD dst_unused:UNUSED_PAD src0_sel:DWORD src1_sel:WORD_1
v_mfma_f32_16x16x32_bf16 v[156:159],v[68:71],v[44:47],v[116:119]
ds_read_u16 v136,v136
v_rcp_f16_sdwa v121,v120 dst_sel:WORD_1 dst_unused:UNUSED_PRESERVE src0_sel:WORD_1
v_mfma_f32_16x16x32_bf16 v[148:151],v[68:71],v[40:43],v[116:119]
ds_read_u16_d16_hi v138,v138
v_lshlrev_b32_sdwa v140,1,v140 dst_sel:DWORD dst_unused:UNUSED_PAD src0_sel:DWORD src1_sel:WORD_1
v_mfma_f32_16x16x32_bf16 v[144:147],v[72:75],v[48:51],v[144:147]
ds_read_b64_tr_b16 v[102:103],v6 offset:40600
ds_read_b64_tr_b16 v[104:105],v6 offset:39120
v_fma_mix_f32 v141,v141,v121,0 op_sel_hi:[0,1,0]
v_lshlrev_b32_sdwa v142,1,v142 dst_sel:DWORD dst_unused:UNUSED_PAD src0_sel:DWORD src1_sel:WORD_1
v_mfma_f32_16x16x32_bf16 v[152:155],v[72:75],v[52:55],v[152:155]
ds_read_u16 v137,v137
v_fma_mix_f32 v143,v143,v121,0 op_sel:[0,1,0] op_sel_hi:[0,1,0]
v_mfma_f32_16x16x32_bf16 v[156:159],v[76:79],v[52:55],v[156:159]
ds_read_b64_tr_b16 v[106:107],v6 offset:40656
ds_read_b64_tr_b16 v[108:109],v6 offset:39128
v_mfma_f32_16x16x32_bf16 v[148:151],v[76:79],v[48:51],v[148:151]
ds_read_u16_d16_hi v139,v139
v_mfma_f32_16x16x32_bf16 v[144:147],v[80:83],v[56:59],v[144:147]
ds_read_b64_tr_b16 v[110:111],v6 offset:40664
ds_read_b64_tr_b16 v[64:65],v8 offset:512
v_mfma_f32_16x16x32_bf16 v[152:155],v[80:83],v[60:63],v[152:155]
s_waitcnt lgkmcnt(12)
ds_read_u16 v140,v140
v_lshl_add_u64 v[128:129],v[128:129],0,v[130:131]
v_lshl_add_u64 v[130:131],v[132:133],0,v[134:135]
v_mfma_f32_16x16x32_bf16 v[156:159],v[84:87],v[60:63],v[156:159]
ds_read_u16_d16_hi v142,v142
v_mfma_f32_16x16x32_bf16 v[148:151],v[84:87],v[56:59],v[148:151]
ds_read_b64_tr_b16 v[66:67],v8 offset:2560
ds_read_b128 v[112:115],v7 offset:1408
v_lshlrev_b32_sdwa v144,1,v144 dst_sel:DWORD dst_unused:UNUSED_PAD src0_sel:DWORD src1_sel:WORD_1
v_lshlrev_b32_sdwa v146,1,v146 dst_sel:DWORD dst_unused:UNUSED_PAD src0_sel:DWORD src1_sel:WORD_1
v_mfma_f32_16x16x32_bf16 v[160:163],v[88:91],v[128:131],v[160:163]
ds_read_u16 v144,v144
v_lshlrev_b32_sdwa v145,1,v145 dst_sel:DWORD dst_unused:UNUSED_PAD src0_sel:DWORD src1_sel:WORD_1
v_lshlrev_b32_sdwa v147,1,v147 dst_sel:DWORD dst_unused:UNUSED_PAD src0_sel:DWORD src1_sel:WORD_1
v_mfma_f32_16x16x32_bf16 v[168:171],v[92:95],v[128:131],v[168:171]
s_waitcnt lgkmcnt(15)
ds_read_b64_tr_b16 v[68:69],v9 offset:512
ds_read_b64_tr_b16 v[70:71],v9 offset:2560
v_fma_mixlo_f16 v120,v157,s99,0
v_lshlrev_b32_sdwa v148,1,v148 dst_sel:DWORD dst_unused:UNUSED_PAD src0_sel:DWORD src1_sel:WORD_1
v_mfma_f32_16x16x32_bf16 v[176:179],v[96:99],v[128:131],v[176:179]
s_waitcnt lgkmcnt(14)
ds_read_u16_d16_hi v146,v146
v_fma_mixhi_f16 v120,v159,s99,0
v_lshlrev_b32_sdwa v150,1,v150 dst_sel:DWORD dst_unused:UNUSED_PAD src0_sel:DWORD src1_sel:WORD_1
v_mfma_f32_16x16x32_bf16 v[184:187],v[100:103],v[128:131],v[184:187]
s_waitcnt lgkmcnt(10)
ds_read_u16 v145,v145
v_lshl_add_u64 v[136:137],v[136:137],0,v[138:139]
v_mfma_f32_16x16x32_bf16 v[192:195],v[104:107],v[128:131],v[192:195]
s_waitcnt lgkmcnt(7)
ds_read_b128 v[116:119],v7 offset:1440
ds_read_b64_tr_b16 v[72:73],v8 offset:4608
v_or_b32 v138,v142,v140
v_cvt_pk_bf16_f32 v139,v141,v143
v_mfma_f32_16x16x32_bf16 v[200:203],v[108:111],v[128:131],v[200:203]
ds_read_u16_d16_hi v147,v147
v_pk_fma_f16 v121,v120,v120,s54
v_lshlrev_b32_sdwa v149,1,v149 dst_sel:DWORD dst_unused:UNUSED_PAD src0_sel:DWORD src1_sel:WORD_1
v_mfma_f32_16x16x32_bf16 v[164:167],v[88:91],v[136:139],v[164:167]
ds_read_b64_tr_b16 v[74:75],v8 offset:6656
ds_read_b64_tr_b16 v[76:77],v9 offset:4608
v_pk_mul_f16 v121,v120,v121
v_lshlrev_b32_sdwa v151,1,v151 dst_sel:DWORD dst_unused:UNUSED_PAD src0_sel:DWORD src1_sel:WORD_1
v_mfma_f32_16x16x32_bf16 v[172:175],v[92:95],v[136:139],v[172:175]
ds_read_u16 v148,v148
v_exp_f16 v120,v121
v_mfma_f32_16x16x32_bf16 v[180:183],v[96:99],v[136:139],v[180:183]
ds_read_b64_tr_b16 v[78:79],v9 offset:6656
ds_read_b64_tr_b16 v[80:81],v8 offset:8704
v_lshlrev_b32_sdwa v152,1,v152 dst_sel:DWORD dst_unused:UNUSED_PAD src0_sel:DWORD src1_sel:WORD_1
v_mfma_f32_16x16x32_bf16 v[188:191],v[100:103],v[136:139],v[188:191]
ds_read_u16_d16_hi v150,v150
v_exp_f16_sdwa v120,v121 dst_sel:WORD_1 dst_unused:UNUSED_PRESERVE src0_sel:WORD_1
v_mfma_f32_16x16x32_bf16 v[196:199],v[104:107],v[136:139],v[196:199]
ds_read_u16 v149,v149
v_lshlrev_b32_sdwa v154,1,v154 dst_sel:DWORD dst_unused:UNUSED_PAD src0_sel:DWORD src1_sel:WORD_1
v_mfma_f32_16x16x32_bf16 v[204:207],v[108:111],v[136:139],v[204:207]
s_waitcnt lgkmcnt(15)
ds_read_b64_tr_b16 v[82:83],v8 offset:10752
v_pk_fma_f16 v120,v120,s55,s55
v_lshlrev_b32_sdwa v153,1,v153 dst_sel:DWORD dst_unused:UNUSED_PAD src0_sel:DWORD src1_sel:WORD_1
v_mfma_f32_16x16x32_bf16 v[128:131],v[64:67],v[16:19],v[112:115]
ds_read_u16_d16_hi v151,v151
v_rcp_f16 v121,v120
v_mfma_f32_16x16x32_bf16 v[136:139],v[64:67],v[20:23],v[112:115]
s_waitcnt lgkmcnt(11)
ds_read_b64_tr_b16 v[84:85],v9 offset:8704
v_lshlrev_b32_sdwa v155,1,v155 dst_sel:DWORD dst_unused:UNUSED_PAD src0_sel:DWORD src1_sel:WORD_1
v_mfma_f32_16x16x32_bf16 v[140:143],v[68:71],v[20:23],v[116:119]
ds_read_u16 v152,v152
v_rcp_f16_sdwa v121,v120 dst_sel:WORD_1 dst_unused:UNUSED_PRESERVE src0_sel:WORD_1
v_mfma_f32_16x16x32_bf16 v[132:135],v[68:71],v[16:19],v[116:119]
s_waitcnt lgkmcnt(10)
ds_read_u16_d16_hi v154,v154
v_lshlrev_b32_sdwa v156,1,v156 dst_sel:DWORD dst_unused:UNUSED_PAD src0_sel:DWORD src1_sel:WORD_1
v_mfma_f32_16x16x32_bf16 v[128:131],v[72:75],v[24:27],v[128:131]
ds_read_b64_tr_b16 v[86:87],v9 offset:10752
v_fma_mix_f32 v157,v157,v121,0 op_sel_hi:[0,1,0]
v_lshlrev_b32_sdwa v158,1,v158 dst_sel:DWORD dst_unused:UNUSED_PAD src0_sel:DWORD src1_sel:WORD_1
v_mfma_f32_16x16x32_bf16 v[136:139],v[72:75],v[28:31],v[136:139]
s_waitcnt lgkmcnt(9)
ds_read_u16 v153,v153
v_fma_mix_f32 v159,v159,v121,0 op_sel:[0,1,0] op_sel_hi:[0,1,0]
v_mfma_f32_16x16x32_bf16 v[140:143],v[76:79],v[28:31],v[140:143]
v_mfma_f32_16x16x32_bf16 v[132:135],v[76:79],v[24:27],v[132:135]
s_waitcnt lgkmcnt(6)
ds_read_u16_d16_hi v155,v155
v_mfma_f32_16x16x32_bf16 v[128:131],v[80:83],v[32:35],v[128:131]
v_mfma_f32_16x16x32_bf16 v[136:139],v[80:83],v[36:39],v[136:139]
s_waitcnt lgkmcnt(2)
ds_read_u16 v156,v156
v_lshl_add_u64 v[144:145],v[144:145],0,v[146:147]
v_lshl_add_u64 v[146:147],v[148:149],0,v[150:151]
v_mfma_f32_16x16x32_bf16 v[140:143],v[84:87],v[36:39],v[140:143]
ds_read_u16_d16_hi v158,v158
v_mfma_f32_16x16x32_bf16 v[132:135],v[84:87],v[32:35],v[132:135]
v_lshlrev_b32_sdwa v128,1,v128 dst_sel:DWORD dst_unused:UNUSED_PAD src0_sel:DWORD src1_sel:WORD_1
v_lshlrev_b32_sdwa v130,1,v130 dst_sel:DWORD dst_unused:UNUSED_PAD src0_sel:DWORD src1_sel:WORD_1
v_mfma_f32_16x16x32_bf16 v[208:211],v[88:91],v[144:147],v[208:211]
ds_read_u16 v128,v128
v_lshlrev_b32_sdwa v129,1,v129 dst_sel:DWORD dst_unused:UNUSED_PAD src0_sel:DWORD src1_sel:WORD_1
v_lshlrev_b32_sdwa v131,1,v131 dst_sel:DWORD dst_unused:UNUSED_PAD src0_sel:DWORD src1_sel:WORD_1
v_mfma_f32_16x16x32_bf16 v[216:219],v[92:95],v[144:147],v[216:219]
v_fma_mixlo_f16 v120,v141,s99,0
v_lshlrev_b32_sdwa v132,1,v132 dst_sel:DWORD dst_unused:UNUSED_PAD src0_sel:DWORD src1_sel:WORD_1
v_mfma_f32_16x16x32_bf16 v[224:227],v[96:99],v[144:147],v[224:227]
ds_read_u16_d16_hi v130,v130
v_fma_mixhi_f16 v120,v143,s99,0
v_lshlrev_b32_sdwa v134,1,v134 dst_sel:DWORD dst_unused:UNUSED_PAD src0_sel:DWORD src1_sel:WORD_1
v_mfma_f32_16x16x32_bf16 v[232:235],v[100:103],v[144:147],v[232:235]
s_waitcnt lgkmcnt(4)
ds_read_u16 v129,v129
v_lshl_add_u64 v[152:153],v[152:153],0,v[154:155]
v_mfma_f32_16x16x32_bf16 v[240:243],v[104:107],v[144:147],v[240:243]
s_waitcnt lgkmcnt(3)
v_or_b32 v154,v158,v156
v_cvt_pk_bf16_f32 v155,v157,v159
v_mfma_f32_16x16x32_bf16 v[248:251],v[108:111],v[144:147],v[248:251]
ds_read_u16_d16_hi v131,v131
v_pk_fma_f16 v121,v120,v120,s54
v_lshlrev_b32_sdwa v133,1,v133 dst_sel:DWORD dst_unused:UNUSED_PAD src0_sel:DWORD src1_sel:WORD_1
v_mfma_f32_16x16x32_bf16 v[212:215],v[88:91],v[152:155],v[212:215]
ds_read_b64_tr_b16 v[88:89],v6 offset:45152
v_pk_mul_f16 v121,v120,v121
v_lshlrev_b32_sdwa v135,1,v135 dst_sel:DWORD dst_unused:UNUSED_PAD src0_sel:DWORD src1_sel:WORD_1
v_mfma_f32_16x16x32_bf16 v[220:223],v[92:95],v[152:155],v[220:223]
ds_read_u16 v132,v132
v_exp_f16 v120,v121
v_mfma_f32_16x16x32_bf16 v[228:231],v[96:99],v[152:155],v[228:231]
ds_read_b64_tr_b16 v[90:91],v6 offset:46688
ds_read_b64_tr_b16 v[92:93],v6 offset:45160
v_lshlrev_b32_sdwa v136,1,v136 dst_sel:DWORD dst_unused:UNUSED_PAD src0_sel:DWORD src1_sel:WORD_1
v_mfma_f32_16x16x32_bf16 v[236:239],v[100:103],v[152:155],v[236:239]
ds_read_u16_d16_hi v134,v134
v_exp_f16_sdwa v120,v121 dst_sel:WORD_1 dst_unused:UNUSED_PRESERVE src0_sel:WORD_1
v_mfma_f32_16x16x32_bf16 v[244:247],v[104:107],v[152:155],v[244:247]
ds_read_u16 v133,v133
v_lshlrev_b32_sdwa v138,1,v138 dst_sel:DWORD dst_unused:UNUSED_PAD src0_sel:DWORD src1_sel:WORD_1
v_mfma_f32_16x16x32_bf16 v[252:255],v[108:111],v[152:155],v[252:255]
ds_read_b64_tr_b16 v[94:95],v6 offset:46696
ds_read_b64_tr_b16 v[96:97],v6 offset:45216
v_pk_fma_f16 v120,v120,s55,s55
v_lshlrev_b32_sdwa v137,1,v137 dst_sel:DWORD dst_unused:UNUSED_PAD src0_sel:DWORD src1_sel:WORD_1
v_mfma_f32_16x16x32_bf16 v[144:147],v[64:67],v[40:43],v[112:115]
ds_read_u16_d16_hi v135,v135
v_rcp_f16 v121,v120
v_mfma_f32_16x16x32_bf16 v[152:155],v[64:67],v[44:47],v[112:115]
ds_read_b64_tr_b16 v[98:99],v6 offset:46752
ds_read_b64_tr_b16 v[100:101],v6 offset:45224
v_lshlrev_b32_sdwa v139,1,v139 dst_sel:DWORD dst_unused:UNUSED_PAD src0_sel:DWORD src1_sel:WORD_1
v_mfma_f32_16x16x32_bf16 v[156:159],v[68:71],v[44:47],v[116:119]
ds_read_u16 v136,v136
v_rcp_f16_sdwa v121,v120 dst_sel:WORD_1 dst_unused:UNUSED_PRESERVE src0_sel:WORD_1
v_mfma_f32_16x16x32_bf16 v[148:151],v[68:71],v[40:43],v[116:119]
ds_read_u16_d16_hi v138,v138
v_lshlrev_b32_sdwa v140,1,v140 dst_sel:DWORD dst_unused:UNUSED_PAD src0_sel:DWORD src1_sel:WORD_1
v_mfma_f32_16x16x32_bf16 v[144:147],v[72:75],v[48:51],v[144:147]
ds_read_b64_tr_b16 v[102:103],v6 offset:46760
ds_read_b64_tr_b16 v[104:105],v6 offset:45280
v_fma_mix_f32 v141,v141,v121,0 op_sel_hi:[0,1,0]
v_lshlrev_b32_sdwa v142,1,v142 dst_sel:DWORD dst_unused:UNUSED_PAD src0_sel:DWORD src1_sel:WORD_1
v_mfma_f32_16x16x32_bf16 v[152:155],v[72:75],v[52:55],v[152:155]
ds_read_u16 v137,v137
v_fma_mix_f32 v143,v143,v121,0 op_sel:[0,1,0] op_sel_hi:[0,1,0]
v_mfma_f32_16x16x32_bf16 v[156:159],v[76:79],v[52:55],v[156:159]
ds_read_b64_tr_b16 v[106:107],v6 offset:46816
ds_read_b64_tr_b16 v[108:109],v6 offset:45288
v_mfma_f32_16x16x32_bf16 v[148:151],v[76:79],v[48:51],v[148:151]
ds_read_u16_d16_hi v139,v139
v_mfma_f32_16x16x32_bf16 v[144:147],v[80:83],v[56:59],v[144:147]
ds_read_b64_tr_b16 v[110:111],v6 offset:46824
ds_read_b64_tr_b16 v[64:65],v8 offset:12320
v_mfma_f32_16x16x32_bf16 v[152:155],v[80:83],v[60:63],v[152:155]
s_waitcnt lgkmcnt(12)
ds_read_u16 v140,v140
v_lshl_add_u64 v[128:129],v[128:129],0,v[130:131]
v_lshl_add_u64 v[130:131],v[132:133],0,v[134:135]
v_mfma_f32_16x16x32_bf16 v[156:159],v[84:87],v[60:63],v[156:159]
ds_read_u16_d16_hi v142,v142
v_mfma_f32_16x16x32_bf16 v[148:151],v[84:87],v[56:59],v[148:151]
ds_read_b64_tr_b16 v[66:67],v8 offset:14368
ds_read_b128 v[112:115],v7 offset:0
v_lshlrev_b32_sdwa v144,1,v144 dst_sel:DWORD dst_unused:UNUSED_PAD src0_sel:DWORD src1_sel:WORD_1
v_lshlrev_b32_sdwa v146,1,v146 dst_sel:DWORD dst_unused:UNUSED_PAD src0_sel:DWORD src1_sel:WORD_1
v_mfma_f32_16x16x32_bf16 v[160:163],v[88:91],v[128:131],v[160:163]
ds_read_u16 v144,v144
v_lshlrev_b32_sdwa v145,1,v145 dst_sel:DWORD dst_unused:UNUSED_PAD src0_sel:DWORD src1_sel:WORD_1
v_lshlrev_b32_sdwa v147,1,v147 dst_sel:DWORD dst_unused:UNUSED_PAD src0_sel:DWORD src1_sel:WORD_1
v_mfma_f32_16x16x32_bf16 v[168:171],v[92:95],v[128:131],v[168:171]
s_waitcnt lgkmcnt(15)
ds_read_b64_tr_b16 v[68:69],v9 offset:12320
ds_read_b64_tr_b16 v[70:71],v9 offset:14368
v_fma_mixlo_f16 v120,v157,s99,0
v_lshlrev_b32_sdwa v148,1,v148 dst_sel:DWORD dst_unused:UNUSED_PAD src0_sel:DWORD src1_sel:WORD_1
v_mfma_f32_16x16x32_bf16 v[176:179],v[96:99],v[128:131],v[176:179]
s_waitcnt lgkmcnt(14)
ds_read_u16_d16_hi v146,v146
v_fma_mixhi_f16 v120,v159,s99,0
v_lshlrev_b32_sdwa v150,1,v150 dst_sel:DWORD dst_unused:UNUSED_PAD src0_sel:DWORD src1_sel:WORD_1
v_mfma_f32_16x16x32_bf16 v[184:187],v[100:103],v[128:131],v[184:187]
s_waitcnt lgkmcnt(10)
ds_read_u16 v145,v145
v_lshl_add_u64 v[136:137],v[136:137],0,v[138:139]
v_mfma_f32_16x16x32_bf16 v[192:195],v[104:107],v[128:131],v[192:195]
s_waitcnt lgkmcnt(7)
ds_read_b128 v[116:119],v7 offset:32
ds_read_b64_tr_b16 v[72:73],v8 offset:16416
v_or_b32 v138,v142,v140
v_cvt_pk_bf16_f32 v139,v141,v143
v_mfma_f32_16x16x32_bf16 v[200:203],v[108:111],v[128:131],v[200:203]
ds_read_u16_d16_hi v147,v147
v_pk_fma_f16 v121,v120,v120,s54
v_lshlrev_b32_sdwa v149,1,v149 dst_sel:DWORD dst_unused:UNUSED_PAD src0_sel:DWORD src1_sel:WORD_1
v_mfma_f32_16x16x32_bf16 v[164:167],v[88:91],v[136:139],v[164:167]
ds_read_b64_tr_b16 v[74:75],v8 offset:18464
ds_read_b64_tr_b16 v[76:77],v9 offset:16416
v_pk_mul_f16 v121,v120,v121
v_lshlrev_b32_sdwa v151,1,v151 dst_sel:DWORD dst_unused:UNUSED_PAD src0_sel:DWORD src1_sel:WORD_1
v_mfma_f32_16x16x32_bf16 v[172:175],v[92:95],v[136:139],v[172:175]
ds_read_u16 v148,v148
v_exp_f16 v120,v121
v_mfma_f32_16x16x32_bf16 v[180:183],v[96:99],v[136:139],v[180:183]
ds_read_b64_tr_b16 v[78:79],v9 offset:18464
ds_read_b64_tr_b16 v[80:81],v8 offset:20512
v_lshlrev_b32_sdwa v152,1,v152 dst_sel:DWORD dst_unused:UNUSED_PAD src0_sel:DWORD src1_sel:WORD_1
v_mfma_f32_16x16x32_bf16 v[188:191],v[100:103],v[136:139],v[188:191]
ds_read_u16_d16_hi v150,v150
v_exp_f16_sdwa v120,v121 dst_sel:WORD_1 dst_unused:UNUSED_PRESERVE src0_sel:WORD_1
v_mfma_f32_16x16x32_bf16 v[196:199],v[104:107],v[136:139],v[196:199]
ds_read_u16 v149,v149
v_lshlrev_b32_sdwa v154,1,v154 dst_sel:DWORD dst_unused:UNUSED_PAD src0_sel:DWORD src1_sel:WORD_1
v_mfma_f32_16x16x32_bf16 v[204:207],v[108:111],v[136:139],v[204:207]
s_waitcnt lgkmcnt(15)
s_nop 0
s_mov_b32 s86,0xbe801d5a
ds_read_b64_tr_b16 v[82:83],v8 offset:22560
v_pk_fma_f16 v120,v120,s55,s55
v_lshlrev_b32_sdwa v153,1,v153 dst_sel:DWORD dst_unused:UNUSED_PAD src0_sel:DWORD src1_sel:WORD_1
v_mfma_f32_16x16x32_bf16 v[128:131],v[64:67],v[16:19],v[112:115]
ds_read_u16_d16_hi v151,v151
v_rcp_f16 v121,v120
v_mfma_f32_16x16x32_bf16 v[136:139],v[64:67],v[20:23],v[112:115]
s_waitcnt lgkmcnt(11)
ds_read_b64_tr_b16 v[84:85],v9 offset:20512
v_lshlrev_b32_sdwa v155,1,v155 dst_sel:DWORD dst_unused:UNUSED_PAD src0_sel:DWORD src1_sel:WORD_1
v_mfma_f32_16x16x32_bf16 v[140:143],v[68:71],v[20:23],v[116:119]
ds_read_u16 v152,v152
v_rcp_f16_sdwa v121,v120 dst_sel:WORD_1 dst_unused:UNUSED_PRESERVE src0_sel:WORD_1
v_mfma_f32_16x16x32_bf16 v[132:135],v[68:71],v[16:19],v[116:119]
s_waitcnt lgkmcnt(10)
ds_read_u16_d16_hi v154,v154
v_lshlrev_b32_sdwa v156,1,v156 dst_sel:DWORD dst_unused:UNUSED_PAD src0_sel:DWORD src1_sel:WORD_1
v_mfma_f32_16x16x32_bf16 v[128:131],v[72:75],v[24:27],v[128:131]
ds_read_b64_tr_b16 v[86:87],v9 offset:22560
v_fma_mix_f32 v157,v157,v121,0 op_sel_hi:[0,1,0]
v_lshlrev_b32_sdwa v158,1,v158 dst_sel:DWORD dst_unused:UNUSED_PAD src0_sel:DWORD src1_sel:WORD_1
v_mfma_f32_16x16x32_bf16 v[136:139],v[72:75],v[28:31],v[136:139]
s_waitcnt lgkmcnt(9)
ds_read_u16 v153,v153
v_fma_mix_f32 v159,v159,v121,0 op_sel:[0,1,0] op_sel_hi:[0,1,0]
v_mfma_f32_16x16x32_bf16 v[140:143],v[76:79],v[28:31],v[140:143]
v_mfma_f32_16x16x32_bf16 v[132:135],v[76:79],v[24:27],v[132:135]
s_waitcnt lgkmcnt(6)
ds_read_u16_d16_hi v155,v155
v_mfma_f32_16x16x32_bf16 v[128:131],v[80:83],v[32:35],v[128:131]
v_mfma_f32_16x16x32_bf16 v[136:139],v[80:83],v[36:39],v[136:139]
s_waitcnt lgkmcnt(2)
ds_read_u16 v156,v156
v_lshl_add_u64 v[144:145],v[144:145],0,v[146:147]
v_lshl_add_u64 v[146:147],v[148:149],0,v[150:151]
v_mfma_f32_16x16x32_bf16 v[140:143],v[84:87],v[36:39],v[140:143]
ds_read_u16_d16_hi v158,v158
v_mfma_f32_16x16x32_bf16 v[132:135],v[84:87],v[32:35],v[132:135]
v_lshlrev_b32_sdwa v128,1,v128 dst_sel:DWORD dst_unused:UNUSED_PAD src0_sel:DWORD src1_sel:WORD_1
v_lshlrev_b32_sdwa v130,1,v130 dst_sel:DWORD dst_unused:UNUSED_PAD src0_sel:DWORD src1_sel:WORD_1
v_mfma_f32_16x16x32_bf16 v[208:211],v[88:91],v[144:147],v[208:211]
ds_read_u16 v128,v128
v_lshlrev_b32_sdwa v129,1,v129 dst_sel:DWORD dst_unused:UNUSED_PAD src0_sel:DWORD src1_sel:WORD_1
v_lshlrev_b32_sdwa v131,1,v131 dst_sel:DWORD dst_unused:UNUSED_PAD src0_sel:DWORD src1_sel:WORD_1
v_mfma_f32_16x16x32_bf16 v[216:219],v[92:95],v[144:147],v[216:219]
v_fma_mixlo_f16 v120,v141,s99,0
v_lshlrev_b32_sdwa v132,1,v132 dst_sel:DWORD dst_unused:UNUSED_PAD src0_sel:DWORD src1_sel:WORD_1
v_mfma_f32_16x16x32_bf16 v[224:227],v[96:99],v[144:147],v[224:227]
ds_read_u16_d16_hi v130,v130
v_fma_mixhi_f16 v120,v143,s99,0
v_lshlrev_b32_sdwa v134,1,v134 dst_sel:DWORD dst_unused:UNUSED_PAD src0_sel:DWORD src1_sel:WORD_1
v_mfma_f32_16x16x32_bf16 v[232:235],v[100:103],v[144:147],v[232:235]
s_waitcnt lgkmcnt(4)
ds_read_u16 v129,v129
v_lshl_add_u64 v[152:153],v[152:153],0,v[154:155]
v_mfma_f32_16x16x32_bf16 v[240:243],v[104:107],v[144:147],v[240:243]
s_waitcnt lgkmcnt(3)
v_or_b32 v154,v158,v156
v_cvt_pk_bf16_f32 v155,v157,v159
v_mfma_f32_16x16x32_bf16 v[248:251],v[108:111],v[144:147],v[248:251]
ds_read_u16_d16_hi v131,v131
v_pk_fma_f16 v121,v120,v120,s54
v_lshlrev_b32_sdwa v133,1,v133 dst_sel:DWORD dst_unused:UNUSED_PAD src0_sel:DWORD src1_sel:WORD_1
v_mfma_f32_16x16x32_bf16 v[212:215],v[88:91],v[152:155],v[212:215]
ds_read_b64_tr_b16 v[88:89],v6 offset:51312
v_pk_mul_f16 v121,v120,v121
v_lshlrev_b32_sdwa v135,1,v135 dst_sel:DWORD dst_unused:UNUSED_PAD src0_sel:DWORD src1_sel:WORD_1
v_mfma_f32_16x16x32_bf16 v[220:223],v[92:95],v[152:155],v[220:223]
ds_read_u16 v132,v132
v_exp_f16 v120,v121
v_mfma_f32_16x16x32_bf16 v[228:231],v[96:99],v[152:155],v[228:231]
ds_read_b64_tr_b16 v[90:91],v6 offset:52848
ds_read_b64_tr_b16 v[92:93],v6 offset:51320
v_lshlrev_b32_sdwa v136,1,v136 dst_sel:DWORD dst_unused:UNUSED_PAD src0_sel:DWORD src1_sel:WORD_1
v_mfma_f32_16x16x32_bf16 v[236:239],v[100:103],v[152:155],v[236:239]
ds_read_u16_d16_hi v134,v134
v_exp_f16_sdwa v120,v121 dst_sel:WORD_1 dst_unused:UNUSED_PRESERVE src0_sel:WORD_1
v_mfma_f32_16x16x32_bf16 v[244:247],v[104:107],v[152:155],v[244:247]
ds_read_u16 v133,v133
v_lshlrev_b32_sdwa v138,1,v138 dst_sel:DWORD dst_unused:UNUSED_PAD src0_sel:DWORD src1_sel:WORD_1
v_mfma_f32_16x16x32_bf16 v[252:255],v[108:111],v[152:155],v[252:255]
ds_read_b64_tr_b16 v[94:95],v6 offset:52856
ds_read_b64_tr_b16 v[96:97],v6 offset:51376
v_pk_fma_f16 v120,v120,s55,s55
v_lshlrev_b32_sdwa v137,1,v137 dst_sel:DWORD dst_unused:UNUSED_PAD src0_sel:DWORD src1_sel:WORD_1
v_mfma_f32_16x16x32_bf16 v[144:147],v[64:67],v[40:43],v[112:115]
ds_read_u16_d16_hi v135,v135
v_rcp_f16 v121,v120
v_mfma_f32_16x16x32_bf16 v[152:155],v[64:67],v[44:47],v[112:115]
ds_read_b64_tr_b16 v[98:99],v6 offset:52912
ds_read_b64_tr_b16 v[100:101],v6 offset:51384
v_lshlrev_b32_sdwa v139,1,v139 dst_sel:DWORD dst_unused:UNUSED_PAD src0_sel:DWORD src1_sel:WORD_1
v_mfma_f32_16x16x32_bf16 v[156:159],v[68:71],v[44:47],v[116:119]
ds_read_u16 v136,v136
v_rcp_f16_sdwa v121,v120 dst_sel:WORD_1 dst_unused:UNUSED_PRESERVE src0_sel:WORD_1
v_mfma_f32_16x16x32_bf16 v[148:151],v[68:71],v[40:43],v[116:119]
ds_read_u16_d16_hi v138,v138
v_lshlrev_b32_sdwa v140,1,v140 dst_sel:DWORD dst_unused:UNUSED_PAD src0_sel:DWORD src1_sel:WORD_1
v_mfma_f32_16x16x32_bf16 v[144:147],v[72:75],v[48:51],v[144:147]
ds_read_b64_tr_b16 v[102:103],v6 offset:52920
ds_read_b64_tr_b16 v[104:105],v6 offset:51440
v_fma_mix_f32 v141,v141,v121,0 op_sel_hi:[0,1,0]
v_lshlrev_b32_sdwa v142,1,v142 dst_sel:DWORD dst_unused:UNUSED_PAD src0_sel:DWORD src1_sel:WORD_1
v_mfma_f32_16x16x32_bf16 v[152:155],v[72:75],v[52:55],v[152:155]
ds_read_u16 v137,v137
v_fma_mix_f32 v143,v143,v121,0 op_sel:[0,1,0] op_sel_hi:[0,1,0]
v_mfma_f32_16x16x32_bf16 v[156:159],v[76:79],v[52:55],v[156:159]
ds_read_b64_tr_b16 v[106:107],v6 offset:52976
ds_read_b64_tr_b16 v[108:109],v6 offset:51448
v_mfma_f32_16x16x32_bf16 v[148:151],v[76:79],v[48:51],v[148:151]
ds_read_u16_d16_hi v139,v139
v_mfma_f32_16x16x32_bf16 v[144:147],v[80:83],v[56:59],v[144:147]
ds_read_b64_tr_b16 v[110:111],v6 offset:52984
ds_read_b64_tr_b16 v[64:65],v8 offset:12832
v_mfma_f32_16x16x32_bf16 v[152:155],v[80:83],v[60:63],v[152:155]
s_waitcnt lgkmcnt(12)
ds_read_u16 v140,v140
v_lshl_add_u64 v[128:129],v[128:129],0,v[130:131]
v_lshl_add_u64 v[130:131],v[132:133],0,v[134:135]
v_mfma_f32_16x16x32_bf16 v[156:159],v[84:87],v[60:63],v[156:159]
ds_read_u16_d16_hi v142,v142
v_mfma_f32_16x16x32_bf16 v[148:151],v[84:87],v[56:59],v[148:151]
ds_read_b64_tr_b16 v[66:67],v8 offset:14880
ds_read_b128 v[112:115],v7 offset:128
v_lshlrev_b32_sdwa v144,1,v144 dst_sel:DWORD dst_unused:UNUSED_PAD src0_sel:DWORD src1_sel:WORD_1
v_lshlrev_b32_sdwa v146,1,v146 dst_sel:DWORD dst_unused:UNUSED_PAD src0_sel:DWORD src1_sel:WORD_1
v_mfma_f32_16x16x32_bf16 v[160:163],v[88:91],v[128:131],v[160:163]
ds_read_u16 v144,v144
v_lshlrev_b32_sdwa v145,1,v145 dst_sel:DWORD dst_unused:UNUSED_PAD src0_sel:DWORD src1_sel:WORD_1
v_lshlrev_b32_sdwa v147,1,v147 dst_sel:DWORD dst_unused:UNUSED_PAD src0_sel:DWORD src1_sel:WORD_1
v_mfma_f32_16x16x32_bf16 v[168:171],v[92:95],v[128:131],v[168:171]
s_waitcnt lgkmcnt(15)
ds_read_b64_tr_b16 v[68:69],v9 offset:12832
ds_read_b64_tr_b16 v[70:71],v9 offset:14880
v_fma_mixlo_f16 v120,v157,s99,0
v_lshlrev_b32_sdwa v148,1,v148 dst_sel:DWORD dst_unused:UNUSED_PAD src0_sel:DWORD src1_sel:WORD_1
v_mfma_f32_16x16x32_bf16 v[176:179],v[96:99],v[128:131],v[176:179]
s_waitcnt lgkmcnt(14)
ds_read_u16_d16_hi v146,v146
v_fma_mixhi_f16 v120,v159,s99,0
v_lshlrev_b32_sdwa v150,1,v150 dst_sel:DWORD dst_unused:UNUSED_PAD src0_sel:DWORD src1_sel:WORD_1
v_mfma_f32_16x16x32_bf16 v[184:187],v[100:103],v[128:131],v[184:187]
s_waitcnt lgkmcnt(10)
ds_read_u16 v145,v145
v_lshl_add_u64 v[136:137],v[136:137],0,v[138:139]
v_mfma_f32_16x16x32_bf16 v[192:195],v[104:107],v[128:131],v[192:195]
s_waitcnt lgkmcnt(7)
ds_read_b128 v[116:119],v7 offset:160
ds_read_b64_tr_b16 v[72:73],v8 offset:16928
v_or_b32 v138,v142,v140
v_cvt_pk_bf16_f32 v139,v141,v143
v_mfma_f32_16x16x32_bf16 v[200:203],v[108:111],v[128:131],v[200:203]
ds_read_u16_d16_hi v147,v147
v_pk_fma_f16 v121,v120,v120,s54
v_lshlrev_b32_sdwa v149,1,v149 dst_sel:DWORD dst_unused:UNUSED_PAD src0_sel:DWORD src1_sel:WORD_1
v_mfma_f32_16x16x32_bf16 v[164:167],v[88:91],v[136:139],v[164:167]
ds_read_b64_tr_b16 v[74:75],v8 offset:18976
ds_read_b64_tr_b16 v[76:77],v9 offset:16928
v_pk_mul_f16 v121,v120,v121
v_lshlrev_b32_sdwa v151,1,v151 dst_sel:DWORD dst_unused:UNUSED_PAD src0_sel:DWORD src1_sel:WORD_1
v_mfma_f32_16x16x32_bf16 v[172:175],v[92:95],v[136:139],v[172:175]
ds_read_u16 v148,v148
v_exp_f16 v120,v121
v_mfma_f32_16x16x32_bf16 v[180:183],v[96:99],v[136:139],v[180:183]
ds_read_b64_tr_b16 v[78:79],v9 offset:18976
ds_read_b64_tr_b16 v[80:81],v8 offset:21024
v_lshlrev_b32_sdwa v152,1,v152 dst_sel:DWORD dst_unused:UNUSED_PAD src0_sel:DWORD src1_sel:WORD_1
v_mfma_f32_16x16x32_bf16 v[188:191],v[100:103],v[136:139],v[188:191]
ds_read_u16_d16_hi v150,v150
v_exp_f16_sdwa v120,v121 dst_sel:WORD_1 dst_unused:UNUSED_PRESERVE src0_sel:WORD_1
v_mfma_f32_16x16x32_bf16 v[196:199],v[104:107],v[136:139],v[196:199]
ds_read_u16 v149,v149
v_lshlrev_b32_sdwa v154,1,v154 dst_sel:DWORD dst_unused:UNUSED_PAD src0_sel:DWORD src1_sel:WORD_1
v_mfma_f32_16x16x32_bf16 v[204:207],v[108:111],v[136:139],v[204:207]
s_waitcnt lgkmcnt(15)
ds_read_b64_tr_b16 v[82:83],v8 offset:23072
v_pk_fma_f16 v120,v120,s55,s55
v_lshlrev_b32_sdwa v153,1,v153 dst_sel:DWORD dst_unused:UNUSED_PAD src0_sel:DWORD src1_sel:WORD_1
v_mfma_f32_16x16x32_bf16 v[128:131],v[64:67],v[16:19],v[112:115]
ds_read_u16_d16_hi v151,v151
v_rcp_f16 v121,v120
v_mfma_f32_16x16x32_bf16 v[136:139],v[64:67],v[20:23],v[112:115]
s_waitcnt lgkmcnt(11)
ds_read_b64_tr_b16 v[84:85],v9 offset:21024
v_lshlrev_b32_sdwa v155,1,v155 dst_sel:DWORD dst_unused:UNUSED_PAD src0_sel:DWORD src1_sel:WORD_1
v_mfma_f32_16x16x32_bf16 v[140:143],v[68:71],v[20:23],v[116:119]
ds_read_u16 v152,v152
v_rcp_f16_sdwa v121,v120 dst_sel:WORD_1 dst_unused:UNUSED_PRESERVE src0_sel:WORD_1
v_mfma_f32_16x16x32_bf16 v[132:135],v[68:71],v[16:19],v[116:119]
s_waitcnt lgkmcnt(10)
ds_read_u16_d16_hi v154,v154
v_lshlrev_b32_sdwa v156,1,v156 dst_sel:DWORD dst_unused:UNUSED_PAD src0_sel:DWORD src1_sel:WORD_1
v_mfma_f32_16x16x32_bf16 v[128:131],v[72:75],v[24:27],v[128:131]
ds_read_b64_tr_b16 v[86:87],v9 offset:23072
v_fma_mix_f32 v157,v157,v121,0 op_sel_hi:[0,1,0]
v_lshlrev_b32_sdwa v158,1,v158 dst_sel:DWORD dst_unused:UNUSED_PAD src0_sel:DWORD src1_sel:WORD_1
v_mfma_f32_16x16x32_bf16 v[136:139],v[72:75],v[28:31],v[136:139]
s_waitcnt lgkmcnt(9)
ds_read_u16 v153,v153
v_fma_mix_f32 v159,v159,v121,0 op_sel:[0,1,0] op_sel_hi:[0,1,0]
v_mfma_f32_16x16x32_bf16 v[140:143],v[76:79],v[28:31],v[140:143]
v_mfma_f32_16x16x32_bf16 v[132:135],v[76:79],v[24:27],v[132:135]
s_waitcnt lgkmcnt(6)
ds_read_u16_d16_hi v155,v155
v_mfma_f32_16x16x32_bf16 v[128:131],v[80:83],v[32:35],v[128:131]
v_mfma_f32_16x16x32_bf16 v[136:139],v[80:83],v[36:39],v[136:139]
s_waitcnt lgkmcnt(2)
ds_read_u16 v156,v156
v_lshl_add_u64 v[144:145],v[144:145],0,v[146:147]
v_lshl_add_u64 v[146:147],v[148:149],0,v[150:151]
v_mfma_f32_16x16x32_bf16 v[140:143],v[84:87],v[36:39],v[140:143]
ds_read_u16_d16_hi v158,v158
v_mfma_f32_16x16x32_bf16 v[132:135],v[84:87],v[32:35],v[132:135]
v_lshlrev_b32_sdwa v128,1,v128 dst_sel:DWORD dst_unused:UNUSED_PAD src0_sel:DWORD src1_sel:WORD_1
v_lshlrev_b32_sdwa v130,1,v130 dst_sel:DWORD dst_unused:UNUSED_PAD src0_sel:DWORD src1_sel:WORD_1
v_mfma_f32_16x16x32_bf16 v[208:211],v[88:91],v[144:147],v[208:211]
ds_read_u16 v128,v128
v_lshlrev_b32_sdwa v129,1,v129 dst_sel:DWORD dst_unused:UNUSED_PAD src0_sel:DWORD src1_sel:WORD_1
v_lshlrev_b32_sdwa v131,1,v131 dst_sel:DWORD dst_unused:UNUSED_PAD src0_sel:DWORD src1_sel:WORD_1
v_mfma_f32_16x16x32_bf16 v[216:219],v[92:95],v[144:147],v[216:219]
v_fma_mixlo_f16 v120,v141,s99,0
v_lshlrev_b32_sdwa v132,1,v132 dst_sel:DWORD dst_unused:UNUSED_PAD src0_sel:DWORD src1_sel:WORD_1
v_mfma_f32_16x16x32_bf16 v[224:227],v[96:99],v[144:147],v[224:227]
ds_read_u16_d16_hi v130,v130
v_fma_mixhi_f16 v120,v143,s99,0
v_lshlrev_b32_sdwa v134,1,v134 dst_sel:DWORD dst_unused:UNUSED_PAD src0_sel:DWORD src1_sel:WORD_1
v_mfma_f32_16x16x32_bf16 v[232:235],v[100:103],v[144:147],v[232:235]
s_waitcnt lgkmcnt(4)
ds_read_u16 v129,v129
v_lshl_add_u64 v[152:153],v[152:153],0,v[154:155]
v_mfma_f32_16x16x32_bf16 v[240:243],v[104:107],v[144:147],v[240:243]
s_waitcnt lgkmcnt(3)
v_or_b32 v154,v158,v156
v_cvt_pk_bf16_f32 v155,v157,v159
v_mfma_f32_16x16x32_bf16 v[248:251],v[108:111],v[144:147],v[248:251]
ds_read_u16_d16_hi v131,v131
v_pk_fma_f16 v121,v120,v120,s54
v_lshlrev_b32_sdwa v133,1,v133 dst_sel:DWORD dst_unused:UNUSED_PAD src0_sel:DWORD src1_sel:WORD_1
v_mfma_f32_16x16x32_bf16 v[212:215],v[88:91],v[152:155],v[212:215]
ds_read_b64_tr_b16 v[88:89],v6 offset:57472
v_pk_mul_f16 v121,v120,v121
v_lshlrev_b32_sdwa v135,1,v135 dst_sel:DWORD dst_unused:UNUSED_PAD src0_sel:DWORD src1_sel:WORD_1
v_mfma_f32_16x16x32_bf16 v[220:223],v[92:95],v[152:155],v[220:223]
ds_read_u16 v132,v132
v_exp_f16 v120,v121
v_mfma_f32_16x16x32_bf16 v[228:231],v[96:99],v[152:155],v[228:231]
ds_read_b64_tr_b16 v[90:91],v6 offset:59008
ds_read_b64_tr_b16 v[92:93],v6 offset:57480
v_lshlrev_b32_sdwa v136,1,v136 dst_sel:DWORD dst_unused:UNUSED_PAD src0_sel:DWORD src1_sel:WORD_1
v_mfma_f32_16x16x32_bf16 v[236:239],v[100:103],v[152:155],v[236:239]
ds_read_u16_d16_hi v134,v134
v_exp_f16_sdwa v120,v121 dst_sel:WORD_1 dst_unused:UNUSED_PRESERVE src0_sel:WORD_1
v_mfma_f32_16x16x32_bf16 v[244:247],v[104:107],v[152:155],v[244:247]
ds_read_u16 v133,v133
v_lshlrev_b32_sdwa v138,1,v138 dst_sel:DWORD dst_unused:UNUSED_PAD src0_sel:DWORD src1_sel:WORD_1
v_mfma_f32_16x16x32_bf16 v[252:255],v[108:111],v[152:155],v[252:255]
ds_read_b64_tr_b16 v[94:95],v6 offset:59016
ds_read_b64_tr_b16 v[96:97],v6 offset:57536
v_pk_fma_f16 v120,v120,s55,s55
v_lshlrev_b32_sdwa v137,1,v137 dst_sel:DWORD dst_unused:UNUSED_PAD src0_sel:DWORD src1_sel:WORD_1
v_mfma_f32_16x16x32_bf16 v[144:147],v[64:67],v[40:43],v[112:115]
ds_read_u16_d16_hi v135,v135
v_rcp_f16 v121,v120
v_mfma_f32_16x16x32_bf16 v[152:155],v[64:67],v[44:47],v[112:115]
ds_read_b64_tr_b16 v[98:99],v6 offset:59072
ds_read_b64_tr_b16 v[100:101],v6 offset:57544
v_lshlrev_b32_sdwa v139,1,v139 dst_sel:DWORD dst_unused:UNUSED_PAD src0_sel:DWORD src1_sel:WORD_1
v_mfma_f32_16x16x32_bf16 v[156:159],v[68:71],v[44:47],v[116:119]
ds_read_u16 v136,v136
v_rcp_f16_sdwa v121,v120 dst_sel:WORD_1 dst_unused:UNUSED_PRESERVE src0_sel:WORD_1
v_mfma_f32_16x16x32_bf16 v[148:151],v[68:71],v[40:43],v[116:119]
ds_read_u16_d16_hi v138,v138
v_lshlrev_b32_sdwa v140,1,v140 dst_sel:DWORD dst_unused:UNUSED_PAD src0_sel:DWORD src1_sel:WORD_1
v_mfma_f32_16x16x32_bf16 v[144:147],v[72:75],v[48:51],v[144:147]
ds_read_b64_tr_b16 v[102:103],v6 offset:59080
ds_read_b64_tr_b16 v[104:105],v6 offset:57600
v_fma_mix_f32 v141,v141,v121,0 op_sel_hi:[0,1,0]
v_lshlrev_b32_sdwa v142,1,v142 dst_sel:DWORD dst_unused:UNUSED_PAD src0_sel:DWORD src1_sel:WORD_1
v_mfma_f32_16x16x32_bf16 v[152:155],v[72:75],v[52:55],v[152:155]
ds_read_u16 v137,v137
v_fma_mix_f32 v143,v143,v121,0 op_sel:[0,1,0] op_sel_hi:[0,1,0]
v_mfma_f32_16x16x32_bf16 v[156:159],v[76:79],v[52:55],v[156:159]
ds_read_b64_tr_b16 v[106:107],v6 offset:59136
ds_read_b64_tr_b16 v[108:109],v6 offset:57608
v_mfma_f32_16x16x32_bf16 v[148:151],v[76:79],v[48:51],v[148:151]
ds_read_u16_d16_hi v139,v139
v_mfma_f32_16x16x32_bf16 v[144:147],v[80:83],v[56:59],v[144:147]
ds_read_b64_tr_b16 v[110:111],v6 offset:59144
ds_read_b64_tr_b16 v[64:65],v8 offset:24640
v_mfma_f32_16x16x32_bf16 v[152:155],v[80:83],v[60:63],v[152:155]
s_waitcnt lgkmcnt(12)
ds_read_u16 v140,v140
v_lshl_add_u64 v[128:129],v[128:129],0,v[130:131]
v_lshl_add_u64 v[130:131],v[132:133],0,v[134:135]
v_mfma_f32_16x16x32_bf16 v[156:159],v[84:87],v[60:63],v[156:159]
ds_read_u16_d16_hi v142,v142
v_mfma_f32_16x16x32_bf16 v[148:151],v[84:87],v[56:59],v[148:151]
ds_read_b64_tr_b16 v[66:67],v8 offset:26688
ds_read_b128 v[112:115],v7 offset:256
v_lshlrev_b32_sdwa v144,1,v144 dst_sel:DWORD dst_unused:UNUSED_PAD src0_sel:DWORD src1_sel:WORD_1
v_lshlrev_b32_sdwa v146,1,v146 dst_sel:DWORD dst_unused:UNUSED_PAD src0_sel:DWORD src1_sel:WORD_1
v_mfma_f32_16x16x32_bf16 v[160:163],v[88:91],v[128:131],v[160:163]
ds_read_u16 v144,v144
v_lshlrev_b32_sdwa v145,1,v145 dst_sel:DWORD dst_unused:UNUSED_PAD src0_sel:DWORD src1_sel:WORD_1
v_lshlrev_b32_sdwa v147,1,v147 dst_sel:DWORD dst_unused:UNUSED_PAD src0_sel:DWORD src1_sel:WORD_1
v_mfma_f32_16x16x32_bf16 v[168:171],v[92:95],v[128:131],v[168:171]
s_waitcnt lgkmcnt(15)
ds_read_b64_tr_b16 v[68:69],v9 offset:24640
ds_read_b64_tr_b16 v[70:71],v9 offset:26688
v_fma_mixlo_f16 v120,v157,s99,0
v_lshlrev_b32_sdwa v148,1,v148 dst_sel:DWORD dst_unused:UNUSED_PAD src0_sel:DWORD src1_sel:WORD_1
v_mfma_f32_16x16x32_bf16 v[176:179],v[96:99],v[128:131],v[176:179]
s_waitcnt lgkmcnt(14)
ds_read_u16_d16_hi v146,v146
v_fma_mixhi_f16 v120,v159,s99,0
v_lshlrev_b32_sdwa v150,1,v150 dst_sel:DWORD dst_unused:UNUSED_PAD src0_sel:DWORD src1_sel:WORD_1
v_mfma_f32_16x16x32_bf16 v[184:187],v[100:103],v[128:131],v[184:187]
s_waitcnt lgkmcnt(10)
ds_read_u16 v145,v145
v_lshl_add_u64 v[136:137],v[136:137],0,v[138:139]
v_mfma_f32_16x16x32_bf16 v[192:195],v[104:107],v[128:131],v[192:195]
s_waitcnt lgkmcnt(7)
ds_read_b128 v[116:119],v7 offset:288
ds_read_b64_tr_b16 v[72:73],v8 offset:28736
v_or_b32 v138,v142,v140
v_cvt_pk_bf16_f32 v139,v141,v143
v_mfma_f32_16x16x32_bf16 v[200:203],v[108:111],v[128:131],v[200:203]
ds_read_u16_d16_hi v147,v147
v_pk_fma_f16 v121,v120,v120,s54
v_lshlrev_b32_sdwa v149,1,v149 dst_sel:DWORD dst_unused:UNUSED_PAD src0_sel:DWORD src1_sel:WORD_1
v_mfma_f32_16x16x32_bf16 v[164:167],v[88:91],v[136:139],v[164:167]
ds_read_b64_tr_b16 v[74:75],v8 offset:30784
ds_read_b64_tr_b16 v[76:77],v9 offset:28736
v_pk_mul_f16 v121,v120,v121
v_lshlrev_b32_sdwa v151,1,v151 dst_sel:DWORD dst_unused:UNUSED_PAD src0_sel:DWORD src1_sel:WORD_1
v_mfma_f32_16x16x32_bf16 v[172:175],v[92:95],v[136:139],v[172:175]
ds_read_u16 v148,v148
v_exp_f16 v120,v121
v_mfma_f32_16x16x32_bf16 v[180:183],v[96:99],v[136:139],v[180:183]
ds_read_b64_tr_b16 v[78:79],v9 offset:30784
ds_read_b64_tr_b16 v[80:81],v8 offset:32832
v_lshlrev_b32_sdwa v152,1,v152 dst_sel:DWORD dst_unused:UNUSED_PAD src0_sel:DWORD src1_sel:WORD_1
v_mfma_f32_16x16x32_bf16 v[188:191],v[100:103],v[136:139],v[188:191]
ds_read_u16_d16_hi v150,v150
v_exp_f16_sdwa v120,v121 dst_sel:WORD_1 dst_unused:UNUSED_PRESERVE src0_sel:WORD_1
v_mfma_f32_16x16x32_bf16 v[196:199],v[104:107],v[136:139],v[196:199]
ds_read_u16 v149,v149
v_lshlrev_b32_sdwa v154,1,v154 dst_sel:DWORD dst_unused:UNUSED_PAD src0_sel:DWORD src1_sel:WORD_1
v_mfma_f32_16x16x32_bf16 v[204:207],v[108:111],v[136:139],v[204:207]
s_waitcnt lgkmcnt(15)
ds_read_b64_tr_b16 v[82:83],v8 offset:34880
v_pk_fma_f16 v120,v120,s55,s55
v_lshlrev_b32_sdwa v153,1,v153 dst_sel:DWORD dst_unused:UNUSED_PAD src0_sel:DWORD src1_sel:WORD_1
v_mfma_f32_16x16x32_bf16 v[128:131],v[64:67],v[16:19],v[112:115]
ds_read_u16_d16_hi v151,v151
v_rcp_f16 v121,v120
v_mfma_f32_16x16x32_bf16 v[136:139],v[64:67],v[20:23],v[112:115]
s_waitcnt lgkmcnt(11)
ds_read_b64_tr_b16 v[84:85],v9 offset:32832
v_lshlrev_b32_sdwa v155,1,v155 dst_sel:DWORD dst_unused:UNUSED_PAD src0_sel:DWORD src1_sel:WORD_1
v_mfma_f32_16x16x32_bf16 v[140:143],v[68:71],v[20:23],v[116:119]
ds_read_u16 v152,v152
v_rcp_f16_sdwa v121,v120 dst_sel:WORD_1 dst_unused:UNUSED_PRESERVE src0_sel:WORD_1
v_mfma_f32_16x16x32_bf16 v[132:135],v[68:71],v[16:19],v[116:119]
s_waitcnt lgkmcnt(10)
ds_read_u16_d16_hi v154,v154
v_lshlrev_b32_sdwa v156,1,v156 dst_sel:DWORD dst_unused:UNUSED_PAD src0_sel:DWORD src1_sel:WORD_1
v_mfma_f32_16x16x32_bf16 v[128:131],v[72:75],v[24:27],v[128:131]
ds_read_b64_tr_b16 v[86:87],v9 offset:34880
v_fma_mix_f32 v157,v157,v121,0 op_sel_hi:[0,1,0]
v_lshlrev_b32_sdwa v158,1,v158 dst_sel:DWORD dst_unused:UNUSED_PAD src0_sel:DWORD src1_sel:WORD_1
v_mfma_f32_16x16x32_bf16 v[136:139],v[72:75],v[28:31],v[136:139]
s_waitcnt lgkmcnt(9)
ds_read_u16 v153,v153
v_fma_mix_f32 v159,v159,v121,0 op_sel:[0,1,0] op_sel_hi:[0,1,0]
v_mfma_f32_16x16x32_bf16 v[140:143],v[76:79],v[28:31],v[140:143]
v_mfma_f32_16x16x32_bf16 v[132:135],v[76:79],v[24:27],v[132:135]
s_waitcnt lgkmcnt(6)
ds_read_u16_d16_hi v155,v155
v_mfma_f32_16x16x32_bf16 v[128:131],v[80:83],v[32:35],v[128:131]
v_mfma_f32_16x16x32_bf16 v[136:139],v[80:83],v[36:39],v[136:139]
s_waitcnt lgkmcnt(2)
ds_read_u16 v156,v156
v_lshl_add_u64 v[144:145],v[144:145],0,v[146:147]
v_lshl_add_u64 v[146:147],v[148:149],0,v[150:151]
v_mfma_f32_16x16x32_bf16 v[140:143],v[84:87],v[36:39],v[140:143]
ds_read_u16_d16_hi v158,v158
v_mfma_f32_16x16x32_bf16 v[132:135],v[84:87],v[32:35],v[132:135]
v_lshlrev_b32_sdwa v128,1,v128 dst_sel:DWORD dst_unused:UNUSED_PAD src0_sel:DWORD src1_sel:WORD_1
v_lshlrev_b32_sdwa v130,1,v130 dst_sel:DWORD dst_unused:UNUSED_PAD src0_sel:DWORD src1_sel:WORD_1
v_mfma_f32_16x16x32_bf16 v[208:211],v[88:91],v[144:147],v[208:211]
ds_read_u16 v128,v128
v_lshlrev_b32_sdwa v129,1,v129 dst_sel:DWORD dst_unused:UNUSED_PAD src0_sel:DWORD src1_sel:WORD_1
v_lshlrev_b32_sdwa v131,1,v131 dst_sel:DWORD dst_unused:UNUSED_PAD src0_sel:DWORD src1_sel:WORD_1
v_mfma_f32_16x16x32_bf16 v[216:219],v[92:95],v[144:147],v[216:219]
v_fma_mixlo_f16 v120,v141,s99,0
v_lshlrev_b32_sdwa v132,1,v132 dst_sel:DWORD dst_unused:UNUSED_PAD src0_sel:DWORD src1_sel:WORD_1
v_mfma_f32_16x16x32_bf16 v[224:227],v[96:99],v[144:147],v[224:227]
ds_read_u16_d16_hi v130,v130
v_fma_mixhi_f16 v120,v143,s99,0
v_lshlrev_b32_sdwa v134,1,v134 dst_sel:DWORD dst_unused:UNUSED_PAD src0_sel:DWORD src1_sel:WORD_1
v_mfma_f32_16x16x32_bf16 v[232:235],v[100:103],v[144:147],v[232:235]
s_waitcnt lgkmcnt(4)
ds_read_u16 v129,v129
v_lshl_add_u64 v[152:153],v[152:153],0,v[154:155]
v_mfma_f32_16x16x32_bf16 v[240:243],v[104:107],v[144:147],v[240:243]
s_waitcnt lgkmcnt(3)
v_or_b32 v154,v158,v156
v_cvt_pk_bf16_f32 v155,v157,v159
v_mfma_f32_16x16x32_bf16 v[248:251],v[108:111],v[144:147],v[248:251]
ds_read_u16_d16_hi v131,v131
v_pk_fma_f16 v121,v120,v120,s54
v_lshlrev_b32_sdwa v133,1,v133 dst_sel:DWORD dst_unused:UNUSED_PAD src0_sel:DWORD src1_sel:WORD_1
v_mfma_f32_16x16x32_bf16 v[212:215],v[88:91],v[152:155],v[212:215]
ds_read_b64_tr_b16 v[88:89],v6 offset:63632
v_pk_mul_f16 v121,v120,v121
v_lshlrev_b32_sdwa v135,1,v135 dst_sel:DWORD dst_unused:UNUSED_PAD src0_sel:DWORD src1_sel:WORD_1
v_mfma_f32_16x16x32_bf16 v[220:223],v[92:95],v[152:155],v[220:223]
ds_read_u16 v132,v132
v_exp_f16 v120,v121
v_mfma_f32_16x16x32_bf16 v[228:231],v[96:99],v[152:155],v[228:231]
ds_read_b64_tr_b16 v[90:91],v6 offset:65168
ds_read_b64_tr_b16 v[92:93],v6 offset:63640
v_lshlrev_b32_sdwa v136,1,v136 dst_sel:DWORD dst_unused:UNUSED_PAD src0_sel:DWORD src1_sel:WORD_1
v_mfma_f32_16x16x32_bf16 v[236:239],v[100:103],v[152:155],v[236:239]
ds_read_u16_d16_hi v134,v134
v_exp_f16_sdwa v120,v121 dst_sel:WORD_1 dst_unused:UNUSED_PRESERVE src0_sel:WORD_1
v_mfma_f32_16x16x32_bf16 v[244:247],v[104:107],v[152:155],v[244:247]
ds_read_u16 v133,v133
v_lshlrev_b32_sdwa v138,1,v138 dst_sel:DWORD dst_unused:UNUSED_PAD src0_sel:DWORD src1_sel:WORD_1
v_mfma_f32_16x16x32_bf16 v[252:255],v[108:111],v[152:155],v[252:255]
ds_read_b64_tr_b16 v[94:95],v6 offset:65176
ds_read_b64_tr_b16 v[96:97],v6 offset:63696
v_pk_fma_f16 v120,v120,s55,s55
v_lshlrev_b32_sdwa v137,1,v137 dst_sel:DWORD dst_unused:UNUSED_PAD src0_sel:DWORD src1_sel:WORD_1
v_mfma_f32_16x16x32_bf16 v[144:147],v[64:67],v[40:43],v[112:115]
ds_read_u16_d16_hi v135,v135
v_rcp_f16 v121,v120
v_mfma_f32_16x16x32_bf16 v[152:155],v[64:67],v[44:47],v[112:115]
ds_read_b64_tr_b16 v[98:99],v6 offset:65232
ds_read_b64_tr_b16 v[100:101],v6 offset:63704
v_lshlrev_b32_sdwa v139,1,v139 dst_sel:DWORD dst_unused:UNUSED_PAD src0_sel:DWORD src1_sel:WORD_1
v_mfma_f32_16x16x32_bf16 v[156:159],v[68:71],v[44:47],v[116:119]
ds_read_u16 v136,v136
v_rcp_f16_sdwa v121,v120 dst_sel:WORD_1 dst_unused:UNUSED_PRESERVE src0_sel:WORD_1
v_mfma_f32_16x16x32_bf16 v[148:151],v[68:71],v[40:43],v[116:119]
ds_read_u16_d16_hi v138,v138
v_lshlrev_b32_sdwa v140,1,v140 dst_sel:DWORD dst_unused:UNUSED_PAD src0_sel:DWORD src1_sel:WORD_1
v_mfma_f32_16x16x32_bf16 v[144:147],v[72:75],v[48:51],v[144:147]
ds_read_b64_tr_b16 v[102:103],v6 offset:65240
ds_read_b64_tr_b16 v[104:105],v6 offset:63760
v_fma_mix_f32 v141,v141,v121,0 op_sel_hi:[0,1,0]
v_lshlrev_b32_sdwa v142,1,v142 dst_sel:DWORD dst_unused:UNUSED_PAD src0_sel:DWORD src1_sel:WORD_1
v_mfma_f32_16x16x32_bf16 v[152:155],v[72:75],v[52:55],v[152:155]
ds_read_u16 v137,v137
v_fma_mix_f32 v143,v143,v121,0 op_sel:[0,1,0] op_sel_hi:[0,1,0]
v_mfma_f32_16x16x32_bf16 v[156:159],v[76:79],v[52:55],v[156:159]
ds_read_b64_tr_b16 v[106:107],v6 offset:65296
ds_read_b64_tr_b16 v[108:109],v6 offset:63768
v_mfma_f32_16x16x32_bf16 v[148:151],v[76:79],v[48:51],v[148:151]
ds_read_u16_d16_hi v139,v139
v_mfma_f32_16x16x32_bf16 v[144:147],v[80:83],v[56:59],v[144:147]
ds_read_b64_tr_b16 v[110:111],v6 offset:65304
ds_read_b128 v[64:67],v7 offset:1536
v_mfma_f32_16x16x32_bf16 v[152:155],v[80:83],v[60:63],v[152:155]
s_waitcnt lgkmcnt(12)
ds_read_u16 v140,v140
v_lshl_add_u64 v[128:129],v[128:129],0,v[130:131]
v_lshl_add_u64 v[130:131],v[132:133],0,v[134:135]
v_mfma_f32_16x16x32_bf16 v[156:159],v[84:87],v[60:63],v[156:159]
ds_read_u16_d16_hi v142,v142
v_mfma_f32_16x16x32_bf16 v[148:151],v[84:87],v[56:59],v[148:151]
ds_read_b128 v[68:71],v7 offset:1568
ds_read_b128 v[72:75],v7 offset:1664
v_lshlrev_b32_sdwa v144,1,v144 dst_sel:DWORD dst_unused:UNUSED_PAD src0_sel:DWORD src1_sel:WORD_1
v_lshlrev_b32_sdwa v146,1,v146 dst_sel:DWORD dst_unused:UNUSED_PAD src0_sel:DWORD src1_sel:WORD_1
v_mfma_f32_16x16x32_bf16 v[160:163],v[88:91],v[128:131],v[160:163]
ds_read_u16 v144,v144
ds_read_b128 v[76:79],v7 offset:1696
v_lshlrev_b32_sdwa v145,1,v145 dst_sel:DWORD dst_unused:UNUSED_PAD src0_sel:DWORD src1_sel:WORD_1
v_lshlrev_b32_sdwa v147,1,v147 dst_sel:DWORD dst_unused:UNUSED_PAD src0_sel:DWORD src1_sel:WORD_1
v_fma_mixlo_f16 v120,v157,s99,0
v_mfma_f32_16x16x32_bf16 v[168:171],v[92:95],v[128:131],v[168:171]
s_waitcnt lgkmcnt(15)
ds_read_u16_d16_hi v146,v146
ds_read_b128 v[80:83],v7 offset:1792
v_lshlrev_b32_sdwa v148,1,v148 dst_sel:DWORD dst_unused:UNUSED_PAD src0_sel:DWORD src1_sel:WORD_1
v_fma_mixhi_f16 v120,v159,s99,0
v_lshlrev_b32_sdwa v150,1,v150 dst_sel:DWORD dst_unused:UNUSED_PAD src0_sel:DWORD src1_sel:WORD_1
v_mfma_f32_16x16x32_bf16 v[176:179],v[96:99],v[128:131],v[176:179]
s_waitcnt lgkmcnt(15)
ds_read_u16 v145,v145
ds_read_b128 v[84:87],v7 offset:1824
s_waitcnt lgkmcnt(12)
v_lshl_add_u64 v[136:137],v[136:137],0,v[138:139]
s_waitcnt lgkmcnt(8)
v_or_b32 v138,v142,v140
v_cvt_pk_bf16_f32 v139,v141,v143
v_mfma_f32_16x16x32_bf16 v[184:187],v[100:103],v[128:131],v[184:187]
ds_read_u16_d16_hi v147,v147
v_pk_fma_f16 v121,v120,v120,s54
v_lshlrev_b32_sdwa v149,1,v149 dst_sel:DWORD dst_unused:UNUSED_PAD src0_sel:DWORD src1_sel:WORD_1
v_pk_mul_f16 v121,v120,v121
v_mfma_f32_16x16x32_bf16 v[192:195],v[104:107],v[128:131],v[192:195]
ds_read_u16 v148,v148
v_lshlrev_b32_sdwa v151,1,v151 dst_sel:DWORD dst_unused:UNUSED_PAD src0_sel:DWORD src1_sel:WORD_1
v_exp_f16 v120,v121
v_mfma_f32_16x16x32_bf16 v[200:203],v[108:111],v[128:131],v[200:203]
ds_read_u16_d16_hi v150,v150
v_lshlrev_b32_sdwa v152,1,v152 dst_sel:DWORD dst_unused:UNUSED_PAD src0_sel:DWORD src1_sel:WORD_1
v_exp_f16_sdwa v120,v121 dst_sel:WORD_1 dst_unused:UNUSED_PRESERVE src0_sel:WORD_1
v_mfma_f32_16x16x32_bf16 v[164:167],v[88:91],v[136:139],v[164:167]
ds_read_u16 v149,v149
v_lshlrev_b32_sdwa v154,1,v154 dst_sel:DWORD dst_unused:UNUSED_PAD src0_sel:DWORD src1_sel:WORD_1
v_pk_fma_f16 v120,v120,s55,s55
v_lshlrev_b32_sdwa v153,1,v153 dst_sel:DWORD dst_unused:UNUSED_PAD src0_sel:DWORD src1_sel:WORD_1
v_mfma_f32_16x16x32_bf16 v[172:175],v[92:95],v[136:139],v[172:175]
ds_read_u16_d16_hi v151,v151
v_rcp_f16 v121,v120
v_mfma_f32_16x16x32_bf16 v[180:183],v[96:99],v[136:139],v[180:183]
ds_read_u16 v152,v152
v_lshlrev_b32_sdwa v155,1,v155 dst_sel:DWORD dst_unused:UNUSED_PAD src0_sel:DWORD src1_sel:WORD_1
v_rcp_f16_sdwa v121,v120 dst_sel:WORD_1 dst_unused:UNUSED_PRESERVE src0_sel:WORD_1
v_mfma_f32_16x16x32_bf16 v[188:191],v[100:103],v[136:139],v[188:191]
ds_read_u16_d16_hi v154,v154
v_lshlrev_b32_sdwa v156,1,v156 dst_sel:DWORD dst_unused:UNUSED_PAD src0_sel:DWORD src1_sel:WORD_1
v_fma_mix_f32 v157,v157,v121,0 op_sel_hi:[0,1,0]
v_lshlrev_b32_sdwa v158,1,v158 dst_sel:DWORD dst_unused:UNUSED_PAD src0_sel:DWORD src1_sel:WORD_1
v_mfma_f32_16x16x32_bf16 v[196:199],v[104:107],v[136:139],v[196:199]
ds_read_u16 v153,v153
v_fma_mix_f32 v159,v159,v121,0 op_sel:[0,1,0] op_sel_hi:[0,1,0]
s_waitcnt lgkmcnt(7)
v_lshl_add_u64 v[144:145],v[144:145],0,v[146:147]
s_waitcnt lgkmcnt(3)
v_lshl_add_u64 v[146:147],v[148:149],0,v[150:151]
v_mfma_f32_16x16x32_bf16 v[204:207],v[108:111],v[136:139],v[204:207]
ds_read_u16_d16_hi v155,v155
s_waitcnt lgkmcnt(0)
v_lshl_add_u64 v[152:153],v[152:153],0,v[154:155]
ds_read_u16 v156,v156
ds_read_u16_d16_hi v158,v158
s_mov_b32 s86,0xbe801d5a
s_waitcnt lgkmcnt(0)
v_or_b32 v154,v158,v156
v_cvt_pk_bf16_f32 v155,v157,v159
v_mfma_f32_16x16x32_bf16 v[208:211],v[88:91],v[144:147],v[208:211]
v_xor_b32 v15,64,v10
v_mul_f32 v160,v160,v64
v_mul_f32 v161,v161,v65
v_mul_f32 v162,v162,v66
v_mul_f32 v163,v163,v67
v_mfma_f32_16x16x32_bf16 v[216:219],v[92:95],v[144:147],v[216:219]
v_mul_f32 v168,v168,v68
v_mul_f32 v169,v169,v69
v_mul_f32 v170,v170,v70
v_mul_f32 v171,v171,v71
v_cvt_pk_bf16_f32 v16,v160,v161
v_mfma_f32_16x16x32_bf16 v[224:227],v[96:99],v[144:147],v[224:227]
v_cvt_pk_bf16_f32 v17,v162,v163
v_cvt_pk_bf16_f32 v18,v168,v169
v_cvt_pk_bf16_f32 v19,v170,v171
v_mul_f32_dpp v176,v176,v72 quad_perm:[1,0,3,2] row_mask:0xf bank_mask:0xf
v_mfma_f32_16x16x32_bf16 v[232:235],v[100:103],v[144:147],v[232:235]
v_mul_f32_dpp v177,v177,v73 quad_perm:[1,0,3,2] row_mask:0xf bank_mask:0xf
v_mul_f32_dpp v178,v178,v74 quad_perm:[1,0,3,2] row_mask:0xf bank_mask:0xf
v_mul_f32_dpp v179,v179,v75 quad_perm:[1,0,3,2] row_mask:0xf bank_mask:0xf
v_mul_f32_dpp v184,v184,v76 quad_perm:[1,0,3,2] row_mask:0xf bank_mask:0xf
v_mul_f32_dpp v185,v185,v77 quad_perm:[1,0,3,2] row_mask:0xf bank_mask:0xf
v_mfma_f32_16x16x32_bf16 v[240:243],v[104:107],v[144:147],v[240:243]
v_mul_f32_dpp v186,v186,v78 quad_perm:[1,0,3,2] row_mask:0xf bank_mask:0xf
v_mul_f32_dpp v187,v187,v79 quad_perm:[1,0,3,2] row_mask:0xf bank_mask:0xf
v_cvt_pk_bf16_f32 v24,v176,v177
v_cvt_pk_bf16_f32 v25,v178,v179
v_mfma_f32_16x16x32_bf16 v[248:251],v[108:111],v[144:147],v[248:251]
v_cvt_pk_bf16_f32 v26,v184,v185
v_cvt_pk_bf16_f32 v27,v186,v187
s_mov_b64 exec,s[12:13]
v_swap_b32 v16,v24
v_swap_b32 v17,v25
v_swap_b32 v18,v26
v_swap_b32 v19,v27
s_mov_b64 exec,-1
s_add_u32 s65,s61,4718592
buffer_store_dwordx4 v[16:19],v10,s[40:43],s65 offen offset:0 sc1
s_add_u32 s65,s61,4718592
buffer_store_dwordx4 v[24:27],v15,s[40:43],s65 offen offset:384 sc1
v_mfma_f32_16x16x32_bf16 v[212:215],v[88:91],v[152:155],v[212:215]
v_mfma_f32_16x16x32_bf16 v[220:223],v[92:95],v[152:155],v[220:223]
v_mul_f32 v192,v192,v80
v_mul_f32 v193,v193,v81
v_mul_f32 v194,v194,v82
v_mul_f32 v195,v195,v83
v_mfma_f32_16x16x32_bf16 v[228:231],v[96:99],v[152:155],v[228:231]
v_mul_f32 v200,v200,v84
v_mul_f32 v201,v201,v85
v_mul_f32 v202,v202,v86
v_mul_f32 v203,v203,v87
v_mfma_f32_16x16x32_bf16 v[236:239],v[100:103],v[152:155],v[236:239]
v_cvt_pk_bf16_f32 v32,v192,v193
v_cvt_pk_bf16_f32 v33,v194,v195
v_cvt_pk_bf16_f32 v34,v200,v201
v_cvt_pk_bf16_f32 v35,v202,v203
v_mul_f32_dpp v164,v164,v64 quad_perm:[1,0,3,2] row_mask:0xf bank_mask:0xf
v_mfma_f32_16x16x32_bf16 v[244:247],v[104:107],v[152:155],v[244:247]
v_mul_f32_dpp v165,v165,v65 quad_perm:[1,0,3,2] row_mask:0xf bank_mask:0xf
v_mul_f32_dpp v166,v166,v66 quad_perm:[1,0,3,2] row_mask:0xf bank_mask:0xf
v_mul_f32_dpp v167,v167,v67 quad_perm:[1,0,3,2] row_mask:0xf bank_mask:0xf
v_mul_f32_dpp v172,v172,v68 quad_perm:[1,0,3,2] row_mask:0xf bank_mask:0xf
v_mfma_f32_16x16x32_bf16 v[252:255],v[108:111],v[152:155],v[252:255]
v_mul_f32_dpp v173,v173,v69 quad_perm:[1,0,3,2] row_mask:0xf bank_mask:0xf
v_mul_f32_dpp v174,v174,v70 quad_perm:[1,0,3,2] row_mask:0xf bank_mask:0xf
v_mul_f32_dpp v175,v175,v71 quad_perm:[1,0,3,2] row_mask:0xf bank_mask:0xf
v_cvt_pk_bf16_f32 v20,v164,v165
v_cvt_pk_bf16_f32 v21,v166,v167
v_cvt_pk_bf16_f32 v22,v172,v173
v_cvt_pk_bf16_f32 v23,v174,v175
s_mov_b64 exec,s[12:13]
v_swap_b32 v32,v20
v_swap_b32 v33,v21
v_swap_b32 v34,v22
v_swap_b32 v35,v23
s_mov_b64 exec,-1
s_add_u32 s65,s61,4718592
buffer_store_dwordx4 v[32:35],v10,s[40:43],s65 offen offset:128 sc1
s_add_u32 s65,s61,4718592
buffer_store_dwordx4 v[20:23],v15,s[40:43],s65 offen offset:512 sc1
v_mul_f32 v180,v180,v72
v_mul_f32 v181,v181,v73
v_mul_f32 v182,v182,v74
v_mul_f32 v183,v183,v75
v_mul_f32 v188,v188,v76
v_mul_f32 v189,v189,v77
v_mul_f32 v190,v190,v78
v_mul_f32 v191,v191,v79
v_cvt_pk_bf16_f32 v28,v180,v181
v_cvt_pk_bf16_f32 v29,v182,v183
v_cvt_pk_bf16_f32 v30,v188,v189
v_cvt_pk_bf16_f32 v31,v190,v191
v_mul_f32_dpp v196,v196,v80 quad_perm:[1,0,3,2] row_mask:0xf bank_mask:0xf
v_mul_f32_dpp v197,v197,v81 quad_perm:[1,0,3,2] row_mask:0xf bank_mask:0xf
v_mul_f32_dpp v198,v198,v82 quad_perm:[1,0,3,2] row_mask:0xf bank_mask:0xf
v_mul_f32_dpp v199,v199,v83 quad_perm:[1,0,3,2] row_mask:0xf bank_mask:0xf
v_mul_f32_dpp v204,v204,v84 quad_perm:[1,0,3,2] row_mask:0xf bank_mask:0xf
v_mul_f32_dpp v205,v205,v85 quad_perm:[1,0,3,2] row_mask:0xf bank_mask:0xf
v_mul_f32_dpp v206,v206,v86 quad_perm:[1,0,3,2] row_mask:0xf bank_mask:0xf
v_mul_f32_dpp v207,v207,v87 quad_perm:[1,0,3,2] row_mask:0xf bank_mask:0xf
v_cvt_pk_bf16_f32 v36,v196,v197
v_cvt_pk_bf16_f32 v37,v198,v199
v_cvt_pk_bf16_f32 v38,v204,v205
v_cvt_pk_bf16_f32 v39,v206,v207
s_mov_b64 exec,s[12:13]
v_swap_b32 v28,v36
v_swap_b32 v29,v37
v_swap_b32 v30,v38
v_swap_b32 v31,v39
s_mov_b64 exec,-1
s_add_u32 s65,s61,4718592
buffer_store_dwordx4 v[28:31],v10,s[40:43],s65 offen offset:256 sc1
s_add_u32 s65,s61,4718592
buffer_store_dwordx4 v[36:39],v15,s[40:43],s65 offen offset:640 sc1
v_mul_f32 v208,v208,v64
v_mul_f32 v209,v209,v65
v_mul_f32 v210,v210,v66
v_mul_f32 v211,v211,v67
v_mul_f32 v216,v216,v68
v_mul_f32 v217,v217,v69
v_mul_f32 v218,v218,v70
v_mul_f32 v219,v219,v71
v_cvt_pk_bf16_f32 v40,v208,v209
v_cvt_pk_bf16_f32 v41,v210,v211
v_cvt_pk_bf16_f32 v42,v216,v217
v_cvt_pk_bf16_f32 v43,v218,v219
v_mul_f32_dpp v224,v224,v72 quad_perm:[1,0,3,2] row_mask:0xf bank_mask:0xf
v_mul_f32_dpp v225,v225,v73 quad_perm:[1,0,3,2] row_mask:0xf bank_mask:0xf
v_mul_f32_dpp v226,v226,v74 quad_perm:[1,0,3,2] row_mask:0xf bank_mask:0xf
v_mul_f32_dpp v227,v227,v75 quad_perm:[1,0,3,2] row_mask:0xf bank_mask:0xf
v_mul_f32_dpp v232,v232,v76 quad_perm:[1,0,3,2] row_mask:0xf bank_mask:0xf
v_mul_f32_dpp v233,v233,v77 quad_perm:[1,0,3,2] row_mask:0xf bank_mask:0xf
v_mul_f32_dpp v234,v234,v78 quad_perm:[1,0,3,2] row_mask:0xf bank_mask:0xf
v_mul_f32_dpp v235,v235,v79 quad_perm:[1,0,3,2] row_mask:0xf bank_mask:0xf
v_cvt_pk_bf16_f32 v48,v224,v225
v_cvt_pk_bf16_f32 v49,v226,v227
v_cvt_pk_bf16_f32 v50,v232,v233
v_cvt_pk_bf16_f32 v51,v234,v235
s_mov_b64 exec,s[12:13]
v_swap_b32 v40,v48
v_swap_b32 v41,v49
v_swap_b32 v42,v50
v_swap_b32 v43,v51
s_mov_b64 exec,-1
s_add_u32 s65,s61,4816896
buffer_store_dwordx4 v[40:43],v10,s[40:43],s65 offen offset:0 sc1
s_add_u32 s65,s61,4816896
buffer_store_dwordx4 v[48:51],v15,s[40:43],s65 offen offset:384 sc1
v_mul_f32 v240,v240,v80
v_mul_f32 v241,v241,v81
v_mul_f32 v242,v242,v82
v_mul_f32 v243,v243,v83
v_mul_f32 v248,v248,v84
v_mul_f32 v249,v249,v85
v_mul_f32 v250,v250,v86
v_mul_f32 v251,v251,v87
v_cvt_pk_bf16_f32 v56,v240,v241
v_cvt_pk_bf16_f32 v57,v242,v243
v_cvt_pk_bf16_f32 v58,v248,v249
v_cvt_pk_bf16_f32 v59,v250,v251
v_mul_f32_dpp v212,v212,v64 quad_perm:[1,0,3,2] row_mask:0xf bank_mask:0xf
v_mul_f32_dpp v213,v213,v65 quad_perm:[1,0,3,2] row_mask:0xf bank_mask:0xf
v_mul_f32_dpp v214,v214,v66 quad_perm:[1,0,3,2] row_mask:0xf bank_mask:0xf
v_mul_f32_dpp v215,v215,v67 quad_perm:[1,0,3,2] row_mask:0xf bank_mask:0xf
v_mul_f32_dpp v220,v220,v68 quad_perm:[1,0,3,2] row_mask:0xf bank_mask:0xf
v_mul_f32_dpp v221,v221,v69 quad_perm:[1,0,3,2] row_mask:0xf bank_mask:0xf
v_mul_f32_dpp v222,v222,v70 quad_perm:[1,0,3,2] row_mask:0xf bank_mask:0xf
v_mul_f32_dpp v223,v223,v71 quad_perm:[1,0,3,2] row_mask:0xf bank_mask:0xf
v_cvt_pk_bf16_f32 v44,v212,v213
v_cvt_pk_bf16_f32 v45,v214,v215
v_cvt_pk_bf16_f32 v46,v220,v221
v_cvt_pk_bf16_f32 v47,v222,v223
s_mov_b64 exec,s[12:13]
v_swap_b32 v56,v44
v_swap_b32 v57,v45
v_swap_b32 v58,v46
v_swap_b32 v59,v47
s_mov_b64 exec,-1
s_add_u32 s65,s61,4816896
buffer_store_dwordx4 v[56:59],v10,s[40:43],s65 offen offset:128 sc1
s_add_u32 s65,s61,4816896
buffer_store_dwordx4 v[44:47],v15,s[40:43],s65 offen offset:512 sc1
v_mul_f32 v228,v228,v72
v_mul_f32 v229,v229,v73
v_mul_f32 v230,v230,v74
v_mul_f32 v231,v231,v75
v_mul_f32 v236,v236,v76
v_mul_f32 v237,v237,v77
v_mul_f32 v238,v238,v78
v_mul_f32 v239,v239,v79
v_cvt_pk_bf16_f32 v52,v228,v229
v_cvt_pk_bf16_f32 v53,v230,v231
v_cvt_pk_bf16_f32 v54,v236,v237
v_cvt_pk_bf16_f32 v55,v238,v239
v_mul_f32_dpp v244,v244,v80 quad_perm:[1,0,3,2] row_mask:0xf bank_mask:0xf
v_mul_f32_dpp v245,v245,v81 quad_perm:[1,0,3,2] row_mask:0xf bank_mask:0xf
v_mul_f32_dpp v246,v246,v82 quad_perm:[1,0,3,2] row_mask:0xf bank_mask:0xf
v_mul_f32_dpp v247,v247,v83 quad_perm:[1,0,3,2] row_mask:0xf bank_mask:0xf
v_mul_f32_dpp v252,v252,v84 quad_perm:[1,0,3,2] row_mask:0xf bank_mask:0xf
v_mul_f32_dpp v253,v253,v85 quad_perm:[1,0,3,2] row_mask:0xf bank_mask:0xf
v_mul_f32_dpp v254,v254,v86 quad_perm:[1,0,3,2] row_mask:0xf bank_mask:0xf
v_mul_f32_dpp v255,v255,v87 quad_perm:[1,0,3,2] row_mask:0xf bank_mask:0xf
v_cvt_pk_bf16_f32 v60,v244,v245
v_cvt_pk_bf16_f32 v61,v246,v247
v_cvt_pk_bf16_f32 v62,v252,v253
v_cvt_pk_bf16_f32 v63,v254,v255
s_mov_b64 exec,s[12:13]
v_swap_b32 v52,v60
v_swap_b32 v53,v61
v_swap_b32 v54,v62
v_swap_b32 v55,v63
s_mov_b64 exec,-1
s_add_u32 s65,s61,4816896
buffer_store_dwordx4 v[52:55],v10,s[40:43],s65 offen offset:256 sc1
s_add_u32 s65,s61,4816896
buffer_store_dwordx4 v[60:63],v15,s[40:43],s65 offen offset:640 sc1
v_and_b32 v1,0x7c007c00,v1
v_lshrrev_b32 v2,16,v1
v_and_b32 v1,0xffff,v1
v_max_u32 v1,v1,v2
v_cmp_eq_u32 vcc,0x7c00,v1
s_or_b32 s64,vcc_lo,vcc_hi
v_mov_b32 v1,s64
v_mov_b32 v2,8192
s_mov_b64 exec,1
ds_or_b32 v2,v1 offset:3456
s_mov_b64 exec,-1
s_waitcnt lgkmcnt(0)
s_barrier
ds_read_b32 v1,v2 offset:3456
s_waitcnt lgkmcnt(0)
v_readfirstlane_b32 s64,v1
s_cmp_lg_u32 s64,0
s_cbranch_scc1 N_fallback_%=
s_endpgm
N_mainend_%=:
N_fallback_%=:
s_setprio 0
s_waitcnt vmcnt(0) lgkmcnt(0)
s_barrier
v_lshrrev_b32 v237,6,%1
v_and_b32 v232,63,%1
v_readfirstlane_b32 s52,v237
v_lshlrev_b32 v248,4,v232
v_min_u32 v244,383,%1
v_lshlrev_b32 v238,1,v244
global_load_ushort v244,v238,s[26:27]
v_min_u32 v245,95,%1
v_lshlrev_b32 v239,1,v245
global_load_ushort v245,v239,s[30:31]
global_load_ushort v246,v239,s[32:33]
s_mov_b64 s[92:93],s[22:23]
s_and_b32 s93,s93,0xffff
s_mov_b32 s94,1728
s_mov_b32 s95,0x00020000
s_lshl_b32 s71,s52,11
s_add_u32 s71,s71,62208
s_mov_b32 m0,s71
s_movk_i32 s64,1024
buffer_load_dwordx4 v248,s[92:95],0 offen lds
s_add_u32 m0,s71,1024
s_nop 0
buffer_load_dwordx4 v248,s[92:95],s64 offen lds
v_mov_b32 v250,s71
s_mov_b64 s[36:37],s[20:21]
s_mov_b64 s[40:41],s[34:35]
s_mov_b64 s[44:45],s[24:25]
s_mov_b64 s[48:49],s[28:29]
s_sub_u32 s36,s36,192
s_subb_u32 s37,s37,0
s_and_b32 s37,s37,0xffff
s_mov_b32 s38,50331840
s_mov_b32 s39,0x00020000
s_and_b32 s41,s41,0xffff
s_mov_b32 s42,50331648
s_mov_b32 s43,0x00020000
s_and_b32 s45,s45,0xffff
s_mov_b32 s46,73728
s_mov_b32 s47,0x00020000
s_and_b32 s49,s49,0xffff
s_mov_b32 s50,73728
s_mov_b32 s51,0x00020000
s_mov_b32 s60,-1
s_mov_b32 s61,0xffff
s_mov_b32 s62,-1
s_mov_b32 s63,0x3fffff
s_and_b32 s64,%0,7
s_lshr_b32 s70,%0,3
s_lshl_b32 s56,s64,6
s_mul_i32 s57,s70,3072
s_and_b32 s66,s52,3
s_mul_i32 s68,s66,864
s_mul_i32 s67,s66,54
s_mul_i32 s58,s66,1536
v_add_u32 v237,s67,v232
v_mul_u32_u24 v238,0xaaab,v237
v_lshrrev_b32 v238,19,v238
v_mul_u32_u24 v239,12,v238
v_sub_u32 v239,v237,v239
v_bfe_u32 v237,v238,2,2
v_xor_b32 v239,v239,v237
v_lshlrev_b32 v239,4,v239
v_mul_u32_u24 v237,192,v238
v_add_u32 v247,v237,v239
s_cmp_eq_u32 s70,0
s_cselect_b32 s71,0,99
s_cmp_eq_u32 s70,31
s_cselect_b32 s71,17,s71
v_cmp_eq_u32 vcc,s71,v238
v_mov_b32 v237,0x7fff0000
s_sub_u32 s67,s56,1
s_mul_i32 s59,s67,98304
s_add_u32 s59,s59,s57
v_cndmask_b32 v247,v247,v237,vcc
s_lshr_b32 s66,s52,2
s_mul_i32 s64,s66,3456
s_add_u32 s64,s64,s68
s_mul_i32 s65,s66,98304
s_add_u32 s65,s65,s59
s_add_u32 s67,s67,s66
s_cmp_lt_i32 s67,0
s_cselect_b32 s71,0x70000000,s65
s_mov_b64 exec,s[62:63]
s_add_u32 m0,s64,0
s_nop 0
buffer_load_dwordx4 v247,s[36:39],s71 offen lds
s_add_u32 m0,s64,6912
s_add_u32 s71,s65,196608
buffer_load_dwordx4 v247,s[36:39],s71 offen lds
s_add_u32 m0,s64,13824
s_add_u32 s71,s65,393216
buffer_load_dwordx4 v247,s[36:39],s71 offen lds
s_add_u32 m0,s64,20736
s_add_u32 s71,s65,589824
buffer_load_dwordx4 v247,s[36:39],s71 offen lds
s_add_u32 m0,s64,27648
s_add_u32 s71,s65,786432
buffer_load_dwordx4 v247,s[36:39],s71 offen lds
s_add_u32 m0,s64,34560
s_add_u32 s71,s65,983040
buffer_load_dwordx4 v247,s[36:39],s71 offen lds
s_add_u32 m0,s64,41472
s_add_u32 s71,s65,1179648
buffer_load_dwordx4 v247,s[36:39],s71 offen lds
s_add_u32 m0,s64,48384
s_add_u32 s71,s65,1376256
buffer_load_dwordx4 v247,s[36:39],s71 offen lds
s_add_u32 m0,s64,55296
s_add_u32 s71,s65,1572864
buffer_load_dwordx4 v247,s[36:39],s71 offen lds
s_mov_b64 exec,-1
v_lshrrev_b32 v237,2,v232
v_mul_u32_u24 v237,768,v237
v_and_b32 v238,3,v232
v_lshl_add_u32 v237,v238,4,v237
s_and_b32 s66,s52,3
s_mul_i32 s71,s66,18432
v_add_u32 v27,s71,v237
v_add_u32 v28,s58,v248
s_mov_b32 s54,62208
s_mov_b32 s53,0
s_mov_b32 s96,0x70000000
s_add_u32 s68,s68,s54
s_add_u32 s59,s59,1572864
s_lshl_b32 s66,s52,1
s_add_u32 s66,s66,s56
s_mul_i32 s69,s66,98304
s_add_u32 s69,s69,s57
s_cmp_ge_u32 s52,4
s_cbranch_scc1 L_noW_%=
s_mov_b64 exec,s[60:61]
s_add_u32 m0,s58,124416
s_movk_i32 s64,0
buffer_load_dwordx4 v27,s[44:47],s64 offen lds
s_add_u32 m0,s58,125184
s_movk_i32 s65,9216
buffer_load_dwordx4 v27,s[44:47],s65 offen lds
s_mov_b64 exec,-1
s_mov_b64 exec,s[60:61]
s_add_u32 m0,s58,142848
s_mov_b32 s64,0
buffer_load_dwordx4 v28,s[48:51],s64 offen lds
s_add_u32 m0,s58,143616
s_mov_b32 s65,768
buffer_load_dwordx4 v28,s[48:51],s65 offen lds
s_mov_b64 exec,-1
s_mov_b64 exec,s[60:61]
s_add_u32 m0,s58,130560
s_movk_i32 s64,64
buffer_load_dwordx4 v27,s[44:47],s64 offen lds
s_add_u32 m0,s58,131328
s_movk_i32 s65,9280
buffer_load_dwordx4 v27,s[44:47],s65 offen lds
s_mov_b64 exec,-1
s_mov_b64 exec,s[60:61]
s_add_u32 m0,s58,136704
s_movk_i32 s64,128
buffer_load_dwordx4 v27,s[44:47],s64 offen lds
s_add_u32 m0,s58,137472
s_movk_i32 s65,9344
buffer_load_dwordx4 v27,s[44:47],s65 offen lds
s_mov_b64 exec,-1
s_branch L_W_%=
L_noW_%=:
v_mov_b32 v27,v247
s_mul_i32 s64,s70,2304
s_and_b32 s65,s52,3
s_mul_i32 s65,s65,576
s_add_u32 s64,s64,s65
v_min_u32 v237,560,v248
buffer_load_dwordx4 v[252:255],v237,s[44:47],s64 offen
buffer_load_dwordx4 v[252:255],v237,s[48:51],s64 offen
L_W_%=:
v_and_b32 v233,15,v232
v_lshrrev_b32 v234,4,v232
v_lshrrev_b32 v235,5,v232
v_and_b32 v237,1,v234
v_lshlrev_b32 v237,1,v237
v_or_b32 v236,v237,v235
v_lshrrev_b32 v237,3,v233
v_lshlrev_b32 v237,2,v237
v_and_b32 v238,3,v233
v_or_b32 v241,v237,v238
v_lshrrev_b32 v237,2,v233
v_and_b32 v242,1,v237
v_sub_u32 v237,v236,v242
v_cmp_gt_u32 vcc,3,v237
v_mov_b32 v238,0
v_mov_b32 v239,-1
v_cndmask_b32 v237,v238,v237,vcc
v_cndmask_b32 v249,v238,v239,vcc
v_mul_u32_u24 v237,576,v237
v_lshl_add_u32 v237,v241,1,v237
v_add_u32 v250,v250,v237
v_lshrrev_b32 v237,1,v241
v_cmp_eq_u32 vcc,0,v237
s_nop 1
v_cndmask_b32 v32,v238,v249,vcc
v_cmp_eq_u32 vcc,1,v237
s_nop 1
v_cndmask_b32 v33,v238,v249,vcc
v_cmp_eq_u32 vcc,2,v237
s_nop 1
v_cndmask_b32 v34,v238,v249,vcc
v_cmp_eq_u32 vcc,3,v237
s_nop 1
v_cndmask_b32 v35,v238,v249,vcc
v_and_b32 v243,1,v241
v_lshlrev_b32 v243,4,v243
s_mul_i32 s71,s52,6912
v_add_u32 v237,0,v233
v_bfe_u32 v238,v237,2,2
v_mul_u32_u24 v239,18,v236
v_add_u32 v239,v239,v237
v_mul_u32_u24 v239,192,v239
v_add_u32 v239,s71,v239
v_xor_b32 v240,0,v238
v_lshl_add_u32 v4,v240,4,v239
v_xor_b32 v240,1,v238
v_lshl_add_u32 v5,v240,4,v239
v_xor_b32 v240,2,v238
v_lshl_add_u32 v6,v240,4,v239
v_xor_b32 v240,3,v238
v_lshl_add_u32 v7,v240,4,v239
v_add_u32 v237,1,v233
v_bfe_u32 v238,v237,2,2
v_mul_u32_u24 v239,18,v236
v_add_u32 v239,v239,v237
v_mul_u32_u24 v239,192,v239
v_add_u32 v239,s71,v239
v_xor_b32 v240,0,v238
v_lshl_add_u32 v8,v240,4,v239
v_xor_b32 v240,1,v238
v_lshl_add_u32 v9,v240,4,v239
v_xor_b32 v240,2,v238
v_lshl_add_u32 v10,v240,4,v239
v_xor_b32 v240,3,v238
v_lshl_add_u32 v11,v240,4,v239
v_add_u32 v237,2,v233
v_bfe_u32 v238,v237,2,2
v_mul_u32_u24 v239,18,v236
v_add_u32 v239,v239,v237
v_mul_u32_u24 v239,192,v239
v_add_u32 v239,s71,v239
v_xor_b32 v240,0,v238
v_lshl_add_u32 v12,v240,4,v239
v_xor_b32 v240,1,v238
v_lshl_add_u32 v13,v240,4,v239
v_xor_b32 v240,2,v238
v_lshl_add_u32 v14,v240,4,v239
v_xor_b32 v240,3,v238
v_lshl_add_u32 v15,v240,4,v239
v_lshrrev_b32 v237,2,v233
v_lshl_add_u32 v237,v235,2,v237
v_and_b32 v238,1,v234
v_lshlrev_b32 v238,5,v238
v_and_b32 v239,3,v233
v_lshl_add_u32 v238,v239,3,v238
v_add_u32 v238,124416,v238
v_lshl_add_u32 v16,v237,6,v238
v_mul_u32_u24 v237,192,v237
v_add_u32 v17,v237,v238
v_add_u32 v17,18432,v17
v_lshlrev_b32 v237,4,v235
v_add_u32 v18,161280,v237
v_add_u32 v237,s71,v248
v_add_u32 v19,3648,v237
v_and_b32 v237,1,v234
v_add_u32 v237,1,v237
v_mul_u32_u24 v237,18,v237
v_add_u32 v238,1,v233
v_add_u32 v237,v237,v238
v_mul_u32_u24 v237,192,v237
v_add_u32 v237,s71,v237
v_lshl_add_u32 v237,v235,3,v237
v_bfe_u32 v238,v238,2,2
v_xor_b32 v240,0,v238
v_lshl_add_u32 v20,v240,4,v237
v_xor_b32 v240,1,v238
v_lshl_add_u32 v21,v240,4,v237
v_xor_b32 v240,2,v238
v_lshl_add_u32 v22,v240,4,v237
v_xor_b32 v240,3,v238
v_lshl_add_u32 v23,v240,4,v237
v_add_u32 v237,0,v232
v_mul_u32_u24 v238,0xaaab,v237
v_lshrrev_b32 v238,19,v238
v_mul_u32_u24 v239,12,v238
v_sub_u32 v239,v237,v239
v_add_u32 v237,1,v238
v_bfe_u32 v237,v237,2,2
v_xor_b32 v239,v239,v237
v_lshlrev_b32 v239,4,v239
v_mul_u32_u24 v237,192,v238
v_add_u32 v24,v237,v239
v_add_u32 v237,64,v232
v_mul_u32_u24 v238,0xaaab,v237
v_lshrrev_b32 v238,19,v238
v_mul_u32_u24 v239,12,v238
v_sub_u32 v239,v237,v239
v_add_u32 v237,1,v238
v_bfe_u32 v237,v237,2,2
v_xor_b32 v239,v239,v237
v_lshlrev_b32 v239,4,v239
v_mul_u32_u24 v237,192,v238
v_add_u32 v25,v237,v239
v_add_u32 v237,128,v232
v_mul_u32_u24 v238,0xaaab,v237
v_lshrrev_b32 v238,19,v238
v_mul_u32_u24 v239,12,v238
v_sub_u32 v239,v237,v239
v_add_u32 v237,1,v238
v_bfe_u32 v237,v237,2,2
v_xor_b32 v239,v239,v237
v_lshlrev_b32 v239,4,v239
v_mul_u32_u24 v237,192,v238
v_add_u32 v26,v237,v239
s_cmp_ge_u32 s52,4
s_cbranch_scc1 L_wB_%=
s_waitcnt vmcnt(17)
s_branch L_wA_%=
L_wB_%=:
s_waitcnt vmcnt(11)
L_wA_%=:
ds_read_u16 v36,v250 offset:0
ds_read_u16 v37,v250 offset:16
ds_read_u16 v38,v250 offset:32
ds_read_u16 v39,v250 offset:48
ds_read_u16 v40,v250 offset:64
ds_read_u16 v41,v250 offset:80
ds_read_u16 v42,v250 offset:96
ds_read_u16 v43,v250 offset:112
ds_read_u16 v44,v250 offset:128
ds_read_u16 v45,v250 offset:144
ds_read_u16 v46,v250 offset:160
ds_read_u16 v47,v250 offset:176
ds_read_u16 v48,v250 offset:192
ds_read_u16 v49,v250 offset:208
ds_read_u16 v50,v250 offset:224
ds_read_u16 v51,v250 offset:240
ds_read_u16 v52,v250 offset:256
ds_read_u16 v53,v250 offset:272
ds_read_u16 v54,v250 offset:288
ds_read_u16 v55,v250 offset:304
ds_read_u16 v56,v250 offset:320
ds_read_u16 v57,v250 offset:336
ds_read_u16 v58,v250 offset:352
ds_read_u16 v59,v250 offset:368
ds_read_u16 v60,v250 offset:384
ds_read_u16 v61,v250 offset:400
ds_read_u16 v62,v250 offset:416
ds_read_u16 v63,v250 offset:432
ds_read_u16 v64,v250 offset:448
ds_read_u16 v65,v250 offset:464
ds_read_u16 v66,v250 offset:480
ds_read_u16 v67,v250 offset:496
ds_read_u16 v68,v250 offset:512
ds_read_u16 v69,v250 offset:528
ds_read_u16 v70,v250 offset:544
ds_read_u16 v71,v250 offset:560
v_lshlrev_b32 v244,16,v244
v_lshlrev_b32 v245,16,v245
v_lshlrev_b32 v246,16,v246
v_mul_f32 v244,0xbeeff558,v244
v_mul_f32 v245,0xbeeff558,v245
v_mul_f32 v246,0xc0088e99,v246
v_min_u32 v237,383,%1
v_lshlrev_b32 v237,2,v237
v_add_u32 v237,161280,v237
ds_write_b32 v237,v244 offset:0
v_min_u32 v237,95,%1
v_lshlrev_b32 v237,2,v237
v_add_u32 v237,161280,v237
ds_write_b32 v237,v245 offset:1920
ds_write_b32 v237,v246 offset:1536
s_waitcnt lgkmcnt(0)
v_lshlrev_b32 v36,v243,v36
v_lshlrev_b32 v37,v243,v37
v_lshlrev_b32 v38,v243,v38
v_lshlrev_b32 v39,v243,v39
v_lshlrev_b32 v40,v243,v40
v_lshlrev_b32 v41,v243,v41
v_lshlrev_b32 v42,v243,v42
v_lshlrev_b32 v43,v243,v43
v_lshlrev_b32 v44,v243,v44
v_lshlrev_b32 v45,v243,v45
v_lshlrev_b32 v46,v243,v46
v_lshlrev_b32 v47,v243,v47
v_lshlrev_b32 v48,v243,v48
v_lshlrev_b32 v49,v243,v49
v_lshlrev_b32 v50,v243,v50
v_lshlrev_b32 v51,v243,v51
v_lshlrev_b32 v52,v243,v52
v_lshlrev_b32 v53,v243,v53
v_lshlrev_b32 v54,v243,v54
v_lshlrev_b32 v55,v243,v55
v_lshlrev_b32 v56,v243,v56
v_lshlrev_b32 v57,v243,v57
v_lshlrev_b32 v58,v243,v58
v_lshlrev_b32 v59,v243,v59
v_lshlrev_b32 v60,v243,v60
v_lshlrev_b32 v61,v243,v61
v_lshlrev_b32 v62,v243,v62
v_lshlrev_b32 v63,v243,v63
v_lshlrev_b32 v64,v243,v64
v_lshlrev_b32 v65,v243,v65
v_lshlrev_b32 v66,v243,v66
v_lshlrev_b32 v67,v243,v67
v_lshlrev_b32 v68,v243,v68
v_lshlrev_b32 v69,v243,v69
v_lshlrev_b32 v70,v243,v70
v_lshlrev_b32 v71,v243,v71
s_cmp_ge_u32 s52,4
s_cbranch_scc1 L_fB_%=
s_waitcnt vmcnt(6)
s_branch L_fA_%=
L_fB_%=:
s_waitcnt vmcnt(2)
L_fA_%=:
s_cmp_ge_u32 s52,4
s_cbranch_scc1 L_pass1_%=
L_pass0_%=:
s_barrier
ds_read_b128 v[200:203],v4 offset:0
ds_read_b128 v[204:207],v8 offset:0
ds_read_b128 v[208:211],v12 offset:0
ds_read_b128 v[212:215],v5 offset:0
ds_read_b128 v[216:219],v9 offset:0
ds_read_b128 v[220:223],v13 offset:0
ds_read_b128 v[96:99],v18 offset:1920
ds_read_b128 v[100:103],v18 offset:1952
ds_read_b128 v[104:107],v18 offset:1984
ds_read_b128 v[108:111],v18 offset:2016
ds_read_b128 v[112:115],v18 offset:2048
ds_read_b128 v[116:119],v18 offset:2080
ds_read_b128 v[120:123],v18 offset:2112
ds_read_b128 v[124:127],v18 offset:2144
ds_read_b128 v[128:131],v18 offset:2176
ds_read_b128 v[132:135],v18 offset:2208
ds_read_b128 v[136:139],v18 offset:2240
ds_read_b128 v[140:143],v18 offset:2272
v_and_b32 v72,v32,v36
v_and_b32 v73,v33,v36
v_and_b32 v74,v34,v36
v_and_b32 v75,v35,v36
v_and_b32 v76,v32,v48
v_and_b32 v77,v33,v48
v_and_b32 v78,v34,v48
v_and_b32 v79,v35,v48
v_and_b32 v80,v32,v60
v_and_b32 v81,v33,v60
v_and_b32 v82,v34,v60
v_and_b32 v83,v35,v60
v_and_b32 v84,v32,v37
v_and_b32 v85,v33,v37
v_and_b32 v86,v34,v37
v_and_b32 v87,v35,v37
v_and_b32 v88,v32,v49
v_and_b32 v89,v33,v49
v_and_b32 v90,v34,v49
v_and_b32 v91,v35,v49
v_and_b32 v92,v32,v61
v_and_b32 v93,v33,v61
v_and_b32 v94,v34,v61
v_and_b32 v95,v35,v61
s_setprio 1
s_waitcnt lgkmcnt(15)
v_mfma_f32_16x16x32_bf16 v[144:147],v[72:75],v[200:203],0
v_mfma_f32_16x16x32_bf16 v[144:147],v[76:79],v[204:207],v[144:147]
v_mfma_f32_16x16x32_bf16 v[144:147],v[80:83],v[208:211],v[144:147]
ds_read_b128 v[200:203],v6 offset:0
ds_read_b128 v[204:207],v10 offset:0
ds_read_b128 v[208:211],v14 offset:0
s_setprio 0
v_and_b32 v72,v32,v38
v_and_b32 v73,v33,v38
v_and_b32 v74,v34,v38
v_and_b32 v75,v35,v38
v_and_b32 v76,v32,v50
v_and_b32 v77,v33,v50
v_and_b32 v78,v34,v50
v_and_b32 v79,v35,v50
v_and_b32 v80,v32,v62
v_and_b32 v81,v33,v62
v_and_b32 v82,v34,v62
v_and_b32 v83,v35,v62
v_mul_f32 v237,v144,v144
v_mul_f32 v236,v145,v145
v_mul_f32 v239,v146,v146
v_mul_f32 v238,v147,v147
s_setprio 1
s_waitcnt lgkmcnt(15)
v_mfma_f32_16x16x32_bf16 v[148:151],v[84:87],v[212:215],0
v_mfma_f32_16x16x32_bf16 v[148:151],v[88:91],v[216:219],v[148:151]
v_mfma_f32_16x16x32_bf16 v[148:151],v[92:95],v[220:223],v[148:151]
ds_read_b128 v[212:215],v7 offset:0
ds_read_b128 v[216:219],v11 offset:0
ds_read_b128 v[220:223],v15 offset:0
s_setprio 0
v_and_b32 v84,v32,v39
v_and_b32 v85,v33,v39
v_and_b32 v86,v34,v39
v_and_b32 v87,v35,v39
v_and_b32 v88,v32,v51
v_and_b32 v89,v33,v51
v_and_b32 v90,v34,v51
v_and_b32 v91,v35,v51
v_and_b32 v92,v32,v63
v_and_b32 v93,v33,v63
v_and_b32 v94,v34,v63
v_and_b32 v95,v35,v63
v_add_f32 v232,v144,v148
v_fmac_f32 v237,v148,v148
v_add_f32 v233,v145,v149
v_fmac_f32 v236,v149,v149
v_add_f32 v234,v146,v150
v_fmac_f32 v239,v150,v150
v_add_f32 v235,v147,v151
v_fmac_f32 v238,v151,v151
s_setprio 1
s_waitcnt lgkmcnt(3)
v_mfma_f32_16x16x32_bf16 v[152:155],v[72:75],v[200:203],0
v_mfma_f32_16x16x32_bf16 v[152:155],v[76:79],v[204:207],v[152:155]
v_mfma_f32_16x16x32_bf16 v[152:155],v[80:83],v[208:211],v[152:155]
ds_read_b128 v[200:203],v4 offset:64
ds_read_b128 v[204:207],v8 offset:64
ds_read_b128 v[208:211],v12 offset:64
s_setprio 0
v_and_b32 v72,v32,v40
v_and_b32 v73,v33,v40
v_and_b32 v74,v34,v40
v_and_b32 v75,v35,v40
v_and_b32 v76,v32,v52
v_and_b32 v77,v33,v52
v_and_b32 v78,v34,v52
v_and_b32 v79,v35,v52
v_and_b32 v80,v32,v64
v_and_b32 v81,v33,v64
v_and_b32 v82,v34,v64
v_and_b32 v83,v35,v64
v_add_f32 v232,v232,v152
v_fmac_f32 v237,v152,v152
v_add_f32 v233,v233,v153
v_fmac_f32 v236,v153,v153
v_add_f32 v234,v234,v154
v_fmac_f32 v239,v154,v154
v_add_f32 v235,v235,v155
v_fmac_f32 v238,v155,v155
s_setprio 1
s_waitcnt lgkmcnt(3)
v_mfma_f32_16x16x32_bf16 v[156:159],v[84:87],v[212:215],0
v_mfma_f32_16x16x32_bf16 v[156:159],v[88:91],v[216:219],v[156:159]
v_mfma_f32_16x16x32_bf16 v[156:159],v[92:95],v[220:223],v[156:159]
ds_read_b128 v[212:215],v5 offset:64
ds_read_b128 v[216:219],v9 offset:64
ds_read_b128 v[220:223],v13 offset:64
s_setprio 0
v_and_b32 v84,v32,v41
v_and_b32 v85,v33,v41
v_and_b32 v86,v34,v41
v_and_b32 v87,v35,v41
v_and_b32 v88,v32,v53
v_and_b32 v89,v33,v53
v_and_b32 v90,v34,v53
v_and_b32 v91,v35,v53
v_and_b32 v92,v32,v65
v_and_b32 v93,v33,v65
v_and_b32 v94,v34,v65
v_and_b32 v95,v35,v65
v_add_f32 v232,v232,v156
v_fmac_f32 v237,v156,v156
v_add_f32 v233,v233,v157
v_fmac_f32 v236,v157,v157
v_add_f32 v234,v234,v158
v_fmac_f32 v239,v158,v158
v_add_f32 v235,v235,v159
v_fmac_f32 v238,v159,v159
s_setprio 1
s_waitcnt lgkmcnt(3)
v_mfma_f32_16x16x32_bf16 v[160:163],v[72:75],v[200:203],0
v_mfma_f32_16x16x32_bf16 v[160:163],v[76:79],v[204:207],v[160:163]
v_mfma_f32_16x16x32_bf16 v[160:163],v[80:83],v[208:211],v[160:163]
ds_read_b128 v[200:203],v6 offset:64
ds_read_b128 v[204:207],v10 offset:64
ds_read_b128 v[208:211],v14 offset:64
s_setprio 0
v_and_b32 v72,v32,v42
v_and_b32 v73,v33,v42
v_and_b32 v74,v34,v42
v_and_b32 v75,v35,v42
v_and_b32 v76,v32,v54
v_and_b32 v77,v33,v54
v_and_b32 v78,v34,v54
v_and_b32 v79,v35,v54
v_and_b32 v80,v32,v66
v_and_b32 v81,v33,v66
v_and_b32 v82,v34,v66
v_and_b32 v83,v35,v66
v_add_f32 v232,v232,v160
v_fmac_f32 v237,v160,v160
v_add_f32 v233,v233,v161
v_fmac_f32 v236,v161,v161
v_add_f32 v234,v234,v162
v_fmac_f32 v239,v162,v162
v_add_f32 v235,v235,v163
v_fmac_f32 v238,v163,v163
s_setprio 1
s_waitcnt lgkmcnt(3)
v_mfma_f32_16x16x32_bf16 v[164:167],v[84:87],v[212:215],0
v_mfma_f32_16x16x32_bf16 v[164:167],v[88:91],v[216:219],v[164:167]
v_mfma_f32_16x16x32_bf16 v[164:167],v[92:95],v[220:223],v[164:167]
ds_read_b128 v[212:215],v7 offset:64
ds_read_b128 v[216:219],v11 offset:64
ds_read_b128 v[220:223],v15 offset:64
s_setprio 0
v_and_b32 v84,v32,v43
v_and_b32 v85,v33,v43
v_and_b32 v86,v34,v43
v_and_b32 v87,v35,v43
v_and_b32 v88,v32,v55
v_and_b32 v89,v33,v55
v_and_b32 v90,v34,v55
v_and_b32 v91,v35,v55
v_and_b32 v92,v32,v67
v_and_b32 v93,v33,v67
v_and_b32 v94,v34,v67
v_and_b32 v95,v35,v67
v_add_f32 v232,v232,v164
v_fmac_f32 v237,v164,v164
v_add_f32 v233,v233,v165
v_fmac_f32 v236,v165,v165
v_add_f32 v234,v234,v166
v_fmac_f32 v239,v166,v166
v_add_f32 v235,v235,v167
v_fmac_f32 v238,v167,v167
s_setprio 1
s_waitcnt lgkmcnt(3)
v_mfma_f32_16x16x32_bf16 v[168:171],v[72:75],v[200:203],0
v_mfma_f32_16x16x32_bf16 v[168:171],v[76:79],v[204:207],v[168:171]
v_mfma_f32_16x16x32_bf16 v[168:171],v[80:83],v[208:211],v[168:171]
ds_read_b128 v[200:203],v4 offset:128
ds_read_b128 v[204:207],v8 offset:128
ds_read_b128 v[208:211],v12 offset:128
s_setprio 0
v_and_b32 v72,v32,v44
v_and_b32 v73,v33,v44
v_and_b32 v74,v34,v44
v_and_b32 v75,v35,v44
v_and_b32 v76,v32,v56
v_and_b32 v77,v33,v56
v_and_b32 v78,v34,v56
v_and_b32 v79,v35,v56
v_and_b32 v80,v32,v68
v_and_b32 v81,v33,v68
v_and_b32 v82,v34,v68
v_and_b32 v83,v35,v68
v_add_f32 v232,v232,v168
v_fmac_f32 v237,v168,v168
v_add_f32 v233,v233,v169
v_fmac_f32 v236,v169,v169
v_add_f32 v234,v234,v170
v_fmac_f32 v239,v170,v170
v_add_f32 v235,v235,v171
v_fmac_f32 v238,v171,v171
s_setprio 1
s_waitcnt lgkmcnt(3)
v_mfma_f32_16x16x32_bf16 v[172:175],v[84:87],v[212:215],0
v_mfma_f32_16x16x32_bf16 v[172:175],v[88:91],v[216:219],v[172:175]
v_mfma_f32_16x16x32_bf16 v[172:175],v[92:95],v[220:223],v[172:175]
ds_read_b128 v[212:215],v5 offset:128
ds_read_b128 v[216:219],v9 offset:128
ds_read_b128 v[220:223],v13 offset:128
s_setprio 0
v_and_b32 v84,v32,v45
v_and_b32 v85,v33,v45
v_and_b32 v86,v34,v45
v_and_b32 v87,v35,v45
v_and_b32 v88,v32,v57
v_and_b32 v89,v33,v57
v_and_b32 v90,v34,v57
v_and_b32 v91,v35,v57
v_and_b32 v92,v32,v69
v_and_b32 v93,v33,v69
v_and_b32 v94,v34,v69
v_and_b32 v95,v35,v69
v_add_f32 v232,v232,v172
v_fmac_f32 v237,v172,v172
v_add_f32 v233,v233,v173
v_fmac_f32 v236,v173,v173
v_add_f32 v234,v234,v174
v_fmac_f32 v239,v174,v174
v_add_f32 v235,v235,v175
v_fmac_f32 v238,v175,v175
s_setprio 1
s_waitcnt lgkmcnt(3)
v_mfma_f32_16x16x32_bf16 v[176:179],v[72:75],v[200:203],0
v_mfma_f32_16x16x32_bf16 v[176:179],v[76:79],v[204:207],v[176:179]
v_mfma_f32_16x16x32_bf16 v[176:179],v[80:83],v[208:211],v[176:179]
ds_read_b128 v[200:203],v6 offset:128
ds_read_b128 v[204:207],v10 offset:128
ds_read_b128 v[208:211],v14 offset:128
s_setprio 0
v_and_b32 v72,v32,v46
v_and_b32 v73,v33,v46
v_and_b32 v74,v34,v46
v_and_b32 v75,v35,v46
v_and_b32 v76,v32,v58
v_and_b32 v77,v33,v58
v_and_b32 v78,v34,v58
v_and_b32 v79,v35,v58
v_and_b32 v80,v32,v70
v_and_b32 v81,v33,v70
v_and_b32 v82,v34,v70
v_and_b32 v83,v35,v70
v_add_f32 v232,v232,v176
v_fmac_f32 v237,v176,v176
v_add_f32 v233,v233,v177
v_fmac_f32 v236,v177,v177
v_add_f32 v234,v234,v178
v_fmac_f32 v239,v178,v178
v_add_f32 v235,v235,v179
v_fmac_f32 v238,v179,v179
s_setprio 1
s_waitcnt lgkmcnt(3)
v_mfma_f32_16x16x32_bf16 v[180:183],v[84:87],v[212:215],0
v_mfma_f32_16x16x32_bf16 v[180:183],v[88:91],v[216:219],v[180:183]
v_mfma_f32_16x16x32_bf16 v[180:183],v[92:95],v[220:223],v[180:183]
ds_read_b128 v[212:215],v7 offset:128
ds_read_b128 v[216:219],v11 offset:128
ds_read_b128 v[220:223],v15 offset:128
s_setprio 0
v_and_b32 v84,v32,v47
v_and_b32 v85,v33,v47
v_and_b32 v86,v34,v47
v_and_b32 v87,v35,v47
v_and_b32 v88,v32,v59
v_and_b32 v89,v33,v59
v_and_b32 v90,v34,v59
v_and_b32 v91,v35,v59
v_and_b32 v92,v32,v71
v_and_b32 v93,v33,v71
v_and_b32 v94,v34,v71
v_and_b32 v95,v35,v71
v_add_f32 v232,v232,v180
v_fmac_f32 v237,v180,v180
v_add_f32 v233,v233,v181
v_fmac_f32 v236,v181,v181
v_add_f32 v234,v234,v182
v_fmac_f32 v239,v182,v182
v_add_f32 v235,v235,v183
v_fmac_f32 v238,v183,v183
s_setprio 1
s_waitcnt lgkmcnt(3)
v_mfma_f32_16x16x32_bf16 v[184:187],v[72:75],v[200:203],0
v_mfma_f32_16x16x32_bf16 v[184:187],v[76:79],v[204:207],v[184:187]
v_mfma_f32_16x16x32_bf16 v[184:187],v[80:83],v[208:211],v[184:187]
s_setprio 0
s_nop 7
v_add_f32 v232,v232,v184
v_fmac_f32 v237,v184,v184
v_add_f32 v233,v233,v185
v_fmac_f32 v236,v185,v185
v_add_f32 v234,v234,v186
v_fmac_f32 v239,v186,v186
v_add_f32 v235,v235,v187
v_fmac_f32 v238,v187,v187
s_setprio 1
s_waitcnt lgkmcnt(0)
v_mfma_f32_16x16x32_bf16 v[188:191],v[84:87],v[212:215],0
v_mfma_f32_16x16x32_bf16 v[188:191],v[88:91],v[216:219],v[188:191]
v_mfma_f32_16x16x32_bf16 v[188:191],v[92:95],v[220:223],v[188:191]
s_setprio 0
ds_read_b64_tr_b16 v[200:201],v16 offset:0
ds_read_b64_tr_b16 v[202:203],v16 offset:512
ds_read_b64_tr_b16 v[204:205],v16 offset:1024
ds_read_b64_tr_b16 v[206:207],v16 offset:1536
ds_read_b64_tr_b16 v[208:209],v16 offset:2048
ds_read_b64_tr_b16 v[210:211],v16 offset:2560
ds_read_b64_tr_b16 v[212:213],v16 offset:3072
ds_read_b64_tr_b16 v[214:215],v16 offset:3584
ds_read_b64_tr_b16 v[216:217],v16 offset:4096
ds_read_b64_tr_b16 v[218:219],v16 offset:4608
ds_read_b64_tr_b16 v[220:221],v16 offset:5120
ds_read_b64_tr_b16 v[222:223],v16 offset:5632
v_add_f32 v232,v232,v188
v_fmac_f32 v237,v188,v188
v_add_f32 v233,v233,v189
v_fmac_f32 v236,v189,v189
v_add_f32 v234,v234,v190
v_fmac_f32 v239,v190,v190
v_add_f32 v235,v235,v191
v_fmac_f32 v238,v191,v191
v_add_f32 v232,v232,v233
v_add_f32 v234,v234,v235
v_add_f32 v237,v237,v236
v_add_f32 v239,v239,v238
v_add_f32 v240,v232,v234
v_add_f32 v241,v237,v239
s_nop 1
v_permlane32_swap_b32 v240,v241
v_add_f32 v240,v240,v241
v_mov_b32 v241,v240
s_nop 1
v_permlane32_swap_b32 v240,v241
v_mul_f32 v240,0x3c2aaaab,v240
v_mul_f32 v241,0x3c2aaaab,v241
v_fma_f32 v241,-v240,v240,v241
v_max_f32 v241,0,v241
v_add_f32 v241,0x3727c5ac,v241
v_rsq_f32 v241,v241
s_nop 0
v_mul_f32 v242,0xbeeff558,v241
v_mul_f32 v243,0x3eeff558,v241
v_mul_f32 v243,v240,v243
v_fma_f32 v144,v144,v242,v243
v_fma_f32 v145,v145,v242,v243
v_fma_f32 v146,v146,v242,v243
v_fma_f32 v147,v147,v242,v243
v_fma_f32 v148,v148,v242,v243
v_fma_f32 v149,v149,v242,v243
v_fma_f32 v150,v150,v242,v243
v_fma_f32 v151,v151,v242,v243
v_fma_f32 v152,v152,v242,v243
v_fma_f32 v153,v153,v242,v243
v_fma_f32 v154,v154,v242,v243
v_fma_f32 v155,v155,v242,v243
v_fma_f32 v156,v156,v242,v243
v_fma_f32 v157,v157,v242,v243
v_fma_f32 v158,v158,v242,v243
v_fma_f32 v159,v159,v242,v243
v_fma_f32 v160,v160,v242,v243
v_fma_f32 v161,v161,v242,v243
v_fma_f32 v162,v162,v242,v243
v_fma_f32 v163,v163,v242,v243
v_fma_f32 v164,v164,v242,v243
v_fma_f32 v165,v165,v242,v243
v_fma_f32 v166,v166,v242,v243
v_fma_f32 v167,v167,v242,v243
v_fma_f32 v168,v168,v242,v243
v_fma_f32 v169,v169,v242,v243
v_fma_f32 v170,v170,v242,v243
v_fma_f32 v171,v171,v242,v243
v_fma_f32 v172,v172,v242,v243
v_fma_f32 v173,v173,v242,v243
v_fma_f32 v174,v174,v242,v243
v_fma_f32 v175,v175,v242,v243
v_fma_f32 v176,v176,v242,v243
v_fma_f32 v177,v177,v242,v243
v_fma_f32 v178,v178,v242,v243
v_fma_f32 v179,v179,v242,v243
v_fma_f32 v180,v180,v242,v243
v_fma_f32 v181,v181,v242,v243
v_fma_f32 v182,v182,v242,v243
v_fma_f32 v183,v183,v242,v243
v_fma_f32 v184,v184,v242,v243
v_fma_f32 v185,v185,v242,v243
v_fma_f32 v186,v186,v242,v243
v_fma_f32 v187,v187,v242,v243
v_fma_f32 v188,v188,v242,v243
v_fma_f32 v189,v189,v242,v243
v_fma_f32 v190,v190,v242,v243
v_fma_f32 v191,v191,v242,v243
s_setprio 1
v_cvt_pk_bf16_f32 v72,v144,v145
v_cvt_pk_bf16_f32 v73,v146,v147
v_cvt_pk_bf16_f32 v74,v148,v149
v_cvt_pk_bf16_f32 v75,v150,v151
v_cvt_pk_bf16_f32 v76,v152,v153
v_cvt_pk_bf16_f32 v77,v154,v155
v_cvt_pk_bf16_f32 v78,v156,v157
v_cvt_pk_bf16_f32 v79,v158,v159
ds_read_b128 v[144:147],v18 offset:0
ds_read_b128 v[148:151],v18 offset:32
ds_read_b128 v[152:155],v18 offset:64
ds_read_b128 v[156:159],v18 offset:96
v_cvt_pk_bf16_f32 v80,v160,v161
v_cvt_pk_bf16_f32 v81,v162,v163
v_cvt_pk_bf16_f32 v82,v164,v165
v_cvt_pk_bf16_f32 v83,v166,v167
v_cvt_pk_bf16_f32 v84,v168,v169
v_cvt_pk_bf16_f32 v85,v170,v171
v_cvt_pk_bf16_f32 v86,v172,v173
v_cvt_pk_bf16_f32 v87,v174,v175
s_waitcnt lgkmcnt(0)
v_mfma_f32_32x32x16_bf16 v[144:159],v[200:203],v[72:75],v[144:159]
v_cvt_pk_bf16_f32 v88,v176,v177
v_cvt_pk_bf16_f32 v89,v178,v179
v_cvt_pk_bf16_f32 v90,v180,v181
v_cvt_pk_bf16_f32 v91,v182,v183
v_mfma_f32_32x32x16_bf16 v[144:159],v[204:207],v[76:79],v[144:159]
v_cvt_pk_bf16_f32 v92,v184,v185
v_cvt_pk_bf16_f32 v93,v186,v187
v_cvt_pk_bf16_f32 v94,v188,v189
v_cvt_pk_bf16_f32 v95,v190,v191
v_mfma_f32_32x32x16_bf16 v[144:159],v[208:211],v[80:83],v[144:159]
v_mfma_f32_32x32x16_bf16 v[144:159],v[212:215],v[84:87],v[144:159]
v_mfma_f32_32x32x16_bf16 v[144:159],v[216:219],v[88:91],v[144:159]
v_mfma_f32_32x32x16_bf16 v[144:159],v[220:223],v[92:95],v[144:159]
s_setprio 0
s_nop 7
s_nop 1
s_waitcnt vmcnt(2)
s_barrier
v_fmaak_f32 v176,v144,v144,0x409d30fc
v_fmaak_f32 v177,v145,v145,0x409d30fc
v_fmaak_f32 v178,v146,v146,0x409d30fc
v_fmaak_f32 v179,v147,v147,0x409d30fc
v_fmaak_f32 v180,v148,v148,0x409d30fc
v_fmaak_f32 v181,v149,v149,0x409d30fc
v_fmaak_f32 v182,v150,v150,0x409d30fc
v_fmaak_f32 v183,v151,v151,0x409d30fc
v_fmaak_f32 v184,v152,v152,0x409d30fc
v_fmaak_f32 v185,v153,v153,0x409d30fc
v_fmaak_f32 v186,v154,v154,0x409d30fc
v_fmaak_f32 v187,v155,v155,0x409d30fc
v_fmaak_f32 v188,v156,v156,0x409d30fc
v_fmaak_f32 v189,v157,v157,0x409d30fc
v_fmaak_f32 v190,v158,v158,0x409d30fc
v_fmaak_f32 v191,v159,v159,0x409d30fc
v_mul_f32 v176,v144,v176
v_mul_f32 v177,v145,v177
v_mul_f32 v178,v146,v178
v_mul_f32 v179,v147,v179
v_mul_f32 v180,v148,v180
v_mul_f32 v181,v149,v181
v_mul_f32 v182,v150,v182
v_mul_f32 v183,v151,v183
v_mul_f32 v184,v152,v184
v_mul_f32 v185,v153,v185
v_mul_f32 v186,v154,v186
v_mul_f32 v187,v155,v187
v_mul_f32 v188,v156,v188
v_mul_f32 v189,v157,v189
v_mul_f32 v190,v158,v190
v_mul_f32 v191,v159,v191
s_setprio 1
v_exp_f32 v176,v176
v_exp_f32 v177,v177
v_exp_f32 v178,v178
v_exp_f32 v179,v179
v_exp_f32 v180,v180
v_exp_f32 v181,v181
v_exp_f32 v182,v182
v_exp_f32 v183,v183
v_exp_f32 v184,v184
v_exp_f32 v185,v185
v_exp_f32 v186,v186
v_exp_f32 v187,v187
v_exp_f32 v188,v188
v_exp_f32 v189,v189
v_exp_f32 v190,v190
v_exp_f32 v191,v191
ds_read_b64_tr_b16 v[200:201],v16 offset:6144
ds_read_b64_tr_b16 v[202:203],v16 offset:6656
ds_read_b64_tr_b16 v[204:205],v16 offset:7168
ds_read_b64_tr_b16 v[206:207],v16 offset:7680
ds_read_b64_tr_b16 v[208:209],v16 offset:8192
ds_read_b64_tr_b16 v[210:211],v16 offset:8704
ds_read_b64_tr_b16 v[212:213],v16 offset:9216
ds_read_b64_tr_b16 v[214:215],v16 offset:9728
ds_read_b64_tr_b16 v[216:217],v16 offset:10240
ds_read_b64_tr_b16 v[218:219],v16 offset:10752
ds_read_b64_tr_b16 v[220:221],v16 offset:11264
ds_read_b64_tr_b16 v[222:223],v16 offset:11776
ds_read_b128 v[160:163],v18 offset:128
ds_read_b128 v[164:167],v18 offset:160
ds_read_b128 v[168:171],v18 offset:192
ds_read_b128 v[172:175],v18 offset:224
s_mov_b64 exec,s[60:61]
s_add_u32 m0,s58,148992
s_mov_b32 s64,6144
buffer_load_dwordx4 v28,s[48:51],s64 offen lds
s_add_u32 m0,s58,149760
s_mov_b32 s65,6912
buffer_load_dwordx4 v28,s[48:51],s65 offen lds
s_mov_b64 exec,-1
s_setprio 0
v_add_f32 v176,1.0,v176
v_add_f32 v177,1.0,v177
v_add_f32 v178,1.0,v178
v_add_f32 v179,1.0,v179
v_add_f32 v180,1.0,v180
v_add_f32 v181,1.0,v181
v_add_f32 v182,1.0,v182
v_add_f32 v183,1.0,v183
v_add_f32 v184,1.0,v184
v_add_f32 v185,1.0,v185
v_add_f32 v186,1.0,v186
v_add_f32 v187,1.0,v187
v_add_f32 v188,1.0,v188
v_add_f32 v189,1.0,v189
v_add_f32 v190,1.0,v190
v_add_f32 v191,1.0,v191
s_setprio 1
s_waitcnt lgkmcnt(0)
v_mfma_f32_32x32x16_bf16 v[160:175],v[200:203],v[72:75],v[160:175]
v_rcp_f32 v176,v176
v_rcp_f32 v177,v177
v_rcp_f32 v178,v178
v_mfma_f32_32x32x16_bf16 v[160:175],v[204:207],v[76:79],v[160:175]
v_rcp_f32 v179,v179
v_rcp_f32 v180,v180
v_rcp_f32 v181,v181
v_mfma_f32_32x32x16_bf16 v[160:175],v[208:211],v[80:83],v[160:175]
v_rcp_f32 v182,v182
v_rcp_f32 v183,v183
v_rcp_f32 v184,v184
v_mfma_f32_32x32x16_bf16 v[160:175],v[212:215],v[84:87],v[160:175]
v_rcp_f32 v185,v185
v_rcp_f32 v186,v186
v_rcp_f32 v187,v187
v_mfma_f32_32x32x16_bf16 v[160:175],v[216:219],v[88:91],v[160:175]
v_rcp_f32 v188,v188
v_rcp_f32 v189,v189
v_mfma_f32_32x32x16_bf16 v[160:175],v[220:223],v[92:95],v[160:175]
v_rcp_f32 v190,v190
v_rcp_f32 v191,v191
ds_read_b64_tr_b16 v[200:201],v17 offset:0
ds_read_b64_tr_b16 v[202:203],v17 offset:1536
ds_read_b64_tr_b16 v[204:205],v17 offset:3072
ds_read_b64_tr_b16 v[206:207],v17 offset:4608
ds_read_b64_tr_b16 v[208:209],v17 offset:64
ds_read_b64_tr_b16 v[210:211],v17 offset:1600
ds_read_b64_tr_b16 v[212:213],v17 offset:3136
ds_read_b64_tr_b16 v[214:215],v17 offset:4672
ds_read_b64_tr_b16 v[216:217],v17 offset:128
ds_read_b64_tr_b16 v[218:219],v17 offset:1664
ds_read_b64_tr_b16 v[220:221],v17 offset:3200
ds_read_b64_tr_b16 v[222:223],v17 offset:4736
s_mov_b64 exec,s[60:61]
s_add_u32 m0,s58,124416
s_movk_i32 s64,192
buffer_load_dwordx4 v27,s[44:47],s64 offen lds
s_add_u32 m0,s58,125184
s_movk_i32 s65,9408
buffer_load_dwordx4 v27,s[44:47],s65 offen lds
s_mov_b64 exec,-1
s_setprio 0
v_mul_f32 v176,v144,v176
v_mul_f32 v177,v145,v177
v_mul_f32 v178,v146,v178
v_mul_f32 v179,v147,v179
v_mul_f32 v180,v148,v180
v_mul_f32 v181,v149,v181
v_mul_f32 v182,v150,v182
v_mul_f32 v183,v151,v183
v_mul_f32 v184,v152,v184
v_mul_f32 v185,v153,v185
v_mul_f32 v186,v154,v186
v_mul_f32 v187,v155,v187
v_mul_f32 v188,v156,v188
v_mul_f32 v189,v157,v189
v_mul_f32 v190,v158,v190
v_mul_f32 v191,v159,v191
v_cvt_pk_bf16_f32 v192,v176,v177
v_cvt_pk_bf16_f32 v193,v178,v179
v_cvt_pk_bf16_f32 v194,v180,v181
v_cvt_pk_bf16_f32 v195,v182,v183
v_cvt_pk_bf16_f32 v196,v184,v185
v_cvt_pk_bf16_f32 v197,v186,v187
v_cvt_pk_bf16_f32 v198,v188,v189
v_cvt_pk_bf16_f32 v199,v190,v191
s_waitcnt lgkmcnt(0)
s_waitcnt vmcnt(2)
s_barrier
v_fmaak_f32 v176,v160,v160,0x409d30fc
v_fmaak_f32 v177,v161,v161,0x409d30fc
v_fmaak_f32 v178,v162,v162,0x409d30fc
v_fmaak_f32 v179,v163,v163,0x409d30fc
v_fmaak_f32 v180,v164,v164,0x409d30fc
v_fmaak_f32 v181,v165,v165,0x409d30fc
v_fmaak_f32 v182,v166,v166,0x409d30fc
v_fmaak_f32 v183,v167,v167,0x409d30fc
v_fmaak_f32 v184,v168,v168,0x409d30fc
v_fmaak_f32 v185,v169,v169,0x409d30fc
v_fmaak_f32 v186,v170,v170,0x409d30fc
v_fmaak_f32 v187,v171,v171,0x409d30fc
v_fmaak_f32 v188,v172,v172,0x409d30fc
v_fmaak_f32 v189,v173,v173,0x409d30fc
v_fmaak_f32 v190,v174,v174,0x409d30fc
v_fmaak_f32 v191,v175,v175,0x409d30fc
v_mul_f32 v176,v160,v176
v_mul_f32 v177,v161,v177
v_mul_f32 v178,v162,v178
v_mul_f32 v179,v163,v179
v_mul_f32 v180,v164,v180
v_mul_f32 v181,v165,v181
v_mul_f32 v182,v166,v182
v_mul_f32 v183,v167,v183
v_mul_f32 v184,v168,v184
v_mul_f32 v185,v169,v185
v_mul_f32 v186,v170,v186
v_mul_f32 v187,v171,v187
v_mul_f32 v188,v172,v188
v_mul_f32 v189,v173,v189
v_mul_f32 v190,v174,v190
v_mul_f32 v191,v175,v191
s_setprio 1
v_mfma_f32_32x32x16_bf16 v[96:111],v[200:203],v[192:195],v[96:111]
v_exp_f32 v176,v176
v_exp_f32 v177,v177
v_exp_f32 v178,v178
v_mfma_f32_32x32x16_bf16 v[96:111],v[204:207],v[196:199],v[96:111]
v_exp_f32 v179,v179
v_exp_f32 v180,v180
v_exp_f32 v181,v181
v_mfma_f32_32x32x16_bf16 v[112:127],v[208:211],v[192:195],v[112:127]
v_exp_f32 v182,v182
v_exp_f32 v183,v183
v_exp_f32 v184,v184
v_mfma_f32_32x32x16_bf16 v[112:127],v[212:215],v[196:199],v[112:127]
v_exp_f32 v185,v185
v_exp_f32 v186,v186
v_exp_f32 v187,v187
v_mfma_f32_32x32x16_bf16 v[128:143],v[216:219],v[192:195],v[128:143]
v_exp_f32 v188,v188
v_exp_f32 v189,v189
v_mfma_f32_32x32x16_bf16 v[128:143],v[220:223],v[196:199],v[128:143]
v_exp_f32 v190,v190
v_exp_f32 v191,v191
ds_read_b64_tr_b16 v[200:201],v16 offset:12288
ds_read_b64_tr_b16 v[202:203],v16 offset:12800
ds_read_b64_tr_b16 v[204:205],v16 offset:13312
ds_read_b64_tr_b16 v[206:207],v16 offset:13824
ds_read_b64_tr_b16 v[208:209],v16 offset:14336
ds_read_b64_tr_b16 v[210:211],v16 offset:14848
ds_read_b64_tr_b16 v[212:213],v16 offset:15360
ds_read_b64_tr_b16 v[214:215],v16 offset:15872
ds_read_b64_tr_b16 v[216:217],v16 offset:16384
ds_read_b64_tr_b16 v[218:219],v16 offset:16896
ds_read_b64_tr_b16 v[220:221],v16 offset:17408
ds_read_b64_tr_b16 v[222:223],v16 offset:17920
ds_read_b128 v[144:147],v18 offset:256
ds_read_b128 v[148:151],v18 offset:288
ds_read_b128 v[152:155],v18 offset:320
ds_read_b128 v[156:159],v18 offset:352
s_mov_b64 exec,s[60:61]
s_add_u32 m0,s58,155136
s_mov_b32 s64,12288
buffer_load_dwordx4 v28,s[48:51],s64 offen lds
s_add_u32 m0,s58,155904
s_mov_b32 s65,13056
buffer_load_dwordx4 v28,s[48:51],s65 offen lds
s_mov_b64 exec,-1
s_setprio 0
v_add_f32 v176,1.0,v176
v_add_f32 v177,1.0,v177
v_add_f32 v178,1.0,v178
v_add_f32 v179,1.0,v179
v_add_f32 v180,1.0,v180
v_add_f32 v181,1.0,v181
v_add_f32 v182,1.0,v182
v_add_f32 v183,1.0,v183
v_add_f32 v184,1.0,v184
v_add_f32 v185,1.0,v185
v_add_f32 v186,1.0,v186
v_add_f32 v187,1.0,v187
v_add_f32 v188,1.0,v188
v_add_f32 v189,1.0,v189
v_add_f32 v190,1.0,v190
v_add_f32 v191,1.0,v191
s_setprio 1
s_waitcnt lgkmcnt(0)
v_mfma_f32_32x32x16_bf16 v[144:159],v[200:203],v[72:75],v[144:159]
v_rcp_f32 v176,v176
v_rcp_f32 v177,v177
v_rcp_f32 v178,v178
v_mfma_f32_32x32x16_bf16 v[144:159],v[204:207],v[76:79],v[144:159]
v_rcp_f32 v179,v179
v_rcp_f32 v180,v180
v_rcp_f32 v181,v181
v_mfma_f32_32x32x16_bf16 v[144:159],v[208:211],v[80:83],v[144:159]
v_rcp_f32 v182,v182
v_rcp_f32 v183,v183
v_rcp_f32 v184,v184
v_mfma_f32_32x32x16_bf16 v[144:159],v[212:215],v[84:87],v[144:159]
v_rcp_f32 v185,v185
v_rcp_f32 v186,v186
v_rcp_f32 v187,v187
v_mfma_f32_32x32x16_bf16 v[144:159],v[216:219],v[88:91],v[144:159]
v_rcp_f32 v188,v188
v_rcp_f32 v189,v189
v_mfma_f32_32x32x16_bf16 v[144:159],v[220:223],v[92:95],v[144:159]
v_rcp_f32 v190,v190
v_rcp_f32 v191,v191
ds_read_b64_tr_b16 v[200:201],v17 offset:6144
ds_read_b64_tr_b16 v[202:203],v17 offset:7680
ds_read_b64_tr_b16 v[204:205],v17 offset:9216
ds_read_b64_tr_b16 v[206:207],v17 offset:10752
ds_read_b64_tr_b16 v[208:209],v17 offset:6208
ds_read_b64_tr_b16 v[210:211],v17 offset:7744
ds_read_b64_tr_b16 v[212:213],v17 offset:9280
ds_read_b64_tr_b16 v[214:215],v17 offset:10816
ds_read_b64_tr_b16 v[216:217],v17 offset:6272
ds_read_b64_tr_b16 v[218:219],v17 offset:7808
ds_read_b64_tr_b16 v[220:221],v17 offset:9344
ds_read_b64_tr_b16 v[222:223],v17 offset:10880
s_mov_b64 exec,s[60:61]
s_add_u32 m0,s58,130560
s_movk_i32 s64,256
buffer_load_dwordx4 v27,s[44:47],s64 offen lds
s_add_u32 m0,s58,131328
s_movk_i32 s65,9472
buffer_load_dwordx4 v27,s[44:47],s65 offen lds
s_mov_b64 exec,-1
s_setprio 0
v_mul_f32 v176,v160,v176
v_mul_f32 v177,v161,v177
v_mul_f32 v178,v162,v178
v_mul_f32 v179,v163,v179
v_mul_f32 v180,v164,v180
v_mul_f32 v181,v165,v181
v_mul_f32 v182,v166,v182
v_mul_f32 v183,v167,v183
v_mul_f32 v184,v168,v184
v_mul_f32 v185,v169,v185
v_mul_f32 v186,v170,v186
v_mul_f32 v187,v171,v187
v_mul_f32 v188,v172,v188
v_mul_f32 v189,v173,v189
v_mul_f32 v190,v174,v190
v_mul_f32 v191,v175,v191
v_cvt_pk_bf16_f32 v192,v176,v177
v_cvt_pk_bf16_f32 v193,v178,v179
v_cvt_pk_bf16_f32 v194,v180,v181
v_cvt_pk_bf16_f32 v195,v182,v183
v_cvt_pk_bf16_f32 v196,v184,v185
v_cvt_pk_bf16_f32 v197,v186,v187
v_cvt_pk_bf16_f32 v198,v188,v189
v_cvt_pk_bf16_f32 v199,v190,v191
s_waitcnt lgkmcnt(0)
s_waitcnt vmcnt(2)
s_barrier
v_fmaak_f32 v176,v144,v144,0x409d30fc
v_fmaak_f32 v177,v145,v145,0x409d30fc
v_fmaak_f32 v178,v146,v146,0x409d30fc
v_fmaak_f32 v179,v147,v147,0x409d30fc
v_fmaak_f32 v180,v148,v148,0x409d30fc
v_fmaak_f32 v181,v149,v149,0x409d30fc
v_fmaak_f32 v182,v150,v150,0x409d30fc
v_fmaak_f32 v183,v151,v151,0x409d30fc
v_fmaak_f32 v184,v152,v152,0x409d30fc
v_fmaak_f32 v185,v153,v153,0x409d30fc
v_fmaak_f32 v186,v154,v154,0x409d30fc
v_fmaak_f32 v187,v155,v155,0x409d30fc
v_fmaak_f32 v188,v156,v156,0x409d30fc
v_fmaak_f32 v189,v157,v157,0x409d30fc
v_fmaak_f32 v190,v158,v158,0x409d30fc
v_fmaak_f32 v191,v159,v159,0x409d30fc
v_mul_f32 v176,v144,v176
v_mul_f32 v177,v145,v177
v_mul_f32 v178,v146,v178
v_mul_f32 v179,v147,v179
v_mul_f32 v180,v148,v180
v_mul_f32 v181,v149,v181
v_mul_f32 v182,v150,v182
v_mul_f32 v183,v151,v183
v_mul_f32 v184,v152,v184
v_mul_f32 v185,v153,v185
v_mul_f32 v186,v154,v186
v_mul_f32 v187,v155,v187
v_mul_f32 v188,v156,v188
v_mul_f32 v189,v157,v189
v_mul_f32 v190,v158,v190
v_mul_f32 v191,v159,v191
s_setprio 1
v_mfma_f32_32x32x16_bf16 v[96:111],v[200:203],v[192:195],v[96:111]
v_exp_f32 v176,v176
v_exp_f32 v177,v177
v_exp_f32 v178,v178
v_mfma_f32_32x32x16_bf16 v[96:111],v[204:207],v[196:199],v[96:111]
v_exp_f32 v179,v179
v_exp_f32 v180,v180
v_exp_f32 v181,v181
v_mfma_f32_32x32x16_bf16 v[112:127],v[208:211],v[192:195],v[112:127]
v_exp_f32 v182,v182
v_exp_f32 v183,v183
v_exp_f32 v184,v184
v_mfma_f32_32x32x16_bf16 v[112:127],v[212:215],v[196:199],v[112:127]
v_exp_f32 v185,v185
v_exp_f32 v186,v186
v_exp_f32 v187,v187
v_mfma_f32_32x32x16_bf16 v[128:143],v[216:219],v[192:195],v[128:143]
v_exp_f32 v188,v188
v_exp_f32 v189,v189
v_mfma_f32_32x32x16_bf16 v[128:143],v[220:223],v[196:199],v[128:143]
v_exp_f32 v190,v190
v_exp_f32 v191,v191
ds_read_b64_tr_b16 v[200:201],v16 offset:0
ds_read_b64_tr_b16 v[202:203],v16 offset:512
ds_read_b64_tr_b16 v[204:205],v16 offset:1024
ds_read_b64_tr_b16 v[206:207],v16 offset:1536
ds_read_b64_tr_b16 v[208:209],v16 offset:2048
ds_read_b64_tr_b16 v[210:211],v16 offset:2560
ds_read_b64_tr_b16 v[212:213],v16 offset:3072
ds_read_b64_tr_b16 v[214:215],v16 offset:3584
ds_read_b64_tr_b16 v[216:217],v16 offset:4096
ds_read_b64_tr_b16 v[218:219],v16 offset:4608
ds_read_b64_tr_b16 v[220:221],v16 offset:5120
ds_read_b64_tr_b16 v[222:223],v16 offset:5632
ds_read_b128 v[160:163],v18 offset:384
ds_read_b128 v[164:167],v18 offset:416
ds_read_b128 v[168:171],v18 offset:448
ds_read_b128 v[172:175],v18 offset:480
s_mov_b64 exec,s[60:61]
s_add_u32 m0,s58,142848
s_mov_b32 s64,18432
buffer_load_dwordx4 v28,s[48:51],s64 offen lds
s_add_u32 m0,s58,143616
s_mov_b32 s65,19200
buffer_load_dwordx4 v28,s[48:51],s65 offen lds
s_mov_b64 exec,-1
s_setprio 0
v_add_f32 v176,1.0,v176
v_add_f32 v177,1.0,v177
v_add_f32 v178,1.0,v178
v_add_f32 v179,1.0,v179
v_add_f32 v180,1.0,v180
v_add_f32 v181,1.0,v181
v_add_f32 v182,1.0,v182
v_add_f32 v183,1.0,v183
v_add_f32 v184,1.0,v184
v_add_f32 v185,1.0,v185
v_add_f32 v186,1.0,v186
v_add_f32 v187,1.0,v187
v_add_f32 v188,1.0,v188
v_add_f32 v189,1.0,v189
v_add_f32 v190,1.0,v190
v_add_f32 v191,1.0,v191
s_setprio 1
s_waitcnt lgkmcnt(0)
v_mfma_f32_32x32x16_bf16 v[160:175],v[200:203],v[72:75],v[160:175]
v_rcp_f32 v176,v176
v_rcp_f32 v177,v177
v_rcp_f32 v178,v178
v_mfma_f32_32x32x16_bf16 v[160:175],v[204:207],v[76:79],v[160:175]
v_rcp_f32 v179,v179
v_rcp_f32 v180,v180
v_rcp_f32 v181,v181
v_mfma_f32_32x32x16_bf16 v[160:175],v[208:211],v[80:83],v[160:175]
v_rcp_f32 v182,v182
v_rcp_f32 v183,v183
v_rcp_f32 v184,v184
v_mfma_f32_32x32x16_bf16 v[160:175],v[212:215],v[84:87],v[160:175]
v_rcp_f32 v185,v185
v_rcp_f32 v186,v186
v_rcp_f32 v187,v187
v_mfma_f32_32x32x16_bf16 v[160:175],v[216:219],v[88:91],v[160:175]
v_rcp_f32 v188,v188
v_rcp_f32 v189,v189
v_mfma_f32_32x32x16_bf16 v[160:175],v[220:223],v[92:95],v[160:175]
v_rcp_f32 v190,v190
v_rcp_f32 v191,v191
ds_read_b64_tr_b16 v[200:201],v17 offset:12288
ds_read_b64_tr_b16 v[202:203],v17 offset:13824
ds_read_b64_tr_b16 v[204:205],v17 offset:15360
ds_read_b64_tr_b16 v[206:207],v17 offset:16896
ds_read_b64_tr_b16 v[208:209],v17 offset:12352
ds_read_b64_tr_b16 v[210:211],v17 offset:13888
ds_read_b64_tr_b16 v[212:213],v17 offset:15424
ds_read_b64_tr_b16 v[214:215],v17 offset:16960
ds_read_b64_tr_b16 v[216:217],v17 offset:12416
ds_read_b64_tr_b16 v[218:219],v17 offset:13952
ds_read_b64_tr_b16 v[220:221],v17 offset:15488
ds_read_b64_tr_b16 v[222:223],v17 offset:17024
s_mov_b64 exec,s[60:61]
s_add_u32 m0,s58,136704
s_movk_i32 s64,320
buffer_load_dwordx4 v27,s[44:47],s64 offen lds
s_add_u32 m0,s58,137472
s_movk_i32 s65,9536
buffer_load_dwordx4 v27,s[44:47],s65 offen lds
s_mov_b64 exec,-1
s_setprio 0
v_mul_f32 v176,v144,v176
v_mul_f32 v177,v145,v177
v_mul_f32 v178,v146,v178
v_mul_f32 v179,v147,v179
v_mul_f32 v180,v148,v180
v_mul_f32 v181,v149,v181
v_mul_f32 v182,v150,v182
v_mul_f32 v183,v151,v183
v_mul_f32 v184,v152,v184
v_mul_f32 v185,v153,v185
v_mul_f32 v186,v154,v186
v_mul_f32 v187,v155,v187
v_mul_f32 v188,v156,v188
v_mul_f32 v189,v157,v189
v_mul_f32 v190,v158,v190
v_mul_f32 v191,v159,v191
v_cvt_pk_bf16_f32 v192,v176,v177
v_cvt_pk_bf16_f32 v193,v178,v179
v_cvt_pk_bf16_f32 v194,v180,v181
v_cvt_pk_bf16_f32 v195,v182,v183
v_cvt_pk_bf16_f32 v196,v184,v185
v_cvt_pk_bf16_f32 v197,v186,v187
v_cvt_pk_bf16_f32 v198,v188,v189
v_cvt_pk_bf16_f32 v199,v190,v191
s_waitcnt lgkmcnt(0)
s_waitcnt vmcnt(2)
s_barrier
v_fmaak_f32 v176,v160,v160,0x409d30fc
v_fmaak_f32 v177,v161,v161,0x409d30fc
v_fmaak_f32 v178,v162,v162,0x409d30fc
v_fmaak_f32 v179,v163,v163,0x409d30fc
v_fmaak_f32 v180,v164,v164,0x409d30fc
v_fmaak_f32 v181,v165,v165,0x409d30fc
v_fmaak_f32 v182,v166,v166,0x409d30fc
v_fmaak_f32 v183,v167,v167,0x409d30fc
v_fmaak_f32 v184,v168,v168,0x409d30fc
v_fmaak_f32 v185,v169,v169,0x409d30fc
v_fmaak_f32 v186,v170,v170,0x409d30fc
v_fmaak_f32 v187,v171,v171,0x409d30fc
v_fmaak_f32 v188,v172,v172,0x409d30fc
v_fmaak_f32 v189,v173,v173,0x409d30fc
v_fmaak_f32 v190,v174,v174,0x409d30fc
v_fmaak_f32 v191,v175,v175,0x409d30fc
v_mul_f32 v176,v160,v176
v_mul_f32 v177,v161,v177
v_mul_f32 v178,v162,v178
v_mul_f32 v179,v163,v179
v_mul_f32 v180,v164,v180
v_mul_f32 v181,v165,v181
v_mul_f32 v182,v166,v182
v_mul_f32 v183,v167,v183
v_mul_f32 v184,v168,v184
v_mul_f32 v185,v169,v185
v_mul_f32 v186,v170,v186
v_mul_f32 v187,v171,v187
v_mul_f32 v188,v172,v188
v_mul_f32 v189,v173,v189
v_mul_f32 v190,v174,v190
v_mul_f32 v191,v175,v191
s_setprio 1
v_mfma_f32_32x32x16_bf16 v[96:111],v[200:203],v[192:195],v[96:111]
v_exp_f32 v176,v176
v_exp_f32 v177,v177
v_exp_f32 v178,v178
v_mfma_f32_32x32x16_bf16 v[96:111],v[204:207],v[196:199],v[96:111]
v_exp_f32 v179,v179
v_exp_f32 v180,v180
v_exp_f32 v181,v181
v_mfma_f32_32x32x16_bf16 v[112:127],v[208:211],v[192:195],v[112:127]
v_exp_f32 v182,v182
v_exp_f32 v183,v183
v_exp_f32 v184,v184
v_mfma_f32_32x32x16_bf16 v[112:127],v[212:215],v[196:199],v[112:127]
v_exp_f32 v185,v185
v_exp_f32 v186,v186
v_exp_f32 v187,v187
v_mfma_f32_32x32x16_bf16 v[128:143],v[216:219],v[192:195],v[128:143]
v_exp_f32 v188,v188
v_exp_f32 v189,v189
v_mfma_f32_32x32x16_bf16 v[128:143],v[220:223],v[196:199],v[128:143]
v_exp_f32 v190,v190
v_exp_f32 v191,v191
ds_read_b64_tr_b16 v[200:201],v16 offset:6144
ds_read_b64_tr_b16 v[202:203],v16 offset:6656
ds_read_b64_tr_b16 v[204:205],v16 offset:7168
ds_read_b64_tr_b16 v[206:207],v16 offset:7680
ds_read_b64_tr_b16 v[208:209],v16 offset:8192
ds_read_b64_tr_b16 v[210:211],v16 offset:8704
ds_read_b64_tr_b16 v[212:213],v16 offset:9216
ds_read_b64_tr_b16 v[214:215],v16 offset:9728
ds_read_b64_tr_b16 v[216:217],v16 offset:10240
ds_read_b64_tr_b16 v[218:219],v16 offset:10752
ds_read_b64_tr_b16 v[220:221],v16 offset:11264
ds_read_b64_tr_b16 v[222:223],v16 offset:11776
ds_read_b128 v[144:147],v18 offset:512
ds_read_b128 v[148:151],v18 offset:544
ds_read_b128 v[152:155],v18 offset:576
ds_read_b128 v[156:159],v18 offset:608
s_mov_b64 exec,s[60:61]
s_add_u32 m0,s58,148992
s_mov_b32 s64,24576
buffer_load_dwordx4 v28,s[48:51],s64 offen lds
s_add_u32 m0,s58,149760
s_mov_b32 s65,25344
buffer_load_dwordx4 v28,s[48:51],s65 offen lds
s_mov_b64 exec,-1
s_setprio 0
v_add_f32 v176,1.0,v176
v_add_f32 v177,1.0,v177
v_add_f32 v178,1.0,v178
v_add_f32 v179,1.0,v179
v_add_f32 v180,1.0,v180
v_add_f32 v181,1.0,v181
v_add_f32 v182,1.0,v182
v_add_f32 v183,1.0,v183
v_add_f32 v184,1.0,v184
v_add_f32 v185,1.0,v185
v_add_f32 v186,1.0,v186
v_add_f32 v187,1.0,v187
v_add_f32 v188,1.0,v188
v_add_f32 v189,1.0,v189
v_add_f32 v190,1.0,v190
v_add_f32 v191,1.0,v191
s_setprio 1
s_waitcnt lgkmcnt(0)
v_mfma_f32_32x32x16_bf16 v[144:159],v[200:203],v[72:75],v[144:159]
v_rcp_f32 v176,v176
v_rcp_f32 v177,v177
v_rcp_f32 v178,v178
v_mfma_f32_32x32x16_bf16 v[144:159],v[204:207],v[76:79],v[144:159]
v_rcp_f32 v179,v179
v_rcp_f32 v180,v180
v_rcp_f32 v181,v181
v_mfma_f32_32x32x16_bf16 v[144:159],v[208:211],v[80:83],v[144:159]
v_rcp_f32 v182,v182
v_rcp_f32 v183,v183
v_rcp_f32 v184,v184
v_mfma_f32_32x32x16_bf16 v[144:159],v[212:215],v[84:87],v[144:159]
v_rcp_f32 v185,v185
v_rcp_f32 v186,v186
v_rcp_f32 v187,v187
v_mfma_f32_32x32x16_bf16 v[144:159],v[216:219],v[88:91],v[144:159]
v_rcp_f32 v188,v188
v_rcp_f32 v189,v189
v_mfma_f32_32x32x16_bf16 v[144:159],v[220:223],v[92:95],v[144:159]
v_rcp_f32 v190,v190
v_rcp_f32 v191,v191
ds_read_b64_tr_b16 v[200:201],v17 offset:0
ds_read_b64_tr_b16 v[202:203],v17 offset:1536
ds_read_b64_tr_b16 v[204:205],v17 offset:3072
ds_read_b64_tr_b16 v[206:207],v17 offset:4608
ds_read_b64_tr_b16 v[208:209],v17 offset:64
ds_read_b64_tr_b16 v[210:211],v17 offset:1600
ds_read_b64_tr_b16 v[212:213],v17 offset:3136
ds_read_b64_tr_b16 v[214:215],v17 offset:4672
ds_read_b64_tr_b16 v[216:217],v17 offset:128
ds_read_b64_tr_b16 v[218:219],v17 offset:1664
ds_read_b64_tr_b16 v[220:221],v17 offset:3200
ds_read_b64_tr_b16 v[222:223],v17 offset:4736
s_mov_b64 exec,s[60:61]
s_add_u32 m0,s58,124416
s_movk_i32 s64,384
buffer_load_dwordx4 v27,s[44:47],s64 offen lds
s_add_u32 m0,s58,125184
s_movk_i32 s65,9600
buffer_load_dwordx4 v27,s[44:47],s65 offen lds
s_mov_b64 exec,-1
s_setprio 0
v_mul_f32 v176,v160,v176
v_mul_f32 v177,v161,v177
v_mul_f32 v178,v162,v178
v_mul_f32 v179,v163,v179
v_mul_f32 v180,v164,v180
v_mul_f32 v181,v165,v181
v_mul_f32 v182,v166,v182
v_mul_f32 v183,v167,v183
v_mul_f32 v184,v168,v184
v_mul_f32 v185,v169,v185
v_mul_f32 v186,v170,v186
v_mul_f32 v187,v171,v187
v_mul_f32 v188,v172,v188
v_mul_f32 v189,v173,v189
v_mul_f32 v190,v174,v190
v_mul_f32 v191,v175,v191
v_cvt_pk_bf16_f32 v192,v176,v177
v_cvt_pk_bf16_f32 v193,v178,v179
v_cvt_pk_bf16_f32 v194,v180,v181
v_cvt_pk_bf16_f32 v195,v182,v183
v_cvt_pk_bf16_f32 v196,v184,v185
v_cvt_pk_bf16_f32 v197,v186,v187
v_cvt_pk_bf16_f32 v198,v188,v189
v_cvt_pk_bf16_f32 v199,v190,v191
s_waitcnt lgkmcnt(0)
s_waitcnt vmcnt(2)
s_barrier
v_fmaak_f32 v176,v144,v144,0x409d30fc
v_fmaak_f32 v177,v145,v145,0x409d30fc
v_fmaak_f32 v178,v146,v146,0x409d30fc
v_fmaak_f32 v179,v147,v147,0x409d30fc
v_fmaak_f32 v180,v148,v148,0x409d30fc
v_fmaak_f32 v181,v149,v149,0x409d30fc
v_fmaak_f32 v182,v150,v150,0x409d30fc
v_fmaak_f32 v183,v151,v151,0x409d30fc
v_fmaak_f32 v184,v152,v152,0x409d30fc
v_fmaak_f32 v185,v153,v153,0x409d30fc
v_fmaak_f32 v186,v154,v154,0x409d30fc
v_fmaak_f32 v187,v155,v155,0x409d30fc
v_fmaak_f32 v188,v156,v156,0x409d30fc
v_fmaak_f32 v189,v157,v157,0x409d30fc
v_fmaak_f32 v190,v158,v158,0x409d30fc
v_fmaak_f32 v191,v159,v159,0x409d30fc
v_mul_f32 v176,v144,v176
v_mul_f32 v177,v145,v177
v_mul_f32 v178,v146,v178
v_mul_f32 v179,v147,v179
v_mul_f32 v180,v148,v180
v_mul_f32 v181,v149,v181
v_mul_f32 v182,v150,v182
v_mul_f32 v183,v151,v183
v_mul_f32 v184,v152,v184
v_mul_f32 v185,v153,v185
v_mul_f32 v186,v154,v186
v_mul_f32 v187,v155,v187
v_mul_f32 v188,v156,v188
v_mul_f32 v189,v157,v189
v_mul_f32 v190,v158,v190
v_mul_f32 v191,v159,v191
s_setprio 1
v_mfma_f32_32x32x16_bf16 v[96:111],v[200:203],v[192:195],v[96:111]
v_exp_f32 v176,v176
v_exp_f32 v177,v177
v_exp_f32 v178,v178
v_mfma_f32_32x32x16_bf16 v[96:111],v[204:207],v[196:199],v[96:111]
v_exp_f32 v179,v179
v_exp_f32 v180,v180
v_exp_f32 v181,v181
v_mfma_f32_32x32x16_bf16 v[112:127],v[208:211],v[192:195],v[112:127]
v_exp_f32 v182,v182
v_exp_f32 v183,v183
v_exp_f32 v184,v184
v_mfma_f32_32x32x16_bf16 v[112:127],v[212:215],v[196:199],v[112:127]
v_exp_f32 v185,v185
v_exp_f32 v186,v186
v_exp_f32 v187,v187
v_mfma_f32_32x32x16_bf16 v[128:143],v[216:219],v[192:195],v[128:143]
v_exp_f32 v188,v188
v_exp_f32 v189,v189
v_mfma_f32_32x32x16_bf16 v[128:143],v[220:223],v[196:199],v[128:143]
v_exp_f32 v190,v190
v_exp_f32 v191,v191
ds_read_b64_tr_b16 v[200:201],v16 offset:12288
ds_read_b64_tr_b16 v[202:203],v16 offset:12800
ds_read_b64_tr_b16 v[204:205],v16 offset:13312
ds_read_b64_tr_b16 v[206:207],v16 offset:13824
ds_read_b64_tr_b16 v[208:209],v16 offset:14336
ds_read_b64_tr_b16 v[210:211],v16 offset:14848
ds_read_b64_tr_b16 v[212:213],v16 offset:15360
ds_read_b64_tr_b16 v[214:215],v16 offset:15872
ds_read_b64_tr_b16 v[216:217],v16 offset:16384
ds_read_b64_tr_b16 v[218:219],v16 offset:16896
ds_read_b64_tr_b16 v[220:221],v16 offset:17408
ds_read_b64_tr_b16 v[222:223],v16 offset:17920
ds_read_b128 v[160:163],v18 offset:640
ds_read_b128 v[164:167],v18 offset:672
ds_read_b128 v[168:171],v18 offset:704
ds_read_b128 v[172:175],v18 offset:736
s_mov_b64 exec,s[60:61]
s_add_u32 m0,s58,155136
s_mov_b32 s64,30720
buffer_load_dwordx4 v28,s[48:51],s64 offen lds
s_add_u32 m0,s58,155904
s_mov_b32 s65,31488
buffer_load_dwordx4 v28,s[48:51],s65 offen lds
s_mov_b64 exec,-1
s_setprio 0
v_add_f32 v176,1.0,v176
v_add_f32 v177,1.0,v177
v_add_f32 v178,1.0,v178
v_add_f32 v179,1.0,v179
v_add_f32 v180,1.0,v180
v_add_f32 v181,1.0,v181
v_add_f32 v182,1.0,v182
v_add_f32 v183,1.0,v183
v_add_f32 v184,1.0,v184
v_add_f32 v185,1.0,v185
v_add_f32 v186,1.0,v186
v_add_f32 v187,1.0,v187
v_add_f32 v188,1.0,v188
v_add_f32 v189,1.0,v189
v_add_f32 v190,1.0,v190
v_add_f32 v191,1.0,v191
s_setprio 1
s_waitcnt lgkmcnt(0)
v_mfma_f32_32x32x16_bf16 v[160:175],v[200:203],v[72:75],v[160:175]
v_rcp_f32 v176,v176
v_rcp_f32 v177,v177
v_rcp_f32 v178,v178
v_mfma_f32_32x32x16_bf16 v[160:175],v[204:207],v[76:79],v[160:175]
v_rcp_f32 v179,v179
v_rcp_f32 v180,v180
v_rcp_f32 v181,v181
v_mfma_f32_32x32x16_bf16 v[160:175],v[208:211],v[80:83],v[160:175]
v_rcp_f32 v182,v182
v_rcp_f32 v183,v183
v_rcp_f32 v184,v184
v_mfma_f32_32x32x16_bf16 v[160:175],v[212:215],v[84:87],v[160:175]
v_rcp_f32 v185,v185
v_rcp_f32 v186,v186
v_rcp_f32 v187,v187
v_mfma_f32_32x32x16_bf16 v[160:175],v[216:219],v[88:91],v[160:175]
v_rcp_f32 v188,v188
v_rcp_f32 v189,v189
v_mfma_f32_32x32x16_bf16 v[160:175],v[220:223],v[92:95],v[160:175]
v_rcp_f32 v190,v190
v_rcp_f32 v191,v191
ds_read_b64_tr_b16 v[200:201],v17 offset:6144
ds_read_b64_tr_b16 v[202:203],v17 offset:7680
ds_read_b64_tr_b16 v[204:205],v17 offset:9216
ds_read_b64_tr_b16 v[206:207],v17 offset:10752
ds_read_b64_tr_b16 v[208:209],v17 offset:6208
ds_read_b64_tr_b16 v[210:211],v17 offset:7744
ds_read_b64_tr_b16 v[212:213],v17 offset:9280
ds_read_b64_tr_b16 v[214:215],v17 offset:10816
ds_read_b64_tr_b16 v[216:217],v17 offset:6272
ds_read_b64_tr_b16 v[218:219],v17 offset:7808
ds_read_b64_tr_b16 v[220:221],v17 offset:9344
ds_read_b64_tr_b16 v[222:223],v17 offset:10880
s_mov_b64 exec,s[60:61]
s_add_u32 m0,s58,130560
s_movk_i32 s64,448
buffer_load_dwordx4 v27,s[44:47],s64 offen lds
s_add_u32 m0,s58,131328
s_movk_i32 s65,9664
buffer_load_dwordx4 v27,s[44:47],s65 offen lds
s_mov_b64 exec,-1
s_setprio 0
v_mul_f32 v176,v144,v176
v_mul_f32 v177,v145,v177
v_mul_f32 v178,v146,v178
v_mul_f32 v179,v147,v179
v_mul_f32 v180,v148,v180
v_mul_f32 v181,v149,v181
v_mul_f32 v182,v150,v182
v_mul_f32 v183,v151,v183
v_mul_f32 v184,v152,v184
v_mul_f32 v185,v153,v185
v_mul_f32 v186,v154,v186
v_mul_f32 v187,v155,v187
v_mul_f32 v188,v156,v188
v_mul_f32 v189,v157,v189
v_mul_f32 v190,v158,v190
v_mul_f32 v191,v159,v191
v_cvt_pk_bf16_f32 v192,v176,v177
v_cvt_pk_bf16_f32 v193,v178,v179
v_cvt_pk_bf16_f32 v194,v180,v181
v_cvt_pk_bf16_f32 v195,v182,v183
v_cvt_pk_bf16_f32 v196,v184,v185
v_cvt_pk_bf16_f32 v197,v186,v187
v_cvt_pk_bf16_f32 v198,v188,v189
v_cvt_pk_bf16_f32 v199,v190,v191
s_waitcnt lgkmcnt(0)
s_waitcnt vmcnt(2)
s_barrier
v_fmaak_f32 v176,v160,v160,0x409d30fc
v_fmaak_f32 v177,v161,v161,0x409d30fc
v_fmaak_f32 v178,v162,v162,0x409d30fc
v_fmaak_f32 v179,v163,v163,0x409d30fc
v_fmaak_f32 v180,v164,v164,0x409d30fc
v_fmaak_f32 v181,v165,v165,0x409d30fc
v_fmaak_f32 v182,v166,v166,0x409d30fc
v_fmaak_f32 v183,v167,v167,0x409d30fc
v_fmaak_f32 v184,v168,v168,0x409d30fc
v_fmaak_f32 v185,v169,v169,0x409d30fc
v_fmaak_f32 v186,v170,v170,0x409d30fc
v_fmaak_f32 v187,v171,v171,0x409d30fc
v_fmaak_f32 v188,v172,v172,0x409d30fc
v_fmaak_f32 v189,v173,v173,0x409d30fc
v_fmaak_f32 v190,v174,v174,0x409d30fc
v_fmaak_f32 v191,v175,v175,0x409d30fc
v_mul_f32 v176,v160,v176
v_mul_f32 v177,v161,v177
v_mul_f32 v178,v162,v178
v_mul_f32 v179,v163,v179
v_mul_f32 v180,v164,v180
v_mul_f32 v181,v165,v181
v_mul_f32 v182,v166,v182
v_mul_f32 v183,v167,v183
v_mul_f32 v184,v168,v184
v_mul_f32 v185,v169,v185
v_mul_f32 v186,v170,v186
v_mul_f32 v187,v171,v187
v_mul_f32 v188,v172,v188
v_mul_f32 v189,v173,v189
v_mul_f32 v190,v174,v190
v_mul_f32 v191,v175,v191
s_setprio 1
v_mfma_f32_32x32x16_bf16 v[96:111],v[200:203],v[192:195],v[96:111]
v_exp_f32 v176,v176
v_exp_f32 v177,v177
v_exp_f32 v178,v178
v_mfma_f32_32x32x16_bf16 v[96:111],v[204:207],v[196:199],v[96:111]
v_exp_f32 v179,v179
v_exp_f32 v180,v180
v_exp_f32 v181,v181
v_mfma_f32_32x32x16_bf16 v[112:127],v[208:211],v[192:195],v[112:127]
v_exp_f32 v182,v182
v_exp_f32 v183,v183
v_exp_f32 v184,v184
v_mfma_f32_32x32x16_bf16 v[112:127],v[212:215],v[196:199],v[112:127]
v_exp_f32 v185,v185
v_exp_f32 v186,v186
v_exp_f32 v187,v187
v_mfma_f32_32x32x16_bf16 v[128:143],v[216:219],v[192:195],v[128:143]
v_exp_f32 v188,v188
v_exp_f32 v189,v189
v_mfma_f32_32x32x16_bf16 v[128:143],v[220:223],v[196:199],v[128:143]
v_exp_f32 v190,v190
v_exp_f32 v191,v191
ds_read_b64_tr_b16 v[200:201],v16 offset:0
ds_read_b64_tr_b16 v[202:203],v16 offset:512
ds_read_b64_tr_b16 v[204:205],v16 offset:1024
ds_read_b64_tr_b16 v[206:207],v16 offset:1536
ds_read_b64_tr_b16 v[208:209],v16 offset:2048
ds_read_b64_tr_b16 v[210:211],v16 offset:2560
ds_read_b64_tr_b16 v[212:213],v16 offset:3072
ds_read_b64_tr_b16 v[214:215],v16 offset:3584
ds_read_b64_tr_b16 v[216:217],v16 offset:4096
ds_read_b64_tr_b16 v[218:219],v16 offset:4608
ds_read_b64_tr_b16 v[220:221],v16 offset:5120
ds_read_b64_tr_b16 v[222:223],v16 offset:5632
ds_read_b128 v[144:147],v18 offset:768
ds_read_b128 v[148:151],v18 offset:800
ds_read_b128 v[152:155],v18 offset:832
ds_read_b128 v[156:159],v18 offset:864
s_mov_b64 exec,s[60:61]
s_add_u32 m0,s58,142848
s_mov_b32 s64,36864
buffer_load_dwordx4 v28,s[48:51],s64 offen lds
s_add_u32 m0,s58,143616
s_mov_b32 s65,37632
buffer_load_dwordx4 v28,s[48:51],s65 offen lds
s_mov_b64 exec,-1
s_setprio 0
v_add_f32 v176,1.0,v176
v_add_f32 v177,1.0,v177
v_add_f32 v178,1.0,v178
v_add_f32 v179,1.0,v179
v_add_f32 v180,1.0,v180
v_add_f32 v181,1.0,v181
v_add_f32 v182,1.0,v182
v_add_f32 v183,1.0,v183
v_add_f32 v184,1.0,v184
v_add_f32 v185,1.0,v185
v_add_f32 v186,1.0,v186
v_add_f32 v187,1.0,v187
v_add_f32 v188,1.0,v188
v_add_f32 v189,1.0,v189
v_add_f32 v190,1.0,v190
v_add_f32 v191,1.0,v191
s_setprio 1
s_waitcnt lgkmcnt(0)
v_mfma_f32_32x32x16_bf16 v[144:159],v[200:203],v[72:75],v[144:159]
v_rcp_f32 v176,v176
v_rcp_f32 v177,v177
v_rcp_f32 v178,v178
v_mfma_f32_32x32x16_bf16 v[144:159],v[204:207],v[76:79],v[144:159]
v_rcp_f32 v179,v179
v_rcp_f32 v180,v180
v_rcp_f32 v181,v181
v_mfma_f32_32x32x16_bf16 v[144:159],v[208:211],v[80:83],v[144:159]
v_rcp_f32 v182,v182
v_rcp_f32 v183,v183
v_rcp_f32 v184,v184
v_mfma_f32_32x32x16_bf16 v[144:159],v[212:215],v[84:87],v[144:159]
v_rcp_f32 v185,v185
v_rcp_f32 v186,v186
v_rcp_f32 v187,v187
v_mfma_f32_32x32x16_bf16 v[144:159],v[216:219],v[88:91],v[144:159]
v_rcp_f32 v188,v188
v_rcp_f32 v189,v189
v_mfma_f32_32x32x16_bf16 v[144:159],v[220:223],v[92:95],v[144:159]
v_rcp_f32 v190,v190
v_rcp_f32 v191,v191
ds_read_b64_tr_b16 v[200:201],v17 offset:12288
ds_read_b64_tr_b16 v[202:203],v17 offset:13824
ds_read_b64_tr_b16 v[204:205],v17 offset:15360
ds_read_b64_tr_b16 v[206:207],v17 offset:16896
ds_read_b64_tr_b16 v[208:209],v17 offset:12352
ds_read_b64_tr_b16 v[210:211],v17 offset:13888
ds_read_b64_tr_b16 v[212:213],v17 offset:15424
ds_read_b64_tr_b16 v[214:215],v17 offset:16960
ds_read_b64_tr_b16 v[216:217],v17 offset:12416
ds_read_b64_tr_b16 v[218:219],v17 offset:13952
ds_read_b64_tr_b16 v[220:221],v17 offset:15488
ds_read_b64_tr_b16 v[222:223],v17 offset:17024
s_mov_b64 exec,s[60:61]
s_add_u32 m0,s58,136704
s_movk_i32 s64,512
buffer_load_dwordx4 v27,s[44:47],s64 offen lds
s_add_u32 m0,s58,137472
s_movk_i32 s65,9728
buffer_load_dwordx4 v27,s[44:47],s65 offen lds
s_mov_b64 exec,-1
s_setprio 0
v_mul_f32 v176,v160,v176
v_mul_f32 v177,v161,v177
v_mul_f32 v178,v162,v178
v_mul_f32 v179,v163,v179
v_mul_f32 v180,v164,v180
v_mul_f32 v181,v165,v181
v_mul_f32 v182,v166,v182
v_mul_f32 v183,v167,v183
v_mul_f32 v184,v168,v184
v_mul_f32 v185,v169,v185
v_mul_f32 v186,v170,v186
v_mul_f32 v187,v171,v187
v_mul_f32 v188,v172,v188
v_mul_f32 v189,v173,v189
v_mul_f32 v190,v174,v190
v_mul_f32 v191,v175,v191
v_cvt_pk_bf16_f32 v192,v176,v177
v_cvt_pk_bf16_f32 v193,v178,v179
v_cvt_pk_bf16_f32 v194,v180,v181
v_cvt_pk_bf16_f32 v195,v182,v183
v_cvt_pk_bf16_f32 v196,v184,v185
v_cvt_pk_bf16_f32 v197,v186,v187
v_cvt_pk_bf16_f32 v198,v188,v189
v_cvt_pk_bf16_f32 v199,v190,v191
s_waitcnt lgkmcnt(0)
s_waitcnt vmcnt(2)
s_barrier
v_fmaak_f32 v176,v144,v144,0x409d30fc
v_fmaak_f32 v177,v145,v145,0x409d30fc
v_fmaak_f32 v178,v146,v146,0x409d30fc
v_fmaak_f32 v179,v147,v147,0x409d30fc
v_fmaak_f32 v180,v148,v148,0x409d30fc
v_fmaak_f32 v181,v149,v149,0x409d30fc
v_fmaak_f32 v182,v150,v150,0x409d30fc
v_fmaak_f32 v183,v151,v151,0x409d30fc
v_fmaak_f32 v184,v152,v152,0x409d30fc
v_fmaak_f32 v185,v153,v153,0x409d30fc
v_fmaak_f32 v186,v154,v154,0x409d30fc
v_fmaak_f32 v187,v155,v155,0x409d30fc
v_fmaak_f32 v188,v156,v156,0x409d30fc
v_fmaak_f32 v189,v157,v157,0x409d30fc
v_fmaak_f32 v190,v158,v158,0x409d30fc
v_fmaak_f32 v191,v159,v159,0x409d30fc
v_mul_f32 v176,v144,v176
v_mul_f32 v177,v145,v177
v_mul_f32 v178,v146,v178
v_mul_f32 v179,v147,v179
v_mul_f32 v180,v148,v180
v_mul_f32 v181,v149,v181
v_mul_f32 v182,v150,v182
v_mul_f32 v183,v151,v183
v_mul_f32 v184,v152,v184
v_mul_f32 v185,v153,v185
v_mul_f32 v186,v154,v186
v_mul_f32 v187,v155,v187
v_mul_f32 v188,v156,v188
v_mul_f32 v189,v157,v189
v_mul_f32 v190,v158,v190
v_mul_f32 v191,v159,v191
s_setprio 1
v_mfma_f32_32x32x16_bf16 v[96:111],v[200:203],v[192:195],v[96:111]
v_exp_f32 v176,v176
v_exp_f32 v177,v177
v_exp_f32 v178,v178
v_mfma_f32_32x32x16_bf16 v[96:111],v[204:207],v[196:199],v[96:111]
v_exp_f32 v179,v179
v_exp_f32 v180,v180
v_exp_f32 v181,v181
v_mfma_f32_32x32x16_bf16 v[112:127],v[208:211],v[192:195],v[112:127]
v_exp_f32 v182,v182
v_exp_f32 v183,v183
v_exp_f32 v184,v184
v_mfma_f32_32x32x16_bf16 v[112:127],v[212:215],v[196:199],v[112:127]
v_exp_f32 v185,v185
v_exp_f32 v186,v186
v_exp_f32 v187,v187
v_mfma_f32_32x32x16_bf16 v[128:143],v[216:219],v[192:195],v[128:143]
v_exp_f32 v188,v188
v_exp_f32 v189,v189
v_mfma_f32_32x32x16_bf16 v[128:143],v[220:223],v[196:199],v[128:143]
v_exp_f32 v190,v190
v_exp_f32 v191,v191
ds_read_b64_tr_b16 v[200:201],v16 offset:6144
ds_read_b64_tr_b16 v[202:203],v16 offset:6656
ds_read_b64_tr_b16 v[204:205],v16 offset:7168
ds_read_b64_tr_b16 v[206:207],v16 offset:7680
ds_read_b64_tr_b16 v[208:209],v16 offset:8192
ds_read_b64_tr_b16 v[210:211],v16 offset:8704
ds_read_b64_tr_b16 v[212:213],v16 offset:9216
ds_read_b64_tr_b16 v[214:215],v16 offset:9728
ds_read_b64_tr_b16 v[216:217],v16 offset:10240
ds_read_b64_tr_b16 v[218:219],v16 offset:10752
ds_read_b64_tr_b16 v[220:221],v16 offset:11264
ds_read_b64_tr_b16 v[222:223],v16 offset:11776
ds_read_b128 v[160:163],v18 offset:896
ds_read_b128 v[164:167],v18 offset:928
ds_read_b128 v[168:171],v18 offset:960
ds_read_b128 v[172:175],v18 offset:992
s_mov_b64 exec,s[60:61]
s_add_u32 m0,s58,148992
s_mov_b32 s64,43008
buffer_load_dwordx4 v28,s[48:51],s64 offen lds
s_add_u32 m0,s58,149760
s_mov_b32 s65,43776
buffer_load_dwordx4 v28,s[48:51],s65 offen lds
s_mov_b64 exec,-1
s_setprio 0
v_add_f32 v176,1.0,v176
v_add_f32 v177,1.0,v177
v_add_f32 v178,1.0,v178
v_add_f32 v179,1.0,v179
v_add_f32 v180,1.0,v180
v_add_f32 v181,1.0,v181
v_add_f32 v182,1.0,v182
v_add_f32 v183,1.0,v183
v_add_f32 v184,1.0,v184
v_add_f32 v185,1.0,v185
v_add_f32 v186,1.0,v186
v_add_f32 v187,1.0,v187
v_add_f32 v188,1.0,v188
v_add_f32 v189,1.0,v189
v_add_f32 v190,1.0,v190
v_add_f32 v191,1.0,v191
s_setprio 1
s_waitcnt lgkmcnt(0)
v_mfma_f32_32x32x16_bf16 v[160:175],v[200:203],v[72:75],v[160:175]
v_rcp_f32 v176,v176
v_rcp_f32 v177,v177
v_rcp_f32 v178,v178
v_mfma_f32_32x32x16_bf16 v[160:175],v[204:207],v[76:79],v[160:175]
v_rcp_f32 v179,v179
v_rcp_f32 v180,v180
v_rcp_f32 v181,v181
v_mfma_f32_32x32x16_bf16 v[160:175],v[208:211],v[80:83],v[160:175]
v_rcp_f32 v182,v182
v_rcp_f32 v183,v183
v_rcp_f32 v184,v184
v_mfma_f32_32x32x16_bf16 v[160:175],v[212:215],v[84:87],v[160:175]
v_rcp_f32 v185,v185
v_rcp_f32 v186,v186
v_rcp_f32 v187,v187
v_mfma_f32_32x32x16_bf16 v[160:175],v[216:219],v[88:91],v[160:175]
v_rcp_f32 v188,v188
v_rcp_f32 v189,v189
v_mfma_f32_32x32x16_bf16 v[160:175],v[220:223],v[92:95],v[160:175]
v_rcp_f32 v190,v190
v_rcp_f32 v191,v191
ds_read_b64_tr_b16 v[200:201],v17 offset:0
ds_read_b64_tr_b16 v[202:203],v17 offset:1536
ds_read_b64_tr_b16 v[204:205],v17 offset:3072
ds_read_b64_tr_b16 v[206:207],v17 offset:4608
ds_read_b64_tr_b16 v[208:209],v17 offset:64
ds_read_b64_tr_b16 v[210:211],v17 offset:1600
ds_read_b64_tr_b16 v[212:213],v17 offset:3136
ds_read_b64_tr_b16 v[214:215],v17 offset:4672
ds_read_b64_tr_b16 v[216:217],v17 offset:128
ds_read_b64_tr_b16 v[218:219],v17 offset:1664
ds_read_b64_tr_b16 v[220:221],v17 offset:3200
ds_read_b64_tr_b16 v[222:223],v17 offset:4736
s_mov_b64 exec,s[60:61]
s_add_u32 m0,s58,124416
s_movk_i32 s64,576
buffer_load_dwordx4 v27,s[44:47],s64 offen lds
s_add_u32 m0,s58,125184
s_movk_i32 s65,9792
buffer_load_dwordx4 v27,s[44:47],s65 offen lds
s_mov_b64 exec,-1
s_setprio 0
v_mul_f32 v176,v144,v176
v_mul_f32 v177,v145,v177
v_mul_f32 v178,v146,v178
v_mul_f32 v179,v147,v179
v_mul_f32 v180,v148,v180
v_mul_f32 v181,v149,v181
v_mul_f32 v182,v150,v182
v_mul_f32 v183,v151,v183
v_mul_f32 v184,v152,v184
v_mul_f32 v185,v153,v185
v_mul_f32 v186,v154,v186
v_mul_f32 v187,v155,v187
v_mul_f32 v188,v156,v188
v_mul_f32 v189,v157,v189
v_mul_f32 v190,v158,v190
v_mul_f32 v191,v159,v191
v_cvt_pk_bf16_f32 v192,v176,v177
v_cvt_pk_bf16_f32 v193,v178,v179
v_cvt_pk_bf16_f32 v194,v180,v181
v_cvt_pk_bf16_f32 v195,v182,v183
v_cvt_pk_bf16_f32 v196,v184,v185
v_cvt_pk_bf16_f32 v197,v186,v187
v_cvt_pk_bf16_f32 v198,v188,v189
v_cvt_pk_bf16_f32 v199,v190,v191
s_waitcnt lgkmcnt(0)
s_waitcnt vmcnt(2)
s_barrier
v_fmaak_f32 v176,v160,v160,0x409d30fc
v_fmaak_f32 v177,v161,v161,0x409d30fc
v_fmaak_f32 v178,v162,v162,0x409d30fc
v_fmaak_f32 v179,v163,v163,0x409d30fc
v_fmaak_f32 v180,v164,v164,0x409d30fc
v_fmaak_f32 v181,v165,v165,0x409d30fc
v_fmaak_f32 v182,v166,v166,0x409d30fc
v_fmaak_f32 v183,v167,v167,0x409d30fc
v_fmaak_f32 v184,v168,v168,0x409d30fc
v_fmaak_f32 v185,v169,v169,0x409d30fc
v_fmaak_f32 v186,v170,v170,0x409d30fc
v_fmaak_f32 v187,v171,v171,0x409d30fc
v_fmaak_f32 v188,v172,v172,0x409d30fc
v_fmaak_f32 v189,v173,v173,0x409d30fc
v_fmaak_f32 v190,v174,v174,0x409d30fc
v_fmaak_f32 v191,v175,v175,0x409d30fc
v_mul_f32 v176,v160,v176
v_mul_f32 v177,v161,v177
v_mul_f32 v178,v162,v178
v_mul_f32 v179,v163,v179
v_mul_f32 v180,v164,v180
v_mul_f32 v181,v165,v181
v_mul_f32 v182,v166,v182
v_mul_f32 v183,v167,v183
v_mul_f32 v184,v168,v184
v_mul_f32 v185,v169,v185
v_mul_f32 v186,v170,v186
v_mul_f32 v187,v171,v187
v_mul_f32 v188,v172,v188
v_mul_f32 v189,v173,v189
v_mul_f32 v190,v174,v190
v_mul_f32 v191,v175,v191
s_setprio 1
v_mfma_f32_32x32x16_bf16 v[96:111],v[200:203],v[192:195],v[96:111]
v_exp_f32 v176,v176
v_exp_f32 v177,v177
v_exp_f32 v178,v178
v_mfma_f32_32x32x16_bf16 v[96:111],v[204:207],v[196:199],v[96:111]
v_exp_f32 v179,v179
v_exp_f32 v180,v180
v_exp_f32 v181,v181
v_mfma_f32_32x32x16_bf16 v[112:127],v[208:211],v[192:195],v[112:127]
v_exp_f32 v182,v182
v_exp_f32 v183,v183
v_exp_f32 v184,v184
v_mfma_f32_32x32x16_bf16 v[112:127],v[212:215],v[196:199],v[112:127]
v_exp_f32 v185,v185
v_exp_f32 v186,v186
v_exp_f32 v187,v187
v_mfma_f32_32x32x16_bf16 v[128:143],v[216:219],v[192:195],v[128:143]
v_exp_f32 v188,v188
v_exp_f32 v189,v189
v_mfma_f32_32x32x16_bf16 v[128:143],v[220:223],v[196:199],v[128:143]
v_exp_f32 v190,v190
v_exp_f32 v191,v191
ds_read_b64_tr_b16 v[200:201],v16 offset:12288
ds_read_b64_tr_b16 v[202:203],v16 offset:12800
ds_read_b64_tr_b16 v[204:205],v16 offset:13312
ds_read_b64_tr_b16 v[206:207],v16 offset:13824
ds_read_b64_tr_b16 v[208:209],v16 offset:14336
ds_read_b64_tr_b16 v[210:211],v16 offset:14848
ds_read_b64_tr_b16 v[212:213],v16 offset:15360
ds_read_b64_tr_b16 v[214:215],v16 offset:15872
ds_read_b64_tr_b16 v[216:217],v16 offset:16384
ds_read_b64_tr_b16 v[218:219],v16 offset:16896
ds_read_b64_tr_b16 v[220:221],v16 offset:17408
ds_read_b64_tr_b16 v[222:223],v16 offset:17920
ds_read_b128 v[144:147],v18 offset:1024
ds_read_b128 v[148:151],v18 offset:1056
ds_read_b128 v[152:155],v18 offset:1088
ds_read_b128 v[156:159],v18 offset:1120
s_mov_b64 exec,s[60:61]
s_add_u32 m0,s58,155136
s_mov_b32 s64,49152
buffer_load_dwordx4 v28,s[48:51],s64 offen lds
s_add_u32 m0,s58,155904
s_mov_b32 s65,49920
buffer_load_dwordx4 v28,s[48:51],s65 offen lds
s_mov_b64 exec,-1
s_setprio 0
v_add_f32 v176,1.0,v176
v_add_f32 v177,1.0,v177
v_add_f32 v178,1.0,v178
v_add_f32 v179,1.0,v179
v_add_f32 v180,1.0,v180
v_add_f32 v181,1.0,v181
v_add_f32 v182,1.0,v182
v_add_f32 v183,1.0,v183
v_add_f32 v184,1.0,v184
v_add_f32 v185,1.0,v185
v_add_f32 v186,1.0,v186
v_add_f32 v187,1.0,v187
v_add_f32 v188,1.0,v188
v_add_f32 v189,1.0,v189
v_add_f32 v190,1.0,v190
v_add_f32 v191,1.0,v191
s_setprio 1
s_waitcnt lgkmcnt(0)
v_mfma_f32_32x32x16_bf16 v[144:159],v[200:203],v[72:75],v[144:159]
v_rcp_f32 v176,v176
v_rcp_f32 v177,v177
v_rcp_f32 v178,v178
v_mfma_f32_32x32x16_bf16 v[144:159],v[204:207],v[76:79],v[144:159]
v_rcp_f32 v179,v179
v_rcp_f32 v180,v180
v_rcp_f32 v181,v181
v_mfma_f32_32x32x16_bf16 v[144:159],v[208:211],v[80:83],v[144:159]
v_rcp_f32 v182,v182
v_rcp_f32 v183,v183
v_rcp_f32 v184,v184
v_mfma_f32_32x32x16_bf16 v[144:159],v[212:215],v[84:87],v[144:159]
v_rcp_f32 v185,v185
v_rcp_f32 v186,v186
v_rcp_f32 v187,v187
v_mfma_f32_32x32x16_bf16 v[144:159],v[216:219],v[88:91],v[144:159]
v_rcp_f32 v188,v188
v_rcp_f32 v189,v189
v_mfma_f32_32x32x16_bf16 v[144:159],v[220:223],v[92:95],v[144:159]
v_rcp_f32 v190,v190
v_rcp_f32 v191,v191
ds_read_b64_tr_b16 v[200:201],v17 offset:6144
ds_read_b64_tr_b16 v[202:203],v17 offset:7680
ds_read_b64_tr_b16 v[204:205],v17 offset:9216
ds_read_b64_tr_b16 v[206:207],v17 offset:10752
ds_read_b64_tr_b16 v[208:209],v17 offset:6208
ds_read_b64_tr_b16 v[210:211],v17 offset:7744
ds_read_b64_tr_b16 v[212:213],v17 offset:9280
ds_read_b64_tr_b16 v[214:215],v17 offset:10816
ds_read_b64_tr_b16 v[216:217],v17 offset:6272
ds_read_b64_tr_b16 v[218:219],v17 offset:7808
ds_read_b64_tr_b16 v[220:221],v17 offset:9344
ds_read_b64_tr_b16 v[222:223],v17 offset:10880
s_mov_b64 exec,s[60:61]
s_add_u32 m0,s58,130560
s_movk_i32 s64,640
buffer_load_dwordx4 v27,s[44:47],s64 offen lds
s_add_u32 m0,s58,131328
s_movk_i32 s65,9856
buffer_load_dwordx4 v27,s[44:47],s65 offen lds
s_mov_b64 exec,-1
s_setprio 0
v_mul_f32 v176,v160,v176
v_mul_f32 v177,v161,v177
v_mul_f32 v178,v162,v178
v_mul_f32 v179,v163,v179
v_mul_f32 v180,v164,v180
v_mul_f32 v181,v165,v181
v_mul_f32 v182,v166,v182
v_mul_f32 v183,v167,v183
v_mul_f32 v184,v168,v184
v_mul_f32 v185,v169,v185
v_mul_f32 v186,v170,v186
v_mul_f32 v187,v171,v187
v_mul_f32 v188,v172,v188
v_mul_f32 v189,v173,v189
v_mul_f32 v190,v174,v190
v_mul_f32 v191,v175,v191
v_cvt_pk_bf16_f32 v192,v176,v177
v_cvt_pk_bf16_f32 v193,v178,v179
v_cvt_pk_bf16_f32 v194,v180,v181
v_cvt_pk_bf16_f32 v195,v182,v183
v_cvt_pk_bf16_f32 v196,v184,v185
v_cvt_pk_bf16_f32 v197,v186,v187
v_cvt_pk_bf16_f32 v198,v188,v189
v_cvt_pk_bf16_f32 v199,v190,v191
s_waitcnt lgkmcnt(0)
s_waitcnt vmcnt(2)
s_barrier
v_fmaak_f32 v176,v144,v144,0x409d30fc
v_fmaak_f32 v177,v145,v145,0x409d30fc
v_fmaak_f32 v178,v146,v146,0x409d30fc
v_fmaak_f32 v179,v147,v147,0x409d30fc
v_fmaak_f32 v180,v148,v148,0x409d30fc
v_fmaak_f32 v181,v149,v149,0x409d30fc
v_fmaak_f32 v182,v150,v150,0x409d30fc
v_fmaak_f32 v183,v151,v151,0x409d30fc
v_fmaak_f32 v184,v152,v152,0x409d30fc
v_fmaak_f32 v185,v153,v153,0x409d30fc
v_fmaak_f32 v186,v154,v154,0x409d30fc
v_fmaak_f32 v187,v155,v155,0x409d30fc
v_fmaak_f32 v188,v156,v156,0x409d30fc
v_fmaak_f32 v189,v157,v157,0x409d30fc
v_fmaak_f32 v190,v158,v158,0x409d30fc
v_fmaak_f32 v191,v159,v159,0x409d30fc
v_mul_f32 v176,v144,v176
v_mul_f32 v177,v145,v177
v_mul_f32 v178,v146,v178
v_mul_f32 v179,v147,v179
v_mul_f32 v180,v148,v180
v_mul_f32 v181,v149,v181
v_mul_f32 v182,v150,v182
v_mul_f32 v183,v151,v183
v_mul_f32 v184,v152,v184
v_mul_f32 v185,v153,v185
v_mul_f32 v186,v154,v186
v_mul_f32 v187,v155,v187
v_mul_f32 v188,v156,v188
v_mul_f32 v189,v157,v189
v_mul_f32 v190,v158,v190
v_mul_f32 v191,v159,v191
s_setprio 1
v_mfma_f32_32x32x16_bf16 v[96:111],v[200:203],v[192:195],v[96:111]
v_exp_f32 v176,v176
v_exp_f32 v177,v177
v_exp_f32 v178,v178
v_mfma_f32_32x32x16_bf16 v[96:111],v[204:207],v[196:199],v[96:111]
v_exp_f32 v179,v179
v_exp_f32 v180,v180
v_exp_f32 v181,v181
v_mfma_f32_32x32x16_bf16 v[112:127],v[208:211],v[192:195],v[112:127]
v_exp_f32 v182,v182
v_exp_f32 v183,v183
v_exp_f32 v184,v184
v_mfma_f32_32x32x16_bf16 v[112:127],v[212:215],v[196:199],v[112:127]
v_exp_f32 v185,v185
v_exp_f32 v186,v186
v_exp_f32 v187,v187
v_mfma_f32_32x32x16_bf16 v[128:143],v[216:219],v[192:195],v[128:143]
v_exp_f32 v188,v188
v_exp_f32 v189,v189
v_mfma_f32_32x32x16_bf16 v[128:143],v[220:223],v[196:199],v[128:143]
v_exp_f32 v190,v190
v_exp_f32 v191,v191
ds_read_b64_tr_b16 v[200:201],v16 offset:0
ds_read_b64_tr_b16 v[202:203],v16 offset:512
ds_read_b64_tr_b16 v[204:205],v16 offset:1024
ds_read_b64_tr_b16 v[206:207],v16 offset:1536
ds_read_b64_tr_b16 v[208:209],v16 offset:2048
ds_read_b64_tr_b16 v[210:211],v16 offset:2560
ds_read_b64_tr_b16 v[212:213],v16 offset:3072
ds_read_b64_tr_b16 v[214:215],v16 offset:3584
ds_read_b64_tr_b16 v[216:217],v16 offset:4096
ds_read_b64_tr_b16 v[218:219],v16 offset:4608
ds_read_b64_tr_b16 v[220:221],v16 offset:5120
ds_read_b64_tr_b16 v[222:223],v16 offset:5632
ds_read_b128 v[160:163],v18 offset:1152
ds_read_b128 v[164:167],v18 offset:1184
ds_read_b128 v[168:171],v18 offset:1216
ds_read_b128 v[172:175],v18 offset:1248
s_mov_b64 exec,s[60:61]
s_add_u32 m0,s58,142848
s_mov_b32 s64,55296
buffer_load_dwordx4 v28,s[48:51],s64 offen lds
s_add_u32 m0,s58,143616
s_mov_b32 s65,56064
buffer_load_dwordx4 v28,s[48:51],s65 offen lds
s_mov_b64 exec,-1
s_setprio 0
v_add_f32 v176,1.0,v176
v_add_f32 v177,1.0,v177
v_add_f32 v178,1.0,v178
v_add_f32 v179,1.0,v179
v_add_f32 v180,1.0,v180
v_add_f32 v181,1.0,v181
v_add_f32 v182,1.0,v182
v_add_f32 v183,1.0,v183
v_add_f32 v184,1.0,v184
v_add_f32 v185,1.0,v185
v_add_f32 v186,1.0,v186
v_add_f32 v187,1.0,v187
v_add_f32 v188,1.0,v188
v_add_f32 v189,1.0,v189
v_add_f32 v190,1.0,v190
v_add_f32 v191,1.0,v191
s_setprio 1
s_waitcnt lgkmcnt(0)
v_mfma_f32_32x32x16_bf16 v[160:175],v[200:203],v[72:75],v[160:175]
v_rcp_f32 v176,v176
v_rcp_f32 v177,v177
v_rcp_f32 v178,v178
v_mfma_f32_32x32x16_bf16 v[160:175],v[204:207],v[76:79],v[160:175]
v_rcp_f32 v179,v179
v_rcp_f32 v180,v180
v_rcp_f32 v181,v181
v_mfma_f32_32x32x16_bf16 v[160:175],v[208:211],v[80:83],v[160:175]
v_rcp_f32 v182,v182
v_rcp_f32 v183,v183
v_rcp_f32 v184,v184
v_mfma_f32_32x32x16_bf16 v[160:175],v[212:215],v[84:87],v[160:175]
v_rcp_f32 v185,v185
v_rcp_f32 v186,v186
v_rcp_f32 v187,v187
v_mfma_f32_32x32x16_bf16 v[160:175],v[216:219],v[88:91],v[160:175]
v_rcp_f32 v188,v188
v_rcp_f32 v189,v189
v_mfma_f32_32x32x16_bf16 v[160:175],v[220:223],v[92:95],v[160:175]
v_rcp_f32 v190,v190
v_rcp_f32 v191,v191
ds_read_b64_tr_b16 v[200:201],v17 offset:12288
ds_read_b64_tr_b16 v[202:203],v17 offset:13824
ds_read_b64_tr_b16 v[204:205],v17 offset:15360
ds_read_b64_tr_b16 v[206:207],v17 offset:16896
ds_read_b64_tr_b16 v[208:209],v17 offset:12352
ds_read_b64_tr_b16 v[210:211],v17 offset:13888
ds_read_b64_tr_b16 v[212:213],v17 offset:15424
ds_read_b64_tr_b16 v[214:215],v17 offset:16960
ds_read_b64_tr_b16 v[216:217],v17 offset:12416
ds_read_b64_tr_b16 v[218:219],v17 offset:13952
ds_read_b64_tr_b16 v[220:221],v17 offset:15488
ds_read_b64_tr_b16 v[222:223],v17 offset:17024
s_mov_b64 exec,s[60:61]
s_add_u32 m0,s58,136704
s_movk_i32 s64,704
buffer_load_dwordx4 v27,s[44:47],s64 offen lds
s_add_u32 m0,s58,137472
s_movk_i32 s65,9920
buffer_load_dwordx4 v27,s[44:47],s65 offen lds
s_mov_b64 exec,-1
s_setprio 0
v_mul_f32 v176,v144,v176
v_mul_f32 v177,v145,v177
v_mul_f32 v178,v146,v178
v_mul_f32 v179,v147,v179
v_mul_f32 v180,v148,v180
v_mul_f32 v181,v149,v181
v_mul_f32 v182,v150,v182
v_mul_f32 v183,v151,v183
v_mul_f32 v184,v152,v184
v_mul_f32 v185,v153,v185
v_mul_f32 v186,v154,v186
v_mul_f32 v187,v155,v187
v_mul_f32 v188,v156,v188
v_mul_f32 v189,v157,v189
v_mul_f32 v190,v158,v190
v_mul_f32 v191,v159,v191
v_cvt_pk_bf16_f32 v192,v176,v177
v_cvt_pk_bf16_f32 v193,v178,v179
v_cvt_pk_bf16_f32 v194,v180,v181
v_cvt_pk_bf16_f32 v195,v182,v183
v_cvt_pk_bf16_f32 v196,v184,v185
v_cvt_pk_bf16_f32 v197,v186,v187
v_cvt_pk_bf16_f32 v198,v188,v189
v_cvt_pk_bf16_f32 v199,v190,v191
s_waitcnt lgkmcnt(0)
s_waitcnt vmcnt(2)
s_barrier
v_fmaak_f32 v176,v160,v160,0x409d30fc
v_fmaak_f32 v177,v161,v161,0x409d30fc
v_fmaak_f32 v178,v162,v162,0x409d30fc
v_fmaak_f32 v179,v163,v163,0x409d30fc
v_fmaak_f32 v180,v164,v164,0x409d30fc
v_fmaak_f32 v181,v165,v165,0x409d30fc
v_fmaak_f32 v182,v166,v166,0x409d30fc
v_fmaak_f32 v183,v167,v167,0x409d30fc
v_fmaak_f32 v184,v168,v168,0x409d30fc
v_fmaak_f32 v185,v169,v169,0x409d30fc
v_fmaak_f32 v186,v170,v170,0x409d30fc
v_fmaak_f32 v187,v171,v171,0x409d30fc
v_fmaak_f32 v188,v172,v172,0x409d30fc
v_fmaak_f32 v189,v173,v173,0x409d30fc
v_fmaak_f32 v190,v174,v174,0x409d30fc
v_fmaak_f32 v191,v175,v175,0x409d30fc
v_mul_f32 v176,v160,v176
v_mul_f32 v177,v161,v177
v_mul_f32 v178,v162,v178
v_mul_f32 v179,v163,v179
v_mul_f32 v180,v164,v180
v_mul_f32 v181,v165,v181
v_mul_f32 v182,v166,v182
v_mul_f32 v183,v167,v183
v_mul_f32 v184,v168,v184
v_mul_f32 v185,v169,v185
v_mul_f32 v186,v170,v186
v_mul_f32 v187,v171,v187
v_mul_f32 v188,v172,v188
v_mul_f32 v189,v173,v189
v_mul_f32 v190,v174,v190
v_mul_f32 v191,v175,v191
s_setprio 1
v_mfma_f32_32x32x16_bf16 v[96:111],v[200:203],v[192:195],v[96:111]
v_exp_f32 v176,v176
v_exp_f32 v177,v177
v_exp_f32 v178,v178
v_mfma_f32_32x32x16_bf16 v[96:111],v[204:207],v[196:199],v[96:111]
v_exp_f32 v179,v179
v_exp_f32 v180,v180
v_exp_f32 v181,v181
v_mfma_f32_32x32x16_bf16 v[112:127],v[208:211],v[192:195],v[112:127]
v_exp_f32 v182,v182
v_exp_f32 v183,v183
v_exp_f32 v184,v184
v_mfma_f32_32x32x16_bf16 v[112:127],v[212:215],v[196:199],v[112:127]
v_exp_f32 v185,v185
v_exp_f32 v186,v186
v_exp_f32 v187,v187
v_mfma_f32_32x32x16_bf16 v[128:143],v[216:219],v[192:195],v[128:143]
v_exp_f32 v188,v188
v_exp_f32 v189,v189
v_mfma_f32_32x32x16_bf16 v[128:143],v[220:223],v[196:199],v[128:143]
v_exp_f32 v190,v190
v_exp_f32 v191,v191
ds_read_b64_tr_b16 v[200:201],v16 offset:6144
ds_read_b64_tr_b16 v[202:203],v16 offset:6656
ds_read_b64_tr_b16 v[204:205],v16 offset:7168
ds_read_b64_tr_b16 v[206:207],v16 offset:7680
ds_read_b64_tr_b16 v[208:209],v16 offset:8192
ds_read_b64_tr_b16 v[210:211],v16 offset:8704
ds_read_b64_tr_b16 v[212:213],v16 offset:9216
ds_read_b64_tr_b16 v[214:215],v16 offset:9728
ds_read_b64_tr_b16 v[216:217],v16 offset:10240
ds_read_b64_tr_b16 v[218:219],v16 offset:10752
ds_read_b64_tr_b16 v[220:221],v16 offset:11264
ds_read_b64_tr_b16 v[222:223],v16 offset:11776
ds_read_b128 v[144:147],v18 offset:1280
ds_read_b128 v[148:151],v18 offset:1312
ds_read_b128 v[152:155],v18 offset:1344
ds_read_b128 v[156:159],v18 offset:1376
s_mov_b64 exec,s[60:61]
s_add_u32 m0,s58,148992
s_mov_b32 s64,61440
buffer_load_dwordx4 v28,s[48:51],s64 offen lds
s_add_u32 m0,s58,149760
s_mov_b32 s65,62208
buffer_load_dwordx4 v28,s[48:51],s65 offen lds
s_mov_b64 exec,-1
s_setprio 0
v_add_f32 v176,1.0,v176
v_add_f32 v177,1.0,v177
v_add_f32 v178,1.0,v178
v_add_f32 v179,1.0,v179
v_add_f32 v180,1.0,v180
v_add_f32 v181,1.0,v181
v_add_f32 v182,1.0,v182
v_add_f32 v183,1.0,v183
v_add_f32 v184,1.0,v184
v_add_f32 v185,1.0,v185
v_add_f32 v186,1.0,v186
v_add_f32 v187,1.0,v187
v_add_f32 v188,1.0,v188
v_add_f32 v189,1.0,v189
v_add_f32 v190,1.0,v190
v_add_f32 v191,1.0,v191
s_setprio 1
s_waitcnt lgkmcnt(0)
v_mfma_f32_32x32x16_bf16 v[144:159],v[200:203],v[72:75],v[144:159]
v_rcp_f32 v176,v176
v_rcp_f32 v177,v177
v_rcp_f32 v178,v178
v_mfma_f32_32x32x16_bf16 v[144:159],v[204:207],v[76:79],v[144:159]
v_rcp_f32 v179,v179
v_rcp_f32 v180,v180
v_rcp_f32 v181,v181
v_mfma_f32_32x32x16_bf16 v[144:159],v[208:211],v[80:83],v[144:159]
v_rcp_f32 v182,v182
v_rcp_f32 v183,v183
v_rcp_f32 v184,v184
v_mfma_f32_32x32x16_bf16 v[144:159],v[212:215],v[84:87],v[144:159]
v_rcp_f32 v185,v185
v_rcp_f32 v186,v186
v_rcp_f32 v187,v187
v_mfma_f32_32x32x16_bf16 v[144:159],v[216:219],v[88:91],v[144:159]
v_rcp_f32 v188,v188
v_rcp_f32 v189,v189
v_mfma_f32_32x32x16_bf16 v[144:159],v[220:223],v[92:95],v[144:159]
v_rcp_f32 v190,v190
v_rcp_f32 v191,v191
ds_read_b64_tr_b16 v[200:201],v17 offset:0
ds_read_b64_tr_b16 v[202:203],v17 offset:1536
ds_read_b64_tr_b16 v[204:205],v17 offset:3072
ds_read_b64_tr_b16 v[206:207],v17 offset:4608
ds_read_b64_tr_b16 v[208:209],v17 offset:64
ds_read_b64_tr_b16 v[210:211],v17 offset:1600
ds_read_b64_tr_b16 v[212:213],v17 offset:3136
ds_read_b64_tr_b16 v[214:215],v17 offset:4672
ds_read_b64_tr_b16 v[216:217],v17 offset:128
ds_read_b64_tr_b16 v[218:219],v17 offset:1664
ds_read_b64_tr_b16 v[220:221],v17 offset:3200
ds_read_b64_tr_b16 v[222:223],v17 offset:4736
s_mov_b64 exec,s[60:61]
s_add_u32 m0,s58,124416
s_movk_i32 s64,0
buffer_load_dwordx4 v27,s[44:47],s64 offen lds
s_add_u32 m0,s58,125184
s_movk_i32 s65,9216
buffer_load_dwordx4 v27,s[44:47],s65 offen lds
s_mov_b64 exec,-1
s_setprio 0
v_mul_f32 v176,v160,v176
v_mul_f32 v177,v161,v177
v_mul_f32 v178,v162,v178
v_mul_f32 v179,v163,v179
v_mul_f32 v180,v164,v180
v_mul_f32 v181,v165,v181
v_mul_f32 v182,v166,v182
v_mul_f32 v183,v167,v183
v_mul_f32 v184,v168,v184
v_mul_f32 v185,v169,v185
v_mul_f32 v186,v170,v186
v_mul_f32 v187,v171,v187
v_mul_f32 v188,v172,v188
v_mul_f32 v189,v173,v189
v_mul_f32 v190,v174,v190
v_mul_f32 v191,v175,v191
v_cvt_pk_bf16_f32 v192,v176,v177
v_cvt_pk_bf16_f32 v193,v178,v179
v_cvt_pk_bf16_f32 v194,v180,v181
v_cvt_pk_bf16_f32 v195,v182,v183
v_cvt_pk_bf16_f32 v196,v184,v185
v_cvt_pk_bf16_f32 v197,v186,v187
v_cvt_pk_bf16_f32 v198,v188,v189
v_cvt_pk_bf16_f32 v199,v190,v191
s_waitcnt lgkmcnt(0)
s_waitcnt vmcnt(2)
s_barrier
v_fmaak_f32 v176,v144,v144,0x409d30fc
v_fmaak_f32 v177,v145,v145,0x409d30fc
v_fmaak_f32 v178,v146,v146,0x409d30fc
v_fmaak_f32 v179,v147,v147,0x409d30fc
v_fmaak_f32 v180,v148,v148,0x409d30fc
v_fmaak_f32 v181,v149,v149,0x409d30fc
v_fmaak_f32 v182,v150,v150,0x409d30fc
v_fmaak_f32 v183,v151,v151,0x409d30fc
v_fmaak_f32 v184,v152,v152,0x409d30fc
v_fmaak_f32 v185,v153,v153,0x409d30fc
v_fmaak_f32 v186,v154,v154,0x409d30fc
v_fmaak_f32 v187,v155,v155,0x409d30fc
v_fmaak_f32 v188,v156,v156,0x409d30fc
v_fmaak_f32 v189,v157,v157,0x409d30fc
v_fmaak_f32 v190,v158,v158,0x409d30fc
v_fmaak_f32 v191,v159,v159,0x409d30fc
v_mul_f32 v176,v144,v176
v_mul_f32 v177,v145,v177
v_mul_f32 v178,v146,v178
v_mul_f32 v179,v147,v179
v_mul_f32 v180,v148,v180
v_mul_f32 v181,v149,v181
v_mul_f32 v182,v150,v182
v_mul_f32 v183,v151,v183
v_mul_f32 v184,v152,v184
v_mul_f32 v185,v153,v185
v_mul_f32 v186,v154,v186
v_mul_f32 v187,v155,v187
v_mul_f32 v188,v156,v188
v_mul_f32 v189,v157,v189
v_mul_f32 v190,v158,v190
v_mul_f32 v191,v159,v191
s_setprio 1
v_mfma_f32_32x32x16_bf16 v[96:111],v[200:203],v[192:195],v[96:111]
v_exp_f32 v176,v176
v_exp_f32 v177,v177
v_exp_f32 v178,v178
v_mfma_f32_32x32x16_bf16 v[96:111],v[204:207],v[196:199],v[96:111]
v_exp_f32 v179,v179
v_exp_f32 v180,v180
v_exp_f32 v181,v181
v_mfma_f32_32x32x16_bf16 v[112:127],v[208:211],v[192:195],v[112:127]
v_exp_f32 v182,v182
v_exp_f32 v183,v183
v_exp_f32 v184,v184
v_mfma_f32_32x32x16_bf16 v[112:127],v[212:215],v[196:199],v[112:127]
v_exp_f32 v185,v185
v_exp_f32 v186,v186
v_exp_f32 v187,v187
v_mfma_f32_32x32x16_bf16 v[128:143],v[216:219],v[192:195],v[128:143]
v_exp_f32 v188,v188
v_exp_f32 v189,v189
v_mfma_f32_32x32x16_bf16 v[128:143],v[220:223],v[196:199],v[128:143]
v_exp_f32 v190,v190
v_exp_f32 v191,v191
ds_read_b64_tr_b16 v[200:201],v16 offset:12288
ds_read_b64_tr_b16 v[202:203],v16 offset:12800
ds_read_b64_tr_b16 v[204:205],v16 offset:13312
ds_read_b64_tr_b16 v[206:207],v16 offset:13824
ds_read_b64_tr_b16 v[208:209],v16 offset:14336
ds_read_b64_tr_b16 v[210:211],v16 offset:14848
ds_read_b64_tr_b16 v[212:213],v16 offset:15360
ds_read_b64_tr_b16 v[214:215],v16 offset:15872
ds_read_b64_tr_b16 v[216:217],v16 offset:16384
ds_read_b64_tr_b16 v[218:219],v16 offset:16896
ds_read_b64_tr_b16 v[220:221],v16 offset:17408
ds_read_b64_tr_b16 v[222:223],v16 offset:17920
ds_read_b128 v[160:163],v18 offset:1408
ds_read_b128 v[164:167],v18 offset:1440
ds_read_b128 v[168:171],v18 offset:1472
ds_read_b128 v[172:175],v18 offset:1504
s_mov_b64 exec,s[60:61]
s_add_u32 m0,s58,155136
s_mov_b32 s64,67584
buffer_load_dwordx4 v28,s[48:51],s64 offen lds
s_add_u32 m0,s58,155904
s_mov_b32 s65,68352
buffer_load_dwordx4 v28,s[48:51],s65 offen lds
s_mov_b64 exec,-1
s_setprio 0
v_add_f32 v176,1.0,v176
v_add_f32 v177,1.0,v177
v_add_f32 v178,1.0,v178
v_add_f32 v179,1.0,v179
v_add_f32 v180,1.0,v180
v_add_f32 v181,1.0,v181
v_add_f32 v182,1.0,v182
v_add_f32 v183,1.0,v183
v_add_f32 v184,1.0,v184
v_add_f32 v185,1.0,v185
v_add_f32 v186,1.0,v186
v_add_f32 v187,1.0,v187
v_add_f32 v188,1.0,v188
v_add_f32 v189,1.0,v189
v_add_f32 v190,1.0,v190
v_add_f32 v191,1.0,v191
s_setprio 1
s_waitcnt lgkmcnt(0)
v_mfma_f32_32x32x16_bf16 v[160:175],v[200:203],v[72:75],v[160:175]
v_rcp_f32 v176,v176
v_rcp_f32 v177,v177
v_rcp_f32 v178,v178
v_mfma_f32_32x32x16_bf16 v[160:175],v[204:207],v[76:79],v[160:175]
v_rcp_f32 v179,v179
v_rcp_f32 v180,v180
v_rcp_f32 v181,v181
v_mfma_f32_32x32x16_bf16 v[160:175],v[208:211],v[80:83],v[160:175]
v_rcp_f32 v182,v182
v_rcp_f32 v183,v183
v_rcp_f32 v184,v184
v_mfma_f32_32x32x16_bf16 v[160:175],v[212:215],v[84:87],v[160:175]
v_rcp_f32 v185,v185
v_rcp_f32 v186,v186
v_rcp_f32 v187,v187
v_mfma_f32_32x32x16_bf16 v[160:175],v[216:219],v[88:91],v[160:175]
v_rcp_f32 v188,v188
v_rcp_f32 v189,v189
v_mfma_f32_32x32x16_bf16 v[160:175],v[220:223],v[92:95],v[160:175]
v_rcp_f32 v190,v190
v_rcp_f32 v191,v191
ds_read_b64_tr_b16 v[200:201],v17 offset:6144
ds_read_b64_tr_b16 v[202:203],v17 offset:7680
ds_read_b64_tr_b16 v[204:205],v17 offset:9216
ds_read_b64_tr_b16 v[206:207],v17 offset:10752
ds_read_b64_tr_b16 v[208:209],v17 offset:6208
ds_read_b64_tr_b16 v[210:211],v17 offset:7744
ds_read_b64_tr_b16 v[212:213],v17 offset:9280
ds_read_b64_tr_b16 v[214:215],v17 offset:10816
ds_read_b64_tr_b16 v[216:217],v17 offset:6272
ds_read_b64_tr_b16 v[218:219],v17 offset:7808
ds_read_b64_tr_b16 v[220:221],v17 offset:9344
ds_read_b64_tr_b16 v[222:223],v17 offset:10880
s_mov_b64 exec,s[60:61]
s_add_u32 m0,s58,130560
s_movk_i32 s64,64
buffer_load_dwordx4 v27,s[44:47],s64 offen lds
s_add_u32 m0,s58,131328
s_movk_i32 s65,9280
buffer_load_dwordx4 v27,s[44:47],s65 offen lds
s_mov_b64 exec,-1
s_setprio 0
v_mul_f32 v176,v144,v176
v_mul_f32 v177,v145,v177
v_mul_f32 v178,v146,v178
v_mul_f32 v179,v147,v179
v_mul_f32 v180,v148,v180
v_mul_f32 v181,v149,v181
v_mul_f32 v182,v150,v182
v_mul_f32 v183,v151,v183
v_mul_f32 v184,v152,v184
v_mul_f32 v185,v153,v185
v_mul_f32 v186,v154,v186
v_mul_f32 v187,v155,v187
v_mul_f32 v188,v156,v188
v_mul_f32 v189,v157,v189
v_mul_f32 v190,v158,v190
v_mul_f32 v191,v159,v191
v_cvt_pk_bf16_f32 v192,v176,v177
v_cvt_pk_bf16_f32 v193,v178,v179
v_cvt_pk_bf16_f32 v194,v180,v181
v_cvt_pk_bf16_f32 v195,v182,v183
v_cvt_pk_bf16_f32 v196,v184,v185
v_cvt_pk_bf16_f32 v197,v186,v187
v_cvt_pk_bf16_f32 v198,v188,v189
v_cvt_pk_bf16_f32 v199,v190,v191
s_waitcnt lgkmcnt(0)
s_waitcnt vmcnt(2)
s_barrier
v_fmaak_f32 v176,v160,v160,0x409d30fc
v_fmaak_f32 v177,v161,v161,0x409d30fc
v_fmaak_f32 v178,v162,v162,0x409d30fc
v_fmaak_f32 v179,v163,v163,0x409d30fc
v_fmaak_f32 v180,v164,v164,0x409d30fc
v_fmaak_f32 v181,v165,v165,0x409d30fc
v_fmaak_f32 v182,v166,v166,0x409d30fc
v_fmaak_f32 v183,v167,v167,0x409d30fc
v_fmaak_f32 v184,v168,v168,0x409d30fc
v_fmaak_f32 v185,v169,v169,0x409d30fc
v_fmaak_f32 v186,v170,v170,0x409d30fc
v_fmaak_f32 v187,v171,v171,0x409d30fc
v_fmaak_f32 v188,v172,v172,0x409d30fc
v_fmaak_f32 v189,v173,v173,0x409d30fc
v_fmaak_f32 v190,v174,v174,0x409d30fc
v_fmaak_f32 v191,v175,v175,0x409d30fc
v_mul_f32 v176,v160,v176
v_mul_f32 v177,v161,v177
v_mul_f32 v178,v162,v178
v_mul_f32 v179,v163,v179
v_mul_f32 v180,v164,v180
v_mul_f32 v181,v165,v181
v_mul_f32 v182,v166,v182
v_mul_f32 v183,v167,v183
v_mul_f32 v184,v168,v184
v_mul_f32 v185,v169,v185
v_mul_f32 v186,v170,v186
v_mul_f32 v187,v171,v187
v_mul_f32 v188,v172,v188
v_mul_f32 v189,v173,v189
v_mul_f32 v190,v174,v190
v_mul_f32 v191,v175,v191
s_setprio 1
v_mfma_f32_32x32x16_bf16 v[96:111],v[200:203],v[192:195],v[96:111]
v_exp_f32 v176,v176
v_exp_f32 v177,v177
v_exp_f32 v178,v178
v_mfma_f32_32x32x16_bf16 v[96:111],v[204:207],v[196:199],v[96:111]
v_exp_f32 v179,v179
v_exp_f32 v180,v180
v_exp_f32 v181,v181
v_mfma_f32_32x32x16_bf16 v[112:127],v[208:211],v[192:195],v[112:127]
v_exp_f32 v182,v182
v_exp_f32 v183,v183
v_exp_f32 v184,v184
v_mfma_f32_32x32x16_bf16 v[112:127],v[212:215],v[196:199],v[112:127]
v_exp_f32 v185,v185
v_exp_f32 v186,v186
v_exp_f32 v187,v187
v_mfma_f32_32x32x16_bf16 v[128:143],v[216:219],v[192:195],v[128:143]
v_exp_f32 v188,v188
v_exp_f32 v189,v189
v_mfma_f32_32x32x16_bf16 v[128:143],v[220:223],v[196:199],v[128:143]
v_exp_f32 v190,v190
v_exp_f32 v191,v191
ds_read_b64 v[72:73],v20 offset:0
ds_read_b64 v[74:75],v21 offset:0
ds_read_b64 v[76:77],v22 offset:0
ds_read_b64 v[78:79],v23 offset:0
ds_read_b64 v[80:81],v20 offset:64
ds_read_b64 v[82:83],v21 offset:64
ds_read_b64 v[84:85],v22 offset:64
ds_read_b64 v[86:87],v23 offset:64
ds_read_b64 v[88:89],v20 offset:128
ds_read_b64 v[90:91],v21 offset:128
ds_read_b64 v[92:93],v22 offset:128
ds_read_b64 v[94:95],v23 offset:128
s_mov_b64 exec,s[60:61]
s_add_u32 m0,s58,142848
s_mov_b32 s64,0
buffer_load_dwordx4 v28,s[48:51],s64 offen lds
s_add_u32 m0,s58,143616
s_mov_b32 s65,768
buffer_load_dwordx4 v28,s[48:51],s65 offen lds
s_mov_b64 exec,-1
s_setprio 0
v_add_f32 v176,1.0,v176
v_add_f32 v177,1.0,v177
v_add_f32 v178,1.0,v178
v_add_f32 v179,1.0,v179
v_add_f32 v180,1.0,v180
v_add_f32 v181,1.0,v181
v_add_f32 v182,1.0,v182
v_add_f32 v183,1.0,v183
v_add_f32 v184,1.0,v184
v_add_f32 v185,1.0,v185
v_add_f32 v186,1.0,v186
v_add_f32 v187,1.0,v187
v_add_f32 v188,1.0,v188
v_add_f32 v189,1.0,v189
v_add_f32 v190,1.0,v190
v_add_f32 v191,1.0,v191
s_setprio 1
s_waitcnt lgkmcnt(0)
v_rcp_f32 v176,v176
v_rcp_f32 v177,v177
v_rcp_f32 v178,v178
v_rcp_f32 v179,v179
v_rcp_f32 v180,v180
v_rcp_f32 v181,v181
v_rcp_f32 v182,v182
v_rcp_f32 v183,v183
v_rcp_f32 v184,v184
v_rcp_f32 v185,v185
v_rcp_f32 v186,v186
v_rcp_f32 v187,v187
v_rcp_f32 v188,v188
v_rcp_f32 v189,v189
v_rcp_f32 v190,v190
v_rcp_f32 v191,v191
ds_read_b64_tr_b16 v[200:201],v17 offset:12288
ds_read_b64_tr_b16 v[202:203],v17 offset:13824
ds_read_b64_tr_b16 v[204:205],v17 offset:15360
ds_read_b64_tr_b16 v[206:207],v17 offset:16896
ds_read_b64_tr_b16 v[208:209],v17 offset:12352
ds_read_b64_tr_b16 v[210:211],v17 offset:13888
ds_read_b64_tr_b16 v[212:213],v17 offset:15424
ds_read_b64_tr_b16 v[214:215],v17 offset:16960
ds_read_b64_tr_b16 v[216:217],v17 offset:12416
ds_read_b64_tr_b16 v[218:219],v17 offset:13952
ds_read_b64_tr_b16 v[220:221],v17 offset:15488
ds_read_b64_tr_b16 v[222:223],v17 offset:17024
s_mov_b64 exec,s[60:61]
s_add_u32 m0,s58,136704
s_movk_i32 s64,128
buffer_load_dwordx4 v27,s[44:47],s64 offen lds
s_add_u32 m0,s58,137472
s_movk_i32 s65,9344
buffer_load_dwordx4 v27,s[44:47],s65 offen lds
s_mov_b64 exec,-1
s_setprio 0
v_mul_f32 v176,v160,v176
v_mul_f32 v177,v161,v177
v_mul_f32 v178,v162,v178
v_mul_f32 v179,v163,v179
v_mul_f32 v180,v164,v180
v_mul_f32 v181,v165,v181
v_mul_f32 v182,v166,v182
v_mul_f32 v183,v167,v183
v_mul_f32 v184,v168,v184
v_mul_f32 v185,v169,v185
v_mul_f32 v186,v170,v186
v_mul_f32 v187,v171,v187
v_mul_f32 v188,v172,v188
v_mul_f32 v189,v173,v189
v_mul_f32 v190,v174,v190
v_mul_f32 v191,v175,v191
v_cvt_pk_bf16_f32 v192,v176,v177
v_cvt_pk_bf16_f32 v193,v178,v179
v_cvt_pk_bf16_f32 v194,v180,v181
v_cvt_pk_bf16_f32 v195,v182,v183
v_cvt_pk_bf16_f32 v196,v184,v185
v_cvt_pk_bf16_f32 v197,v186,v187
v_cvt_pk_bf16_f32 v198,v188,v189
v_cvt_pk_bf16_f32 v199,v190,v191
ds_read_b128 v[144:147],v18 offset:1536
ds_read_b128 v[148:151],v18 offset:1568
ds_read_b128 v[152:155],v18 offset:1600
ds_read_b128 v[156:159],v18 offset:1632
ds_read_b128 v[160:163],v18 offset:1664
ds_read_b128 v[164:167],v18 offset:1696
ds_read_b128 v[168:171],v18 offset:1728
ds_read_b128 v[172:175],v18 offset:1760
ds_read_b128 v[176:179],v18 offset:1792
ds_read_b128 v[180:183],v18 offset:1824
ds_read_b128 v[184:187],v18 offset:1856
ds_read_b128 v[188:191],v18 offset:1888
s_waitcnt lgkmcnt(12)
s_setprio 1
v_mfma_f32_32x32x16_bf16 v[96:111],v[200:203],v[192:195],v[96:111]
v_lshlrev_b32 v232,16,v72
v_lshlrev_b32 v233,16,v73
v_lshlrev_b32 v234,16,v74
v_lshlrev_b32 v235,16,v75
v_mfma_f32_32x32x16_bf16 v[96:111],v[204:207],v[196:199],v[96:111]
v_lshlrev_b32 v236,16,v76
v_lshlrev_b32 v237,16,v77
v_lshlrev_b32 v238,16,v78
v_lshlrev_b32 v239,16,v79
v_mfma_f32_32x32x16_bf16 v[112:127],v[208:211],v[192:195],v[112:127]
v_lshlrev_b32 v240,16,v80
v_lshlrev_b32 v241,16,v81
v_lshlrev_b32 v242,16,v82
v_lshlrev_b32 v243,16,v83
v_mfma_f32_32x32x16_bf16 v[112:127],v[212:215],v[196:199],v[112:127]
v_lshlrev_b32 v244,16,v84
v_lshlrev_b32 v245,16,v85
v_lshlrev_b32 v246,16,v86
v_lshlrev_b32 v247,16,v87
v_mfma_f32_32x32x16_bf16 v[128:143],v[216:219],v[192:195],v[128:143]
v_lshlrev_b32 v248,16,v88
v_lshlrev_b32 v249,16,v89
v_lshlrev_b32 v250,16,v90
v_lshlrev_b32 v251,16,v91
v_mfma_f32_32x32x16_bf16 v[128:143],v[220:223],v[196:199],v[128:143]
v_lshlrev_b32 v252,16,v92
v_lshlrev_b32 v253,16,v93
v_lshlrev_b32 v254,16,v94
v_lshlrev_b32 v255,16,v95
s_setprio 0
s_waitcnt lgkmcnt(0)
v_and_b32 v72,0xffff0000,v72
v_and_b32 v73,0xffff0000,v73
v_and_b32 v74,0xffff0000,v74
v_and_b32 v75,0xffff0000,v75
v_and_b32 v76,0xffff0000,v76
v_and_b32 v77,0xffff0000,v77
v_and_b32 v78,0xffff0000,v78
v_and_b32 v79,0xffff0000,v79
v_and_b32 v80,0xffff0000,v80
v_and_b32 v81,0xffff0000,v81
v_and_b32 v82,0xffff0000,v82
v_and_b32 v83,0xffff0000,v83
v_and_b32 v84,0xffff0000,v84
v_and_b32 v85,0xffff0000,v85
v_and_b32 v86,0xffff0000,v86
v_and_b32 v87,0xffff0000,v87
v_and_b32 v88,0xffff0000,v88
v_and_b32 v89,0xffff0000,v89
v_and_b32 v90,0xffff0000,v90
v_and_b32 v91,0xffff0000,v91
v_and_b32 v92,0xffff0000,v92
v_and_b32 v93,0xffff0000,v93
v_and_b32 v94,0xffff0000,v94
v_and_b32 v95,0xffff0000,v95
v_fma_f32 v232,v96,v144,v232
v_fma_f32 v72,v97,v145,v72
v_fma_f32 v233,v98,v146,v233
v_fma_f32 v73,v99,v147,v73
v_fma_f32 v234,v100,v148,v234
v_fma_f32 v74,v101,v149,v74
v_fma_f32 v235,v102,v150,v235
v_fma_f32 v75,v103,v151,v75
v_fma_f32 v236,v104,v152,v236
v_fma_f32 v76,v105,v153,v76
v_fma_f32 v237,v106,v154,v237
v_fma_f32 v77,v107,v155,v77
v_fma_f32 v238,v108,v156,v238
v_fma_f32 v78,v109,v157,v78
v_fma_f32 v239,v110,v158,v239
v_fma_f32 v79,v111,v159,v79
v_fma_f32 v240,v112,v160,v240
v_fma_f32 v80,v113,v161,v80
v_fma_f32 v241,v114,v162,v241
v_fma_f32 v81,v115,v163,v81
v_fma_f32 v242,v116,v164,v242
v_fma_f32 v82,v117,v165,v82
v_fma_f32 v243,v118,v166,v243
v_fma_f32 v83,v119,v167,v83
v_fma_f32 v244,v120,v168,v244
v_fma_f32 v84,v121,v169,v84
v_fma_f32 v245,v122,v170,v245
v_fma_f32 v85,v123,v171,v85
v_fma_f32 v246,v124,v172,v246
v_fma_f32 v86,v125,v173,v86
v_fma_f32 v247,v126,v174,v247
v_fma_f32 v87,v127,v175,v87
v_fma_f32 v248,v128,v176,v248
v_fma_f32 v88,v129,v177,v88
v_fma_f32 v249,v130,v178,v249
v_fma_f32 v89,v131,v179,v89
v_fma_f32 v250,v132,v180,v250
v_fma_f32 v90,v133,v181,v90
v_fma_f32 v251,v134,v182,v251
v_fma_f32 v91,v135,v183,v91
v_fma_f32 v252,v136,v184,v252
v_fma_f32 v92,v137,v185,v92
v_fma_f32 v253,v138,v186,v253
v_fma_f32 v93,v139,v187,v93
v_fma_f32 v254,v140,v188,v254
v_fma_f32 v94,v141,v189,v94
v_fma_f32 v255,v142,v190,v255
v_fma_f32 v95,v143,v191,v95
v_add_u32 v4,s54,v4
v_add_u32 v5,s54,v5
v_add_u32 v6,s54,v6
v_add_u32 v7,s54,v7
v_add_u32 v8,s54,v8
v_add_u32 v9,s54,v9
v_add_u32 v10,s54,v10
v_add_u32 v11,s54,v11
v_add_u32 v12,s54,v12
v_add_u32 v13,s54,v13
v_add_u32 v14,s54,v14
v_add_u32 v15,s54,v15
v_cvt_pk_bf16_f32 v200,v232,v72
v_cvt_pk_bf16_f32 v201,v233,v73
v_cvt_pk_bf16_f32 v202,v234,v74
v_cvt_pk_bf16_f32 v203,v235,v75
v_cvt_pk_bf16_f32 v204,v236,v76
v_cvt_pk_bf16_f32 v205,v237,v77
v_cvt_pk_bf16_f32 v206,v238,v78
v_cvt_pk_bf16_f32 v207,v239,v79
v_cvt_pk_bf16_f32 v208,v240,v80
v_cvt_pk_bf16_f32 v209,v241,v81
v_cvt_pk_bf16_f32 v210,v242,v82
v_cvt_pk_bf16_f32 v211,v243,v83
v_cvt_pk_bf16_f32 v212,v244,v84
v_cvt_pk_bf16_f32 v213,v245,v85
v_cvt_pk_bf16_f32 v214,v246,v86
v_cvt_pk_bf16_f32 v215,v247,v87
v_cvt_pk_bf16_f32 v216,v248,v88
v_cvt_pk_bf16_f32 v217,v249,v89
v_cvt_pk_bf16_f32 v218,v250,v90
v_cvt_pk_bf16_f32 v219,v251,v91
v_cvt_pk_bf16_f32 v220,v252,v92
v_cvt_pk_bf16_f32 v221,v253,v93
v_cvt_pk_bf16_f32 v222,v254,v94
v_cvt_pk_bf16_f32 v223,v255,v95
ds_write_b64 v20,v[200:201] offset:0
ds_write_b64 v21,v[202:203] offset:0
ds_write_b64 v22,v[204:205] offset:0
ds_write_b64 v23,v[206:207] offset:0
ds_write_b64 v20,v[208:209] offset:64
ds_write_b64 v21,v[210:211] offset:64
ds_write_b64 v22,v[212:213] offset:64
ds_write_b64 v23,v[214:215] offset:64
ds_write_b64 v20,v[216:217] offset:128
ds_write_b64 v21,v[218:219] offset:128
ds_write_b64 v22,v[220:221] offset:128
ds_write_b64 v23,v[222:223] offset:128
ds_read_b128 v[72:75],v19 offset:0
ds_read_b128 v[76:79],v19 offset:1024
ds_read_b128 v[80:83],v19 offset:2048
ds_read_b128 v[84:87],v19 offset:3456
ds_read_b128 v[88:91],v19 offset:4480
ds_read_b128 v[92:95],v19 offset:5504
v_add_u32 v20,s54,v20
v_add_u32 v21,s54,v21
v_add_u32 v22,s54,v22
v_add_u32 v23,s54,v23
s_sub_u32 s68,s68,s54
s_add_u32 s59,s59,1572864
s_add_u32 s53,s53,1
s_cmp_eq_u32 s53,3
s_cselect_b32 s59,0x70000000,s59
s_add_u32 s67,s69,98304
s_waitcnt lgkmcnt(5)
buffer_store_dwordx4 v[72:75],v24,s[40:43],s69 offen nt
s_waitcnt lgkmcnt(4)
buffer_store_dwordx4 v[76:79],v25,s[40:43],s69 offen nt
s_waitcnt lgkmcnt(3)
buffer_store_dwordx4 v[80:83],v26,s[40:43],s69 offen nt
s_waitcnt lgkmcnt(2)
buffer_store_dwordx4 v[84:87],v24,s[40:43],s67 offen nt
s_waitcnt lgkmcnt(1)
buffer_store_dwordx4 v[88:91],v25,s[40:43],s67 offen nt
s_waitcnt lgkmcnt(0)
buffer_store_dwordx4 v[92:95],v26,s[40:43],s67 offen nt
v_add_u32 v19,s54,v19
s_add_u32 s69,s69,1572864
s_sub_u32 s54,0,s54
s_cmp_lt_u32 s53,4
s_cbranch_scc1 L_pass0_%=
s_branch L_end_%=
L_pass1_%=:
s_waitcnt vmcnt(6)
s_barrier
ds_read_b128 v[200:203],v4 offset:0
ds_read_b128 v[204:207],v8 offset:0
ds_read_b128 v[208:211],v12 offset:0
ds_read_b128 v[212:215],v5 offset:0
ds_read_b128 v[216:219],v9 offset:0
ds_read_b128 v[220:223],v13 offset:0
ds_read_b128 v[96:99],v18 offset:1920
ds_read_b128 v[100:103],v18 offset:1952
ds_read_b128 v[104:107],v18 offset:1984
ds_read_b128 v[108:111],v18 offset:2016
ds_read_b128 v[112:115],v18 offset:2048
ds_read_b128 v[116:119],v18 offset:2080
ds_read_b128 v[120:123],v18 offset:2112
ds_read_b128 v[124:127],v18 offset:2144
ds_read_b128 v[128:131],v18 offset:2176
ds_read_b128 v[132:135],v18 offset:2208
ds_read_b128 v[136:139],v18 offset:2240
ds_read_b128 v[140:143],v18 offset:2272
v_and_b32 v72,v32,v36
v_and_b32 v73,v33,v36
v_and_b32 v74,v34,v36
v_and_b32 v75,v35,v36
v_and_b32 v76,v32,v48
v_and_b32 v77,v33,v48
v_and_b32 v78,v34,v48
v_and_b32 v79,v35,v48
v_and_b32 v80,v32,v60
v_and_b32 v81,v33,v60
v_and_b32 v82,v34,v60
v_and_b32 v83,v35,v60
v_and_b32 v84,v32,v37
v_and_b32 v85,v33,v37
v_and_b32 v86,v34,v37
v_and_b32 v87,v35,v37
v_and_b32 v88,v32,v49
v_and_b32 v89,v33,v49
v_and_b32 v90,v34,v49
v_and_b32 v91,v35,v49
v_and_b32 v92,v32,v61
v_and_b32 v93,v33,v61
v_and_b32 v94,v34,v61
v_and_b32 v95,v35,v61
s_setprio 1
s_waitcnt lgkmcnt(15)
v_mfma_f32_16x16x32_bf16 v[144:147],v[72:75],v[200:203],0
v_mfma_f32_16x16x32_bf16 v[144:147],v[76:79],v[204:207],v[144:147]
v_mfma_f32_16x16x32_bf16 v[144:147],v[80:83],v[208:211],v[144:147]
ds_read_b128 v[200:203],v6 offset:0
ds_read_b128 v[204:207],v10 offset:0
ds_read_b128 v[208:211],v14 offset:0
s_setprio 0
v_and_b32 v72,v32,v38
v_and_b32 v73,v33,v38
v_and_b32 v74,v34,v38
v_and_b32 v75,v35,v38
v_and_b32 v76,v32,v50
v_and_b32 v77,v33,v50
v_and_b32 v78,v34,v50
v_and_b32 v79,v35,v50
v_and_b32 v80,v32,v62
v_and_b32 v81,v33,v62
v_and_b32 v82,v34,v62
v_and_b32 v83,v35,v62
v_mul_f32 v237,v144,v144
v_mul_f32 v236,v145,v145
v_mul_f32 v239,v146,v146
v_mul_f32 v238,v147,v147
s_setprio 1
s_waitcnt lgkmcnt(15)
v_mfma_f32_16x16x32_bf16 v[148:151],v[84:87],v[212:215],0
v_mfma_f32_16x16x32_bf16 v[148:151],v[88:91],v[216:219],v[148:151]
v_mfma_f32_16x16x32_bf16 v[148:151],v[92:95],v[220:223],v[148:151]
ds_read_b128 v[212:215],v7 offset:0
ds_read_b128 v[216:219],v11 offset:0
ds_read_b128 v[220:223],v15 offset:0
s_setprio 0
v_and_b32 v84,v32,v39
v_and_b32 v85,v33,v39
v_and_b32 v86,v34,v39
v_and_b32 v87,v35,v39
v_and_b32 v88,v32,v51
v_and_b32 v89,v33,v51
v_and_b32 v90,v34,v51
v_and_b32 v91,v35,v51
v_and_b32 v92,v32,v63
v_and_b32 v93,v33,v63
v_and_b32 v94,v34,v63
v_and_b32 v95,v35,v63
v_add_f32 v232,v144,v148
v_fmac_f32 v237,v148,v148
v_add_f32 v233,v145,v149
v_fmac_f32 v236,v149,v149
v_add_f32 v234,v146,v150
v_fmac_f32 v239,v150,v150
v_add_f32 v235,v147,v151
v_fmac_f32 v238,v151,v151
s_setprio 1
s_waitcnt lgkmcnt(3)
v_mfma_f32_16x16x32_bf16 v[152:155],v[72:75],v[200:203],0
v_mfma_f32_16x16x32_bf16 v[152:155],v[76:79],v[204:207],v[152:155]
v_mfma_f32_16x16x32_bf16 v[152:155],v[80:83],v[208:211],v[152:155]
ds_read_b128 v[200:203],v4 offset:64
ds_read_b128 v[204:207],v8 offset:64
ds_read_b128 v[208:211],v12 offset:64
s_setprio 0
v_and_b32 v72,v32,v40
v_and_b32 v73,v33,v40
v_and_b32 v74,v34,v40
v_and_b32 v75,v35,v40
v_and_b32 v76,v32,v52
v_and_b32 v77,v33,v52
v_and_b32 v78,v34,v52
v_and_b32 v79,v35,v52
v_and_b32 v80,v32,v64
v_and_b32 v81,v33,v64
v_and_b32 v82,v34,v64
v_and_b32 v83,v35,v64
v_add_f32 v232,v232,v152
v_fmac_f32 v237,v152,v152
v_add_f32 v233,v233,v153
v_fmac_f32 v236,v153,v153
v_add_f32 v234,v234,v154
v_fmac_f32 v239,v154,v154
v_add_f32 v235,v235,v155
v_fmac_f32 v238,v155,v155
s_setprio 1
s_waitcnt lgkmcnt(3)
v_mfma_f32_16x16x32_bf16 v[156:159],v[84:87],v[212:215],0
v_mfma_f32_16x16x32_bf16 v[156:159],v[88:91],v[216:219],v[156:159]
v_mfma_f32_16x16x32_bf16 v[156:159],v[92:95],v[220:223],v[156:159]
ds_read_b128 v[212:215],v5 offset:64
ds_read_b128 v[216:219],v9 offset:64
ds_read_b128 v[220:223],v13 offset:64
s_setprio 0
v_and_b32 v84,v32,v41
v_and_b32 v85,v33,v41
v_and_b32 v86,v34,v41
v_and_b32 v87,v35,v41
v_and_b32 v88,v32,v53
v_and_b32 v89,v33,v53
v_and_b32 v90,v34,v53
v_and_b32 v91,v35,v53
v_and_b32 v92,v32,v65
v_and_b32 v93,v33,v65
v_and_b32 v94,v34,v65
v_and_b32 v95,v35,v65
v_add_f32 v232,v232,v156
v_fmac_f32 v237,v156,v156
v_add_f32 v233,v233,v157
v_fmac_f32 v236,v157,v157
v_add_f32 v234,v234,v158
v_fmac_f32 v239,v158,v158
v_add_f32 v235,v235,v159
v_fmac_f32 v238,v159,v159
s_setprio 1
s_waitcnt lgkmcnt(3)
v_mfma_f32_16x16x32_bf16 v[160:163],v[72:75],v[200:203],0
v_mfma_f32_16x16x32_bf16 v[160:163],v[76:79],v[204:207],v[160:163]
v_mfma_f32_16x16x32_bf16 v[160:163],v[80:83],v[208:211],v[160:163]
ds_read_b128 v[200:203],v6 offset:64
ds_read_b128 v[204:207],v10 offset:64
ds_read_b128 v[208:211],v14 offset:64
s_setprio 0
v_and_b32 v72,v32,v42
v_and_b32 v73,v33,v42
v_and_b32 v74,v34,v42
v_and_b32 v75,v35,v42
v_and_b32 v76,v32,v54
v_and_b32 v77,v33,v54
v_and_b32 v78,v34,v54
v_and_b32 v79,v35,v54
v_and_b32 v80,v32,v66
v_and_b32 v81,v33,v66
v_and_b32 v82,v34,v66
v_and_b32 v83,v35,v66
v_add_f32 v232,v232,v160
v_fmac_f32 v237,v160,v160
v_add_f32 v233,v233,v161
v_fmac_f32 v236,v161,v161
v_add_f32 v234,v234,v162
v_fmac_f32 v239,v162,v162
v_add_f32 v235,v235,v163
v_fmac_f32 v238,v163,v163
s_setprio 1
s_waitcnt lgkmcnt(3)
v_mfma_f32_16x16x32_bf16 v[164:167],v[84:87],v[212:215],0
v_mfma_f32_16x16x32_bf16 v[164:167],v[88:91],v[216:219],v[164:167]
v_mfma_f32_16x16x32_bf16 v[164:167],v[92:95],v[220:223],v[164:167]
ds_read_b128 v[212:215],v7 offset:64
ds_read_b128 v[216:219],v11 offset:64
ds_read_b128 v[220:223],v15 offset:64
s_setprio 0
v_and_b32 v84,v32,v43
v_and_b32 v85,v33,v43
v_and_b32 v86,v34,v43
v_and_b32 v87,v35,v43
v_and_b32 v88,v32,v55
v_and_b32 v89,v33,v55
v_and_b32 v90,v34,v55
v_and_b32 v91,v35,v55
v_and_b32 v92,v32,v67
v_and_b32 v93,v33,v67
v_and_b32 v94,v34,v67
v_and_b32 v95,v35,v67
v_add_f32 v232,v232,v164
v_fmac_f32 v237,v164,v164
v_add_f32 v233,v233,v165
v_fmac_f32 v236,v165,v165
v_add_f32 v234,v234,v166
v_fmac_f32 v239,v166,v166
v_add_f32 v235,v235,v167
v_fmac_f32 v238,v167,v167
s_setprio 1
s_waitcnt lgkmcnt(3)
v_mfma_f32_16x16x32_bf16 v[168:171],v[72:75],v[200:203],0
v_mfma_f32_16x16x32_bf16 v[168:171],v[76:79],v[204:207],v[168:171]
v_mfma_f32_16x16x32_bf16 v[168:171],v[80:83],v[208:211],v[168:171]
ds_read_b128 v[200:203],v4 offset:128
ds_read_b128 v[204:207],v8 offset:128
ds_read_b128 v[208:211],v12 offset:128
s_setprio 0
v_and_b32 v72,v32,v44
v_and_b32 v73,v33,v44
v_and_b32 v74,v34,v44
v_and_b32 v75,v35,v44
v_and_b32 v76,v32,v56
v_and_b32 v77,v33,v56
v_and_b32 v78,v34,v56
v_and_b32 v79,v35,v56
v_and_b32 v80,v32,v68
v_and_b32 v81,v33,v68
v_and_b32 v82,v34,v68
v_and_b32 v83,v35,v68
v_add_f32 v232,v232,v168
v_fmac_f32 v237,v168,v168
v_add_f32 v233,v233,v169
v_fmac_f32 v236,v169,v169
v_add_f32 v234,v234,v170
v_fmac_f32 v239,v170,v170
v_add_f32 v235,v235,v171
v_fmac_f32 v238,v171,v171
s_setprio 1
s_waitcnt lgkmcnt(3)
v_mfma_f32_16x16x32_bf16 v[172:175],v[84:87],v[212:215],0
v_mfma_f32_16x16x32_bf16 v[172:175],v[88:91],v[216:219],v[172:175]
v_mfma_f32_16x16x32_bf16 v[172:175],v[92:95],v[220:223],v[172:175]
ds_read_b128 v[212:215],v5 offset:128
ds_read_b128 v[216:219],v9 offset:128
ds_read_b128 v[220:223],v13 offset:128
s_setprio 0
v_and_b32 v84,v32,v45
v_and_b32 v85,v33,v45
v_and_b32 v86,v34,v45
v_and_b32 v87,v35,v45
v_and_b32 v88,v32,v57
v_and_b32 v89,v33,v57
v_and_b32 v90,v34,v57
v_and_b32 v91,v35,v57
v_and_b32 v92,v32,v69
v_and_b32 v93,v33,v69
v_and_b32 v94,v34,v69
v_and_b32 v95,v35,v69
v_add_f32 v232,v232,v172
v_fmac_f32 v237,v172,v172
v_add_f32 v233,v233,v173
v_fmac_f32 v236,v173,v173
v_add_f32 v234,v234,v174
v_fmac_f32 v239,v174,v174
v_add_f32 v235,v235,v175
v_fmac_f32 v238,v175,v175
s_setprio 1
s_waitcnt lgkmcnt(3)
v_mfma_f32_16x16x32_bf16 v[176:179],v[72:75],v[200:203],0
v_mfma_f32_16x16x32_bf16 v[176:179],v[76:79],v[204:207],v[176:179]
v_mfma_f32_16x16x32_bf16 v[176:179],v[80:83],v[208:211],v[176:179]
ds_read_b128 v[200:203],v6 offset:128
ds_read_b128 v[204:207],v10 offset:128
ds_read_b128 v[208:211],v14 offset:128
s_setprio 0
v_and_b32 v72,v32,v46
v_and_b32 v73,v33,v46
v_and_b32 v74,v34,v46
v_and_b32 v75,v35,v46
v_and_b32 v76,v32,v58
v_and_b32 v77,v33,v58
v_and_b32 v78,v34,v58
v_and_b32 v79,v35,v58
v_and_b32 v80,v32,v70
v_and_b32 v81,v33,v70
v_and_b32 v82,v34,v70
v_and_b32 v83,v35,v70
v_add_f32 v232,v232,v176
v_fmac_f32 v237,v176,v176
v_add_f32 v233,v233,v177
v_fmac_f32 v236,v177,v177
v_add_f32 v234,v234,v178
v_fmac_f32 v239,v178,v178
v_add_f32 v235,v235,v179
v_fmac_f32 v238,v179,v179
s_setprio 1
s_waitcnt lgkmcnt(3)
v_mfma_f32_16x16x32_bf16 v[180:183],v[84:87],v[212:215],0
v_mfma_f32_16x16x32_bf16 v[180:183],v[88:91],v[216:219],v[180:183]
v_mfma_f32_16x16x32_bf16 v[180:183],v[92:95],v[220:223],v[180:183]
ds_read_b128 v[212:215],v7 offset:128
ds_read_b128 v[216:219],v11 offset:128
ds_read_b128 v[220:223],v15 offset:128
s_setprio 0
v_and_b32 v84,v32,v47
v_and_b32 v85,v33,v47
v_and_b32 v86,v34,v47
v_and_b32 v87,v35,v47
v_and_b32 v88,v32,v59
v_and_b32 v89,v33,v59
v_and_b32 v90,v34,v59
v_and_b32 v91,v35,v59
v_and_b32 v92,v32,v71
v_and_b32 v93,v33,v71
v_and_b32 v94,v34,v71
v_and_b32 v95,v35,v71
v_add_f32 v232,v232,v180
v_fmac_f32 v237,v180,v180
v_add_f32 v233,v233,v181
v_fmac_f32 v236,v181,v181
v_add_f32 v234,v234,v182
v_fmac_f32 v239,v182,v182
v_add_f32 v235,v235,v183
v_fmac_f32 v238,v183,v183
s_setprio 1
s_waitcnt lgkmcnt(3)
v_mfma_f32_16x16x32_bf16 v[184:187],v[72:75],v[200:203],0
v_mfma_f32_16x16x32_bf16 v[184:187],v[76:79],v[204:207],v[184:187]
v_mfma_f32_16x16x32_bf16 v[184:187],v[80:83],v[208:211],v[184:187]
s_setprio 0
s_nop 7
v_add_f32 v232,v232,v184
v_fmac_f32 v237,v184,v184
v_add_f32 v233,v233,v185
v_fmac_f32 v236,v185,v185
v_add_f32 v234,v234,v186
v_fmac_f32 v239,v186,v186
v_add_f32 v235,v235,v187
v_fmac_f32 v238,v187,v187
s_setprio 1
s_waitcnt lgkmcnt(0)
v_mfma_f32_16x16x32_bf16 v[188:191],v[84:87],v[212:215],0
v_mfma_f32_16x16x32_bf16 v[188:191],v[88:91],v[216:219],v[188:191]
v_mfma_f32_16x16x32_bf16 v[188:191],v[92:95],v[220:223],v[188:191]
s_setprio 0
ds_read_b64_tr_b16 v[200:201],v16 offset:0
ds_read_b64_tr_b16 v[202:203],v16 offset:512
ds_read_b64_tr_b16 v[204:205],v16 offset:1024
ds_read_b64_tr_b16 v[206:207],v16 offset:1536
ds_read_b64_tr_b16 v[208:209],v16 offset:2048
ds_read_b64_tr_b16 v[210:211],v16 offset:2560
ds_read_b64_tr_b16 v[212:213],v16 offset:3072
ds_read_b64_tr_b16 v[214:215],v16 offset:3584
ds_read_b64_tr_b16 v[216:217],v16 offset:4096
ds_read_b64_tr_b16 v[218:219],v16 offset:4608
ds_read_b64_tr_b16 v[220:221],v16 offset:5120
ds_read_b64_tr_b16 v[222:223],v16 offset:5632
v_add_f32 v232,v232,v188
v_fmac_f32 v237,v188,v188
v_add_f32 v233,v233,v189
v_fmac_f32 v236,v189,v189
v_add_f32 v234,v234,v190
v_fmac_f32 v239,v190,v190
v_add_f32 v235,v235,v191
v_fmac_f32 v238,v191,v191
v_add_f32 v232,v232,v233
v_add_f32 v234,v234,v235
v_add_f32 v237,v237,v236
v_add_f32 v239,v239,v238
v_add_f32 v240,v232,v234
v_add_f32 v241,v237,v239
s_nop 1
v_permlane32_swap_b32 v240,v241
v_add_f32 v240,v240,v241
v_mov_b32 v241,v240
s_nop 1
v_permlane32_swap_b32 v240,v241
v_mul_f32 v240,0x3c2aaaab,v240
v_mul_f32 v241,0x3c2aaaab,v241
v_fma_f32 v241,-v240,v240,v241
v_max_f32 v241,0,v241
v_add_f32 v241,0x3727c5ac,v241
v_rsq_f32 v241,v241
s_nop 0
v_mul_f32 v242,0xbeeff558,v241
v_mul_f32 v243,0x3eeff558,v241
v_mul_f32 v243,v240,v243
v_fma_f32 v144,v144,v242,v243
v_fma_f32 v145,v145,v242,v243
v_fma_f32 v146,v146,v242,v243
v_fma_f32 v147,v147,v242,v243
v_fma_f32 v148,v148,v242,v243
v_fma_f32 v149,v149,v242,v243
v_fma_f32 v150,v150,v242,v243
v_fma_f32 v151,v151,v242,v243
v_fma_f32 v152,v152,v242,v243
v_fma_f32 v153,v153,v242,v243
v_fma_f32 v154,v154,v242,v243
v_fma_f32 v155,v155,v242,v243
v_fma_f32 v156,v156,v242,v243
v_fma_f32 v157,v157,v242,v243
v_fma_f32 v158,v158,v242,v243
v_fma_f32 v159,v159,v242,v243
v_fma_f32 v160,v160,v242,v243
v_fma_f32 v161,v161,v242,v243
v_fma_f32 v162,v162,v242,v243
v_fma_f32 v163,v163,v242,v243
v_fma_f32 v164,v164,v242,v243
v_fma_f32 v165,v165,v242,v243
v_fma_f32 v166,v166,v242,v243
v_fma_f32 v167,v167,v242,v243
v_fma_f32 v168,v168,v242,v243
v_fma_f32 v169,v169,v242,v243
v_fma_f32 v170,v170,v242,v243
v_fma_f32 v171,v171,v242,v243
v_fma_f32 v172,v172,v242,v243
v_fma_f32 v173,v173,v242,v243
v_fma_f32 v174,v174,v242,v243
v_fma_f32 v175,v175,v242,v243
v_fma_f32 v176,v176,v242,v243
v_fma_f32 v177,v177,v242,v243
v_fma_f32 v178,v178,v242,v243
v_fma_f32 v179,v179,v242,v243
v_fma_f32 v180,v180,v242,v243
v_fma_f32 v181,v181,v242,v243
v_fma_f32 v182,v182,v242,v243
v_fma_f32 v183,v183,v242,v243
v_fma_f32 v184,v184,v242,v243
v_fma_f32 v185,v185,v242,v243
v_fma_f32 v186,v186,v242,v243
v_fma_f32 v187,v187,v242,v243
v_fma_f32 v188,v188,v242,v243
v_fma_f32 v189,v189,v242,v243
v_fma_f32 v190,v190,v242,v243
v_fma_f32 v191,v191,v242,v243
s_setprio 1
v_cvt_pk_bf16_f32 v72,v144,v145
v_cvt_pk_bf16_f32 v73,v146,v147
v_cvt_pk_bf16_f32 v74,v148,v149
v_cvt_pk_bf16_f32 v75,v150,v151
v_cvt_pk_bf16_f32 v76,v152,v153
v_cvt_pk_bf16_f32 v77,v154,v155
v_cvt_pk_bf16_f32 v78,v156,v157
v_cvt_pk_bf16_f32 v79,v158,v159
ds_read_b128 v[144:147],v18 offset:0
ds_read_b128 v[148:151],v18 offset:32
ds_read_b128 v[152:155],v18 offset:64
ds_read_b128 v[156:159],v18 offset:96
v_cvt_pk_bf16_f32 v80,v160,v161
v_cvt_pk_bf16_f32 v81,v162,v163
v_cvt_pk_bf16_f32 v82,v164,v165
v_cvt_pk_bf16_f32 v83,v166,v167
v_cvt_pk_bf16_f32 v84,v168,v169
v_cvt_pk_bf16_f32 v85,v170,v171
v_cvt_pk_bf16_f32 v86,v172,v173
v_cvt_pk_bf16_f32 v87,v174,v175
s_waitcnt lgkmcnt(0)
v_mfma_f32_32x32x16_bf16 v[144:159],v[200:203],v[72:75],v[144:159]
v_cvt_pk_bf16_f32 v88,v176,v177
v_cvt_pk_bf16_f32 v89,v178,v179
v_cvt_pk_bf16_f32 v90,v180,v181
v_cvt_pk_bf16_f32 v91,v182,v183
v_mfma_f32_32x32x16_bf16 v[144:159],v[204:207],v[76:79],v[144:159]
v_cvt_pk_bf16_f32 v92,v184,v185
v_cvt_pk_bf16_f32 v93,v186,v187
v_cvt_pk_bf16_f32 v94,v188,v189
v_cvt_pk_bf16_f32 v95,v190,v191
v_mfma_f32_32x32x16_bf16 v[144:159],v[208:211],v[80:83],v[144:159]
v_mfma_f32_32x32x16_bf16 v[144:159],v[212:215],v[84:87],v[144:159]
v_mfma_f32_32x32x16_bf16 v[144:159],v[216:219],v[88:91],v[144:159]
v_mfma_f32_32x32x16_bf16 v[144:159],v[220:223],v[92:95],v[144:159]
s_setprio 0
s_nop 7
s_nop 1
s_waitcnt vmcnt(0)
s_barrier
v_fmaak_f32 v176,v144,v144,0x409d30fc
v_fmaak_f32 v177,v145,v145,0x409d30fc
v_fmaak_f32 v178,v146,v146,0x409d30fc
v_fmaak_f32 v179,v147,v147,0x409d30fc
v_fmaak_f32 v180,v148,v148,0x409d30fc
v_fmaak_f32 v181,v149,v149,0x409d30fc
v_fmaak_f32 v182,v150,v150,0x409d30fc
v_fmaak_f32 v183,v151,v151,0x409d30fc
v_fmaak_f32 v184,v152,v152,0x409d30fc
v_fmaak_f32 v185,v153,v153,0x409d30fc
v_fmaak_f32 v186,v154,v154,0x409d30fc
v_fmaak_f32 v187,v155,v155,0x409d30fc
v_fmaak_f32 v188,v156,v156,0x409d30fc
v_fmaak_f32 v189,v157,v157,0x409d30fc
v_fmaak_f32 v190,v158,v158,0x409d30fc
v_fmaak_f32 v191,v159,v159,0x409d30fc
v_mul_f32 v176,v144,v176
v_mul_f32 v177,v145,v177
v_mul_f32 v178,v146,v178
v_mul_f32 v179,v147,v179
v_mul_f32 v180,v148,v180
v_mul_f32 v181,v149,v181
v_mul_f32 v182,v150,v182
v_mul_f32 v183,v151,v183
v_mul_f32 v184,v152,v184
v_mul_f32 v185,v153,v185
v_mul_f32 v186,v154,v186
v_mul_f32 v187,v155,v187
v_mul_f32 v188,v156,v188
v_mul_f32 v189,v157,v189
v_mul_f32 v190,v158,v190
v_mul_f32 v191,v159,v191
s_setprio 1
v_exp_f32 v176,v176
v_exp_f32 v177,v177
v_exp_f32 v178,v178
v_exp_f32 v179,v179
v_exp_f32 v180,v180
v_exp_f32 v181,v181
v_exp_f32 v182,v182
v_exp_f32 v183,v183
v_exp_f32 v184,v184
v_exp_f32 v185,v185
v_exp_f32 v186,v186
v_exp_f32 v187,v187
v_exp_f32 v188,v188
v_exp_f32 v189,v189
v_exp_f32 v190,v190
v_exp_f32 v191,v191
ds_read_b64_tr_b16 v[200:201],v16 offset:6144
ds_read_b64_tr_b16 v[202:203],v16 offset:6656
ds_read_b64_tr_b16 v[204:205],v16 offset:7168
ds_read_b64_tr_b16 v[206:207],v16 offset:7680
ds_read_b64_tr_b16 v[208:209],v16 offset:8192
ds_read_b64_tr_b16 v[210:211],v16 offset:8704
ds_read_b64_tr_b16 v[212:213],v16 offset:9216
ds_read_b64_tr_b16 v[214:215],v16 offset:9728
ds_read_b64_tr_b16 v[216:217],v16 offset:10240
ds_read_b64_tr_b16 v[218:219],v16 offset:10752
ds_read_b64_tr_b16 v[220:221],v16 offset:11264
ds_read_b64_tr_b16 v[222:223],v16 offset:11776
ds_read_b128 v[160:163],v18 offset:128
ds_read_b128 v[164:167],v18 offset:160
ds_read_b128 v[168:171],v18 offset:192
ds_read_b128 v[172:175],v18 offset:224
s_mov_b64 exec,s[62:63]
s_add_u32 m0,s68,0
s_add_u32 s64,s59,0
buffer_load_dwordx4 v27,s[36:39],s64 offen lds
s_mov_b64 exec,-1
s_setprio 0
v_add_f32 v176,1.0,v176
v_add_f32 v177,1.0,v177
v_add_f32 v178,1.0,v178
v_add_f32 v179,1.0,v179
v_add_f32 v180,1.0,v180
v_add_f32 v181,1.0,v181
v_add_f32 v182,1.0,v182
v_add_f32 v183,1.0,v183
v_add_f32 v184,1.0,v184
v_add_f32 v185,1.0,v185
v_add_f32 v186,1.0,v186
v_add_f32 v187,1.0,v187
v_add_f32 v188,1.0,v188
v_add_f32 v189,1.0,v189
v_add_f32 v190,1.0,v190
v_add_f32 v191,1.0,v191
s_setprio 1
s_waitcnt lgkmcnt(0)
v_mfma_f32_32x32x16_bf16 v[160:175],v[200:203],v[72:75],v[160:175]
v_rcp_f32 v176,v176
v_rcp_f32 v177,v177
v_rcp_f32 v178,v178
v_mfma_f32_32x32x16_bf16 v[160:175],v[204:207],v[76:79],v[160:175]
v_rcp_f32 v179,v179
v_rcp_f32 v180,v180
v_rcp_f32 v181,v181
v_mfma_f32_32x32x16_bf16 v[160:175],v[208:211],v[80:83],v[160:175]
v_rcp_f32 v182,v182
v_rcp_f32 v183,v183
v_rcp_f32 v184,v184
v_mfma_f32_32x32x16_bf16 v[160:175],v[212:215],v[84:87],v[160:175]
v_rcp_f32 v185,v185
v_rcp_f32 v186,v186
v_rcp_f32 v187,v187
v_mfma_f32_32x32x16_bf16 v[160:175],v[216:219],v[88:91],v[160:175]
v_rcp_f32 v188,v188
v_rcp_f32 v189,v189
v_mfma_f32_32x32x16_bf16 v[160:175],v[220:223],v[92:95],v[160:175]
v_rcp_f32 v190,v190
v_rcp_f32 v191,v191
ds_read_b64_tr_b16 v[200:201],v17 offset:0
ds_read_b64_tr_b16 v[202:203],v17 offset:1536
ds_read_b64_tr_b16 v[204:205],v17 offset:3072
ds_read_b64_tr_b16 v[206:207],v17 offset:4608
ds_read_b64_tr_b16 v[208:209],v17 offset:64
ds_read_b64_tr_b16 v[210:211],v17 offset:1600
ds_read_b64_tr_b16 v[212:213],v17 offset:3136
ds_read_b64_tr_b16 v[214:215],v17 offset:4672
ds_read_b64_tr_b16 v[216:217],v17 offset:128
ds_read_b64_tr_b16 v[218:219],v17 offset:1664
ds_read_b64_tr_b16 v[220:221],v17 offset:3200
ds_read_b64_tr_b16 v[222:223],v17 offset:4736
s_mov_b64 exec,s[62:63]
s_add_u32 m0,s68,3456
s_add_u32 s64,s59,98304
buffer_load_dwordx4 v27,s[36:39],s64 offen lds
s_mov_b64 exec,-1
s_setprio 0
v_mul_f32 v176,v144,v176
v_mul_f32 v177,v145,v177
v_mul_f32 v178,v146,v178
v_mul_f32 v179,v147,v179
v_mul_f32 v180,v148,v180
v_mul_f32 v181,v149,v181
v_mul_f32 v182,v150,v182
v_mul_f32 v183,v151,v183
v_mul_f32 v184,v152,v184
v_mul_f32 v185,v153,v185
v_mul_f32 v186,v154,v186
v_mul_f32 v187,v155,v187
v_mul_f32 v188,v156,v188
v_mul_f32 v189,v157,v189
v_mul_f32 v190,v158,v190
v_mul_f32 v191,v159,v191
v_cvt_pk_bf16_f32 v192,v176,v177
v_cvt_pk_bf16_f32 v193,v178,v179
v_cvt_pk_bf16_f32 v194,v180,v181
v_cvt_pk_bf16_f32 v195,v182,v183
v_cvt_pk_bf16_f32 v196,v184,v185
v_cvt_pk_bf16_f32 v197,v186,v187
v_cvt_pk_bf16_f32 v198,v188,v189
v_cvt_pk_bf16_f32 v199,v190,v191
s_waitcnt lgkmcnt(0)
s_barrier
v_fmaak_f32 v176,v160,v160,0x409d30fc
v_fmaak_f32 v177,v161,v161,0x409d30fc
v_fmaak_f32 v178,v162,v162,0x409d30fc
v_fmaak_f32 v179,v163,v163,0x409d30fc
v_fmaak_f32 v180,v164,v164,0x409d30fc
v_fmaak_f32 v181,v165,v165,0x409d30fc
v_fmaak_f32 v182,v166,v166,0x409d30fc
v_fmaak_f32 v183,v167,v167,0x409d30fc
v_fmaak_f32 v184,v168,v168,0x409d30fc
v_fmaak_f32 v185,v169,v169,0x409d30fc
v_fmaak_f32 v186,v170,v170,0x409d30fc
v_fmaak_f32 v187,v171,v171,0x409d30fc
v_fmaak_f32 v188,v172,v172,0x409d30fc
v_fmaak_f32 v189,v173,v173,0x409d30fc
v_fmaak_f32 v190,v174,v174,0x409d30fc
v_fmaak_f32 v191,v175,v175,0x409d30fc
v_mul_f32 v176,v160,v176
v_mul_f32 v177,v161,v177
v_mul_f32 v178,v162,v178
v_mul_f32 v179,v163,v179
v_mul_f32 v180,v164,v180
v_mul_f32 v181,v165,v181
v_mul_f32 v182,v166,v182
v_mul_f32 v183,v167,v183
v_mul_f32 v184,v168,v184
v_mul_f32 v185,v169,v185
v_mul_f32 v186,v170,v186
v_mul_f32 v187,v171,v187
v_mul_f32 v188,v172,v188
v_mul_f32 v189,v173,v189
v_mul_f32 v190,v174,v190
v_mul_f32 v191,v175,v191
s_setprio 1
v_mfma_f32_32x32x16_bf16 v[96:111],v[200:203],v[192:195],v[96:111]
v_exp_f32 v176,v176
v_exp_f32 v177,v177
v_exp_f32 v178,v178
v_mfma_f32_32x32x16_bf16 v[96:111],v[204:207],v[196:199],v[96:111]
v_exp_f32 v179,v179
v_exp_f32 v180,v180
v_exp_f32 v181,v181
v_mfma_f32_32x32x16_bf16 v[112:127],v[208:211],v[192:195],v[112:127]
v_exp_f32 v182,v182
v_exp_f32 v183,v183
v_exp_f32 v184,v184
v_mfma_f32_32x32x16_bf16 v[112:127],v[212:215],v[196:199],v[112:127]
v_exp_f32 v185,v185
v_exp_f32 v186,v186
v_exp_f32 v187,v187
v_mfma_f32_32x32x16_bf16 v[128:143],v[216:219],v[192:195],v[128:143]
v_exp_f32 v188,v188
v_exp_f32 v189,v189
v_mfma_f32_32x32x16_bf16 v[128:143],v[220:223],v[196:199],v[128:143]
v_exp_f32 v190,v190
v_exp_f32 v191,v191
ds_read_b64_tr_b16 v[200:201],v16 offset:12288
ds_read_b64_tr_b16 v[202:203],v16 offset:12800
ds_read_b64_tr_b16 v[204:205],v16 offset:13312
ds_read_b64_tr_b16 v[206:207],v16 offset:13824
ds_read_b64_tr_b16 v[208:209],v16 offset:14336
ds_read_b64_tr_b16 v[210:211],v16 offset:14848
ds_read_b64_tr_b16 v[212:213],v16 offset:15360
ds_read_b64_tr_b16 v[214:215],v16 offset:15872
ds_read_b64_tr_b16 v[216:217],v16 offset:16384
ds_read_b64_tr_b16 v[218:219],v16 offset:16896
ds_read_b64_tr_b16 v[220:221],v16 offset:17408
ds_read_b64_tr_b16 v[222:223],v16 offset:17920
ds_read_b128 v[144:147],v18 offset:256
ds_read_b128 v[148:151],v18 offset:288
ds_read_b128 v[152:155],v18 offset:320
ds_read_b128 v[156:159],v18 offset:352
s_mov_b64 exec,s[62:63]
s_add_u32 m0,s68,6912
s_add_u32 s64,s59,196608
buffer_load_dwordx4 v27,s[36:39],s64 offen lds
s_mov_b64 exec,-1
s_setprio 0
v_add_f32 v176,1.0,v176
v_add_f32 v177,1.0,v177
v_add_f32 v178,1.0,v178
v_add_f32 v179,1.0,v179
v_add_f32 v180,1.0,v180
v_add_f32 v181,1.0,v181
v_add_f32 v182,1.0,v182
v_add_f32 v183,1.0,v183
v_add_f32 v184,1.0,v184
v_add_f32 v185,1.0,v185
v_add_f32 v186,1.0,v186
v_add_f32 v187,1.0,v187
v_add_f32 v188,1.0,v188
v_add_f32 v189,1.0,v189
v_add_f32 v190,1.0,v190
v_add_f32 v191,1.0,v191
s_setprio 1
s_waitcnt lgkmcnt(0)
v_mfma_f32_32x32x16_bf16 v[144:159],v[200:203],v[72:75],v[144:159]
v_rcp_f32 v176,v176
v_rcp_f32 v177,v177
v_rcp_f32 v178,v178
v_mfma_f32_32x32x16_bf16 v[144:159],v[204:207],v[76:79],v[144:159]
v_rcp_f32 v179,v179
v_rcp_f32 v180,v180
v_rcp_f32 v181,v181
v_mfma_f32_32x32x16_bf16 v[144:159],v[208:211],v[80:83],v[144:159]
v_rcp_f32 v182,v182
v_rcp_f32 v183,v183
v_rcp_f32 v184,v184
v_mfma_f32_32x32x16_bf16 v[144:159],v[212:215],v[84:87],v[144:159]
v_rcp_f32 v185,v185
v_rcp_f32 v186,v186
v_rcp_f32 v187,v187
v_mfma_f32_32x32x16_bf16 v[144:159],v[216:219],v[88:91],v[144:159]
v_rcp_f32 v188,v188
v_rcp_f32 v189,v189
v_mfma_f32_32x32x16_bf16 v[144:159],v[220:223],v[92:95],v[144:159]
v_rcp_f32 v190,v190
v_rcp_f32 v191,v191
ds_read_b64_tr_b16 v[200:201],v17 offset:6144
ds_read_b64_tr_b16 v[202:203],v17 offset:7680
ds_read_b64_tr_b16 v[204:205],v17 offset:9216
ds_read_b64_tr_b16 v[206:207],v17 offset:10752
ds_read_b64_tr_b16 v[208:209],v17 offset:6208
ds_read_b64_tr_b16 v[210:211],v17 offset:7744
ds_read_b64_tr_b16 v[212:213],v17 offset:9280
ds_read_b64_tr_b16 v[214:215],v17 offset:10816
ds_read_b64_tr_b16 v[216:217],v17 offset:6272
ds_read_b64_tr_b16 v[218:219],v17 offset:7808
ds_read_b64_tr_b16 v[220:221],v17 offset:9344
ds_read_b64_tr_b16 v[222:223],v17 offset:10880
s_mov_b64 exec,s[62:63]
s_add_u32 m0,s68,10368
s_add_u32 s64,s59,294912
buffer_load_dwordx4 v27,s[36:39],s64 offen lds
s_mov_b64 exec,-1
s_setprio 0
v_mul_f32 v176,v160,v176
v_mul_f32 v177,v161,v177
v_mul_f32 v178,v162,v178
v_mul_f32 v179,v163,v179
v_mul_f32 v180,v164,v180
v_mul_f32 v181,v165,v181
v_mul_f32 v182,v166,v182
v_mul_f32 v183,v167,v183
v_mul_f32 v184,v168,v184
v_mul_f32 v185,v169,v185
v_mul_f32 v186,v170,v186
v_mul_f32 v187,v171,v187
v_mul_f32 v188,v172,v188
v_mul_f32 v189,v173,v189
v_mul_f32 v190,v174,v190
v_mul_f32 v191,v175,v191
v_cvt_pk_bf16_f32 v192,v176,v177
v_cvt_pk_bf16_f32 v193,v178,v179
v_cvt_pk_bf16_f32 v194,v180,v181
v_cvt_pk_bf16_f32 v195,v182,v183
v_cvt_pk_bf16_f32 v196,v184,v185
v_cvt_pk_bf16_f32 v197,v186,v187
v_cvt_pk_bf16_f32 v198,v188,v189
v_cvt_pk_bf16_f32 v199,v190,v191
s_waitcnt lgkmcnt(0)
s_barrier
v_fmaak_f32 v176,v144,v144,0x409d30fc
v_fmaak_f32 v177,v145,v145,0x409d30fc
v_fmaak_f32 v178,v146,v146,0x409d30fc
v_fmaak_f32 v179,v147,v147,0x409d30fc
v_fmaak_f32 v180,v148,v148,0x409d30fc
v_fmaak_f32 v181,v149,v149,0x409d30fc
v_fmaak_f32 v182,v150,v150,0x409d30fc
v_fmaak_f32 v183,v151,v151,0x409d30fc
v_fmaak_f32 v184,v152,v152,0x409d30fc
v_fmaak_f32 v185,v153,v153,0x409d30fc
v_fmaak_f32 v186,v154,v154,0x409d30fc
v_fmaak_f32 v187,v155,v155,0x409d30fc
v_fmaak_f32 v188,v156,v156,0x409d30fc
v_fmaak_f32 v189,v157,v157,0x409d30fc
v_fmaak_f32 v190,v158,v158,0x409d30fc
v_fmaak_f32 v191,v159,v159,0x409d30fc
v_mul_f32 v176,v144,v176
v_mul_f32 v177,v145,v177
v_mul_f32 v178,v146,v178
v_mul_f32 v179,v147,v179
v_mul_f32 v180,v148,v180
v_mul_f32 v181,v149,v181
v_mul_f32 v182,v150,v182
v_mul_f32 v183,v151,v183
v_mul_f32 v184,v152,v184
v_mul_f32 v185,v153,v185
v_mul_f32 v186,v154,v186
v_mul_f32 v187,v155,v187
v_mul_f32 v188,v156,v188
v_mul_f32 v189,v157,v189
v_mul_f32 v190,v158,v190
v_mul_f32 v191,v159,v191
s_setprio 1
v_mfma_f32_32x32x16_bf16 v[96:111],v[200:203],v[192:195],v[96:111]
v_exp_f32 v176,v176
v_exp_f32 v177,v177
v_exp_f32 v178,v178
v_mfma_f32_32x32x16_bf16 v[96:111],v[204:207],v[196:199],v[96:111]
v_exp_f32 v179,v179
v_exp_f32 v180,v180
v_exp_f32 v181,v181
v_mfma_f32_32x32x16_bf16 v[112:127],v[208:211],v[192:195],v[112:127]
v_exp_f32 v182,v182
v_exp_f32 v183,v183
v_exp_f32 v184,v184
v_mfma_f32_32x32x16_bf16 v[112:127],v[212:215],v[196:199],v[112:127]
v_exp_f32 v185,v185
v_exp_f32 v186,v186
v_exp_f32 v187,v187
v_mfma_f32_32x32x16_bf16 v[128:143],v[216:219],v[192:195],v[128:143]
v_exp_f32 v188,v188
v_exp_f32 v189,v189
v_mfma_f32_32x32x16_bf16 v[128:143],v[220:223],v[196:199],v[128:143]
v_exp_f32 v190,v190
v_exp_f32 v191,v191
ds_read_b64_tr_b16 v[200:201],v16 offset:0
ds_read_b64_tr_b16 v[202:203],v16 offset:512
ds_read_b64_tr_b16 v[204:205],v16 offset:1024
ds_read_b64_tr_b16 v[206:207],v16 offset:1536
ds_read_b64_tr_b16 v[208:209],v16 offset:2048
ds_read_b64_tr_b16 v[210:211],v16 offset:2560
ds_read_b64_tr_b16 v[212:213],v16 offset:3072
ds_read_b64_tr_b16 v[214:215],v16 offset:3584
ds_read_b64_tr_b16 v[216:217],v16 offset:4096
ds_read_b64_tr_b16 v[218:219],v16 offset:4608
ds_read_b64_tr_b16 v[220:221],v16 offset:5120
ds_read_b64_tr_b16 v[222:223],v16 offset:5632
ds_read_b128 v[160:163],v18 offset:384
ds_read_b128 v[164:167],v18 offset:416
ds_read_b128 v[168:171],v18 offset:448
ds_read_b128 v[172:175],v18 offset:480
s_mov_b64 exec,s[62:63]
s_add_u32 m0,s68,13824
s_add_u32 s64,s59,393216
buffer_load_dwordx4 v27,s[36:39],s64 offen lds
s_mov_b64 exec,-1
s_setprio 0
v_add_f32 v176,1.0,v176
v_add_f32 v177,1.0,v177
v_add_f32 v178,1.0,v178
v_add_f32 v179,1.0,v179
v_add_f32 v180,1.0,v180
v_add_f32 v181,1.0,v181
v_add_f32 v182,1.0,v182
v_add_f32 v183,1.0,v183
v_add_f32 v184,1.0,v184
v_add_f32 v185,1.0,v185
v_add_f32 v186,1.0,v186
v_add_f32 v187,1.0,v187
v_add_f32 v188,1.0,v188
v_add_f32 v189,1.0,v189
v_add_f32 v190,1.0,v190
v_add_f32 v191,1.0,v191
s_setprio 1
s_waitcnt lgkmcnt(0)
v_mfma_f32_32x32x16_bf16 v[160:175],v[200:203],v[72:75],v[160:175]
v_rcp_f32 v176,v176
v_rcp_f32 v177,v177
v_rcp_f32 v178,v178
v_mfma_f32_32x32x16_bf16 v[160:175],v[204:207],v[76:79],v[160:175]
v_rcp_f32 v179,v179
v_rcp_f32 v180,v180
v_rcp_f32 v181,v181
v_mfma_f32_32x32x16_bf16 v[160:175],v[208:211],v[80:83],v[160:175]
v_rcp_f32 v182,v182
v_rcp_f32 v183,v183
v_rcp_f32 v184,v184
v_mfma_f32_32x32x16_bf16 v[160:175],v[212:215],v[84:87],v[160:175]
v_rcp_f32 v185,v185
v_rcp_f32 v186,v186
v_rcp_f32 v187,v187
v_mfma_f32_32x32x16_bf16 v[160:175],v[216:219],v[88:91],v[160:175]
v_rcp_f32 v188,v188
v_rcp_f32 v189,v189
v_mfma_f32_32x32x16_bf16 v[160:175],v[220:223],v[92:95],v[160:175]
v_rcp_f32 v190,v190
v_rcp_f32 v191,v191
ds_read_b64_tr_b16 v[200:201],v17 offset:12288
ds_read_b64_tr_b16 v[202:203],v17 offset:13824
ds_read_b64_tr_b16 v[204:205],v17 offset:15360
ds_read_b64_tr_b16 v[206:207],v17 offset:16896
ds_read_b64_tr_b16 v[208:209],v17 offset:12352
ds_read_b64_tr_b16 v[210:211],v17 offset:13888
ds_read_b64_tr_b16 v[212:213],v17 offset:15424
ds_read_b64_tr_b16 v[214:215],v17 offset:16960
ds_read_b64_tr_b16 v[216:217],v17 offset:12416
ds_read_b64_tr_b16 v[218:219],v17 offset:13952
ds_read_b64_tr_b16 v[220:221],v17 offset:15488
ds_read_b64_tr_b16 v[222:223],v17 offset:17024
s_mov_b64 exec,s[62:63]
s_add_u32 m0,s68,17280
s_add_u32 s64,s59,491520
buffer_load_dwordx4 v27,s[36:39],s64 offen lds
s_mov_b64 exec,-1
s_setprio 0
v_mul_f32 v176,v144,v176
v_mul_f32 v177,v145,v177
v_mul_f32 v178,v146,v178
v_mul_f32 v179,v147,v179
v_mul_f32 v180,v148,v180
v_mul_f32 v181,v149,v181
v_mul_f32 v182,v150,v182
v_mul_f32 v183,v151,v183
v_mul_f32 v184,v152,v184
v_mul_f32 v185,v153,v185
v_mul_f32 v186,v154,v186
v_mul_f32 v187,v155,v187
v_mul_f32 v188,v156,v188
v_mul_f32 v189,v157,v189
v_mul_f32 v190,v158,v190
v_mul_f32 v191,v159,v191
v_cvt_pk_bf16_f32 v192,v176,v177
v_cvt_pk_bf16_f32 v193,v178,v179
v_cvt_pk_bf16_f32 v194,v180,v181
v_cvt_pk_bf16_f32 v195,v182,v183
v_cvt_pk_bf16_f32 v196,v184,v185
v_cvt_pk_bf16_f32 v197,v186,v187
v_cvt_pk_bf16_f32 v198,v188,v189
v_cvt_pk_bf16_f32 v199,v190,v191
s_waitcnt lgkmcnt(0)
s_barrier
v_fmaak_f32 v176,v160,v160,0x409d30fc
v_fmaak_f32 v177,v161,v161,0x409d30fc
v_fmaak_f32 v178,v162,v162,0x409d30fc
v_fmaak_f32 v179,v163,v163,0x409d30fc
v_fmaak_f32 v180,v164,v164,0x409d30fc
v_fmaak_f32 v181,v165,v165,0x409d30fc
v_fmaak_f32 v182,v166,v166,0x409d30fc
v_fmaak_f32 v183,v167,v167,0x409d30fc
v_fmaak_f32 v184,v168,v168,0x409d30fc
v_fmaak_f32 v185,v169,v169,0x409d30fc
v_fmaak_f32 v186,v170,v170,0x409d30fc
v_fmaak_f32 v187,v171,v171,0x409d30fc
v_fmaak_f32 v188,v172,v172,0x409d30fc
v_fmaak_f32 v189,v173,v173,0x409d30fc
v_fmaak_f32 v190,v174,v174,0x409d30fc
v_fmaak_f32 v191,v175,v175,0x409d30fc
v_mul_f32 v176,v160,v176
v_mul_f32 v177,v161,v177
v_mul_f32 v178,v162,v178
v_mul_f32 v179,v163,v179
v_mul_f32 v180,v164,v180
v_mul_f32 v181,v165,v181
v_mul_f32 v182,v166,v182
v_mul_f32 v183,v167,v183
v_mul_f32 v184,v168,v184
v_mul_f32 v185,v169,v185
v_mul_f32 v186,v170,v186
v_mul_f32 v187,v171,v187
v_mul_f32 v188,v172,v188
v_mul_f32 v189,v173,v189
v_mul_f32 v190,v174,v190
v_mul_f32 v191,v175,v191
s_setprio 1
v_mfma_f32_32x32x16_bf16 v[96:111],v[200:203],v[192:195],v[96:111]
v_exp_f32 v176,v176
v_exp_f32 v177,v177
v_exp_f32 v178,v178
v_mfma_f32_32x32x16_bf16 v[96:111],v[204:207],v[196:199],v[96:111]
v_exp_f32 v179,v179
v_exp_f32 v180,v180
v_exp_f32 v181,v181
v_mfma_f32_32x32x16_bf16 v[112:127],v[208:211],v[192:195],v[112:127]
v_exp_f32 v182,v182
v_exp_f32 v183,v183
v_exp_f32 v184,v184
v_mfma_f32_32x32x16_bf16 v[112:127],v[212:215],v[196:199],v[112:127]
v_exp_f32 v185,v185
v_exp_f32 v186,v186
v_exp_f32 v187,v187
v_mfma_f32_32x32x16_bf16 v[128:143],v[216:219],v[192:195],v[128:143]
v_exp_f32 v188,v188
v_exp_f32 v189,v189
v_mfma_f32_32x32x16_bf16 v[128:143],v[220:223],v[196:199],v[128:143]
v_exp_f32 v190,v190
v_exp_f32 v191,v191
ds_read_b64_tr_b16 v[200:201],v16 offset:6144
ds_read_b64_tr_b16 v[202:203],v16 offset:6656
ds_read_b64_tr_b16 v[204:205],v16 offset:7168
ds_read_b64_tr_b16 v[206:207],v16 offset:7680
ds_read_b64_tr_b16 v[208:209],v16 offset:8192
ds_read_b64_tr_b16 v[210:211],v16 offset:8704
ds_read_b64_tr_b16 v[212:213],v16 offset:9216
ds_read_b64_tr_b16 v[214:215],v16 offset:9728
ds_read_b64_tr_b16 v[216:217],v16 offset:10240
ds_read_b64_tr_b16 v[218:219],v16 offset:10752
ds_read_b64_tr_b16 v[220:221],v16 offset:11264
ds_read_b64_tr_b16 v[222:223],v16 offset:11776
ds_read_b128 v[144:147],v18 offset:512
ds_read_b128 v[148:151],v18 offset:544
ds_read_b128 v[152:155],v18 offset:576
ds_read_b128 v[156:159],v18 offset:608
s_mov_b64 exec,s[62:63]
s_add_u32 m0,s68,20736
s_add_u32 s64,s59,589824
buffer_load_dwordx4 v27,s[36:39],s64 offen lds
s_mov_b64 exec,-1
s_setprio 0
v_add_f32 v176,1.0,v176
v_add_f32 v177,1.0,v177
v_add_f32 v178,1.0,v178
v_add_f32 v179,1.0,v179
v_add_f32 v180,1.0,v180
v_add_f32 v181,1.0,v181
v_add_f32 v182,1.0,v182
v_add_f32 v183,1.0,v183
v_add_f32 v184,1.0,v184
v_add_f32 v185,1.0,v185
v_add_f32 v186,1.0,v186
v_add_f32 v187,1.0,v187
v_add_f32 v188,1.0,v188
v_add_f32 v189,1.0,v189
v_add_f32 v190,1.0,v190
v_add_f32 v191,1.0,v191
s_setprio 1
s_waitcnt lgkmcnt(0)
v_mfma_f32_32x32x16_bf16 v[144:159],v[200:203],v[72:75],v[144:159]
v_rcp_f32 v176,v176
v_rcp_f32 v177,v177
v_rcp_f32 v178,v178
v_mfma_f32_32x32x16_bf16 v[144:159],v[204:207],v[76:79],v[144:159]
v_rcp_f32 v179,v179
v_rcp_f32 v180,v180
v_rcp_f32 v181,v181
v_mfma_f32_32x32x16_bf16 v[144:159],v[208:211],v[80:83],v[144:159]
v_rcp_f32 v182,v182
v_rcp_f32 v183,v183
v_rcp_f32 v184,v184
v_mfma_f32_32x32x16_bf16 v[144:159],v[212:215],v[84:87],v[144:159]
v_rcp_f32 v185,v185
v_rcp_f32 v186,v186
v_rcp_f32 v187,v187
v_mfma_f32_32x32x16_bf16 v[144:159],v[216:219],v[88:91],v[144:159]
v_rcp_f32 v188,v188
v_rcp_f32 v189,v189
v_mfma_f32_32x32x16_bf16 v[144:159],v[220:223],v[92:95],v[144:159]
v_rcp_f32 v190,v190
v_rcp_f32 v191,v191
ds_read_b64_tr_b16 v[200:201],v17 offset:0
ds_read_b64_tr_b16 v[202:203],v17 offset:1536
ds_read_b64_tr_b16 v[204:205],v17 offset:3072
ds_read_b64_tr_b16 v[206:207],v17 offset:4608
ds_read_b64_tr_b16 v[208:209],v17 offset:64
ds_read_b64_tr_b16 v[210:211],v17 offset:1600
ds_read_b64_tr_b16 v[212:213],v17 offset:3136
ds_read_b64_tr_b16 v[214:215],v17 offset:4672
ds_read_b64_tr_b16 v[216:217],v17 offset:128
ds_read_b64_tr_b16 v[218:219],v17 offset:1664
ds_read_b64_tr_b16 v[220:221],v17 offset:3200
ds_read_b64_tr_b16 v[222:223],v17 offset:4736
s_mov_b64 exec,s[62:63]
s_add_u32 m0,s68,24192
s_add_u32 s64,s59,688128
buffer_load_dwordx4 v27,s[36:39],s64 offen lds
s_mov_b64 exec,-1
s_setprio 0
v_mul_f32 v176,v160,v176
v_mul_f32 v177,v161,v177
v_mul_f32 v178,v162,v178
v_mul_f32 v179,v163,v179
v_mul_f32 v180,v164,v180
v_mul_f32 v181,v165,v181
v_mul_f32 v182,v166,v182
v_mul_f32 v183,v167,v183
v_mul_f32 v184,v168,v184
v_mul_f32 v185,v169,v185
v_mul_f32 v186,v170,v186
v_mul_f32 v187,v171,v187
v_mul_f32 v188,v172,v188
v_mul_f32 v189,v173,v189
v_mul_f32 v190,v174,v190
v_mul_f32 v191,v175,v191
v_cvt_pk_bf16_f32 v192,v176,v177
v_cvt_pk_bf16_f32 v193,v178,v179
v_cvt_pk_bf16_f32 v194,v180,v181
v_cvt_pk_bf16_f32 v195,v182,v183
v_cvt_pk_bf16_f32 v196,v184,v185
v_cvt_pk_bf16_f32 v197,v186,v187
v_cvt_pk_bf16_f32 v198,v188,v189
v_cvt_pk_bf16_f32 v199,v190,v191
s_waitcnt lgkmcnt(0)
s_barrier
v_fmaak_f32 v176,v144,v144,0x409d30fc
v_fmaak_f32 v177,v145,v145,0x409d30fc
v_fmaak_f32 v178,v146,v146,0x409d30fc
v_fmaak_f32 v179,v147,v147,0x409d30fc
v_fmaak_f32 v180,v148,v148,0x409d30fc
v_fmaak_f32 v181,v149,v149,0x409d30fc
v_fmaak_f32 v182,v150,v150,0x409d30fc
v_fmaak_f32 v183,v151,v151,0x409d30fc
v_fmaak_f32 v184,v152,v152,0x409d30fc
v_fmaak_f32 v185,v153,v153,0x409d30fc
v_fmaak_f32 v186,v154,v154,0x409d30fc
v_fmaak_f32 v187,v155,v155,0x409d30fc
v_fmaak_f32 v188,v156,v156,0x409d30fc
v_fmaak_f32 v189,v157,v157,0x409d30fc
v_fmaak_f32 v190,v158,v158,0x409d30fc
v_fmaak_f32 v191,v159,v159,0x409d30fc
v_mul_f32 v176,v144,v176
v_mul_f32 v177,v145,v177
v_mul_f32 v178,v146,v178
v_mul_f32 v179,v147,v179
v_mul_f32 v180,v148,v180
v_mul_f32 v181,v149,v181
v_mul_f32 v182,v150,v182
v_mul_f32 v183,v151,v183
v_mul_f32 v184,v152,v184
v_mul_f32 v185,v153,v185
v_mul_f32 v186,v154,v186
v_mul_f32 v187,v155,v187
v_mul_f32 v188,v156,v188
v_mul_f32 v189,v157,v189
v_mul_f32 v190,v158,v190
v_mul_f32 v191,v159,v191
s_setprio 1
v_mfma_f32_32x32x16_bf16 v[96:111],v[200:203],v[192:195],v[96:111]
v_exp_f32 v176,v176
v_exp_f32 v177,v177
v_exp_f32 v178,v178
v_mfma_f32_32x32x16_bf16 v[96:111],v[204:207],v[196:199],v[96:111]
v_exp_f32 v179,v179
v_exp_f32 v180,v180
v_exp_f32 v181,v181
v_mfma_f32_32x32x16_bf16 v[112:127],v[208:211],v[192:195],v[112:127]
v_exp_f32 v182,v182
v_exp_f32 v183,v183
v_exp_f32 v184,v184
v_mfma_f32_32x32x16_bf16 v[112:127],v[212:215],v[196:199],v[112:127]
v_exp_f32 v185,v185
v_exp_f32 v186,v186
v_exp_f32 v187,v187
v_mfma_f32_32x32x16_bf16 v[128:143],v[216:219],v[192:195],v[128:143]
v_exp_f32 v188,v188
v_exp_f32 v189,v189
v_mfma_f32_32x32x16_bf16 v[128:143],v[220:223],v[196:199],v[128:143]
v_exp_f32 v190,v190
v_exp_f32 v191,v191
ds_read_b64_tr_b16 v[200:201],v16 offset:12288
ds_read_b64_tr_b16 v[202:203],v16 offset:12800
ds_read_b64_tr_b16 v[204:205],v16 offset:13312
ds_read_b64_tr_b16 v[206:207],v16 offset:13824
ds_read_b64_tr_b16 v[208:209],v16 offset:14336
ds_read_b64_tr_b16 v[210:211],v16 offset:14848
ds_read_b64_tr_b16 v[212:213],v16 offset:15360
ds_read_b64_tr_b16 v[214:215],v16 offset:15872
ds_read_b64_tr_b16 v[216:217],v16 offset:16384
ds_read_b64_tr_b16 v[218:219],v16 offset:16896
ds_read_b64_tr_b16 v[220:221],v16 offset:17408
ds_read_b64_tr_b16 v[222:223],v16 offset:17920
ds_read_b128 v[160:163],v18 offset:640
ds_read_b128 v[164:167],v18 offset:672
ds_read_b128 v[168:171],v18 offset:704
ds_read_b128 v[172:175],v18 offset:736
s_mov_b64 exec,s[62:63]
s_add_u32 m0,s68,27648
s_add_u32 s64,s59,786432
buffer_load_dwordx4 v27,s[36:39],s64 offen lds
s_mov_b64 exec,-1
s_setprio 0
v_add_f32 v176,1.0,v176
v_add_f32 v177,1.0,v177
v_add_f32 v178,1.0,v178
v_add_f32 v179,1.0,v179
v_add_f32 v180,1.0,v180
v_add_f32 v181,1.0,v181
v_add_f32 v182,1.0,v182
v_add_f32 v183,1.0,v183
v_add_f32 v184,1.0,v184
v_add_f32 v185,1.0,v185
v_add_f32 v186,1.0,v186
v_add_f32 v187,1.0,v187
v_add_f32 v188,1.0,v188
v_add_f32 v189,1.0,v189
v_add_f32 v190,1.0,v190
v_add_f32 v191,1.0,v191
s_setprio 1
s_waitcnt lgkmcnt(0)
v_mfma_f32_32x32x16_bf16 v[160:175],v[200:203],v[72:75],v[160:175]
v_rcp_f32 v176,v176
v_rcp_f32 v177,v177
v_rcp_f32 v178,v178
v_mfma_f32_32x32x16_bf16 v[160:175],v[204:207],v[76:79],v[160:175]
v_rcp_f32 v179,v179
v_rcp_f32 v180,v180
v_rcp_f32 v181,v181
v_mfma_f32_32x32x16_bf16 v[160:175],v[208:211],v[80:83],v[160:175]
v_rcp_f32 v182,v182
v_rcp_f32 v183,v183
v_rcp_f32 v184,v184
v_mfma_f32_32x32x16_bf16 v[160:175],v[212:215],v[84:87],v[160:175]
v_rcp_f32 v185,v185
v_rcp_f32 v186,v186
v_rcp_f32 v187,v187
v_mfma_f32_32x32x16_bf16 v[160:175],v[216:219],v[88:91],v[160:175]
v_rcp_f32 v188,v188
v_rcp_f32 v189,v189
v_mfma_f32_32x32x16_bf16 v[160:175],v[220:223],v[92:95],v[160:175]
v_rcp_f32 v190,v190
v_rcp_f32 v191,v191
ds_read_b64_tr_b16 v[200:201],v17 offset:6144
ds_read_b64_tr_b16 v[202:203],v17 offset:7680
ds_read_b64_tr_b16 v[204:205],v17 offset:9216
ds_read_b64_tr_b16 v[206:207],v17 offset:10752
ds_read_b64_tr_b16 v[208:209],v17 offset:6208
ds_read_b64_tr_b16 v[210:211],v17 offset:7744
ds_read_b64_tr_b16 v[212:213],v17 offset:9280
ds_read_b64_tr_b16 v[214:215],v17 offset:10816
ds_read_b64_tr_b16 v[216:217],v17 offset:6272
ds_read_b64_tr_b16 v[218:219],v17 offset:7808
ds_read_b64_tr_b16 v[220:221],v17 offset:9344
ds_read_b64_tr_b16 v[222:223],v17 offset:10880
s_mov_b64 exec,s[62:63]
s_add_u32 m0,s68,31104
s_add_u32 s64,s59,884736
buffer_load_dwordx4 v27,s[36:39],s64 offen lds
s_mov_b64 exec,-1
s_setprio 0
v_mul_f32 v176,v144,v176
v_mul_f32 v177,v145,v177
v_mul_f32 v178,v146,v178
v_mul_f32 v179,v147,v179
v_mul_f32 v180,v148,v180
v_mul_f32 v181,v149,v181
v_mul_f32 v182,v150,v182
v_mul_f32 v183,v151,v183
v_mul_f32 v184,v152,v184
v_mul_f32 v185,v153,v185
v_mul_f32 v186,v154,v186
v_mul_f32 v187,v155,v187
v_mul_f32 v188,v156,v188
v_mul_f32 v189,v157,v189
v_mul_f32 v190,v158,v190
v_mul_f32 v191,v159,v191
v_cvt_pk_bf16_f32 v192,v176,v177
v_cvt_pk_bf16_f32 v193,v178,v179
v_cvt_pk_bf16_f32 v194,v180,v181
v_cvt_pk_bf16_f32 v195,v182,v183
v_cvt_pk_bf16_f32 v196,v184,v185
v_cvt_pk_bf16_f32 v197,v186,v187
v_cvt_pk_bf16_f32 v198,v188,v189
v_cvt_pk_bf16_f32 v199,v190,v191
s_waitcnt lgkmcnt(0)
s_barrier
v_fmaak_f32 v176,v160,v160,0x409d30fc
v_fmaak_f32 v177,v161,v161,0x409d30fc
v_fmaak_f32 v178,v162,v162,0x409d30fc
v_fmaak_f32 v179,v163,v163,0x409d30fc
v_fmaak_f32 v180,v164,v164,0x409d30fc
v_fmaak_f32 v181,v165,v165,0x409d30fc
v_fmaak_f32 v182,v166,v166,0x409d30fc
v_fmaak_f32 v183,v167,v167,0x409d30fc
v_fmaak_f32 v184,v168,v168,0x409d30fc
v_fmaak_f32 v185,v169,v169,0x409d30fc
v_fmaak_f32 v186,v170,v170,0x409d30fc
v_fmaak_f32 v187,v171,v171,0x409d30fc
v_fmaak_f32 v188,v172,v172,0x409d30fc
v_fmaak_f32 v189,v173,v173,0x409d30fc
v_fmaak_f32 v190,v174,v174,0x409d30fc
v_fmaak_f32 v191,v175,v175,0x409d30fc
v_mul_f32 v176,v160,v176
v_mul_f32 v177,v161,v177
v_mul_f32 v178,v162,v178
v_mul_f32 v179,v163,v179
v_mul_f32 v180,v164,v180
v_mul_f32 v181,v165,v181
v_mul_f32 v182,v166,v182
v_mul_f32 v183,v167,v183
v_mul_f32 v184,v168,v184
v_mul_f32 v185,v169,v185
v_mul_f32 v186,v170,v186
v_mul_f32 v187,v171,v187
v_mul_f32 v188,v172,v188
v_mul_f32 v189,v173,v189
v_mul_f32 v190,v174,v190
v_mul_f32 v191,v175,v191
s_setprio 1
v_mfma_f32_32x32x16_bf16 v[96:111],v[200:203],v[192:195],v[96:111]
v_exp_f32 v176,v176
v_exp_f32 v177,v177
v_exp_f32 v178,v178
v_mfma_f32_32x32x16_bf16 v[96:111],v[204:207],v[196:199],v[96:111]
v_exp_f32 v179,v179
v_exp_f32 v180,v180
v_exp_f32 v181,v181
v_mfma_f32_32x32x16_bf16 v[112:127],v[208:211],v[192:195],v[112:127]
v_exp_f32 v182,v182
v_exp_f32 v183,v183
v_exp_f32 v184,v184
v_mfma_f32_32x32x16_bf16 v[112:127],v[212:215],v[196:199],v[112:127]
v_exp_f32 v185,v185
v_exp_f32 v186,v186
v_exp_f32 v187,v187
v_mfma_f32_32x32x16_bf16 v[128:143],v[216:219],v[192:195],v[128:143]
v_exp_f32 v188,v188
v_exp_f32 v189,v189
v_mfma_f32_32x32x16_bf16 v[128:143],v[220:223],v[196:199],v[128:143]
v_exp_f32 v190,v190
v_exp_f32 v191,v191
ds_read_b64_tr_b16 v[200:201],v16 offset:0
ds_read_b64_tr_b16 v[202:203],v16 offset:512
ds_read_b64_tr_b16 v[204:205],v16 offset:1024
ds_read_b64_tr_b16 v[206:207],v16 offset:1536
ds_read_b64_tr_b16 v[208:209],v16 offset:2048
ds_read_b64_tr_b16 v[210:211],v16 offset:2560
ds_read_b64_tr_b16 v[212:213],v16 offset:3072
ds_read_b64_tr_b16 v[214:215],v16 offset:3584
ds_read_b64_tr_b16 v[216:217],v16 offset:4096
ds_read_b64_tr_b16 v[218:219],v16 offset:4608
ds_read_b64_tr_b16 v[220:221],v16 offset:5120
ds_read_b64_tr_b16 v[222:223],v16 offset:5632
ds_read_b128 v[144:147],v18 offset:768
ds_read_b128 v[148:151],v18 offset:800
ds_read_b128 v[152:155],v18 offset:832
ds_read_b128 v[156:159],v18 offset:864
s_mov_b64 exec,s[62:63]
s_add_u32 m0,s68,34560
s_add_u32 s64,s59,983040
buffer_load_dwordx4 v27,s[36:39],s64 offen lds
s_mov_b64 exec,-1
s_setprio 0
v_add_f32 v176,1.0,v176
v_add_f32 v177,1.0,v177
v_add_f32 v178,1.0,v178
v_add_f32 v179,1.0,v179
v_add_f32 v180,1.0,v180
v_add_f32 v181,1.0,v181
v_add_f32 v182,1.0,v182
v_add_f32 v183,1.0,v183
v_add_f32 v184,1.0,v184
v_add_f32 v185,1.0,v185
v_add_f32 v186,1.0,v186
v_add_f32 v187,1.0,v187
v_add_f32 v188,1.0,v188
v_add_f32 v189,1.0,v189
v_add_f32 v190,1.0,v190
v_add_f32 v191,1.0,v191
s_setprio 1
s_waitcnt lgkmcnt(0)
v_mfma_f32_32x32x16_bf16 v[144:159],v[200:203],v[72:75],v[144:159]
v_rcp_f32 v176,v176
v_rcp_f32 v177,v177
v_rcp_f32 v178,v178
v_mfma_f32_32x32x16_bf16 v[144:159],v[204:207],v[76:79],v[144:159]
v_rcp_f32 v179,v179
v_rcp_f32 v180,v180
v_rcp_f32 v181,v181
v_mfma_f32_32x32x16_bf16 v[144:159],v[208:211],v[80:83],v[144:159]
v_rcp_f32 v182,v182
v_rcp_f32 v183,v183
v_rcp_f32 v184,v184
v_mfma_f32_32x32x16_bf16 v[144:159],v[212:215],v[84:87],v[144:159]
v_rcp_f32 v185,v185
v_rcp_f32 v186,v186
v_rcp_f32 v187,v187
v_mfma_f32_32x32x16_bf16 v[144:159],v[216:219],v[88:91],v[144:159]
v_rcp_f32 v188,v188
v_rcp_f32 v189,v189
v_mfma_f32_32x32x16_bf16 v[144:159],v[220:223],v[92:95],v[144:159]
v_rcp_f32 v190,v190
v_rcp_f32 v191,v191
ds_read_b64_tr_b16 v[200:201],v17 offset:12288
ds_read_b64_tr_b16 v[202:203],v17 offset:13824
ds_read_b64_tr_b16 v[204:205],v17 offset:15360
ds_read_b64_tr_b16 v[206:207],v17 offset:16896
ds_read_b64_tr_b16 v[208:209],v17 offset:12352
ds_read_b64_tr_b16 v[210:211],v17 offset:13888
ds_read_b64_tr_b16 v[212:213],v17 offset:15424
ds_read_b64_tr_b16 v[214:215],v17 offset:16960
ds_read_b64_tr_b16 v[216:217],v17 offset:12416
ds_read_b64_tr_b16 v[218:219],v17 offset:13952
ds_read_b64_tr_b16 v[220:221],v17 offset:15488
ds_read_b64_tr_b16 v[222:223],v17 offset:17024
s_mov_b64 exec,s[62:63]
s_add_u32 m0,s68,38016
s_add_u32 s64,s59,1081344
buffer_load_dwordx4 v27,s[36:39],s64 offen lds
s_mov_b64 exec,-1
s_setprio 0
v_mul_f32 v176,v160,v176
v_mul_f32 v177,v161,v177
v_mul_f32 v178,v162,v178
v_mul_f32 v179,v163,v179
v_mul_f32 v180,v164,v180
v_mul_f32 v181,v165,v181
v_mul_f32 v182,v166,v182
v_mul_f32 v183,v167,v183
v_mul_f32 v184,v168,v184
v_mul_f32 v185,v169,v185
v_mul_f32 v186,v170,v186
v_mul_f32 v187,v171,v187
v_mul_f32 v188,v172,v188
v_mul_f32 v189,v173,v189
v_mul_f32 v190,v174,v190
v_mul_f32 v191,v175,v191
v_cvt_pk_bf16_f32 v192,v176,v177
v_cvt_pk_bf16_f32 v193,v178,v179
v_cvt_pk_bf16_f32 v194,v180,v181
v_cvt_pk_bf16_f32 v195,v182,v183
v_cvt_pk_bf16_f32 v196,v184,v185
v_cvt_pk_bf16_f32 v197,v186,v187
v_cvt_pk_bf16_f32 v198,v188,v189
v_cvt_pk_bf16_f32 v199,v190,v191
s_waitcnt lgkmcnt(0)
s_barrier
v_fmaak_f32 v176,v144,v144,0x409d30fc
v_fmaak_f32 v177,v145,v145,0x409d30fc
v_fmaak_f32 v178,v146,v146,0x409d30fc
v_fmaak_f32 v179,v147,v147,0x409d30fc
v_fmaak_f32 v180,v148,v148,0x409d30fc
v_fmaak_f32 v181,v149,v149,0x409d30fc
v_fmaak_f32 v182,v150,v150,0x409d30fc
v_fmaak_f32 v183,v151,v151,0x409d30fc
v_fmaak_f32 v184,v152,v152,0x409d30fc
v_fmaak_f32 v185,v153,v153,0x409d30fc
v_fmaak_f32 v186,v154,v154,0x409d30fc
v_fmaak_f32 v187,v155,v155,0x409d30fc
v_fmaak_f32 v188,v156,v156,0x409d30fc
v_fmaak_f32 v189,v157,v157,0x409d30fc
v_fmaak_f32 v190,v158,v158,0x409d30fc
v_fmaak_f32 v191,v159,v159,0x409d30fc
v_mul_f32 v176,v144,v176
v_mul_f32 v177,v145,v177
v_mul_f32 v178,v146,v178
v_mul_f32 v179,v147,v179
v_mul_f32 v180,v148,v180
v_mul_f32 v181,v149,v181
v_mul_f32 v182,v150,v182
v_mul_f32 v183,v151,v183
v_mul_f32 v184,v152,v184
v_mul_f32 v185,v153,v185
v_mul_f32 v186,v154,v186
v_mul_f32 v187,v155,v187
v_mul_f32 v188,v156,v188
v_mul_f32 v189,v157,v189
v_mul_f32 v190,v158,v190
v_mul_f32 v191,v159,v191
s_setprio 1
v_mfma_f32_32x32x16_bf16 v[96:111],v[200:203],v[192:195],v[96:111]
v_exp_f32 v176,v176
v_exp_f32 v177,v177
v_exp_f32 v178,v178
v_mfma_f32_32x32x16_bf16 v[96:111],v[204:207],v[196:199],v[96:111]
v_exp_f32 v179,v179
v_exp_f32 v180,v180
v_exp_f32 v181,v181
v_mfma_f32_32x32x16_bf16 v[112:127],v[208:211],v[192:195],v[112:127]
v_exp_f32 v182,v182
v_exp_f32 v183,v183
v_exp_f32 v184,v184
v_mfma_f32_32x32x16_bf16 v[112:127],v[212:215],v[196:199],v[112:127]
v_exp_f32 v185,v185
v_exp_f32 v186,v186
v_exp_f32 v187,v187
v_mfma_f32_32x32x16_bf16 v[128:143],v[216:219],v[192:195],v[128:143]
v_exp_f32 v188,v188
v_exp_f32 v189,v189
v_mfma_f32_32x32x16_bf16 v[128:143],v[220:223],v[196:199],v[128:143]
v_exp_f32 v190,v190
v_exp_f32 v191,v191
ds_read_b64_tr_b16 v[200:201],v16 offset:6144
ds_read_b64_tr_b16 v[202:203],v16 offset:6656
ds_read_b64_tr_b16 v[204:205],v16 offset:7168
ds_read_b64_tr_b16 v[206:207],v16 offset:7680
ds_read_b64_tr_b16 v[208:209],v16 offset:8192
ds_read_b64_tr_b16 v[210:211],v16 offset:8704
ds_read_b64_tr_b16 v[212:213],v16 offset:9216
ds_read_b64_tr_b16 v[214:215],v16 offset:9728
ds_read_b64_tr_b16 v[216:217],v16 offset:10240
ds_read_b64_tr_b16 v[218:219],v16 offset:10752
ds_read_b64_tr_b16 v[220:221],v16 offset:11264
ds_read_b64_tr_b16 v[222:223],v16 offset:11776
ds_read_b128 v[160:163],v18 offset:896
ds_read_b128 v[164:167],v18 offset:928
ds_read_b128 v[168:171],v18 offset:960
ds_read_b128 v[172:175],v18 offset:992
s_mov_b64 exec,s[62:63]
s_add_u32 m0,s68,41472
s_add_u32 s64,s59,1179648
buffer_load_dwordx4 v27,s[36:39],s64 offen lds
s_mov_b64 exec,-1
s_setprio 0
v_add_f32 v176,1.0,v176
v_add_f32 v177,1.0,v177
v_add_f32 v178,1.0,v178
v_add_f32 v179,1.0,v179
v_add_f32 v180,1.0,v180
v_add_f32 v181,1.0,v181
v_add_f32 v182,1.0,v182
v_add_f32 v183,1.0,v183
v_add_f32 v184,1.0,v184
v_add_f32 v185,1.0,v185
v_add_f32 v186,1.0,v186
v_add_f32 v187,1.0,v187
v_add_f32 v188,1.0,v188
v_add_f32 v189,1.0,v189
v_add_f32 v190,1.0,v190
v_add_f32 v191,1.0,v191
s_setprio 1
s_waitcnt lgkmcnt(0)
v_mfma_f32_32x32x16_bf16 v[160:175],v[200:203],v[72:75],v[160:175]
v_rcp_f32 v176,v176
v_rcp_f32 v177,v177
v_rcp_f32 v178,v178
v_mfma_f32_32x32x16_bf16 v[160:175],v[204:207],v[76:79],v[160:175]
v_rcp_f32 v179,v179
v_rcp_f32 v180,v180
v_rcp_f32 v181,v181
v_mfma_f32_32x32x16_bf16 v[160:175],v[208:211],v[80:83],v[160:175]
v_rcp_f32 v182,v182
v_rcp_f32 v183,v183
v_rcp_f32 v184,v184
v_mfma_f32_32x32x16_bf16 v[160:175],v[212:215],v[84:87],v[160:175]
v_rcp_f32 v185,v185
v_rcp_f32 v186,v186
v_rcp_f32 v187,v187
v_mfma_f32_32x32x16_bf16 v[160:175],v[216:219],v[88:91],v[160:175]
v_rcp_f32 v188,v188
v_rcp_f32 v189,v189
v_mfma_f32_32x32x16_bf16 v[160:175],v[220:223],v[92:95],v[160:175]
v_rcp_f32 v190,v190
v_rcp_f32 v191,v191
ds_read_b64_tr_b16 v[200:201],v17 offset:0
ds_read_b64_tr_b16 v[202:203],v17 offset:1536
ds_read_b64_tr_b16 v[204:205],v17 offset:3072
ds_read_b64_tr_b16 v[206:207],v17 offset:4608
ds_read_b64_tr_b16 v[208:209],v17 offset:64
ds_read_b64_tr_b16 v[210:211],v17 offset:1600
ds_read_b64_tr_b16 v[212:213],v17 offset:3136
ds_read_b64_tr_b16 v[214:215],v17 offset:4672
ds_read_b64_tr_b16 v[216:217],v17 offset:128
ds_read_b64_tr_b16 v[218:219],v17 offset:1664
ds_read_b64_tr_b16 v[220:221],v17 offset:3200
ds_read_b64_tr_b16 v[222:223],v17 offset:4736
s_mov_b64 exec,s[62:63]
s_add_u32 m0,s68,44928
s_add_u32 s64,s59,1277952
buffer_load_dwordx4 v27,s[36:39],s64 offen lds
s_mov_b64 exec,-1
s_setprio 0
v_mul_f32 v176,v144,v176
v_mul_f32 v177,v145,v177
v_mul_f32 v178,v146,v178
v_mul_f32 v179,v147,v179
v_mul_f32 v180,v148,v180
v_mul_f32 v181,v149,v181
v_mul_f32 v182,v150,v182
v_mul_f32 v183,v151,v183
v_mul_f32 v184,v152,v184
v_mul_f32 v185,v153,v185
v_mul_f32 v186,v154,v186
v_mul_f32 v187,v155,v187
v_mul_f32 v188,v156,v188
v_mul_f32 v189,v157,v189
v_mul_f32 v190,v158,v190
v_mul_f32 v191,v159,v191
v_cvt_pk_bf16_f32 v192,v176,v177
v_cvt_pk_bf16_f32 v193,v178,v179
v_cvt_pk_bf16_f32 v194,v180,v181
v_cvt_pk_bf16_f32 v195,v182,v183
v_cvt_pk_bf16_f32 v196,v184,v185
v_cvt_pk_bf16_f32 v197,v186,v187
v_cvt_pk_bf16_f32 v198,v188,v189
v_cvt_pk_bf16_f32 v199,v190,v191
s_waitcnt lgkmcnt(0)
s_barrier
v_fmaak_f32 v176,v160,v160,0x409d30fc
v_fmaak_f32 v177,v161,v161,0x409d30fc
v_fmaak_f32 v178,v162,v162,0x409d30fc
v_fmaak_f32 v179,v163,v163,0x409d30fc
v_fmaak_f32 v180,v164,v164,0x409d30fc
v_fmaak_f32 v181,v165,v165,0x409d30fc
v_fmaak_f32 v182,v166,v166,0x409d30fc
v_fmaak_f32 v183,v167,v167,0x409d30fc
v_fmaak_f32 v184,v168,v168,0x409d30fc
v_fmaak_f32 v185,v169,v169,0x409d30fc
v_fmaak_f32 v186,v170,v170,0x409d30fc
v_fmaak_f32 v187,v171,v171,0x409d30fc
v_fmaak_f32 v188,v172,v172,0x409d30fc
v_fmaak_f32 v189,v173,v173,0x409d30fc
v_fmaak_f32 v190,v174,v174,0x409d30fc
v_fmaak_f32 v191,v175,v175,0x409d30fc
v_mul_f32 v176,v160,v176
v_mul_f32 v177,v161,v177
v_mul_f32 v178,v162,v178
v_mul_f32 v179,v163,v179
v_mul_f32 v180,v164,v180
v_mul_f32 v181,v165,v181
v_mul_f32 v182,v166,v182
v_mul_f32 v183,v167,v183
v_mul_f32 v184,v168,v184
v_mul_f32 v185,v169,v185
v_mul_f32 v186,v170,v186
v_mul_f32 v187,v171,v187
v_mul_f32 v188,v172,v188
v_mul_f32 v189,v173,v189
v_mul_f32 v190,v174,v190
v_mul_f32 v191,v175,v191
s_setprio 1
v_mfma_f32_32x32x16_bf16 v[96:111],v[200:203],v[192:195],v[96:111]
v_exp_f32 v176,v176
v_exp_f32 v177,v177
v_exp_f32 v178,v178
v_mfma_f32_32x32x16_bf16 v[96:111],v[204:207],v[196:199],v[96:111]
v_exp_f32 v179,v179
v_exp_f32 v180,v180
v_exp_f32 v181,v181
v_mfma_f32_32x32x16_bf16 v[112:127],v[208:211],v[192:195],v[112:127]
v_exp_f32 v182,v182
v_exp_f32 v183,v183
v_exp_f32 v184,v184
v_mfma_f32_32x32x16_bf16 v[112:127],v[212:215],v[196:199],v[112:127]
v_exp_f32 v185,v185
v_exp_f32 v186,v186
v_exp_f32 v187,v187
v_mfma_f32_32x32x16_bf16 v[128:143],v[216:219],v[192:195],v[128:143]
v_exp_f32 v188,v188
v_exp_f32 v189,v189
v_mfma_f32_32x32x16_bf16 v[128:143],v[220:223],v[196:199],v[128:143]
v_exp_f32 v190,v190
v_exp_f32 v191,v191
ds_read_b64_tr_b16 v[200:201],v16 offset:12288
ds_read_b64_tr_b16 v[202:203],v16 offset:12800
ds_read_b64_tr_b16 v[204:205],v16 offset:13312
ds_read_b64_tr_b16 v[206:207],v16 offset:13824
ds_read_b64_tr_b16 v[208:209],v16 offset:14336
ds_read_b64_tr_b16 v[210:211],v16 offset:14848
ds_read_b64_tr_b16 v[212:213],v16 offset:15360
ds_read_b64_tr_b16 v[214:215],v16 offset:15872
ds_read_b64_tr_b16 v[216:217],v16 offset:16384
ds_read_b64_tr_b16 v[218:219],v16 offset:16896
ds_read_b64_tr_b16 v[220:221],v16 offset:17408
ds_read_b64_tr_b16 v[222:223],v16 offset:17920
ds_read_b128 v[144:147],v18 offset:1024
ds_read_b128 v[148:151],v18 offset:1056
ds_read_b128 v[152:155],v18 offset:1088
ds_read_b128 v[156:159],v18 offset:1120
s_mov_b64 exec,s[62:63]
s_add_u32 m0,s68,48384
s_add_u32 s64,s59,1376256
buffer_load_dwordx4 v27,s[36:39],s64 offen lds
s_mov_b64 exec,-1
s_setprio 0
v_add_f32 v176,1.0,v176
v_add_f32 v177,1.0,v177
v_add_f32 v178,1.0,v178
v_add_f32 v179,1.0,v179
v_add_f32 v180,1.0,v180
v_add_f32 v181,1.0,v181
v_add_f32 v182,1.0,v182
v_add_f32 v183,1.0,v183
v_add_f32 v184,1.0,v184
v_add_f32 v185,1.0,v185
v_add_f32 v186,1.0,v186
v_add_f32 v187,1.0,v187
v_add_f32 v188,1.0,v188
v_add_f32 v189,1.0,v189
v_add_f32 v190,1.0,v190
v_add_f32 v191,1.0,v191
s_setprio 1
s_waitcnt lgkmcnt(0)
v_mfma_f32_32x32x16_bf16 v[144:159],v[200:203],v[72:75],v[144:159]
v_rcp_f32 v176,v176
v_rcp_f32 v177,v177
v_rcp_f32 v178,v178
v_mfma_f32_32x32x16_bf16 v[144:159],v[204:207],v[76:79],v[144:159]
v_rcp_f32 v179,v179
v_rcp_f32 v180,v180
v_rcp_f32 v181,v181
v_mfma_f32_32x32x16_bf16 v[144:159],v[208:211],v[80:83],v[144:159]
v_rcp_f32 v182,v182
v_rcp_f32 v183,v183
v_rcp_f32 v184,v184
v_mfma_f32_32x32x16_bf16 v[144:159],v[212:215],v[84:87],v[144:159]
v_rcp_f32 v185,v185
v_rcp_f32 v186,v186
v_rcp_f32 v187,v187
v_mfma_f32_32x32x16_bf16 v[144:159],v[216:219],v[88:91],v[144:159]
v_rcp_f32 v188,v188
v_rcp_f32 v189,v189
v_mfma_f32_32x32x16_bf16 v[144:159],v[220:223],v[92:95],v[144:159]
v_rcp_f32 v190,v190
v_rcp_f32 v191,v191
ds_read_b64_tr_b16 v[200:201],v17 offset:6144
ds_read_b64_tr_b16 v[202:203],v17 offset:7680
ds_read_b64_tr_b16 v[204:205],v17 offset:9216
ds_read_b64_tr_b16 v[206:207],v17 offset:10752
ds_read_b64_tr_b16 v[208:209],v17 offset:6208
ds_read_b64_tr_b16 v[210:211],v17 offset:7744
ds_read_b64_tr_b16 v[212:213],v17 offset:9280
ds_read_b64_tr_b16 v[214:215],v17 offset:10816
ds_read_b64_tr_b16 v[216:217],v17 offset:6272
ds_read_b64_tr_b16 v[218:219],v17 offset:7808
ds_read_b64_tr_b16 v[220:221],v17 offset:9344
ds_read_b64_tr_b16 v[222:223],v17 offset:10880
s_mov_b64 exec,s[62:63]
s_add_u32 m0,s68,51840
s_add_u32 s64,s59,1474560
buffer_load_dwordx4 v27,s[36:39],s64 offen lds
s_mov_b64 exec,-1
s_setprio 0
v_mul_f32 v176,v160,v176
v_mul_f32 v177,v161,v177
v_mul_f32 v178,v162,v178
v_mul_f32 v179,v163,v179
v_mul_f32 v180,v164,v180
v_mul_f32 v181,v165,v181
v_mul_f32 v182,v166,v182
v_mul_f32 v183,v167,v183
v_mul_f32 v184,v168,v184
v_mul_f32 v185,v169,v185
v_mul_f32 v186,v170,v186
v_mul_f32 v187,v171,v187
v_mul_f32 v188,v172,v188
v_mul_f32 v189,v173,v189
v_mul_f32 v190,v174,v190
v_mul_f32 v191,v175,v191
v_cvt_pk_bf16_f32 v192,v176,v177
v_cvt_pk_bf16_f32 v193,v178,v179
v_cvt_pk_bf16_f32 v194,v180,v181
v_cvt_pk_bf16_f32 v195,v182,v183
v_cvt_pk_bf16_f32 v196,v184,v185
v_cvt_pk_bf16_f32 v197,v186,v187
v_cvt_pk_bf16_f32 v198,v188,v189
v_cvt_pk_bf16_f32 v199,v190,v191
s_waitcnt lgkmcnt(0)
s_barrier
v_fmaak_f32 v176,v144,v144,0x409d30fc
v_fmaak_f32 v177,v145,v145,0x409d30fc
v_fmaak_f32 v178,v146,v146,0x409d30fc
v_fmaak_f32 v179,v147,v147,0x409d30fc
v_fmaak_f32 v180,v148,v148,0x409d30fc
v_fmaak_f32 v181,v149,v149,0x409d30fc
v_fmaak_f32 v182,v150,v150,0x409d30fc
v_fmaak_f32 v183,v151,v151,0x409d30fc
v_fmaak_f32 v184,v152,v152,0x409d30fc
v_fmaak_f32 v185,v153,v153,0x409d30fc
v_fmaak_f32 v186,v154,v154,0x409d30fc
v_fmaak_f32 v187,v155,v155,0x409d30fc
v_fmaak_f32 v188,v156,v156,0x409d30fc
v_fmaak_f32 v189,v157,v157,0x409d30fc
v_fmaak_f32 v190,v158,v158,0x409d30fc
v_fmaak_f32 v191,v159,v159,0x409d30fc
v_mul_f32 v176,v144,v176
v_mul_f32 v177,v145,v177
v_mul_f32 v178,v146,v178
v_mul_f32 v179,v147,v179
v_mul_f32 v180,v148,v180
v_mul_f32 v181,v149,v181
v_mul_f32 v182,v150,v182
v_mul_f32 v183,v151,v183
v_mul_f32 v184,v152,v184
v_mul_f32 v185,v153,v185
v_mul_f32 v186,v154,v186
v_mul_f32 v187,v155,v187
v_mul_f32 v188,v156,v188
v_mul_f32 v189,v157,v189
v_mul_f32 v190,v158,v190
v_mul_f32 v191,v159,v191
s_setprio 1
v_mfma_f32_32x32x16_bf16 v[96:111],v[200:203],v[192:195],v[96:111]
v_exp_f32 v176,v176
v_exp_f32 v177,v177
v_exp_f32 v178,v178
v_mfma_f32_32x32x16_bf16 v[96:111],v[204:207],v[196:199],v[96:111]
v_exp_f32 v179,v179
v_exp_f32 v180,v180
v_exp_f32 v181,v181
v_mfma_f32_32x32x16_bf16 v[112:127],v[208:211],v[192:195],v[112:127]
v_exp_f32 v182,v182
v_exp_f32 v183,v183
v_exp_f32 v184,v184
v_mfma_f32_32x32x16_bf16 v[112:127],v[212:215],v[196:199],v[112:127]
v_exp_f32 v185,v185
v_exp_f32 v186,v186
v_exp_f32 v187,v187
v_mfma_f32_32x32x16_bf16 v[128:143],v[216:219],v[192:195],v[128:143]
v_exp_f32 v188,v188
v_exp_f32 v189,v189
v_mfma_f32_32x32x16_bf16 v[128:143],v[220:223],v[196:199],v[128:143]
v_exp_f32 v190,v190
v_exp_f32 v191,v191
ds_read_b64_tr_b16 v[200:201],v16 offset:0
ds_read_b64_tr_b16 v[202:203],v16 offset:512
ds_read_b64_tr_b16 v[204:205],v16 offset:1024
ds_read_b64_tr_b16 v[206:207],v16 offset:1536
ds_read_b64_tr_b16 v[208:209],v16 offset:2048
ds_read_b64_tr_b16 v[210:211],v16 offset:2560
ds_read_b64_tr_b16 v[212:213],v16 offset:3072
ds_read_b64_tr_b16 v[214:215],v16 offset:3584
ds_read_b64_tr_b16 v[216:217],v16 offset:4096
ds_read_b64_tr_b16 v[218:219],v16 offset:4608
ds_read_b64_tr_b16 v[220:221],v16 offset:5120
ds_read_b64_tr_b16 v[222:223],v16 offset:5632
ds_read_b128 v[160:163],v18 offset:1152
ds_read_b128 v[164:167],v18 offset:1184
ds_read_b128 v[168:171],v18 offset:1216
ds_read_b128 v[172:175],v18 offset:1248
s_mov_b64 exec,s[62:63]
s_add_u32 m0,s68,55296
s_add_u32 s64,s59,1572864
buffer_load_dwordx4 v27,s[36:39],s64 offen lds
s_mov_b64 exec,-1
s_setprio 0
v_add_f32 v176,1.0,v176
v_add_f32 v177,1.0,v177
v_add_f32 v178,1.0,v178
v_add_f32 v179,1.0,v179
v_add_f32 v180,1.0,v180
v_add_f32 v181,1.0,v181
v_add_f32 v182,1.0,v182
v_add_f32 v183,1.0,v183
v_add_f32 v184,1.0,v184
v_add_f32 v185,1.0,v185
v_add_f32 v186,1.0,v186
v_add_f32 v187,1.0,v187
v_add_f32 v188,1.0,v188
v_add_f32 v189,1.0,v189
v_add_f32 v190,1.0,v190
v_add_f32 v191,1.0,v191
s_setprio 1
s_waitcnt lgkmcnt(0)
v_mfma_f32_32x32x16_bf16 v[160:175],v[200:203],v[72:75],v[160:175]
v_rcp_f32 v176,v176
v_rcp_f32 v177,v177
v_rcp_f32 v178,v178
v_mfma_f32_32x32x16_bf16 v[160:175],v[204:207],v[76:79],v[160:175]
v_rcp_f32 v179,v179
v_rcp_f32 v180,v180
v_rcp_f32 v181,v181
v_mfma_f32_32x32x16_bf16 v[160:175],v[208:211],v[80:83],v[160:175]
v_rcp_f32 v182,v182
v_rcp_f32 v183,v183
v_rcp_f32 v184,v184
v_mfma_f32_32x32x16_bf16 v[160:175],v[212:215],v[84:87],v[160:175]
v_rcp_f32 v185,v185
v_rcp_f32 v186,v186
v_rcp_f32 v187,v187
v_mfma_f32_32x32x16_bf16 v[160:175],v[216:219],v[88:91],v[160:175]
v_rcp_f32 v188,v188
v_rcp_f32 v189,v189
v_mfma_f32_32x32x16_bf16 v[160:175],v[220:223],v[92:95],v[160:175]
v_rcp_f32 v190,v190
v_rcp_f32 v191,v191
ds_read_b64_tr_b16 v[200:201],v17 offset:12288
ds_read_b64_tr_b16 v[202:203],v17 offset:13824
ds_read_b64_tr_b16 v[204:205],v17 offset:15360
ds_read_b64_tr_b16 v[206:207],v17 offset:16896
ds_read_b64_tr_b16 v[208:209],v17 offset:12352
ds_read_b64_tr_b16 v[210:211],v17 offset:13888
ds_read_b64_tr_b16 v[212:213],v17 offset:15424
ds_read_b64_tr_b16 v[214:215],v17 offset:16960
ds_read_b64_tr_b16 v[216:217],v17 offset:12416
ds_read_b64_tr_b16 v[218:219],v17 offset:13952
ds_read_b64_tr_b16 v[220:221],v17 offset:15488
ds_read_b64_tr_b16 v[222:223],v17 offset:17024
s_mov_b64 exec,s[62:63]
s_add_u32 m0,s68,58752
s_add_u32 s64,s59,1671168
buffer_load_dwordx4 v27,s[36:39],s64 offen lds
s_mov_b64 exec,-1
s_setprio 0
v_mul_f32 v176,v144,v176
v_mul_f32 v177,v145,v177
v_mul_f32 v178,v146,v178
v_mul_f32 v179,v147,v179
v_mul_f32 v180,v148,v180
v_mul_f32 v181,v149,v181
v_mul_f32 v182,v150,v182
v_mul_f32 v183,v151,v183
v_mul_f32 v184,v152,v184
v_mul_f32 v185,v153,v185
v_mul_f32 v186,v154,v186
v_mul_f32 v187,v155,v187
v_mul_f32 v188,v156,v188
v_mul_f32 v189,v157,v189
v_mul_f32 v190,v158,v190
v_mul_f32 v191,v159,v191
v_cvt_pk_bf16_f32 v192,v176,v177
v_cvt_pk_bf16_f32 v193,v178,v179
v_cvt_pk_bf16_f32 v194,v180,v181
v_cvt_pk_bf16_f32 v195,v182,v183
v_cvt_pk_bf16_f32 v196,v184,v185
v_cvt_pk_bf16_f32 v197,v186,v187
v_cvt_pk_bf16_f32 v198,v188,v189
v_cvt_pk_bf16_f32 v199,v190,v191
s_waitcnt lgkmcnt(0)
s_barrier
v_fmaak_f32 v176,v160,v160,0x409d30fc
v_fmaak_f32 v177,v161,v161,0x409d30fc
v_fmaak_f32 v178,v162,v162,0x409d30fc
v_fmaak_f32 v179,v163,v163,0x409d30fc
v_fmaak_f32 v180,v164,v164,0x409d30fc
v_fmaak_f32 v181,v165,v165,0x409d30fc
v_fmaak_f32 v182,v166,v166,0x409d30fc
v_fmaak_f32 v183,v167,v167,0x409d30fc
v_fmaak_f32 v184,v168,v168,0x409d30fc
v_fmaak_f32 v185,v169,v169,0x409d30fc
v_fmaak_f32 v186,v170,v170,0x409d30fc
v_fmaak_f32 v187,v171,v171,0x409d30fc
v_fmaak_f32 v188,v172,v172,0x409d30fc
v_fmaak_f32 v189,v173,v173,0x409d30fc
v_fmaak_f32 v190,v174,v174,0x409d30fc
v_fmaak_f32 v191,v175,v175,0x409d30fc
v_mul_f32 v176,v160,v176
v_mul_f32 v177,v161,v177
v_mul_f32 v178,v162,v178
v_mul_f32 v179,v163,v179
v_mul_f32 v180,v164,v180
v_mul_f32 v181,v165,v181
v_mul_f32 v182,v166,v182
v_mul_f32 v183,v167,v183
v_mul_f32 v184,v168,v184
v_mul_f32 v185,v169,v185
v_mul_f32 v186,v170,v186
v_mul_f32 v187,v171,v187
v_mul_f32 v188,v172,v188
v_mul_f32 v189,v173,v189
v_mul_f32 v190,v174,v190
v_mul_f32 v191,v175,v191
s_setprio 1
v_mfma_f32_32x32x16_bf16 v[96:111],v[200:203],v[192:195],v[96:111]
v_exp_f32 v176,v176
v_exp_f32 v177,v177
v_exp_f32 v178,v178
v_mfma_f32_32x32x16_bf16 v[96:111],v[204:207],v[196:199],v[96:111]
v_exp_f32 v179,v179
v_exp_f32 v180,v180
v_exp_f32 v181,v181
v_mfma_f32_32x32x16_bf16 v[112:127],v[208:211],v[192:195],v[112:127]
v_exp_f32 v182,v182
v_exp_f32 v183,v183
v_exp_f32 v184,v184
v_mfma_f32_32x32x16_bf16 v[112:127],v[212:215],v[196:199],v[112:127]
v_exp_f32 v185,v185
v_exp_f32 v186,v186
v_exp_f32 v187,v187
v_mfma_f32_32x32x16_bf16 v[128:143],v[216:219],v[192:195],v[128:143]
v_exp_f32 v188,v188
v_exp_f32 v189,v189
v_mfma_f32_32x32x16_bf16 v[128:143],v[220:223],v[196:199],v[128:143]
v_exp_f32 v190,v190
v_exp_f32 v191,v191
ds_read_b64_tr_b16 v[200:201],v16 offset:6144
ds_read_b64_tr_b16 v[202:203],v16 offset:6656
ds_read_b64_tr_b16 v[204:205],v16 offset:7168
ds_read_b64_tr_b16 v[206:207],v16 offset:7680
ds_read_b64_tr_b16 v[208:209],v16 offset:8192
ds_read_b64_tr_b16 v[210:211],v16 offset:8704
ds_read_b64_tr_b16 v[212:213],v16 offset:9216
ds_read_b64_tr_b16 v[214:215],v16 offset:9728
ds_read_b64_tr_b16 v[216:217],v16 offset:10240
ds_read_b64_tr_b16 v[218:219],v16 offset:10752
ds_read_b64_tr_b16 v[220:221],v16 offset:11264
ds_read_b64_tr_b16 v[222:223],v16 offset:11776
ds_read_b128 v[144:147],v18 offset:1280
ds_read_b128 v[148:151],v18 offset:1312
ds_read_b128 v[152:155],v18 offset:1344
ds_read_b128 v[156:159],v18 offset:1376
s_setprio 0
v_add_f32 v176,1.0,v176
v_add_f32 v177,1.0,v177
v_add_f32 v178,1.0,v178
v_add_f32 v179,1.0,v179
v_add_f32 v180,1.0,v180
v_add_f32 v181,1.0,v181
v_add_f32 v182,1.0,v182
v_add_f32 v183,1.0,v183
v_add_f32 v184,1.0,v184
v_add_f32 v185,1.0,v185
v_add_f32 v186,1.0,v186
v_add_f32 v187,1.0,v187
v_add_f32 v188,1.0,v188
v_add_f32 v189,1.0,v189
v_add_f32 v190,1.0,v190
v_add_f32 v191,1.0,v191
s_setprio 1
s_waitcnt lgkmcnt(0)
v_mfma_f32_32x32x16_bf16 v[144:159],v[200:203],v[72:75],v[144:159]
v_rcp_f32 v176,v176
v_rcp_f32 v177,v177
v_rcp_f32 v178,v178
v_mfma_f32_32x32x16_bf16 v[144:159],v[204:207],v[76:79],v[144:159]
v_rcp_f32 v179,v179
v_rcp_f32 v180,v180
v_rcp_f32 v181,v181
v_mfma_f32_32x32x16_bf16 v[144:159],v[208:211],v[80:83],v[144:159]
v_rcp_f32 v182,v182
v_rcp_f32 v183,v183
v_rcp_f32 v184,v184
v_mfma_f32_32x32x16_bf16 v[144:159],v[212:215],v[84:87],v[144:159]
v_rcp_f32 v185,v185
v_rcp_f32 v186,v186
v_rcp_f32 v187,v187
v_mfma_f32_32x32x16_bf16 v[144:159],v[216:219],v[88:91],v[144:159]
v_rcp_f32 v188,v188
v_rcp_f32 v189,v189
v_mfma_f32_32x32x16_bf16 v[144:159],v[220:223],v[92:95],v[144:159]
v_rcp_f32 v190,v190
v_rcp_f32 v191,v191
ds_read_b64_tr_b16 v[200:201],v17 offset:0
ds_read_b64_tr_b16 v[202:203],v17 offset:1536
ds_read_b64_tr_b16 v[204:205],v17 offset:3072
ds_read_b64_tr_b16 v[206:207],v17 offset:4608
ds_read_b64_tr_b16 v[208:209],v17 offset:64
ds_read_b64_tr_b16 v[210:211],v17 offset:1600
ds_read_b64_tr_b16 v[212:213],v17 offset:3136
ds_read_b64_tr_b16 v[214:215],v17 offset:4672
ds_read_b64_tr_b16 v[216:217],v17 offset:128
ds_read_b64_tr_b16 v[218:219],v17 offset:1664
ds_read_b64_tr_b16 v[220:221],v17 offset:3200
ds_read_b64_tr_b16 v[222:223],v17 offset:4736
s_setprio 0
v_mul_f32 v176,v160,v176
v_mul_f32 v177,v161,v177
v_mul_f32 v178,v162,v178
v_mul_f32 v179,v163,v179
v_mul_f32 v180,v164,v180
v_mul_f32 v181,v165,v181
v_mul_f32 v182,v166,v182
v_mul_f32 v183,v167,v183
v_mul_f32 v184,v168,v184
v_mul_f32 v185,v169,v185
v_mul_f32 v186,v170,v186
v_mul_f32 v187,v171,v187
v_mul_f32 v188,v172,v188
v_mul_f32 v189,v173,v189
v_mul_f32 v190,v174,v190
v_mul_f32 v191,v175,v191
v_cvt_pk_bf16_f32 v192,v176,v177
v_cvt_pk_bf16_f32 v193,v178,v179
v_cvt_pk_bf16_f32 v194,v180,v181
v_cvt_pk_bf16_f32 v195,v182,v183
v_cvt_pk_bf16_f32 v196,v184,v185
v_cvt_pk_bf16_f32 v197,v186,v187
v_cvt_pk_bf16_f32 v198,v188,v189
v_cvt_pk_bf16_f32 v199,v190,v191
s_waitcnt lgkmcnt(0)
s_barrier
v_fmaak_f32 v176,v144,v144,0x409d30fc
v_fmaak_f32 v177,v145,v145,0x409d30fc
v_fmaak_f32 v178,v146,v146,0x409d30fc
v_fmaak_f32 v179,v147,v147,0x409d30fc
v_fmaak_f32 v180,v148,v148,0x409d30fc
v_fmaak_f32 v181,v149,v149,0x409d30fc
v_fmaak_f32 v182,v150,v150,0x409d30fc
v_fmaak_f32 v183,v151,v151,0x409d30fc
v_fmaak_f32 v184,v152,v152,0x409d30fc
v_fmaak_f32 v185,v153,v153,0x409d30fc
v_fmaak_f32 v186,v154,v154,0x409d30fc
v_fmaak_f32 v187,v155,v155,0x409d30fc
v_fmaak_f32 v188,v156,v156,0x409d30fc
v_fmaak_f32 v189,v157,v157,0x409d30fc
v_fmaak_f32 v190,v158,v158,0x409d30fc
v_fmaak_f32 v191,v159,v159,0x409d30fc
v_mul_f32 v176,v144,v176
v_mul_f32 v177,v145,v177
v_mul_f32 v178,v146,v178
v_mul_f32 v179,v147,v179
v_mul_f32 v180,v148,v180
v_mul_f32 v181,v149,v181
v_mul_f32 v182,v150,v182
v_mul_f32 v183,v151,v183
v_mul_f32 v184,v152,v184
v_mul_f32 v185,v153,v185
v_mul_f32 v186,v154,v186
v_mul_f32 v187,v155,v187
v_mul_f32 v188,v156,v188
v_mul_f32 v189,v157,v189
v_mul_f32 v190,v158,v190
v_mul_f32 v191,v159,v191
s_setprio 1
v_mfma_f32_32x32x16_bf16 v[96:111],v[200:203],v[192:195],v[96:111]
v_exp_f32 v176,v176
v_exp_f32 v177,v177
v_exp_f32 v178,v178
v_mfma_f32_32x32x16_bf16 v[96:111],v[204:207],v[196:199],v[96:111]
v_exp_f32 v179,v179
v_exp_f32 v180,v180
v_exp_f32 v181,v181
v_mfma_f32_32x32x16_bf16 v[112:127],v[208:211],v[192:195],v[112:127]
v_exp_f32 v182,v182
v_exp_f32 v183,v183
v_exp_f32 v184,v184
v_mfma_f32_32x32x16_bf16 v[112:127],v[212:215],v[196:199],v[112:127]
v_exp_f32 v185,v185
v_exp_f32 v186,v186
v_exp_f32 v187,v187
v_mfma_f32_32x32x16_bf16 v[128:143],v[216:219],v[192:195],v[128:143]
v_exp_f32 v188,v188
v_exp_f32 v189,v189
v_mfma_f32_32x32x16_bf16 v[128:143],v[220:223],v[196:199],v[128:143]
v_exp_f32 v190,v190
v_exp_f32 v191,v191
ds_read_b64_tr_b16 v[200:201],v16 offset:12288
ds_read_b64_tr_b16 v[202:203],v16 offset:12800
ds_read_b64_tr_b16 v[204:205],v16 offset:13312
ds_read_b64_tr_b16 v[206:207],v16 offset:13824
ds_read_b64_tr_b16 v[208:209],v16 offset:14336
ds_read_b64_tr_b16 v[210:211],v16 offset:14848
ds_read_b64_tr_b16 v[212:213],v16 offset:15360
ds_read_b64_tr_b16 v[214:215],v16 offset:15872
ds_read_b64_tr_b16 v[216:217],v16 offset:16384
ds_read_b64_tr_b16 v[218:219],v16 offset:16896
ds_read_b64_tr_b16 v[220:221],v16 offset:17408
ds_read_b64_tr_b16 v[222:223],v16 offset:17920
ds_read_b128 v[160:163],v18 offset:1408
ds_read_b128 v[164:167],v18 offset:1440
ds_read_b128 v[168:171],v18 offset:1472
ds_read_b128 v[172:175],v18 offset:1504
s_setprio 0
v_add_f32 v176,1.0,v176
v_add_f32 v177,1.0,v177
v_add_f32 v178,1.0,v178
v_add_f32 v179,1.0,v179
v_add_f32 v180,1.0,v180
v_add_f32 v181,1.0,v181
v_add_f32 v182,1.0,v182
v_add_f32 v183,1.0,v183
v_add_f32 v184,1.0,v184
v_add_f32 v185,1.0,v185
v_add_f32 v186,1.0,v186
v_add_f32 v187,1.0,v187
v_add_f32 v188,1.0,v188
v_add_f32 v189,1.0,v189
v_add_f32 v190,1.0,v190
v_add_f32 v191,1.0,v191
s_setprio 1
s_waitcnt lgkmcnt(0)
v_mfma_f32_32x32x16_bf16 v[160:175],v[200:203],v[72:75],v[160:175]
v_rcp_f32 v176,v176
v_rcp_f32 v177,v177
v_rcp_f32 v178,v178
v_mfma_f32_32x32x16_bf16 v[160:175],v[204:207],v[76:79],v[160:175]
v_rcp_f32 v179,v179
v_rcp_f32 v180,v180
v_rcp_f32 v181,v181
v_mfma_f32_32x32x16_bf16 v[160:175],v[208:211],v[80:83],v[160:175]
v_rcp_f32 v182,v182
v_rcp_f32 v183,v183
v_rcp_f32 v184,v184
v_mfma_f32_32x32x16_bf16 v[160:175],v[212:215],v[84:87],v[160:175]
v_rcp_f32 v185,v185
v_rcp_f32 v186,v186
v_rcp_f32 v187,v187
v_mfma_f32_32x32x16_bf16 v[160:175],v[216:219],v[88:91],v[160:175]
v_rcp_f32 v188,v188
v_rcp_f32 v189,v189
v_mfma_f32_32x32x16_bf16 v[160:175],v[220:223],v[92:95],v[160:175]
v_rcp_f32 v190,v190
v_rcp_f32 v191,v191
ds_read_b64_tr_b16 v[200:201],v17 offset:6144
ds_read_b64_tr_b16 v[202:203],v17 offset:7680
ds_read_b64_tr_b16 v[204:205],v17 offset:9216
ds_read_b64_tr_b16 v[206:207],v17 offset:10752
ds_read_b64_tr_b16 v[208:209],v17 offset:6208
ds_read_b64_tr_b16 v[210:211],v17 offset:7744
ds_read_b64_tr_b16 v[212:213],v17 offset:9280
ds_read_b64_tr_b16 v[214:215],v17 offset:10816
ds_read_b64_tr_b16 v[216:217],v17 offset:6272
ds_read_b64_tr_b16 v[218:219],v17 offset:7808
ds_read_b64_tr_b16 v[220:221],v17 offset:9344
ds_read_b64_tr_b16 v[222:223],v17 offset:10880
s_setprio 0
v_mul_f32 v176,v144,v176
v_mul_f32 v177,v145,v177
v_mul_f32 v178,v146,v178
v_mul_f32 v179,v147,v179
v_mul_f32 v180,v148,v180
v_mul_f32 v181,v149,v181
v_mul_f32 v182,v150,v182
v_mul_f32 v183,v151,v183
v_mul_f32 v184,v152,v184
v_mul_f32 v185,v153,v185
v_mul_f32 v186,v154,v186
v_mul_f32 v187,v155,v187
v_mul_f32 v188,v156,v188
v_mul_f32 v189,v157,v189
v_mul_f32 v190,v158,v190
v_mul_f32 v191,v159,v191
v_cvt_pk_bf16_f32 v192,v176,v177
v_cvt_pk_bf16_f32 v193,v178,v179
v_cvt_pk_bf16_f32 v194,v180,v181
v_cvt_pk_bf16_f32 v195,v182,v183
v_cvt_pk_bf16_f32 v196,v184,v185
v_cvt_pk_bf16_f32 v197,v186,v187
v_cvt_pk_bf16_f32 v198,v188,v189
v_cvt_pk_bf16_f32 v199,v190,v191
s_waitcnt lgkmcnt(0)
s_barrier
v_fmaak_f32 v176,v160,v160,0x409d30fc
v_fmaak_f32 v177,v161,v161,0x409d30fc
v_fmaak_f32 v178,v162,v162,0x409d30fc
v_fmaak_f32 v179,v163,v163,0x409d30fc
v_fmaak_f32 v180,v164,v164,0x409d30fc
v_fmaak_f32 v181,v165,v165,0x409d30fc
v_fmaak_f32 v182,v166,v166,0x409d30fc
v_fmaak_f32 v183,v167,v167,0x409d30fc
v_fmaak_f32 v184,v168,v168,0x409d30fc
v_fmaak_f32 v185,v169,v169,0x409d30fc
v_fmaak_f32 v186,v170,v170,0x409d30fc
v_fmaak_f32 v187,v171,v171,0x409d30fc
v_fmaak_f32 v188,v172,v172,0x409d30fc
v_fmaak_f32 v189,v173,v173,0x409d30fc
v_fmaak_f32 v190,v174,v174,0x409d30fc
v_fmaak_f32 v191,v175,v175,0x409d30fc
v_mul_f32 v176,v160,v176
v_mul_f32 v177,v161,v177
v_mul_f32 v178,v162,v178
v_mul_f32 v179,v163,v179
v_mul_f32 v180,v164,v180
v_mul_f32 v181,v165,v181
v_mul_f32 v182,v166,v182
v_mul_f32 v183,v167,v183
v_mul_f32 v184,v168,v184
v_mul_f32 v185,v169,v185
v_mul_f32 v186,v170,v186
v_mul_f32 v187,v171,v187
v_mul_f32 v188,v172,v188
v_mul_f32 v189,v173,v189
v_mul_f32 v190,v174,v190
v_mul_f32 v191,v175,v191
s_setprio 1
v_mfma_f32_32x32x16_bf16 v[96:111],v[200:203],v[192:195],v[96:111]
v_exp_f32 v176,v176
v_exp_f32 v177,v177
v_exp_f32 v178,v178
v_mfma_f32_32x32x16_bf16 v[96:111],v[204:207],v[196:199],v[96:111]
v_exp_f32 v179,v179
v_exp_f32 v180,v180
v_exp_f32 v181,v181
v_mfma_f32_32x32x16_bf16 v[112:127],v[208:211],v[192:195],v[112:127]
v_exp_f32 v182,v182
v_exp_f32 v183,v183
v_exp_f32 v184,v184
v_mfma_f32_32x32x16_bf16 v[112:127],v[212:215],v[196:199],v[112:127]
v_exp_f32 v185,v185
v_exp_f32 v186,v186
v_exp_f32 v187,v187
v_mfma_f32_32x32x16_bf16 v[128:143],v[216:219],v[192:195],v[128:143]
v_exp_f32 v188,v188
v_exp_f32 v189,v189
v_mfma_f32_32x32x16_bf16 v[128:143],v[220:223],v[196:199],v[128:143]
v_exp_f32 v190,v190
v_exp_f32 v191,v191
ds_read_b64 v[72:73],v20 offset:0
ds_read_b64 v[74:75],v21 offset:0
ds_read_b64 v[76:77],v22 offset:0
ds_read_b64 v[78:79],v23 offset:0
ds_read_b64 v[80:81],v20 offset:64
ds_read_b64 v[82:83],v21 offset:64
ds_read_b64 v[84:85],v22 offset:64
ds_read_b64 v[86:87],v23 offset:64
ds_read_b64 v[88:89],v20 offset:128
ds_read_b64 v[90:91],v21 offset:128
ds_read_b64 v[92:93],v22 offset:128
ds_read_b64 v[94:95],v23 offset:128
s_setprio 0
v_add_f32 v176,1.0,v176
v_add_f32 v177,1.0,v177
v_add_f32 v178,1.0,v178
v_add_f32 v179,1.0,v179
v_add_f32 v180,1.0,v180
v_add_f32 v181,1.0,v181
v_add_f32 v182,1.0,v182
v_add_f32 v183,1.0,v183
v_add_f32 v184,1.0,v184
v_add_f32 v185,1.0,v185
v_add_f32 v186,1.0,v186
v_add_f32 v187,1.0,v187
v_add_f32 v188,1.0,v188
v_add_f32 v189,1.0,v189
v_add_f32 v190,1.0,v190
v_add_f32 v191,1.0,v191
s_setprio 1
s_waitcnt lgkmcnt(0)
v_rcp_f32 v176,v176
v_rcp_f32 v177,v177
v_rcp_f32 v178,v178
v_rcp_f32 v179,v179
v_rcp_f32 v180,v180
v_rcp_f32 v181,v181
v_rcp_f32 v182,v182
v_rcp_f32 v183,v183
v_rcp_f32 v184,v184
v_rcp_f32 v185,v185
v_rcp_f32 v186,v186
v_rcp_f32 v187,v187
v_rcp_f32 v188,v188
v_rcp_f32 v189,v189
v_rcp_f32 v190,v190
v_rcp_f32 v191,v191
ds_read_b64_tr_b16 v[200:201],v17 offset:12288
ds_read_b64_tr_b16 v[202:203],v17 offset:13824
ds_read_b64_tr_b16 v[204:205],v17 offset:15360
ds_read_b64_tr_b16 v[206:207],v17 offset:16896
ds_read_b64_tr_b16 v[208:209],v17 offset:12352
ds_read_b64_tr_b16 v[210:211],v17 offset:13888
ds_read_b64_tr_b16 v[212:213],v17 offset:15424
ds_read_b64_tr_b16 v[214:215],v17 offset:16960
ds_read_b64_tr_b16 v[216:217],v17 offset:12416
ds_read_b64_tr_b16 v[218:219],v17 offset:13952
ds_read_b64_tr_b16 v[220:221],v17 offset:15488
ds_read_b64_tr_b16 v[222:223],v17 offset:17024
s_setprio 0
v_mul_f32 v176,v160,v176
v_mul_f32 v177,v161,v177
v_mul_f32 v178,v162,v178
v_mul_f32 v179,v163,v179
v_mul_f32 v180,v164,v180
v_mul_f32 v181,v165,v181
v_mul_f32 v182,v166,v182
v_mul_f32 v183,v167,v183
v_mul_f32 v184,v168,v184
v_mul_f32 v185,v169,v185
v_mul_f32 v186,v170,v186
v_mul_f32 v187,v171,v187
v_mul_f32 v188,v172,v188
v_mul_f32 v189,v173,v189
v_mul_f32 v190,v174,v190
v_mul_f32 v191,v175,v191
v_cvt_pk_bf16_f32 v192,v176,v177
v_cvt_pk_bf16_f32 v193,v178,v179
v_cvt_pk_bf16_f32 v194,v180,v181
v_cvt_pk_bf16_f32 v195,v182,v183
v_cvt_pk_bf16_f32 v196,v184,v185
v_cvt_pk_bf16_f32 v197,v186,v187
v_cvt_pk_bf16_f32 v198,v188,v189
v_cvt_pk_bf16_f32 v199,v190,v191
ds_read_b128 v[144:147],v18 offset:1536
ds_read_b128 v[148:151],v18 offset:1568
ds_read_b128 v[152:155],v18 offset:1600
ds_read_b128 v[156:159],v18 offset:1632
ds_read_b128 v[160:163],v18 offset:1664
ds_read_b128 v[164:167],v18 offset:1696
ds_read_b128 v[168:171],v18 offset:1728
ds_read_b128 v[172:175],v18 offset:1760
ds_read_b128 v[176:179],v18 offset:1792
ds_read_b128 v[180:183],v18 offset:1824
ds_read_b128 v[184:187],v18 offset:1856
ds_read_b128 v[188:191],v18 offset:1888
s_waitcnt lgkmcnt(12)
s_setprio 1
v_mfma_f32_32x32x16_bf16 v[96:111],v[200:203],v[192:195],v[96:111]
v_lshlrev_b32 v232,16,v72
v_lshlrev_b32 v233,16,v73
v_lshlrev_b32 v234,16,v74
v_lshlrev_b32 v235,16,v75
v_mfma_f32_32x32x16_bf16 v[96:111],v[204:207],v[196:199],v[96:111]
v_lshlrev_b32 v236,16,v76
v_lshlrev_b32 v237,16,v77
v_lshlrev_b32 v238,16,v78
v_lshlrev_b32 v239,16,v79
v_mfma_f32_32x32x16_bf16 v[112:127],v[208:211],v[192:195],v[112:127]
v_lshlrev_b32 v240,16,v80
v_lshlrev_b32 v241,16,v81
v_lshlrev_b32 v242,16,v82
v_lshlrev_b32 v243,16,v83
v_mfma_f32_32x32x16_bf16 v[112:127],v[212:215],v[196:199],v[112:127]
v_lshlrev_b32 v244,16,v84
v_lshlrev_b32 v245,16,v85
v_lshlrev_b32 v246,16,v86
v_lshlrev_b32 v247,16,v87
v_mfma_f32_32x32x16_bf16 v[128:143],v[216:219],v[192:195],v[128:143]
v_lshlrev_b32 v248,16,v88
v_lshlrev_b32 v249,16,v89
v_lshlrev_b32 v250,16,v90
v_lshlrev_b32 v251,16,v91
v_mfma_f32_32x32x16_bf16 v[128:143],v[220:223],v[196:199],v[128:143]
v_lshlrev_b32 v252,16,v92
v_lshlrev_b32 v253,16,v93
v_lshlrev_b32 v254,16,v94
v_lshlrev_b32 v255,16,v95
s_setprio 0
s_waitcnt lgkmcnt(0)
v_and_b32 v72,0xffff0000,v72
v_and_b32 v73,0xffff0000,v73
v_and_b32 v74,0xffff0000,v74
v_and_b32 v75,0xffff0000,v75
v_and_b32 v76,0xffff0000,v76
v_and_b32 v77,0xffff0000,v77
v_and_b32 v78,0xffff0000,v78
v_and_b32 v79,0xffff0000,v79
v_and_b32 v80,0xffff0000,v80
v_and_b32 v81,0xffff0000,v81
v_and_b32 v82,0xffff0000,v82
v_and_b32 v83,0xffff0000,v83
v_and_b32 v84,0xffff0000,v84
v_and_b32 v85,0xffff0000,v85
v_and_b32 v86,0xffff0000,v86
v_and_b32 v87,0xffff0000,v87
v_and_b32 v88,0xffff0000,v88
v_and_b32 v89,0xffff0000,v89
v_and_b32 v90,0xffff0000,v90
v_and_b32 v91,0xffff0000,v91
v_and_b32 v92,0xffff0000,v92
v_and_b32 v93,0xffff0000,v93
v_and_b32 v94,0xffff0000,v94
v_and_b32 v95,0xffff0000,v95
v_fma_f32 v232,v96,v144,v232
v_fma_f32 v72,v97,v145,v72
v_fma_f32 v233,v98,v146,v233
v_fma_f32 v73,v99,v147,v73
v_fma_f32 v234,v100,v148,v234
v_fma_f32 v74,v101,v149,v74
v_fma_f32 v235,v102,v150,v235
v_fma_f32 v75,v103,v151,v75
v_fma_f32 v236,v104,v152,v236
v_fma_f32 v76,v105,v153,v76
v_fma_f32 v237,v106,v154,v237
v_fma_f32 v77,v107,v155,v77
v_fma_f32 v238,v108,v156,v238
v_fma_f32 v78,v109,v157,v78
v_fma_f32 v239,v110,v158,v239
v_fma_f32 v79,v111,v159,v79
v_fma_f32 v240,v112,v160,v240
v_fma_f32 v80,v113,v161,v80
v_fma_f32 v241,v114,v162,v241
v_fma_f32 v81,v115,v163,v81
v_fma_f32 v242,v116,v164,v242
v_fma_f32 v82,v117,v165,v82
v_fma_f32 v243,v118,v166,v243
v_fma_f32 v83,v119,v167,v83
v_fma_f32 v244,v120,v168,v244
v_fma_f32 v84,v121,v169,v84
v_fma_f32 v245,v122,v170,v245
v_fma_f32 v85,v123,v171,v85
v_fma_f32 v246,v124,v172,v246
v_fma_f32 v86,v125,v173,v86
v_fma_f32 v247,v126,v174,v247
v_fma_f32 v87,v127,v175,v87
v_fma_f32 v248,v128,v176,v248
v_fma_f32 v88,v129,v177,v88
v_fma_f32 v249,v130,v178,v249
v_fma_f32 v89,v131,v179,v89
v_fma_f32 v250,v132,v180,v250
v_fma_f32 v90,v133,v181,v90
v_fma_f32 v251,v134,v182,v251
v_fma_f32 v91,v135,v183,v91
v_fma_f32 v252,v136,v184,v252
v_fma_f32 v92,v137,v185,v92
v_fma_f32 v253,v138,v186,v253
v_fma_f32 v93,v139,v187,v93
v_fma_f32 v254,v140,v188,v254
v_fma_f32 v94,v141,v189,v94
v_fma_f32 v255,v142,v190,v255
v_fma_f32 v95,v143,v191,v95
v_add_u32 v4,s54,v4
v_add_u32 v5,s54,v5
v_add_u32 v6,s54,v6
v_add_u32 v7,s54,v7
v_add_u32 v8,s54,v8
v_add_u32 v9,s54,v9
v_add_u32 v10,s54,v10
v_add_u32 v11,s54,v11
v_add_u32 v12,s54,v12
v_add_u32 v13,s54,v13
v_add_u32 v14,s54,v14
v_add_u32 v15,s54,v15
v_cvt_pk_bf16_f32 v200,v232,v72
v_cvt_pk_bf16_f32 v201,v233,v73
v_cvt_pk_bf16_f32 v202,v234,v74
v_cvt_pk_bf16_f32 v203,v235,v75
v_cvt_pk_bf16_f32 v204,v236,v76
v_cvt_pk_bf16_f32 v205,v237,v77
v_cvt_pk_bf16_f32 v206,v238,v78
v_cvt_pk_bf16_f32 v207,v239,v79
v_cvt_pk_bf16_f32 v208,v240,v80
v_cvt_pk_bf16_f32 v209,v241,v81
v_cvt_pk_bf16_f32 v210,v242,v82
v_cvt_pk_bf16_f32 v211,v243,v83
v_cvt_pk_bf16_f32 v212,v244,v84
v_cvt_pk_bf16_f32 v213,v245,v85
v_cvt_pk_bf16_f32 v214,v246,v86
v_cvt_pk_bf16_f32 v215,v247,v87
v_cvt_pk_bf16_f32 v216,v248,v88
v_cvt_pk_bf16_f32 v217,v249,v89
v_cvt_pk_bf16_f32 v218,v250,v90
v_cvt_pk_bf16_f32 v219,v251,v91
v_cvt_pk_bf16_f32 v220,v252,v92
v_cvt_pk_bf16_f32 v221,v253,v93
v_cvt_pk_bf16_f32 v222,v254,v94
v_cvt_pk_bf16_f32 v223,v255,v95
ds_write_b64 v20,v[200:201] offset:0
ds_write_b64 v21,v[202:203] offset:0
ds_write_b64 v22,v[204:205] offset:0
ds_write_b64 v23,v[206:207] offset:0
ds_write_b64 v20,v[208:209] offset:64
ds_write_b64 v21,v[210:211] offset:64
ds_write_b64 v22,v[212:213] offset:64
ds_write_b64 v23,v[214:215] offset:64
ds_write_b64 v20,v[216:217] offset:128
ds_write_b64 v21,v[218:219] offset:128
ds_write_b64 v22,v[220:221] offset:128
ds_write_b64 v23,v[222:223] offset:128
ds_read_b128 v[72:75],v19 offset:0
ds_read_b128 v[76:79],v19 offset:1024
ds_read_b128 v[80:83],v19 offset:2048
ds_read_b128 v[84:87],v19 offset:3456
ds_read_b128 v[88:91],v19 offset:4480
ds_read_b128 v[92:95],v19 offset:5504
v_add_u32 v20,s54,v20
v_add_u32 v21,s54,v21
v_add_u32 v22,s54,v22
v_add_u32 v23,s54,v23
s_sub_u32 s68,s68,s54
s_add_u32 s59,s59,1572864
s_add_u32 s53,s53,1
s_cmp_eq_u32 s53,3
s_cselect_b32 s59,0x70000000,s59
s_add_u32 s67,s69,98304
s_waitcnt lgkmcnt(5)
buffer_store_dwordx4 v[72:75],v24,s[40:43],s69 offen nt
s_waitcnt lgkmcnt(4)
buffer_store_dwordx4 v[76:79],v25,s[40:43],s69 offen nt
s_waitcnt lgkmcnt(3)
buffer_store_dwordx4 v[80:83],v26,s[40:43],s69 offen nt
s_waitcnt lgkmcnt(2)
buffer_store_dwordx4 v[84:87],v24,s[40:43],s67 offen nt
s_waitcnt lgkmcnt(1)
buffer_store_dwordx4 v[88:91],v25,s[40:43],s67 offen nt
s_waitcnt lgkmcnt(0)
buffer_store_dwordx4 v[92:95],v26,s[40:43],s67 offen nt
v_add_u32 v19,s54,v19
s_add_u32 s69,s69,1572864
s_sub_u32 s54,0,s54
s_cmp_lt_u32 s53,4
s_cbranch_scc1 L_pass1_%=
L_end_%=:
s_waitcnt vmcnt(0) lgkmcnt(0)
s_endpgm
.p2align 12
)ASM"
    :: "s"(blockIdx.x), "v"(tid), "s"((unsigned long long)__builtin_amdgcn_kernarg_segment_ptr())
    : "v1", "v2", "v3", "v4", "v5", "v6", "v7", "v8", "v9", "v10", "v11", "v12", "v13", "v14", "v15", "v16", "v17", "v18", "v19", "v20", "v21", "v22", "v23", "v24", "v25", "v26", "v27", "v28", "v29", "v30", "v31", "v32", "v33", "v34", "v35", "v36", "v37", "v38", "v39", "v40", "v41", "v42", "v43", "v44", "v45", "v46", "v47", "v48", "v49", "v50", "v51", "v52", "v53", "v54", "v55", "v56", "v57", "v58", "v59", "v60", "v61", "v62", "v63", "v64", "v65", "v66", "v67", "v68", "v69", "v70", "v71", "v72", "v73", "v74", "v75", "v76", "v77", "v78", "v79", "v80", "v81", "v82", "v83", "v84", "v85", "v86", "v87", "v88", "v89", "v90", "v91", "v92", "v93", "v94", "v95", "v96", "v97", "v98", "v99", "v100", "v101", "v102", "v103", "v104", "v105", "v106", "v107", "v108", "v109", "v110", "v111", "v112", "v113", "v114", "v115", "v116", "v117", "v118", "v119", "v120", "v121", "v122", "v123", "v124", "v125", "v126", "v127", "v128", "v129", "v130", "v131", "v132", "v133", "v134", "v135", "v136", "v137", "v138", "v139", "v140", "v141", "v142", "v143", "v144", "v145", "v146", "v147", "v148", "v149", "v150", "v151", "v152", "v153", "v154", "v155", "v156", "v157", "v158", "v159", "v160", "v161", "v162", "v163", "v164", "v165", "v166", "v167", "v168", "v169", "v170", "v171", "v172", "v173", "v174", "v175", "v176", "v177", "v178", "v179", "v180", "v181", "v182", "v183", "v184", "v185", "v186", "v187", "v188", "v189", "v190", "v191", "v192", "v193", "v194", "v195", "v196", "v197", "v198", "v199", "v200", "v201", "v202", "v203", "v204", "v205", "v206", "v207", "v208", "v209", "v210", "v211", "v212", "v213", "v214", "v215", "v216", "v217", "v218", "v219", "v220", "v221", "v222", "v223", "v224", "v225", "v226", "v227", "v228", "v229", "v230", "v231", "v232", "v233", "v234", "v235", "v236", "v237", "v238", "v239", "v240", "v241", "v242", "v243", "v244", "v245", "v246", "v247", "v248", "v249", "v250", "v251", "v252", "v253", "v254", "v255", "s4", "s5", "s6", "s7", "s8", "s9", "s10", "s11", "s12", "s13", "s14", "s15", "s16", "s17", "s18", "s19", "s20", "s21", "s22", "s23", "s24", "s25", "s26", "s27", "s28", "s29", "s30", "s31", "s32", "s33", "s34", "s35", "s36", "s37", "s38", "s39", "s40", "s41", "s42", "s43", "s44", "s45", "s46", "s47", "s48", "s49", "s50", "s51", "s52", "s53", "s54", "s55", "s56", "s57", "s58", "s59", "s60", "s61", "s62", "s63", "s64", "s65", "s66", "s67", "s68", "s69", "s70", "s71", "s72", "s73", "s74", "s75", "s76", "s77", "s78", "s79", "s80", "s81", "s82", "s83", "s84", "s85", "s86", "s87", "s88", "s89", "s90", "s91", "s92", "s93", "s94", "s95", "s96", "s97", "s98", "s99", "s100", "s101", "vcc", "scc", "memory");
}

extern "C" void kernel_launch(void* const* d_in, const int* in_sizes, int n_in,
                              void* d_out, int out_size, void* d_ws, size_t ws_size,
                              hipStream_t stream) {
    (void)hipFuncSetAttribute((const void*)convnext_kernel, hipFuncAttributeMaxDynamicSharedMemorySize, LDS_BYTES);
    convnext_kernel<<<256, 512, LDS_BYTES, stream>>>(
        (const unsigned short*)d_in[0], (const unsigned short*)d_in[1], (const unsigned short*)d_in[2], (const unsigned short*)d_in[3],
        (const unsigned short*)d_in[4], (const unsigned short*)d_in[5], (const unsigned short*)d_in[6], (unsigned short*)d_out);
}
```

```cpp
#include <hip/hip_runtime.h>
#include <stdint.h>

constexpr int LDS_BYTES = 163584;

__global__ __launch_bounds__(512, 2) void convnext_kernel(
    const unsigned short* __restrict__ x, const unsigned short* __restrict__ dw, const unsigned short* __restrict__ w1,
    const unsigned short* __restrict__ b1, const unsigned short* __restrict__ w2, const unsigned short* __restrict__ b2,
    const unsigned short* __restrict__ gamma, unsigned short* __restrict__ out) {
    const unsigned tid = threadIdx.x;
    asm volatile(
R"ASM(
N_main_%=:
s_load_dwordx16 s[20:35],%2,0
v_lshrrev_b32 v236,6,%1
v_and_b32 v232,63,%1
v_readfirstlane_b32 s52,v236
s_getpc_b64 s[14:15]
N_ep_%=:
v_lshlrev_b32 v252,7,v232
v_lshrrev_b32 v253,8,%1
v_mul_u32_u24 v253,N_B_%=-N_ep_%=,v253
v_add_u32 v252,v252,v253
global_load_dword v254,v252,s[14:15]
v_mov_b32 v228,0
v_mov_b32 v229,0
v_mov_b32 v230,0
v_mov_b32 v231,0
s_and_b32 s64,%0,7
s_lshr_b32 s70,%0,3
s_lshl_b32 s71,s64,6
s_mul_i32 s68,s70,3072
s_and_b32 s64,s52,3
s_mul_i32 s67,s64,54
s_cmp_eq_u32 s70,0
s_cselect_b32 s66,0,99
s_cmp_eq_u32 s70,31
s_cselect_b32 s66,17,s66
v_add_u32 v236,s67,v232
s_mul_i32 s57,s64,864
s_add_u32 s57,s57,73728
v_mul_u32_u24 v237,0xaaab,v236
s_sub_u32 s65,s71,1
v_lshrrev_b32 v237,19,v237
s_mul_i32 s56,s65,98304
v_mul_u32_u24 v238,12,v237
s_add_u32 s56,s56,s68
v_sub_u32 v238,v236,v238
s_cmp_lt_i32 s65,0
s_cselect_b32 s101,0x70000000,s56
v_add_u32 v236,1,v237
s_mov_b32 s62,-1
s_mov_b32 s63,0x3fffff
v_bfe_u32 v236,v236,2,2
s_mov_b32 s38,50331840
s_mov_b32 s39,0x00020000
v_xor_b32 v238,v238,v236
s_mov_b32 s96,0x8befe000
s_mov_b32 s99,0x72800000
v_mul_u32_u24 v236,192,v237
s_mov_b32 s98,0xffff0000
s_bitcmp1_b32 s52,1
s_cselect_b64 vcc,-1,0
v_lshl_add_u32 v241,v238,4,v236
v_cmp_eq_u32_e64 s[92:93],s66,v237
v_mov_b32 v236,0x7fff0000
s_nop 0
v_cndmask_b32_e64 v241,v241,v236,s[92:93]
s_cmp_ge_u32 s52,4
s_cbranch_scc1 N_B_%=
s_setprio 3
s_waitcnt lgkmcnt(0)
s_sub_u32 s36,s20,192
s_subb_u32 s37,s21,0
s_and_b32 s37,s37,0xffff
s_bitcmp0_b32 s52,0
s_cselect_b64 s[44:45],s[24:25],s[28:29]
s_and_b32 s45,s45,0xffff
s_mov_b32 s40,s34
s_and_b32 s41,s35,0xffff
s_mov_b64 exec,s[62:63]
s_add_u32 m0,s57,0
s_nop 0
buffer_load_dwordx4 v241,s[36:39],s101 offen lds
s_add_u32 m0,s57,3488
s_add_u32 s101,s56,98304
buffer_load_dwordx4 v241,s[36:39],s101 offen lds
s_add_u32 m0,s57,6976
s_add_u32 s101,s56,196608
buffer_load_dwordx4 v241,s[36:39],s101 offen lds
s_add_u32 m0,s57,10464
s_add_u32 s101,s56,294912
buffer_load_dwordx4 v241,s[36:39],s101 offen lds
s_mov_b64 exec,-1
s_and_b32 s66,s52,3
s_cmp_eq_u32 s66,3
s_cselect_b32 s14,1,s66
v_min_u32 v236,47,v232
s_mul_i32 s64,s14,192
s_lshl_b32 s65,s14,7
v_lshl_add_u32 v200,v236,2,s64
s_add_u32 s65,s65,144256
v_and_b32 v199,255,%1
v_min_u32 v237,191,v199
v_lshlrev_b32 v201,2,v237
v_min_u32 v249,95,v199
v_lshlrev_b32 v202,1,v249
s_lshl_b32 s64,s66,3
s_lshr_b32 s67,0x30011100,s64
s_and_b32 s67,s67,0xff
s_lshl_b32 s67,s67,3
s_add_u32 s67,s67,143488
v_lshrrev_b32 v238,2,v236
v_and_b32 v239,3,v236
v_mul_u32_u24 v238,1152,v238
v_lshl_add_u32 v238,v239,5,v238
s_and_b32 s66,s66,1
s_lshl_b32 s64,s66,3
s_cmp_eq_u32 s66,0
s_movk_i32 s66,128
s_cselect_b32 s66,640,s66
v_add_u32 v203,s65,v238
v_add_u32 v209,s67,v238
v_lshl_add_u32 v204,v239,2,v203
v_add_u32 v215,s66,v209
v_xor_b32 v237,s64,v209
v_lshrrev_b32 v239,1,v239
v_xor_b32 v238,s64,v215
ds_write_b128 v203,v[228:231] offset:0
ds_write_b128 v203,v[228:231] offset:16
ds_write2_b64 v209,v[228:229],v[228:229] offset0:0 offset1:2
ds_write2_b64 v215,v[228:229],v[228:229] offset0:0 offset1:2
ds_write2_b64 v237,v[228:229],v[228:229] offset0:0 offset1:2
ds_write2_b64 v238,v[228:229],v[228:229] offset0:0 offset1:2
v_lshl_add_u32 v209,v239,2,v209
v_lshl_add_u32 v215,v239,2,v215
ds_write_b32 v228,v228 offset:11648
global_load_dword v242,v200,s[22:23] offset:0
global_load_dword v243,v200,s[22:23] offset:576
global_load_dword v244,v200,s[22:23] offset:1152
global_load_ushort v247,v202,s[32:33]
global_load_dword v245,v201,s[26:27]
global_load_ushort v246,v202,s[30:31]
s_mov_b64 exec,s[62:63]
s_add_u32 m0,s57,13952
s_add_u32 s101,s56,393216
buffer_load_dwordx4 v241,s[36:39],s101 offen lds
s_add_u32 m0,s57,17440
s_add_u32 s101,s56,491520
buffer_load_dwordx4 v241,s[36:39],s101 offen lds
s_add_u32 m0,s57,20928
s_add_u32 s101,s56,589824
buffer_load_dwordx4 v241,s[36:39],s101 offen lds
s_add_u32 m0,s57,24416
s_add_u32 s101,s56,688128
buffer_load_dwordx4 v241,s[36:39],s101 offen lds
s_add_u32 m0,s57,27904
s_add_u32 s101,s56,786432
buffer_load_dwordx4 v241,s[36:39],s101 offen lds
s_add_u32 m0,s57,31392
s_add_u32 s101,s56,884736
buffer_load_dwordx4 v241,s[36:39],s101 offen lds
s_mov_b64 exec,-1
s_and_b32 s64,s52,3
s_lshl_b32 s10,s64,2
s_add_u32 s64,s10,s71
s_mul_i32 s61,s64,98304
s_add_u32 s61,s61,s68
s_mov_b32 s54,0x44ea44ea
s_mov_b32 s55,0x877f877f
s_mov_b32 s8,0x0ff00ff0
s_mov_b32 s9,0x0ff00ff0
s_and_b32 s64,s52,3
s_mul_i32 s67,s64,54
s_cmp_eq_u32 s70,31
s_cselect_b32 s66,17,99
s_mov_b32 s12,0xaaaaaaaa
s_mov_b32 s13,0xaaaaaaaa
s_mov_b32 s42,50331648
s_mov_b32 s43,0x00020000
s_bitcmp0_b32 s52,0
s_mov_b32 s58,40896
s_cselect_b32 s58,16256,s58
s_cselect_b32 s59,64,6144
s_movk_i32 s53,1024
s_cselect_b32 s53,6144,s53
s_cselect_b32 s60,0,67584
s_cselect_b32 s97,64,0
s_cselect_b32 s69,0,0x70000000
s_mov_b32 s100,73728
s_cselect_b32 s100,110688,s100
s_cselect_b32 s64,16,0
s_movk_i32 s50,1032
s_cselect_b32 s50,1024,s50
s_cselect_b32 s51,s53,-72704
s_mov_b32 s46,73728
s_mov_b32 s47,0x00020000
v_mov_b32 v1,0
v_lshlrev_b32 v233,4,v232
v_lshlrev_b32 v234,3,%1
v_add_u32 v233,157312,v233
v_add_u32 v234,157312,v234
v_lshrrev_b32 v236,5,v249
v_bfe_u32 v237,v249,2,1
v_and_b32 v238,3,v249
v_lshlrev_b32 v206,7,v236
v_lshl_add_u32 v206,v237,5,v206
v_lshl_add_u32 v206,v238,2,v206
v_bfe_u32 v239,v249,3,1
v_lshl_add_u32 v206,v239,6,v206
v_bfe_u32 v239,v249,4,1
v_lshl_add_u32 v206,v239,4,v206
v_add_u32 v206,9728,v206
v_lshl_add_u32 v207,v236,1,v237
v_lshlrev_b32 v207,4,v207
v_bfe_u32 v239,v249,3,2
v_lshl_add_u32 v207,v239,2,v207
v_add_u32 v207,v207,v238
v_lshlrev_b32 v207,4,v207
v_add_u32 v207,10112,v207
v_lshlrev_b32 v208,4,v238
v_cmp_eq_u32_e64 s[48:49],1,v237
v_lshlrev_b32 v205,1,v201
v_add_u32 v205,8192,v205
v_mov_b32 v221,0
v_mov_b32 v222,0x3f800000
s_waitcnt vmcnt(6)
v_and_b32 v212,0xffff,v242
v_and_b32 v242,s98,v242
ds_write2_b32 v209,v212,v242 offset0:0 offset1:4
v_and_b32 v213,0xffff,v243
v_and_b32 v243,s98,v243
ds_write2_b32 v215,v213,v243 offset0:0 offset1:4
v_and_b32 v214,0xffff,v244
v_and_b32 v244,s98,v244
ds_write2_b32 v204,v214,v244 offset0:0 offset1:4
v_lshlrev_b32 v247,16,v247
v_lshlrev_b32 v246,16,v246
v_mul_f32 v247,0x6c800000,v247
v_mul_f32 v246,0x12800000,v246
v_rcp_f32 v224,v247
v_lshlrev_b32 v210,16,v245
v_and_b32 v211,s98,v245
v_med3_f32 v224,v224,v222,-v222
v_pk_mul_f32 v[210:211],v[210:211],s[96:97] op_sel_hi:[1,0]
v_cvt_pk_bf16_f32 v220,v224,v228
ds_write_b64 v205,v[210:211]
ds_write_b32 v206,v246 offset:2048
v_lshlrev_b32 v222,16,v220
v_rcp_f32 v222,v222
v_lshlrev_b64 v[220:221],v208,v[220:221]
v_cndmask_b32_e64 v224,v220,0,s[48:49]
v_cndmask_b32_e64 v226,0,v220,s[48:49]
v_cndmask_b32_e64 v225,v221,0,s[48:49]
v_cndmask_b32_e64 v227,0,v221,s[48:49]
ds_write_b128 v207,v[224:227]
ds_write_b32 v206,v222
s_waitcnt lgkmcnt(0)
s_lshl_b32 s4,s52,1
s_and_b32 s5,s52,1
s_sub_u32 s4,s4,s5
s_sub_u32 s6,s4,s10
s_mul_i32 s4,s4,3488
s_mul_i32 s6,s6,98304
s_add_u32 s49,s61,s6
v_mov_b32 v124,s4
s_mov_b64 exec,s[8:9]
v_add_u32 v124,6976,v124
s_mov_b64 exec,-1
s_waitcnt vmcnt(0)
s_barrier
s_setprio 3
ds_read_b128 v[12:15],v233 offset:2048
ds_read_b128 v[8:11],v233 offset:1024
ds_read_b128 v[4:7],v233 offset:0
ds_read_b64 v[2:3],v234 offset:3072
s_waitcnt lgkmcnt(2)
v_xad_u32 v125,v15,16,v124
v_xad_u32 v126,v15,32,v124
v_xad_u32 v127,v15,48,v124
v_add_u32 v124,v15,v124
ds_read_b128 v[88:91],v11 offset:768
ds_read_b128 v[92:95],v12 offset:0
ds_read_b128 v[96:99],v11 offset:1920
ds_read_b128 v[100:103],v12 offset:1152
ds_read_b128 v[48:51],v124 offset:6976
ds_read_b128 v[40:43],v124 offset:0
ds_read_b128 v[44:47],v124 offset:3488
ds_read_b128 v[60:63],v125 offset:6976
ds_read_b128 v[52:55],v125 offset:0
ds_read_b128 v[56:59],v125 offset:3488
s_waitcnt lgkmcnt(5)
v_mfma_f32_16x16x32_bf16 v[160:163],v[88:91],v[48:51],0
s_waitcnt lgkmcnt(3)
ds_read_b128 v[48:51],v124 offset:7040
ds_read_b128 v[88:91],v11 offset:5376
v_smfmac_f32_16x16x64_bf16 v[160:163],v[92:95],v[40:47],v13
s_waitcnt lgkmcnt(4)
ds_read_b128 v[40:43],v124 offset:64
ds_read_b128 v[44:47],v124 offset:3552
ds_read_b128 v[92:95],v12 offset:4608
v_mfma_f32_16x16x32_bf16 v[164:167],v[96:99],v[60:63],0
s_waitcnt lgkmcnt(5)
ds_read_b128 v[60:63],v125 offset:7040
ds_read_b128 v[96:99],v11 offset:6528
v_smfmac_f32_16x16x64_bf16 v[164:167],v[100:103],v[52:59],v13
s_waitcnt lgkmcnt(5)
ds_read_b128 v[52:55],v125 offset:64
ds_read_b128 v[56:59],v125 offset:3552
ds_read_b128 v[100:103],v12 offset:5760
v_mfma_f32_16x16x32_bf16 v[176:179],v[88:91],v[48:51],0
s_waitcnt lgkmcnt(5)
ds_read_b128 v[48:51],v124 offset:7104
ds_read_b128 v[88:91],v11 offset:9984
v_add_f32 v120,v160,v161
v_mul_f32 v122,v160,v160
v_fmac_f32 v122,v161,v161
v_smfmac_f32_16x16x64_bf16 v[176:179],v[92:95],v[40:47],v13
s_waitcnt lgkmcnt(5)
ds_read_b128 v[40:43],v124 offset:128
ds_read_b128 v[44:47],v124 offset:3616
ds_read_b128 v[92:95],v12 offset:9216
v_add_f32 v121,v162,v163
v_mul_f32 v123,v162,v162
v_fmac_f32 v123,v163,v163
v_mfma_f32_16x16x32_bf16 v[180:183],v[96:99],v[60:63],0
s_waitcnt lgkmcnt(5)
ds_read_b128 v[60:63],v125 offset:7104
ds_read_b128 v[96:99],v11 offset:11136
v_add_f32 v120,v120,v164
v_fmac_f32 v122,v164,v164
v_add_f32 v120,v120,v165
v_fmac_f32 v122,v165,v165
v_smfmac_f32_16x16x64_bf16 v[180:183],v[100:103],v[52:59],v13
s_waitcnt lgkmcnt(5)
ds_read_b128 v[52:55],v125 offset:128
ds_read_b128 v[56:59],v125 offset:3616
ds_read_b128 v[100:103],v12 offset:10368
v_add_f32 v121,v121,v166
v_fmac_f32 v123,v166,v166
v_add_f32 v121,v121,v167
v_fmac_f32 v123,v167,v167
v_mfma_f32_16x16x32_bf16 v[192:195],v[88:91],v[48:51],0
s_waitcnt lgkmcnt(5)
ds_read_b128 v[48:51],v126 offset:6976
ds_read_b128 v[88:91],v11 offset:3072
v_add_f32 v120,v120,v176
v_fmac_f32 v122,v176,v176
v_add_f32 v120,v120,v177
v_fmac_f32 v122,v177,v177
v_smfmac_f32_16x16x64_bf16 v[192:195],v[92:95],v[40:47],v13
s_waitcnt lgkmcnt(5)
ds_read_b128 v[40:43],v126 offset:0
ds_read_b128 v[44:47],v126 offset:3488
ds_read_b128 v[92:95],v12 offset:2304
v_add_f32 v121,v121,v178
v_fmac_f32 v123,v178,v178
v_add_f32 v121,v121,v179
v_fmac_f32 v123,v179,v179
v_mfma_f32_16x16x32_bf16 v[196:199],v[96:99],v[60:63],0
s_waitcnt lgkmcnt(5)
ds_read_b128 v[60:63],v127 offset:6976
ds_read_b128 v[96:99],v11 offset:4224
v_add_f32 v120,v120,v180
v_fmac_f32 v122,v180,v180
v_add_f32 v120,v120,v181
v_fmac_f32 v122,v181,v181
v_smfmac_f32_16x16x64_bf16 v[196:199],v[100:103],v[52:59],v13
s_waitcnt lgkmcnt(5)
ds_read_b128 v[52:55],v127 offset:0
ds_read_b128 v[56:59],v127 offset:3488
ds_read_b128 v[100:103],v12 offset:3456
v_add_f32 v121,v121,v182
v_fmac_f32 v123,v182,v182
v_add_f32 v121,v121,v183
v_fmac_f32 v123,v183,v183
v_mfma_f32_16x16x32_bf16 v[168:171],v[88:91],v[48:51],0
s_waitcnt lgkmcnt(5)
ds_read_b128 v[48:51],v126 offset:7040
ds_read_b128 v[88:91],v11 offset:7680
v_add_f32 v120,v120,v192
v_fmac_f32 v122,v192,v192
v_add_f32 v120,v120,v193
v_fmac_f32 v122,v193,v193
v_smfmac_f32_16x16x64_bf16 v[168:171],v[92:95],v[40:47],v13
s_waitcnt lgkmcnt(5)
ds_read_b128 v[40:43],v126 offset:64
ds_read_b128 v[44:47],v126 offset:3552
ds_read_b128 v[92:95],v12 offset:6912
v_add_f32 v121,v121,v194
v_fmac_f32 v123,v194,v194
v_add_f32 v121,v121,v195
v_fmac_f32 v123,v195,v195
v_mfma_f32_16x16x32_bf16 v[172:175],v[96:99],v[60:63],0
s_waitcnt lgkmcnt(5)
ds_read_b128 v[60:63],v127 offset:7040
ds_read_b128 v[96:99],v11 offset:8832
v_add_f32 v120,v120,v196
v_fmac_f32 v122,v196,v196
v_add_f32 v120,v120,v197
v_fmac_f32 v122,v197,v197
v_add_f32 v121,v121,v198
v_smfmac_f32_16x16x64_bf16 v[172:175],v[100:103],v[52:59],v13
s_waitcnt lgkmcnt(5)
ds_read_b128 v[52:55],v127 offset:64
ds_read_b128 v[56:59],v127 offset:3552
ds_read_b128 v[100:103],v12 offset:8064
v_fmac_f32 v123,v198,v198
v_add_f32 v121,v121,v199
v_fmac_f32 v123,v199,v199
v_add_u32 v124,v124,v8
v_mfma_f32_16x16x32_bf16 v[184:187],v[88:91],v[48:51],0
s_waitcnt lgkmcnt(5)
ds_read_b128 v[48:51],v126 offset:7104
ds_read_b128 v[88:91],v11 offset:12288
v_add_f32 v120,v120,v168
v_fmac_f32 v122,v168,v168
v_add_f32 v120,v120,v169
v_fmac_f32 v122,v169,v169
v_smfmac_f32_16x16x64_bf16 v[184:187],v[92:95],v[40:47],v13
s_waitcnt lgkmcnt(5)
ds_read_b128 v[40:43],v126 offset:128
ds_read_b128 v[44:47],v126 offset:3616
ds_read_b128 v[92:95],v12 offset:11520
v_add_f32 v121,v121,v170
v_fmac_f32 v123,v170,v170
v_add_f32 v121,v121,v171
v_fmac_f32 v123,v171,v171
v_mfma_f32_16x16x32_bf16 v[188:191],v[96:99],v[60:63],0
s_waitcnt lgkmcnt(5)
ds_read_b128 v[60:63],v127 offset:7104
ds_read_b128 v[96:99],v11 offset:13440
v_add_f32 v120,v120,v172
v_fmac_f32 v122,v172,v172
v_add_f32 v120,v120,v173
v_fmac_f32 v122,v173,v173
v_smfmac_f32_16x16x64_bf16 v[188:191],v[100:103],v[52:59],v13
s_waitcnt lgkmcnt(5)
ds_read_b128 v[52:55],v127 offset:128
ds_read_b128 v[56:59],v127 offset:3616
ds_read_b128 v[100:103],v12 offset:12672
v_add_f32 v121,v121,v174
v_fmac_f32 v123,v174,v174
v_add_f32 v121,v121,v175
v_fmac_f32 v123,v175,v175
v_mfma_f32_16x16x32_bf16 v[200:203],v[88:91],v[48:51],0
s_waitcnt lgkmcnt(5)
ds_read_b128 v[144:147],v124 offset:3488
ds_read_b128 v[148:151],v124 offset:3680
ds_read_b128 v[88:91],v124 offset:3552
v_add_f32 v120,v120,v184
v_fmac_f32 v122,v184,v184
v_add_f32 v120,v120,v185
v_fmac_f32 v122,v185,v185
v_smfmac_f32_16x16x64_bf16 v[200:203],v[92:95],v[40:47],v13
s_waitcnt lgkmcnt(6)
ds_read_b128 v[64:67],v14 offset:512
ds_read_b128 v[68:71],v7 offset:3712
ds_read_b128 v[92:95],v124 offset:3744
v_add_f32 v121,v121,v186
v_fmac_f32 v123,v186,v186
v_add_f32 v121,v121,v187
v_fmac_f32 v123,v187,v187
v_mfma_f32_16x16x32_bf16 v[204:207],v[96:99],v[60:63],0
s_waitcnt lgkmcnt(6)
ds_read_b128 v[104:107],v14 offset:0
ds_read_b128 v[108:111],v7 offset:3584
ds_read_b128 v[80:83],v14 offset:1024
v_add_f32 v120,v120,v188
v_fmac_f32 v122,v188,v188
v_add_f32 v120,v120,v189
v_fmac_f32 v122,v189,v189
v_smfmac_f32_16x16x64_bf16 v[204:207],v[100:103],v[52:59],v13
ds_read_b128 v[84:87],v7 offset:3840
ds_read_b128 v[96:99],v124 offset:3616
ds_read_b128 v[72:75],v14 offset:768
v_add_f32 v121,v121,v190
v_fmac_f32 v123,v190,v190
v_add_f32 v121,v121,v191
v_fmac_f32 v123,v191,v191
ds_read_b128 v[76:79],v7 offset:3744
ds_read_b128 v[100:103],v124 offset:3808
ds_read_b128 v[152:155],v14 offset:256
ds_read_b128 v[156:159],v7 offset:3616
ds_read_b128 v[128:131],v14 offset:1280
ds_read_b128 v[132:135],v7 offset:3872
v_add_f32 v120,v120,v200
v_fmac_f32 v122,v200,v200
v_add_f32 v120,v120,v201
v_fmac_f32 v122,v201,v201
v_add_f32 v121,v121,v202
v_fmac_f32 v123,v202,v202
v_add_f32 v121,v121,v203
v_fmac_f32 v123,v203,v203
v_add_f32 v120,v120,v204
v_fmac_f32 v122,v204,v204
v_add_f32 v120,v120,v205
v_fmac_f32 v122,v205,v205
v_add_f32 v121,v121,v206
v_fmac_f32 v123,v206,v206
v_add_f32 v121,v121,v207
v_fmac_f32 v123,v207,v207
v_sub_u32 v124,v124,v8
v_permlane32_swap_b32 v120,v121
v_permlane32_swap_b32 v122,v123
v_add_f32 v120,v120,v121
v_add_f32 v122,v122,v123
s_nop 1
v_permlane16_swap_b32 v120,v122
v_add_f32 v61,v120,v122
v_add_f32 v62,v120,v122
s_nop 1
v_permlane16_swap_b32 v61,v62
v_mul_f32 v61,0x3c2aaaab,v61
v_mul_f32 v62,0x3c2aaaab,v62
v_mul_f32 v60,0x5e000000,v62
v_fma_f32 v62,-v61,v61,v62
v_pk_maximum3_f16 v1,v1,v60,v60 op_sel:[0,1,1] op_sel_hi:[1,1,1]
v_max_f32 v62,0,v62
v_add_f32 v62,0x3727c5ac,v62
v_rsq_f32 v62,v62
s_nop 0
v_mul_f32 v120,0x8befe000,v62
v_mul_f32 v121,0x8befe000,v62
v_mul_f32 v122,-v61,v120
v_mul_f32 v123,-v61,v120
v_permlane32_swap_b32 v120,v121
s_nop 0
v_permlane32_swap_b32 v122,v123
v_pk_fma_f32 v[160:161],v[160:161],v[120:121],v[122:123] op_sel:[0,0,0] op_sel_hi:[1,0,0]
v_pk_fma_f32 v[162:163],v[162:163],v[120:121],v[122:123] op_sel:[0,1,1] op_sel_hi:[1,1,1]
v_pk_fma_f32 v[164:165],v[164:165],v[120:121],v[122:123] op_sel:[0,0,0] op_sel_hi:[1,0,0]
v_pk_fma_f32 v[166:167],v[166:167],v[120:121],v[122:123] op_sel:[0,1,1] op_sel_hi:[1,1,1]
v_pk_fma_f32 v[168:169],v[168:169],v[120:121],v[122:123] op_sel:[0,0,0] op_sel_hi:[1,0,0]
v_pk_fma_f32 v[170:171],v[170:171],v[120:121],v[122:123] op_sel:[0,1,1] op_sel_hi:[1,1,1]
v_pk_fma_f32 v[172:173],v[172:173],v[120:121],v[122:123] op_sel:[0,0,0] op_sel_hi:[1,0,0]
v_pk_fma_f32 v[174:175],v[174:175],v[120:121],v[122:123] op_sel:[0,1,1] op_sel_hi:[1,1,1]
v_pk_fma_f32 v[176:177],v[176:177],v[120:121],v[122:123] op_sel:[0,0,0] op_sel_hi:[1,0,0]
v_pk_fma_f32 v[178:179],v[178:179],v[120:121],v[122:123] op_sel:[0,1,1] op_sel_hi:[1,1,1]
v_pk_fma_f32 v[180:181],v[180:181],v[120:121],v[122:123] op_sel:[0,0,0] op_sel_hi:[1,0,0]
v_pk_fma_f32 v[182:183],v[182:183],v[120:121],v[122:123] op_sel:[0,1,1] op_sel_hi:[1,1,1]
v_pk_fma_f32 v[184:185],v[184:185],v[120:121],v[122:123] op_sel:[0,0,0] op_sel_hi:[1,0,0]
v_pk_fma_f32 v[186:187],v[186:187],v[120:121],v[122:123] op_sel:[0,1,1] op_sel_hi:[1,1,1]
v_pk_fma_f32 v[188:189],v[188:189],v[120:121],v[122:123] op_sel:[0,0,0] op_sel_hi:[1,0,0]
v_pk_fma_f32 v[190:191],v[190:191],v[120:121],v[122:123] op_sel:[0,1,1] op_sel_hi:[1,1,1]
v_pk_fma_f32 v[192:193],v[192:193],v[120:121],v[122:123] op_sel:[0,0,0] op_sel_hi:[1,0,0]
v_pk_fma_f32 v[194:195],v[194:195],v[120:121],v[122:123] op_sel:[0,1,1] op_sel_hi:[1,1,1]
v_pk_fma_f32 v[196:197],v[196:197],v[120:121],v[122:123] op_sel:[0,0,0] op_sel_hi:[1,0,0]
v_pk_fma_f32 v[198:199],v[198:199],v[120:121],v[122:123] op_sel:[0,1,1] op_sel_hi:[1,1,1]
v_pk_fma_f32 v[200:201],v[200:201],v[120:121],v[122:123] op_sel:[0,0,0] op_sel_hi:[1,0,0]
v_pk_fma_f32 v[202:203],v[202:203],v[120:121],v[122:123] op_sel:[0,1,1] op_sel_hi:[1,1,1]
v_pk_fma_f32 v[204:205],v[204:205],v[120:121],v[122:123] op_sel:[0,0,0] op_sel_hi:[1,0,0]
v_pk_fma_f32 v[206:207],v[206:207],v[120:121],v[122:123] op_sel:[0,1,1] op_sel_hi:[1,1,1]
v_cvt_pk_bf16_f32 v16,v160,v161
v_cvt_pk_bf16_f32 v20,v162,v163
v_cvt_pk_bf16_f32 v17,v164,v165
v_cvt_pk_bf16_f32 v21,v166,v167
v_cvt_pk_bf16_f32 v18,v168,v169
v_cvt_pk_bf16_f32 v22,v170,v171
v_cvt_pk_bf16_f32 v19,v172,v173
v_cvt_pk_bf16_f32 v23,v174,v175
v_cvt_pk_bf16_f32 v24,v176,v177
s_waitcnt lgkmcnt(10)
v_mfma_f32_16x16x32_bf16 v[160:163],v[104:107],v[144:147],v[108:111]
v_cvt_pk_bf16_f32 v28,v178,v179
v_cvt_pk_bf16_f32 v25,v180,v181
v_mfma_f32_16x16x32_bf16 v[164:167],v[104:107],v[148:151],v[108:111]
s_waitcnt lgkmcnt(2)
v_cvt_pk_bf16_f32 v29,v182,v183
v_cvt_pk_bf16_f32 v26,v184,v185
v_mfma_f32_16x16x32_bf16 v[168:171],v[152:155],v[144:147],v[156:159]
v_cvt_pk_bf16_f32 v30,v186,v187
v_cvt_pk_bf16_f32 v27,v188,v189
v_mfma_f32_16x16x32_bf16 v[172:175],v[152:155],v[148:151],v[156:159]
v_cvt_pk_bf16_f32 v31,v190,v191
v_cvt_pk_bf16_f32 v32,v192,v193
v_mfma_f32_16x16x32_bf16 v[176:179],v[64:67],v[88:91],v[68:71]
v_cvt_pk_bf16_f32 v36,v194,v195
v_cvt_pk_bf16_f32 v33,v196,v197
v_mfma_f32_16x16x32_bf16 v[180:183],v[64:67],v[92:95],v[68:71]
v_cvt_pk_bf16_f32 v37,v198,v199
v_cvt_pk_bf16_f32 v34,v200,v201
v_mfma_f32_16x16x32_bf16 v[184:187],v[72:75],v[88:91],v[76:79]
v_cvt_pk_bf16_f32 v38,v202,v203
v_cvt_pk_bf16_f32 v35,v204,v205
v_mfma_f32_16x16x32_bf16 v[188:191],v[72:75],v[92:95],v[76:79]
v_cvt_pk_bf16_f32 v39,v206,v207
v_mfma_f32_16x16x32_bf16 v[192:195],v[80:83],v[96:99],v[84:87]
v_mfma_f32_16x16x32_bf16 v[196:199],v[80:83],v[100:103],v[84:87]
s_waitcnt lgkmcnt(0)
v_mfma_f32_16x16x32_bf16 v[200:203],v[128:131],v[96:99],v[132:135]
v_mfma_f32_16x16x32_bf16 v[204:207],v[128:131],v[100:103],v[132:135]
ds_read_b128 v[88:91],v11 offset:768
ds_read_b128 v[92:95],v12 offset:0
ds_read_b128 v[96:99],v11 offset:1920
ds_read_b128 v[100:103],v12 offset:1152
s_waitcnt lgkmcnt(0)
s_barrier
ds_read_b128 v[48:51],v124 offset:34880
ds_read_b128 v[40:43],v124 offset:27904
ds_read_b128 v[44:47],v124 offset:31392
ds_read_b128 v[60:63],v125 offset:34880
ds_read_b128 v[52:55],v125 offset:27904
ds_read_b128 v[56:59],v125 offset:31392
ds_read_b64_tr_b16 v[64:65],v4 offset:12832
ds_read_b64_tr_b16 v[66:67],v4 offset:14880
ds_read_b128 v[112:115],v7 offset:128
ds_read_b64_tr_b16 v[68:69],v5 offset:12832
ds_read_b64_tr_b16 v[70:71],v5 offset:14880
ds_read_b128 v[116:119],v7 offset:160
ds_read_b64_tr_b16 v[72:73],v4 offset:16928
ds_read_b64_tr_b16 v[74:75],v4 offset:18976
ds_read_b64_tr_b16 v[76:77],v5 offset:16928
ds_read_b64_tr_b16 v[78:79],v5 offset:18976
ds_read_b64_tr_b16 v[80:81],v4 offset:21024
ds_read_b64_tr_b16 v[82:83],v4 offset:23072
ds_read_b64_tr_b16 v[84:85],v5 offset:21024
ds_read_b64_tr_b16 v[86:87],v5 offset:23072
s_waitcnt lgkmcnt(15)
v_mfma_f32_16x16x32_bf16 v[208:211],v[88:91],v[48:51],0
ds_read_b128 v[48:51],v124 offset:34944
ds_read_b128 v[88:91],v11 offset:5376
v_smfmac_f32_16x16x64_bf16 v[208:211],v[92:95],v[40:47],v13
ds_read_b128 v[40:43],v124 offset:27968
ds_read_b128 v[44:47],v124 offset:31456
ds_read_b128 v[92:95],v12 offset:4608
v_mfma_f32_16x16x32_bf16 v[212:215],v[96:99],v[60:63],0
s_waitcnt lgkmcnt(15)
ds_read_b128 v[60:63],v125 offset:34944
ds_read_b128 v[96:99],v11 offset:6528
v_smfmac_f32_16x16x64_bf16 v[212:215],v[100:103],v[52:59],v13
s_waitcnt lgkmcnt(5)
ds_read_b128 v[52:55],v125 offset:27968
ds_read_b128 v[56:59],v125 offset:31456
ds_read_b128 v[100:103],v12 offset:5760
v_mfma_f32_16x16x32_bf16 v[224:227],v[88:91],v[48:51],0
s_waitcnt lgkmcnt(5)
ds_read_b128 v[48:51],v124 offset:35008
ds_read_b128 v[88:91],v11 offset:9984
v_add_f32 v120,v208,v209
v_mul_f32 v122,v208,v208
v_smfmac_f32_16x16x64_bf16 v[224:227],v[92:95],v[40:47],v13
ds_read_b128 v[40:43],v124 offset:28032
ds_read_b128 v[44:47],v124 offset:31520
ds_read_b128 v[92:95],v12 offset:9216
v_fmac_f32 v122,v209,v209
v_mfma_f32_16x16x32_bf16 v[128:131],v[64:67],v[16:19],v[112:115]
v_add_f32 v121,v210,v211
v_mul_f32 v123,v210,v210
v_mfma_f32_16x16x32_bf16 v[136:139],v[64:67],v[20:23],v[112:115]
s_waitcnt lgkmcnt(8)
v_fmac_f32 v123,v211,v211
v_mfma_f32_16x16x32_bf16 v[228:231],v[96:99],v[60:63],0
s_waitcnt lgkmcnt(5)
ds_read_b128 v[60:63],v125 offset:35008
ds_read_b128 v[96:99],v11 offset:11136
v_add_f32 v120,v120,v212
v_fmac_f32 v122,v212,v212
v_smfmac_f32_16x16x64_bf16 v[228:231],v[100:103],v[52:59],v13
ds_read_b128 v[52:55],v125 offset:28032
ds_read_b128 v[56:59],v125 offset:31520
ds_read_b128 v[100:103],v12 offset:10368
v_add_f32 v120,v120,v213
s_mov_b32 s86,0xbe801d5a
v_fmac_f32 v122,v213,v213
v_mfma_f32_16x16x32_bf16 v[140:143],v[68:71],v[20:23],v[116:119]
v_add_f32 v121,v121,v214
v_fmac_f32 v123,v214,v214
v_mfma_f32_16x16x32_bf16 v[132:135],v[68:71],v[16:19],v[116:119]
s_waitcnt lgkmcnt(8)
v_add_f32 v121,v121,v215
v_fmac_f32 v123,v215,v215
v_mfma_f32_16x16x32_bf16 v[240:243],v[88:91],v[48:51],0
s_waitcnt lgkmcnt(5)
ds_read_b128 v[48:51],v126 offset:34880
ds_read_b128 v[88:91],v11 offset:3072
v_add_f32 v120,v120,v224
v_fmac_f32 v122,v224,v224
v_add_f32 v120,v120,v225
v_smfmac_f32_16x16x64_bf16 v[240:243],v[92:95],v[40:47],v13
ds_read_b128 v[40:43],v126 offset:27904
ds_read_b128 v[44:47],v126 offset:31392
ds_read_b128 v[92:95],v12 offset:2304
v_fmac_f32 v122,v225,v225
v_add_f32 v121,v121,v226
v_fmac_f32 v123,v226,v226
v_mfma_f32_16x16x32_bf16 v[128:131],v[72:75],v[24:27],v[128:131]
s_waitcnt lgkmcnt(8)
v_add_f32 v121,v121,v227
v_fmac_f32 v123,v227,v227
v_mfma_f32_16x16x32_bf16 v[244:247],v[96:99],v[60:63],0
s_waitcnt lgkmcnt(5)
ds_read_b128 v[60:63],v127 offset:34880
ds_read_b128 v[96:99],v11 offset:4224
v_add_f32 v120,v120,v228
v_fmac_f32 v122,v228,v228
v_add_f32 v120,v120,v229
v_smfmac_f32_16x16x64_bf16 v[244:247],v[100:103],v[52:59],v13
ds_read_b128 v[52:55],v127 offset:27904
ds_read_b128 v[56:59],v127 offset:31392
ds_read_b128 v[100:103],v12 offset:3456
v_fmac_f32 v122,v229,v229
v_add_f32 v121,v121,v230
v_fmac_f32 v123,v230,v230
v_mfma_f32_16x16x32_bf16 v[136:139],v[72:75],v[28:31],v[136:139]
s_waitcnt lgkmcnt(8)
v_add_f32 v121,v121,v231
v_fmac_f32 v123,v231,v231
v_mfma_f32_16x16x32_bf16 v[216:219],v[88:91],v[48:51],0
s_waitcnt lgkmcnt(5)
ds_read_b128 v[48:51],v126 offset:34944
ds_read_b128 v[88:91],v11 offset:7680
v_add_f32 v120,v120,v240
v_fmac_f32 v122,v240,v240
v_add_f32 v120,v120,v241
v_smfmac_f32_16x16x64_bf16 v[216:219],v[92:95],v[40:47],v13
ds_read_b128 v[40:43],v126 offset:27968
ds_read_b128 v[44:47],v126 offset:31456
ds_read_b128 v[92:95],v12 offset:6912
v_fmac_f32 v122,v241,v241
v_add_f32 v121,v121,v242
v_fmac_f32 v123,v242,v242
v_mfma_f32_16x16x32_bf16 v[140:143],v[76:79],v[28:31],v[140:143]
s_waitcnt lgkmcnt(8)
v_add_f32 v121,v121,v243
v_fmac_f32 v123,v243,v243
v_mfma_f32_16x16x32_bf16 v[220:223],v[96:99],v[60:63],0
s_waitcnt lgkmcnt(5)
ds_read_b128 v[60:63],v127 offset:34944
ds_read_b128 v[96:99],v11 offset:8832
v_add_f32 v120,v120,v244
v_fmac_f32 v122,v244,v244
v_add_f32 v120,v120,v245
v_fmac_f32 v122,v245,v245
v_smfmac_f32_16x16x64_bf16 v[220:223],v[100:103],v[52:59],v13
ds_read_b128 v[52:55],v127 offset:27968
ds_read_b128 v[56:59],v127 offset:31456
ds_read_b128 v[100:103],v12 offset:8064
v_add_f32 v121,v121,v246
v_fmac_f32 v123,v246,v246
v_add_f32 v121,v121,v247
v_mfma_f32_16x16x32_bf16 v[132:135],v[76:79],v[24:27],v[132:135]
s_waitcnt lgkmcnt(8)
v_fmac_f32 v123,v247,v247
v_add_u32 v124,v124,v8
v_mfma_f32_16x16x32_bf16 v[232:235],v[88:91],v[48:51],0
s_waitcnt lgkmcnt(5)
ds_read_b128 v[48:51],v126 offset:35008
ds_read_b128 v[88:91],v11 offset:12288
v_add_f32 v120,v120,v216
v_fmac_f32 v122,v216,v216
v_add_f32 v120,v120,v217
v_smfmac_f32_16x16x64_bf16 v[232:235],v[92:95],v[40:47],v13
ds_read_b128 v[40:43],v126 offset:28032
ds_read_b128 v[44:47],v126 offset:31520
ds_read_b128 v[92:95],v12 offset:11520
v_fmac_f32 v122,v217,v217
v_add_f32 v121,v121,v218
v_fmac_f32 v123,v218,v218
v_mfma_f32_16x16x32_bf16 v[128:131],v[80:83],v[32:35],v[128:131]
s_waitcnt lgkmcnt(8)
v_add_f32 v121,v121,v219
v_fmac_f32 v123,v219,v219
v_mfma_f32_16x16x32_bf16 v[236:239],v[96:99],v[60:63],0
s_waitcnt lgkmcnt(5)
ds_read_b128 v[60:63],v127 offset:35008
ds_read_b128 v[96:99],v11 offset:13440
v_add_f32 v120,v120,v220
v_fmac_f32 v122,v220,v220
v_add_f32 v120,v120,v221
v_smfmac_f32_16x16x64_bf16 v[236:239],v[100:103],v[52:59],v13
ds_read_b128 v[52:55],v127 offset:28032
ds_read_b128 v[56:59],v127 offset:31520
ds_read_b128 v[100:103],v12 offset:12672
v_fmac_f32 v122,v221,v221
v_add_f32 v121,v121,v222
v_fmac_f32 v123,v222,v222
v_mfma_f32_16x16x32_bf16 v[136:139],v[80:83],v[36:39],v[136:139]
s_waitcnt lgkmcnt(8)
ds_read_b128 v[144:147],v124 offset:31392
ds_read_b128 v[148:151],v124 offset:31584
v_add_f32 v121,v121,v223
v_fmac_f32 v123,v223,v223
v_mfma_f32_16x16x32_bf16 v[248:251],v[88:91],v[48:51],0
s_waitcnt lgkmcnt(7)
ds_read_b128 v[88:91],v124 offset:31456
v_add_f32 v120,v120,v232
v_fmac_f32 v122,v232,v232
v_add_f32 v120,v120,v233
v_smfmac_f32_16x16x64_bf16 v[248:251],v[92:95],v[40:47],v13
ds_read_b128 v[92:95],v124 offset:31648
ds_read_b128 v[104:107],v14 offset:0
ds_read_b128 v[108:111],v7 offset:3584
v_fmac_f32 v122,v233,v233
v_add_f32 v121,v121,v234
v_fmac_f32 v123,v234,v234
v_mfma_f32_16x16x32_bf16 v[140:143],v[84:87],v[36:39],v[140:143]
s_waitcnt lgkmcnt(9)
v_add_f32 v121,v121,v235
v_fmac_f32 v123,v235,v235
v_mfma_f32_16x16x32_bf16 v[252:255],v[96:99],v[60:63],0
s_waitcnt lgkmcnt(6)
ds_read_b128 v[96:99],v124 offset:31520
v_add_f32 v120,v120,v236
v_fmac_f32 v122,v236,v236
v_add_f32 v120,v120,v237
v_smfmac_f32_16x16x64_bf16 v[252:255],v[100:103],v[52:59],v13
ds_read_b128 v[100:103],v124 offset:31712
ds_read_b128 v[152:155],v14 offset:256
ds_read_b128 v[156:159],v7 offset:3616
v_fmac_f32 v122,v237,v237
v_add_f32 v121,v121,v238
v_fmac_f32 v123,v238,v238
v_mfma_f32_16x16x32_bf16 v[132:135],v[84:87],v[32:35],v[132:135]
v_add_f32 v121,v121,v239
v_fmac_f32 v123,v239,v239
v_add_f32 v120,v120,v248
v_fmac_f32 v122,v248,v248
v_add_f32 v120,v120,v249
v_fmac_f32 v122,v249,v249
v_add_f32 v121,v121,v250
v_fmac_f32 v123,v250,v250
v_add_f32 v121,v121,v251
v_fmac_f32 v123,v251,v251
s_waitcnt lgkmcnt(2)
s_barrier
v_add_f32 v120,v120,v252
v_fmac_f32 v122,v252,v252
v_add_f32 v120,v120,v253
v_fmac_f32 v122,v253,v253
v_add_f32 v121,v121,v254
v_fmac_f32 v123,v254,v254
v_add_f32 v121,v121,v255
v_fmac_f32 v123,v255,v255
s_nop 0
v_permlane32_swap_b32 v120,v121
v_permlane32_swap_b32 v122,v123
v_add_f32 v120,v120,v121
v_add_f32 v122,v122,v123
s_nop 1
v_permlane16_swap_b32 v120,v122
v_add_f32 v61,v120,v122
v_add_f32 v62,v120,v122
s_nop 1
v_permlane16_swap_b32 v61,v62
v_mul_f32 v61,0x3c2aaaab,v61
v_mul_f32 v62,0x3c2aaaab,v62
v_mul_f32 v60,0x5e000000,v62
v_fma_f32 v62,-v61,v61,v62
v_pk_maximum3_f16 v1,v1,v60,v60 op_sel:[0,1,1] op_sel_hi:[1,1,1]
v_max_f32 v62,0,v62
v_add_f32 v62,0x3727c5ac,v62
v_rsq_f32 v62,v62
s_nop 0
v_mul_f32 v120,0x8befe000,v62
v_mul_f32 v121,0x8befe000,v62
v_mul_f32 v122,-v61,v120
v_mul_f32 v123,-v61,v120
v_permlane32_swap_b32 v120,v121
s_nop 0
v_permlane32_swap_b32 v122,v123
v_pk_fma_f32 v[208:209],v[208:209],v[120:121],v[122:123] op_sel:[0,0,0] op_sel_hi:[1,0,0]
v_pk_fma_f32 v[210:211],v[210:211],v[120:121],v[122:123] op_sel:[0,1,1] op_sel_hi:[1,1,1]
v_pk_fma_f32 v[212:213],v[212:213],v[120:121],v[122:123] op_sel:[0,0,0] op_sel_hi:[1,0,0]
v_pk_fma_f32 v[214:215],v[214:215],v[120:121],v[122:123] op_sel:[0,1,1] op_sel_hi:[1,1,1]
v_pk_fma_f32 v[216:217],v[216:217],v[120:121],v[122:123] op_sel:[0,0,0] op_sel_hi:[1,0,0]
v_pk_fma_f32 v[218:219],v[218:219],v[120:121],v[122:123] op_sel:[0,1,1] op_sel_hi:[1,1,1]
v_pk_fma_f32 v[220:221],v[220:221],v[120:121],v[122:123] op_sel:[0,0,0] op_sel_hi:[1,0,0]
v_pk_fma_f32 v[222:223],v[222:223],v[120:121],v[122:123] op_sel:[0,1,1] op_sel_hi:[1,1,1]
v_pk_fma_f32 v[224:225],v[224:225],v[120:121],v[122:123] op_sel:[0,0,0] op_sel_hi:[1,0,0]
v_pk_fma_f32 v[226:227],v[226:227],v[120:121],v[122:123] op_sel:[0,1,1] op_sel_hi:[1,1,1]
v_pk_fma_f32 v[228:229],v[228:229],v[120:121],v[122:123] op_sel:[0,0,0] op_sel_hi:[1,0,0]
v_pk_fma_f32 v[230:231],v[230:231],v[120:121],v[122:123] op_sel:[0,1,1] op_sel_hi:[1,1,1]
v_pk_fma_f32 v[232:233],v[232:233],v[120:121],v[122:123] op_sel:[0,0,0] op_sel_hi:[1,0,0]
v_pk_fma_f32 v[234:235],v[234:235],v[120:121],v[122:123] op_sel:[0,1,1] op_sel_hi:[1,1,1]
v_pk_fma_f32 v[236:237],v[236:237],v[120:121],v[122:123] op_sel:[0,0,0] op_sel_hi:[1,0,0]
v_pk_fma_f32 v[238:239],v[238:239],v[120:121],v[122:123] op_sel:[0,1,1] op_sel_hi:[1,1,1]
v_pk_fma_f32 v[240:241],v[240:241],v[120:121],v[122:123] op_sel:[0,0,0] op_sel_hi:[1,0,0]
v_pk_fma_f32 v[242:243],v[242:243],v[120:121],v[122:123] op_sel:[0,1,1] op_sel_hi:[1,1,1]
v_pk_fma_f32 v[244:245],v[244:245],v[120:121],v[122:123] op_sel:[0,0,0] op_sel_hi:[1,0,0]
v_pk_fma_f32 v[246:247],v[246:247],v[120:121],v[122:123] op_sel:[0,1,1] op_sel_hi:[1,1,1]
v_pk_fma_f32 v[248:249],v[248:249],v[120:121],v[122:123] op_sel:[0,0,0] op_sel_hi:[1,0,0]
v_pk_fma_f32 v[250:251],v[250:251],v[120:121],v[122:123] op_sel:[0,1,1] op_sel_hi:[1,1,1]
v_pk_fma_f32 v[252:253],v[252:253],v[120:121],v[122:123] op_sel:[0,0,0] op_sel_hi:[1,0,0]
v_pk_fma_f32 v[254:255],v[254:255],v[120:121],v[122:123] op_sel:[0,1,1] op_sel_hi:[1,1,1]
v_cvt_pk_bf16_f32 v40,v208,v209
v_cvt_pk_bf16_f32 v44,v210,v211
v_cvt_pk_bf16_f32 v41,v212,v213
v_cvt_pk_bf16_f32 v45,v214,v215
v_cvt_pk_bf16_f32 v42,v216,v217
v_cvt_pk_bf16_f32 v46,v218,v219
v_cvt_pk_bf16_f32 v43,v220,v221
v_cvt_pk_bf16_f32 v47,v222,v223
v_cvt_pk_bf16_f32 v48,v224,v225
v_mfma_f32_16x16x32_bf16 v[208:211],v[104:107],v[144:147],v[108:111]
v_cvt_pk_bf16_f32 v52,v226,v227
v_cvt_pk_bf16_f32 v49,v228,v229
v_mfma_f32_16x16x32_bf16 v[212:215],v[104:107],v[148:151],v[108:111]
s_waitcnt lgkmcnt(0)
ds_read_b128 v[104:107],v14 offset:512
ds_read_b128 v[108:111],v7 offset:3712
v_cvt_pk_bf16_f32 v53,v230,v231
v_cvt_pk_bf16_f32 v50,v232,v233
v_mfma_f32_16x16x32_bf16 v[216:219],v[152:155],v[144:147],v[156:159]
v_cvt_pk_bf16_f32 v54,v234,v235
v_cvt_pk_bf16_f32 v51,v236,v237
v_mfma_f32_16x16x32_bf16 v[220:223],v[152:155],v[148:151],v[156:159]
ds_read_b128 v[152:155],v14 offset:768
ds_read_b128 v[156:159],v7 offset:3744
v_cvt_pk_bf16_f32 v55,v238,v239
v_cvt_pk_bf16_f32 v56,v240,v241
s_waitcnt lgkmcnt(2)
v_mfma_f32_16x16x32_bf16 v[224:227],v[104:107],v[88:91],v[108:111]
v_cvt_pk_bf16_f32 v60,v242,v243
v_cvt_pk_bf16_f32 v57,v244,v245
v_mfma_f32_16x16x32_bf16 v[228:231],v[104:107],v[92:95],v[108:111]
s_waitcnt lgkmcnt(0)
ds_read_b128 v[104:107],v14 offset:1024
ds_read_b128 v[108:111],v7 offset:3840
v_cvt_pk_bf16_f32 v61,v246,v247
v_cvt_pk_bf16_f32 v58,v248,v249
v_mfma_f32_16x16x32_bf16 v[232:235],v[152:155],v[88:91],v[156:159]
v_cvt_pk_bf16_f32 v62,v250,v251
v_cvt_pk_bf16_f32 v59,v252,v253
v_mfma_f32_16x16x32_bf16 v[236:239],v[152:155],v[92:95],v[156:159]
ds_read_b128 v[152:155],v14 offset:1280
ds_read_b128 v[156:159],v7 offset:3872
v_cvt_pk_bf16_f32 v63,v254,v255
s_waitcnt lgkmcnt(2)
v_mfma_f32_16x16x32_bf16 v[240:243],v[104:107],v[96:99],v[108:111]
s_barrier
s_setprio 2
ds_read_b64_tr_b16 v[88:89],v6 offset:0
v_lshlrev_b32_sdwa v128,1,v128 dst_sel:DWORD dst_unused:UNUSED_PAD src0_sel:DWORD src1_sel:WORD_1
v_lshlrev_b32_sdwa v130,1,v130 dst_sel:DWORD dst_unused:UNUSED_PAD src0_sel:DWORD src1_sel:WORD_1
v_mfma_f32_16x16x32_bf16 v[244:247],v[104:107],v[100:103],v[108:111]
s_waitcnt lgkmcnt(1)
ds_read_u16 v128,v128
ds_read_b64_tr_b16 v[90:91],v6 offset:1536
v_lshlrev_b32_sdwa v129,1,v129 dst_sel:DWORD dst_unused:UNUSED_PAD src0_sel:DWORD src1_sel:WORD_1
v_lshlrev_b32_sdwa v131,1,v131 dst_sel:DWORD dst_unused:UNUSED_PAD src0_sel:DWORD src1_sel:WORD_1
v_mfma_f32_16x16x32_bf16 v[248:251],v[152:155],v[96:99],v[156:159]
ds_read_u16_d16_hi v130,v130
ds_read_b64_tr_b16 v[92:93],v6 offset:8
v_fma_mixlo_f16 v120,v141,s99,0
v_lshlrev_b32_sdwa v132,1,v132 dst_sel:DWORD dst_unused:UNUSED_PAD src0_sel:DWORD src1_sel:WORD_1
v_mfma_f32_16x16x32_bf16 v[252:255],v[152:155],v[100:103],v[156:159]
ds_read_u16 v129,v129
ds_read_b64_tr_b16 v[94:95],v6 offset:1544
v_fma_mixhi_f16 v120,v143,s99,0
v_lshlrev_b32_sdwa v134,1,v134 dst_sel:DWORD dst_unused:UNUSED_PAD src0_sel:DWORD src1_sel:WORD_1
v_pk_fma_f16 v121,v120,v120,s54
v_mfma_f32_16x16x32_bf16 v[144:147],v[64:67],v[40:43],v[112:115]
ds_read_u16_d16_hi v131,v131
ds_read_b64_tr_b16 v[104:105],v6 offset:128
v_lshlrev_b32_sdwa v133,1,v133 dst_sel:DWORD dst_unused:UNUSED_PAD src0_sel:DWORD src1_sel:WORD_1
v_pk_mul_f16 v121,v120,v121
v_mfma_f32_16x16x32_bf16 v[152:155],v[64:67],v[44:47],v[112:115]
ds_read_u16 v132,v132
ds_read_b64_tr_b16 v[106:107],v6 offset:1664
v_lshlrev_b32_sdwa v135,1,v135 dst_sel:DWORD dst_unused:UNUSED_PAD src0_sel:DWORD src1_sel:WORD_1
v_exp_f16 v120,v121
v_mfma_f32_16x16x32_bf16 v[156:159],v[68:71],v[44:47],v[116:119]
ds_read_u16_d16_hi v134,v134
ds_read_b64_tr_b16 v[108:109],v6 offset:136
v_lshlrev_b32_sdwa v136,1,v136 dst_sel:DWORD dst_unused:UNUSED_PAD src0_sel:DWORD src1_sel:WORD_1
v_mfma_f32_16x16x32_bf16 v[148:151],v[68:71],v[40:43],v[116:119]
ds_read_u16 v133,v133
ds_read_b64_tr_b16 v[110:111],v6 offset:1672
v_exp_f16_sdwa v120,v121 dst_sel:WORD_1 dst_unused:UNUSED_PRESERVE src0_sel:WORD_1
v_mfma_f32_16x16x32_bf16 v[144:147],v[72:75],v[48:51],v[144:147]
ds_read_u16_d16_hi v135,v135
ds_read_b64_tr_b16 v[96:97],v6 offset:64
v_lshlrev_b32_sdwa v138,1,v138 dst_sel:DWORD dst_unused:UNUSED_PAD src0_sel:DWORD src1_sel:WORD_1
v_mfma_f32_16x16x32_bf16 v[152:155],v[72:75],v[52:55],v[152:155]
ds_read_u16 v136,v136
ds_read_b64_tr_b16 v[98:99],v6 offset:1600
v_pk_fma_f16 v120,v120,s55,s55
v_lshlrev_b32_sdwa v137,1,v137 dst_sel:DWORD dst_unused:UNUSED_PAD src0_sel:DWORD src1_sel:WORD_1
v_mfma_f32_16x16x32_bf16 v[156:159],v[76:79],v[52:55],v[156:159]
ds_read_u16_d16_hi v138,v138
ds_read_b64_tr_b16 v[100:101],v6 offset:72
v_rcp_f16 v121,v120
v_mfma_f32_16x16x32_bf16 v[148:151],v[76:79],v[48:51],v[148:151]
ds_read_u16 v137,v137
ds_read_b64_tr_b16 v[102:103],v6 offset:1608
v_lshlrev_b32_sdwa v139,1,v139 dst_sel:DWORD dst_unused:UNUSED_PAD src0_sel:DWORD src1_sel:WORD_1
v_rcp_f16_sdwa v121,v120 dst_sel:WORD_1 dst_unused:UNUSED_PRESERVE src0_sel:WORD_1
v_mfma_f32_16x16x32_bf16 v[144:147],v[80:83],v[56:59],v[144:147]
ds_read_u16_d16_hi v139,v139
ds_read_b64_tr_b16 v[64:65],v4 offset:0
v_lshlrev_b32_sdwa v140,1,v140 dst_sel:DWORD dst_unused:UNUSED_PAD src0_sel:DWORD src1_sel:WORD_1
v_mfma_f32_16x16x32_bf16 v[152:155],v[80:83],v[60:63],v[152:155]
ds_read_u16 v140,v140
ds_read_b64_tr_b16 v[66:67],v4 offset:2048
v_fma_mix_f32 v141,v141,v121,0 op_sel_hi:[0,1,0]
v_lshlrev_b32_sdwa v142,1,v142 dst_sel:DWORD dst_unused:UNUSED_PAD src0_sel:DWORD src1_sel:WORD_1
v_mfma_f32_16x16x32_bf16 v[156:159],v[84:87],v[60:63],v[156:159]
ds_read_u16_d16_hi v142,v142
ds_read_b128 v[112:115],v7 offset:256
v_fma_mix_f32 v143,v143,v121,0 op_sel:[0,1,0] op_sel_hi:[0,1,0]
s_waitcnt lgkmcnt(15)
v_lshl_add_u64 v[128:129],v[128:129],0,v[130:131]
v_mfma_f32_16x16x32_bf16 v[148:151],v[84:87],v[56:59],v[148:151]
ds_read_b64_tr_b16 v[68:69],v5 offset:0
s_waitcnt lgkmcnt(14)
v_lshl_add_u64 v[130:131],v[132:133],0,v[134:135]
v_lshlrev_b32_sdwa v144,1,v144 dst_sel:DWORD dst_unused:UNUSED_PAD src0_sel:DWORD src1_sel:WORD_1
v_lshlrev_b32_sdwa v146,1,v146 dst_sel:DWORD dst_unused:UNUSED_PAD src0_sel:DWORD src1_sel:WORD_1
v_mfma_f32_16x16x32_bf16 v[160:163],v[88:91],v[128:131],v[160:163]
ds_read_u16 v144,v144
v_lshlrev_b32_sdwa v145,1,v145 dst_sel:DWORD dst_unused:UNUSED_PAD src0_sel:DWORD src1_sel:WORD_1
v_lshlrev_b32_sdwa v147,1,v147 dst_sel:DWORD dst_unused:UNUSED_PAD src0_sel:DWORD src1_sel:WORD_1
v_mfma_f32_16x16x32_bf16 v[168:171],v[92:95],v[128:131],v[168:171]
s_waitcnt lgkmcnt(12)
ds_read_b64_tr_b16 v[70:71],v5 offset:2048
ds_read_b128 v[116:119],v7 offset:288
v_fma_mixlo_f16 v120,v157,s99,0
v_mfma_f32_16x16x32_bf16 v[176:179],v[96:99],v[128:131],v[176:179]
s_waitcnt lgkmcnt(9)
ds_read_u16_d16_hi v146,v146
v_lshlrev_b32_sdwa v148,1,v148 dst_sel:DWORD dst_unused:UNUSED_PAD src0_sel:DWORD src1_sel:WORD_1
v_lshl_add_u64 v[136:137],v[136:137],0,v[138:139]
v_fma_mixhi_f16 v120,v159,s99,0
v_lshlrev_b32_sdwa v150,1,v150 dst_sel:DWORD dst_unused:UNUSED_PAD src0_sel:DWORD src1_sel:WORD_1
v_mfma_f32_16x16x32_bf16 v[184:187],v[100:103],v[128:131],v[184:187]
ds_read_u16 v145,v145
v_mfma_f32_16x16x32_bf16 v[192:195],v[104:107],v[128:131],v[192:195]
s_waitcnt lgkmcnt(7)
ds_read_b64_tr_b16 v[72:73],v4 offset:4096
ds_read_b64_tr_b16 v[74:75],v4 offset:6144
v_or_b32 v138,v142,v140
v_cvt_pk_bf16_f32 v139,v141,v143
v_mfma_f32_16x16x32_bf16 v[200:203],v[108:111],v[128:131],v[200:203]
ds_read_b64_tr_b16 v[76:77],v5 offset:4096
ds_read_b64_tr_b16 v[78:79],v5 offset:6144
v_pk_fma_f16 v121,v120,v120,s54
v_lshlrev_b32_sdwa v149,1,v149 dst_sel:DWORD dst_unused:UNUSED_PAD src0_sel:DWORD src1_sel:WORD_1
v_mfma_f32_16x16x32_bf16 v[164:167],v[88:91],v[136:139],v[164:167]
ds_read_u16_d16_hi v147,v147
v_pk_mul_f16 v121,v120,v121
v_lshlrev_b32_sdwa v151,1,v151 dst_sel:DWORD dst_unused:UNUSED_PAD src0_sel:DWORD src1_sel:WORD_1
v_mfma_f32_16x16x32_bf16 v[172:175],v[92:95],v[136:139],v[172:175]
ds_read_u16 v148,v148
v_lshlrev_b32_sdwa v152,1,v152 dst_sel:DWORD dst_unused:UNUSED_PAD src0_sel:DWORD src1_sel:WORD_1
v_exp_f16 v120,v121
v_mfma_f32_16x16x32_bf16 v[180:183],v[96:99],v[136:139],v[180:183]
ds_read_b64_tr_b16 v[80:81],v4 offset:8192
ds_read_b64_tr_b16 v[82:83],v4 offset:10240
v_mfma_f32_16x16x32_bf16 v[188:191],v[100:103],v[136:139],v[188:191]
v_exp_f16_sdwa v120,v121 dst_sel:WORD_1 dst_unused:UNUSED_PRESERVE src0_sel:WORD_1
v_mfma_f32_16x16x32_bf16 v[196:199],v[104:107],v[136:139],v[196:199]
ds_read_u16_d16_hi v150,v150
ds_read_u16 v149,v149
v_lshlrev_b32_sdwa v154,1,v154 dst_sel:DWORD dst_unused:UNUSED_PAD src0_sel:DWORD src1_sel:WORD_1
v_mfma_f32_16x16x32_bf16 v[204:207],v[108:111],v[136:139],v[204:207]
s_waitcnt lgkmcnt(15)
ds_read_b64_tr_b16 v[84:85],v5 offset:8192
v_pk_fma_f16 v120,v120,s55,s55
v_lshlrev_b32_sdwa v153,1,v153 dst_sel:DWORD dst_unused:UNUSED_PAD src0_sel:DWORD src1_sel:WORD_1
v_mfma_f32_16x16x32_bf16 v[128:131],v[64:67],v[16:19],v[112:115]
ds_read_u16_d16_hi v151,v151
v_rcp_f16 v121,v120
v_mfma_f32_16x16x32_bf16 v[136:139],v[64:67],v[20:23],v[112:115]
s_waitcnt lgkmcnt(14)
ds_read_b64_tr_b16 v[86:87],v5 offset:10240
v_lshlrev_b32_sdwa v155,1,v155 dst_sel:DWORD dst_unused:UNUSED_PAD src0_sel:DWORD src1_sel:WORD_1
v_mfma_f32_16x16x32_bf16 v[140:143],v[68:71],v[20:23],v[116:119]
ds_read_u16 v152,v152
v_rcp_f16_sdwa v121,v120 dst_sel:WORD_1 dst_unused:UNUSED_PRESERVE src0_sel:WORD_1
v_mfma_f32_16x16x32_bf16 v[132:135],v[68:71],v[16:19],v[116:119]
s_waitcnt lgkmcnt(12)
ds_read_u16_d16_hi v154,v154
v_lshlrev_b32_sdwa v156,1,v156 dst_sel:DWORD dst_unused:UNUSED_PAD src0_sel:DWORD src1_sel:WORD_1
v_mfma_f32_16x16x32_bf16 v[128:131],v[72:75],v[24:27],v[128:131]
v_fma_mix_f32 v157,v157,v121,0 op_sel_hi:[0,1,0]
v_mfma_f32_16x16x32_bf16 v[136:139],v[72:75],v[28:31],v[136:139]
s_waitcnt lgkmcnt(11)
ds_read_u16 v153,v153
v_fma_mix_f32 v159,v159,v121,0 op_sel:[0,1,0] op_sel_hi:[0,1,0]
v_mfma_f32_16x16x32_bf16 v[140:143],v[76:79],v[28:31],v[140:143]
v_mfma_f32_16x16x32_bf16 v[132:135],v[76:79],v[24:27],v[132:135]
s_waitcnt lgkmcnt(8)
ds_read_u16_d16_hi v155,v155
v_lshlrev_b32_sdwa v158,1,v158 dst_sel:DWORD dst_unused:UNUSED_PAD src0_sel:DWORD src1_sel:WORD_1
v_mfma_f32_16x16x32_bf16 v[128:131],v[80:83],v[32:35],v[128:131]
ds_read_u16 v156,v156
v_mfma_f32_16x16x32_bf16 v[136:139],v[80:83],v[36:39],v[136:139]
s_waitcnt lgkmcnt(5)
v_lshl_add_u64 v[144:145],v[144:145],0,v[146:147]
v_mfma_f32_16x16x32_bf16 v[140:143],v[84:87],v[36:39],v[140:143]
ds_read_u16_d16_hi v158,v158
v_lshl_add_u64 v[146:147],v[148:149],0,v[150:151]
v_mfma_f32_16x16x32_bf16 v[132:135],v[84:87],v[32:35],v[132:135]
v_lshlrev_b32_sdwa v128,1,v128 dst_sel:DWORD dst_unused:UNUSED_PAD src0_sel:DWORD src1_sel:WORD_1
v_lshlrev_b32_sdwa v130,1,v130 dst_sel:DWORD dst_unused:UNUSED_PAD src0_sel:DWORD src1_sel:WORD_1
v_mfma_f32_16x16x32_bf16 v[208:211],v[88:91],v[144:147],v[208:211]
ds_read_u16 v128,v128
v_lshlrev_b32_sdwa v129,1,v129 dst_sel:DWORD dst_unused:UNUSED_PAD src0_sel:DWORD src1_sel:WORD_1
v_lshlrev_b32_sdwa v131,1,v131 dst_sel:DWORD dst_unused:UNUSED_PAD src0_sel:DWORD src1_sel:WORD_1
v_mfma_f32_16x16x32_bf16 v[216:219],v[92:95],v[144:147],v[216:219]
v_fma_mixlo_f16 v120,v141,s99,0
v_lshlrev_b32_sdwa v132,1,v132 dst_sel:DWORD dst_unused:UNUSED_PAD src0_sel:DWORD src1_sel:WORD_1
v_mfma_f32_16x16x32_bf16 v[224:227],v[96:99],v[144:147],v[224:227]
ds_read_u16_d16_hi v130,v130
v_fma_mixhi_f16 v120,v143,s99,0
v_lshlrev_b32_sdwa v134,1,v134 dst_sel:DWORD dst_unused:UNUSED_PAD src0_sel:DWORD src1_sel:WORD_1
v_mfma_f32_16x16x32_bf16 v[232:235],v[100:103],v[144:147],v[232:235]
s_waitcnt lgkmcnt(4)
ds_read_u16 v129,v129
v_lshl_add_u64 v[152:153],v[152:153],0,v[154:155]
v_mfma_f32_16x16x32_bf16 v[240:243],v[104:107],v[144:147],v[240:243]
s_waitcnt lgkmcnt(3)
ds_read_u16_d16_hi v131,v131
v_or_b32 v154,v158,v156
v_cvt_pk_bf16_f32 v155,v157,v159
v_mfma_f32_16x16x32_bf16 v[248:251],v[108:111],v[144:147],v[248:251]
v_pk_fma_f16 v121,v120,v120,s54
v_lshlrev_b32_sdwa v133,1,v133 dst_sel:DWORD dst_unused:UNUSED_PAD src0_sel:DWORD src1_sel:WORD_1
v_mfma_f32_16x16x32_bf16 v[212:215],v[88:91],v[152:155],v[212:215]
ds_read_u16 v132,v132
ds_read_b64_tr_b16 v[88:89],v6 offset:6160
v_pk_mul_f16 v121,v120,v121
v_lshlrev_b32_sdwa v135,1,v135 dst_sel:DWORD dst_unused:UNUSED_PAD src0_sel:DWORD src1_sel:WORD_1
v_mfma_f32_16x16x32_bf16 v[220:223],v[92:95],v[152:155],v[220:223]
ds_read_u16_d16_hi v134,v134
v_exp_f16 v120,v121
v_mfma_f32_16x16x32_bf16 v[228:231],v[96:99],v[152:155],v[228:231]
ds_read_b64_tr_b16 v[90:91],v6 offset:7696
ds_read_b64_tr_b16 v[92:93],v6 offset:6168
v_lshlrev_b32_sdwa v136,1,v136 dst_sel:DWORD dst_unused:UNUSED_PAD src0_sel:DWORD src1_sel:WORD_1
v_mfma_f32_16x16x32_bf16 v[236:239],v[100:103],v[152:155],v[236:239]
ds_read_u16 v133,v133
v_exp_f16_sdwa v120,v121 dst_sel:WORD_1 dst_unused:UNUSED_PRESERVE src0_sel:WORD_1
v_mfma_f32_16x16x32_bf16 v[244:247],v[104:107],v[152:155],v[244:247]
v_lshlrev_b32_sdwa v138,1,v138 dst_sel:DWORD dst_unused:UNUSED_PAD src0_sel:DWORD src1_sel:WORD_1
v_mfma_f32_16x16x32_bf16 v[252:255],v[108:111],v[152:155],v[252:255]
ds_read_b64_tr_b16 v[94:95],v6 offset:7704
ds_read_b64_tr_b16 v[96:97],v6 offset:6224
v_pk_fma_f16 v120,v120,s55,s55
v_lshlrev_b32_sdwa v137,1,v137 dst_sel:DWORD dst_unused:UNUSED_PAD src0_sel:DWORD src1_sel:WORD_1
v_mfma_f32_16x16x32_bf16 v[144:147],v[64:67],v[40:43],v[112:115]
ds_read_u16_d16_hi v135,v135
v_rcp_f16 v121,v120
v_mfma_f32_16x16x32_bf16 v[152:155],v[64:67],v[44:47],v[112:115]
ds_read_b64_tr_b16 v[98:99],v6 offset:7760
ds_read_b64_tr_b16 v[100:101],v6 offset:6232
v_lshlrev_b32_sdwa v139,1,v139 dst_sel:DWORD dst_unused:UNUSED_PAD src0_sel:DWORD src1_sel:WORD_1
v_mfma_f32_16x16x32_bf16 v[156:159],v[68:71],v[44:47],v[116:119]
ds_read_u16 v136,v136
v_rcp_f16_sdwa v121,v120 dst_sel:WORD_1 dst_unused:UNUSED_PRESERVE src0_sel:WORD_1
v_mfma_f32_16x16x32_bf16 v[148:151],v[68:71],v[40:43],v[116:119]
ds_read_u16_d16_hi v138,v138
v_lshlrev_b32_sdwa v140,1,v140 dst_sel:DWORD dst_unused:UNUSED_PAD src0_sel:DWORD src1_sel:WORD_1
v_mfma_f32_16x16x32_bf16 v[144:147],v[72:75],v[48:51],v[144:147]
ds_read_b64_tr_b16 v[102:103],v6 offset:7768
ds_read_b64_tr_b16 v[104:105],v6 offset:6288
v_fma_mix_f32 v141,v141,v121,0 op_sel_hi:[0,1,0]
v_lshlrev_b32_sdwa v142,1,v142 dst_sel:DWORD dst_unused:UNUSED_PAD src0_sel:DWORD src1_sel:WORD_1
v_mfma_f32_16x16x32_bf16 v[152:155],v[72:75],v[52:55],v[152:155]
ds_read_u16 v137,v137
v_fma_mix_f32 v143,v143,v121,0 op_sel:[0,1,0] op_sel_hi:[0,1,0]
v_mfma_f32_16x16x32_bf16 v[156:159],v[76:79],v[52:55],v[156:159]
ds_read_b64_tr_b16 v[106:107],v6 offset:7824
ds_read_b64_tr_b16 v[108:109],v6 offset:6296
v_mfma_f32_16x16x32_bf16 v[148:151],v[76:79],v[48:51],v[148:151]
ds_read_u16_d16_hi v139,v139
v_mfma_f32_16x16x32_bf16 v[144:147],v[80:83],v[56:59],v[144:147]
ds_read_b64_tr_b16 v[110:111],v6 offset:7832
ds_read_b64_tr_b16 v[64:65],v4 offset:512
v_mfma_f32_16x16x32_bf16 v[152:155],v[80:83],v[60:63],v[152:155]
s_waitcnt lgkmcnt(12)
ds_read_u16 v140,v140
v_lshl_add_u64 v[128:129],v[128:129],0,v[130:131]
v_lshl_add_u64 v[130:131],v[132:133],0,v[134:135]
v_mfma_f32_16x16x32_bf16 v[156:159],v[84:87],v[60:63],v[156:159]
ds_read_u16_d16_hi v142,v142
v_mfma_f32_16x16x32_bf16 v[148:151],v[84:87],v[56:59],v[148:151]
ds_read_b64_tr_b16 v[66:67],v4 offset:2560
ds_read_b128 v[112:115],v7 offset:384
v_lshlrev_b32_sdwa v144,1,v144 dst_sel:DWORD dst_unused:UNUSED_PAD src0_sel:DWORD src1_sel:WORD_1
v_lshlrev_b32_sdwa v146,1,v146 dst_sel:DWORD dst_unused:UNUSED_PAD src0_sel:DWORD src1_sel:WORD_1
v_mfma_f32_16x16x32_bf16 v[160:163],v[88:91],v[128:131],v[160:163]
ds_read_u16 v144,v144
v_lshlrev_b32_sdwa v145,1,v145 dst_sel:DWORD dst_unused:UNUSED_PAD src0_sel:DWORD src1_sel:WORD_1
v_lshlrev_b32_sdwa v147,1,v147 dst_sel:DWORD dst_unused:UNUSED_PAD src0_sel:DWORD src1_sel:WORD_1
v_mfma_f32_16x16x32_bf16 v[168:171],v[92:95],v[128:131],v[168:171]
s_waitcnt lgkmcnt(15)
ds_read_b64_tr_b16 v[68:69],v5 offset:512
ds_read_b64_tr_b16 v[70:71],v5 offset:2560
v_fma_mixlo_f16 v120,v157,s99,0
v_mfma_f32_16x16x32_bf16 v[176:179],v[96:99],v[128:131],v[176:179]
s_waitcnt lgkmcnt(9)
ds_read_u16_d16_hi v146,v146
v_lshlrev_b32_sdwa v148,1,v148 dst_sel:DWORD dst_unused:UNUSED_PAD src0_sel:DWORD src1_sel:WORD_1
v_lshl_add_u64 v[136:137],v[136:137],0,v[138:139]
v_fma_mixhi_f16 v120,v159,s99,0
v_lshlrev_b32_sdwa v150,1,v150 dst_sel:DWORD dst_unused:UNUSED_PAD src0_sel:DWORD src1_sel:WORD_1
v_mfma_f32_16x16x32_bf16 v[184:187],v[100:103],v[128:131],v[184:187]
ds_read_u16 v145,v145
v_mfma_f32_16x16x32_bf16 v[192:195],v[104:107],v[128:131],v[192:195]
s_waitcnt lgkmcnt(7)
ds_read_b128 v[116:119],v7 offset:416
ds_read_b64_tr_b16 v[72:73],v4 offset:4608
v_or_b32 v138,v142,v140
v_cvt_pk_bf16_f32 v139,v141,v143
v_mfma_f32_16x16x32_bf16 v[200:203],v[108:111],v[128:131],v[200:203]
ds_read_b64_tr_b16 v[74:75],v4 offset:6656
ds_read_b64_tr_b16 v[76:77],v5 offset:4608
v_pk_fma_f16 v121,v120,v120,s54
v_lshlrev_b32_sdwa v149,1,v149 dst_sel:DWORD dst_unused:UNUSED_PAD src0_sel:DWORD src1_sel:WORD_1
v_mfma_f32_16x16x32_bf16 v[164:167],v[88:91],v[136:139],v[164:167]
ds_read_u16_d16_hi v147,v147
v_pk_mul_f16 v121,v120,v121
v_lshlrev_b32_sdwa v151,1,v151 dst_sel:DWORD dst_unused:UNUSED_PAD src0_sel:DWORD src1_sel:WORD_1
v_mfma_f32_16x16x32_bf16 v[172:175],v[92:95],v[136:139],v[172:175]
ds_read_u16 v148,v148
v_lshlrev_b32_sdwa v152,1,v152 dst_sel:DWORD dst_unused:UNUSED_PAD src0_sel:DWORD src1_sel:WORD_1
v_exp_f16 v120,v121
v_mfma_f32_16x16x32_bf16 v[180:183],v[96:99],v[136:139],v[180:183]
ds_read_b64_tr_b16 v[78:79],v5 offset:6656
ds_read_b64_tr_b16 v[80:81],v4 offset:8704
v_mfma_f32_16x16x32_bf16 v[188:191],v[100:103],v[136:139],v[188:191]
v_exp_f16_sdwa v120,v121 dst_sel:WORD_1 dst_unused:UNUSED_PRESERVE src0_sel:WORD_1
v_mfma_f32_16x16x32_bf16 v[196:199],v[104:107],v[136:139],v[196:199]
ds_read_u16_d16_hi v150,v150
ds_read_u16 v149,v149
v_lshlrev_b32_sdwa v154,1,v154 dst_sel:DWORD dst_unused:UNUSED_PAD src0_sel:DWORD src1_sel:WORD_1
v_mfma_f32_16x16x32_bf16 v[204:207],v[108:111],v[136:139],v[204:207]
s_waitcnt lgkmcnt(15)
ds_read_b64_tr_b16 v[82:83],v4 offset:10752
v_pk_fma_f16 v120,v120,s55,s55
v_lshlrev_b32_sdwa v153,1,v153 dst_sel:DWORD dst_unused:UNUSED_PAD src0_sel:DWORD src1_sel:WORD_1
v_mfma_f32_16x16x32_bf16 v[128:131],v[64:67],v[16:19],v[112:115]
ds_read_u16_d16_hi v151,v151
v_rcp_f16 v121,v120
v_mfma_f32_16x16x32_bf16 v[136:139],v[64:67],v[20:23],v[112:115]
s_waitcnt lgkmcnt(11)
ds_read_b64_tr_b16 v[84:85],v5 offset:8704
v_lshlrev_b32_sdwa v155,1,v155 dst_sel:DWORD dst_unused:UNUSED_PAD src0_sel:DWORD src1_sel:WORD_1
v_mfma_f32_16x16x32_bf16 v[140:143],v[68:71],v[20:23],v[116:119]
ds_read_u16 v152,v152
v_rcp_f16_sdwa v121,v120 dst_sel:WORD_1 dst_unused:UNUSED_PRESERVE src0_sel:WORD_1
v_mfma_f32_16x16x32_bf16 v[132:135],v[68:71],v[16:19],v[116:119]
s_waitcnt lgkmcnt(11)
ds_read_u16_d16_hi v154,v154
v_lshlrev_b32_sdwa v156,1,v156 dst_sel:DWORD dst_unused:UNUSED_PAD src0_sel:DWORD src1_sel:WORD_1
v_mfma_f32_16x16x32_bf16 v[128:131],v[72:75],v[24:27],v[128:131]
ds_read_b64_tr_b16 v[86:87],v5 offset:10752
v_fma_mix_f32 v157,v157,v121,0 op_sel_hi:[0,1,0]
v_mfma_f32_16x16x32_bf16 v[136:139],v[72:75],v[28:31],v[136:139]
s_waitcnt lgkmcnt(9)
ds_read_u16 v153,v153
v_fma_mix_f32 v159,v159,v121,0 op_sel:[0,1,0] op_sel_hi:[0,1,0]
v_mfma_f32_16x16x32_bf16 v[140:143],v[76:79],v[28:31],v[140:143]
s_mov_b32 s86,0xbe801d5a
v_mfma_f32_16x16x32_bf16 v[132:135],v[76:79],v[24:27],v[132:135]
s_waitcnt lgkmcnt(6)
ds_read_u16_d16_hi v155,v155
v_lshlrev_b32_sdwa v158,1,v158 dst_sel:DWORD dst_unused:UNUSED_PAD src0_sel:DWORD src1_sel:WORD_1
v_mfma_f32_16x16x32_bf16 v[128:131],v[80:83],v[32:35],v[128:131]
ds_read_u16 v156,v156
v_mfma_f32_16x16x32_bf16 v[136:139],v[80:83],v[36:39],v[136:139]
s_waitcnt lgkmcnt(3)
v_lshl_add_u64 v[144:145],v[144:145],0,v[146:147]
v_mfma_f32_16x16x32_bf16 v[140:143],v[84:87],v[36:39],v[140:143]
ds_read_u16_d16_hi v158,v158
v_lshl_add_u64 v[146:147],v[148:149],0,v[150:151]
v_mfma_f32_16x16x32_bf16 v[132:135],v[84:87],v[32:35],v[132:135]
v_lshlrev_b32_sdwa v128,1,v128 dst_sel:DWORD dst_unused:UNUSED_PAD src0_sel:DWORD src1_sel:WORD_1
v_lshlrev_b32_sdwa v130,1,v130 dst_sel:DWORD dst_unused:UNUSED_PAD src0_sel:DWORD src1_sel:WORD_1
v_mfma_f32_16x16x32_bf16 v[208:211],v[88:91],v[144:147],v[208:211]
s_barrier
ds_read_u16 v128,v128
v_lshlrev_b32_sdwa v129,1,v129 dst_sel:DWORD dst_unused:UNUSED_PAD src0_sel:DWORD src1_sel:WORD_1
v_lshlrev_b32_sdwa v131,1,v131 dst_sel:DWORD dst_unused:UNUSED_PAD src0_sel:DWORD src1_sel:WORD_1
v_mfma_f32_16x16x32_bf16 v[216:219],v[92:95],v[144:147],v[216:219]
v_fma_mixlo_f16 v120,v141,s99,0
v_lshlrev_b32_sdwa v132,1,v132 dst_sel:DWORD dst_unused:UNUSED_PAD src0_sel:DWORD src1_sel:WORD_1
v_mfma_f32_16x16x32_bf16 v[224:227],v[96:99],v[144:147],v[224:227]
ds_read_u16_d16_hi v130,v130
v_fma_mixhi_f16 v120,v143,s99,0
v_lshlrev_b32_sdwa v134,1,v134 dst_sel:DWORD dst_unused:UNUSED_PAD src0_sel:DWORD src1_sel:WORD_1
v_mfma_f32_16x16x32_bf16 v[232:235],v[100:103],v[144:147],v[232:235]
s_waitcnt lgkmcnt(4)
ds_read_u16 v129,v129
v_lshl_add_u64 v[152:153],v[152:153],0,v[154:155]
v_mfma_f32_16x16x32_bf16 v[240:243],v[104:107],v[144:147],v[240:243]
s_waitcnt lgkmcnt(3)
ds_read_u16_d16_hi v131,v131
v_or_b32 v154,v158,v156
v_cvt_pk_bf16_f32 v155,v157,v159
v_mfma_f32_16x16x32_bf16 v[248:251],v[108:111],v[144:147],v[248:251]
v_pk_fma_f16 v121,v120,v120,s54
v_lshlrev_b32_sdwa v133,1,v133 dst_sel:DWORD dst_unused:UNUSED_PAD src0_sel:DWORD src1_sel:WORD_1
v_mfma_f32_16x16x32_bf16 v[212:215],v[88:91],v[152:155],v[212:215]
ds_read_u16 v132,v132
ds_read_b64_tr_b16 v[88:89],v6 offset:12320
v_pk_mul_f16 v121,v120,v121
v_lshlrev_b32_sdwa v135,1,v135 dst_sel:DWORD dst_unused:UNUSED_PAD src0_sel:DWORD src1_sel:WORD_1
v_mfma_f32_16x16x32_bf16 v[220:223],v[92:95],v[152:155],v[220:223]
ds_read_u16_d16_hi v134,v134
v_exp_f16 v120,v121
v_mfma_f32_16x16x32_bf16 v[228:231],v[96:99],v[152:155],v[228:231]
ds_read_b64_tr_b16 v[90:91],v6 offset:13856
ds_read_b64_tr_b16 v[92:93],v6 offset:12328
v_lshlrev_b32_sdwa v136,1,v136 dst_sel:DWORD dst_unused:UNUSED_PAD src0_sel:DWORD src1_sel:WORD_1
v_mfma_f32_16x16x32_bf16 v[236:239],v[100:103],v[152:155],v[236:239]
ds_read_u16 v133,v133
v_exp_f16_sdwa v120,v121 dst_sel:WORD_1 dst_unused:UNUSED_PRESERVE src0_sel:WORD_1
v_mfma_f32_16x16x32_bf16 v[244:247],v[104:107],v[152:155],v[244:247]
v_lshlrev_b32_sdwa v138,1,v138 dst_sel:DWORD dst_unused:UNUSED_PAD src0_sel:DWORD src1_sel:WORD_1
v_mfma_f32_16x16x32_bf16 v[252:255],v[108:111],v[152:155],v[252:255]
ds_read_b64_tr_b16 v[94:95],v6 offset:13864
ds_read_b64_tr_b16 v[96:97],v6 offset:12384
v_pk_fma_f16 v120,v120,s55,s55
v_lshlrev_b32_sdwa v137,1,v137 dst_sel:DWORD dst_unused:UNUSED_PAD src0_sel:DWORD src1_sel:WORD_1
v_mfma_f32_16x16x32_bf16 v[144:147],v[64:67],v[40:43],v[112:115]
ds_read_u16_d16_hi v135,v135
v_rcp_f16 v121,v120
v_mfma_f32_16x16x32_bf16 v[152:155],v[64:67],v[44:47],v[112:115]
ds_read_b64_tr_b16 v[98:99],v6 offset:13920
ds_read_b64_tr_b16 v[100:101],v6 offset:12392
v_lshlrev_b32_sdwa v139,1,v139 dst_sel:DWORD dst_unused:UNUSED_PAD src0_sel:DWORD src1_sel:WORD_1
v_mfma_f32_16x16x32_bf16 v[156:159],v[68:71],v[44:47],v[116:119]
ds_read_u16 v136,v136
v_rcp_f16_sdwa v121,v120 dst_sel:WORD_1 dst_unused:UNUSED_PRESERVE src0_sel:WORD_1
v_mfma_f32_16x16x32_bf16 v[148:151],v[68:71],v[40:43],v[116:119]
ds_read_u16_d16_hi v138,v138
v_lshlrev_b32_sdwa v140,1,v140 dst_sel:DWORD dst_unused:UNUSED_PAD src0_sel:DWORD src1_sel:WORD_1
v_mfma_f32_16x16x32_bf16 v[144:147],v[72:75],v[48:51],v[144:147]
ds_read_b64_tr_b16 v[102:103],v6 offset:13928
ds_read_b64_tr_b16 v[104:105],v6 offset:12448
v_fma_mix_f32 v141,v141,v121,0 op_sel_hi:[0,1,0]
v_lshlrev_b32_sdwa v142,1,v142 dst_sel:DWORD dst_unused:UNUSED_PAD src0_sel:DWORD src1_sel:WORD_1
v_mfma_f32_16x16x32_bf16 v[152:155],v[72:75],v[52:55],v[152:155]
ds_read_u16 v137,v137
v_fma_mix_f32 v143,v143,v121,0 op_sel:[0,1,0] op_sel_hi:[0,1,0]
v_mfma_f32_16x16x32_bf16 v[156:159],v[76:79],v[52:55],v[156:159]
ds_read_b64_tr_b16 v[106:107],v6 offset:13984
ds_read_b64_tr_b16 v[108:109],v6 offset:12456
v_mfma_f32_16x16x32_bf16 v[148:151],v[76:79],v[48:51],v[148:151]
ds_read_u16_d16_hi v139,v139
v_mfma_f32_16x16x32_bf16 v[144:147],v[80:83],v[56:59],v[144:147]
ds_read_b64_tr_b16 v[110:111],v6 offset:13992
ds_read_b64_tr_b16 v[64:65],v4 offset:12320
v_mfma_f32_16x16x32_bf16 v[152:155],v[80:83],v[60:63],v[152:155]
s_waitcnt lgkmcnt(12)
ds_read_u16 v140,v140
v_lshl_add_u64 v[128:129],v[128:129],0,v[130:131]
v_lshl_add_u64 v[130:131],v[132:133],0,v[134:135]
v_mfma_f32_16x16x32_bf16 v[156:159],v[84:87],v[60:63],v[156:159]
ds_read_u16_d16_hi v142,v142
v_mfma_f32_16x16x32_bf16 v[148:151],v[84:87],v[56:59],v[148:151]
ds_read_b64_tr_b16 v[66:67],v4 offset:14368
ds_read_b128 v[112:115],v7 offset:512
v_lshlrev_b32_sdwa v144,1,v144 dst_sel:DWORD dst_unused:UNUSED_PAD src0_sel:DWORD src1_sel:WORD_1
v_lshlrev_b32_sdwa v146,1,v146 dst_sel:DWORD dst_unused:UNUSED_PAD src0_sel:DWORD src1_sel:WORD_1
v_mfma_f32_16x16x32_bf16 v[160:163],v[88:91],v[128:131],v[160:163]
ds_read_u16 v144,v144
v_lshlrev_b32_sdwa v145,1,v145 dst_sel:DWORD dst_unused:UNUSED_PAD src0_sel:DWORD src1_sel:WORD_1
v_lshlrev_b32_sdwa v147,1,v147 dst_sel:DWORD dst_unused:UNUSED_PAD src0_sel:DWORD src1_sel:WORD_1
v_mfma_f32_16x16x32_bf16 v[168:171],v[92:95],v[128:131],v[168:171]
s_waitcnt lgkmcnt(15)
ds_read_b64_tr_b16 v[68:69],v5 offset:12320
ds_read_b64_tr_b16 v[70:71],v5 offset:14368
v_fma_mixlo_f16 v120,v157,s99,0
v_mfma_f32_16x16x32_bf16 v[176:179],v[96:99],v[128:131],v[176:179]
s_waitcnt lgkmcnt(9)
ds_read_u16_d16_hi v146,v146
v_lshlrev_b32_sdwa v148,1,v148 dst_sel:DWORD dst_unused:UNUSED_PAD src0_sel:DWORD src1_sel:WORD_1
v_lshl_add_u64 v[136:137],v[136:137],0,v[138:139]
v_fma_mixhi_f16 v120,v159,s99,0
v_lshlrev_b32_sdwa v150,1,v150 dst_sel:DWORD dst_unused:UNUSED_PAD src0_sel:DWORD src1_sel:WORD_1
v_mfma_f32_16x16x32_bf16 v[184:187],v[100:103],v[128:131],v[184:187]
ds_read_u16 v145,v145
v_mfma_f32_16x16x32_bf16 v[192:195],v[104:107],v[128:131],v[192:195]
s_waitcnt lgkmcnt(7)
ds_read_b128 v[116:119],v7 offset:544
ds_read_b64_tr_b16 v[72:73],v4 offset:16416
v_or_b32 v138,v142,v140
v_cvt_pk_bf16_f32 v139,v141,v143
v_mfma_f32_16x16x32_bf16 v[200:203],v[108:111],v[128:131],v[200:203]
ds_read_b64_tr_b16 v[74:75],v4 offset:18464
ds_read_b64_tr_b16 v[76:77],v5 offset:16416
v_pk_fma_f16 v121,v120,v120,s54
v_lshlrev_b32_sdwa v149,1,v149 dst_sel:DWORD dst_unused:UNUSED_PAD src0_sel:DWORD src1_sel:WORD_1
v_mfma_f32_16x16x32_bf16 v[164:167],v[88:91],v[136:139],v[164:167]
ds_read_u16_d16_hi v147,v147
v_pk_mul_f16 v121,v120,v121
v_lshlrev_b32_sdwa v151,1,v151 dst_sel:DWORD dst_unused:UNUSED_PAD src0_sel:DWORD src1_sel:WORD_1
v_mfma_f32_16x16x32_bf16 v[172:175],v[92:95],v[136:139],v[172:175]
ds_read_u16 v148,v148
v_lshlrev_b32_sdwa v152,1,v152 dst_sel:DWORD dst_unused:UNUSED_PAD src0_sel:DWORD src1_sel:WORD_1
v_exp_f16 v120,v121
v_mfma_f32_16x16x32_bf16 v[180:183],v[96:99],v[136:139],v[180:183]
ds_read_b64_tr_b16 v[78:79],v5 offset:18464
ds_read_b64_tr_b16 v[80:81],v4 offset:20512
v_mfma_f32_16x16x32_bf16 v[188:191],v[100:103],v[136:139],v[188:191]
v_exp_f16_sdwa v120,v121 dst_sel:WORD_1 dst_unused:UNUSED_PRESERVE src0_sel:WORD_1
v_mfma_f32_16x16x32_bf16 v[196:199],v[104:107],v[136:139],v[196:199]
ds_read_u16_d16_hi v150,v150
ds_read_u16 v149,v149
v_lshlrev_b32_sdwa v154,1,v154 dst_sel:DWORD dst_unused:UNUSED_PAD src0_sel:DWORD src1_sel:WORD_1
v_mfma_f32_16x16x32_bf16 v[204:207],v[108:111],v[136:139],v[204:207]
s_waitcnt lgkmcnt(15)
ds_read_b64_tr_b16 v[82:83],v4 offset:22560
v_pk_fma_f16 v120,v120,s55,s55
v_lshlrev_b32_sdwa v153,1,v153 dst_sel:DWORD dst_unused:UNUSED_PAD src0_sel:DWORD src1_sel:WORD_1
v_mfma_f32_16x16x32_bf16 v[128:131],v[64:67],v[16:19],v[112:115]
ds_read_u16_d16_hi v151,v151
v_rcp_f16 v121,v120
v_mfma_f32_16x16x32_bf16 v[136:139],v[64:67],v[20:23],v[112:115]
s_waitcnt lgkmcnt(11)
ds_read_b64_tr_b16 v[84:85],v5 offset:20512
v_lshlrev_b32_sdwa v155,1,v155 dst_sel:DWORD dst_unused:UNUSED_PAD src0_sel:DWORD src1_sel:WORD_1
v_mfma_f32_16x16x32_bf16 v[140:143],v[68:71],v[20:23],v[116:119]
ds_read_u16 v152,v152
v_rcp_f16_sdwa v121,v120 dst_sel:WORD_1 dst_unused:UNUSED_PRESERVE src0_sel:WORD_1
v_mfma_f32_16x16x32_bf16 v[132:135],v[68:71],v[16:19],v[116:119]
s_waitcnt lgkmcnt(11)
ds_read_u16_d16_hi v154,v154
v_lshlrev_b32_sdwa v156,1,v156 dst_sel:DWORD dst_unused:UNUSED_PAD src0_sel:DWORD src1_sel:WORD_1
v_mfma_f32_16x16x32_bf16 v[128:131],v[72:75],v[24:27],v[128:131]
ds_read_b64_tr_b16 v[86:87],v5 offset:22560
v_fma_mix_f32 v157,v157,v121,0 op_sel_hi:[0,1,0]
v_mfma_f32_16x16x32_bf16 v[136:139],v[72:75],v[28:31],v[136:139]
s_waitcnt lgkmcnt(9)
ds_read_u16 v153,v153
v_fma_mix_f32 v159,v159,v121,0 op_sel:[0,1,0] op_sel_hi:[0,1,0]
v_mfma_f32_16x16x32_bf16 v[140:143],v[76:79],v[28:31],v[140:143]
v_mfma_f32_16x16x32_bf16 v[132:135],v[76:79],v[24:27],v[132:135]
s_waitcnt lgkmcnt(6)
ds_read_u16_d16_hi v155,v155
v_lshlrev_b32_sdwa v158,1,v158 dst_sel:DWORD dst_unused:UNUSED_PAD src0_sel:DWORD src1_sel:WORD_1
v_mfma_f32_16x16x32_bf16 v[128:131],v[80:83],v[32:35],v[128:131]
ds_read_u16 v156,v156
v_mfma_f32_16x16x32_bf16 v[136:139],v[80:83],v[36:39],v[136:139]
s_waitcnt lgkmcnt(3)
v_lshl_add_u64 v[144:145],v[144:145],0,v[146:147]
v_mfma_f32_16x16x32_bf16 v[140:143],v[84:87],v[36:39],v[140:143]
ds_read_u16_d16_hi v158,v158
v_lshl_add_u64 v[146:147],v[148:149],0,v[150:151]
v_mfma_f32_16x16x32_bf16 v[132:135],v[84:87],v[32:35],v[132:135]
v_lshlrev_b32_sdwa v128,1,v128 dst_sel:DWORD dst_unused:UNUSED_PAD src0_sel:DWORD src1_sel:WORD_1
v_lshlrev_b32_sdwa v130,1,v130 dst_sel:DWORD dst_unused:UNUSED_PAD src0_sel:DWORD src1_sel:WORD_1
v_mfma_f32_16x16x32_bf16 v[208:211],v[88:91],v[144:147],v[208:211]
ds_read_u16 v128,v128
v_lshlrev_b32_sdwa v129,1,v129 dst_sel:DWORD dst_unused:UNUSED_PAD src0_sel:DWORD src1_sel:WORD_1
v_lshlrev_b32_sdwa v131,1,v131 dst_sel:DWORD dst_unused:UNUSED_PAD src0_sel:DWORD src1_sel:WORD_1
v_mfma_f32_16x16x32_bf16 v[216:219],v[92:95],v[144:147],v[216:219]
v_fma_mixlo_f16 v120,v141,s99,0
v_lshlrev_b32_sdwa v132,1,v132 dst_sel:DWORD dst_unused:UNUSED_PAD src0_sel:DWORD src1_sel:WORD_1
v_mfma_f32_16x16x32_bf16 v[224:227],v[96:99],v[144:147],v[224:227]
ds_read_u16_d16_hi v130,v130
v_fma_mixhi_f16 v120,v143,s99,0
v_lshlrev_b32_sdwa v134,1,v134 dst_sel:DWORD dst_unused:UNUSED_PAD src0_sel:DWORD src1_sel:WORD_1
v_mfma_f32_16x16x32_bf16 v[232:235],v[100:103],v[144:147],v[232:235]
s_waitcnt lgkmcnt(4)
ds_read_u16 v129,v129
v_lshl_add_u64 v[152:153],v[152:153],0,v[154:155]
v_mfma_f32_16x16x32_bf16 v[240:243],v[104:107],v[144:147],v[240:243]
s_waitcnt lgkmcnt(3)
ds_read_u16_d16_hi v131,v131
v_or_b32 v154,v158,v156
v_cvt_pk_bf16_f32 v155,v157,v159
v_mfma_f32_16x16x32_bf16 v[248:251],v[108:111],v[144:147],v[248:251]
v_pk_fma_f16 v121,v120,v120,s54
v_lshlrev_b32_sdwa v133,1,v133 dst_sel:DWORD dst_unused:UNUSED_PAD src0_sel:DWORD src1_sel:WORD_1
v_mfma_f32_16x16x32_bf16 v[212:215],v[88:91],v[152:155],v[212:215]
ds_read_u16 v132,v132
ds_read_b64_tr_b16 v[88:89],v6 offset:18480
v_pk_mul_f16 v121,v120,v121
v_lshlrev_b32_sdwa v135,1,v135 dst_sel:DWORD dst_unused:UNUSED_PAD src0_sel:DWORD src1_sel:WORD_1
v_mfma_f32_16x16x32_bf16 v[220:223],v[92:95],v[152:155],v[220:223]
ds_read_u16_d16_hi v134,v134
v_exp_f16 v120,v121
v_mfma_f32_16x16x32_bf16 v[228:231],v[96:99],v[152:155],v[228:231]
ds_read_b64_tr_b16 v[90:91],v6 offset:20016
ds_read_b64_tr_b16 v[92:93],v6 offset:18488
v_lshlrev_b32_sdwa v136,1,v136 dst_sel:DWORD dst_unused:UNUSED_PAD src0_sel:DWORD src1_sel:WORD_1
v_mfma_f32_16x16x32_bf16 v[236:239],v[100:103],v[152:155],v[236:239]
ds_read_u16 v133,v133
v_exp_f16_sdwa v120,v121 dst_sel:WORD_1 dst_unused:UNUSED_PRESERVE src0_sel:WORD_1
v_mfma_f32_16x16x32_bf16 v[244:247],v[104:107],v[152:155],v[244:247]
v_lshlrev_b32_sdwa v138,1,v138 dst_sel:DWORD dst_unused:UNUSED_PAD src0_sel:DWORD src1_sel:WORD_1
v_mfma_f32_16x16x32_bf16 v[252:255],v[108:111],v[152:155],v[252:255]
ds_read_b64_tr_b16 v[94:95],v6 offset:20024
ds_read_b64_tr_b16 v[96:97],v6 offset:18544
v_pk_fma_f16 v120,v120,s55,s55
v_lshlrev_b32_sdwa v137,1,v137 dst_sel:DWORD dst_unused:UNUSED_PAD src0_sel:DWORD src1_sel:WORD_1
v_mfma_f32_16x16x32_bf16 v[144:147],v[64:67],v[40:43],v[112:115]
ds_read_u16_d16_hi v135,v135
v_rcp_f16 v121,v120
v_mfma_f32_16x16x32_bf16 v[152:155],v[64:67],v[44:47],v[112:115]
ds_read_b64_tr_b16 v[98:99],v6 offset:20080
ds_read_b64_tr_b16 v[100:101],v6 offset:18552
v_lshlrev_b32_sdwa v139,1,v139 dst_sel:DWORD dst_unused:UNUSED_PAD src0_sel:DWORD src1_sel:WORD_1
v_mfma_f32_16x16x32_bf16 v[156:159],v[68:71],v[44:47],v[116:119]
ds_read_u16 v136,v136
v_rcp_f16_sdwa v121,v120 dst_sel:WORD_1 dst_unused:UNUSED_PRESERVE src0_sel:WORD_1
v_mfma_f32_16x16x32_bf16 v[148:151],v[68:71],v[40:43],v[116:119]
ds_read_u16_d16_hi v138,v138
v_lshlrev_b32_sdwa v140,1,v140 dst_sel:DWORD dst_unused:UNUSED_PAD src0_sel:DWORD src1_sel:WORD_1
v_mfma_f32_16x16x32_bf16 v[144:147],v[72:75],v[48:51],v[144:147]
ds_read_b64_tr_b16 v[102:103],v6 offset:20088
ds_read_b64_tr_b16 v[104:105],v6 offset:18608
v_fma_mix_f32 v141,v141,v121,0 op_sel_hi:[0,1,0]
v_lshlrev_b32_sdwa v142,1,v142 dst_sel:DWORD dst_unused:UNUSED_PAD src0_sel:DWORD src1_sel:WORD_1
v_mfma_f32_16x16x32_bf16 v[152:155],v[72:75],v[52:55],v[152:155]
ds_read_u16 v137,v137
v_fma_mix_f32 v143,v143,v121,0 op_sel:[0,1,0] op_sel_hi:[0,1,0]
v_mfma_f32_16x16x32_bf16 v[156:159],v[76:79],v[52:55],v[156:159]
ds_read_b64_tr_b16 v[106:107],v6 offset:20144
ds_read_b64_tr_b16 v[108:109],v6 offset:18616
v_mfma_f32_16x16x32_bf16 v[148:151],v[76:79],v[48:51],v[148:151]
ds_read_u16_d16_hi v139,v139
v_mfma_f32_16x16x32_bf16 v[144:147],v[80:83],v[56:59],v[144:147]
ds_read_b64_tr_b16 v[110:111],v6 offset:20152
ds_read_b64_tr_b16 v[64:65],v4 offset:12832
v_mfma_f32_16x16x32_bf16 v[152:155],v[80:83],v[60:63],v[152:155]
s_waitcnt lgkmcnt(12)
ds_read_u16 v140,v140
v_lshl_add_u64 v[128:129],v[128:129],0,v[130:131]
v_lshl_add_u64 v[130:131],v[132:133],0,v[134:135]
v_mfma_f32_16x16x32_bf16 v[156:159],v[84:87],v[60:63],v[156:159]
ds_read_u16_d16_hi v142,v142
v_mfma_f32_16x16x32_bf16 v[148:151],v[84:87],v[56:59],v[148:151]
ds_read_b64_tr_b16 v[66:67],v4 offset:14880
ds_read_b128 v[112:115],v7 offset:640
v_lshlrev_b32_sdwa v144,1,v144 dst_sel:DWORD dst_unused:UNUSED_PAD src0_sel:DWORD src1_sel:WORD_1
v_lshlrev_b32_sdwa v146,1,v146 dst_sel:DWORD dst_unused:UNUSED_PAD src0_sel:DWORD src1_sel:WORD_1
v_mfma_f32_16x16x32_bf16 v[160:163],v[88:91],v[128:131],v[160:163]
ds_read_u16 v144,v144
v_lshlrev_b32_sdwa v145,1,v145 dst_sel:DWORD dst_unused:UNUSED_PAD src0_sel:DWORD src1_sel:WORD_1
v_lshlrev_b32_sdwa v147,1,v147 dst_sel:DWORD dst_unused:UNUSED_PAD src0_sel:DWORD src1_sel:WORD_1
v_mfma_f32_16x16x32_bf16 v[168:171],v[92:95],v[128:131],v[168:171]
s_waitcnt lgkmcnt(15)
ds_read_b64_tr_b16 v[68:69],v5 offset:12832
ds_read_b64_tr_b16 v[70:71],v5 offset:14880
v_fma_mixlo_f16 v120,v157,s99,0
v_mfma_f32_16x16x32_bf16 v[176:179],v[96:99],v[128:131],v[176:179]
s_waitcnt lgkmcnt(9)
ds_read_u16_d16_hi v146,v146
v_lshlrev_b32_sdwa v148,1,v148 dst_sel:DWORD dst_unused:UNUSED_PAD src0_sel:DWORD src1_sel:WORD_1
v_lshl_add_u64 v[136:137],v[136:137],0,v[138:139]
v_fma_mixhi_f16 v120,v159,s99,0
v_lshlrev_b32_sdwa v150,1,v150 dst_sel:DWORD dst_unused:UNUSED_PAD src0_sel:DWORD src1_sel:WORD_1
v_mfma_f32_16x16x32_bf16 v[184:187],v[100:103],v[128:131],v[184:187]
ds_read_u16 v145,v145
v_mfma_f32_16x16x32_bf16 v[192:195],v[104:107],v[128:131],v[192:195]
s_waitcnt lgkmcnt(7)
ds_read_b128 v[116:119],v7 offset:672
ds_read_b64_tr_b16 v[72:73],v4 offset:16928
v_or_b32 v138,v142,v140
v_cvt_pk_bf16_f32 v139,v141,v143
v_mfma_f32_16x16x32_bf16 v[200:203],v[108:111],v[128:131],v[200:203]
ds_read_b64_tr_b16 v[74:75],v4 offset:18976
ds_read_b64_tr_b16 v[76:77],v5 offset:16928
v_pk_fma_f16 v121,v120,v120,s54
v_lshlrev_b32_sdwa v149,1,v149 dst_sel:DWORD dst_unused:UNUSED_PAD src0_sel:DWORD src1_sel:WORD_1
v_mfma_f32_16x16x32_bf16 v[164:167],v[88:91],v[136:139],v[164:167]
ds_read_u16_d16_hi v147,v147
v_pk_mul_f16 v121,v120,v121
v_lshlrev_b32_sdwa v151,1,v151 dst_sel:DWORD dst_unused:UNUSED_PAD src0_sel:DWORD src1_sel:WORD_1
v_mfma_f32_16x16x32_bf16 v[172:175],v[92:95],v[136:139],v[172:175]
ds_read_u16 v148,v148
v_lshlrev_b32_sdwa v152,1,v152 dst_sel:DWORD dst_unused:UNUSED_PAD src0_sel:DWORD src1_sel:WORD_1
v_exp_f16 v120,v121
v_mfma_f32_16x16x32_bf16 v[180:183],v[96:99],v[136:139],v[180:183]
ds_read_b64_tr_b16 v[78:79],v5 offset:18976
ds_read_b64_tr_b16 v[80:81],v4 offset:21024
v_mfma_f32_16x16x32_bf16 v[188:191],v[100:103],v[136:139],v[188:191]
v_exp_f16_sdwa v120,v121 dst_sel:WORD_1 dst_unused:UNUSED_PRESERVE src0_sel:WORD_1
v_mfma_f32_16x16x32_bf16 v[196:199],v[104:107],v[136:139],v[196:199]
ds_read_u16_d16_hi v150,v150
ds_read_u16 v149,v149
v_lshlrev_b32_sdwa v154,1,v154 dst_sel:DWORD dst_unused:UNUSED_PAD src0_sel:DWORD src1_sel:WORD_1
v_mfma_f32_16x16x32_bf16 v[204:207],v[108:111],v[136:139],v[204:207]
s_waitcnt lgkmcnt(15)
ds_read_b64_tr_b16 v[82:83],v4 offset:23072
v_pk_fma_f16 v120,v120,s55,s55
v_lshlrev_b32_sdwa v153,1,v153 dst_sel:DWORD dst_unused:UNUSED_PAD src0_sel:DWORD src1_sel:WORD_1
v_mfma_f32_16x16x32_bf16 v[128:131],v[64:67],v[16:19],v[112:115]
ds_read_u16_d16_hi v151,v151
v_rcp_f16 v121,v120
v_mfma_f32_16x16x32_bf16 v[136:139],v[64:67],v[20:23],v[112:115]
s_waitcnt lgkmcnt(11)
ds_read_b64_tr_b16 v[84:85],v5 offset:21024
v_lshlrev_b32_sdwa v155,1,v155 dst_sel:DWORD dst_unused:UNUSED_PAD src0_sel:DWORD src1_sel:WORD_1
v_mfma_f32_16x16x32_bf16 v[140:143],v[68:71],v[20:23],v[116:119]
ds_read_u16 v152,v152
v_rcp_f16_sdwa v121,v120 dst_sel:WORD_1 dst_unused:UNUSED_PRESERVE src0_sel:WORD_1
v_mfma_f32_16x16x32_bf16 v[132:135],v[68:71],v[16:19],v[116:119]
s_waitcnt lgkmcnt(11)
ds_read_u16_d16_hi v154,v154
v_lshlrev_b32_sdwa v156,1,v156 dst_sel:DWORD dst_unused:UNUSED_PAD src0_sel:DWORD src1_sel:WORD_1
v_mfma_f32_16x16x32_bf16 v[128:131],v[72:75],v[24:27],v[128:131]
ds_read_b64_tr_b16 v[86:87],v5 offset:23072
v_fma_mix_f32 v157,v157,v121,0 op_sel_hi:[0,1,0]
v_mfma_f32_16x16x32_bf16 v[136:139],v[72:75],v[28:31],v[136:139]
s_waitcnt lgkmcnt(9)
ds_read_u16 v153,v153
v_fma_mix_f32 v159,v159,v121,0 op_sel:[0,1,0] op_sel_hi:[0,1,0]
v_mfma_f32_16x16x32_bf16 v[140:143],v[76:79],v[28:31],v[140:143]
v_mfma_f32_16x16x32_bf16 v[132:135],v[76:79],v[24:27],v[132:135]
s_waitcnt lgkmcnt(6)
ds_read_u16_d16_hi v155,v155
v_lshlrev_b32_sdwa v158,1,v158 dst_sel:DWORD dst_unused:UNUSED_PAD src0_sel:DWORD src1_sel:WORD_1
v_mfma_f32_16x16x32_bf16 v[128:131],v[80:83],v[32:35],v[128:131]
ds_read_u16 v156,v156
v_mfma_f32_16x16x32_bf16 v[136:139],v[80:83],v[36:39],v[136:139]
s_waitcnt lgkmcnt(3)
v_lshl_add_u64 v[144:145],v[144:145],0,v[146:147]
v_mfma_f32_16x16x32_bf16 v[140:143],v[84:87],v[36:39],v[140:143]
ds_read_u16_d16_hi v158,v158
v_lshl_add_u64 v[146:147],v[148:149],0,v[150:151]
v_mfma_f32_16x16x32_bf16 v[132:135],v[84:87],v[32:35],v[132:135]
v_lshlrev_b32_sdwa v128,1,v128 dst_sel:DWORD dst_unused:UNUSED_PAD src0_sel:DWORD src1_sel:WORD_1
v_lshlrev_b32_sdwa v130,1,v130 dst_sel:DWORD dst_unused:UNUSED_PAD src0_sel:DWORD src1_sel:WORD_1
v_mfma_f32_16x16x32_bf16 v[208:211],v[88:91],v[144:147],v[208:211]
s_barrier
s_setprio 3
ds_read_u16 v128,v128
v_fma_mixlo_f16 v120,v140,s99,0
v_lshlrev_b32_sdwa v129,1,v129 dst_sel:DWORD dst_unused:UNUSED_PAD src0_sel:DWORD src1_sel:WORD_1
v_lshlrev_b32_sdwa v131,1,v131 dst_sel:DWORD dst_unused:UNUSED_PAD src0_sel:DWORD src1_sel:WORD_1
v_mfma_f32_16x16x32_bf16 v[216:219],v[92:95],v[144:147],v[216:219]
s_cbranch_vccnz RA1_%=
s_mov_b64 exec,s[62:63]
s_add_u32 m0,s57,48832
s_add_u32 s64,s56,3342336
buffer_load_dwordx4 v2,s[36:39],s64 offen lds
s_add_u32 m0,s57,50560
s_nop 0
buffer_load_dwordx4 v3,s[36:39],s64 offen lds
s_mov_b64 exec,-1
RA1_%=:
v_fma_mixlo_f16 v122,v141,s99,0
v_lshlrev_b32_sdwa v132,1,v132 dst_sel:DWORD dst_unused:UNUSED_PAD src0_sel:DWORD src1_sel:WORD_1
v_mfma_f32_16x16x32_bf16 v[224:227],v[96:99],v[144:147],v[224:227]
v_lshlrev_b32_sdwa v134,1,v134 dst_sel:DWORD dst_unused:UNUSED_PAD src0_sel:DWORD src1_sel:WORD_1
v_mfma_f32_16x16x32_bf16 v[232:235],v[100:103],v[144:147],v[232:235]
s_waitcnt lgkmcnt(3)
ds_read_u16_d16_hi v130,v130
v_lshl_add_u64 v[152:153],v[152:153],0,v[154:155]
v_mfma_f32_16x16x32_bf16 v[240:243],v[104:107],v[144:147],v[240:243]
s_waitcnt lgkmcnt(2)
ds_read_u16 v129,v129
v_or_b32 v154,v158,v156
v_cvt_pk_bf16_f32 v155,v157,v159
v_mfma_f32_16x16x32_bf16 v[248:251],v[108:111],v[144:147],v[248:251]
v_fma_mixhi_f16 v120,v142,s99,0
v_fma_mixhi_f16 v122,v143,s99,0
v_mfma_f32_16x16x32_bf16 v[212:215],v[88:91],v[152:155],v[212:215]
ds_read_u16_d16_hi v131,v131
v_lshlrev_b32_sdwa v133,1,v133 dst_sel:DWORD dst_unused:UNUSED_PAD src0_sel:DWORD src1_sel:WORD_1
v_lshlrev_b32_sdwa v135,1,v135 dst_sel:DWORD dst_unused:UNUSED_PAD src0_sel:DWORD src1_sel:WORD_1
v_mfma_f32_16x16x32_bf16 v[220:223],v[92:95],v[152:155],v[220:223]
v_pk_fma_f16 v121,v120,v120,s54
v_pk_fma_f16 v123,v122,v122,s54
v_mfma_f32_16x16x32_bf16 v[228:231],v[96:99],v[152:155],v[228:231]
s_cbranch_vccnz RA2_%=
s_mov_b64 exec,s[62:63]
s_add_u32 m0,s57,52320
s_add_u32 s64,s56,3440640
buffer_load_dwordx4 v2,s[36:39],s64 offen lds
s_add_u32 m0,s57,54048
s_nop 0
buffer_load_dwordx4 v3,s[36:39],s64 offen lds
s_mov_b64 exec,-1
RA2_%=:
ds_read_u16 v132,v132
v_pk_mul_f16 v121,v120,v121
v_pk_mul_f16 v123,v122,v123
v_mfma_f32_16x16x32_bf16 v[236:239],v[100:103],v[152:155],v[236:239]
ds_read_b64_tr_b16 v[88:89],v6 offset:0
ds_read_b64_tr_b16 v[90:91],v6 offset:1536
v_lshlrev_b32_sdwa v136,1,v136 dst_sel:DWORD dst_unused:UNUSED_PAD src0_sel:DWORD src1_sel:WORD_1
v_mfma_f32_16x16x32_bf16 v[244:247],v[104:107],v[152:155],v[244:247]
ds_read_u16_d16_hi v134,v134
v_lshlrev_b32_sdwa v138,1,v138 dst_sel:DWORD dst_unused:UNUSED_PAD src0_sel:DWORD src1_sel:WORD_1
v_exp_f16 v120,v121
v_mfma_f32_16x16x32_bf16 v[252:255],v[108:111],v[152:155],v[252:255]
ds_read_b64_tr_b16 v[92:93],v6 offset:8
v_exp_f16 v122,v123
v_mfma_f32_16x16x32_bf16 v[144:147],v[64:67],v[40:43],v[112:115]
ds_read_u16 v133,v133
v_lshlrev_b32_sdwa v137,1,v137 dst_sel:DWORD dst_unused:UNUSED_PAD src0_sel:DWORD src1_sel:WORD_1
v_lshlrev_b32_sdwa v139,1,v139 dst_sel:DWORD dst_unused:UNUSED_PAD src0_sel:DWORD src1_sel:WORD_1
v_mfma_f32_16x16x32_bf16 v[152:155],v[64:67],v[44:47],v[112:115]
ds_read_b64_tr_b16 v[94:95],v6 offset:1544
v_exp_f16_sdwa v120,v121 dst_sel:WORD_1 dst_unused:UNUSED_PRESERVE src0_sel:WORD_1
v_mfma_f32_16x16x32_bf16 v[156:159],v[68:71],v[44:47],v[116:119]
ds_read_u16_d16_hi v135,v135
v_exp_f16_sdwa v122,v123 dst_sel:WORD_1 dst_unused:UNUSED_PRESERVE src0_sel:WORD_1
v_mfma_f32_16x16x32_bf16 v[148:151],v[68:71],v[40:43],v[116:119]
ds_read_b64_tr_b16 v[96:97],v6 offset:64
v_pk_fma_f16 v120,v120,s55,s55
v_mfma_f32_16x16x32_bf16 v[144:147],v[72:75],v[48:51],v[144:147]
ds_read_u16 v136,v136
v_rcp_f16 v121,v120
v_pk_fma_f16 v122,v122,s55,s55
v_mfma_f32_16x16x32_bf16 v[152:155],v[72:75],v[52:55],v[152:155]
ds_read_b64_tr_b16 v[98:99],v6 offset:1600
v_rcp_f16 v123,v122
v_mfma_f32_16x16x32_bf16 v[156:159],v[76:79],v[52:55],v[156:159]
ds_read_u16_d16_hi v138,v138
v_rcp_f16_sdwa v121,v120 dst_sel:WORD_1 dst_unused:UNUSED_PRESERVE src0_sel:WORD_1
v_mfma_f32_16x16x32_bf16 v[148:151],v[76:79],v[48:51],v[148:151]
ds_read_b64_tr_b16 v[100:101],v6 offset:72
ds_read_b64_tr_b16 v[102:103],v6 offset:1608
v_rcp_f16_sdwa v123,v122 dst_sel:WORD_1 dst_unused:UNUSED_PRESERVE src0_sel:WORD_1
v_mfma_f32_16x16x32_bf16 v[144:147],v[80:83],v[56:59],v[144:147]
s_waitcnt lgkmcnt(14)
ds_read_u16 v137,v137
v_lshl_add_u64 v[128:129],v[128:129],0,v[130:131]
v_fma_mix_f32 v140,v140,v121,0 op_sel_hi:[0,1,0]
v_fma_mix_f32 v142,v142,v121,0 op_sel:[0,1,0] op_sel_hi:[0,1,0]
v_mfma_f32_16x16x32_bf16 v[152:155],v[80:83],v[60:63],v[152:155]
ds_read_b64_tr_b16 v[104:105],v6 offset:128
ds_read_b64_tr_b16 v[106:107],v6 offset:1664
v_mfma_f32_16x16x32_bf16 v[156:159],v[84:87],v[60:63],v[156:159]
s_waitcnt lgkmcnt(9)
ds_read_u16_d16_hi v139,v139
v_lshl_add_u64 v[130:131],v[132:133],0,v[134:135]
v_fma_mix_f32 v141,v141,v123,0 op_sel_hi:[0,1,0]
v_fma_mix_f32 v143,v143,v123,0 op_sel:[0,1,0] op_sel_hi:[0,1,0]
v_mfma_f32_16x16x32_bf16 v[148:151],v[84:87],v[56:59],v[148:151]
ds_read_b64_tr_b16 v[108:109],v6 offset:136
v_lshlrev_b32_sdwa v144,1,v144 dst_sel:DWORD dst_unused:UNUSED_PAD src0_sel:DWORD src1_sel:WORD_1
v_lshlrev_b32_sdwa v146,1,v146 dst_sel:DWORD dst_unused:UNUSED_PAD src0_sel:DWORD src1_sel:WORD_1
v_mfma_f32_16x16x32_bf16 v[160:163],v[88:91],v[128:131],v[160:163]
ds_read_b64_tr_b16 v[110:111],v6 offset:1672
ds_read_b64_tr_b16 v[64:65],v4 offset:0
v_lshlrev_b32_sdwa v145,1,v145 dst_sel:DWORD dst_unused:UNUSED_PAD src0_sel:DWORD src1_sel:WORD_1
v_lshlrev_b32_sdwa v147,1,v147 dst_sel:DWORD dst_unused:UNUSED_PAD src0_sel:DWORD src1_sel:WORD_1
v_mfma_f32_16x16x32_bf16 v[168:171],v[92:95],v[128:131],v[168:171]
s_waitcnt lgkmcnt(10)
ds_read_b64_tr_b16 v[66:67],v4 offset:2048
ds_read_b128 v[112:115],v7 offset:768
v_fma_mixlo_f16 v120,v156,s99,0
v_mfma_f32_16x16x32_bf16 v[176:179],v[96:99],v[128:131],v[176:179]
s_waitcnt lgkmcnt(9)
ds_read_b64_tr_b16 v[68:69],v5 offset:0
ds_read_u16 v144,v144
v_fma_mixlo_f16 v122,v157,s99,0
v_lshlrev_b32_sdwa v148,1,v148 dst_sel:DWORD dst_unused:UNUSED_PAD src0_sel:DWORD src1_sel:WORD_1
v_lshlrev_b32_sdwa v150,1,v150 dst_sel:DWORD dst_unused:UNUSED_PAD src0_sel:DWORD src1_sel:WORD_1
v_mfma_f32_16x16x32_bf16 v[184:187],v[100:103],v[128:131],v[184:187]
s_waitcnt lgkmcnt(7)
ds_read_b64_tr_b16 v[70:71],v5 offset:2048
v_lshl_add_u64 v[136:137],v[136:137],0,v[138:139]
v_mfma_f32_16x16x32_bf16 v[192:195],v[104:107],v[128:131],v[192:195]
s_waitcnt lgkmcnt(6)
ds_read_u16_d16_hi v146,v146
v_cvt_pk_bf16_f32 v138,v140,v142
v_cvt_pk_bf16_f32 v139,v141,v143
v_mfma_f32_16x16x32_bf16 v[200:203],v[108:111],v[128:131],v[200:203]
ds_read_b128 v[116:119],v7 offset:800
ds_read_b64_tr_b16 v[72:73],v4 offset:4096
v_fma_mixhi_f16 v120,v158,s99,0
v_fma_mixhi_f16 v122,v159,s99,0
v_mfma_f32_16x16x32_bf16 v[164:167],v[88:91],v[136:139],v[164:167]
v_lshlrev_b32_sdwa v149,1,v149 dst_sel:DWORD dst_unused:UNUSED_PAD src0_sel:DWORD src1_sel:WORD_1
v_lshlrev_b32_sdwa v151,1,v151 dst_sel:DWORD dst_unused:UNUSED_PAD src0_sel:DWORD src1_sel:WORD_1
v_mfma_f32_16x16x32_bf16 v[172:175],v[92:95],v[136:139],v[172:175]
ds_read_b64_tr_b16 v[74:75],v4 offset:6144
ds_read_b64_tr_b16 v[76:77],v5 offset:4096
v_pk_fma_f16 v123,v122,v122,s54
v_pk_fma_f16 v121,v120,v120,s54
v_mfma_f32_16x16x32_bf16 v[180:183],v[96:99],v[136:139],v[180:183]
ds_read_u16 v145,v145
v_pk_mul_f16 v121,v120,v121
v_pk_mul_f16 v123,v122,v123
v_mfma_f32_16x16x32_bf16 v[188:191],v[100:103],v[136:139],v[188:191]
ds_read_b64_tr_b16 v[78:79],v5 offset:6144
v_lshlrev_b32_sdwa v152,1,v152 dst_sel:DWORD dst_unused:UNUSED_PAD src0_sel:DWORD src1_sel:WORD_1
v_lshlrev_b32_sdwa v154,1,v154 dst_sel:DWORD dst_unused:UNUSED_PAD src0_sel:DWORD src1_sel:WORD_1
v_mfma_f32_16x16x32_bf16 v[196:199],v[104:107],v[136:139],v[196:199]
ds_read_u16_d16_hi v147,v147
v_exp_f16 v120,v121
v_mfma_f32_16x16x32_bf16 v[204:207],v[108:111],v[136:139],v[204:207]
s_waitcnt lgkmcnt(11)
ds_read_b64_tr_b16 v[80:81],v4 offset:8192
v_exp_f16 v122,v123
v_mfma_f32_16x16x32_bf16 v[128:131],v[64:67],v[16:19],v[112:115]
ds_read_u16 v148,v148
ds_read_u16_d16_hi v150,v150
v_lshlrev_b32_sdwa v153,1,v153 dst_sel:DWORD dst_unused:UNUSED_PAD src0_sel:DWORD src1_sel:WORD_1
v_lshlrev_b32_sdwa v155,1,v155 dst_sel:DWORD dst_unused:UNUSED_PAD src0_sel:DWORD src1_sel:WORD_1
v_exp_f16_sdwa v122,v123 dst_sel:WORD_1 dst_unused:UNUSED_PRESERVE src0_sel:WORD_1
v_mfma_f32_16x16x32_bf16 v[136:139],v[64:67],v[20:23],v[112:115]
s_waitcnt lgkmcnt(9)
v_exp_f16_sdwa v120,v121 dst_sel:WORD_1 dst_unused:UNUSED_PRESERVE src0_sel:WORD_1
v_mfma_f32_16x16x32_bf16 v[140:143],v[68:71],v[20:23],v[116:119]
ds_read_b64_tr_b16 v[82:83],v4 offset:10240
ds_read_b64_tr_b16 v[84:85],v5 offset:8192
v_pk_fma_f16 v120,v120,s55,s55
v_mfma_f32_16x16x32_bf16 v[132:135],v[68:71],v[16:19],v[116:119]
s_waitcnt lgkmcnt(9)
ds_read_u16 v149,v149
s_nop 0
s_mov_b32 s86,0xbe801d5a
v_pk_fma_f16 v122,v122,s55,s55
v_mfma_f32_16x16x32_bf16 v[128:131],v[72:75],v[24:27],v[128:131]
ds_read_b64_tr_b16 v[86:87],v5 offset:10240
ds_read_u16_d16_hi v151,v151
v_rcp_f16 v121,v120
v_mfma_f32_16x16x32_bf16 v[136:139],v[72:75],v[28:31],v[136:139]
s_waitcnt lgkmcnt(9)
v_rcp_f16 v123,v122
v_mfma_f32_16x16x32_bf16 v[140:143],v[76:79],v[28:31],v[140:143]
ds_read_u16 v152,v152
v_rcp_f16_sdwa v121,v120 dst_sel:WORD_1 dst_unused:UNUSED_PRESERVE src0_sel:WORD_1
v_mfma_f32_16x16x32_bf16 v[132:135],v[76:79],v[24:27],v[132:135]
s_waitcnt lgkmcnt(5)
v_rcp_f16_sdwa v123,v122 dst_sel:WORD_1 dst_unused:UNUSED_PRESERVE src0_sel:WORD_1
v_mfma_f32_16x16x32_bf16 v[128:131],v[80:83],v[32:35],v[128:131]
ds_read_u16_d16_hi v154,v154
v_fma_mix_f32 v156,v156,v121,0 op_sel_hi:[0,1,0]
v_mfma_f32_16x16x32_bf16 v[136:139],v[80:83],v[36:39],v[136:139]
s_waitcnt lgkmcnt(2)
v_lshl_add_u64 v[144:145],v[144:145],0,v[146:147]
v_fma_mix_f32 v158,v158,v121,0 op_sel:[0,1,0] op_sel_hi:[0,1,0]
v_lshl_add_u64 v[146:147],v[148:149],0,v[150:151]
v_mfma_f32_16x16x32_bf16 v[140:143],v[84:87],v[36:39],v[140:143]
ds_read_u16 v153,v153
v_fma_mix_f32 v157,v157,v123,0 op_sel_hi:[0,1,0]
v_fma_mix_f32 v159,v159,v123,0 op_sel:[0,1,0] op_sel_hi:[0,1,0]
v_mfma_f32_16x16x32_bf16 v[132:135],v[84:87],v[32:35],v[132:135]
v_lshlrev_b32_sdwa v128,1,v128 dst_sel:DWORD dst_unused:UNUSED_PAD src0_sel:DWORD src1_sel:WORD_1
v_lshlrev_b32_sdwa v130,1,v130 dst_sel:DWORD dst_unused:UNUSED_PAD src0_sel:DWORD src1_sel:WORD_1
v_mfma_f32_16x16x32_bf16 v[208:211],v[88:91],v[144:147],v[208:211]
ds_read_u16_d16_hi v155,v155
v_fma_mixlo_f16 v120,v140,s99,0
v_lshlrev_b32_sdwa v129,1,v129 dst_sel:DWORD dst_unused:UNUSED_PAD src0_sel:DWORD src1_sel:WORD_1
v_lshlrev_b32_sdwa v131,1,v131 dst_sel:DWORD dst_unused:UNUSED_PAD src0_sel:DWORD src1_sel:WORD_1
v_mfma_f32_16x16x32_bf16 v[216:219],v[92:95],v[144:147],v[216:219]
v_fma_mixlo_f16 v122,v141,s99,0
v_lshlrev_b32_sdwa v132,1,v132 dst_sel:DWORD dst_unused:UNUSED_PAD src0_sel:DWORD src1_sel:WORD_1
v_mfma_f32_16x16x32_bf16 v[224:227],v[96:99],v[144:147],v[224:227]
v_lshlrev_b32_sdwa v134,1,v134 dst_sel:DWORD dst_unused:UNUSED_PAD src0_sel:DWORD src1_sel:WORD_1
v_mfma_f32_16x16x32_bf16 v[232:235],v[100:103],v[144:147],v[232:235]
s_waitcnt lgkmcnt(0)
ds_read_u16 v128,v128
v_lshl_add_u64 v[152:153],v[152:153],0,v[154:155]
v_mfma_f32_16x16x32_bf16 v[240:243],v[104:107],v[144:147],v[240:243]
ds_read_u16_d16_hi v130,v130
v_cvt_pk_bf16_f32 v155,v157,v159
v_cvt_pk_bf16_f32 v154,v156,v158
v_mfma_f32_16x16x32_bf16 v[248:251],v[108:111],v[144:147],v[248:251]
s_setprio 2
v_fma_mixhi_f16 v120,v142,s99,0
v_fma_mixhi_f16 v122,v143,s99,0
v_mfma_f32_16x16x32_bf16 v[212:215],v[88:91],v[152:155],v[212:215]
ds_read_u16 v129,v129
v_lshlrev_b32_sdwa v133,1,v133 dst_sel:DWORD dst_unused:UNUSED_PAD src0_sel:DWORD src1_sel:WORD_1
v_lshlrev_b32_sdwa v135,1,v135 dst_sel:DWORD dst_unused:UNUSED_PAD src0_sel:DWORD src1_sel:WORD_1
v_mfma_f32_16x16x32_bf16 v[220:223],v[92:95],v[152:155],v[220:223]
v_pk_fma_f16 v121,v120,v120,s54
v_pk_fma_f16 v123,v122,v122,s54
v_mfma_f32_16x16x32_bf16 v[228:231],v[96:99],v[152:155],v[228:231]
ds_read_u16_d16_hi v131,v131
v_pk_mul_f16 v121,v120,v121
v_pk_mul_f16 v123,v122,v123
v_mfma_f32_16x16x32_bf16 v[236:239],v[100:103],v[152:155],v[236:239]
ds_read_b64_tr_b16 v[88:89],v6 offset:6160
ds_read_b64_tr_b16 v[90:91],v6 offset:7696
v_lshlrev_b32_sdwa v136,1,v136 dst_sel:DWORD dst_unused:UNUSED_PAD src0_sel:DWORD src1_sel:WORD_1
v_mfma_f32_16x16x32_bf16 v[244:247],v[104:107],v[152:155],v[244:247]
ds_read_u16 v132,v132
v_lshlrev_b32_sdwa v138,1,v138 dst_sel:DWORD dst_unused:UNUSED_PAD src0_sel:DWORD src1_sel:WORD_1
v_exp_f16 v120,v121
v_mfma_f32_16x16x32_bf16 v[252:255],v[108:111],v[152:155],v[252:255]
ds_read_b64_tr_b16 v[92:93],v6 offset:6168
v_exp_f16 v122,v123
v_mfma_f32_16x16x32_bf16 v[144:147],v[64:67],v[40:43],v[112:115]
ds_read_u16_d16_hi v134,v134
v_lshlrev_b32_sdwa v137,1,v137 dst_sel:DWORD dst_unused:UNUSED_PAD src0_sel:DWORD src1_sel:WORD_1
v_lshlrev_b32_sdwa v139,1,v139 dst_sel:DWORD dst_unused:UNUSED_PAD src0_sel:DWORD src1_sel:WORD_1
v_mfma_f32_16x16x32_bf16 v[152:155],v[64:67],v[44:47],v[112:115]
ds_read_b64_tr_b16 v[94:95],v6 offset:7704
v_exp_f16_sdwa v120,v121 dst_sel:WORD_1 dst_unused:UNUSED_PRESERVE src0_sel:WORD_1
v_mfma_f32_16x16x32_bf16 v[156:159],v[68:71],v[44:47],v[116:119]
ds_read_u16 v133,v133
v_exp_f16_sdwa v122,v123 dst_sel:WORD_1 dst_unused:UNUSED_PRESERVE src0_sel:WORD_1
v_mfma_f32_16x16x32_bf16 v[148:151],v[68:71],v[40:43],v[116:119]
ds_read_b64_tr_b16 v[96:97],v6 offset:6224
v_pk_fma_f16 v120,v120,s55,s55
v_mfma_f32_16x16x32_bf16 v[144:147],v[72:75],v[48:51],v[144:147]
ds_read_u16_d16_hi v135,v135
v_rcp_f16 v121,v120
v_pk_fma_f16 v122,v122,s55,s55
v_mfma_f32_16x16x32_bf16 v[152:155],v[72:75],v[52:55],v[152:155]
ds_read_b64_tr_b16 v[98:99],v6 offset:7760
v_rcp_f16 v123,v122
v_mfma_f32_16x16x32_bf16 v[156:159],v[76:79],v[52:55],v[156:159]
ds_read_u16 v136,v136
v_rcp_f16_sdwa v121,v120 dst_sel:WORD_1 dst_unused:UNUSED_PRESERVE src0_sel:WORD_1
v_mfma_f32_16x16x32_bf16 v[148:151],v[76:79],v[48:51],v[148:151]
ds_read_b64_tr_b16 v[100:101],v6 offset:6232
ds_read_b64_tr_b16 v[102:103],v6 offset:7768
v_rcp_f16_sdwa v123,v122 dst_sel:WORD_1 dst_unused:UNUSED_PRESERVE src0_sel:WORD_1
v_mfma_f32_16x16x32_bf16 v[144:147],v[80:83],v[56:59],v[144:147]
s_waitcnt lgkmcnt(13)
ds_read_u16_d16_hi v138,v138
v_lshl_add_u64 v[128:129],v[128:129],0,v[130:131]
v_fma_mix_f32 v140,v140,v121,0 op_sel_hi:[0,1,0]
v_fma_mix_f32 v142,v142,v121,0 op_sel:[0,1,0] op_sel_hi:[0,1,0]
v_mfma_f32_16x16x32_bf16 v[152:155],v[80:83],v[60:63],v[152:155]
ds_read_b64_tr_b16 v[104:105],v6 offset:6288
ds_read_b64_tr_b16 v[106:107],v6 offset:7824
v_mfma_f32_16x16x32_bf16 v[156:159],v[84:87],v[60:63],v[156:159]
s_waitcnt lgkmcnt(7)
ds_read_u16 v137,v137
v_lshl_add_u64 v[130:131],v[132:133],0,v[134:135]
v_fma_mix_f32 v141,v141,v123,0 op_sel_hi:[0,1,0]
v_fma_mix_f32 v143,v143,v123,0 op_sel:[0,1,0] op_sel_hi:[0,1,0]
v_mfma_f32_16x16x32_bf16 v[148:151],v[84:87],v[56:59],v[148:151]
ds_read_u16_d16_hi v139,v139
v_lshlrev_b32_sdwa v144,1,v144 dst_sel:DWORD dst_unused:UNUSED_PAD src0_sel:DWORD src1_sel:WORD_1
v_lshlrev_b32_sdwa v146,1,v146 dst_sel:DWORD dst_unused:UNUSED_PAD src0_sel:DWORD src1_sel:WORD_1
v_mfma_f32_16x16x32_bf16 v[160:163],v[88:91],v[128:131],v[160:163]
ds_read_b64_tr_b16 v[108:109],v6 offset:6296
ds_read_b64_tr_b16 v[110:111],v6 offset:7832
v_lshlrev_b32_sdwa v145,1,v145 dst_sel:DWORD dst_unused:UNUSED_PAD src0_sel:DWORD src1_sel:WORD_1
v_lshlrev_b32_sdwa v147,1,v147 dst_sel:DWORD dst_unused:UNUSED_PAD src0_sel:DWORD src1_sel:WORD_1
v_mfma_f32_16x16x32_bf16 v[168:171],v[92:95],v[128:131],v[168:171]
s_waitcnt lgkmcnt(10)
ds_read_b64_tr_b16 v[64:65],v4 offset:512
ds_read_b64_tr_b16 v[66:67],v4 offset:2560
v_fma_mixlo_f16 v120,v156,s99,0
v_mfma_f32_16x16x32_bf16 v[176:179],v[96:99],v[128:131],v[176:179]
s_waitcnt lgkmcnt(9)
ds_read_b128 v[112:115],v7 offset:896
ds_read_u16 v144,v144
v_fma_mixlo_f16 v122,v157,s99,0
v_lshlrev_b32_sdwa v148,1,v148 dst_sel:DWORD dst_unused:UNUSED_PAD src0_sel:DWORD src1_sel:WORD_1
v_lshlrev_b32_sdwa v150,1,v150 dst_sel:DWORD dst_unused:UNUSED_PAD src0_sel:DWORD src1_sel:WORD_1
v_mfma_f32_16x16x32_bf16 v[184:187],v[100:103],v[128:131],v[184:187]
s_waitcnt lgkmcnt(6)
s_setprio 1
ds_read_b64_tr_b16 v[68:69],v5 offset:512
v_lshl_add_u64 v[136:137],v[136:137],0,v[138:139]
v_mfma_f32_16x16x32_bf16 v[192:195],v[104:107],v[128:131],v[192:195]
s_waitcnt lgkmcnt(5)
ds_read_u16_d16_hi v146,v146
v_cvt_pk_bf16_f32 v138,v140,v142
v_cvt_pk_bf16_f32 v139,v141,v143
v_mfma_f32_16x16x32_bf16 v[200:203],v[108:111],v[128:131],v[200:203]
ds_read_b64_tr_b16 v[70:71],v5 offset:2560
ds_read_b128 v[116:119],v7 offset:928
v_fma_mixhi_f16 v120,v158,s99,0
v_fma_mixhi_f16 v122,v159,s99,0
v_mfma_f32_16x16x32_bf16 v[164:167],v[88:91],v[136:139],v[164:167]
v_lshlrev_b32_sdwa v149,1,v149 dst_sel:DWORD dst_unused:UNUSED_PAD src0_sel:DWORD src1_sel:WORD_1
v_lshlrev_b32_sdwa v151,1,v151 dst_sel:DWORD dst_unused:UNUSED_PAD src0_sel:DWORD src1_sel:WORD_1
v_mfma_f32_16x16x32_bf16 v[172:175],v[92:95],v[136:139],v[172:175]
ds_read_b64_tr_b16 v[72:73],v4 offset:4608
ds_read_b64_tr_b16 v[74:75],v4 offset:6656
v_pk_fma_f16 v123,v122,v122,s54
v_pk_fma_f16 v121,v120,v120,s54
v_mfma_f32_16x16x32_bf16 v[180:183],v[96:99],v[136:139],v[180:183]
ds_read_u16 v145,v145
v_pk_mul_f16 v121,v120,v121
v_pk_mul_f16 v123,v122,v123
v_mfma_f32_16x16x32_bf16 v[188:191],v[100:103],v[136:139],v[188:191]
ds_read_b64_tr_b16 v[76:77],v5 offset:4608
v_lshlrev_b32_sdwa v152,1,v152 dst_sel:DWORD dst_unused:UNUSED_PAD src0_sel:DWORD src1_sel:WORD_1
v_lshlrev_b32_sdwa v154,1,v154 dst_sel:DWORD dst_unused:UNUSED_PAD src0_sel:DWORD src1_sel:WORD_1
v_mfma_f32_16x16x32_bf16 v[196:199],v[104:107],v[136:139],v[196:199]
ds_read_u16_d16_hi v147,v147
v_exp_f16 v120,v121
v_mfma_f32_16x16x32_bf16 v[204:207],v[108:111],v[136:139],v[204:207]
s_waitcnt lgkmcnt(10)
ds_read_b64_tr_b16 v[78:79],v5 offset:6656
v_exp_f16 v122,v123
v_mfma_f32_16x16x32_bf16 v[128:131],v[64:67],v[16:19],v[112:115]
ds_read_u16 v148,v148
ds_read_u16_d16_hi v150,v150
v_lshlrev_b32_sdwa v153,1,v153 dst_sel:DWORD dst_unused:UNUSED_PAD src0_sel:DWORD src1_sel:WORD_1
v_lshlrev_b32_sdwa v155,1,v155 dst_sel:DWORD dst_unused:UNUSED_PAD src0_sel:DWORD src1_sel:WORD_1
v_exp_f16_sdwa v122,v123 dst_sel:WORD_1 dst_unused:UNUSED_PRESERVE src0_sel:WORD_1
v_mfma_f32_16x16x32_bf16 v[136:139],v[64:67],v[20:23],v[112:115]
s_waitcnt lgkmcnt(8)
s_setprio 0
v_exp_f16_sdwa v120,v121 dst_sel:WORD_1 dst_unused:UNUSED_PRESERVE src0_sel:WORD_1
v_mfma_f32_16x16x32_bf16 v[140:143],v[68:71],v[20:23],v[116:119]
ds_read_b64_tr_b16 v[80:81],v4 offset:8704
ds_read_b64_tr_b16 v[82:83],v4 offset:10752
v_pk_fma_f16 v120,v120,s55,s55
v_mfma_f32_16x16x32_bf16 v[132:135],v[68:71],v[16:19],v[116:119]
s_waitcnt lgkmcnt(8)
ds_read_u16 v149,v149
v_pk_fma_f16 v122,v122,s55,s55
v_mfma_f32_16x16x32_bf16 v[128:131],v[72:75],v[24:27],v[128:131]
ds_read_b64_tr_b16 v[84:85],v5 offset:8704
ds_read_u16_d16_hi v151,v151
v_rcp_f16 v121,v120
v_mfma_f32_16x16x32_bf16 v[136:139],v[72:75],v[28:31],v[136:139]
s_waitcnt lgkmcnt(7)
ds_read_b64_tr_b16 v[86:87],v5 offset:10752
v_rcp_f16 v123,v122
v_mfma_f32_16x16x32_bf16 v[140:143],v[76:79],v[28:31],v[140:143]
ds_read_u16 v152,v152
v_rcp_f16_sdwa v121,v120 dst_sel:WORD_1 dst_unused:UNUSED_PRESERVE src0_sel:WORD_1
v_mfma_f32_16x16x32_bf16 v[132:135],v[76:79],v[24:27],v[132:135]
s_waitcnt lgkmcnt(5)
v_rcp_f16_sdwa v123,v122 dst_sel:WORD_1 dst_unused:UNUSED_PRESERVE src0_sel:WORD_1
v_mfma_f32_16x16x32_bf16 v[128:131],v[80:83],v[32:35],v[128:131]
ds_read_u16_d16_hi v154,v154
v_fma_mix_f32 v156,v156,v121,0 op_sel_hi:[0,1,0]
v_mfma_f32_16x16x32_bf16 v[136:139],v[80:83],v[36:39],v[136:139]
s_waitcnt lgkmcnt(2)
v_lshl_add_u64 v[144:145],v[144:145],0,v[146:147]
v_fma_mix_f32 v158,v158,v121,0 op_sel:[0,1,0] op_sel_hi:[0,1,0]
v_lshl_add_u64 v[146:147],v[148:149],0,v[150:151]
v_mfma_f32_16x16x32_bf16 v[140:143],v[84:87],v[36:39],v[140:143]
ds_read_u16 v153,v153
v_fma_mix_f32 v157,v157,v123,0 op_sel_hi:[0,1,0]
v_fma_mix_f32 v159,v159,v123,0 op_sel:[0,1,0] op_sel_hi:[0,1,0]
v_mfma_f32_16x16x32_bf16 v[132:135],v[84:87],v[32:35],v[132:135]
v_lshlrev_b32_sdwa v128,1,v128 dst_sel:DWORD dst_unused:UNUSED_PAD src0_sel:DWORD src1_sel:WORD_1
v_lshlrev_b32_sdwa v130,1,v130 dst_sel:DWORD dst_unused:UNUSED_PAD src0_sel:DWORD src1_sel:WORD_1
v_mfma_f32_16x16x32_bf16 v[208:211],v[88:91],v[144:147],v[208:211]
s_barrier
s_setprio 2
v_lshlrev_b32_sdwa v129,1,v129 dst_sel:DWORD dst_unused:UNUSED_PAD src0_sel:DWORD src1_sel:WORD_1
v_lshlrev_b32_sdwa v131,1,v131 dst_sel:DWORD dst_unused:UNUSED_PAD src0_sel:DWORD src1_sel:WORD_1
v_mfma_f32_16x16x32_bf16 v[216:219],v[92:95],v[144:147],v[216:219]
s_cbranch_vccnz RA3_%=
s_mov_b64 exec,s[62:63]
s_add_u32 m0,s57,55808
s_add_u32 s64,s56,3538944
buffer_load_dwordx4 v2,s[36:39],s64 offen lds
s_add_u32 m0,s57,57536
s_nop 0
buffer_load_dwordx4 v3,s[36:39],s64 offen lds
s_mov_b64 exec,-1
RA3_%=:
ds_read_u16_d16_hi v155,v155
v_mfma_f32_16x16x32_bf16 v[224:227],v[96:99],v[144:147],v[224:227]
v_lshlrev_b32_sdwa v132,1,v132 dst_sel:DWORD dst_unused:UNUSED_PAD src0_sel:DWORD src1_sel:WORD_1
v_mfma_f32_16x16x32_bf16 v[232:235],v[100:103],v[144:147],v[232:235]
s_waitcnt lgkmcnt(0)
ds_read_u16 v128,v128
v_lshl_add_u64 v[152:153],v[152:153],0,v[154:155]
v_lshlrev_b32_sdwa v134,1,v134 dst_sel:DWORD dst_unused:UNUSED_PAD src0_sel:DWORD src1_sel:WORD_1
v_cvt_pk_bf16_f32 v154,v156,v158
v_cvt_pk_bf16_f32 v155,v157,v159
v_mfma_f32_16x16x32_bf16 v[240:243],v[104:107],v[144:147],v[240:243]
ds_read_u16_d16_hi v130,v130
v_mfma_f32_16x16x32_bf16 v[248:251],v[108:111],v[144:147],v[248:251]
ds_read_u16 v129,v129
v_lshlrev_b32_sdwa v133,1,v133 dst_sel:DWORD dst_unused:UNUSED_PAD src0_sel:DWORD src1_sel:WORD_1
v_mfma_f32_16x16x32_bf16 v[212:215],v[88:91],v[152:155],v[212:215]
v_lshlrev_b32_sdwa v135,1,v135 dst_sel:DWORD dst_unused:UNUSED_PAD src0_sel:DWORD src1_sel:WORD_1
v_mfma_f32_16x16x32_bf16 v[220:223],v[92:95],v[152:155],v[220:223]
ds_read_u16_d16_hi v131,v131
v_lshlrev_b32_sdwa v136,1,v136 dst_sel:DWORD dst_unused:UNUSED_PAD src0_sel:DWORD src1_sel:WORD_1
v_mfma_f32_16x16x32_bf16 v[228:231],v[96:99],v[152:155],v[228:231]
ds_read_b64_tr_b16 v[88:89],v6 offset:12320
ds_read_b64_tr_b16 v[90:91],v6 offset:13856
v_lshlrev_b32_sdwa v138,1,v138 dst_sel:DWORD dst_unused:UNUSED_PAD src0_sel:DWORD src1_sel:WORD_1
v_mfma_f32_16x16x32_bf16 v[236:239],v[100:103],v[152:155],v[236:239]
ds_read_u16 v132,v132
v_lshlrev_b32_sdwa v137,1,v137 dst_sel:DWORD dst_unused:UNUSED_PAD src0_sel:DWORD src1_sel:WORD_1
v_mfma_f32_16x16x32_bf16 v[244:247],v[104:107],v[152:155],v[244:247]
ds_read_u16_d16_hi v134,v134
v_lshlrev_b32_sdwa v139,1,v139 dst_sel:DWORD dst_unused:UNUSED_PAD src0_sel:DWORD src1_sel:WORD_1
v_mfma_f32_16x16x32_bf16 v[252:255],v[108:111],v[152:155],v[252:255]
ds_read_b64_tr_b16 v[92:93],v6 offset:12328
ds_read_b64_tr_b16 v[94:95],v6 offset:13864
v_lshlrev_b32_sdwa v140,1,v140 dst_sel:DWORD dst_unused:UNUSED_PAD src0_sel:DWORD src1_sel:WORD_1
v_mfma_f32_16x16x32_bf16 v[144:147],v[64:67],v[40:43],v[112:115]
ds_read_u16 v133,v133
v_lshlrev_b32_sdwa v142,1,v142 dst_sel:DWORD dst_unused:UNUSED_PAD src0_sel:DWORD src1_sel:WORD_1
v_mfma_f32_16x16x32_bf16 v[152:155],v[64:67],v[44:47],v[112:115]
ds_read_b64_tr_b16 v[96:97],v6 offset:12384
ds_read_b64_tr_b16 v[98:99],v6 offset:13920
v_lshlrev_b32_sdwa v141,1,v141 dst_sel:DWORD dst_unused:UNUSED_PAD src0_sel:DWORD src1_sel:WORD_1
v_lshlrev_b32_sdwa v143,1,v143 dst_sel:DWORD dst_unused:UNUSED_PAD src0_sel:DWORD src1_sel:WORD_1
v_mfma_f32_16x16x32_bf16 v[156:159],v[68:71],v[44:47],v[116:119]
ds_read_u16_d16_hi v135,v135
v_mfma_f32_16x16x32_bf16 v[148:151],v[68:71],v[40:43],v[116:119]
ds_read_u16 v136,v136
v_mfma_f32_16x16x32_bf16 v[144:147],v[72:75],v[48:51],v[144:147]
ds_read_u16_d16_hi v138,v138
v_mfma_f32_16x16x32_bf16 v[152:155],v[72:75],v[52:55],v[152:155]
ds_read_b64_tr_b16 v[100:101],v6 offset:12392
ds_read_b64_tr_b16 v[102:103],v6 offset:13928
v_mfma_f32_16x16x32_bf16 v[156:159],v[76:79],v[52:55],v[156:159]
ds_read_u16 v137,v137
v_mfma_f32_16x16x32_bf16 v[148:151],v[76:79],v[48:51],v[148:151]
s_waitcnt lgkmcnt(5)
ds_read_u16_d16_hi v139,v139
v_lshl_add_u64 v[128:129],v[128:129],0,v[130:131]
v_lshl_add_u64 v[130:131],v[132:133],0,v[134:135]
v_mfma_f32_16x16x32_bf16 v[144:147],v[80:83],v[56:59],v[144:147]
ds_read_b64_tr_b16 v[104:105],v6 offset:12448
ds_read_b64_tr_b16 v[106:107],v6 offset:13984
v_mfma_f32_16x16x32_bf16 v[152:155],v[80:83],v[60:63],v[152:155]
s_cbranch_vccnz RA4_%=
s_mov_b64 exec,s[62:63]
s_add_u32 m0,s57,59296
s_add_u32 s64,s56,3637248
buffer_load_dwordx4 v2,s[36:39],s64 offen lds
s_add_u32 m0,s57,61024
s_nop 0
buffer_load_dwordx4 v3,s[36:39],s64 offen lds
s_mov_b64 exec,-1
RA4_%=:
ds_read_u16 v140,v140
v_mfma_f32_16x16x32_bf16 v[156:159],v[84:87],v[60:63],v[156:159]
ds_read_u16_d16_hi v142,v142
v_mfma_f32_16x16x32_bf16 v[148:151],v[84:87],v[56:59],v[148:151]
ds_read_b64_tr_b16 v[108:109],v6 offset:12456
ds_read_b64_tr_b16 v[110:111],v6 offset:13992
v_lshlrev_b32_sdwa v144,1,v144 dst_sel:DWORD dst_unused:UNUSED_PAD src0_sel:DWORD src1_sel:WORD_1
v_lshlrev_b32_sdwa v146,1,v146 dst_sel:DWORD dst_unused:UNUSED_PAD src0_sel:DWORD src1_sel:WORD_1
v_mfma_f32_16x16x32_bf16 v[160:163],v[88:91],v[128:131],v[160:163]
ds_read_b64_tr_b16 v[64:65],v4 offset:12320
ds_read_u16 v141,v141
v_lshlrev_b32_sdwa v145,1,v145 dst_sel:DWORD dst_unused:UNUSED_PAD src0_sel:DWORD src1_sel:WORD_1
v_mfma_f32_16x16x32_bf16 v[168:171],v[92:95],v[128:131],v[168:171]
ds_read_u16_d16_hi v143,v143
v_lshlrev_b32_sdwa v147,1,v147 dst_sel:DWORD dst_unused:UNUSED_PAD src0_sel:DWORD src1_sel:WORD_1
v_mfma_f32_16x16x32_bf16 v[176:179],v[96:99],v[128:131],v[176:179]
s_waitcnt lgkmcnt(11)
ds_read_b64_tr_b16 v[66:67],v4 offset:14368
v_lshlrev_b32_sdwa v148,1,v148 dst_sel:DWORD dst_unused:UNUSED_PAD src0_sel:DWORD src1_sel:WORD_1
v_mfma_f32_16x16x32_bf16 v[184:187],v[100:103],v[128:131],v[184:187]
s_waitcnt lgkmcnt(8)
ds_read_u16 v144,v144
v_lshl_add_u64 v[136:137],v[136:137],0,v[138:139]
v_lshlrev_b32_sdwa v150,1,v150 dst_sel:DWORD dst_unused:UNUSED_PAD src0_sel:DWORD src1_sel:WORD_1
v_mfma_f32_16x16x32_bf16 v[192:195],v[104:107],v[128:131],v[192:195]
s_waitcnt lgkmcnt(2)
ds_read_u16_d16_hi v146,v146
v_lshl_add_u64 v[138:139],v[140:141],0,v[142:143]
v_mfma_f32_16x16x32_bf16 v[200:203],v[108:111],v[128:131],v[200:203]
ds_read_b128 v[112:115],v7 offset:1024
v_lshlrev_b32_sdwa v149,1,v149 dst_sel:DWORD dst_unused:UNUSED_PAD src0_sel:DWORD src1_sel:WORD_1
v_mfma_f32_16x16x32_bf16 v[164:167],v[88:91],v[136:139],v[164:167]
ds_read_u16 v145,v145
v_lshlrev_b32_sdwa v151,1,v151 dst_sel:DWORD dst_unused:UNUSED_PAD src0_sel:DWORD src1_sel:WORD_1
v_mfma_f32_16x16x32_bf16 v[172:175],v[92:95],v[136:139],v[172:175]
ds_read_u16_d16_hi v147,v147
ds_read_b64_tr_b16 v[68:69],v5 offset:12320
v_lshlrev_b32_sdwa v152,1,v152 dst_sel:DWORD dst_unused:UNUSED_PAD src0_sel:DWORD src1_sel:WORD_1
v_mfma_f32_16x16x32_bf16 v[180:183],v[96:99],v[136:139],v[180:183]
ds_read_b64_tr_b16 v[70:71],v5 offset:14368
ds_read_b128 v[116:119],v7 offset:1056
v_lshlrev_b32_sdwa v154,1,v154 dst_sel:DWORD dst_unused:UNUSED_PAD src0_sel:DWORD src1_sel:WORD_1
v_mfma_f32_16x16x32_bf16 v[188:191],v[100:103],v[136:139],v[188:191]
ds_read_u16 v148,v148
ds_read_b64_tr_b16 v[72:73],v4 offset:16416
v_lshlrev_b32_sdwa v153,1,v153 dst_sel:DWORD dst_unused:UNUSED_PAD src0_sel:DWORD src1_sel:WORD_1
v_mfma_f32_16x16x32_bf16 v[196:199],v[104:107],v[136:139],v[196:199]
ds_read_u16_d16_hi v150,v150
v_lshlrev_b32_sdwa v155,1,v155 dst_sel:DWORD dst_unused:UNUSED_PAD src0_sel:DWORD src1_sel:WORD_1
v_mfma_f32_16x16x32_bf16 v[204:207],v[108:111],v[136:139],v[204:207]
s_waitcnt lgkmcnt(8)
ds_read_b64_tr_b16 v[74:75],v4 offset:18464
ds_read_b64_tr_b16 v[76:77],v5 offset:16416
v_mfma_f32_16x16x32_bf16 v[128:131],v[64:67],v[16:19],v[112:115]
ds_read_u16 v149,v149
v_lshlrev_b32_sdwa v156,1,v156 dst_sel:DWORD dst_unused:UNUSED_PAD src0_sel:DWORD src1_sel:WORD_1
v_lshlrev_b32_sdwa v158,1,v158 dst_sel:DWORD dst_unused:UNUSED_PAD src0_sel:DWORD src1_sel:WORD_1
v_mfma_f32_16x16x32_bf16 v[136:139],v[64:67],v[20:23],v[112:115]
s_waitcnt lgkmcnt(6)
ds_read_b64_tr_b16 v[78:79],v5 offset:18464
ds_read_b64_tr_b16 v[80:81],v4 offset:20512
v_lshlrev_b32_sdwa v157,1,v157 dst_sel:DWORD dst_unused:UNUSED_PAD src0_sel:DWORD src1_sel:WORD_1
v_mfma_f32_16x16x32_bf16 v[140:143],v[68:71],v[20:23],v[116:119]
ds_read_u16_d16_hi v151,v151
v_lshlrev_b32_sdwa v159,1,v159 dst_sel:DWORD dst_unused:UNUSED_PAD src0_sel:DWORD src1_sel:WORD_1
v_mfma_f32_16x16x32_bf16 v[132:135],v[68:71],v[16:19],v[116:119]
s_waitcnt lgkmcnt(5)
ds_read_b64_tr_b16 v[82:83],v4 offset:22560
ds_read_u16 v152,v152
v_mfma_f32_16x16x32_bf16 v[128:131],v[72:75],v[24:27],v[128:131]
s_cbranch_vccnz RA5_%=
s_mov_b64 exec,s[62:63]
s_add_u32 m0,s57,62784
s_add_u32 s64,s56,3735552
buffer_load_dwordx4 v2,s[36:39],s64 offen lds
s_add_u32 m0,s57,64512
s_nop 0
buffer_load_dwordx4 v3,s[36:39],s64 offen lds
s_mov_b64 exec,-1
RA5_%=:
ds_read_b64_tr_b16 v[84:85],v5 offset:20512
ds_read_b64_tr_b16 v[86:87],v5 offset:22560
v_mfma_f32_16x16x32_bf16 v[136:139],v[72:75],v[28:31],v[136:139]
s_waitcnt lgkmcnt(6)
ds_read_u16_d16_hi v154,v154
v_mfma_f32_16x16x32_bf16 v[140:143],v[76:79],v[28:31],v[140:143]
ds_read_u16 v153,v153
v_mfma_f32_16x16x32_bf16 v[132:135],v[76:79],v[24:27],v[132:135]
s_waitcnt lgkmcnt(5)
ds_read_u16_d16_hi v155,v155
v_lshl_add_u64 v[144:145],v[144:145],0,v[146:147]
v_mfma_f32_16x16x32_bf16 v[128:131],v[80:83],v[32:35],v[128:131]
v_lshl_add_u64 v[146:147],v[148:149],0,v[150:151]
v_mfma_f32_16x16x32_bf16 v[136:139],v[80:83],v[36:39],v[136:139]
s_waitcnt lgkmcnt(3)
ds_read_u16 v156,v156
v_mfma_f32_16x16x32_bf16 v[140:143],v[84:87],v[36:39],v[140:143]
ds_read_u16_d16_hi v158,v158
v_mfma_f32_16x16x32_bf16 v[132:135],v[84:87],v[32:35],v[132:135]
ds_read_u16 v157,v157
v_lshlrev_b32_sdwa v128,1,v128 dst_sel:DWORD dst_unused:UNUSED_PAD src0_sel:DWORD src1_sel:WORD_1
v_lshlrev_b32_sdwa v130,1,v130 dst_sel:DWORD dst_unused:UNUSED_PAD src0_sel:DWORD src1_sel:WORD_1
v_mfma_f32_16x16x32_bf16 v[208:211],v[88:91],v[144:147],v[208:211]
v_lshlrev_b32_sdwa v129,1,v129 dst_sel:DWORD dst_unused:UNUSED_PAD src0_sel:DWORD src1_sel:WORD_1
v_lshlrev_b32_sdwa v131,1,v131 dst_sel:DWORD dst_unused:UNUSED_PAD src0_sel:DWORD src1_sel:WORD_1
v_mfma_f32_16x16x32_bf16 v[216:219],v[92:95],v[144:147],v[216:219]
ds_read_u16_d16_hi v159,v159
v_mfma_f32_16x16x32_bf16 v[224:227],v[96:99],v[144:147],v[224:227]
v_lshlrev_b32_sdwa v132,1,v132 dst_sel:DWORD dst_unused:UNUSED_PAD src0_sel:DWORD src1_sel:WORD_1
v_mfma_f32_16x16x32_bf16 v[232:235],v[100:103],v[144:147],v[232:235]
s_waitcnt lgkmcnt(4)
ds_read_u16 v128,v128
v_lshl_add_u64 v[152:153],v[152:153],0,v[154:155]
v_lshlrev_b32_sdwa v134,1,v134 dst_sel:DWORD dst_unused:UNUSED_PAD src0_sel:DWORD src1_sel:WORD_1
v_mfma_f32_16x16x32_bf16 v[240:243],v[104:107],v[144:147],v[240:243]
s_waitcnt lgkmcnt(1)
ds_read_u16_d16_hi v130,v130
v_lshl_add_u64 v[154:155],v[156:157],0,v[158:159]
v_mfma_f32_16x16x32_bf16 v[248:251],v[108:111],v[144:147],v[248:251]
ds_read_u16 v129,v129
v_lshlrev_b32_sdwa v133,1,v133 dst_sel:DWORD dst_unused:UNUSED_PAD src0_sel:DWORD src1_sel:WORD_1
v_mfma_f32_16x16x32_bf16 v[212:215],v[88:91],v[152:155],v[212:215]
v_lshlrev_b32_sdwa v135,1,v135 dst_sel:DWORD dst_unused:UNUSED_PAD src0_sel:DWORD src1_sel:WORD_1
v_mfma_f32_16x16x32_bf16 v[220:223],v[92:95],v[152:155],v[220:223]
ds_read_u16_d16_hi v131,v131
v_lshlrev_b32_sdwa v136,1,v136 dst_sel:DWORD dst_unused:UNUSED_PAD src0_sel:DWORD src1_sel:WORD_1
v_mfma_f32_16x16x32_bf16 v[228:231],v[96:99],v[152:155],v[228:231]
ds_read_b64_tr_b16 v[88:89],v6 offset:18480
ds_read_b64_tr_b16 v[90:91],v6 offset:20016
v_lshlrev_b32_sdwa v138,1,v138 dst_sel:DWORD dst_unused:UNUSED_PAD src0_sel:DWORD src1_sel:WORD_1
v_mfma_f32_16x16x32_bf16 v[236:239],v[100:103],v[152:155],v[236:239]
ds_read_u16 v132,v132
v_lshlrev_b32_sdwa v137,1,v137 dst_sel:DWORD dst_unused:UNUSED_PAD src0_sel:DWORD src1_sel:WORD_1
v_mfma_f32_16x16x32_bf16 v[244:247],v[104:107],v[152:155],v[244:247]
ds_read_u16_d16_hi v134,v134
v_lshlrev_b32_sdwa v139,1,v139 dst_sel:DWORD dst_unused:UNUSED_PAD src0_sel:DWORD src1_sel:WORD_1
v_mfma_f32_16x16x32_bf16 v[252:255],v[108:111],v[152:155],v[252:255]
ds_read_b64_tr_b16 v[92:93],v6 offset:18488
ds_read_b64_tr_b16 v[94:95],v6 offset:20024
v_lshlrev_b32_sdwa v140,1,v140 dst_sel:DWORD dst_unused:UNUSED_PAD src0_sel:DWORD src1_sel:WORD_1
v_mfma_f32_16x16x32_bf16 v[144:147],v[64:67],v[40:43],v[112:115]
s_cbranch_vccnz RA6_%=
s_mov_b64 exec,s[62:63]
s_add_u32 m0,s57,66272
s_add_u32 s64,s56,3833856
buffer_load_dwordx4 v2,s[36:39],s64 offen lds
s_add_u32 m0,s57,68000
s_nop 0
buffer_load_dwordx4 v3,s[36:39],s64 offen lds
s_mov_b64 exec,-1
RA6_%=:
ds_read_u16 v133,v133
v_lshlrev_b32_sdwa v142,1,v142 dst_sel:DWORD dst_unused:UNUSED_PAD src0_sel:DWORD src1_sel:WORD_1
v_mfma_f32_16x16x32_bf16 v[152:155],v[64:67],v[44:47],v[112:115]
ds_read_b64_tr_b16 v[96:97],v6 offset:18544
ds_read_b64_tr_b16 v[98:99],v6 offset:20080
v_lshlrev_b32_sdwa v141,1,v141 dst_sel:DWORD dst_unused:UNUSED_PAD src0_sel:DWORD src1_sel:WORD_1
v_lshlrev_b32_sdwa v143,1,v143 dst_sel:DWORD dst_unused:UNUSED_PAD src0_sel:DWORD src1_sel:WORD_1
v_mfma_f32_16x16x32_bf16 v[156:159],v[68:71],v[44:47],v[116:119]
ds_read_u16_d16_hi v135,v135
v_mfma_f32_16x16x32_bf16 v[148:151],v[68:71],v[40:43],v[116:119]
ds_read_u16 v136,v136
v_mfma_f32_16x16x32_bf16 v[144:147],v[72:75],v[48:51],v[144:147]
ds_read_u16_d16_hi v138,v138
v_mfma_f32_16x16x32_bf16 v[152:155],v[72:75],v[52:55],v[152:155]
ds_read_b64_tr_b16 v[100:101],v6 offset:18552
ds_read_b64_tr_b16 v[102:103],v6 offset:20088
v_mfma_f32_16x16x32_bf16 v[156:159],v[76:79],v[52:55],v[156:159]
ds_read_u16 v137,v137
v_mfma_f32_16x16x32_bf16 v[148:151],v[76:79],v[48:51],v[148:151]
s_waitcnt lgkmcnt(5)
ds_read_u16_d16_hi v139,v139
v_lshl_add_u64 v[128:129],v[128:129],0,v[130:131]
v_lshl_add_u64 v[130:131],v[132:133],0,v[134:135]
v_mfma_f32_16x16x32_bf16 v[144:147],v[80:83],v[56:59],v[144:147]
ds_read_b64_tr_b16 v[104:105],v6 offset:18608
ds_read_b64_tr_b16 v[106:107],v6 offset:20144
v_mfma_f32_16x16x32_bf16 v[152:155],v[80:83],v[60:63],v[152:155]
ds_read_u16 v140,v140
v_mfma_f32_16x16x32_bf16 v[156:159],v[84:87],v[60:63],v[156:159]
ds_read_u16_d16_hi v142,v142
v_mfma_f32_16x16x32_bf16 v[148:151],v[84:87],v[56:59],v[148:151]
ds_read_b64_tr_b16 v[108:109],v6 offset:18616
ds_read_b64_tr_b16 v[110:111],v6 offset:20152
v_lshlrev_b32_sdwa v144,1,v144 dst_sel:DWORD dst_unused:UNUSED_PAD src0_sel:DWORD src1_sel:WORD_1
v_lshlrev_b32_sdwa v146,1,v146 dst_sel:DWORD dst_unused:UNUSED_PAD src0_sel:DWORD src1_sel:WORD_1
v_mfma_f32_16x16x32_bf16 v[160:163],v[88:91],v[128:131],v[160:163]
ds_read_b64_tr_b16 v[64:65],v4 offset:12832
ds_read_u16 v141,v141
v_lshlrev_b32_sdwa v145,1,v145 dst_sel:DWORD dst_unused:UNUSED_PAD src0_sel:DWORD src1_sel:WORD_1
v_mfma_f32_16x16x32_bf16 v[168:171],v[92:95],v[128:131],v[168:171]
ds_read_u16_d16_hi v143,v143
v_lshlrev_b32_sdwa v147,1,v147 dst_sel:DWORD dst_unused:UNUSED_PAD src0_sel:DWORD src1_sel:WORD_1
v_mfma_f32_16x16x32_bf16 v[176:179],v[96:99],v[128:131],v[176:179]
s_waitcnt lgkmcnt(11)
ds_read_b64_tr_b16 v[66:67],v4 offset:14880
v_lshlrev_b32_sdwa v148,1,v148 dst_sel:DWORD dst_unused:UNUSED_PAD src0_sel:DWORD src1_sel:WORD_1
v_mfma_f32_16x16x32_bf16 v[184:187],v[100:103],v[128:131],v[184:187]
s_waitcnt lgkmcnt(8)
ds_read_u16 v144,v144
v_lshl_add_u64 v[136:137],v[136:137],0,v[138:139]
v_lshlrev_b32_sdwa v150,1,v150 dst_sel:DWORD dst_unused:UNUSED_PAD src0_sel:DWORD src1_sel:WORD_1
v_mfma_f32_16x16x32_bf16 v[192:195],v[104:107],v[128:131],v[192:195]
s_waitcnt lgkmcnt(2)
ds_read_u16_d16_hi v146,v146
v_lshl_add_u64 v[138:139],v[140:141],0,v[142:143]
v_mfma_f32_16x16x32_bf16 v[200:203],v[108:111],v[128:131],v[200:203]
ds_read_b128 v[112:115],v7 offset:1152
v_lshlrev_b32_sdwa v149,1,v149 dst_sel:DWORD dst_unused:UNUSED_PAD src0_sel:DWORD src1_sel:WORD_1
v_mfma_f32_16x16x32_bf16 v[164:167],v[88:91],v[136:139],v[164:167]
ds_read_u16 v145,v145
v_lshlrev_b32_sdwa v151,1,v151 dst_sel:DWORD dst_unused:UNUSED_PAD src0_sel:DWORD src1_sel:WORD_1
v_mfma_f32_16x16x32_bf16 v[172:175],v[92:95],v[136:139],v[172:175]
ds_read_u16_d16_hi v147,v147
ds_read_b64_tr_b16 v[68:69],v5 offset:12832
v_lshlrev_b32_sdwa v152,1,v152 dst_sel:DWORD dst_unused:UNUSED_PAD src0_sel:DWORD src1_sel:WORD_1
v_mfma_f32_16x16x32_bf16 v[180:183],v[96:99],v[136:139],v[180:183]
ds_read_b64_tr_b16 v[70:71],v5 offset:14880
ds_read_b128 v[116:119],v7 offset:1184
v_lshlrev_b32_sdwa v154,1,v154 dst_sel:DWORD dst_unused:UNUSED_PAD src0_sel:DWORD src1_sel:WORD_1
v_mfma_f32_16x16x32_bf16 v[188:191],v[100:103],v[136:139],v[188:191]
s_cbranch_vccnz RA7_%=
s_mov_b64 exec,s[62:63]
s_add_u32 m0,s57,0
s_add_u32 s64,s56,3932160
buffer_load_dwordx4 v2,s[36:39],s64 offen lds
s_add_u32 m0,s57,1728
s_nop 0
buffer_load_dwordx4 v3,s[36:39],s64 offen lds
s_mov_b64 exec,-1
RA7_%=:
ds_read_u16 v148,v148
ds_read_b64_tr_b16 v[72:73],v4 offset:16928
v_lshlrev_b32_sdwa v153,1,v153 dst_sel:DWORD dst_unused:UNUSED_PAD src0_sel:DWORD src1_sel:WORD_1
v_mfma_f32_16x16x32_bf16 v[196:199],v[104:107],v[136:139],v[196:199]
ds_read_u16_d16_hi v150,v150
v_lshlrev_b32_sdwa v155,1,v155 dst_sel:DWORD dst_unused:UNUSED_PAD src0_sel:DWORD src1_sel:WORD_1
v_mfma_f32_16x16x32_bf16 v[204:207],v[108:111],v[136:139],v[204:207]
s_waitcnt lgkmcnt(8)
ds_read_b64_tr_b16 v[74:75],v4 offset:18976
ds_read_b64_tr_b16 v[76:77],v5 offset:16928
v_mfma_f32_16x16x32_bf16 v[128:131],v[64:67],v[16:19],v[112:115]
s_mov_b32 s86,0xbe801d5a
ds_read_u16 v149,v149
v_lshlrev_b32_sdwa v156,1,v156 dst_sel:DWORD dst_unused:UNUSED_PAD src0_sel:DWORD src1_sel:WORD_1
v_lshlrev_b32_sdwa v158,1,v158 dst_sel:DWORD dst_unused:UNUSED_PAD src0_sel:DWORD src1_sel:WORD_1
v_mfma_f32_16x16x32_bf16 v[136:139],v[64:67],v[20:23],v[112:115]
s_waitcnt lgkmcnt(6)
ds_read_b64_tr_b16 v[78:79],v5 offset:18976
ds_read_b64_tr_b16 v[80:81],v4 offset:21024
v_lshlrev_b32_sdwa v157,1,v157 dst_sel:DWORD dst_unused:UNUSED_PAD src0_sel:DWORD src1_sel:WORD_1
v_mfma_f32_16x16x32_bf16 v[140:143],v[68:71],v[20:23],v[116:119]
ds_read_u16_d16_hi v151,v151
v_lshlrev_b32_sdwa v159,1,v159 dst_sel:DWORD dst_unused:UNUSED_PAD src0_sel:DWORD src1_sel:WORD_1
v_mfma_f32_16x16x32_bf16 v[132:135],v[68:71],v[16:19],v[116:119]
s_waitcnt lgkmcnt(5)
ds_read_b64_tr_b16 v[82:83],v4 offset:23072
ds_read_u16 v152,v152
v_mfma_f32_16x16x32_bf16 v[128:131],v[72:75],v[24:27],v[128:131]
ds_read_b64_tr_b16 v[84:85],v5 offset:21024
ds_read_b64_tr_b16 v[86:87],v5 offset:23072
v_mfma_f32_16x16x32_bf16 v[136:139],v[72:75],v[28:31],v[136:139]
s_waitcnt lgkmcnt(6)
ds_read_u16_d16_hi v154,v154
v_mfma_f32_16x16x32_bf16 v[140:143],v[76:79],v[28:31],v[140:143]
ds_read_u16 v153,v153
v_mfma_f32_16x16x32_bf16 v[132:135],v[76:79],v[24:27],v[132:135]
s_waitcnt lgkmcnt(5)
ds_read_u16_d16_hi v155,v155
v_lshl_add_u64 v[144:145],v[144:145],0,v[146:147]
v_mfma_f32_16x16x32_bf16 v[128:131],v[80:83],v[32:35],v[128:131]
v_lshl_add_u64 v[146:147],v[148:149],0,v[150:151]
v_mfma_f32_16x16x32_bf16 v[136:139],v[80:83],v[36:39],v[136:139]
s_waitcnt lgkmcnt(3)
ds_read_u16 v156,v156
v_mfma_f32_16x16x32_bf16 v[140:143],v[84:87],v[36:39],v[140:143]
ds_read_u16_d16_hi v158,v158
v_mfma_f32_16x16x32_bf16 v[132:135],v[84:87],v[32:35],v[132:135]
ds_read_u16 v157,v157
v_lshlrev_b32_sdwa v128,1,v128 dst_sel:DWORD dst_unused:UNUSED_PAD src0_sel:DWORD src1_sel:WORD_1
v_lshlrev_b32_sdwa v130,1,v130 dst_sel:DWORD dst_unused:UNUSED_PAD src0_sel:DWORD src1_sel:WORD_1
v_mfma_f32_16x16x32_bf16 v[208:211],v[88:91],v[144:147],v[208:211]
s_barrier
s_setprio 3
v_lshlrev_b32_sdwa v129,1,v129 dst_sel:DWORD dst_unused:UNUSED_PAD src0_sel:DWORD src1_sel:WORD_1
v_lshlrev_b32_sdwa v131,1,v131 dst_sel:DWORD dst_unused:UNUSED_PAD src0_sel:DWORD src1_sel:WORD_1
v_mfma_f32_16x16x32_bf16 v[216:219],v[92:95],v[144:147],v[216:219]
ds_read_u16_d16_hi v159,v159
ds_read_u16 v128,v128
v_fma_mixlo_f16 v120,v140,s99,0
v_fma_mixlo_f16 v122,v141,s99,0
v_mfma_f32_16x16x32_bf16 v[224:227],v[96:99],v[144:147],v[224:227]
s_cbranch_vccz RA8_%=
s_add_u32 m0,s58,12320
s_mov_b32 s69,s60
buffer_load_dwordx4 v2,s[44:47],s69 offen lds
s_add_u32 m0,m0,1024
s_add_u32 s69,s69,s53
buffer_load_dwordx4 v3,s[44:47],s69 offen lds
s_add_u32 m0,m0,1024
s_add_u32 s69,s69,s53
buffer_load_dwordx4 v2,s[44:47],s69 offen lds
s_add_u32 m0,m0,s50
s_add_u32 s69,s69,s53
buffer_load_dwordx4 v3,s[44:47],s69 offen lds
RA8_%=:
ds_read_u16_d16_hi v130,v130
v_lshlrev_b32_sdwa v132,1,v132 dst_sel:DWORD dst_unused:UNUSED_PAD src0_sel:DWORD src1_sel:WORD_1
v_lshlrev_b32_sdwa v134,1,v134 dst_sel:DWORD dst_unused:UNUSED_PAD src0_sel:DWORD src1_sel:WORD_1
v_mfma_f32_16x16x32_bf16 v[232:235],v[100:103],v[144:147],v[232:235]
s_waitcnt lgkmcnt(6)
v_lshl_add_u64 v[152:153],v[152:153],0,v[154:155]
v_mfma_f32_16x16x32_bf16 v[240:243],v[104:107],v[144:147],v[240:243]
s_waitcnt lgkmcnt(2)
s_cbranch_vccnz RA9_%=
s_mov_b64 exec,s[62:63]
s_add_u32 m0,s57,3488
s_add_u32 s64,s56,4030464
buffer_load_dwordx4 v2,s[36:39],s64 offen lds
s_add_u32 m0,s57,5216
s_nop 0
buffer_load_dwordx4 v3,s[36:39],s64 offen lds
s_mov_b64 exec,-1
RA9_%=:
v_lshl_add_u64 v[154:155],v[156:157],0,v[158:159]
v_fma_mixhi_f16 v120,v142,s99,0
v_mfma_f32_16x16x32_bf16 v[248:251],v[108:111],v[144:147],v[248:251]
ds_read_u16 v129,v129
v_fma_mixhi_f16 v122,v143,s99,0
v_mfma_f32_16x16x32_bf16 v[212:215],v[88:91],v[152:155],v[212:215]
s_cbranch_vccz RA10_%=
s_add_u32 m0,m0,1024
s_add_u32 s69,s69,s53
buffer_load_dwordx4 v2,s[44:47],s69 offen lds
s_add_u32 m0,m0,1024
s_add_u32 s69,s69,s53
buffer_load_dwordx4 v3,s[44:47],s69 offen lds
s_add_u32 m0,m0,s50
s_add_u32 s69,s69,s51
buffer_load_dwordx4 v2,s[44:47],s69 offen lds
s_add_u32 m0,m0,1024
s_add_u32 s69,s69,s53
buffer_load_dwordx4 v3,s[44:47],s69 offen lds
RA10_%=:
v_lshlrev_b32_sdwa v133,1,v133 dst_sel:DWORD dst_unused:UNUSED_PAD src0_sel:DWORD src1_sel:WORD_1
v_pk_fma_f16 v121,v120,v120,s54
v_lshlrev_b32_sdwa v135,1,v135 dst_sel:DWORD dst_unused:UNUSED_PAD src0_sel:DWORD src1_sel:WORD_1
v_mfma_f32_16x16x32_bf16 v[220:223],v[92:95],v[152:155],v[220:223]
ds_read_u16_d16_hi v131,v131
ds_read_u16 v132,v132
v_pk_fma_f16 v123,v122,v122,s54
v_mfma_f32_16x16x32_bf16 v[228:231],v[96:99],v[152:155],v[228:231]
ds_read_b64_tr_b16 v[88:89],v6 offset:0
v_pk_mul_f16 v121,v120,v121
v_lshlrev_b32_sdwa v136,1,v136 dst_sel:DWORD dst_unused:UNUSED_PAD src0_sel:DWORD src1_sel:WORD_1
v_pk_mul_f16 v123,v122,v123
v_mfma_f32_16x16x32_bf16 v[236:239],v[100:103],v[152:155],v[236:239]
s_cbranch_vccz RA11_%=
s_add_u32 m0,m0,1024
s_add_u32 s69,s69,s53
buffer_load_dwordx4 v2,s[44:47],s69 offen lds
s_add_u32 m0,m0,s50
s_add_u32 s69,s69,s53
buffer_load_dwordx4 v3,s[44:47],s69 offen lds
s_add_u32 m0,m0,1024
s_add_u32 s69,s69,s53
buffer_load_dwordx4 v2,s[44:47],s69 offen lds
s_add_u32 m0,m0,1024
s_add_u32 s69,s69,s53
buffer_load_dwordx4 v3,s[44:47],s69 offen lds
RA11_%=:
ds_read_b64_tr_b16 v[90:91],v6 offset:1536
v_lshlrev_b32_sdwa v138,1,v138 dst_sel:DWORD dst_unused:UNUSED_PAD src0_sel:DWORD src1_sel:WORD_1
v_mfma_f32_16x16x32_bf16 v[244:247],v[104:107],v[152:155],v[244:247]
ds_read_u16_d16_hi v134,v134
v_exp_f16 v120,v121
v_mfma_f32_16x16x32_bf16 v[252:255],v[108:111],v[152:155],v[252:255]
ds_read_b64_tr_b16 v[92:93],v6 offset:8
v_lshlrev_b32_sdwa v137,1,v137 dst_sel:DWORD dst_unused:UNUSED_PAD src0_sel:DWORD src1_sel:WORD_1
v_mfma_f32_16x16x32_bf16 v[144:147],v[64:67],v[40:43],v[112:115]
ds_read_b64_tr_b16 v[94:95],v6 offset:1544
v_lshlrev_b32_sdwa v139,1,v139 dst_sel:DWORD dst_unused:UNUSED_PAD src0_sel:DWORD src1_sel:WORD_1
v_exp_f16 v122,v123
v_mfma_f32_16x16x32_bf16 v[152:155],v[64:67],v[44:47],v[112:115]
ds_read_u16 v133,v133
v_mfma_f32_16x16x32_bf16 v[156:159],v[68:71],v[44:47],v[116:119]
ds_read_u16_d16_hi v135,v135
v_exp_f16_sdwa v122,v123 dst_sel:WORD_1 dst_unused:UNUSED_PRESERVE src0_sel:WORD_1
v_exp_f16_sdwa v120,v121 dst_sel:WORD_1 dst_unused:UNUSED_PRESERVE src0_sel:WORD_1
v_mfma_f32_16x16x32_bf16 v[148:151],v[68:71],v[40:43],v[116:119]
ds_read_b64_tr_b16 v[96:97],v6 offset:64
ds_read_b64_tr_b16 v[98:99],v6 offset:1600
v_pk_fma_f16 v120,v120,s55,s55
v_pk_fma_f16 v122,v122,s55,s55
v_mfma_f32_16x16x32_bf16 v[144:147],v[72:75],v[48:51],v[144:147]
v_rcp_f16 v121,v120
v_mfma_f32_16x16x32_bf16 v[152:155],v[72:75],v[52:55],v[152:155]
ds_read_b64_tr_b16 v[100:101],v6 offset:72
v_rcp_f16 v123,v122
v_mfma_f32_16x16x32_bf16 v[156:159],v[76:79],v[52:55],v[156:159]
ds_read_u16 v136,v136
v_rcp_f16_sdwa v121,v120 dst_sel:WORD_1 dst_unused:UNUSED_PRESERVE src0_sel:WORD_1
v_mfma_f32_16x16x32_bf16 v[148:151],v[76:79],v[48:51],v[148:151]
s_cbranch_vccnz RA12_%=
s_mov_b64 exec,s[62:63]
s_add_u32 m0,s57,6976
s_add_u32 s64,s56,4128768
buffer_load_dwordx4 v2,s[36:39],s64 offen lds
s_add_u32 m0,s57,8704
s_nop 0
buffer_load_dwordx4 v3,s[36:39],s64 offen lds
s_mov_b64 exec,-1
RA12_%=:
ds_read_b64_tr_b16 v[102:103],v6 offset:1608
ds_read_b64_tr_b16 v[104:105],v6 offset:128
v_rcp_f16_sdwa v123,v122 dst_sel:WORD_1 dst_unused:UNUSED_PRESERVE src0_sel:WORD_1
v_mfma_f32_16x16x32_bf16 v[144:147],v[80:83],v[56:59],v[144:147]
ds_read_u16_d16_hi v138,v138
v_fma_mix_f32 v142,v142,v121,0 op_sel:[0,1,0] op_sel_hi:[0,1,0]
v_mfma_f32_16x16x32_bf16 v[152:155],v[80:83],v[60:63],v[152:155]
s_waitcnt lgkmcnt(7)
ds_read_b64_tr_b16 v[106:107],v6 offset:1664
ds_read_b64_tr_b16 v[108:109],v6 offset:136
v_fma_mix_f32 v140,v140,v121,0 op_sel_hi:[0,1,0]
v_lshl_add_u64 v[128:129],v[128:129],0,v[130:131]
v_lshl_add_u64 v[130:131],v[132:133],0,v[134:135]
v_mfma_f32_16x16x32_bf16 v[156:159],v[84:87],v[60:63],v[156:159]
ds_read_u16 v137,v137
v_fma_mix_f32 v141,v141,v123,0 op_sel_hi:[0,1,0]
v_fma_mix_f32 v143,v143,v123,0 op_sel:[0,1,0] op_sel_hi:[0,1,0]
v_mfma_f32_16x16x32_bf16 v[148:151],v[84:87],v[56:59],v[148:151]
ds_read_b64_tr_b16 v[110:111],v6 offset:1672
ds_read_b64_tr_b16 v[64:65],v4 offset:0
v_lshlrev_b32_sdwa v144,1,v144 dst_sel:DWORD dst_unused:UNUSED_PAD src0_sel:DWORD src1_sel:WORD_1
v_lshlrev_b32_sdwa v146,1,v146 dst_sel:DWORD dst_unused:UNUSED_PAD src0_sel:DWORD src1_sel:WORD_1
v_mfma_f32_16x16x32_bf16 v[160:163],v[88:91],v[128:131],v[160:163]
ds_read_u16_d16_hi v139,v139
v_lshlrev_b32_sdwa v145,1,v145 dst_sel:DWORD dst_unused:UNUSED_PAD src0_sel:DWORD src1_sel:WORD_1
v_lshlrev_b32_sdwa v147,1,v147 dst_sel:DWORD dst_unused:UNUSED_PAD src0_sel:DWORD src1_sel:WORD_1
v_mfma_f32_16x16x32_bf16 v[168:171],v[92:95],v[128:131],v[168:171]
s_waitcnt lgkmcnt(11)
ds_read_b64_tr_b16 v[66:67],v4 offset:2048
ds_read_b128 v[112:115],v7 offset:1280
v_fma_mixlo_f16 v120,v156,s99,0
v_fma_mixlo_f16 v122,v157,s99,0
v_mfma_f32_16x16x32_bf16 v[176:179],v[96:99],v[128:131],v[176:179]
s_waitcnt lgkmcnt(10)
ds_read_u16 v144,v144
v_lshlrev_b32_sdwa v148,1,v148 dst_sel:DWORD dst_unused:UNUSED_PAD src0_sel:DWORD src1_sel:WORD_1
v_lshlrev_b32_sdwa v150,1,v150 dst_sel:DWORD dst_unused:UNUSED_PAD src0_sel:DWORD src1_sel:WORD_1
v_mfma_f32_16x16x32_bf16 v[184:187],v[100:103],v[128:131],v[184:187]
s_waitcnt lgkmcnt(3)
ds_read_b64_tr_b16 v[68:69],v5 offset:0
ds_read_b64_tr_b16 v[70:71],v5 offset:2048
v_lshl_add_u64 v[136:137],v[136:137],0,v[138:139]
v_fma_mixhi_f16 v120,v158,s99,0
v_mfma_f32_16x16x32_bf16 v[192:195],v[104:107],v[128:131],v[192:195]
ds_read_u16_d16_hi v146,v146
v_cvt_pk_bf16_f32 v138,v140,v142
v_cvt_pk_bf16_f32 v139,v141,v143
v_mfma_f32_16x16x32_bf16 v[200:203],v[108:111],v[128:131],v[200:203]
ds_read_b128 v[116:119],v7 offset:1312
ds_read_b64_tr_b16 v[72:73],v4 offset:4096
v_fma_mixhi_f16 v122,v159,s99,0
v_mfma_f32_16x16x32_bf16 v[164:167],v[88:91],v[136:139],v[164:167]
v_lshlrev_b32_sdwa v149,1,v149 dst_sel:DWORD dst_unused:UNUSED_PAD src0_sel:DWORD src1_sel:WORD_1
v_lshlrev_b32_sdwa v151,1,v151 dst_sel:DWORD dst_unused:UNUSED_PAD src0_sel:DWORD src1_sel:WORD_1
v_mfma_f32_16x16x32_bf16 v[172:175],v[92:95],v[136:139],v[172:175]
ds_read_b64_tr_b16 v[74:75],v4 offset:6144
ds_read_b64_tr_b16 v[76:77],v5 offset:4096
v_pk_fma_f16 v121,v120,v120,s54
v_pk_fma_f16 v123,v122,v122,s54
v_mfma_f32_16x16x32_bf16 v[180:183],v[96:99],v[136:139],v[180:183]
ds_read_u16 v145,v145
v_pk_mul_f16 v121,v120,v121
v_pk_mul_f16 v123,v122,v123
v_mfma_f32_16x16x32_bf16 v[188:191],v[100:103],v[136:139],v[188:191]
s_cbranch_vccnz RA13_%=
s_mov_b64 exec,s[62:63]
s_add_u32 m0,s57,10464
s_add_u32 s64,s56,4227072
buffer_load_dwordx4 v2,s[36:39],s64 offen lds
s_add_u32 m0,s57,12192
s_nop 0
buffer_load_dwordx4 v3,s[36:39],s64 offen lds
s_mov_b64 exec,-1
RA13_%=:
ds_read_u16_d16_hi v147,v147
v_lshlrev_b32_sdwa v152,1,v152 dst_sel:DWORD dst_unused:UNUSED_PAD src0_sel:DWORD src1_sel:WORD_1
v_lshlrev_b32_sdwa v154,1,v154 dst_sel:DWORD dst_unused:UNUSED_PAD src0_sel:DWORD src1_sel:WORD_1
v_mfma_f32_16x16x32_bf16 v[196:199],v[104:107],v[136:139],v[196:199]
v_exp_f16 v120,v121
v_mfma_f32_16x16x32_bf16 v[204:207],v[108:111],v[136:139],v[204:207]
s_waitcnt lgkmcnt(10)
ds_read_b64_tr_b16 v[78:79],v5 offset:6144
ds_read_b64_tr_b16 v[80:81],v4 offset:8192
v_exp_f16 v122,v123
v_mfma_f32_16x16x32_bf16 v[128:131],v[64:67],v[16:19],v[112:115]
v_lshlrev_b32_sdwa v153,1,v153 dst_sel:DWORD dst_unused:UNUSED_PAD src0_sel:DWORD src1_sel:WORD_1
v_lshlrev_b32_sdwa v155,1,v155 dst_sel:DWORD dst_unused:UNUSED_PAD src0_sel:DWORD src1_sel:WORD_1
v_mfma_f32_16x16x32_bf16 v[136:139],v[64:67],v[20:23],v[112:115]
s_waitcnt lgkmcnt(7)
ds_read_u16 v148,v148
ds_read_u16_d16_hi v150,v150
v_exp_f16_sdwa v120,v121 dst_sel:WORD_1 dst_unused:UNUSED_PRESERVE src0_sel:WORD_1
v_mfma_f32_16x16x32_bf16 v[140:143],v[68:71],v[20:23],v[116:119]
v_exp_f16_sdwa v122,v123 dst_sel:WORD_1 dst_unused:UNUSED_PRESERVE src0_sel:WORD_1
v_mfma_f32_16x16x32_bf16 v[132:135],v[68:71],v[16:19],v[116:119]
s_waitcnt lgkmcnt(7)
ds_read_b64_tr_b16 v[82:83],v4 offset:10240
v_pk_fma_f16 v120,v120,s55,s55
v_mfma_f32_16x16x32_bf16 v[128:131],v[72:75],v[24:27],v[128:131]
ds_read_b64_tr_b16 v[84:85],v5 offset:8192
v_pk_fma_f16 v122,v122,s55,s55
v_rcp_f16 v121,v120
v_mfma_f32_16x16x32_bf16 v[136:139],v[72:75],v[28:31],v[136:139]
s_waitcnt lgkmcnt(5)
ds_read_b64_tr_b16 v[86:87],v5 offset:10240
ds_read_u16 v149,v149
v_rcp_f16 v123,v122
v_mfma_f32_16x16x32_bf16 v[140:143],v[76:79],v[28:31],v[140:143]
ds_read_u16_d16_hi v151,v151
ds_read_u16 v152,v152
v_rcp_f16_sdwa v121,v120 dst_sel:WORD_1 dst_unused:UNUSED_PRESERVE src0_sel:WORD_1
v_mfma_f32_16x16x32_bf16 v[132:135],v[76:79],v[24:27],v[132:135]
s_waitcnt lgkmcnt(5)
v_rcp_f16_sdwa v123,v122 dst_sel:WORD_1 dst_unused:UNUSED_PRESERVE src0_sel:WORD_1
v_mfma_f32_16x16x32_bf16 v[128:131],v[80:83],v[32:35],v[128:131]
ds_read_u16_d16_hi v154,v154
v_fma_mix_f32 v156,v156,v121,0 op_sel_hi:[0,1,0]
v_fma_mix_f32 v158,v158,v121,0 op_sel:[0,1,0] op_sel_hi:[0,1,0]
v_mfma_f32_16x16x32_bf16 v[136:139],v[80:83],v[36:39],v[136:139]
s_waitcnt lgkmcnt(2)
v_lshl_add_u64 v[144:145],v[144:145],0,v[146:147]
v_lshl_add_u64 v[146:147],v[148:149],0,v[150:151]
v_mfma_f32_16x16x32_bf16 v[140:143],v[84:87],v[36:39],v[140:143]
ds_read_u16 v153,v153
v_fma_mix_f32 v157,v157,v123,0 op_sel_hi:[0,1,0]
v_fma_mix_f32 v159,v159,v123,0 op_sel:[0,1,0] op_sel_hi:[0,1,0]
v_mfma_f32_16x16x32_bf16 v[132:135],v[84:87],v[32:35],v[132:135]
v_lshlrev_b32_sdwa v128,1,v128 dst_sel:DWORD dst_unused:UNUSED_PAD src0_sel:DWORD src1_sel:WORD_1
v_lshlrev_b32_sdwa v130,1,v130 dst_sel:DWORD dst_unused:UNUSED_PAD src0_sel:DWORD src1_sel:WORD_1
v_mfma_f32_16x16x32_bf16 v[208:211],v[88:91],v[144:147],v[208:211]
s_cbranch_vccnz RA14_%=
s_mov_b64 exec,s[62:63]
s_add_u32 m0,s57,13952
s_add_u32 s64,s56,4325376
buffer_load_dwordx4 v2,s[36:39],s64 offen lds
s_add_u32 m0,s57,15680
s_nop 0
buffer_load_dwordx4 v3,s[36:39],s64 offen lds
s_mov_b64 exec,-1
RA14_%=:
v_lshlrev_b32_sdwa v129,1,v129 dst_sel:DWORD dst_unused:UNUSED_PAD src0_sel:DWORD src1_sel:WORD_1
v_lshlrev_b32_sdwa v131,1,v131 dst_sel:DWORD dst_unused:UNUSED_PAD src0_sel:DWORD src1_sel:WORD_1
v_mfma_f32_16x16x32_bf16 v[216:219],v[92:95],v[144:147],v[216:219]
ds_read_u16_d16_hi v155,v155
ds_read_u16 v128,v128
v_fma_mixlo_f16 v120,v140,s99,0
v_fma_mixlo_f16 v122,v141,s99,0
v_mfma_f32_16x16x32_bf16 v[224:227],v[96:99],v[144:147],v[224:227]
ds_read_u16_d16_hi v130,v130
v_lshlrev_b32_sdwa v132,1,v132 dst_sel:DWORD dst_unused:UNUSED_PAD src0_sel:DWORD src1_sel:WORD_1
v_lshlrev_b32_sdwa v134,1,v134 dst_sel:DWORD dst_unused:UNUSED_PAD src0_sel:DWORD src1_sel:WORD_1
v_mfma_f32_16x16x32_bf16 v[232:235],v[100:103],v[144:147],v[232:235]
s_waitcnt lgkmcnt(2)
v_lshl_add_u64 v[152:153],v[152:153],0,v[154:155]
v_cvt_pk_bf16_f32 v154,v156,v158
v_mfma_f32_16x16x32_bf16 v[240:243],v[104:107],v[144:147],v[240:243]
v_fma_mixhi_f16 v120,v142,s99,0
v_cvt_pk_bf16_f32 v155,v157,v159
v_mfma_f32_16x16x32_bf16 v[248:251],v[108:111],v[144:147],v[248:251]
s_setprio 2
ds_read_u16 v129,v129
v_fma_mixhi_f16 v122,v143,s99,0
v_mfma_f32_16x16x32_bf16 v[212:215],v[88:91],v[152:155],v[212:215]
v_lshlrev_b32_sdwa v133,1,v133 dst_sel:DWORD dst_unused:UNUSED_PAD src0_sel:DWORD src1_sel:WORD_1
v_pk_fma_f16 v121,v120,v120,s54
v_lshlrev_b32_sdwa v135,1,v135 dst_sel:DWORD dst_unused:UNUSED_PAD src0_sel:DWORD src1_sel:WORD_1
v_mfma_f32_16x16x32_bf16 v[220:223],v[92:95],v[152:155],v[220:223]
ds_read_u16_d16_hi v131,v131
ds_read_u16 v132,v132
v_pk_fma_f16 v123,v122,v122,s54
v_mfma_f32_16x16x32_bf16 v[228:231],v[96:99],v[152:155],v[228:231]
ds_read_b64_tr_b16 v[88:89],v6 offset:6160
v_pk_mul_f16 v121,v120,v121
v_lshlrev_b32_sdwa v136,1,v136 dst_sel:DWORD dst_unused:UNUSED_PAD src0_sel:DWORD src1_sel:WORD_1
v_pk_mul_f16 v123,v122,v123
v_mfma_f32_16x16x32_bf16 v[236:239],v[100:103],v[152:155],v[236:239]
ds_read_b64_tr_b16 v[90:91],v6 offset:7696
v_lshlrev_b32_sdwa v138,1,v138 dst_sel:DWORD dst_unused:UNUSED_PAD src0_sel:DWORD src1_sel:WORD_1
v_mfma_f32_16x16x32_bf16 v[244:247],v[104:107],v[152:155],v[244:247]
ds_read_u16_d16_hi v134,v134
v_exp_f16 v120,v121
v_mfma_f32_16x16x32_bf16 v[252:255],v[108:111],v[152:155],v[252:255]
ds_read_b64_tr_b16 v[92:93],v6 offset:6168
v_lshlrev_b32_sdwa v137,1,v137 dst_sel:DWORD dst_unused:UNUSED_PAD src0_sel:DWORD src1_sel:WORD_1
v_mfma_f32_16x16x32_bf16 v[144:147],v[64:67],v[40:43],v[112:115]
ds_read_b64_tr_b16 v[94:95],v6 offset:7704
v_lshlrev_b32_sdwa v139,1,v139 dst_sel:DWORD dst_unused:UNUSED_PAD src0_sel:DWORD src1_sel:WORD_1
v_exp_f16 v122,v123
v_mfma_f32_16x16x32_bf16 v[152:155],v[64:67],v[44:47],v[112:115]
ds_read_u16 v133,v133
v_mfma_f32_16x16x32_bf16 v[156:159],v[68:71],v[44:47],v[116:119]
ds_read_u16_d16_hi v135,v135
v_exp_f16_sdwa v122,v123 dst_sel:WORD_1 dst_unused:UNUSED_PRESERVE src0_sel:WORD_1
v_exp_f16_sdwa v120,v121 dst_sel:WORD_1 dst_unused:UNUSED_PRESERVE src0_sel:WORD_1
v_mfma_f32_16x16x32_bf16 v[148:151],v[68:71],v[40:43],v[116:119]
s_cbranch_vccnz RA15_%=
s_mov_b64 exec,s[62:63]
s_add_u32 m0,s57,17440
s_add_u32 s64,s56,4423680
buffer_load_dwordx4 v2,s[36:39],s64 offen lds
s_add_u32 m0,s57,19168
s_nop 0
buffer_load_dwordx4 v3,s[36:39],s64 offen lds
s_mov_b64 exec,-1
RA15_%=:
ds_read_b64_tr_b16 v[96:97],v6 offset:6224
ds_read_b64_tr_b16 v[98:99],v6 offset:7760
v_pk_fma_f16 v120,v120,s55,s55
v_pk_fma_f16 v122,v122,s55,s55
v_mfma_f32_16x16x32_bf16 v[144:147],v[72:75],v[48:51],v[144:147]
v_rcp_f16 v121,v120
v_mfma_f32_16x16x32_bf16 v[152:155],v[72:75],v[52:55],v[152:155]
ds_read_b64_tr_b16 v[100:101],v6 offset:6232
v_rcp_f16 v123,v122
v_mfma_f32_16x16x32_bf16 v[156:159],v[76:79],v[52:55],v[156:159]
ds_read_u16 v136,v136
v_rcp_f16_sdwa v121,v120 dst_sel:WORD_1 dst_unused:UNUSED_PRESERVE src0_sel:WORD_1
v_mfma_f32_16x16x32_bf16 v[148:151],v[76:79],v[48:51],v[148:151]
ds_read_b64_tr_b16 v[102:103],v6 offset:7768
ds_read_b64_tr_b16 v[104:105],v6 offset:6288
v_rcp_f16_sdwa v123,v122 dst_sel:WORD_1 dst_unused:UNUSED_PRESERVE src0_sel:WORD_1
v_mfma_f32_16x16x32_bf16 v[144:147],v[80:83],v[56:59],v[144:147]
ds_read_u16_d16_hi v138,v138
v_fma_mix_f32 v142,v142,v121,0 op_sel:[0,1,0] op_sel_hi:[0,1,0]
v_mfma_f32_16x16x32_bf16 v[152:155],v[80:83],v[60:63],v[152:155]
s_waitcnt lgkmcnt(7)
ds_read_b64_tr_b16 v[106:107],v6 offset:7824
ds_read_b64_tr_b16 v[108:109],v6 offset:6296
v_fma_mix_f32 v140,v140,v121,0 op_sel_hi:[0,1,0]
v_lshl_add_u64 v[128:129],v[128:129],0,v[130:131]
v_lshl_add_u64 v[130:131],v[132:133],0,v[134:135]
v_mfma_f32_16x16x32_bf16 v[156:159],v[84:87],v[60:63],v[156:159]
ds_read_u16 v137,v137
v_fma_mix_f32 v141,v141,v123,0 op_sel_hi:[0,1,0]
v_fma_mix_f32 v143,v143,v123,0 op_sel:[0,1,0] op_sel_hi:[0,1,0]
v_mfma_f32_16x16x32_bf16 v[148:151],v[84:87],v[56:59],v[148:151]
ds_read_b64_tr_b16 v[110:111],v6 offset:7832
ds_read_b64_tr_b16 v[64:65],v4 offset:512
v_lshlrev_b32_sdwa v144,1,v144 dst_sel:DWORD dst_unused:UNUSED_PAD src0_sel:DWORD src1_sel:WORD_1
v_lshlrev_b32_sdwa v146,1,v146 dst_sel:DWORD dst_unused:UNUSED_PAD src0_sel:DWORD src1_sel:WORD_1
v_mfma_f32_16x16x32_bf16 v[160:163],v[88:91],v[128:131],v[160:163]
ds_read_u16_d16_hi v139,v139
v_lshlrev_b32_sdwa v145,1,v145 dst_sel:DWORD dst_unused:UNUSED_PAD src0_sel:DWORD src1_sel:WORD_1
v_lshlrev_b32_sdwa v147,1,v147 dst_sel:DWORD dst_unused:UNUSED_PAD src0_sel:DWORD src1_sel:WORD_1
v_mfma_f32_16x16x32_bf16 v[168:171],v[92:95],v[128:131],v[168:171]
s_waitcnt lgkmcnt(11)
ds_read_b64_tr_b16 v[66:67],v4 offset:2560
ds_read_b128 v[112:115],v7 offset:1408
v_fma_mixlo_f16 v120,v156,s99,0
v_fma_mixlo_f16 v122,v157,s99,0
v_mfma_f32_16x16x32_bf16 v[176:179],v[96:99],v[128:131],v[176:179]
s_waitcnt lgkmcnt(10)
ds_read_u16 v144,v144
v_lshlrev_b32_sdwa v148,1,v148 dst_sel:DWORD dst_unused:UNUSED_PAD src0_sel:DWORD src1_sel:WORD_1
v_lshlrev_b32_sdwa v150,1,v150 dst_sel:DWORD dst_unused:UNUSED_PAD src0_sel:DWORD src1_sel:WORD_1
v_mfma_f32_16x16x32_bf16 v[184:187],v[100:103],v[128:131],v[184:187]
s_waitcnt lgkmcnt(3)
ds_read_b64_tr_b16 v[68:69],v5 offset:512
ds_read_b64_tr_b16 v[70:71],v5 offset:2560
v_lshl_add_u64 v[136:137],v[136:137],0,v[138:139]
v_fma_mixhi_f16 v120,v158,s99,0
v_mfma_f32_16x16x32_bf16 v[192:195],v[104:107],v[128:131],v[192:195]
s_cbranch_vccnz RA16_%=
s_mov_b64 exec,s[62:63]
s_add_u32 m0,s57,20928
s_add_u32 s64,s56,4521984
buffer_load_dwordx4 v2,s[36:39],s64 offen lds
s_add_u32 m0,s57,22656
s_nop 0
buffer_load_dwordx4 v3,s[36:39],s64 offen lds
s_mov_b64 exec,-1
RA16_%=:
ds_read_u16_d16_hi v146,v146
v_cvt_pk_bf16_f32 v138,v140,v142
v_cvt_pk_bf16_f32 v139,v141,v143
v_mfma_f32_16x16x32_bf16 v[200:203],v[108:111],v[128:131],v[200:203]
ds_read_b128 v[116:119],v7 offset:1440
ds_read_b64_tr_b16 v[72:73],v4 offset:4608
v_fma_mixhi_f16 v122,v159,s99,0
v_mfma_f32_16x16x32_bf16 v[164:167],v[88:91],v[136:139],v[164:167]
v_lshlrev_b32_sdwa v149,1,v149 dst_sel:DWORD dst_unused:UNUSED_PAD src0_sel:DWORD src1_sel:WORD_1
v_lshlrev_b32_sdwa v151,1,v151 dst_sel:DWORD dst_unused:UNUSED_PAD src0_sel:DWORD src1_sel:WORD_1
v_mfma_f32_16x16x32_bf16 v[172:175],v[92:95],v[136:139],v[172:175]
s_setprio 1
ds_read_b64_tr_b16 v[74:75],v4 offset:6656
ds_read_b64_tr_b16 v[76:77],v5 offset:4608
v_pk_fma_f16 v121,v120,v120,s54
v_pk_fma_f16 v123,v122,v122,s54
v_mfma_f32_16x16x32_bf16 v[180:183],v[96:99],v[136:139],v[180:183]
ds_read_u16 v145,v145
v_pk_mul_f16 v121,v120,v121
v_pk_mul_f16 v123,v122,v123
v_mfma_f32_16x16x32_bf16 v[188:191],v[100:103],v[136:139],v[188:191]
ds_read_u16_d16_hi v147,v147
v_lshlrev_b32_sdwa v152,1,v152 dst_sel:DWORD dst_unused:UNUSED_PAD src0_sel:DWORD src1_sel:WORD_1
v_lshlrev_b32_sdwa v154,1,v154 dst_sel:DWORD dst_unused:UNUSED_PAD src0_sel:DWORD src1_sel:WORD_1
v_mfma_f32_16x16x32_bf16 v[196:199],v[104:107],v[136:139],v[196:199]
v_exp_f16 v120,v121
v_mfma_f32_16x16x32_bf16 v[204:207],v[108:111],v[136:139],v[204:207]
s_waitcnt lgkmcnt(10)
ds_read_b64_tr_b16 v[78:79],v5 offset:6656
ds_read_b64_tr_b16 v[80:81],v4 offset:8704
v_exp_f16 v122,v123
v_mfma_f32_16x16x32_bf16 v[128:131],v[64:67],v[16:19],v[112:115]
v_lshlrev_b32_sdwa v153,1,v153 dst_sel:DWORD dst_unused:UNUSED_PAD src0_sel:DWORD src1_sel:WORD_1
v_lshlrev_b32_sdwa v155,1,v155 dst_sel:DWORD dst_unused:UNUSED_PAD src0_sel:DWORD src1_sel:WORD_1
v_mfma_f32_16x16x32_bf16 v[136:139],v[64:67],v[20:23],v[112:115]
s_waitcnt lgkmcnt(7)
ds_read_u16 v148,v148
ds_read_u16_d16_hi v150,v150
v_exp_f16_sdwa v120,v121 dst_sel:WORD_1 dst_unused:UNUSED_PRESERVE src0_sel:WORD_1
v_mfma_f32_16x16x32_bf16 v[140:143],v[68:71],v[20:23],v[116:119]
v_exp_f16_sdwa v122,v123 dst_sel:WORD_1 dst_unused:UNUSED_PRESERVE src0_sel:WORD_1
v_mfma_f32_16x16x32_bf16 v[132:135],v[68:71],v[16:19],v[116:119]
s_waitcnt lgkmcnt(7)
ds_read_b64_tr_b16 v[82:83],v4 offset:10752
v_pk_fma_f16 v120,v120,s55,s55
v_mfma_f32_16x16x32_bf16 v[128:131],v[72:75],v[24:27],v[128:131]
ds_read_b64_tr_b16 v[84:85],v5 offset:8704
v_pk_fma_f16 v122,v122,s55,s55
v_rcp_f16 v121,v120
v_mfma_f32_16x16x32_bf16 v[136:139],v[72:75],v[28:31],v[136:139]
s_waitcnt lgkmcnt(5)
ds_read_b64_tr_b16 v[86:87],v5 offset:10752
ds_read_u16 v149,v149
v_rcp_f16 v123,v122
v_mfma_f32_16x16x32_bf16 v[140:143],v[76:79],v[28:31],v[140:143]
ds_read_u16_d16_hi v151,v151
ds_read_u16 v152,v152
v_rcp_f16_sdwa v121,v120 dst_sel:WORD_1 dst_unused:UNUSED_PRESERVE src0_sel:WORD_1
v_mfma_f32_16x16x32_bf16 v[132:135],v[76:79],v[24:27],v[132:135]
s_waitcnt lgkmcnt(5)
s_cbranch_vccnz RA17_%=
s_mov_b64 exec,s[62:63]
s_add_u32 m0,s57,24416
s_add_u32 s64,s56,4620288
buffer_load_dwordx4 v2,s[36:39],s64 offen lds
s_add_u32 m0,s57,26144
s_nop 0
buffer_load_dwordx4 v3,s[36:39],s64 offen lds
s_mov_b64 exec,-1
RA17_%=:
v_rcp_f16_sdwa v123,v122 dst_sel:WORD_1 dst_unused:UNUSED_PRESERVE src0_sel:WORD_1
v_mfma_f32_16x16x32_bf16 v[128:131],v[80:83],v[32:35],v[128:131]
ds_read_u16_d16_hi v154,v154
v_fma_mix_f32 v156,v156,v121,0 op_sel_hi:[0,1,0]
v_fma_mix_f32 v158,v158,v121,0 op_sel:[0,1,0] op_sel_hi:[0,1,0]
v_mfma_f32_16x16x32_bf16 v[136:139],v[80:83],v[36:39],v[136:139]
s_waitcnt lgkmcnt(2)
s_setprio 0
v_lshl_add_u64 v[144:145],v[144:145],0,v[146:147]
v_lshl_add_u64 v[146:147],v[148:149],0,v[150:151]
v_mfma_f32_16x16x32_bf16 v[140:143],v[84:87],v[36:39],v[140:143]
ds_read_u16 v153,v153
v_fma_mix_f32 v157,v157,v123,0 op_sel_hi:[0,1,0]
v_fma_mix_f32 v159,v159,v123,0 op_sel:[0,1,0] op_sel_hi:[0,1,0]
v_mfma_f32_16x16x32_bf16 v[132:135],v[84:87],v[32:35],v[132:135]
v_lshlrev_b32_sdwa v128,1,v128 dst_sel:DWORD dst_unused:UNUSED_PAD src0_sel:DWORD src1_sel:WORD_1
v_lshlrev_b32_sdwa v130,1,v130 dst_sel:DWORD dst_unused:UNUSED_PAD src0_sel:DWORD src1_sel:WORD_1
v_mfma_f32_16x16x32_bf16 v[208:211],v[88:91],v[144:147],v[208:211]
s_cbranch_vccz RA18_%=
s_waitcnt vmcnt(0)
RA18_%=:
s_barrier
s_setprio 3
v_lshlrev_b32_sdwa v129,1,v129 dst_sel:DWORD dst_unused:UNUSED_PAD src0_sel:DWORD src1_sel:WORD_1
v_lshlrev_b32_sdwa v131,1,v131 dst_sel:DWORD dst_unused:UNUSED_PAD src0_sel:DWORD src1_sel:WORD_1
v_mfma_f32_16x16x32_bf16 v[216:219],v[92:95],v[144:147],v[216:219]
ds_read_u16_d16_hi v155,v155
ds_read_u16 v128,v128
v_fma_mixlo_f16 v120,v140,s99,0
v_fma_mixlo_f16 v122,v141,s99,0
v_mfma_f32_16x16x32_bf16 v[224:227],v[96:99],v[144:147],v[224:227]
s_cbranch_vccz RA19_%=
s_add_u32 m0,s58,0
s_mul_i32 s69,s59,1
s_add_u32 s69,s69,s97
buffer_load_dwordx4 v2,s[44:47],s69 offen lds
s_add_u32 m0,m0,1024
s_add_u32 s69,s69,s53
buffer_load_dwordx4 v3,s[44:47],s69 offen lds
s_add_u32 m0,m0,1024
s_add_u32 s69,s69,s53
buffer_load_dwordx4 v2,s[44:47],s69 offen lds
s_add_u32 m0,m0,s50
s_add_u32 s69,s69,s53
buffer_load_dwordx4 v3,s[44:47],s69 offen lds
RA19_%=:
ds_read_u16_d16_hi v130,v130
v_lshlrev_b32_sdwa v132,1,v132 dst_sel:DWORD dst_unused:UNUSED_PAD src0_sel:DWORD src1_sel:WORD_1
v_lshlrev_b32_sdwa v134,1,v134 dst_sel:DWORD dst_unused:UNUSED_PAD src0_sel:DWORD src1_sel:WORD_1
v_mfma_f32_16x16x32_bf16 v[232:235],v[100:103],v[144:147],v[232:235]
s_waitcnt lgkmcnt(2)
v_lshl_add_u64 v[152:153],v[152:153],0,v[154:155]
v_cvt_pk_bf16_f32 v154,v156,v158
v_mfma_f32_16x16x32_bf16 v[240:243],v[104:107],v[144:147],v[240:243]
v_fma_mixhi_f16 v120,v142,s99,0
v_cvt_pk_bf16_f32 v155,v157,v159
v_mfma_f32_16x16x32_bf16 v[248:251],v[108:111],v[144:147],v[248:251]
s_cbranch_vccz RA20_%=
s_add_u32 m0,m0,1024
s_add_u32 s69,s69,s53
buffer_load_dwordx4 v2,s[44:47],s69 offen lds
s_add_u32 m0,m0,1024
s_add_u32 s69,s69,s53
buffer_load_dwordx4 v3,s[44:47],s69 offen lds
s_add_u32 m0,m0,s50
s_add_u32 s69,s69,s53
buffer_load_dwordx4 v2,s[44:47],s69 offen lds
s_nop 0
s_mov_b32 s86,0xbe801d5a
s_add_u32 m0,m0,1024
s_add_u32 s69,s69,s53
buffer_load_dwordx4 v3,s[44:47],s69 offen lds
RA20_%=:
ds_read_u16 v129,v129
v_fma_mixhi_f16 v122,v143,s99,0
v_mfma_f32_16x16x32_bf16 v[212:215],v[88:91],v[152:155],v[212:215]
v_lshlrev_b32_sdwa v133,1,v133 dst_sel:DWORD dst_unused:UNUSED_PAD src0_sel:DWORD src1_sel:WORD_1
v_pk_fma_f16 v121,v120,v120,s54
v_lshlrev_b32_sdwa v135,1,v135 dst_sel:DWORD dst_unused:UNUSED_PAD src0_sel:DWORD src1_sel:WORD_1
v_mfma_f32_16x16x32_bf16 v[220:223],v[92:95],v[152:155],v[220:223]
ds_read_u16_d16_hi v131,v131
ds_read_u16 v132,v132
v_pk_fma_f16 v123,v122,v122,s54
v_mfma_f32_16x16x32_bf16 v[228:231],v[96:99],v[152:155],v[228:231]
ds_read_b64_tr_b16 v[88:89],v6 offset:12320
v_pk_mul_f16 v121,v120,v121
v_lshlrev_b32_sdwa v136,1,v136 dst_sel:DWORD dst_unused:UNUSED_PAD src0_sel:DWORD src1_sel:WORD_1
v_pk_mul_f16 v123,v122,v123
v_mfma_f32_16x16x32_bf16 v[236:239],v[100:103],v[152:155],v[236:239]
s_cbranch_vccnz RA21_%=
s_mov_b64 exec,s[62:63]
s_add_u32 m0,s57,27904
s_add_u32 s64,s56,4718592
buffer_load_dwordx4 v2,s[36:39],s64 offen lds
s_add_u32 m0,s57,29632
s_nop 0
buffer_load_dwordx4 v3,s[36:39],s64 offen lds
s_mov_b64 exec,-1
RA21_%=:
ds_read_b64_tr_b16 v[90:91],v6 offset:13856
v_lshlrev_b32_sdwa v138,1,v138 dst_sel:DWORD dst_unused:UNUSED_PAD src0_sel:DWORD src1_sel:WORD_1
v_mfma_f32_16x16x32_bf16 v[244:247],v[104:107],v[152:155],v[244:247]
s_cbranch_vccz RA22_%=
s_add_u32 m0,m0,1024
s_add_u32 s69,s69,s53
buffer_load_dwordx4 v2,s[44:47],s69 offen lds
s_add_u32 m0,m0,s50
s_add_u32 s69,s69,s53
buffer_load_dwordx4 v3,s[44:47],s69 offen lds
s_add_u32 m0,m0,1024
s_add_u32 s69,s69,s53
buffer_load_dwordx4 v2,s[44:47],s69 offen lds
s_add_u32 m0,m0,1024
s_add_u32 s69,s69,s53
buffer_load_dwordx4 v3,s[44:47],s69 offen lds
RA22_%=:
ds_read_u16_d16_hi v134,v134
v_exp_f16 v120,v121
v_mfma_f32_16x16x32_bf16 v[252:255],v[108:111],v[152:155],v[252:255]
ds_read_b64_tr_b16 v[92:93],v6 offset:12328
v_lshlrev_b32_sdwa v137,1,v137 dst_sel:DWORD dst_unused:UNUSED_PAD src0_sel:DWORD src1_sel:WORD_1
v_mfma_f32_16x16x32_bf16 v[144:147],v[64:67],v[40:43],v[112:115]
ds_read_b64_tr_b16 v[94:95],v6 offset:13864
v_lshlrev_b32_sdwa v139,1,v139 dst_sel:DWORD dst_unused:UNUSED_PAD src0_sel:DWORD src1_sel:WORD_1
v_exp_f16 v122,v123
v_mfma_f32_16x16x32_bf16 v[152:155],v[64:67],v[44:47],v[112:115]
ds_read_u16 v133,v133
v_mfma_f32_16x16x32_bf16 v[156:159],v[68:71],v[44:47],v[116:119]
ds_read_u16_d16_hi v135,v135
v_exp_f16_sdwa v122,v123 dst_sel:WORD_1 dst_unused:UNUSED_PRESERVE src0_sel:WORD_1
v_exp_f16_sdwa v120,v121 dst_sel:WORD_1 dst_unused:UNUSED_PRESERVE src0_sel:WORD_1
v_mfma_f32_16x16x32_bf16 v[148:151],v[68:71],v[40:43],v[116:119]
ds_read_b64_tr_b16 v[96:97],v6 offset:12384
ds_read_b64_tr_b16 v[98:99],v6 offset:13920
v_pk_fma_f16 v120,v120,s55,s55
v_pk_fma_f16 v122,v122,s55,s55
v_mfma_f32_16x16x32_bf16 v[144:147],v[72:75],v[48:51],v[144:147]
v_rcp_f16 v121,v120
v_mfma_f32_16x16x32_bf16 v[152:155],v[72:75],v[52:55],v[152:155]
ds_read_b64_tr_b16 v[100:101],v6 offset:12392
v_rcp_f16 v123,v122
v_mfma_f32_16x16x32_bf16 v[156:159],v[76:79],v[52:55],v[156:159]
ds_read_u16 v136,v136
v_rcp_f16_sdwa v121,v120 dst_sel:WORD_1 dst_unused:UNUSED_PRESERVE src0_sel:WORD_1
v_mfma_f32_16x16x32_bf16 v[148:151],v[76:79],v[48:51],v[148:151]
ds_read_b64_tr_b16 v[102:103],v6 offset:13928
ds_read_b64_tr_b16 v[104:105],v6 offset:12448
v_rcp_f16_sdwa v123,v122 dst_sel:WORD_1 dst_unused:UNUSED_PRESERVE src0_sel:WORD_1
v_mfma_f32_16x16x32_bf16 v[144:147],v[80:83],v[56:59],v[144:147]
ds_read_u16_d16_hi v138,v138
v_fma_mix_f32 v142,v142,v121,0 op_sel:[0,1,0] op_sel_hi:[0,1,0]
v_mfma_f32_16x16x32_bf16 v[152:155],v[80:83],v[60:63],v[152:155]
s_waitcnt lgkmcnt(7)
ds_read_b64_tr_b16 v[106:107],v6 offset:13984
ds_read_b64_tr_b16 v[108:109],v6 offset:12456
v_fma_mix_f32 v140,v140,v121,0 op_sel_hi:[0,1,0]
v_lshl_add_u64 v[128:129],v[128:129],0,v[130:131]
v_lshl_add_u64 v[130:131],v[132:133],0,v[134:135]
v_mfma_f32_16x16x32_bf16 v[156:159],v[84:87],v[60:63],v[156:159]
ds_read_u16 v137,v137
v_fma_mix_f32 v141,v141,v123,0 op_sel_hi:[0,1,0]
v_fma_mix_f32 v143,v143,v123,0 op_sel:[0,1,0] op_sel_hi:[0,1,0]
v_mfma_f32_16x16x32_bf16 v[148:151],v[84:87],v[56:59],v[148:151]
ds_read_b64_tr_b16 v[110:111],v6 offset:13992
ds_read_b64_tr_b16 v[64:65],v4 offset:12320
v_lshlrev_b32_sdwa v144,1,v144 dst_sel:DWORD dst_unused:UNUSED_PAD src0_sel:DWORD src1_sel:WORD_1
v_lshlrev_b32_sdwa v146,1,v146 dst_sel:DWORD dst_unused:UNUSED_PAD src0_sel:DWORD src1_sel:WORD_1
v_mfma_f32_16x16x32_bf16 v[160:163],v[88:91],v[128:131],v[160:163]
s_cbranch_vccnz RA23_%=
s_mov_b64 exec,s[62:63]
s_add_u32 m0,s57,31392
s_add_u32 s64,s56,4816896
buffer_load_dwordx4 v2,s[36:39],s64 offen lds
s_add_u32 m0,s57,33120
s_nop 0
buffer_load_dwordx4 v3,s[36:39],s64 offen lds
s_mov_b64 exec,-1
RA23_%=:
ds_read_u16_d16_hi v139,v139
v_lshlrev_b32_sdwa v145,1,v145 dst_sel:DWORD dst_unused:UNUSED_PAD src0_sel:DWORD src1_sel:WORD_1
v_lshlrev_b32_sdwa v147,1,v147 dst_sel:DWORD dst_unused:UNUSED_PAD src0_sel:DWORD src1_sel:WORD_1
v_mfma_f32_16x16x32_bf16 v[168:171],v[92:95],v[128:131],v[168:171]
s_waitcnt lgkmcnt(11)
ds_read_b64_tr_b16 v[66:67],v4 offset:14368
ds_read_b128 v[112:115],v7 offset:0
v_fma_mixlo_f16 v120,v156,s99,0
v_fma_mixlo_f16 v122,v157,s99,0
v_mfma_f32_16x16x32_bf16 v[176:179],v[96:99],v[128:131],v[176:179]
s_waitcnt lgkmcnt(10)
ds_read_u16 v144,v144
v_lshlrev_b32_sdwa v148,1,v148 dst_sel:DWORD dst_unused:UNUSED_PAD src0_sel:DWORD src1_sel:WORD_1
v_lshlrev_b32_sdwa v150,1,v150 dst_sel:DWORD dst_unused:UNUSED_PAD src0_sel:DWORD src1_sel:WORD_1
v_mfma_f32_16x16x32_bf16 v[184:187],v[100:103],v[128:131],v[184:187]
s_waitcnt lgkmcnt(3)
ds_read_b64_tr_b16 v[68:69],v5 offset:12320
ds_read_b64_tr_b16 v[70:71],v5 offset:14368
v_lshl_add_u64 v[136:137],v[136:137],0,v[138:139]
v_fma_mixhi_f16 v120,v158,s99,0
v_mfma_f32_16x16x32_bf16 v[192:195],v[104:107],v[128:131],v[192:195]
ds_read_u16_d16_hi v146,v146
v_cvt_pk_bf16_f32 v138,v140,v142
v_cvt_pk_bf16_f32 v139,v141,v143
v_mfma_f32_16x16x32_bf16 v[200:203],v[108:111],v[128:131],v[200:203]
ds_read_b128 v[116:119],v7 offset:32
ds_read_b64_tr_b16 v[72:73],v4 offset:16416
v_fma_mixhi_f16 v122,v159,s99,0
v_mfma_f32_16x16x32_bf16 v[164:167],v[88:91],v[136:139],v[164:167]
v_lshlrev_b32_sdwa v149,1,v149 dst_sel:DWORD dst_unused:UNUSED_PAD src0_sel:DWORD src1_sel:WORD_1
v_lshlrev_b32_sdwa v151,1,v151 dst_sel:DWORD dst_unused:UNUSED_PAD src0_sel:DWORD src1_sel:WORD_1
v_mfma_f32_16x16x32_bf16 v[172:175],v[92:95],v[136:139],v[172:175]
ds_read_b64_tr_b16 v[74:75],v4 offset:18464
ds_read_b64_tr_b16 v[76:77],v5 offset:16416
v_pk_fma_f16 v121,v120,v120,s54
v_pk_fma_f16 v123,v122,v122,s54
v_mfma_f32_16x16x32_bf16 v[180:183],v[96:99],v[136:139],v[180:183]
ds_read_u16 v145,v145
v_pk_mul_f16 v121,v120,v121
v_pk_mul_f16 v123,v122,v123
v_mfma_f32_16x16x32_bf16 v[188:191],v[100:103],v[136:139],v[188:191]
ds_read_u16_d16_hi v147,v147
v_lshlrev_b32_sdwa v152,1,v152 dst_sel:DWORD dst_unused:UNUSED_PAD src0_sel:DWORD src1_sel:WORD_1
v_lshlrev_b32_sdwa v154,1,v154 dst_sel:DWORD dst_unused:UNUSED_PAD src0_sel:DWORD src1_sel:WORD_1
v_mfma_f32_16x16x32_bf16 v[196:199],v[104:107],v[136:139],v[196:199]
v_exp_f16 v120,v121
v_mfma_f32_16x16x32_bf16 v[204:207],v[108:111],v[136:139],v[204:207]
s_waitcnt lgkmcnt(10)
ds_read_b64_tr_b16 v[78:79],v5 offset:18464
ds_read_b64_tr_b16 v[80:81],v4 offset:20512
v_exp_f16 v122,v123
v_mfma_f32_16x16x32_bf16 v[128:131],v[64:67],v[16:19],v[112:115]
v_lshlrev_b32_sdwa v153,1,v153 dst_sel:DWORD dst_unused:UNUSED_PAD src0_sel:DWORD src1_sel:WORD_1
v_lshlrev_b32_sdwa v155,1,v155 dst_sel:DWORD dst_unused:UNUSED_PAD src0_sel:DWORD src1_sel:WORD_1
v_mfma_f32_16x16x32_bf16 v[136:139],v[64:67],v[20:23],v[112:115]
s_waitcnt lgkmcnt(7)
ds_read_u16 v148,v148
ds_read_u16_d16_hi v150,v150
v_exp_f16_sdwa v120,v121 dst_sel:WORD_1 dst_unused:UNUSED_PRESERVE src0_sel:WORD_1
v_mfma_f32_16x16x32_bf16 v[140:143],v[68:71],v[20:23],v[116:119]
v_exp_f16_sdwa v122,v123 dst_sel:WORD_1 dst_unused:UNUSED_PRESERVE src0_sel:WORD_1
v_mfma_f32_16x16x32_bf16 v[132:135],v[68:71],v[16:19],v[116:119]
s_waitcnt lgkmcnt(7)
ds_read_b64_tr_b16 v[82:83],v4 offset:22560
v_pk_fma_f16 v120,v120,s55,s55
v_mfma_f32_16x16x32_bf16 v[128:131],v[72:75],v[24:27],v[128:131]
ds_read_b64_tr_b16 v[84:85],v5 offset:20512
v_pk_fma_f16 v122,v122,s55,s55
v_rcp_f16 v121,v120
v_mfma_f32_16x16x32_bf16 v[136:139],v[72:75],v[28:31],v[136:139]
s_waitcnt lgkmcnt(5)
ds_read_b64_tr_b16 v[86:87],v5 offset:22560
ds_read_u16 v149,v149
v_rcp_f16 v123,v122
v_mfma_f32_16x16x32_bf16 v[140:143],v[76:79],v[28:31],v[140:143]
ds_read_u16_d16_hi v151,v151
ds_read_u16 v152,v152
v_rcp_f16_sdwa v121,v120 dst_sel:WORD_1 dst_unused:UNUSED_PRESERVE src0_sel:WORD_1
v_mfma_f32_16x16x32_bf16 v[132:135],v[76:79],v[24:27],v[132:135]
s_waitcnt lgkmcnt(5)
v_rcp_f16_sdwa v123,v122 dst_sel:WORD_1 dst_unused:UNUSED_PRESERVE src0_sel:WORD_1
v_mfma_f32_16x16x32_bf16 v[128:131],v[80:83],v[32:35],v[128:131]
ds_read_u16_d16_hi v154,v154
v_fma_mix_f32 v156,v156,v121,0 op_sel_hi:[0,1,0]
v_fma_mix_f32 v158,v158,v121,0 op_sel:[0,1,0] op_sel_hi:[0,1,0]
v_mfma_f32_16x16x32_bf16 v[136:139],v[80:83],v[36:39],v[136:139]
s_waitcnt lgkmcnt(2)
v_lshl_add_u64 v[144:145],v[144:145],0,v[146:147]
v_lshl_add_u64 v[146:147],v[148:149],0,v[150:151]
v_mfma_f32_16x16x32_bf16 v[140:143],v[84:87],v[36:39],v[140:143]
ds_read_u16 v153,v153
v_fma_mix_f32 v157,v157,v123,0 op_sel_hi:[0,1,0]
v_fma_mix_f32 v159,v159,v123,0 op_sel:[0,1,0] op_sel_hi:[0,1,0]
v_mfma_f32_16x16x32_bf16 v[132:135],v[84:87],v[32:35],v[132:135]
v_lshlrev_b32_sdwa v128,1,v128 dst_sel:DWORD dst_unused:UNUSED_PAD src0_sel:DWORD src1_sel:WORD_1
v_lshlrev_b32_sdwa v130,1,v130 dst_sel:DWORD dst_unused:UNUSED_PAD src0_sel:DWORD src1_sel:WORD_1
v_mfma_f32_16x16x32_bf16 v[208:211],v[88:91],v[144:147],v[208:211]
v_lshlrev_b32_sdwa v129,1,v129 dst_sel:DWORD dst_unused:UNUSED_PAD src0_sel:DWORD src1_sel:WORD_1
v_lshlrev_b32_sdwa v131,1,v131 dst_sel:DWORD dst_unused:UNUSED_PAD src0_sel:DWORD src1_sel:WORD_1
v_mfma_f32_16x16x32_bf16 v[216:219],v[92:95],v[144:147],v[216:219]
ds_read_u16_d16_hi v155,v155
ds_read_u16 v128,v128
v_fma_mixlo_f16 v120,v140,s99,0
v_fma_mixlo_f16 v122,v141,s99,0
v_mfma_f32_16x16x32_bf16 v[224:227],v[96:99],v[144:147],v[224:227]
s_setprio 2
ds_read_u16_d16_hi v130,v130
v_lshlrev_b32_sdwa v132,1,v132 dst_sel:DWORD dst_unused:UNUSED_PAD src0_sel:DWORD src1_sel:WORD_1
v_lshlrev_b32_sdwa v134,1,v134 dst_sel:DWORD dst_unused:UNUSED_PAD src0_sel:DWORD src1_sel:WORD_1
v_mfma_f32_16x16x32_bf16 v[232:235],v[100:103],v[144:147],v[232:235]
s_waitcnt lgkmcnt(2)
v_lshl_add_u64 v[152:153],v[152:153],0,v[154:155]
v_cvt_pk_bf16_f32 v154,v156,v158
v_mfma_f32_16x16x32_bf16 v[240:243],v[104:107],v[144:147],v[240:243]
v_fma_mixhi_f16 v120,v142,s99,0
v_cvt_pk_bf16_f32 v155,v157,v159
v_mfma_f32_16x16x32_bf16 v[248:251],v[108:111],v[144:147],v[248:251]
ds_read_u16 v129,v129
v_fma_mixhi_f16 v122,v143,s99,0
v_mfma_f32_16x16x32_bf16 v[212:215],v[88:91],v[152:155],v[212:215]
v_lshlrev_b32_sdwa v133,1,v133 dst_sel:DWORD dst_unused:UNUSED_PAD src0_sel:DWORD src1_sel:WORD_1
v_pk_fma_f16 v121,v120,v120,s54
v_lshlrev_b32_sdwa v135,1,v135 dst_sel:DWORD dst_unused:UNUSED_PAD src0_sel:DWORD src1_sel:WORD_1
v_mfma_f32_16x16x32_bf16 v[220:223],v[92:95],v[152:155],v[220:223]
ds_read_u16_d16_hi v131,v131
ds_read_u16 v132,v132
v_pk_fma_f16 v123,v122,v122,s54
v_mfma_f32_16x16x32_bf16 v[228:231],v[96:99],v[152:155],v[228:231]
ds_read_b64_tr_b16 v[88:89],v6 offset:18480
v_pk_mul_f16 v121,v120,v121
v_lshlrev_b32_sdwa v136,1,v136 dst_sel:DWORD dst_unused:UNUSED_PAD src0_sel:DWORD src1_sel:WORD_1
v_pk_mul_f16 v123,v122,v123
v_mfma_f32_16x16x32_bf16 v[236:239],v[100:103],v[152:155],v[236:239]
ds_read_b64_tr_b16 v[90:91],v6 offset:20016
v_lshlrev_b32_sdwa v138,1,v138 dst_sel:DWORD dst_unused:UNUSED_PAD src0_sel:DWORD src1_sel:WORD_1
v_mfma_f32_16x16x32_bf16 v[244:247],v[104:107],v[152:155],v[244:247]
ds_read_u16_d16_hi v134,v134
v_exp_f16 v120,v121
v_mfma_f32_16x16x32_bf16 v[252:255],v[108:111],v[152:155],v[252:255]
ds_read_b64_tr_b16 v[92:93],v6 offset:18488
v_lshlrev_b32_sdwa v137,1,v137 dst_sel:DWORD dst_unused:UNUSED_PAD src0_sel:DWORD src1_sel:WORD_1
v_mfma_f32_16x16x32_bf16 v[144:147],v[64:67],v[40:43],v[112:115]
ds_read_b64_tr_b16 v[94:95],v6 offset:20024
v_lshlrev_b32_sdwa v139,1,v139 dst_sel:DWORD dst_unused:UNUSED_PAD src0_sel:DWORD src1_sel:WORD_1
v_exp_f16 v122,v123
v_mfma_f32_16x16x32_bf16 v[152:155],v[64:67],v[44:47],v[112:115]
ds_read_u16 v133,v133
v_mfma_f32_16x16x32_bf16 v[156:159],v[68:71],v[44:47],v[116:119]
ds_read_u16_d16_hi v135,v135
v_exp_f16_sdwa v122,v123 dst_sel:WORD_1 dst_unused:UNUSED_PRESERVE src0_sel:WORD_1
v_exp_f16_sdwa v120,v121 dst_sel:WORD_1 dst_unused:UNUSED_PRESERVE src0_sel:WORD_1
v_mfma_f32_16x16x32_bf16 v[148:151],v[68:71],v[40:43],v[116:119]
ds_read_b64_tr_b16 v[96:97],v6 offset:18544
ds_read_b64_tr_b16 v[98:99],v6 offset:20080
v_pk_fma_f16 v120,v120,s55,s55
v_pk_fma_f16 v122,v122,s55,s55
v_mfma_f32_16x16x32_bf16 v[144:147],v[72:75],v[48:51],v[144:147]
v_rcp_f16 v121,v120
v_mfma_f32_16x16x32_bf16 v[152:155],v[72:75],v[52:55],v[152:155]
ds_read_b64_tr_b16 v[100:101],v6 offset:18552
v_rcp_f16 v123,v122
v_mfma_f32_16x16x32_bf16 v[156:159],v[76:79],v[52:55],v[156:159]
ds_read_u16 v136,v136
v_rcp_f16_sdwa v121,v120 dst_sel:WORD_1 dst_unused:UNUSED_PRESERVE src0_sel:WORD_1
v_mfma_f32_16x16x32_bf16 v[148:151],v[76:79],v[48:51],v[148:151]
ds_read_b64_tr_b16 v[102:103],v6 offset:20088
ds_read_b64_tr_b16 v[104:105],v6 offset:18608
v_rcp_f16_sdwa v123,v122 dst_sel:WORD_1 dst_unused:UNUSED_PRESERVE src0_sel:WORD_1
v_mfma_f32_16x16x32_bf16 v[144:147],v[80:83],v[56:59],v[144:147]
ds_read_u16_d16_hi v138,v138
v_fma_mix_f32 v142,v142,v121,0 op_sel:[0,1,0] op_sel_hi:[0,1,0]
v_mfma_f32_16x16x32_bf16 v[152:155],v[80:83],v[60:63],v[152:155]
s_waitcnt lgkmcnt(7)
ds_read_b64_tr_b16 v[106:107],v6 offset:20144
ds_read_b64_tr_b16 v[108:109],v6 offset:18616
v_fma_mix_f32 v140,v140,v121,0 op_sel_hi:[0,1,0]
v_lshl_add_u64 v[128:129],v[128:129],0,v[130:131]
v_lshl_add_u64 v[130:131],v[132:133],0,v[134:135]
v_mfma_f32_16x16x32_bf16 v[156:159],v[84:87],v[60:63],v[156:159]
ds_read_u16 v137,v137
v_fma_mix_f32 v141,v141,v123,0 op_sel_hi:[0,1,0]
v_fma_mix_f32 v143,v143,v123,0 op_sel:[0,1,0] op_sel_hi:[0,1,0]
v_mfma_f32_16x16x32_bf16 v[148:151],v[84:87],v[56:59],v[148:151]
ds_read_b64_tr_b16 v[110:111],v6 offset:20152
ds_read_b128 v[64:67],v7 offset:1536
v_lshlrev_b32_sdwa v144,1,v144 dst_sel:DWORD dst_unused:UNUSED_PAD src0_sel:DWORD src1_sel:WORD_1
v_lshlrev_b32_sdwa v146,1,v146 dst_sel:DWORD dst_unused:UNUSED_PAD src0_sel:DWORD src1_sel:WORD_1
v_mfma_f32_16x16x32_bf16 v[160:163],v[88:91],v[128:131],v[160:163]
ds_read_u16_d16_hi v139,v139
ds_read_b128 v[68:71],v7 offset:1568
v_lshlrev_b32_sdwa v145,1,v145 dst_sel:DWORD dst_unused:UNUSED_PAD src0_sel:DWORD src1_sel:WORD_1
v_lshlrev_b32_sdwa v147,1,v147 dst_sel:DWORD dst_unused:UNUSED_PAD src0_sel:DWORD src1_sel:WORD_1
v_fma_mixlo_f16 v120,v156,s99,0
v_fma_mixlo_f16 v122,v157,s99,0
v_mfma_f32_16x16x32_bf16 v[168:171],v[92:95],v[128:131],v[168:171]
s_waitcnt lgkmcnt(12)
ds_read_u16 v144,v144
ds_read_b128 v[72:75],v7 offset:1664
v_lshlrev_b32_sdwa v148,1,v148 dst_sel:DWORD dst_unused:UNUSED_PAD src0_sel:DWORD src1_sel:WORD_1
v_lshlrev_b32_sdwa v150,1,v150 dst_sel:DWORD dst_unused:UNUSED_PAD src0_sel:DWORD src1_sel:WORD_1
s_waitcnt lgkmcnt(3)
v_lshl_add_u64 v[136:137],v[136:137],0,v[138:139]
v_fma_mixhi_f16 v120,v158,s99,0
v_mfma_f32_16x16x32_bf16 v[176:179],v[96:99],v[128:131],v[176:179]
s_setprio 1
ds_read_u16_d16_hi v146,v146
ds_read_b128 v[76:79],v7 offset:1696
v_cvt_pk_bf16_f32 v138,v140,v142
v_cvt_pk_bf16_f32 v139,v141,v143
v_fma_mixhi_f16 v122,v159,s99,0
v_lshlrev_b32_sdwa v149,1,v149 dst_sel:DWORD dst_unused:UNUSED_PAD src0_sel:DWORD src1_sel:WORD_1
v_mfma_f32_16x16x32_bf16 v[184:187],v[100:103],v[128:131],v[184:187]
ds_read_u16 v145,v145
ds_read_b128 v[80:83],v7 offset:1792
v_lshlrev_b32_sdwa v151,1,v151 dst_sel:DWORD dst_unused:UNUSED_PAD src0_sel:DWORD src1_sel:WORD_1
v_pk_fma_f16 v121,v120,v120,s54
v_pk_fma_f16 v123,v122,v122,s54
v_pk_mul_f16 v121,v120,v121
v_mfma_f32_16x16x32_bf16 v[192:195],v[104:107],v[128:131],v[192:195]
ds_read_u16_d16_hi v147,v147
ds_read_b128 v[84:87],v7 offset:1824
v_pk_mul_f16 v123,v122,v123
v_lshlrev_b32_sdwa v152,1,v152 dst_sel:DWORD dst_unused:UNUSED_PAD src0_sel:DWORD src1_sel:WORD_1
v_lshlrev_b32_sdwa v154,1,v154 dst_sel:DWORD dst_unused:UNUSED_PAD src0_sel:DWORD src1_sel:WORD_1
v_exp_f16 v120,v121
v_mfma_f32_16x16x32_bf16 v[200:203],v[108:111],v[128:131],v[200:203]
ds_read_u16 v148,v148
v_exp_f16 v122,v123
v_lshlrev_b32_sdwa v153,1,v153 dst_sel:DWORD dst_unused:UNUSED_PAD src0_sel:DWORD src1_sel:WORD_1
v_mfma_f32_16x16x32_bf16 v[164:167],v[88:91],v[136:139],v[164:167]
ds_read_u16_d16_hi v150,v150
v_lshlrev_b32_sdwa v155,1,v155 dst_sel:DWORD dst_unused:UNUSED_PAD src0_sel:DWORD src1_sel:WORD_1
v_exp_f16_sdwa v120,v121 dst_sel:WORD_1 dst_unused:UNUSED_PRESERVE src0_sel:WORD_1
v_exp_f16_sdwa v122,v123 dst_sel:WORD_1 dst_unused:UNUSED_PRESERVE src0_sel:WORD_1
v_mfma_f32_16x16x32_bf16 v[172:175],v[92:95],v[136:139],v[172:175]
ds_read_u16 v149,v149
v_pk_fma_f16 v120,v120,s55,s55
v_pk_fma_f16 v122,v122,s55,s55
v_rcp_f16 v121,v120
v_mfma_f32_16x16x32_bf16 v[180:183],v[96:99],v[136:139],v[180:183]
ds_read_u16_d16_hi v151,v151
v_rcp_f16 v123,v122
v_rcp_f16_sdwa v121,v120 dst_sel:WORD_1 dst_unused:UNUSED_PRESERVE src0_sel:WORD_1
v_mfma_f32_16x16x32_bf16 v[188:191],v[100:103],v[136:139],v[188:191]
ds_read_u16 v152,v152
v_rcp_f16_sdwa v123,v122 dst_sel:WORD_1 dst_unused:UNUSED_PRESERVE src0_sel:WORD_1
v_fma_mix_f32 v156,v156,v121,0 op_sel_hi:[0,1,0]
v_mfma_f32_16x16x32_bf16 v[196:199],v[104:107],v[136:139],v[196:199]
s_setprio 0
ds_read_u16_d16_hi v154,v154
v_fma_mix_f32 v158,v158,v121,0 op_sel:[0,1,0] op_sel_hi:[0,1,0]
s_waitcnt lgkmcnt(7)
v_lshl_add_u64 v[144:145],v[144:145],0,v[146:147]
s_waitcnt lgkmcnt(2)
v_lshl_add_u64 v[146:147],v[148:149],0,v[150:151]
v_fma_mix_f32 v157,v157,v123,0 op_sel_hi:[0,1,0]
v_mfma_f32_16x16x32_bf16 v[204:207],v[108:111],v[136:139],v[204:207]
ds_read_u16 v153,v153
v_fma_mix_f32 v159,v159,v123,0 op_sel:[0,1,0] op_sel_hi:[0,1,0]
ds_read_u16_d16_hi v155,v155
s_waitcnt lgkmcnt(0)
v_lshl_add_u64 v[152:153],v[152:153],0,v[154:155]
v_cvt_pk_bf16_f32 v154,v156,v158
v_cvt_pk_bf16_f32 v155,v157,v159
v_mfma_f32_16x16x32_bf16 v[208:211],v[88:91],v[144:147],v[208:211]
s_cbranch_vccz RA24_%=
s_waitcnt vmcnt(0)
RA24_%=:
s_barrier
s_setprio 0
v_xor_b32 v9,64,v10
v_mul_f32 v160,v160,v64
v_mul_f32 v161,v161,v65
v_mul_f32 v162,v162,v66
v_mul_f32 v163,v163,v67
v_mfma_f32_16x16x32_bf16 v[216:219],v[92:95],v[144:147],v[216:219]
v_mul_f32 v168,v168,v68
v_mul_f32 v169,v169,v69
v_mul_f32 v170,v170,v70
v_mul_f32 v171,v171,v71
v_cvt_pk_bf16_f32 v16,v160,v161
v_mfma_f32_16x16x32_bf16 v[224:227],v[96:99],v[144:147],v[224:227]
v_cvt_pk_bf16_f32 v17,v162,v163
v_cvt_pk_bf16_f32 v18,v168,v169
v_cvt_pk_bf16_f32 v19,v170,v171
v_mul_f32_dpp v176,v176,v72 quad_perm:[1,0,3,2] row_mask:0xf bank_mask:0xf
v_mfma_f32_16x16x32_bf16 v[232:235],v[100:103],v[144:147],v[232:235]
v_mul_f32_dpp v177,v177,v73 quad_perm:[1,0,3,2] row_mask:0xf bank_mask:0xf
v_mul_f32_dpp v178,v178,v74 quad_perm:[1,0,3,2] row_mask:0xf bank_mask:0xf
v_mul_f32_dpp v179,v179,v75 quad_perm:[1,0,3,2] row_mask:0xf bank_mask:0xf
v_mul_f32_dpp v184,v184,v76 quad_perm:[1,0,3,2] row_mask:0xf bank_mask:0xf
v_mul_f32_dpp v185,v185,v77 quad_perm:[1,0,3,2] row_mask:0xf bank_mask:0xf
v_mfma_f32_16x16x32_bf16 v[240:243],v[104:107],v[144:147],v[240:243]
v_mul_f32_dpp v186,v186,v78 quad_perm:[1,0,3,2] row_mask:0xf bank_mask:0xf
v_mul_f32_dpp v187,v187,v79 quad_perm:[1,0,3,2] row_mask:0xf bank_mask:0xf
v_cvt_pk_bf16_f32 v24,v176,v177
v_cvt_pk_bf16_f32 v25,v178,v179
v_mfma_f32_16x16x32_bf16 v[248:251],v[108:111],v[144:147],v[248:251]
v_cvt_pk_bf16_f32 v26,v184,v185
v_cvt_pk_bf16_f32 v27,v186,v187
s_mov_b64 exec,s[12:13]
v_swap_b32 v16,v24
v_swap_b32 v17,v25
v_swap_b32 v18,v26
v_swap_b32 v19,v27
s_mov_b64 exec,-1
v_mfma_f32_16x16x32_bf16 v[212:215],v[88:91],v[152:155],v[212:215]
s_add_u32 s65,s49,0
buffer_store_dwordx4 v[16:19],v10,s[40:43],s65 offen offset:0 sc1
v_mfma_f32_16x16x32_bf16 v[220:223],v[92:95],v[152:155],v[220:223]
v_mul_f32 v192,v192,v80
v_mul_f32 v193,v193,v81
v_mul_f32 v194,v194,v82
v_mul_f32 v195,v195,v83
v_mfma_f32_16x16x32_bf16 v[228:231],v[96:99],v[152:155],v[228:231]
v_mul_f32 v200,v200,v84
v_mul_f32 v201,v201,v85
v_mul_f32 v202,v202,v86
v_mul_f32 v203,v203,v87
v_mfma_f32_16x16x32_bf16 v[236:239],v[100:103],v[152:155],v[236:239]
v_cvt_pk_bf16_f32 v32,v192,v193
v_cvt_pk_bf16_f32 v33,v194,v195
v_cvt_pk_bf16_f32 v34,v200,v201
v_cvt_pk_bf16_f32 v35,v202,v203
v_mul_f32_dpp v164,v164,v64 quad_perm:[1,0,3,2] row_mask:0xf bank_mask:0xf
v_mfma_f32_16x16x32_bf16 v[244:247],v[104:107],v[152:155],v[244:247]
v_mul_f32_dpp v165,v165,v65 quad_perm:[1,0,3,2] row_mask:0xf bank_mask:0xf
v_mul_f32_dpp v166,v166,v66 quad_perm:[1,0,3,2] row_mask:0xf bank_mask:0xf
v_mul_f32_dpp v167,v167,v67 quad_perm:[1,0,3,2] row_mask:0xf bank_mask:0xf
v_mul_f32_dpp v172,v172,v68 quad_perm:[1,0,3,2] row_mask:0xf bank_mask:0xf
v_mfma_f32_16x16x32_bf16 v[252:255],v[108:111],v[152:155],v[252:255]
s_add_u32 s65,s49,0
buffer_store_dwordx4 v[24:27],v9,s[40:43],s65 offen offset:384 sc1
v_mul_f32_dpp v173,v173,v69 quad_perm:[1,0,3,2] row_mask:0xf bank_mask:0xf
v_mul_f32_dpp v174,v174,v70 quad_perm:[1,0,3,2] row_mask:0xf bank_mask:0xf
v_mul_f32_dpp v175,v175,v71 quad_perm:[1,0,3,2] row_mask:0xf bank_mask:0xf
v_cvt_pk_bf16_f32 v20,v164,v165
v_cvt_pk_bf16_f32 v21,v166,v167
v_cvt_pk_bf16_f32 v22,v172,v173
v_cvt_pk_bf16_f32 v23,v174,v175
s_mov_b64 exec,s[12:13]
v_swap_b32 v32,v20
v_swap_b32 v33,v21
v_swap_b32 v34,v22
v_swap_b32 v35,v23
s_mov_b64 exec,-1
v_mul_f32 v180,v180,v72
v_mul_f32 v181,v181,v73
v_mul_f32 v182,v182,v74
v_mul_f32 v183,v183,v75
v_mul_f32 v188,v188,v76
v_mul_f32 v189,v189,v77
s_add_u32 s65,s49,0
buffer_store_dwordx4 v[32:35],v10,s[40:43],s65 offen offset:128 sc1
v_mul_f32 v190,v190,v78
v_mul_f32 v191,v191,v79
v_cvt_pk_bf16_f32 v28,v180,v181
v_cvt_pk_bf16_f32 v29,v182,v183
v_cvt_pk_bf16_f32 v30,v188,v189
v_cvt_pk_bf16_f32 v31,v190,v191
v_mul_f32_dpp v196,v196,v80 quad_perm:[1,0,3,2] row_mask:0xf bank_mask:0xf
v_mul_f32_dpp v197,v197,v81 quad_perm:[1,0,3,2] row_mask:0xf bank_mask:0xf
v_mul_f32_dpp v198,v198,v82 quad_perm:[1,0,3,2] row_mask:0xf bank_mask:0xf
v_mul_f32_dpp v199,v199,v83 quad_perm:[1,0,3,2] row_mask:0xf bank_mask:0xf
v_mul_f32_dpp v204,v204,v84 quad_perm:[1,0,3,2] row_mask:0xf bank_mask:0xf
v_mul_f32_dpp v205,v205,v85 quad_perm:[1,0,3,2] row_mask:0xf bank_mask:0xf
v_mul_f32_dpp v206,v206,v86 quad_perm:[1,0,3,2] row_mask:0xf bank_mask:0xf
v_mul_f32_dpp v207,v207,v87 quad_perm:[1,0,3,2] row_mask:0xf bank_mask:0xf
v_cvt_pk_bf16_f32 v36,v196,v197
v_cvt_pk_bf16_f32 v37,v198,v199
v_cvt_pk_bf16_f32 v38,v204,v205
v_cvt_pk_bf16_f32 v39,v206,v207
s_mov_b64 exec,s[12:13]
v_swap_b32 v28,v36
v_swap_b32 v29,v37
v_swap_b32 v30,v38
v_swap_b32 v31,v39
s_mov_b64 exec,-1
s_add_u32 s65,s49,0
buffer_store_dwordx4 v[20:23],v9,s[40:43],s65 offen offset:512 sc1
v_mul_f32 v208,v208,v64
v_mul_f32 v209,v209,v65
v_mul_f32 v210,v210,v66
v_mul_f32 v211,v211,v67
v_mul_f32 v216,v216,v68
v_mul_f32 v217,v217,v69
v_mul_f32 v218,v218,v70
v_mul_f32 v219,v219,v71
v_cvt_pk_bf16_f32 v40,v208,v209
v_cvt_pk_bf16_f32 v41,v210,v211
v_cvt_pk_bf16_f32 v42,v216,v217
v_cvt_pk_bf16_f32 v43,v218,v219
v_mul_f32_dpp v224,v224,v72 quad_perm:[1,0,3,2] row_mask:0xf bank_mask:0xf
v_mul_f32_dpp v225,v225,v73 quad_perm:[1,0,3,2] row_mask:0xf bank_mask:0xf
v_mul_f32_dpp v226,v226,v74 quad_perm:[1,0,3,2] row_mask:0xf bank_mask:0xf
v_mul_f32_dpp v227,v227,v75 quad_perm:[1,0,3,2] row_mask:0xf bank_mask:0xf
v_mul_f32_dpp v232,v232,v76 quad_perm:[1,0,3,2] row_mask:0xf bank_mask:0xf
v_mul_f32_dpp v233,v233,v77 quad_perm:[1,0,3,2] row_mask:0xf bank_mask:0xf
v_mul_f32_dpp v234,v234,v78 quad_perm:[1,0,3,2] row_mask:0xf bank_mask:0xf
v_mul_f32_dpp v235,v235,v79 quad_perm:[1,0,3,2] row_mask:0xf bank_mask:0xf
v_cvt_pk_bf16_f32 v48,v224,v225
v_cvt_pk_bf16_f32 v49,v226,v227
v_cvt_pk_bf16_f32 v50,v232,v233
v_cvt_pk_bf16_f32 v51,v234,v235
s_add_u32 s65,s49,0
buffer_store_dwordx4 v[28:31],v10,s[40:43],s65 offen offset:256 sc1
s_mov_b64 exec,s[12:13]
v_swap_b32 v40,v48
v_swap_b32 v41,v49
v_swap_b32 v42,v50
v_swap_b32 v43,v51
s_mov_b64 exec,-1
v_mul_f32 v240,v240,v80
v_mul_f32 v241,v241,v81
v_mul_f32 v242,v242,v82
v_mul_f32 v243,v243,v83
v_mul_f32 v248,v248,v84
v_mul_f32 v249,v249,v85
v_mul_f32 v250,v250,v86
v_mul_f32 v251,v251,v87
v_cvt_pk_bf16_f32 v56,v240,v241
v_cvt_pk_bf16_f32 v57,v242,v243
v_cvt_pk_bf16_f32 v58,v248,v249
v_cvt_pk_bf16_f32 v59,v250,v251
v_mul_f32_dpp v212,v212,v64 quad_perm:[1,0,3,2] row_mask:0xf bank_mask:0xf
v_mul_f32_dpp v213,v213,v65 quad_perm:[1,0,3,2] row_mask:0xf bank_mask:0xf
v_mul_f32_dpp v214,v214,v66 quad_perm:[1,0,3,2] row_mask:0xf bank_mask:0xf
v_mul_f32_dpp v215,v215,v67 quad_perm:[1,0,3,2] row_mask:0xf bank_mask:0xf
v_mul_f32_dpp v220,v220,v68 quad_perm:[1,0,3,2] row_mask:0xf bank_mask:0xf
s_add_u32 s65,s49,0
buffer_store_dwordx4 v[36:39],v9,s[40:43],s65 offen offset:640 sc1
v_mul_f32_dpp v221,v221,v69 quad_perm:[1,0,3,2] row_mask:0xf bank_mask:0xf
v_mul_f32_dpp v222,v222,v70 quad_perm:[1,0,3,2] row_mask:0xf bank_mask:0xf
v_mul_f32_dpp v223,v223,v71 quad_perm:[1,0,3,2] row_mask:0xf bank_mask:0xf
v_cvt_pk_bf16_f32 v44,v212,v213
v_cvt_pk_bf16_f32 v45,v214,v215
v_cvt_pk_bf16_f32 v46,v220,v221
v_cvt_pk_bf16_f32 v47,v222,v223
s_mov_b64 exec,s[12:13]
v_swap_b32 v56,v44
v_swap_b32 v57,v45
v_swap_b32 v58,v46
v_swap_b32 v59,v47
s_mov_b64 exec,-1
v_mul_f32 v228,v228,v72
v_mul_f32 v229,v229,v73
v_mul_f32 v230,v230,v74
v_mul_f32 v231,v231,v75
v_mul_f32 v236,v236,v76
v_mul_f32 v237,v237,v77
v_mul_f32 v238,v238,v78
v_mul_f32 v239,v239,v79
v_cvt_pk_bf16_f32 v52,v228,v229
v_cvt_pk_bf16_f32 v53,v230,v231
s_add_u32 s65,s49,786432
buffer_store_dwordx4 v[40:43],v10,s[40:43],s65 offen offset:0 sc1
v_cvt_pk_bf16_f32 v54,v236,v237
v_cvt_pk_bf16_f32 v55,v238,v239
v_mul_f32_dpp v244,v244,v80 quad_perm:[1,0,3,2] row_mask:0xf bank_mask:0xf
v_mul_f32_dpp v245,v245,v81 quad_perm:[1,0,3,2] row_mask:0xf bank_mask:0xf
v_mul_f32_dpp v246,v246,v82 quad_perm:[1,0,3,2] row_mask:0xf bank_mask:0xf
v_mul_f32_dpp v247,v247,v83 quad_perm:[1,0,3,2] row_mask:0xf bank_mask:0xf
v_mul_f32_dpp v252,v252,v84 quad_perm:[1,0,3,2] row_mask:0xf bank_mask:0xf
v_mul_f32_dpp v253,v253,v85 quad_perm:[1,0,3,2] row_mask:0xf bank_mask:0xf
v_mul_f32_dpp v254,v254,v86 quad_perm:[1,0,3,2] row_mask:0xf bank_mask:0xf
v_mul_f32_dpp v255,v255,v87 quad_perm:[1,0,3,2] row_mask:0xf bank_mask:0xf
v_cvt_pk_bf16_f32 v60,v244,v245
v_cvt_pk_bf16_f32 v61,v246,v247
v_cvt_pk_bf16_f32 v62,v252,v253
v_cvt_pk_bf16_f32 v63,v254,v255
s_mov_b64 exec,s[12:13]
v_swap_b32 v52,v60
v_swap_b32 v53,v61
v_swap_b32 v54,v62
v_swap_b32 v55,v63
s_mov_b64 exec,-1
s_cbranch_vccnz RA26_%=
v_and_b32 v112,63,%1
s_bfe_u32 s64,%0,0x20003
s_lshl_b32 s64,s64,13
s_bitcmp1_b32 s52,0
s_mov_b32 s66,28672
s_mov_b32 s86,0xbe801d5a
s_cselect_b32 s66,69632,s66
s_add_u32 s64,s64,s66
v_lshl_add_u32 v112,v112,7,s64
s_getpc_b64 s[14:15]
N_pgA25_%=:
s_sub_u32 s14,s14,N_pgA25_%=-N_main_%=
s_subb_u32 s15,s15,0
s_mov_b32 m0,157312
v_min_u32 v112,N_mainend_%=-N_main_%=,v112
global_load_lds_dword v112,s[14:15]
RA26_%=:
s_add_u32 s4,s10,12
s_cmp_ge_u32 s4,20
s_cselect_b32 s7,20,0
s_sub_u32 s4,s4,s7
s_mul_i32 s4,s4,3488
s_add_u32 s5,s10,14
s_cmp_ge_u32 s5,20
s_cselect_b32 s7,20,0
s_sub_u32 s5,s5,s7
s_mul_i32 s5,s5,3488
s_add_u32 s6,s10,16
s_cmp_ge_u32 s6,20
s_cselect_b32 s7,20,0
s_sub_u32 s6,s6,s7
s_mul_i32 s6,s6,3488
v_add_u32 v112,s4,v15
v_add_u32 v116,s5,v15
s_mov_b64 exec,s[8:9]
v_add_u32 v112,s5,v15
v_add_u32 v116,s6,v15
s_mov_b64 exec,-1
v_xor_b32 v113,16,v112
v_xor_b32 v117,16,v116
v_xor_b32 v114,32,v112
v_xor_b32 v118,32,v116
v_xor_b32 v115,48,v112
v_xor_b32 v119,48,v116
s_waitcnt vmcnt(8)
s_barrier
s_setprio 3
ds_read_b128 v[136:139],v116 offset:0
ds_read_b128 v[88:91],v11 offset:768
ds_read_b128 v[140:143],v116 offset:3488
ds_read_b128 v[128:131],v112 offset:0
ds_read_b128 v[132:135],v112 offset:3488
ds_read_b128 v[92:95],v12 offset:0
ds_read_b128 v[152:155],v117 offset:0
ds_read_b128 v[96:99],v11 offset:1920
ds_read_b128 v[156:159],v117 offset:3488
ds_read_b128 v[144:147],v113 offset:0
ds_read_b128 v[148:151],v113 offset:3488
ds_read_b128 v[100:103],v12 offset:1152
s_waitcnt lgkmcnt(10)
v_mfma_f32_16x16x32_bf16 v[160:163],v[88:91],v[136:139],0
s_waitcnt lgkmcnt(9)
s_add_u32 s65,s49,786432
buffer_store_dwordx4 v[48:51],v9,s[40:43],s65 offen offset:384 sc1
v_mfma_f32_16x16x32_bf16 v[184:187],v[88:91],v[140:143],0
s_waitcnt lgkmcnt(6)
ds_read_b128 v[140:143],v116 offset:3552
ds_read_b128 v[88:91],v11 offset:5376
v_smfmac_f32_16x16x64_bf16 v[160:163],v[92:95],v[128:135],v13
ds_read_b128 v[128:131],v112 offset:64
v_smfmac_f32_16x16x64_bf16 v[184:187],v[92:95],v[132:139],v13
s_waitcnt lgkmcnt(7)
ds_read_b128 v[136:139],v116 offset:64
ds_read_b128 v[132:135],v112 offset:3552
v_mfma_f32_16x16x32_bf16 v[164:167],v[96:99],v[152:155],0
s_waitcnt lgkmcnt(8)
ds_read_b128 v[92:95],v12 offset:4608
v_add_f32 v120,v160,v161
v_mul_f32 v124,v160,v160
v_fmac_f32 v124,v161,v161
v_mfma_f32_16x16x32_bf16 v[188:191],v[96:99],v[156:159],0
s_waitcnt lgkmcnt(6)
s_add_u32 s65,s49,786432
buffer_store_dwordx4 v[56:59],v10,s[40:43],s65 offen offset:128 sc1
ds_read_b128 v[156:159],v117 offset:3552
ds_read_b128 v[96:99],v11 offset:6528
v_add_f32 v121,v162,v163
v_mul_f32 v125,v162,v162
v_fmac_f32 v125,v163,v163
v_smfmac_f32_16x16x64_bf16 v[164:167],v[100:103],v[144:151],v13
ds_read_b128 v[144:147],v113 offset:64
v_add_f32 v122,v184,v185
v_mul_f32 v126,v184,v184
v_fmac_f32 v126,v185,v185
v_smfmac_f32_16x16x64_bf16 v[188:191],v[100:103],v[148:155],v13
s_waitcnt lgkmcnt(5)
ds_read_b128 v[152:155],v117 offset:64
ds_read_b128 v[148:151],v113 offset:3552
v_add_f32 v123,v186,v187
v_mul_f32 v127,v186,v186
v_fmac_f32 v127,v187,v187
v_mfma_f32_16x16x32_bf16 v[168:171],v[88:91],v[136:139],0
ds_read_b128 v[100:103],v12 offset:5760
v_add_f32 v120,v120,v164
v_fmac_f32 v124,v164,v164
v_add_f32 v120,v120,v165
v_fmac_f32 v124,v165,v165
v_mfma_f32_16x16x32_bf16 v[192:195],v[88:91],v[140:143],0
s_waitcnt lgkmcnt(6)
s_add_u32 s65,s49,786432
buffer_store_dwordx4 v[44:47],v9,s[40:43],s65 offen offset:512 sc1
ds_read_b128 v[140:143],v116 offset:3616
ds_read_b128 v[88:91],v11 offset:9984
v_add_f32 v121,v121,v166
v_fmac_f32 v125,v166,v166
v_add_f32 v121,v121,v167
v_fmac_f32 v125,v167,v167
v_smfmac_f32_16x16x64_bf16 v[168:171],v[92:95],v[128:135],v13
ds_read_b128 v[128:131],v112 offset:128
v_add_f32 v122,v122,v188
v_fmac_f32 v126,v188,v188
v_add_f32 v122,v122,v189
v_fmac_f32 v126,v189,v189
v_smfmac_f32_16x16x64_bf16 v[192:195],v[92:95],v[132:139],v13
s_waitcnt lgkmcnt(5)
ds_read_b128 v[136:139],v116 offset:128
ds_read_b128 v[132:135],v112 offset:3616
v_add_f32 v123,v123,v190
v_fmac_f32 v127,v190,v190
v_add_f32 v123,v123,v191
v_fmac_f32 v127,v191,v191
v_mfma_f32_16x16x32_bf16 v[172:175],v[96:99],v[152:155],0
ds_read_b128 v[92:95],v12 offset:9216
v_add_f32 v120,v120,v168
v_fmac_f32 v124,v168,v168
v_add_f32 v120,v120,v169
v_fmac_f32 v124,v169,v169
v_mfma_f32_16x16x32_bf16 v[196:199],v[96:99],v[156:159],0
s_waitcnt lgkmcnt(6)
s_add_u32 s65,s49,786432
buffer_store_dwordx4 v[52:55],v10,s[40:43],s65 offen offset:256 sc1
ds_read_b128 v[156:159],v117 offset:3616
ds_read_b128 v[96:99],v11 offset:11136
v_add_f32 v121,v121,v170
v_fmac_f32 v125,v170,v170
v_add_f32 v121,v121,v171
v_fmac_f32 v125,v171,v171
v_smfmac_f32_16x16x64_bf16 v[172:175],v[100:103],v[144:151],v13
ds_read_b128 v[144:147],v113 offset:128
v_add_f32 v122,v122,v192
v_fmac_f32 v126,v192,v192
v_add_f32 v122,v122,v193
v_fmac_f32 v126,v193,v193
v_smfmac_f32_16x16x64_bf16 v[196:199],v[100:103],v[148:155],v13
s_waitcnt lgkmcnt(5)
ds_read_b128 v[152:155],v117 offset:128
ds_read_b128 v[148:151],v113 offset:3616
v_add_f32 v123,v123,v194
v_fmac_f32 v127,v194,v194
v_add_f32 v123,v123,v195
v_fmac_f32 v127,v195,v195
v_mfma_f32_16x16x32_bf16 v[176:179],v[88:91],v[136:139],0
ds_read_b128 v[100:103],v12 offset:10368
v_add_f32 v120,v120,v172
v_fmac_f32 v124,v172,v172
v_add_f32 v120,v120,v173
v_fmac_f32 v124,v173,v173
v_mfma_f32_16x16x32_bf16 v[200:203],v[88:91],v[140:143],0
s_waitcnt lgkmcnt(6)
s_add_u32 s65,s49,786432
buffer_store_dwordx4 v[60:63],v9,s[40:43],s65 offen offset:640 sc1
ds_read_b128 v[140:143],v118 offset:3488
ds_read_b128 v[88:91],v11 offset:3072
v_add_f32 v121,v121,v174
v_fmac_f32 v125,v174,v174
v_add_f32 v121,v121,v175
v_fmac_f32 v125,v175,v175
v_smfmac_f32_16x16x64_bf16 v[176:179],v[92:95],v[128:135],v13
ds_read_b128 v[128:131],v114 offset:0
v_add_f32 v122,v122,v196
v_fmac_f32 v126,v196,v196
v_add_f32 v122,v122,v197
v_fmac_f32 v126,v197,v197
v_smfmac_f32_16x16x64_bf16 v[200:203],v[92:95],v[132:139],v13
s_waitcnt lgkmcnt(5)
ds_read_b128 v[136:139],v118 offset:0
ds_read_b128 v[132:135],v114 offset:3488
v_add_f32 v123,v123,v198
v_fmac_f32 v127,v198,v198
v_add_f32 v123,v123,v199
v_fmac_f32 v127,v199,v199
v_mfma_f32_16x16x32_bf16 v[180:183],v[96:99],v[152:155],0
ds_read_b128 v[92:95],v12 offset:2304
v_add_f32 v120,v120,v176
v_fmac_f32 v124,v176,v176
v_add_f32 v120,v120,v177
v_fmac_f32 v124,v177,v177
v_mfma_f32_16x16x32_bf16 v[204:207],v[96:99],v[156:159],0
s_waitcnt lgkmcnt(6)
ds_read_b128 v[156:159],v119 offset:3488
ds_read_b128 v[96:99],v11 offset:4224
v_add_f32 v121,v121,v178
v_fmac_f32 v125,v178,v178
v_add_f32 v121,v121,v179
v_fmac_f32 v125,v179,v179
v_smfmac_f32_16x16x64_bf16 v[180:183],v[100:103],v[144:151],v13
ds_read_b128 v[144:147],v115 offset:0
v_add_f32 v122,v122,v200
v_fmac_f32 v126,v200,v200
v_add_f32 v122,v122,v201
v_fmac_f32 v126,v201,v201
v_smfmac_f32_16x16x64_bf16 v[204:207],v[100:103],v[148:155],v13
s_waitcnt lgkmcnt(5)
ds_read_b128 v[152:155],v119 offset:0
ds_read_b128 v[148:151],v115 offset:3488
v_add_f32 v123,v123,v202
v_fmac_f32 v127,v202,v202
v_add_f32 v123,v123,v203
v_fmac_f32 v127,v203,v203
v_mfma_f32_16x16x32_bf16 v[208:211],v[88:91],v[136:139],0
ds_read_b128 v[100:103],v12 offset:3456
v_add_f32 v120,v120,v180
v_fmac_f32 v124,v180,v180
v_add_f32 v120,v120,v181
v_fmac_f32 v124,v181,v181
v_mfma_f32_16x16x32_bf16 v[232:235],v[88:91],v[140:143],0
s_waitcnt lgkmcnt(6)
ds_read_b128 v[140:143],v118 offset:3552
ds_read_b128 v[88:91],v11 offset:7680
v_add_f32 v121,v121,v182
v_fmac_f32 v125,v182,v182
v_add_f32 v121,v121,v183
v_fmac_f32 v125,v183,v183
v_smfmac_f32_16x16x64_bf16 v[208:211],v[92:95],v[128:135],v13
ds_read_b128 v[128:131],v114 offset:64
v_add_f32 v122,v122,v204
v_fmac_f32 v126,v204,v204
v_add_f32 v122,v122,v205
v_fmac_f32 v126,v205,v205
v_smfmac_f32_16x16x64_bf16 v[232:235],v[92:95],v[132:139],v13
s_waitcnt lgkmcnt(5)
ds_read_b128 v[136:139],v118 offset:64
ds_read_b128 v[132:135],v114 offset:3552
v_add_f32 v123,v123,v206
v_fmac_f32 v127,v206,v206
v_add_f32 v123,v123,v207
v_fmac_f32 v127,v207,v207
v_mfma_f32_16x16x32_bf16 v[212:215],v[96:99],v[152:155],0
ds_read_b128 v[92:95],v12 offset:6912
v_add_f32 v120,v120,v208
v_fmac_f32 v124,v208,v208
v_add_f32 v120,v120,v209
v_fmac_f32 v124,v209,v209
v_mfma_f32_16x16x32_bf16 v[236:239],v[96:99],v[156:159],0
s_waitcnt lgkmcnt(6)
s_setprio 2
ds_read_b128 v[156:159],v119 offset:3552
ds_read_b128 v[96:99],v11 offset:8832
v_add_f32 v121,v121,v210
v_fmac_f32 v125,v210,v210
v_add_f32 v121,v121,v211
v_fmac_f32 v125,v211,v211
v_smfmac_f32_16x16x64_bf16 v[212:215],v[100:103],v[144:151],v13
ds_read_b128 v[144:147],v115 offset:64
v_add_f32 v122,v122,v232
v_fmac_f32 v126,v232,v232
v_add_f32 v122,v122,v233
v_fmac_f32 v126,v233,v233
v_smfmac_f32_16x16x64_bf16 v[236:239],v[100:103],v[148:155],v13
s_waitcnt lgkmcnt(5)
ds_read_b128 v[152:155],v119 offset:64
ds_read_b128 v[148:151],v115 offset:3552
v_add_f32 v123,v123,v234
v_fmac_f32 v127,v234,v234
v_add_f32 v123,v123,v235
v_fmac_f32 v127,v235,v235
v_mfma_f32_16x16x32_bf16 v[216:219],v[88:91],v[136:139],0
ds_read_b128 v[100:103],v12 offset:8064
v_add_f32 v120,v120,v212
v_fmac_f32 v124,v212,v212
v_add_f32 v120,v120,v213
v_fmac_f32 v124,v213,v213
v_add_f32 v121,v121,v214
v_mfma_f32_16x16x32_bf16 v[240:243],v[88:91],v[140:143],0
s_waitcnt lgkmcnt(6)
ds_read_b128 v[140:143],v118 offset:3616
ds_read_b128 v[88:91],v11 offset:12288
v_fmac_f32 v125,v214,v214
v_add_f32 v121,v121,v215
v_fmac_f32 v125,v215,v215
v_add_f32 v122,v122,v236
v_smfmac_f32_16x16x64_bf16 v[216:219],v[92:95],v[128:135],v13
ds_read_b128 v[128:131],v114 offset:128
v_fmac_f32 v126,v236,v236
v_add_f32 v122,v122,v237
v_fmac_f32 v126,v237,v237
v_add_f32 v123,v123,v238
v_fmac_f32 v127,v238,v238
v_smfmac_f32_16x16x64_bf16 v[240:243],v[92:95],v[132:139],v13
s_waitcnt lgkmcnt(5)
ds_read_b128 v[136:139],v118 offset:128
ds_read_b128 v[132:135],v114 offset:3616
v_add_f32 v123,v123,v239
v_fmac_f32 v127,v239,v239
v_add_u32 v112,v112,v8
v_add_u32 v116,v116,v8
v_mfma_f32_16x16x32_bf16 v[220:223],v[96:99],v[152:155],0
ds_read_b128 v[92:95],v12 offset:11520
ds_read_b128 v[104:107],v14 offset:0
v_add_f32 v120,v120,v216
v_fmac_f32 v124,v216,v216
v_add_f32 v120,v120,v217
v_fmac_f32 v124,v217,v217
v_mfma_f32_16x16x32_bf16 v[244:247],v[96:99],v[156:159],0
s_waitcnt lgkmcnt(7)
ds_read_b128 v[108:111],v7 offset:3584
ds_read_b128 v[156:159],v119 offset:3616
v_add_f32 v121,v121,v218
v_fmac_f32 v125,v218,v218
v_add_f32 v121,v121,v219
v_fmac_f32 v125,v219,v219
v_smfmac_f32_16x16x64_bf16 v[220:223],v[100:103],v[144:151],v13
ds_read_b128 v[96:99],v11 offset:13440
ds_read_b128 v[144:147],v115 offset:128
v_add_f32 v122,v122,v240
v_fmac_f32 v126,v240,v240
v_add_f32 v122,v122,v241
v_fmac_f32 v126,v241,v241
v_smfmac_f32_16x16x64_bf16 v[244:247],v[100:103],v[148:155],v13
s_waitcnt lgkmcnt(7)
ds_read_b128 v[152:155],v119 offset:128
ds_read_b128 v[148:151],v115 offset:3616
v_add_f32 v123,v123,v242
v_fmac_f32 v127,v242,v242
v_add_f32 v123,v123,v243
v_fmac_f32 v127,v243,v243
v_mfma_f32_16x16x32_bf16 v[224:227],v[88:91],v[136:139],0
ds_read_b128 v[100:103],v12 offset:12672
v_add_f32 v120,v120,v220
v_fmac_f32 v124,v220,v220
v_add_f32 v120,v120,v221
v_fmac_f32 v124,v221,v221
v_mfma_f32_16x16x32_bf16 v[248:251],v[88:91],v[140:143],0
s_waitcnt lgkmcnt(8)
s_setprio 1
ds_read_b128 v[140:143],v116 offset:192
ds_read_b128 v[88:91],v112 offset:3616
v_add_f32 v121,v121,v222
v_fmac_f32 v125,v222,v222
v_add_f32 v121,v121,v223
v_fmac_f32 v125,v223,v223
v_smfmac_f32_16x16x64_bf16 v[224:227],v[92:95],v[128:135],v13
ds_read_b128 v[128:131],v112 offset:3488
v_add_f32 v122,v122,v244
v_fmac_f32 v126,v244,v244
v_add_f32 v122,v122,v245
v_fmac_f32 v126,v245,v245
v_smfmac_f32_16x16x64_bf16 v[248:251],v[92:95],v[132:139],v13
s_waitcnt lgkmcnt(5)
ds_read_b128 v[136:139],v116 offset:0
ds_read_b128 v[132:135],v112 offset:3680
v_add_f32 v123,v123,v246
v_fmac_f32 v127,v246,v246
v_add_f32 v123,v123,v247
v_fmac_f32 v127,v247,v247
v_mfma_f32_16x16x32_bf16 v[228:231],v[96:99],v[152:155],0
ds_read_b128 v[92:95],v112 offset:3808
v_add_f32 v120,v120,v224
v_fmac_f32 v124,v224,v224
v_add_f32 v120,v120,v225
v_fmac_f32 v124,v225,v225
v_mfma_f32_16x16x32_bf16 v[252:255],v[96:99],v[156:159],0
s_waitcnt lgkmcnt(6)
s_cbranch_vccnz RA27_%=
s_mov_b64 exec,s[62:63]
s_add_u32 m0,s57,34880
s_add_u32 s64,s56,4915200
buffer_load_dwordx4 v2,s[36:39],s64 offen lds
s_add_u32 m0,s57,36608
s_nop 0
buffer_load_dwordx4 v3,s[36:39],s64 offen lds
s_mov_b64 exec,-1
RA27_%=:
ds_read_b128 v[156:159],v116 offset:256
ds_read_b128 v[96:99],v116 offset:128
v_add_f32 v121,v121,v226
v_fmac_f32 v125,v226,v226
v_add_f32 v121,v121,v227
v_fmac_f32 v125,v227,v227
v_smfmac_f32_16x16x64_bf16 v[228:231],v[100:103],v[144:151],v13
ds_read_b128 v[144:147],v112 offset:3552
v_add_f32 v122,v122,v248
v_fmac_f32 v126,v248,v248
v_add_f32 v122,v122,v249
v_fmac_f32 v126,v249,v249
v_smfmac_f32_16x16x64_bf16 v[252:255],v[100:103],v[148:155],v13
s_setprio 0
ds_read_b128 v[152:155],v116 offset:64
ds_read_b128 v[148:151],v112 offset:3744
v_add_f32 v123,v123,v250
v_fmac_f32 v127,v250,v250
v_add_f32 v123,v123,v251
v_fmac_f32 v127,v251,v251
ds_read_b128 v[100:103],v116 offset:320
v_add_f32 v120,v120,v228
v_fmac_f32 v124,v228,v228
v_add_f32 v120,v120,v229
v_fmac_f32 v124,v229,v229
v_add_f32 v121,v121,v230
v_fmac_f32 v125,v230,v230
v_add_f32 v121,v121,v231
v_fmac_f32 v125,v231,v231
s_waitcnt lgkmcnt(0)
s_barrier
s_setprio 0
v_add_f32 v122,v122,v252
s_cbranch_vccnz RA28_%=
s_mov_b64 exec,s[62:63]
s_add_u32 m0,s57,38368
s_add_u32 s64,s56,5013504
buffer_load_dwordx4 v2,s[36:39],s64 offen lds
s_add_u32 m0,s57,40096
s_nop 0
buffer_load_dwordx4 v3,s[36:39],s64 offen lds
s_mov_b64 exec,-1
RA28_%=:
v_fmac_f32 v126,v252,v252
v_add_f32 v122,v122,v253
s_cbranch_vccnz RA29_%=
s_mov_b64 exec,s[62:63]
s_add_u32 m0,s57,41856
s_add_u32 s64,s56,5111808
buffer_load_dwordx4 v2,s[36:39],s64 offen lds
s_add_u32 m0,s57,43584
s_nop 0
buffer_load_dwordx4 v3,s[36:39],s64 offen lds
s_mov_b64 exec,-1
RA29_%=:
v_fmac_f32 v126,v253,v253
v_add_f32 v123,v123,v254
v_fmac_f32 v127,v254,v254
v_add_f32 v123,v123,v255
v_fmac_f32 v127,v255,v255
s_setprio 3
ds_read_b128 v[56:59],v14 offset:256
ds_read_b128 v[60:63],v7 offset:3616
s_setprio 0
v_permlane32_swap_b32 v120,v122
v_permlane32_swap_b32 v121,v123
v_permlane32_swap_b32 v124,v126
v_permlane32_swap_b32 v125,v127
v_add_f32 v120,v120,v122
v_add_f32 v121,v121,v123
v_add_f32 v124,v124,v126
v_add_f32 v125,v125,v127
v_permlane16_swap_b32 v120,v121
s_nop 0
v_permlane16_swap_b32 v124,v125
v_add_f32 v113,v120,v121
v_add_f32 v114,v124,v125
v_mul_f32 v113,0x3c2aaaab,v113
v_mul_f32 v114,0x3c2aaaab,v114
v_mul_f32 v115,0x5e000000,v114
v_fma_f32 v114,-v113,v113,v114
v_pk_maximum3_f16 v1,v1,v115,v115 op_sel:[0,1,1] op_sel_hi:[1,1,1]
v_max_f32 v114,0,v114
v_add_f32 v114,0x3727c5ac,v114
v_rsq_f32 v114,v114
s_nop 0
v_mul_f32 v120,0x8befe000,v114
v_mul_f32 v121,0x8befe000,v114
v_mul_f32 v124,-v113,v120
v_mul_f32 v125,-v113,v120
v_permlane16_swap_b32 v120,v121
s_nop 0
v_permlane16_swap_b32 v124,v125
v_mov_b32 v122,v120
v_mov_b32 v123,v121
s_cbranch_vccnz RA30_%=
s_mov_b64 exec,s[62:63]
s_add_u32 m0,s57,45344
s_add_u32 s64,s56,5210112
buffer_load_dwordx4 v2,s[36:39],s64 offen lds
s_add_u32 m0,s57,47072
s_nop 0
buffer_load_dwordx4 v3,s[36:39],s64 offen lds
s_mov_b64 exec,-1
RA30_%=:
v_mov_b32 v126,v124
v_mov_b32 v127,v125
v_permlane32_swap_b32 v120,v122
v_permlane32_swap_b32 v124,v126
v_permlane32_swap_b32 v121,v123
v_permlane32_swap_b32 v125,v127
v_add_u32 v8,94432,v4
v_add_u32 v9,94432,v5
v_fma_f32 v160,v160,v120,v124
s_setprio 3
ds_read_b64_tr_b16 v[64:65],v4 offset:512
s_setprio 0
v_fma_f32 v161,v161,v120,v124
s_setprio 3
ds_read_b64_tr_b16 v[66:67],v4 offset:2560
s_setprio 0
v_fma_f32 v162,v162,v121,v125
s_setprio 3
ds_read_b128 v[112:115],v7 offset:896
s_setprio 0
v_fma_f32 v163,v163,v121,v125
s_setprio 3
ds_read_b64_tr_b16 v[68:69],v5 offset:512
s_setprio 0
v_fma_f32 v164,v164,v120,v124
s_setprio 3
ds_read_b64_tr_b16 v[70:71],v5 offset:2560
s_setprio 0
v_fma_f32 v165,v165,v120,v124
s_setprio 3
ds_read_b128 v[116:119],v7 offset:928
s_setprio 0
v_fma_f32 v166,v166,v121,v125
s_setprio 3
ds_read_b64_tr_b16 v[72:73],v4 offset:4608
s_setprio 0
v_fma_f32 v167,v167,v121,v125
s_setprio 3
ds_read_b64_tr_b16 v[74:75],v4 offset:6656
s_setprio 0
v_fma_f32 v208,v208,v120,v124
s_setprio 3
ds_read_b64_tr_b16 v[76:77],v5 offset:4608
s_setprio 0
v_fma_f32 v209,v209,v120,v124
s_setprio 3
ds_read_b64_tr_b16 v[78:79],v5 offset:6656
s_setprio 0
v_fma_f32 v210,v210,v121,v125
s_setprio 3
ds_read_b64_tr_b16 v[80:81],v4 offset:8704
s_setprio 0
v_fma_f32 v211,v211,v121,v125
s_setprio 3
ds_read_b64_tr_b16 v[82:83],v4 offset:10752
s_setprio 0
v_fma_f32 v212,v212,v120,v124
s_setprio 3
ds_read_b64_tr_b16 v[84:85],v5 offset:8704
s_setprio 0
v_fma_f32 v213,v213,v120,v124
s_setprio 3
ds_read_b64_tr_b16 v[86:87],v5 offset:10752
s_setprio 0
v_fma_f32 v214,v214,v121,v125
v_fma_f32 v215,v215,v121,v125
v_cvt_pk_bf16_f32 v16,v160,v161
v_cvt_pk_bf16_f32 v20,v162,v163
v_cvt_pk_bf16_f32 v17,v164,v165
v_cvt_pk_bf16_f32 v21,v166,v167
v_cvt_pk_bf16_f32 v18,v208,v209
v_cvt_pk_bf16_f32 v22,v210,v211
v_cvt_pk_bf16_f32 v19,v212,v213
v_cvt_pk_bf16_f32 v23,v214,v215
v_fma_f32 v168,v168,v120,v124
v_fma_f32 v169,v169,v120,v124
v_mfma_f32_16x16x32_bf16 v[160:163],v[104:107],v[128:131],v[108:111]
v_fma_f32 v170,v170,v121,v125
v_fma_f32 v171,v171,v121,v125
v_fma_f32 v172,v172,v120,v124
v_fma_f32 v173,v173,v120,v124
v_fma_f32 v174,v174,v121,v125
v_fma_f32 v175,v175,v121,v125
v_mfma_f32_16x16x32_bf16 v[164:167],v[104:107],v[132:135],v[108:111]
s_cbranch_vccnz RA31_%=
s_mov_b64 exec,s[62:63]
s_add_u32 m0,s57,48832
s_add_u32 s64,s56,5308416
buffer_load_dwordx4 v2,s[36:39],s64 offen lds
s_add_u32 m0,s57,50560
s_nop 0
buffer_load_dwordx4 v3,s[36:39],s64 offen lds
s_mov_b64 exec,-1
RA31_%=:
v_fma_f32 v216,v216,v120,v124
v_fma_f32 v217,v217,v120,v124
v_fma_f32 v218,v218,v121,v125
v_fma_f32 v219,v219,v121,v125
v_fma_f32 v220,v220,v120,v124
v_fma_f32 v221,v221,v120,v124
v_mfma_f32_16x16x32_bf16 v[208:211],v[104:107],v[136:139],v[108:111]
v_fma_f32 v222,v222,v121,v125
v_fma_f32 v223,v223,v121,v125
v_cvt_pk_bf16_f32 v24,v168,v169
v_cvt_pk_bf16_f32 v28,v170,v171
v_cvt_pk_bf16_f32 v25,v172,v173
v_cvt_pk_bf16_f32 v29,v174,v175
s_setprio 3
v_mfma_f32_16x16x32_bf16 v[212:215],v[104:107],v[140:143],v[108:111]
ds_read_b128 v[104:107],v14 offset:512
s_setprio 0
v_cvt_pk_bf16_f32 v26,v216,v217
v_cvt_pk_bf16_f32 v30,v218,v219
v_cvt_pk_bf16_f32 v27,v220,v221
v_cvt_pk_bf16_f32 v31,v222,v223
v_fma_f32 v176,v176,v120,v124
v_fma_f32 v177,v177,v120,v124
s_waitcnt lgkmcnt(15)
s_setprio 3
v_mfma_f32_16x16x32_bf16 v[168:171],v[56:59],v[128:131],v[60:63]
ds_read_b128 v[108:111],v7 offset:3712
s_setprio 0
v_fma_f32 v178,v178,v121,v125
v_fma_f32 v179,v179,v121,v125
v_fma_f32 v180,v180,v120,v124
v_fma_f32 v181,v181,v120,v124
v_fma_f32 v182,v182,v121,v125
v_fma_f32 v183,v183,v121,v125
v_mfma_f32_16x16x32_bf16 v[172:175],v[56:59],v[132:135],v[60:63]
v_fma_f32 v224,v224,v120,v124
v_fma_f32 v225,v225,v120,v124
v_fma_f32 v226,v226,v121,v125
v_fma_f32 v227,v227,v121,v125
v_fma_f32 v228,v228,v120,v124
v_fma_f32 v229,v229,v120,v124
v_mfma_f32_16x16x32_bf16 v[216:219],v[56:59],v[136:139],v[60:63]
s_cbranch_vccnz RA32_%=
s_mov_b64 exec,s[62:63]
s_add_u32 m0,s57,52320
s_add_u32 s64,s56,5406720
buffer_load_dwordx4 v2,s[36:39],s64 offen lds
s_add_u32 m0,s57,54048
s_nop 0
buffer_load_dwordx4 v3,s[36:39],s64 offen lds
s_mov_b64 exec,-1
RA32_%=:
v_fma_f32 v230,v230,v121,v125
v_fma_f32 v231,v231,v121,v125
v_cvt_pk_bf16_f32 v32,v176,v177
v_cvt_pk_bf16_f32 v36,v178,v179
v_cvt_pk_bf16_f32 v33,v180,v181
v_cvt_pk_bf16_f32 v37,v182,v183
s_setprio 3
v_mfma_f32_16x16x32_bf16 v[220:223],v[56:59],v[140:143],v[60:63]
ds_read_b128 v[56:59],v14 offset:768
s_setprio 0
v_cvt_pk_bf16_f32 v34,v224,v225
v_cvt_pk_bf16_f32 v38,v226,v227
v_cvt_pk_bf16_f32 v35,v228,v229
v_cvt_pk_bf16_f32 v39,v230,v231
v_fma_f32 v184,v184,v122,v126
s_waitcnt lgkmcnt(1)
s_setprio 3
v_mfma_f32_16x16x32_bf16 v[176:179],v[104:107],v[144:147],v[108:111]
ds_read_b128 v[60:63],v7 offset:3744
s_setprio 0
v_fma_f32 v185,v185,v122,v126
v_fma_f32 v186,v186,v123,v127
v_fma_f32 v187,v187,v123,v127
v_mfma_f32_16x16x32_bf16 v[128:131],v[64:67],v[16:19],v[112:115]
v_fma_f32 v188,v188,v122,v126
v_fma_f32 v189,v189,v122,v126
v_fma_f32 v190,v190,v123,v127
v_mfma_f32_16x16x32_bf16 v[180:183],v[104:107],v[148:151],v[108:111]
v_fma_f32 v191,v191,v123,v127
v_fma_f32 v232,v232,v122,v126
v_fma_f32 v233,v233,v122,v126
v_mfma_f32_16x16x32_bf16 v[136:139],v[64:67],v[20:23],v[112:115]
v_fma_f32 v234,v234,v123,v127
v_fma_f32 v235,v235,v123,v127
v_fma_f32 v236,v236,v122,v126
v_mfma_f32_16x16x32_bf16 v[224:227],v[104:107],v[152:155],v[108:111]
v_fma_f32 v237,v237,v122,v126
v_fma_f32 v238,v238,v123,v127
v_fma_f32 v239,v239,v123,v127
v_mfma_f32_16x16x32_bf16 v[140:143],v[68:71],v[20:23],v[116:119]
s_cbranch_vccnz RA33_%=
s_mov_b64 exec,s[62:63]
s_add_u32 m0,s57,55808
s_add_u32 s64,s56,5505024
buffer_load_dwordx4 v2,s[36:39],s64 offen lds
s_add_u32 m0,s57,57536
s_nop 0
buffer_load_dwordx4 v3,s[36:39],s64 offen lds
s_mov_b64 exec,-1
RA33_%=:
v_cvt_pk_bf16_f32 v40,v184,v185
v_cvt_pk_bf16_f32 v44,v186,v187
v_cvt_pk_bf16_f32 v41,v188,v189
s_setprio 3
v_mfma_f32_16x16x32_bf16 v[228:231],v[104:107],v[156:159],v[108:111]
ds_read_b128 v[104:107],v14 offset:1024
s_setprio 0
v_cvt_pk_bf16_f32 v45,v190,v191
v_cvt_pk_bf16_f32 v42,v232,v233
v_cvt_pk_bf16_f32 v46,v234,v235
s_setprio 3
v_mfma_f32_16x16x32_bf16 v[132:135],v[68:71],v[16:19],v[116:119]
ds_read_b128 v[108:111],v7 offset:3840
s_setprio 0
v_cvt_pk_bf16_f32 v43,v236,v237
v_cvt_pk_bf16_f32 v47,v238,v239
v_fma_f32 v192,v192,v122,v126
s_waitcnt lgkmcnt(2)
s_mov_b32 s86,0xbe801d5a
v_mfma_f32_16x16x32_bf16 v[184:187],v[56:59],v[144:147],v[60:63]
v_fma_f32 v193,v193,v122,v126
v_fma_f32 v194,v194,v123,v127
v_mfma_f32_16x16x32_bf16 v[128:131],v[72:75],v[24:27],v[128:131]
v_fma_f32 v195,v195,v123,v127
v_fma_f32 v196,v196,v122,v126
v_mfma_f32_16x16x32_bf16 v[188:191],v[56:59],v[148:151],v[60:63]
v_fma_f32 v197,v197,v122,v126
v_fma_f32 v198,v198,v123,v127
v_mfma_f32_16x16x32_bf16 v[136:139],v[72:75],v[28:31],v[136:139]
v_fma_f32 v199,v199,v123,v127
v_fma_f32 v240,v240,v122,v126
v_mfma_f32_16x16x32_bf16 v[232:235],v[56:59],v[152:155],v[60:63]
v_fma_f32 v241,v241,v122,v126
v_fma_f32 v242,v242,v123,v127
v_mfma_f32_16x16x32_bf16 v[140:143],v[76:79],v[28:31],v[140:143]
v_fma_f32 v243,v243,v123,v127
v_fma_f32 v244,v244,v122,v126
v_mfma_f32_16x16x32_bf16 v[236:239],v[56:59],v[156:159],v[60:63]
s_cbranch_vccnz RA34_%=
s_mov_b64 exec,s[62:63]
s_add_u32 m0,s57,59296
s_add_u32 s64,s56,5603328
buffer_load_dwordx4 v2,s[36:39],s64 offen lds
s_add_u32 m0,s57,61024
s_nop 0
buffer_load_dwordx4 v3,s[36:39],s64 offen lds
s_mov_b64 exec,-1
RA34_%=:
s_setprio 3
ds_read_b128 v[144:147],v14 offset:1280
s_setprio 0
v_fma_f32 v245,v245,v122,v126
v_fma_f32 v246,v246,v123,v127
s_setprio 3
v_mfma_f32_16x16x32_bf16 v[132:135],v[76:79],v[24:27],v[132:135]
ds_read_b128 v[148:151],v7 offset:3872
s_setprio 0
v_fma_f32 v247,v247,v123,v127
v_cvt_pk_bf16_f32 v48,v192,v193
v_cvt_pk_bf16_f32 v52,v194,v195
v_cvt_pk_bf16_f32 v49,v196,v197
v_cvt_pk_bf16_f32 v53,v198,v199
v_cvt_pk_bf16_f32 v50,v240,v241
v_cvt_pk_bf16_f32 v54,v242,v243
v_cvt_pk_bf16_f32 v51,v244,v245
v_cvt_pk_bf16_f32 v55,v246,v247
v_fma_f32 v200,v200,v122,v126
s_waitcnt lgkmcnt(2)
v_mfma_f32_16x16x32_bf16 v[192:195],v[104:107],v[88:91],v[108:111]
v_fma_f32 v201,v201,v122,v126
v_fma_f32 v202,v202,v123,v127
v_fma_f32 v203,v203,v123,v127
v_mfma_f32_16x16x32_bf16 v[128:131],v[80:83],v[32:35],v[128:131]
v_fma_f32 v204,v204,v122,v126
v_fma_f32 v205,v205,v122,v126
v_fma_f32 v206,v206,v123,v127
v_mfma_f32_16x16x32_bf16 v[196:199],v[104:107],v[92:95],v[108:111]
v_fma_f32 v207,v207,v123,v127
v_fma_f32 v248,v248,v122,v126
v_fma_f32 v249,v249,v122,v126
v_mfma_f32_16x16x32_bf16 v[136:139],v[80:83],v[36:39],v[136:139]
v_fma_f32 v250,v250,v123,v127
v_fma_f32 v251,v251,v123,v127
v_fma_f32 v252,v252,v122,v126
v_mfma_f32_16x16x32_bf16 v[240:243],v[104:107],v[96:99],v[108:111]
v_fma_f32 v253,v253,v122,v126
v_fma_f32 v254,v254,v123,v127
v_fma_f32 v255,v255,v123,v127
v_mfma_f32_16x16x32_bf16 v[140:143],v[84:87],v[36:39],v[140:143]
s_cbranch_vccnz RA35_%=
s_mov_b64 exec,s[62:63]
s_add_u32 m0,s57,62784
s_add_u32 s64,s56,5701632
buffer_load_dwordx4 v2,s[36:39],s64 offen lds
s_add_u32 m0,s57,64512
s_nop 0
buffer_load_dwordx4 v3,s[36:39],s64 offen lds
s_mov_b64 exec,-1
RA35_%=:
v_cvt_pk_bf16_f32 v56,v200,v201
v_cvt_pk_bf16_f32 v60,v202,v203
v_cvt_pk_bf16_f32 v57,v204,v205
v_mfma_f32_16x16x32_bf16 v[244:247],v[104:107],v[100:103],v[108:111]
v_cvt_pk_bf16_f32 v61,v206,v207
v_cvt_pk_bf16_f32 v58,v248,v249
v_cvt_pk_bf16_f32 v62,v250,v251
v_mfma_f32_16x16x32_bf16 v[132:135],v[84:87],v[32:35],v[132:135]
v_cvt_pk_bf16_f32 v59,v252,v253
v_cvt_pk_bf16_f32 v63,v254,v255
s_waitcnt lgkmcnt(0)
v_mfma_f32_16x16x32_bf16 v[200:203],v[144:147],v[88:91],v[148:151]
s_barrier
s_setprio 3
ds_read_b64_tr_b16 v[104:105],v6 offset:12448
v_lshlrev_b32_sdwa v128,1,v128 dst_sel:DWORD dst_unused:UNUSED_PAD src0_sel:DWORD src1_sel:WORD_1
v_lshlrev_b32_sdwa v130,1,v130 dst_sel:DWORD dst_unused:UNUSED_PAD src0_sel:DWORD src1_sel:WORD_1
v_lshlrev_b32_sdwa v129,1,v129 dst_sel:DWORD dst_unused:UNUSED_PAD src0_sel:DWORD src1_sel:WORD_1
v_mfma_f32_16x16x32_bf16 v[204:207],v[144:147],v[92:95],v[148:151]
s_cbranch_vccz RA36_%=
s_add_u32 m0,s58,0
s_mul_i32 s69,s59,9
s_add_u32 s69,s69,s97
buffer_load_dwordx4 v2,s[44:47],s69 offen lds
s_add_u32 m0,m0,1024
s_add_u32 s69,s69,s53
buffer_load_dwordx4 v3,s[44:47],s69 offen lds
s_add_u32 m0,m0,1024
s_add_u32 s69,s69,s53
buffer_load_dwordx4 v2,s[44:47],s69 offen lds
s_add_u32 m0,m0,s50
s_add_u32 s69,s69,s53
buffer_load_dwordx4 v3,s[44:47],s69 offen lds
RA36_%=:
ds_read_u16 v128,v128
ds_read_b64_tr_b16 v[106:107],v6 offset:13984
v_lshlrev_b32_sdwa v131,1,v131 dst_sel:DWORD dst_unused:UNUSED_PAD src0_sel:DWORD src1_sel:WORD_1
v_fma_mixlo_f16 v120,v140,s99,0
v_fma_mixlo_f16 v122,v141,s99,0
v_mfma_f32_16x16x32_bf16 v[248:251],v[144:147],v[96:99],v[148:151]
ds_read_u16_d16_hi v130,v130
ds_read_b64_tr_b16 v[108:109],v6 offset:12456
v_lshlrev_b32_sdwa v132,1,v132 dst_sel:DWORD dst_unused:UNUSED_PAD src0_sel:DWORD src1_sel:WORD_1
v_lshlrev_b32_sdwa v134,1,v134 dst_sel:DWORD dst_unused:UNUSED_PAD src0_sel:DWORD src1_sel:WORD_1
v_mfma_f32_16x16x32_bf16 v[252:255],v[144:147],v[100:103],v[148:151]
ds_read_u16 v129,v129
ds_read_b64_tr_b16 v[110:111],v6 offset:13992
v_fma_mixhi_f16 v120,v142,s99,0
v_fma_mixhi_f16 v122,v143,s99,0
v_lshlrev_b32_sdwa v133,1,v133 dst_sel:DWORD dst_unused:UNUSED_PAD src0_sel:DWORD src1_sel:WORD_1
v_pk_fma_f16 v121,v120,v120,s54
v_mfma_f32_16x16x32_bf16 v[144:147],v[64:67],v[40:43],v[112:115]
s_cbranch_vccz RA37_%=
s_add_u32 m0,m0,1024
s_add_u32 s69,s69,s53
buffer_load_dwordx4 v2,s[44:47],s69 offen lds
s_add_u32 m0,m0,1024
s_add_u32 s69,s69,s53
buffer_load_dwordx4 v3,s[44:47],s69 offen lds
s_add_u32 m0,m0,s50
s_add_u32 s69,s69,s53
buffer_load_dwordx4 v2,s[44:47],s69 offen lds
s_add_u32 m0,m0,1024
s_add_u32 s69,s69,s53
buffer_load_dwordx4 v3,s[44:47],s69 offen lds
RA37_%=:
ds_read_u16_d16_hi v131,v131
ds_read_b64_tr_b16 v[88:89],v6 offset:12320
v_lshlrev_b32_sdwa v135,1,v135 dst_sel:DWORD dst_unused:UNUSED_PAD src0_sel:DWORD src1_sel:WORD_1
v_pk_fma_f16 v123,v122,v122,s54
v_pk_mul_f16 v121,v120,v121
v_mfma_f32_16x16x32_bf16 v[152:155],v[64:67],v[44:47],v[112:115]
ds_read_u16 v132,v132
ds_read_b64_tr_b16 v[90:91],v6 offset:13856
v_lshlrev_b32_sdwa v136,1,v136 dst_sel:DWORD dst_unused:UNUSED_PAD src0_sel:DWORD src1_sel:WORD_1
v_pk_mul_f16 v123,v122,v123
v_mfma_f32_16x16x32_bf16 v[156:159],v[68:71],v[44:47],v[116:119]
s_cbranch_vccnz RA38_%=
s_mov_b64 exec,s[62:63]
s_add_u32 m0,s57,66272
s_add_u32 s64,s56,5799936
buffer_load_dwordx4 v2,s[36:39],s64 offen lds
s_add_u32 m0,s57,68000
s_nop 0
buffer_load_dwordx4 v3,s[36:39],s64 offen lds
s_mov_b64 exec,-1
RA38_%=:
ds_read_u16_d16_hi v134,v134
ds_read_b64_tr_b16 v[92:93],v6 offset:12328
v_lshlrev_b32_sdwa v138,1,v138 dst_sel:DWORD dst_unused:UNUSED_PAD src0_sel:DWORD src1_sel:WORD_1
v_exp_f16 v120,v121
v_mfma_f32_16x16x32_bf16 v[148:151],v[68:71],v[40:43],v[116:119]
ds_read_u16 v133,v133
ds_read_b64_tr_b16 v[94:95],v6 offset:13864
v_lshlrev_b32_sdwa v137,1,v137 dst_sel:DWORD dst_unused:UNUSED_PAD src0_sel:DWORD src1_sel:WORD_1
v_lshlrev_b32_sdwa v139,1,v139 dst_sel:DWORD dst_unused:UNUSED_PAD src0_sel:DWORD src1_sel:WORD_1
v_exp_f16 v122,v123
v_mfma_f32_16x16x32_bf16 v[144:147],v[72:75],v[48:51],v[144:147]
s_cbranch_vccz RA39_%=
s_add_u32 m0,m0,1024
s_add_u32 s69,s69,s53
buffer_load_dwordx4 v2,s[44:47],s69 offen lds
s_add_u32 m0,m0,s50
s_add_u32 s69,s69,s53
buffer_load_dwordx4 v3,s[44:47],s69 offen lds
s_add_u32 m0,m0,1024
s_add_u32 s69,s69,s53
buffer_load_dwordx4 v2,s[44:47],s69 offen lds
s_add_u32 m0,m0,1024
s_add_u32 s69,s69,s53
buffer_load_dwordx4 v3,s[44:47],s69 offen lds
RA39_%=:
ds_read_u16_d16_hi v135,v135
ds_read_b64_tr_b16 v[96:97],v6 offset:12384
v_exp_f16_sdwa v122,v123 dst_sel:WORD_1 dst_unused:UNUSED_PRESERVE src0_sel:WORD_1
v_mfma_f32_16x16x32_bf16 v[152:155],v[72:75],v[52:55],v[152:155]
ds_read_u16 v136,v136
ds_read_b64_tr_b16 v[98:99],v6 offset:13920
v_exp_f16_sdwa v120,v121 dst_sel:WORD_1 dst_unused:UNUSED_PRESERVE src0_sel:WORD_1
v_mfma_f32_16x16x32_bf16 v[156:159],v[76:79],v[52:55],v[156:159]
ds_read_u16_d16_hi v138,v138
ds_read_b64_tr_b16 v[100:101],v6 offset:12392
v_pk_fma_f16 v120,v120,s55,s55
v_pk_fma_f16 v122,v122,s55,s55
v_rcp_f16 v121,v120
v_mfma_f32_16x16x32_bf16 v[148:151],v[76:79],v[48:51],v[148:151]
ds_read_u16 v137,v137
ds_read_b64_tr_b16 v[102:103],v6 offset:13928
v_rcp_f16 v123,v122
v_mfma_f32_16x16x32_bf16 v[144:147],v[80:83],v[56:59],v[144:147]
ds_read_u16_d16_hi v139,v139
ds_read_b64_tr_b16 v[64:65],v4 offset:12320
v_rcp_f16_sdwa v121,v120 dst_sel:WORD_1 dst_unused:UNUSED_PRESERVE src0_sel:WORD_1
v_mfma_f32_16x16x32_bf16 v[152:155],v[80:83],v[60:63],v[152:155]
ds_read_b64_tr_b16 v[66:67],v4 offset:14368
v_rcp_f16_sdwa v123,v122 dst_sel:WORD_1 dst_unused:UNUSED_PRESERVE src0_sel:WORD_1
v_fma_mix_f32 v142,v142,v121,0 op_sel:[0,1,0] op_sel_hi:[0,1,0]
v_mfma_f32_16x16x32_bf16 v[156:159],v[84:87],v[60:63],v[156:159]
ds_read_b128 v[112:115],v7 offset:1024
v_fma_mix_f32 v140,v140,v121,0 op_sel_hi:[0,1,0]
s_waitcnt lgkmcnt(15)
v_lshl_add_u64 v[128:129],v[128:129],0,v[130:131]
s_waitcnt lgkmcnt(11)
v_lshl_add_u64 v[130:131],v[132:133],0,v[134:135]
v_mfma_f32_16x16x32_bf16 v[148:151],v[84:87],v[56:59],v[148:151]
s_cbranch_vccnz RA40_%=
s_mov_b64 exec,s[62:63]
s_add_u32 m0,s57,0
s_add_u32 s64,s56,5898240
buffer_load_dwordx4 v2,s[36:39],s64 offen lds
s_add_u32 m0,s57,1728
s_nop 0
buffer_load_dwordx4 v3,s[36:39],s64 offen lds
s_mov_b64 exec,-1
RA40_%=:
ds_read_b64_tr_b16 v[68:69],v5 offset:12320
v_fma_mix_f32 v141,v141,v123,0 op_sel_hi:[0,1,0]
v_fma_mix_f32 v143,v143,v123,0 op_sel:[0,1,0] op_sel_hi:[0,1,0]
v_lshlrev_b32_sdwa v144,1,v144 dst_sel:DWORD dst_unused:UNUSED_PAD src0_sel:DWORD src1_sel:WORD_1
v_lshlrev_b32_sdwa v146,1,v146 dst_sel:DWORD dst_unused:UNUSED_PAD src0_sel:DWORD src1_sel:WORD_1
v_mfma_f32_16x16x32_bf16 v[160:163],v[88:91],v[128:131],v[160:163]
ds_read_u16 v144,v144
v_lshlrev_b32_sdwa v145,1,v145 dst_sel:DWORD dst_unused:UNUSED_PAD src0_sel:DWORD src1_sel:WORD_1
v_lshlrev_b32_sdwa v147,1,v147 dst_sel:DWORD dst_unused:UNUSED_PAD src0_sel:DWORD src1_sel:WORD_1
v_mfma_f32_16x16x32_bf16 v[168:171],v[92:95],v[128:131],v[168:171]
s_waitcnt lgkmcnt(10)
ds_read_b64_tr_b16 v[70:71],v5 offset:14368
ds_read_b128 v[116:119],v7 offset:1056
v_fma_mixlo_f16 v120,v156,s99,0
v_fma_mixlo_f16 v122,v157,s99,0
v_mfma_f32_16x16x32_bf16 v[176:179],v[96:99],v[128:131],v[176:179]
s_waitcnt lgkmcnt(8)
ds_read_u16_d16_hi v146,v146
v_lshlrev_b32_sdwa v148,1,v148 dst_sel:DWORD dst_unused:UNUSED_PAD src0_sel:DWORD src1_sel:WORD_1
v_lshlrev_b32_sdwa v150,1,v150 dst_sel:DWORD dst_unused:UNUSED_PAD src0_sel:DWORD src1_sel:WORD_1
v_mfma_f32_16x16x32_bf16 v[184:187],v[100:103],v[128:131],v[184:187]
s_waitcnt lgkmcnt(8)
ds_read_b64_tr_b16 v[72:73],v4 offset:16416
ds_read_b64_tr_b16 v[74:75],v4 offset:18464
v_lshl_add_u64 v[136:137],v[136:137],0,v[138:139]
v_fma_mixhi_f16 v120,v158,s99,0
v_mfma_f32_16x16x32_bf16 v[192:195],v[104:107],v[128:131],v[192:195]
ds_read_u16 v145,v145
v_cvt_pk_bf16_f32 v138,v140,v142
v_cvt_pk_bf16_f32 v139,v141,v143
v_mfma_f32_16x16x32_bf16 v[200:203],v[108:111],v[128:131],v[200:203]
ds_read_b64_tr_b16 v[76:77],v5 offset:16416
ds_read_b64_tr_b16 v[78:79],v5 offset:18464
v_fma_mixhi_f16 v122,v159,s99,0
v_mfma_f32_16x16x32_bf16 v[164:167],v[88:91],v[136:139],v[164:167]
v_lshlrev_b32_sdwa v149,1,v149 dst_sel:DWORD dst_unused:UNUSED_PAD src0_sel:DWORD src1_sel:WORD_1
v_lshlrev_b32_sdwa v151,1,v151 dst_sel:DWORD dst_unused:UNUSED_PAD src0_sel:DWORD src1_sel:WORD_1
v_mfma_f32_16x16x32_bf16 v[172:175],v[92:95],v[136:139],v[172:175]
ds_read_b64_tr_b16 v[80:81],v4 offset:20512
ds_read_b64_tr_b16 v[82:83],v4 offset:22560
v_pk_fma_f16 v121,v120,v120,s54
v_pk_fma_f16 v123,v122,v122,s54
v_mfma_f32_16x16x32_bf16 v[180:183],v[96:99],v[136:139],v[180:183]
ds_read_u16_d16_hi v147,v147
v_pk_mul_f16 v121,v120,v121
v_pk_mul_f16 v123,v122,v123
v_mfma_f32_16x16x32_bf16 v[188:191],v[100:103],v[136:139],v[188:191]
s_cbranch_vccnz RA41_%=
s_mov_b64 exec,s[62:63]
s_add_u32 m0,s57,3488
s_add_u32 s64,s56,5996544
buffer_load_dwordx4 v2,s[36:39],s64 offen lds
s_add_u32 m0,s57,5216
s_nop 0
buffer_load_dwordx4 v3,s[36:39],s64 offen lds
s_mov_b64 exec,-1
RA41_%=:
ds_read_u16 v148,v148
v_lshlrev_b32_sdwa v152,1,v152 dst_sel:DWORD dst_unused:UNUSED_PAD src0_sel:DWORD src1_sel:WORD_1
v_lshlrev_b32_sdwa v154,1,v154 dst_sel:DWORD dst_unused:UNUSED_PAD src0_sel:DWORD src1_sel:WORD_1
v_mfma_f32_16x16x32_bf16 v[196:199],v[104:107],v[136:139],v[196:199]
v_exp_f16 v120,v121
v_mfma_f32_16x16x32_bf16 v[204:207],v[108:111],v[136:139],v[204:207]
s_waitcnt lgkmcnt(14)
ds_read_b64_tr_b16 v[84:85],v5 offset:20512
ds_read_b64_tr_b16 v[86:87],v5 offset:22560
v_exp_f16 v122,v123
v_mfma_f32_16x16x32_bf16 v[128:131],v[64:67],v[16:19],v[112:115]
v_lshlrev_b32_sdwa v153,1,v153 dst_sel:DWORD dst_unused:UNUSED_PAD src0_sel:DWORD src1_sel:WORD_1
v_lshlrev_b32_sdwa v155,1,v155 dst_sel:DWORD dst_unused:UNUSED_PAD src0_sel:DWORD src1_sel:WORD_1
v_mfma_f32_16x16x32_bf16 v[136:139],v[64:67],v[20:23],v[112:115]
s_waitcnt lgkmcnt(12)
ds_read_u16_d16_hi v150,v150
ds_read_u16 v149,v149
v_exp_f16_sdwa v120,v121 dst_sel:WORD_1 dst_unused:UNUSED_PRESERVE src0_sel:WORD_1
v_mfma_f32_16x16x32_bf16 v[140:143],v[68:71],v[20:23],v[116:119]
v_exp_f16_sdwa v122,v123 dst_sel:WORD_1 dst_unused:UNUSED_PRESERVE src0_sel:WORD_1
v_mfma_f32_16x16x32_bf16 v[132:135],v[68:71],v[16:19],v[116:119]
s_waitcnt lgkmcnt(11)
v_pk_fma_f16 v120,v120,s55,s55
v_mfma_f32_16x16x32_bf16 v[128:131],v[72:75],v[24:27],v[128:131]
v_pk_fma_f16 v122,v122,s55,s55
v_rcp_f16 v121,v120
v_mfma_f32_16x16x32_bf16 v[136:139],v[72:75],v[28:31],v[136:139]
s_waitcnt lgkmcnt(8)
ds_read_u16_d16_hi v151,v151
v_rcp_f16 v123,v122
v_mfma_f32_16x16x32_bf16 v[140:143],v[76:79],v[28:31],v[140:143]
ds_read_u16 v152,v152
ds_read_u16_d16_hi v154,v154
v_rcp_f16_sdwa v121,v120 dst_sel:WORD_1 dst_unused:UNUSED_PRESERVE src0_sel:WORD_1
v_mfma_f32_16x16x32_bf16 v[132:135],v[76:79],v[24:27],v[132:135]
s_waitcnt lgkmcnt(9)
s_cbranch_vccnz RA42_%=
s_mov_b64 exec,s[62:63]
s_add_u32 m0,s57,6976
s_add_u32 s64,s56,6094848
buffer_load_dwordx4 v2,s[36:39],s64 offen lds
s_add_u32 m0,s57,8704
s_nop 0
buffer_load_dwordx4 v3,s[36:39],s64 offen lds
s_mov_b64 exec,-1
RA42_%=:
v_rcp_f16_sdwa v123,v122 dst_sel:WORD_1 dst_unused:UNUSED_PRESERVE src0_sel:WORD_1
v_mfma_f32_16x16x32_bf16 v[128:131],v[80:83],v[32:35],v[128:131]
ds_read_u16 v153,v153
v_fma_mix_f32 v156,v156,v121,0 op_sel_hi:[0,1,0]
v_fma_mix_f32 v158,v158,v121,0 op_sel:[0,1,0] op_sel_hi:[0,1,0]
v_mfma_f32_16x16x32_bf16 v[136:139],v[80:83],v[36:39],v[136:139]
s_waitcnt lgkmcnt(3)
v_lshl_add_u64 v[144:145],v[144:145],0,v[146:147]
v_lshl_add_u64 v[146:147],v[148:149],0,v[150:151]
v_mfma_f32_16x16x32_bf16 v[140:143],v[84:87],v[36:39],v[140:143]
ds_read_u16_d16_hi v155,v155
v_fma_mix_f32 v157,v157,v123,0 op_sel_hi:[0,1,0]
v_fma_mix_f32 v159,v159,v123,0 op_sel:[0,1,0] op_sel_hi:[0,1,0]
v_mfma_f32_16x16x32_bf16 v[132:135],v[84:87],v[32:35],v[132:135]
v_lshlrev_b32_sdwa v128,1,v128 dst_sel:DWORD dst_unused:UNUSED_PAD src0_sel:DWORD src1_sel:WORD_1
v_lshlrev_b32_sdwa v130,1,v130 dst_sel:DWORD dst_unused:UNUSED_PAD src0_sel:DWORD src1_sel:WORD_1
v_mfma_f32_16x16x32_bf16 v[208:211],v[88:91],v[144:147],v[208:211]
v_lshlrev_b32_sdwa v129,1,v129 dst_sel:DWORD dst_unused:UNUSED_PAD src0_sel:DWORD src1_sel:WORD_1
v_lshlrev_b32_sdwa v131,1,v131 dst_sel:DWORD dst_unused:UNUSED_PAD src0_sel:DWORD src1_sel:WORD_1
v_mfma_f32_16x16x32_bf16 v[216:219],v[92:95],v[144:147],v[216:219]
ds_read_u16 v128,v128
ds_read_u16_d16_hi v130,v130
v_fma_mixlo_f16 v120,v140,s99,0
v_fma_mixlo_f16 v122,v141,s99,0
v_mfma_f32_16x16x32_bf16 v[224:227],v[96:99],v[144:147],v[224:227]
ds_read_u16 v129,v129
v_lshlrev_b32_sdwa v132,1,v132 dst_sel:DWORD dst_unused:UNUSED_PAD src0_sel:DWORD src1_sel:WORD_1
v_lshlrev_b32_sdwa v134,1,v134 dst_sel:DWORD dst_unused:UNUSED_PAD src0_sel:DWORD src1_sel:WORD_1
v_mfma_f32_16x16x32_bf16 v[232:235],v[100:103],v[144:147],v[232:235]
s_waitcnt lgkmcnt(3)
v_lshl_add_u64 v[152:153],v[152:153],0,v[154:155]
v_cvt_pk_bf16_f32 v154,v156,v158
v_mfma_f32_16x16x32_bf16 v[240:243],v[104:107],v[144:147],v[240:243]
v_fma_mixhi_f16 v120,v142,s99,0
v_cvt_pk_bf16_f32 v155,v157,v159
v_mfma_f32_16x16x32_bf16 v[248:251],v[108:111],v[144:147],v[248:251]
s_cbranch_vccnz RA43_%=
s_mov_b64 exec,s[62:63]
s_add_u32 m0,s57,10464
s_add_u32 s64,s56,6193152
buffer_load_dwordx4 v2,s[36:39],s64 offen lds
s_add_u32 m0,s57,12192
s_nop 0
buffer_load_dwordx4 v3,s[36:39],s64 offen lds
s_mov_b64 exec,-1
RA43_%=:
ds_read_u16_d16_hi v131,v131
v_fma_mixhi_f16 v122,v143,s99,0
v_mfma_f32_16x16x32_bf16 v[212:215],v[88:91],v[152:155],v[212:215]
v_lshlrev_b32_sdwa v133,1,v133 dst_sel:DWORD dst_unused:UNUSED_PAD src0_sel:DWORD src1_sel:WORD_1
v_pk_fma_f16 v121,v120,v120,s54
v_lshlrev_b32_sdwa v135,1,v135 dst_sel:DWORD dst_unused:UNUSED_PAD src0_sel:DWORD src1_sel:WORD_1
v_mfma_f32_16x16x32_bf16 v[220:223],v[92:95],v[152:155],v[220:223]
s_setprio 2
ds_read_u16 v132,v132
ds_read_u16_d16_hi v134,v134
v_pk_fma_f16 v123,v122,v122,s54
v_mfma_f32_16x16x32_bf16 v[228:231],v[96:99],v[152:155],v[228:231]
ds_read_b64_tr_b16 v[88:89],v6 offset:18480
v_pk_mul_f16 v121,v120,v121
v_lshlrev_b32_sdwa v136,1,v136 dst_sel:DWORD dst_unused:UNUSED_PAD src0_sel:DWORD src1_sel:WORD_1
v_pk_mul_f16 v123,v122,v123
v_mfma_f32_16x16x32_bf16 v[236:239],v[100:103],v[152:155],v[236:239]
ds_read_b64_tr_b16 v[90:91],v6 offset:20016
v_lshlrev_b32_sdwa v138,1,v138 dst_sel:DWORD dst_unused:UNUSED_PAD src0_sel:DWORD src1_sel:WORD_1
v_mfma_f32_16x16x32_bf16 v[244:247],v[104:107],v[152:155],v[244:247]
ds_read_u16 v133,v133
v_exp_f16 v120,v121
v_mfma_f32_16x16x32_bf16 v[252:255],v[108:111],v[152:155],v[252:255]
ds_read_b64_tr_b16 v[92:93],v6 offset:18488
v_lshlrev_b32_sdwa v137,1,v137 dst_sel:DWORD dst_unused:UNUSED_PAD src0_sel:DWORD src1_sel:WORD_1
v_mfma_f32_16x16x32_bf16 v[144:147],v[64:67],v[40:43],v[112:115]
ds_read_b64_tr_b16 v[94:95],v6 offset:20024
v_lshlrev_b32_sdwa v139,1,v139 dst_sel:DWORD dst_unused:UNUSED_PAD src0_sel:DWORD src1_sel:WORD_1
v_exp_f16 v122,v123
v_mfma_f32_16x16x32_bf16 v[152:155],v[64:67],v[44:47],v[112:115]
ds_read_u16_d16_hi v135,v135
v_mfma_f32_16x16x32_bf16 v[156:159],v[68:71],v[44:47],v[116:119]
ds_read_u16 v136,v136
v_exp_f16_sdwa v122,v123 dst_sel:WORD_1 dst_unused:UNUSED_PRESERVE src0_sel:WORD_1
v_exp_f16_sdwa v120,v121 dst_sel:WORD_1 dst_unused:UNUSED_PRESERVE src0_sel:WORD_1
v_mfma_f32_16x16x32_bf16 v[148:151],v[68:71],v[40:43],v[116:119]
ds_read_b64_tr_b16 v[96:97],v6 offset:18544
ds_read_b64_tr_b16 v[98:99],v6 offset:20080
v_pk_fma_f16 v120,v120,s55,s55
v_pk_fma_f16 v122,v122,s55,s55
v_mfma_f32_16x16x32_bf16 v[144:147],v[72:75],v[48:51],v[144:147]
s_cbranch_vccnz RA44_%=
s_mov_b64 exec,s[62:63]
s_add_u32 m0,s57,13952
s_add_u32 s64,s56,6291456
buffer_load_dwordx4 v2,s[36:39],s64 offen lds
s_add_u32 m0,s57,15680
s_nop 0
buffer_load_dwordx4 v3,s[36:39],s64 offen lds
s_mov_b64 exec,-1
RA44_%=:
v_rcp_f16 v121,v120
v_mfma_f32_16x16x32_bf16 v[152:155],v[72:75],v[52:55],v[152:155]
ds_read_b64_tr_b16 v[100:101],v6 offset:18552
v_rcp_f16 v123,v122
v_mfma_f32_16x16x32_bf16 v[156:159],v[76:79],v[52:55],v[156:159]
ds_read_u16_d16_hi v138,v138
v_rcp_f16_sdwa v121,v120 dst_sel:WORD_1 dst_unused:UNUSED_PRESERVE src0_sel:WORD_1
v_mfma_f32_16x16x32_bf16 v[148:151],v[76:79],v[48:51],v[148:151]
ds_read_b64_tr_b16 v[102:103],v6 offset:20088
ds_read_b64_tr_b16 v[104:105],v6 offset:18608
v_rcp_f16_sdwa v123,v122 dst_sel:WORD_1 dst_unused:UNUSED_PRESERVE src0_sel:WORD_1
v_mfma_f32_16x16x32_bf16 v[144:147],v[80:83],v[56:59],v[144:147]
ds_read_u16 v137,v137
v_fma_mix_f32 v142,v142,v121,0 op_sel:[0,1,0] op_sel_hi:[0,1,0]
v_mfma_f32_16x16x32_bf16 v[152:155],v[80:83],v[60:63],v[152:155]
s_waitcnt lgkmcnt(8)
ds_read_b64_tr_b16 v[106:107],v6 offset:20144
ds_read_b64_tr_b16 v[108:109],v6 offset:18616
v_fma_mix_f32 v140,v140,v121,0 op_sel_hi:[0,1,0]
v_lshl_add_u64 v[128:129],v[128:129],0,v[130:131]
v_lshl_add_u64 v[130:131],v[132:133],0,v[134:135]
v_mfma_f32_16x16x32_bf16 v[156:159],v[84:87],v[60:63],v[156:159]
ds_read_u16_d16_hi v139,v139
v_fma_mix_f32 v141,v141,v123,0 op_sel_hi:[0,1,0]
v_fma_mix_f32 v143,v143,v123,0 op_sel:[0,1,0] op_sel_hi:[0,1,0]
v_mfma_f32_16x16x32_bf16 v[148:151],v[84:87],v[56:59],v[148:151]
ds_read_b64_tr_b16 v[110:111],v6 offset:20152
ds_read_b64_tr_b16 v[64:65],v4 offset:12832
v_lshlrev_b32_sdwa v144,1,v144 dst_sel:DWORD dst_unused:UNUSED_PAD src0_sel:DWORD src1_sel:WORD_1
v_lshlrev_b32_sdwa v146,1,v146 dst_sel:DWORD dst_unused:UNUSED_PAD src0_sel:DWORD src1_sel:WORD_1
v_mfma_f32_16x16x32_bf16 v[160:163],v[88:91],v[128:131],v[160:163]
ds_read_u16 v144,v144
v_lshlrev_b32_sdwa v145,1,v145 dst_sel:DWORD dst_unused:UNUSED_PAD src0_sel:DWORD src1_sel:WORD_1
v_lshlrev_b32_sdwa v147,1,v147 dst_sel:DWORD dst_unused:UNUSED_PAD src0_sel:DWORD src1_sel:WORD_1
v_mfma_f32_16x16x32_bf16 v[168:171],v[92:95],v[128:131],v[168:171]
s_waitcnt lgkmcnt(11)
ds_read_b64_tr_b16 v[66:67],v4 offset:14880
ds_read_b128 v[112:115],v7 offset:1152
v_fma_mixlo_f16 v120,v156,s99,0
v_fma_mixlo_f16 v122,v157,s99,0
v_mfma_f32_16x16x32_bf16 v[176:179],v[96:99],v[128:131],v[176:179]
s_waitcnt lgkmcnt(10)
s_cbranch_vccnz RA45_%=
s_mov_b64 exec,s[62:63]
s_add_u32 m0,s57,17440
s_add_u32 s64,s56,6389760
buffer_load_dwordx4 v2,s[36:39],s64 offen lds
s_add_u32 m0,s57,19168
s_nop 0
buffer_load_dwordx4 v3,s[36:39],s64 offen lds
s_mov_b64 exec,-1
RA45_%=:
ds_read_u16_d16_hi v146,v146
v_lshlrev_b32_sdwa v148,1,v148 dst_sel:DWORD dst_unused:UNUSED_PAD src0_sel:DWORD src1_sel:WORD_1
v_lshlrev_b32_sdwa v150,1,v150 dst_sel:DWORD dst_unused:UNUSED_PAD src0_sel:DWORD src1_sel:WORD_1
v_mfma_f32_16x16x32_bf16 v[184:187],v[100:103],v[128:131],v[184:187]
s_waitcnt lgkmcnt(6)
ds_read_b64_tr_b16 v[68:69],v5 offset:12832
ds_read_b64_tr_b16 v[70:71],v5 offset:14880
v_lshl_add_u64 v[136:137],v[136:137],0,v[138:139]
v_fma_mixhi_f16 v120,v158,s99,0
v_mfma_f32_16x16x32_bf16 v[192:195],v[104:107],v[128:131],v[192:195]
s_waitcnt lgkmcnt(7)
ds_read_u16 v145,v145
v_cvt_pk_bf16_f32 v138,v140,v142
v_cvt_pk_bf16_f32 v139,v141,v143
v_mfma_f32_16x16x32_bf16 v[200:203],v[108:111],v[128:131],v[200:203]
ds_read_b128 v[116:119],v7 offset:1184
ds_read_b64_tr_b16 v[72:73],v4 offset:16928
v_fma_mixhi_f16 v122,v159,s99,0
v_mfma_f32_16x16x32_bf16 v[164:167],v[88:91],v[136:139],v[164:167]
v_lshlrev_b32_sdwa v149,1,v149 dst_sel:DWORD dst_unused:UNUSED_PAD src0_sel:DWORD src1_sel:WORD_1
v_lshlrev_b32_sdwa v151,1,v151 dst_sel:DWORD dst_unused:UNUSED_PAD src0_sel:DWORD src1_sel:WORD_1
v_mfma_f32_16x16x32_bf16 v[172:175],v[92:95],v[136:139],v[172:175]
ds_read_b64_tr_b16 v[74:75],v4 offset:18976
ds_read_b64_tr_b16 v[76:77],v5 offset:16928
v_pk_fma_f16 v121,v120,v120,s54
v_pk_fma_f16 v123,v122,v122,s54
v_mfma_f32_16x16x32_bf16 v[180:183],v[96:99],v[136:139],v[180:183]
s_setprio 1
ds_read_u16_d16_hi v147,v147
v_pk_mul_f16 v121,v120,v121
v_pk_mul_f16 v123,v122,v123
v_mfma_f32_16x16x32_bf16 v[188:191],v[100:103],v[136:139],v[188:191]
ds_read_u16 v148,v148
v_lshlrev_b32_sdwa v152,1,v152 dst_sel:DWORD dst_unused:UNUSED_PAD src0_sel:DWORD src1_sel:WORD_1
v_lshlrev_b32_sdwa v154,1,v154 dst_sel:DWORD dst_unused:UNUSED_PAD src0_sel:DWORD src1_sel:WORD_1
v_mfma_f32_16x16x32_bf16 v[196:199],v[104:107],v[136:139],v[196:199]
v_exp_f16 v120,v121
v_mfma_f32_16x16x32_bf16 v[204:207],v[108:111],v[136:139],v[204:207]
s_waitcnt lgkmcnt(10)
ds_read_b64_tr_b16 v[78:79],v5 offset:18976
ds_read_b64_tr_b16 v[80:81],v4 offset:21024
v_exp_f16 v122,v123
v_mfma_f32_16x16x32_bf16 v[128:131],v[64:67],v[16:19],v[112:115]
v_lshlrev_b32_sdwa v153,1,v153 dst_sel:DWORD dst_unused:UNUSED_PAD src0_sel:DWORD src1_sel:WORD_1
v_lshlrev_b32_sdwa v155,1,v155 dst_sel:DWORD dst_unused:UNUSED_PAD src0_sel:DWORD src1_sel:WORD_1
v_mfma_f32_16x16x32_bf16 v[136:139],v[64:67],v[20:23],v[112:115]
s_waitcnt lgkmcnt(7)
ds_read_u16_d16_hi v150,v150
ds_read_u16 v149,v149
v_exp_f16_sdwa v120,v121 dst_sel:WORD_1 dst_unused:UNUSED_PRESERVE src0_sel:WORD_1
v_mfma_f32_16x16x32_bf16 v[140:143],v[68:71],v[20:23],v[116:119]
v_exp_f16_sdwa v122,v123 dst_sel:WORD_1 dst_unused:UNUSED_PRESERVE src0_sel:WORD_1
v_mfma_f32_16x16x32_bf16 v[132:135],v[68:71],v[16:19],v[116:119]
s_waitcnt lgkmcnt(7)
ds_read_b64_tr_b16 v[82:83],v4 offset:23072
v_pk_fma_f16 v120,v120,s55,s55
v_mfma_f32_16x16x32_bf16 v[128:131],v[72:75],v[24:27],v[128:131]
ds_read_b64_tr_b16 v[84:85],v5 offset:21024
v_pk_fma_f16 v122,v122,s55,s55
v_rcp_f16 v121,v120
v_mfma_f32_16x16x32_bf16 v[136:139],v[72:75],v[28:31],v[136:139]
s_waitcnt lgkmcnt(5)
ds_read_b64_tr_b16 v[86:87],v5 offset:23072
ds_read_u16_d16_hi v151,v151
v_rcp_f16 v123,v122
v_mfma_f32_16x16x32_bf16 v[140:143],v[76:79],v[28:31],v[140:143]
ds_read_u16 v152,v152
ds_read_u16_d16_hi v154,v154
v_rcp_f16_sdwa v121,v120 dst_sel:WORD_1 dst_unused:UNUSED_PRESERVE src0_sel:WORD_1
v_mfma_f32_16x16x32_bf16 v[132:135],v[76:79],v[24:27],v[132:135]
s_waitcnt lgkmcnt(5)
v_rcp_f16_sdwa v123,v122 dst_sel:WORD_1 dst_unused:UNUSED_PRESERVE src0_sel:WORD_1
v_mfma_f32_16x16x32_bf16 v[128:131],v[80:83],v[32:35],v[128:131]
ds_read_u16 v153,v153
v_fma_mix_f32 v156,v156,v121,0 op_sel_hi:[0,1,0]
v_fma_mix_f32 v158,v158,v121,0 op_sel:[0,1,0] op_sel_hi:[0,1,0]
v_mfma_f32_16x16x32_bf16 v[136:139],v[80:83],v[36:39],v[136:139]
s_waitcnt lgkmcnt(3)
s_setprio 0
v_lshl_add_u64 v[144:145],v[144:145],0,v[146:147]
v_lshl_add_u64 v[146:147],v[148:149],0,v[150:151]
v_mfma_f32_16x16x32_bf16 v[140:143],v[84:87],v[36:39],v[140:143]
ds_read_u16_d16_hi v155,v155
v_fma_mix_f32 v157,v157,v123,0 op_sel_hi:[0,1,0]
v_fma_mix_f32 v159,v159,v123,0 op_sel:[0,1,0] op_sel_hi:[0,1,0]
v_mfma_f32_16x16x32_bf16 v[132:135],v[84:87],v[32:35],v[132:135]
v_lshlrev_b32_sdwa v128,1,v128 dst_sel:DWORD dst_unused:UNUSED_PAD src0_sel:DWORD src1_sel:WORD_1
v_lshlrev_b32_sdwa v130,1,v130 dst_sel:DWORD dst_unused:UNUSED_PAD src0_sel:DWORD src1_sel:WORD_1
v_mfma_f32_16x16x32_bf16 v[208:211],v[88:91],v[144:147],v[208:211]
s_cbranch_vccz RA46_%=
s_mov_b32 s86,0xbe801d5a
s_waitcnt vmcnt(0)
RA46_%=:
s_barrier
s_setprio 2
ds_read_u16 v128,v128
v_mfma_f32_16x16x32_bf16 v[216:219],v[92:95],v[144:147],v[216:219]
ds_read_u16_d16_hi v130,v130
v_lshlrev_b32_sdwa v129,1,v129 dst_sel:DWORD dst_unused:UNUSED_PAD src0_sel:DWORD src1_sel:WORD_1
v_lshlrev_b32_sdwa v131,1,v131 dst_sel:DWORD dst_unused:UNUSED_PAD src0_sel:DWORD src1_sel:WORD_1
v_mfma_f32_16x16x32_bf16 v[224:227],v[96:99],v[144:147],v[224:227]
s_cbranch_vccz RA47_%=
s_add_u32 m0,s58,12320
s_mov_b32 s69,s60
buffer_load_dwordx4 v2,s[44:47],s69 offen lds
s_add_u32 m0,m0,1024
s_add_u32 s69,s69,s53
buffer_load_dwordx4 v3,s[44:47],s69 offen lds
s_add_u32 m0,m0,1024
s_add_u32 s69,s69,s53
buffer_load_dwordx4 v2,s[44:47],s69 offen lds
s_add_u32 m0,m0,s50
s_add_u32 s69,s69,s53
buffer_load_dwordx4 v3,s[44:47],s69 offen lds
RA47_%=:
v_lshlrev_b32_sdwa v132,1,v132 dst_sel:DWORD dst_unused:UNUSED_PAD src0_sel:DWORD src1_sel:WORD_1
v_mfma_f32_16x16x32_bf16 v[232:235],v[100:103],v[144:147],v[232:235]
s_waitcnt lgkmcnt(2)
ds_read_u16 v129,v129
v_lshl_add_u64 v[152:153],v[152:153],0,v[154:155]
v_lshlrev_b32_sdwa v134,1,v134 dst_sel:DWORD dst_unused:UNUSED_PAD src0_sel:DWORD src1_sel:WORD_1
v_mfma_f32_16x16x32_bf16 v[240:243],v[104:107],v[144:147],v[240:243]
v_cvt_pk_bf16_f32 v155,v157,v159
v_cvt_pk_bf16_f32 v154,v156,v158
v_mfma_f32_16x16x32_bf16 v[248:251],v[108:111],v[144:147],v[248:251]
ds_read_u16_d16_hi v131,v131
v_lshlrev_b32_sdwa v133,1,v133 dst_sel:DWORD dst_unused:UNUSED_PAD src0_sel:DWORD src1_sel:WORD_1
v_mfma_f32_16x16x32_bf16 v[212:215],v[88:91],v[152:155],v[212:215]
s_cbranch_vccz RA48_%=
s_add_u32 m0,m0,1024
s_add_u32 s69,s69,s53
buffer_load_dwordx4 v2,s[44:47],s69 offen lds
s_add_u32 m0,m0,1024
s_add_u32 s69,s69,s53
buffer_load_dwordx4 v3,s[44:47],s69 offen lds
s_add_u32 m0,m0,s50
s_add_u32 s69,s69,s51
buffer_load_dwordx4 v2,s[44:47],s69 offen lds
s_add_u32 m0,m0,1024
s_add_u32 s69,s69,s53
buffer_load_dwordx4 v3,s[44:47],s69 offen lds
RA48_%=:
ds_read_u16 v132,v132
v_lshlrev_b32_sdwa v135,1,v135 dst_sel:DWORD dst_unused:UNUSED_PAD src0_sel:DWORD src1_sel:WORD_1
v_mfma_f32_16x16x32_bf16 v[220:223],v[92:95],v[152:155],v[220:223]
ds_read_u16_d16_hi v134,v134
v_lshlrev_b32_sdwa v136,1,v136 dst_sel:DWORD dst_unused:UNUSED_PAD src0_sel:DWORD src1_sel:WORD_1
v_mfma_f32_16x16x32_bf16 v[228:231],v[96:99],v[152:155],v[228:231]
ds_read_b64_tr_b16 v[88:89],v6 offset:0
ds_read_b64_tr_b16 v[90:91],v6 offset:1536
v_lshlrev_b32_sdwa v138,1,v138 dst_sel:DWORD dst_unused:UNUSED_PAD src0_sel:DWORD src1_sel:WORD_1
v_mfma_f32_16x16x32_bf16 v[236:239],v[100:103],v[152:155],v[236:239]
ds_read_u16 v133,v133
v_lshlrev_b32_sdwa v137,1,v137 dst_sel:DWORD dst_unused:UNUSED_PAD src0_sel:DWORD src1_sel:WORD_1
v_mfma_f32_16x16x32_bf16 v[244:247],v[104:107],v[152:155],v[244:247]
ds_read_u16_d16_hi v135,v135
v_lshlrev_b32_sdwa v139,1,v139 dst_sel:DWORD dst_unused:UNUSED_PAD src0_sel:DWORD src1_sel:WORD_1
v_mfma_f32_16x16x32_bf16 v[252:255],v[108:111],v[152:155],v[252:255]
s_cbranch_vccz RA49_%=
s_add_u32 m0,m0,1024
s_add_u32 s69,s69,s53
buffer_load_dwordx4 v2,s[44:47],s69 offen lds
s_add_u32 m0,m0,s50
s_add_u32 s69,s69,s53
buffer_load_dwordx4 v3,s[44:47],s69 offen lds
s_add_u32 m0,m0,1024
s_add_u32 s69,s69,s53
buffer_load_dwordx4 v2,s[44:47],s69 offen lds
s_add_u32 m0,m0,1024
s_add_u32 s69,s69,s53
buffer_load_dwordx4 v3,s[44:47],s69 offen lds
RA49_%=:
ds_read_b64_tr_b16 v[92:93],v6 offset:8
ds_read_b64_tr_b16 v[94:95],v6 offset:1544
v_lshlrev_b32_sdwa v140,1,v140 dst_sel:DWORD dst_unused:UNUSED_PAD src0_sel:DWORD src1_sel:WORD_1
v_mfma_f32_16x16x32_bf16 v[144:147],v[64:67],v[40:43],v[112:115]
ds_read_u16 v136,v136
v_lshlrev_b32_sdwa v142,1,v142 dst_sel:DWORD dst_unused:UNUSED_PAD src0_sel:DWORD src1_sel:WORD_1
v_mfma_f32_16x16x32_bf16 v[152:155],v[64:67],v[44:47],v[112:115]
ds_read_u16_d16_hi v138,v138
ds_read_b64_tr_b16 v[96:97],v6 offset:64
v_lshlrev_b32_sdwa v141,1,v141 dst_sel:DWORD dst_unused:UNUSED_PAD src0_sel:DWORD src1_sel:WORD_1
v_mfma_f32_16x16x32_bf16 v[156:159],v[68:71],v[44:47],v[116:119]
ds_read_b64_tr_b16 v[98:99],v6 offset:1600
ds_read_b64_tr_b16 v[100:101],v6 offset:72
v_mfma_f32_16x16x32_bf16 v[148:151],v[68:71],v[40:43],v[116:119]
ds_read_u16 v137,v137
v_lshlrev_b32_sdwa v143,1,v143 dst_sel:DWORD dst_unused:UNUSED_PAD src0_sel:DWORD src1_sel:WORD_1
v_mfma_f32_16x16x32_bf16 v[144:147],v[72:75],v[48:51],v[144:147]
ds_read_u16_d16_hi v139,v139
v_mfma_f32_16x16x32_bf16 v[152:155],v[72:75],v[52:55],v[152:155]
ds_read_b64_tr_b16 v[102:103],v6 offset:1608
ds_read_b64_tr_b16 v[104:105],v6 offset:128
v_mfma_f32_16x16x32_bf16 v[156:159],v[76:79],v[52:55],v[156:159]
ds_read_u16 v140,v140
v_mfma_f32_16x16x32_bf16 v[148:151],v[76:79],v[48:51],v[148:151]
s_waitcnt lgkmcnt(15)
ds_read_u16_d16_hi v142,v142
v_lshl_add_u64 v[128:129],v[128:129],0,v[130:131]
v_mfma_f32_16x16x32_bf16 v[144:147],v[80:83],v[56:59],v[144:147]
ds_read_b64_tr_b16 v[106:107],v6 offset:1664
ds_read_b64_tr_b16 v[108:109],v6 offset:136
v_mfma_f32_16x16x32_bf16 v[152:155],v[80:83],v[60:63],v[152:155]
s_waitcnt lgkmcnt(15)
ds_read_u16 v141,v141
v_lshl_add_u64 v[130:131],v[132:133],0,v[134:135]
v_mfma_f32_16x16x32_bf16 v[156:159],v[84:87],v[60:63],v[156:159]
ds_read_u16_d16_hi v143,v143
v_mfma_f32_16x16x32_bf16 v[148:151],v[84:87],v[56:59],v[148:151]
ds_read_b64_tr_b16 v[110:111],v6 offset:1672
ds_read_b64_tr_b16 v[64:65],v4 offset:0
v_lshlrev_b32_sdwa v144,1,v144 dst_sel:DWORD dst_unused:UNUSED_PAD src0_sel:DWORD src1_sel:WORD_1
v_lshlrev_b32_sdwa v146,1,v146 dst_sel:DWORD dst_unused:UNUSED_PAD src0_sel:DWORD src1_sel:WORD_1
v_mfma_f32_16x16x32_bf16 v[160:163],v[88:91],v[128:131],v[160:163]
s_waitcnt lgkmcnt(15)
v_lshlrev_b32_sdwa v145,1,v145 dst_sel:DWORD dst_unused:UNUSED_PAD src0_sel:DWORD src1_sel:WORD_1
v_mfma_f32_16x16x32_bf16 v[168:171],v[92:95],v[128:131],v[168:171]
s_waitcnt lgkmcnt(13)
ds_read_u16 v144,v144
v_lshlrev_b32_sdwa v147,1,v147 dst_sel:DWORD dst_unused:UNUSED_PAD src0_sel:DWORD src1_sel:WORD_1
v_mfma_f32_16x16x32_bf16 v[176:179],v[96:99],v[128:131],v[176:179]
s_waitcnt lgkmcnt(10)
ds_read_b64_tr_b16 v[66:67],v4 offset:2048
ds_read_b128 v[112:115],v7 offset:1280
v_mfma_f32_16x16x32_bf16 v[184:187],v[100:103],v[128:131],v[184:187]
s_waitcnt lgkmcnt(8)
ds_read_b64_tr_b16 v[68:69],v5 offset:0
ds_read_u16_d16_hi v146,v146
v_lshlrev_b32_sdwa v148,1,v148 dst_sel:DWORD dst_unused:UNUSED_PAD src0_sel:DWORD src1_sel:WORD_1
v_lshl_add_u64 v[136:137],v[136:137],0,v[138:139]
v_lshlrev_b32_sdwa v150,1,v150 dst_sel:DWORD dst_unused:UNUSED_PAD src0_sel:DWORD src1_sel:WORD_1
v_mfma_f32_16x16x32_bf16 v[192:195],v[104:107],v[128:131],v[192:195]
s_waitcnt lgkmcnt(6)
ds_read_u16 v145,v145
v_lshl_add_u64 v[138:139],v[140:141],0,v[142:143]
v_mfma_f32_16x16x32_bf16 v[200:203],v[108:111],v[128:131],v[200:203]
ds_read_b64_tr_b16 v[70:71],v5 offset:2048
ds_read_b128 v[116:119],v7 offset:1312
v_mfma_f32_16x16x32_bf16 v[164:167],v[88:91],v[136:139],v[164:167]
ds_read_u16_d16_hi v147,v147
v_lshlrev_b32_sdwa v149,1,v149 dst_sel:DWORD dst_unused:UNUSED_PAD src0_sel:DWORD src1_sel:WORD_1
v_lshlrev_b32_sdwa v151,1,v151 dst_sel:DWORD dst_unused:UNUSED_PAD src0_sel:DWORD src1_sel:WORD_1
v_mfma_f32_16x16x32_bf16 v[172:175],v[92:95],v[136:139],v[172:175]
ds_read_u16 v148,v148
ds_read_b64_tr_b16 v[72:73],v4 offset:4096
v_lshlrev_b32_sdwa v152,1,v152 dst_sel:DWORD dst_unused:UNUSED_PAD src0_sel:DWORD src1_sel:WORD_1
v_mfma_f32_16x16x32_bf16 v[180:183],v[96:99],v[136:139],v[180:183]
ds_read_b64_tr_b16 v[74:75],v4 offset:6144
ds_read_b64_tr_b16 v[76:77],v5 offset:4096
v_lshlrev_b32_sdwa v154,1,v154 dst_sel:DWORD dst_unused:UNUSED_PAD src0_sel:DWORD src1_sel:WORD_1
v_mfma_f32_16x16x32_bf16 v[188:191],v[100:103],v[136:139],v[188:191]
ds_read_u16_d16_hi v150,v150
v_lshlrev_b32_sdwa v153,1,v153 dst_sel:DWORD dst_unused:UNUSED_PAD src0_sel:DWORD src1_sel:WORD_1
v_mfma_f32_16x16x32_bf16 v[196:199],v[104:107],v[136:139],v[196:199]
ds_read_u16 v149,v149
v_lshlrev_b32_sdwa v155,1,v155 dst_sel:DWORD dst_unused:UNUSED_PAD src0_sel:DWORD src1_sel:WORD_1
v_mfma_f32_16x16x32_bf16 v[204:207],v[108:111],v[136:139],v[204:207]
s_waitcnt lgkmcnt(12)
ds_read_b64_tr_b16 v[78:79],v5 offset:6144
ds_read_b64_tr_b16 v[80:81],v4 offset:8192
v_lshlrev_b32_sdwa v156,1,v156 dst_sel:DWORD dst_unused:UNUSED_PAD src0_sel:DWORD src1_sel:WORD_1
v_mfma_f32_16x16x32_bf16 v[128:131],v[64:67],v[16:19],v[112:115]
ds_read_u16_d16_hi v151,v151
v_lshlrev_b32_sdwa v158,1,v158 dst_sel:DWORD dst_unused:UNUSED_PAD src0_sel:DWORD src1_sel:WORD_1
v_mfma_f32_16x16x32_bf16 v[136:139],v[64:67],v[20:23],v[112:115]
s_waitcnt lgkmcnt(10)
ds_read_u16 v152,v152
ds_read_b64_tr_b16 v[82:83],v4 offset:10240
v_lshlrev_b32_sdwa v157,1,v157 dst_sel:DWORD dst_unused:UNUSED_PAD src0_sel:DWORD src1_sel:WORD_1
v_mfma_f32_16x16x32_bf16 v[140:143],v[68:71],v[20:23],v[116:119]
ds_read_b64_tr_b16 v[84:85],v5 offset:8192
ds_read_b64_tr_b16 v[86:87],v5 offset:10240
v_lshlrev_b32_sdwa v159,1,v159 dst_sel:DWORD dst_unused:UNUSED_PAD src0_sel:DWORD src1_sel:WORD_1
v_mfma_f32_16x16x32_bf16 v[132:135],v[68:71],v[16:19],v[116:119]
s_waitcnt lgkmcnt(10)
ds_read_u16_d16_hi v154,v154
v_mfma_f32_16x16x32_bf16 v[128:131],v[72:75],v[24:27],v[128:131]
ds_read_u16 v153,v153
v_mfma_f32_16x16x32_bf16 v[136:139],v[72:75],v[28:31],v[136:139]
s_waitcnt lgkmcnt(8)
v_mfma_f32_16x16x32_bf16 v[140:143],v[76:79],v[28:31],v[140:143]
ds_read_u16_d16_hi v155,v155
v_mfma_f32_16x16x32_bf16 v[132:135],v[76:79],v[24:27],v[132:135]
s_waitcnt lgkmcnt(5)
ds_read_u16 v156,v156
v_lshl_add_u64 v[144:145],v[144:145],0,v[146:147]
v_mfma_f32_16x16x32_bf16 v[128:131],v[80:83],v[32:35],v[128:131]
v_lshl_add_u64 v[146:147],v[148:149],0,v[150:151]
v_mfma_f32_16x16x32_bf16 v[136:139],v[80:83],v[36:39],v[136:139]
s_waitcnt lgkmcnt(4)
ds_read_u16_d16_hi v158,v158
v_mfma_f32_16x16x32_bf16 v[140:143],v[84:87],v[36:39],v[140:143]
ds_read_u16 v157,v157
v_mfma_f32_16x16x32_bf16 v[132:135],v[84:87],v[32:35],v[132:135]
s_nop 0
v_lshlrev_b32_sdwa v128,1,v128 dst_sel:DWORD dst_unused:UNUSED_PAD src0_sel:DWORD src1_sel:WORD_1
v_lshlrev_b32_sdwa v130,1,v130 dst_sel:DWORD dst_unused:UNUSED_PAD src0_sel:DWORD src1_sel:WORD_1
v_mfma_f32_16x16x32_bf16 v[208:211],v[88:91],v[144:147],v[208:211]
ds_read_u16_d16_hi v159,v159
v_mfma_f32_16x16x32_bf16 v[216:219],v[92:95],v[144:147],v[216:219]
ds_read_u16 v128,v128
v_lshlrev_b32_sdwa v129,1,v129 dst_sel:DWORD dst_unused:UNUSED_PAD src0_sel:DWORD src1_sel:WORD_1
v_lshlrev_b32_sdwa v131,1,v131 dst_sel:DWORD dst_unused:UNUSED_PAD src0_sel:DWORD src1_sel:WORD_1
v_mfma_f32_16x16x32_bf16 v[224:227],v[96:99],v[144:147],v[224:227]
v_lshlrev_b32_sdwa v132,1,v132 dst_sel:DWORD dst_unused:UNUSED_PAD src0_sel:DWORD src1_sel:WORD_1
v_mfma_f32_16x16x32_bf16 v[232:235],v[100:103],v[144:147],v[232:235]
s_waitcnt lgkmcnt(5)
ds_read_u16_d16_hi v130,v130
v_lshl_add_u64 v[152:153],v[152:153],0,v[154:155]
v_lshlrev_b32_sdwa v134,1,v134 dst_sel:DWORD dst_unused:UNUSED_PAD src0_sel:DWORD src1_sel:WORD_1
v_mfma_f32_16x16x32_bf16 v[240:243],v[104:107],v[144:147],v[240:243]
s_waitcnt lgkmcnt(2)
v_lshl_add_u64 v[154:155],v[156:157],0,v[158:159]
v_mfma_f32_16x16x32_bf16 v[248:251],v[108:111],v[144:147],v[248:251]
ds_read_u16 v129,v129
v_lshlrev_b32_sdwa v133,1,v133 dst_sel:DWORD dst_unused:UNUSED_PAD src0_sel:DWORD src1_sel:WORD_1
v_mfma_f32_16x16x32_bf16 v[212:215],v[88:91],v[152:155],v[212:215]
ds_read_u16_d16_hi v131,v131
v_lshlrev_b32_sdwa v135,1,v135 dst_sel:DWORD dst_unused:UNUSED_PAD src0_sel:DWORD src1_sel:WORD_1
v_mfma_f32_16x16x32_bf16 v[220:223],v[92:95],v[152:155],v[220:223]
ds_read_u16 v132,v132
v_lshlrev_b32_sdwa v136,1,v136 dst_sel:DWORD dst_unused:UNUSED_PAD src0_sel:DWORD src1_sel:WORD_1
v_mfma_f32_16x16x32_bf16 v[228:231],v[96:99],v[152:155],v[228:231]
ds_read_b64_tr_b16 v[88:89],v6 offset:6160
ds_read_b64_tr_b16 v[90:91],v6 offset:7696
v_lshlrev_b32_sdwa v138,1,v138 dst_sel:DWORD dst_unused:UNUSED_PAD src0_sel:DWORD src1_sel:WORD_1
v_mfma_f32_16x16x32_bf16 v[236:239],v[100:103],v[152:155],v[236:239]
ds_read_u16_d16_hi v134,v134
v_lshlrev_b32_sdwa v137,1,v137 dst_sel:DWORD dst_unused:UNUSED_PAD src0_sel:DWORD src1_sel:WORD_1
v_mfma_f32_16x16x32_bf16 v[244:247],v[104:107],v[152:155],v[244:247]
ds_read_u16 v133,v133
v_lshlrev_b32_sdwa v139,1,v139 dst_sel:DWORD dst_unused:UNUSED_PAD src0_sel:DWORD src1_sel:WORD_1
v_mfma_f32_16x16x32_bf16 v[252:255],v[108:111],v[152:155],v[252:255]
ds_read_b64_tr_b16 v[92:93],v6 offset:6168
ds_read_b64_tr_b16 v[94:95],v6 offset:7704
v_lshlrev_b32_sdwa v140,1,v140 dst_sel:DWORD dst_unused:UNUSED_PAD src0_sel:DWORD src1_sel:WORD_1
v_mfma_f32_16x16x32_bf16 v[144:147],v[64:67],v[40:43],v[112:115]
ds_read_u16_d16_hi v135,v135
v_lshlrev_b32_sdwa v142,1,v142 dst_sel:DWORD dst_unused:UNUSED_PAD src0_sel:DWORD src1_sel:WORD_1
v_mfma_f32_16x16x32_bf16 v[152:155],v[64:67],v[44:47],v[112:115]
ds_read_u16 v136,v136
ds_read_b64_tr_b16 v[96:97],v6 offset:6224
v_lshlrev_b32_sdwa v141,1,v141 dst_sel:DWORD dst_unused:UNUSED_PAD src0_sel:DWORD src1_sel:WORD_1
v_mfma_f32_16x16x32_bf16 v[156:159],v[68:71],v[44:47],v[116:119]
ds_read_b64_tr_b16 v[98:99],v6 offset:7760
ds_read_b64_tr_b16 v[100:101],v6 offset:6232
v_mfma_f32_16x16x32_bf16 v[148:151],v[68:71],v[40:43],v[116:119]
ds_read_u16_d16_hi v138,v138
v_lshlrev_b32_sdwa v143,1,v143 dst_sel:DWORD dst_unused:UNUSED_PAD src0_sel:DWORD src1_sel:WORD_1
v_mfma_f32_16x16x32_bf16 v[144:147],v[72:75],v[48:51],v[144:147]
ds_read_u16 v137,v137
v_mfma_f32_16x16x32_bf16 v[152:155],v[72:75],v[52:55],v[152:155]
ds_read_b64_tr_b16 v[102:103],v6 offset:7768
ds_read_b64_tr_b16 v[104:105],v6 offset:6288
v_mfma_f32_16x16x32_bf16 v[156:159],v[76:79],v[52:55],v[156:159]
ds_read_u16_d16_hi v139,v139
v_mfma_f32_16x16x32_bf16 v[148:151],v[76:79],v[48:51],v[148:151]
s_waitcnt lgkmcnt(15)
ds_read_u16 v140,v140
v_lshl_add_u64 v[128:129],v[128:129],0,v[130:131]
v_mfma_f32_16x16x32_bf16 v[144:147],v[80:83],v[56:59],v[144:147]
ds_read_b64_tr_b16 v[106:107],v6 offset:7824
ds_read_b64_tr_b16 v[108:109],v6 offset:6296
v_mfma_f32_16x16x32_bf16 v[152:155],v[80:83],v[60:63],v[152:155]
s_waitcnt lgkmcnt(12)
ds_read_u16_d16_hi v142,v142
v_lshl_add_u64 v[130:131],v[132:133],0,v[134:135]
v_mfma_f32_16x16x32_bf16 v[156:159],v[84:87],v[60:63],v[156:159]
ds_read_u16 v141,v141
ds_read_u16_d16_hi v143,v143
v_mfma_f32_16x16x32_bf16 v[148:151],v[84:87],v[56:59],v[148:151]
ds_read_b64_tr_b16 v[110:111],v6 offset:7832
ds_read_b64_tr_b16 v[64:65],v4 offset:512
v_lshlrev_b32_sdwa v144,1,v144 dst_sel:DWORD dst_unused:UNUSED_PAD src0_sel:DWORD src1_sel:WORD_1
v_lshlrev_b32_sdwa v146,1,v146 dst_sel:DWORD dst_unused:UNUSED_PAD src0_sel:DWORD src1_sel:WORD_1
v_mfma_f32_16x16x32_bf16 v[160:163],v[88:91],v[128:131],v[160:163]
v_lshlrev_b32_sdwa v145,1,v145 dst_sel:DWORD dst_unused:UNUSED_PAD src0_sel:DWORD src1_sel:WORD_1
v_mfma_f32_16x16x32_bf16 v[168:171],v[92:95],v[128:131],v[168:171]
s_waitcnt lgkmcnt(14)
ds_read_u16 v144,v144
v_lshlrev_b32_sdwa v147,1,v147 dst_sel:DWORD dst_unused:UNUSED_PAD src0_sel:DWORD src1_sel:WORD_1
v_mfma_f32_16x16x32_bf16 v[176:179],v[96:99],v[128:131],v[176:179]
s_waitcnt lgkmcnt(11)
ds_read_b64_tr_b16 v[66:67],v4 offset:2560
ds_read_b128 v[112:115],v7 offset:1408
v_mfma_f32_16x16x32_bf16 v[184:187],v[100:103],v[128:131],v[184:187]
s_waitcnt lgkmcnt(9)
ds_read_b64_tr_b16 v[68:69],v5 offset:512
ds_read_u16_d16_hi v146,v146
v_lshlrev_b32_sdwa v148,1,v148 dst_sel:DWORD dst_unused:UNUSED_PAD src0_sel:DWORD src1_sel:WORD_1
v_lshl_add_u64 v[136:137],v[136:137],0,v[138:139]
v_lshlrev_b32_sdwa v150,1,v150 dst_sel:DWORD dst_unused:UNUSED_PAD src0_sel:DWORD src1_sel:WORD_1
v_mfma_f32_16x16x32_bf16 v[192:195],v[104:107],v[128:131],v[192:195]
s_waitcnt lgkmcnt(6)
ds_read_u16 v145,v145
v_lshl_add_u64 v[138:139],v[140:141],0,v[142:143]
v_mfma_f32_16x16x32_bf16 v[200:203],v[108:111],v[128:131],v[200:203]
ds_read_b64_tr_b16 v[70:71],v5 offset:2560
ds_read_b128 v[116:119],v7 offset:1440
v_mfma_f32_16x16x32_bf16 v[164:167],v[88:91],v[136:139],v[164:167]
ds_read_u16_d16_hi v147,v147
v_lshlrev_b32_sdwa v149,1,v149 dst_sel:DWORD dst_unused:UNUSED_PAD src0_sel:DWORD src1_sel:WORD_1
v_lshlrev_b32_sdwa v151,1,v151 dst_sel:DWORD dst_unused:UNUSED_PAD src0_sel:DWORD src1_sel:WORD_1
v_mfma_f32_16x16x32_bf16 v[172:175],v[92:95],v[136:139],v[172:175]
ds_read_u16 v148,v148
ds_read_b64_tr_b16 v[72:73],v4 offset:4608
v_lshlrev_b32_sdwa v152,1,v152 dst_sel:DWORD dst_unused:UNUSED_PAD src0_sel:DWORD src1_sel:WORD_1
v_mfma_f32_16x16x32_bf16 v[180:183],v[96:99],v[136:139],v[180:183]
ds_read_b64_tr_b16 v[74:75],v4 offset:6656
ds_read_b64_tr_b16 v[76:77],v5 offset:4608
v_lshlrev_b32_sdwa v154,1,v154 dst_sel:DWORD dst_unused:UNUSED_PAD src0_sel:DWORD src1_sel:WORD_1
v_mfma_f32_16x16x32_bf16 v[188:191],v[100:103],v[136:139],v[188:191]
ds_read_u16_d16_hi v150,v150
v_lshlrev_b32_sdwa v153,1,v153 dst_sel:DWORD dst_unused:UNUSED_PAD src0_sel:DWORD src1_sel:WORD_1
v_mfma_f32_16x16x32_bf16 v[196:199],v[104:107],v[136:139],v[196:199]
ds_read_u16 v149,v149
v_lshlrev_b32_sdwa v155,1,v155 dst_sel:DWORD dst_unused:UNUSED_PAD src0_sel:DWORD src1_sel:WORD_1
v_mfma_f32_16x16x32_bf16 v[204:207],v[108:111],v[136:139],v[204:207]
s_waitcnt lgkmcnt(12)
ds_read_b64_tr_b16 v[78:79],v5 offset:6656
ds_read_b64_tr_b16 v[80:81],v4 offset:8704
v_lshlrev_b32_sdwa v156,1,v156 dst_sel:DWORD dst_unused:UNUSED_PAD src0_sel:DWORD src1_sel:WORD_1
v_mfma_f32_16x16x32_bf16 v[128:131],v[64:67],v[16:19],v[112:115]
ds_read_u16_d16_hi v151,v151
v_lshlrev_b32_sdwa v158,1,v158 dst_sel:DWORD dst_unused:UNUSED_PAD src0_sel:DWORD src1_sel:WORD_1
v_mfma_f32_16x16x32_bf16 v[136:139],v[64:67],v[20:23],v[112:115]
s_waitcnt lgkmcnt(10)
ds_read_u16 v152,v152
ds_read_b64_tr_b16 v[82:83],v4 offset:10752
v_lshlrev_b32_sdwa v157,1,v157 dst_sel:DWORD dst_unused:UNUSED_PAD src0_sel:DWORD src1_sel:WORD_1
v_mfma_f32_16x16x32_bf16 v[140:143],v[68:71],v[20:23],v[116:119]
ds_read_b64_tr_b16 v[84:85],v5 offset:8704
ds_read_b64_tr_b16 v[86:87],v5 offset:10752
v_lshlrev_b32_sdwa v159,1,v159 dst_sel:DWORD dst_unused:UNUSED_PAD src0_sel:DWORD src1_sel:WORD_1
v_mfma_f32_16x16x32_bf16 v[132:135],v[68:71],v[16:19],v[116:119]
s_waitcnt lgkmcnt(10)
ds_read_u16_d16_hi v154,v154
v_mfma_f32_16x16x32_bf16 v[128:131],v[72:75],v[24:27],v[128:131]
ds_read_u16 v153,v153
v_mfma_f32_16x16x32_bf16 v[136:139],v[72:75],v[28:31],v[136:139]
s_waitcnt lgkmcnt(8)
v_mfma_f32_16x16x32_bf16 v[140:143],v[76:79],v[28:31],v[140:143]
ds_read_u16_d16_hi v155,v155
v_mfma_f32_16x16x32_bf16 v[132:135],v[76:79],v[24:27],v[132:135]
s_waitcnt lgkmcnt(5)
ds_read_u16 v156,v156
v_lshl_add_u64 v[144:145],v[144:145],0,v[146:147]
v_mfma_f32_16x16x32_bf16 v[128:131],v[80:83],v[32:35],v[128:131]
v_lshl_add_u64 v[146:147],v[148:149],0,v[150:151]
v_mfma_f32_16x16x32_bf16 v[136:139],v[80:83],v[36:39],v[136:139]
s_waitcnt lgkmcnt(4)
ds_read_u16_d16_hi v158,v158
v_mfma_f32_16x16x32_bf16 v[140:143],v[84:87],v[36:39],v[140:143]
ds_read_u16 v157,v157
v_mfma_f32_16x16x32_bf16 v[132:135],v[84:87],v[32:35],v[132:135]
s_nop 0
v_lshlrev_b32_sdwa v128,1,v128 dst_sel:DWORD dst_unused:UNUSED_PAD src0_sel:DWORD src1_sel:WORD_1
v_lshlrev_b32_sdwa v130,1,v130 dst_sel:DWORD dst_unused:UNUSED_PAD src0_sel:DWORD src1_sel:WORD_1
v_mfma_f32_16x16x32_bf16 v[208:211],v[88:91],v[144:147],v[208:211]
s_waitcnt vmcnt(0)
s_barrier
s_setprio 3
ds_read_u16_d16_hi v159,v159
v_fma_mixlo_f16 v120,v140,s99,0
v_lshlrev_b32_sdwa v129,1,v129 dst_sel:DWORD dst_unused:UNUSED_PAD src0_sel:DWORD src1_sel:WORD_1
v_lshlrev_b32_sdwa v131,1,v131 dst_sel:DWORD dst_unused:UNUSED_PAD src0_sel:DWORD src1_sel:WORD_1
v_mfma_f32_16x16x32_bf16 v[216:219],v[92:95],v[144:147],v[216:219]
s_cbranch_vccz RA50_%=
s_add_u32 m0,s58,0
s_mul_i32 s69,s59,1
s_add_u32 s69,s69,s97
buffer_load_dwordx4 v2,s[44:47],s69 offen lds
s_add_u32 m0,m0,1024
s_add_u32 s69,s69,s53
buffer_load_dwordx4 v3,s[44:47],s69 offen lds
s_add_u32 m0,m0,1024
s_add_u32 s69,s69,s53
buffer_load_dwordx4 v2,s[44:47],s69 offen lds
s_add_u32 m0,m0,s50
s_add_u32 s69,s69,s53
buffer_load_dwordx4 v3,s[44:47],s69 offen lds
RA50_%=:
v_fma_mixlo_f16 v122,v141,s99,0
v_lshlrev_b32_sdwa v132,1,v132 dst_sel:DWORD dst_unused:UNUSED_PAD src0_sel:DWORD src1_sel:WORD_1
v_mfma_f32_16x16x32_bf16 v[224:227],v[96:99],v[144:147],v[224:227]
v_lshlrev_b32_sdwa v134,1,v134 dst_sel:DWORD dst_unused:UNUSED_PAD src0_sel:DWORD src1_sel:WORD_1
v_mfma_f32_16x16x32_bf16 v[232:235],v[100:103],v[144:147],v[232:235]
s_waitcnt lgkmcnt(4)
ds_read_u16 v128,v128
v_lshl_add_u64 v[152:153],v[152:153],0,v[154:155]
v_mfma_f32_16x16x32_bf16 v[240:243],v[104:107],v[144:147],v[240:243]
s_waitcnt lgkmcnt(1)
ds_read_u16_d16_hi v130,v130
v_lshl_add_u64 v[154:155],v[156:157],0,v[158:159]
v_mfma_f32_16x16x32_bf16 v[248:251],v[108:111],v[144:147],v[248:251]
v_fma_mixhi_f16 v120,v142,s99,0
v_fma_mixhi_f16 v122,v143,s99,0
v_mfma_f32_16x16x32_bf16 v[212:215],v[88:91],v[152:155],v[212:215]
s_cbranch_vccz RA51_%=
s_add_u32 m0,m0,1024
s_add_u32 s69,s69,s53
buffer_load_dwordx4 v2,s[44:47],s69 offen lds
s_add_u32 m0,m0,1024
s_add_u32 s69,s69,s53
buffer_load_dwordx4 v3,s[44:47],s69 offen lds
s_add_u32 m0,m0,s50
s_add_u32 s69,s69,s53
buffer_load_dwordx4 v2,s[44:47],s69 offen lds
s_add_u32 m0,m0,1024
s_add_u32 s69,s69,s53
buffer_load_dwordx4 v3,s[44:47],s69 offen lds
RA51_%=:
ds_read_u16 v129,v129
v_lshlrev_b32_sdwa v133,1,v133 dst_sel:DWORD dst_unused:UNUSED_PAD src0_sel:DWORD src1_sel:WORD_1
v_lshlrev_b32_sdwa v135,1,v135 dst_sel:DWORD dst_unused:UNUSED_PAD src0_sel:DWORD src1_sel:WORD_1
v_mfma_f32_16x16x32_bf16 v[220:223],v[92:95],v[152:155],v[220:223]
v_pk_fma_f16 v121,v120,v120,s54
v_pk_fma_f16 v123,v122,v122,s54
v_mfma_f32_16x16x32_bf16 v[228:231],v[96:99],v[152:155],v[228:231]
ds_read_u16_d16_hi v131,v131
v_pk_mul_f16 v121,v120,v121
v_pk_mul_f16 v123,v122,v123
v_mfma_f32_16x16x32_bf16 v[236:239],v[100:103],v[152:155],v[236:239]
ds_read_b64_tr_b16 v[88:89],v6 offset:12320
ds_read_b64_tr_b16 v[90:91],v6 offset:13856
v_lshlrev_b32_sdwa v136,1,v136 dst_sel:DWORD dst_unused:UNUSED_PAD src0_sel:DWORD src1_sel:WORD_1
v_mfma_f32_16x16x32_bf16 v[244:247],v[104:107],v[152:155],v[244:247]
s_cbranch_vccz RA52_%=
s_add_u32 m0,m0,1024
s_add_u32 s69,s69,s53
buffer_load_dwordx4 v2,s[44:47],s69 offen lds
s_add_u32 m0,m0,s50
s_add_u32 s69,s69,s53
buffer_load_dwordx4 v3,s[44:47],s69 offen lds
s_add_u32 m0,m0,1024
s_add_u32 s69,s69,s53
buffer_load_dwordx4 v2,s[44:47],s69 offen lds
s_add_u32 m0,m0,1024
s_add_u32 s69,s69,s53
buffer_load_dwordx4 v3,s[44:47],s69 offen lds
RA52_%=:
ds_read_u16 v132,v132
v_lshlrev_b32_sdwa v138,1,v138 dst_sel:DWORD dst_unused:UNUSED_PAD src0_sel:DWORD src1_sel:WORD_1
v_exp_f16 v120,v121
v_mfma_f32_16x16x32_bf16 v[252:255],v[108:111],v[152:155],v[252:255]
ds_read_b64_tr_b16 v[92:93],v6 offset:12328
v_exp_f16 v122,v123
v_mfma_f32_16x16x32_bf16 v[144:147],v[64:67],v[40:43],v[112:115]
ds_read_u16_d16_hi v134,v134
v_lshlrev_b32_sdwa v137,1,v137 dst_sel:DWORD dst_unused:UNUSED_PAD src0_sel:DWORD src1_sel:WORD_1
v_lshlrev_b32_sdwa v139,1,v139 dst_sel:DWORD dst_unused:UNUSED_PAD src0_sel:DWORD src1_sel:WORD_1
v_mfma_f32_16x16x32_bf16 v[152:155],v[64:67],v[44:47],v[112:115]
ds_read_b64_tr_b16 v[94:95],v6 offset:13864
v_exp_f16_sdwa v120,v121 dst_sel:WORD_1 dst_unused:UNUSED_PRESERVE src0_sel:WORD_1
v_mfma_f32_16x16x32_bf16 v[156:159],v[68:71],v[44:47],v[116:119]
ds_read_u16 v133,v133
v_exp_f16_sdwa v122,v123 dst_sel:WORD_1 dst_unused:UNUSED_PRESERVE src0_sel:WORD_1
v_mfma_f32_16x16x32_bf16 v[148:151],v[68:71],v[40:43],v[116:119]
ds_read_b64_tr_b16 v[96:97],v6 offset:12384
v_pk_fma_f16 v120,v120,s55,s55
v_mfma_f32_16x16x32_bf16 v[144:147],v[72:75],v[48:51],v[144:147]
ds_read_u16_d16_hi v135,v135
v_rcp_f16 v121,v120
v_pk_fma_f16 v122,v122,s55,s55
v_mfma_f32_16x16x32_bf16 v[152:155],v[72:75],v[52:55],v[152:155]
ds_read_b64_tr_b16 v[98:99],v6 offset:13920
v_rcp_f16 v123,v122
v_mfma_f32_16x16x32_bf16 v[156:159],v[76:79],v[52:55],v[156:159]
ds_read_u16 v136,v136
v_rcp_f16_sdwa v121,v120 dst_sel:WORD_1 dst_unused:UNUSED_PRESERVE src0_sel:WORD_1
v_mfma_f32_16x16x32_bf16 v[148:151],v[76:79],v[48:51],v[148:151]
ds_read_b64_tr_b16 v[100:101],v6 offset:12392
ds_read_b64_tr_b16 v[102:103],v6 offset:13928
v_rcp_f16_sdwa v123,v122 dst_sel:WORD_1 dst_unused:UNUSED_PRESERVE src0_sel:WORD_1
v_mfma_f32_16x16x32_bf16 v[144:147],v[80:83],v[56:59],v[144:147]
s_waitcnt lgkmcnt(13)
ds_read_u16_d16_hi v138,v138
v_lshl_add_u64 v[128:129],v[128:129],0,v[130:131]
v_fma_mix_f32 v140,v140,v121,0 op_sel_hi:[0,1,0]
v_fma_mix_f32 v142,v142,v121,0 op_sel:[0,1,0] op_sel_hi:[0,1,0]
v_mfma_f32_16x16x32_bf16 v[152:155],v[80:83],v[60:63],v[152:155]
ds_read_b64_tr_b16 v[104:105],v6 offset:12448
ds_read_b64_tr_b16 v[106:107],v6 offset:13984
v_mfma_f32_16x16x32_bf16 v[156:159],v[84:87],v[60:63],v[156:159]
s_waitcnt lgkmcnt(7)
ds_read_u16 v137,v137
v_lshl_add_u64 v[130:131],v[132:133],0,v[134:135]
v_fma_mix_f32 v141,v141,v123,0 op_sel_hi:[0,1,0]
v_fma_mix_f32 v143,v143,v123,0 op_sel:[0,1,0] op_sel_hi:[0,1,0]
v_mfma_f32_16x16x32_bf16 v[148:151],v[84:87],v[56:59],v[148:151]
ds_read_u16_d16_hi v139,v139
v_lshlrev_b32_sdwa v144,1,v144 dst_sel:DWORD dst_unused:UNUSED_PAD src0_sel:DWORD src1_sel:WORD_1
v_lshlrev_b32_sdwa v146,1,v146 dst_sel:DWORD dst_unused:UNUSED_PAD src0_sel:DWORD src1_sel:WORD_1
v_mfma_f32_16x16x32_bf16 v[160:163],v[88:91],v[128:131],v[160:163]
ds_read_b64_tr_b16 v[108:109],v6 offset:12456
ds_read_b64_tr_b16 v[110:111],v6 offset:13992
v_lshlrev_b32_sdwa v145,1,v145 dst_sel:DWORD dst_unused:UNUSED_PAD src0_sel:DWORD src1_sel:WORD_1
v_lshlrev_b32_sdwa v147,1,v147 dst_sel:DWORD dst_unused:UNUSED_PAD src0_sel:DWORD src1_sel:WORD_1
v_mfma_f32_16x16x32_bf16 v[168:171],v[92:95],v[128:131],v[168:171]
s_waitcnt lgkmcnt(10)
ds_read_b64_tr_b16 v[64:65],v4 offset:12320
ds_read_b64_tr_b16 v[66:67],v4 offset:14368
v_fma_mixlo_f16 v120,v156,s99,0
v_mfma_f32_16x16x32_bf16 v[176:179],v[96:99],v[128:131],v[176:179]
s_waitcnt lgkmcnt(9)
ds_read_b128 v[112:115],v7 offset:0
ds_read_u16 v144,v144
v_fma_mixlo_f16 v122,v157,s99,0
v_lshlrev_b32_sdwa v148,1,v148 dst_sel:DWORD dst_unused:UNUSED_PAD src0_sel:DWORD src1_sel:WORD_1
v_lshlrev_b32_sdwa v150,1,v150 dst_sel:DWORD dst_unused:UNUSED_PAD src0_sel:DWORD src1_sel:WORD_1
v_mfma_f32_16x16x32_bf16 v[184:187],v[100:103],v[128:131],v[184:187]
s_waitcnt lgkmcnt(6)
ds_read_b64_tr_b16 v[68:69],v5 offset:12320
v_lshl_add_u64 v[136:137],v[136:137],0,v[138:139]
v_mfma_f32_16x16x32_bf16 v[192:195],v[104:107],v[128:131],v[192:195]
s_waitcnt lgkmcnt(5)
ds_read_u16_d16_hi v146,v146
v_cvt_pk_bf16_f32 v138,v140,v142
v_cvt_pk_bf16_f32 v139,v141,v143
v_mfma_f32_16x16x32_bf16 v[200:203],v[108:111],v[128:131],v[200:203]
ds_read_b64_tr_b16 v[70:71],v5 offset:14368
ds_read_b128 v[116:119],v7 offset:32
v_fma_mixhi_f16 v120,v158,s99,0
v_fma_mixhi_f16 v122,v159,s99,0
v_mfma_f32_16x16x32_bf16 v[164:167],v[88:91],v[136:139],v[164:167]
v_lshlrev_b32_sdwa v149,1,v149 dst_sel:DWORD dst_unused:UNUSED_PAD src0_sel:DWORD src1_sel:WORD_1
v_lshlrev_b32_sdwa v151,1,v151 dst_sel:DWORD dst_unused:UNUSED_PAD src0_sel:DWORD src1_sel:WORD_1
v_mfma_f32_16x16x32_bf16 v[172:175],v[92:95],v[136:139],v[172:175]
ds_read_b64_tr_b16 v[72:73],v4 offset:16416
ds_read_b64_tr_b16 v[74:75],v4 offset:18464
v_pk_fma_f16 v123,v122,v122,s54
v_pk_fma_f16 v121,v120,v120,s54
v_mfma_f32_16x16x32_bf16 v[180:183],v[96:99],v[136:139],v[180:183]
ds_read_u16 v145,v145
v_pk_mul_f16 v121,v120,v121
v_pk_mul_f16 v123,v122,v123
v_mfma_f32_16x16x32_bf16 v[188:191],v[100:103],v[136:139],v[188:191]
ds_read_b64_tr_b16 v[76:77],v5 offset:16416
v_lshlrev_b32_sdwa v152,1,v152 dst_sel:DWORD dst_unused:UNUSED_PAD src0_sel:DWORD src1_sel:WORD_1
v_lshlrev_b32_sdwa v154,1,v154 dst_sel:DWORD dst_unused:UNUSED_PAD src0_sel:DWORD src1_sel:WORD_1
v_mfma_f32_16x16x32_bf16 v[196:199],v[104:107],v[136:139],v[196:199]
ds_read_u16_d16_hi v147,v147
v_exp_f16 v120,v121
v_mfma_f32_16x16x32_bf16 v[204:207],v[108:111],v[136:139],v[204:207]
s_waitcnt lgkmcnt(10)
ds_read_b64_tr_b16 v[78:79],v5 offset:18464
v_exp_f16 v122,v123
v_mfma_f32_16x16x32_bf16 v[128:131],v[64:67],v[16:19],v[112:115]
ds_read_u16 v148,v148
ds_read_u16_d16_hi v150,v150
v_lshlrev_b32_sdwa v153,1,v153 dst_sel:DWORD dst_unused:UNUSED_PAD src0_sel:DWORD src1_sel:WORD_1
v_lshlrev_b32_sdwa v155,1,v155 dst_sel:DWORD dst_unused:UNUSED_PAD src0_sel:DWORD src1_sel:WORD_1
v_exp_f16_sdwa v122,v123 dst_sel:WORD_1 dst_unused:UNUSED_PRESERVE src0_sel:WORD_1
s_nop 0
s_mov_b32 s86,0xbe801d5a
v_mfma_f32_16x16x32_bf16 v[136:139],v[64:67],v[20:23],v[112:115]
s_waitcnt lgkmcnt(8)
v_exp_f16_sdwa v120,v121 dst_sel:WORD_1 dst_unused:UNUSED_PRESERVE src0_sel:WORD_1
v_mfma_f32_16x16x32_bf16 v[140:143],v[68:71],v[20:23],v[116:119]
ds_read_b64_tr_b16 v[80:81],v4 offset:20512
ds_read_b64_tr_b16 v[82:83],v4 offset:22560
v_pk_fma_f16 v120,v120,s55,s55
v_mfma_f32_16x16x32_bf16 v[132:135],v[68:71],v[16:19],v[116:119]
s_waitcnt lgkmcnt(8)
ds_read_u16 v149,v149
v_pk_fma_f16 v122,v122,s55,s55
v_mfma_f32_16x16x32_bf16 v[128:131],v[72:75],v[24:27],v[128:131]
ds_read_b64_tr_b16 v[84:85],v5 offset:20512
ds_read_u16_d16_hi v151,v151
v_rcp_f16 v121,v120
v_mfma_f32_16x16x32_bf16 v[136:139],v[72:75],v[28:31],v[136:139]
s_waitcnt lgkmcnt(7)
ds_read_b64_tr_b16 v[86:87],v5 offset:22560
v_rcp_f16 v123,v122
v_mfma_f32_16x16x32_bf16 v[140:143],v[76:79],v[28:31],v[140:143]
ds_read_u16 v152,v152
v_rcp_f16_sdwa v121,v120 dst_sel:WORD_1 dst_unused:UNUSED_PRESERVE src0_sel:WORD_1
v_mfma_f32_16x16x32_bf16 v[132:135],v[76:79],v[24:27],v[132:135]
s_waitcnt lgkmcnt(5)
v_rcp_f16_sdwa v123,v122 dst_sel:WORD_1 dst_unused:UNUSED_PRESERVE src0_sel:WORD_1
v_mfma_f32_16x16x32_bf16 v[128:131],v[80:83],v[32:35],v[128:131]
ds_read_u16_d16_hi v154,v154
v_fma_mix_f32 v156,v156,v121,0 op_sel_hi:[0,1,0]
v_mfma_f32_16x16x32_bf16 v[136:139],v[80:83],v[36:39],v[136:139]
s_waitcnt lgkmcnt(2)
v_lshl_add_u64 v[144:145],v[144:145],0,v[146:147]
v_fma_mix_f32 v158,v158,v121,0 op_sel:[0,1,0] op_sel_hi:[0,1,0]
v_lshl_add_u64 v[146:147],v[148:149],0,v[150:151]
v_mfma_f32_16x16x32_bf16 v[140:143],v[84:87],v[36:39],v[140:143]
ds_read_u16 v153,v153
v_fma_mix_f32 v157,v157,v123,0 op_sel_hi:[0,1,0]
v_fma_mix_f32 v159,v159,v123,0 op_sel:[0,1,0] op_sel_hi:[0,1,0]
v_mfma_f32_16x16x32_bf16 v[132:135],v[84:87],v[32:35],v[132:135]
v_lshlrev_b32_sdwa v128,1,v128 dst_sel:DWORD dst_unused:UNUSED_PAD src0_sel:DWORD src1_sel:WORD_1
v_lshlrev_b32_sdwa v130,1,v130 dst_sel:DWORD dst_unused:UNUSED_PAD src0_sel:DWORD src1_sel:WORD_1
v_mfma_f32_16x16x32_bf16 v[208:211],v[88:91],v[144:147],v[208:211]
ds_read_u16_d16_hi v155,v155
v_fma_mixlo_f16 v120,v140,s99,0
v_lshlrev_b32_sdwa v129,1,v129 dst_sel:DWORD dst_unused:UNUSED_PAD src0_sel:DWORD src1_sel:WORD_1
v_lshlrev_b32_sdwa v131,1,v131 dst_sel:DWORD dst_unused:UNUSED_PAD src0_sel:DWORD src1_sel:WORD_1
v_mfma_f32_16x16x32_bf16 v[216:219],v[92:95],v[144:147],v[216:219]
v_fma_mixlo_f16 v122,v141,s99,0
v_lshlrev_b32_sdwa v132,1,v132 dst_sel:DWORD dst_unused:UNUSED_PAD src0_sel:DWORD src1_sel:WORD_1
v_mfma_f32_16x16x32_bf16 v[224:227],v[96:99],v[144:147],v[224:227]
v_lshlrev_b32_sdwa v134,1,v134 dst_sel:DWORD dst_unused:UNUSED_PAD src0_sel:DWORD src1_sel:WORD_1
v_mfma_f32_16x16x32_bf16 v[232:235],v[100:103],v[144:147],v[232:235]
s_waitcnt lgkmcnt(0)
ds_read_u16 v128,v128
v_lshl_add_u64 v[152:153],v[152:153],0,v[154:155]
v_mfma_f32_16x16x32_bf16 v[240:243],v[104:107],v[144:147],v[240:243]
ds_read_u16_d16_hi v130,v130
v_cvt_pk_bf16_f32 v155,v157,v159
v_cvt_pk_bf16_f32 v154,v156,v158
v_mfma_f32_16x16x32_bf16 v[248:251],v[108:111],v[144:147],v[248:251]
v_fma_mixhi_f16 v120,v142,s99,0
v_fma_mixhi_f16 v122,v143,s99,0
v_mfma_f32_16x16x32_bf16 v[212:215],v[88:91],v[152:155],v[212:215]
s_setprio 2
ds_read_u16 v129,v129
v_lshlrev_b32_sdwa v133,1,v133 dst_sel:DWORD dst_unused:UNUSED_PAD src0_sel:DWORD src1_sel:WORD_1
v_lshlrev_b32_sdwa v135,1,v135 dst_sel:DWORD dst_unused:UNUSED_PAD src0_sel:DWORD src1_sel:WORD_1
v_mfma_f32_16x16x32_bf16 v[220:223],v[92:95],v[152:155],v[220:223]
v_pk_fma_f16 v121,v120,v120,s54
v_pk_fma_f16 v123,v122,v122,s54
v_mfma_f32_16x16x32_bf16 v[228:231],v[96:99],v[152:155],v[228:231]
ds_read_u16_d16_hi v131,v131
v_pk_mul_f16 v121,v120,v121
v_pk_mul_f16 v123,v122,v123
v_mfma_f32_16x16x32_bf16 v[236:239],v[100:103],v[152:155],v[236:239]
ds_read_b64_tr_b16 v[88:89],v6 offset:18480
ds_read_b64_tr_b16 v[90:91],v6 offset:20016
v_lshlrev_b32_sdwa v136,1,v136 dst_sel:DWORD dst_unused:UNUSED_PAD src0_sel:DWORD src1_sel:WORD_1
v_mfma_f32_16x16x32_bf16 v[244:247],v[104:107],v[152:155],v[244:247]
ds_read_u16 v132,v132
v_lshlrev_b32_sdwa v138,1,v138 dst_sel:DWORD dst_unused:UNUSED_PAD src0_sel:DWORD src1_sel:WORD_1
v_exp_f16 v120,v121
v_mfma_f32_16x16x32_bf16 v[252:255],v[108:111],v[152:155],v[252:255]
ds_read_b64_tr_b16 v[92:93],v6 offset:18488
v_exp_f16 v122,v123
v_mfma_f32_16x16x32_bf16 v[144:147],v[64:67],v[40:43],v[112:115]
ds_read_u16_d16_hi v134,v134
v_lshlrev_b32_sdwa v137,1,v137 dst_sel:DWORD dst_unused:UNUSED_PAD src0_sel:DWORD src1_sel:WORD_1
v_lshlrev_b32_sdwa v139,1,v139 dst_sel:DWORD dst_unused:UNUSED_PAD src0_sel:DWORD src1_sel:WORD_1
v_mfma_f32_16x16x32_bf16 v[152:155],v[64:67],v[44:47],v[112:115]
ds_read_b64_tr_b16 v[94:95],v6 offset:20024
v_exp_f16_sdwa v120,v121 dst_sel:WORD_1 dst_unused:UNUSED_PRESERVE src0_sel:WORD_1
v_mfma_f32_16x16x32_bf16 v[156:159],v[68:71],v[44:47],v[116:119]
ds_read_u16 v133,v133
v_exp_f16_sdwa v122,v123 dst_sel:WORD_1 dst_unused:UNUSED_PRESERVE src0_sel:WORD_1
v_mfma_f32_16x16x32_bf16 v[148:151],v[68:71],v[40:43],v[116:119]
ds_read_b64_tr_b16 v[96:97],v6 offset:18544
v_pk_fma_f16 v120,v120,s55,s55
v_mfma_f32_16x16x32_bf16 v[144:147],v[72:75],v[48:51],v[144:147]
ds_read_u16_d16_hi v135,v135
v_rcp_f16 v121,v120
v_pk_fma_f16 v122,v122,s55,s55
v_mfma_f32_16x16x32_bf16 v[152:155],v[72:75],v[52:55],v[152:155]
ds_read_b64_tr_b16 v[98:99],v6 offset:20080
v_rcp_f16 v123,v122
v_mfma_f32_16x16x32_bf16 v[156:159],v[76:79],v[52:55],v[156:159]
ds_read_u16 v136,v136
v_rcp_f16_sdwa v121,v120 dst_sel:WORD_1 dst_unused:UNUSED_PRESERVE src0_sel:WORD_1
v_mfma_f32_16x16x32_bf16 v[148:151],v[76:79],v[48:51],v[148:151]
ds_read_b64_tr_b16 v[100:101],v6 offset:18552
ds_read_b64_tr_b16 v[102:103],v6 offset:20088
v_rcp_f16_sdwa v123,v122 dst_sel:WORD_1 dst_unused:UNUSED_PRESERVE src0_sel:WORD_1
v_mfma_f32_16x16x32_bf16 v[144:147],v[80:83],v[56:59],v[144:147]
s_waitcnt lgkmcnt(13)
ds_read_u16_d16_hi v138,v138
v_lshl_add_u64 v[128:129],v[128:129],0,v[130:131]
v_fma_mix_f32 v140,v140,v121,0 op_sel_hi:[0,1,0]
v_fma_mix_f32 v142,v142,v121,0 op_sel:[0,1,0] op_sel_hi:[0,1,0]
v_mfma_f32_16x16x32_bf16 v[152:155],v[80:83],v[60:63],v[152:155]
ds_read_b64_tr_b16 v[104:105],v6 offset:18608
ds_read_b64_tr_b16 v[106:107],v6 offset:20144
v_mfma_f32_16x16x32_bf16 v[156:159],v[84:87],v[60:63],v[156:159]
s_waitcnt lgkmcnt(7)
ds_read_u16 v137,v137
v_lshl_add_u64 v[130:131],v[132:133],0,v[134:135]
v_fma_mix_f32 v141,v141,v123,0 op_sel_hi:[0,1,0]
v_fma_mix_f32 v143,v143,v123,0 op_sel:[0,1,0] op_sel_hi:[0,1,0]
v_mfma_f32_16x16x32_bf16 v[148:151],v[84:87],v[56:59],v[148:151]
ds_read_u16_d16_hi v139,v139
v_lshlrev_b32_sdwa v144,1,v144 dst_sel:DWORD dst_unused:UNUSED_PAD src0_sel:DWORD src1_sel:WORD_1
v_lshlrev_b32_sdwa v146,1,v146 dst_sel:DWORD dst_unused:UNUSED_PAD src0_sel:DWORD src1_sel:WORD_1
v_mfma_f32_16x16x32_bf16 v[160:163],v[88:91],v[128:131],v[160:163]
ds_read_b64_tr_b16 v[108:109],v6 offset:18616
ds_read_b64_tr_b16 v[110:111],v6 offset:20152
v_lshlrev_b32_sdwa v145,1,v145 dst_sel:DWORD dst_unused:UNUSED_PAD src0_sel:DWORD src1_sel:WORD_1
v_lshlrev_b32_sdwa v147,1,v147 dst_sel:DWORD dst_unused:UNUSED_PAD src0_sel:DWORD src1_sel:WORD_1
v_mfma_f32_16x16x32_bf16 v[168:171],v[92:95],v[128:131],v[168:171]
s_waitcnt lgkmcnt(10)
ds_read_b64_tr_b16 v[64:65],v4 offset:12832
ds_read_b64_tr_b16 v[66:67],v4 offset:14880
v_fma_mixlo_f16 v120,v156,s99,0
v_mfma_f32_16x16x32_bf16 v[176:179],v[96:99],v[128:131],v[176:179]
s_waitcnt lgkmcnt(9)
ds_read_b128 v[112:115],v7 offset:128
ds_read_u16 v144,v144
v_fma_mixlo_f16 v122,v157,s99,0
v_lshlrev_b32_sdwa v148,1,v148 dst_sel:DWORD dst_unused:UNUSED_PAD src0_sel:DWORD src1_sel:WORD_1
v_lshlrev_b32_sdwa v150,1,v150 dst_sel:DWORD dst_unused:UNUSED_PAD src0_sel:DWORD src1_sel:WORD_1
v_mfma_f32_16x16x32_bf16 v[184:187],v[100:103],v[128:131],v[184:187]
s_waitcnt lgkmcnt(6)
s_setprio 1
ds_read_b64_tr_b16 v[68:69],v5 offset:12832
v_lshl_add_u64 v[136:137],v[136:137],0,v[138:139]
v_mfma_f32_16x16x32_bf16 v[192:195],v[104:107],v[128:131],v[192:195]
s_waitcnt lgkmcnt(5)
ds_read_u16_d16_hi v146,v146
v_cvt_pk_bf16_f32 v138,v140,v142
v_cvt_pk_bf16_f32 v139,v141,v143
v_mfma_f32_16x16x32_bf16 v[200:203],v[108:111],v[128:131],v[200:203]
ds_read_b64_tr_b16 v[70:71],v5 offset:14880
ds_read_b128 v[116:119],v7 offset:160
v_fma_mixhi_f16 v120,v158,s99,0
v_fma_mixhi_f16 v122,v159,s99,0
v_mfma_f32_16x16x32_bf16 v[164:167],v[88:91],v[136:139],v[164:167]
v_lshlrev_b32_sdwa v149,1,v149 dst_sel:DWORD dst_unused:UNUSED_PAD src0_sel:DWORD src1_sel:WORD_1
v_lshlrev_b32_sdwa v151,1,v151 dst_sel:DWORD dst_unused:UNUSED_PAD src0_sel:DWORD src1_sel:WORD_1
v_mfma_f32_16x16x32_bf16 v[172:175],v[92:95],v[136:139],v[172:175]
ds_read_b64_tr_b16 v[72:73],v4 offset:16928
ds_read_b64_tr_b16 v[74:75],v4 offset:18976
v_pk_fma_f16 v123,v122,v122,s54
v_pk_fma_f16 v121,v120,v120,s54
v_mfma_f32_16x16x32_bf16 v[180:183],v[96:99],v[136:139],v[180:183]
ds_read_u16 v145,v145
v_pk_mul_f16 v121,v120,v121
v_pk_mul_f16 v123,v122,v123
v_mfma_f32_16x16x32_bf16 v[188:191],v[100:103],v[136:139],v[188:191]
ds_read_b64_tr_b16 v[76:77],v5 offset:16928
v_lshlrev_b32_sdwa v152,1,v152 dst_sel:DWORD dst_unused:UNUSED_PAD src0_sel:DWORD src1_sel:WORD_1
v_lshlrev_b32_sdwa v154,1,v154 dst_sel:DWORD dst_unused:UNUSED_PAD src0_sel:DWORD src1_sel:WORD_1
v_mfma_f32_16x16x32_bf16 v[196:199],v[104:107],v[136:139],v[196:199]
ds_read_u16_d16_hi v147,v147
v_exp_f16 v120,v121
v_mfma_f32_16x16x32_bf16 v[204:207],v[108:111],v[136:139],v[204:207]
s_waitcnt lgkmcnt(10)
ds_read_b64_tr_b16 v[78:79],v5 offset:18976
v_exp_f16 v122,v123
v_mfma_f32_16x16x32_bf16 v[128:131],v[64:67],v[16:19],v[112:115]
ds_read_u16 v148,v148
ds_read_u16_d16_hi v150,v150
v_lshlrev_b32_sdwa v153,1,v153 dst_sel:DWORD dst_unused:UNUSED_PAD src0_sel:DWORD src1_sel:WORD_1
v_lshlrev_b32_sdwa v155,1,v155 dst_sel:DWORD dst_unused:UNUSED_PAD src0_sel:DWORD src1_sel:WORD_1
v_exp_f16_sdwa v122,v123 dst_sel:WORD_1 dst_unused:UNUSED_PRESERVE src0_sel:WORD_1
v_mfma_f32_16x16x32_bf16 v[136:139],v[64:67],v[20:23],v[112:115]
s_waitcnt lgkmcnt(8)
s_setprio 0
v_exp_f16_sdwa v120,v121 dst_sel:WORD_1 dst_unused:UNUSED_PRESERVE src0_sel:WORD_1
v_mfma_f32_16x16x32_bf16 v[140:143],v[68:71],v[20:23],v[116:119]
ds_read_b64_tr_b16 v[80:81],v4 offset:21024
ds_read_b64_tr_b16 v[82:83],v4 offset:23072
v_pk_fma_f16 v120,v120,s55,s55
v_mfma_f32_16x16x32_bf16 v[132:135],v[68:71],v[16:19],v[116:119]
s_waitcnt lgkmcnt(8)
ds_read_u16 v149,v149
v_pk_fma_f16 v122,v122,s55,s55
v_mfma_f32_16x16x32_bf16 v[128:131],v[72:75],v[24:27],v[128:131]
ds_read_b64_tr_b16 v[84:85],v5 offset:21024
ds_read_u16_d16_hi v151,v151
v_rcp_f16 v121,v120
v_mfma_f32_16x16x32_bf16 v[136:139],v[72:75],v[28:31],v[136:139]
s_waitcnt lgkmcnt(7)
ds_read_b64_tr_b16 v[86:87],v5 offset:23072
v_rcp_f16 v123,v122
v_mfma_f32_16x16x32_bf16 v[140:143],v[76:79],v[28:31],v[140:143]
ds_read_u16 v152,v152
v_rcp_f16_sdwa v121,v120 dst_sel:WORD_1 dst_unused:UNUSED_PRESERVE src0_sel:WORD_1
v_mfma_f32_16x16x32_bf16 v[132:135],v[76:79],v[24:27],v[132:135]
s_waitcnt lgkmcnt(5)
v_rcp_f16_sdwa v123,v122 dst_sel:WORD_1 dst_unused:UNUSED_PRESERVE src0_sel:WORD_1
v_mfma_f32_16x16x32_bf16 v[128:131],v[80:83],v[32:35],v[128:131]
ds_read_u16_d16_hi v154,v154
v_fma_mix_f32 v156,v156,v121,0 op_sel_hi:[0,1,0]
v_mfma_f32_16x16x32_bf16 v[136:139],v[80:83],v[36:39],v[136:139]
s_waitcnt lgkmcnt(2)
v_lshl_add_u64 v[144:145],v[144:145],0,v[146:147]
v_fma_mix_f32 v158,v158,v121,0 op_sel:[0,1,0] op_sel_hi:[0,1,0]
v_lshl_add_u64 v[146:147],v[148:149],0,v[150:151]
v_mfma_f32_16x16x32_bf16 v[140:143],v[84:87],v[36:39],v[140:143]
ds_read_u16 v153,v153
v_fma_mix_f32 v157,v157,v123,0 op_sel_hi:[0,1,0]
v_fma_mix_f32 v159,v159,v123,0 op_sel:[0,1,0] op_sel_hi:[0,1,0]
v_mfma_f32_16x16x32_bf16 v[132:135],v[84:87],v[32:35],v[132:135]
v_lshlrev_b32_sdwa v128,1,v128 dst_sel:DWORD dst_unused:UNUSED_PAD src0_sel:DWORD src1_sel:WORD_1
v_lshlrev_b32_sdwa v130,1,v130 dst_sel:DWORD dst_unused:UNUSED_PAD src0_sel:DWORD src1_sel:WORD_1
v_mfma_f32_16x16x32_bf16 v[208:211],v[88:91],v[144:147],v[208:211]
s_waitcnt vmcnt(0)
s_barrier
s_setprio 2
v_lshlrev_b32_sdwa v129,1,v129 dst_sel:DWORD dst_unused:UNUSED_PAD src0_sel:DWORD src1_sel:WORD_1
v_lshlrev_b32_sdwa v131,1,v131 dst_sel:DWORD dst_unused:UNUSED_PAD src0_sel:DWORD src1_sel:WORD_1
v_mfma_f32_16x16x32_bf16 v[216:219],v[92:95],v[144:147],v[216:219]
ds_read_u16_d16_hi v155,v155
v_mfma_f32_16x16x32_bf16 v[224:227],v[96:99],v[144:147],v[224:227]
s_cbranch_vccz RA53_%=
s_add_u32 m0,s58,12320
s_mul_i32 s69,s59,3
s_add_u32 s69,s69,s97
buffer_load_dwordx4 v2,s[44:47],s69 offen lds
s_add_u32 m0,m0,1024
s_add_u32 s69,s69,s53
buffer_load_dwordx4 v3,s[44:47],s69 offen lds
s_add_u32 m0,m0,1024
s_add_u32 s69,s69,s53
buffer_load_dwordx4 v2,s[44:47],s69 offen lds
s_add_u32 m0,m0,s50
s_add_u32 s69,s69,s53
buffer_load_dwordx4 v3,s[44:47],s69 offen lds
RA53_%=:
v_lshlrev_b32_sdwa v132,1,v132 dst_sel:DWORD dst_unused:UNUSED_PAD src0_sel:DWORD src1_sel:WORD_1
v_mfma_f32_16x16x32_bf16 v[232:235],v[100:103],v[144:147],v[232:235]
s_waitcnt lgkmcnt(0)
ds_read_u16 v128,v128
v_lshl_add_u64 v[152:153],v[152:153],0,v[154:155]
v_lshlrev_b32_sdwa v134,1,v134 dst_sel:DWORD dst_unused:UNUSED_PAD src0_sel:DWORD src1_sel:WORD_1
v_cvt_pk_bf16_f32 v154,v156,v158
v_cvt_pk_bf16_f32 v155,v157,v159
v_mfma_f32_16x16x32_bf16 v[240:243],v[104:107],v[144:147],v[240:243]
ds_read_u16_d16_hi v130,v130
v_mfma_f32_16x16x32_bf16 v[248:251],v[108:111],v[144:147],v[248:251]
ds_read_u16 v129,v129
v_lshlrev_b32_sdwa v133,1,v133 dst_sel:DWORD dst_unused:UNUSED_PAD src0_sel:DWORD src1_sel:WORD_1
v_mfma_f32_16x16x32_bf16 v[212:215],v[88:91],v[152:155],v[212:215]
s_cbranch_vccz RA54_%=
s_add_u32 m0,m0,1024
s_add_u32 s69,s69,s53
buffer_load_dwordx4 v2,s[44:47],s69 offen lds
s_add_u32 m0,m0,1024
s_add_u32 s69,s69,s53
buffer_load_dwordx4 v3,s[44:47],s69 offen lds
s_add_u32 m0,m0,s50
s_add_u32 s69,s69,s53
buffer_load_dwordx4 v2,s[44:47],s69 offen lds
s_add_u32 m0,m0,1024
s_add_u32 s69,s69,s53
buffer_load_dwordx4 v3,s[44:47],s69 offen lds
RA54_%=:
v_lshlrev_b32_sdwa v135,1,v135 dst_sel:DWORD dst_unused:UNUSED_PAD src0_sel:DWORD src1_sel:WORD_1
v_mfma_f32_16x16x32_bf16 v[220:223],v[92:95],v[152:155],v[220:223]
ds_read_u16_d16_hi v131,v131
v_lshlrev_b32_sdwa v136,1,v136 dst_sel:DWORD dst_unused:UNUSED_PAD src0_sel:DWORD src1_sel:WORD_1
v_mfma_f32_16x16x32_bf16 v[228:231],v[96:99],v[152:155],v[228:231]
ds_read_b64_tr_b16 v[88:89],v6 offset:0
ds_read_b64_tr_b16 v[90:91],v6 offset:1536
v_lshlrev_b32_sdwa v138,1,v138 dst_sel:DWORD dst_unused:UNUSED_PAD src0_sel:DWORD src1_sel:WORD_1
v_mfma_f32_16x16x32_bf16 v[236:239],v[100:103],v[152:155],v[236:239]
ds_read_u16 v132,v132
v_lshlrev_b32_sdwa v137,1,v137 dst_sel:DWORD dst_unused:UNUSED_PAD src0_sel:DWORD src1_sel:WORD_1
v_mfma_f32_16x16x32_bf16 v[244:247],v[104:107],v[152:155],v[244:247]
ds_read_u16_d16_hi v134,v134
v_lshlrev_b32_sdwa v139,1,v139 dst_sel:DWORD dst_unused:UNUSED_PAD src0_sel:DWORD src1_sel:WORD_1
v_mfma_f32_16x16x32_bf16 v[252:255],v[108:111],v[152:155],v[252:255]
s_cbranch_vccz RA55_%=
s_add_u32 m0,m0,1024
s_add_u32 s69,s69,s53
buffer_load_dwordx4 v2,s[44:47],s69 offen lds
s_add_u32 m0,m0,s50
s_add_u32 s69,s69,s53
buffer_load_dwordx4 v3,s[44:47],s69 offen lds
s_add_u32 m0,m0,1024
s_add_u32 s69,s69,s53
buffer_load_dwordx4 v2,s[44:47],s69 offen lds
s_add_u32 m0,m0,1024
s_add_u32 s69,s69,s53
buffer_load_dwordx4 v3,s[44:47],s69 offen lds
RA55_%=:
ds_read_b64_tr_b16 v[92:93],v6 offset:8
ds_read_b64_tr_b16 v[94:95],v6 offset:1544
v_lshlrev_b32_sdwa v140,1,v140 dst_sel:DWORD dst_unused:UNUSED_PAD src0_sel:DWORD src1_sel:WORD_1
v_mfma_f32_16x16x32_bf16 v[144:147],v[64:67],v[40:43],v[112:115]
ds_read_u16 v133,v133
v_lshlrev_b32_sdwa v142,1,v142 dst_sel:DWORD dst_unused:UNUSED_PAD src0_sel:DWORD src1_sel:WORD_1
v_mfma_f32_16x16x32_bf16 v[152:155],v[64:67],v[44:47],v[112:115]
ds_read_b64_tr_b16 v[96:97],v6 offset:64
ds_read_b64_tr_b16 v[98:99],v6 offset:1600
v_lshlrev_b32_sdwa v141,1,v141 dst_sel:DWORD dst_unused:UNUSED_PAD src0_sel:DWORD src1_sel:WORD_1
v_lshlrev_b32_sdwa v143,1,v143 dst_sel:DWORD dst_unused:UNUSED_PAD src0_sel:DWORD src1_sel:WORD_1
v_mfma_f32_16x16x32_bf16 v[156:159],v[68:71],v[44:47],v[116:119]
ds_read_u16_d16_hi v135,v135
v_mfma_f32_16x16x32_bf16 v[148:151],v[68:71],v[40:43],v[116:119]
ds_read_u16 v136,v136
v_mfma_f32_16x16x32_bf16 v[144:147],v[72:75],v[48:51],v[144:147]
ds_read_u16_d16_hi v138,v138
v_mfma_f32_16x16x32_bf16 v[152:155],v[72:75],v[52:55],v[152:155]
s_cbranch_vccz RA56_%=
s_add_u32 m0,s100,0
s_mul_i32 s69,s59,9
s_add_u32 s69,s69,s97
buffer_load_dwordx4 v2,s[44:47],s69 offen lds
s_add_u32 m0,m0,1024
s_add_u32 s69,s69,s53
buffer_load_dwordx4 v3,s[44:47],s69 offen lds
s_add_u32 m0,m0,1024
s_add_u32 s69,s69,s53
buffer_load_dwordx4 v2,s[44:47],s69 offen lds
s_add_u32 m0,m0,s50
s_add_u32 s69,s69,s53
buffer_load_dwordx4 v3,s[44:47],s69 offen lds
RA56_%=:
ds_read_b64_tr_b16 v[100:101],v6 offset:72
ds_read_b64_tr_b16 v[102:103],v6 offset:1608
v_mfma_f32_16x16x32_bf16 v[156:159],v[76:79],v[52:55],v[156:159]
ds_read_u16 v137,v137
v_mfma_f32_16x16x32_bf16 v[148:151],v[76:79],v[48:51],v[148:151]
s_waitcnt lgkmcnt(5)
ds_read_u16_d16_hi v139,v139
v_lshl_add_u64 v[128:129],v[128:129],0,v[130:131]
v_lshl_add_u64 v[130:131],v[132:133],0,v[134:135]
v_mfma_f32_16x16x32_bf16 v[144:147],v[80:83],v[56:59],v[144:147]
ds_read_b64_tr_b16 v[104:105],v6 offset:128
ds_read_b64_tr_b16 v[106:107],v6 offset:1664
v_mfma_f32_16x16x32_bf16 v[152:155],v[80:83],v[60:63],v[152:155]
ds_read_u16 v140,v140
v_mfma_f32_16x16x32_bf16 v[156:159],v[84:87],v[60:63],v[156:159]
ds_read_u16_d16_hi v142,v142
v_mfma_f32_16x16x32_bf16 v[148:151],v[84:87],v[56:59],v[148:151]
s_cbranch_vccz RA57_%=
s_add_u32 m0,m0,1024
s_add_u32 s69,s69,s53
buffer_load_dwordx4 v2,s[44:47],s69 offen lds
s_add_u32 m0,m0,1024
s_add_u32 s69,s69,s53
buffer_load_dwordx4 v3,s[44:47],s69 offen lds
s_add_u32 m0,m0,s50
s_add_u32 s69,s69,s53
buffer_load_dwordx4 v2,s[44:47],s69 offen lds
s_add_u32 m0,m0,1024
s_add_u32 s69,s69,s53
buffer_load_dwordx4 v3,s[44:47],s69 offen lds
RA57_%=:
ds_read_b64_tr_b16 v[108:109],v6 offset:136
ds_read_b64_tr_b16 v[110:111],v6 offset:1672
v_lshlrev_b32_sdwa v144,1,v144 dst_sel:DWORD dst_unused:UNUSED_PAD src0_sel:DWORD src1_sel:WORD_1
v_lshlrev_b32_sdwa v146,1,v146 dst_sel:DWORD dst_unused:UNUSED_PAD src0_sel:DWORD src1_sel:WORD_1
v_mfma_f32_16x16x32_bf16 v[160:163],v[88:91],v[128:131],v[160:163]
ds_read_b64_tr_b16 v[64:65],v4 offset:0
ds_read_u16 v141,v141
v_lshlrev_b32_sdwa v145,1,v145 dst_sel:DWORD dst_unused:UNUSED_PAD src0_sel:DWORD src1_sel:WORD_1
v_mfma_f32_16x16x32_bf16 v[168:171],v[92:95],v[128:131],v[168:171]
ds_read_u16_d16_hi v143,v143
v_lshlrev_b32_sdwa v147,1,v147 dst_sel:DWORD dst_unused:UNUSED_PAD src0_sel:DWORD src1_sel:WORD_1
v_mfma_f32_16x16x32_bf16 v[176:179],v[96:99],v[128:131],v[176:179]
s_waitcnt lgkmcnt(11)
ds_read_b64_tr_b16 v[66:67],v4 offset:2048
v_lshlrev_b32_sdwa v148,1,v148 dst_sel:DWORD dst_unused:UNUSED_PAD src0_sel:DWORD src1_sel:WORD_1
v_mfma_f32_16x16x32_bf16 v[184:187],v[100:103],v[128:131],v[184:187]
s_waitcnt lgkmcnt(8)
ds_read_u16 v144,v144
v_lshl_add_u64 v[136:137],v[136:137],0,v[138:139]
v_lshlrev_b32_sdwa v150,1,v150 dst_sel:DWORD dst_unused:UNUSED_PAD src0_sel:DWORD src1_sel:WORD_1
v_mfma_f32_16x16x32_bf16 v[192:195],v[104:107],v[128:131],v[192:195]
s_waitcnt lgkmcnt(2)
s_cbranch_vccz RA58_%=
s_add_u32 m0,m0,1024
s_add_u32 s69,s69,s53
buffer_load_dwordx4 v2,s[44:47],s69 offen lds
s_add_u32 m0,m0,s50
s_add_u32 s69,s69,s53
buffer_load_dwordx4 v3,s[44:47],s69 offen lds
s_add_u32 m0,m0,1024
s_add_u32 s69,s69,s53
buffer_load_dwordx4 v2,s[44:47],s69 offen lds
s_add_u32 m0,m0,1024
s_add_u32 s69,s69,s53
buffer_load_dwordx4 v3,s[44:47],s69 offen lds
RA58_%=:
ds_read_u16_d16_hi v146,v146
v_lshl_add_u64 v[138:139],v[140:141],0,v[142:143]
v_mfma_f32_16x16x32_bf16 v[200:203],v[108:111],v[128:131],v[200:203]
ds_read_b128 v[112:115],v7 offset:256
v_lshlrev_b32_sdwa v149,1,v149 dst_sel:DWORD dst_unused:UNUSED_PAD src0_sel:DWORD src1_sel:WORD_1
v_mfma_f32_16x16x32_bf16 v[164:167],v[88:91],v[136:139],v[164:167]
ds_read_u16 v145,v145
v_lshlrev_b32_sdwa v151,1,v151 dst_sel:DWORD dst_unused:UNUSED_PAD src0_sel:DWORD src1_sel:WORD_1
v_mfma_f32_16x16x32_bf16 v[172:175],v[92:95],v[136:139],v[172:175]
ds_read_u16_d16_hi v147,v147
ds_read_b64_tr_b16 v[68:69],v5 offset:0
v_lshlrev_b32_sdwa v152,1,v152 dst_sel:DWORD dst_unused:UNUSED_PAD src0_sel:DWORD src1_sel:WORD_1
v_mfma_f32_16x16x32_bf16 v[180:183],v[96:99],v[136:139],v[180:183]
ds_read_b64_tr_b16 v[70:71],v5 offset:2048
ds_read_b128 v[116:119],v7 offset:288
v_lshlrev_b32_sdwa v154,1,v154 dst_sel:DWORD dst_unused:UNUSED_PAD src0_sel:DWORD src1_sel:WORD_1
v_mfma_f32_16x16x32_bf16 v[188:191],v[100:103],v[136:139],v[188:191]
ds_read_u16 v148,v148
ds_read_b64_tr_b16 v[72:73],v4 offset:4096
v_lshlrev_b32_sdwa v153,1,v153 dst_sel:DWORD dst_unused:UNUSED_PAD src0_sel:DWORD src1_sel:WORD_1
v_mfma_f32_16x16x32_bf16 v[196:199],v[104:107],v[136:139],v[196:199]
ds_read_u16_d16_hi v150,v150
v_lshlrev_b32_sdwa v155,1,v155 dst_sel:DWORD dst_unused:UNUSED_PAD src0_sel:DWORD src1_sel:WORD_1
v_mfma_f32_16x16x32_bf16 v[204:207],v[108:111],v[136:139],v[204:207]
s_waitcnt lgkmcnt(8)
ds_read_b64_tr_b16 v[74:75],v4 offset:6144
ds_read_b64_tr_b16 v[76:77],v5 offset:4096
v_mfma_f32_16x16x32_bf16 v[128:131],v[64:67],v[16:19],v[112:115]
ds_read_u16 v149,v149
v_lshlrev_b32_sdwa v156,1,v156 dst_sel:DWORD dst_unused:UNUSED_PAD src0_sel:DWORD src1_sel:WORD_1
v_lshlrev_b32_sdwa v158,1,v158 dst_sel:DWORD dst_unused:UNUSED_PAD src0_sel:DWORD src1_sel:WORD_1
v_mfma_f32_16x16x32_bf16 v[136:139],v[64:67],v[20:23],v[112:115]
s_waitcnt lgkmcnt(6)
ds_read_b64_tr_b16 v[78:79],v5 offset:6144
ds_read_b64_tr_b16 v[80:81],v4 offset:8192
v_lshlrev_b32_sdwa v157,1,v157 dst_sel:DWORD dst_unused:UNUSED_PAD src0_sel:DWORD src1_sel:WORD_1
v_mfma_f32_16x16x32_bf16 v[140:143],v[68:71],v[20:23],v[116:119]
ds_read_u16_d16_hi v151,v151
v_lshlrev_b32_sdwa v159,1,v159 dst_sel:DWORD dst_unused:UNUSED_PAD src0_sel:DWORD src1_sel:WORD_1
v_mfma_f32_16x16x32_bf16 v[132:135],v[68:71],v[16:19],v[116:119]
s_waitcnt lgkmcnt(5)
ds_read_b64_tr_b16 v[82:83],v4 offset:10240
ds_read_u16 v152,v152
v_mfma_f32_16x16x32_bf16 v[128:131],v[72:75],v[24:27],v[128:131]
ds_read_b64_tr_b16 v[84:85],v5 offset:8192
ds_read_b64_tr_b16 v[86:87],v5 offset:10240
v_mfma_f32_16x16x32_bf16 v[136:139],v[72:75],v[28:31],v[136:139]
s_waitcnt lgkmcnt(6)
ds_read_u16_d16_hi v154,v154
v_mfma_f32_16x16x32_bf16 v[140:143],v[76:79],v[28:31],v[140:143]
ds_read_u16 v153,v153
v_mfma_f32_16x16x32_bf16 v[132:135],v[76:79],v[24:27],v[132:135]
s_waitcnt lgkmcnt(5)
ds_read_u16_d16_hi v155,v155
v_lshl_add_u64 v[144:145],v[144:145],0,v[146:147]
v_mfma_f32_16x16x32_bf16 v[128:131],v[80:83],v[32:35],v[128:131]
v_lshl_add_u64 v[146:147],v[148:149],0,v[150:151]
v_mfma_f32_16x16x32_bf16 v[136:139],v[80:83],v[36:39],v[136:139]
s_waitcnt lgkmcnt(3)
ds_read_u16 v156,v156
v_mfma_f32_16x16x32_bf16 v[140:143],v[84:87],v[36:39],v[140:143]
ds_read_u16_d16_hi v158,v158
v_mfma_f32_16x16x32_bf16 v[132:135],v[84:87],v[32:35],v[132:135]
ds_read_u16 v157,v157
v_lshlrev_b32_sdwa v128,1,v128 dst_sel:DWORD dst_unused:UNUSED_PAD src0_sel:DWORD src1_sel:WORD_1
v_lshlrev_b32_sdwa v130,1,v130 dst_sel:DWORD dst_unused:UNUSED_PAD src0_sel:DWORD src1_sel:WORD_1
v_mfma_f32_16x16x32_bf16 v[208:211],v[88:91],v[144:147],v[208:211]
v_lshlrev_b32_sdwa v129,1,v129 dst_sel:DWORD dst_unused:UNUSED_PAD src0_sel:DWORD src1_sel:WORD_1
v_lshlrev_b32_sdwa v131,1,v131 dst_sel:DWORD dst_unused:UNUSED_PAD src0_sel:DWORD src1_sel:WORD_1
v_mfma_f32_16x16x32_bf16 v[216:219],v[92:95],v[144:147],v[216:219]
ds_read_u16_d16_hi v159,v159
v_mfma_f32_16x16x32_bf16 v[224:227],v[96:99],v[144:147],v[224:227]
v_lshlrev_b32_sdwa v132,1,v132 dst_sel:DWORD dst_unused:UNUSED_PAD src0_sel:DWORD src1_sel:WORD_1
v_mfma_f32_16x16x32_bf16 v[232:235],v[100:103],v[144:147],v[232:235]
s_waitcnt lgkmcnt(4)
ds_read_u16 v128,v128
v_lshl_add_u64 v[152:153],v[152:153],0,v[154:155]
v_lshlrev_b32_sdwa v134,1,v134 dst_sel:DWORD dst_unused:UNUSED_PAD src0_sel:DWORD src1_sel:WORD_1
v_mfma_f32_16x16x32_bf16 v[240:243],v[104:107],v[144:147],v[240:243]
s_waitcnt lgkmcnt(1)
ds_read_u16_d16_hi v130,v130
v_lshl_add_u64 v[154:155],v[156:157],0,v[158:159]
v_mfma_f32_16x16x32_bf16 v[248:251],v[108:111],v[144:147],v[248:251]
ds_read_u16 v129,v129
v_lshlrev_b32_sdwa v133,1,v133 dst_sel:DWORD dst_unused:UNUSED_PAD src0_sel:DWORD src1_sel:WORD_1
v_mfma_f32_16x16x32_bf16 v[212:215],v[88:91],v[152:155],v[212:215]
v_lshlrev_b32_sdwa v135,1,v135 dst_sel:DWORD dst_unused:UNUSED_PAD src0_sel:DWORD src1_sel:WORD_1
v_mfma_f32_16x16x32_bf16 v[220:223],v[92:95],v[152:155],v[220:223]
ds_read_u16_d16_hi v131,v131
v_lshlrev_b32_sdwa v136,1,v136 dst_sel:DWORD dst_unused:UNUSED_PAD src0_sel:DWORD src1_sel:WORD_1
v_mfma_f32_16x16x32_bf16 v[228:231],v[96:99],v[152:155],v[228:231]
ds_read_b64_tr_b16 v[88:89],v6 offset:6160
ds_read_b64_tr_b16 v[90:91],v6 offset:7696
v_lshlrev_b32_sdwa v138,1,v138 dst_sel:DWORD dst_unused:UNUSED_PAD src0_sel:DWORD src1_sel:WORD_1
v_mfma_f32_16x16x32_bf16 v[236:239],v[100:103],v[152:155],v[236:239]
ds_read_u16 v132,v132
v_lshlrev_b32_sdwa v137,1,v137 dst_sel:DWORD dst_unused:UNUSED_PAD src0_sel:DWORD src1_sel:WORD_1
v_mfma_f32_16x16x32_bf16 v[244:247],v[104:107],v[152:155],v[244:247]
ds_read_u16_d16_hi v134,v134
v_lshlrev_b32_sdwa v139,1,v139 dst_sel:DWORD dst_unused:UNUSED_PAD src0_sel:DWORD src1_sel:WORD_1
v_mfma_f32_16x16x32_bf16 v[252:255],v[108:111],v[152:155],v[252:255]
ds_read_b64_tr_b16 v[92:93],v6 offset:6168
ds_read_b64_tr_b16 v[94:95],v6 offset:7704
v_lshlrev_b32_sdwa v140,1,v140 dst_sel:DWORD dst_unused:UNUSED_PAD src0_sel:DWORD src1_sel:WORD_1
v_mfma_f32_16x16x32_bf16 v[144:147],v[64:67],v[40:43],v[112:115]
ds_read_u16 v133,v133
v_lshlrev_b32_sdwa v142,1,v142 dst_sel:DWORD dst_unused:UNUSED_PAD src0_sel:DWORD src1_sel:WORD_1
v_mfma_f32_16x16x32_bf16 v[152:155],v[64:67],v[44:47],v[112:115]
ds_read_b64_tr_b16 v[96:97],v6 offset:6224
ds_read_b64_tr_b16 v[98:99],v6 offset:7760
v_lshlrev_b32_sdwa v141,1,v141 dst_sel:DWORD dst_unused:UNUSED_PAD src0_sel:DWORD src1_sel:WORD_1
v_lshlrev_b32_sdwa v143,1,v143 dst_sel:DWORD dst_unused:UNUSED_PAD src0_sel:DWORD src1_sel:WORD_1
v_mfma_f32_16x16x32_bf16 v[156:159],v[68:71],v[44:47],v[116:119]
ds_read_u16_d16_hi v135,v135
v_mfma_f32_16x16x32_bf16 v[148:151],v[68:71],v[40:43],v[116:119]
ds_read_u16 v136,v136
v_mfma_f32_16x16x32_bf16 v[144:147],v[72:75],v[48:51],v[144:147]
ds_read_u16_d16_hi v138,v138
v_mfma_f32_16x16x32_bf16 v[152:155],v[72:75],v[52:55],v[152:155]
ds_read_b64_tr_b16 v[100:101],v6 offset:6232
ds_read_b64_tr_b16 v[102:103],v6 offset:7768
v_mfma_f32_16x16x32_bf16 v[156:159],v[76:79],v[52:55],v[156:159]
ds_read_u16 v137,v137
v_mfma_f32_16x16x32_bf16 v[148:151],v[76:79],v[48:51],v[148:151]
s_waitcnt lgkmcnt(5)
ds_read_u16_d16_hi v139,v139
v_lshl_add_u64 v[128:129],v[128:129],0,v[130:131]
v_lshl_add_u64 v[130:131],v[132:133],0,v[134:135]
v_mfma_f32_16x16x32_bf16 v[144:147],v[80:83],v[56:59],v[144:147]
ds_read_b64_tr_b16 v[104:105],v6 offset:6288
ds_read_b64_tr_b16 v[106:107],v6 offset:7824
v_mfma_f32_16x16x32_bf16 v[152:155],v[80:83],v[60:63],v[152:155]
ds_read_u16 v140,v140
v_mfma_f32_16x16x32_bf16 v[156:159],v[84:87],v[60:63],v[156:159]
ds_read_u16_d16_hi v142,v142
v_mfma_f32_16x16x32_bf16 v[148:151],v[84:87],v[56:59],v[148:151]
ds_read_b64_tr_b16 v[108:109],v6 offset:6296
ds_read_b64_tr_b16 v[110:111],v6 offset:7832
v_lshlrev_b32_sdwa v144,1,v144 dst_sel:DWORD dst_unused:UNUSED_PAD src0_sel:DWORD src1_sel:WORD_1
v_lshlrev_b32_sdwa v146,1,v146 dst_sel:DWORD dst_unused:UNUSED_PAD src0_sel:DWORD src1_sel:WORD_1
v_mfma_f32_16x16x32_bf16 v[160:163],v[88:91],v[128:131],v[160:163]
ds_read_b64_tr_b16 v[64:65],v4 offset:512
ds_read_u16 v141,v141
v_lshlrev_b32_sdwa v145,1,v145 dst_sel:DWORD dst_unused:UNUSED_PAD src0_sel:DWORD src1_sel:WORD_1
v_mfma_f32_16x16x32_bf16 v[168:171],v[92:95],v[128:131],v[168:171]
ds_read_u16_d16_hi v143,v143
v_lshlrev_b32_sdwa v147,1,v147 dst_sel:DWORD dst_unused:UNUSED_PAD src0_sel:DWORD src1_sel:WORD_1
s_nop 0
s_mov_b32 s86,0xbe801d5a
v_mfma_f32_16x16x32_bf16 v[176:179],v[96:99],v[128:131],v[176:179]
s_waitcnt lgkmcnt(11)
ds_read_b64_tr_b16 v[66:67],v4 offset:2560
v_lshlrev_b32_sdwa v148,1,v148 dst_sel:DWORD dst_unused:UNUSED_PAD src0_sel:DWORD src1_sel:WORD_1
v_mfma_f32_16x16x32_bf16 v[184:187],v[100:103],v[128:131],v[184:187]
s_waitcnt lgkmcnt(8)
ds_read_u16 v144,v144
v_lshl_add_u64 v[136:137],v[136:137],0,v[138:139]
v_lshlrev_b32_sdwa v150,1,v150 dst_sel:DWORD dst_unused:UNUSED_PAD src0_sel:DWORD src1_sel:WORD_1
v_mfma_f32_16x16x32_bf16 v[192:195],v[104:107],v[128:131],v[192:195]
s_waitcnt lgkmcnt(2)
ds_read_u16_d16_hi v146,v146
v_lshl_add_u64 v[138:139],v[140:141],0,v[142:143]
v_mfma_f32_16x16x32_bf16 v[200:203],v[108:111],v[128:131],v[200:203]
ds_read_b128 v[112:115],v7 offset:384
v_lshlrev_b32_sdwa v149,1,v149 dst_sel:DWORD dst_unused:UNUSED_PAD src0_sel:DWORD src1_sel:WORD_1
v_mfma_f32_16x16x32_bf16 v[164:167],v[88:91],v[136:139],v[164:167]
ds_read_u16 v145,v145
v_lshlrev_b32_sdwa v151,1,v151 dst_sel:DWORD dst_unused:UNUSED_PAD src0_sel:DWORD src1_sel:WORD_1
v_mfma_f32_16x16x32_bf16 v[172:175],v[92:95],v[136:139],v[172:175]
ds_read_u16_d16_hi v147,v147
ds_read_b64_tr_b16 v[68:69],v5 offset:512
v_lshlrev_b32_sdwa v152,1,v152 dst_sel:DWORD dst_unused:UNUSED_PAD src0_sel:DWORD src1_sel:WORD_1
v_mfma_f32_16x16x32_bf16 v[180:183],v[96:99],v[136:139],v[180:183]
ds_read_b64_tr_b16 v[70:71],v5 offset:2560
ds_read_b128 v[116:119],v7 offset:416
v_lshlrev_b32_sdwa v154,1,v154 dst_sel:DWORD dst_unused:UNUSED_PAD src0_sel:DWORD src1_sel:WORD_1
v_mfma_f32_16x16x32_bf16 v[188:191],v[100:103],v[136:139],v[188:191]
ds_read_u16 v148,v148
ds_read_b64_tr_b16 v[72:73],v4 offset:4608
v_lshlrev_b32_sdwa v153,1,v153 dst_sel:DWORD dst_unused:UNUSED_PAD src0_sel:DWORD src1_sel:WORD_1
v_mfma_f32_16x16x32_bf16 v[196:199],v[104:107],v[136:139],v[196:199]
ds_read_u16_d16_hi v150,v150
v_lshlrev_b32_sdwa v155,1,v155 dst_sel:DWORD dst_unused:UNUSED_PAD src0_sel:DWORD src1_sel:WORD_1
v_mfma_f32_16x16x32_bf16 v[204:207],v[108:111],v[136:139],v[204:207]
s_waitcnt lgkmcnt(8)
ds_read_b64_tr_b16 v[74:75],v4 offset:6656
ds_read_b64_tr_b16 v[76:77],v5 offset:4608
v_mfma_f32_16x16x32_bf16 v[128:131],v[64:67],v[16:19],v[112:115]
ds_read_u16 v149,v149
v_lshlrev_b32_sdwa v156,1,v156 dst_sel:DWORD dst_unused:UNUSED_PAD src0_sel:DWORD src1_sel:WORD_1
v_lshlrev_b32_sdwa v158,1,v158 dst_sel:DWORD dst_unused:UNUSED_PAD src0_sel:DWORD src1_sel:WORD_1
v_mfma_f32_16x16x32_bf16 v[136:139],v[64:67],v[20:23],v[112:115]
s_waitcnt lgkmcnt(6)
ds_read_b64_tr_b16 v[78:79],v5 offset:6656
ds_read_b64_tr_b16 v[80:81],v4 offset:8704
v_lshlrev_b32_sdwa v157,1,v157 dst_sel:DWORD dst_unused:UNUSED_PAD src0_sel:DWORD src1_sel:WORD_1
v_mfma_f32_16x16x32_bf16 v[140:143],v[68:71],v[20:23],v[116:119]
ds_read_u16_d16_hi v151,v151
v_lshlrev_b32_sdwa v159,1,v159 dst_sel:DWORD dst_unused:UNUSED_PAD src0_sel:DWORD src1_sel:WORD_1
v_mfma_f32_16x16x32_bf16 v[132:135],v[68:71],v[16:19],v[116:119]
s_waitcnt lgkmcnt(5)
ds_read_b64_tr_b16 v[82:83],v4 offset:10752
ds_read_u16 v152,v152
v_mfma_f32_16x16x32_bf16 v[128:131],v[72:75],v[24:27],v[128:131]
ds_read_b64_tr_b16 v[84:85],v5 offset:8704
ds_read_b64_tr_b16 v[86:87],v5 offset:10752
v_mfma_f32_16x16x32_bf16 v[136:139],v[72:75],v[28:31],v[136:139]
s_waitcnt lgkmcnt(6)
ds_read_u16_d16_hi v154,v154
v_mfma_f32_16x16x32_bf16 v[140:143],v[76:79],v[28:31],v[140:143]
ds_read_u16 v153,v153
v_mfma_f32_16x16x32_bf16 v[132:135],v[76:79],v[24:27],v[132:135]
s_waitcnt lgkmcnt(5)
ds_read_u16_d16_hi v155,v155
v_lshl_add_u64 v[144:145],v[144:145],0,v[146:147]
v_mfma_f32_16x16x32_bf16 v[128:131],v[80:83],v[32:35],v[128:131]
v_lshl_add_u64 v[146:147],v[148:149],0,v[150:151]
v_mfma_f32_16x16x32_bf16 v[136:139],v[80:83],v[36:39],v[136:139]
s_waitcnt lgkmcnt(3)
ds_read_u16 v156,v156
v_mfma_f32_16x16x32_bf16 v[140:143],v[84:87],v[36:39],v[140:143]
ds_read_u16_d16_hi v158,v158
v_mfma_f32_16x16x32_bf16 v[132:135],v[84:87],v[32:35],v[132:135]
ds_read_u16 v157,v157
v_lshlrev_b32_sdwa v128,1,v128 dst_sel:DWORD dst_unused:UNUSED_PAD src0_sel:DWORD src1_sel:WORD_1
v_lshlrev_b32_sdwa v130,1,v130 dst_sel:DWORD dst_unused:UNUSED_PAD src0_sel:DWORD src1_sel:WORD_1
v_mfma_f32_16x16x32_bf16 v[208:211],v[88:91],v[144:147],v[208:211]
s_waitcnt vmcnt(12)
s_barrier
s_setprio 3
v_lshlrev_b32_sdwa v129,1,v129 dst_sel:DWORD dst_unused:UNUSED_PAD src0_sel:DWORD src1_sel:WORD_1
v_lshlrev_b32_sdwa v131,1,v131 dst_sel:DWORD dst_unused:UNUSED_PAD src0_sel:DWORD src1_sel:WORD_1
v_mfma_f32_16x16x32_bf16 v[216:219],v[92:95],v[144:147],v[216:219]
ds_read_u16_d16_hi v159,v159
ds_read_u16 v128,v128
v_fma_mixlo_f16 v120,v140,s99,0
v_fma_mixlo_f16 v122,v141,s99,0
v_mfma_f32_16x16x32_bf16 v[224:227],v[96:99],v[144:147],v[224:227]
s_cbranch_vccz RA59_%=
s_add_u32 m0,s58,0
s_mul_i32 s69,s59,5
s_add_u32 s69,s69,s97
buffer_load_dwordx4 v2,s[44:47],s69 offen lds
s_add_u32 m0,m0,1024
s_add_u32 s69,s69,s53
buffer_load_dwordx4 v3,s[44:47],s69 offen lds
s_add_u32 m0,m0,1024
s_add_u32 s69,s69,s53
buffer_load_dwordx4 v2,s[44:47],s69 offen lds
s_add_u32 m0,m0,s50
s_add_u32 s69,s69,s53
buffer_load_dwordx4 v3,s[44:47],s69 offen lds
RA59_%=:
ds_read_u16_d16_hi v130,v130
v_lshlrev_b32_sdwa v132,1,v132 dst_sel:DWORD dst_unused:UNUSED_PAD src0_sel:DWORD src1_sel:WORD_1
v_lshlrev_b32_sdwa v134,1,v134 dst_sel:DWORD dst_unused:UNUSED_PAD src0_sel:DWORD src1_sel:WORD_1
v_mfma_f32_16x16x32_bf16 v[232:235],v[100:103],v[144:147],v[232:235]
s_waitcnt lgkmcnt(6)
v_lshl_add_u64 v[152:153],v[152:153],0,v[154:155]
v_mfma_f32_16x16x32_bf16 v[240:243],v[104:107],v[144:147],v[240:243]
s_waitcnt lgkmcnt(2)
v_lshl_add_u64 v[154:155],v[156:157],0,v[158:159]
v_fma_mixhi_f16 v120,v142,s99,0
v_mfma_f32_16x16x32_bf16 v[248:251],v[108:111],v[144:147],v[248:251]
ds_read_u16 v129,v129
v_fma_mixhi_f16 v122,v143,s99,0
v_mfma_f32_16x16x32_bf16 v[212:215],v[88:91],v[152:155],v[212:215]
s_cbranch_vccz RA60_%=
s_add_u32 m0,m0,1024
s_add_u32 s69,s69,s53
buffer_load_dwordx4 v2,s[44:47],s69 offen lds
s_add_u32 m0,m0,1024
s_add_u32 s69,s69,s53
buffer_load_dwordx4 v3,s[44:47],s69 offen lds
s_add_u32 m0,m0,s50
s_add_u32 s69,s69,s53
buffer_load_dwordx4 v2,s[44:47],s69 offen lds
s_add_u32 m0,m0,1024
s_add_u32 s69,s69,s53
buffer_load_dwordx4 v3,s[44:47],s69 offen lds
RA60_%=:
v_lshlrev_b32_sdwa v133,1,v133 dst_sel:DWORD dst_unused:UNUSED_PAD src0_sel:DWORD src1_sel:WORD_1
v_pk_fma_f16 v121,v120,v120,s54
v_lshlrev_b32_sdwa v135,1,v135 dst_sel:DWORD dst_unused:UNUSED_PAD src0_sel:DWORD src1_sel:WORD_1
v_mfma_f32_16x16x32_bf16 v[220:223],v[92:95],v[152:155],v[220:223]
ds_read_u16_d16_hi v131,v131
ds_read_u16 v132,v132
v_pk_fma_f16 v123,v122,v122,s54
v_mfma_f32_16x16x32_bf16 v[228:231],v[96:99],v[152:155],v[228:231]
ds_read_b64_tr_b16 v[88:89],v6 offset:12320
v_pk_mul_f16 v121,v120,v121
v_lshlrev_b32_sdwa v136,1,v136 dst_sel:DWORD dst_unused:UNUSED_PAD src0_sel:DWORD src1_sel:WORD_1
v_pk_mul_f16 v123,v122,v123
v_mfma_f32_16x16x32_bf16 v[236:239],v[100:103],v[152:155],v[236:239]
s_cbranch_vccz RA61_%=
s_add_u32 m0,m0,1024
s_add_u32 s69,s69,s53
buffer_load_dwordx4 v2,s[44:47],s69 offen lds
s_add_u32 m0,m0,s50
s_add_u32 s69,s69,s53
buffer_load_dwordx4 v3,s[44:47],s69 offen lds
s_add_u32 m0,m0,1024
s_add_u32 s69,s69,s53
buffer_load_dwordx4 v2,s[44:47],s69 offen lds
s_add_u32 m0,m0,1024
s_add_u32 s69,s69,s53
buffer_load_dwordx4 v3,s[44:47],s69 offen lds
RA61_%=:
ds_read_b64_tr_b16 v[90:91],v6 offset:13856
v_lshlrev_b32_sdwa v138,1,v138 dst_sel:DWORD dst_unused:UNUSED_PAD src0_sel:DWORD src1_sel:WORD_1
v_mfma_f32_16x16x32_bf16 v[244:247],v[104:107],v[152:155],v[244:247]
ds_read_u16_d16_hi v134,v134
v_exp_f16 v120,v121
v_mfma_f32_16x16x32_bf16 v[252:255],v[108:111],v[152:155],v[252:255]
ds_read_b64_tr_b16 v[92:93],v6 offset:12328
v_lshlrev_b32_sdwa v137,1,v137 dst_sel:DWORD dst_unused:UNUSED_PAD src0_sel:DWORD src1_sel:WORD_1
v_mfma_f32_16x16x32_bf16 v[144:147],v[64:67],v[40:43],v[112:115]
ds_read_b64_tr_b16 v[94:95],v6 offset:13864
v_lshlrev_b32_sdwa v139,1,v139 dst_sel:DWORD dst_unused:UNUSED_PAD src0_sel:DWORD src1_sel:WORD_1
v_exp_f16 v122,v123
v_mfma_f32_16x16x32_bf16 v[152:155],v[64:67],v[44:47],v[112:115]
s_cbranch_vccz RA62_%=
s_add_u32 m0,s100,12320
s_mov_b32 s69,s60
buffer_load_dwordx4 v2,s[44:47],s69 offen lds
s_add_u32 m0,m0,1024
s_add_u32 s69,s69,s53
buffer_load_dwordx4 v3,s[44:47],s69 offen lds
s_add_u32 m0,m0,1024
s_add_u32 s69,s69,s53
buffer_load_dwordx4 v2,s[44:47],s69 offen lds
s_add_u32 m0,m0,s50
s_add_u32 s69,s69,s53
buffer_load_dwordx4 v3,s[44:47],s69 offen lds
RA62_%=:
ds_read_u16 v133,v133
v_mfma_f32_16x16x32_bf16 v[156:159],v[68:71],v[44:47],v[116:119]
ds_read_u16_d16_hi v135,v135
v_exp_f16_sdwa v122,v123 dst_sel:WORD_1 dst_unused:UNUSED_PRESERVE src0_sel:WORD_1
v_exp_f16_sdwa v120,v121 dst_sel:WORD_1 dst_unused:UNUSED_PRESERVE src0_sel:WORD_1
v_mfma_f32_16x16x32_bf16 v[148:151],v[68:71],v[40:43],v[116:119]
ds_read_b64_tr_b16 v[96:97],v6 offset:12384
ds_read_b64_tr_b16 v[98:99],v6 offset:13920
v_pk_fma_f16 v120,v120,s55,s55
v_pk_fma_f16 v122,v122,s55,s55
v_mfma_f32_16x16x32_bf16 v[144:147],v[72:75],v[48:51],v[144:147]
v_rcp_f16 v121,v120
v_mfma_f32_16x16x32_bf16 v[152:155],v[72:75],v[52:55],v[152:155]
s_cbranch_vccz RA63_%=
s_add_u32 m0,m0,1024
s_add_u32 s69,s69,s53
buffer_load_dwordx4 v2,s[44:47],s69 offen lds
s_add_u32 m0,m0,1024
s_add_u32 s69,s69,s53
buffer_load_dwordx4 v3,s[44:47],s69 offen lds
s_add_u32 m0,m0,s50
s_add_u32 s69,s69,s51
buffer_load_dwordx4 v2,s[44:47],s69 offen lds
s_add_u32 m0,m0,1024
s_add_u32 s69,s69,s53
buffer_load_dwordx4 v3,s[44:47],s69 offen lds
RA63_%=:
ds_read_b64_tr_b16 v[100:101],v6 offset:12392
v_rcp_f16 v123,v122
v_mfma_f32_16x16x32_bf16 v[156:159],v[76:79],v[52:55],v[156:159]
ds_read_u16 v136,v136
v_rcp_f16_sdwa v121,v120 dst_sel:WORD_1 dst_unused:UNUSED_PRESERVE src0_sel:WORD_1
v_mfma_f32_16x16x32_bf16 v[148:151],v[76:79],v[48:51],v[148:151]
ds_read_b64_tr_b16 v[102:103],v6 offset:13928
ds_read_b64_tr_b16 v[104:105],v6 offset:12448
v_rcp_f16_sdwa v123,v122 dst_sel:WORD_1 dst_unused:UNUSED_PRESERVE src0_sel:WORD_1
v_mfma_f32_16x16x32_bf16 v[144:147],v[80:83],v[56:59],v[144:147]
ds_read_u16_d16_hi v138,v138
v_fma_mix_f32 v142,v142,v121,0 op_sel:[0,1,0] op_sel_hi:[0,1,0]
v_mfma_f32_16x16x32_bf16 v[152:155],v[80:83],v[60:63],v[152:155]
s_waitcnt lgkmcnt(7)
s_cbranch_vccz RA64_%=
s_add_u32 m0,m0,1024
s_add_u32 s69,s69,s53
buffer_load_dwordx4 v2,s[44:47],s69 offen lds
s_add_u32 m0,m0,s50
s_add_u32 s69,s69,s53
buffer_load_dwordx4 v3,s[44:47],s69 offen lds
s_add_u32 m0,m0,1024
s_add_u32 s69,s69,s53
buffer_load_dwordx4 v2,s[44:47],s69 offen lds
s_add_u32 m0,m0,1024
s_add_u32 s69,s69,s53
buffer_load_dwordx4 v3,s[44:47],s69 offen lds
RA64_%=:
ds_read_b64_tr_b16 v[106:107],v6 offset:13984
ds_read_b64_tr_b16 v[108:109],v6 offset:12456
v_fma_mix_f32 v140,v140,v121,0 op_sel_hi:[0,1,0]
v_lshl_add_u64 v[128:129],v[128:129],0,v[130:131]
v_lshl_add_u64 v[130:131],v[132:133],0,v[134:135]
v_mfma_f32_16x16x32_bf16 v[156:159],v[84:87],v[60:63],v[156:159]
ds_read_u16 v137,v137
v_fma_mix_f32 v141,v141,v123,0 op_sel_hi:[0,1,0]
v_fma_mix_f32 v143,v143,v123,0 op_sel:[0,1,0] op_sel_hi:[0,1,0]
v_mfma_f32_16x16x32_bf16 v[148:151],v[84:87],v[56:59],v[148:151]
ds_read_b64_tr_b16 v[110:111],v6 offset:13992
ds_read_b64_tr_b16 v[64:65],v4 offset:12320
v_lshlrev_b32_sdwa v144,1,v144 dst_sel:DWORD dst_unused:UNUSED_PAD src0_sel:DWORD src1_sel:WORD_1
v_lshlrev_b32_sdwa v146,1,v146 dst_sel:DWORD dst_unused:UNUSED_PAD src0_sel:DWORD src1_sel:WORD_1
v_mfma_f32_16x16x32_bf16 v[160:163],v[88:91],v[128:131],v[160:163]
ds_read_u16_d16_hi v139,v139
v_lshlrev_b32_sdwa v145,1,v145 dst_sel:DWORD dst_unused:UNUSED_PAD src0_sel:DWORD src1_sel:WORD_1
v_lshlrev_b32_sdwa v147,1,v147 dst_sel:DWORD dst_unused:UNUSED_PAD src0_sel:DWORD src1_sel:WORD_1
v_mfma_f32_16x16x32_bf16 v[168:171],v[92:95],v[128:131],v[168:171]
s_waitcnt lgkmcnt(11)
ds_read_b64_tr_b16 v[66:67],v4 offset:14368
ds_read_b128 v[112:115],v7 offset:512
v_fma_mixlo_f16 v120,v156,s99,0
v_fma_mixlo_f16 v122,v157,s99,0
v_mfma_f32_16x16x32_bf16 v[176:179],v[96:99],v[128:131],v[176:179]
s_waitcnt lgkmcnt(10)
ds_read_u16 v144,v144
v_lshlrev_b32_sdwa v148,1,v148 dst_sel:DWORD dst_unused:UNUSED_PAD src0_sel:DWORD src1_sel:WORD_1
v_lshlrev_b32_sdwa v150,1,v150 dst_sel:DWORD dst_unused:UNUSED_PAD src0_sel:DWORD src1_sel:WORD_1
v_mfma_f32_16x16x32_bf16 v[184:187],v[100:103],v[128:131],v[184:187]
s_waitcnt lgkmcnt(3)
ds_read_b64_tr_b16 v[68:69],v5 offset:12320
ds_read_b64_tr_b16 v[70:71],v5 offset:14368
v_lshl_add_u64 v[136:137],v[136:137],0,v[138:139]
v_fma_mixhi_f16 v120,v158,s99,0
v_mfma_f32_16x16x32_bf16 v[192:195],v[104:107],v[128:131],v[192:195]
ds_read_u16_d16_hi v146,v146
v_cvt_pk_bf16_f32 v138,v140,v142
v_cvt_pk_bf16_f32 v139,v141,v143
v_mfma_f32_16x16x32_bf16 v[200:203],v[108:111],v[128:131],v[200:203]
ds_read_b128 v[116:119],v7 offset:544
ds_read_b64_tr_b16 v[72:73],v4 offset:16416
v_fma_mixhi_f16 v122,v159,s99,0
v_mfma_f32_16x16x32_bf16 v[164:167],v[88:91],v[136:139],v[164:167]
v_lshlrev_b32_sdwa v149,1,v149 dst_sel:DWORD dst_unused:UNUSED_PAD src0_sel:DWORD src1_sel:WORD_1
v_lshlrev_b32_sdwa v151,1,v151 dst_sel:DWORD dst_unused:UNUSED_PAD src0_sel:DWORD src1_sel:WORD_1
v_mfma_f32_16x16x32_bf16 v[172:175],v[92:95],v[136:139],v[172:175]
ds_read_b64_tr_b16 v[74:75],v4 offset:18464
ds_read_b64_tr_b16 v[76:77],v5 offset:16416
v_pk_fma_f16 v121,v120,v120,s54
v_pk_fma_f16 v123,v122,v122,s54
v_mfma_f32_16x16x32_bf16 v[180:183],v[96:99],v[136:139],v[180:183]
ds_read_u16 v145,v145
v_pk_mul_f16 v121,v120,v121
v_pk_mul_f16 v123,v122,v123
v_mfma_f32_16x16x32_bf16 v[188:191],v[100:103],v[136:139],v[188:191]
ds_read_u16_d16_hi v147,v147
v_lshlrev_b32_sdwa v152,1,v152 dst_sel:DWORD dst_unused:UNUSED_PAD src0_sel:DWORD src1_sel:WORD_1
v_lshlrev_b32_sdwa v154,1,v154 dst_sel:DWORD dst_unused:UNUSED_PAD src0_sel:DWORD src1_sel:WORD_1
v_mfma_f32_16x16x32_bf16 v[196:199],v[104:107],v[136:139],v[196:199]
v_exp_f16 v120,v121
v_mfma_f32_16x16x32_bf16 v[204:207],v[108:111],v[136:139],v[204:207]
s_waitcnt lgkmcnt(10)
ds_read_b64_tr_b16 v[78:79],v5 offset:18464
ds_read_b64_tr_b16 v[80:81],v4 offset:20512
v_exp_f16 v122,v123
v_mfma_f32_16x16x32_bf16 v[128:131],v[64:67],v[16:19],v[112:115]
v_lshlrev_b32_sdwa v153,1,v153 dst_sel:DWORD dst_unused:UNUSED_PAD src0_sel:DWORD src1_sel:WORD_1
v_lshlrev_b32_sdwa v155,1,v155 dst_sel:DWORD dst_unused:UNUSED_PAD src0_sel:DWORD src1_sel:WORD_1
v_mfma_f32_16x16x32_bf16 v[136:139],v[64:67],v[20:23],v[112:115]
s_waitcnt lgkmcnt(7)
ds_read_u16 v148,v148
ds_read_u16_d16_hi v150,v150
v_exp_f16_sdwa v120,v121 dst_sel:WORD_1 dst_unused:UNUSED_PRESERVE src0_sel:WORD_1
v_mfma_f32_16x16x32_bf16 v[140:143],v[68:71],v[20:23],v[116:119]
v_exp_f16_sdwa v122,v123 dst_sel:WORD_1 dst_unused:UNUSED_PRESERVE src0_sel:WORD_1
v_mfma_f32_16x16x32_bf16 v[132:135],v[68:71],v[16:19],v[116:119]
s_waitcnt lgkmcnt(7)
ds_read_b64_tr_b16 v[82:83],v4 offset:22560
v_pk_fma_f16 v120,v120,s55,s55
v_mfma_f32_16x16x32_bf16 v[128:131],v[72:75],v[24:27],v[128:131]
ds_read_b64_tr_b16 v[84:85],v5 offset:20512
v_pk_fma_f16 v122,v122,s55,s55
v_rcp_f16 v121,v120
v_mfma_f32_16x16x32_bf16 v[136:139],v[72:75],v[28:31],v[136:139]
s_waitcnt lgkmcnt(5)
ds_read_b64_tr_b16 v[86:87],v5 offset:22560
ds_read_u16 v149,v149
v_rcp_f16 v123,v122
v_mfma_f32_16x16x32_bf16 v[140:143],v[76:79],v[28:31],v[140:143]
ds_read_u16_d16_hi v151,v151
ds_read_u16 v152,v152
v_rcp_f16_sdwa v121,v120 dst_sel:WORD_1 dst_unused:UNUSED_PRESERVE src0_sel:WORD_1
v_mfma_f32_16x16x32_bf16 v[132:135],v[76:79],v[24:27],v[132:135]
s_waitcnt lgkmcnt(5)
v_rcp_f16_sdwa v123,v122 dst_sel:WORD_1 dst_unused:UNUSED_PRESERVE src0_sel:WORD_1
v_mfma_f32_16x16x32_bf16 v[128:131],v[80:83],v[32:35],v[128:131]
ds_read_u16_d16_hi v154,v154
v_fma_mix_f32 v156,v156,v121,0 op_sel_hi:[0,1,0]
v_fma_mix_f32 v158,v158,v121,0 op_sel:[0,1,0] op_sel_hi:[0,1,0]
v_mfma_f32_16x16x32_bf16 v[136:139],v[80:83],v[36:39],v[136:139]
s_waitcnt lgkmcnt(2)
v_lshl_add_u64 v[144:145],v[144:145],0,v[146:147]
v_lshl_add_u64 v[146:147],v[148:149],0,v[150:151]
v_mfma_f32_16x16x32_bf16 v[140:143],v[84:87],v[36:39],v[140:143]
ds_read_u16 v153,v153
v_fma_mix_f32 v157,v157,v123,0 op_sel_hi:[0,1,0]
v_fma_mix_f32 v159,v159,v123,0 op_sel:[0,1,0] op_sel_hi:[0,1,0]
v_mfma_f32_16x16x32_bf16 v[132:135],v[84:87],v[32:35],v[132:135]
v_lshlrev_b32_sdwa v128,1,v128 dst_sel:DWORD dst_unused:UNUSED_PAD src0_sel:DWORD src1_sel:WORD_1
v_lshlrev_b32_sdwa v130,1,v130 dst_sel:DWORD dst_unused:UNUSED_PAD src0_sel:DWORD src1_sel:WORD_1
v_mfma_f32_16x16x32_bf16 v[208:211],v[88:91],v[144:147],v[208:211]
v_lshlrev_b32_sdwa v129,1,v129 dst_sel:DWORD dst_unused:UNUSED_PAD src0_sel:DWORD src1_sel:WORD_1
v_lshlrev_b32_sdwa v131,1,v131 dst_sel:DWORD dst_unused:UNUSED_PAD src0_sel:DWORD src1_sel:WORD_1
v_mfma_f32_16x16x32_bf16 v[216:219],v[92:95],v[144:147],v[216:219]
ds_read_u16_d16_hi v155,v155
ds_read_u16 v128,v128
v_fma_mixlo_f16 v120,v140,s99,0
v_fma_mixlo_f16 v122,v141,s99,0
v_mfma_f32_16x16x32_bf16 v[224:227],v[96:99],v[144:147],v[224:227]
ds_read_u16_d16_hi v130,v130
v_lshlrev_b32_sdwa v132,1,v132 dst_sel:DWORD dst_unused:UNUSED_PAD src0_sel:DWORD src1_sel:WORD_1
v_lshlrev_b32_sdwa v134,1,v134 dst_sel:DWORD dst_unused:UNUSED_PAD src0_sel:DWORD src1_sel:WORD_1
v_mfma_f32_16x16x32_bf16 v[232:235],v[100:103],v[144:147],v[232:235]
s_waitcnt lgkmcnt(2)
v_lshl_add_u64 v[152:153],v[152:153],0,v[154:155]
v_cvt_pk_bf16_f32 v154,v156,v158
v_mfma_f32_16x16x32_bf16 v[240:243],v[104:107],v[144:147],v[240:243]
v_fma_mixhi_f16 v120,v142,s99,0
v_cvt_pk_bf16_f32 v155,v157,v159
v_mfma_f32_16x16x32_bf16 v[248:251],v[108:111],v[144:147],v[248:251]
s_setprio 2
ds_read_u16 v129,v129
v_fma_mixhi_f16 v122,v143,s99,0
v_mfma_f32_16x16x32_bf16 v[212:215],v[88:91],v[152:155],v[212:215]
v_lshlrev_b32_sdwa v133,1,v133 dst_sel:DWORD dst_unused:UNUSED_PAD src0_sel:DWORD src1_sel:WORD_1
v_pk_fma_f16 v121,v120,v120,s54
v_lshlrev_b32_sdwa v135,1,v135 dst_sel:DWORD dst_unused:UNUSED_PAD src0_sel:DWORD src1_sel:WORD_1
v_mfma_f32_16x16x32_bf16 v[220:223],v[92:95],v[152:155],v[220:223]
ds_read_u16_d16_hi v131,v131
ds_read_u16 v132,v132
v_pk_fma_f16 v123,v122,v122,s54
v_mfma_f32_16x16x32_bf16 v[228:231],v[96:99],v[152:155],v[228:231]
ds_read_b64_tr_b16 v[88:89],v6 offset:18480
v_pk_mul_f16 v121,v120,v121
v_lshlrev_b32_sdwa v136,1,v136 dst_sel:DWORD dst_unused:UNUSED_PAD src0_sel:DWORD src1_sel:WORD_1
v_pk_mul_f16 v123,v122,v123
v_mfma_f32_16x16x32_bf16 v[236:239],v[100:103],v[152:155],v[236:239]
ds_read_b64_tr_b16 v[90:91],v6 offset:20016
v_lshlrev_b32_sdwa v138,1,v138 dst_sel:DWORD dst_unused:UNUSED_PAD src0_sel:DWORD src1_sel:WORD_1
v_mfma_f32_16x16x32_bf16 v[244:247],v[104:107],v[152:155],v[244:247]
ds_read_u16_d16_hi v134,v134
v_exp_f16 v120,v121
v_mfma_f32_16x16x32_bf16 v[252:255],v[108:111],v[152:155],v[252:255]
ds_read_b64_tr_b16 v[92:93],v6 offset:18488
v_lshlrev_b32_sdwa v137,1,v137 dst_sel:DWORD dst_unused:UNUSED_PAD src0_sel:DWORD src1_sel:WORD_1
v_mfma_f32_16x16x32_bf16 v[144:147],v[64:67],v[40:43],v[112:115]
ds_read_b64_tr_b16 v[94:95],v6 offset:20024
v_lshlrev_b32_sdwa v139,1,v139 dst_sel:DWORD dst_unused:UNUSED_PAD src0_sel:DWORD src1_sel:WORD_1
v_exp_f16 v122,v123
v_mfma_f32_16x16x32_bf16 v[152:155],v[64:67],v[44:47],v[112:115]
ds_read_u16 v133,v133
v_mfma_f32_16x16x32_bf16 v[156:159],v[68:71],v[44:47],v[116:119]
ds_read_u16_d16_hi v135,v135
v_exp_f16_sdwa v122,v123 dst_sel:WORD_1 dst_unused:UNUSED_PRESERVE src0_sel:WORD_1
v_exp_f16_sdwa v120,v121 dst_sel:WORD_1 dst_unused:UNUSED_PRESERVE src0_sel:WORD_1
v_mfma_f32_16x16x32_bf16 v[148:151],v[68:71],v[40:43],v[116:119]
ds_read_b64_tr_b16 v[96:97],v6 offset:18544
ds_read_b64_tr_b16 v[98:99],v6 offset:20080
v_pk_fma_f16 v120,v120,s55,s55
v_pk_fma_f16 v122,v122,s55,s55
v_mfma_f32_16x16x32_bf16 v[144:147],v[72:75],v[48:51],v[144:147]
v_rcp_f16 v121,v120
v_mfma_f32_16x16x32_bf16 v[152:155],v[72:75],v[52:55],v[152:155]
ds_read_b64_tr_b16 v[100:101],v6 offset:18552
v_rcp_f16 v123,v122
v_mfma_f32_16x16x32_bf16 v[156:159],v[76:79],v[52:55],v[156:159]
ds_read_u16 v136,v136
v_rcp_f16_sdwa v121,v120 dst_sel:WORD_1 dst_unused:UNUSED_PRESERVE src0_sel:WORD_1
v_mfma_f32_16x16x32_bf16 v[148:151],v[76:79],v[48:51],v[148:151]
ds_read_b64_tr_b16 v[102:103],v6 offset:20088
ds_read_b64_tr_b16 v[104:105],v6 offset:18608
v_rcp_f16_sdwa v123,v122 dst_sel:WORD_1 dst_unused:UNUSED_PRESERVE src0_sel:WORD_1
v_mfma_f32_16x16x32_bf16 v[144:147],v[80:83],v[56:59],v[144:147]
ds_read_u16_d16_hi v138,v138
v_fma_mix_f32 v142,v142,v121,0 op_sel:[0,1,0] op_sel_hi:[0,1,0]
v_mfma_f32_16x16x32_bf16 v[152:155],v[80:83],v[60:63],v[152:155]
s_waitcnt lgkmcnt(7)
ds_read_b64_tr_b16 v[106:107],v6 offset:20144
ds_read_b64_tr_b16 v[108:109],v6 offset:18616
v_fma_mix_f32 v140,v140,v121,0 op_sel_hi:[0,1,0]
v_lshl_add_u64 v[128:129],v[128:129],0,v[130:131]
v_lshl_add_u64 v[130:131],v[132:133],0,v[134:135]
v_mfma_f32_16x16x32_bf16 v[156:159],v[84:87],v[60:63],v[156:159]
ds_read_u16 v137,v137
v_fma_mix_f32 v141,v141,v123,0 op_sel_hi:[0,1,0]
v_fma_mix_f32 v143,v143,v123,0 op_sel:[0,1,0] op_sel_hi:[0,1,0]
v_mfma_f32_16x16x32_bf16 v[148:151],v[84:87],v[56:59],v[148:151]
ds_read_b64_tr_b16 v[110:111],v6 offset:20152
ds_read_b64_tr_b16 v[64:65],v4 offset:12832
v_lshlrev_b32_sdwa v144,1,v144 dst_sel:DWORD dst_unused:UNUSED_PAD src0_sel:DWORD src1_sel:WORD_1
v_lshlrev_b32_sdwa v146,1,v146 dst_sel:DWORD dst_unused:UNUSED_PAD src0_sel:DWORD src1_sel:WORD_1
v_mfma_f32_16x16x32_bf16 v[160:163],v[88:91],v[128:131],v[160:163]
ds_read_u16_d16_hi v139,v139
v_lshlrev_b32_sdwa v145,1,v145 dst_sel:DWORD dst_unused:UNUSED_PAD src0_sel:DWORD src1_sel:WORD_1
v_lshlrev_b32_sdwa v147,1,v147 dst_sel:DWORD dst_unused:UNUSED_PAD src0_sel:DWORD src1_sel:WORD_1
v_mfma_f32_16x16x32_bf16 v[168:171],v[92:95],v[128:131],v[168:171]
s_waitcnt lgkmcnt(11)
ds_read_b64_tr_b16 v[66:67],v4 offset:14880
ds_read_b128 v[112:115],v7 offset:640
v_fma_mixlo_f16 v120,v156,s99,0
v_fma_mixlo_f16 v122,v157,s99,0
v_mfma_f32_16x16x32_bf16 v[176:179],v[96:99],v[128:131],v[176:179]
s_waitcnt lgkmcnt(10)
ds_read_u16 v144,v144
v_lshlrev_b32_sdwa v148,1,v148 dst_sel:DWORD dst_unused:UNUSED_PAD src0_sel:DWORD src1_sel:WORD_1
v_lshlrev_b32_sdwa v150,1,v150 dst_sel:DWORD dst_unused:UNUSED_PAD src0_sel:DWORD src1_sel:WORD_1
v_mfma_f32_16x16x32_bf16 v[184:187],v[100:103],v[128:131],v[184:187]
s_waitcnt lgkmcnt(3)
ds_read_b64_tr_b16 v[68:69],v5 offset:12832
ds_read_b64_tr_b16 v[70:71],v5 offset:14880
v_lshl_add_u64 v[136:137],v[136:137],0,v[138:139]
v_fma_mixhi_f16 v120,v158,s99,0
v_mfma_f32_16x16x32_bf16 v[192:195],v[104:107],v[128:131],v[192:195]
ds_read_u16_d16_hi v146,v146
v_cvt_pk_bf16_f32 v138,v140,v142
v_cvt_pk_bf16_f32 v139,v141,v143
v_mfma_f32_16x16x32_bf16 v[200:203],v[108:111],v[128:131],v[200:203]
ds_read_b128 v[116:119],v7 offset:672
ds_read_b64_tr_b16 v[72:73],v4 offset:16928
v_fma_mixhi_f16 v122,v159,s99,0
v_mfma_f32_16x16x32_bf16 v[164:167],v[88:91],v[136:139],v[164:167]
v_lshlrev_b32_sdwa v149,1,v149 dst_sel:DWORD dst_unused:UNUSED_PAD src0_sel:DWORD src1_sel:WORD_1
v_lshlrev_b32_sdwa v151,1,v151 dst_sel:DWORD dst_unused:UNUSED_PAD src0_sel:DWORD src1_sel:WORD_1
v_mfma_f32_16x16x32_bf16 v[172:175],v[92:95],v[136:139],v[172:175]
s_setprio 1
ds_read_b64_tr_b16 v[74:75],v4 offset:18976
ds_read_b64_tr_b16 v[76:77],v5 offset:16928
v_pk_fma_f16 v121,v120,v120,s54
v_pk_fma_f16 v123,v122,v122,s54
v_mfma_f32_16x16x32_bf16 v[180:183],v[96:99],v[136:139],v[180:183]
ds_read_u16 v145,v145
v_pk_mul_f16 v121,v120,v121
v_pk_mul_f16 v123,v122,v123
v_mfma_f32_16x16x32_bf16 v[188:191],v[100:103],v[136:139],v[188:191]
ds_read_u16_d16_hi v147,v147
v_lshlrev_b32_sdwa v152,1,v152 dst_sel:DWORD dst_unused:UNUSED_PAD src0_sel:DWORD src1_sel:WORD_1
v_lshlrev_b32_sdwa v154,1,v154 dst_sel:DWORD dst_unused:UNUSED_PAD src0_sel:DWORD src1_sel:WORD_1
v_mfma_f32_16x16x32_bf16 v[196:199],v[104:107],v[136:139],v[196:199]
v_exp_f16 v120,v121
v_mfma_f32_16x16x32_bf16 v[204:207],v[108:111],v[136:139],v[204:207]
s_waitcnt lgkmcnt(10)
ds_read_b64_tr_b16 v[78:79],v5 offset:18976
ds_read_b64_tr_b16 v[80:81],v4 offset:21024
v_exp_f16 v122,v123
v_mfma_f32_16x16x32_bf16 v[128:131],v[64:67],v[16:19],v[112:115]
v_lshlrev_b32_sdwa v153,1,v153 dst_sel:DWORD dst_unused:UNUSED_PAD src0_sel:DWORD src1_sel:WORD_1
v_lshlrev_b32_sdwa v155,1,v155 dst_sel:DWORD dst_unused:UNUSED_PAD src0_sel:DWORD src1_sel:WORD_1
v_mfma_f32_16x16x32_bf16 v[136:139],v[64:67],v[20:23],v[112:115]
s_waitcnt lgkmcnt(7)
ds_read_u16 v148,v148
ds_read_u16_d16_hi v150,v150
v_exp_f16_sdwa v120,v121 dst_sel:WORD_1 dst_unused:UNUSED_PRESERVE src0_sel:WORD_1
v_mfma_f32_16x16x32_bf16 v[140:143],v[68:71],v[20:23],v[116:119]
v_exp_f16_sdwa v122,v123 dst_sel:WORD_1 dst_unused:UNUSED_PRESERVE src0_sel:WORD_1
v_mfma_f32_16x16x32_bf16 v[132:135],v[68:71],v[16:19],v[116:119]
s_waitcnt lgkmcnt(7)
ds_read_b64_tr_b16 v[82:83],v4 offset:23072
v_pk_fma_f16 v120,v120,s55,s55
v_mfma_f32_16x16x32_bf16 v[128:131],v[72:75],v[24:27],v[128:131]
ds_read_b64_tr_b16 v[84:85],v5 offset:21024
v_pk_fma_f16 v122,v122,s55,s55
v_rcp_f16 v121,v120
v_mfma_f32_16x16x32_bf16 v[136:139],v[72:75],v[28:31],v[136:139]
s_waitcnt lgkmcnt(5)
ds_read_b64_tr_b16 v[86:87],v5 offset:23072
ds_read_u16 v149,v149
v_rcp_f16 v123,v122
v_mfma_f32_16x16x32_bf16 v[140:143],v[76:79],v[28:31],v[140:143]
ds_read_u16_d16_hi v151,v151
ds_read_u16 v152,v152
v_rcp_f16_sdwa v121,v120 dst_sel:WORD_1 dst_unused:UNUSED_PRESERVE src0_sel:WORD_1
v_mfma_f32_16x16x32_bf16 v[132:135],v[76:79],v[24:27],v[132:135]
s_waitcnt lgkmcnt(5)
v_rcp_f16_sdwa v123,v122 dst_sel:WORD_1 dst_unused:UNUSED_PRESERVE src0_sel:WORD_1
v_mfma_f32_16x16x32_bf16 v[128:131],v[80:83],v[32:35],v[128:131]
ds_read_u16_d16_hi v154,v154
v_fma_mix_f32 v156,v156,v121,0 op_sel_hi:[0,1,0]
v_fma_mix_f32 v158,v158,v121,0 op_sel:[0,1,0] op_sel_hi:[0,1,0]
v_mfma_f32_16x16x32_bf16 v[136:139],v[80:83],v[36:39],v[136:139]
s_waitcnt lgkmcnt(2)
s_setprio 0
v_lshl_add_u64 v[144:145],v[144:145],0,v[146:147]
v_lshl_add_u64 v[146:147],v[148:149],0,v[150:151]
v_mfma_f32_16x16x32_bf16 v[140:143],v[84:87],v[36:39],v[140:143]
ds_read_u16 v153,v153
v_fma_mix_f32 v157,v157,v123,0 op_sel_hi:[0,1,0]
v_fma_mix_f32 v159,v159,v123,0 op_sel:[0,1,0] op_sel_hi:[0,1,0]
v_mfma_f32_16x16x32_bf16 v[132:135],v[84:87],v[32:35],v[132:135]
v_lshlrev_b32_sdwa v128,1,v128 dst_sel:DWORD dst_unused:UNUSED_PAD src0_sel:DWORD src1_sel:WORD_1
v_lshlrev_b32_sdwa v130,1,v130 dst_sel:DWORD dst_unused:UNUSED_PAD src0_sel:DWORD src1_sel:WORD_1
v_mfma_f32_16x16x32_bf16 v[208:211],v[88:91],v[144:147],v[208:211]
s_waitcnt vmcnt(12)
s_barrier
s_setprio 3
v_lshlrev_b32_sdwa v129,1,v129 dst_sel:DWORD dst_unused:UNUSED_PAD src0_sel:DWORD src1_sel:WORD_1
v_lshlrev_b32_sdwa v131,1,v131 dst_sel:DWORD dst_unused:UNUSED_PAD src0_sel:DWORD src1_sel:WORD_1
v_mfma_f32_16x16x32_bf16 v[216:219],v[92:95],v[144:147],v[216:219]
ds_read_u16_d16_hi v155,v155
ds_read_u16 v128,v128
v_fma_mixlo_f16 v120,v140,s99,0
v_fma_mixlo_f16 v122,v141,s99,0
v_mfma_f32_16x16x32_bf16 v[224:227],v[96:99],v[144:147],v[224:227]
s_cbranch_vccz RA65_%=
s_add_u32 m0,s58,12320
s_mul_i32 s69,s59,7
s_add_u32 s69,s69,s97
buffer_load_dwordx4 v2,s[44:47],s69 offen lds
s_add_u32 m0,m0,1024
s_add_u32 s69,s69,s53
buffer_load_dwordx4 v3,s[44:47],s69 offen lds
s_add_u32 m0,m0,1024
s_add_u32 s69,s69,s53
buffer_load_dwordx4 v2,s[44:47],s69 offen lds
s_add_u32 m0,m0,s50
s_add_u32 s69,s69,s53
buffer_load_dwordx4 v3,s[44:47],s69 offen lds
RA65_%=:
s_mov_b32 s86,0xbe801d5a
ds_read_u16_d16_hi v130,v130
v_lshlrev_b32_sdwa v132,1,v132 dst_sel:DWORD dst_unused:UNUSED_PAD src0_sel:DWORD src1_sel:WORD_1
v_lshlrev_b32_sdwa v134,1,v134 dst_sel:DWORD dst_unused:UNUSED_PAD src0_sel:DWORD src1_sel:WORD_1
v_mfma_f32_16x16x32_bf16 v[232:235],v[100:103],v[144:147],v[232:235]
s_waitcnt lgkmcnt(2)
v_lshl_add_u64 v[152:153],v[152:153],0,v[154:155]
v_cvt_pk_bf16_f32 v154,v156,v158
v_mfma_f32_16x16x32_bf16 v[240:243],v[104:107],v[144:147],v[240:243]
v_fma_mixhi_f16 v120,v142,s99,0
v_cvt_pk_bf16_f32 v155,v157,v159
v_mfma_f32_16x16x32_bf16 v[248:251],v[108:111],v[144:147],v[248:251]
s_cbranch_vccz RA66_%=
s_add_u32 m0,m0,1024
s_add_u32 s69,s69,s53
buffer_load_dwordx4 v2,s[44:47],s69 offen lds
s_add_u32 m0,m0,1024
s_add_u32 s69,s69,s53
buffer_load_dwordx4 v3,s[44:47],s69 offen lds
s_add_u32 m0,m0,s50
s_add_u32 s69,s69,s53
buffer_load_dwordx4 v2,s[44:47],s69 offen lds
s_add_u32 m0,m0,1024
s_add_u32 s69,s69,s53
buffer_load_dwordx4 v3,s[44:47],s69 offen lds
RA66_%=:
ds_read_u16 v129,v129
v_fma_mixhi_f16 v122,v143,s99,0
v_mfma_f32_16x16x32_bf16 v[212:215],v[88:91],v[152:155],v[212:215]
v_lshlrev_b32_sdwa v133,1,v133 dst_sel:DWORD dst_unused:UNUSED_PAD src0_sel:DWORD src1_sel:WORD_1
v_pk_fma_f16 v121,v120,v120,s54
v_lshlrev_b32_sdwa v135,1,v135 dst_sel:DWORD dst_unused:UNUSED_PAD src0_sel:DWORD src1_sel:WORD_1
v_mfma_f32_16x16x32_bf16 v[220:223],v[92:95],v[152:155],v[220:223]
ds_read_u16_d16_hi v131,v131
ds_read_u16 v132,v132
v_pk_fma_f16 v123,v122,v122,s54
v_mfma_f32_16x16x32_bf16 v[228:231],v[96:99],v[152:155],v[228:231]
ds_read_b64_tr_b16 v[88:89],v6 offset:0
v_pk_mul_f16 v121,v120,v121
v_lshlrev_b32_sdwa v136,1,v136 dst_sel:DWORD dst_unused:UNUSED_PAD src0_sel:DWORD src1_sel:WORD_1
v_pk_mul_f16 v123,v122,v123
v_mfma_f32_16x16x32_bf16 v[236:239],v[100:103],v[152:155],v[236:239]
s_cbranch_vccz RA67_%=
s_add_u32 m0,m0,1024
s_add_u32 s69,s69,s53
buffer_load_dwordx4 v2,s[44:47],s69 offen lds
s_add_u32 m0,m0,s50
s_add_u32 s69,s69,s53
buffer_load_dwordx4 v3,s[44:47],s69 offen lds
s_add_u32 m0,m0,1024
s_add_u32 s69,s69,s53
buffer_load_dwordx4 v2,s[44:47],s69 offen lds
s_add_u32 m0,m0,1024
s_add_u32 s69,s69,s53
buffer_load_dwordx4 v3,s[44:47],s69 offen lds
RA67_%=:
ds_read_b64_tr_b16 v[90:91],v6 offset:1536
v_lshlrev_b32_sdwa v138,1,v138 dst_sel:DWORD dst_unused:UNUSED_PAD src0_sel:DWORD src1_sel:WORD_1
v_mfma_f32_16x16x32_bf16 v[244:247],v[104:107],v[152:155],v[244:247]
ds_read_u16_d16_hi v134,v134
v_exp_f16 v120,v121
v_mfma_f32_16x16x32_bf16 v[252:255],v[108:111],v[152:155],v[252:255]
ds_read_b64_tr_b16 v[92:93],v6 offset:8
v_lshlrev_b32_sdwa v137,1,v137 dst_sel:DWORD dst_unused:UNUSED_PAD src0_sel:DWORD src1_sel:WORD_1
v_mfma_f32_16x16x32_bf16 v[144:147],v[64:67],v[40:43],v[112:115]
ds_read_b64_tr_b16 v[94:95],v6 offset:1544
v_lshlrev_b32_sdwa v139,1,v139 dst_sel:DWORD dst_unused:UNUSED_PAD src0_sel:DWORD src1_sel:WORD_1
v_exp_f16 v122,v123
v_mfma_f32_16x16x32_bf16 v[152:155],v[64:67],v[44:47],v[112:115]
s_cbranch_vccz RA68_%=
s_add_u32 m0,s100,24640
s_mul_i32 s69,s59,1
s_add_u32 s69,s69,s97
buffer_load_dwordx4 v2,s[44:47],s69 offen lds
s_add_u32 m0,m0,1024
s_add_u32 s69,s69,s53
buffer_load_dwordx4 v3,s[44:47],s69 offen lds
s_add_u32 m0,m0,1024
s_add_u32 s69,s69,s53
buffer_load_dwordx4 v2,s[44:47],s69 offen lds
s_add_u32 m0,m0,s50
s_add_u32 s69,s69,s53
buffer_load_dwordx4 v3,s[44:47],s69 offen lds
RA68_%=:
ds_read_u16 v133,v133
v_mfma_f32_16x16x32_bf16 v[156:159],v[68:71],v[44:47],v[116:119]
ds_read_u16_d16_hi v135,v135
v_exp_f16_sdwa v122,v123 dst_sel:WORD_1 dst_unused:UNUSED_PRESERVE src0_sel:WORD_1
v_exp_f16_sdwa v120,v121 dst_sel:WORD_1 dst_unused:UNUSED_PRESERVE src0_sel:WORD_1
v_mfma_f32_16x16x32_bf16 v[148:151],v[68:71],v[40:43],v[116:119]
ds_read_b64_tr_b16 v[96:97],v6 offset:64
ds_read_b64_tr_b16 v[98:99],v6 offset:1600
v_pk_fma_f16 v120,v120,s55,s55
v_pk_fma_f16 v122,v122,s55,s55
v_mfma_f32_16x16x32_bf16 v[144:147],v[72:75],v[48:51],v[144:147]
v_rcp_f16 v121,v120
v_mfma_f32_16x16x32_bf16 v[152:155],v[72:75],v[52:55],v[152:155]
s_cbranch_vccz RA69_%=
s_add_u32 m0,m0,1024
s_add_u32 s69,s69,s53
buffer_load_dwordx4 v2,s[44:47],s69 offen lds
s_add_u32 m0,m0,1024
s_add_u32 s69,s69,s53
buffer_load_dwordx4 v3,s[44:47],s69 offen lds
s_add_u32 m0,m0,s50
s_add_u32 s69,s69,s53
buffer_load_dwordx4 v2,s[44:47],s69 offen lds
s_add_u32 m0,m0,1024
s_add_u32 s69,s69,s53
buffer_load_dwordx4 v3,s[44:47],s69 offen lds
RA69_%=:
ds_read_b64_tr_b16 v[100:101],v6 offset:72
v_rcp_f16 v123,v122
v_mfma_f32_16x16x32_bf16 v[156:159],v[76:79],v[52:55],v[156:159]
ds_read_u16 v136,v136
v_rcp_f16_sdwa v121,v120 dst_sel:WORD_1 dst_unused:UNUSED_PRESERVE src0_sel:WORD_1
v_mfma_f32_16x16x32_bf16 v[148:151],v[76:79],v[48:51],v[148:151]
ds_read_b64_tr_b16 v[102:103],v6 offset:1608
ds_read_b64_tr_b16 v[104:105],v6 offset:128
v_rcp_f16_sdwa v123,v122 dst_sel:WORD_1 dst_unused:UNUSED_PRESERVE src0_sel:WORD_1
v_mfma_f32_16x16x32_bf16 v[144:147],v[80:83],v[56:59],v[144:147]
ds_read_u16_d16_hi v138,v138
v_fma_mix_f32 v142,v142,v121,0 op_sel:[0,1,0] op_sel_hi:[0,1,0]
v_mfma_f32_16x16x32_bf16 v[152:155],v[80:83],v[60:63],v[152:155]
s_waitcnt lgkmcnt(7)
s_cbranch_vccz RA70_%=
s_add_u32 m0,m0,1024
s_add_u32 s69,s69,s53
buffer_load_dwordx4 v2,s[44:47],s69 offen lds
s_add_u32 m0,m0,s50
s_add_u32 s69,s69,s53
buffer_load_dwordx4 v3,s[44:47],s69 offen lds
s_add_u32 m0,m0,1024
s_add_u32 s69,s69,s53
buffer_load_dwordx4 v2,s[44:47],s69 offen lds
s_add_u32 m0,m0,1024
s_add_u32 s69,s69,s53
buffer_load_dwordx4 v3,s[44:47],s69 offen lds
RA70_%=:
ds_read_b64_tr_b16 v[106:107],v6 offset:1664
ds_read_b64_tr_b16 v[108:109],v6 offset:136
v_fma_mix_f32 v140,v140,v121,0 op_sel_hi:[0,1,0]
v_lshl_add_u64 v[128:129],v[128:129],0,v[130:131]
v_lshl_add_u64 v[130:131],v[132:133],0,v[134:135]
v_mfma_f32_16x16x32_bf16 v[156:159],v[84:87],v[60:63],v[156:159]
ds_read_u16 v137,v137
v_fma_mix_f32 v141,v141,v123,0 op_sel_hi:[0,1,0]
v_fma_mix_f32 v143,v143,v123,0 op_sel:[0,1,0] op_sel_hi:[0,1,0]
v_mfma_f32_16x16x32_bf16 v[148:151],v[84:87],v[56:59],v[148:151]
ds_read_b64_tr_b16 v[110:111],v6 offset:1672
ds_read_b64_tr_b16 v[64:65],v4 offset:0
v_lshlrev_b32_sdwa v144,1,v144 dst_sel:DWORD dst_unused:UNUSED_PAD src0_sel:DWORD src1_sel:WORD_1
v_lshlrev_b32_sdwa v146,1,v146 dst_sel:DWORD dst_unused:UNUSED_PAD src0_sel:DWORD src1_sel:WORD_1
v_mfma_f32_16x16x32_bf16 v[160:163],v[88:91],v[128:131],v[160:163]
ds_read_u16_d16_hi v139,v139
v_lshlrev_b32_sdwa v145,1,v145 dst_sel:DWORD dst_unused:UNUSED_PAD src0_sel:DWORD src1_sel:WORD_1
v_lshlrev_b32_sdwa v147,1,v147 dst_sel:DWORD dst_unused:UNUSED_PAD src0_sel:DWORD src1_sel:WORD_1
v_mfma_f32_16x16x32_bf16 v[168:171],v[92:95],v[128:131],v[168:171]
s_waitcnt lgkmcnt(11)
ds_read_b64_tr_b16 v[66:67],v4 offset:2048
ds_read_b128 v[112:115],v7 offset:768
v_fma_mixlo_f16 v120,v156,s99,0
v_fma_mixlo_f16 v122,v157,s99,0
v_mfma_f32_16x16x32_bf16 v[176:179],v[96:99],v[128:131],v[176:179]
s_waitcnt lgkmcnt(10)
ds_read_u16 v144,v144
v_lshlrev_b32_sdwa v148,1,v148 dst_sel:DWORD dst_unused:UNUSED_PAD src0_sel:DWORD src1_sel:WORD_1
v_lshlrev_b32_sdwa v150,1,v150 dst_sel:DWORD dst_unused:UNUSED_PAD src0_sel:DWORD src1_sel:WORD_1
v_mfma_f32_16x16x32_bf16 v[184:187],v[100:103],v[128:131],v[184:187]
s_waitcnt lgkmcnt(3)
ds_read_b64_tr_b16 v[68:69],v5 offset:0
ds_read_b64_tr_b16 v[70:71],v5 offset:2048
v_lshl_add_u64 v[136:137],v[136:137],0,v[138:139]
v_fma_mixhi_f16 v120,v158,s99,0
v_mfma_f32_16x16x32_bf16 v[192:195],v[104:107],v[128:131],v[192:195]
ds_read_u16_d16_hi v146,v146
v_cvt_pk_bf16_f32 v138,v140,v142
v_cvt_pk_bf16_f32 v139,v141,v143
v_mfma_f32_16x16x32_bf16 v[200:203],v[108:111],v[128:131],v[200:203]
ds_read_b128 v[116:119],v7 offset:800
ds_read_b64_tr_b16 v[72:73],v4 offset:4096
v_fma_mixhi_f16 v122,v159,s99,0
v_mfma_f32_16x16x32_bf16 v[164:167],v[88:91],v[136:139],v[164:167]
v_lshlrev_b32_sdwa v149,1,v149 dst_sel:DWORD dst_unused:UNUSED_PAD src0_sel:DWORD src1_sel:WORD_1
v_lshlrev_b32_sdwa v151,1,v151 dst_sel:DWORD dst_unused:UNUSED_PAD src0_sel:DWORD src1_sel:WORD_1
v_mfma_f32_16x16x32_bf16 v[172:175],v[92:95],v[136:139],v[172:175]
ds_read_b64_tr_b16 v[74:75],v4 offset:6144
ds_read_b64_tr_b16 v[76:77],v5 offset:4096
v_pk_fma_f16 v121,v120,v120,s54
v_pk_fma_f16 v123,v122,v122,s54
v_mfma_f32_16x16x32_bf16 v[180:183],v[96:99],v[136:139],v[180:183]
ds_read_u16 v145,v145
v_pk_mul_f16 v121,v120,v121
v_pk_mul_f16 v123,v122,v123
v_mfma_f32_16x16x32_bf16 v[188:191],v[100:103],v[136:139],v[188:191]
ds_read_u16_d16_hi v147,v147
v_lshlrev_b32_sdwa v152,1,v152 dst_sel:DWORD dst_unused:UNUSED_PAD src0_sel:DWORD src1_sel:WORD_1
v_lshlrev_b32_sdwa v154,1,v154 dst_sel:DWORD dst_unused:UNUSED_PAD src0_sel:DWORD src1_sel:WORD_1
v_mfma_f32_16x16x32_bf16 v[196:199],v[104:107],v[136:139],v[196:199]
v_exp_f16 v120,v121
v_mfma_f32_16x16x32_bf16 v[204:207],v[108:111],v[136:139],v[204:207]
s_waitcnt lgkmcnt(10)
ds_read_b64_tr_b16 v[78:79],v5 offset:6144
ds_read_b64_tr_b16 v[80:81],v4 offset:8192
v_exp_f16 v122,v123
v_mfma_f32_16x16x32_bf16 v[128:131],v[64:67],v[16:19],v[112:115]
v_lshlrev_b32_sdwa v153,1,v153 dst_sel:DWORD dst_unused:UNUSED_PAD src0_sel:DWORD src1_sel:WORD_1
v_lshlrev_b32_sdwa v155,1,v155 dst_sel:DWORD dst_unused:UNUSED_PAD src0_sel:DWORD src1_sel:WORD_1
v_mfma_f32_16x16x32_bf16 v[136:139],v[64:67],v[20:23],v[112:115]
s_waitcnt lgkmcnt(7)
ds_read_u16 v148,v148
ds_read_u16_d16_hi v150,v150
v_exp_f16_sdwa v120,v121 dst_sel:WORD_1 dst_unused:UNUSED_PRESERVE src0_sel:WORD_1
v_mfma_f32_16x16x32_bf16 v[140:143],v[68:71],v[20:23],v[116:119]
v_exp_f16_sdwa v122,v123 dst_sel:WORD_1 dst_unused:UNUSED_PRESERVE src0_sel:WORD_1
v_mfma_f32_16x16x32_bf16 v[132:135],v[68:71],v[16:19],v[116:119]
s_waitcnt lgkmcnt(7)
ds_read_b64_tr_b16 v[82:83],v4 offset:10240
v_pk_fma_f16 v120,v120,s55,s55
v_mfma_f32_16x16x32_bf16 v[128:131],v[72:75],v[24:27],v[128:131]
ds_read_b64_tr_b16 v[84:85],v5 offset:8192
v_pk_fma_f16 v122,v122,s55,s55
v_rcp_f16 v121,v120
v_mfma_f32_16x16x32_bf16 v[136:139],v[72:75],v[28:31],v[136:139]
s_waitcnt lgkmcnt(5)
ds_read_b64_tr_b16 v[86:87],v5 offset:10240
ds_read_u16 v149,v149
v_rcp_f16 v123,v122
v_mfma_f32_16x16x32_bf16 v[140:143],v[76:79],v[28:31],v[140:143]
ds_read_u16_d16_hi v151,v151
ds_read_u16 v152,v152
v_rcp_f16_sdwa v121,v120 dst_sel:WORD_1 dst_unused:UNUSED_PRESERVE src0_sel:WORD_1
v_mfma_f32_16x16x32_bf16 v[132:135],v[76:79],v[24:27],v[132:135]
s_waitcnt lgkmcnt(5)
v_rcp_f16_sdwa v123,v122 dst_sel:WORD_1 dst_unused:UNUSED_PRESERVE src0_sel:WORD_1
v_mfma_f32_16x16x32_bf16 v[128:131],v[80:83],v[32:35],v[128:131]
ds_read_u16_d16_hi v154,v154
v_fma_mix_f32 v156,v156,v121,0 op_sel_hi:[0,1,0]
v_fma_mix_f32 v158,v158,v121,0 op_sel:[0,1,0] op_sel_hi:[0,1,0]
v_mfma_f32_16x16x32_bf16 v[136:139],v[80:83],v[36:39],v[136:139]
s_waitcnt lgkmcnt(2)
v_lshl_add_u64 v[144:145],v[144:145],0,v[146:147]
v_lshl_add_u64 v[146:147],v[148:149],0,v[150:151]
v_mfma_f32_16x16x32_bf16 v[140:143],v[84:87],v[36:39],v[140:143]
ds_read_u16 v153,v153
v_fma_mix_f32 v157,v157,v123,0 op_sel_hi:[0,1,0]
v_fma_mix_f32 v159,v159,v123,0 op_sel:[0,1,0] op_sel_hi:[0,1,0]
v_mfma_f32_16x16x32_bf16 v[132:135],v[84:87],v[32:35],v[132:135]
v_lshlrev_b32_sdwa v128,1,v128 dst_sel:DWORD dst_unused:UNUSED_PAD src0_sel:DWORD src1_sel:WORD_1
v_lshlrev_b32_sdwa v130,1,v130 dst_sel:DWORD dst_unused:UNUSED_PAD src0_sel:DWORD src1_sel:WORD_1
v_mfma_f32_16x16x32_bf16 v[208:211],v[88:91],v[144:147],v[208:211]
v_lshlrev_b32_sdwa v129,1,v129 dst_sel:DWORD dst_unused:UNUSED_PAD src0_sel:DWORD src1_sel:WORD_1
v_lshlrev_b32_sdwa v131,1,v131 dst_sel:DWORD dst_unused:UNUSED_PAD src0_sel:DWORD src1_sel:WORD_1
v_mfma_f32_16x16x32_bf16 v[216:219],v[92:95],v[144:147],v[216:219]
ds_read_u16_d16_hi v155,v155
ds_read_u16 v128,v128
v_fma_mixlo_f16 v120,v140,s99,0
v_fma_mixlo_f16 v122,v141,s99,0
v_mfma_f32_16x16x32_bf16 v[224:227],v[96:99],v[144:147],v[224:227]
s_setprio 2
ds_read_u16_d16_hi v130,v130
v_lshlrev_b32_sdwa v132,1,v132 dst_sel:DWORD dst_unused:UNUSED_PAD src0_sel:DWORD src1_sel:WORD_1
v_lshlrev_b32_sdwa v134,1,v134 dst_sel:DWORD dst_unused:UNUSED_PAD src0_sel:DWORD src1_sel:WORD_1
v_mfma_f32_16x16x32_bf16 v[232:235],v[100:103],v[144:147],v[232:235]
s_waitcnt lgkmcnt(2)
v_lshl_add_u64 v[152:153],v[152:153],0,v[154:155]
v_cvt_pk_bf16_f32 v154,v156,v158
v_mfma_f32_16x16x32_bf16 v[240:243],v[104:107],v[144:147],v[240:243]
v_fma_mixhi_f16 v120,v142,s99,0
v_cvt_pk_bf16_f32 v155,v157,v159
v_mfma_f32_16x16x32_bf16 v[248:251],v[108:111],v[144:147],v[248:251]
ds_read_u16 v129,v129
v_fma_mixhi_f16 v122,v143,s99,0
v_mfma_f32_16x16x32_bf16 v[212:215],v[88:91],v[152:155],v[212:215]
v_lshlrev_b32_sdwa v133,1,v133 dst_sel:DWORD dst_unused:UNUSED_PAD src0_sel:DWORD src1_sel:WORD_1
v_pk_fma_f16 v121,v120,v120,s54
v_lshlrev_b32_sdwa v135,1,v135 dst_sel:DWORD dst_unused:UNUSED_PAD src0_sel:DWORD src1_sel:WORD_1
v_mfma_f32_16x16x32_bf16 v[220:223],v[92:95],v[152:155],v[220:223]
ds_read_u16_d16_hi v131,v131
ds_read_u16 v132,v132
v_pk_fma_f16 v123,v122,v122,s54
v_mfma_f32_16x16x32_bf16 v[228:231],v[96:99],v[152:155],v[228:231]
ds_read_b64_tr_b16 v[88:89],v6 offset:6160
v_pk_mul_f16 v121,v120,v121
v_lshlrev_b32_sdwa v136,1,v136 dst_sel:DWORD dst_unused:UNUSED_PAD src0_sel:DWORD src1_sel:WORD_1
v_pk_mul_f16 v123,v122,v123
v_mfma_f32_16x16x32_bf16 v[236:239],v[100:103],v[152:155],v[236:239]
ds_read_b64_tr_b16 v[90:91],v6 offset:7696
v_lshlrev_b32_sdwa v138,1,v138 dst_sel:DWORD dst_unused:UNUSED_PAD src0_sel:DWORD src1_sel:WORD_1
v_mfma_f32_16x16x32_bf16 v[244:247],v[104:107],v[152:155],v[244:247]
ds_read_u16_d16_hi v134,v134
v_exp_f16 v120,v121
v_mfma_f32_16x16x32_bf16 v[252:255],v[108:111],v[152:155],v[252:255]
ds_read_b64_tr_b16 v[92:93],v6 offset:6168
v_lshlrev_b32_sdwa v137,1,v137 dst_sel:DWORD dst_unused:UNUSED_PAD src0_sel:DWORD src1_sel:WORD_1
v_mfma_f32_16x16x32_bf16 v[144:147],v[64:67],v[40:43],v[112:115]
ds_read_b64_tr_b16 v[94:95],v6 offset:7704
v_lshlrev_b32_sdwa v139,1,v139 dst_sel:DWORD dst_unused:UNUSED_PAD src0_sel:DWORD src1_sel:WORD_1
v_exp_f16 v122,v123
v_mfma_f32_16x16x32_bf16 v[152:155],v[64:67],v[44:47],v[112:115]
ds_read_u16 v133,v133
v_mfma_f32_16x16x32_bf16 v[156:159],v[68:71],v[44:47],v[116:119]
ds_read_u16_d16_hi v135,v135
v_exp_f16_sdwa v122,v123 dst_sel:WORD_1 dst_unused:UNUSED_PRESERVE src0_sel:WORD_1
v_exp_f16_sdwa v120,v121 dst_sel:WORD_1 dst_unused:UNUSED_PRESERVE src0_sel:WORD_1
v_mfma_f32_16x16x32_bf16 v[148:151],v[68:71],v[40:43],v[116:119]
ds_read_b64_tr_b16 v[96:97],v6 offset:6224
ds_read_b64_tr_b16 v[98:99],v6 offset:7760
v_pk_fma_f16 v120,v120,s55,s55
v_pk_fma_f16 v122,v122,s55,s55
v_mfma_f32_16x16x32_bf16 v[144:147],v[72:75],v[48:51],v[144:147]
v_rcp_f16 v121,v120
v_mfma_f32_16x16x32_bf16 v[152:155],v[72:75],v[52:55],v[152:155]
ds_read_b64_tr_b16 v[100:101],v6 offset:6232
v_rcp_f16 v123,v122
v_mfma_f32_16x16x32_bf16 v[156:159],v[76:79],v[52:55],v[156:159]
ds_read_u16 v136,v136
v_rcp_f16_sdwa v121,v120 dst_sel:WORD_1 dst_unused:UNUSED_PRESERVE src0_sel:WORD_1
v_mfma_f32_16x16x32_bf16 v[148:151],v[76:79],v[48:51],v[148:151]
ds_read_b64_tr_b16 v[102:103],v6 offset:7768
ds_read_b64_tr_b16 v[104:105],v6 offset:6288
v_rcp_f16_sdwa v123,v122 dst_sel:WORD_1 dst_unused:UNUSED_PRESERVE src0_sel:WORD_1
v_mfma_f32_16x16x32_bf16 v[144:147],v[80:83],v[56:59],v[144:147]
ds_read_u16_d16_hi v138,v138
v_fma_mix_f32 v142,v142,v121,0 op_sel:[0,1,0] op_sel_hi:[0,1,0]
v_mfma_f32_16x16x32_bf16 v[152:155],v[80:83],v[60:63],v[152:155]
s_waitcnt lgkmcnt(7)
ds_read_b64_tr_b16 v[106:107],v6 offset:7824
ds_read_b64_tr_b16 v[108:109],v6 offset:6296
v_fma_mix_f32 v140,v140,v121,0 op_sel_hi:[0,1,0]
v_lshl_add_u64 v[128:129],v[128:129],0,v[130:131]
v_lshl_add_u64 v[130:131],v[132:133],0,v[134:135]
v_mfma_f32_16x16x32_bf16 v[156:159],v[84:87],v[60:63],v[156:159]
ds_read_u16 v137,v137
v_fma_mix_f32 v141,v141,v123,0 op_sel_hi:[0,1,0]
v_fma_mix_f32 v143,v143,v123,0 op_sel:[0,1,0] op_sel_hi:[0,1,0]
v_mfma_f32_16x16x32_bf16 v[148:151],v[84:87],v[56:59],v[148:151]
ds_read_b64_tr_b16 v[110:111],v6 offset:7832
ds_read_b128 v[64:67],v7 offset:1536
v_lshlrev_b32_sdwa v144,1,v144 dst_sel:DWORD dst_unused:UNUSED_PAD src0_sel:DWORD src1_sel:WORD_1
v_lshlrev_b32_sdwa v146,1,v146 dst_sel:DWORD dst_unused:UNUSED_PAD src0_sel:DWORD src1_sel:WORD_1
v_mfma_f32_16x16x32_bf16 v[160:163],v[88:91],v[128:131],v[160:163]
ds_read_u16_d16_hi v139,v139
ds_read_b128 v[68:71],v7 offset:1568
v_lshlrev_b32_sdwa v145,1,v145 dst_sel:DWORD dst_unused:UNUSED_PAD src0_sel:DWORD src1_sel:WORD_1
v_lshlrev_b32_sdwa v147,1,v147 dst_sel:DWORD dst_unused:UNUSED_PAD src0_sel:DWORD src1_sel:WORD_1
v_fma_mixlo_f16 v120,v156,s99,0
v_fma_mixlo_f16 v122,v157,s99,0
v_mfma_f32_16x16x32_bf16 v[168:171],v[92:95],v[128:131],v[168:171]
s_waitcnt lgkmcnt(12)
ds_read_u16 v144,v144
ds_read_b128 v[72:75],v7 offset:1664
v_lshlrev_b32_sdwa v148,1,v148 dst_sel:DWORD dst_unused:UNUSED_PAD src0_sel:DWORD src1_sel:WORD_1
v_lshlrev_b32_sdwa v150,1,v150 dst_sel:DWORD dst_unused:UNUSED_PAD src0_sel:DWORD src1_sel:WORD_1
s_waitcnt lgkmcnt(3)
v_lshl_add_u64 v[136:137],v[136:137],0,v[138:139]
v_fma_mixhi_f16 v120,v158,s99,0
v_mfma_f32_16x16x32_bf16 v[176:179],v[96:99],v[128:131],v[176:179]
s_setprio 1
ds_read_u16_d16_hi v146,v146
ds_read_b128 v[76:79],v7 offset:1696
v_cvt_pk_bf16_f32 v138,v140,v142
v_cvt_pk_bf16_f32 v139,v141,v143
v_fma_mixhi_f16 v122,v159,s99,0
v_lshlrev_b32_sdwa v149,1,v149 dst_sel:DWORD dst_unused:UNUSED_PAD src0_sel:DWORD src1_sel:WORD_1
v_mfma_f32_16x16x32_bf16 v[184:187],v[100:103],v[128:131],v[184:187]
ds_read_u16 v145,v145
ds_read_b128 v[80:83],v7 offset:1792
v_lshlrev_b32_sdwa v151,1,v151 dst_sel:DWORD dst_unused:UNUSED_PAD src0_sel:DWORD src1_sel:WORD_1
v_pk_fma_f16 v121,v120,v120,s54
v_pk_fma_f16 v123,v122,v122,s54
v_pk_mul_f16 v121,v120,v121
v_mfma_f32_16x16x32_bf16 v[192:195],v[104:107],v[128:131],v[192:195]
ds_read_u16_d16_hi v147,v147
ds_read_b128 v[84:87],v7 offset:1824
v_pk_mul_f16 v123,v122,v123
v_lshlrev_b32_sdwa v152,1,v152 dst_sel:DWORD dst_unused:UNUSED_PAD src0_sel:DWORD src1_sel:WORD_1
v_lshlrev_b32_sdwa v154,1,v154 dst_sel:DWORD dst_unused:UNUSED_PAD src0_sel:DWORD src1_sel:WORD_1
v_exp_f16 v120,v121
v_mfma_f32_16x16x32_bf16 v[200:203],v[108:111],v[128:131],v[200:203]
ds_read_u16 v148,v148
v_exp_f16 v122,v123
v_lshlrev_b32_sdwa v153,1,v153 dst_sel:DWORD dst_unused:UNUSED_PAD src0_sel:DWORD src1_sel:WORD_1
v_mfma_f32_16x16x32_bf16 v[164:167],v[88:91],v[136:139],v[164:167]
ds_read_u16_d16_hi v150,v150
v_lshlrev_b32_sdwa v155,1,v155 dst_sel:DWORD dst_unused:UNUSED_PAD src0_sel:DWORD src1_sel:WORD_1
v_exp_f16_sdwa v120,v121 dst_sel:WORD_1 dst_unused:UNUSED_PRESERVE src0_sel:WORD_1
v_exp_f16_sdwa v122,v123 dst_sel:WORD_1 dst_unused:UNUSED_PRESERVE src0_sel:WORD_1
v_mfma_f32_16x16x32_bf16 v[172:175],v[92:95],v[136:139],v[172:175]
ds_read_u16 v149,v149
v_pk_fma_f16 v120,v120,s55,s55
v_pk_fma_f16 v122,v122,s55,s55
v_rcp_f16 v121,v120
v_mfma_f32_16x16x32_bf16 v[180:183],v[96:99],v[136:139],v[180:183]
ds_read_u16_d16_hi v151,v151
v_rcp_f16 v123,v122
v_rcp_f16_sdwa v121,v120 dst_sel:WORD_1 dst_unused:UNUSED_PRESERVE src0_sel:WORD_1
v_mfma_f32_16x16x32_bf16 v[188:191],v[100:103],v[136:139],v[188:191]
ds_read_u16 v152,v152
v_rcp_f16_sdwa v123,v122 dst_sel:WORD_1 dst_unused:UNUSED_PRESERVE src0_sel:WORD_1
v_fma_mix_f32 v156,v156,v121,0 op_sel_hi:[0,1,0]
v_mfma_f32_16x16x32_bf16 v[196:199],v[104:107],v[136:139],v[196:199]
s_setprio 0
ds_read_u16_d16_hi v154,v154
v_fma_mix_f32 v158,v158,v121,0 op_sel:[0,1,0] op_sel_hi:[0,1,0]
s_waitcnt lgkmcnt(7)
v_lshl_add_u64 v[144:145],v[144:145],0,v[146:147]
s_waitcnt lgkmcnt(2)
v_lshl_add_u64 v[146:147],v[148:149],0,v[150:151]
v_fma_mix_f32 v157,v157,v123,0 op_sel_hi:[0,1,0]
v_mfma_f32_16x16x32_bf16 v[204:207],v[108:111],v[136:139],v[204:207]
ds_read_u16 v153,v153
v_fma_mix_f32 v159,v159,v123,0 op_sel:[0,1,0] op_sel_hi:[0,1,0]
ds_read_u16_d16_hi v155,v155
s_waitcnt lgkmcnt(0)
v_lshl_add_u64 v[152:153],v[152:153],0,v[154:155]
v_cvt_pk_bf16_f32 v154,v156,v158
v_cvt_pk_bf16_f32 v155,v157,v159
v_mfma_f32_16x16x32_bf16 v[208:211],v[88:91],v[144:147],v[208:211]
s_waitcnt vmcnt(12)
s_barrier
s_setprio 0
v_xor_b32 v15,64,v10
v_mul_f32 v160,v160,v64
v_mul_f32 v161,v161,v65
v_mul_f32 v162,v162,v66
v_mul_f32 v163,v163,v67
v_mfma_f32_16x16x32_bf16 v[216:219],v[92:95],v[144:147],v[216:219]
v_mul_f32 v168,v168,v68
v_mul_f32 v169,v169,v69
v_mul_f32 v170,v170,v70
v_mul_f32 v171,v171,v71
v_cvt_pk_bf16_f32 v16,v160,v161
v_mfma_f32_16x16x32_bf16 v[224:227],v[96:99],v[144:147],v[224:227]
v_cvt_pk_bf16_f32 v17,v162,v163
v_cvt_pk_bf16_f32 v18,v168,v169
v_cvt_pk_bf16_f32 v19,v170,v171
v_mul_f32_dpp v176,v176,v72 quad_perm:[1,0,3,2] row_mask:0xf bank_mask:0xf
v_mfma_f32_16x16x32_bf16 v[232:235],v[100:103],v[144:147],v[232:235]
v_mul_f32_dpp v177,v177,v73 quad_perm:[1,0,3,2] row_mask:0xf bank_mask:0xf
v_mul_f32_dpp v178,v178,v74 quad_perm:[1,0,3,2] row_mask:0xf bank_mask:0xf
v_mul_f32_dpp v179,v179,v75 quad_perm:[1,0,3,2] row_mask:0xf bank_mask:0xf
v_mul_f32_dpp v184,v184,v76 quad_perm:[1,0,3,2] row_mask:0xf bank_mask:0xf
v_mul_f32_dpp v185,v185,v77 quad_perm:[1,0,3,2] row_mask:0xf bank_mask:0xf
v_mfma_f32_16x16x32_bf16 v[240:243],v[104:107],v[144:147],v[240:243]
v_mul_f32_dpp v186,v186,v78 quad_perm:[1,0,3,2] row_mask:0xf bank_mask:0xf
v_mul_f32_dpp v187,v187,v79 quad_perm:[1,0,3,2] row_mask:0xf bank_mask:0xf
v_cvt_pk_bf16_f32 v24,v176,v177
v_cvt_pk_bf16_f32 v25,v178,v179
v_mfma_f32_16x16x32_bf16 v[248:251],v[108:111],v[144:147],v[248:251]
v_cvt_pk_bf16_f32 v26,v184,v185
v_cvt_pk_bf16_f32 v27,v186,v187
s_mov_b64 exec,s[12:13]
v_swap_b32 v16,v24
v_swap_b32 v17,v25
v_swap_b32 v18,v26
v_swap_b32 v19,v27
s_mov_b64 exec,-1
v_mfma_f32_16x16x32_bf16 v[212:215],v[88:91],v[152:155],v[212:215]
s_add_u32 s65,s61,3145728
buffer_store_dwordx4 v[16:19],v10,s[40:43],s65 offen offset:0 sc1
v_mfma_f32_16x16x32_bf16 v[220:223],v[92:95],v[152:155],v[220:223]
v_mul_f32 v192,v192,v80
v_mul_f32 v193,v193,v81
v_mul_f32 v194,v194,v82
v_mul_f32 v195,v195,v83
v_mfma_f32_16x16x32_bf16 v[228:231],v[96:99],v[152:155],v[228:231]
v_mul_f32 v200,v200,v84
v_mul_f32 v201,v201,v85
v_mul_f32 v202,v202,v86
v_mul_f32 v203,v203,v87
v_mfma_f32_16x16x32_bf16 v[236:239],v[100:103],v[152:155],v[236:239]
v_cvt_pk_bf16_f32 v32,v192,v193
v_cvt_pk_bf16_f32 v33,v194,v195
v_cvt_pk_bf16_f32 v34,v200,v201
v_cvt_pk_bf16_f32 v35,v202,v203
v_mul_f32_dpp v164,v164,v64 quad_perm:[1,0,3,2] row_mask:0xf bank_mask:0xf
v_mfma_f32_16x16x32_bf16 v[244:247],v[104:107],v[152:155],v[244:247]
v_mul_f32_dpp v165,v165,v65 quad_perm:[1,0,3,2] row_mask:0xf bank_mask:0xf
v_mul_f32_dpp v166,v166,v66 quad_perm:[1,0,3,2] row_mask:0xf bank_mask:0xf
v_mul_f32_dpp v167,v167,v67 quad_perm:[1,0,3,2] row_mask:0xf bank_mask:0xf
v_mul_f32_dpp v172,v172,v68 quad_perm:[1,0,3,2] row_mask:0xf bank_mask:0xf
v_mfma_f32_16x16x32_bf16 v[252:255],v[108:111],v[152:155],v[252:255]
v_mul_f32_dpp v173,v173,v69 quad_perm:[1,0,3,2] row_mask:0xf bank_mask:0xf
v_mul_f32_dpp v174,v174,v70 quad_perm:[1,0,3,2] row_mask:0xf bank_mask:0xf
v_mul_f32_dpp v175,v175,v71 quad_perm:[1,0,3,2] row_mask:0xf bank_mask:0xf
v_cvt_pk_bf16_f32 v20,v164,v165
v_cvt_pk_bf16_f32 v21,v166,v167
v_cvt_pk_bf16_f32 v22,v172,v173
v_cvt_pk_bf16_f32 v23,v174,v175
s_mov_b64 exec,s[12:13]
v_swap_b32 v32,v20
v_swap_b32 v33,v21
v_swap_b32 v34,v22
v_swap_b32 v35,v23
s_mov_b64 exec,-1
v_mul_f32 v180,v180,v72
v_mul_f32 v181,v181,v73
v_mul_f32 v182,v182,v74
v_mul_f32 v183,v183,v75
s_add_u32 s65,s61,3145728
buffer_store_dwordx4 v[24:27],v15,s[40:43],s65 offen offset:384 sc1
v_mul_f32 v188,v188,v76
v_mul_f32 v189,v189,v77
v_mul_f32 v190,v190,v78
v_mul_f32 v191,v191,v79
v_cvt_pk_bf16_f32 v28,v180,v181
v_cvt_pk_bf16_f32 v29,v182,v183
v_cvt_pk_bf16_f32 v30,v188,v189
v_cvt_pk_bf16_f32 v31,v190,v191
v_mul_f32_dpp v196,v196,v80 quad_perm:[1,0,3,2] row_mask:0xf bank_mask:0xf
v_mul_f32_dpp v197,v197,v81 quad_perm:[1,0,3,2] row_mask:0xf bank_mask:0xf
v_mul_f32_dpp v198,v198,v82 quad_perm:[1,0,3,2] row_mask:0xf bank_mask:0xf
v_mul_f32_dpp v199,v199,v83 quad_perm:[1,0,3,2] row_mask:0xf bank_mask:0xf
v_mul_f32_dpp v204,v204,v84 quad_perm:[1,0,3,2] row_mask:0xf bank_mask:0xf
v_mul_f32_dpp v205,v205,v85 quad_perm:[1,0,3,2] row_mask:0xf bank_mask:0xf
v_mul_f32_dpp v206,v206,v86 quad_perm:[1,0,3,2] row_mask:0xf bank_mask:0xf
v_mul_f32_dpp v207,v207,v87 quad_perm:[1,0,3,2] row_mask:0xf bank_mask:0xf
v_cvt_pk_bf16_f32 v36,v196,v197
v_cvt_pk_bf16_f32 v37,v198,v199
v_cvt_pk_bf16_f32 v38,v204,v205
v_cvt_pk_bf16_f32 v39,v206,v207
s_mov_b64 exec,s[12:13]
v_swap_b32 v28,v36
v_swap_b32 v29,v37
v_swap_b32 v30,v38
v_swap_b32 v31,v39
s_mov_b64 exec,-1
v_mul_f32 v208,v208,v64
v_mul_f32 v209,v209,v65
v_mul_f32 v210,v210,v66
v_mul_f32 v211,v211,v67
v_mul_f32 v216,v216,v68
v_mul_f32 v217,v217,v69
v_mul_f32 v218,v218,v70
v_mul_f32 v219,v219,v71
v_cvt_pk_bf16_f32 v40,v208,v209
v_cvt_pk_bf16_f32 v41,v210,v211
v_cvt_pk_bf16_f32 v42,v216,v217
v_cvt_pk_bf16_f32 v43,v218,v219
v_mul_f32_dpp v224,v224,v72 quad_perm:[1,0,3,2] row_mask:0xf bank_mask:0xf
v_mul_f32_dpp v225,v225,v73 quad_perm:[1,0,3,2] row_mask:0xf bank_mask:0xf
v_mul_f32_dpp v226,v226,v74 quad_perm:[1,0,3,2] row_mask:0xf bank_mask:0xf
v_mul_f32_dpp v227,v227,v75 quad_perm:[1,0,3,2] row_mask:0xf bank_mask:0xf
v_mul_f32_dpp v232,v232,v76 quad_perm:[1,0,3,2] row_mask:0xf bank_mask:0xf
v_mul_f32_dpp v233,v233,v77 quad_perm:[1,0,3,2] row_mask:0xf bank_mask:0xf
v_mul_f32_dpp v234,v234,v78 quad_perm:[1,0,3,2] row_mask:0xf bank_mask:0xf
s_add_u32 s65,s61,3145728
buffer_store_dwordx4 v[32:35],v10,s[40:43],s65 offen offset:128 sc1
v_mul_f32_dpp v235,v235,v79 quad_perm:[1,0,3,2] row_mask:0xf bank_mask:0xf
v_cvt_pk_bf16_f32 v48,v224,v225
v_cvt_pk_bf16_f32 v49,v226,v227
v_cvt_pk_bf16_f32 v50,v232,v233
v_cvt_pk_bf16_f32 v51,v234,v235
s_mov_b64 exec,s[12:13]
v_swap_b32 v40,v48
v_swap_b32 v41,v49
v_swap_b32 v42,v50
v_swap_b32 v43,v51
s_mov_b64 exec,-1
v_mul_f32 v240,v240,v80
v_mul_f32 v241,v241,v81
v_mul_f32 v242,v242,v82
v_mul_f32 v243,v243,v83
v_mul_f32 v248,v248,v84
v_mul_f32 v249,v249,v85
v_mul_f32 v250,v250,v86
v_mul_f32 v251,v251,v87
v_cvt_pk_bf16_f32 v56,v240,v241
v_cvt_pk_bf16_f32 v57,v242,v243
v_cvt_pk_bf16_f32 v58,v248,v249
v_cvt_pk_bf16_f32 v59,v250,v251
v_mul_f32_dpp v212,v212,v64 quad_perm:[1,0,3,2] row_mask:0xf bank_mask:0xf
v_mul_f32_dpp v213,v213,v65 quad_perm:[1,0,3,2] row_mask:0xf bank_mask:0xf
v_mul_f32_dpp v214,v214,v66 quad_perm:[1,0,3,2] row_mask:0xf bank_mask:0xf
v_mul_f32_dpp v215,v215,v67 quad_perm:[1,0,3,2] row_mask:0xf bank_mask:0xf
v_mul_f32_dpp v220,v220,v68 quad_perm:[1,0,3,2] row_mask:0xf bank_mask:0xf
v_mul_f32_dpp v221,v221,v69 quad_perm:[1,0,3,2] row_mask:0xf bank_mask:0xf
v_mul_f32_dpp v222,v222,v70 quad_perm:[1,0,3,2] row_mask:0xf bank_mask:0xf
v_mul_f32_dpp v223,v223,v71 quad_perm:[1,0,3,2] row_mask:0xf bank_mask:0xf
v_cvt_pk_bf16_f32 v44,v212,v213
v_cvt_pk_bf16_f32 v45,v214,v215
v_cvt_pk_bf16_f32 v46,v220,v221
v_cvt_pk_bf16_f32 v47,v222,v223
s_mov_b64 exec,s[12:13]
v_swap_b32 v56,v44
s_mov_b32 s86,0xbe801d5a
v_swap_b32 v57,v45
v_swap_b32 v58,v46
v_swap_b32 v59,v47
s_mov_b64 exec,-1
v_mul_f32 v228,v228,v72
v_mul_f32 v229,v229,v73
v_mul_f32 v230,v230,v74
s_add_u32 s65,s61,3145728
buffer_store_dwordx4 v[20:23],v15,s[40:43],s65 offen offset:512 sc1
v_mul_f32 v231,v231,v75
v_mul_f32 v236,v236,v76
v_mul_f32 v237,v237,v77
v_mul_f32 v238,v238,v78
v_mul_f32 v239,v239,v79
v_cvt_pk_bf16_f32 v52,v228,v229
v_cvt_pk_bf16_f32 v53,v230,v231
v_cvt_pk_bf16_f32 v54,v236,v237
v_cvt_pk_bf16_f32 v55,v238,v239
v_mul_f32_dpp v244,v244,v80 quad_perm:[1,0,3,2] row_mask:0xf bank_mask:0xf
v_mul_f32_dpp v245,v245,v81 quad_perm:[1,0,3,2] row_mask:0xf bank_mask:0xf
v_mul_f32_dpp v246,v246,v82 quad_perm:[1,0,3,2] row_mask:0xf bank_mask:0xf
v_mul_f32_dpp v247,v247,v83 quad_perm:[1,0,3,2] row_mask:0xf bank_mask:0xf
v_mul_f32_dpp v252,v252,v84 quad_perm:[1,0,3,2] row_mask:0xf bank_mask:0xf
v_mul_f32_dpp v253,v253,v85 quad_perm:[1,0,3,2] row_mask:0xf bank_mask:0xf
v_mul_f32_dpp v254,v254,v86 quad_perm:[1,0,3,2] row_mask:0xf bank_mask:0xf
v_mul_f32_dpp v255,v255,v87 quad_perm:[1,0,3,2] row_mask:0xf bank_mask:0xf
v_cvt_pk_bf16_f32 v60,v244,v245
v_cvt_pk_bf16_f32 v61,v246,v247
v_cvt_pk_bf16_f32 v62,v252,v253
v_cvt_pk_bf16_f32 v63,v254,v255
s_mov_b64 exec,s[12:13]
v_swap_b32 v52,v60
v_swap_b32 v53,v61
v_swap_b32 v54,v62
v_swap_b32 v55,v63
s_mov_b64 exec,-1
s_waitcnt vmcnt(4)
s_barrier
s_sleep 1
s_add_u32 s65,s61,3145728
buffer_store_dwordx4 v[28:31],v10,s[40:43],s65 offen offset:256 sc1
s_sleep 2
s_add_u32 s65,s61,3145728
buffer_store_dwordx4 v[36:39],v15,s[40:43],s65 offen offset:640 sc1
s_sleep 2
s_add_u32 s65,s61,3244032
buffer_store_dwordx4 v[40:43],v10,s[40:43],s65 offen offset:0 sc1
s_sleep 2
s_add_u32 s65,s61,3244032
buffer_store_dwordx4 v[48:51],v15,s[40:43],s65 offen offset:384 sc1
s_sleep 2
s_add_u32 s65,s61,3244032
buffer_store_dwordx4 v[56:59],v10,s[40:43],s65 offen offset:128 sc1
s_sleep 2
s_add_u32 s65,s61,3244032
buffer_store_dwordx4 v[44:47],v15,s[40:43],s65 offen offset:512 sc1
s_sleep 2
s_add_u32 s65,s61,3244032
buffer_store_dwordx4 v[52:55],v10,s[40:43],s65 offen offset:256 sc1
s_sleep 2
s_add_u32 s65,s61,3244032
buffer_store_dwordx4 v[60:63],v15,s[40:43],s65 offen offset:640 sc1
v_and_b32 v232,63,%1
s_getpc_b64 s[88:89]
N_itA71_%=:
s_lshl_b32 s87,s10,10
s_add_u32 s87,s87,86024
s_sub_u32 s88,s88,N_itA71_%=-N_main_%=
s_subb_u32 s89,s89,0
s_add_u32 s88,s88,s87
s_addc_u32 s89,s89,0
s_swappc_b64 s[90:91],s[88:89]
s_cmp_lg_u32 s52,1
s_cbranch_scc1 N_s0_%=
v_min_u32 v236,47,v232
v_lshlrev_b32 v152,4,v236
v_lshlrev_b32 v153,2,v236
v_min_u32 v236,59,v232
v_mul_u32_u24 v237,0xaaab,v236
v_lshrrev_b32 v237,19,v237
v_mul_u32_u24 v237,14592,v237
v_lshl_add_u32 v148,v236,4,v237
s_add_u32 s64,s24,0
s_addc_u32 s65,s25,0
global_load_dwordx4 v[112:115],v152,s[64:65]
s_add_u32 s64,s24,9216
s_addc_u32 s65,s25,0
global_load_dwordx4 v[116:119],v152,s[64:65]
s_add_u32 s64,s24,18432
s_addc_u32 s65,s25,0
global_load_dwordx4 v[120:123],v152,s[64:65]
s_add_u32 s64,s24,27648
s_addc_u32 s65,s25,0
global_load_dwordx4 v[124:127],v152,s[64:65]
s_add_u32 s64,s24,36864
s_addc_u32 s65,s25,0
global_load_dwordx4 v[128:131],v152,s[64:65]
s_add_u32 s64,s24,46080
s_addc_u32 s65,s25,0
global_load_dwordx4 v[132:135],v152,s[64:65]
s_add_u32 s64,s24,55296
s_addc_u32 s65,s25,0
global_load_dwordx4 v[136:139],v152,s[64:65]
s_add_u32 s64,s24,64512
s_addc_u32 s65,s25,0
global_load_dwordx4 v[140:143],v152,s[64:65]
global_load_dwordx4 v[144:147],v148,s[28:29]
global_load_dwordx4 v[148:151],v148,s[28:29] offset:3648
global_load_dwordx4 v[156:159],v152,s[26:27]
global_load_dword v152,v153,s[32:33]
global_load_dword v153,v153,s[30:31]
N_s0_%=:
s_and_b32 s64,s52,3
s_mul_i32 s64,s64,18432
s_add_u32 s64,s64,4096
v_lshl_add_u32 v255,v232,4,s64
global_load_dwordx4 v[160:163],v255,s[24:25] offset:-4096
global_load_dwordx4 v[164:167],v255,s[24:25] offset:-3072
global_load_dwordx4 v[168:171],v255,s[24:25] offset:-2048
global_load_dwordx4 v[172:175],v255,s[24:25] offset:-1024
global_load_dwordx4 v[176:179],v255,s[24:25] offset:0
global_load_dwordx4 v[180:183],v255,s[24:25] offset:1024
global_load_dwordx4 v[184:187],v255,s[24:25] offset:2048
global_load_dwordx4 v[188:191],v255,s[24:25] offset:3072
v_add_u32 v255,8192,v255
global_load_dwordx4 v[192:195],v255,s[24:25] offset:-4096
global_load_dwordx4 v[196:199],v255,s[24:25] offset:-3072
global_load_dwordx4 v[200:203],v255,s[24:25] offset:-2048
global_load_dwordx4 v[204:207],v255,s[24:25] offset:-1024
global_load_dwordx4 v[208:211],v255,s[24:25] offset:0
global_load_dwordx4 v[212:215],v255,s[24:25] offset:1024
global_load_dwordx4 v[216:219],v255,s[24:25] offset:2048
global_load_dwordx4 v[220:223],v255,s[24:25] offset:3072
v_add_u32 v255,8192,v255
global_load_dwordx4 v[224:227],v255,s[24:25] offset:-4096
global_load_dwordx4 v[228:231],v255,s[24:25] offset:-3072
s_waitcnt vmcnt(0)
v_or3_b32 v160,v160,v164,v165
v_or3_b32 v162,v162,v166,v167
v_or3_b32 v160,v160,v168,v169
v_or3_b32 v162,v162,v170,v171
v_or3_b32 v160,v160,v172,v173
v_or3_b32 v162,v162,v174,v175
v_or3_b32 v160,v160,v176,v177
v_or3_b32 v162,v162,v178,v179
v_or3_b32 v160,v160,v180,v181
v_or3_b32 v162,v162,v182,v183
v_or3_b32 v160,v160,v184,v185
v_or3_b32 v162,v162,v186,v187
v_or3_b32 v160,v160,v188,v189
v_or3_b32 v162,v162,v190,v191
v_or3_b32 v160,v160,v192,v193
v_or3_b32 v162,v162,v194,v195
v_or3_b32 v160,v160,v196,v197
v_or3_b32 v162,v162,v198,v199
v_or3_b32 v160,v160,v200,v201
v_or3_b32 v162,v162,v202,v203
v_or3_b32 v160,v160,v204,v205
v_or3_b32 v162,v162,v206,v207
v_or3_b32 v160,v160,v208,v209
v_or3_b32 v162,v162,v210,v211
v_or3_b32 v160,v160,v212,v213
v_or3_b32 v162,v162,v214,v215
v_or3_b32 v160,v160,v216,v217
v_or3_b32 v162,v162,v218,v219
v_or3_b32 v160,v160,v220,v221
v_or3_b32 v162,v162,v222,v223
v_or3_b32 v160,v160,v224,v225
v_or3_b32 v162,v162,v226,v227
v_or3_b32 v160,v160,v228,v229
v_or3_b32 v162,v162,v230,v231
v_or3_b32 v160,v160,v161,v162
v_or_b32 v160,v160,v163
s_mov_b32 s64,0x7bff7bff
v_and_b32 v160,0x40004000,v160
v_pk_mul_f16 v160,v160,s64
v_or_b32 v1,v1,v160
s_cmp_lg_u32 s52,1
s_cbranch_scc1 N_s1_%=
s_mov_b32 s65,0x7fff7fff
v_and_b32 v112,s65,v112
v_and_b32 v113,s65,v113
v_and_b32 v114,s65,v114
v_and_b32 v115,s65,v115
v_and_b32 v116,s65,v116
v_and_b32 v117,s65,v117
v_and_b32 v118,s65,v118
v_and_b32 v119,s65,v119
v_and_b32 v120,s65,v120
v_and_b32 v121,s65,v121
v_and_b32 v122,s65,v122
v_and_b32 v123,s65,v123
v_and_b32 v124,s65,v124
v_and_b32 v125,s65,v125
v_and_b32 v126,s65,v126
v_and_b32 v127,s65,v127
v_and_b32 v128,s65,v128
v_and_b32 v129,s65,v129
v_and_b32 v130,s65,v130
v_and_b32 v131,s65,v131
v_and_b32 v132,s65,v132
v_and_b32 v133,s65,v133
v_and_b32 v134,s65,v134
v_and_b32 v135,s65,v135
v_and_b32 v136,s65,v136
v_and_b32 v137,s65,v137
v_and_b32 v138,s65,v138
v_and_b32 v139,s65,v139
v_and_b32 v140,s65,v140
v_and_b32 v141,s65,v141
v_and_b32 v142,s65,v142
v_and_b32 v143,s65,v143
v_pk_max_u16 v100,v112,v116
v_pk_max_u16 v101,v113,v117
v_pk_max_u16 v102,v114,v118
v_pk_max_u16 v103,v115,v119
v_pk_max_u16 v100,v100,v120
v_pk_max_u16 v101,v101,v121
v_pk_max_u16 v102,v102,v122
v_pk_max_u16 v103,v103,v123
v_pk_max_u16 v100,v100,v124
v_pk_max_u16 v101,v101,v125
v_pk_max_u16 v102,v102,v126
v_pk_max_u16 v103,v103,v127
v_pk_max_u16 v100,v100,v128
v_pk_max_u16 v101,v101,v129
v_pk_max_u16 v102,v102,v130
v_pk_max_u16 v103,v103,v131
v_pk_max_u16 v100,v100,v132
v_pk_max_u16 v101,v101,v133
v_pk_max_u16 v102,v102,v134
v_pk_max_u16 v103,v103,v135
v_pk_max_u16 v100,v100,v136
v_pk_max_u16 v101,v101,v137
v_pk_max_u16 v102,v102,v138
v_pk_max_u16 v103,v103,v139
v_pk_max_u16 v100,v100,v140
v_pk_max_u16 v101,v101,v141
v_pk_max_u16 v102,v102,v142
v_pk_max_u16 v103,v103,v143
v_pk_min_u16 v100,v100,v101
v_pk_min_u16 v102,v102,v103
v_pk_min_u16 v100,v100,v102
v_lshrrev_b32 v101,16,v100
v_and_b32 v100,0xffff,v100
v_min_u32 v100,v100,v101
v_mov_b32 v101,0x3a00
v_cmp_lt_u32_e64 s[92:93],v100,v101
s_nop 0
s_or_b32 s64,s92,s93
v_and_b32 v152,s65,v152
v_lshrrev_b32 v101,16,v152
v_and_b32 v152,0xffff,v152
v_max_u32 v152,v152,v101
v_mov_b32 v101,0x4580
v_cmp_le_u32_e64 s[92:93],v101,v152
s_nop 0
s_or_b32 s64,s64,s92
s_or_b32 s64,s64,s93
v_and_b32 v153,s65,v153
v_and_b32 v144,s65,v144
v_pk_max_u16 v153,v153,v144
v_and_b32 v145,s65,v145
v_pk_max_u16 v153,v153,v145
v_and_b32 v146,s65,v146
v_pk_max_u16 v153,v153,v146
v_and_b32 v147,s65,v147
v_pk_max_u16 v153,v153,v147
v_and_b32 v148,s65,v148
v_pk_max_u16 v153,v153,v148
v_and_b32 v149,s65,v149
v_pk_max_u16 v153,v153,v149
v_and_b32 v150,s65,v150
v_pk_max_u16 v153,v153,v150
v_and_b32 v151,s65,v151
v_pk_max_u16 v153,v153,v151
v_lshrrev_b32 v101,16,v153
v_and_b32 v153,0xffff,v153
v_max_u32 v153,v153,v101
v_mov_b32 v101,0x5380
v_cmp_le_u32_e64 s[92:93],v101,v153
s_nop 0
s_or_b32 s64,s64,s92
s_or_b32 s64,s64,s93
v_and_b32 v156,s65,v156
v_and_b32 v157,s65,v157
v_and_b32 v158,s65,v158
v_and_b32 v159,s65,v159
v_pk_max_u16 v156,v156,v157
v_pk_max_u16 v156,v156,v158
v_pk_max_u16 v156,v156,v159
v_lshrrev_b32 v101,16,v156
v_and_b32 v156,0xffff,v156
v_max_u32 v156,v156,v101
v_mov_b32 v101,0x4280
v_cmp_le_u32_e64 s[92:93],v101,v156
s_nop 0
s_or_b32 s64,s64,s92
s_or_b32 s64,s64,s93
s_cmp_eq_u32 s64,0
s_cbranch_scc1 N_s1_%=
v_mov_b32 v1,0x7c007c00
N_s1_%=:
v_and_b32 v1,0x7c007c00,v1
v_lshrrev_b32 v2,16,v1
v_and_b32 v1,0xffff,v1
v_max_u32 v1,v1,v2
v_cmp_eq_u32 vcc,0x7c00,v1
s_or_b32 s64,vcc_lo,vcc_hi
v_mov_b32 v1,s64
v_mov_b32 v2,8192
s_mov_b64 exec,1
ds_or_b32 v2,v1 offset:3456
s_mov_b64 exec,-1
s_waitcnt lgkmcnt(0)
s_barrier
s_setprio 3
ds_read_b32 v1,v2 offset:3456
s_waitcnt lgkmcnt(0)
s_setprio 0
v_readfirstlane_b32 s64,v1
s_cmp_lg_u32 s64,0
s_cbranch_scc1 N_fallback_%=
s_endpgm
s_nop 4
N_B_%=:
v_mov_b32 v192,0x409d3ef8
v_and_b32 v248,255,%1
v_lshlrev_b32 v198,4,v248
v_lshlrev_b32 v248,19,v248
v_cmp_eq_u32_e64 s[94:95],0,v248
v_add_u32 v199,0x8000,v198
s_mov_b32 s4,0x3f000000
s_mov_b32 s5,0x3fc00000
s_mov_b32 s6,0x40200000
s_mov_b32 s7,0x40600000
s_mov_b32 s8,0x40900000
s_mov_b32 s9,0x40b00000
s_mov_b32 s10,0x40d00000
s_mov_b32 s11,0x40f00000
s_mov_b32 s12,0x3f80222e
s_mov_b32 s13,0x13089abf
s_mov_b32 s96,0xebefe000
s_mov_b32 s15,0
N_tl_%=:
v_add_u32 v237,0x10000,v248
v_mul_f32 v194,s99,v248
v_fma_f32 v196,v237,s99,-v194
v_pk_fma_f32 v[160:161],v[196:197],s[4:5],v[194:195] op_sel_hi:[0,1,0]
v_pk_fma_f32 v[162:163],v[196:197],s[6:7],v[194:195] op_sel_hi:[0,1,0]
v_pk_fma_f32 v[164:165],v[196:197],s[8:9],v[194:195] op_sel_hi:[0,1,0]
v_pk_fma_f32 v[166:167],v[196:197],s[10:11],v[194:195] op_sel_hi:[0,1,0]
v_cndmask_b32_e64 v160,v160,0,s[94:95]
v_pk_mul_f32 v[168:169],v[160:161],v[160:161]
v_pk_mul_f32 v[170:171],v[162:163],v[162:163]
v_pk_mul_f32 v[172:173],v[164:165],v[164:165]
v_pk_mul_f32 v[174:175],v[166:167],v[166:167]
v_pk_fma_f32 v[168:169],v[168:169],s[12:13],v[192:193] op_sel_hi:[1,0,0]
v_pk_fma_f32 v[170:171],v[170:171],s[12:13],v[192:193] op_sel_hi:[1,0,0]
v_pk_fma_f32 v[172:173],v[172:173],s[12:13],v[192:193] op_sel_hi:[1,0,0]
v_pk_fma_f32 v[174:175],v[174:175],s[12:13],v[192:193] op_sel_hi:[1,0,0]
v_pk_mul_f32 v[168:169],v[168:169],v[160:161]
v_pk_mul_f32 v[170:171],v[170:171],v[162:163]
v_pk_mul_f32 v[172:173],v[172:173],v[164:165]
v_pk_mul_f32 v[174:175],v[174:175],v[166:167]
v_exp_f32 v168,v168
v_exp_f32 v169,v169
v_exp_f32 v170,v170
v_exp_f32 v171,v171
v_exp_f32 v172,v172
v_exp_f32 v173,v173
v_exp_f32 v174,v174
v_exp_f32 v175,v175
v_pk_fma_f32 v[168:169],v[168:169],s[96:97],s[96:97] op_sel_hi:[1,0,0]
v_pk_fma_f32 v[170:171],v[170:171],s[96:97],s[96:97] op_sel_hi:[1,0,0]
v_pk_fma_f32 v[172:173],v[172:173],s[96:97],s[96:97] op_sel_hi:[1,0,0]
v_pk_fma_f32 v[174:175],v[174:175],s[96:97],s[96:97] op_sel_hi:[1,0,0]
v_rcp_f32 v168,v168
v_rcp_f32 v169,v169
v_rcp_f32 v170,v170
v_rcp_f32 v171,v171
v_rcp_f32 v172,v172
v_rcp_f32 v173,v173
v_rcp_f32 v174,v174
v_rcp_f32 v175,v175
v_pk_mul_f32 v[168:169],v[168:169],v[160:161]
v_pk_mul_f32 v[170:171],v[170:171],v[162:163]
v_pk_mul_f32 v[172:173],v[172:173],v[164:165]
v_pk_mul_f32 v[174:175],v[174:175],v[166:167]
v_pk_fma_f32 v[160:161],v[160:161],s[12:13],v[168:169] op_sel:[0,1,0] op_sel_hi:[1,1,1]
v_pk_fma_f32 v[162:163],v[162:163],s[12:13],v[170:171] op_sel:[0,1,0] op_sel_hi:[1,1,1]
v_pk_fma_f32 v[164:165],v[164:165],s[12:13],v[172:173] op_sel:[0,1,0] op_sel_hi:[1,1,1]
v_pk_fma_f32 v[166:167],v[166:167],s[12:13],v[174:175] op_sel:[0,1,0] op_sel_hi:[1,1,1]
v_cvt_pk_bf16_f32 v176,v168,v169
v_cvt_pk_bf16_f32 v177,v170,v171
v_cvt_pk_bf16_f32 v178,v172,v173
v_cvt_pk_bf16_f32 v179,v174,v175
v_cvt_pk_bf16_f32 v180,v160,v161
v_cvt_pk_bf16_f32 v181,v162,v163
v_cvt_pk_bf16_f32 v182,v164,v165
v_cvt_pk_bf16_f32 v183,v166,v167
ds_write_b128 v198,v[176:179] offset:0
ds_write_b128 v199,v[180:183] offset:32768
s_mov_b64 s[94:95],0
v_add_u32 v248,0x8000000,v248
v_add_u32 v198,4096,v198
v_add_u32 v199,4096,v199
s_add_u32 s15,s15,1
s_cmp_lt_u32 s15,2
s_cbranch_scc1 N_tl_%=
s_and_b32 s64,s52,3
s_lshl_b32 s10,s64,2
s_mov_b32 s8,0x0ff00ff0
s_mov_b32 s9,0x0ff00ff0
s_and_b32 s64,s52,3
s_mov_b32 s12,0xaaaaaaaa
s_mov_b32 s13,0xaaaaaaaa
s_and_b32 s64,s52,3
s_lshl_b32 s10,s64,2
s_add_u32 s64,s10,s71
v_and_b32 v233,15,v232
v_lshrrev_b32 v234,4,v232
s_mul_i32 s61,s64,98304
s_add_u32 s61,s61,s68
v_and_b32 v236,1,v234
v_lshlrev_b32 v236,1,v236
v_lshrrev_b32 v237,5,v232
v_or_b32 v235,v236,v237
s_mov_b32 s54,0x44ea44ea
s_mov_b32 s55,0x877f877f
s_mov_b32 s8,0x0ff00ff0
s_mov_b32 s9,0x0ff00ff0
v_mov_b32 v1,0
v_mov_b32 v239,0x70000
v_bfe_u32 v236,v233,1,1
v_sub_u32 v236,v235,v236
v_cmp_gt_u32_e64 s[92:93],3,v236
v_lshrrev_b32 v237,2,v233
v_lshlrev_b32 v237,1,v237
v_and_b32 v238,1,v233
v_or_b32 v237,v237,v238
v_lshl_add_u32 v236,v236,3,v237
v_lshlrev_b32 v236,4,v236
v_add_u32 v236,143488,v236
v_cndmask_b32_e64 v11,v239,v236,s[92:93]
v_and_b32 v13,3,v237
v_lshlrev_b32 v236,1,v233
v_and_b32 v236,4,v236
v_lshl_add_u32 v236,v234,3,v236
v_mov_b32 v238,0x54250310
v_mul_u32_u24 v13,0x5555,v13
v_bfe_u32 v236,v238,v236,3
v_lshl_add_u32 v236,v236,3,v237
v_lshlrev_b32 v236,4,v236
v_add_u32 v12,143488,v236
v_lshrrev_b32 v237,2,v233
v_cmp_eq_u32_e64 s[92:93],v234,v237
v_lshlrev_b32 v236,4,v233
v_add_u32 v236,10112,v236
v_cndmask_b32_e64 v14,v239,v236,s[92:93]
v_and_b32 v239,7,v233
v_lshl_add_u32 v236,v239,1,v235
v_add_u32 v237,1,v236
v_bfe_u32 v237,v237,2,2
v_mul_u32_u24 v236,192,v236
v_lshl_add_u32 v236,v237,4,v236
v_add_u32 v237,1,v239
v_bfe_u32 v237,v237,1,2
v_xor_b32 v237,v237,v234
v_mul_u32_u24 v239,384,v239
v_lshl_add_u32 v237,v237,4,v239
v_sub_u32 v237,v237,v236
v_add_u32 v8,192,v237
v_add_u32 v15,73728,v236
v_and_b32 v238,1,v234
v_lshrrev_b32 v237,1,v234
v_lshlrev_b32 v238,6,v238
v_lshl_add_u32 v238,v237,4,v238
v_add_u32 v7,8192,v238
s_and_b32 s64,s52,3
s_mul_i32 s67,s64,54
s_cmp_eq_u32 s70,31
s_cselect_b32 s66,17,99
v_add_u32 v236,s67,v232
v_add_u32 v236,108,v236
v_mul_u32_u24 v237,0xaaab,v236
v_lshrrev_b32 v237,19,v237
v_mul_u32_u24 v238,12,v237
v_sub_u32 v238,v236,v238
v_add_u32 v236,1,v237
v_bfe_u32 v236,v236,2,2
v_xor_b32 v238,v238,v236
v_mul_u32_u24 v236,192,v237
v_lshl_add_u32 v239,v238,4,v236
v_cmp_eq_u32_e64 s[92:93],s66,v237
v_mov_b32 v236,0x7fff0000
s_nop 0
v_cndmask_b32_e64 v239,v239,v236,s[92:93]
v_cndmask_b32_e64 v3,v239,v3,vcc
v_and_b32 v239,7,v233
v_mul_u32_u24 v239,384,v239
v_and_b32 v238,1,v233
v_mul_u32_u24 v238,320,v238
v_sub_u32 v239,v239,v238
s_mov_b32 s12,0xaaaaaaaa
s_mov_b32 s13,0xaaaaaaaa
v_lshl_add_u32 v10,v234,4,v239
s_mov_b64 exec,s[8:9]
v_add_u32 v10,196608,v10
s_mov_b64 exec,-1
v_lshrrev_b32 v236,3,v233
v_bfe_u32 v237,v233,2,1
v_lshl_add_u32 v237,v236,4,v237
v_lshl_add_u32 v237,v234,1,v237
v_lshlrev_b32 v237,6,v237
v_lshl_add_u32 v237,v236,4,v237
v_and_b32 v238,1,v233
v_lshl_add_u32 v237,v238,5,v237
v_bfe_u32 v238,v233,1,1
v_lshl_add_u32 v237,v238,3,v237
v_xor_b32 v238,16,v237
v_add_u32 v4,16256,v237
v_add_u32 v5,16256,v238
v_lshrrev_b32 v236,3,v233
v_bfe_u32 v237,v233,2,1
v_lshl_add_u32 v236,v237,1,v236
v_lshrrev_b32 v237,1,v234
v_lshl_add_u32 v236,v237,2,v236
v_mul_u32_u24 v236,192,v236
v_and_b32 v238,1,v234
v_mul_u32_u24 v239,3080,v238
v_add_u32 v236,v236,v239
v_and_b32 v239,3,v233
v_lshl_add_u32 v236,v239,4,v236
v_add_u32 v6,40896,v236
s_mov_b32 s42,50331648
s_mov_b32 s43,0x00020000
v_bfe_u32 v236,v232,2,3
v_mul_u32_u24 v236,768,v236
v_and_b32 v237,3,v232
v_lshrrev_b32 v238,5,v232
v_lshl_add_u32 v237,v238,2,v237
v_lshl_add_u32 v236,v237,4,v236
v_lshlrev_b32 v237,4,v232
s_bitcmp0_b32 s52,0
s_mov_b32 s58,40896
s_cselect_b32 s58,16256,s58
s_cselect_b64 s[92:93],-1,0
s_cselect_b32 s59,64,6144
s_movk_i32 s53,1024
s_cselect_b32 s53,6144,s53
s_cselect_b32 s60,0,67584
s_cselect_b32 s97,64,0
s_cselect_b32 s69,0,0x70000000
s_mov_b32 s100,73728
s_cselect_b32 s100,110688,s100
s_cselect_b32 s64,16,0
s_movk_i32 s50,1032
s_cselect_b32 s50,1024,s50
s_cselect_b32 s51,s53,-72704
v_cndmask_b32_e64 v236,v237,v236,s[92:93]
v_cndmask_b32_e64 v2,v241,v236,vcc
v_xor_b32 v236,s64,v236
v_cndmask_b32_e64 v3,v3,v236,vcc
s_mov_b32 s46,73728
s_mov_b32 s47,0x00020000
v_lshlrev_b32 v236,4,v232
v_and_b32 v237,255,%1
v_add_u32 v236,157312,v236
v_lshlrev_b32 v237,3,v237
v_add_u32 v237,157312,v237
ds_write_b128 v236,v[4:7] offset:0
ds_write_b128 v236,v[8:11] offset:1024
ds_write_b128 v236,v[12:15] offset:2048
ds_write_b64 v237,v[2:3] offset:3072
s_waitcnt lgkmcnt(0)
s_sub_u32 s36,s20,192
s_subb_u32 s37,s21,0
s_and_b32 s37,s37,0xffff
s_bitcmp0_b32 s52,0
s_cselect_b64 s[44:45],s[24:25],s[28:29]
s_and_b32 s45,s45,0xffff
s_mov_b32 s40,s34
s_and_b32 s41,s35,0xffff
s_cbranch_vccz RB1_%=
s_add_u32 m0,s58,12320
s_nop 0
buffer_load_dwordx4 v2,s[44:47],s69 offen lds
s_add_u32 m0,m0,1024
s_add_u32 s69,s69,s53
buffer_load_dwordx4 v3,s[44:47],s69 offen lds
s_add_u32 m0,m0,1024
s_add_u32 s69,s69,s53
buffer_load_dwordx4 v2,s[44:47],s69 offen lds
s_add_u32 m0,m0,s50
s_add_u32 s69,s69,s53
buffer_load_dwordx4 v3,s[44:47],s69 offen lds
s_add_u32 m0,m0,1024
s_add_u32 s69,s69,s53
buffer_load_dwordx4 v2,s[44:47],s69 offen lds
s_add_u32 m0,m0,1024
s_add_u32 s69,s69,s53
buffer_load_dwordx4 v3,s[44:47],s69 offen lds
s_add_u32 m0,m0,s50
s_add_u32 s69,s69,s51
buffer_load_dwordx4 v2,s[44:47],s69 offen lds
s_add_u32 m0,m0,1024
s_add_u32 s69,s69,s53
buffer_load_dwordx4 v3,s[44:47],s69 offen lds
s_add_u32 m0,m0,1024
s_add_u32 s69,s69,s53
buffer_load_dwordx4 v2,s[44:47],s69 offen lds
s_add_u32 m0,m0,s50
s_add_u32 s69,s69,s53
buffer_load_dwordx4 v3,s[44:47],s69 offen lds
s_add_u32 m0,m0,1024
s_add_u32 s69,s69,s53
buffer_load_dwordx4 v2,s[44:47],s69 offen lds
s_add_u32 m0,m0,1024
s_add_u32 s69,s69,s53
buffer_load_dwordx4 v3,s[44:47],s69 offen lds
RB1_%=:
s_mov_b64 exec,s[62:63]
s_add_u32 m0,s57,34880
s_add_u32 s101,s56,983040
buffer_load_dwordx4 v241,s[36:39],s101 offen lds
s_add_u32 m0,s57,38368
s_add_u32 s101,s56,1081344
buffer_load_dwordx4 v241,s[36:39],s101 offen lds
s_add_u32 m0,s57,41856
s_add_u32 s101,s56,1179648
buffer_load_dwordx4 v241,s[36:39],s101 offen lds
s_add_u32 m0,s57,45344
s_add_u32 s101,s56,1277952
buffer_load_dwordx4 v241,s[36:39],s101 offen lds
s_add_u32 m0,s57,48832
s_add_u32 s101,s56,1376256
buffer_load_dwordx4 v241,s[36:39],s101 offen lds
s_add_u32 m0,s57,52320
s_add_u32 s101,s56,1474560
buffer_load_dwordx4 v241,s[36:39],s101 offen lds
s_add_u32 m0,s57,55808
s_add_u32 s101,s56,1572864
buffer_load_dwordx4 v241,s[36:39],s101 offen lds
s_add_u32 m0,s57,59296
s_nop 0
s_mov_b32 s86,0xbe801d5a
s_add_u32 s101,s56,1671168
buffer_load_dwordx4 v241,s[36:39],s101 offen lds
s_mov_b64 exec,-1
s_waitcnt lgkmcnt(0)
s_barrier
s_setprio 0
s_cbranch_vccz RB2_%=
s_add_u32 m0,s58,0
s_mul_i32 s69,s59,1
s_add_u32 s69,s69,s97
buffer_load_dwordx4 v2,s[44:47],s69 offen lds
s_add_u32 m0,m0,1024
s_add_u32 s69,s69,s53
buffer_load_dwordx4 v3,s[44:47],s69 offen lds
s_add_u32 m0,m0,1024
s_add_u32 s69,s69,s53
buffer_load_dwordx4 v2,s[44:47],s69 offen lds
s_add_u32 m0,m0,s50
s_add_u32 s69,s69,s53
buffer_load_dwordx4 v3,s[44:47],s69 offen lds
s_add_u32 m0,m0,1024
s_add_u32 s69,s69,s53
buffer_load_dwordx4 v2,s[44:47],s69 offen lds
s_add_u32 m0,m0,1024
s_add_u32 s69,s69,s53
buffer_load_dwordx4 v3,s[44:47],s69 offen lds
s_add_u32 m0,m0,s50
s_add_u32 s69,s69,s53
buffer_load_dwordx4 v2,s[44:47],s69 offen lds
s_add_u32 m0,m0,1024
s_add_u32 s69,s69,s53
buffer_load_dwordx4 v3,s[44:47],s69 offen lds
s_add_u32 m0,m0,1024
s_add_u32 s69,s69,s53
buffer_load_dwordx4 v2,s[44:47],s69 offen lds
s_add_u32 m0,m0,s50
s_add_u32 s69,s69,s53
buffer_load_dwordx4 v3,s[44:47],s69 offen lds
s_add_u32 m0,m0,1024
s_add_u32 s69,s69,s53
buffer_load_dwordx4 v2,s[44:47],s69 offen lds
s_add_u32 m0,m0,1024
s_add_u32 s69,s69,s53
buffer_load_dwordx4 v3,s[44:47],s69 offen lds
RB2_%=:
s_waitcnt lgkmcnt(0)
s_cbranch_vccnz RB3_%=
s_waitcnt vmcnt(0)
RB3_%=:
s_cbranch_vccz RB4_%=
s_waitcnt vmcnt(12)
RB4_%=:
s_barrier
v_lshlrev_b32 v253,7,v232
s_bfe_u32 s64,%0,0x20003
s_and_b32 s65,s52,3
s_lshl_b32 s65,s65,2
s_add_u32 s64,s64,s65
s_lshl_b32 s65,s64,13
v_add_u32 v252,s65,v253
s_getpc_b64 s[14:15]
N_pf_%=:
s_sub_u32 s14,s14,N_pf_%=-N_main_%=
s_subb_u32 s15,s15,0
v_min_u32 v252,N_mainend_%=-N_main_%=,v252
global_load_dword v254,v252,s[14:15]
s_getpc_b64 s[88:89]
N_itB5_%=:
s_lshl_b32 s87,s10,10
s_add_u32 s87,s87,4104
s_sub_u32 s88,s88,N_itB5_%=-N_main_%=
s_subb_u32 s89,s89,0
s_add_u32 s88,s88,s87
s_addc_u32 s89,s89,0
s_swappc_b64 s[90:91],s[88:89]
s_cbranch_vccnz RB6_%=
s_mov_b64 exec,s[62:63]
s_add_u32 m0,s57,62784
s_add_u32 s64,s56,1769472
buffer_load_dwordx4 v2,s[36:39],s64 offen lds
s_add_u32 m0,s57,64512
s_nop 0
buffer_load_dwordx4 v3,s[36:39],s64 offen lds
s_mov_b64 exec,-1
RB6_%=:
s_cbranch_vccnz RB7_%=
s_mov_b64 exec,s[62:63]
s_add_u32 m0,s57,0
s_add_u32 s64,s56,1966080
buffer_load_dwordx4 v2,s[36:39],s64 offen lds
s_add_u32 m0,s57,1728
s_nop 0
buffer_load_dwordx4 v3,s[36:39],s64 offen lds
s_mov_b64 exec,-1
RB7_%=:
s_cbranch_vccnz RB8_%=
s_mov_b64 exec,s[62:63]
s_add_u32 m0,s57,3488
s_add_u32 s64,s56,2064384
buffer_load_dwordx4 v2,s[36:39],s64 offen lds
s_add_u32 m0,s57,5216
s_nop 0
buffer_load_dwordx4 v3,s[36:39],s64 offen lds
s_mov_b64 exec,-1
RB8_%=:
s_cbranch_vccnz RB9_%=
s_mov_b64 exec,s[62:63]
s_add_u32 m0,s57,6976
s_add_u32 s64,s56,2162688
buffer_load_dwordx4 v2,s[36:39],s64 offen lds
s_add_u32 m0,s57,8704
s_nop 0
buffer_load_dwordx4 v3,s[36:39],s64 offen lds
s_mov_b64 exec,-1
RB9_%=:
s_cbranch_vccnz RB10_%=
s_mov_b64 exec,s[62:63]
s_add_u32 m0,s57,66272
s_add_u32 s64,s56,1867776
buffer_load_dwordx4 v2,s[36:39],s64 offen lds
s_add_u32 m0,s57,68000
s_nop 0
buffer_load_dwordx4 v3,s[36:39],s64 offen lds
s_mov_b64 exec,-1
RB10_%=:
s_barrier
s_cbranch_vccz RB11_%=
s_add_u32 m0,s58,12320
s_mul_i32 s69,s59,3
s_add_u32 s69,s69,s97
buffer_load_dwordx4 v2,s[44:47],s69 offen lds
s_add_u32 m0,m0,1024
s_add_u32 s69,s69,s53
buffer_load_dwordx4 v3,s[44:47],s69 offen lds
s_add_u32 m0,m0,1024
s_add_u32 s69,s69,s53
buffer_load_dwordx4 v2,s[44:47],s69 offen lds
s_add_u32 m0,m0,s50
s_add_u32 s69,s69,s53
buffer_load_dwordx4 v3,s[44:47],s69 offen lds
RB11_%=:
s_cbranch_vccz RB12_%=
s_add_u32 m0,m0,1024
s_add_u32 s69,s69,s53
buffer_load_dwordx4 v2,s[44:47],s69 offen lds
s_add_u32 m0,m0,1024
s_add_u32 s69,s69,s53
buffer_load_dwordx4 v3,s[44:47],s69 offen lds
s_add_u32 m0,m0,s50
s_add_u32 s69,s69,s53
buffer_load_dwordx4 v2,s[44:47],s69 offen lds
s_add_u32 m0,m0,1024
s_add_u32 s69,s69,s53
buffer_load_dwordx4 v3,s[44:47],s69 offen lds
RB12_%=:
s_cbranch_vccz RB13_%=
s_add_u32 m0,m0,1024
s_add_u32 s69,s69,s53
buffer_load_dwordx4 v2,s[44:47],s69 offen lds
s_add_u32 m0,m0,s50
s_add_u32 s69,s69,s53
buffer_load_dwordx4 v3,s[44:47],s69 offen lds
s_add_u32 m0,m0,1024
s_add_u32 s69,s69,s53
buffer_load_dwordx4 v2,s[44:47],s69 offen lds
s_add_u32 m0,m0,1024
s_add_u32 s69,s69,s53
buffer_load_dwordx4 v3,s[44:47],s69 offen lds
RB13_%=:
s_waitcnt vmcnt(13)
s_barrier
s_cbranch_vccnz RB14_%=
s_mov_b64 exec,s[62:63]
s_add_u32 m0,s57,10464
s_add_u32 s64,s56,2260992
buffer_load_dwordx4 v2,s[36:39],s64 offen lds
s_add_u32 m0,s57,12192
s_nop 0
buffer_load_dwordx4 v3,s[36:39],s64 offen lds
s_mov_b64 exec,-1
RB14_%=:
s_cbranch_vccnz RB15_%=
s_mov_b64 exec,s[62:63]
s_add_u32 m0,s57,13952
s_add_u32 s64,s56,2359296
buffer_load_dwordx4 v2,s[36:39],s64 offen lds
s_add_u32 m0,s57,15680
s_nop 0
buffer_load_dwordx4 v3,s[36:39],s64 offen lds
s_mov_b64 exec,-1
RB15_%=:
s_cbranch_vccnz RB16_%=
s_mov_b64 exec,s[62:63]
s_add_u32 m0,s57,17440
s_add_u32 s64,s56,2457600
buffer_load_dwordx4 v2,s[36:39],s64 offen lds
s_add_u32 m0,s57,19168
s_nop 0
buffer_load_dwordx4 v3,s[36:39],s64 offen lds
s_mov_b64 exec,-1
RB16_%=:
s_cbranch_vccnz RB17_%=
s_mov_b64 exec,s[62:63]
s_add_u32 m0,s57,20928
s_add_u32 s64,s56,2555904
buffer_load_dwordx4 v2,s[36:39],s64 offen lds
s_add_u32 m0,s57,22656
s_nop 0
buffer_load_dwordx4 v3,s[36:39],s64 offen lds
s_mov_b64 exec,-1
RB17_%=:
s_cbranch_vccnz RB18_%=
s_mov_b64 exec,s[62:63]
s_add_u32 m0,s57,24416
s_add_u32 s64,s56,2654208
buffer_load_dwordx4 v2,s[36:39],s64 offen lds
s_add_u32 m0,s57,26144
s_nop 0
buffer_load_dwordx4 v3,s[36:39],s64 offen lds
s_mov_b64 exec,-1
RB18_%=:
s_cbranch_vccnz RB19_%=
s_mov_b64 exec,s[62:63]
s_add_u32 m0,s57,27904
s_add_u32 s64,s56,2752512
buffer_load_dwordx4 v2,s[36:39],s64 offen lds
s_add_u32 m0,s57,29632
s_nop 0
buffer_load_dwordx4 v3,s[36:39],s64 offen lds
s_mov_b64 exec,-1
RB19_%=:
s_cbranch_vccnz RB20_%=
s_mov_b64 exec,s[62:63]
s_add_u32 m0,s57,31392
s_add_u32 s64,s56,2850816
buffer_load_dwordx4 v2,s[36:39],s64 offen lds
s_add_u32 m0,s57,33120
s_nop 0
buffer_load_dwordx4 v3,s[36:39],s64 offen lds
s_mov_b64 exec,-1
RB20_%=:
s_cbranch_vccnz RB21_%=
s_mov_b64 exec,s[62:63]
s_add_u32 m0,s57,34880
s_add_u32 s64,s56,2949120
buffer_load_dwordx4 v2,s[36:39],s64 offen lds
s_add_u32 m0,s57,36608
s_nop 0
buffer_load_dwordx4 v3,s[36:39],s64 offen lds
s_mov_b64 exec,-1
RB21_%=:
s_cbranch_vccnz RB22_%=
s_mov_b64 exec,s[62:63]
s_add_u32 m0,s57,38368
s_add_u32 s64,s56,3047424
buffer_load_dwordx4 v2,s[36:39],s64 offen lds
s_add_u32 m0,s57,40096
s_nop 0
buffer_load_dwordx4 v3,s[36:39],s64 offen lds
s_mov_b64 exec,-1
RB22_%=:
s_cbranch_vccnz RB23_%=
s_mov_b64 exec,s[62:63]
s_add_u32 m0,s57,41856
s_add_u32 s64,s56,3145728
buffer_load_dwordx4 v2,s[36:39],s64 offen lds
s_add_u32 m0,s57,43584
s_nop 0
buffer_load_dwordx4 v3,s[36:39],s64 offen lds
s_mov_b64 exec,-1
RB23_%=:
s_cbranch_vccnz RB24_%=
s_mov_b64 exec,s[62:63]
s_add_u32 m0,s57,45344
s_add_u32 s64,s56,3244032
buffer_load_dwordx4 v2,s[36:39],s64 offen lds
s_add_u32 m0,s57,47072
s_nop 0
buffer_load_dwordx4 v3,s[36:39],s64 offen lds
s_mov_b64 exec,-1
RB24_%=:
s_cbranch_vccz RB25_%=
s_waitcnt vmcnt(0)
RB25_%=:
s_barrier
s_cbranch_vccz RB26_%=
s_add_u32 m0,s58,0
s_mul_i32 s69,s59,5
s_add_u32 s69,s69,s97
buffer_load_dwordx4 v2,s[44:47],s69 offen lds
s_add_u32 m0,m0,1024
s_add_u32 s69,s69,s53
buffer_load_dwordx4 v3,s[44:47],s69 offen lds
s_add_u32 m0,m0,1024
s_add_u32 s69,s69,s53
buffer_load_dwordx4 v2,s[44:47],s69 offen lds
s_add_u32 m0,m0,s50
s_add_u32 s69,s69,s53
buffer_load_dwordx4 v3,s[44:47],s69 offen lds
RB26_%=:
s_cbranch_vccz RB27_%=
s_add_u32 m0,m0,1024
s_add_u32 s69,s69,s53
buffer_load_dwordx4 v2,s[44:47],s69 offen lds
s_add_u32 m0,m0,1024
s_add_u32 s69,s69,s53
buffer_load_dwordx4 v3,s[44:47],s69 offen lds
s_add_u32 m0,m0,s50
s_add_u32 s69,s69,s53
buffer_load_dwordx4 v2,s[44:47],s69 offen lds
s_add_u32 m0,m0,1024
s_add_u32 s69,s69,s53
buffer_load_dwordx4 v3,s[44:47],s69 offen lds
RB27_%=:
s_cbranch_vccz RB28_%=
s_add_u32 m0,m0,1024
s_add_u32 s69,s69,s53
buffer_load_dwordx4 v2,s[44:47],s69 offen lds
s_add_u32 m0,m0,s50
s_add_u32 s69,s69,s53
buffer_load_dwordx4 v3,s[44:47],s69 offen lds
s_add_u32 m0,m0,1024
s_add_u32 s69,s69,s53
buffer_load_dwordx4 v2,s[44:47],s69 offen lds
s_add_u32 m0,m0,1024
s_add_u32 s69,s69,s53
buffer_load_dwordx4 v3,s[44:47],s69 offen lds
RB28_%=:
s_add_u32 s4,s10,16
s_cmp_ge_u32 s4,20
s_cselect_b32 s7,20,0
s_sub_u32 s4,s4,s7
s_mul_i32 s4,s4,3488
s_add_u32 s5,s10,18
s_cmp_ge_u32 s5,20
s_cselect_b32 s7,20,0
s_sub_u32 s5,s5,s7
s_mul_i32 s5,s5,3488
s_add_u32 s6,s10,0
s_cmp_ge_u32 s6,20
s_cselect_b32 s7,20,0
s_sub_u32 s6,s6,s7
s_mul_i32 s6,s6,3488
v_add_u32 v112,s4,v15
v_add_u32 v116,s5,v15
s_mov_b64 exec,s[8:9]
v_add_u32 v112,s5,v15
v_add_u32 v116,s6,v15
s_mov_b64 exec,-1
v_xor_b32 v113,16,v112
v_xor_b32 v117,16,v116
v_xor_b32 v114,32,v112
v_xor_b32 v118,32,v116
v_xor_b32 v115,48,v112
v_xor_b32 v119,48,v116
s_waitcnt vmcnt(0)
s_barrier
s_setprio 3
ds_read_b128 v[136:139],v116 offset:0
ds_read_b128 v[88:91],v11 offset:768
ds_read_b128 v[140:143],v116 offset:3488
ds_read_b128 v[128:131],v112 offset:0
ds_read_b128 v[132:135],v112 offset:3488
ds_read_b128 v[92:95],v12 offset:0
ds_read_b128 v[152:155],v117 offset:0
ds_read_b128 v[96:99],v11 offset:1920
ds_read_b128 v[156:159],v117 offset:3488
ds_read_b128 v[144:147],v113 offset:0
ds_read_b128 v[148:151],v113 offset:3488
ds_read_b128 v[100:103],v12 offset:1152
s_waitcnt lgkmcnt(10)
v_mfma_f32_16x16x32_bf16 v[160:163],v[88:91],v[136:139],0
s_waitcnt lgkmcnt(9)
v_mfma_f32_16x16x32_bf16 v[184:187],v[88:91],v[140:143],0
s_waitcnt lgkmcnt(6)
ds_read_b128 v[140:143],v116 offset:3552
ds_read_b128 v[88:91],v11 offset:5376
v_smfmac_f32_16x16x64_bf16 v[160:163],v[92:95],v[128:135],v13
s_cbranch_vccz RB29_%=
s_add_u32 m0,s58,12320
s_mul_i32 s69,s59,7
s_add_u32 s69,s69,s97
buffer_load_dwordx4 v2,s[44:47],s69 offen lds
s_add_u32 m0,m0,1024
s_add_u32 s69,s69,s53
buffer_load_dwordx4 v3,s[44:47],s69 offen lds
s_add_u32 m0,m0,1024
s_add_u32 s69,s69,s53
buffer_load_dwordx4 v2,s[44:47],s69 offen lds
s_add_u32 m0,m0,s50
s_add_u32 s69,s69,s53
buffer_load_dwordx4 v3,s[44:47],s69 offen lds
RB29_%=:
ds_read_b128 v[128:131],v112 offset:64
v_smfmac_f32_16x16x64_bf16 v[184:187],v[92:95],v[132:139],v13
s_waitcnt lgkmcnt(7)
ds_read_b128 v[136:139],v116 offset:64
ds_read_b128 v[132:135],v112 offset:3552
v_mfma_f32_16x16x32_bf16 v[164:167],v[96:99],v[152:155],0
s_waitcnt lgkmcnt(8)
ds_read_b128 v[92:95],v12 offset:4608
v_add_f32 v120,v160,v161
v_mul_f32 v124,v160,v160
v_fmac_f32 v124,v161,v161
v_mfma_f32_16x16x32_bf16 v[188:191],v[96:99],v[156:159],0
s_waitcnt lgkmcnt(6)
ds_read_b128 v[156:159],v117 offset:3552
ds_read_b128 v[96:99],v11 offset:6528
v_add_f32 v121,v162,v163
v_mul_f32 v125,v162,v162
v_fmac_f32 v125,v163,v163
v_smfmac_f32_16x16x64_bf16 v[164:167],v[100:103],v[144:151],v13
s_cbranch_vccz RB30_%=
s_add_u32 m0,m0,1024
s_add_u32 s69,s69,s53
buffer_load_dwordx4 v2,s[44:47],s69 offen lds
s_add_u32 m0,m0,1024
s_add_u32 s69,s69,s53
buffer_load_dwordx4 v3,s[44:47],s69 offen lds
s_add_u32 m0,m0,s50
s_add_u32 s69,s69,s53
buffer_load_dwordx4 v2,s[44:47],s69 offen lds
s_add_u32 m0,m0,1024
s_add_u32 s69,s69,s53
buffer_load_dwordx4 v3,s[44:47],s69 offen lds
RB30_%=:
ds_read_b128 v[144:147],v113 offset:64
v_add_f32 v122,v184,v185
v_mul_f32 v126,v184,v184
v_fmac_f32 v126,v185,v185
v_smfmac_f32_16x16x64_bf16 v[188:191],v[100:103],v[148:155],v13
s_waitcnt lgkmcnt(5)
ds_read_b128 v[152:155],v117 offset:64
ds_read_b128 v[148:151],v113 offset:3552
v_add_f32 v123,v186,v187
v_mul_f32 v127,v186,v186
v_fmac_f32 v127,v187,v187
v_mfma_f32_16x16x32_bf16 v[168:171],v[88:91],v[136:139],0
ds_read_b128 v[100:103],v12 offset:5760
v_add_f32 v120,v120,v164
v_fmac_f32 v124,v164,v164
v_add_f32 v120,v120,v165
v_fmac_f32 v124,v165,v165
v_mfma_f32_16x16x32_bf16 v[192:195],v[88:91],v[140:143],0
s_waitcnt lgkmcnt(6)
s_cbranch_vccz RB31_%=
s_add_u32 m0,m0,1024
s_add_u32 s69,s69,s53
buffer_load_dwordx4 v2,s[44:47],s69 offen lds
s_add_u32 m0,m0,s50
s_add_u32 s69,s69,s53
buffer_load_dwordx4 v3,s[44:47],s69 offen lds
s_add_u32 m0,m0,1024
s_add_u32 s69,s69,s53
buffer_load_dwordx4 v2,s[44:47],s69 offen lds
s_add_u32 m0,m0,1024
s_add_u32 s69,s69,s53
buffer_load_dwordx4 v3,s[44:47],s69 offen lds
RB31_%=:
ds_read_b128 v[140:143],v116 offset:3616
ds_read_b128 v[88:91],v11 offset:9984
v_add_f32 v121,v121,v166
v_fmac_f32 v125,v166,v166
v_add_f32 v121,v121,v167
v_fmac_f32 v125,v167,v167
v_smfmac_f32_16x16x64_bf16 v[168:171],v[92:95],v[128:135],v13
ds_read_b128 v[128:131],v112 offset:128
v_add_f32 v122,v122,v188
v_fmac_f32 v126,v188,v188
v_add_f32 v122,v122,v189
v_fmac_f32 v126,v189,v189
v_smfmac_f32_16x16x64_bf16 v[192:195],v[92:95],v[132:139],v13
s_waitcnt lgkmcnt(5)
ds_read_b128 v[136:139],v116 offset:128
ds_read_b128 v[132:135],v112 offset:3616
v_add_f32 v123,v123,v190
v_fmac_f32 v127,v190,v190
v_add_f32 v123,v123,v191
v_fmac_f32 v127,v191,v191
v_mfma_f32_16x16x32_bf16 v[172:175],v[96:99],v[152:155],0
ds_read_b128 v[92:95],v12 offset:9216
v_add_f32 v120,v120,v168
v_fmac_f32 v124,v168,v168
v_add_f32 v120,v120,v169
v_fmac_f32 v124,v169,v169
v_mfma_f32_16x16x32_bf16 v[196:199],v[96:99],v[156:159],0
s_waitcnt lgkmcnt(6)
ds_read_b128 v[156:159],v117 offset:3616
ds_read_b128 v[96:99],v11 offset:11136
v_add_f32 v121,v121,v170
v_fmac_f32 v125,v170,v170
v_add_f32 v121,v121,v171
v_fmac_f32 v125,v171,v171
v_smfmac_f32_16x16x64_bf16 v[172:175],v[100:103],v[144:151],v13
ds_read_b128 v[144:147],v113 offset:128
v_add_f32 v122,v122,v192
v_fmac_f32 v126,v192,v192
v_add_f32 v122,v122,v193
v_fmac_f32 v126,v193,v193
v_smfmac_f32_16x16x64_bf16 v[196:199],v[100:103],v[148:155],v13
s_waitcnt lgkmcnt(5)
ds_read_b128 v[152:155],v117 offset:128
ds_read_b128 v[148:151],v113 offset:3616
v_add_f32 v123,v123,v194
v_fmac_f32 v127,v194,v194
v_add_f32 v123,v123,v195
v_fmac_f32 v127,v195,v195
v_mfma_f32_16x16x32_bf16 v[176:179],v[88:91],v[136:139],0
ds_read_b128 v[100:103],v12 offset:10368
v_add_f32 v120,v120,v172
v_fmac_f32 v124,v172,v172
v_add_f32 v120,v120,v173
v_fmac_f32 v124,v173,v173
v_mfma_f32_16x16x32_bf16 v[200:203],v[88:91],v[140:143],0
s_waitcnt lgkmcnt(6)
ds_read_b128 v[140:143],v118 offset:3488
ds_read_b128 v[88:91],v11 offset:3072
v_add_f32 v121,v121,v174
v_fmac_f32 v125,v174,v174
v_add_f32 v121,v121,v175
v_fmac_f32 v125,v175,v175
v_smfmac_f32_16x16x64_bf16 v[176:179],v[92:95],v[128:135],v13
ds_read_b128 v[128:131],v114 offset:0
v_add_f32 v122,v122,v196
v_fmac_f32 v126,v196,v196
v_add_f32 v122,v122,v197
v_fmac_f32 v126,v197,v197
v_smfmac_f32_16x16x64_bf16 v[200:203],v[92:95],v[132:139],v13
s_waitcnt lgkmcnt(5)
ds_read_b128 v[136:139],v118 offset:0
ds_read_b128 v[132:135],v114 offset:3488
v_add_f32 v123,v123,v198
v_fmac_f32 v127,v198,v198
v_add_f32 v123,v123,v199
v_fmac_f32 v127,v199,v199
v_mfma_f32_16x16x32_bf16 v[180:183],v[96:99],v[152:155],0
ds_read_b128 v[92:95],v12 offset:2304
v_add_f32 v120,v120,v176
v_fmac_f32 v124,v176,v176
v_add_f32 v120,v120,v177
v_fmac_f32 v124,v177,v177
v_mfma_f32_16x16x32_bf16 v[204:207],v[96:99],v[156:159],0
s_waitcnt lgkmcnt(6)
ds_read_b128 v[156:159],v119 offset:3488
ds_read_b128 v[96:99],v11 offset:4224
v_add_f32 v121,v121,v178
v_fmac_f32 v125,v178,v178
v_add_f32 v121,v121,v179
v_fmac_f32 v125,v179,v179
v_smfmac_f32_16x16x64_bf16 v[180:183],v[100:103],v[144:151],v13
ds_read_b128 v[144:147],v115 offset:0
v_add_f32 v122,v122,v200
v_fmac_f32 v126,v200,v200
v_add_f32 v122,v122,v201
v_fmac_f32 v126,v201,v201
v_smfmac_f32_16x16x64_bf16 v[204:207],v[100:103],v[148:155],v13
s_waitcnt lgkmcnt(5)
ds_read_b128 v[152:155],v119 offset:0
ds_read_b128 v[148:151],v115 offset:3488
v_add_f32 v123,v123,v202
v_fmac_f32 v127,v202,v202
v_add_f32 v123,v123,v203
v_fmac_f32 v127,v203,v203
v_mfma_f32_16x16x32_bf16 v[208:211],v[88:91],v[136:139],0
ds_read_b128 v[100:103],v12 offset:3456
v_add_f32 v120,v120,v180
v_fmac_f32 v124,v180,v180
v_add_f32 v120,v120,v181
v_fmac_f32 v124,v181,v181
v_mfma_f32_16x16x32_bf16 v[232:235],v[88:91],v[140:143],0
s_waitcnt lgkmcnt(6)
ds_read_b128 v[140:143],v118 offset:3552
ds_read_b128 v[88:91],v11 offset:7680
v_add_f32 v121,v121,v182
v_fmac_f32 v125,v182,v182
v_add_f32 v121,v121,v183
v_fmac_f32 v125,v183,v183
v_smfmac_f32_16x16x64_bf16 v[208:211],v[92:95],v[128:135],v13
ds_read_b128 v[128:131],v114 offset:64
v_add_f32 v122,v122,v204
v_fmac_f32 v126,v204,v204
v_add_f32 v122,v122,v205
v_fmac_f32 v126,v205,v205
v_smfmac_f32_16x16x64_bf16 v[232:235],v[92:95],v[132:139],v13
s_waitcnt lgkmcnt(5)
ds_read_b128 v[136:139],v118 offset:64
ds_read_b128 v[132:135],v114 offset:3552
v_add_f32 v123,v123,v206
v_fmac_f32 v127,v206,v206
v_add_f32 v123,v123,v207
v_fmac_f32 v127,v207,v207
v_mfma_f32_16x16x32_bf16 v[212:215],v[96:99],v[152:155],0
ds_read_b128 v[92:95],v12 offset:6912
v_add_f32 v120,v120,v208
v_fmac_f32 v124,v208,v208
v_add_f32 v120,v120,v209
v_fmac_f32 v124,v209,v209
v_mfma_f32_16x16x32_bf16 v[236:239],v[96:99],v[156:159],0
s_waitcnt lgkmcnt(6)
s_setprio 2
ds_read_b128 v[156:159],v119 offset:3552
ds_read_b128 v[96:99],v11 offset:8832
v_add_f32 v121,v121,v210
v_fmac_f32 v125,v210,v210
v_add_f32 v121,v121,v211
v_fmac_f32 v125,v211,v211
v_smfmac_f32_16x16x64_bf16 v[212:215],v[100:103],v[144:151],v13
ds_read_b128 v[144:147],v115 offset:64
v_add_f32 v122,v122,v232
v_fmac_f32 v126,v232,v232
v_add_f32 v122,v122,v233
v_fmac_f32 v126,v233,v233
v_smfmac_f32_16x16x64_bf16 v[236:239],v[100:103],v[148:155],v13
s_waitcnt lgkmcnt(5)
ds_read_b128 v[152:155],v119 offset:64
ds_read_b128 v[148:151],v115 offset:3552
v_add_f32 v123,v123,v234
v_fmac_f32 v127,v234,v234
v_add_f32 v123,v123,v235
v_fmac_f32 v127,v235,v235
v_mfma_f32_16x16x32_bf16 v[216:219],v[88:91],v[136:139],0
ds_read_b128 v[100:103],v12 offset:8064
v_add_f32 v120,v120,v212
v_fmac_f32 v124,v212,v212
v_add_f32 v120,v120,v213
v_fmac_f32 v124,v213,v213
v_add_f32 v121,v121,v214
v_mfma_f32_16x16x32_bf16 v[240:243],v[88:91],v[140:143],0
s_waitcnt lgkmcnt(6)
ds_read_b128 v[140:143],v118 offset:3616
ds_read_b128 v[88:91],v11 offset:12288
v_fmac_f32 v125,v214,v214
v_add_f32 v121,v121,v215
v_fmac_f32 v125,v215,v215
v_add_f32 v122,v122,v236
v_smfmac_f32_16x16x64_bf16 v[216:219],v[92:95],v[128:135],v13
ds_read_b128 v[128:131],v114 offset:128
v_fmac_f32 v126,v236,v236
v_add_f32 v122,v122,v237
v_fmac_f32 v126,v237,v237
v_add_f32 v123,v123,v238
v_fmac_f32 v127,v238,v238
v_smfmac_f32_16x16x64_bf16 v[240:243],v[92:95],v[132:139],v13
s_waitcnt lgkmcnt(5)
ds_read_b128 v[136:139],v118 offset:128
ds_read_b128 v[132:135],v114 offset:3616
v_add_f32 v123,v123,v239
v_fmac_f32 v127,v239,v239
v_add_u32 v112,v112,v8
v_add_u32 v116,v116,v8
v_mfma_f32_16x16x32_bf16 v[220:223],v[96:99],v[152:155],0
ds_read_b128 v[92:95],v12 offset:11520
ds_read_b128 v[104:107],v14 offset:0
v_add_f32 v120,v120,v216
v_fmac_f32 v124,v216,v216
v_add_f32 v120,v120,v217
v_fmac_f32 v124,v217,v217
v_mfma_f32_16x16x32_bf16 v[244:247],v[96:99],v[156:159],0
s_waitcnt lgkmcnt(7)
ds_read_b128 v[108:111],v7 offset:3584
ds_read_b128 v[156:159],v119 offset:3616
v_add_f32 v121,v121,v218
v_fmac_f32 v125,v218,v218
v_add_f32 v121,v121,v219
v_fmac_f32 v125,v219,v219
v_smfmac_f32_16x16x64_bf16 v[220:223],v[100:103],v[144:151],v13
ds_read_b128 v[96:99],v11 offset:13440
ds_read_b128 v[144:147],v115 offset:128
v_add_f32 v122,v122,v240
v_fmac_f32 v126,v240,v240
v_add_f32 v122,v122,v241
v_fmac_f32 v126,v241,v241
v_smfmac_f32_16x16x64_bf16 v[244:247],v[100:103],v[148:155],v13
s_waitcnt lgkmcnt(7)
ds_read_b128 v[152:155],v119 offset:128
ds_read_b128 v[148:151],v115 offset:3616
v_add_f32 v123,v123,v242
v_fmac_f32 v127,v242,v242
v_add_f32 v123,v123,v243
v_fmac_f32 v127,v243,v243
v_mfma_f32_16x16x32_bf16 v[224:227],v[88:91],v[136:139],0
ds_read_b128 v[100:103],v12 offset:12672
v_add_f32 v120,v120,v220
v_fmac_f32 v124,v220,v220
v_add_f32 v120,v120,v221
v_fmac_f32 v124,v221,v221
v_mfma_f32_16x16x32_bf16 v[248:251],v[88:91],v[140:143],0
s_waitcnt lgkmcnt(8)
s_setprio 1
ds_read_b128 v[140:143],v116 offset:192
ds_read_b128 v[88:91],v112 offset:3616
v_add_f32 v121,v121,v222
v_fmac_f32 v125,v222,v222
v_add_f32 v121,v121,v223
v_fmac_f32 v125,v223,v223
v_smfmac_f32_16x16x64_bf16 v[224:227],v[92:95],v[128:135],v13
ds_read_b128 v[128:131],v112 offset:3488
v_add_f32 v122,v122,v244
v_fmac_f32 v126,v244,v244
v_add_f32 v122,v122,v245
v_fmac_f32 v126,v245,v245
v_smfmac_f32_16x16x64_bf16 v[248:251],v[92:95],v[132:139],v13
s_waitcnt lgkmcnt(5)
ds_read_b128 v[136:139],v116 offset:0
ds_read_b128 v[132:135],v112 offset:3680
v_add_f32 v123,v123,v246
v_fmac_f32 v127,v246,v246
v_add_f32 v123,v123,v247
v_fmac_f32 v127,v247,v247
v_mfma_f32_16x16x32_bf16 v[228:231],v[96:99],v[152:155],0
ds_read_b128 v[92:95],v112 offset:3808
v_add_f32 v120,v120,v224
v_fmac_f32 v124,v224,v224
v_add_f32 v120,v120,v225
v_fmac_f32 v124,v225,v225
v_mfma_f32_16x16x32_bf16 v[252:255],v[96:99],v[156:159],0
s_waitcnt lgkmcnt(6)
ds_read_b128 v[156:159],v116 offset:256
ds_read_b128 v[96:99],v116 offset:128
v_add_f32 v121,v121,v226
v_fmac_f32 v125,v226,v226
v_add_f32 v121,v121,v227
v_fmac_f32 v125,v227,v227
v_smfmac_f32_16x16x64_bf16 v[228:231],v[100:103],v[144:151],v13
ds_read_b128 v[144:147],v112 offset:3552
v_add_f32 v122,v122,v248
v_fmac_f32 v126,v248,v248
v_add_f32 v122,v122,v249
v_fmac_f32 v126,v249,v249
v_smfmac_f32_16x16x64_bf16 v[252:255],v[100:103],v[148:155],v13
s_setprio 0
ds_read_b128 v[152:155],v116 offset:64
ds_read_b128 v[148:151],v112 offset:3744
v_add_f32 v123,v123,v250
v_fmac_f32 v127,v250,v250
v_add_f32 v123,v123,v251
v_fmac_f32 v127,v251,v251
ds_read_b128 v[100:103],v116 offset:320
v_add_f32 v120,v120,v228
v_fmac_f32 v124,v228,v228
v_add_f32 v120,v120,v229
v_fmac_f32 v124,v229,v229
v_add_f32 v121,v121,v230
v_fmac_f32 v125,v230,v230
v_add_f32 v121,v121,v231
v_fmac_f32 v125,v231,v231
s_waitcnt lgkmcnt(0)
s_mov_b32 s86,0xbe801d5a
s_waitcnt vmcnt(0)
s_barrier
s_setprio 0
v_add_f32 v122,v122,v252
v_fmac_f32 v126,v252,v252
v_add_f32 v122,v122,v253
v_fmac_f32 v126,v253,v253
v_add_f32 v123,v123,v254
s_cbranch_vccz RB32_%=
s_add_u32 m0,s58,0
s_mul_i32 s69,s59,9
s_add_u32 s69,s69,s97
buffer_load_dwordx4 v2,s[44:47],s69 offen lds
s_add_u32 m0,m0,1024
s_add_u32 s69,s69,s53
buffer_load_dwordx4 v3,s[44:47],s69 offen lds
s_add_u32 m0,m0,1024
s_add_u32 s69,s69,s53
buffer_load_dwordx4 v2,s[44:47],s69 offen lds
s_add_u32 m0,m0,s50
s_add_u32 s69,s69,s53
buffer_load_dwordx4 v3,s[44:47],s69 offen lds
RB32_%=:
v_fmac_f32 v127,v254,v254
v_add_f32 v123,v123,v255
v_fmac_f32 v127,v255,v255
s_setprio 3
ds_read_b128 v[56:59],v14 offset:256
ds_read_b128 v[60:63],v7 offset:3616
s_setprio 0
v_permlane32_swap_b32 v120,v122
v_permlane32_swap_b32 v121,v123
v_permlane32_swap_b32 v124,v126
v_permlane32_swap_b32 v125,v127
v_add_f32 v120,v120,v122
v_add_f32 v121,v121,v123
v_add_f32 v124,v124,v126
v_add_f32 v125,v125,v127
s_cbranch_vccz RB33_%=
s_add_u32 m0,m0,1024
s_add_u32 s69,s69,s53
buffer_load_dwordx4 v2,s[44:47],s69 offen lds
s_add_u32 m0,m0,1024
s_add_u32 s69,s69,s53
buffer_load_dwordx4 v3,s[44:47],s69 offen lds
s_add_u32 m0,m0,s50
s_add_u32 s69,s69,s53
buffer_load_dwordx4 v2,s[44:47],s69 offen lds
s_add_u32 m0,m0,1024
s_add_u32 s69,s69,s53
buffer_load_dwordx4 v3,s[44:47],s69 offen lds
RB33_%=:
v_permlane16_swap_b32 v120,v121
v_permlane16_swap_b32 v124,v125
v_add_f32 v113,v120,v121
v_add_f32 v114,v124,v125
v_mul_f32 v113,0x3c2aaaab,v113
v_mul_f32 v114,0x3c2aaaab,v114
v_mul_f32 v115,0x5e000000,v114
v_fma_f32 v114,-v113,v113,v114
v_pk_maximum3_f16 v1,v1,v115,v115 op_sel:[0,1,1] op_sel_hi:[1,1,1]
v_max_f32 v114,0,v114
v_add_f32 v114,0x3727c5ac,v114
v_rsq_f32 v114,v114
s_cbranch_vccz RB34_%=
s_add_u32 m0,m0,1024
s_add_u32 s69,s69,s53
buffer_load_dwordx4 v2,s[44:47],s69 offen lds
s_add_u32 m0,m0,s50
s_add_u32 s69,s69,s53
buffer_load_dwordx4 v3,s[44:47],s69 offen lds
s_add_u32 m0,m0,1024
s_add_u32 s69,s69,s53
buffer_load_dwordx4 v2,s[44:47],s69 offen lds
s_add_u32 m0,m0,1024
s_add_u32 s69,s69,s53
buffer_load_dwordx4 v3,s[44:47],s69 offen lds
RB34_%=:
v_mul_f32 v120,0x8befe000,v114
v_mul_f32 v121,0x8befe000,v114
v_mul_f32 v124,-v113,v120
v_mul_f32 v125,-v113,v120
v_permlane16_swap_b32 v120,v121
s_nop 0
v_permlane16_swap_b32 v124,v125
v_mov_b32 v122,v120
v_mov_b32 v123,v121
v_mov_b32 v126,v124
v_mov_b32 v127,v125
v_permlane32_swap_b32 v120,v122
v_permlane32_swap_b32 v124,v126
v_permlane32_swap_b32 v121,v123
v_permlane32_swap_b32 v125,v127
v_fma_f32 v160,v160,v120,v124
s_setprio 3
ds_read_b64_tr_b16 v[64:65],v4 offset:12832
s_setprio 0
v_fma_f32 v161,v161,v120,v124
s_setprio 3
ds_read_b64_tr_b16 v[66:67],v4 offset:14880
s_setprio 0
v_fma_f32 v162,v162,v121,v125
s_setprio 3
ds_read_b128 v[112:115],v7 offset:1152
s_setprio 0
v_fma_f32 v163,v163,v121,v125
s_setprio 3
ds_read_b64_tr_b16 v[68:69],v5 offset:12832
s_setprio 0
v_fma_f32 v164,v164,v120,v124
s_setprio 3
ds_read_b64_tr_b16 v[70:71],v5 offset:14880
s_setprio 0
v_fma_f32 v165,v165,v120,v124
s_setprio 3
ds_read_b128 v[116:119],v7 offset:1184
s_setprio 0
v_fma_f32 v166,v166,v121,v125
s_setprio 3
ds_read_b64_tr_b16 v[72:73],v4 offset:16928
s_setprio 0
v_fma_f32 v167,v167,v121,v125
s_setprio 3
ds_read_b64_tr_b16 v[74:75],v4 offset:18976
s_setprio 0
v_fma_f32 v208,v208,v120,v124
s_setprio 3
ds_read_b64_tr_b16 v[76:77],v5 offset:16928
s_setprio 0
v_fma_f32 v209,v209,v120,v124
s_setprio 3
ds_read_b64_tr_b16 v[78:79],v5 offset:18976
s_setprio 0
v_fma_f32 v210,v210,v121,v125
s_setprio 3
ds_read_b64_tr_b16 v[80:81],v4 offset:21024
s_setprio 0
v_fma_f32 v211,v211,v121,v125
s_setprio 3
ds_read_b64_tr_b16 v[82:83],v4 offset:23072
s_setprio 0
v_fma_f32 v212,v212,v120,v124
s_setprio 3
ds_read_b64_tr_b16 v[84:85],v5 offset:21024
s_setprio 0
v_fma_f32 v213,v213,v120,v124
s_setprio 3
ds_read_b64_tr_b16 v[86:87],v5 offset:23072
s_setprio 0
v_fma_f32 v214,v214,v121,v125
v_fma_f32 v215,v215,v121,v125
v_cvt_pk_bf16_f32 v16,v160,v161
v_cvt_pk_bf16_f32 v20,v162,v163
v_cvt_pk_bf16_f32 v17,v164,v165
v_cvt_pk_bf16_f32 v21,v166,v167
v_cvt_pk_bf16_f32 v18,v208,v209
v_cvt_pk_bf16_f32 v22,v210,v211
v_cvt_pk_bf16_f32 v19,v212,v213
v_cvt_pk_bf16_f32 v23,v214,v215
v_fma_f32 v168,v168,v120,v124
v_fma_f32 v169,v169,v120,v124
v_mfma_f32_16x16x32_bf16 v[160:163],v[104:107],v[128:131],v[108:111]
v_fma_f32 v170,v170,v121,v125
v_fma_f32 v171,v171,v121,v125
v_fma_f32 v172,v172,v120,v124
v_fma_f32 v173,v173,v120,v124
v_fma_f32 v174,v174,v121,v125
v_fma_f32 v175,v175,v121,v125
v_mfma_f32_16x16x32_bf16 v[164:167],v[104:107],v[132:135],v[108:111]
v_fma_f32 v216,v216,v120,v124
v_fma_f32 v217,v217,v120,v124
v_fma_f32 v218,v218,v121,v125
v_fma_f32 v219,v219,v121,v125
v_fma_f32 v220,v220,v120,v124
v_fma_f32 v221,v221,v120,v124
v_mfma_f32_16x16x32_bf16 v[208:211],v[104:107],v[136:139],v[108:111]
v_fma_f32 v222,v222,v121,v125
v_fma_f32 v223,v223,v121,v125
v_cvt_pk_bf16_f32 v24,v168,v169
v_cvt_pk_bf16_f32 v28,v170,v171
v_cvt_pk_bf16_f32 v25,v172,v173
v_cvt_pk_bf16_f32 v29,v174,v175
s_setprio 3
v_mfma_f32_16x16x32_bf16 v[212:215],v[104:107],v[140:143],v[108:111]
ds_read_b128 v[104:107],v14 offset:512
s_setprio 0
v_cvt_pk_bf16_f32 v26,v216,v217
v_cvt_pk_bf16_f32 v30,v218,v219
v_cvt_pk_bf16_f32 v27,v220,v221
v_cvt_pk_bf16_f32 v31,v222,v223
v_fma_f32 v176,v176,v120,v124
v_fma_f32 v177,v177,v120,v124
s_waitcnt lgkmcnt(15)
s_setprio 3
v_mfma_f32_16x16x32_bf16 v[168:171],v[56:59],v[128:131],v[60:63]
ds_read_b128 v[108:111],v7 offset:3712
s_setprio 0
v_fma_f32 v178,v178,v121,v125
v_fma_f32 v179,v179,v121,v125
v_fma_f32 v180,v180,v120,v124
v_fma_f32 v181,v181,v120,v124
v_fma_f32 v182,v182,v121,v125
v_fma_f32 v183,v183,v121,v125
v_mfma_f32_16x16x32_bf16 v[172:175],v[56:59],v[132:135],v[60:63]
v_fma_f32 v224,v224,v120,v124
v_fma_f32 v225,v225,v120,v124
v_fma_f32 v226,v226,v121,v125
v_fma_f32 v227,v227,v121,v125
v_fma_f32 v228,v228,v120,v124
v_fma_f32 v229,v229,v120,v124
v_mfma_f32_16x16x32_bf16 v[216:219],v[56:59],v[136:139],v[60:63]
v_fma_f32 v230,v230,v121,v125
v_fma_f32 v231,v231,v121,v125
v_cvt_pk_bf16_f32 v32,v176,v177
v_cvt_pk_bf16_f32 v36,v178,v179
v_cvt_pk_bf16_f32 v33,v180,v181
v_cvt_pk_bf16_f32 v37,v182,v183
s_setprio 3
v_mfma_f32_16x16x32_bf16 v[220:223],v[56:59],v[140:143],v[60:63]
ds_read_b128 v[56:59],v14 offset:768
s_setprio 0
v_cvt_pk_bf16_f32 v34,v224,v225
v_cvt_pk_bf16_f32 v38,v226,v227
v_cvt_pk_bf16_f32 v35,v228,v229
v_cvt_pk_bf16_f32 v39,v230,v231
v_fma_f32 v184,v184,v122,v126
s_waitcnt lgkmcnt(1)
s_setprio 3
v_mfma_f32_16x16x32_bf16 v[176:179],v[104:107],v[144:147],v[108:111]
ds_read_b128 v[60:63],v7 offset:3744
s_setprio 0
v_fma_f32 v185,v185,v122,v126
v_fma_f32 v186,v186,v123,v127
v_fma_f32 v187,v187,v123,v127
v_mfma_f32_16x16x32_bf16 v[128:131],v[64:67],v[16:19],v[112:115]
v_fma_f32 v188,v188,v122,v126
v_fma_f32 v189,v189,v122,v126
v_fma_f32 v190,v190,v123,v127
v_mfma_f32_16x16x32_bf16 v[180:183],v[104:107],v[148:151],v[108:111]
v_fma_f32 v191,v191,v123,v127
v_fma_f32 v232,v232,v122,v126
v_fma_f32 v233,v233,v122,v126
v_mfma_f32_16x16x32_bf16 v[136:139],v[64:67],v[20:23],v[112:115]
v_fma_f32 v234,v234,v123,v127
v_fma_f32 v235,v235,v123,v127
v_fma_f32 v236,v236,v122,v126
v_mfma_f32_16x16x32_bf16 v[224:227],v[104:107],v[152:155],v[108:111]
v_fma_f32 v237,v237,v122,v126
v_fma_f32 v238,v238,v123,v127
v_fma_f32 v239,v239,v123,v127
v_mfma_f32_16x16x32_bf16 v[140:143],v[68:71],v[20:23],v[116:119]
v_cvt_pk_bf16_f32 v40,v184,v185
v_cvt_pk_bf16_f32 v44,v186,v187
v_cvt_pk_bf16_f32 v41,v188,v189
s_setprio 3
v_mfma_f32_16x16x32_bf16 v[228:231],v[104:107],v[156:159],v[108:111]
ds_read_b128 v[104:107],v14 offset:1024
s_setprio 0
v_cvt_pk_bf16_f32 v45,v190,v191
v_cvt_pk_bf16_f32 v42,v232,v233
v_cvt_pk_bf16_f32 v46,v234,v235
s_setprio 3
v_mfma_f32_16x16x32_bf16 v[132:135],v[68:71],v[16:19],v[116:119]
ds_read_b128 v[108:111],v7 offset:3840
s_setprio 0
v_cvt_pk_bf16_f32 v43,v236,v237
v_cvt_pk_bf16_f32 v47,v238,v239
v_fma_f32 v192,v192,v122,v126
s_waitcnt lgkmcnt(2)
v_mfma_f32_16x16x32_bf16 v[184:187],v[56:59],v[144:147],v[60:63]
v_fma_f32 v193,v193,v122,v126
v_fma_f32 v194,v194,v123,v127
v_mfma_f32_16x16x32_bf16 v[128:131],v[72:75],v[24:27],v[128:131]
v_fma_f32 v195,v195,v123,v127
v_fma_f32 v196,v196,v122,v126
v_mfma_f32_16x16x32_bf16 v[188:191],v[56:59],v[148:151],v[60:63]
v_fma_f32 v197,v197,v122,v126
v_fma_f32 v198,v198,v123,v127
v_mfma_f32_16x16x32_bf16 v[136:139],v[72:75],v[28:31],v[136:139]
v_fma_f32 v199,v199,v123,v127
v_fma_f32 v240,v240,v122,v126
v_mfma_f32_16x16x32_bf16 v[232:235],v[56:59],v[152:155],v[60:63]
v_fma_f32 v241,v241,v122,v126
v_fma_f32 v242,v242,v123,v127
v_mfma_f32_16x16x32_bf16 v[140:143],v[76:79],v[28:31],v[140:143]
v_fma_f32 v243,v243,v123,v127
v_fma_f32 v244,v244,v122,v126
s_setprio 3
v_mfma_f32_16x16x32_bf16 v[236:239],v[56:59],v[156:159],v[60:63]
ds_read_b128 v[144:147],v14 offset:1280
s_setprio 0
v_fma_f32 v245,v245,v122,v126
v_fma_f32 v246,v246,v123,v127
s_setprio 3
v_mfma_f32_16x16x32_bf16 v[132:135],v[76:79],v[24:27],v[132:135]
ds_read_b128 v[148:151],v7 offset:3872
s_setprio 0
v_fma_f32 v247,v247,v123,v127
v_cvt_pk_bf16_f32 v48,v192,v193
v_cvt_pk_bf16_f32 v52,v194,v195
v_cvt_pk_bf16_f32 v49,v196,v197
v_cvt_pk_bf16_f32 v53,v198,v199
v_cvt_pk_bf16_f32 v50,v240,v241
v_cvt_pk_bf16_f32 v54,v242,v243
v_cvt_pk_bf16_f32 v51,v244,v245
v_cvt_pk_bf16_f32 v55,v246,v247
v_fma_f32 v200,v200,v122,v126
s_waitcnt lgkmcnt(2)
v_mfma_f32_16x16x32_bf16 v[192:195],v[104:107],v[88:91],v[108:111]
v_fma_f32 v201,v201,v122,v126
v_fma_f32 v202,v202,v123,v127
v_fma_f32 v203,v203,v123,v127
v_mfma_f32_16x16x32_bf16 v[128:131],v[80:83],v[32:35],v[128:131]
v_fma_f32 v204,v204,v122,v126
v_fma_f32 v205,v205,v122,v126
v_fma_f32 v206,v206,v123,v127
v_mfma_f32_16x16x32_bf16 v[196:199],v[104:107],v[92:95],v[108:111]
v_fma_f32 v207,v207,v123,v127
v_fma_f32 v248,v248,v122,v126
v_fma_f32 v249,v249,v122,v126
v_mfma_f32_16x16x32_bf16 v[136:139],v[80:83],v[36:39],v[136:139]
v_fma_f32 v250,v250,v123,v127
v_fma_f32 v251,v251,v123,v127
v_fma_f32 v252,v252,v122,v126
v_mfma_f32_16x16x32_bf16 v[240:243],v[104:107],v[96:99],v[108:111]
v_fma_f32 v253,v253,v122,v126
v_fma_f32 v254,v254,v123,v127
v_fma_f32 v255,v255,v123,v127
v_mfma_f32_16x16x32_bf16 v[140:143],v[84:87],v[36:39],v[140:143]
v_cvt_pk_bf16_f32 v56,v200,v201
v_cvt_pk_bf16_f32 v60,v202,v203
v_cvt_pk_bf16_f32 v57,v204,v205
v_mfma_f32_16x16x32_bf16 v[244:247],v[104:107],v[100:103],v[108:111]
v_cvt_pk_bf16_f32 v61,v206,v207
v_cvt_pk_bf16_f32 v58,v248,v249
v_cvt_pk_bf16_f32 v62,v250,v251
v_mfma_f32_16x16x32_bf16 v[132:135],v[84:87],v[32:35],v[132:135]
v_cvt_pk_bf16_f32 v59,v252,v253
v_cvt_pk_bf16_f32 v63,v254,v255
s_waitcnt lgkmcnt(0)
v_mfma_f32_16x16x32_bf16 v[200:203],v[144:147],v[88:91],v[148:151]
s_waitcnt vmcnt(0)
s_barrier
s_setprio 3
ds_read_b64_tr_b16 v[104:105],v6 offset:128
v_lshlrev_b32_sdwa v128,1,v128 dst_sel:DWORD dst_unused:UNUSED_PAD src0_sel:DWORD src1_sel:WORD_1
v_lshlrev_b32_sdwa v130,1,v130 dst_sel:DWORD dst_unused:UNUSED_PAD src0_sel:DWORD src1_sel:WORD_1
v_lshlrev_b32_sdwa v129,1,v129 dst_sel:DWORD dst_unused:UNUSED_PAD src0_sel:DWORD src1_sel:WORD_1
v_mfma_f32_16x16x32_bf16 v[204:207],v[144:147],v[92:95],v[148:151]
ds_read_u16 v128,v128
ds_read_b64_tr_b16 v[106:107],v6 offset:1664
v_lshlrev_b32_sdwa v131,1,v131 dst_sel:DWORD dst_unused:UNUSED_PAD src0_sel:DWORD src1_sel:WORD_1
v_fma_mixlo_f16 v120,v140,s99,0
v_fma_mixlo_f16 v122,v141,s99,0
v_mfma_f32_16x16x32_bf16 v[248:251],v[144:147],v[96:99],v[148:151]
ds_read_u16_d16_hi v130,v130
ds_read_b64_tr_b16 v[108:109],v6 offset:136
v_lshlrev_b32_sdwa v132,1,v132 dst_sel:DWORD dst_unused:UNUSED_PAD src0_sel:DWORD src1_sel:WORD_1
v_lshlrev_b32_sdwa v134,1,v134 dst_sel:DWORD dst_unused:UNUSED_PAD src0_sel:DWORD src1_sel:WORD_1
v_mfma_f32_16x16x32_bf16 v[252:255],v[144:147],v[100:103],v[148:151]
ds_read_u16 v129,v129
ds_read_b64_tr_b16 v[110:111],v6 offset:1672
v_fma_mixhi_f16 v120,v142,s99,0
v_fma_mixhi_f16 v122,v143,s99,0
v_lshlrev_b32_sdwa v133,1,v133 dst_sel:DWORD dst_unused:UNUSED_PAD src0_sel:DWORD src1_sel:WORD_1
v_pk_fma_f16 v121,v120,v120,s54
v_mfma_f32_16x16x32_bf16 v[144:147],v[64:67],v[40:43],v[112:115]
ds_read_u16_d16_hi v131,v131
ds_read_b64_tr_b16 v[88:89],v6 offset:0
v_lshlrev_b32_sdwa v135,1,v135 dst_sel:DWORD dst_unused:UNUSED_PAD src0_sel:DWORD src1_sel:WORD_1
v_pk_fma_f16 v123,v122,v122,s54
v_pk_mul_f16 v121,v120,v121
v_mfma_f32_16x16x32_bf16 v[152:155],v[64:67],v[44:47],v[112:115]
ds_read_u16 v132,v132
ds_read_b64_tr_b16 v[90:91],v6 offset:1536
v_lshlrev_b32_sdwa v136,1,v136 dst_sel:DWORD dst_unused:UNUSED_PAD src0_sel:DWORD src1_sel:WORD_1
v_pk_mul_f16 v123,v122,v123
v_mfma_f32_16x16x32_bf16 v[156:159],v[68:71],v[44:47],v[116:119]
ds_read_u16_d16_hi v134,v134
ds_read_b64_tr_b16 v[92:93],v6 offset:8
v_lshlrev_b32_sdwa v138,1,v138 dst_sel:DWORD dst_unused:UNUSED_PAD src0_sel:DWORD src1_sel:WORD_1
v_exp_f16 v120,v121
v_mfma_f32_16x16x32_bf16 v[148:151],v[68:71],v[40:43],v[116:119]
ds_read_u16 v133,v133
ds_read_b64_tr_b16 v[94:95],v6 offset:1544
v_lshlrev_b32_sdwa v137,1,v137 dst_sel:DWORD dst_unused:UNUSED_PAD src0_sel:DWORD src1_sel:WORD_1
v_lshlrev_b32_sdwa v139,1,v139 dst_sel:DWORD dst_unused:UNUSED_PAD src0_sel:DWORD src1_sel:WORD_1
v_exp_f16 v122,v123
v_mfma_f32_16x16x32_bf16 v[144:147],v[72:75],v[48:51],v[144:147]
ds_read_u16_d16_hi v135,v135
ds_read_b64_tr_b16 v[96:97],v6 offset:64
v_exp_f16_sdwa v122,v123 dst_sel:WORD_1 dst_unused:UNUSED_PRESERVE src0_sel:WORD_1
v_mfma_f32_16x16x32_bf16 v[152:155],v[72:75],v[52:55],v[152:155]
ds_read_u16 v136,v136
ds_read_b64_tr_b16 v[98:99],v6 offset:1600
v_exp_f16_sdwa v120,v121 dst_sel:WORD_1 dst_unused:UNUSED_PRESERVE src0_sel:WORD_1
v_mfma_f32_16x16x32_bf16 v[156:159],v[76:79],v[52:55],v[156:159]
ds_read_u16_d16_hi v138,v138
ds_read_b64_tr_b16 v[100:101],v6 offset:72
v_pk_fma_f16 v120,v120,s55,s55
v_pk_fma_f16 v122,v122,s55,s55
v_rcp_f16 v121,v120
v_mfma_f32_16x16x32_bf16 v[148:151],v[76:79],v[48:51],v[148:151]
ds_read_u16 v137,v137
ds_read_b64_tr_b16 v[102:103],v6 offset:1608
v_rcp_f16 v123,v122
v_mfma_f32_16x16x32_bf16 v[144:147],v[80:83],v[56:59],v[144:147]
ds_read_u16_d16_hi v139,v139
ds_read_b64_tr_b16 v[64:65],v4 offset:0
v_rcp_f16_sdwa v121,v120 dst_sel:WORD_1 dst_unused:UNUSED_PRESERVE src0_sel:WORD_1
v_mfma_f32_16x16x32_bf16 v[152:155],v[80:83],v[60:63],v[152:155]
ds_read_b64_tr_b16 v[66:67],v4 offset:2048
v_rcp_f16_sdwa v123,v122 dst_sel:WORD_1 dst_unused:UNUSED_PRESERVE src0_sel:WORD_1
v_fma_mix_f32 v142,v142,v121,0 op_sel:[0,1,0] op_sel_hi:[0,1,0]
v_mfma_f32_16x16x32_bf16 v[156:159],v[84:87],v[60:63],v[156:159]
ds_read_b128 v[112:115],v7 offset:1280
v_fma_mix_f32 v140,v140,v121,0 op_sel_hi:[0,1,0]
s_waitcnt lgkmcnt(15)
v_lshl_add_u64 v[128:129],v[128:129],0,v[130:131]
s_waitcnt lgkmcnt(11)
v_lshl_add_u64 v[130:131],v[132:133],0,v[134:135]
v_mfma_f32_16x16x32_bf16 v[148:151],v[84:87],v[56:59],v[148:151]
ds_read_b64_tr_b16 v[68:69],v5 offset:0
v_fma_mix_f32 v141,v141,v123,0 op_sel_hi:[0,1,0]
v_fma_mix_f32 v143,v143,v123,0 op_sel:[0,1,0] op_sel_hi:[0,1,0]
v_lshlrev_b32_sdwa v144,1,v144 dst_sel:DWORD dst_unused:UNUSED_PAD src0_sel:DWORD src1_sel:WORD_1
v_lshlrev_b32_sdwa v146,1,v146 dst_sel:DWORD dst_unused:UNUSED_PAD src0_sel:DWORD src1_sel:WORD_1
v_mfma_f32_16x16x32_bf16 v[160:163],v[88:91],v[128:131],v[160:163]
ds_read_u16 v144,v144
v_lshlrev_b32_sdwa v145,1,v145 dst_sel:DWORD dst_unused:UNUSED_PAD src0_sel:DWORD src1_sel:WORD_1
v_lshlrev_b32_sdwa v147,1,v147 dst_sel:DWORD dst_unused:UNUSED_PAD src0_sel:DWORD src1_sel:WORD_1
v_mfma_f32_16x16x32_bf16 v[168:171],v[92:95],v[128:131],v[168:171]
s_waitcnt lgkmcnt(10)
ds_read_b64_tr_b16 v[70:71],v5 offset:2048
ds_read_b128 v[116:119],v7 offset:1312
v_fma_mixlo_f16 v120,v156,s99,0
v_fma_mixlo_f16 v122,v157,s99,0
v_mfma_f32_16x16x32_bf16 v[176:179],v[96:99],v[128:131],v[176:179]
s_waitcnt lgkmcnt(8)
ds_read_u16_d16_hi v146,v146
v_lshlrev_b32_sdwa v148,1,v148 dst_sel:DWORD dst_unused:UNUSED_PAD src0_sel:DWORD src1_sel:WORD_1
v_lshlrev_b32_sdwa v150,1,v150 dst_sel:DWORD dst_unused:UNUSED_PAD src0_sel:DWORD src1_sel:WORD_1
v_mfma_f32_16x16x32_bf16 v[184:187],v[100:103],v[128:131],v[184:187]
s_waitcnt lgkmcnt(8)
ds_read_b64_tr_b16 v[72:73],v4 offset:4096
ds_read_b64_tr_b16 v[74:75],v4 offset:6144
v_lshl_add_u64 v[136:137],v[136:137],0,v[138:139]
v_fma_mixhi_f16 v120,v158,s99,0
v_mfma_f32_16x16x32_bf16 v[192:195],v[104:107],v[128:131],v[192:195]
ds_read_u16 v145,v145
v_cvt_pk_bf16_f32 v138,v140,v142
v_cvt_pk_bf16_f32 v139,v141,v143
v_mfma_f32_16x16x32_bf16 v[200:203],v[108:111],v[128:131],v[200:203]
ds_read_b64_tr_b16 v[76:77],v5 offset:4096
ds_read_b64_tr_b16 v[78:79],v5 offset:6144
v_fma_mixhi_f16 v122,v159,s99,0
v_mfma_f32_16x16x32_bf16 v[164:167],v[88:91],v[136:139],v[164:167]
v_lshlrev_b32_sdwa v149,1,v149 dst_sel:DWORD dst_unused:UNUSED_PAD src0_sel:DWORD src1_sel:WORD_1
v_lshlrev_b32_sdwa v151,1,v151 dst_sel:DWORD dst_unused:UNUSED_PAD src0_sel:DWORD src1_sel:WORD_1
v_mfma_f32_16x16x32_bf16 v[172:175],v[92:95],v[136:139],v[172:175]
ds_read_b64_tr_b16 v[80:81],v4 offset:8192
ds_read_b64_tr_b16 v[82:83],v4 offset:10240
v_pk_fma_f16 v121,v120,v120,s54
v_pk_fma_f16 v123,v122,v122,s54
v_mfma_f32_16x16x32_bf16 v[180:183],v[96:99],v[136:139],v[180:183]
ds_read_u16_d16_hi v147,v147
v_pk_mul_f16 v121,v120,v121
v_pk_mul_f16 v123,v122,v123
v_mfma_f32_16x16x32_bf16 v[188:191],v[100:103],v[136:139],v[188:191]
ds_read_u16 v148,v148
v_lshlrev_b32_sdwa v152,1,v152 dst_sel:DWORD dst_unused:UNUSED_PAD src0_sel:DWORD src1_sel:WORD_1
v_lshlrev_b32_sdwa v154,1,v154 dst_sel:DWORD dst_unused:UNUSED_PAD src0_sel:DWORD src1_sel:WORD_1
v_mfma_f32_16x16x32_bf16 v[196:199],v[104:107],v[136:139],v[196:199]
v_exp_f16 v120,v121
v_mfma_f32_16x16x32_bf16 v[204:207],v[108:111],v[136:139],v[204:207]
s_waitcnt lgkmcnt(14)
ds_read_b64_tr_b16 v[84:85],v5 offset:8192
ds_read_b64_tr_b16 v[86:87],v5 offset:10240
v_exp_f16 v122,v123
v_mfma_f32_16x16x32_bf16 v[128:131],v[64:67],v[16:19],v[112:115]
v_lshlrev_b32_sdwa v153,1,v153 dst_sel:DWORD dst_unused:UNUSED_PAD src0_sel:DWORD src1_sel:WORD_1
v_lshlrev_b32_sdwa v155,1,v155 dst_sel:DWORD dst_unused:UNUSED_PAD src0_sel:DWORD src1_sel:WORD_1
v_mfma_f32_16x16x32_bf16 v[136:139],v[64:67],v[20:23],v[112:115]
s_waitcnt lgkmcnt(12)
ds_read_u16_d16_hi v150,v150
ds_read_u16 v149,v149
v_exp_f16_sdwa v120,v121 dst_sel:WORD_1 dst_unused:UNUSED_PRESERVE src0_sel:WORD_1
v_mfma_f32_16x16x32_bf16 v[140:143],v[68:71],v[20:23],v[116:119]
v_exp_f16_sdwa v122,v123 dst_sel:WORD_1 dst_unused:UNUSED_PRESERVE src0_sel:WORD_1
v_mfma_f32_16x16x32_bf16 v[132:135],v[68:71],v[16:19],v[116:119]
s_waitcnt lgkmcnt(11)
v_pk_fma_f16 v120,v120,s55,s55
v_mfma_f32_16x16x32_bf16 v[128:131],v[72:75],v[24:27],v[128:131]
v_pk_fma_f16 v122,v122,s55,s55
v_rcp_f16 v121,v120
v_mfma_f32_16x16x32_bf16 v[136:139],v[72:75],v[28:31],v[136:139]
s_waitcnt lgkmcnt(8)
ds_read_u16_d16_hi v151,v151
v_rcp_f16 v123,v122
v_mfma_f32_16x16x32_bf16 v[140:143],v[76:79],v[28:31],v[140:143]
ds_read_u16 v152,v152
ds_read_u16_d16_hi v154,v154
v_rcp_f16_sdwa v121,v120 dst_sel:WORD_1 dst_unused:UNUSED_PRESERVE src0_sel:WORD_1
v_mfma_f32_16x16x32_bf16 v[132:135],v[76:79],v[24:27],v[132:135]
s_waitcnt lgkmcnt(9)
v_rcp_f16_sdwa v123,v122 dst_sel:WORD_1 dst_unused:UNUSED_PRESERVE src0_sel:WORD_1
v_mfma_f32_16x16x32_bf16 v[128:131],v[80:83],v[32:35],v[128:131]
ds_read_u16 v153,v153
v_fma_mix_f32 v156,v156,v121,0 op_sel_hi:[0,1,0]
v_fma_mix_f32 v158,v158,v121,0 op_sel:[0,1,0] op_sel_hi:[0,1,0]
v_mfma_f32_16x16x32_bf16 v[136:139],v[80:83],v[36:39],v[136:139]
s_waitcnt lgkmcnt(3)
v_lshl_add_u64 v[144:145],v[144:145],0,v[146:147]
v_lshl_add_u64 v[146:147],v[148:149],0,v[150:151]
v_mfma_f32_16x16x32_bf16 v[140:143],v[84:87],v[36:39],v[140:143]
ds_read_u16_d16_hi v155,v155
v_fma_mix_f32 v157,v157,v123,0 op_sel_hi:[0,1,0]
v_fma_mix_f32 v159,v159,v123,0 op_sel:[0,1,0] op_sel_hi:[0,1,0]
v_mfma_f32_16x16x32_bf16 v[132:135],v[84:87],v[32:35],v[132:135]
v_lshlrev_b32_sdwa v128,1,v128 dst_sel:DWORD dst_unused:UNUSED_PAD src0_sel:DWORD src1_sel:WORD_1
v_lshlrev_b32_sdwa v130,1,v130 dst_sel:DWORD dst_unused:UNUSED_PAD src0_sel:DWORD src1_sel:WORD_1
v_mfma_f32_16x16x32_bf16 v[208:211],v[88:91],v[144:147],v[208:211]
v_lshlrev_b32_sdwa v129,1,v129 dst_sel:DWORD dst_unused:UNUSED_PAD src0_sel:DWORD src1_sel:WORD_1
v_lshlrev_b32_sdwa v131,1,v131 dst_sel:DWORD dst_unused:UNUSED_PAD src0_sel:DWORD src1_sel:WORD_1
v_mfma_f32_16x16x32_bf16 v[216:219],v[92:95],v[144:147],v[216:219]
ds_read_u16 v128,v128
ds_read_u16_d16_hi v130,v130
v_fma_mixlo_f16 v120,v140,s99,0
v_fma_mixlo_f16 v122,v141,s99,0
v_mfma_f32_16x16x32_bf16 v[224:227],v[96:99],v[144:147],v[224:227]
ds_read_u16 v129,v129
v_lshlrev_b32_sdwa v132,1,v132 dst_sel:DWORD dst_unused:UNUSED_PAD src0_sel:DWORD src1_sel:WORD_1
v_lshlrev_b32_sdwa v134,1,v134 dst_sel:DWORD dst_unused:UNUSED_PAD src0_sel:DWORD src1_sel:WORD_1
v_mfma_f32_16x16x32_bf16 v[232:235],v[100:103],v[144:147],v[232:235]
s_waitcnt lgkmcnt(3)
v_lshl_add_u64 v[152:153],v[152:153],0,v[154:155]
v_cvt_pk_bf16_f32 v154,v156,v158
v_mfma_f32_16x16x32_bf16 v[240:243],v[104:107],v[144:147],v[240:243]
v_fma_mixhi_f16 v120,v142,s99,0
v_cvt_pk_bf16_f32 v155,v157,v159
v_mfma_f32_16x16x32_bf16 v[248:251],v[108:111],v[144:147],v[248:251]
ds_read_u16_d16_hi v131,v131
v_fma_mixhi_f16 v122,v143,s99,0
v_mfma_f32_16x16x32_bf16 v[212:215],v[88:91],v[152:155],v[212:215]
v_lshlrev_b32_sdwa v133,1,v133 dst_sel:DWORD dst_unused:UNUSED_PAD src0_sel:DWORD src1_sel:WORD_1
v_pk_fma_f16 v121,v120,v120,s54
v_lshlrev_b32_sdwa v135,1,v135 dst_sel:DWORD dst_unused:UNUSED_PAD src0_sel:DWORD src1_sel:WORD_1
v_mfma_f32_16x16x32_bf16 v[220:223],v[92:95],v[152:155],v[220:223]
s_setprio 2
ds_read_u16 v132,v132
ds_read_u16_d16_hi v134,v134
v_pk_fma_f16 v123,v122,v122,s54
v_mfma_f32_16x16x32_bf16 v[228:231],v[96:99],v[152:155],v[228:231]
ds_read_b64_tr_b16 v[88:89],v6 offset:6160
v_pk_mul_f16 v121,v120,v121
v_lshlrev_b32_sdwa v136,1,v136 dst_sel:DWORD dst_unused:UNUSED_PAD src0_sel:DWORD src1_sel:WORD_1
v_pk_mul_f16 v123,v122,v123
v_mfma_f32_16x16x32_bf16 v[236:239],v[100:103],v[152:155],v[236:239]
ds_read_b64_tr_b16 v[90:91],v6 offset:7696
v_lshlrev_b32_sdwa v138,1,v138 dst_sel:DWORD dst_unused:UNUSED_PAD src0_sel:DWORD src1_sel:WORD_1
v_mfma_f32_16x16x32_bf16 v[244:247],v[104:107],v[152:155],v[244:247]
ds_read_u16 v133,v133
v_exp_f16 v120,v121
v_mfma_f32_16x16x32_bf16 v[252:255],v[108:111],v[152:155],v[252:255]
ds_read_b64_tr_b16 v[92:93],v6 offset:6168
v_lshlrev_b32_sdwa v137,1,v137 dst_sel:DWORD dst_unused:UNUSED_PAD src0_sel:DWORD src1_sel:WORD_1
v_mfma_f32_16x16x32_bf16 v[144:147],v[64:67],v[40:43],v[112:115]
ds_read_b64_tr_b16 v[94:95],v6 offset:7704
v_lshlrev_b32_sdwa v139,1,v139 dst_sel:DWORD dst_unused:UNUSED_PAD src0_sel:DWORD src1_sel:WORD_1
v_exp_f16 v122,v123
v_mfma_f32_16x16x32_bf16 v[152:155],v[64:67],v[44:47],v[112:115]
ds_read_u16_d16_hi v135,v135
v_mfma_f32_16x16x32_bf16 v[156:159],v[68:71],v[44:47],v[116:119]
ds_read_u16 v136,v136
v_exp_f16_sdwa v122,v123 dst_sel:WORD_1 dst_unused:UNUSED_PRESERVE src0_sel:WORD_1
v_exp_f16_sdwa v120,v121 dst_sel:WORD_1 dst_unused:UNUSED_PRESERVE src0_sel:WORD_1
v_mfma_f32_16x16x32_bf16 v[148:151],v[68:71],v[40:43],v[116:119]
ds_read_b64_tr_b16 v[96:97],v6 offset:6224
ds_read_b64_tr_b16 v[98:99],v6 offset:7760
v_pk_fma_f16 v120,v120,s55,s55
s_nop 0
s_mov_b32 s86,0xbe801d5a
v_pk_fma_f16 v122,v122,s55,s55
v_mfma_f32_16x16x32_bf16 v[144:147],v[72:75],v[48:51],v[144:147]
v_rcp_f16 v121,v120
v_mfma_f32_16x16x32_bf16 v[152:155],v[72:75],v[52:55],v[152:155]
ds_read_b64_tr_b16 v[100:101],v6 offset:6232
v_rcp_f16 v123,v122
v_mfma_f32_16x16x32_bf16 v[156:159],v[76:79],v[52:55],v[156:159]
ds_read_u16_d16_hi v138,v138
v_rcp_f16_sdwa v121,v120 dst_sel:WORD_1 dst_unused:UNUSED_PRESERVE src0_sel:WORD_1
v_mfma_f32_16x16x32_bf16 v[148:151],v[76:79],v[48:51],v[148:151]
ds_read_b64_tr_b16 v[102:103],v6 offset:7768
ds_read_b64_tr_b16 v[104:105],v6 offset:6288
v_rcp_f16_sdwa v123,v122 dst_sel:WORD_1 dst_unused:UNUSED_PRESERVE src0_sel:WORD_1
v_mfma_f32_16x16x32_bf16 v[144:147],v[80:83],v[56:59],v[144:147]
ds_read_u16 v137,v137
v_fma_mix_f32 v142,v142,v121,0 op_sel:[0,1,0] op_sel_hi:[0,1,0]
v_mfma_f32_16x16x32_bf16 v[152:155],v[80:83],v[60:63],v[152:155]
s_waitcnt lgkmcnt(8)
ds_read_b64_tr_b16 v[106:107],v6 offset:7824
ds_read_b64_tr_b16 v[108:109],v6 offset:6296
v_fma_mix_f32 v140,v140,v121,0 op_sel_hi:[0,1,0]
v_lshl_add_u64 v[128:129],v[128:129],0,v[130:131]
v_lshl_add_u64 v[130:131],v[132:133],0,v[134:135]
v_mfma_f32_16x16x32_bf16 v[156:159],v[84:87],v[60:63],v[156:159]
ds_read_u16_d16_hi v139,v139
v_fma_mix_f32 v141,v141,v123,0 op_sel_hi:[0,1,0]
v_fma_mix_f32 v143,v143,v123,0 op_sel:[0,1,0] op_sel_hi:[0,1,0]
v_mfma_f32_16x16x32_bf16 v[148:151],v[84:87],v[56:59],v[148:151]
ds_read_b64_tr_b16 v[110:111],v6 offset:7832
ds_read_b64_tr_b16 v[64:65],v4 offset:512
v_lshlrev_b32_sdwa v144,1,v144 dst_sel:DWORD dst_unused:UNUSED_PAD src0_sel:DWORD src1_sel:WORD_1
v_lshlrev_b32_sdwa v146,1,v146 dst_sel:DWORD dst_unused:UNUSED_PAD src0_sel:DWORD src1_sel:WORD_1
v_mfma_f32_16x16x32_bf16 v[160:163],v[88:91],v[128:131],v[160:163]
ds_read_u16 v144,v144
v_lshlrev_b32_sdwa v145,1,v145 dst_sel:DWORD dst_unused:UNUSED_PAD src0_sel:DWORD src1_sel:WORD_1
v_lshlrev_b32_sdwa v147,1,v147 dst_sel:DWORD dst_unused:UNUSED_PAD src0_sel:DWORD src1_sel:WORD_1
v_mfma_f32_16x16x32_bf16 v[168:171],v[92:95],v[128:131],v[168:171]
s_waitcnt lgkmcnt(11)
ds_read_b64_tr_b16 v[66:67],v4 offset:2560
ds_read_b128 v[112:115],v7 offset:1408
v_fma_mixlo_f16 v120,v156,s99,0
v_fma_mixlo_f16 v122,v157,s99,0
v_mfma_f32_16x16x32_bf16 v[176:179],v[96:99],v[128:131],v[176:179]
s_waitcnt lgkmcnt(10)
ds_read_u16_d16_hi v146,v146
v_lshlrev_b32_sdwa v148,1,v148 dst_sel:DWORD dst_unused:UNUSED_PAD src0_sel:DWORD src1_sel:WORD_1
v_lshlrev_b32_sdwa v150,1,v150 dst_sel:DWORD dst_unused:UNUSED_PAD src0_sel:DWORD src1_sel:WORD_1
v_mfma_f32_16x16x32_bf16 v[184:187],v[100:103],v[128:131],v[184:187]
s_waitcnt lgkmcnt(6)
ds_read_b64_tr_b16 v[68:69],v5 offset:512
ds_read_b64_tr_b16 v[70:71],v5 offset:2560
v_lshl_add_u64 v[136:137],v[136:137],0,v[138:139]
v_fma_mixhi_f16 v120,v158,s99,0
v_mfma_f32_16x16x32_bf16 v[192:195],v[104:107],v[128:131],v[192:195]
s_waitcnt lgkmcnt(7)
ds_read_u16 v145,v145
v_cvt_pk_bf16_f32 v138,v140,v142
v_cvt_pk_bf16_f32 v139,v141,v143
v_mfma_f32_16x16x32_bf16 v[200:203],v[108:111],v[128:131],v[200:203]
ds_read_b128 v[116:119],v7 offset:1440
ds_read_b64_tr_b16 v[72:73],v4 offset:4608
v_fma_mixhi_f16 v122,v159,s99,0
v_mfma_f32_16x16x32_bf16 v[164:167],v[88:91],v[136:139],v[164:167]
v_lshlrev_b32_sdwa v149,1,v149 dst_sel:DWORD dst_unused:UNUSED_PAD src0_sel:DWORD src1_sel:WORD_1
v_lshlrev_b32_sdwa v151,1,v151 dst_sel:DWORD dst_unused:UNUSED_PAD src0_sel:DWORD src1_sel:WORD_1
v_mfma_f32_16x16x32_bf16 v[172:175],v[92:95],v[136:139],v[172:175]
ds_read_b64_tr_b16 v[74:75],v4 offset:6656
ds_read_b64_tr_b16 v[76:77],v5 offset:4608
v_pk_fma_f16 v121,v120,v120,s54
v_pk_fma_f16 v123,v122,v122,s54
v_mfma_f32_16x16x32_bf16 v[180:183],v[96:99],v[136:139],v[180:183]
s_setprio 1
ds_read_u16_d16_hi v147,v147
v_pk_mul_f16 v121,v120,v121
v_pk_mul_f16 v123,v122,v123
v_mfma_f32_16x16x32_bf16 v[188:191],v[100:103],v[136:139],v[188:191]
ds_read_u16 v148,v148
v_lshlrev_b32_sdwa v152,1,v152 dst_sel:DWORD dst_unused:UNUSED_PAD src0_sel:DWORD src1_sel:WORD_1
v_lshlrev_b32_sdwa v154,1,v154 dst_sel:DWORD dst_unused:UNUSED_PAD src0_sel:DWORD src1_sel:WORD_1
v_mfma_f32_16x16x32_bf16 v[196:199],v[104:107],v[136:139],v[196:199]
v_exp_f16 v120,v121
v_mfma_f32_16x16x32_bf16 v[204:207],v[108:111],v[136:139],v[204:207]
s_waitcnt lgkmcnt(10)
ds_read_b64_tr_b16 v[78:79],v5 offset:6656
ds_read_b64_tr_b16 v[80:81],v4 offset:8704
v_exp_f16 v122,v123
v_mfma_f32_16x16x32_bf16 v[128:131],v[64:67],v[16:19],v[112:115]
v_lshlrev_b32_sdwa v153,1,v153 dst_sel:DWORD dst_unused:UNUSED_PAD src0_sel:DWORD src1_sel:WORD_1
v_lshlrev_b32_sdwa v155,1,v155 dst_sel:DWORD dst_unused:UNUSED_PAD src0_sel:DWORD src1_sel:WORD_1
v_mfma_f32_16x16x32_bf16 v[136:139],v[64:67],v[20:23],v[112:115]
s_waitcnt lgkmcnt(7)
ds_read_u16_d16_hi v150,v150
ds_read_u16 v149,v149
v_exp_f16_sdwa v120,v121 dst_sel:WORD_1 dst_unused:UNUSED_PRESERVE src0_sel:WORD_1
v_mfma_f32_16x16x32_bf16 v[140:143],v[68:71],v[20:23],v[116:119]
v_exp_f16_sdwa v122,v123 dst_sel:WORD_1 dst_unused:UNUSED_PRESERVE src0_sel:WORD_1
v_mfma_f32_16x16x32_bf16 v[132:135],v[68:71],v[16:19],v[116:119]
s_waitcnt lgkmcnt(7)
ds_read_b64_tr_b16 v[82:83],v4 offset:10752
v_pk_fma_f16 v120,v120,s55,s55
v_mfma_f32_16x16x32_bf16 v[128:131],v[72:75],v[24:27],v[128:131]
ds_read_b64_tr_b16 v[84:85],v5 offset:8704
v_pk_fma_f16 v122,v122,s55,s55
v_rcp_f16 v121,v120
v_mfma_f32_16x16x32_bf16 v[136:139],v[72:75],v[28:31],v[136:139]
s_waitcnt lgkmcnt(5)
ds_read_b64_tr_b16 v[86:87],v5 offset:10752
ds_read_u16_d16_hi v151,v151
v_rcp_f16 v123,v122
v_mfma_f32_16x16x32_bf16 v[140:143],v[76:79],v[28:31],v[140:143]
ds_read_u16 v152,v152
ds_read_u16_d16_hi v154,v154
v_rcp_f16_sdwa v121,v120 dst_sel:WORD_1 dst_unused:UNUSED_PRESERVE src0_sel:WORD_1
v_mfma_f32_16x16x32_bf16 v[132:135],v[76:79],v[24:27],v[132:135]
s_waitcnt lgkmcnt(5)
v_rcp_f16_sdwa v123,v122 dst_sel:WORD_1 dst_unused:UNUSED_PRESERVE src0_sel:WORD_1
v_mfma_f32_16x16x32_bf16 v[128:131],v[80:83],v[32:35],v[128:131]
ds_read_u16 v153,v153
v_fma_mix_f32 v156,v156,v121,0 op_sel_hi:[0,1,0]
v_fma_mix_f32 v158,v158,v121,0 op_sel:[0,1,0] op_sel_hi:[0,1,0]
v_mfma_f32_16x16x32_bf16 v[136:139],v[80:83],v[36:39],v[136:139]
s_waitcnt lgkmcnt(3)
s_setprio 0
v_lshl_add_u64 v[144:145],v[144:145],0,v[146:147]
v_lshl_add_u64 v[146:147],v[148:149],0,v[150:151]
v_mfma_f32_16x16x32_bf16 v[140:143],v[84:87],v[36:39],v[140:143]
ds_read_u16_d16_hi v155,v155
v_fma_mix_f32 v157,v157,v123,0 op_sel_hi:[0,1,0]
v_fma_mix_f32 v159,v159,v123,0 op_sel:[0,1,0] op_sel_hi:[0,1,0]
v_mfma_f32_16x16x32_bf16 v[132:135],v[84:87],v[32:35],v[132:135]
v_lshlrev_b32_sdwa v128,1,v128 dst_sel:DWORD dst_unused:UNUSED_PAD src0_sel:DWORD src1_sel:WORD_1
v_lshlrev_b32_sdwa v130,1,v130 dst_sel:DWORD dst_unused:UNUSED_PAD src0_sel:DWORD src1_sel:WORD_1
v_mfma_f32_16x16x32_bf16 v[208:211],v[88:91],v[144:147],v[208:211]
s_barrier
s_setprio 3
v_lshlrev_b32_sdwa v129,1,v129 dst_sel:DWORD dst_unused:UNUSED_PAD src0_sel:DWORD src1_sel:WORD_1
v_lshlrev_b32_sdwa v131,1,v131 dst_sel:DWORD dst_unused:UNUSED_PAD src0_sel:DWORD src1_sel:WORD_1
v_mfma_f32_16x16x32_bf16 v[216:219],v[92:95],v[144:147],v[216:219]
ds_read_u16 v128,v128
ds_read_u16_d16_hi v130,v130
v_fma_mixlo_f16 v120,v140,s99,0
v_fma_mixlo_f16 v122,v141,s99,0
v_mfma_f32_16x16x32_bf16 v[224:227],v[96:99],v[144:147],v[224:227]
ds_read_u16 v129,v129
v_lshlrev_b32_sdwa v132,1,v132 dst_sel:DWORD dst_unused:UNUSED_PAD src0_sel:DWORD src1_sel:WORD_1
v_lshlrev_b32_sdwa v134,1,v134 dst_sel:DWORD dst_unused:UNUSED_PAD src0_sel:DWORD src1_sel:WORD_1
v_mfma_f32_16x16x32_bf16 v[232:235],v[100:103],v[144:147],v[232:235]
s_waitcnt lgkmcnt(3)
v_lshl_add_u64 v[152:153],v[152:153],0,v[154:155]
v_cvt_pk_bf16_f32 v154,v156,v158
v_mfma_f32_16x16x32_bf16 v[240:243],v[104:107],v[144:147],v[240:243]
v_fma_mixhi_f16 v120,v142,s99,0
v_cvt_pk_bf16_f32 v155,v157,v159
v_mfma_f32_16x16x32_bf16 v[248:251],v[108:111],v[144:147],v[248:251]
ds_read_u16_d16_hi v131,v131
v_fma_mixhi_f16 v122,v143,s99,0
v_mfma_f32_16x16x32_bf16 v[212:215],v[88:91],v[152:155],v[212:215]
v_lshlrev_b32_sdwa v133,1,v133 dst_sel:DWORD dst_unused:UNUSED_PAD src0_sel:DWORD src1_sel:WORD_1
v_pk_fma_f16 v121,v120,v120,s54
v_lshlrev_b32_sdwa v135,1,v135 dst_sel:DWORD dst_unused:UNUSED_PAD src0_sel:DWORD src1_sel:WORD_1
v_mfma_f32_16x16x32_bf16 v[220:223],v[92:95],v[152:155],v[220:223]
ds_read_u16 v132,v132
ds_read_u16_d16_hi v134,v134
v_pk_fma_f16 v123,v122,v122,s54
v_mfma_f32_16x16x32_bf16 v[228:231],v[96:99],v[152:155],v[228:231]
ds_read_b64_tr_b16 v[88:89],v6 offset:12320
v_pk_mul_f16 v121,v120,v121
v_lshlrev_b32_sdwa v136,1,v136 dst_sel:DWORD dst_unused:UNUSED_PAD src0_sel:DWORD src1_sel:WORD_1
v_pk_mul_f16 v123,v122,v123
v_mfma_f32_16x16x32_bf16 v[236:239],v[100:103],v[152:155],v[236:239]
ds_read_b64_tr_b16 v[90:91],v6 offset:13856
v_lshlrev_b32_sdwa v138,1,v138 dst_sel:DWORD dst_unused:UNUSED_PAD src0_sel:DWORD src1_sel:WORD_1
v_mfma_f32_16x16x32_bf16 v[244:247],v[104:107],v[152:155],v[244:247]
ds_read_u16 v133,v133
v_exp_f16 v120,v121
v_mfma_f32_16x16x32_bf16 v[252:255],v[108:111],v[152:155],v[252:255]
ds_read_b64_tr_b16 v[92:93],v6 offset:12328
v_lshlrev_b32_sdwa v137,1,v137 dst_sel:DWORD dst_unused:UNUSED_PAD src0_sel:DWORD src1_sel:WORD_1
v_mfma_f32_16x16x32_bf16 v[144:147],v[64:67],v[40:43],v[112:115]
ds_read_b64_tr_b16 v[94:95],v6 offset:13864
v_lshlrev_b32_sdwa v139,1,v139 dst_sel:DWORD dst_unused:UNUSED_PAD src0_sel:DWORD src1_sel:WORD_1
v_exp_f16 v122,v123
v_mfma_f32_16x16x32_bf16 v[152:155],v[64:67],v[44:47],v[112:115]
ds_read_u16_d16_hi v135,v135
v_mfma_f32_16x16x32_bf16 v[156:159],v[68:71],v[44:47],v[116:119]
ds_read_u16 v136,v136
v_exp_f16_sdwa v122,v123 dst_sel:WORD_1 dst_unused:UNUSED_PRESERVE src0_sel:WORD_1
v_exp_f16_sdwa v120,v121 dst_sel:WORD_1 dst_unused:UNUSED_PRESERVE src0_sel:WORD_1
v_mfma_f32_16x16x32_bf16 v[148:151],v[68:71],v[40:43],v[116:119]
ds_read_b64_tr_b16 v[96:97],v6 offset:12384
ds_read_b64_tr_b16 v[98:99],v6 offset:13920
v_pk_fma_f16 v120,v120,s55,s55
v_pk_fma_f16 v122,v122,s55,s55
v_mfma_f32_16x16x32_bf16 v[144:147],v[72:75],v[48:51],v[144:147]
v_rcp_f16 v121,v120
v_mfma_f32_16x16x32_bf16 v[152:155],v[72:75],v[52:55],v[152:155]
ds_read_b64_tr_b16 v[100:101],v6 offset:12392
v_rcp_f16 v123,v122
v_mfma_f32_16x16x32_bf16 v[156:159],v[76:79],v[52:55],v[156:159]
ds_read_u16_d16_hi v138,v138
v_rcp_f16_sdwa v121,v120 dst_sel:WORD_1 dst_unused:UNUSED_PRESERVE src0_sel:WORD_1
v_mfma_f32_16x16x32_bf16 v[148:151],v[76:79],v[48:51],v[148:151]
ds_read_b64_tr_b16 v[102:103],v6 offset:13928
ds_read_b64_tr_b16 v[104:105],v6 offset:12448
v_rcp_f16_sdwa v123,v122 dst_sel:WORD_1 dst_unused:UNUSED_PRESERVE src0_sel:WORD_1
v_mfma_f32_16x16x32_bf16 v[144:147],v[80:83],v[56:59],v[144:147]
ds_read_u16 v137,v137
v_fma_mix_f32 v142,v142,v121,0 op_sel:[0,1,0] op_sel_hi:[0,1,0]
v_mfma_f32_16x16x32_bf16 v[152:155],v[80:83],v[60:63],v[152:155]
s_waitcnt lgkmcnt(8)
ds_read_b64_tr_b16 v[106:107],v6 offset:13984
ds_read_b64_tr_b16 v[108:109],v6 offset:12456
v_fma_mix_f32 v140,v140,v121,0 op_sel_hi:[0,1,0]
v_lshl_add_u64 v[128:129],v[128:129],0,v[130:131]
v_lshl_add_u64 v[130:131],v[132:133],0,v[134:135]
v_mfma_f32_16x16x32_bf16 v[156:159],v[84:87],v[60:63],v[156:159]
ds_read_u16_d16_hi v139,v139
v_fma_mix_f32 v141,v141,v123,0 op_sel_hi:[0,1,0]
v_fma_mix_f32 v143,v143,v123,0 op_sel:[0,1,0] op_sel_hi:[0,1,0]
v_mfma_f32_16x16x32_bf16 v[148:151],v[84:87],v[56:59],v[148:151]
ds_read_b64_tr_b16 v[110:111],v6 offset:13992
ds_read_b64_tr_b16 v[64:65],v4 offset:12320
v_lshlrev_b32_sdwa v144,1,v144 dst_sel:DWORD dst_unused:UNUSED_PAD src0_sel:DWORD src1_sel:WORD_1
v_lshlrev_b32_sdwa v146,1,v146 dst_sel:DWORD dst_unused:UNUSED_PAD src0_sel:DWORD src1_sel:WORD_1
v_mfma_f32_16x16x32_bf16 v[160:163],v[88:91],v[128:131],v[160:163]
ds_read_u16 v144,v144
v_lshlrev_b32_sdwa v145,1,v145 dst_sel:DWORD dst_unused:UNUSED_PAD src0_sel:DWORD src1_sel:WORD_1
v_lshlrev_b32_sdwa v147,1,v147 dst_sel:DWORD dst_unused:UNUSED_PAD src0_sel:DWORD src1_sel:WORD_1
v_mfma_f32_16x16x32_bf16 v[168:171],v[92:95],v[128:131],v[168:171]
s_waitcnt lgkmcnt(11)
ds_read_b64_tr_b16 v[66:67],v4 offset:14368
ds_read_b128 v[112:115],v7 offset:0
v_fma_mixlo_f16 v120,v156,s99,0
v_fma_mixlo_f16 v122,v157,s99,0
v_mfma_f32_16x16x32_bf16 v[176:179],v[96:99],v[128:131],v[176:179]
s_waitcnt lgkmcnt(10)
ds_read_u16_d16_hi v146,v146
v_lshlrev_b32_sdwa v148,1,v148 dst_sel:DWORD dst_unused:UNUSED_PAD src0_sel:DWORD src1_sel:WORD_1
v_lshlrev_b32_sdwa v150,1,v150 dst_sel:DWORD dst_unused:UNUSED_PAD src0_sel:DWORD src1_sel:WORD_1
v_mfma_f32_16x16x32_bf16 v[184:187],v[100:103],v[128:131],v[184:187]
s_waitcnt lgkmcnt(6)
ds_read_b64_tr_b16 v[68:69],v5 offset:12320
ds_read_b64_tr_b16 v[70:71],v5 offset:14368
v_lshl_add_u64 v[136:137],v[136:137],0,v[138:139]
v_fma_mixhi_f16 v120,v158,s99,0
v_mfma_f32_16x16x32_bf16 v[192:195],v[104:107],v[128:131],v[192:195]
s_waitcnt lgkmcnt(7)
ds_read_u16 v145,v145
v_cvt_pk_bf16_f32 v138,v140,v142
v_cvt_pk_bf16_f32 v139,v141,v143
v_mfma_f32_16x16x32_bf16 v[200:203],v[108:111],v[128:131],v[200:203]
ds_read_b128 v[116:119],v7 offset:32
ds_read_b64_tr_b16 v[72:73],v4 offset:16416
v_fma_mixhi_f16 v122,v159,s99,0
v_mfma_f32_16x16x32_bf16 v[164:167],v[88:91],v[136:139],v[164:167]
v_lshlrev_b32_sdwa v149,1,v149 dst_sel:DWORD dst_unused:UNUSED_PAD src0_sel:DWORD src1_sel:WORD_1
v_lshlrev_b32_sdwa v151,1,v151 dst_sel:DWORD dst_unused:UNUSED_PAD src0_sel:DWORD src1_sel:WORD_1
v_mfma_f32_16x16x32_bf16 v[172:175],v[92:95],v[136:139],v[172:175]
ds_read_b64_tr_b16 v[74:75],v4 offset:18464
ds_read_b64_tr_b16 v[76:77],v5 offset:16416
v_pk_fma_f16 v121,v120,v120,s54
v_pk_fma_f16 v123,v122,v122,s54
v_mfma_f32_16x16x32_bf16 v[180:183],v[96:99],v[136:139],v[180:183]
ds_read_u16_d16_hi v147,v147
v_pk_mul_f16 v121,v120,v121
v_pk_mul_f16 v123,v122,v123
v_mfma_f32_16x16x32_bf16 v[188:191],v[100:103],v[136:139],v[188:191]
ds_read_u16 v148,v148
v_lshlrev_b32_sdwa v152,1,v152 dst_sel:DWORD dst_unused:UNUSED_PAD src0_sel:DWORD src1_sel:WORD_1
v_lshlrev_b32_sdwa v154,1,v154 dst_sel:DWORD dst_unused:UNUSED_PAD src0_sel:DWORD src1_sel:WORD_1
v_mfma_f32_16x16x32_bf16 v[196:199],v[104:107],v[136:139],v[196:199]
v_exp_f16 v120,v121
v_mfma_f32_16x16x32_bf16 v[204:207],v[108:111],v[136:139],v[204:207]
s_waitcnt lgkmcnt(10)
ds_read_b64_tr_b16 v[78:79],v5 offset:18464
ds_read_b64_tr_b16 v[80:81],v4 offset:20512
v_exp_f16 v122,v123
v_mfma_f32_16x16x32_bf16 v[128:131],v[64:67],v[16:19],v[112:115]
v_lshlrev_b32_sdwa v153,1,v153 dst_sel:DWORD dst_unused:UNUSED_PAD src0_sel:DWORD src1_sel:WORD_1
v_lshlrev_b32_sdwa v155,1,v155 dst_sel:DWORD dst_unused:UNUSED_PAD src0_sel:DWORD src1_sel:WORD_1
v_mfma_f32_16x16x32_bf16 v[136:139],v[64:67],v[20:23],v[112:115]
s_waitcnt lgkmcnt(7)
ds_read_u16_d16_hi v150,v150
ds_read_u16 v149,v149
v_exp_f16_sdwa v120,v121 dst_sel:WORD_1 dst_unused:UNUSED_PRESERVE src0_sel:WORD_1
v_mfma_f32_16x16x32_bf16 v[140:143],v[68:71],v[20:23],v[116:119]
v_exp_f16_sdwa v122,v123 dst_sel:WORD_1 dst_unused:UNUSED_PRESERVE src0_sel:WORD_1
v_mfma_f32_16x16x32_bf16 v[132:135],v[68:71],v[16:19],v[116:119]
s_waitcnt lgkmcnt(7)
ds_read_b64_tr_b16 v[82:83],v4 offset:22560
v_pk_fma_f16 v120,v120,s55,s55
v_mfma_f32_16x16x32_bf16 v[128:131],v[72:75],v[24:27],v[128:131]
ds_read_b64_tr_b16 v[84:85],v5 offset:20512
v_pk_fma_f16 v122,v122,s55,s55
v_rcp_f16 v121,v120
v_mfma_f32_16x16x32_bf16 v[136:139],v[72:75],v[28:31],v[136:139]
s_waitcnt lgkmcnt(5)
ds_read_b64_tr_b16 v[86:87],v5 offset:22560
ds_read_u16_d16_hi v151,v151
v_rcp_f16 v123,v122
v_mfma_f32_16x16x32_bf16 v[140:143],v[76:79],v[28:31],v[140:143]
ds_read_u16 v152,v152
ds_read_u16_d16_hi v154,v154
v_rcp_f16_sdwa v121,v120 dst_sel:WORD_1 dst_unused:UNUSED_PRESERVE src0_sel:WORD_1
v_mfma_f32_16x16x32_bf16 v[132:135],v[76:79],v[24:27],v[132:135]
s_waitcnt lgkmcnt(5)
v_rcp_f16_sdwa v123,v122 dst_sel:WORD_1 dst_unused:UNUSED_PRESERVE src0_sel:WORD_1
v_mfma_f32_16x16x32_bf16 v[128:131],v[80:83],v[32:35],v[128:131]
ds_read_u16 v153,v153
v_fma_mix_f32 v156,v156,v121,0 op_sel_hi:[0,1,0]
v_fma_mix_f32 v158,v158,v121,0 op_sel:[0,1,0] op_sel_hi:[0,1,0]
v_mfma_f32_16x16x32_bf16 v[136:139],v[80:83],v[36:39],v[136:139]
s_waitcnt lgkmcnt(3)
v_lshl_add_u64 v[144:145],v[144:145],0,v[146:147]
v_lshl_add_u64 v[146:147],v[148:149],0,v[150:151]
v_mfma_f32_16x16x32_bf16 v[140:143],v[84:87],v[36:39],v[140:143]
ds_read_u16_d16_hi v155,v155
v_fma_mix_f32 v157,v157,v123,0 op_sel_hi:[0,1,0]
v_fma_mix_f32 v159,v159,v123,0 op_sel:[0,1,0] op_sel_hi:[0,1,0]
v_mfma_f32_16x16x32_bf16 v[132:135],v[84:87],v[32:35],v[132:135]
v_lshlrev_b32_sdwa v128,1,v128 dst_sel:DWORD dst_unused:UNUSED_PAD src0_sel:DWORD src1_sel:WORD_1
v_lshlrev_b32_sdwa v130,1,v130 dst_sel:DWORD dst_unused:UNUSED_PAD src0_sel:DWORD src1_sel:WORD_1
v_mfma_f32_16x16x32_bf16 v[208:211],v[88:91],v[144:147],v[208:211]
v_lshlrev_b32_sdwa v129,1,v129 dst_sel:DWORD dst_unused:UNUSED_PAD src0_sel:DWORD src1_sel:WORD_1
v_lshlrev_b32_sdwa v131,1,v131 dst_sel:DWORD dst_unused:UNUSED_PAD src0_sel:DWORD src1_sel:WORD_1
v_mfma_f32_16x16x32_bf16 v[216:219],v[92:95],v[144:147],v[216:219]
ds_read_u16 v128,v128
ds_read_u16_d16_hi v130,v130
v_fma_mixlo_f16 v120,v140,s99,0
v_fma_mixlo_f16 v122,v141,s99,0
v_mfma_f32_16x16x32_bf16 v[224:227],v[96:99],v[144:147],v[224:227]
ds_read_u16 v129,v129
v_lshlrev_b32_sdwa v132,1,v132 dst_sel:DWORD dst_unused:UNUSED_PAD src0_sel:DWORD src1_sel:WORD_1
v_lshlrev_b32_sdwa v134,1,v134 dst_sel:DWORD dst_unused:UNUSED_PAD src0_sel:DWORD src1_sel:WORD_1
v_mfma_f32_16x16x32_bf16 v[232:235],v[100:103],v[144:147],v[232:235]
s_waitcnt lgkmcnt(3)
v_lshl_add_u64 v[152:153],v[152:153],0,v[154:155]
v_cvt_pk_bf16_f32 v154,v156,v158
v_mfma_f32_16x16x32_bf16 v[240:243],v[104:107],v[144:147],v[240:243]
v_fma_mixhi_f16 v120,v142,s99,0
v_cvt_pk_bf16_f32 v155,v157,v159
v_mfma_f32_16x16x32_bf16 v[248:251],v[108:111],v[144:147],v[248:251]
s_setprio 2
ds_read_u16_d16_hi v131,v131
v_fma_mixhi_f16 v122,v143,s99,0
v_mfma_f32_16x16x32_bf16 v[212:215],v[88:91],v[152:155],v[212:215]
v_lshlrev_b32_sdwa v133,1,v133 dst_sel:DWORD dst_unused:UNUSED_PAD src0_sel:DWORD src1_sel:WORD_1
v_pk_fma_f16 v121,v120,v120,s54
v_lshlrev_b32_sdwa v135,1,v135 dst_sel:DWORD dst_unused:UNUSED_PAD src0_sel:DWORD src1_sel:WORD_1
v_mfma_f32_16x16x32_bf16 v[220:223],v[92:95],v[152:155],v[220:223]
ds_read_u16 v132,v132
ds_read_u16_d16_hi v134,v134
v_pk_fma_f16 v123,v122,v122,s54
v_mfma_f32_16x16x32_bf16 v[228:231],v[96:99],v[152:155],v[228:231]
ds_read_b64_tr_b16 v[88:89],v6 offset:18480
v_pk_mul_f16 v121,v120,v121
v_lshlrev_b32_sdwa v136,1,v136 dst_sel:DWORD dst_unused:UNUSED_PAD src0_sel:DWORD src1_sel:WORD_1
v_pk_mul_f16 v123,v122,v123
v_mfma_f32_16x16x32_bf16 v[236:239],v[100:103],v[152:155],v[236:239]
ds_read_b64_tr_b16 v[90:91],v6 offset:20016
v_lshlrev_b32_sdwa v138,1,v138 dst_sel:DWORD dst_unused:UNUSED_PAD src0_sel:DWORD src1_sel:WORD_1
v_mfma_f32_16x16x32_bf16 v[244:247],v[104:107],v[152:155],v[244:247]
ds_read_u16 v133,v133
v_exp_f16 v120,v121
v_mfma_f32_16x16x32_bf16 v[252:255],v[108:111],v[152:155],v[252:255]
ds_read_b64_tr_b16 v[92:93],v6 offset:18488
v_lshlrev_b32_sdwa v137,1,v137 dst_sel:DWORD dst_unused:UNUSED_PAD src0_sel:DWORD src1_sel:WORD_1
v_mfma_f32_16x16x32_bf16 v[144:147],v[64:67],v[40:43],v[112:115]
ds_read_b64_tr_b16 v[94:95],v6 offset:20024
v_lshlrev_b32_sdwa v139,1,v139 dst_sel:DWORD dst_unused:UNUSED_PAD src0_sel:DWORD src1_sel:WORD_1
v_exp_f16 v122,v123
v_mfma_f32_16x16x32_bf16 v[152:155],v[64:67],v[44:47],v[112:115]
ds_read_u16_d16_hi v135,v135
v_mfma_f32_16x16x32_bf16 v[156:159],v[68:71],v[44:47],v[116:119]
ds_read_u16 v136,v136
v_exp_f16_sdwa v122,v123 dst_sel:WORD_1 dst_unused:UNUSED_PRESERVE src0_sel:WORD_1
v_exp_f16_sdwa v120,v121 dst_sel:WORD_1 dst_unused:UNUSED_PRESERVE src0_sel:WORD_1
v_mfma_f32_16x16x32_bf16 v[148:151],v[68:71],v[40:43],v[116:119]
ds_read_b64_tr_b16 v[96:97],v6 offset:18544
ds_read_b64_tr_b16 v[98:99],v6 offset:20080
v_pk_fma_f16 v120,v120,s55,s55
v_pk_fma_f16 v122,v122,s55,s55
v_mfma_f32_16x16x32_bf16 v[144:147],v[72:75],v[48:51],v[144:147]
v_rcp_f16 v121,v120
v_mfma_f32_16x16x32_bf16 v[152:155],v[72:75],v[52:55],v[152:155]
ds_read_b64_tr_b16 v[100:101],v6 offset:18552
v_rcp_f16 v123,v122
v_mfma_f32_16x16x32_bf16 v[156:159],v[76:79],v[52:55],v[156:159]
ds_read_u16_d16_hi v138,v138
v_rcp_f16_sdwa v121,v120 dst_sel:WORD_1 dst_unused:UNUSED_PRESERVE src0_sel:WORD_1
v_mfma_f32_16x16x32_bf16 v[148:151],v[76:79],v[48:51],v[148:151]
ds_read_b64_tr_b16 v[102:103],v6 offset:20088
ds_read_b64_tr_b16 v[104:105],v6 offset:18608
v_rcp_f16_sdwa v123,v122 dst_sel:WORD_1 dst_unused:UNUSED_PRESERVE src0_sel:WORD_1
v_mfma_f32_16x16x32_bf16 v[144:147],v[80:83],v[56:59],v[144:147]
ds_read_u16 v137,v137
v_fma_mix_f32 v142,v142,v121,0 op_sel:[0,1,0] op_sel_hi:[0,1,0]
v_mfma_f32_16x16x32_bf16 v[152:155],v[80:83],v[60:63],v[152:155]
s_waitcnt lgkmcnt(8)
ds_read_b64_tr_b16 v[106:107],v6 offset:20144
ds_read_b64_tr_b16 v[108:109],v6 offset:18616
v_fma_mix_f32 v140,v140,v121,0 op_sel_hi:[0,1,0]
v_lshl_add_u64 v[128:129],v[128:129],0,v[130:131]
v_lshl_add_u64 v[130:131],v[132:133],0,v[134:135]
v_mfma_f32_16x16x32_bf16 v[156:159],v[84:87],v[60:63],v[156:159]
ds_read_u16_d16_hi v139,v139
v_fma_mix_f32 v141,v141,v123,0 op_sel_hi:[0,1,0]
v_fma_mix_f32 v143,v143,v123,0 op_sel:[0,1,0] op_sel_hi:[0,1,0]
v_mfma_f32_16x16x32_bf16 v[148:151],v[84:87],v[56:59],v[148:151]
ds_read_b64_tr_b16 v[110:111],v6 offset:20152
ds_read_b64_tr_b16 v[64:65],v4 offset:12832
v_lshlrev_b32_sdwa v144,1,v144 dst_sel:DWORD dst_unused:UNUSED_PAD src0_sel:DWORD src1_sel:WORD_1
v_lshlrev_b32_sdwa v146,1,v146 dst_sel:DWORD dst_unused:UNUSED_PAD src0_sel:DWORD src1_sel:WORD_1
v_mfma_f32_16x16x32_bf16 v[160:163],v[88:91],v[128:131],v[160:163]
ds_read_u16 v144,v144
v_lshlrev_b32_sdwa v145,1,v145 dst_sel:DWORD dst_unused:UNUSED_PAD src0_sel:DWORD src1_sel:WORD_1
v_lshlrev_b32_sdwa v147,1,v147 dst_sel:DWORD dst_unused:UNUSED_PAD src0_sel:DWORD src1_sel:WORD_1
v_mfma_f32_16x16x32_bf16 v[168:171],v[92:95],v[128:131],v[168:171]
s_waitcnt lgkmcnt(11)
ds_read_b64_tr_b16 v[66:67],v4 offset:14880
ds_read_b128 v[112:115],v7 offset:128
v_fma_mixlo_f16 v120,v156,s99,0
v_fma_mixlo_f16 v122,v157,s99,0
v_mfma_f32_16x16x32_bf16 v[176:179],v[96:99],v[128:131],v[176:179]
s_waitcnt lgkmcnt(10)
ds_read_u16_d16_hi v146,v146
v_lshlrev_b32_sdwa v148,1,v148 dst_sel:DWORD dst_unused:UNUSED_PAD src0_sel:DWORD src1_sel:WORD_1
v_lshlrev_b32_sdwa v150,1,v150 dst_sel:DWORD dst_unused:UNUSED_PAD src0_sel:DWORD src1_sel:WORD_1
v_mfma_f32_16x16x32_bf16 v[184:187],v[100:103],v[128:131],v[184:187]
s_waitcnt lgkmcnt(6)
ds_read_b64_tr_b16 v[68:69],v5 offset:12832
ds_read_b64_tr_b16 v[70:71],v5 offset:14880
v_lshl_add_u64 v[136:137],v[136:137],0,v[138:139]
v_fma_mixhi_f16 v120,v158,s99,0
v_mfma_f32_16x16x32_bf16 v[192:195],v[104:107],v[128:131],v[192:195]
s_waitcnt lgkmcnt(7)
ds_read_u16 v145,v145
v_cvt_pk_bf16_f32 v138,v140,v142
v_cvt_pk_bf16_f32 v139,v141,v143
v_mfma_f32_16x16x32_bf16 v[200:203],v[108:111],v[128:131],v[200:203]
ds_read_b128 v[116:119],v7 offset:160
ds_read_b64_tr_b16 v[72:73],v4 offset:16928
v_fma_mixhi_f16 v122,v159,s99,0
v_mfma_f32_16x16x32_bf16 v[164:167],v[88:91],v[136:139],v[164:167]
v_lshlrev_b32_sdwa v149,1,v149 dst_sel:DWORD dst_unused:UNUSED_PAD src0_sel:DWORD src1_sel:WORD_1
v_lshlrev_b32_sdwa v151,1,v151 dst_sel:DWORD dst_unused:UNUSED_PAD src0_sel:DWORD src1_sel:WORD_1
v_mfma_f32_16x16x32_bf16 v[172:175],v[92:95],v[136:139],v[172:175]
s_setprio 1
ds_read_b64_tr_b16 v[74:75],v4 offset:18976
ds_read_b64_tr_b16 v[76:77],v5 offset:16928
v_pk_fma_f16 v121,v120,v120,s54
v_pk_fma_f16 v123,v122,v122,s54
v_mfma_f32_16x16x32_bf16 v[180:183],v[96:99],v[136:139],v[180:183]
ds_read_u16_d16_hi v147,v147
v_pk_mul_f16 v121,v120,v121
v_pk_mul_f16 v123,v122,v123
v_mfma_f32_16x16x32_bf16 v[188:191],v[100:103],v[136:139],v[188:191]
ds_read_u16 v148,v148
v_lshlrev_b32_sdwa v152,1,v152 dst_sel:DWORD dst_unused:UNUSED_PAD src0_sel:DWORD src1_sel:WORD_1
v_lshlrev_b32_sdwa v154,1,v154 dst_sel:DWORD dst_unused:UNUSED_PAD src0_sel:DWORD src1_sel:WORD_1
v_mfma_f32_16x16x32_bf16 v[196:199],v[104:107],v[136:139],v[196:199]
v_exp_f16 v120,v121
v_mfma_f32_16x16x32_bf16 v[204:207],v[108:111],v[136:139],v[204:207]
s_waitcnt lgkmcnt(10)
ds_read_b64_tr_b16 v[78:79],v5 offset:18976
ds_read_b64_tr_b16 v[80:81],v4 offset:21024
v_exp_f16 v122,v123
v_mfma_f32_16x16x32_bf16 v[128:131],v[64:67],v[16:19],v[112:115]
v_lshlrev_b32_sdwa v153,1,v153 dst_sel:DWORD dst_unused:UNUSED_PAD src0_sel:DWORD src1_sel:WORD_1
v_lshlrev_b32_sdwa v155,1,v155 dst_sel:DWORD dst_unused:UNUSED_PAD src0_sel:DWORD src1_sel:WORD_1
v_mfma_f32_16x16x32_bf16 v[136:139],v[64:67],v[20:23],v[112:115]
s_waitcnt lgkmcnt(7)
ds_read_u16_d16_hi v150,v150
ds_read_u16 v149,v149
v_exp_f16_sdwa v120,v121 dst_sel:WORD_1 dst_unused:UNUSED_PRESERVE src0_sel:WORD_1
v_mfma_f32_16x16x32_bf16 v[140:143],v[68:71],v[20:23],v[116:119]
v_exp_f16_sdwa v122,v123 dst_sel:WORD_1 dst_unused:UNUSED_PRESERVE src0_sel:WORD_1
v_mfma_f32_16x16x32_bf16 v[132:135],v[68:71],v[16:19],v[116:119]
s_waitcnt lgkmcnt(7)
ds_read_b64_tr_b16 v[82:83],v4 offset:23072
v_pk_fma_f16 v120,v120,s55,s55
v_mfma_f32_16x16x32_bf16 v[128:131],v[72:75],v[24:27],v[128:131]
ds_read_b64_tr_b16 v[84:85],v5 offset:21024
v_pk_fma_f16 v122,v122,s55,s55
v_rcp_f16 v121,v120
v_mfma_f32_16x16x32_bf16 v[136:139],v[72:75],v[28:31],v[136:139]
s_waitcnt lgkmcnt(5)
ds_read_b64_tr_b16 v[86:87],v5 offset:23072
ds_read_u16_d16_hi v151,v151
v_rcp_f16 v123,v122
v_mfma_f32_16x16x32_bf16 v[140:143],v[76:79],v[28:31],v[140:143]
ds_read_u16 v152,v152
ds_read_u16_d16_hi v154,v154
v_rcp_f16_sdwa v121,v120 dst_sel:WORD_1 dst_unused:UNUSED_PRESERVE src0_sel:WORD_1
v_mfma_f32_16x16x32_bf16 v[132:135],v[76:79],v[24:27],v[132:135]
s_waitcnt lgkmcnt(5)
v_rcp_f16_sdwa v123,v122 dst_sel:WORD_1 dst_unused:UNUSED_PRESERVE src0_sel:WORD_1
v_mfma_f32_16x16x32_bf16 v[128:131],v[80:83],v[32:35],v[128:131]
ds_read_u16 v153,v153
v_fma_mix_f32 v156,v156,v121,0 op_sel_hi:[0,1,0]
v_fma_mix_f32 v158,v158,v121,0 op_sel:[0,1,0] op_sel_hi:[0,1,0]
v_mfma_f32_16x16x32_bf16 v[136:139],v[80:83],v[36:39],v[136:139]
s_waitcnt lgkmcnt(3)
s_setprio 0
v_lshl_add_u64 v[144:145],v[144:145],0,v[146:147]
v_lshl_add_u64 v[146:147],v[148:149],0,v[150:151]
v_mfma_f32_16x16x32_bf16 v[140:143],v[84:87],v[36:39],v[140:143]
ds_read_u16_d16_hi v155,v155
v_fma_mix_f32 v157,v157,v123,0 op_sel_hi:[0,1,0]
v_fma_mix_f32 v159,v159,v123,0 op_sel:[0,1,0] op_sel_hi:[0,1,0]
v_mfma_f32_16x16x32_bf16 v[132:135],v[84:87],v[32:35],v[132:135]
v_lshlrev_b32_sdwa v128,1,v128 dst_sel:DWORD dst_unused:UNUSED_PAD src0_sel:DWORD src1_sel:WORD_1
v_lshlrev_b32_sdwa v130,1,v130 dst_sel:DWORD dst_unused:UNUSED_PAD src0_sel:DWORD src1_sel:WORD_1
v_mfma_f32_16x16x32_bf16 v[208:211],v[88:91],v[144:147],v[208:211]
s_barrier
s_setprio 2
ds_read_u16 v128,v128
v_mfma_f32_16x16x32_bf16 v[216:219],v[92:95],v[144:147],v[216:219]
ds_read_u16_d16_hi v130,v130
v_lshlrev_b32_sdwa v129,1,v129 dst_sel:DWORD dst_unused:UNUSED_PAD src0_sel:DWORD src1_sel:WORD_1
v_lshlrev_b32_sdwa v131,1,v131 dst_sel:DWORD dst_unused:UNUSED_PAD src0_sel:DWORD src1_sel:WORD_1
v_mfma_f32_16x16x32_bf16 v[224:227],v[96:99],v[144:147],v[224:227]
s_cbranch_vccz RB35_%=
s_add_u32 m0,s58,12320
s_mul_i32 s69,s59,3
s_add_u32 s69,s69,s97
buffer_load_dwordx4 v2,s[44:47],s69 offen lds
s_add_u32 m0,m0,1024
s_add_u32 s69,s69,s53
buffer_load_dwordx4 v3,s[44:47],s69 offen lds
s_add_u32 m0,m0,1024
s_add_u32 s69,s69,s53
buffer_load_dwordx4 v2,s[44:47],s69 offen lds
s_add_u32 m0,m0,s50
s_add_u32 s69,s69,s53
buffer_load_dwordx4 v3,s[44:47],s69 offen lds
RB35_%=:
v_lshlrev_b32_sdwa v132,1,v132 dst_sel:DWORD dst_unused:UNUSED_PAD src0_sel:DWORD src1_sel:WORD_1
v_mfma_f32_16x16x32_bf16 v[232:235],v[100:103],v[144:147],v[232:235]
s_waitcnt lgkmcnt(2)
ds_read_u16 v129,v129
v_lshl_add_u64 v[152:153],v[152:153],0,v[154:155]
v_lshlrev_b32_sdwa v134,1,v134 dst_sel:DWORD dst_unused:UNUSED_PAD src0_sel:DWORD src1_sel:WORD_1
v_mfma_f32_16x16x32_bf16 v[240:243],v[104:107],v[144:147],v[240:243]
v_cvt_pk_bf16_f32 v155,v157,v159
v_cvt_pk_bf16_f32 v154,v156,v158
v_mfma_f32_16x16x32_bf16 v[248:251],v[108:111],v[144:147],v[248:251]
ds_read_u16_d16_hi v131,v131
v_lshlrev_b32_sdwa v133,1,v133 dst_sel:DWORD dst_unused:UNUSED_PAD src0_sel:DWORD src1_sel:WORD_1
s_mov_b32 s86,0xbe801d5a
v_mfma_f32_16x16x32_bf16 v[212:215],v[88:91],v[152:155],v[212:215]
s_cbranch_vccz RB36_%=
s_add_u32 m0,m0,1024
s_add_u32 s69,s69,s53
buffer_load_dwordx4 v2,s[44:47],s69 offen lds
s_add_u32 m0,m0,1024
s_add_u32 s69,s69,s53
buffer_load_dwordx4 v3,s[44:47],s69 offen lds
s_add_u32 m0,m0,s50
s_add_u32 s69,s69,s53
buffer_load_dwordx4 v2,s[44:47],s69 offen lds
s_add_u32 m0,m0,1024
s_add_u32 s69,s69,s53
buffer_load_dwordx4 v3,s[44:47],s69 offen lds
RB36_%=:
ds_read_u16 v132,v132
v_lshlrev_b32_sdwa v135,1,v135 dst_sel:DWORD dst_unused:UNUSED_PAD src0_sel:DWORD src1_sel:WORD_1
v_mfma_f32_16x16x32_bf16 v[220:223],v[92:95],v[152:155],v[220:223]
ds_read_u16_d16_hi v134,v134
v_lshlrev_b32_sdwa v136,1,v136 dst_sel:DWORD dst_unused:UNUSED_PAD src0_sel:DWORD src1_sel:WORD_1
v_mfma_f32_16x16x32_bf16 v[228:231],v[96:99],v[152:155],v[228:231]
ds_read_b64_tr_b16 v[88:89],v6 offset:0
ds_read_b64_tr_b16 v[90:91],v6 offset:1536
v_lshlrev_b32_sdwa v138,1,v138 dst_sel:DWORD dst_unused:UNUSED_PAD src0_sel:DWORD src1_sel:WORD_1
v_mfma_f32_16x16x32_bf16 v[236:239],v[100:103],v[152:155],v[236:239]
ds_read_u16 v133,v133
v_lshlrev_b32_sdwa v137,1,v137 dst_sel:DWORD dst_unused:UNUSED_PAD src0_sel:DWORD src1_sel:WORD_1
v_mfma_f32_16x16x32_bf16 v[244:247],v[104:107],v[152:155],v[244:247]
ds_read_u16_d16_hi v135,v135
v_lshlrev_b32_sdwa v139,1,v139 dst_sel:DWORD dst_unused:UNUSED_PAD src0_sel:DWORD src1_sel:WORD_1
v_mfma_f32_16x16x32_bf16 v[252:255],v[108:111],v[152:155],v[252:255]
s_cbranch_vccz RB37_%=
s_add_u32 m0,m0,1024
s_add_u32 s69,s69,s53
buffer_load_dwordx4 v2,s[44:47],s69 offen lds
s_add_u32 m0,m0,s50
s_add_u32 s69,s69,s53
buffer_load_dwordx4 v3,s[44:47],s69 offen lds
s_add_u32 m0,m0,1024
s_add_u32 s69,s69,s53
buffer_load_dwordx4 v2,s[44:47],s69 offen lds
s_add_u32 m0,m0,1024
s_add_u32 s69,s69,s53
buffer_load_dwordx4 v3,s[44:47],s69 offen lds
RB37_%=:
ds_read_b64_tr_b16 v[92:93],v6 offset:8
ds_read_b64_tr_b16 v[94:95],v6 offset:1544
v_lshlrev_b32_sdwa v140,1,v140 dst_sel:DWORD dst_unused:UNUSED_PAD src0_sel:DWORD src1_sel:WORD_1
v_mfma_f32_16x16x32_bf16 v[144:147],v[64:67],v[40:43],v[112:115]
ds_read_u16 v136,v136
v_lshlrev_b32_sdwa v142,1,v142 dst_sel:DWORD dst_unused:UNUSED_PAD src0_sel:DWORD src1_sel:WORD_1
v_mfma_f32_16x16x32_bf16 v[152:155],v[64:67],v[44:47],v[112:115]
ds_read_u16_d16_hi v138,v138
ds_read_b64_tr_b16 v[96:97],v6 offset:64
v_lshlrev_b32_sdwa v141,1,v141 dst_sel:DWORD dst_unused:UNUSED_PAD src0_sel:DWORD src1_sel:WORD_1
v_mfma_f32_16x16x32_bf16 v[156:159],v[68:71],v[44:47],v[116:119]
ds_read_b64_tr_b16 v[98:99],v6 offset:1600
ds_read_b64_tr_b16 v[100:101],v6 offset:72
v_mfma_f32_16x16x32_bf16 v[148:151],v[68:71],v[40:43],v[116:119]
ds_read_u16 v137,v137
v_lshlrev_b32_sdwa v143,1,v143 dst_sel:DWORD dst_unused:UNUSED_PAD src0_sel:DWORD src1_sel:WORD_1
v_mfma_f32_16x16x32_bf16 v[144:147],v[72:75],v[48:51],v[144:147]
ds_read_u16_d16_hi v139,v139
v_mfma_f32_16x16x32_bf16 v[152:155],v[72:75],v[52:55],v[152:155]
ds_read_b64_tr_b16 v[102:103],v6 offset:1608
ds_read_b64_tr_b16 v[104:105],v6 offset:128
v_mfma_f32_16x16x32_bf16 v[156:159],v[76:79],v[52:55],v[156:159]
ds_read_u16 v140,v140
v_mfma_f32_16x16x32_bf16 v[148:151],v[76:79],v[48:51],v[148:151]
s_waitcnt lgkmcnt(15)
ds_read_u16_d16_hi v142,v142
v_lshl_add_u64 v[128:129],v[128:129],0,v[130:131]
v_mfma_f32_16x16x32_bf16 v[144:147],v[80:83],v[56:59],v[144:147]
ds_read_b64_tr_b16 v[106:107],v6 offset:1664
ds_read_b64_tr_b16 v[108:109],v6 offset:136
v_mfma_f32_16x16x32_bf16 v[152:155],v[80:83],v[60:63],v[152:155]
s_waitcnt lgkmcnt(15)
ds_read_u16 v141,v141
v_lshl_add_u64 v[130:131],v[132:133],0,v[134:135]
v_mfma_f32_16x16x32_bf16 v[156:159],v[84:87],v[60:63],v[156:159]
ds_read_u16_d16_hi v143,v143
v_mfma_f32_16x16x32_bf16 v[148:151],v[84:87],v[56:59],v[148:151]
ds_read_b64_tr_b16 v[110:111],v6 offset:1672
ds_read_b64_tr_b16 v[64:65],v4 offset:0
v_lshlrev_b32_sdwa v144,1,v144 dst_sel:DWORD dst_unused:UNUSED_PAD src0_sel:DWORD src1_sel:WORD_1
v_lshlrev_b32_sdwa v146,1,v146 dst_sel:DWORD dst_unused:UNUSED_PAD src0_sel:DWORD src1_sel:WORD_1
v_mfma_f32_16x16x32_bf16 v[160:163],v[88:91],v[128:131],v[160:163]
s_waitcnt lgkmcnt(15)
v_lshlrev_b32_sdwa v145,1,v145 dst_sel:DWORD dst_unused:UNUSED_PAD src0_sel:DWORD src1_sel:WORD_1
v_mfma_f32_16x16x32_bf16 v[168:171],v[92:95],v[128:131],v[168:171]
s_waitcnt lgkmcnt(13)
ds_read_u16 v144,v144
v_lshlrev_b32_sdwa v147,1,v147 dst_sel:DWORD dst_unused:UNUSED_PAD src0_sel:DWORD src1_sel:WORD_1
v_mfma_f32_16x16x32_bf16 v[176:179],v[96:99],v[128:131],v[176:179]
s_waitcnt lgkmcnt(10)
ds_read_b64_tr_b16 v[66:67],v4 offset:2048
ds_read_b128 v[112:115],v7 offset:256
v_mfma_f32_16x16x32_bf16 v[184:187],v[100:103],v[128:131],v[184:187]
s_waitcnt lgkmcnt(8)
ds_read_b64_tr_b16 v[68:69],v5 offset:0
ds_read_u16_d16_hi v146,v146
v_lshlrev_b32_sdwa v148,1,v148 dst_sel:DWORD dst_unused:UNUSED_PAD src0_sel:DWORD src1_sel:WORD_1
v_lshl_add_u64 v[136:137],v[136:137],0,v[138:139]
v_lshlrev_b32_sdwa v150,1,v150 dst_sel:DWORD dst_unused:UNUSED_PAD src0_sel:DWORD src1_sel:WORD_1
v_mfma_f32_16x16x32_bf16 v[192:195],v[104:107],v[128:131],v[192:195]
s_waitcnt lgkmcnt(6)
ds_read_u16 v145,v145
v_lshl_add_u64 v[138:139],v[140:141],0,v[142:143]
v_mfma_f32_16x16x32_bf16 v[200:203],v[108:111],v[128:131],v[200:203]
ds_read_b64_tr_b16 v[70:71],v5 offset:2048
ds_read_b128 v[116:119],v7 offset:288
v_mfma_f32_16x16x32_bf16 v[164:167],v[88:91],v[136:139],v[164:167]
ds_read_u16_d16_hi v147,v147
v_lshlrev_b32_sdwa v149,1,v149 dst_sel:DWORD dst_unused:UNUSED_PAD src0_sel:DWORD src1_sel:WORD_1
v_lshlrev_b32_sdwa v151,1,v151 dst_sel:DWORD dst_unused:UNUSED_PAD src0_sel:DWORD src1_sel:WORD_1
v_mfma_f32_16x16x32_bf16 v[172:175],v[92:95],v[136:139],v[172:175]
ds_read_u16 v148,v148
ds_read_b64_tr_b16 v[72:73],v4 offset:4096
v_lshlrev_b32_sdwa v152,1,v152 dst_sel:DWORD dst_unused:UNUSED_PAD src0_sel:DWORD src1_sel:WORD_1
v_mfma_f32_16x16x32_bf16 v[180:183],v[96:99],v[136:139],v[180:183]
ds_read_b64_tr_b16 v[74:75],v4 offset:6144
ds_read_b64_tr_b16 v[76:77],v5 offset:4096
v_lshlrev_b32_sdwa v154,1,v154 dst_sel:DWORD dst_unused:UNUSED_PAD src0_sel:DWORD src1_sel:WORD_1
v_mfma_f32_16x16x32_bf16 v[188:191],v[100:103],v[136:139],v[188:191]
ds_read_u16_d16_hi v150,v150
v_lshlrev_b32_sdwa v153,1,v153 dst_sel:DWORD dst_unused:UNUSED_PAD src0_sel:DWORD src1_sel:WORD_1
v_mfma_f32_16x16x32_bf16 v[196:199],v[104:107],v[136:139],v[196:199]
ds_read_u16 v149,v149
v_lshlrev_b32_sdwa v155,1,v155 dst_sel:DWORD dst_unused:UNUSED_PAD src0_sel:DWORD src1_sel:WORD_1
v_mfma_f32_16x16x32_bf16 v[204:207],v[108:111],v[136:139],v[204:207]
s_waitcnt lgkmcnt(12)
ds_read_b64_tr_b16 v[78:79],v5 offset:6144
ds_read_b64_tr_b16 v[80:81],v4 offset:8192
v_lshlrev_b32_sdwa v156,1,v156 dst_sel:DWORD dst_unused:UNUSED_PAD src0_sel:DWORD src1_sel:WORD_1
v_mfma_f32_16x16x32_bf16 v[128:131],v[64:67],v[16:19],v[112:115]
ds_read_u16_d16_hi v151,v151
v_lshlrev_b32_sdwa v158,1,v158 dst_sel:DWORD dst_unused:UNUSED_PAD src0_sel:DWORD src1_sel:WORD_1
v_mfma_f32_16x16x32_bf16 v[136:139],v[64:67],v[20:23],v[112:115]
s_waitcnt lgkmcnt(10)
ds_read_u16 v152,v152
ds_read_b64_tr_b16 v[82:83],v4 offset:10240
v_lshlrev_b32_sdwa v157,1,v157 dst_sel:DWORD dst_unused:UNUSED_PAD src0_sel:DWORD src1_sel:WORD_1
v_mfma_f32_16x16x32_bf16 v[140:143],v[68:71],v[20:23],v[116:119]
ds_read_b64_tr_b16 v[84:85],v5 offset:8192
ds_read_b64_tr_b16 v[86:87],v5 offset:10240
v_lshlrev_b32_sdwa v159,1,v159 dst_sel:DWORD dst_unused:UNUSED_PAD src0_sel:DWORD src1_sel:WORD_1
v_mfma_f32_16x16x32_bf16 v[132:135],v[68:71],v[16:19],v[116:119]
s_waitcnt lgkmcnt(10)
ds_read_u16_d16_hi v154,v154
v_mfma_f32_16x16x32_bf16 v[128:131],v[72:75],v[24:27],v[128:131]
ds_read_u16 v153,v153
v_mfma_f32_16x16x32_bf16 v[136:139],v[72:75],v[28:31],v[136:139]
s_waitcnt lgkmcnt(8)
v_mfma_f32_16x16x32_bf16 v[140:143],v[76:79],v[28:31],v[140:143]
ds_read_u16_d16_hi v155,v155
v_mfma_f32_16x16x32_bf16 v[132:135],v[76:79],v[24:27],v[132:135]
s_waitcnt lgkmcnt(5)
ds_read_u16 v156,v156
v_lshl_add_u64 v[144:145],v[144:145],0,v[146:147]
v_mfma_f32_16x16x32_bf16 v[128:131],v[80:83],v[32:35],v[128:131]
v_lshl_add_u64 v[146:147],v[148:149],0,v[150:151]
v_mfma_f32_16x16x32_bf16 v[136:139],v[80:83],v[36:39],v[136:139]
s_waitcnt lgkmcnt(4)
ds_read_u16_d16_hi v158,v158
v_mfma_f32_16x16x32_bf16 v[140:143],v[84:87],v[36:39],v[140:143]
ds_read_u16 v157,v157
v_mfma_f32_16x16x32_bf16 v[132:135],v[84:87],v[32:35],v[132:135]
s_nop 0
v_lshlrev_b32_sdwa v128,1,v128 dst_sel:DWORD dst_unused:UNUSED_PAD src0_sel:DWORD src1_sel:WORD_1
v_lshlrev_b32_sdwa v130,1,v130 dst_sel:DWORD dst_unused:UNUSED_PAD src0_sel:DWORD src1_sel:WORD_1
v_mfma_f32_16x16x32_bf16 v[208:211],v[88:91],v[144:147],v[208:211]
ds_read_u16_d16_hi v159,v159
v_mfma_f32_16x16x32_bf16 v[216:219],v[92:95],v[144:147],v[216:219]
ds_read_u16 v128,v128
v_lshlrev_b32_sdwa v129,1,v129 dst_sel:DWORD dst_unused:UNUSED_PAD src0_sel:DWORD src1_sel:WORD_1
v_lshlrev_b32_sdwa v131,1,v131 dst_sel:DWORD dst_unused:UNUSED_PAD src0_sel:DWORD src1_sel:WORD_1
v_mfma_f32_16x16x32_bf16 v[224:227],v[96:99],v[144:147],v[224:227]
v_lshlrev_b32_sdwa v132,1,v132 dst_sel:DWORD dst_unused:UNUSED_PAD src0_sel:DWORD src1_sel:WORD_1
v_mfma_f32_16x16x32_bf16 v[232:235],v[100:103],v[144:147],v[232:235]
s_waitcnt lgkmcnt(5)
ds_read_u16_d16_hi v130,v130
v_lshl_add_u64 v[152:153],v[152:153],0,v[154:155]
v_lshlrev_b32_sdwa v134,1,v134 dst_sel:DWORD dst_unused:UNUSED_PAD src0_sel:DWORD src1_sel:WORD_1
v_mfma_f32_16x16x32_bf16 v[240:243],v[104:107],v[144:147],v[240:243]
s_waitcnt lgkmcnt(2)
v_lshl_add_u64 v[154:155],v[156:157],0,v[158:159]
v_mfma_f32_16x16x32_bf16 v[248:251],v[108:111],v[144:147],v[248:251]
ds_read_u16 v129,v129
v_lshlrev_b32_sdwa v133,1,v133 dst_sel:DWORD dst_unused:UNUSED_PAD src0_sel:DWORD src1_sel:WORD_1
v_mfma_f32_16x16x32_bf16 v[212:215],v[88:91],v[152:155],v[212:215]
ds_read_u16_d16_hi v131,v131
v_lshlrev_b32_sdwa v135,1,v135 dst_sel:DWORD dst_unused:UNUSED_PAD src0_sel:DWORD src1_sel:WORD_1
v_mfma_f32_16x16x32_bf16 v[220:223],v[92:95],v[152:155],v[220:223]
ds_read_u16 v132,v132
v_lshlrev_b32_sdwa v136,1,v136 dst_sel:DWORD dst_unused:UNUSED_PAD src0_sel:DWORD src1_sel:WORD_1
v_mfma_f32_16x16x32_bf16 v[228:231],v[96:99],v[152:155],v[228:231]
ds_read_b64_tr_b16 v[88:89],v6 offset:6160
ds_read_b64_tr_b16 v[90:91],v6 offset:7696
v_lshlrev_b32_sdwa v138,1,v138 dst_sel:DWORD dst_unused:UNUSED_PAD src0_sel:DWORD src1_sel:WORD_1
v_mfma_f32_16x16x32_bf16 v[236:239],v[100:103],v[152:155],v[236:239]
ds_read_u16_d16_hi v134,v134
v_lshlrev_b32_sdwa v137,1,v137 dst_sel:DWORD dst_unused:UNUSED_PAD src0_sel:DWORD src1_sel:WORD_1
v_mfma_f32_16x16x32_bf16 v[244:247],v[104:107],v[152:155],v[244:247]
ds_read_u16 v133,v133
v_lshlrev_b32_sdwa v139,1,v139 dst_sel:DWORD dst_unused:UNUSED_PAD src0_sel:DWORD src1_sel:WORD_1
v_mfma_f32_16x16x32_bf16 v[252:255],v[108:111],v[152:155],v[252:255]
ds_read_b64_tr_b16 v[92:93],v6 offset:6168
ds_read_b64_tr_b16 v[94:95],v6 offset:7704
v_lshlrev_b32_sdwa v140,1,v140 dst_sel:DWORD dst_unused:UNUSED_PAD src0_sel:DWORD src1_sel:WORD_1
v_mfma_f32_16x16x32_bf16 v[144:147],v[64:67],v[40:43],v[112:115]
ds_read_u16_d16_hi v135,v135
v_lshlrev_b32_sdwa v142,1,v142 dst_sel:DWORD dst_unused:UNUSED_PAD src0_sel:DWORD src1_sel:WORD_1
v_mfma_f32_16x16x32_bf16 v[152:155],v[64:67],v[44:47],v[112:115]
ds_read_u16 v136,v136
ds_read_b64_tr_b16 v[96:97],v6 offset:6224
v_lshlrev_b32_sdwa v141,1,v141 dst_sel:DWORD dst_unused:UNUSED_PAD src0_sel:DWORD src1_sel:WORD_1
v_mfma_f32_16x16x32_bf16 v[156:159],v[68:71],v[44:47],v[116:119]
ds_read_b64_tr_b16 v[98:99],v6 offset:7760
ds_read_b64_tr_b16 v[100:101],v6 offset:6232
v_mfma_f32_16x16x32_bf16 v[148:151],v[68:71],v[40:43],v[116:119]
ds_read_u16_d16_hi v138,v138
v_lshlrev_b32_sdwa v143,1,v143 dst_sel:DWORD dst_unused:UNUSED_PAD src0_sel:DWORD src1_sel:WORD_1
v_mfma_f32_16x16x32_bf16 v[144:147],v[72:75],v[48:51],v[144:147]
ds_read_u16 v137,v137
v_mfma_f32_16x16x32_bf16 v[152:155],v[72:75],v[52:55],v[152:155]
ds_read_b64_tr_b16 v[102:103],v6 offset:7768
ds_read_b64_tr_b16 v[104:105],v6 offset:6288
v_mfma_f32_16x16x32_bf16 v[156:159],v[76:79],v[52:55],v[156:159]
ds_read_u16_d16_hi v139,v139
v_mfma_f32_16x16x32_bf16 v[148:151],v[76:79],v[48:51],v[148:151]
s_waitcnt lgkmcnt(15)
ds_read_u16 v140,v140
v_lshl_add_u64 v[128:129],v[128:129],0,v[130:131]
v_mfma_f32_16x16x32_bf16 v[144:147],v[80:83],v[56:59],v[144:147]
ds_read_b64_tr_b16 v[106:107],v6 offset:7824
ds_read_b64_tr_b16 v[108:109],v6 offset:6296
v_mfma_f32_16x16x32_bf16 v[152:155],v[80:83],v[60:63],v[152:155]
s_waitcnt lgkmcnt(12)
ds_read_u16_d16_hi v142,v142
v_lshl_add_u64 v[130:131],v[132:133],0,v[134:135]
v_mfma_f32_16x16x32_bf16 v[156:159],v[84:87],v[60:63],v[156:159]
ds_read_u16 v141,v141
ds_read_u16_d16_hi v143,v143
v_mfma_f32_16x16x32_bf16 v[148:151],v[84:87],v[56:59],v[148:151]
ds_read_b64_tr_b16 v[110:111],v6 offset:7832
ds_read_b64_tr_b16 v[64:65],v4 offset:512
v_lshlrev_b32_sdwa v144,1,v144 dst_sel:DWORD dst_unused:UNUSED_PAD src0_sel:DWORD src1_sel:WORD_1
v_lshlrev_b32_sdwa v146,1,v146 dst_sel:DWORD dst_unused:UNUSED_PAD src0_sel:DWORD src1_sel:WORD_1
v_mfma_f32_16x16x32_bf16 v[160:163],v[88:91],v[128:131],v[160:163]
v_lshlrev_b32_sdwa v145,1,v145 dst_sel:DWORD dst_unused:UNUSED_PAD src0_sel:DWORD src1_sel:WORD_1
v_mfma_f32_16x16x32_bf16 v[168:171],v[92:95],v[128:131],v[168:171]
s_waitcnt lgkmcnt(14)
ds_read_u16 v144,v144
v_lshlrev_b32_sdwa v147,1,v147 dst_sel:DWORD dst_unused:UNUSED_PAD src0_sel:DWORD src1_sel:WORD_1
v_mfma_f32_16x16x32_bf16 v[176:179],v[96:99],v[128:131],v[176:179]
s_waitcnt lgkmcnt(11)
ds_read_b64_tr_b16 v[66:67],v4 offset:2560
ds_read_b128 v[112:115],v7 offset:384
v_mfma_f32_16x16x32_bf16 v[184:187],v[100:103],v[128:131],v[184:187]
s_waitcnt lgkmcnt(9)
ds_read_b64_tr_b16 v[68:69],v5 offset:512
ds_read_u16_d16_hi v146,v146
v_lshlrev_b32_sdwa v148,1,v148 dst_sel:DWORD dst_unused:UNUSED_PAD src0_sel:DWORD src1_sel:WORD_1
v_lshl_add_u64 v[136:137],v[136:137],0,v[138:139]
v_lshlrev_b32_sdwa v150,1,v150 dst_sel:DWORD dst_unused:UNUSED_PAD src0_sel:DWORD src1_sel:WORD_1
v_mfma_f32_16x16x32_bf16 v[192:195],v[104:107],v[128:131],v[192:195]
s_waitcnt lgkmcnt(6)
ds_read_u16 v145,v145
v_lshl_add_u64 v[138:139],v[140:141],0,v[142:143]
v_mfma_f32_16x16x32_bf16 v[200:203],v[108:111],v[128:131],v[200:203]
ds_read_b64_tr_b16 v[70:71],v5 offset:2560
ds_read_b128 v[116:119],v7 offset:416
v_mfma_f32_16x16x32_bf16 v[164:167],v[88:91],v[136:139],v[164:167]
ds_read_u16_d16_hi v147,v147
v_lshlrev_b32_sdwa v149,1,v149 dst_sel:DWORD dst_unused:UNUSED_PAD src0_sel:DWORD src1_sel:WORD_1
v_lshlrev_b32_sdwa v151,1,v151 dst_sel:DWORD dst_unused:UNUSED_PAD src0_sel:DWORD src1_sel:WORD_1
v_mfma_f32_16x16x32_bf16 v[172:175],v[92:95],v[136:139],v[172:175]
ds_read_u16 v148,v148
ds_read_b64_tr_b16 v[72:73],v4 offset:4608
v_lshlrev_b32_sdwa v152,1,v152 dst_sel:DWORD dst_unused:UNUSED_PAD src0_sel:DWORD src1_sel:WORD_1
v_mfma_f32_16x16x32_bf16 v[180:183],v[96:99],v[136:139],v[180:183]
ds_read_b64_tr_b16 v[74:75],v4 offset:6656
ds_read_b64_tr_b16 v[76:77],v5 offset:4608
v_lshlrev_b32_sdwa v154,1,v154 dst_sel:DWORD dst_unused:UNUSED_PAD src0_sel:DWORD src1_sel:WORD_1
v_mfma_f32_16x16x32_bf16 v[188:191],v[100:103],v[136:139],v[188:191]
ds_read_u16_d16_hi v150,v150
v_lshlrev_b32_sdwa v153,1,v153 dst_sel:DWORD dst_unused:UNUSED_PAD src0_sel:DWORD src1_sel:WORD_1
v_mfma_f32_16x16x32_bf16 v[196:199],v[104:107],v[136:139],v[196:199]
ds_read_u16 v149,v149
v_lshlrev_b32_sdwa v155,1,v155 dst_sel:DWORD dst_unused:UNUSED_PAD src0_sel:DWORD src1_sel:WORD_1
v_mfma_f32_16x16x32_bf16 v[204:207],v[108:111],v[136:139],v[204:207]
s_waitcnt lgkmcnt(12)
ds_read_b64_tr_b16 v[78:79],v5 offset:6656
ds_read_b64_tr_b16 v[80:81],v4 offset:8704
v_lshlrev_b32_sdwa v156,1,v156 dst_sel:DWORD dst_unused:UNUSED_PAD src0_sel:DWORD src1_sel:WORD_1
v_mfma_f32_16x16x32_bf16 v[128:131],v[64:67],v[16:19],v[112:115]
ds_read_u16_d16_hi v151,v151
v_lshlrev_b32_sdwa v158,1,v158 dst_sel:DWORD dst_unused:UNUSED_PAD src0_sel:DWORD src1_sel:WORD_1
v_mfma_f32_16x16x32_bf16 v[136:139],v[64:67],v[20:23],v[112:115]
s_waitcnt lgkmcnt(10)
ds_read_u16 v152,v152
ds_read_b64_tr_b16 v[82:83],v4 offset:10752
v_lshlrev_b32_sdwa v157,1,v157 dst_sel:DWORD dst_unused:UNUSED_PAD src0_sel:DWORD src1_sel:WORD_1
v_mfma_f32_16x16x32_bf16 v[140:143],v[68:71],v[20:23],v[116:119]
ds_read_b64_tr_b16 v[84:85],v5 offset:8704
ds_read_b64_tr_b16 v[86:87],v5 offset:10752
v_lshlrev_b32_sdwa v159,1,v159 dst_sel:DWORD dst_unused:UNUSED_PAD src0_sel:DWORD src1_sel:WORD_1
v_mfma_f32_16x16x32_bf16 v[132:135],v[68:71],v[16:19],v[116:119]
s_waitcnt lgkmcnt(10)
ds_read_u16_d16_hi v154,v154
v_mfma_f32_16x16x32_bf16 v[128:131],v[72:75],v[24:27],v[128:131]
ds_read_u16 v153,v153
v_mfma_f32_16x16x32_bf16 v[136:139],v[72:75],v[28:31],v[136:139]
s_waitcnt lgkmcnt(8)
v_mfma_f32_16x16x32_bf16 v[140:143],v[76:79],v[28:31],v[140:143]
ds_read_u16_d16_hi v155,v155
v_mfma_f32_16x16x32_bf16 v[132:135],v[76:79],v[24:27],v[132:135]
s_waitcnt lgkmcnt(5)
ds_read_u16 v156,v156
v_lshl_add_u64 v[144:145],v[144:145],0,v[146:147]
v_mfma_f32_16x16x32_bf16 v[128:131],v[80:83],v[32:35],v[128:131]
v_lshl_add_u64 v[146:147],v[148:149],0,v[150:151]
v_mfma_f32_16x16x32_bf16 v[136:139],v[80:83],v[36:39],v[136:139]
s_waitcnt lgkmcnt(4)
ds_read_u16_d16_hi v158,v158
v_mfma_f32_16x16x32_bf16 v[140:143],v[84:87],v[36:39],v[140:143]
ds_read_u16 v157,v157
v_mfma_f32_16x16x32_bf16 v[132:135],v[84:87],v[32:35],v[132:135]
s_nop 0
v_lshlrev_b32_sdwa v128,1,v128 dst_sel:DWORD dst_unused:UNUSED_PAD src0_sel:DWORD src1_sel:WORD_1
v_lshlrev_b32_sdwa v130,1,v130 dst_sel:DWORD dst_unused:UNUSED_PAD src0_sel:DWORD src1_sel:WORD_1
v_mfma_f32_16x16x32_bf16 v[208:211],v[88:91],v[144:147],v[208:211]
s_waitcnt vmcnt(0)
s_barrier
s_setprio 3
ds_read_u16_d16_hi v159,v159
v_fma_mixlo_f16 v120,v140,s99,0
v_lshlrev_b32_sdwa v129,1,v129 dst_sel:DWORD dst_unused:UNUSED_PAD src0_sel:DWORD src1_sel:WORD_1
v_lshlrev_b32_sdwa v131,1,v131 dst_sel:DWORD dst_unused:UNUSED_PAD src0_sel:DWORD src1_sel:WORD_1
v_mfma_f32_16x16x32_bf16 v[216:219],v[92:95],v[144:147],v[216:219]
s_cbranch_vccz RB38_%=
s_add_u32 m0,s58,0
s_mul_i32 s69,s59,5
s_add_u32 s69,s69,s97
buffer_load_dwordx4 v2,s[44:47],s69 offen lds
s_add_u32 m0,m0,1024
s_add_u32 s69,s69,s53
buffer_load_dwordx4 v3,s[44:47],s69 offen lds
s_add_u32 m0,m0,1024
s_add_u32 s69,s69,s53
buffer_load_dwordx4 v2,s[44:47],s69 offen lds
s_add_u32 m0,m0,s50
s_add_u32 s69,s69,s53
buffer_load_dwordx4 v3,s[44:47],s69 offen lds
RB38_%=:
v_fma_mixlo_f16 v122,v141,s99,0
v_lshlrev_b32_sdwa v132,1,v132 dst_sel:DWORD dst_unused:UNUSED_PAD src0_sel:DWORD src1_sel:WORD_1
v_mfma_f32_16x16x32_bf16 v[224:227],v[96:99],v[144:147],v[224:227]
v_lshlrev_b32_sdwa v134,1,v134 dst_sel:DWORD dst_unused:UNUSED_PAD src0_sel:DWORD src1_sel:WORD_1
v_mfma_f32_16x16x32_bf16 v[232:235],v[100:103],v[144:147],v[232:235]
s_waitcnt lgkmcnt(4)
ds_read_u16 v128,v128
v_lshl_add_u64 v[152:153],v[152:153],0,v[154:155]
v_mfma_f32_16x16x32_bf16 v[240:243],v[104:107],v[144:147],v[240:243]
s_waitcnt lgkmcnt(1)
ds_read_u16_d16_hi v130,v130
v_lshl_add_u64 v[154:155],v[156:157],0,v[158:159]
v_mfma_f32_16x16x32_bf16 v[248:251],v[108:111],v[144:147],v[248:251]
v_fma_mixhi_f16 v120,v142,s99,0
v_fma_mixhi_f16 v122,v143,s99,0
v_mfma_f32_16x16x32_bf16 v[212:215],v[88:91],v[152:155],v[212:215]
s_cbranch_vccz RB39_%=
s_add_u32 m0,m0,1024
s_add_u32 s69,s69,s53
buffer_load_dwordx4 v2,s[44:47],s69 offen lds
s_add_u32 m0,m0,1024
s_add_u32 s69,s69,s53
buffer_load_dwordx4 v3,s[44:47],s69 offen lds
s_add_u32 m0,m0,s50
s_add_u32 s69,s69,s53
buffer_load_dwordx4 v2,s[44:47],s69 offen lds
s_add_u32 m0,m0,1024
s_add_u32 s69,s69,s53
buffer_load_dwordx4 v3,s[44:47],s69 offen lds
RB39_%=:
ds_read_u16 v129,v129
v_lshlrev_b32_sdwa v133,1,v133 dst_sel:DWORD dst_unused:UNUSED_PAD src0_sel:DWORD src1_sel:WORD_1
v_lshlrev_b32_sdwa v135,1,v135 dst_sel:DWORD dst_unused:UNUSED_PAD src0_sel:DWORD src1_sel:WORD_1
v_mfma_f32_16x16x32_bf16 v[220:223],v[92:95],v[152:155],v[220:223]
v_pk_fma_f16 v121,v120,v120,s54
v_pk_fma_f16 v123,v122,v122,s54
v_mfma_f32_16x16x32_bf16 v[228:231],v[96:99],v[152:155],v[228:231]
ds_read_u16_d16_hi v131,v131
v_pk_mul_f16 v121,v120,v121
v_pk_mul_f16 v123,v122,v123
v_mfma_f32_16x16x32_bf16 v[236:239],v[100:103],v[152:155],v[236:239]
ds_read_b64_tr_b16 v[88:89],v6 offset:12320
ds_read_b64_tr_b16 v[90:91],v6 offset:13856
v_lshlrev_b32_sdwa v136,1,v136 dst_sel:DWORD dst_unused:UNUSED_PAD src0_sel:DWORD src1_sel:WORD_1
v_mfma_f32_16x16x32_bf16 v[244:247],v[104:107],v[152:155],v[244:247]
s_cbranch_vccz RB40_%=
s_add_u32 m0,m0,1024
s_add_u32 s69,s69,s53
buffer_load_dwordx4 v2,s[44:47],s69 offen lds
s_add_u32 m0,m0,s50
s_add_u32 s69,s69,s53
buffer_load_dwordx4 v3,s[44:47],s69 offen lds
s_add_u32 m0,m0,1024
s_add_u32 s69,s69,s53
buffer_load_dwordx4 v2,s[44:47],s69 offen lds
s_add_u32 m0,m0,1024
s_add_u32 s69,s69,s53
buffer_load_dwordx4 v3,s[44:47],s69 offen lds
RB40_%=:
ds_read_u16 v132,v132
v_lshlrev_b32_sdwa v138,1,v138 dst_sel:DWORD dst_unused:UNUSED_PAD src0_sel:DWORD src1_sel:WORD_1
v_exp_f16 v120,v121
v_mfma_f32_16x16x32_bf16 v[252:255],v[108:111],v[152:155],v[252:255]
ds_read_b64_tr_b16 v[92:93],v6 offset:12328
v_exp_f16 v122,v123
v_mfma_f32_16x16x32_bf16 v[144:147],v[64:67],v[40:43],v[112:115]
ds_read_u16_d16_hi v134,v134
v_lshlrev_b32_sdwa v137,1,v137 dst_sel:DWORD dst_unused:UNUSED_PAD src0_sel:DWORD src1_sel:WORD_1
v_lshlrev_b32_sdwa v139,1,v139 dst_sel:DWORD dst_unused:UNUSED_PAD src0_sel:DWORD src1_sel:WORD_1
v_mfma_f32_16x16x32_bf16 v[152:155],v[64:67],v[44:47],v[112:115]
ds_read_b64_tr_b16 v[94:95],v6 offset:13864
v_exp_f16_sdwa v120,v121 dst_sel:WORD_1 dst_unused:UNUSED_PRESERVE src0_sel:WORD_1
v_mfma_f32_16x16x32_bf16 v[156:159],v[68:71],v[44:47],v[116:119]
ds_read_u16 v133,v133
v_exp_f16_sdwa v122,v123 dst_sel:WORD_1 dst_unused:UNUSED_PRESERVE src0_sel:WORD_1
v_mfma_f32_16x16x32_bf16 v[148:151],v[68:71],v[40:43],v[116:119]
ds_read_b64_tr_b16 v[96:97],v6 offset:12384
v_pk_fma_f16 v120,v120,s55,s55
v_mfma_f32_16x16x32_bf16 v[144:147],v[72:75],v[48:51],v[144:147]
ds_read_u16_d16_hi v135,v135
v_rcp_f16 v121,v120
v_pk_fma_f16 v122,v122,s55,s55
v_mfma_f32_16x16x32_bf16 v[152:155],v[72:75],v[52:55],v[152:155]
ds_read_b64_tr_b16 v[98:99],v6 offset:13920
v_rcp_f16 v123,v122
v_mfma_f32_16x16x32_bf16 v[156:159],v[76:79],v[52:55],v[156:159]
ds_read_u16 v136,v136
v_rcp_f16_sdwa v121,v120 dst_sel:WORD_1 dst_unused:UNUSED_PRESERVE src0_sel:WORD_1
v_mfma_f32_16x16x32_bf16 v[148:151],v[76:79],v[48:51],v[148:151]
ds_read_b64_tr_b16 v[100:101],v6 offset:12392
ds_read_b64_tr_b16 v[102:103],v6 offset:13928
v_rcp_f16_sdwa v123,v122 dst_sel:WORD_1 dst_unused:UNUSED_PRESERVE src0_sel:WORD_1
v_mfma_f32_16x16x32_bf16 v[144:147],v[80:83],v[56:59],v[144:147]
s_waitcnt lgkmcnt(13)
ds_read_u16_d16_hi v138,v138
v_lshl_add_u64 v[128:129],v[128:129],0,v[130:131]
v_fma_mix_f32 v140,v140,v121,0 op_sel_hi:[0,1,0]
v_fma_mix_f32 v142,v142,v121,0 op_sel:[0,1,0] op_sel_hi:[0,1,0]
v_mfma_f32_16x16x32_bf16 v[152:155],v[80:83],v[60:63],v[152:155]
ds_read_b64_tr_b16 v[104:105],v6 offset:12448
ds_read_b64_tr_b16 v[106:107],v6 offset:13984
v_mfma_f32_16x16x32_bf16 v[156:159],v[84:87],v[60:63],v[156:159]
s_waitcnt lgkmcnt(7)
ds_read_u16 v137,v137
v_lshl_add_u64 v[130:131],v[132:133],0,v[134:135]
v_fma_mix_f32 v141,v141,v123,0 op_sel_hi:[0,1,0]
v_fma_mix_f32 v143,v143,v123,0 op_sel:[0,1,0] op_sel_hi:[0,1,0]
v_mfma_f32_16x16x32_bf16 v[148:151],v[84:87],v[56:59],v[148:151]
ds_read_u16_d16_hi v139,v139
v_lshlrev_b32_sdwa v144,1,v144 dst_sel:DWORD dst_unused:UNUSED_PAD src0_sel:DWORD src1_sel:WORD_1
v_lshlrev_b32_sdwa v146,1,v146 dst_sel:DWORD dst_unused:UNUSED_PAD src0_sel:DWORD src1_sel:WORD_1
v_mfma_f32_16x16x32_bf16 v[160:163],v[88:91],v[128:131],v[160:163]
ds_read_b64_tr_b16 v[108:109],v6 offset:12456
ds_read_b64_tr_b16 v[110:111],v6 offset:13992
v_lshlrev_b32_sdwa v145,1,v145 dst_sel:DWORD dst_unused:UNUSED_PAD src0_sel:DWORD src1_sel:WORD_1
v_lshlrev_b32_sdwa v147,1,v147 dst_sel:DWORD dst_unused:UNUSED_PAD src0_sel:DWORD src1_sel:WORD_1
v_mfma_f32_16x16x32_bf16 v[168:171],v[92:95],v[128:131],v[168:171]
s_waitcnt lgkmcnt(10)
ds_read_b64_tr_b16 v[64:65],v4 offset:12320
ds_read_b64_tr_b16 v[66:67],v4 offset:14368
v_fma_mixlo_f16 v120,v156,s99,0
v_mfma_f32_16x16x32_bf16 v[176:179],v[96:99],v[128:131],v[176:179]
s_waitcnt lgkmcnt(9)
ds_read_b128 v[112:115],v7 offset:512
ds_read_u16 v144,v144
v_fma_mixlo_f16 v122,v157,s99,0
v_lshlrev_b32_sdwa v148,1,v148 dst_sel:DWORD dst_unused:UNUSED_PAD src0_sel:DWORD src1_sel:WORD_1
v_lshlrev_b32_sdwa v150,1,v150 dst_sel:DWORD dst_unused:UNUSED_PAD src0_sel:DWORD src1_sel:WORD_1
v_mfma_f32_16x16x32_bf16 v[184:187],v[100:103],v[128:131],v[184:187]
s_waitcnt lgkmcnt(6)
ds_read_b64_tr_b16 v[68:69],v5 offset:12320
v_lshl_add_u64 v[136:137],v[136:137],0,v[138:139]
v_mfma_f32_16x16x32_bf16 v[192:195],v[104:107],v[128:131],v[192:195]
s_waitcnt lgkmcnt(5)
ds_read_u16_d16_hi v146,v146
v_cvt_pk_bf16_f32 v138,v140,v142
v_cvt_pk_bf16_f32 v139,v141,v143
v_mfma_f32_16x16x32_bf16 v[200:203],v[108:111],v[128:131],v[200:203]
ds_read_b64_tr_b16 v[70:71],v5 offset:14368
ds_read_b128 v[116:119],v7 offset:544
v_fma_mixhi_f16 v120,v158,s99,0
v_fma_mixhi_f16 v122,v159,s99,0
v_mfma_f32_16x16x32_bf16 v[164:167],v[88:91],v[136:139],v[164:167]
v_lshlrev_b32_sdwa v149,1,v149 dst_sel:DWORD dst_unused:UNUSED_PAD src0_sel:DWORD src1_sel:WORD_1
v_lshlrev_b32_sdwa v151,1,v151 dst_sel:DWORD dst_unused:UNUSED_PAD src0_sel:DWORD src1_sel:WORD_1
v_mfma_f32_16x16x32_bf16 v[172:175],v[92:95],v[136:139],v[172:175]
ds_read_b64_tr_b16 v[72:73],v4 offset:16416
ds_read_b64_tr_b16 v[74:75],v4 offset:18464
v_pk_fma_f16 v123,v122,v122,s54
v_pk_fma_f16 v121,v120,v120,s54
v_mfma_f32_16x16x32_bf16 v[180:183],v[96:99],v[136:139],v[180:183]
ds_read_u16 v145,v145
v_pk_mul_f16 v121,v120,v121
v_pk_mul_f16 v123,v122,v123
v_mfma_f32_16x16x32_bf16 v[188:191],v[100:103],v[136:139],v[188:191]
ds_read_b64_tr_b16 v[76:77],v5 offset:16416
v_lshlrev_b32_sdwa v152,1,v152 dst_sel:DWORD dst_unused:UNUSED_PAD src0_sel:DWORD src1_sel:WORD_1
v_lshlrev_b32_sdwa v154,1,v154 dst_sel:DWORD dst_unused:UNUSED_PAD src0_sel:DWORD src1_sel:WORD_1
v_mfma_f32_16x16x32_bf16 v[196:199],v[104:107],v[136:139],v[196:199]
ds_read_u16_d16_hi v147,v147
v_exp_f16 v120,v121
v_mfma_f32_16x16x32_bf16 v[204:207],v[108:111],v[136:139],v[204:207]
s_waitcnt lgkmcnt(10)
ds_read_b64_tr_b16 v[78:79],v5 offset:18464
v_exp_f16 v122,v123
v_mfma_f32_16x16x32_bf16 v[128:131],v[64:67],v[16:19],v[112:115]
ds_read_u16 v148,v148
ds_read_u16_d16_hi v150,v150
v_lshlrev_b32_sdwa v153,1,v153 dst_sel:DWORD dst_unused:UNUSED_PAD src0_sel:DWORD src1_sel:WORD_1
v_lshlrev_b32_sdwa v155,1,v155 dst_sel:DWORD dst_unused:UNUSED_PAD src0_sel:DWORD src1_sel:WORD_1
v_exp_f16_sdwa v122,v123 dst_sel:WORD_1 dst_unused:UNUSED_PRESERVE src0_sel:WORD_1
v_mfma_f32_16x16x32_bf16 v[136:139],v[64:67],v[20:23],v[112:115]
s_waitcnt lgkmcnt(8)
v_exp_f16_sdwa v120,v121 dst_sel:WORD_1 dst_unused:UNUSED_PRESERVE src0_sel:WORD_1
v_mfma_f32_16x16x32_bf16 v[140:143],v[68:71],v[20:23],v[116:119]
ds_read_b64_tr_b16 v[80:81],v4 offset:20512
ds_read_b64_tr_b16 v[82:83],v4 offset:22560
v_pk_fma_f16 v120,v120,s55,s55
v_mfma_f32_16x16x32_bf16 v[132:135],v[68:71],v[16:19],v[116:119]
s_waitcnt lgkmcnt(8)
ds_read_u16 v149,v149
v_pk_fma_f16 v122,v122,s55,s55
v_mfma_f32_16x16x32_bf16 v[128:131],v[72:75],v[24:27],v[128:131]
ds_read_b64_tr_b16 v[84:85],v5 offset:20512
ds_read_u16_d16_hi v151,v151
v_rcp_f16 v121,v120
v_mfma_f32_16x16x32_bf16 v[136:139],v[72:75],v[28:31],v[136:139]
s_waitcnt lgkmcnt(7)
ds_read_b64_tr_b16 v[86:87],v5 offset:22560
v_rcp_f16 v123,v122
v_mfma_f32_16x16x32_bf16 v[140:143],v[76:79],v[28:31],v[140:143]
ds_read_u16 v152,v152
v_rcp_f16_sdwa v121,v120 dst_sel:WORD_1 dst_unused:UNUSED_PRESERVE src0_sel:WORD_1
v_mfma_f32_16x16x32_bf16 v[132:135],v[76:79],v[24:27],v[132:135]
s_waitcnt lgkmcnt(5)
v_rcp_f16_sdwa v123,v122 dst_sel:WORD_1 dst_unused:UNUSED_PRESERVE src0_sel:WORD_1
v_mfma_f32_16x16x32_bf16 v[128:131],v[80:83],v[32:35],v[128:131]
ds_read_u16_d16_hi v154,v154
v_fma_mix_f32 v156,v156,v121,0 op_sel_hi:[0,1,0]
v_mfma_f32_16x16x32_bf16 v[136:139],v[80:83],v[36:39],v[136:139]
s_waitcnt lgkmcnt(2)
v_lshl_add_u64 v[144:145],v[144:145],0,v[146:147]
v_fma_mix_f32 v158,v158,v121,0 op_sel:[0,1,0] op_sel_hi:[0,1,0]
v_lshl_add_u64 v[146:147],v[148:149],0,v[150:151]
s_mov_b32 s86,0xbe801d5a
v_mfma_f32_16x16x32_bf16 v[140:143],v[84:87],v[36:39],v[140:143]
ds_read_u16 v153,v153
v_fma_mix_f32 v157,v157,v123,0 op_sel_hi:[0,1,0]
v_fma_mix_f32 v159,v159,v123,0 op_sel:[0,1,0] op_sel_hi:[0,1,0]
v_mfma_f32_16x16x32_bf16 v[132:135],v[84:87],v[32:35],v[132:135]
v_lshlrev_b32_sdwa v128,1,v128 dst_sel:DWORD dst_unused:UNUSED_PAD src0_sel:DWORD src1_sel:WORD_1
v_lshlrev_b32_sdwa v130,1,v130 dst_sel:DWORD dst_unused:UNUSED_PAD src0_sel:DWORD src1_sel:WORD_1
v_mfma_f32_16x16x32_bf16 v[208:211],v[88:91],v[144:147],v[208:211]
ds_read_u16_d16_hi v155,v155
v_fma_mixlo_f16 v120,v140,s99,0
v_lshlrev_b32_sdwa v129,1,v129 dst_sel:DWORD dst_unused:UNUSED_PAD src0_sel:DWORD src1_sel:WORD_1
v_lshlrev_b32_sdwa v131,1,v131 dst_sel:DWORD dst_unused:UNUSED_PAD src0_sel:DWORD src1_sel:WORD_1
v_mfma_f32_16x16x32_bf16 v[216:219],v[92:95],v[144:147],v[216:219]
v_fma_mixlo_f16 v122,v141,s99,0
v_lshlrev_b32_sdwa v132,1,v132 dst_sel:DWORD dst_unused:UNUSED_PAD src0_sel:DWORD src1_sel:WORD_1
v_mfma_f32_16x16x32_bf16 v[224:227],v[96:99],v[144:147],v[224:227]
v_lshlrev_b32_sdwa v134,1,v134 dst_sel:DWORD dst_unused:UNUSED_PAD src0_sel:DWORD src1_sel:WORD_1
v_mfma_f32_16x16x32_bf16 v[232:235],v[100:103],v[144:147],v[232:235]
s_waitcnt lgkmcnt(0)
ds_read_u16 v128,v128
v_lshl_add_u64 v[152:153],v[152:153],0,v[154:155]
v_mfma_f32_16x16x32_bf16 v[240:243],v[104:107],v[144:147],v[240:243]
ds_read_u16_d16_hi v130,v130
v_cvt_pk_bf16_f32 v155,v157,v159
v_cvt_pk_bf16_f32 v154,v156,v158
v_mfma_f32_16x16x32_bf16 v[248:251],v[108:111],v[144:147],v[248:251]
v_fma_mixhi_f16 v120,v142,s99,0
v_fma_mixhi_f16 v122,v143,s99,0
v_mfma_f32_16x16x32_bf16 v[212:215],v[88:91],v[152:155],v[212:215]
s_setprio 2
ds_read_u16 v129,v129
v_lshlrev_b32_sdwa v133,1,v133 dst_sel:DWORD dst_unused:UNUSED_PAD src0_sel:DWORD src1_sel:WORD_1
v_lshlrev_b32_sdwa v135,1,v135 dst_sel:DWORD dst_unused:UNUSED_PAD src0_sel:DWORD src1_sel:WORD_1
v_mfma_f32_16x16x32_bf16 v[220:223],v[92:95],v[152:155],v[220:223]
v_pk_fma_f16 v121,v120,v120,s54
v_pk_fma_f16 v123,v122,v122,s54
v_mfma_f32_16x16x32_bf16 v[228:231],v[96:99],v[152:155],v[228:231]
ds_read_u16_d16_hi v131,v131
v_pk_mul_f16 v121,v120,v121
v_pk_mul_f16 v123,v122,v123
v_mfma_f32_16x16x32_bf16 v[236:239],v[100:103],v[152:155],v[236:239]
ds_read_b64_tr_b16 v[88:89],v6 offset:18480
ds_read_b64_tr_b16 v[90:91],v6 offset:20016
v_lshlrev_b32_sdwa v136,1,v136 dst_sel:DWORD dst_unused:UNUSED_PAD src0_sel:DWORD src1_sel:WORD_1
v_mfma_f32_16x16x32_bf16 v[244:247],v[104:107],v[152:155],v[244:247]
ds_read_u16 v132,v132
v_lshlrev_b32_sdwa v138,1,v138 dst_sel:DWORD dst_unused:UNUSED_PAD src0_sel:DWORD src1_sel:WORD_1
v_exp_f16 v120,v121
v_mfma_f32_16x16x32_bf16 v[252:255],v[108:111],v[152:155],v[252:255]
ds_read_b64_tr_b16 v[92:93],v6 offset:18488
v_exp_f16 v122,v123
v_mfma_f32_16x16x32_bf16 v[144:147],v[64:67],v[40:43],v[112:115]
ds_read_u16_d16_hi v134,v134
v_lshlrev_b32_sdwa v137,1,v137 dst_sel:DWORD dst_unused:UNUSED_PAD src0_sel:DWORD src1_sel:WORD_1
v_lshlrev_b32_sdwa v139,1,v139 dst_sel:DWORD dst_unused:UNUSED_PAD src0_sel:DWORD src1_sel:WORD_1
v_mfma_f32_16x16x32_bf16 v[152:155],v[64:67],v[44:47],v[112:115]
ds_read_b64_tr_b16 v[94:95],v6 offset:20024
v_exp_f16_sdwa v120,v121 dst_sel:WORD_1 dst_unused:UNUSED_PRESERVE src0_sel:WORD_1
v_mfma_f32_16x16x32_bf16 v[156:159],v[68:71],v[44:47],v[116:119]
ds_read_u16 v133,v133
v_exp_f16_sdwa v122,v123 dst_sel:WORD_1 dst_unused:UNUSED_PRESERVE src0_sel:WORD_1
v_mfma_f32_16x16x32_bf16 v[148:151],v[68:71],v[40:43],v[116:119]
ds_read_b64_tr_b16 v[96:97],v6 offset:18544
v_pk_fma_f16 v120,v120,s55,s55
v_mfma_f32_16x16x32_bf16 v[144:147],v[72:75],v[48:51],v[144:147]
ds_read_u16_d16_hi v135,v135
v_rcp_f16 v121,v120
v_pk_fma_f16 v122,v122,s55,s55
v_mfma_f32_16x16x32_bf16 v[152:155],v[72:75],v[52:55],v[152:155]
ds_read_b64_tr_b16 v[98:99],v6 offset:20080
v_rcp_f16 v123,v122
v_mfma_f32_16x16x32_bf16 v[156:159],v[76:79],v[52:55],v[156:159]
ds_read_u16 v136,v136
v_rcp_f16_sdwa v121,v120 dst_sel:WORD_1 dst_unused:UNUSED_PRESERVE src0_sel:WORD_1
v_mfma_f32_16x16x32_bf16 v[148:151],v[76:79],v[48:51],v[148:151]
ds_read_b64_tr_b16 v[100:101],v6 offset:18552
ds_read_b64_tr_b16 v[102:103],v6 offset:20088
v_rcp_f16_sdwa v123,v122 dst_sel:WORD_1 dst_unused:UNUSED_PRESERVE src0_sel:WORD_1
v_mfma_f32_16x16x32_bf16 v[144:147],v[80:83],v[56:59],v[144:147]
s_waitcnt lgkmcnt(13)
ds_read_u16_d16_hi v138,v138
v_lshl_add_u64 v[128:129],v[128:129],0,v[130:131]
v_fma_mix_f32 v140,v140,v121,0 op_sel_hi:[0,1,0]
v_fma_mix_f32 v142,v142,v121,0 op_sel:[0,1,0] op_sel_hi:[0,1,0]
v_mfma_f32_16x16x32_bf16 v[152:155],v[80:83],v[60:63],v[152:155]
ds_read_b64_tr_b16 v[104:105],v6 offset:18608
ds_read_b64_tr_b16 v[106:107],v6 offset:20144
v_mfma_f32_16x16x32_bf16 v[156:159],v[84:87],v[60:63],v[156:159]
s_waitcnt lgkmcnt(7)
ds_read_u16 v137,v137
v_lshl_add_u64 v[130:131],v[132:133],0,v[134:135]
v_fma_mix_f32 v141,v141,v123,0 op_sel_hi:[0,1,0]
v_fma_mix_f32 v143,v143,v123,0 op_sel:[0,1,0] op_sel_hi:[0,1,0]
v_mfma_f32_16x16x32_bf16 v[148:151],v[84:87],v[56:59],v[148:151]
ds_read_u16_d16_hi v139,v139
v_lshlrev_b32_sdwa v144,1,v144 dst_sel:DWORD dst_unused:UNUSED_PAD src0_sel:DWORD src1_sel:WORD_1
v_lshlrev_b32_sdwa v146,1,v146 dst_sel:DWORD dst_unused:UNUSED_PAD src0_sel:DWORD src1_sel:WORD_1
v_mfma_f32_16x16x32_bf16 v[160:163],v[88:91],v[128:131],v[160:163]
ds_read_b64_tr_b16 v[108:109],v6 offset:18616
ds_read_b64_tr_b16 v[110:111],v6 offset:20152
v_lshlrev_b32_sdwa v145,1,v145 dst_sel:DWORD dst_unused:UNUSED_PAD src0_sel:DWORD src1_sel:WORD_1
v_lshlrev_b32_sdwa v147,1,v147 dst_sel:DWORD dst_unused:UNUSED_PAD src0_sel:DWORD src1_sel:WORD_1
v_mfma_f32_16x16x32_bf16 v[168:171],v[92:95],v[128:131],v[168:171]
s_waitcnt lgkmcnt(10)
ds_read_b64_tr_b16 v[64:65],v4 offset:12832
ds_read_b64_tr_b16 v[66:67],v4 offset:14880
v_fma_mixlo_f16 v120,v156,s99,0
v_mfma_f32_16x16x32_bf16 v[176:179],v[96:99],v[128:131],v[176:179]
s_waitcnt lgkmcnt(9)
ds_read_b128 v[112:115],v7 offset:640
ds_read_u16 v144,v144
v_fma_mixlo_f16 v122,v157,s99,0
v_lshlrev_b32_sdwa v148,1,v148 dst_sel:DWORD dst_unused:UNUSED_PAD src0_sel:DWORD src1_sel:WORD_1
v_lshlrev_b32_sdwa v150,1,v150 dst_sel:DWORD dst_unused:UNUSED_PAD src0_sel:DWORD src1_sel:WORD_1
v_mfma_f32_16x16x32_bf16 v[184:187],v[100:103],v[128:131],v[184:187]
s_waitcnt lgkmcnt(6)
s_setprio 1
ds_read_b64_tr_b16 v[68:69],v5 offset:12832
v_lshl_add_u64 v[136:137],v[136:137],0,v[138:139]
v_mfma_f32_16x16x32_bf16 v[192:195],v[104:107],v[128:131],v[192:195]
s_waitcnt lgkmcnt(5)
ds_read_u16_d16_hi v146,v146
v_cvt_pk_bf16_f32 v138,v140,v142
v_cvt_pk_bf16_f32 v139,v141,v143
v_mfma_f32_16x16x32_bf16 v[200:203],v[108:111],v[128:131],v[200:203]
ds_read_b64_tr_b16 v[70:71],v5 offset:14880
ds_read_b128 v[116:119],v7 offset:672
v_fma_mixhi_f16 v120,v158,s99,0
v_fma_mixhi_f16 v122,v159,s99,0
v_mfma_f32_16x16x32_bf16 v[164:167],v[88:91],v[136:139],v[164:167]
v_lshlrev_b32_sdwa v149,1,v149 dst_sel:DWORD dst_unused:UNUSED_PAD src0_sel:DWORD src1_sel:WORD_1
v_lshlrev_b32_sdwa v151,1,v151 dst_sel:DWORD dst_unused:UNUSED_PAD src0_sel:DWORD src1_sel:WORD_1
v_mfma_f32_16x16x32_bf16 v[172:175],v[92:95],v[136:139],v[172:175]
ds_read_b64_tr_b16 v[72:73],v4 offset:16928
ds_read_b64_tr_b16 v[74:75],v4 offset:18976
v_pk_fma_f16 v123,v122,v122,s54
v_pk_fma_f16 v121,v120,v120,s54
v_mfma_f32_16x16x32_bf16 v[180:183],v[96:99],v[136:139],v[180:183]
ds_read_u16 v145,v145
v_pk_mul_f16 v121,v120,v121
v_pk_mul_f16 v123,v122,v123
v_mfma_f32_16x16x32_bf16 v[188:191],v[100:103],v[136:139],v[188:191]
ds_read_b64_tr_b16 v[76:77],v5 offset:16928
v_lshlrev_b32_sdwa v152,1,v152 dst_sel:DWORD dst_unused:UNUSED_PAD src0_sel:DWORD src1_sel:WORD_1
v_lshlrev_b32_sdwa v154,1,v154 dst_sel:DWORD dst_unused:UNUSED_PAD src0_sel:DWORD src1_sel:WORD_1
v_mfma_f32_16x16x32_bf16 v[196:199],v[104:107],v[136:139],v[196:199]
ds_read_u16_d16_hi v147,v147
v_exp_f16 v120,v121
v_mfma_f32_16x16x32_bf16 v[204:207],v[108:111],v[136:139],v[204:207]
s_waitcnt lgkmcnt(10)
ds_read_b64_tr_b16 v[78:79],v5 offset:18976
v_exp_f16 v122,v123
v_mfma_f32_16x16x32_bf16 v[128:131],v[64:67],v[16:19],v[112:115]
ds_read_u16 v148,v148
ds_read_u16_d16_hi v150,v150
v_lshlrev_b32_sdwa v153,1,v153 dst_sel:DWORD dst_unused:UNUSED_PAD src0_sel:DWORD src1_sel:WORD_1
v_lshlrev_b32_sdwa v155,1,v155 dst_sel:DWORD dst_unused:UNUSED_PAD src0_sel:DWORD src1_sel:WORD_1
v_exp_f16_sdwa v122,v123 dst_sel:WORD_1 dst_unused:UNUSED_PRESERVE src0_sel:WORD_1
v_mfma_f32_16x16x32_bf16 v[136:139],v[64:67],v[20:23],v[112:115]
s_waitcnt lgkmcnt(8)
s_setprio 0
v_exp_f16_sdwa v120,v121 dst_sel:WORD_1 dst_unused:UNUSED_PRESERVE src0_sel:WORD_1
v_mfma_f32_16x16x32_bf16 v[140:143],v[68:71],v[20:23],v[116:119]
ds_read_b64_tr_b16 v[80:81],v4 offset:21024
ds_read_b64_tr_b16 v[82:83],v4 offset:23072
v_pk_fma_f16 v120,v120,s55,s55
v_mfma_f32_16x16x32_bf16 v[132:135],v[68:71],v[16:19],v[116:119]
s_waitcnt lgkmcnt(8)
ds_read_u16 v149,v149
v_pk_fma_f16 v122,v122,s55,s55
v_mfma_f32_16x16x32_bf16 v[128:131],v[72:75],v[24:27],v[128:131]
ds_read_b64_tr_b16 v[84:85],v5 offset:21024
ds_read_u16_d16_hi v151,v151
v_rcp_f16 v121,v120
v_mfma_f32_16x16x32_bf16 v[136:139],v[72:75],v[28:31],v[136:139]
s_waitcnt lgkmcnt(7)
ds_read_b64_tr_b16 v[86:87],v5 offset:23072
v_rcp_f16 v123,v122
v_mfma_f32_16x16x32_bf16 v[140:143],v[76:79],v[28:31],v[140:143]
ds_read_u16 v152,v152
v_rcp_f16_sdwa v121,v120 dst_sel:WORD_1 dst_unused:UNUSED_PRESERVE src0_sel:WORD_1
v_mfma_f32_16x16x32_bf16 v[132:135],v[76:79],v[24:27],v[132:135]
s_waitcnt lgkmcnt(5)
v_rcp_f16_sdwa v123,v122 dst_sel:WORD_1 dst_unused:UNUSED_PRESERVE src0_sel:WORD_1
v_mfma_f32_16x16x32_bf16 v[128:131],v[80:83],v[32:35],v[128:131]
ds_read_u16_d16_hi v154,v154
v_fma_mix_f32 v156,v156,v121,0 op_sel_hi:[0,1,0]
v_mfma_f32_16x16x32_bf16 v[136:139],v[80:83],v[36:39],v[136:139]
s_waitcnt lgkmcnt(2)
v_lshl_add_u64 v[144:145],v[144:145],0,v[146:147]
v_fma_mix_f32 v158,v158,v121,0 op_sel:[0,1,0] op_sel_hi:[0,1,0]
v_lshl_add_u64 v[146:147],v[148:149],0,v[150:151]
v_mfma_f32_16x16x32_bf16 v[140:143],v[84:87],v[36:39],v[140:143]
ds_read_u16 v153,v153
v_fma_mix_f32 v157,v157,v123,0 op_sel_hi:[0,1,0]
v_fma_mix_f32 v159,v159,v123,0 op_sel:[0,1,0] op_sel_hi:[0,1,0]
v_mfma_f32_16x16x32_bf16 v[132:135],v[84:87],v[32:35],v[132:135]
v_lshlrev_b32_sdwa v128,1,v128 dst_sel:DWORD dst_unused:UNUSED_PAD src0_sel:DWORD src1_sel:WORD_1
v_lshlrev_b32_sdwa v130,1,v130 dst_sel:DWORD dst_unused:UNUSED_PAD src0_sel:DWORD src1_sel:WORD_1
v_mfma_f32_16x16x32_bf16 v[208:211],v[88:91],v[144:147],v[208:211]
s_waitcnt vmcnt(0)
s_barrier
s_setprio 2
v_lshlrev_b32_sdwa v129,1,v129 dst_sel:DWORD dst_unused:UNUSED_PAD src0_sel:DWORD src1_sel:WORD_1
v_lshlrev_b32_sdwa v131,1,v131 dst_sel:DWORD dst_unused:UNUSED_PAD src0_sel:DWORD src1_sel:WORD_1
v_mfma_f32_16x16x32_bf16 v[216:219],v[92:95],v[144:147],v[216:219]
ds_read_u16_d16_hi v155,v155
v_mfma_f32_16x16x32_bf16 v[224:227],v[96:99],v[144:147],v[224:227]
s_cbranch_vccz RB41_%=
s_add_u32 m0,s58,12320
s_mul_i32 s69,s59,7
s_add_u32 s69,s69,s97
buffer_load_dwordx4 v2,s[44:47],s69 offen lds
s_add_u32 m0,m0,1024
s_add_u32 s69,s69,s53
buffer_load_dwordx4 v3,s[44:47],s69 offen lds
s_add_u32 m0,m0,1024
s_add_u32 s69,s69,s53
buffer_load_dwordx4 v2,s[44:47],s69 offen lds
s_add_u32 m0,m0,s50
s_add_u32 s69,s69,s53
buffer_load_dwordx4 v3,s[44:47],s69 offen lds
RB41_%=:
v_lshlrev_b32_sdwa v132,1,v132 dst_sel:DWORD dst_unused:UNUSED_PAD src0_sel:DWORD src1_sel:WORD_1
v_mfma_f32_16x16x32_bf16 v[232:235],v[100:103],v[144:147],v[232:235]
s_waitcnt lgkmcnt(0)
ds_read_u16 v128,v128
v_lshl_add_u64 v[152:153],v[152:153],0,v[154:155]
v_lshlrev_b32_sdwa v134,1,v134 dst_sel:DWORD dst_unused:UNUSED_PAD src0_sel:DWORD src1_sel:WORD_1
v_cvt_pk_bf16_f32 v154,v156,v158
v_cvt_pk_bf16_f32 v155,v157,v159
v_mfma_f32_16x16x32_bf16 v[240:243],v[104:107],v[144:147],v[240:243]
ds_read_u16_d16_hi v130,v130
v_mfma_f32_16x16x32_bf16 v[248:251],v[108:111],v[144:147],v[248:251]
ds_read_u16 v129,v129
v_lshlrev_b32_sdwa v133,1,v133 dst_sel:DWORD dst_unused:UNUSED_PAD src0_sel:DWORD src1_sel:WORD_1
v_mfma_f32_16x16x32_bf16 v[212:215],v[88:91],v[152:155],v[212:215]
s_cbranch_vccz RB42_%=
s_add_u32 m0,m0,1024
s_add_u32 s69,s69,s53
buffer_load_dwordx4 v2,s[44:47],s69 offen lds
s_add_u32 m0,m0,1024
s_add_u32 s69,s69,s53
buffer_load_dwordx4 v3,s[44:47],s69 offen lds
s_add_u32 m0,m0,s50
s_add_u32 s69,s69,s53
buffer_load_dwordx4 v2,s[44:47],s69 offen lds
s_add_u32 m0,m0,1024
s_add_u32 s69,s69,s53
buffer_load_dwordx4 v3,s[44:47],s69 offen lds
RB42_%=:
v_lshlrev_b32_sdwa v135,1,v135 dst_sel:DWORD dst_unused:UNUSED_PAD src0_sel:DWORD src1_sel:WORD_1
v_mfma_f32_16x16x32_bf16 v[220:223],v[92:95],v[152:155],v[220:223]
ds_read_u16_d16_hi v131,v131
v_lshlrev_b32_sdwa v136,1,v136 dst_sel:DWORD dst_unused:UNUSED_PAD src0_sel:DWORD src1_sel:WORD_1
v_mfma_f32_16x16x32_bf16 v[228:231],v[96:99],v[152:155],v[228:231]
ds_read_b64_tr_b16 v[88:89],v6 offset:0
ds_read_b64_tr_b16 v[90:91],v6 offset:1536
v_lshlrev_b32_sdwa v138,1,v138 dst_sel:DWORD dst_unused:UNUSED_PAD src0_sel:DWORD src1_sel:WORD_1
v_mfma_f32_16x16x32_bf16 v[236:239],v[100:103],v[152:155],v[236:239]
ds_read_u16 v132,v132
v_lshlrev_b32_sdwa v137,1,v137 dst_sel:DWORD dst_unused:UNUSED_PAD src0_sel:DWORD src1_sel:WORD_1
v_mfma_f32_16x16x32_bf16 v[244:247],v[104:107],v[152:155],v[244:247]
ds_read_u16_d16_hi v134,v134
v_lshlrev_b32_sdwa v139,1,v139 dst_sel:DWORD dst_unused:UNUSED_PAD src0_sel:DWORD src1_sel:WORD_1
v_mfma_f32_16x16x32_bf16 v[252:255],v[108:111],v[152:155],v[252:255]
s_cbranch_vccz RB43_%=
s_add_u32 m0,m0,1024
s_add_u32 s69,s69,s53
buffer_load_dwordx4 v2,s[44:47],s69 offen lds
s_add_u32 m0,m0,s50
s_add_u32 s69,s69,s53
buffer_load_dwordx4 v3,s[44:47],s69 offen lds
s_add_u32 m0,m0,1024
s_add_u32 s69,s69,s53
buffer_load_dwordx4 v2,s[44:47],s69 offen lds
s_add_u32 m0,m0,1024
s_add_u32 s69,s69,s53
buffer_load_dwordx4 v3,s[44:47],s69 offen lds
RB43_%=:
ds_read_b64_tr_b16 v[92:93],v6 offset:8
ds_read_b64_tr_b16 v[94:95],v6 offset:1544
v_lshlrev_b32_sdwa v140,1,v140 dst_sel:DWORD dst_unused:UNUSED_PAD src0_sel:DWORD src1_sel:WORD_1
v_mfma_f32_16x16x32_bf16 v[144:147],v[64:67],v[40:43],v[112:115]
ds_read_u16 v133,v133
v_lshlrev_b32_sdwa v142,1,v142 dst_sel:DWORD dst_unused:UNUSED_PAD src0_sel:DWORD src1_sel:WORD_1
v_mfma_f32_16x16x32_bf16 v[152:155],v[64:67],v[44:47],v[112:115]
ds_read_b64_tr_b16 v[96:97],v6 offset:64
ds_read_b64_tr_b16 v[98:99],v6 offset:1600
v_lshlrev_b32_sdwa v141,1,v141 dst_sel:DWORD dst_unused:UNUSED_PAD src0_sel:DWORD src1_sel:WORD_1
v_lshlrev_b32_sdwa v143,1,v143 dst_sel:DWORD dst_unused:UNUSED_PAD src0_sel:DWORD src1_sel:WORD_1
v_mfma_f32_16x16x32_bf16 v[156:159],v[68:71],v[44:47],v[116:119]
ds_read_u16_d16_hi v135,v135
v_mfma_f32_16x16x32_bf16 v[148:151],v[68:71],v[40:43],v[116:119]
ds_read_u16 v136,v136
v_mfma_f32_16x16x32_bf16 v[144:147],v[72:75],v[48:51],v[144:147]
ds_read_u16_d16_hi v138,v138
v_mfma_f32_16x16x32_bf16 v[152:155],v[72:75],v[52:55],v[152:155]
ds_read_b64_tr_b16 v[100:101],v6 offset:72
ds_read_b64_tr_b16 v[102:103],v6 offset:1608
v_mfma_f32_16x16x32_bf16 v[156:159],v[76:79],v[52:55],v[156:159]
ds_read_u16 v137,v137
v_mfma_f32_16x16x32_bf16 v[148:151],v[76:79],v[48:51],v[148:151]
s_waitcnt lgkmcnt(5)
ds_read_u16_d16_hi v139,v139
v_lshl_add_u64 v[128:129],v[128:129],0,v[130:131]
v_lshl_add_u64 v[130:131],v[132:133],0,v[134:135]
v_mfma_f32_16x16x32_bf16 v[144:147],v[80:83],v[56:59],v[144:147]
ds_read_b64_tr_b16 v[104:105],v6 offset:128
ds_read_b64_tr_b16 v[106:107],v6 offset:1664
v_mfma_f32_16x16x32_bf16 v[152:155],v[80:83],v[60:63],v[152:155]
ds_read_u16 v140,v140
v_mfma_f32_16x16x32_bf16 v[156:159],v[84:87],v[60:63],v[156:159]
ds_read_u16_d16_hi v142,v142
v_mfma_f32_16x16x32_bf16 v[148:151],v[84:87],v[56:59],v[148:151]
ds_read_b64_tr_b16 v[108:109],v6 offset:136
ds_read_b64_tr_b16 v[110:111],v6 offset:1672
v_lshlrev_b32_sdwa v144,1,v144 dst_sel:DWORD dst_unused:UNUSED_PAD src0_sel:DWORD src1_sel:WORD_1
v_lshlrev_b32_sdwa v146,1,v146 dst_sel:DWORD dst_unused:UNUSED_PAD src0_sel:DWORD src1_sel:WORD_1
v_mfma_f32_16x16x32_bf16 v[160:163],v[88:91],v[128:131],v[160:163]
ds_read_b64_tr_b16 v[64:65],v4 offset:0
ds_read_u16 v141,v141
v_lshlrev_b32_sdwa v145,1,v145 dst_sel:DWORD dst_unused:UNUSED_PAD src0_sel:DWORD src1_sel:WORD_1
v_mfma_f32_16x16x32_bf16 v[168:171],v[92:95],v[128:131],v[168:171]
ds_read_u16_d16_hi v143,v143
v_lshlrev_b32_sdwa v147,1,v147 dst_sel:DWORD dst_unused:UNUSED_PAD src0_sel:DWORD src1_sel:WORD_1
v_mfma_f32_16x16x32_bf16 v[176:179],v[96:99],v[128:131],v[176:179]
s_waitcnt lgkmcnt(11)
ds_read_b64_tr_b16 v[66:67],v4 offset:2048
v_lshlrev_b32_sdwa v148,1,v148 dst_sel:DWORD dst_unused:UNUSED_PAD src0_sel:DWORD src1_sel:WORD_1
v_mfma_f32_16x16x32_bf16 v[184:187],v[100:103],v[128:131],v[184:187]
s_waitcnt lgkmcnt(8)
ds_read_u16 v144,v144
v_lshl_add_u64 v[136:137],v[136:137],0,v[138:139]
v_lshlrev_b32_sdwa v150,1,v150 dst_sel:DWORD dst_unused:UNUSED_PAD src0_sel:DWORD src1_sel:WORD_1
v_mfma_f32_16x16x32_bf16 v[192:195],v[104:107],v[128:131],v[192:195]
s_waitcnt lgkmcnt(2)
ds_read_u16_d16_hi v146,v146
v_lshl_add_u64 v[138:139],v[140:141],0,v[142:143]
v_mfma_f32_16x16x32_bf16 v[200:203],v[108:111],v[128:131],v[200:203]
ds_read_b128 v[112:115],v7 offset:768
v_lshlrev_b32_sdwa v149,1,v149 dst_sel:DWORD dst_unused:UNUSED_PAD src0_sel:DWORD src1_sel:WORD_1
v_mfma_f32_16x16x32_bf16 v[164:167],v[88:91],v[136:139],v[164:167]
ds_read_u16 v145,v145
v_lshlrev_b32_sdwa v151,1,v151 dst_sel:DWORD dst_unused:UNUSED_PAD src0_sel:DWORD src1_sel:WORD_1
v_mfma_f32_16x16x32_bf16 v[172:175],v[92:95],v[136:139],v[172:175]
ds_read_u16_d16_hi v147,v147
ds_read_b64_tr_b16 v[68:69],v5 offset:0
v_lshlrev_b32_sdwa v152,1,v152 dst_sel:DWORD dst_unused:UNUSED_PAD src0_sel:DWORD src1_sel:WORD_1
v_mfma_f32_16x16x32_bf16 v[180:183],v[96:99],v[136:139],v[180:183]
ds_read_b64_tr_b16 v[70:71],v5 offset:2048
ds_read_b128 v[116:119],v7 offset:800
v_lshlrev_b32_sdwa v154,1,v154 dst_sel:DWORD dst_unused:UNUSED_PAD src0_sel:DWORD src1_sel:WORD_1
v_mfma_f32_16x16x32_bf16 v[188:191],v[100:103],v[136:139],v[188:191]
ds_read_u16 v148,v148
ds_read_b64_tr_b16 v[72:73],v4 offset:4096
v_lshlrev_b32_sdwa v153,1,v153 dst_sel:DWORD dst_unused:UNUSED_PAD src0_sel:DWORD src1_sel:WORD_1
v_mfma_f32_16x16x32_bf16 v[196:199],v[104:107],v[136:139],v[196:199]
ds_read_u16_d16_hi v150,v150
v_lshlrev_b32_sdwa v155,1,v155 dst_sel:DWORD dst_unused:UNUSED_PAD src0_sel:DWORD src1_sel:WORD_1
v_mfma_f32_16x16x32_bf16 v[204:207],v[108:111],v[136:139],v[204:207]
s_waitcnt lgkmcnt(8)
ds_read_b64_tr_b16 v[74:75],v4 offset:6144
ds_read_b64_tr_b16 v[76:77],v5 offset:4096
v_mfma_f32_16x16x32_bf16 v[128:131],v[64:67],v[16:19],v[112:115]
ds_read_u16 v149,v149
v_lshlrev_b32_sdwa v156,1,v156 dst_sel:DWORD dst_unused:UNUSED_PAD src0_sel:DWORD src1_sel:WORD_1
v_lshlrev_b32_sdwa v158,1,v158 dst_sel:DWORD dst_unused:UNUSED_PAD src0_sel:DWORD src1_sel:WORD_1
v_mfma_f32_16x16x32_bf16 v[136:139],v[64:67],v[20:23],v[112:115]
s_waitcnt lgkmcnt(6)
ds_read_b64_tr_b16 v[78:79],v5 offset:6144
ds_read_b64_tr_b16 v[80:81],v4 offset:8192
v_lshlrev_b32_sdwa v157,1,v157 dst_sel:DWORD dst_unused:UNUSED_PAD src0_sel:DWORD src1_sel:WORD_1
v_mfma_f32_16x16x32_bf16 v[140:143],v[68:71],v[20:23],v[116:119]
ds_read_u16_d16_hi v151,v151
v_lshlrev_b32_sdwa v159,1,v159 dst_sel:DWORD dst_unused:UNUSED_PAD src0_sel:DWORD src1_sel:WORD_1
v_mfma_f32_16x16x32_bf16 v[132:135],v[68:71],v[16:19],v[116:119]
s_waitcnt lgkmcnt(5)
ds_read_b64_tr_b16 v[82:83],v4 offset:10240
ds_read_u16 v152,v152
v_mfma_f32_16x16x32_bf16 v[128:131],v[72:75],v[24:27],v[128:131]
ds_read_b64_tr_b16 v[84:85],v5 offset:8192
ds_read_b64_tr_b16 v[86:87],v5 offset:10240
v_mfma_f32_16x16x32_bf16 v[136:139],v[72:75],v[28:31],v[136:139]
s_waitcnt lgkmcnt(6)
ds_read_u16_d16_hi v154,v154
v_mfma_f32_16x16x32_bf16 v[140:143],v[76:79],v[28:31],v[140:143]
ds_read_u16 v153,v153
v_mfma_f32_16x16x32_bf16 v[132:135],v[76:79],v[24:27],v[132:135]
s_waitcnt lgkmcnt(5)
ds_read_u16_d16_hi v155,v155
v_lshl_add_u64 v[144:145],v[144:145],0,v[146:147]
v_mfma_f32_16x16x32_bf16 v[128:131],v[80:83],v[32:35],v[128:131]
v_lshl_add_u64 v[146:147],v[148:149],0,v[150:151]
v_mfma_f32_16x16x32_bf16 v[136:139],v[80:83],v[36:39],v[136:139]
s_waitcnt lgkmcnt(3)
ds_read_u16 v156,v156
v_mfma_f32_16x16x32_bf16 v[140:143],v[84:87],v[36:39],v[140:143]
ds_read_u16_d16_hi v158,v158
v_mfma_f32_16x16x32_bf16 v[132:135],v[84:87],v[32:35],v[132:135]
ds_read_u16 v157,v157
v_lshlrev_b32_sdwa v128,1,v128 dst_sel:DWORD dst_unused:UNUSED_PAD src0_sel:DWORD src1_sel:WORD_1
v_lshlrev_b32_sdwa v130,1,v130 dst_sel:DWORD dst_unused:UNUSED_PAD src0_sel:DWORD src1_sel:WORD_1
v_mfma_f32_16x16x32_bf16 v[208:211],v[88:91],v[144:147],v[208:211]
v_lshlrev_b32_sdwa v129,1,v129 dst_sel:DWORD dst_unused:UNUSED_PAD src0_sel:DWORD src1_sel:WORD_1
v_lshlrev_b32_sdwa v131,1,v131 dst_sel:DWORD dst_unused:UNUSED_PAD src0_sel:DWORD src1_sel:WORD_1
v_mfma_f32_16x16x32_bf16 v[216:219],v[92:95],v[144:147],v[216:219]
ds_read_u16_d16_hi v159,v159
v_mfma_f32_16x16x32_bf16 v[224:227],v[96:99],v[144:147],v[224:227]
v_lshlrev_b32_sdwa v132,1,v132 dst_sel:DWORD dst_unused:UNUSED_PAD src0_sel:DWORD src1_sel:WORD_1
v_mfma_f32_16x16x32_bf16 v[232:235],v[100:103],v[144:147],v[232:235]
s_waitcnt lgkmcnt(4)
ds_read_u16 v128,v128
v_lshl_add_u64 v[152:153],v[152:153],0,v[154:155]
v_lshlrev_b32_sdwa v134,1,v134 dst_sel:DWORD dst_unused:UNUSED_PAD src0_sel:DWORD src1_sel:WORD_1
v_mfma_f32_16x16x32_bf16 v[240:243],v[104:107],v[144:147],v[240:243]
s_waitcnt lgkmcnt(1)
ds_read_u16_d16_hi v130,v130
v_lshl_add_u64 v[154:155],v[156:157],0,v[158:159]
v_mfma_f32_16x16x32_bf16 v[248:251],v[108:111],v[144:147],v[248:251]
ds_read_u16 v129,v129
v_lshlrev_b32_sdwa v133,1,v133 dst_sel:DWORD dst_unused:UNUSED_PAD src0_sel:DWORD src1_sel:WORD_1
v_mfma_f32_16x16x32_bf16 v[212:215],v[88:91],v[152:155],v[212:215]
v_lshlrev_b32_sdwa v135,1,v135 dst_sel:DWORD dst_unused:UNUSED_PAD src0_sel:DWORD src1_sel:WORD_1
v_mfma_f32_16x16x32_bf16 v[220:223],v[92:95],v[152:155],v[220:223]
ds_read_u16_d16_hi v131,v131
v_lshlrev_b32_sdwa v136,1,v136 dst_sel:DWORD dst_unused:UNUSED_PAD src0_sel:DWORD src1_sel:WORD_1
v_mfma_f32_16x16x32_bf16 v[228:231],v[96:99],v[152:155],v[228:231]
ds_read_b64_tr_b16 v[88:89],v6 offset:6160
ds_read_b64_tr_b16 v[90:91],v6 offset:7696
v_lshlrev_b32_sdwa v138,1,v138 dst_sel:DWORD dst_unused:UNUSED_PAD src0_sel:DWORD src1_sel:WORD_1
v_mfma_f32_16x16x32_bf16 v[236:239],v[100:103],v[152:155],v[236:239]
ds_read_u16 v132,v132
v_lshlrev_b32_sdwa v137,1,v137 dst_sel:DWORD dst_unused:UNUSED_PAD src0_sel:DWORD src1_sel:WORD_1
v_mfma_f32_16x16x32_bf16 v[244:247],v[104:107],v[152:155],v[244:247]
ds_read_u16_d16_hi v134,v134
v_lshlrev_b32_sdwa v139,1,v139 dst_sel:DWORD dst_unused:UNUSED_PAD src0_sel:DWORD src1_sel:WORD_1
v_mfma_f32_16x16x32_bf16 v[252:255],v[108:111],v[152:155],v[252:255]
ds_read_b64_tr_b16 v[92:93],v6 offset:6168
ds_read_b64_tr_b16 v[94:95],v6 offset:7704
v_lshlrev_b32_sdwa v140,1,v140 dst_sel:DWORD dst_unused:UNUSED_PAD src0_sel:DWORD src1_sel:WORD_1
v_mfma_f32_16x16x32_bf16 v[144:147],v[64:67],v[40:43],v[112:115]
ds_read_u16 v133,v133
v_lshlrev_b32_sdwa v142,1,v142 dst_sel:DWORD dst_unused:UNUSED_PAD src0_sel:DWORD src1_sel:WORD_1
v_mfma_f32_16x16x32_bf16 v[152:155],v[64:67],v[44:47],v[112:115]
ds_read_b64_tr_b16 v[96:97],v6 offset:6224
ds_read_b64_tr_b16 v[98:99],v6 offset:7760
v_lshlrev_b32_sdwa v141,1,v141 dst_sel:DWORD dst_unused:UNUSED_PAD src0_sel:DWORD src1_sel:WORD_1
v_lshlrev_b32_sdwa v143,1,v143 dst_sel:DWORD dst_unused:UNUSED_PAD src0_sel:DWORD src1_sel:WORD_1
v_mfma_f32_16x16x32_bf16 v[156:159],v[68:71],v[44:47],v[116:119]
ds_read_u16_d16_hi v135,v135
v_mfma_f32_16x16x32_bf16 v[148:151],v[68:71],v[40:43],v[116:119]
ds_read_u16 v136,v136
v_mfma_f32_16x16x32_bf16 v[144:147],v[72:75],v[48:51],v[144:147]
ds_read_u16_d16_hi v138,v138
v_mfma_f32_16x16x32_bf16 v[152:155],v[72:75],v[52:55],v[152:155]
ds_read_b64_tr_b16 v[100:101],v6 offset:6232
ds_read_b64_tr_b16 v[102:103],v6 offset:7768
v_mfma_f32_16x16x32_bf16 v[156:159],v[76:79],v[52:55],v[156:159]
ds_read_u16 v137,v137
v_mfma_f32_16x16x32_bf16 v[148:151],v[76:79],v[48:51],v[148:151]
s_waitcnt lgkmcnt(5)
ds_read_u16_d16_hi v139,v139
v_lshl_add_u64 v[128:129],v[128:129],0,v[130:131]
v_lshl_add_u64 v[130:131],v[132:133],0,v[134:135]
v_mfma_f32_16x16x32_bf16 v[144:147],v[80:83],v[56:59],v[144:147]
ds_read_b64_tr_b16 v[104:105],v6 offset:6288
ds_read_b64_tr_b16 v[106:107],v6 offset:7824
v_mfma_f32_16x16x32_bf16 v[152:155],v[80:83],v[60:63],v[152:155]
ds_read_u16 v140,v140
v_mfma_f32_16x16x32_bf16 v[156:159],v[84:87],v[60:63],v[156:159]
ds_read_u16_d16_hi v142,v142
v_mfma_f32_16x16x32_bf16 v[148:151],v[84:87],v[56:59],v[148:151]
ds_read_b64_tr_b16 v[108:109],v6 offset:6296
ds_read_b64_tr_b16 v[110:111],v6 offset:7832
v_lshlrev_b32_sdwa v144,1,v144 dst_sel:DWORD dst_unused:UNUSED_PAD src0_sel:DWORD src1_sel:WORD_1
v_lshlrev_b32_sdwa v146,1,v146 dst_sel:DWORD dst_unused:UNUSED_PAD src0_sel:DWORD src1_sel:WORD_1
v_mfma_f32_16x16x32_bf16 v[160:163],v[88:91],v[128:131],v[160:163]
ds_read_b64_tr_b16 v[64:65],v4 offset:512
ds_read_u16 v141,v141
v_lshlrev_b32_sdwa v145,1,v145 dst_sel:DWORD dst_unused:UNUSED_PAD src0_sel:DWORD src1_sel:WORD_1
v_mfma_f32_16x16x32_bf16 v[168:171],v[92:95],v[128:131],v[168:171]
ds_read_u16_d16_hi v143,v143
v_lshlrev_b32_sdwa v147,1,v147 dst_sel:DWORD dst_unused:UNUSED_PAD src0_sel:DWORD src1_sel:WORD_1
v_mfma_f32_16x16x32_bf16 v[176:179],v[96:99],v[128:131],v[176:179]
s_waitcnt lgkmcnt(11)
ds_read_b64_tr_b16 v[66:67],v4 offset:2560
v_lshlrev_b32_sdwa v148,1,v148 dst_sel:DWORD dst_unused:UNUSED_PAD src0_sel:DWORD src1_sel:WORD_1
v_mfma_f32_16x16x32_bf16 v[184:187],v[100:103],v[128:131],v[184:187]
s_waitcnt lgkmcnt(8)
ds_read_u16 v144,v144
v_lshl_add_u64 v[136:137],v[136:137],0,v[138:139]
v_lshlrev_b32_sdwa v150,1,v150 dst_sel:DWORD dst_unused:UNUSED_PAD src0_sel:DWORD src1_sel:WORD_1
v_mfma_f32_16x16x32_bf16 v[192:195],v[104:107],v[128:131],v[192:195]
s_waitcnt lgkmcnt(2)
ds_read_u16_d16_hi v146,v146
v_lshl_add_u64 v[138:139],v[140:141],0,v[142:143]
v_mfma_f32_16x16x32_bf16 v[200:203],v[108:111],v[128:131],v[200:203]
ds_read_b128 v[112:115],v7 offset:896
v_lshlrev_b32_sdwa v149,1,v149 dst_sel:DWORD dst_unused:UNUSED_PAD src0_sel:DWORD src1_sel:WORD_1
v_mfma_f32_16x16x32_bf16 v[164:167],v[88:91],v[136:139],v[164:167]
ds_read_u16 v145,v145
v_lshlrev_b32_sdwa v151,1,v151 dst_sel:DWORD dst_unused:UNUSED_PAD src0_sel:DWORD src1_sel:WORD_1
v_mfma_f32_16x16x32_bf16 v[172:175],v[92:95],v[136:139],v[172:175]
ds_read_u16_d16_hi v147,v147
ds_read_b64_tr_b16 v[68:69],v5 offset:512
v_lshlrev_b32_sdwa v152,1,v152 dst_sel:DWORD dst_unused:UNUSED_PAD src0_sel:DWORD src1_sel:WORD_1
v_mfma_f32_16x16x32_bf16 v[180:183],v[96:99],v[136:139],v[180:183]
ds_read_b64_tr_b16 v[70:71],v5 offset:2560
ds_read_b128 v[116:119],v7 offset:928
v_lshlrev_b32_sdwa v154,1,v154 dst_sel:DWORD dst_unused:UNUSED_PAD src0_sel:DWORD src1_sel:WORD_1
v_mfma_f32_16x16x32_bf16 v[188:191],v[100:103],v[136:139],v[188:191]
ds_read_u16 v148,v148
ds_read_b64_tr_b16 v[72:73],v4 offset:4608
v_lshlrev_b32_sdwa v153,1,v153 dst_sel:DWORD dst_unused:UNUSED_PAD src0_sel:DWORD src1_sel:WORD_1
v_mfma_f32_16x16x32_bf16 v[196:199],v[104:107],v[136:139],v[196:199]
ds_read_u16_d16_hi v150,v150
v_lshlrev_b32_sdwa v155,1,v155 dst_sel:DWORD dst_unused:UNUSED_PAD src0_sel:DWORD src1_sel:WORD_1
v_mfma_f32_16x16x32_bf16 v[204:207],v[108:111],v[136:139],v[204:207]
s_waitcnt lgkmcnt(8)
ds_read_b64_tr_b16 v[74:75],v4 offset:6656
ds_read_b64_tr_b16 v[76:77],v5 offset:4608
v_mfma_f32_16x16x32_bf16 v[128:131],v[64:67],v[16:19],v[112:115]
ds_read_u16 v149,v149
v_lshlrev_b32_sdwa v156,1,v156 dst_sel:DWORD dst_unused:UNUSED_PAD src0_sel:DWORD src1_sel:WORD_1
v_lshlrev_b32_sdwa v158,1,v158 dst_sel:DWORD dst_unused:UNUSED_PAD src0_sel:DWORD src1_sel:WORD_1
v_mfma_f32_16x16x32_bf16 v[136:139],v[64:67],v[20:23],v[112:115]
s_waitcnt lgkmcnt(6)
ds_read_b64_tr_b16 v[78:79],v5 offset:6656
ds_read_b64_tr_b16 v[80:81],v4 offset:8704
v_lshlrev_b32_sdwa v157,1,v157 dst_sel:DWORD dst_unused:UNUSED_PAD src0_sel:DWORD src1_sel:WORD_1
v_mfma_f32_16x16x32_bf16 v[140:143],v[68:71],v[20:23],v[116:119]
ds_read_u16_d16_hi v151,v151
v_lshlrev_b32_sdwa v159,1,v159 dst_sel:DWORD dst_unused:UNUSED_PAD src0_sel:DWORD src1_sel:WORD_1
v_mfma_f32_16x16x32_bf16 v[132:135],v[68:71],v[16:19],v[116:119]
s_waitcnt lgkmcnt(5)
ds_read_b64_tr_b16 v[82:83],v4 offset:10752
ds_read_u16 v152,v152
v_mfma_f32_16x16x32_bf16 v[128:131],v[72:75],v[24:27],v[128:131]
ds_read_b64_tr_b16 v[84:85],v5 offset:8704
ds_read_b64_tr_b16 v[86:87],v5 offset:10752
v_mfma_f32_16x16x32_bf16 v[136:139],v[72:75],v[28:31],v[136:139]
s_waitcnt lgkmcnt(6)
ds_read_u16_d16_hi v154,v154
v_mfma_f32_16x16x32_bf16 v[140:143],v[76:79],v[28:31],v[140:143]
ds_read_u16 v153,v153
v_mfma_f32_16x16x32_bf16 v[132:135],v[76:79],v[24:27],v[132:135]
s_waitcnt lgkmcnt(5)
s_mov_b32 s86,0xbe801d5a
ds_read_u16_d16_hi v155,v155
v_lshl_add_u64 v[144:145],v[144:145],0,v[146:147]
v_mfma_f32_16x16x32_bf16 v[128:131],v[80:83],v[32:35],v[128:131]
v_lshl_add_u64 v[146:147],v[148:149],0,v[150:151]
v_mfma_f32_16x16x32_bf16 v[136:139],v[80:83],v[36:39],v[136:139]
s_waitcnt lgkmcnt(3)
ds_read_u16 v156,v156
v_mfma_f32_16x16x32_bf16 v[140:143],v[84:87],v[36:39],v[140:143]
ds_read_u16_d16_hi v158,v158
v_mfma_f32_16x16x32_bf16 v[132:135],v[84:87],v[32:35],v[132:135]
ds_read_u16 v157,v157
v_lshlrev_b32_sdwa v128,1,v128 dst_sel:DWORD dst_unused:UNUSED_PAD src0_sel:DWORD src1_sel:WORD_1
v_lshlrev_b32_sdwa v130,1,v130 dst_sel:DWORD dst_unused:UNUSED_PAD src0_sel:DWORD src1_sel:WORD_1
v_mfma_f32_16x16x32_bf16 v[208:211],v[88:91],v[144:147],v[208:211]
s_waitcnt vmcnt(0)
s_barrier
s_setprio 3
v_lshlrev_b32_sdwa v129,1,v129 dst_sel:DWORD dst_unused:UNUSED_PAD src0_sel:DWORD src1_sel:WORD_1
v_lshlrev_b32_sdwa v131,1,v131 dst_sel:DWORD dst_unused:UNUSED_PAD src0_sel:DWORD src1_sel:WORD_1
v_mfma_f32_16x16x32_bf16 v[216:219],v[92:95],v[144:147],v[216:219]
ds_read_u16_d16_hi v159,v159
ds_read_u16 v128,v128
v_fma_mixlo_f16 v120,v140,s99,0
v_fma_mixlo_f16 v122,v141,s99,0
v_mfma_f32_16x16x32_bf16 v[224:227],v[96:99],v[144:147],v[224:227]
ds_read_u16_d16_hi v130,v130
v_lshlrev_b32_sdwa v132,1,v132 dst_sel:DWORD dst_unused:UNUSED_PAD src0_sel:DWORD src1_sel:WORD_1
v_lshlrev_b32_sdwa v134,1,v134 dst_sel:DWORD dst_unused:UNUSED_PAD src0_sel:DWORD src1_sel:WORD_1
v_mfma_f32_16x16x32_bf16 v[232:235],v[100:103],v[144:147],v[232:235]
s_waitcnt lgkmcnt(6)
v_lshl_add_u64 v[152:153],v[152:153],0,v[154:155]
v_mfma_f32_16x16x32_bf16 v[240:243],v[104:107],v[144:147],v[240:243]
s_waitcnt lgkmcnt(2)
v_lshl_add_u64 v[154:155],v[156:157],0,v[158:159]
v_fma_mixhi_f16 v120,v142,s99,0
v_mfma_f32_16x16x32_bf16 v[248:251],v[108:111],v[144:147],v[248:251]
ds_read_u16 v129,v129
v_fma_mixhi_f16 v122,v143,s99,0
v_mfma_f32_16x16x32_bf16 v[212:215],v[88:91],v[152:155],v[212:215]
v_lshlrev_b32_sdwa v133,1,v133 dst_sel:DWORD dst_unused:UNUSED_PAD src0_sel:DWORD src1_sel:WORD_1
v_pk_fma_f16 v121,v120,v120,s54
v_lshlrev_b32_sdwa v135,1,v135 dst_sel:DWORD dst_unused:UNUSED_PAD src0_sel:DWORD src1_sel:WORD_1
v_mfma_f32_16x16x32_bf16 v[220:223],v[92:95],v[152:155],v[220:223]
ds_read_u16_d16_hi v131,v131
ds_read_u16 v132,v132
v_pk_fma_f16 v123,v122,v122,s54
v_mfma_f32_16x16x32_bf16 v[228:231],v[96:99],v[152:155],v[228:231]
ds_read_b64_tr_b16 v[88:89],v6 offset:12320
v_pk_mul_f16 v121,v120,v121
v_lshlrev_b32_sdwa v136,1,v136 dst_sel:DWORD dst_unused:UNUSED_PAD src0_sel:DWORD src1_sel:WORD_1
v_pk_mul_f16 v123,v122,v123
v_mfma_f32_16x16x32_bf16 v[236:239],v[100:103],v[152:155],v[236:239]
ds_read_b64_tr_b16 v[90:91],v6 offset:13856
v_lshlrev_b32_sdwa v138,1,v138 dst_sel:DWORD dst_unused:UNUSED_PAD src0_sel:DWORD src1_sel:WORD_1
v_mfma_f32_16x16x32_bf16 v[244:247],v[104:107],v[152:155],v[244:247]
ds_read_u16_d16_hi v134,v134
v_exp_f16 v120,v121
v_mfma_f32_16x16x32_bf16 v[252:255],v[108:111],v[152:155],v[252:255]
ds_read_b64_tr_b16 v[92:93],v6 offset:12328
v_lshlrev_b32_sdwa v137,1,v137 dst_sel:DWORD dst_unused:UNUSED_PAD src0_sel:DWORD src1_sel:WORD_1
v_mfma_f32_16x16x32_bf16 v[144:147],v[64:67],v[40:43],v[112:115]
ds_read_b64_tr_b16 v[94:95],v6 offset:13864
v_lshlrev_b32_sdwa v139,1,v139 dst_sel:DWORD dst_unused:UNUSED_PAD src0_sel:DWORD src1_sel:WORD_1
v_exp_f16 v122,v123
v_mfma_f32_16x16x32_bf16 v[152:155],v[64:67],v[44:47],v[112:115]
ds_read_u16 v133,v133
v_mfma_f32_16x16x32_bf16 v[156:159],v[68:71],v[44:47],v[116:119]
ds_read_u16_d16_hi v135,v135
v_exp_f16_sdwa v122,v123 dst_sel:WORD_1 dst_unused:UNUSED_PRESERVE src0_sel:WORD_1
v_exp_f16_sdwa v120,v121 dst_sel:WORD_1 dst_unused:UNUSED_PRESERVE src0_sel:WORD_1
v_mfma_f32_16x16x32_bf16 v[148:151],v[68:71],v[40:43],v[116:119]
ds_read_b64_tr_b16 v[96:97],v6 offset:12384
ds_read_b64_tr_b16 v[98:99],v6 offset:13920
v_pk_fma_f16 v120,v120,s55,s55
v_pk_fma_f16 v122,v122,s55,s55
v_mfma_f32_16x16x32_bf16 v[144:147],v[72:75],v[48:51],v[144:147]
v_rcp_f16 v121,v120
v_mfma_f32_16x16x32_bf16 v[152:155],v[72:75],v[52:55],v[152:155]
ds_read_b64_tr_b16 v[100:101],v6 offset:12392
v_rcp_f16 v123,v122
v_mfma_f32_16x16x32_bf16 v[156:159],v[76:79],v[52:55],v[156:159]
ds_read_u16 v136,v136
v_rcp_f16_sdwa v121,v120 dst_sel:WORD_1 dst_unused:UNUSED_PRESERVE src0_sel:WORD_1
v_mfma_f32_16x16x32_bf16 v[148:151],v[76:79],v[48:51],v[148:151]
ds_read_b64_tr_b16 v[102:103],v6 offset:13928
ds_read_b64_tr_b16 v[104:105],v6 offset:12448
v_rcp_f16_sdwa v123,v122 dst_sel:WORD_1 dst_unused:UNUSED_PRESERVE src0_sel:WORD_1
v_mfma_f32_16x16x32_bf16 v[144:147],v[80:83],v[56:59],v[144:147]
ds_read_u16_d16_hi v138,v138
v_fma_mix_f32 v142,v142,v121,0 op_sel:[0,1,0] op_sel_hi:[0,1,0]
v_mfma_f32_16x16x32_bf16 v[152:155],v[80:83],v[60:63],v[152:155]
s_waitcnt lgkmcnt(7)
ds_read_b64_tr_b16 v[106:107],v6 offset:13984
ds_read_b64_tr_b16 v[108:109],v6 offset:12456
v_fma_mix_f32 v140,v140,v121,0 op_sel_hi:[0,1,0]
v_lshl_add_u64 v[128:129],v[128:129],0,v[130:131]
v_lshl_add_u64 v[130:131],v[132:133],0,v[134:135]
v_mfma_f32_16x16x32_bf16 v[156:159],v[84:87],v[60:63],v[156:159]
ds_read_u16 v137,v137
v_fma_mix_f32 v141,v141,v123,0 op_sel_hi:[0,1,0]
v_fma_mix_f32 v143,v143,v123,0 op_sel:[0,1,0] op_sel_hi:[0,1,0]
v_mfma_f32_16x16x32_bf16 v[148:151],v[84:87],v[56:59],v[148:151]
ds_read_b64_tr_b16 v[110:111],v6 offset:13992
ds_read_b64_tr_b16 v[64:65],v4 offset:12320
v_lshlrev_b32_sdwa v144,1,v144 dst_sel:DWORD dst_unused:UNUSED_PAD src0_sel:DWORD src1_sel:WORD_1
v_lshlrev_b32_sdwa v146,1,v146 dst_sel:DWORD dst_unused:UNUSED_PAD src0_sel:DWORD src1_sel:WORD_1
v_mfma_f32_16x16x32_bf16 v[160:163],v[88:91],v[128:131],v[160:163]
ds_read_u16_d16_hi v139,v139
v_lshlrev_b32_sdwa v145,1,v145 dst_sel:DWORD dst_unused:UNUSED_PAD src0_sel:DWORD src1_sel:WORD_1
v_lshlrev_b32_sdwa v147,1,v147 dst_sel:DWORD dst_unused:UNUSED_PAD src0_sel:DWORD src1_sel:WORD_1
v_mfma_f32_16x16x32_bf16 v[168:171],v[92:95],v[128:131],v[168:171]
s_waitcnt lgkmcnt(11)
ds_read_b64_tr_b16 v[66:67],v4 offset:14368
ds_read_b128 v[112:115],v7 offset:1024
v_fma_mixlo_f16 v120,v156,s99,0
v_fma_mixlo_f16 v122,v157,s99,0
v_mfma_f32_16x16x32_bf16 v[176:179],v[96:99],v[128:131],v[176:179]
s_waitcnt lgkmcnt(10)
ds_read_u16 v144,v144
v_lshlrev_b32_sdwa v148,1,v148 dst_sel:DWORD dst_unused:UNUSED_PAD src0_sel:DWORD src1_sel:WORD_1
v_lshlrev_b32_sdwa v150,1,v150 dst_sel:DWORD dst_unused:UNUSED_PAD src0_sel:DWORD src1_sel:WORD_1
v_mfma_f32_16x16x32_bf16 v[184:187],v[100:103],v[128:131],v[184:187]
s_waitcnt lgkmcnt(3)
ds_read_b64_tr_b16 v[68:69],v5 offset:12320
ds_read_b64_tr_b16 v[70:71],v5 offset:14368
v_lshl_add_u64 v[136:137],v[136:137],0,v[138:139]
v_fma_mixhi_f16 v120,v158,s99,0
v_mfma_f32_16x16x32_bf16 v[192:195],v[104:107],v[128:131],v[192:195]
ds_read_u16_d16_hi v146,v146
v_cvt_pk_bf16_f32 v138,v140,v142
v_cvt_pk_bf16_f32 v139,v141,v143
v_mfma_f32_16x16x32_bf16 v[200:203],v[108:111],v[128:131],v[200:203]
ds_read_b128 v[116:119],v7 offset:1056
ds_read_b64_tr_b16 v[72:73],v4 offset:16416
v_fma_mixhi_f16 v122,v159,s99,0
v_mfma_f32_16x16x32_bf16 v[164:167],v[88:91],v[136:139],v[164:167]
v_lshlrev_b32_sdwa v149,1,v149 dst_sel:DWORD dst_unused:UNUSED_PAD src0_sel:DWORD src1_sel:WORD_1
v_lshlrev_b32_sdwa v151,1,v151 dst_sel:DWORD dst_unused:UNUSED_PAD src0_sel:DWORD src1_sel:WORD_1
v_mfma_f32_16x16x32_bf16 v[172:175],v[92:95],v[136:139],v[172:175]
ds_read_b64_tr_b16 v[74:75],v4 offset:18464
ds_read_b64_tr_b16 v[76:77],v5 offset:16416
v_pk_fma_f16 v121,v120,v120,s54
v_pk_fma_f16 v123,v122,v122,s54
v_mfma_f32_16x16x32_bf16 v[180:183],v[96:99],v[136:139],v[180:183]
ds_read_u16 v145,v145
v_pk_mul_f16 v121,v120,v121
v_pk_mul_f16 v123,v122,v123
v_mfma_f32_16x16x32_bf16 v[188:191],v[100:103],v[136:139],v[188:191]
ds_read_u16_d16_hi v147,v147
v_lshlrev_b32_sdwa v152,1,v152 dst_sel:DWORD dst_unused:UNUSED_PAD src0_sel:DWORD src1_sel:WORD_1
v_lshlrev_b32_sdwa v154,1,v154 dst_sel:DWORD dst_unused:UNUSED_PAD src0_sel:DWORD src1_sel:WORD_1
v_mfma_f32_16x16x32_bf16 v[196:199],v[104:107],v[136:139],v[196:199]
v_exp_f16 v120,v121
v_mfma_f32_16x16x32_bf16 v[204:207],v[108:111],v[136:139],v[204:207]
s_waitcnt lgkmcnt(10)
ds_read_b64_tr_b16 v[78:79],v5 offset:18464
ds_read_b64_tr_b16 v[80:81],v4 offset:20512
v_exp_f16 v122,v123
v_mfma_f32_16x16x32_bf16 v[128:131],v[64:67],v[16:19],v[112:115]
v_lshlrev_b32_sdwa v153,1,v153 dst_sel:DWORD dst_unused:UNUSED_PAD src0_sel:DWORD src1_sel:WORD_1
v_lshlrev_b32_sdwa v155,1,v155 dst_sel:DWORD dst_unused:UNUSED_PAD src0_sel:DWORD src1_sel:WORD_1
v_mfma_f32_16x16x32_bf16 v[136:139],v[64:67],v[20:23],v[112:115]
s_waitcnt lgkmcnt(7)
ds_read_u16 v148,v148
ds_read_u16_d16_hi v150,v150
v_exp_f16_sdwa v120,v121 dst_sel:WORD_1 dst_unused:UNUSED_PRESERVE src0_sel:WORD_1
v_mfma_f32_16x16x32_bf16 v[140:143],v[68:71],v[20:23],v[116:119]
v_exp_f16_sdwa v122,v123 dst_sel:WORD_1 dst_unused:UNUSED_PRESERVE src0_sel:WORD_1
v_mfma_f32_16x16x32_bf16 v[132:135],v[68:71],v[16:19],v[116:119]
s_waitcnt lgkmcnt(7)
ds_read_b64_tr_b16 v[82:83],v4 offset:22560
v_pk_fma_f16 v120,v120,s55,s55
v_mfma_f32_16x16x32_bf16 v[128:131],v[72:75],v[24:27],v[128:131]
ds_read_b64_tr_b16 v[84:85],v5 offset:20512
v_pk_fma_f16 v122,v122,s55,s55
v_rcp_f16 v121,v120
v_mfma_f32_16x16x32_bf16 v[136:139],v[72:75],v[28:31],v[136:139]
s_waitcnt lgkmcnt(5)
ds_read_b64_tr_b16 v[86:87],v5 offset:22560
ds_read_u16 v149,v149
v_rcp_f16 v123,v122
v_mfma_f32_16x16x32_bf16 v[140:143],v[76:79],v[28:31],v[140:143]
ds_read_u16_d16_hi v151,v151
ds_read_u16 v152,v152
v_rcp_f16_sdwa v121,v120 dst_sel:WORD_1 dst_unused:UNUSED_PRESERVE src0_sel:WORD_1
v_mfma_f32_16x16x32_bf16 v[132:135],v[76:79],v[24:27],v[132:135]
s_waitcnt lgkmcnt(5)
v_rcp_f16_sdwa v123,v122 dst_sel:WORD_1 dst_unused:UNUSED_PRESERVE src0_sel:WORD_1
v_mfma_f32_16x16x32_bf16 v[128:131],v[80:83],v[32:35],v[128:131]
ds_read_u16_d16_hi v154,v154
v_fma_mix_f32 v156,v156,v121,0 op_sel_hi:[0,1,0]
v_fma_mix_f32 v158,v158,v121,0 op_sel:[0,1,0] op_sel_hi:[0,1,0]
v_mfma_f32_16x16x32_bf16 v[136:139],v[80:83],v[36:39],v[136:139]
s_waitcnt lgkmcnt(2)
v_lshl_add_u64 v[144:145],v[144:145],0,v[146:147]
v_lshl_add_u64 v[146:147],v[148:149],0,v[150:151]
v_mfma_f32_16x16x32_bf16 v[140:143],v[84:87],v[36:39],v[140:143]
ds_read_u16 v153,v153
v_fma_mix_f32 v157,v157,v123,0 op_sel_hi:[0,1,0]
v_fma_mix_f32 v159,v159,v123,0 op_sel:[0,1,0] op_sel_hi:[0,1,0]
v_mfma_f32_16x16x32_bf16 v[132:135],v[84:87],v[32:35],v[132:135]
v_lshlrev_b32_sdwa v128,1,v128 dst_sel:DWORD dst_unused:UNUSED_PAD src0_sel:DWORD src1_sel:WORD_1
v_lshlrev_b32_sdwa v130,1,v130 dst_sel:DWORD dst_unused:UNUSED_PAD src0_sel:DWORD src1_sel:WORD_1
v_mfma_f32_16x16x32_bf16 v[208:211],v[88:91],v[144:147],v[208:211]
v_lshlrev_b32_sdwa v129,1,v129 dst_sel:DWORD dst_unused:UNUSED_PAD src0_sel:DWORD src1_sel:WORD_1
v_lshlrev_b32_sdwa v131,1,v131 dst_sel:DWORD dst_unused:UNUSED_PAD src0_sel:DWORD src1_sel:WORD_1
v_mfma_f32_16x16x32_bf16 v[216:219],v[92:95],v[144:147],v[216:219]
ds_read_u16_d16_hi v155,v155
ds_read_u16 v128,v128
v_fma_mixlo_f16 v120,v140,s99,0
v_fma_mixlo_f16 v122,v141,s99,0
v_mfma_f32_16x16x32_bf16 v[224:227],v[96:99],v[144:147],v[224:227]
s_setprio 2
ds_read_u16_d16_hi v130,v130
v_lshlrev_b32_sdwa v132,1,v132 dst_sel:DWORD dst_unused:UNUSED_PAD src0_sel:DWORD src1_sel:WORD_1
v_lshlrev_b32_sdwa v134,1,v134 dst_sel:DWORD dst_unused:UNUSED_PAD src0_sel:DWORD src1_sel:WORD_1
v_mfma_f32_16x16x32_bf16 v[232:235],v[100:103],v[144:147],v[232:235]
s_waitcnt lgkmcnt(2)
v_lshl_add_u64 v[152:153],v[152:153],0,v[154:155]
v_cvt_pk_bf16_f32 v154,v156,v158
v_mfma_f32_16x16x32_bf16 v[240:243],v[104:107],v[144:147],v[240:243]
v_fma_mixhi_f16 v120,v142,s99,0
v_cvt_pk_bf16_f32 v155,v157,v159
v_mfma_f32_16x16x32_bf16 v[248:251],v[108:111],v[144:147],v[248:251]
ds_read_u16 v129,v129
v_fma_mixhi_f16 v122,v143,s99,0
v_mfma_f32_16x16x32_bf16 v[212:215],v[88:91],v[152:155],v[212:215]
v_lshlrev_b32_sdwa v133,1,v133 dst_sel:DWORD dst_unused:UNUSED_PAD src0_sel:DWORD src1_sel:WORD_1
v_pk_fma_f16 v121,v120,v120,s54
v_lshlrev_b32_sdwa v135,1,v135 dst_sel:DWORD dst_unused:UNUSED_PAD src0_sel:DWORD src1_sel:WORD_1
v_mfma_f32_16x16x32_bf16 v[220:223],v[92:95],v[152:155],v[220:223]
ds_read_u16_d16_hi v131,v131
ds_read_u16 v132,v132
v_pk_fma_f16 v123,v122,v122,s54
v_mfma_f32_16x16x32_bf16 v[228:231],v[96:99],v[152:155],v[228:231]
ds_read_b64_tr_b16 v[88:89],v6 offset:18480
v_pk_mul_f16 v121,v120,v121
v_lshlrev_b32_sdwa v136,1,v136 dst_sel:DWORD dst_unused:UNUSED_PAD src0_sel:DWORD src1_sel:WORD_1
v_pk_mul_f16 v123,v122,v123
v_mfma_f32_16x16x32_bf16 v[236:239],v[100:103],v[152:155],v[236:239]
ds_read_b64_tr_b16 v[90:91],v6 offset:20016
v_lshlrev_b32_sdwa v138,1,v138 dst_sel:DWORD dst_unused:UNUSED_PAD src0_sel:DWORD src1_sel:WORD_1
v_mfma_f32_16x16x32_bf16 v[244:247],v[104:107],v[152:155],v[244:247]
ds_read_u16_d16_hi v134,v134
v_exp_f16 v120,v121
v_mfma_f32_16x16x32_bf16 v[252:255],v[108:111],v[152:155],v[252:255]
ds_read_b64_tr_b16 v[92:93],v6 offset:18488
v_lshlrev_b32_sdwa v137,1,v137 dst_sel:DWORD dst_unused:UNUSED_PAD src0_sel:DWORD src1_sel:WORD_1
v_mfma_f32_16x16x32_bf16 v[144:147],v[64:67],v[40:43],v[112:115]
ds_read_b64_tr_b16 v[94:95],v6 offset:20024
v_lshlrev_b32_sdwa v139,1,v139 dst_sel:DWORD dst_unused:UNUSED_PAD src0_sel:DWORD src1_sel:WORD_1
v_exp_f16 v122,v123
v_mfma_f32_16x16x32_bf16 v[152:155],v[64:67],v[44:47],v[112:115]
ds_read_u16 v133,v133
v_mfma_f32_16x16x32_bf16 v[156:159],v[68:71],v[44:47],v[116:119]
ds_read_u16_d16_hi v135,v135
v_exp_f16_sdwa v122,v123 dst_sel:WORD_1 dst_unused:UNUSED_PRESERVE src0_sel:WORD_1
v_exp_f16_sdwa v120,v121 dst_sel:WORD_1 dst_unused:UNUSED_PRESERVE src0_sel:WORD_1
v_mfma_f32_16x16x32_bf16 v[148:151],v[68:71],v[40:43],v[116:119]
ds_read_b64_tr_b16 v[96:97],v6 offset:18544
ds_read_b64_tr_b16 v[98:99],v6 offset:20080
v_pk_fma_f16 v120,v120,s55,s55
v_pk_fma_f16 v122,v122,s55,s55
v_mfma_f32_16x16x32_bf16 v[144:147],v[72:75],v[48:51],v[144:147]
v_rcp_f16 v121,v120
v_mfma_f32_16x16x32_bf16 v[152:155],v[72:75],v[52:55],v[152:155]
ds_read_b64_tr_b16 v[100:101],v6 offset:18552
v_rcp_f16 v123,v122
v_mfma_f32_16x16x32_bf16 v[156:159],v[76:79],v[52:55],v[156:159]
ds_read_u16 v136,v136
v_rcp_f16_sdwa v121,v120 dst_sel:WORD_1 dst_unused:UNUSED_PRESERVE src0_sel:WORD_1
v_mfma_f32_16x16x32_bf16 v[148:151],v[76:79],v[48:51],v[148:151]
ds_read_b64_tr_b16 v[102:103],v6 offset:20088
ds_read_b64_tr_b16 v[104:105],v6 offset:18608
v_rcp_f16_sdwa v123,v122 dst_sel:WORD_1 dst_unused:UNUSED_PRESERVE src0_sel:WORD_1
v_mfma_f32_16x16x32_bf16 v[144:147],v[80:83],v[56:59],v[144:147]
ds_read_u16_d16_hi v138,v138
v_fma_mix_f32 v142,v142,v121,0 op_sel:[0,1,0] op_sel_hi:[0,1,0]
v_mfma_f32_16x16x32_bf16 v[152:155],v[80:83],v[60:63],v[152:155]
s_waitcnt lgkmcnt(7)
ds_read_b64_tr_b16 v[106:107],v6 offset:20144
ds_read_b64_tr_b16 v[108:109],v6 offset:18616
v_fma_mix_f32 v140,v140,v121,0 op_sel_hi:[0,1,0]
v_lshl_add_u64 v[128:129],v[128:129],0,v[130:131]
v_lshl_add_u64 v[130:131],v[132:133],0,v[134:135]
v_mfma_f32_16x16x32_bf16 v[156:159],v[84:87],v[60:63],v[156:159]
ds_read_u16 v137,v137
v_fma_mix_f32 v141,v141,v123,0 op_sel_hi:[0,1,0]
v_fma_mix_f32 v143,v143,v123,0 op_sel:[0,1,0] op_sel_hi:[0,1,0]
v_mfma_f32_16x16x32_bf16 v[148:151],v[84:87],v[56:59],v[148:151]
ds_read_b64_tr_b16 v[110:111],v6 offset:20152
ds_read_b128 v[64:67],v7 offset:1536
v_lshlrev_b32_sdwa v144,1,v144 dst_sel:DWORD dst_unused:UNUSED_PAD src0_sel:DWORD src1_sel:WORD_1
v_lshlrev_b32_sdwa v146,1,v146 dst_sel:DWORD dst_unused:UNUSED_PAD src0_sel:DWORD src1_sel:WORD_1
v_mfma_f32_16x16x32_bf16 v[160:163],v[88:91],v[128:131],v[160:163]
ds_read_u16_d16_hi v139,v139
ds_read_b128 v[68:71],v7 offset:1568
v_lshlrev_b32_sdwa v145,1,v145 dst_sel:DWORD dst_unused:UNUSED_PAD src0_sel:DWORD src1_sel:WORD_1
v_lshlrev_b32_sdwa v147,1,v147 dst_sel:DWORD dst_unused:UNUSED_PAD src0_sel:DWORD src1_sel:WORD_1
v_fma_mixlo_f16 v120,v156,s99,0
v_fma_mixlo_f16 v122,v157,s99,0
v_mfma_f32_16x16x32_bf16 v[168:171],v[92:95],v[128:131],v[168:171]
s_waitcnt lgkmcnt(12)
ds_read_u16 v144,v144
ds_read_b128 v[72:75],v7 offset:1664
v_lshlrev_b32_sdwa v148,1,v148 dst_sel:DWORD dst_unused:UNUSED_PAD src0_sel:DWORD src1_sel:WORD_1
v_lshlrev_b32_sdwa v150,1,v150 dst_sel:DWORD dst_unused:UNUSED_PAD src0_sel:DWORD src1_sel:WORD_1
s_waitcnt lgkmcnt(3)
v_lshl_add_u64 v[136:137],v[136:137],0,v[138:139]
v_fma_mixhi_f16 v120,v158,s99,0
v_mfma_f32_16x16x32_bf16 v[176:179],v[96:99],v[128:131],v[176:179]
s_setprio 1
ds_read_u16_d16_hi v146,v146
ds_read_b128 v[76:79],v7 offset:1696
v_cvt_pk_bf16_f32 v138,v140,v142
v_cvt_pk_bf16_f32 v139,v141,v143
v_fma_mixhi_f16 v122,v159,s99,0
v_lshlrev_b32_sdwa v149,1,v149 dst_sel:DWORD dst_unused:UNUSED_PAD src0_sel:DWORD src1_sel:WORD_1
v_mfma_f32_16x16x32_bf16 v[184:187],v[100:103],v[128:131],v[184:187]
ds_read_u16 v145,v145
ds_read_b128 v[80:83],v7 offset:1792
v_lshlrev_b32_sdwa v151,1,v151 dst_sel:DWORD dst_unused:UNUSED_PAD src0_sel:DWORD src1_sel:WORD_1
v_pk_fma_f16 v121,v120,v120,s54
v_pk_fma_f16 v123,v122,v122,s54
v_pk_mul_f16 v121,v120,v121
v_mfma_f32_16x16x32_bf16 v[192:195],v[104:107],v[128:131],v[192:195]
ds_read_u16_d16_hi v147,v147
ds_read_b128 v[84:87],v7 offset:1824
v_pk_mul_f16 v123,v122,v123
v_lshlrev_b32_sdwa v152,1,v152 dst_sel:DWORD dst_unused:UNUSED_PAD src0_sel:DWORD src1_sel:WORD_1
v_lshlrev_b32_sdwa v154,1,v154 dst_sel:DWORD dst_unused:UNUSED_PAD src0_sel:DWORD src1_sel:WORD_1
v_exp_f16 v120,v121
v_mfma_f32_16x16x32_bf16 v[200:203],v[108:111],v[128:131],v[200:203]
ds_read_u16 v148,v148
v_exp_f16 v122,v123
v_lshlrev_b32_sdwa v153,1,v153 dst_sel:DWORD dst_unused:UNUSED_PAD src0_sel:DWORD src1_sel:WORD_1
v_mfma_f32_16x16x32_bf16 v[164:167],v[88:91],v[136:139],v[164:167]
ds_read_u16_d16_hi v150,v150
v_lshlrev_b32_sdwa v155,1,v155 dst_sel:DWORD dst_unused:UNUSED_PAD src0_sel:DWORD src1_sel:WORD_1
v_exp_f16_sdwa v120,v121 dst_sel:WORD_1 dst_unused:UNUSED_PRESERVE src0_sel:WORD_1
v_exp_f16_sdwa v122,v123 dst_sel:WORD_1 dst_unused:UNUSED_PRESERVE src0_sel:WORD_1
v_mfma_f32_16x16x32_bf16 v[172:175],v[92:95],v[136:139],v[172:175]
ds_read_u16 v149,v149
v_pk_fma_f16 v120,v120,s55,s55
v_pk_fma_f16 v122,v122,s55,s55
v_rcp_f16 v121,v120
v_mfma_f32_16x16x32_bf16 v[180:183],v[96:99],v[136:139],v[180:183]
ds_read_u16_d16_hi v151,v151
v_rcp_f16 v123,v122
v_rcp_f16_sdwa v121,v120 dst_sel:WORD_1 dst_unused:UNUSED_PRESERVE src0_sel:WORD_1
v_mfma_f32_16x16x32_bf16 v[188:191],v[100:103],v[136:139],v[188:191]
ds_read_u16 v152,v152
v_rcp_f16_sdwa v123,v122 dst_sel:WORD_1 dst_unused:UNUSED_PRESERVE src0_sel:WORD_1
v_fma_mix_f32 v156,v156,v121,0 op_sel_hi:[0,1,0]
v_mfma_f32_16x16x32_bf16 v[196:199],v[104:107],v[136:139],v[196:199]
s_setprio 0
ds_read_u16_d16_hi v154,v154
v_fma_mix_f32 v158,v158,v121,0 op_sel:[0,1,0] op_sel_hi:[0,1,0]
s_waitcnt lgkmcnt(7)
v_lshl_add_u64 v[144:145],v[144:145],0,v[146:147]
s_waitcnt lgkmcnt(2)
v_lshl_add_u64 v[146:147],v[148:149],0,v[150:151]
v_fma_mix_f32 v157,v157,v123,0 op_sel_hi:[0,1,0]
v_mfma_f32_16x16x32_bf16 v[204:207],v[108:111],v[136:139],v[204:207]
ds_read_u16 v153,v153
v_fma_mix_f32 v159,v159,v123,0 op_sel:[0,1,0] op_sel_hi:[0,1,0]
ds_read_u16_d16_hi v155,v155
s_waitcnt lgkmcnt(0)
v_lshl_add_u64 v[152:153],v[152:153],0,v[154:155]
v_cvt_pk_bf16_f32 v154,v156,v158
v_cvt_pk_bf16_f32 v155,v157,v159
v_mfma_f32_16x16x32_bf16 v[208:211],v[88:91],v[144:147],v[208:211]
s_barrier
s_setprio 0
v_xor_b32 v9,64,v10
v_mul_f32 v160,v160,v64
v_mul_f32 v161,v161,v65
v_mul_f32 v162,v162,v66
v_mul_f32 v163,v163,v67
v_mfma_f32_16x16x32_bf16 v[216:219],v[92:95],v[144:147],v[216:219]
v_mul_f32 v168,v168,v68
v_mul_f32 v169,v169,v69
v_mul_f32 v170,v170,v70
v_mul_f32 v171,v171,v71
v_cvt_pk_bf16_f32 v16,v160,v161
v_mfma_f32_16x16x32_bf16 v[224:227],v[96:99],v[144:147],v[224:227]
v_cvt_pk_bf16_f32 v17,v162,v163
v_cvt_pk_bf16_f32 v18,v168,v169
v_cvt_pk_bf16_f32 v19,v170,v171
v_mul_f32_dpp v176,v176,v72 quad_perm:[1,0,3,2] row_mask:0xf bank_mask:0xf
v_mfma_f32_16x16x32_bf16 v[232:235],v[100:103],v[144:147],v[232:235]
v_mul_f32_dpp v177,v177,v73 quad_perm:[1,0,3,2] row_mask:0xf bank_mask:0xf
v_mul_f32_dpp v178,v178,v74 quad_perm:[1,0,3,2] row_mask:0xf bank_mask:0xf
v_mul_f32_dpp v179,v179,v75 quad_perm:[1,0,3,2] row_mask:0xf bank_mask:0xf
v_mul_f32_dpp v184,v184,v76 quad_perm:[1,0,3,2] row_mask:0xf bank_mask:0xf
v_mul_f32_dpp v185,v185,v77 quad_perm:[1,0,3,2] row_mask:0xf bank_mask:0xf
v_mfma_f32_16x16x32_bf16 v[240:243],v[104:107],v[144:147],v[240:243]
v_mul_f32_dpp v186,v186,v78 quad_perm:[1,0,3,2] row_mask:0xf bank_mask:0xf
v_mul_f32_dpp v187,v187,v79 quad_perm:[1,0,3,2] row_mask:0xf bank_mask:0xf
v_cvt_pk_bf16_f32 v24,v176,v177
v_cvt_pk_bf16_f32 v25,v178,v179
v_mfma_f32_16x16x32_bf16 v[248:251],v[108:111],v[144:147],v[248:251]
v_cvt_pk_bf16_f32 v26,v184,v185
v_cvt_pk_bf16_f32 v27,v186,v187
s_mov_b64 exec,s[12:13]
v_swap_b32 v16,v24
v_swap_b32 v17,v25
v_swap_b32 v18,v26
v_swap_b32 v19,v27
s_mov_b64 exec,-1
v_mfma_f32_16x16x32_bf16 v[212:215],v[88:91],v[152:155],v[212:215]
s_add_u32 s65,s61,1572864
buffer_store_dwordx4 v[16:19],v10,s[40:43],s65 offen offset:0 sc1
v_mfma_f32_16x16x32_bf16 v[220:223],v[92:95],v[152:155],v[220:223]
v_mul_f32 v192,v192,v80
v_mul_f32 v193,v193,v81
v_mul_f32 v194,v194,v82
v_mul_f32 v195,v195,v83
v_mfma_f32_16x16x32_bf16 v[228:231],v[96:99],v[152:155],v[228:231]
v_mul_f32 v200,v200,v84
v_mul_f32 v201,v201,v85
v_mul_f32 v202,v202,v86
v_mul_f32 v203,v203,v87
v_mfma_f32_16x16x32_bf16 v[236:239],v[100:103],v[152:155],v[236:239]
v_cvt_pk_bf16_f32 v32,v192,v193
v_cvt_pk_bf16_f32 v33,v194,v195
v_cvt_pk_bf16_f32 v34,v200,v201
v_cvt_pk_bf16_f32 v35,v202,v203
v_mul_f32_dpp v164,v164,v64 quad_perm:[1,0,3,2] row_mask:0xf bank_mask:0xf
v_mfma_f32_16x16x32_bf16 v[244:247],v[104:107],v[152:155],v[244:247]
v_mul_f32_dpp v165,v165,v65 quad_perm:[1,0,3,2] row_mask:0xf bank_mask:0xf
v_mul_f32_dpp v166,v166,v66 quad_perm:[1,0,3,2] row_mask:0xf bank_mask:0xf
v_mul_f32_dpp v167,v167,v67 quad_perm:[1,0,3,2] row_mask:0xf bank_mask:0xf
v_mul_f32_dpp v172,v172,v68 quad_perm:[1,0,3,2] row_mask:0xf bank_mask:0xf
v_mfma_f32_16x16x32_bf16 v[252:255],v[108:111],v[152:155],v[252:255]
s_add_u32 s65,s61,1572864
buffer_store_dwordx4 v[24:27],v9,s[40:43],s65 offen offset:384 sc1
v_mul_f32_dpp v173,v173,v69 quad_perm:[1,0,3,2] row_mask:0xf bank_mask:0xf
v_mul_f32_dpp v174,v174,v70 quad_perm:[1,0,3,2] row_mask:0xf bank_mask:0xf
v_mul_f32_dpp v175,v175,v71 quad_perm:[1,0,3,2] row_mask:0xf bank_mask:0xf
v_cvt_pk_bf16_f32 v20,v164,v165
v_cvt_pk_bf16_f32 v21,v166,v167
v_cvt_pk_bf16_f32 v22,v172,v173
v_cvt_pk_bf16_f32 v23,v174,v175
s_mov_b64 exec,s[12:13]
v_swap_b32 v32,v20
v_swap_b32 v33,v21
v_swap_b32 v34,v22
v_swap_b32 v35,v23
s_mov_b64 exec,-1
v_mul_f32 v180,v180,v72
v_mul_f32 v181,v181,v73
v_mul_f32 v182,v182,v74
v_mul_f32 v183,v183,v75
v_mul_f32 v188,v188,v76
v_mul_f32 v189,v189,v77
s_add_u32 s65,s61,1572864
buffer_store_dwordx4 v[32:35],v10,s[40:43],s65 offen offset:128 sc1
v_mul_f32 v190,v190,v78
v_mul_f32 v191,v191,v79
v_cvt_pk_bf16_f32 v28,v180,v181
v_cvt_pk_bf16_f32 v29,v182,v183
v_cvt_pk_bf16_f32 v30,v188,v189
v_cvt_pk_bf16_f32 v31,v190,v191
v_mul_f32_dpp v196,v196,v80 quad_perm:[1,0,3,2] row_mask:0xf bank_mask:0xf
v_mul_f32_dpp v197,v197,v81 quad_perm:[1,0,3,2] row_mask:0xf bank_mask:0xf
v_mul_f32_dpp v198,v198,v82 quad_perm:[1,0,3,2] row_mask:0xf bank_mask:0xf
v_mul_f32_dpp v199,v199,v83 quad_perm:[1,0,3,2] row_mask:0xf bank_mask:0xf
v_mul_f32_dpp v204,v204,v84 quad_perm:[1,0,3,2] row_mask:0xf bank_mask:0xf
v_mul_f32_dpp v205,v205,v85 quad_perm:[1,0,3,2] row_mask:0xf bank_mask:0xf
v_mul_f32_dpp v206,v206,v86 quad_perm:[1,0,3,2] row_mask:0xf bank_mask:0xf
v_mul_f32_dpp v207,v207,v87 quad_perm:[1,0,3,2] row_mask:0xf bank_mask:0xf
v_cvt_pk_bf16_f32 v36,v196,v197
v_cvt_pk_bf16_f32 v37,v198,v199
v_cvt_pk_bf16_f32 v38,v204,v205
v_cvt_pk_bf16_f32 v39,v206,v207
s_mov_b64 exec,s[12:13]
v_swap_b32 v28,v36
v_swap_b32 v29,v37
v_swap_b32 v30,v38
v_swap_b32 v31,v39
s_mov_b64 exec,-1
s_add_u32 s65,s61,1572864
buffer_store_dwordx4 v[20:23],v9,s[40:43],s65 offen offset:512 sc1
v_mul_f32 v208,v208,v64
v_mul_f32 v209,v209,v65
v_mul_f32 v210,v210,v66
v_mul_f32 v211,v211,v67
v_mul_f32 v216,v216,v68
v_mul_f32 v217,v217,v69
v_mul_f32 v218,v218,v70
v_mul_f32 v219,v219,v71
v_cvt_pk_bf16_f32 v40,v208,v209
v_cvt_pk_bf16_f32 v41,v210,v211
v_cvt_pk_bf16_f32 v42,v216,v217
v_cvt_pk_bf16_f32 v43,v218,v219
v_mul_f32_dpp v224,v224,v72 quad_perm:[1,0,3,2] row_mask:0xf bank_mask:0xf
v_mul_f32_dpp v225,v225,v73 quad_perm:[1,0,3,2] row_mask:0xf bank_mask:0xf
v_mul_f32_dpp v226,v226,v74 quad_perm:[1,0,3,2] row_mask:0xf bank_mask:0xf
v_mul_f32_dpp v227,v227,v75 quad_perm:[1,0,3,2] row_mask:0xf bank_mask:0xf
v_mul_f32_dpp v232,v232,v76 quad_perm:[1,0,3,2] row_mask:0xf bank_mask:0xf
v_mul_f32_dpp v233,v233,v77 quad_perm:[1,0,3,2] row_mask:0xf bank_mask:0xf
v_mul_f32_dpp v234,v234,v78 quad_perm:[1,0,3,2] row_mask:0xf bank_mask:0xf
v_mul_f32_dpp v235,v235,v79 quad_perm:[1,0,3,2] row_mask:0xf bank_mask:0xf
v_cvt_pk_bf16_f32 v48,v224,v225
v_cvt_pk_bf16_f32 v49,v226,v227
v_cvt_pk_bf16_f32 v50,v232,v233
v_cvt_pk_bf16_f32 v51,v234,v235
s_add_u32 s65,s61,1572864
buffer_store_dwordx4 v[28:31],v10,s[40:43],s65 offen offset:256 sc1
s_mov_b64 exec,s[12:13]
v_swap_b32 v40,v48
v_swap_b32 v41,v49
v_swap_b32 v42,v50
v_swap_b32 v43,v51
s_mov_b64 exec,-1
v_mul_f32 v240,v240,v80
v_mul_f32 v241,v241,v81
v_mul_f32 v242,v242,v82
v_mul_f32 v243,v243,v83
v_mul_f32 v248,v248,v84
v_mul_f32 v249,v249,v85
v_mul_f32 v250,v250,v86
v_mul_f32 v251,v251,v87
v_cvt_pk_bf16_f32 v56,v240,v241
v_cvt_pk_bf16_f32 v57,v242,v243
v_cvt_pk_bf16_f32 v58,v248,v249
v_cvt_pk_bf16_f32 v59,v250,v251
v_mul_f32_dpp v212,v212,v64 quad_perm:[1,0,3,2] row_mask:0xf bank_mask:0xf
v_mul_f32_dpp v213,v213,v65 quad_perm:[1,0,3,2] row_mask:0xf bank_mask:0xf
v_mul_f32_dpp v214,v214,v66 quad_perm:[1,0,3,2] row_mask:0xf bank_mask:0xf
v_mul_f32_dpp v215,v215,v67 quad_perm:[1,0,3,2] row_mask:0xf bank_mask:0xf
v_mul_f32_dpp v220,v220,v68 quad_perm:[1,0,3,2] row_mask:0xf bank_mask:0xf
s_add_u32 s65,s61,1572864
buffer_store_dwordx4 v[36:39],v9,s[40:43],s65 offen offset:640 sc1
v_mul_f32_dpp v221,v221,v69 quad_perm:[1,0,3,2] row_mask:0xf bank_mask:0xf
v_mul_f32_dpp v222,v222,v70 quad_perm:[1,0,3,2] row_mask:0xf bank_mask:0xf
v_mul_f32_dpp v223,v223,v71 quad_perm:[1,0,3,2] row_mask:0xf bank_mask:0xf
v_cvt_pk_bf16_f32 v44,v212,v213
v_cvt_pk_bf16_f32 v45,v214,v215
v_cvt_pk_bf16_f32 v46,v220,v221
v_cvt_pk_bf16_f32 v47,v222,v223
s_mov_b64 exec,s[12:13]
v_swap_b32 v56,v44
v_swap_b32 v57,v45
v_swap_b32 v58,v46
v_swap_b32 v59,v47
s_mov_b64 exec,-1
v_mul_f32 v228,v228,v72
v_mul_f32 v229,v229,v73
v_mul_f32 v230,v230,v74
v_mul_f32 v231,v231,v75
v_mul_f32 v236,v236,v76
v_mul_f32 v237,v237,v77
v_mul_f32 v238,v238,v78
v_mul_f32 v239,v239,v79
v_cvt_pk_bf16_f32 v52,v228,v229
v_cvt_pk_bf16_f32 v53,v230,v231
s_add_u32 s65,s61,1671168
buffer_store_dwordx4 v[40:43],v10,s[40:43],s65 offen offset:0 sc1
v_cvt_pk_bf16_f32 v54,v236,v237
v_cvt_pk_bf16_f32 v55,v238,v239
v_mul_f32_dpp v244,v244,v80 quad_perm:[1,0,3,2] row_mask:0xf bank_mask:0xf
v_mul_f32_dpp v245,v245,v81 quad_perm:[1,0,3,2] row_mask:0xf bank_mask:0xf
v_mul_f32_dpp v246,v246,v82 quad_perm:[1,0,3,2] row_mask:0xf bank_mask:0xf
v_mul_f32_dpp v247,v247,v83 quad_perm:[1,0,3,2] row_mask:0xf bank_mask:0xf
v_mul_f32_dpp v252,v252,v84 quad_perm:[1,0,3,2] row_mask:0xf bank_mask:0xf
v_mul_f32_dpp v253,v253,v85 quad_perm:[1,0,3,2] row_mask:0xf bank_mask:0xf
v_mul_f32_dpp v254,v254,v86 quad_perm:[1,0,3,2] row_mask:0xf bank_mask:0xf
v_mul_f32_dpp v255,v255,v87 quad_perm:[1,0,3,2] row_mask:0xf bank_mask:0xf
v_cvt_pk_bf16_f32 v60,v244,v245
v_cvt_pk_bf16_f32 v61,v246,v247
s_nop 0
s_mov_b32 s86,0xbe801d5a
v_cvt_pk_bf16_f32 v62,v252,v253
v_cvt_pk_bf16_f32 v63,v254,v255
s_mov_b64 exec,s[12:13]
v_swap_b32 v52,v60
v_swap_b32 v53,v61
v_swap_b32 v54,v62
v_swap_b32 v55,v63
s_mov_b64 exec,-1
s_add_u32 s4,s10,8
s_cmp_ge_u32 s4,20
s_cselect_b32 s7,20,0
s_sub_u32 s4,s4,s7
s_mul_i32 s4,s4,3488
s_add_u32 s5,s10,10
s_cmp_ge_u32 s5,20
s_cselect_b32 s7,20,0
s_sub_u32 s5,s5,s7
s_mul_i32 s5,s5,3488
s_add_u32 s6,s10,12
s_cmp_ge_u32 s6,20
s_cselect_b32 s7,20,0
s_sub_u32 s6,s6,s7
s_mul_i32 s6,s6,3488
v_add_u32 v112,s4,v15
v_add_u32 v116,s5,v15
s_mov_b64 exec,s[8:9]
v_add_u32 v112,s5,v15
v_add_u32 v116,s6,v15
s_mov_b64 exec,-1
v_xor_b32 v113,16,v112
v_xor_b32 v117,16,v116
v_xor_b32 v114,32,v112
v_xor_b32 v118,32,v116
v_xor_b32 v115,48,v112
v_xor_b32 v119,48,v116
s_barrier
s_setprio 3
ds_read_b128 v[136:139],v116 offset:0
ds_read_b128 v[88:91],v11 offset:768
ds_read_b128 v[140:143],v116 offset:3488
ds_read_b128 v[128:131],v112 offset:0
ds_read_b128 v[132:135],v112 offset:3488
ds_read_b128 v[92:95],v12 offset:0
ds_read_b128 v[152:155],v117 offset:0
ds_read_b128 v[96:99],v11 offset:1920
ds_read_b128 v[156:159],v117 offset:3488
ds_read_b128 v[144:147],v113 offset:0
ds_read_b128 v[148:151],v113 offset:3488
ds_read_b128 v[100:103],v12 offset:1152
s_waitcnt lgkmcnt(10)
v_mfma_f32_16x16x32_bf16 v[160:163],v[88:91],v[136:139],0
s_waitcnt lgkmcnt(9)
s_add_u32 s65,s61,1671168
buffer_store_dwordx4 v[48:51],v9,s[40:43],s65 offen offset:384 sc1
v_mfma_f32_16x16x32_bf16 v[184:187],v[88:91],v[140:143],0
s_waitcnt lgkmcnt(6)
ds_read_b128 v[140:143],v116 offset:3552
ds_read_b128 v[88:91],v11 offset:5376
v_smfmac_f32_16x16x64_bf16 v[160:163],v[92:95],v[128:135],v13
ds_read_b128 v[128:131],v112 offset:64
v_smfmac_f32_16x16x64_bf16 v[184:187],v[92:95],v[132:139],v13
s_waitcnt lgkmcnt(7)
ds_read_b128 v[136:139],v116 offset:64
ds_read_b128 v[132:135],v112 offset:3552
v_mfma_f32_16x16x32_bf16 v[164:167],v[96:99],v[152:155],0
s_waitcnt lgkmcnt(8)
ds_read_b128 v[92:95],v12 offset:4608
v_add_f32 v120,v160,v161
v_mul_f32 v124,v160,v160
v_fmac_f32 v124,v161,v161
v_mfma_f32_16x16x32_bf16 v[188:191],v[96:99],v[156:159],0
s_waitcnt lgkmcnt(6)
ds_read_b128 v[156:159],v117 offset:3552
ds_read_b128 v[96:99],v11 offset:6528
v_add_f32 v121,v162,v163
v_mul_f32 v125,v162,v162
v_fmac_f32 v125,v163,v163
v_smfmac_f32_16x16x64_bf16 v[164:167],v[100:103],v[144:151],v13
s_add_u32 s65,s61,1671168
buffer_store_dwordx4 v[56:59],v10,s[40:43],s65 offen offset:128 sc1
ds_read_b128 v[144:147],v113 offset:64
v_add_f32 v122,v184,v185
v_mul_f32 v126,v184,v184
v_fmac_f32 v126,v185,v185
v_smfmac_f32_16x16x64_bf16 v[188:191],v[100:103],v[148:155],v13
s_waitcnt lgkmcnt(5)
ds_read_b128 v[152:155],v117 offset:64
ds_read_b128 v[148:151],v113 offset:3552
v_add_f32 v123,v186,v187
v_mul_f32 v127,v186,v186
v_fmac_f32 v127,v187,v187
v_mfma_f32_16x16x32_bf16 v[168:171],v[88:91],v[136:139],0
ds_read_b128 v[100:103],v12 offset:5760
v_add_f32 v120,v120,v164
v_fmac_f32 v124,v164,v164
v_add_f32 v120,v120,v165
v_fmac_f32 v124,v165,v165
v_mfma_f32_16x16x32_bf16 v[192:195],v[88:91],v[140:143],0
s_waitcnt lgkmcnt(6)
ds_read_b128 v[140:143],v116 offset:3616
ds_read_b128 v[88:91],v11 offset:9984
v_add_f32 v121,v121,v166
v_fmac_f32 v125,v166,v166
v_add_f32 v121,v121,v167
v_fmac_f32 v125,v167,v167
v_smfmac_f32_16x16x64_bf16 v[168:171],v[92:95],v[128:135],v13
s_add_u32 s65,s61,1671168
buffer_store_dwordx4 v[44:47],v9,s[40:43],s65 offen offset:512 sc1
ds_read_b128 v[128:131],v112 offset:128
v_add_f32 v122,v122,v188
v_fmac_f32 v126,v188,v188
v_add_f32 v122,v122,v189
v_fmac_f32 v126,v189,v189
v_smfmac_f32_16x16x64_bf16 v[192:195],v[92:95],v[132:139],v13
s_waitcnt lgkmcnt(5)
ds_read_b128 v[136:139],v116 offset:128
ds_read_b128 v[132:135],v112 offset:3616
v_add_f32 v123,v123,v190
v_fmac_f32 v127,v190,v190
v_add_f32 v123,v123,v191
v_fmac_f32 v127,v191,v191
v_mfma_f32_16x16x32_bf16 v[172:175],v[96:99],v[152:155],0
ds_read_b128 v[92:95],v12 offset:9216
v_add_f32 v120,v120,v168
v_fmac_f32 v124,v168,v168
v_add_f32 v120,v120,v169
v_fmac_f32 v124,v169,v169
v_mfma_f32_16x16x32_bf16 v[196:199],v[96:99],v[156:159],0
s_waitcnt lgkmcnt(6)
ds_read_b128 v[156:159],v117 offset:3616
ds_read_b128 v[96:99],v11 offset:11136
v_add_f32 v121,v121,v170
v_fmac_f32 v125,v170,v170
v_add_f32 v121,v121,v171
v_fmac_f32 v125,v171,v171
v_smfmac_f32_16x16x64_bf16 v[172:175],v[100:103],v[144:151],v13
s_add_u32 s65,s61,1671168
buffer_store_dwordx4 v[52:55],v10,s[40:43],s65 offen offset:256 sc1
ds_read_b128 v[144:147],v113 offset:128
v_add_f32 v122,v122,v192
v_fmac_f32 v126,v192,v192
v_add_f32 v122,v122,v193
v_fmac_f32 v126,v193,v193
v_smfmac_f32_16x16x64_bf16 v[196:199],v[100:103],v[148:155],v13
s_waitcnt lgkmcnt(5)
ds_read_b128 v[152:155],v117 offset:128
ds_read_b128 v[148:151],v113 offset:3616
v_add_f32 v123,v123,v194
v_fmac_f32 v127,v194,v194
v_add_f32 v123,v123,v195
v_fmac_f32 v127,v195,v195
v_mfma_f32_16x16x32_bf16 v[176:179],v[88:91],v[136:139],0
ds_read_b128 v[100:103],v12 offset:10368
v_add_f32 v120,v120,v172
v_fmac_f32 v124,v172,v172
v_add_f32 v120,v120,v173
v_fmac_f32 v124,v173,v173
v_mfma_f32_16x16x32_bf16 v[200:203],v[88:91],v[140:143],0
s_waitcnt lgkmcnt(6)
ds_read_b128 v[140:143],v118 offset:3488
ds_read_b128 v[88:91],v11 offset:3072
v_add_f32 v121,v121,v174
v_fmac_f32 v125,v174,v174
v_add_f32 v121,v121,v175
v_fmac_f32 v125,v175,v175
v_smfmac_f32_16x16x64_bf16 v[176:179],v[92:95],v[128:135],v13
s_add_u32 s65,s61,1671168
buffer_store_dwordx4 v[60:63],v9,s[40:43],s65 offen offset:640 sc1
ds_read_b128 v[128:131],v114 offset:0
v_add_f32 v122,v122,v196
v_fmac_f32 v126,v196,v196
v_add_f32 v122,v122,v197
v_fmac_f32 v126,v197,v197
v_smfmac_f32_16x16x64_bf16 v[200:203],v[92:95],v[132:139],v13
s_waitcnt lgkmcnt(5)
ds_read_b128 v[136:139],v118 offset:0
ds_read_b128 v[132:135],v114 offset:3488
v_add_f32 v123,v123,v198
v_fmac_f32 v127,v198,v198
v_add_f32 v123,v123,v199
v_fmac_f32 v127,v199,v199
v_mfma_f32_16x16x32_bf16 v[180:183],v[96:99],v[152:155],0
ds_read_b128 v[92:95],v12 offset:2304
v_add_f32 v120,v120,v176
v_fmac_f32 v124,v176,v176
v_add_f32 v120,v120,v177
v_fmac_f32 v124,v177,v177
v_mfma_f32_16x16x32_bf16 v[204:207],v[96:99],v[156:159],0
s_waitcnt lgkmcnt(6)
ds_read_b128 v[156:159],v119 offset:3488
ds_read_b128 v[96:99],v11 offset:4224
v_add_f32 v121,v121,v178
v_fmac_f32 v125,v178,v178
v_add_f32 v121,v121,v179
v_fmac_f32 v125,v179,v179
v_smfmac_f32_16x16x64_bf16 v[180:183],v[100:103],v[144:151],v13
ds_read_b128 v[144:147],v115 offset:0
v_add_f32 v122,v122,v200
v_fmac_f32 v126,v200,v200
v_add_f32 v122,v122,v201
v_fmac_f32 v126,v201,v201
v_smfmac_f32_16x16x64_bf16 v[204:207],v[100:103],v[148:155],v13
s_waitcnt lgkmcnt(5)
ds_read_b128 v[152:155],v119 offset:0
ds_read_b128 v[148:151],v115 offset:3488
v_add_f32 v123,v123,v202
v_fmac_f32 v127,v202,v202
v_add_f32 v123,v123,v203
v_fmac_f32 v127,v203,v203
v_mfma_f32_16x16x32_bf16 v[208:211],v[88:91],v[136:139],0
ds_read_b128 v[100:103],v12 offset:3456
v_add_f32 v120,v120,v180
v_fmac_f32 v124,v180,v180
v_add_f32 v120,v120,v181
v_fmac_f32 v124,v181,v181
v_mfma_f32_16x16x32_bf16 v[232:235],v[88:91],v[140:143],0
s_waitcnt lgkmcnt(6)
ds_read_b128 v[140:143],v118 offset:3552
ds_read_b128 v[88:91],v11 offset:7680
v_add_f32 v121,v121,v182
v_fmac_f32 v125,v182,v182
v_add_f32 v121,v121,v183
v_fmac_f32 v125,v183,v183
v_smfmac_f32_16x16x64_bf16 v[208:211],v[92:95],v[128:135],v13
ds_read_b128 v[128:131],v114 offset:64
v_add_f32 v122,v122,v204
v_fmac_f32 v126,v204,v204
v_add_f32 v122,v122,v205
v_fmac_f32 v126,v205,v205
v_smfmac_f32_16x16x64_bf16 v[232:235],v[92:95],v[132:139],v13
s_waitcnt lgkmcnt(5)
ds_read_b128 v[136:139],v118 offset:64
ds_read_b128 v[132:135],v114 offset:3552
v_add_f32 v123,v123,v206
v_fmac_f32 v127,v206,v206
v_add_f32 v123,v123,v207
v_fmac_f32 v127,v207,v207
v_mfma_f32_16x16x32_bf16 v[212:215],v[96:99],v[152:155],0
ds_read_b128 v[92:95],v12 offset:6912
v_add_f32 v120,v120,v208
v_fmac_f32 v124,v208,v208
v_add_f32 v120,v120,v209
v_fmac_f32 v124,v209,v209
v_mfma_f32_16x16x32_bf16 v[236:239],v[96:99],v[156:159],0
s_waitcnt lgkmcnt(6)
s_setprio 2
ds_read_b128 v[156:159],v119 offset:3552
ds_read_b128 v[96:99],v11 offset:8832
v_add_f32 v121,v121,v210
v_fmac_f32 v125,v210,v210
v_add_f32 v121,v121,v211
v_fmac_f32 v125,v211,v211
v_smfmac_f32_16x16x64_bf16 v[212:215],v[100:103],v[144:151],v13
ds_read_b128 v[144:147],v115 offset:64
v_add_f32 v122,v122,v232
v_fmac_f32 v126,v232,v232
v_add_f32 v122,v122,v233
v_fmac_f32 v126,v233,v233
v_smfmac_f32_16x16x64_bf16 v[236:239],v[100:103],v[148:155],v13
s_waitcnt lgkmcnt(5)
ds_read_b128 v[152:155],v119 offset:64
ds_read_b128 v[148:151],v115 offset:3552
v_add_f32 v123,v123,v234
v_fmac_f32 v127,v234,v234
v_add_f32 v123,v123,v235
v_fmac_f32 v127,v235,v235
v_mfma_f32_16x16x32_bf16 v[216:219],v[88:91],v[136:139],0
ds_read_b128 v[100:103],v12 offset:8064
v_add_f32 v120,v120,v212
v_fmac_f32 v124,v212,v212
v_add_f32 v120,v120,v213
v_fmac_f32 v124,v213,v213
v_add_f32 v121,v121,v214
v_mfma_f32_16x16x32_bf16 v[240:243],v[88:91],v[140:143],0
s_waitcnt lgkmcnt(6)
ds_read_b128 v[140:143],v118 offset:3616
ds_read_b128 v[88:91],v11 offset:12288
v_fmac_f32 v125,v214,v214
v_add_f32 v121,v121,v215
v_fmac_f32 v125,v215,v215
v_add_f32 v122,v122,v236
v_smfmac_f32_16x16x64_bf16 v[216:219],v[92:95],v[128:135],v13
ds_read_b128 v[128:131],v114 offset:128
v_fmac_f32 v126,v236,v236
v_add_f32 v122,v122,v237
v_fmac_f32 v126,v237,v237
v_add_f32 v123,v123,v238
v_fmac_f32 v127,v238,v238
v_smfmac_f32_16x16x64_bf16 v[240:243],v[92:95],v[132:139],v13
s_waitcnt lgkmcnt(5)
ds_read_b128 v[136:139],v118 offset:128
ds_read_b128 v[132:135],v114 offset:3616
v_add_f32 v123,v123,v239
v_fmac_f32 v127,v239,v239
v_add_u32 v112,v112,v8
v_add_u32 v116,v116,v8
v_mfma_f32_16x16x32_bf16 v[220:223],v[96:99],v[152:155],0
ds_read_b128 v[92:95],v12 offset:11520
ds_read_b128 v[104:107],v14 offset:0
v_add_f32 v120,v120,v216
v_fmac_f32 v124,v216,v216
v_add_f32 v120,v120,v217
v_fmac_f32 v124,v217,v217
v_mfma_f32_16x16x32_bf16 v[244:247],v[96:99],v[156:159],0
s_waitcnt lgkmcnt(7)
ds_read_b128 v[108:111],v7 offset:3584
ds_read_b128 v[156:159],v119 offset:3616
v_add_f32 v121,v121,v218
v_fmac_f32 v125,v218,v218
v_add_f32 v121,v121,v219
v_fmac_f32 v125,v219,v219
v_smfmac_f32_16x16x64_bf16 v[220:223],v[100:103],v[144:151],v13
ds_read_b128 v[96:99],v11 offset:13440
ds_read_b128 v[144:147],v115 offset:128
v_add_f32 v122,v122,v240
v_fmac_f32 v126,v240,v240
v_add_f32 v122,v122,v241
v_fmac_f32 v126,v241,v241
v_smfmac_f32_16x16x64_bf16 v[244:247],v[100:103],v[148:155],v13
s_waitcnt lgkmcnt(7)
ds_read_b128 v[152:155],v119 offset:128
ds_read_b128 v[148:151],v115 offset:3616
v_add_f32 v123,v123,v242
v_fmac_f32 v127,v242,v242
v_add_f32 v123,v123,v243
v_fmac_f32 v127,v243,v243
v_mfma_f32_16x16x32_bf16 v[224:227],v[88:91],v[136:139],0
ds_read_b128 v[100:103],v12 offset:12672
v_add_f32 v120,v120,v220
v_fmac_f32 v124,v220,v220
v_add_f32 v120,v120,v221
v_fmac_f32 v124,v221,v221
v_mfma_f32_16x16x32_bf16 v[248:251],v[88:91],v[140:143],0
s_waitcnt lgkmcnt(8)
s_setprio 1
ds_read_b128 v[140:143],v116 offset:192
ds_read_b128 v[88:91],v112 offset:3616
v_add_f32 v121,v121,v222
v_fmac_f32 v125,v222,v222
v_add_f32 v121,v121,v223
v_fmac_f32 v125,v223,v223
v_smfmac_f32_16x16x64_bf16 v[224:227],v[92:95],v[128:135],v13
ds_read_b128 v[128:131],v112 offset:3488
v_add_f32 v122,v122,v244
v_fmac_f32 v126,v244,v244
v_add_f32 v122,v122,v245
v_fmac_f32 v126,v245,v245
v_smfmac_f32_16x16x64_bf16 v[248:251],v[92:95],v[132:139],v13
s_waitcnt lgkmcnt(5)
ds_read_b128 v[136:139],v116 offset:0
ds_read_b128 v[132:135],v112 offset:3680
v_add_f32 v123,v123,v246
v_fmac_f32 v127,v246,v246
v_add_f32 v123,v123,v247
v_fmac_f32 v127,v247,v247
v_mfma_f32_16x16x32_bf16 v[228:231],v[96:99],v[152:155],0
ds_read_b128 v[92:95],v112 offset:3808
v_add_f32 v120,v120,v224
v_fmac_f32 v124,v224,v224
v_add_f32 v120,v120,v225
v_fmac_f32 v124,v225,v225
v_mfma_f32_16x16x32_bf16 v[252:255],v[96:99],v[156:159],0
s_waitcnt lgkmcnt(6)
ds_read_b128 v[156:159],v116 offset:256
ds_read_b128 v[96:99],v116 offset:128
v_add_f32 v121,v121,v226
v_fmac_f32 v125,v226,v226
v_add_f32 v121,v121,v227
v_fmac_f32 v125,v227,v227
v_smfmac_f32_16x16x64_bf16 v[228:231],v[100:103],v[144:151],v13
ds_read_b128 v[144:147],v112 offset:3552
v_add_f32 v122,v122,v248
v_fmac_f32 v126,v248,v248
v_add_f32 v122,v122,v249
v_fmac_f32 v126,v249,v249
v_smfmac_f32_16x16x64_bf16 v[252:255],v[100:103],v[148:155],v13
s_setprio 0
ds_read_b128 v[152:155],v116 offset:64
ds_read_b128 v[148:151],v112 offset:3744
v_add_f32 v123,v123,v250
v_fmac_f32 v127,v250,v250
v_add_f32 v123,v123,v251
v_fmac_f32 v127,v251,v251
ds_read_b128 v[100:103],v116 offset:320
v_add_f32 v120,v120,v228
v_fmac_f32 v124,v228,v228
v_add_f32 v120,v120,v229
v_fmac_f32 v124,v229,v229
v_add_f32 v121,v121,v230
v_fmac_f32 v125,v230,v230
v_add_f32 v121,v121,v231
v_fmac_f32 v125,v231,v231
s_waitcnt lgkmcnt(0)
s_barrier
s_setprio 0
v_add_f32 v122,v122,v252
v_fmac_f32 v126,v252,v252
v_add_f32 v122,v122,v253
v_fmac_f32 v126,v253,v253
v_add_f32 v123,v123,v254
v_fmac_f32 v127,v254,v254
v_add_f32 v123,v123,v255
v_fmac_f32 v127,v255,v255
s_setprio 3
ds_read_b128 v[56:59],v14 offset:256
ds_read_b128 v[60:63],v7 offset:3616
s_setprio 0
v_permlane32_swap_b32 v120,v122
v_permlane32_swap_b32 v121,v123
v_permlane32_swap_b32 v124,v126
v_permlane32_swap_b32 v125,v127
v_add_f32 v120,v120,v122
v_add_f32 v121,v121,v123
v_add_f32 v124,v124,v126
v_add_f32 v125,v125,v127
v_permlane16_swap_b32 v120,v121
s_nop 0
v_permlane16_swap_b32 v124,v125
v_add_f32 v113,v120,v121
v_add_f32 v114,v124,v125
v_mul_f32 v113,0x3c2aaaab,v113
v_mul_f32 v114,0x3c2aaaab,v114
v_mul_f32 v115,0x5e000000,v114
v_fma_f32 v114,-v113,v113,v114
v_pk_maximum3_f16 v1,v1,v115,v115 op_sel:[0,1,1] op_sel_hi:[1,1,1]
v_max_f32 v114,0,v114
v_add_f32 v114,0x3727c5ac,v114
v_rsq_f32 v114,v114
s_nop 0
v_mul_f32 v120,0x8befe000,v114
v_mul_f32 v121,0x8befe000,v114
v_mul_f32 v124,-v113,v120
v_mul_f32 v125,-v113,v120
v_permlane16_swap_b32 v120,v121
s_nop 0
v_permlane16_swap_b32 v124,v125
v_mov_b32 v122,v120
v_mov_b32 v123,v121
v_mov_b32 v126,v124
v_mov_b32 v127,v125
v_permlane32_swap_b32 v120,v122
v_permlane32_swap_b32 v124,v126
v_permlane32_swap_b32 v121,v123
v_permlane32_swap_b32 v125,v127
v_add_u32 v8,94432,v4
v_add_u32 v9,94432,v5
v_fma_f32 v160,v160,v120,v124
s_setprio 3
ds_read_b64_tr_b16 v[64:65],v4 offset:512
s_setprio 0
v_fma_f32 v161,v161,v120,v124
s_setprio 3
ds_read_b64_tr_b16 v[66:67],v4 offset:2560
s_setprio 0
v_fma_f32 v162,v162,v121,v125
s_setprio 3
ds_read_b128 v[112:115],v7 offset:384
s_setprio 0
v_fma_f32 v163,v163,v121,v125
s_setprio 3
ds_read_b64_tr_b16 v[68:69],v5 offset:512
s_setprio 0
v_fma_f32 v164,v164,v120,v124
s_setprio 3
ds_read_b64_tr_b16 v[70:71],v5 offset:2560
s_setprio 0
v_fma_f32 v165,v165,v120,v124
s_setprio 3
ds_read_b128 v[116:119],v7 offset:416
s_setprio 0
v_fma_f32 v166,v166,v121,v125
s_setprio 3
ds_read_b64_tr_b16 v[72:73],v4 offset:4608
s_setprio 0
v_fma_f32 v167,v167,v121,v125
s_setprio 3
ds_read_b64_tr_b16 v[74:75],v4 offset:6656
s_setprio 0
v_fma_f32 v208,v208,v120,v124
s_setprio 3
ds_read_b64_tr_b16 v[76:77],v5 offset:4608
s_setprio 0
v_fma_f32 v209,v209,v120,v124
s_setprio 3
ds_read_b64_tr_b16 v[78:79],v5 offset:6656
s_setprio 0
v_fma_f32 v210,v210,v121,v125
s_setprio 3
ds_read_b64_tr_b16 v[80:81],v4 offset:8704
s_setprio 0
v_fma_f32 v211,v211,v121,v125
s_setprio 3
ds_read_b64_tr_b16 v[82:83],v4 offset:10752
s_setprio 0
v_fma_f32 v212,v212,v120,v124
s_setprio 3
ds_read_b64_tr_b16 v[84:85],v5 offset:8704
s_setprio 0
v_fma_f32 v213,v213,v120,v124
s_setprio 3
ds_read_b64_tr_b16 v[86:87],v5 offset:10752
s_setprio 0
v_fma_f32 v214,v214,v121,v125
v_fma_f32 v215,v215,v121,v125
v_cvt_pk_bf16_f32 v16,v160,v161
v_cvt_pk_bf16_f32 v20,v162,v163
v_cvt_pk_bf16_f32 v17,v164,v165
v_cvt_pk_bf16_f32 v21,v166,v167
v_cvt_pk_bf16_f32 v18,v208,v209
v_cvt_pk_bf16_f32 v22,v210,v211
v_cvt_pk_bf16_f32 v19,v212,v213
v_cvt_pk_bf16_f32 v23,v214,v215
v_fma_f32 v168,v168,v120,v124
v_fma_f32 v169,v169,v120,v124
v_mfma_f32_16x16x32_bf16 v[160:163],v[104:107],v[128:131],v[108:111]
v_fma_f32 v170,v170,v121,v125
v_fma_f32 v171,v171,v121,v125
v_fma_f32 v172,v172,v120,v124
v_fma_f32 v173,v173,v120,v124
v_fma_f32 v174,v174,v121,v125
v_fma_f32 v175,v175,v121,v125
v_mfma_f32_16x16x32_bf16 v[164:167],v[104:107],v[132:135],v[108:111]
v_fma_f32 v216,v216,v120,v124
v_fma_f32 v217,v217,v120,v124
v_fma_f32 v218,v218,v121,v125
v_fma_f32 v219,v219,v121,v125
v_fma_f32 v220,v220,v120,v124
v_fma_f32 v221,v221,v120,v124
v_mfma_f32_16x16x32_bf16 v[208:211],v[104:107],v[136:139],v[108:111]
v_fma_f32 v222,v222,v121,v125
v_fma_f32 v223,v223,v121,v125
v_cvt_pk_bf16_f32 v24,v168,v169
v_cvt_pk_bf16_f32 v28,v170,v171
v_cvt_pk_bf16_f32 v25,v172,v173
v_cvt_pk_bf16_f32 v29,v174,v175
s_setprio 3
v_mfma_f32_16x16x32_bf16 v[212:215],v[104:107],v[140:143],v[108:111]
ds_read_b128 v[104:107],v14 offset:512
s_setprio 0
v_cvt_pk_bf16_f32 v26,v216,v217
v_cvt_pk_bf16_f32 v30,v218,v219
v_cvt_pk_bf16_f32 v27,v220,v221
v_cvt_pk_bf16_f32 v31,v222,v223
v_fma_f32 v176,v176,v120,v124
v_fma_f32 v177,v177,v120,v124
s_waitcnt lgkmcnt(15)
s_setprio 3
v_mfma_f32_16x16x32_bf16 v[168:171],v[56:59],v[128:131],v[60:63]
ds_read_b128 v[108:111],v7 offset:3712
s_setprio 0
v_fma_f32 v178,v178,v121,v125
v_fma_f32 v179,v179,v121,v125
v_fma_f32 v180,v180,v120,v124
v_fma_f32 v181,v181,v120,v124
v_fma_f32 v182,v182,v121,v125
v_fma_f32 v183,v183,v121,v125
v_mfma_f32_16x16x32_bf16 v[172:175],v[56:59],v[132:135],v[60:63]
v_fma_f32 v224,v224,v120,v124
v_fma_f32 v225,v225,v120,v124
v_fma_f32 v226,v226,v121,v125
v_fma_f32 v227,v227,v121,v125
v_fma_f32 v228,v228,v120,v124
v_fma_f32 v229,v229,v120,v124
v_mfma_f32_16x16x32_bf16 v[216:219],v[56:59],v[136:139],v[60:63]
v_fma_f32 v230,v230,v121,v125
v_fma_f32 v231,v231,v121,v125
v_cvt_pk_bf16_f32 v32,v176,v177
v_cvt_pk_bf16_f32 v36,v178,v179
v_cvt_pk_bf16_f32 v33,v180,v181
v_cvt_pk_bf16_f32 v37,v182,v183
s_setprio 3
v_mfma_f32_16x16x32_bf16 v[220:223],v[56:59],v[140:143],v[60:63]
ds_read_b128 v[56:59],v14 offset:768
s_setprio 0
v_cvt_pk_bf16_f32 v34,v224,v225
v_cvt_pk_bf16_f32 v38,v226,v227
v_cvt_pk_bf16_f32 v35,v228,v229
v_cvt_pk_bf16_f32 v39,v230,v231
v_fma_f32 v184,v184,v122,v126
s_waitcnt lgkmcnt(1)
s_setprio 3
v_mfma_f32_16x16x32_bf16 v[176:179],v[104:107],v[144:147],v[108:111]
ds_read_b128 v[60:63],v7 offset:3744
s_setprio 0
v_fma_f32 v185,v185,v122,v126
v_fma_f32 v186,v186,v123,v127
v_fma_f32 v187,v187,v123,v127
v_mfma_f32_16x16x32_bf16 v[128:131],v[64:67],v[16:19],v[112:115]
v_fma_f32 v188,v188,v122,v126
v_fma_f32 v189,v189,v122,v126
v_fma_f32 v190,v190,v123,v127
v_mfma_f32_16x16x32_bf16 v[180:183],v[104:107],v[148:151],v[108:111]
v_fma_f32 v191,v191,v123,v127
v_fma_f32 v232,v232,v122,v126
v_fma_f32 v233,v233,v122,v126
v_mfma_f32_16x16x32_bf16 v[136:139],v[64:67],v[20:23],v[112:115]
v_fma_f32 v234,v234,v123,v127
v_fma_f32 v235,v235,v123,v127
v_fma_f32 v236,v236,v122,v126
v_mfma_f32_16x16x32_bf16 v[224:227],v[104:107],v[152:155],v[108:111]
v_fma_f32 v237,v237,v122,v126
v_fma_f32 v238,v238,v123,v127
v_fma_f32 v239,v239,v123,v127
v_mfma_f32_16x16x32_bf16 v[140:143],v[68:71],v[20:23],v[116:119]
v_cvt_pk_bf16_f32 v40,v184,v185
v_cvt_pk_bf16_f32 v44,v186,v187
v_cvt_pk_bf16_f32 v41,v188,v189
s_setprio 3
v_mfma_f32_16x16x32_bf16 v[228:231],v[104:107],v[156:159],v[108:111]
ds_read_b128 v[104:107],v14 offset:1024
s_setprio 0
v_cvt_pk_bf16_f32 v45,v190,v191
v_cvt_pk_bf16_f32 v42,v232,v233
v_cvt_pk_bf16_f32 v46,v234,v235
s_setprio 3
v_mfma_f32_16x16x32_bf16 v[132:135],v[68:71],v[16:19],v[116:119]
ds_read_b128 v[108:111],v7 offset:3840
s_setprio 0
v_cvt_pk_bf16_f32 v43,v236,v237
v_cvt_pk_bf16_f32 v47,v238,v239
v_fma_f32 v192,v192,v122,v126
s_waitcnt lgkmcnt(2)
v_mfma_f32_16x16x32_bf16 v[184:187],v[56:59],v[144:147],v[60:63]
v_fma_f32 v193,v193,v122,v126
v_fma_f32 v194,v194,v123,v127
v_mfma_f32_16x16x32_bf16 v[128:131],v[72:75],v[24:27],v[128:131]
v_fma_f32 v195,v195,v123,v127
v_fma_f32 v196,v196,v122,v126
v_mfma_f32_16x16x32_bf16 v[188:191],v[56:59],v[148:151],v[60:63]
v_fma_f32 v197,v197,v122,v126
v_fma_f32 v198,v198,v123,v127
v_mfma_f32_16x16x32_bf16 v[136:139],v[72:75],v[28:31],v[136:139]
v_fma_f32 v199,v199,v123,v127
v_fma_f32 v240,v240,v122,v126
v_mfma_f32_16x16x32_bf16 v[232:235],v[56:59],v[152:155],v[60:63]
v_fma_f32 v241,v241,v122,v126
v_fma_f32 v242,v242,v123,v127
v_mfma_f32_16x16x32_bf16 v[140:143],v[76:79],v[28:31],v[140:143]
v_fma_f32 v243,v243,v123,v127
v_fma_f32 v244,v244,v122,v126
s_setprio 3
v_mfma_f32_16x16x32_bf16 v[236:239],v[56:59],v[156:159],v[60:63]
ds_read_b128 v[144:147],v14 offset:1280
s_setprio 0
v_fma_f32 v245,v245,v122,v126
v_fma_f32 v246,v246,v123,v127
s_setprio 3
v_mfma_f32_16x16x32_bf16 v[132:135],v[76:79],v[24:27],v[132:135]
ds_read_b128 v[148:151],v7 offset:3872
s_setprio 0
v_fma_f32 v247,v247,v123,v127
v_cvt_pk_bf16_f32 v48,v192,v193
v_cvt_pk_bf16_f32 v52,v194,v195
v_cvt_pk_bf16_f32 v49,v196,v197
v_cvt_pk_bf16_f32 v53,v198,v199
v_cvt_pk_bf16_f32 v50,v240,v241
v_cvt_pk_bf16_f32 v54,v242,v243
v_cvt_pk_bf16_f32 v51,v244,v245
v_cvt_pk_bf16_f32 v55,v246,v247
v_fma_f32 v200,v200,v122,v126
s_waitcnt lgkmcnt(2)
v_mfma_f32_16x16x32_bf16 v[192:195],v[104:107],v[88:91],v[108:111]
v_fma_f32 v201,v201,v122,v126
v_fma_f32 v202,v202,v123,v127
v_fma_f32 v203,v203,v123,v127
v_mfma_f32_16x16x32_bf16 v[128:131],v[80:83],v[32:35],v[128:131]
v_fma_f32 v204,v204,v122,v126
v_fma_f32 v205,v205,v122,v126
v_fma_f32 v206,v206,v123,v127
v_mfma_f32_16x16x32_bf16 v[196:199],v[104:107],v[92:95],v[108:111]
v_fma_f32 v207,v207,v123,v127
v_fma_f32 v248,v248,v122,v126
v_fma_f32 v249,v249,v122,v126
v_mfma_f32_16x16x32_bf16 v[136:139],v[80:83],v[36:39],v[136:139]
v_fma_f32 v250,v250,v123,v127
v_fma_f32 v251,v251,v123,v127
s_nop 0
s_mov_b32 s86,0xbe801d5a
v_fma_f32 v252,v252,v122,v126
v_mfma_f32_16x16x32_bf16 v[240:243],v[104:107],v[96:99],v[108:111]
v_fma_f32 v253,v253,v122,v126
v_fma_f32 v254,v254,v123,v127
v_fma_f32 v255,v255,v123,v127
v_mfma_f32_16x16x32_bf16 v[140:143],v[84:87],v[36:39],v[140:143]
v_cvt_pk_bf16_f32 v56,v200,v201
v_cvt_pk_bf16_f32 v60,v202,v203
v_cvt_pk_bf16_f32 v57,v204,v205
v_mfma_f32_16x16x32_bf16 v[244:247],v[104:107],v[100:103],v[108:111]
v_cvt_pk_bf16_f32 v61,v206,v207
v_cvt_pk_bf16_f32 v58,v248,v249
v_cvt_pk_bf16_f32 v62,v250,v251
v_mfma_f32_16x16x32_bf16 v[132:135],v[84:87],v[32:35],v[132:135]
v_cvt_pk_bf16_f32 v59,v252,v253
v_cvt_pk_bf16_f32 v63,v254,v255
s_waitcnt lgkmcnt(0)
v_mfma_f32_16x16x32_bf16 v[200:203],v[144:147],v[88:91],v[148:151]
s_barrier
s_setprio 3
ds_read_b64_tr_b16 v[104:105],v6 offset:12448
v_lshlrev_b32_sdwa v128,1,v128 dst_sel:DWORD dst_unused:UNUSED_PAD src0_sel:DWORD src1_sel:WORD_1
v_lshlrev_b32_sdwa v130,1,v130 dst_sel:DWORD dst_unused:UNUSED_PAD src0_sel:DWORD src1_sel:WORD_1
v_lshlrev_b32_sdwa v129,1,v129 dst_sel:DWORD dst_unused:UNUSED_PAD src0_sel:DWORD src1_sel:WORD_1
v_mfma_f32_16x16x32_bf16 v[204:207],v[144:147],v[92:95],v[148:151]
ds_read_u16 v128,v128
ds_read_b64_tr_b16 v[106:107],v6 offset:13984
v_lshlrev_b32_sdwa v131,1,v131 dst_sel:DWORD dst_unused:UNUSED_PAD src0_sel:DWORD src1_sel:WORD_1
v_fma_mixlo_f16 v120,v140,s99,0
v_fma_mixlo_f16 v122,v141,s99,0
v_mfma_f32_16x16x32_bf16 v[248:251],v[144:147],v[96:99],v[148:151]
ds_read_u16_d16_hi v130,v130
ds_read_b64_tr_b16 v[108:109],v6 offset:12456
v_lshlrev_b32_sdwa v132,1,v132 dst_sel:DWORD dst_unused:UNUSED_PAD src0_sel:DWORD src1_sel:WORD_1
v_lshlrev_b32_sdwa v134,1,v134 dst_sel:DWORD dst_unused:UNUSED_PAD src0_sel:DWORD src1_sel:WORD_1
v_mfma_f32_16x16x32_bf16 v[252:255],v[144:147],v[100:103],v[148:151]
ds_read_u16 v129,v129
ds_read_b64_tr_b16 v[110:111],v6 offset:13992
v_fma_mixhi_f16 v120,v142,s99,0
v_fma_mixhi_f16 v122,v143,s99,0
v_lshlrev_b32_sdwa v133,1,v133 dst_sel:DWORD dst_unused:UNUSED_PAD src0_sel:DWORD src1_sel:WORD_1
v_pk_fma_f16 v121,v120,v120,s54
v_mfma_f32_16x16x32_bf16 v[144:147],v[64:67],v[40:43],v[112:115]
ds_read_u16_d16_hi v131,v131
ds_read_b64_tr_b16 v[88:89],v6 offset:12320
v_lshlrev_b32_sdwa v135,1,v135 dst_sel:DWORD dst_unused:UNUSED_PAD src0_sel:DWORD src1_sel:WORD_1
v_pk_fma_f16 v123,v122,v122,s54
v_pk_mul_f16 v121,v120,v121
v_mfma_f32_16x16x32_bf16 v[152:155],v[64:67],v[44:47],v[112:115]
ds_read_u16 v132,v132
ds_read_b64_tr_b16 v[90:91],v6 offset:13856
v_lshlrev_b32_sdwa v136,1,v136 dst_sel:DWORD dst_unused:UNUSED_PAD src0_sel:DWORD src1_sel:WORD_1
v_pk_mul_f16 v123,v122,v123
v_mfma_f32_16x16x32_bf16 v[156:159],v[68:71],v[44:47],v[116:119]
ds_read_u16_d16_hi v134,v134
ds_read_b64_tr_b16 v[92:93],v6 offset:12328
v_lshlrev_b32_sdwa v138,1,v138 dst_sel:DWORD dst_unused:UNUSED_PAD src0_sel:DWORD src1_sel:WORD_1
v_exp_f16 v120,v121
v_mfma_f32_16x16x32_bf16 v[148:151],v[68:71],v[40:43],v[116:119]
ds_read_u16 v133,v133
ds_read_b64_tr_b16 v[94:95],v6 offset:13864
v_lshlrev_b32_sdwa v137,1,v137 dst_sel:DWORD dst_unused:UNUSED_PAD src0_sel:DWORD src1_sel:WORD_1
v_lshlrev_b32_sdwa v139,1,v139 dst_sel:DWORD dst_unused:UNUSED_PAD src0_sel:DWORD src1_sel:WORD_1
v_exp_f16 v122,v123
v_mfma_f32_16x16x32_bf16 v[144:147],v[72:75],v[48:51],v[144:147]
ds_read_u16_d16_hi v135,v135
ds_read_b64_tr_b16 v[96:97],v6 offset:12384
v_exp_f16_sdwa v122,v123 dst_sel:WORD_1 dst_unused:UNUSED_PRESERVE src0_sel:WORD_1
v_mfma_f32_16x16x32_bf16 v[152:155],v[72:75],v[52:55],v[152:155]
ds_read_u16 v136,v136
ds_read_b64_tr_b16 v[98:99],v6 offset:13920
v_exp_f16_sdwa v120,v121 dst_sel:WORD_1 dst_unused:UNUSED_PRESERVE src0_sel:WORD_1
v_mfma_f32_16x16x32_bf16 v[156:159],v[76:79],v[52:55],v[156:159]
ds_read_u16_d16_hi v138,v138
ds_read_b64_tr_b16 v[100:101],v6 offset:12392
v_pk_fma_f16 v120,v120,s55,s55
v_pk_fma_f16 v122,v122,s55,s55
v_rcp_f16 v121,v120
v_mfma_f32_16x16x32_bf16 v[148:151],v[76:79],v[48:51],v[148:151]
ds_read_u16 v137,v137
ds_read_b64_tr_b16 v[102:103],v6 offset:13928
v_rcp_f16 v123,v122
v_mfma_f32_16x16x32_bf16 v[144:147],v[80:83],v[56:59],v[144:147]
ds_read_u16_d16_hi v139,v139
ds_read_b64_tr_b16 v[64:65],v4 offset:12320
v_rcp_f16_sdwa v121,v120 dst_sel:WORD_1 dst_unused:UNUSED_PRESERVE src0_sel:WORD_1
v_mfma_f32_16x16x32_bf16 v[152:155],v[80:83],v[60:63],v[152:155]
ds_read_b64_tr_b16 v[66:67],v4 offset:14368
v_rcp_f16_sdwa v123,v122 dst_sel:WORD_1 dst_unused:UNUSED_PRESERVE src0_sel:WORD_1
v_fma_mix_f32 v142,v142,v121,0 op_sel:[0,1,0] op_sel_hi:[0,1,0]
v_mfma_f32_16x16x32_bf16 v[156:159],v[84:87],v[60:63],v[156:159]
ds_read_b128 v[112:115],v7 offset:512
v_fma_mix_f32 v140,v140,v121,0 op_sel_hi:[0,1,0]
s_waitcnt lgkmcnt(15)
v_lshl_add_u64 v[128:129],v[128:129],0,v[130:131]
s_waitcnt lgkmcnt(11)
v_lshl_add_u64 v[130:131],v[132:133],0,v[134:135]
v_mfma_f32_16x16x32_bf16 v[148:151],v[84:87],v[56:59],v[148:151]
ds_read_b64_tr_b16 v[68:69],v5 offset:12320
v_fma_mix_f32 v141,v141,v123,0 op_sel_hi:[0,1,0]
v_fma_mix_f32 v143,v143,v123,0 op_sel:[0,1,0] op_sel_hi:[0,1,0]
v_lshlrev_b32_sdwa v144,1,v144 dst_sel:DWORD dst_unused:UNUSED_PAD src0_sel:DWORD src1_sel:WORD_1
v_lshlrev_b32_sdwa v146,1,v146 dst_sel:DWORD dst_unused:UNUSED_PAD src0_sel:DWORD src1_sel:WORD_1
v_mfma_f32_16x16x32_bf16 v[160:163],v[88:91],v[128:131],v[160:163]
ds_read_u16 v144,v144
v_lshlrev_b32_sdwa v145,1,v145 dst_sel:DWORD dst_unused:UNUSED_PAD src0_sel:DWORD src1_sel:WORD_1
v_lshlrev_b32_sdwa v147,1,v147 dst_sel:DWORD dst_unused:UNUSED_PAD src0_sel:DWORD src1_sel:WORD_1
v_mfma_f32_16x16x32_bf16 v[168:171],v[92:95],v[128:131],v[168:171]
s_waitcnt lgkmcnt(10)
ds_read_b64_tr_b16 v[70:71],v5 offset:14368
ds_read_b128 v[116:119],v7 offset:544
v_fma_mixlo_f16 v120,v156,s99,0
v_fma_mixlo_f16 v122,v157,s99,0
v_mfma_f32_16x16x32_bf16 v[176:179],v[96:99],v[128:131],v[176:179]
s_waitcnt lgkmcnt(8)
ds_read_u16_d16_hi v146,v146
v_lshlrev_b32_sdwa v148,1,v148 dst_sel:DWORD dst_unused:UNUSED_PAD src0_sel:DWORD src1_sel:WORD_1
v_lshlrev_b32_sdwa v150,1,v150 dst_sel:DWORD dst_unused:UNUSED_PAD src0_sel:DWORD src1_sel:WORD_1
v_mfma_f32_16x16x32_bf16 v[184:187],v[100:103],v[128:131],v[184:187]
s_waitcnt lgkmcnt(8)
ds_read_b64_tr_b16 v[72:73],v4 offset:16416
ds_read_b64_tr_b16 v[74:75],v4 offset:18464
v_lshl_add_u64 v[136:137],v[136:137],0,v[138:139]
v_fma_mixhi_f16 v120,v158,s99,0
v_mfma_f32_16x16x32_bf16 v[192:195],v[104:107],v[128:131],v[192:195]
ds_read_u16 v145,v145
v_cvt_pk_bf16_f32 v138,v140,v142
v_cvt_pk_bf16_f32 v139,v141,v143
v_mfma_f32_16x16x32_bf16 v[200:203],v[108:111],v[128:131],v[200:203]
ds_read_b64_tr_b16 v[76:77],v5 offset:16416
ds_read_b64_tr_b16 v[78:79],v5 offset:18464
v_fma_mixhi_f16 v122,v159,s99,0
v_mfma_f32_16x16x32_bf16 v[164:167],v[88:91],v[136:139],v[164:167]
v_lshlrev_b32_sdwa v149,1,v149 dst_sel:DWORD dst_unused:UNUSED_PAD src0_sel:DWORD src1_sel:WORD_1
v_lshlrev_b32_sdwa v151,1,v151 dst_sel:DWORD dst_unused:UNUSED_PAD src0_sel:DWORD src1_sel:WORD_1
v_mfma_f32_16x16x32_bf16 v[172:175],v[92:95],v[136:139],v[172:175]
ds_read_b64_tr_b16 v[80:81],v4 offset:20512
ds_read_b64_tr_b16 v[82:83],v4 offset:22560
v_pk_fma_f16 v121,v120,v120,s54
v_pk_fma_f16 v123,v122,v122,s54
v_mfma_f32_16x16x32_bf16 v[180:183],v[96:99],v[136:139],v[180:183]
ds_read_u16_d16_hi v147,v147
v_pk_mul_f16 v121,v120,v121
v_pk_mul_f16 v123,v122,v123
v_mfma_f32_16x16x32_bf16 v[188:191],v[100:103],v[136:139],v[188:191]
ds_read_u16 v148,v148
v_lshlrev_b32_sdwa v152,1,v152 dst_sel:DWORD dst_unused:UNUSED_PAD src0_sel:DWORD src1_sel:WORD_1
v_lshlrev_b32_sdwa v154,1,v154 dst_sel:DWORD dst_unused:UNUSED_PAD src0_sel:DWORD src1_sel:WORD_1
v_mfma_f32_16x16x32_bf16 v[196:199],v[104:107],v[136:139],v[196:199]
v_exp_f16 v120,v121
v_mfma_f32_16x16x32_bf16 v[204:207],v[108:111],v[136:139],v[204:207]
s_waitcnt lgkmcnt(14)
ds_read_b64_tr_b16 v[84:85],v5 offset:20512
ds_read_b64_tr_b16 v[86:87],v5 offset:22560
v_exp_f16 v122,v123
v_mfma_f32_16x16x32_bf16 v[128:131],v[64:67],v[16:19],v[112:115]
v_lshlrev_b32_sdwa v153,1,v153 dst_sel:DWORD dst_unused:UNUSED_PAD src0_sel:DWORD src1_sel:WORD_1
v_lshlrev_b32_sdwa v155,1,v155 dst_sel:DWORD dst_unused:UNUSED_PAD src0_sel:DWORD src1_sel:WORD_1
v_mfma_f32_16x16x32_bf16 v[136:139],v[64:67],v[20:23],v[112:115]
s_waitcnt lgkmcnt(12)
ds_read_u16_d16_hi v150,v150
ds_read_u16 v149,v149
v_exp_f16_sdwa v120,v121 dst_sel:WORD_1 dst_unused:UNUSED_PRESERVE src0_sel:WORD_1
v_mfma_f32_16x16x32_bf16 v[140:143],v[68:71],v[20:23],v[116:119]
v_exp_f16_sdwa v122,v123 dst_sel:WORD_1 dst_unused:UNUSED_PRESERVE src0_sel:WORD_1
v_mfma_f32_16x16x32_bf16 v[132:135],v[68:71],v[16:19],v[116:119]
s_waitcnt lgkmcnt(11)
v_pk_fma_f16 v120,v120,s55,s55
v_mfma_f32_16x16x32_bf16 v[128:131],v[72:75],v[24:27],v[128:131]
v_pk_fma_f16 v122,v122,s55,s55
v_rcp_f16 v121,v120
v_mfma_f32_16x16x32_bf16 v[136:139],v[72:75],v[28:31],v[136:139]
s_waitcnt lgkmcnt(8)
ds_read_u16_d16_hi v151,v151
v_rcp_f16 v123,v122
v_mfma_f32_16x16x32_bf16 v[140:143],v[76:79],v[28:31],v[140:143]
ds_read_u16 v152,v152
ds_read_u16_d16_hi v154,v154
v_rcp_f16_sdwa v121,v120 dst_sel:WORD_1 dst_unused:UNUSED_PRESERVE src0_sel:WORD_1
v_mfma_f32_16x16x32_bf16 v[132:135],v[76:79],v[24:27],v[132:135]
s_waitcnt lgkmcnt(9)
v_rcp_f16_sdwa v123,v122 dst_sel:WORD_1 dst_unused:UNUSED_PRESERVE src0_sel:WORD_1
v_mfma_f32_16x16x32_bf16 v[128:131],v[80:83],v[32:35],v[128:131]
ds_read_u16 v153,v153
v_fma_mix_f32 v156,v156,v121,0 op_sel_hi:[0,1,0]
v_fma_mix_f32 v158,v158,v121,0 op_sel:[0,1,0] op_sel_hi:[0,1,0]
v_mfma_f32_16x16x32_bf16 v[136:139],v[80:83],v[36:39],v[136:139]
s_waitcnt lgkmcnt(3)
v_lshl_add_u64 v[144:145],v[144:145],0,v[146:147]
v_lshl_add_u64 v[146:147],v[148:149],0,v[150:151]
v_mfma_f32_16x16x32_bf16 v[140:143],v[84:87],v[36:39],v[140:143]
ds_read_u16_d16_hi v155,v155
v_fma_mix_f32 v157,v157,v123,0 op_sel_hi:[0,1,0]
v_fma_mix_f32 v159,v159,v123,0 op_sel:[0,1,0] op_sel_hi:[0,1,0]
v_mfma_f32_16x16x32_bf16 v[132:135],v[84:87],v[32:35],v[132:135]
v_lshlrev_b32_sdwa v128,1,v128 dst_sel:DWORD dst_unused:UNUSED_PAD src0_sel:DWORD src1_sel:WORD_1
v_lshlrev_b32_sdwa v130,1,v130 dst_sel:DWORD dst_unused:UNUSED_PAD src0_sel:DWORD src1_sel:WORD_1
v_mfma_f32_16x16x32_bf16 v[208:211],v[88:91],v[144:147],v[208:211]
v_lshlrev_b32_sdwa v129,1,v129 dst_sel:DWORD dst_unused:UNUSED_PAD src0_sel:DWORD src1_sel:WORD_1
v_lshlrev_b32_sdwa v131,1,v131 dst_sel:DWORD dst_unused:UNUSED_PAD src0_sel:DWORD src1_sel:WORD_1
v_mfma_f32_16x16x32_bf16 v[216:219],v[92:95],v[144:147],v[216:219]
ds_read_u16 v128,v128
ds_read_u16_d16_hi v130,v130
v_fma_mixlo_f16 v120,v140,s99,0
v_fma_mixlo_f16 v122,v141,s99,0
v_mfma_f32_16x16x32_bf16 v[224:227],v[96:99],v[144:147],v[224:227]
ds_read_u16 v129,v129
v_lshlrev_b32_sdwa v132,1,v132 dst_sel:DWORD dst_unused:UNUSED_PAD src0_sel:DWORD src1_sel:WORD_1
v_lshlrev_b32_sdwa v134,1,v134 dst_sel:DWORD dst_unused:UNUSED_PAD src0_sel:DWORD src1_sel:WORD_1
v_mfma_f32_16x16x32_bf16 v[232:235],v[100:103],v[144:147],v[232:235]
s_waitcnt lgkmcnt(3)
v_lshl_add_u64 v[152:153],v[152:153],0,v[154:155]
v_cvt_pk_bf16_f32 v154,v156,v158
v_mfma_f32_16x16x32_bf16 v[240:243],v[104:107],v[144:147],v[240:243]
v_fma_mixhi_f16 v120,v142,s99,0
v_cvt_pk_bf16_f32 v155,v157,v159
v_mfma_f32_16x16x32_bf16 v[248:251],v[108:111],v[144:147],v[248:251]
ds_read_u16_d16_hi v131,v131
v_fma_mixhi_f16 v122,v143,s99,0
v_mfma_f32_16x16x32_bf16 v[212:215],v[88:91],v[152:155],v[212:215]
v_lshlrev_b32_sdwa v133,1,v133 dst_sel:DWORD dst_unused:UNUSED_PAD src0_sel:DWORD src1_sel:WORD_1
v_pk_fma_f16 v121,v120,v120,s54
v_lshlrev_b32_sdwa v135,1,v135 dst_sel:DWORD dst_unused:UNUSED_PAD src0_sel:DWORD src1_sel:WORD_1
v_mfma_f32_16x16x32_bf16 v[220:223],v[92:95],v[152:155],v[220:223]
s_setprio 2
ds_read_u16 v132,v132
ds_read_u16_d16_hi v134,v134
v_pk_fma_f16 v123,v122,v122,s54
v_mfma_f32_16x16x32_bf16 v[228:231],v[96:99],v[152:155],v[228:231]
ds_read_b64_tr_b16 v[88:89],v6 offset:18480
v_pk_mul_f16 v121,v120,v121
v_lshlrev_b32_sdwa v136,1,v136 dst_sel:DWORD dst_unused:UNUSED_PAD src0_sel:DWORD src1_sel:WORD_1
v_pk_mul_f16 v123,v122,v123
v_mfma_f32_16x16x32_bf16 v[236:239],v[100:103],v[152:155],v[236:239]
ds_read_b64_tr_b16 v[90:91],v6 offset:20016
v_lshlrev_b32_sdwa v138,1,v138 dst_sel:DWORD dst_unused:UNUSED_PAD src0_sel:DWORD src1_sel:WORD_1
v_mfma_f32_16x16x32_bf16 v[244:247],v[104:107],v[152:155],v[244:247]
ds_read_u16 v133,v133
v_exp_f16 v120,v121
v_mfma_f32_16x16x32_bf16 v[252:255],v[108:111],v[152:155],v[252:255]
ds_read_b64_tr_b16 v[92:93],v6 offset:18488
v_lshlrev_b32_sdwa v137,1,v137 dst_sel:DWORD dst_unused:UNUSED_PAD src0_sel:DWORD src1_sel:WORD_1
v_mfma_f32_16x16x32_bf16 v[144:147],v[64:67],v[40:43],v[112:115]
ds_read_b64_tr_b16 v[94:95],v6 offset:20024
v_lshlrev_b32_sdwa v139,1,v139 dst_sel:DWORD dst_unused:UNUSED_PAD src0_sel:DWORD src1_sel:WORD_1
v_exp_f16 v122,v123
v_mfma_f32_16x16x32_bf16 v[152:155],v[64:67],v[44:47],v[112:115]
ds_read_u16_d16_hi v135,v135
v_mfma_f32_16x16x32_bf16 v[156:159],v[68:71],v[44:47],v[116:119]
ds_read_u16 v136,v136
v_exp_f16_sdwa v122,v123 dst_sel:WORD_1 dst_unused:UNUSED_PRESERVE src0_sel:WORD_1
v_exp_f16_sdwa v120,v121 dst_sel:WORD_1 dst_unused:UNUSED_PRESERVE src0_sel:WORD_1
v_mfma_f32_16x16x32_bf16 v[148:151],v[68:71],v[40:43],v[116:119]
ds_read_b64_tr_b16 v[96:97],v6 offset:18544
ds_read_b64_tr_b16 v[98:99],v6 offset:20080
v_pk_fma_f16 v120,v120,s55,s55
v_pk_fma_f16 v122,v122,s55,s55
v_mfma_f32_16x16x32_bf16 v[144:147],v[72:75],v[48:51],v[144:147]
v_rcp_f16 v121,v120
v_mfma_f32_16x16x32_bf16 v[152:155],v[72:75],v[52:55],v[152:155]
ds_read_b64_tr_b16 v[100:101],v6 offset:18552
v_rcp_f16 v123,v122
v_mfma_f32_16x16x32_bf16 v[156:159],v[76:79],v[52:55],v[156:159]
ds_read_u16_d16_hi v138,v138
v_rcp_f16_sdwa v121,v120 dst_sel:WORD_1 dst_unused:UNUSED_PRESERVE src0_sel:WORD_1
v_mfma_f32_16x16x32_bf16 v[148:151],v[76:79],v[48:51],v[148:151]
ds_read_b64_tr_b16 v[102:103],v6 offset:20088
ds_read_b64_tr_b16 v[104:105],v6 offset:18608
v_rcp_f16_sdwa v123,v122 dst_sel:WORD_1 dst_unused:UNUSED_PRESERVE src0_sel:WORD_1
v_mfma_f32_16x16x32_bf16 v[144:147],v[80:83],v[56:59],v[144:147]
ds_read_u16 v137,v137
v_fma_mix_f32 v142,v142,v121,0 op_sel:[0,1,0] op_sel_hi:[0,1,0]
v_mfma_f32_16x16x32_bf16 v[152:155],v[80:83],v[60:63],v[152:155]
s_waitcnt lgkmcnt(8)
ds_read_b64_tr_b16 v[106:107],v6 offset:20144
ds_read_b64_tr_b16 v[108:109],v6 offset:18616
v_fma_mix_f32 v140,v140,v121,0 op_sel_hi:[0,1,0]
v_lshl_add_u64 v[128:129],v[128:129],0,v[130:131]
v_lshl_add_u64 v[130:131],v[132:133],0,v[134:135]
v_mfma_f32_16x16x32_bf16 v[156:159],v[84:87],v[60:63],v[156:159]
ds_read_u16_d16_hi v139,v139
v_fma_mix_f32 v141,v141,v123,0 op_sel_hi:[0,1,0]
v_fma_mix_f32 v143,v143,v123,0 op_sel:[0,1,0] op_sel_hi:[0,1,0]
v_mfma_f32_16x16x32_bf16 v[148:151],v[84:87],v[56:59],v[148:151]
ds_read_b64_tr_b16 v[110:111],v6 offset:20152
ds_read_b64_tr_b16 v[64:65],v4 offset:12832
v_lshlrev_b32_sdwa v144,1,v144 dst_sel:DWORD dst_unused:UNUSED_PAD src0_sel:DWORD src1_sel:WORD_1
v_lshlrev_b32_sdwa v146,1,v146 dst_sel:DWORD dst_unused:UNUSED_PAD src0_sel:DWORD src1_sel:WORD_1
v_mfma_f32_16x16x32_bf16 v[160:163],v[88:91],v[128:131],v[160:163]
ds_read_u16 v144,v144
v_lshlrev_b32_sdwa v145,1,v145 dst_sel:DWORD dst_unused:UNUSED_PAD src0_sel:DWORD src1_sel:WORD_1
v_lshlrev_b32_sdwa v147,1,v147 dst_sel:DWORD dst_unused:UNUSED_PAD src0_sel:DWORD src1_sel:WORD_1
v_mfma_f32_16x16x32_bf16 v[168:171],v[92:95],v[128:131],v[168:171]
s_waitcnt lgkmcnt(11)
ds_read_b64_tr_b16 v[66:67],v4 offset:14880
ds_read_b128 v[112:115],v7 offset:640
v_fma_mixlo_f16 v120,v156,s99,0
v_fma_mixlo_f16 v122,v157,s99,0
v_mfma_f32_16x16x32_bf16 v[176:179],v[96:99],v[128:131],v[176:179]
s_waitcnt lgkmcnt(10)
ds_read_u16_d16_hi v146,v146
v_lshlrev_b32_sdwa v148,1,v148 dst_sel:DWORD dst_unused:UNUSED_PAD src0_sel:DWORD src1_sel:WORD_1
v_lshlrev_b32_sdwa v150,1,v150 dst_sel:DWORD dst_unused:UNUSED_PAD src0_sel:DWORD src1_sel:WORD_1
v_mfma_f32_16x16x32_bf16 v[184:187],v[100:103],v[128:131],v[184:187]
s_waitcnt lgkmcnt(6)
ds_read_b64_tr_b16 v[68:69],v5 offset:12832
ds_read_b64_tr_b16 v[70:71],v5 offset:14880
v_lshl_add_u64 v[136:137],v[136:137],0,v[138:139]
v_fma_mixhi_f16 v120,v158,s99,0
v_mfma_f32_16x16x32_bf16 v[192:195],v[104:107],v[128:131],v[192:195]
s_waitcnt lgkmcnt(7)
ds_read_u16 v145,v145
v_cvt_pk_bf16_f32 v138,v140,v142
v_cvt_pk_bf16_f32 v139,v141,v143
v_mfma_f32_16x16x32_bf16 v[200:203],v[108:111],v[128:131],v[200:203]
ds_read_b128 v[116:119],v7 offset:672
ds_read_b64_tr_b16 v[72:73],v4 offset:16928
v_fma_mixhi_f16 v122,v159,s99,0
v_mfma_f32_16x16x32_bf16 v[164:167],v[88:91],v[136:139],v[164:167]
v_lshlrev_b32_sdwa v149,1,v149 dst_sel:DWORD dst_unused:UNUSED_PAD src0_sel:DWORD src1_sel:WORD_1
v_lshlrev_b32_sdwa v151,1,v151 dst_sel:DWORD dst_unused:UNUSED_PAD src0_sel:DWORD src1_sel:WORD_1
v_mfma_f32_16x16x32_bf16 v[172:175],v[92:95],v[136:139],v[172:175]
ds_read_b64_tr_b16 v[74:75],v4 offset:18976
ds_read_b64_tr_b16 v[76:77],v5 offset:16928
v_pk_fma_f16 v121,v120,v120,s54
v_pk_fma_f16 v123,v122,v122,s54
v_mfma_f32_16x16x32_bf16 v[180:183],v[96:99],v[136:139],v[180:183]
s_setprio 1
ds_read_u16_d16_hi v147,v147
v_pk_mul_f16 v121,v120,v121
v_pk_mul_f16 v123,v122,v123
v_mfma_f32_16x16x32_bf16 v[188:191],v[100:103],v[136:139],v[188:191]
ds_read_u16 v148,v148
v_lshlrev_b32_sdwa v152,1,v152 dst_sel:DWORD dst_unused:UNUSED_PAD src0_sel:DWORD src1_sel:WORD_1
v_lshlrev_b32_sdwa v154,1,v154 dst_sel:DWORD dst_unused:UNUSED_PAD src0_sel:DWORD src1_sel:WORD_1
v_mfma_f32_16x16x32_bf16 v[196:199],v[104:107],v[136:139],v[196:199]
v_exp_f16 v120,v121
v_mfma_f32_16x16x32_bf16 v[204:207],v[108:111],v[136:139],v[204:207]
s_waitcnt lgkmcnt(10)
ds_read_b64_tr_b16 v[78:79],v5 offset:18976
ds_read_b64_tr_b16 v[80:81],v4 offset:21024
v_exp_f16 v122,v123
v_mfma_f32_16x16x32_bf16 v[128:131],v[64:67],v[16:19],v[112:115]
v_lshlrev_b32_sdwa v153,1,v153 dst_sel:DWORD dst_unused:UNUSED_PAD src0_sel:DWORD src1_sel:WORD_1
v_lshlrev_b32_sdwa v155,1,v155 dst_sel:DWORD dst_unused:UNUSED_PAD src0_sel:DWORD src1_sel:WORD_1
v_mfma_f32_16x16x32_bf16 v[136:139],v[64:67],v[20:23],v[112:115]
s_waitcnt lgkmcnt(7)
ds_read_u16_d16_hi v150,v150
ds_read_u16 v149,v149
v_exp_f16_sdwa v120,v121 dst_sel:WORD_1 dst_unused:UNUSED_PRESERVE src0_sel:WORD_1
v_mfma_f32_16x16x32_bf16 v[140:143],v[68:71],v[20:23],v[116:119]
v_exp_f16_sdwa v122,v123 dst_sel:WORD_1 dst_unused:UNUSED_PRESERVE src0_sel:WORD_1
v_mfma_f32_16x16x32_bf16 v[132:135],v[68:71],v[16:19],v[116:119]
s_waitcnt lgkmcnt(7)
ds_read_b64_tr_b16 v[82:83],v4 offset:23072
v_pk_fma_f16 v120,v120,s55,s55
v_mfma_f32_16x16x32_bf16 v[128:131],v[72:75],v[24:27],v[128:131]
ds_read_b64_tr_b16 v[84:85],v5 offset:21024
v_pk_fma_f16 v122,v122,s55,s55
v_rcp_f16 v121,v120
v_mfma_f32_16x16x32_bf16 v[136:139],v[72:75],v[28:31],v[136:139]
s_waitcnt lgkmcnt(5)
ds_read_b64_tr_b16 v[86:87],v5 offset:23072
ds_read_u16_d16_hi v151,v151
v_rcp_f16 v123,v122
v_mfma_f32_16x16x32_bf16 v[140:143],v[76:79],v[28:31],v[140:143]
ds_read_u16 v152,v152
ds_read_u16_d16_hi v154,v154
v_rcp_f16_sdwa v121,v120 dst_sel:WORD_1 dst_unused:UNUSED_PRESERVE src0_sel:WORD_1
v_mfma_f32_16x16x32_bf16 v[132:135],v[76:79],v[24:27],v[132:135]
s_waitcnt lgkmcnt(5)
v_rcp_f16_sdwa v123,v122 dst_sel:WORD_1 dst_unused:UNUSED_PRESERVE src0_sel:WORD_1
v_mfma_f32_16x16x32_bf16 v[128:131],v[80:83],v[32:35],v[128:131]
ds_read_u16 v153,v153
v_fma_mix_f32 v156,v156,v121,0 op_sel_hi:[0,1,0]
v_fma_mix_f32 v158,v158,v121,0 op_sel:[0,1,0] op_sel_hi:[0,1,0]
v_mfma_f32_16x16x32_bf16 v[136:139],v[80:83],v[36:39],v[136:139]
s_waitcnt lgkmcnt(3)
s_setprio 0
v_lshl_add_u64 v[144:145],v[144:145],0,v[146:147]
v_lshl_add_u64 v[146:147],v[148:149],0,v[150:151]
v_mfma_f32_16x16x32_bf16 v[140:143],v[84:87],v[36:39],v[140:143]
ds_read_u16_d16_hi v155,v155
v_fma_mix_f32 v157,v157,v123,0 op_sel_hi:[0,1,0]
v_fma_mix_f32 v159,v159,v123,0 op_sel:[0,1,0] op_sel_hi:[0,1,0]
v_mfma_f32_16x16x32_bf16 v[132:135],v[84:87],v[32:35],v[132:135]
v_lshlrev_b32_sdwa v128,1,v128 dst_sel:DWORD dst_unused:UNUSED_PAD src0_sel:DWORD src1_sel:WORD_1
v_lshlrev_b32_sdwa v130,1,v130 dst_sel:DWORD dst_unused:UNUSED_PAD src0_sel:DWORD src1_sel:WORD_1
v_mfma_f32_16x16x32_bf16 v[208:211],v[88:91],v[144:147],v[208:211]
s_barrier
s_setprio 3
v_lshlrev_b32_sdwa v129,1,v129 dst_sel:DWORD dst_unused:UNUSED_PAD src0_sel:DWORD src1_sel:WORD_1
v_lshlrev_b32_sdwa v131,1,v131 dst_sel:DWORD dst_unused:UNUSED_PAD src0_sel:DWORD src1_sel:WORD_1
v_mfma_f32_16x16x32_bf16 v[216:219],v[92:95],v[144:147],v[216:219]
ds_read_u16 v128,v128
ds_read_u16_d16_hi v130,v130
v_fma_mixlo_f16 v120,v140,s99,0
v_fma_mixlo_f16 v122,v141,s99,0
v_mfma_f32_16x16x32_bf16 v[224:227],v[96:99],v[144:147],v[224:227]
ds_read_u16 v129,v129
v_lshlrev_b32_sdwa v132,1,v132 dst_sel:DWORD dst_unused:UNUSED_PAD src0_sel:DWORD src1_sel:WORD_1
v_lshlrev_b32_sdwa v134,1,v134 dst_sel:DWORD dst_unused:UNUSED_PAD src0_sel:DWORD src1_sel:WORD_1
v_mfma_f32_16x16x32_bf16 v[232:235],v[100:103],v[144:147],v[232:235]
s_waitcnt lgkmcnt(3)
v_lshl_add_u64 v[152:153],v[152:153],0,v[154:155]
v_cvt_pk_bf16_f32 v154,v156,v158
v_mfma_f32_16x16x32_bf16 v[240:243],v[104:107],v[144:147],v[240:243]
v_fma_mixhi_f16 v120,v142,s99,0
v_cvt_pk_bf16_f32 v155,v157,v159
v_mfma_f32_16x16x32_bf16 v[248:251],v[108:111],v[144:147],v[248:251]
ds_read_u16_d16_hi v131,v131
v_fma_mixhi_f16 v122,v143,s99,0
v_mfma_f32_16x16x32_bf16 v[212:215],v[88:91],v[152:155],v[212:215]
v_lshlrev_b32_sdwa v133,1,v133 dst_sel:DWORD dst_unused:UNUSED_PAD src0_sel:DWORD src1_sel:WORD_1
v_pk_fma_f16 v121,v120,v120,s54
v_lshlrev_b32_sdwa v135,1,v135 dst_sel:DWORD dst_unused:UNUSED_PAD src0_sel:DWORD src1_sel:WORD_1
v_mfma_f32_16x16x32_bf16 v[220:223],v[92:95],v[152:155],v[220:223]
ds_read_u16 v132,v132
ds_read_u16_d16_hi v134,v134
v_pk_fma_f16 v123,v122,v122,s54
v_mfma_f32_16x16x32_bf16 v[228:231],v[96:99],v[152:155],v[228:231]
ds_read_b64_tr_b16 v[88:89],v6 offset:0
v_pk_mul_f16 v121,v120,v121
v_lshlrev_b32_sdwa v136,1,v136 dst_sel:DWORD dst_unused:UNUSED_PAD src0_sel:DWORD src1_sel:WORD_1
v_pk_mul_f16 v123,v122,v123
v_mfma_f32_16x16x32_bf16 v[236:239],v[100:103],v[152:155],v[236:239]
ds_read_b64_tr_b16 v[90:91],v6 offset:1536
v_lshlrev_b32_sdwa v138,1,v138 dst_sel:DWORD dst_unused:UNUSED_PAD src0_sel:DWORD src1_sel:WORD_1
v_mfma_f32_16x16x32_bf16 v[244:247],v[104:107],v[152:155],v[244:247]
ds_read_u16 v133,v133
v_exp_f16 v120,v121
v_mfma_f32_16x16x32_bf16 v[252:255],v[108:111],v[152:155],v[252:255]
ds_read_b64_tr_b16 v[92:93],v6 offset:8
v_lshlrev_b32_sdwa v137,1,v137 dst_sel:DWORD dst_unused:UNUSED_PAD src0_sel:DWORD src1_sel:WORD_1
v_mfma_f32_16x16x32_bf16 v[144:147],v[64:67],v[40:43],v[112:115]
ds_read_b64_tr_b16 v[94:95],v6 offset:1544
v_lshlrev_b32_sdwa v139,1,v139 dst_sel:DWORD dst_unused:UNUSED_PAD src0_sel:DWORD src1_sel:WORD_1
v_exp_f16 v122,v123
v_mfma_f32_16x16x32_bf16 v[152:155],v[64:67],v[44:47],v[112:115]
ds_read_u16_d16_hi v135,v135
v_mfma_f32_16x16x32_bf16 v[156:159],v[68:71],v[44:47],v[116:119]
ds_read_u16 v136,v136
v_exp_f16_sdwa v122,v123 dst_sel:WORD_1 dst_unused:UNUSED_PRESERVE src0_sel:WORD_1
v_exp_f16_sdwa v120,v121 dst_sel:WORD_1 dst_unused:UNUSED_PRESERVE src0_sel:WORD_1
v_mfma_f32_16x16x32_bf16 v[148:151],v[68:71],v[40:43],v[116:119]
ds_read_b64_tr_b16 v[96:97],v6 offset:64
ds_read_b64_tr_b16 v[98:99],v6 offset:1600
v_pk_fma_f16 v120,v120,s55,s55
v_pk_fma_f16 v122,v122,s55,s55
v_mfma_f32_16x16x32_bf16 v[144:147],v[72:75],v[48:51],v[144:147]
v_rcp_f16 v121,v120
v_mfma_f32_16x16x32_bf16 v[152:155],v[72:75],v[52:55],v[152:155]
ds_read_b64_tr_b16 v[100:101],v6 offset:72
v_rcp_f16 v123,v122
v_mfma_f32_16x16x32_bf16 v[156:159],v[76:79],v[52:55],v[156:159]
ds_read_u16_d16_hi v138,v138
v_rcp_f16_sdwa v121,v120 dst_sel:WORD_1 dst_unused:UNUSED_PRESERVE src0_sel:WORD_1
v_mfma_f32_16x16x32_bf16 v[148:151],v[76:79],v[48:51],v[148:151]
ds_read_b64_tr_b16 v[102:103],v6 offset:1608
ds_read_b64_tr_b16 v[104:105],v6 offset:128
v_rcp_f16_sdwa v123,v122 dst_sel:WORD_1 dst_unused:UNUSED_PRESERVE src0_sel:WORD_1
v_mfma_f32_16x16x32_bf16 v[144:147],v[80:83],v[56:59],v[144:147]
ds_read_u16 v137,v137
v_fma_mix_f32 v142,v142,v121,0 op_sel:[0,1,0] op_sel_hi:[0,1,0]
v_mfma_f32_16x16x32_bf16 v[152:155],v[80:83],v[60:63],v[152:155]
s_waitcnt lgkmcnt(8)
ds_read_b64_tr_b16 v[106:107],v6 offset:1664
ds_read_b64_tr_b16 v[108:109],v6 offset:136
v_fma_mix_f32 v140,v140,v121,0 op_sel_hi:[0,1,0]
v_lshl_add_u64 v[128:129],v[128:129],0,v[130:131]
v_lshl_add_u64 v[130:131],v[132:133],0,v[134:135]
v_mfma_f32_16x16x32_bf16 v[156:159],v[84:87],v[60:63],v[156:159]
ds_read_u16_d16_hi v139,v139
v_fma_mix_f32 v141,v141,v123,0 op_sel_hi:[0,1,0]
v_fma_mix_f32 v143,v143,v123,0 op_sel:[0,1,0] op_sel_hi:[0,1,0]
v_mfma_f32_16x16x32_bf16 v[148:151],v[84:87],v[56:59],v[148:151]
ds_read_b64_tr_b16 v[110:111],v6 offset:1672
ds_read_b64_tr_b16 v[64:65],v4 offset:0
v_lshlrev_b32_sdwa v144,1,v144 dst_sel:DWORD dst_unused:UNUSED_PAD src0_sel:DWORD src1_sel:WORD_1
v_lshlrev_b32_sdwa v146,1,v146 dst_sel:DWORD dst_unused:UNUSED_PAD src0_sel:DWORD src1_sel:WORD_1
v_mfma_f32_16x16x32_bf16 v[160:163],v[88:91],v[128:131],v[160:163]
ds_read_u16 v144,v144
v_lshlrev_b32_sdwa v145,1,v145 dst_sel:DWORD dst_unused:UNUSED_PAD src0_sel:DWORD src1_sel:WORD_1
v_lshlrev_b32_sdwa v147,1,v147 dst_sel:DWORD dst_unused:UNUSED_PAD src0_sel:DWORD src1_sel:WORD_1
v_mfma_f32_16x16x32_bf16 v[168:171],v[92:95],v[128:131],v[168:171]
s_waitcnt lgkmcnt(11)
ds_read_b64_tr_b16 v[66:67],v4 offset:2048
ds_read_b128 v[112:115],v7 offset:768
v_fma_mixlo_f16 v120,v156,s99,0
v_fma_mixlo_f16 v122,v157,s99,0
v_mfma_f32_16x16x32_bf16 v[176:179],v[96:99],v[128:131],v[176:179]
s_waitcnt lgkmcnt(10)
ds_read_u16_d16_hi v146,v146
v_lshlrev_b32_sdwa v148,1,v148 dst_sel:DWORD dst_unused:UNUSED_PAD src0_sel:DWORD src1_sel:WORD_1
v_lshlrev_b32_sdwa v150,1,v150 dst_sel:DWORD dst_unused:UNUSED_PAD src0_sel:DWORD src1_sel:WORD_1
v_mfma_f32_16x16x32_bf16 v[184:187],v[100:103],v[128:131],v[184:187]
s_waitcnt lgkmcnt(6)
ds_read_b64_tr_b16 v[68:69],v5 offset:0
ds_read_b64_tr_b16 v[70:71],v5 offset:2048
v_lshl_add_u64 v[136:137],v[136:137],0,v[138:139]
v_fma_mixhi_f16 v120,v158,s99,0
v_mfma_f32_16x16x32_bf16 v[192:195],v[104:107],v[128:131],v[192:195]
s_waitcnt lgkmcnt(7)
ds_read_u16 v145,v145
v_cvt_pk_bf16_f32 v138,v140,v142
v_cvt_pk_bf16_f32 v139,v141,v143
v_mfma_f32_16x16x32_bf16 v[200:203],v[108:111],v[128:131],v[200:203]
ds_read_b128 v[116:119],v7 offset:800
ds_read_b64_tr_b16 v[72:73],v4 offset:4096
v_fma_mixhi_f16 v122,v159,s99,0
v_mfma_f32_16x16x32_bf16 v[164:167],v[88:91],v[136:139],v[164:167]
v_lshlrev_b32_sdwa v149,1,v149 dst_sel:DWORD dst_unused:UNUSED_PAD src0_sel:DWORD src1_sel:WORD_1
v_lshlrev_b32_sdwa v151,1,v151 dst_sel:DWORD dst_unused:UNUSED_PAD src0_sel:DWORD src1_sel:WORD_1
v_mfma_f32_16x16x32_bf16 v[172:175],v[92:95],v[136:139],v[172:175]
ds_read_b64_tr_b16 v[74:75],v4 offset:6144
ds_read_b64_tr_b16 v[76:77],v5 offset:4096
v_pk_fma_f16 v121,v120,v120,s54
v_pk_fma_f16 v123,v122,v122,s54
v_mfma_f32_16x16x32_bf16 v[180:183],v[96:99],v[136:139],v[180:183]
ds_read_u16_d16_hi v147,v147
v_pk_mul_f16 v121,v120,v121
v_pk_mul_f16 v123,v122,v123
v_mfma_f32_16x16x32_bf16 v[188:191],v[100:103],v[136:139],v[188:191]
ds_read_u16 v148,v148
v_lshlrev_b32_sdwa v152,1,v152 dst_sel:DWORD dst_unused:UNUSED_PAD src0_sel:DWORD src1_sel:WORD_1
v_lshlrev_b32_sdwa v154,1,v154 dst_sel:DWORD dst_unused:UNUSED_PAD src0_sel:DWORD src1_sel:WORD_1
v_mfma_f32_16x16x32_bf16 v[196:199],v[104:107],v[136:139],v[196:199]
v_exp_f16 v120,v121
v_mfma_f32_16x16x32_bf16 v[204:207],v[108:111],v[136:139],v[204:207]
s_waitcnt lgkmcnt(10)
ds_read_b64_tr_b16 v[78:79],v5 offset:6144
ds_read_b64_tr_b16 v[80:81],v4 offset:8192
v_exp_f16 v122,v123
v_mfma_f32_16x16x32_bf16 v[128:131],v[64:67],v[16:19],v[112:115]
v_lshlrev_b32_sdwa v153,1,v153 dst_sel:DWORD dst_unused:UNUSED_PAD src0_sel:DWORD src1_sel:WORD_1
v_lshlrev_b32_sdwa v155,1,v155 dst_sel:DWORD dst_unused:UNUSED_PAD src0_sel:DWORD src1_sel:WORD_1
v_mfma_f32_16x16x32_bf16 v[136:139],v[64:67],v[20:23],v[112:115]
s_waitcnt lgkmcnt(7)
ds_read_u16_d16_hi v150,v150
ds_read_u16 v149,v149
v_exp_f16_sdwa v120,v121 dst_sel:WORD_1 dst_unused:UNUSED_PRESERVE src0_sel:WORD_1
s_nop 0
s_mov_b32 s86,0xbe801d5a
v_mfma_f32_16x16x32_bf16 v[140:143],v[68:71],v[20:23],v[116:119]
v_exp_f16_sdwa v122,v123 dst_sel:WORD_1 dst_unused:UNUSED_PRESERVE src0_sel:WORD_1
v_mfma_f32_16x16x32_bf16 v[132:135],v[68:71],v[16:19],v[116:119]
s_waitcnt lgkmcnt(7)
ds_read_b64_tr_b16 v[82:83],v4 offset:10240
v_pk_fma_f16 v120,v120,s55,s55
v_mfma_f32_16x16x32_bf16 v[128:131],v[72:75],v[24:27],v[128:131]
ds_read_b64_tr_b16 v[84:85],v5 offset:8192
v_pk_fma_f16 v122,v122,s55,s55
v_rcp_f16 v121,v120
v_mfma_f32_16x16x32_bf16 v[136:139],v[72:75],v[28:31],v[136:139]
s_waitcnt lgkmcnt(5)
ds_read_b64_tr_b16 v[86:87],v5 offset:10240
ds_read_u16_d16_hi v151,v151
v_rcp_f16 v123,v122
v_mfma_f32_16x16x32_bf16 v[140:143],v[76:79],v[28:31],v[140:143]
ds_read_u16 v152,v152
ds_read_u16_d16_hi v154,v154
v_rcp_f16_sdwa v121,v120 dst_sel:WORD_1 dst_unused:UNUSED_PRESERVE src0_sel:WORD_1
v_mfma_f32_16x16x32_bf16 v[132:135],v[76:79],v[24:27],v[132:135]
s_waitcnt lgkmcnt(5)
v_rcp_f16_sdwa v123,v122 dst_sel:WORD_1 dst_unused:UNUSED_PRESERVE src0_sel:WORD_1
v_mfma_f32_16x16x32_bf16 v[128:131],v[80:83],v[32:35],v[128:131]
ds_read_u16 v153,v153
v_fma_mix_f32 v156,v156,v121,0 op_sel_hi:[0,1,0]
v_fma_mix_f32 v158,v158,v121,0 op_sel:[0,1,0] op_sel_hi:[0,1,0]
v_mfma_f32_16x16x32_bf16 v[136:139],v[80:83],v[36:39],v[136:139]
s_waitcnt lgkmcnt(3)
v_lshl_add_u64 v[144:145],v[144:145],0,v[146:147]
v_lshl_add_u64 v[146:147],v[148:149],0,v[150:151]
v_mfma_f32_16x16x32_bf16 v[140:143],v[84:87],v[36:39],v[140:143]
ds_read_u16_d16_hi v155,v155
v_fma_mix_f32 v157,v157,v123,0 op_sel_hi:[0,1,0]
v_fma_mix_f32 v159,v159,v123,0 op_sel:[0,1,0] op_sel_hi:[0,1,0]
v_mfma_f32_16x16x32_bf16 v[132:135],v[84:87],v[32:35],v[132:135]
v_lshlrev_b32_sdwa v128,1,v128 dst_sel:DWORD dst_unused:UNUSED_PAD src0_sel:DWORD src1_sel:WORD_1
v_lshlrev_b32_sdwa v130,1,v130 dst_sel:DWORD dst_unused:UNUSED_PAD src0_sel:DWORD src1_sel:WORD_1
v_mfma_f32_16x16x32_bf16 v[208:211],v[88:91],v[144:147],v[208:211]
v_lshlrev_b32_sdwa v129,1,v129 dst_sel:DWORD dst_unused:UNUSED_PAD src0_sel:DWORD src1_sel:WORD_1
v_lshlrev_b32_sdwa v131,1,v131 dst_sel:DWORD dst_unused:UNUSED_PAD src0_sel:DWORD src1_sel:WORD_1
v_mfma_f32_16x16x32_bf16 v[216:219],v[92:95],v[144:147],v[216:219]
ds_read_u16 v128,v128
ds_read_u16_d16_hi v130,v130
v_fma_mixlo_f16 v120,v140,s99,0
v_fma_mixlo_f16 v122,v141,s99,0
v_mfma_f32_16x16x32_bf16 v[224:227],v[96:99],v[144:147],v[224:227]
ds_read_u16 v129,v129
v_lshlrev_b32_sdwa v132,1,v132 dst_sel:DWORD dst_unused:UNUSED_PAD src0_sel:DWORD src1_sel:WORD_1
v_lshlrev_b32_sdwa v134,1,v134 dst_sel:DWORD dst_unused:UNUSED_PAD src0_sel:DWORD src1_sel:WORD_1
v_mfma_f32_16x16x32_bf16 v[232:235],v[100:103],v[144:147],v[232:235]
s_waitcnt lgkmcnt(3)
v_lshl_add_u64 v[152:153],v[152:153],0,v[154:155]
v_cvt_pk_bf16_f32 v154,v156,v158
v_mfma_f32_16x16x32_bf16 v[240:243],v[104:107],v[144:147],v[240:243]
v_fma_mixhi_f16 v120,v142,s99,0
v_cvt_pk_bf16_f32 v155,v157,v159
v_mfma_f32_16x16x32_bf16 v[248:251],v[108:111],v[144:147],v[248:251]
s_setprio 2
ds_read_u16_d16_hi v131,v131
v_fma_mixhi_f16 v122,v143,s99,0
v_mfma_f32_16x16x32_bf16 v[212:215],v[88:91],v[152:155],v[212:215]
v_lshlrev_b32_sdwa v133,1,v133 dst_sel:DWORD dst_unused:UNUSED_PAD src0_sel:DWORD src1_sel:WORD_1
v_pk_fma_f16 v121,v120,v120,s54
v_lshlrev_b32_sdwa v135,1,v135 dst_sel:DWORD dst_unused:UNUSED_PAD src0_sel:DWORD src1_sel:WORD_1
v_mfma_f32_16x16x32_bf16 v[220:223],v[92:95],v[152:155],v[220:223]
ds_read_u16 v132,v132
ds_read_u16_d16_hi v134,v134
v_pk_fma_f16 v123,v122,v122,s54
v_mfma_f32_16x16x32_bf16 v[228:231],v[96:99],v[152:155],v[228:231]
ds_read_b64_tr_b16 v[88:89],v6 offset:6160
v_pk_mul_f16 v121,v120,v121
v_lshlrev_b32_sdwa v136,1,v136 dst_sel:DWORD dst_unused:UNUSED_PAD src0_sel:DWORD src1_sel:WORD_1
v_pk_mul_f16 v123,v122,v123
v_mfma_f32_16x16x32_bf16 v[236:239],v[100:103],v[152:155],v[236:239]
ds_read_b64_tr_b16 v[90:91],v6 offset:7696
v_lshlrev_b32_sdwa v138,1,v138 dst_sel:DWORD dst_unused:UNUSED_PAD src0_sel:DWORD src1_sel:WORD_1
v_mfma_f32_16x16x32_bf16 v[244:247],v[104:107],v[152:155],v[244:247]
ds_read_u16 v133,v133
v_exp_f16 v120,v121
v_mfma_f32_16x16x32_bf16 v[252:255],v[108:111],v[152:155],v[252:255]
ds_read_b64_tr_b16 v[92:93],v6 offset:6168
v_lshlrev_b32_sdwa v137,1,v137 dst_sel:DWORD dst_unused:UNUSED_PAD src0_sel:DWORD src1_sel:WORD_1
v_mfma_f32_16x16x32_bf16 v[144:147],v[64:67],v[40:43],v[112:115]
ds_read_b64_tr_b16 v[94:95],v6 offset:7704
v_lshlrev_b32_sdwa v139,1,v139 dst_sel:DWORD dst_unused:UNUSED_PAD src0_sel:DWORD src1_sel:WORD_1
v_exp_f16 v122,v123
v_mfma_f32_16x16x32_bf16 v[152:155],v[64:67],v[44:47],v[112:115]
ds_read_u16_d16_hi v135,v135
v_mfma_f32_16x16x32_bf16 v[156:159],v[68:71],v[44:47],v[116:119]
ds_read_u16 v136,v136
v_exp_f16_sdwa v122,v123 dst_sel:WORD_1 dst_unused:UNUSED_PRESERVE src0_sel:WORD_1
v_exp_f16_sdwa v120,v121 dst_sel:WORD_1 dst_unused:UNUSED_PRESERVE src0_sel:WORD_1
v_mfma_f32_16x16x32_bf16 v[148:151],v[68:71],v[40:43],v[116:119]
ds_read_b64_tr_b16 v[96:97],v6 offset:6224
ds_read_b64_tr_b16 v[98:99],v6 offset:7760
v_pk_fma_f16 v120,v120,s55,s55
v_pk_fma_f16 v122,v122,s55,s55
v_mfma_f32_16x16x32_bf16 v[144:147],v[72:75],v[48:51],v[144:147]
v_rcp_f16 v121,v120
v_mfma_f32_16x16x32_bf16 v[152:155],v[72:75],v[52:55],v[152:155]
ds_read_b64_tr_b16 v[100:101],v6 offset:6232
v_rcp_f16 v123,v122
v_mfma_f32_16x16x32_bf16 v[156:159],v[76:79],v[52:55],v[156:159]
ds_read_u16_d16_hi v138,v138
v_rcp_f16_sdwa v121,v120 dst_sel:WORD_1 dst_unused:UNUSED_PRESERVE src0_sel:WORD_1
v_mfma_f32_16x16x32_bf16 v[148:151],v[76:79],v[48:51],v[148:151]
ds_read_b64_tr_b16 v[102:103],v6 offset:7768
ds_read_b64_tr_b16 v[104:105],v6 offset:6288
v_rcp_f16_sdwa v123,v122 dst_sel:WORD_1 dst_unused:UNUSED_PRESERVE src0_sel:WORD_1
v_mfma_f32_16x16x32_bf16 v[144:147],v[80:83],v[56:59],v[144:147]
ds_read_u16 v137,v137
v_fma_mix_f32 v142,v142,v121,0 op_sel:[0,1,0] op_sel_hi:[0,1,0]
v_mfma_f32_16x16x32_bf16 v[152:155],v[80:83],v[60:63],v[152:155]
s_waitcnt lgkmcnt(8)
ds_read_b64_tr_b16 v[106:107],v6 offset:7824
ds_read_b64_tr_b16 v[108:109],v6 offset:6296
v_fma_mix_f32 v140,v140,v121,0 op_sel_hi:[0,1,0]
v_lshl_add_u64 v[128:129],v[128:129],0,v[130:131]
v_lshl_add_u64 v[130:131],v[132:133],0,v[134:135]
v_mfma_f32_16x16x32_bf16 v[156:159],v[84:87],v[60:63],v[156:159]
ds_read_u16_d16_hi v139,v139
v_fma_mix_f32 v141,v141,v123,0 op_sel_hi:[0,1,0]
v_fma_mix_f32 v143,v143,v123,0 op_sel:[0,1,0] op_sel_hi:[0,1,0]
v_mfma_f32_16x16x32_bf16 v[148:151],v[84:87],v[56:59],v[148:151]
ds_read_b64_tr_b16 v[110:111],v6 offset:7832
ds_read_b64_tr_b16 v[64:65],v4 offset:512
v_lshlrev_b32_sdwa v144,1,v144 dst_sel:DWORD dst_unused:UNUSED_PAD src0_sel:DWORD src1_sel:WORD_1
v_lshlrev_b32_sdwa v146,1,v146 dst_sel:DWORD dst_unused:UNUSED_PAD src0_sel:DWORD src1_sel:WORD_1
v_mfma_f32_16x16x32_bf16 v[160:163],v[88:91],v[128:131],v[160:163]
ds_read_u16 v144,v144
v_lshlrev_b32_sdwa v145,1,v145 dst_sel:DWORD dst_unused:UNUSED_PAD src0_sel:DWORD src1_sel:WORD_1
v_lshlrev_b32_sdwa v147,1,v147 dst_sel:DWORD dst_unused:UNUSED_PAD src0_sel:DWORD src1_sel:WORD_1
v_mfma_f32_16x16x32_bf16 v[168:171],v[92:95],v[128:131],v[168:171]
s_waitcnt lgkmcnt(11)
ds_read_b64_tr_b16 v[66:67],v4 offset:2560
ds_read_b128 v[112:115],v7 offset:896
v_fma_mixlo_f16 v120,v156,s99,0
v_fma_mixlo_f16 v122,v157,s99,0
v_mfma_f32_16x16x32_bf16 v[176:179],v[96:99],v[128:131],v[176:179]
s_waitcnt lgkmcnt(10)
ds_read_u16_d16_hi v146,v146
v_lshlrev_b32_sdwa v148,1,v148 dst_sel:DWORD dst_unused:UNUSED_PAD src0_sel:DWORD src1_sel:WORD_1
v_lshlrev_b32_sdwa v150,1,v150 dst_sel:DWORD dst_unused:UNUSED_PAD src0_sel:DWORD src1_sel:WORD_1
v_mfma_f32_16x16x32_bf16 v[184:187],v[100:103],v[128:131],v[184:187]
s_waitcnt lgkmcnt(6)
ds_read_b64_tr_b16 v[68:69],v5 offset:512
ds_read_b64_tr_b16 v[70:71],v5 offset:2560
v_lshl_add_u64 v[136:137],v[136:137],0,v[138:139]
v_fma_mixhi_f16 v120,v158,s99,0
v_mfma_f32_16x16x32_bf16 v[192:195],v[104:107],v[128:131],v[192:195]
s_waitcnt lgkmcnt(7)
ds_read_u16 v145,v145
v_cvt_pk_bf16_f32 v138,v140,v142
v_cvt_pk_bf16_f32 v139,v141,v143
v_mfma_f32_16x16x32_bf16 v[200:203],v[108:111],v[128:131],v[200:203]
ds_read_b128 v[116:119],v7 offset:928
ds_read_b64_tr_b16 v[72:73],v4 offset:4608
v_fma_mixhi_f16 v122,v159,s99,0
v_mfma_f32_16x16x32_bf16 v[164:167],v[88:91],v[136:139],v[164:167]
v_lshlrev_b32_sdwa v149,1,v149 dst_sel:DWORD dst_unused:UNUSED_PAD src0_sel:DWORD src1_sel:WORD_1
v_lshlrev_b32_sdwa v151,1,v151 dst_sel:DWORD dst_unused:UNUSED_PAD src0_sel:DWORD src1_sel:WORD_1
v_mfma_f32_16x16x32_bf16 v[172:175],v[92:95],v[136:139],v[172:175]
s_setprio 1
ds_read_b64_tr_b16 v[74:75],v4 offset:6656
ds_read_b64_tr_b16 v[76:77],v5 offset:4608
v_pk_fma_f16 v121,v120,v120,s54
v_pk_fma_f16 v123,v122,v122,s54
v_mfma_f32_16x16x32_bf16 v[180:183],v[96:99],v[136:139],v[180:183]
ds_read_u16_d16_hi v147,v147
v_pk_mul_f16 v121,v120,v121
v_pk_mul_f16 v123,v122,v123
v_mfma_f32_16x16x32_bf16 v[188:191],v[100:103],v[136:139],v[188:191]
ds_read_u16 v148,v148
v_lshlrev_b32_sdwa v152,1,v152 dst_sel:DWORD dst_unused:UNUSED_PAD src0_sel:DWORD src1_sel:WORD_1
v_lshlrev_b32_sdwa v154,1,v154 dst_sel:DWORD dst_unused:UNUSED_PAD src0_sel:DWORD src1_sel:WORD_1
v_mfma_f32_16x16x32_bf16 v[196:199],v[104:107],v[136:139],v[196:199]
v_exp_f16 v120,v121
v_mfma_f32_16x16x32_bf16 v[204:207],v[108:111],v[136:139],v[204:207]
s_waitcnt lgkmcnt(10)
ds_read_b64_tr_b16 v[78:79],v5 offset:6656
ds_read_b64_tr_b16 v[80:81],v4 offset:8704
v_exp_f16 v122,v123
v_mfma_f32_16x16x32_bf16 v[128:131],v[64:67],v[16:19],v[112:115]
v_lshlrev_b32_sdwa v153,1,v153 dst_sel:DWORD dst_unused:UNUSED_PAD src0_sel:DWORD src1_sel:WORD_1
v_lshlrev_b32_sdwa v155,1,v155 dst_sel:DWORD dst_unused:UNUSED_PAD src0_sel:DWORD src1_sel:WORD_1
v_mfma_f32_16x16x32_bf16 v[136:139],v[64:67],v[20:23],v[112:115]
s_waitcnt lgkmcnt(7)
ds_read_u16_d16_hi v150,v150
ds_read_u16 v149,v149
v_exp_f16_sdwa v120,v121 dst_sel:WORD_1 dst_unused:UNUSED_PRESERVE src0_sel:WORD_1
v_mfma_f32_16x16x32_bf16 v[140:143],v[68:71],v[20:23],v[116:119]
v_exp_f16_sdwa v122,v123 dst_sel:WORD_1 dst_unused:UNUSED_PRESERVE src0_sel:WORD_1
v_mfma_f32_16x16x32_bf16 v[132:135],v[68:71],v[16:19],v[116:119]
s_waitcnt lgkmcnt(7)
ds_read_b64_tr_b16 v[82:83],v4 offset:10752
v_pk_fma_f16 v120,v120,s55,s55
v_mfma_f32_16x16x32_bf16 v[128:131],v[72:75],v[24:27],v[128:131]
ds_read_b64_tr_b16 v[84:85],v5 offset:8704
v_pk_fma_f16 v122,v122,s55,s55
v_rcp_f16 v121,v120
v_mfma_f32_16x16x32_bf16 v[136:139],v[72:75],v[28:31],v[136:139]
s_waitcnt lgkmcnt(5)
ds_read_b64_tr_b16 v[86:87],v5 offset:10752
ds_read_u16_d16_hi v151,v151
v_rcp_f16 v123,v122
v_mfma_f32_16x16x32_bf16 v[140:143],v[76:79],v[28:31],v[140:143]
ds_read_u16 v152,v152
ds_read_u16_d16_hi v154,v154
v_rcp_f16_sdwa v121,v120 dst_sel:WORD_1 dst_unused:UNUSED_PRESERVE src0_sel:WORD_1
v_mfma_f32_16x16x32_bf16 v[132:135],v[76:79],v[24:27],v[132:135]
s_waitcnt lgkmcnt(5)
v_rcp_f16_sdwa v123,v122 dst_sel:WORD_1 dst_unused:UNUSED_PRESERVE src0_sel:WORD_1
v_mfma_f32_16x16x32_bf16 v[128:131],v[80:83],v[32:35],v[128:131]
ds_read_u16 v153,v153
v_fma_mix_f32 v156,v156,v121,0 op_sel_hi:[0,1,0]
v_fma_mix_f32 v158,v158,v121,0 op_sel:[0,1,0] op_sel_hi:[0,1,0]
v_mfma_f32_16x16x32_bf16 v[136:139],v[80:83],v[36:39],v[136:139]
s_waitcnt lgkmcnt(3)
s_setprio 0
v_lshl_add_u64 v[144:145],v[144:145],0,v[146:147]
v_lshl_add_u64 v[146:147],v[148:149],0,v[150:151]
v_mfma_f32_16x16x32_bf16 v[140:143],v[84:87],v[36:39],v[140:143]
ds_read_u16_d16_hi v155,v155
v_fma_mix_f32 v157,v157,v123,0 op_sel_hi:[0,1,0]
v_fma_mix_f32 v159,v159,v123,0 op_sel:[0,1,0] op_sel_hi:[0,1,0]
v_mfma_f32_16x16x32_bf16 v[132:135],v[84:87],v[32:35],v[132:135]
v_lshlrev_b32_sdwa v128,1,v128 dst_sel:DWORD dst_unused:UNUSED_PAD src0_sel:DWORD src1_sel:WORD_1
v_lshlrev_b32_sdwa v130,1,v130 dst_sel:DWORD dst_unused:UNUSED_PAD src0_sel:DWORD src1_sel:WORD_1
v_mfma_f32_16x16x32_bf16 v[208:211],v[88:91],v[144:147],v[208:211]
s_barrier
s_setprio 2
ds_read_u16 v128,v128
v_mfma_f32_16x16x32_bf16 v[216:219],v[92:95],v[144:147],v[216:219]
ds_read_u16_d16_hi v130,v130
v_lshlrev_b32_sdwa v129,1,v129 dst_sel:DWORD dst_unused:UNUSED_PAD src0_sel:DWORD src1_sel:WORD_1
v_lshlrev_b32_sdwa v131,1,v131 dst_sel:DWORD dst_unused:UNUSED_PAD src0_sel:DWORD src1_sel:WORD_1
v_mfma_f32_16x16x32_bf16 v[224:227],v[96:99],v[144:147],v[224:227]
v_lshlrev_b32_sdwa v132,1,v132 dst_sel:DWORD dst_unused:UNUSED_PAD src0_sel:DWORD src1_sel:WORD_1
v_mfma_f32_16x16x32_bf16 v[232:235],v[100:103],v[144:147],v[232:235]
s_waitcnt lgkmcnt(2)
ds_read_u16 v129,v129
v_lshl_add_u64 v[152:153],v[152:153],0,v[154:155]
v_lshlrev_b32_sdwa v134,1,v134 dst_sel:DWORD dst_unused:UNUSED_PAD src0_sel:DWORD src1_sel:WORD_1
v_mfma_f32_16x16x32_bf16 v[240:243],v[104:107],v[144:147],v[240:243]
v_cvt_pk_bf16_f32 v155,v157,v159
v_cvt_pk_bf16_f32 v154,v156,v158
v_mfma_f32_16x16x32_bf16 v[248:251],v[108:111],v[144:147],v[248:251]
ds_read_u16_d16_hi v131,v131
v_lshlrev_b32_sdwa v133,1,v133 dst_sel:DWORD dst_unused:UNUSED_PAD src0_sel:DWORD src1_sel:WORD_1
v_mfma_f32_16x16x32_bf16 v[212:215],v[88:91],v[152:155],v[212:215]
ds_read_u16 v132,v132
v_lshlrev_b32_sdwa v135,1,v135 dst_sel:DWORD dst_unused:UNUSED_PAD src0_sel:DWORD src1_sel:WORD_1
v_mfma_f32_16x16x32_bf16 v[220:223],v[92:95],v[152:155],v[220:223]
ds_read_u16_d16_hi v134,v134
v_lshlrev_b32_sdwa v136,1,v136 dst_sel:DWORD dst_unused:UNUSED_PAD src0_sel:DWORD src1_sel:WORD_1
v_mfma_f32_16x16x32_bf16 v[228:231],v[96:99],v[152:155],v[228:231]
ds_read_b64_tr_b16 v[88:89],v6 offset:12320
ds_read_b64_tr_b16 v[90:91],v6 offset:13856
v_lshlrev_b32_sdwa v138,1,v138 dst_sel:DWORD dst_unused:UNUSED_PAD src0_sel:DWORD src1_sel:WORD_1
v_mfma_f32_16x16x32_bf16 v[236:239],v[100:103],v[152:155],v[236:239]
ds_read_u16 v133,v133
v_lshlrev_b32_sdwa v137,1,v137 dst_sel:DWORD dst_unused:UNUSED_PAD src0_sel:DWORD src1_sel:WORD_1
v_mfma_f32_16x16x32_bf16 v[244:247],v[104:107],v[152:155],v[244:247]
ds_read_u16_d16_hi v135,v135
v_lshlrev_b32_sdwa v139,1,v139 dst_sel:DWORD dst_unused:UNUSED_PAD src0_sel:DWORD src1_sel:WORD_1
v_mfma_f32_16x16x32_bf16 v[252:255],v[108:111],v[152:155],v[252:255]
ds_read_b64_tr_b16 v[92:93],v6 offset:12328
ds_read_b64_tr_b16 v[94:95],v6 offset:13864
v_lshlrev_b32_sdwa v140,1,v140 dst_sel:DWORD dst_unused:UNUSED_PAD src0_sel:DWORD src1_sel:WORD_1
v_mfma_f32_16x16x32_bf16 v[144:147],v[64:67],v[40:43],v[112:115]
ds_read_u16 v136,v136
v_lshlrev_b32_sdwa v142,1,v142 dst_sel:DWORD dst_unused:UNUSED_PAD src0_sel:DWORD src1_sel:WORD_1
v_mfma_f32_16x16x32_bf16 v[152:155],v[64:67],v[44:47],v[112:115]
ds_read_u16_d16_hi v138,v138
ds_read_b64_tr_b16 v[96:97],v6 offset:12384
v_lshlrev_b32_sdwa v141,1,v141 dst_sel:DWORD dst_unused:UNUSED_PAD src0_sel:DWORD src1_sel:WORD_1
v_mfma_f32_16x16x32_bf16 v[156:159],v[68:71],v[44:47],v[116:119]
ds_read_b64_tr_b16 v[98:99],v6 offset:13920
ds_read_b64_tr_b16 v[100:101],v6 offset:12392
v_mfma_f32_16x16x32_bf16 v[148:151],v[68:71],v[40:43],v[116:119]
ds_read_u16 v137,v137
v_lshlrev_b32_sdwa v143,1,v143 dst_sel:DWORD dst_unused:UNUSED_PAD src0_sel:DWORD src1_sel:WORD_1
v_mfma_f32_16x16x32_bf16 v[144:147],v[72:75],v[48:51],v[144:147]
ds_read_u16_d16_hi v139,v139
v_mfma_f32_16x16x32_bf16 v[152:155],v[72:75],v[52:55],v[152:155]
ds_read_b64_tr_b16 v[102:103],v6 offset:13928
ds_read_b64_tr_b16 v[104:105],v6 offset:12448
v_mfma_f32_16x16x32_bf16 v[156:159],v[76:79],v[52:55],v[156:159]
ds_read_u16 v140,v140
v_mfma_f32_16x16x32_bf16 v[148:151],v[76:79],v[48:51],v[148:151]
s_waitcnt lgkmcnt(15)
ds_read_u16_d16_hi v142,v142
v_lshl_add_u64 v[128:129],v[128:129],0,v[130:131]
v_mfma_f32_16x16x32_bf16 v[144:147],v[80:83],v[56:59],v[144:147]
ds_read_b64_tr_b16 v[106:107],v6 offset:13984
ds_read_b64_tr_b16 v[108:109],v6 offset:12456
v_mfma_f32_16x16x32_bf16 v[152:155],v[80:83],v[60:63],v[152:155]
s_waitcnt lgkmcnt(15)
ds_read_u16 v141,v141
v_lshl_add_u64 v[130:131],v[132:133],0,v[134:135]
v_mfma_f32_16x16x32_bf16 v[156:159],v[84:87],v[60:63],v[156:159]
ds_read_u16_d16_hi v143,v143
v_mfma_f32_16x16x32_bf16 v[148:151],v[84:87],v[56:59],v[148:151]
ds_read_b64_tr_b16 v[110:111],v6 offset:13992
ds_read_b64_tr_b16 v[64:65],v4 offset:12320
v_lshlrev_b32_sdwa v144,1,v144 dst_sel:DWORD dst_unused:UNUSED_PAD src0_sel:DWORD src1_sel:WORD_1
v_lshlrev_b32_sdwa v146,1,v146 dst_sel:DWORD dst_unused:UNUSED_PAD src0_sel:DWORD src1_sel:WORD_1
v_mfma_f32_16x16x32_bf16 v[160:163],v[88:91],v[128:131],v[160:163]
s_waitcnt lgkmcnt(15)
v_lshlrev_b32_sdwa v145,1,v145 dst_sel:DWORD dst_unused:UNUSED_PAD src0_sel:DWORD src1_sel:WORD_1
v_mfma_f32_16x16x32_bf16 v[168:171],v[92:95],v[128:131],v[168:171]
s_waitcnt lgkmcnt(13)
ds_read_u16 v144,v144
v_lshlrev_b32_sdwa v147,1,v147 dst_sel:DWORD dst_unused:UNUSED_PAD src0_sel:DWORD src1_sel:WORD_1
v_mfma_f32_16x16x32_bf16 v[176:179],v[96:99],v[128:131],v[176:179]
s_waitcnt lgkmcnt(10)
ds_read_b64_tr_b16 v[66:67],v4 offset:14368
ds_read_b128 v[112:115],v7 offset:1024
v_mfma_f32_16x16x32_bf16 v[184:187],v[100:103],v[128:131],v[184:187]
s_waitcnt lgkmcnt(8)
ds_read_b64_tr_b16 v[68:69],v5 offset:12320
ds_read_u16_d16_hi v146,v146
v_lshlrev_b32_sdwa v148,1,v148 dst_sel:DWORD dst_unused:UNUSED_PAD src0_sel:DWORD src1_sel:WORD_1
v_lshl_add_u64 v[136:137],v[136:137],0,v[138:139]
v_lshlrev_b32_sdwa v150,1,v150 dst_sel:DWORD dst_unused:UNUSED_PAD src0_sel:DWORD src1_sel:WORD_1
v_mfma_f32_16x16x32_bf16 v[192:195],v[104:107],v[128:131],v[192:195]
s_waitcnt lgkmcnt(6)
ds_read_u16 v145,v145
v_lshl_add_u64 v[138:139],v[140:141],0,v[142:143]
v_mfma_f32_16x16x32_bf16 v[200:203],v[108:111],v[128:131],v[200:203]
ds_read_b64_tr_b16 v[70:71],v5 offset:14368
ds_read_b128 v[116:119],v7 offset:1056
v_mfma_f32_16x16x32_bf16 v[164:167],v[88:91],v[136:139],v[164:167]
ds_read_u16_d16_hi v147,v147
v_lshlrev_b32_sdwa v149,1,v149 dst_sel:DWORD dst_unused:UNUSED_PAD src0_sel:DWORD src1_sel:WORD_1
v_lshlrev_b32_sdwa v151,1,v151 dst_sel:DWORD dst_unused:UNUSED_PAD src0_sel:DWORD src1_sel:WORD_1
v_mfma_f32_16x16x32_bf16 v[172:175],v[92:95],v[136:139],v[172:175]
ds_read_u16 v148,v148
ds_read_b64_tr_b16 v[72:73],v4 offset:16416
v_lshlrev_b32_sdwa v152,1,v152 dst_sel:DWORD dst_unused:UNUSED_PAD src0_sel:DWORD src1_sel:WORD_1
v_mfma_f32_16x16x32_bf16 v[180:183],v[96:99],v[136:139],v[180:183]
ds_read_b64_tr_b16 v[74:75],v4 offset:18464
ds_read_b64_tr_b16 v[76:77],v5 offset:16416
v_lshlrev_b32_sdwa v154,1,v154 dst_sel:DWORD dst_unused:UNUSED_PAD src0_sel:DWORD src1_sel:WORD_1
v_mfma_f32_16x16x32_bf16 v[188:191],v[100:103],v[136:139],v[188:191]
ds_read_u16_d16_hi v150,v150
v_lshlrev_b32_sdwa v153,1,v153 dst_sel:DWORD dst_unused:UNUSED_PAD src0_sel:DWORD src1_sel:WORD_1
v_mfma_f32_16x16x32_bf16 v[196:199],v[104:107],v[136:139],v[196:199]
ds_read_u16 v149,v149
v_lshlrev_b32_sdwa v155,1,v155 dst_sel:DWORD dst_unused:UNUSED_PAD src0_sel:DWORD src1_sel:WORD_1
v_mfma_f32_16x16x32_bf16 v[204:207],v[108:111],v[136:139],v[204:207]
s_waitcnt lgkmcnt(12)
ds_read_b64_tr_b16 v[78:79],v5 offset:18464
ds_read_b64_tr_b16 v[80:81],v4 offset:20512
v_lshlrev_b32_sdwa v156,1,v156 dst_sel:DWORD dst_unused:UNUSED_PAD src0_sel:DWORD src1_sel:WORD_1
v_mfma_f32_16x16x32_bf16 v[128:131],v[64:67],v[16:19],v[112:115]
ds_read_u16_d16_hi v151,v151
v_lshlrev_b32_sdwa v158,1,v158 dst_sel:DWORD dst_unused:UNUSED_PAD src0_sel:DWORD src1_sel:WORD_1
v_mfma_f32_16x16x32_bf16 v[136:139],v[64:67],v[20:23],v[112:115]
s_waitcnt lgkmcnt(10)
ds_read_u16 v152,v152
ds_read_b64_tr_b16 v[82:83],v4 offset:22560
v_lshlrev_b32_sdwa v157,1,v157 dst_sel:DWORD dst_unused:UNUSED_PAD src0_sel:DWORD src1_sel:WORD_1
v_mfma_f32_16x16x32_bf16 v[140:143],v[68:71],v[20:23],v[116:119]
ds_read_b64_tr_b16 v[84:85],v5 offset:20512
ds_read_b64_tr_b16 v[86:87],v5 offset:22560
v_lshlrev_b32_sdwa v159,1,v159 dst_sel:DWORD dst_unused:UNUSED_PAD src0_sel:DWORD src1_sel:WORD_1
v_mfma_f32_16x16x32_bf16 v[132:135],v[68:71],v[16:19],v[116:119]
s_waitcnt lgkmcnt(10)
ds_read_u16_d16_hi v154,v154
v_mfma_f32_16x16x32_bf16 v[128:131],v[72:75],v[24:27],v[128:131]
ds_read_u16 v153,v153
v_mfma_f32_16x16x32_bf16 v[136:139],v[72:75],v[28:31],v[136:139]
s_waitcnt lgkmcnt(8)
v_mfma_f32_16x16x32_bf16 v[140:143],v[76:79],v[28:31],v[140:143]
ds_read_u16_d16_hi v155,v155
v_mfma_f32_16x16x32_bf16 v[132:135],v[76:79],v[24:27],v[132:135]
s_waitcnt lgkmcnt(5)
ds_read_u16 v156,v156
v_lshl_add_u64 v[144:145],v[144:145],0,v[146:147]
v_mfma_f32_16x16x32_bf16 v[128:131],v[80:83],v[32:35],v[128:131]
v_lshl_add_u64 v[146:147],v[148:149],0,v[150:151]
v_mfma_f32_16x16x32_bf16 v[136:139],v[80:83],v[36:39],v[136:139]
s_waitcnt lgkmcnt(4)
ds_read_u16_d16_hi v158,v158
v_mfma_f32_16x16x32_bf16 v[140:143],v[84:87],v[36:39],v[140:143]
ds_read_u16 v157,v157
v_mfma_f32_16x16x32_bf16 v[132:135],v[84:87],v[32:35],v[132:135]
s_nop 0
v_lshlrev_b32_sdwa v128,1,v128 dst_sel:DWORD dst_unused:UNUSED_PAD src0_sel:DWORD src1_sel:WORD_1
v_lshlrev_b32_sdwa v130,1,v130 dst_sel:DWORD dst_unused:UNUSED_PAD src0_sel:DWORD src1_sel:WORD_1
v_mfma_f32_16x16x32_bf16 v[208:211],v[88:91],v[144:147],v[208:211]
ds_read_u16_d16_hi v159,v159
v_mfma_f32_16x16x32_bf16 v[216:219],v[92:95],v[144:147],v[216:219]
ds_read_u16 v128,v128
v_lshlrev_b32_sdwa v129,1,v129 dst_sel:DWORD dst_unused:UNUSED_PAD src0_sel:DWORD src1_sel:WORD_1
v_lshlrev_b32_sdwa v131,1,v131 dst_sel:DWORD dst_unused:UNUSED_PAD src0_sel:DWORD src1_sel:WORD_1
v_mfma_f32_16x16x32_bf16 v[224:227],v[96:99],v[144:147],v[224:227]
v_lshlrev_b32_sdwa v132,1,v132 dst_sel:DWORD dst_unused:UNUSED_PAD src0_sel:DWORD src1_sel:WORD_1
v_mfma_f32_16x16x32_bf16 v[232:235],v[100:103],v[144:147],v[232:235]
s_waitcnt lgkmcnt(5)
ds_read_u16_d16_hi v130,v130
v_lshl_add_u64 v[152:153],v[152:153],0,v[154:155]
v_lshlrev_b32_sdwa v134,1,v134 dst_sel:DWORD dst_unused:UNUSED_PAD src0_sel:DWORD src1_sel:WORD_1
v_mfma_f32_16x16x32_bf16 v[240:243],v[104:107],v[144:147],v[240:243]
s_waitcnt lgkmcnt(2)
v_lshl_add_u64 v[154:155],v[156:157],0,v[158:159]
v_mfma_f32_16x16x32_bf16 v[248:251],v[108:111],v[144:147],v[248:251]
ds_read_u16 v129,v129
v_lshlrev_b32_sdwa v133,1,v133 dst_sel:DWORD dst_unused:UNUSED_PAD src0_sel:DWORD src1_sel:WORD_1
v_mfma_f32_16x16x32_bf16 v[212:215],v[88:91],v[152:155],v[212:215]
ds_read_u16_d16_hi v131,v131
v_lshlrev_b32_sdwa v135,1,v135 dst_sel:DWORD dst_unused:UNUSED_PAD src0_sel:DWORD src1_sel:WORD_1
v_mfma_f32_16x16x32_bf16 v[220:223],v[92:95],v[152:155],v[220:223]
ds_read_u16 v132,v132
v_lshlrev_b32_sdwa v136,1,v136 dst_sel:DWORD dst_unused:UNUSED_PAD src0_sel:DWORD src1_sel:WORD_1
v_mfma_f32_16x16x32_bf16 v[228:231],v[96:99],v[152:155],v[228:231]
ds_read_b64_tr_b16 v[88:89],v6 offset:18480
ds_read_b64_tr_b16 v[90:91],v6 offset:20016
v_lshlrev_b32_sdwa v138,1,v138 dst_sel:DWORD dst_unused:UNUSED_PAD src0_sel:DWORD src1_sel:WORD_1
v_mfma_f32_16x16x32_bf16 v[236:239],v[100:103],v[152:155],v[236:239]
ds_read_u16_d16_hi v134,v134
v_lshlrev_b32_sdwa v137,1,v137 dst_sel:DWORD dst_unused:UNUSED_PAD src0_sel:DWORD src1_sel:WORD_1
v_mfma_f32_16x16x32_bf16 v[244:247],v[104:107],v[152:155],v[244:247]
ds_read_u16 v133,v133
v_lshlrev_b32_sdwa v139,1,v139 dst_sel:DWORD dst_unused:UNUSED_PAD src0_sel:DWORD src1_sel:WORD_1
v_mfma_f32_16x16x32_bf16 v[252:255],v[108:111],v[152:155],v[252:255]
ds_read_b64_tr_b16 v[92:93],v6 offset:18488
ds_read_b64_tr_b16 v[94:95],v6 offset:20024
v_lshlrev_b32_sdwa v140,1,v140 dst_sel:DWORD dst_unused:UNUSED_PAD src0_sel:DWORD src1_sel:WORD_1
v_mfma_f32_16x16x32_bf16 v[144:147],v[64:67],v[40:43],v[112:115]
ds_read_u16_d16_hi v135,v135
v_lshlrev_b32_sdwa v142,1,v142 dst_sel:DWORD dst_unused:UNUSED_PAD src0_sel:DWORD src1_sel:WORD_1
v_mfma_f32_16x16x32_bf16 v[152:155],v[64:67],v[44:47],v[112:115]
ds_read_u16 v136,v136
ds_read_b64_tr_b16 v[96:97],v6 offset:18544
v_lshlrev_b32_sdwa v141,1,v141 dst_sel:DWORD dst_unused:UNUSED_PAD src0_sel:DWORD src1_sel:WORD_1
v_mfma_f32_16x16x32_bf16 v[156:159],v[68:71],v[44:47],v[116:119]
ds_read_b64_tr_b16 v[98:99],v6 offset:20080
ds_read_b64_tr_b16 v[100:101],v6 offset:18552
v_mfma_f32_16x16x32_bf16 v[148:151],v[68:71],v[40:43],v[116:119]
ds_read_u16_d16_hi v138,v138
v_lshlrev_b32_sdwa v143,1,v143 dst_sel:DWORD dst_unused:UNUSED_PAD src0_sel:DWORD src1_sel:WORD_1
v_mfma_f32_16x16x32_bf16 v[144:147],v[72:75],v[48:51],v[144:147]
ds_read_u16 v137,v137
v_mfma_f32_16x16x32_bf16 v[152:155],v[72:75],v[52:55],v[152:155]
ds_read_b64_tr_b16 v[102:103],v6 offset:20088
ds_read_b64_tr_b16 v[104:105],v6 offset:18608
v_mfma_f32_16x16x32_bf16 v[156:159],v[76:79],v[52:55],v[156:159]
ds_read_u16_d16_hi v139,v139
v_mfma_f32_16x16x32_bf16 v[148:151],v[76:79],v[48:51],v[148:151]
s_waitcnt lgkmcnt(15)
ds_read_u16 v140,v140
v_lshl_add_u64 v[128:129],v[128:129],0,v[130:131]
v_mfma_f32_16x16x32_bf16 v[144:147],v[80:83],v[56:59],v[144:147]
ds_read_b64_tr_b16 v[106:107],v6 offset:20144
ds_read_b64_tr_b16 v[108:109],v6 offset:18616
v_mfma_f32_16x16x32_bf16 v[152:155],v[80:83],v[60:63],v[152:155]
s_waitcnt lgkmcnt(12)
ds_read_u16_d16_hi v142,v142
v_lshl_add_u64 v[130:131],v[132:133],0,v[134:135]
v_mfma_f32_16x16x32_bf16 v[156:159],v[84:87],v[60:63],v[156:159]
ds_read_u16 v141,v141
ds_read_u16_d16_hi v143,v143
v_mfma_f32_16x16x32_bf16 v[148:151],v[84:87],v[56:59],v[148:151]
ds_read_b64_tr_b16 v[110:111],v6 offset:20152
ds_read_b64_tr_b16 v[64:65],v4 offset:12832
v_lshlrev_b32_sdwa v144,1,v144 dst_sel:DWORD dst_unused:UNUSED_PAD src0_sel:DWORD src1_sel:WORD_1
v_lshlrev_b32_sdwa v146,1,v146 dst_sel:DWORD dst_unused:UNUSED_PAD src0_sel:DWORD src1_sel:WORD_1
v_mfma_f32_16x16x32_bf16 v[160:163],v[88:91],v[128:131],v[160:163]
v_lshlrev_b32_sdwa v145,1,v145 dst_sel:DWORD dst_unused:UNUSED_PAD src0_sel:DWORD src1_sel:WORD_1
v_mfma_f32_16x16x32_bf16 v[168:171],v[92:95],v[128:131],v[168:171]
s_waitcnt lgkmcnt(14)
ds_read_u16 v144,v144
v_lshlrev_b32_sdwa v147,1,v147 dst_sel:DWORD dst_unused:UNUSED_PAD src0_sel:DWORD src1_sel:WORD_1
v_mfma_f32_16x16x32_bf16 v[176:179],v[96:99],v[128:131],v[176:179]
s_waitcnt lgkmcnt(11)
ds_read_b64_tr_b16 v[66:67],v4 offset:14880
ds_read_b128 v[112:115],v7 offset:1152
v_mfma_f32_16x16x32_bf16 v[184:187],v[100:103],v[128:131],v[184:187]
s_waitcnt lgkmcnt(9)
ds_read_b64_tr_b16 v[68:69],v5 offset:12832
ds_read_u16_d16_hi v146,v146
v_lshlrev_b32_sdwa v148,1,v148 dst_sel:DWORD dst_unused:UNUSED_PAD src0_sel:DWORD src1_sel:WORD_1
v_lshl_add_u64 v[136:137],v[136:137],0,v[138:139]
v_lshlrev_b32_sdwa v150,1,v150 dst_sel:DWORD dst_unused:UNUSED_PAD src0_sel:DWORD src1_sel:WORD_1
v_mfma_f32_16x16x32_bf16 v[192:195],v[104:107],v[128:131],v[192:195]
s_waitcnt lgkmcnt(6)
ds_read_u16 v145,v145
v_lshl_add_u64 v[138:139],v[140:141],0,v[142:143]
v_mfma_f32_16x16x32_bf16 v[200:203],v[108:111],v[128:131],v[200:203]
ds_read_b64_tr_b16 v[70:71],v5 offset:14880
ds_read_b128 v[116:119],v7 offset:1184
v_mfma_f32_16x16x32_bf16 v[164:167],v[88:91],v[136:139],v[164:167]
ds_read_u16_d16_hi v147,v147
v_lshlrev_b32_sdwa v149,1,v149 dst_sel:DWORD dst_unused:UNUSED_PAD src0_sel:DWORD src1_sel:WORD_1
v_lshlrev_b32_sdwa v151,1,v151 dst_sel:DWORD dst_unused:UNUSED_PAD src0_sel:DWORD src1_sel:WORD_1
v_mfma_f32_16x16x32_bf16 v[172:175],v[92:95],v[136:139],v[172:175]
ds_read_u16 v148,v148
ds_read_b64_tr_b16 v[72:73],v4 offset:16928
v_lshlrev_b32_sdwa v152,1,v152 dst_sel:DWORD dst_unused:UNUSED_PAD src0_sel:DWORD src1_sel:WORD_1
v_mfma_f32_16x16x32_bf16 v[180:183],v[96:99],v[136:139],v[180:183]
ds_read_b64_tr_b16 v[74:75],v4 offset:18976
ds_read_b64_tr_b16 v[76:77],v5 offset:16928
v_lshlrev_b32_sdwa v154,1,v154 dst_sel:DWORD dst_unused:UNUSED_PAD src0_sel:DWORD src1_sel:WORD_1
v_mfma_f32_16x16x32_bf16 v[188:191],v[100:103],v[136:139],v[188:191]
ds_read_u16_d16_hi v150,v150
v_lshlrev_b32_sdwa v153,1,v153 dst_sel:DWORD dst_unused:UNUSED_PAD src0_sel:DWORD src1_sel:WORD_1
v_mfma_f32_16x16x32_bf16 v[196:199],v[104:107],v[136:139],v[196:199]
ds_read_u16 v149,v149
v_lshlrev_b32_sdwa v155,1,v155 dst_sel:DWORD dst_unused:UNUSED_PAD src0_sel:DWORD src1_sel:WORD_1
v_mfma_f32_16x16x32_bf16 v[204:207],v[108:111],v[136:139],v[204:207]
s_waitcnt lgkmcnt(12)
ds_read_b64_tr_b16 v[78:79],v5 offset:18976
ds_read_b64_tr_b16 v[80:81],v4 offset:21024
v_lshlrev_b32_sdwa v156,1,v156 dst_sel:DWORD dst_unused:UNUSED_PAD src0_sel:DWORD src1_sel:WORD_1
v_mfma_f32_16x16x32_bf16 v[128:131],v[64:67],v[16:19],v[112:115]
ds_read_u16_d16_hi v151,v151
v_lshlrev_b32_sdwa v158,1,v158 dst_sel:DWORD dst_unused:UNUSED_PAD src0_sel:DWORD src1_sel:WORD_1
v_mfma_f32_16x16x32_bf16 v[136:139],v[64:67],v[20:23],v[112:115]
s_waitcnt lgkmcnt(10)
ds_read_u16 v152,v152
ds_read_b64_tr_b16 v[82:83],v4 offset:23072
v_lshlrev_b32_sdwa v157,1,v157 dst_sel:DWORD dst_unused:UNUSED_PAD src0_sel:DWORD src1_sel:WORD_1
v_mfma_f32_16x16x32_bf16 v[140:143],v[68:71],v[20:23],v[116:119]
ds_read_b64_tr_b16 v[84:85],v5 offset:21024
ds_read_b64_tr_b16 v[86:87],v5 offset:23072
v_lshlrev_b32_sdwa v159,1,v159 dst_sel:DWORD dst_unused:UNUSED_PAD src0_sel:DWORD src1_sel:WORD_1
v_mfma_f32_16x16x32_bf16 v[132:135],v[68:71],v[16:19],v[116:119]
s_waitcnt lgkmcnt(10)
ds_read_u16_d16_hi v154,v154
v_mfma_f32_16x16x32_bf16 v[128:131],v[72:75],v[24:27],v[128:131]
ds_read_u16 v153,v153
v_mfma_f32_16x16x32_bf16 v[136:139],v[72:75],v[28:31],v[136:139]
s_waitcnt lgkmcnt(8)
v_mfma_f32_16x16x32_bf16 v[140:143],v[76:79],v[28:31],v[140:143]
ds_read_u16_d16_hi v155,v155
v_mfma_f32_16x16x32_bf16 v[132:135],v[76:79],v[24:27],v[132:135]
s_waitcnt lgkmcnt(5)
ds_read_u16 v156,v156
s_nop 0
s_mov_b32 s86,0xbe801d5a
v_lshl_add_u64 v[144:145],v[144:145],0,v[146:147]
v_mfma_f32_16x16x32_bf16 v[128:131],v[80:83],v[32:35],v[128:131]
v_lshl_add_u64 v[146:147],v[148:149],0,v[150:151]
v_mfma_f32_16x16x32_bf16 v[136:139],v[80:83],v[36:39],v[136:139]
s_waitcnt lgkmcnt(4)
ds_read_u16_d16_hi v158,v158
v_mfma_f32_16x16x32_bf16 v[140:143],v[84:87],v[36:39],v[140:143]
ds_read_u16 v157,v157
v_mfma_f32_16x16x32_bf16 v[132:135],v[84:87],v[32:35],v[132:135]
s_nop 0
v_lshlrev_b32_sdwa v128,1,v128 dst_sel:DWORD dst_unused:UNUSED_PAD src0_sel:DWORD src1_sel:WORD_1
v_lshlrev_b32_sdwa v130,1,v130 dst_sel:DWORD dst_unused:UNUSED_PAD src0_sel:DWORD src1_sel:WORD_1
v_mfma_f32_16x16x32_bf16 v[208:211],v[88:91],v[144:147],v[208:211]
s_barrier
ds_read_u16_d16_hi v159,v159
v_lshlrev_b32_sdwa v129,1,v129 dst_sel:DWORD dst_unused:UNUSED_PAD src0_sel:DWORD src1_sel:WORD_1
v_lshlrev_b32_sdwa v131,1,v131 dst_sel:DWORD dst_unused:UNUSED_PAD src0_sel:DWORD src1_sel:WORD_1
v_mfma_f32_16x16x32_bf16 v[216:219],v[92:95],v[144:147],v[216:219]
v_fma_mixlo_f16 v120,v141,s99,0
v_lshlrev_b32_sdwa v132,1,v132 dst_sel:DWORD dst_unused:UNUSED_PAD src0_sel:DWORD src1_sel:WORD_1
v_mfma_f32_16x16x32_bf16 v[224:227],v[96:99],v[144:147],v[224:227]
ds_read_u16 v128,v128
v_fma_mixhi_f16 v120,v143,s99,0
v_lshlrev_b32_sdwa v134,1,v134 dst_sel:DWORD dst_unused:UNUSED_PAD src0_sel:DWORD src1_sel:WORD_1
v_mfma_f32_16x16x32_bf16 v[232:235],v[100:103],v[144:147],v[232:235]
s_waitcnt lgkmcnt(5)
ds_read_u16_d16_hi v130,v130
v_lshl_add_u64 v[152:153],v[152:153],0,v[154:155]
v_mfma_f32_16x16x32_bf16 v[240:243],v[104:107],v[144:147],v[240:243]
s_waitcnt lgkmcnt(2)
ds_read_u16 v129,v129
v_lshl_add_u64 v[154:155],v[156:157],0,v[158:159]
v_mfma_f32_16x16x32_bf16 v[248:251],v[108:111],v[144:147],v[248:251]
v_pk_fma_f16 v121,v120,v120,s54
v_lshlrev_b32_sdwa v133,1,v133 dst_sel:DWORD dst_unused:UNUSED_PAD src0_sel:DWORD src1_sel:WORD_1
v_mfma_f32_16x16x32_bf16 v[212:215],v[88:91],v[152:155],v[212:215]
ds_read_u16_d16_hi v131,v131
ds_read_b64_tr_b16 v[88:89],v6 offset:32832
v_pk_mul_f16 v121,v120,v121
v_lshlrev_b32_sdwa v135,1,v135 dst_sel:DWORD dst_unused:UNUSED_PAD src0_sel:DWORD src1_sel:WORD_1
v_mfma_f32_16x16x32_bf16 v[220:223],v[92:95],v[152:155],v[220:223]
ds_read_u16 v132,v132
v_exp_f16 v120,v121
v_mfma_f32_16x16x32_bf16 v[228:231],v[96:99],v[152:155],v[228:231]
ds_read_b64_tr_b16 v[90:91],v6 offset:34368
ds_read_b64_tr_b16 v[92:93],v6 offset:32840
v_lshlrev_b32_sdwa v136,1,v136 dst_sel:DWORD dst_unused:UNUSED_PAD src0_sel:DWORD src1_sel:WORD_1
v_mfma_f32_16x16x32_bf16 v[236:239],v[100:103],v[152:155],v[236:239]
ds_read_u16_d16_hi v134,v134
v_exp_f16_sdwa v120,v121 dst_sel:WORD_1 dst_unused:UNUSED_PRESERVE src0_sel:WORD_1
v_mfma_f32_16x16x32_bf16 v[244:247],v[104:107],v[152:155],v[244:247]
v_lshlrev_b32_sdwa v138,1,v138 dst_sel:DWORD dst_unused:UNUSED_PAD src0_sel:DWORD src1_sel:WORD_1
v_mfma_f32_16x16x32_bf16 v[252:255],v[108:111],v[152:155],v[252:255]
ds_read_b64_tr_b16 v[94:95],v6 offset:34376
ds_read_b64_tr_b16 v[96:97],v6 offset:32896
v_pk_fma_f16 v120,v120,s55,s55
v_lshlrev_b32_sdwa v137,1,v137 dst_sel:DWORD dst_unused:UNUSED_PAD src0_sel:DWORD src1_sel:WORD_1
v_mfma_f32_16x16x32_bf16 v[144:147],v[64:67],v[40:43],v[112:115]
ds_read_u16 v133,v133
v_rcp_f16 v121,v120
v_mfma_f32_16x16x32_bf16 v[152:155],v[64:67],v[44:47],v[112:115]
ds_read_b64_tr_b16 v[98:99],v6 offset:34432
ds_read_b64_tr_b16 v[100:101],v6 offset:32904
v_lshlrev_b32_sdwa v139,1,v139 dst_sel:DWORD dst_unused:UNUSED_PAD src0_sel:DWORD src1_sel:WORD_1
v_mfma_f32_16x16x32_bf16 v[156:159],v[68:71],v[44:47],v[116:119]
ds_read_u16_d16_hi v135,v135
v_rcp_f16_sdwa v121,v120 dst_sel:WORD_1 dst_unused:UNUSED_PRESERVE src0_sel:WORD_1
v_mfma_f32_16x16x32_bf16 v[148:151],v[68:71],v[40:43],v[116:119]
ds_read_u16 v136,v136
v_lshlrev_b32_sdwa v140,1,v140 dst_sel:DWORD dst_unused:UNUSED_PAD src0_sel:DWORD src1_sel:WORD_1
v_mfma_f32_16x16x32_bf16 v[144:147],v[72:75],v[48:51],v[144:147]
ds_read_b64_tr_b16 v[102:103],v6 offset:34440
ds_read_b64_tr_b16 v[104:105],v6 offset:32960
v_fma_mix_f32 v141,v141,v121,0 op_sel_hi:[0,1,0]
v_lshlrev_b32_sdwa v142,1,v142 dst_sel:DWORD dst_unused:UNUSED_PAD src0_sel:DWORD src1_sel:WORD_1
v_mfma_f32_16x16x32_bf16 v[152:155],v[72:75],v[52:55],v[152:155]
ds_read_u16_d16_hi v138,v138
v_fma_mix_f32 v143,v143,v121,0 op_sel:[0,1,0] op_sel_hi:[0,1,0]
v_mfma_f32_16x16x32_bf16 v[156:159],v[76:79],v[52:55],v[156:159]
ds_read_b64_tr_b16 v[106:107],v6 offset:34496
ds_read_b64_tr_b16 v[108:109],v6 offset:32968
v_mfma_f32_16x16x32_bf16 v[148:151],v[76:79],v[48:51],v[148:151]
ds_read_u16 v137,v137
v_mfma_f32_16x16x32_bf16 v[144:147],v[80:83],v[56:59],v[144:147]
ds_read_b64_tr_b16 v[110:111],v6 offset:34504
ds_read_b64_tr_b16 v[64:65],v8 offset:0
v_mfma_f32_16x16x32_bf16 v[152:155],v[80:83],v[60:63],v[152:155]
s_waitcnt lgkmcnt(9)
ds_read_u16_d16_hi v139,v139
v_lshl_add_u64 v[128:129],v[128:129],0,v[130:131]
v_lshl_add_u64 v[130:131],v[132:133],0,v[134:135]
v_mfma_f32_16x16x32_bf16 v[156:159],v[84:87],v[60:63],v[156:159]
ds_read_u16 v140,v140
v_mfma_f32_16x16x32_bf16 v[148:151],v[84:87],v[56:59],v[148:151]
ds_read_b64_tr_b16 v[66:67],v8 offset:2048
ds_read_b128 v[112:115],v7 offset:1280
v_lshlrev_b32_sdwa v144,1,v144 dst_sel:DWORD dst_unused:UNUSED_PAD src0_sel:DWORD src1_sel:WORD_1
v_lshlrev_b32_sdwa v146,1,v146 dst_sel:DWORD dst_unused:UNUSED_PAD src0_sel:DWORD src1_sel:WORD_1
v_mfma_f32_16x16x32_bf16 v[160:163],v[88:91],v[128:131],v[160:163]
ds_read_u16_d16_hi v142,v142
v_lshlrev_b32_sdwa v145,1,v145 dst_sel:DWORD dst_unused:UNUSED_PAD src0_sel:DWORD src1_sel:WORD_1
v_lshlrev_b32_sdwa v147,1,v147 dst_sel:DWORD dst_unused:UNUSED_PAD src0_sel:DWORD src1_sel:WORD_1
v_mfma_f32_16x16x32_bf16 v[168:171],v[92:95],v[128:131],v[168:171]
ds_read_b64_tr_b16 v[68:69],v9 offset:0
ds_read_b64_tr_b16 v[70:71],v9 offset:2048
v_fma_mixlo_f16 v120,v157,s99,0
v_mfma_f32_16x16x32_bf16 v[176:179],v[96:99],v[128:131],v[176:179]
s_waitcnt lgkmcnt(6)
ds_read_u16 v144,v144
v_lshlrev_b32_sdwa v148,1,v148 dst_sel:DWORD dst_unused:UNUSED_PAD src0_sel:DWORD src1_sel:WORD_1
v_lshl_add_u64 v[136:137],v[136:137],0,v[138:139]
v_fma_mixhi_f16 v120,v159,s99,0
v_lshlrev_b32_sdwa v150,1,v150 dst_sel:DWORD dst_unused:UNUSED_PAD src0_sel:DWORD src1_sel:WORD_1
v_mfma_f32_16x16x32_bf16 v[184:187],v[100:103],v[128:131],v[184:187]
ds_read_u16_d16_hi v146,v146
v_mfma_f32_16x16x32_bf16 v[192:195],v[104:107],v[128:131],v[192:195]
s_waitcnt lgkmcnt(4)
ds_read_b128 v[116:119],v7 offset:1312
ds_read_b64_tr_b16 v[72:73],v8 offset:4096
v_or_b32 v138,v142,v140
v_cvt_pk_bf16_f32 v139,v141,v143
v_mfma_f32_16x16x32_bf16 v[200:203],v[108:111],v[128:131],v[200:203]
ds_read_b64_tr_b16 v[74:75],v8 offset:6144
ds_read_b64_tr_b16 v[76:77],v9 offset:4096
v_pk_fma_f16 v121,v120,v120,s54
v_lshlrev_b32_sdwa v149,1,v149 dst_sel:DWORD dst_unused:UNUSED_PAD src0_sel:DWORD src1_sel:WORD_1
v_mfma_f32_16x16x32_bf16 v[164:167],v[88:91],v[136:139],v[164:167]
ds_read_u16 v145,v145
v_pk_mul_f16 v121,v120,v121
v_lshlrev_b32_sdwa v151,1,v151 dst_sel:DWORD dst_unused:UNUSED_PAD src0_sel:DWORD src1_sel:WORD_1
v_mfma_f32_16x16x32_bf16 v[172:175],v[92:95],v[136:139],v[172:175]
ds_read_u16_d16_hi v147,v147
v_lshlrev_b32_sdwa v152,1,v152 dst_sel:DWORD dst_unused:UNUSED_PAD src0_sel:DWORD src1_sel:WORD_1
v_exp_f16 v120,v121
v_mfma_f32_16x16x32_bf16 v[180:183],v[96:99],v[136:139],v[180:183]
ds_read_b64_tr_b16 v[78:79],v9 offset:6144
ds_read_b64_tr_b16 v[80:81],v8 offset:8192
v_mfma_f32_16x16x32_bf16 v[188:191],v[100:103],v[136:139],v[188:191]
v_exp_f16_sdwa v120,v121 dst_sel:WORD_1 dst_unused:UNUSED_PRESERVE src0_sel:WORD_1
v_mfma_f32_16x16x32_bf16 v[196:199],v[104:107],v[136:139],v[196:199]
ds_read_u16 v148,v148
ds_read_u16_d16_hi v150,v150
v_lshlrev_b32_sdwa v154,1,v154 dst_sel:DWORD dst_unused:UNUSED_PAD src0_sel:DWORD src1_sel:WORD_1
v_mfma_f32_16x16x32_bf16 v[204:207],v[108:111],v[136:139],v[204:207]
ds_read_b64_tr_b16 v[82:83],v8 offset:10240
v_pk_fma_f16 v120,v120,s55,s55
v_lshlrev_b32_sdwa v153,1,v153 dst_sel:DWORD dst_unused:UNUSED_PAD src0_sel:DWORD src1_sel:WORD_1
v_mfma_f32_16x16x32_bf16 v[128:131],v[64:67],v[16:19],v[112:115]
ds_read_u16 v149,v149
v_rcp_f16 v121,v120
v_mfma_f32_16x16x32_bf16 v[136:139],v[64:67],v[20:23],v[112:115]
s_waitcnt lgkmcnt(11)
ds_read_b64_tr_b16 v[84:85],v9 offset:8192
v_lshlrev_b32_sdwa v155,1,v155 dst_sel:DWORD dst_unused:UNUSED_PAD src0_sel:DWORD src1_sel:WORD_1
v_mfma_f32_16x16x32_bf16 v[140:143],v[68:71],v[20:23],v[116:119]
ds_read_u16_d16_hi v151,v151
v_rcp_f16_sdwa v121,v120 dst_sel:WORD_1 dst_unused:UNUSED_PRESERVE src0_sel:WORD_1
v_mfma_f32_16x16x32_bf16 v[132:135],v[68:71],v[16:19],v[116:119]
s_waitcnt lgkmcnt(11)
ds_read_u16 v152,v152
v_lshlrev_b32_sdwa v156,1,v156 dst_sel:DWORD dst_unused:UNUSED_PAD src0_sel:DWORD src1_sel:WORD_1
v_mfma_f32_16x16x32_bf16 v[128:131],v[72:75],v[24:27],v[128:131]
ds_read_b64_tr_b16 v[86:87],v9 offset:10240
v_fma_mix_f32 v157,v157,v121,0 op_sel_hi:[0,1,0]
v_mfma_f32_16x16x32_bf16 v[136:139],v[72:75],v[28:31],v[136:139]
s_waitcnt lgkmcnt(9)
ds_read_u16_d16_hi v154,v154
v_fma_mix_f32 v159,v159,v121,0 op_sel:[0,1,0] op_sel_hi:[0,1,0]
v_mfma_f32_16x16x32_bf16 v[140:143],v[76:79],v[28:31],v[140:143]
v_mfma_f32_16x16x32_bf16 v[132:135],v[76:79],v[24:27],v[132:135]
s_waitcnt lgkmcnt(6)
ds_read_u16 v153,v153
v_lshlrev_b32_sdwa v158,1,v158 dst_sel:DWORD dst_unused:UNUSED_PAD src0_sel:DWORD src1_sel:WORD_1
v_mfma_f32_16x16x32_bf16 v[128:131],v[80:83],v[32:35],v[128:131]
ds_read_u16_d16_hi v155,v155
v_mfma_f32_16x16x32_bf16 v[136:139],v[80:83],v[36:39],v[136:139]
s_waitcnt lgkmcnt(3)
v_lshl_add_u64 v[144:145],v[144:145],0,v[146:147]
v_mfma_f32_16x16x32_bf16 v[140:143],v[84:87],v[36:39],v[140:143]
ds_read_u16 v156,v156
v_lshl_add_u64 v[146:147],v[148:149],0,v[150:151]
v_mfma_f32_16x16x32_bf16 v[132:135],v[84:87],v[32:35],v[132:135]
v_lshlrev_b32_sdwa v128,1,v128 dst_sel:DWORD dst_unused:UNUSED_PAD src0_sel:DWORD src1_sel:WORD_1
v_lshlrev_b32_sdwa v130,1,v130 dst_sel:DWORD dst_unused:UNUSED_PAD src0_sel:DWORD src1_sel:WORD_1
v_mfma_f32_16x16x32_bf16 v[208:211],v[88:91],v[144:147],v[208:211]
ds_read_u16_d16_hi v158,v158
v_lshlrev_b32_sdwa v129,1,v129 dst_sel:DWORD dst_unused:UNUSED_PAD src0_sel:DWORD src1_sel:WORD_1
v_lshlrev_b32_sdwa v131,1,v131 dst_sel:DWORD dst_unused:UNUSED_PAD src0_sel:DWORD src1_sel:WORD_1
v_mfma_f32_16x16x32_bf16 v[216:219],v[92:95],v[144:147],v[216:219]
v_fma_mixlo_f16 v120,v141,s99,0
v_lshlrev_b32_sdwa v132,1,v132 dst_sel:DWORD dst_unused:UNUSED_PAD src0_sel:DWORD src1_sel:WORD_1
v_mfma_f32_16x16x32_bf16 v[224:227],v[96:99],v[144:147],v[224:227]
ds_read_u16 v128,v128
v_fma_mixhi_f16 v120,v143,s99,0
v_lshlrev_b32_sdwa v134,1,v134 dst_sel:DWORD dst_unused:UNUSED_PAD src0_sel:DWORD src1_sel:WORD_1
v_mfma_f32_16x16x32_bf16 v[232:235],v[100:103],v[144:147],v[232:235]
s_waitcnt lgkmcnt(3)
ds_read_u16_d16_hi v130,v130
v_lshl_add_u64 v[152:153],v[152:153],0,v[154:155]
v_mfma_f32_16x16x32_bf16 v[240:243],v[104:107],v[144:147],v[240:243]
s_waitcnt lgkmcnt(2)
ds_read_u16 v129,v129
v_or_b32 v154,v158,v156
v_cvt_pk_bf16_f32 v155,v157,v159
v_mfma_f32_16x16x32_bf16 v[248:251],v[108:111],v[144:147],v[248:251]
v_pk_fma_f16 v121,v120,v120,s54
v_lshlrev_b32_sdwa v133,1,v133 dst_sel:DWORD dst_unused:UNUSED_PAD src0_sel:DWORD src1_sel:WORD_1
v_mfma_f32_16x16x32_bf16 v[212:215],v[88:91],v[152:155],v[212:215]
ds_read_u16_d16_hi v131,v131
ds_read_b64_tr_b16 v[88:89],v6 offset:38992
v_pk_mul_f16 v121,v120,v121
v_lshlrev_b32_sdwa v135,1,v135 dst_sel:DWORD dst_unused:UNUSED_PAD src0_sel:DWORD src1_sel:WORD_1
v_mfma_f32_16x16x32_bf16 v[220:223],v[92:95],v[152:155],v[220:223]
ds_read_u16 v132,v132
v_exp_f16 v120,v121
v_mfma_f32_16x16x32_bf16 v[228:231],v[96:99],v[152:155],v[228:231]
ds_read_b64_tr_b16 v[90:91],v6 offset:40528
ds_read_b64_tr_b16 v[92:93],v6 offset:39000
v_lshlrev_b32_sdwa v136,1,v136 dst_sel:DWORD dst_unused:UNUSED_PAD src0_sel:DWORD src1_sel:WORD_1
v_mfma_f32_16x16x32_bf16 v[236:239],v[100:103],v[152:155],v[236:239]
ds_read_u16_d16_hi v134,v134
v_exp_f16_sdwa v120,v121 dst_sel:WORD_1 dst_unused:UNUSED_PRESERVE src0_sel:WORD_1
v_mfma_f32_16x16x32_bf16 v[244:247],v[104:107],v[152:155],v[244:247]
v_lshlrev_b32_sdwa v138,1,v138 dst_sel:DWORD dst_unused:UNUSED_PAD src0_sel:DWORD src1_sel:WORD_1
v_mfma_f32_16x16x32_bf16 v[252:255],v[108:111],v[152:155],v[252:255]
ds_read_b64_tr_b16 v[94:95],v6 offset:40536
ds_read_b64_tr_b16 v[96:97],v6 offset:39056
v_pk_fma_f16 v120,v120,s55,s55
v_lshlrev_b32_sdwa v137,1,v137 dst_sel:DWORD dst_unused:UNUSED_PAD src0_sel:DWORD src1_sel:WORD_1
v_mfma_f32_16x16x32_bf16 v[144:147],v[64:67],v[40:43],v[112:115]
ds_read_u16 v133,v133
v_rcp_f16 v121,v120
v_mfma_f32_16x16x32_bf16 v[152:155],v[64:67],v[44:47],v[112:115]
ds_read_b64_tr_b16 v[98:99],v6 offset:40592
ds_read_b64_tr_b16 v[100:101],v6 offset:39064
v_lshlrev_b32_sdwa v139,1,v139 dst_sel:DWORD dst_unused:UNUSED_PAD src0_sel:DWORD src1_sel:WORD_1
v_mfma_f32_16x16x32_bf16 v[156:159],v[68:71],v[44:47],v[116:119]
ds_read_u16_d16_hi v135,v135
v_rcp_f16_sdwa v121,v120 dst_sel:WORD_1 dst_unused:UNUSED_PRESERVE src0_sel:WORD_1
v_mfma_f32_16x16x32_bf16 v[148:151],v[68:71],v[40:43],v[116:119]
ds_read_u16 v136,v136
v_lshlrev_b32_sdwa v140,1,v140 dst_sel:DWORD dst_unused:UNUSED_PAD src0_sel:DWORD src1_sel:WORD_1
v_mfma_f32_16x16x32_bf16 v[144:147],v[72:75],v[48:51],v[144:147]
ds_read_b64_tr_b16 v[102:103],v6 offset:40600
ds_read_b64_tr_b16 v[104:105],v6 offset:39120
v_fma_mix_f32 v141,v141,v121,0 op_sel_hi:[0,1,0]
v_lshlrev_b32_sdwa v142,1,v142 dst_sel:DWORD dst_unused:UNUSED_PAD src0_sel:DWORD src1_sel:WORD_1
v_mfma_f32_16x16x32_bf16 v[152:155],v[72:75],v[52:55],v[152:155]
ds_read_u16_d16_hi v138,v138
v_fma_mix_f32 v143,v143,v121,0 op_sel:[0,1,0] op_sel_hi:[0,1,0]
v_mfma_f32_16x16x32_bf16 v[156:159],v[76:79],v[52:55],v[156:159]
ds_read_b64_tr_b16 v[106:107],v6 offset:40656
ds_read_b64_tr_b16 v[108:109],v6 offset:39128
v_mfma_f32_16x16x32_bf16 v[148:151],v[76:79],v[48:51],v[148:151]
ds_read_u16 v137,v137
v_mfma_f32_16x16x32_bf16 v[144:147],v[80:83],v[56:59],v[144:147]
ds_read_b64_tr_b16 v[110:111],v6 offset:40664
ds_read_b64_tr_b16 v[64:65],v8 offset:512
v_mfma_f32_16x16x32_bf16 v[152:155],v[80:83],v[60:63],v[152:155]
s_waitcnt lgkmcnt(9)
ds_read_u16_d16_hi v139,v139
v_lshl_add_u64 v[128:129],v[128:129],0,v[130:131]
v_lshl_add_u64 v[130:131],v[132:133],0,v[134:135]
v_mfma_f32_16x16x32_bf16 v[156:159],v[84:87],v[60:63],v[156:159]
ds_read_u16 v140,v140
v_mfma_f32_16x16x32_bf16 v[148:151],v[84:87],v[56:59],v[148:151]
ds_read_b64_tr_b16 v[66:67],v8 offset:2560
ds_read_b128 v[112:115],v7 offset:1408
v_lshlrev_b32_sdwa v144,1,v144 dst_sel:DWORD dst_unused:UNUSED_PAD src0_sel:DWORD src1_sel:WORD_1
v_lshlrev_b32_sdwa v146,1,v146 dst_sel:DWORD dst_unused:UNUSED_PAD src0_sel:DWORD src1_sel:WORD_1
v_mfma_f32_16x16x32_bf16 v[160:163],v[88:91],v[128:131],v[160:163]
ds_read_u16_d16_hi v142,v142
v_lshlrev_b32_sdwa v145,1,v145 dst_sel:DWORD dst_unused:UNUSED_PAD src0_sel:DWORD src1_sel:WORD_1
v_lshlrev_b32_sdwa v147,1,v147 dst_sel:DWORD dst_unused:UNUSED_PAD src0_sel:DWORD src1_sel:WORD_1
v_mfma_f32_16x16x32_bf16 v[168:171],v[92:95],v[128:131],v[168:171]
ds_read_b64_tr_b16 v[68:69],v9 offset:512
ds_read_b64_tr_b16 v[70:71],v9 offset:2560
v_fma_mixlo_f16 v120,v157,s99,0
v_mfma_f32_16x16x32_bf16 v[176:179],v[96:99],v[128:131],v[176:179]
s_waitcnt lgkmcnt(6)
ds_read_u16 v144,v144
v_lshlrev_b32_sdwa v148,1,v148 dst_sel:DWORD dst_unused:UNUSED_PAD src0_sel:DWORD src1_sel:WORD_1
v_lshl_add_u64 v[136:137],v[136:137],0,v[138:139]
v_fma_mixhi_f16 v120,v159,s99,0
v_lshlrev_b32_sdwa v150,1,v150 dst_sel:DWORD dst_unused:UNUSED_PAD src0_sel:DWORD src1_sel:WORD_1
v_mfma_f32_16x16x32_bf16 v[184:187],v[100:103],v[128:131],v[184:187]
ds_read_u16_d16_hi v146,v146
v_mfma_f32_16x16x32_bf16 v[192:195],v[104:107],v[128:131],v[192:195]
s_waitcnt lgkmcnt(4)
ds_read_b128 v[116:119],v7 offset:1440
ds_read_b64_tr_b16 v[72:73],v8 offset:4608
v_or_b32 v138,v142,v140
v_cvt_pk_bf16_f32 v139,v141,v143
v_mfma_f32_16x16x32_bf16 v[200:203],v[108:111],v[128:131],v[200:203]
ds_read_b64_tr_b16 v[74:75],v8 offset:6656
ds_read_b64_tr_b16 v[76:77],v9 offset:4608
v_pk_fma_f16 v121,v120,v120,s54
v_lshlrev_b32_sdwa v149,1,v149 dst_sel:DWORD dst_unused:UNUSED_PAD src0_sel:DWORD src1_sel:WORD_1
v_mfma_f32_16x16x32_bf16 v[164:167],v[88:91],v[136:139],v[164:167]
ds_read_u16 v145,v145
v_pk_mul_f16 v121,v120,v121
v_lshlrev_b32_sdwa v151,1,v151 dst_sel:DWORD dst_unused:UNUSED_PAD src0_sel:DWORD src1_sel:WORD_1
v_mfma_f32_16x16x32_bf16 v[172:175],v[92:95],v[136:139],v[172:175]
ds_read_u16_d16_hi v147,v147
v_lshlrev_b32_sdwa v152,1,v152 dst_sel:DWORD dst_unused:UNUSED_PAD src0_sel:DWORD src1_sel:WORD_1
v_exp_f16 v120,v121
v_mfma_f32_16x16x32_bf16 v[180:183],v[96:99],v[136:139],v[180:183]
ds_read_b64_tr_b16 v[78:79],v9 offset:6656
ds_read_b64_tr_b16 v[80:81],v8 offset:8704
v_mfma_f32_16x16x32_bf16 v[188:191],v[100:103],v[136:139],v[188:191]
v_exp_f16_sdwa v120,v121 dst_sel:WORD_1 dst_unused:UNUSED_PRESERVE src0_sel:WORD_1
v_mfma_f32_16x16x32_bf16 v[196:199],v[104:107],v[136:139],v[196:199]
ds_read_u16 v148,v148
ds_read_u16_d16_hi v150,v150
v_lshlrev_b32_sdwa v154,1,v154 dst_sel:DWORD dst_unused:UNUSED_PAD src0_sel:DWORD src1_sel:WORD_1
v_mfma_f32_16x16x32_bf16 v[204:207],v[108:111],v[136:139],v[204:207]
ds_read_b64_tr_b16 v[82:83],v8 offset:10752
v_pk_fma_f16 v120,v120,s55,s55
v_lshlrev_b32_sdwa v153,1,v153 dst_sel:DWORD dst_unused:UNUSED_PAD src0_sel:DWORD src1_sel:WORD_1
v_mfma_f32_16x16x32_bf16 v[128:131],v[64:67],v[16:19],v[112:115]
ds_read_u16 v149,v149
v_rcp_f16 v121,v120
v_mfma_f32_16x16x32_bf16 v[136:139],v[64:67],v[20:23],v[112:115]
s_waitcnt lgkmcnt(11)
ds_read_b64_tr_b16 v[84:85],v9 offset:8704
v_lshlrev_b32_sdwa v155,1,v155 dst_sel:DWORD dst_unused:UNUSED_PAD src0_sel:DWORD src1_sel:WORD_1
v_mfma_f32_16x16x32_bf16 v[140:143],v[68:71],v[20:23],v[116:119]
ds_read_u16_d16_hi v151,v151
v_rcp_f16_sdwa v121,v120 dst_sel:WORD_1 dst_unused:UNUSED_PRESERVE src0_sel:WORD_1
v_mfma_f32_16x16x32_bf16 v[132:135],v[68:71],v[16:19],v[116:119]
s_waitcnt lgkmcnt(11)
ds_read_u16 v152,v152
v_lshlrev_b32_sdwa v156,1,v156 dst_sel:DWORD dst_unused:UNUSED_PAD src0_sel:DWORD src1_sel:WORD_1
v_mfma_f32_16x16x32_bf16 v[128:131],v[72:75],v[24:27],v[128:131]
ds_read_b64_tr_b16 v[86:87],v9 offset:10752
v_fma_mix_f32 v157,v157,v121,0 op_sel_hi:[0,1,0]
v_mfma_f32_16x16x32_bf16 v[136:139],v[72:75],v[28:31],v[136:139]
s_waitcnt lgkmcnt(9)
ds_read_u16_d16_hi v154,v154
v_fma_mix_f32 v159,v159,v121,0 op_sel:[0,1,0] op_sel_hi:[0,1,0]
v_mfma_f32_16x16x32_bf16 v[140:143],v[76:79],v[28:31],v[140:143]
v_mfma_f32_16x16x32_bf16 v[132:135],v[76:79],v[24:27],v[132:135]
s_waitcnt lgkmcnt(6)
ds_read_u16 v153,v153
v_lshlrev_b32_sdwa v158,1,v158 dst_sel:DWORD dst_unused:UNUSED_PAD src0_sel:DWORD src1_sel:WORD_1
v_mfma_f32_16x16x32_bf16 v[128:131],v[80:83],v[32:35],v[128:131]
ds_read_u16_d16_hi v155,v155
v_mfma_f32_16x16x32_bf16 v[136:139],v[80:83],v[36:39],v[136:139]
s_waitcnt lgkmcnt(3)
v_lshl_add_u64 v[144:145],v[144:145],0,v[146:147]
v_mfma_f32_16x16x32_bf16 v[140:143],v[84:87],v[36:39],v[140:143]
ds_read_u16 v156,v156
v_lshl_add_u64 v[146:147],v[148:149],0,v[150:151]
v_mfma_f32_16x16x32_bf16 v[132:135],v[84:87],v[32:35],v[132:135]
v_lshlrev_b32_sdwa v128,1,v128 dst_sel:DWORD dst_unused:UNUSED_PAD src0_sel:DWORD src1_sel:WORD_1
v_lshlrev_b32_sdwa v130,1,v130 dst_sel:DWORD dst_unused:UNUSED_PAD src0_sel:DWORD src1_sel:WORD_1
v_mfma_f32_16x16x32_bf16 v[208:211],v[88:91],v[144:147],v[208:211]
ds_read_u16_d16_hi v158,v158
v_lshlrev_b32_sdwa v129,1,v129 dst_sel:DWORD dst_unused:UNUSED_PAD src0_sel:DWORD src1_sel:WORD_1
v_lshlrev_b32_sdwa v131,1,v131 dst_sel:DWORD dst_unused:UNUSED_PAD src0_sel:DWORD src1_sel:WORD_1
v_mfma_f32_16x16x32_bf16 v[216:219],v[92:95],v[144:147],v[216:219]
v_fma_mixlo_f16 v120,v141,s99,0
v_lshlrev_b32_sdwa v132,1,v132 dst_sel:DWORD dst_unused:UNUSED_PAD src0_sel:DWORD src1_sel:WORD_1
v_mfma_f32_16x16x32_bf16 v[224:227],v[96:99],v[144:147],v[224:227]
ds_read_u16 v128,v128
v_fma_mixhi_f16 v120,v143,s99,0
v_lshlrev_b32_sdwa v134,1,v134 dst_sel:DWORD dst_unused:UNUSED_PAD src0_sel:DWORD src1_sel:WORD_1
v_mfma_f32_16x16x32_bf16 v[232:235],v[100:103],v[144:147],v[232:235]
s_waitcnt lgkmcnt(3)
ds_read_u16_d16_hi v130,v130
v_lshl_add_u64 v[152:153],v[152:153],0,v[154:155]
v_mfma_f32_16x16x32_bf16 v[240:243],v[104:107],v[144:147],v[240:243]
s_waitcnt lgkmcnt(2)
ds_read_u16 v129,v129
v_or_b32 v154,v158,v156
v_cvt_pk_bf16_f32 v155,v157,v159
v_mfma_f32_16x16x32_bf16 v[248:251],v[108:111],v[144:147],v[248:251]
v_pk_fma_f16 v121,v120,v120,s54
v_lshlrev_b32_sdwa v133,1,v133 dst_sel:DWORD dst_unused:UNUSED_PAD src0_sel:DWORD src1_sel:WORD_1
v_mfma_f32_16x16x32_bf16 v[212:215],v[88:91],v[152:155],v[212:215]
ds_read_u16_d16_hi v131,v131
ds_read_b64_tr_b16 v[88:89],v6 offset:45152
v_pk_mul_f16 v121,v120,v121
v_lshlrev_b32_sdwa v135,1,v135 dst_sel:DWORD dst_unused:UNUSED_PAD src0_sel:DWORD src1_sel:WORD_1
v_mfma_f32_16x16x32_bf16 v[220:223],v[92:95],v[152:155],v[220:223]
ds_read_u16 v132,v132
v_exp_f16 v120,v121
v_mfma_f32_16x16x32_bf16 v[228:231],v[96:99],v[152:155],v[228:231]
ds_read_b64_tr_b16 v[90:91],v6 offset:46688
ds_read_b64_tr_b16 v[92:93],v6 offset:45160
v_lshlrev_b32_sdwa v136,1,v136 dst_sel:DWORD dst_unused:UNUSED_PAD src0_sel:DWORD src1_sel:WORD_1
v_mfma_f32_16x16x32_bf16 v[236:239],v[100:103],v[152:155],v[236:239]
ds_read_u16_d16_hi v134,v134
v_exp_f16_sdwa v120,v121 dst_sel:WORD_1 dst_unused:UNUSED_PRESERVE src0_sel:WORD_1
v_mfma_f32_16x16x32_bf16 v[244:247],v[104:107],v[152:155],v[244:247]
v_lshlrev_b32_sdwa v138,1,v138 dst_sel:DWORD dst_unused:UNUSED_PAD src0_sel:DWORD src1_sel:WORD_1
v_mfma_f32_16x16x32_bf16 v[252:255],v[108:111],v[152:155],v[252:255]
ds_read_b64_tr_b16 v[94:95],v6 offset:46696
ds_read_b64_tr_b16 v[96:97],v6 offset:45216
v_pk_fma_f16 v120,v120,s55,s55
v_lshlrev_b32_sdwa v137,1,v137 dst_sel:DWORD dst_unused:UNUSED_PAD src0_sel:DWORD src1_sel:WORD_1
v_mfma_f32_16x16x32_bf16 v[144:147],v[64:67],v[40:43],v[112:115]
ds_read_u16 v133,v133
v_rcp_f16 v121,v120
v_mfma_f32_16x16x32_bf16 v[152:155],v[64:67],v[44:47],v[112:115]
ds_read_b64_tr_b16 v[98:99],v6 offset:46752
ds_read_b64_tr_b16 v[100:101],v6 offset:45224
v_lshlrev_b32_sdwa v139,1,v139 dst_sel:DWORD dst_unused:UNUSED_PAD src0_sel:DWORD src1_sel:WORD_1
v_mfma_f32_16x16x32_bf16 v[156:159],v[68:71],v[44:47],v[116:119]
ds_read_u16_d16_hi v135,v135
v_rcp_f16_sdwa v121,v120 dst_sel:WORD_1 dst_unused:UNUSED_PRESERVE src0_sel:WORD_1
v_mfma_f32_16x16x32_bf16 v[148:151],v[68:71],v[40:43],v[116:119]
ds_read_u16 v136,v136
v_lshlrev_b32_sdwa v140,1,v140 dst_sel:DWORD dst_unused:UNUSED_PAD src0_sel:DWORD src1_sel:WORD_1
v_mfma_f32_16x16x32_bf16 v[144:147],v[72:75],v[48:51],v[144:147]
ds_read_b64_tr_b16 v[102:103],v6 offset:46760
ds_read_b64_tr_b16 v[104:105],v6 offset:45280
v_fma_mix_f32 v141,v141,v121,0 op_sel_hi:[0,1,0]
v_lshlrev_b32_sdwa v142,1,v142 dst_sel:DWORD dst_unused:UNUSED_PAD src0_sel:DWORD src1_sel:WORD_1
v_mfma_f32_16x16x32_bf16 v[152:155],v[72:75],v[52:55],v[152:155]
ds_read_u16_d16_hi v138,v138
v_fma_mix_f32 v143,v143,v121,0 op_sel:[0,1,0] op_sel_hi:[0,1,0]
v_mfma_f32_16x16x32_bf16 v[156:159],v[76:79],v[52:55],v[156:159]
ds_read_b64_tr_b16 v[106:107],v6 offset:46816
ds_read_b64_tr_b16 v[108:109],v6 offset:45288
v_mfma_f32_16x16x32_bf16 v[148:151],v[76:79],v[48:51],v[148:151]
ds_read_u16 v137,v137
v_mfma_f32_16x16x32_bf16 v[144:147],v[80:83],v[56:59],v[144:147]
ds_read_b64_tr_b16 v[110:111],v6 offset:46824
ds_read_b64_tr_b16 v[64:65],v8 offset:12320
v_mfma_f32_16x16x32_bf16 v[152:155],v[80:83],v[60:63],v[152:155]
s_waitcnt lgkmcnt(9)
ds_read_u16_d16_hi v139,v139
v_lshl_add_u64 v[128:129],v[128:129],0,v[130:131]
v_lshl_add_u64 v[130:131],v[132:133],0,v[134:135]
v_mfma_f32_16x16x32_bf16 v[156:159],v[84:87],v[60:63],v[156:159]
ds_read_u16 v140,v140
v_mfma_f32_16x16x32_bf16 v[148:151],v[84:87],v[56:59],v[148:151]
ds_read_b64_tr_b16 v[66:67],v8 offset:14368
ds_read_b128 v[112:115],v7 offset:0
v_lshlrev_b32_sdwa v144,1,v144 dst_sel:DWORD dst_unused:UNUSED_PAD src0_sel:DWORD src1_sel:WORD_1
v_lshlrev_b32_sdwa v146,1,v146 dst_sel:DWORD dst_unused:UNUSED_PAD src0_sel:DWORD src1_sel:WORD_1
v_mfma_f32_16x16x32_bf16 v[160:163],v[88:91],v[128:131],v[160:163]
ds_read_u16_d16_hi v142,v142
v_lshlrev_b32_sdwa v145,1,v145 dst_sel:DWORD dst_unused:UNUSED_PAD src0_sel:DWORD src1_sel:WORD_1
v_lshlrev_b32_sdwa v147,1,v147 dst_sel:DWORD dst_unused:UNUSED_PAD src0_sel:DWORD src1_sel:WORD_1
v_mfma_f32_16x16x32_bf16 v[168:171],v[92:95],v[128:131],v[168:171]
ds_read_b64_tr_b16 v[68:69],v9 offset:12320
ds_read_b64_tr_b16 v[70:71],v9 offset:14368
v_fma_mixlo_f16 v120,v157,s99,0
v_mfma_f32_16x16x32_bf16 v[176:179],v[96:99],v[128:131],v[176:179]
s_waitcnt lgkmcnt(6)
ds_read_u16 v144,v144
v_lshlrev_b32_sdwa v148,1,v148 dst_sel:DWORD dst_unused:UNUSED_PAD src0_sel:DWORD src1_sel:WORD_1
v_lshl_add_u64 v[136:137],v[136:137],0,v[138:139]
v_fma_mixhi_f16 v120,v159,s99,0
v_lshlrev_b32_sdwa v150,1,v150 dst_sel:DWORD dst_unused:UNUSED_PAD src0_sel:DWORD src1_sel:WORD_1
v_mfma_f32_16x16x32_bf16 v[184:187],v[100:103],v[128:131],v[184:187]
ds_read_u16_d16_hi v146,v146
v_mfma_f32_16x16x32_bf16 v[192:195],v[104:107],v[128:131],v[192:195]
s_waitcnt lgkmcnt(4)
ds_read_b128 v[116:119],v7 offset:32
ds_read_b64_tr_b16 v[72:73],v8 offset:16416
v_or_b32 v138,v142,v140
v_cvt_pk_bf16_f32 v139,v141,v143
v_mfma_f32_16x16x32_bf16 v[200:203],v[108:111],v[128:131],v[200:203]
ds_read_b64_tr_b16 v[74:75],v8 offset:18464
ds_read_b64_tr_b16 v[76:77],v9 offset:16416
v_pk_fma_f16 v121,v120,v120,s54
v_lshlrev_b32_sdwa v149,1,v149 dst_sel:DWORD dst_unused:UNUSED_PAD src0_sel:DWORD src1_sel:WORD_1
v_mfma_f32_16x16x32_bf16 v[164:167],v[88:91],v[136:139],v[164:167]
ds_read_u16 v145,v145
v_pk_mul_f16 v121,v120,v121
v_lshlrev_b32_sdwa v151,1,v151 dst_sel:DWORD dst_unused:UNUSED_PAD src0_sel:DWORD src1_sel:WORD_1
v_mfma_f32_16x16x32_bf16 v[172:175],v[92:95],v[136:139],v[172:175]
ds_read_u16_d16_hi v147,v147
v_lshlrev_b32_sdwa v152,1,v152 dst_sel:DWORD dst_unused:UNUSED_PAD src0_sel:DWORD src1_sel:WORD_1
v_exp_f16 v120,v121
v_mfma_f32_16x16x32_bf16 v[180:183],v[96:99],v[136:139],v[180:183]
ds_read_b64_tr_b16 v[78:79],v9 offset:18464
ds_read_b64_tr_b16 v[80:81],v8 offset:20512
v_mfma_f32_16x16x32_bf16 v[188:191],v[100:103],v[136:139],v[188:191]
v_exp_f16_sdwa v120,v121 dst_sel:WORD_1 dst_unused:UNUSED_PRESERVE src0_sel:WORD_1
v_mfma_f32_16x16x32_bf16 v[196:199],v[104:107],v[136:139],v[196:199]
ds_read_u16 v148,v148
ds_read_u16_d16_hi v150,v150
v_lshlrev_b32_sdwa v154,1,v154 dst_sel:DWORD dst_unused:UNUSED_PAD src0_sel:DWORD src1_sel:WORD_1
v_mfma_f32_16x16x32_bf16 v[204:207],v[108:111],v[136:139],v[204:207]
ds_read_b64_tr_b16 v[82:83],v8 offset:22560
v_pk_fma_f16 v120,v120,s55,s55
v_lshlrev_b32_sdwa v153,1,v153 dst_sel:DWORD dst_unused:UNUSED_PAD src0_sel:DWORD src1_sel:WORD_1
v_mfma_f32_16x16x32_bf16 v[128:131],v[64:67],v[16:19],v[112:115]
ds_read_u16 v149,v149
v_rcp_f16 v121,v120
v_mfma_f32_16x16x32_bf16 v[136:139],v[64:67],v[20:23],v[112:115]
s_waitcnt lgkmcnt(11)
ds_read_b64_tr_b16 v[84:85],v9 offset:20512
v_lshlrev_b32_sdwa v155,1,v155 dst_sel:DWORD dst_unused:UNUSED_PAD src0_sel:DWORD src1_sel:WORD_1
v_mfma_f32_16x16x32_bf16 v[140:143],v[68:71],v[20:23],v[116:119]
ds_read_u16_d16_hi v151,v151
v_rcp_f16_sdwa v121,v120 dst_sel:WORD_1 dst_unused:UNUSED_PRESERVE src0_sel:WORD_1
v_mfma_f32_16x16x32_bf16 v[132:135],v[68:71],v[16:19],v[116:119]
s_waitcnt lgkmcnt(11)
ds_read_u16 v152,v152
v_lshlrev_b32_sdwa v156,1,v156 dst_sel:DWORD dst_unused:UNUSED_PAD src0_sel:DWORD src1_sel:WORD_1
v_mfma_f32_16x16x32_bf16 v[128:131],v[72:75],v[24:27],v[128:131]
ds_read_b64_tr_b16 v[86:87],v9 offset:22560
v_fma_mix_f32 v157,v157,v121,0 op_sel_hi:[0,1,0]
v_mfma_f32_16x16x32_bf16 v[136:139],v[72:75],v[28:31],v[136:139]
s_waitcnt lgkmcnt(9)
ds_read_u16_d16_hi v154,v154
v_fma_mix_f32 v159,v159,v121,0 op_sel:[0,1,0] op_sel_hi:[0,1,0]
v_mfma_f32_16x16x32_bf16 v[140:143],v[76:79],v[28:31],v[140:143]
v_mfma_f32_16x16x32_bf16 v[132:135],v[76:79],v[24:27],v[132:135]
s_waitcnt lgkmcnt(6)
ds_read_u16 v153,v153
v_lshlrev_b32_sdwa v158,1,v158 dst_sel:DWORD dst_unused:UNUSED_PAD src0_sel:DWORD src1_sel:WORD_1
v_mfma_f32_16x16x32_bf16 v[128:131],v[80:83],v[32:35],v[128:131]
ds_read_u16_d16_hi v155,v155
v_mfma_f32_16x16x32_bf16 v[136:139],v[80:83],v[36:39],v[136:139]
s_waitcnt lgkmcnt(3)
v_lshl_add_u64 v[144:145],v[144:145],0,v[146:147]
v_mfma_f32_16x16x32_bf16 v[140:143],v[84:87],v[36:39],v[140:143]
ds_read_u16 v156,v156
v_lshl_add_u64 v[146:147],v[148:149],0,v[150:151]
v_mfma_f32_16x16x32_bf16 v[132:135],v[84:87],v[32:35],v[132:135]
v_lshlrev_b32_sdwa v128,1,v128 dst_sel:DWORD dst_unused:UNUSED_PAD src0_sel:DWORD src1_sel:WORD_1
v_lshlrev_b32_sdwa v130,1,v130 dst_sel:DWORD dst_unused:UNUSED_PAD src0_sel:DWORD src1_sel:WORD_1
v_mfma_f32_16x16x32_bf16 v[208:211],v[88:91],v[144:147],v[208:211]
ds_read_u16_d16_hi v158,v158
v_lshlrev_b32_sdwa v129,1,v129 dst_sel:DWORD dst_unused:UNUSED_PAD src0_sel:DWORD src1_sel:WORD_1
v_lshlrev_b32_sdwa v131,1,v131 dst_sel:DWORD dst_unused:UNUSED_PAD src0_sel:DWORD src1_sel:WORD_1
v_mfma_f32_16x16x32_bf16 v[216:219],v[92:95],v[144:147],v[216:219]
v_fma_mixlo_f16 v120,v141,s99,0
v_lshlrev_b32_sdwa v132,1,v132 dst_sel:DWORD dst_unused:UNUSED_PAD src0_sel:DWORD src1_sel:WORD_1
v_mfma_f32_16x16x32_bf16 v[224:227],v[96:99],v[144:147],v[224:227]
s_mov_b32 s86,0xbe801d5a
ds_read_u16 v128,v128
v_fma_mixhi_f16 v120,v143,s99,0
v_lshlrev_b32_sdwa v134,1,v134 dst_sel:DWORD dst_unused:UNUSED_PAD src0_sel:DWORD src1_sel:WORD_1
v_mfma_f32_16x16x32_bf16 v[232:235],v[100:103],v[144:147],v[232:235]
s_waitcnt lgkmcnt(3)
ds_read_u16_d16_hi v130,v130
v_lshl_add_u64 v[152:153],v[152:153],0,v[154:155]
v_mfma_f32_16x16x32_bf16 v[240:243],v[104:107],v[144:147],v[240:243]
s_waitcnt lgkmcnt(2)
ds_read_u16 v129,v129
v_or_b32 v154,v158,v156
v_cvt_pk_bf16_f32 v155,v157,v159
v_mfma_f32_16x16x32_bf16 v[248:251],v[108:111],v[144:147],v[248:251]
v_pk_fma_f16 v121,v120,v120,s54
v_lshlrev_b32_sdwa v133,1,v133 dst_sel:DWORD dst_unused:UNUSED_PAD src0_sel:DWORD src1_sel:WORD_1
v_mfma_f32_16x16x32_bf16 v[212:215],v[88:91],v[152:155],v[212:215]
ds_read_u16_d16_hi v131,v131
ds_read_b64_tr_b16 v[88:89],v6 offset:51312
v_pk_mul_f16 v121,v120,v121
v_lshlrev_b32_sdwa v135,1,v135 dst_sel:DWORD dst_unused:UNUSED_PAD src0_sel:DWORD src1_sel:WORD_1
v_mfma_f32_16x16x32_bf16 v[220:223],v[92:95],v[152:155],v[220:223]
ds_read_u16 v132,v132
v_exp_f16 v120,v121
v_mfma_f32_16x16x32_bf16 v[228:231],v[96:99],v[152:155],v[228:231]
ds_read_b64_tr_b16 v[90:91],v6 offset:52848
ds_read_b64_tr_b16 v[92:93],v6 offset:51320
v_lshlrev_b32_sdwa v136,1,v136 dst_sel:DWORD dst_unused:UNUSED_PAD src0_sel:DWORD src1_sel:WORD_1
v_mfma_f32_16x16x32_bf16 v[236:239],v[100:103],v[152:155],v[236:239]
ds_read_u16_d16_hi v134,v134
v_exp_f16_sdwa v120,v121 dst_sel:WORD_1 dst_unused:UNUSED_PRESERVE src0_sel:WORD_1
v_mfma_f32_16x16x32_bf16 v[244:247],v[104:107],v[152:155],v[244:247]
v_lshlrev_b32_sdwa v138,1,v138 dst_sel:DWORD dst_unused:UNUSED_PAD src0_sel:DWORD src1_sel:WORD_1
v_mfma_f32_16x16x32_bf16 v[252:255],v[108:111],v[152:155],v[252:255]
ds_read_b64_tr_b16 v[94:95],v6 offset:52856
ds_read_b64_tr_b16 v[96:97],v6 offset:51376
v_pk_fma_f16 v120,v120,s55,s55
v_lshlrev_b32_sdwa v137,1,v137 dst_sel:DWORD dst_unused:UNUSED_PAD src0_sel:DWORD src1_sel:WORD_1
v_mfma_f32_16x16x32_bf16 v[144:147],v[64:67],v[40:43],v[112:115]
ds_read_u16 v133,v133
v_rcp_f16 v121,v120
v_mfma_f32_16x16x32_bf16 v[152:155],v[64:67],v[44:47],v[112:115]
ds_read_b64_tr_b16 v[98:99],v6 offset:52912
ds_read_b64_tr_b16 v[100:101],v6 offset:51384
v_lshlrev_b32_sdwa v139,1,v139 dst_sel:DWORD dst_unused:UNUSED_PAD src0_sel:DWORD src1_sel:WORD_1
v_mfma_f32_16x16x32_bf16 v[156:159],v[68:71],v[44:47],v[116:119]
ds_read_u16_d16_hi v135,v135
v_rcp_f16_sdwa v121,v120 dst_sel:WORD_1 dst_unused:UNUSED_PRESERVE src0_sel:WORD_1
v_mfma_f32_16x16x32_bf16 v[148:151],v[68:71],v[40:43],v[116:119]
ds_read_u16 v136,v136
v_lshlrev_b32_sdwa v140,1,v140 dst_sel:DWORD dst_unused:UNUSED_PAD src0_sel:DWORD src1_sel:WORD_1
v_mfma_f32_16x16x32_bf16 v[144:147],v[72:75],v[48:51],v[144:147]
ds_read_b64_tr_b16 v[102:103],v6 offset:52920
ds_read_b64_tr_b16 v[104:105],v6 offset:51440
v_fma_mix_f32 v141,v141,v121,0 op_sel_hi:[0,1,0]
v_lshlrev_b32_sdwa v142,1,v142 dst_sel:DWORD dst_unused:UNUSED_PAD src0_sel:DWORD src1_sel:WORD_1
v_mfma_f32_16x16x32_bf16 v[152:155],v[72:75],v[52:55],v[152:155]
ds_read_u16_d16_hi v138,v138
v_fma_mix_f32 v143,v143,v121,0 op_sel:[0,1,0] op_sel_hi:[0,1,0]
v_mfma_f32_16x16x32_bf16 v[156:159],v[76:79],v[52:55],v[156:159]
ds_read_b64_tr_b16 v[106:107],v6 offset:52976
ds_read_b64_tr_b16 v[108:109],v6 offset:51448
v_mfma_f32_16x16x32_bf16 v[148:151],v[76:79],v[48:51],v[148:151]
ds_read_u16 v137,v137
v_mfma_f32_16x16x32_bf16 v[144:147],v[80:83],v[56:59],v[144:147]
ds_read_b64_tr_b16 v[110:111],v6 offset:52984
ds_read_b64_tr_b16 v[64:65],v8 offset:12832
v_mfma_f32_16x16x32_bf16 v[152:155],v[80:83],v[60:63],v[152:155]
s_waitcnt lgkmcnt(9)
ds_read_u16_d16_hi v139,v139
v_lshl_add_u64 v[128:129],v[128:129],0,v[130:131]
v_lshl_add_u64 v[130:131],v[132:133],0,v[134:135]
v_mfma_f32_16x16x32_bf16 v[156:159],v[84:87],v[60:63],v[156:159]
ds_read_u16 v140,v140
v_mfma_f32_16x16x32_bf16 v[148:151],v[84:87],v[56:59],v[148:151]
ds_read_b64_tr_b16 v[66:67],v8 offset:14880
ds_read_b128 v[112:115],v7 offset:128
v_lshlrev_b32_sdwa v144,1,v144 dst_sel:DWORD dst_unused:UNUSED_PAD src0_sel:DWORD src1_sel:WORD_1
v_lshlrev_b32_sdwa v146,1,v146 dst_sel:DWORD dst_unused:UNUSED_PAD src0_sel:DWORD src1_sel:WORD_1
v_mfma_f32_16x16x32_bf16 v[160:163],v[88:91],v[128:131],v[160:163]
ds_read_u16_d16_hi v142,v142
v_lshlrev_b32_sdwa v145,1,v145 dst_sel:DWORD dst_unused:UNUSED_PAD src0_sel:DWORD src1_sel:WORD_1
v_lshlrev_b32_sdwa v147,1,v147 dst_sel:DWORD dst_unused:UNUSED_PAD src0_sel:DWORD src1_sel:WORD_1
v_mfma_f32_16x16x32_bf16 v[168:171],v[92:95],v[128:131],v[168:171]
ds_read_b64_tr_b16 v[68:69],v9 offset:12832
ds_read_b64_tr_b16 v[70:71],v9 offset:14880
v_fma_mixlo_f16 v120,v157,s99,0
v_mfma_f32_16x16x32_bf16 v[176:179],v[96:99],v[128:131],v[176:179]
s_waitcnt lgkmcnt(6)
ds_read_u16 v144,v144
v_lshlrev_b32_sdwa v148,1,v148 dst_sel:DWORD dst_unused:UNUSED_PAD src0_sel:DWORD src1_sel:WORD_1
v_lshl_add_u64 v[136:137],v[136:137],0,v[138:139]
v_fma_mixhi_f16 v120,v159,s99,0
v_lshlrev_b32_sdwa v150,1,v150 dst_sel:DWORD dst_unused:UNUSED_PAD src0_sel:DWORD src1_sel:WORD_1
v_mfma_f32_16x16x32_bf16 v[184:187],v[100:103],v[128:131],v[184:187]
ds_read_u16_d16_hi v146,v146
v_mfma_f32_16x16x32_bf16 v[192:195],v[104:107],v[128:131],v[192:195]
s_waitcnt lgkmcnt(4)
ds_read_b128 v[116:119],v7 offset:160
ds_read_b64_tr_b16 v[72:73],v8 offset:16928
v_or_b32 v138,v142,v140
v_cvt_pk_bf16_f32 v139,v141,v143
v_mfma_f32_16x16x32_bf16 v[200:203],v[108:111],v[128:131],v[200:203]
ds_read_b64_tr_b16 v[74:75],v8 offset:18976
ds_read_b64_tr_b16 v[76:77],v9 offset:16928
v_pk_fma_f16 v121,v120,v120,s54
v_lshlrev_b32_sdwa v149,1,v149 dst_sel:DWORD dst_unused:UNUSED_PAD src0_sel:DWORD src1_sel:WORD_1
v_mfma_f32_16x16x32_bf16 v[164:167],v[88:91],v[136:139],v[164:167]
ds_read_u16 v145,v145
v_pk_mul_f16 v121,v120,v121
v_lshlrev_b32_sdwa v151,1,v151 dst_sel:DWORD dst_unused:UNUSED_PAD src0_sel:DWORD src1_sel:WORD_1
v_mfma_f32_16x16x32_bf16 v[172:175],v[92:95],v[136:139],v[172:175]
ds_read_u16_d16_hi v147,v147
v_lshlrev_b32_sdwa v152,1,v152 dst_sel:DWORD dst_unused:UNUSED_PAD src0_sel:DWORD src1_sel:WORD_1
v_exp_f16 v120,v121
v_mfma_f32_16x16x32_bf16 v[180:183],v[96:99],v[136:139],v[180:183]
ds_read_b64_tr_b16 v[78:79],v9 offset:18976
ds_read_b64_tr_b16 v[80:81],v8 offset:21024
v_mfma_f32_16x16x32_bf16 v[188:191],v[100:103],v[136:139],v[188:191]
v_exp_f16_sdwa v120,v121 dst_sel:WORD_1 dst_unused:UNUSED_PRESERVE src0_sel:WORD_1
v_mfma_f32_16x16x32_bf16 v[196:199],v[104:107],v[136:139],v[196:199]
ds_read_u16 v148,v148
ds_read_u16_d16_hi v150,v150
v_lshlrev_b32_sdwa v154,1,v154 dst_sel:DWORD dst_unused:UNUSED_PAD src0_sel:DWORD src1_sel:WORD_1
v_mfma_f32_16x16x32_bf16 v[204:207],v[108:111],v[136:139],v[204:207]
ds_read_b64_tr_b16 v[82:83],v8 offset:23072
v_pk_fma_f16 v120,v120,s55,s55
v_lshlrev_b32_sdwa v153,1,v153 dst_sel:DWORD dst_unused:UNUSED_PAD src0_sel:DWORD src1_sel:WORD_1
v_mfma_f32_16x16x32_bf16 v[128:131],v[64:67],v[16:19],v[112:115]
ds_read_u16 v149,v149
v_rcp_f16 v121,v120
v_mfma_f32_16x16x32_bf16 v[136:139],v[64:67],v[20:23],v[112:115]
s_waitcnt lgkmcnt(11)
ds_read_b64_tr_b16 v[84:85],v9 offset:21024
v_lshlrev_b32_sdwa v155,1,v155 dst_sel:DWORD dst_unused:UNUSED_PAD src0_sel:DWORD src1_sel:WORD_1
v_mfma_f32_16x16x32_bf16 v[140:143],v[68:71],v[20:23],v[116:119]
ds_read_u16_d16_hi v151,v151
v_rcp_f16_sdwa v121,v120 dst_sel:WORD_1 dst_unused:UNUSED_PRESERVE src0_sel:WORD_1
v_mfma_f32_16x16x32_bf16 v[132:135],v[68:71],v[16:19],v[116:119]
s_waitcnt lgkmcnt(11)
ds_read_u16 v152,v152
v_lshlrev_b32_sdwa v156,1,v156 dst_sel:DWORD dst_unused:UNUSED_PAD src0_sel:DWORD src1_sel:WORD_1
v_mfma_f32_16x16x32_bf16 v[128:131],v[72:75],v[24:27],v[128:131]
ds_read_b64_tr_b16 v[86:87],v9 offset:23072
v_fma_mix_f32 v157,v157,v121,0 op_sel_hi:[0,1,0]
v_mfma_f32_16x16x32_bf16 v[136:139],v[72:75],v[28:31],v[136:139]
s_waitcnt lgkmcnt(9)
ds_read_u16_d16_hi v154,v154
v_fma_mix_f32 v159,v159,v121,0 op_sel:[0,1,0] op_sel_hi:[0,1,0]
v_mfma_f32_16x16x32_bf16 v[140:143],v[76:79],v[28:31],v[140:143]
v_mfma_f32_16x16x32_bf16 v[132:135],v[76:79],v[24:27],v[132:135]
s_waitcnt lgkmcnt(6)
ds_read_u16 v153,v153
v_lshlrev_b32_sdwa v158,1,v158 dst_sel:DWORD dst_unused:UNUSED_PAD src0_sel:DWORD src1_sel:WORD_1
v_mfma_f32_16x16x32_bf16 v[128:131],v[80:83],v[32:35],v[128:131]
ds_read_u16_d16_hi v155,v155
v_mfma_f32_16x16x32_bf16 v[136:139],v[80:83],v[36:39],v[136:139]
s_waitcnt lgkmcnt(3)
v_lshl_add_u64 v[144:145],v[144:145],0,v[146:147]
v_mfma_f32_16x16x32_bf16 v[140:143],v[84:87],v[36:39],v[140:143]
ds_read_u16 v156,v156
v_lshl_add_u64 v[146:147],v[148:149],0,v[150:151]
v_mfma_f32_16x16x32_bf16 v[132:135],v[84:87],v[32:35],v[132:135]
v_lshlrev_b32_sdwa v128,1,v128 dst_sel:DWORD dst_unused:UNUSED_PAD src0_sel:DWORD src1_sel:WORD_1
v_lshlrev_b32_sdwa v130,1,v130 dst_sel:DWORD dst_unused:UNUSED_PAD src0_sel:DWORD src1_sel:WORD_1
v_mfma_f32_16x16x32_bf16 v[208:211],v[88:91],v[144:147],v[208:211]
ds_read_u16_d16_hi v158,v158
v_lshlrev_b32_sdwa v129,1,v129 dst_sel:DWORD dst_unused:UNUSED_PAD src0_sel:DWORD src1_sel:WORD_1
v_lshlrev_b32_sdwa v131,1,v131 dst_sel:DWORD dst_unused:UNUSED_PAD src0_sel:DWORD src1_sel:WORD_1
v_mfma_f32_16x16x32_bf16 v[216:219],v[92:95],v[144:147],v[216:219]
v_fma_mixlo_f16 v120,v141,s99,0
v_lshlrev_b32_sdwa v132,1,v132 dst_sel:DWORD dst_unused:UNUSED_PAD src0_sel:DWORD src1_sel:WORD_1
v_mfma_f32_16x16x32_bf16 v[224:227],v[96:99],v[144:147],v[224:227]
ds_read_u16 v128,v128
v_fma_mixhi_f16 v120,v143,s99,0
v_lshlrev_b32_sdwa v134,1,v134 dst_sel:DWORD dst_unused:UNUSED_PAD src0_sel:DWORD src1_sel:WORD_1
v_mfma_f32_16x16x32_bf16 v[232:235],v[100:103],v[144:147],v[232:235]
s_waitcnt lgkmcnt(3)
ds_read_u16_d16_hi v130,v130
v_lshl_add_u64 v[152:153],v[152:153],0,v[154:155]
v_mfma_f32_16x16x32_bf16 v[240:243],v[104:107],v[144:147],v[240:243]
s_waitcnt lgkmcnt(2)
ds_read_u16 v129,v129
v_or_b32 v154,v158,v156
v_cvt_pk_bf16_f32 v155,v157,v159
v_mfma_f32_16x16x32_bf16 v[248:251],v[108:111],v[144:147],v[248:251]
v_pk_fma_f16 v121,v120,v120,s54
v_lshlrev_b32_sdwa v133,1,v133 dst_sel:DWORD dst_unused:UNUSED_PAD src0_sel:DWORD src1_sel:WORD_1
v_mfma_f32_16x16x32_bf16 v[212:215],v[88:91],v[152:155],v[212:215]
ds_read_u16_d16_hi v131,v131
ds_read_b64_tr_b16 v[88:89],v6 offset:57472
v_pk_mul_f16 v121,v120,v121
v_lshlrev_b32_sdwa v135,1,v135 dst_sel:DWORD dst_unused:UNUSED_PAD src0_sel:DWORD src1_sel:WORD_1
v_mfma_f32_16x16x32_bf16 v[220:223],v[92:95],v[152:155],v[220:223]
ds_read_u16 v132,v132
v_exp_f16 v120,v121
v_mfma_f32_16x16x32_bf16 v[228:231],v[96:99],v[152:155],v[228:231]
ds_read_b64_tr_b16 v[90:91],v6 offset:59008
ds_read_b64_tr_b16 v[92:93],v6 offset:57480
v_lshlrev_b32_sdwa v136,1,v136 dst_sel:DWORD dst_unused:UNUSED_PAD src0_sel:DWORD src1_sel:WORD_1
v_mfma_f32_16x16x32_bf16 v[236:239],v[100:103],v[152:155],v[236:239]
ds_read_u16_d16_hi v134,v134
v_exp_f16_sdwa v120,v121 dst_sel:WORD_1 dst_unused:UNUSED_PRESERVE src0_sel:WORD_1
v_mfma_f32_16x16x32_bf16 v[244:247],v[104:107],v[152:155],v[244:247]
v_lshlrev_b32_sdwa v138,1,v138 dst_sel:DWORD dst_unused:UNUSED_PAD src0_sel:DWORD src1_sel:WORD_1
v_mfma_f32_16x16x32_bf16 v[252:255],v[108:111],v[152:155],v[252:255]
ds_read_b64_tr_b16 v[94:95],v6 offset:59016
ds_read_b64_tr_b16 v[96:97],v6 offset:57536
v_pk_fma_f16 v120,v120,s55,s55
v_lshlrev_b32_sdwa v137,1,v137 dst_sel:DWORD dst_unused:UNUSED_PAD src0_sel:DWORD src1_sel:WORD_1
v_mfma_f32_16x16x32_bf16 v[144:147],v[64:67],v[40:43],v[112:115]
ds_read_u16 v133,v133
v_rcp_f16 v121,v120
v_mfma_f32_16x16x32_bf16 v[152:155],v[64:67],v[44:47],v[112:115]
ds_read_b64_tr_b16 v[98:99],v6 offset:59072
ds_read_b64_tr_b16 v[100:101],v6 offset:57544
v_lshlrev_b32_sdwa v139,1,v139 dst_sel:DWORD dst_unused:UNUSED_PAD src0_sel:DWORD src1_sel:WORD_1
v_mfma_f32_16x16x32_bf16 v[156:159],v[68:71],v[44:47],v[116:119]
ds_read_u16_d16_hi v135,v135
v_rcp_f16_sdwa v121,v120 dst_sel:WORD_1 dst_unused:UNUSED_PRESERVE src0_sel:WORD_1
v_mfma_f32_16x16x32_bf16 v[148:151],v[68:71],v[40:43],v[116:119]
ds_read_u16 v136,v136
v_lshlrev_b32_sdwa v140,1,v140 dst_sel:DWORD dst_unused:UNUSED_PAD src0_sel:DWORD src1_sel:WORD_1
v_mfma_f32_16x16x32_bf16 v[144:147],v[72:75],v[48:51],v[144:147]
ds_read_b64_tr_b16 v[102:103],v6 offset:59080
ds_read_b64_tr_b16 v[104:105],v6 offset:57600
v_fma_mix_f32 v141,v141,v121,0 op_sel_hi:[0,1,0]
v_lshlrev_b32_sdwa v142,1,v142 dst_sel:DWORD dst_unused:UNUSED_PAD src0_sel:DWORD src1_sel:WORD_1
v_mfma_f32_16x16x32_bf16 v[152:155],v[72:75],v[52:55],v[152:155]
ds_read_u16_d16_hi v138,v138
v_fma_mix_f32 v143,v143,v121,0 op_sel:[0,1,0] op_sel_hi:[0,1,0]
v_mfma_f32_16x16x32_bf16 v[156:159],v[76:79],v[52:55],v[156:159]
ds_read_b64_tr_b16 v[106:107],v6 offset:59136
ds_read_b64_tr_b16 v[108:109],v6 offset:57608
v_mfma_f32_16x16x32_bf16 v[148:151],v[76:79],v[48:51],v[148:151]
ds_read_u16 v137,v137
v_mfma_f32_16x16x32_bf16 v[144:147],v[80:83],v[56:59],v[144:147]
ds_read_b64_tr_b16 v[110:111],v6 offset:59144
ds_read_b64_tr_b16 v[64:65],v8 offset:24640
v_mfma_f32_16x16x32_bf16 v[152:155],v[80:83],v[60:63],v[152:155]
s_waitcnt lgkmcnt(9)
ds_read_u16_d16_hi v139,v139
v_lshl_add_u64 v[128:129],v[128:129],0,v[130:131]
v_lshl_add_u64 v[130:131],v[132:133],0,v[134:135]
v_mfma_f32_16x16x32_bf16 v[156:159],v[84:87],v[60:63],v[156:159]
ds_read_u16 v140,v140
v_mfma_f32_16x16x32_bf16 v[148:151],v[84:87],v[56:59],v[148:151]
ds_read_b64_tr_b16 v[66:67],v8 offset:26688
ds_read_b128 v[112:115],v7 offset:256
v_lshlrev_b32_sdwa v144,1,v144 dst_sel:DWORD dst_unused:UNUSED_PAD src0_sel:DWORD src1_sel:WORD_1
v_lshlrev_b32_sdwa v146,1,v146 dst_sel:DWORD dst_unused:UNUSED_PAD src0_sel:DWORD src1_sel:WORD_1
v_mfma_f32_16x16x32_bf16 v[160:163],v[88:91],v[128:131],v[160:163]
ds_read_u16_d16_hi v142,v142
v_lshlrev_b32_sdwa v145,1,v145 dst_sel:DWORD dst_unused:UNUSED_PAD src0_sel:DWORD src1_sel:WORD_1
v_lshlrev_b32_sdwa v147,1,v147 dst_sel:DWORD dst_unused:UNUSED_PAD src0_sel:DWORD src1_sel:WORD_1
v_mfma_f32_16x16x32_bf16 v[168:171],v[92:95],v[128:131],v[168:171]
ds_read_b64_tr_b16 v[68:69],v9 offset:24640
ds_read_b64_tr_b16 v[70:71],v9 offset:26688
v_fma_mixlo_f16 v120,v157,s99,0
v_mfma_f32_16x16x32_bf16 v[176:179],v[96:99],v[128:131],v[176:179]
s_waitcnt lgkmcnt(6)
ds_read_u16 v144,v144
v_lshlrev_b32_sdwa v148,1,v148 dst_sel:DWORD dst_unused:UNUSED_PAD src0_sel:DWORD src1_sel:WORD_1
v_lshl_add_u64 v[136:137],v[136:137],0,v[138:139]
v_fma_mixhi_f16 v120,v159,s99,0
v_lshlrev_b32_sdwa v150,1,v150 dst_sel:DWORD dst_unused:UNUSED_PAD src0_sel:DWORD src1_sel:WORD_1
v_mfma_f32_16x16x32_bf16 v[184:187],v[100:103],v[128:131],v[184:187]
ds_read_u16_d16_hi v146,v146
v_mfma_f32_16x16x32_bf16 v[192:195],v[104:107],v[128:131],v[192:195]
s_waitcnt lgkmcnt(4)
ds_read_b128 v[116:119],v7 offset:288
ds_read_b64_tr_b16 v[72:73],v8 offset:28736
v_or_b32 v138,v142,v140
v_cvt_pk_bf16_f32 v139,v141,v143
v_mfma_f32_16x16x32_bf16 v[200:203],v[108:111],v[128:131],v[200:203]
ds_read_b64_tr_b16 v[74:75],v8 offset:30784
ds_read_b64_tr_b16 v[76:77],v9 offset:28736
v_pk_fma_f16 v121,v120,v120,s54
v_lshlrev_b32_sdwa v149,1,v149 dst_sel:DWORD dst_unused:UNUSED_PAD src0_sel:DWORD src1_sel:WORD_1
v_mfma_f32_16x16x32_bf16 v[164:167],v[88:91],v[136:139],v[164:167]
ds_read_u16 v145,v145
v_pk_mul_f16 v121,v120,v121
v_lshlrev_b32_sdwa v151,1,v151 dst_sel:DWORD dst_unused:UNUSED_PAD src0_sel:DWORD src1_sel:WORD_1
v_mfma_f32_16x16x32_bf16 v[172:175],v[92:95],v[136:139],v[172:175]
ds_read_u16_d16_hi v147,v147
v_lshlrev_b32_sdwa v152,1,v152 dst_sel:DWORD dst_unused:UNUSED_PAD src0_sel:DWORD src1_sel:WORD_1
v_exp_f16 v120,v121
v_mfma_f32_16x16x32_bf16 v[180:183],v[96:99],v[136:139],v[180:183]
ds_read_b64_tr_b16 v[78:79],v9 offset:30784
ds_read_b64_tr_b16 v[80:81],v8 offset:32832
v_mfma_f32_16x16x32_bf16 v[188:191],v[100:103],v[136:139],v[188:191]
v_exp_f16_sdwa v120,v121 dst_sel:WORD_1 dst_unused:UNUSED_PRESERVE src0_sel:WORD_1
v_mfma_f32_16x16x32_bf16 v[196:199],v[104:107],v[136:139],v[196:199]
ds_read_u16 v148,v148
ds_read_u16_d16_hi v150,v150
v_lshlrev_b32_sdwa v154,1,v154 dst_sel:DWORD dst_unused:UNUSED_PAD src0_sel:DWORD src1_sel:WORD_1
v_mfma_f32_16x16x32_bf16 v[204:207],v[108:111],v[136:139],v[204:207]
ds_read_b64_tr_b16 v[82:83],v8 offset:34880
v_pk_fma_f16 v120,v120,s55,s55
v_lshlrev_b32_sdwa v153,1,v153 dst_sel:DWORD dst_unused:UNUSED_PAD src0_sel:DWORD src1_sel:WORD_1
v_mfma_f32_16x16x32_bf16 v[128:131],v[64:67],v[16:19],v[112:115]
ds_read_u16 v149,v149
v_rcp_f16 v121,v120
v_mfma_f32_16x16x32_bf16 v[136:139],v[64:67],v[20:23],v[112:115]
s_waitcnt lgkmcnt(11)
ds_read_b64_tr_b16 v[84:85],v9 offset:32832
v_lshlrev_b32_sdwa v155,1,v155 dst_sel:DWORD dst_unused:UNUSED_PAD src0_sel:DWORD src1_sel:WORD_1
v_mfma_f32_16x16x32_bf16 v[140:143],v[68:71],v[20:23],v[116:119]
ds_read_u16_d16_hi v151,v151
v_rcp_f16_sdwa v121,v120 dst_sel:WORD_1 dst_unused:UNUSED_PRESERVE src0_sel:WORD_1
v_mfma_f32_16x16x32_bf16 v[132:135],v[68:71],v[16:19],v[116:119]
s_waitcnt lgkmcnt(11)
ds_read_u16 v152,v152
v_lshlrev_b32_sdwa v156,1,v156 dst_sel:DWORD dst_unused:UNUSED_PAD src0_sel:DWORD src1_sel:WORD_1
v_mfma_f32_16x16x32_bf16 v[128:131],v[72:75],v[24:27],v[128:131]
ds_read_b64_tr_b16 v[86:87],v9 offset:34880
v_fma_mix_f32 v157,v157,v121,0 op_sel_hi:[0,1,0]
v_mfma_f32_16x16x32_bf16 v[136:139],v[72:75],v[28:31],v[136:139]
s_waitcnt lgkmcnt(9)
ds_read_u16_d16_hi v154,v154
v_fma_mix_f32 v159,v159,v121,0 op_sel:[0,1,0] op_sel_hi:[0,1,0]
v_mfma_f32_16x16x32_bf16 v[140:143],v[76:79],v[28:31],v[140:143]
v_mfma_f32_16x16x32_bf16 v[132:135],v[76:79],v[24:27],v[132:135]
s_waitcnt lgkmcnt(6)
ds_read_u16 v153,v153
v_lshlrev_b32_sdwa v158,1,v158 dst_sel:DWORD dst_unused:UNUSED_PAD src0_sel:DWORD src1_sel:WORD_1
v_mfma_f32_16x16x32_bf16 v[128:131],v[80:83],v[32:35],v[128:131]
ds_read_u16_d16_hi v155,v155
v_mfma_f32_16x16x32_bf16 v[136:139],v[80:83],v[36:39],v[136:139]
s_waitcnt lgkmcnt(3)
v_lshl_add_u64 v[144:145],v[144:145],0,v[146:147]
v_mfma_f32_16x16x32_bf16 v[140:143],v[84:87],v[36:39],v[140:143]
ds_read_u16 v156,v156
v_lshl_add_u64 v[146:147],v[148:149],0,v[150:151]
v_mfma_f32_16x16x32_bf16 v[132:135],v[84:87],v[32:35],v[132:135]
v_lshlrev_b32_sdwa v128,1,v128 dst_sel:DWORD dst_unused:UNUSED_PAD src0_sel:DWORD src1_sel:WORD_1
v_lshlrev_b32_sdwa v130,1,v130 dst_sel:DWORD dst_unused:UNUSED_PAD src0_sel:DWORD src1_sel:WORD_1
v_mfma_f32_16x16x32_bf16 v[208:211],v[88:91],v[144:147],v[208:211]
ds_read_u16_d16_hi v158,v158
v_lshlrev_b32_sdwa v129,1,v129 dst_sel:DWORD dst_unused:UNUSED_PAD src0_sel:DWORD src1_sel:WORD_1
v_lshlrev_b32_sdwa v131,1,v131 dst_sel:DWORD dst_unused:UNUSED_PAD src0_sel:DWORD src1_sel:WORD_1
v_mfma_f32_16x16x32_bf16 v[216:219],v[92:95],v[144:147],v[216:219]
v_fma_mixlo_f16 v120,v141,s99,0
v_lshlrev_b32_sdwa v132,1,v132 dst_sel:DWORD dst_unused:UNUSED_PAD src0_sel:DWORD src1_sel:WORD_1
v_mfma_f32_16x16x32_bf16 v[224:227],v[96:99],v[144:147],v[224:227]
ds_read_u16 v128,v128
v_fma_mixhi_f16 v120,v143,s99,0
v_lshlrev_b32_sdwa v134,1,v134 dst_sel:DWORD dst_unused:UNUSED_PAD src0_sel:DWORD src1_sel:WORD_1
v_mfma_f32_16x16x32_bf16 v[232:235],v[100:103],v[144:147],v[232:235]
s_waitcnt lgkmcnt(3)
ds_read_u16_d16_hi v130,v130
v_lshl_add_u64 v[152:153],v[152:153],0,v[154:155]
v_mfma_f32_16x16x32_bf16 v[240:243],v[104:107],v[144:147],v[240:243]
s_waitcnt lgkmcnt(2)
ds_read_u16 v129,v129
v_or_b32 v154,v158,v156
v_cvt_pk_bf16_f32 v155,v157,v159
v_mfma_f32_16x16x32_bf16 v[248:251],v[108:111],v[144:147],v[248:251]
v_pk_fma_f16 v121,v120,v120,s54
v_lshlrev_b32_sdwa v133,1,v133 dst_sel:DWORD dst_unused:UNUSED_PAD src0_sel:DWORD src1_sel:WORD_1
v_mfma_f32_16x16x32_bf16 v[212:215],v[88:91],v[152:155],v[212:215]
ds_read_u16_d16_hi v131,v131
ds_read_b64_tr_b16 v[88:89],v6 offset:63632
v_pk_mul_f16 v121,v120,v121
v_lshlrev_b32_sdwa v135,1,v135 dst_sel:DWORD dst_unused:UNUSED_PAD src0_sel:DWORD src1_sel:WORD_1
v_mfma_f32_16x16x32_bf16 v[220:223],v[92:95],v[152:155],v[220:223]
ds_read_u16 v132,v132
v_exp_f16 v120,v121
v_mfma_f32_16x16x32_bf16 v[228:231],v[96:99],v[152:155],v[228:231]
ds_read_b64_tr_b16 v[90:91],v6 offset:65168
ds_read_b64_tr_b16 v[92:93],v6 offset:63640
v_lshlrev_b32_sdwa v136,1,v136 dst_sel:DWORD dst_unused:UNUSED_PAD src0_sel:DWORD src1_sel:WORD_1
v_mfma_f32_16x16x32_bf16 v[236:239],v[100:103],v[152:155],v[236:239]
ds_read_u16_d16_hi v134,v134
v_exp_f16_sdwa v120,v121 dst_sel:WORD_1 dst_unused:UNUSED_PRESERVE src0_sel:WORD_1
v_mfma_f32_16x16x32_bf16 v[244:247],v[104:107],v[152:155],v[244:247]
v_lshlrev_b32_sdwa v138,1,v138 dst_sel:DWORD dst_unused:UNUSED_PAD src0_sel:DWORD src1_sel:WORD_1
v_mfma_f32_16x16x32_bf16 v[252:255],v[108:111],v[152:155],v[252:255]
ds_read_b64_tr_b16 v[94:95],v6 offset:65176
ds_read_b64_tr_b16 v[96:97],v6 offset:63696
v_pk_fma_f16 v120,v120,s55,s55
v_lshlrev_b32_sdwa v137,1,v137 dst_sel:DWORD dst_unused:UNUSED_PAD src0_sel:DWORD src1_sel:WORD_1
v_mfma_f32_16x16x32_bf16 v[144:147],v[64:67],v[40:43],v[112:115]
ds_read_u16 v133,v133
v_rcp_f16 v121,v120
v_mfma_f32_16x16x32_bf16 v[152:155],v[64:67],v[44:47],v[112:115]
ds_read_b64_tr_b16 v[98:99],v6 offset:65232
ds_read_b64_tr_b16 v[100:101],v6 offset:63704
v_lshlrev_b32_sdwa v139,1,v139 dst_sel:DWORD dst_unused:UNUSED_PAD src0_sel:DWORD src1_sel:WORD_1
v_mfma_f32_16x16x32_bf16 v[156:159],v[68:71],v[44:47],v[116:119]
ds_read_u16_d16_hi v135,v135
v_rcp_f16_sdwa v121,v120 dst_sel:WORD_1 dst_unused:UNUSED_PRESERVE src0_sel:WORD_1
v_mfma_f32_16x16x32_bf16 v[148:151],v[68:71],v[40:43],v[116:119]
ds_read_u16 v136,v136
v_lshlrev_b32_sdwa v140,1,v140 dst_sel:DWORD dst_unused:UNUSED_PAD src0_sel:DWORD src1_sel:WORD_1
v_mfma_f32_16x16x32_bf16 v[144:147],v[72:75],v[48:51],v[144:147]
ds_read_b64_tr_b16 v[102:103],v6 offset:65240
ds_read_b64_tr_b16 v[104:105],v6 offset:63760
v_fma_mix_f32 v141,v141,v121,0 op_sel_hi:[0,1,0]
v_lshlrev_b32_sdwa v142,1,v142 dst_sel:DWORD dst_unused:UNUSED_PAD src0_sel:DWORD src1_sel:WORD_1
v_mfma_f32_16x16x32_bf16 v[152:155],v[72:75],v[52:55],v[152:155]
ds_read_u16_d16_hi v138,v138
v_fma_mix_f32 v143,v143,v121,0 op_sel:[0,1,0] op_sel_hi:[0,1,0]
v_mfma_f32_16x16x32_bf16 v[156:159],v[76:79],v[52:55],v[156:159]
ds_read_b64_tr_b16 v[106:107],v6 offset:65296
ds_read_b64_tr_b16 v[108:109],v6 offset:63768
v_mfma_f32_16x16x32_bf16 v[148:151],v[76:79],v[48:51],v[148:151]
ds_read_u16 v137,v137
v_mfma_f32_16x16x32_bf16 v[144:147],v[80:83],v[56:59],v[144:147]
ds_read_b64_tr_b16 v[110:111],v6 offset:65304
ds_read_b128 v[64:67],v7 offset:1536
v_mfma_f32_16x16x32_bf16 v[152:155],v[80:83],v[60:63],v[152:155]
s_waitcnt lgkmcnt(9)
ds_read_u16_d16_hi v139,v139
v_lshl_add_u64 v[128:129],v[128:129],0,v[130:131]
v_lshl_add_u64 v[130:131],v[132:133],0,v[134:135]
v_mfma_f32_16x16x32_bf16 v[156:159],v[84:87],v[60:63],v[156:159]
ds_read_u16 v140,v140
v_mfma_f32_16x16x32_bf16 v[148:151],v[84:87],v[56:59],v[148:151]
ds_read_b128 v[68:71],v7 offset:1568
ds_read_b128 v[72:75],v7 offset:1664
v_lshlrev_b32_sdwa v144,1,v144 dst_sel:DWORD dst_unused:UNUSED_PAD src0_sel:DWORD src1_sel:WORD_1
v_lshlrev_b32_sdwa v146,1,v146 dst_sel:DWORD dst_unused:UNUSED_PAD src0_sel:DWORD src1_sel:WORD_1
v_mfma_f32_16x16x32_bf16 v[160:163],v[88:91],v[128:131],v[160:163]
ds_read_u16_d16_hi v142,v142
ds_read_b128 v[76:79],v7 offset:1696
v_lshlrev_b32_sdwa v145,1,v145 dst_sel:DWORD dst_unused:UNUSED_PAD src0_sel:DWORD src1_sel:WORD_1
v_lshlrev_b32_sdwa v147,1,v147 dst_sel:DWORD dst_unused:UNUSED_PAD src0_sel:DWORD src1_sel:WORD_1
v_fma_mixlo_f16 v120,v157,s99,0
v_mfma_f32_16x16x32_bf16 v[168:171],v[92:95],v[128:131],v[168:171]
ds_read_u16 v144,v144
ds_read_b128 v[80:83],v7 offset:1792
v_lshlrev_b32_sdwa v148,1,v148 dst_sel:DWORD dst_unused:UNUSED_PAD src0_sel:DWORD src1_sel:WORD_1
s_waitcnt lgkmcnt(7)
v_lshl_add_u64 v[136:137],v[136:137],0,v[138:139]
v_fma_mixhi_f16 v120,v159,s99,0
v_mfma_f32_16x16x32_bf16 v[176:179],v[96:99],v[128:131],v[176:179]
ds_read_u16_d16_hi v146,v146
ds_read_b128 v[84:87],v7 offset:1824
v_lshlrev_b32_sdwa v150,1,v150 dst_sel:DWORD dst_unused:UNUSED_PAD src0_sel:DWORD src1_sel:WORD_1
s_waitcnt lgkmcnt(5)
v_or_b32 v138,v142,v140
v_cvt_pk_bf16_f32 v139,v141,v143
v_mfma_f32_16x16x32_bf16 v[184:187],v[100:103],v[128:131],v[184:187]
ds_read_u16 v145,v145
v_pk_fma_f16 v121,v120,v120,s54
v_lshlrev_b32_sdwa v149,1,v149 dst_sel:DWORD dst_unused:UNUSED_PAD src0_sel:DWORD src1_sel:WORD_1
v_pk_mul_f16 v121,v120,v121
v_mfma_f32_16x16x32_bf16 v[192:195],v[104:107],v[128:131],v[192:195]
ds_read_u16_d16_hi v147,v147
v_lshlrev_b32_sdwa v151,1,v151 dst_sel:DWORD dst_unused:UNUSED_PAD src0_sel:DWORD src1_sel:WORD_1
v_lshlrev_b32_sdwa v152,1,v152 dst_sel:DWORD dst_unused:UNUSED_PAD src0_sel:DWORD src1_sel:WORD_1
v_exp_f16 v120,v121
v_mfma_f32_16x16x32_bf16 v[200:203],v[108:111],v[128:131],v[200:203]
ds_read_u16 v148,v148
v_exp_f16_sdwa v120,v121 dst_sel:WORD_1 dst_unused:UNUSED_PRESERVE src0_sel:WORD_1
v_mfma_f32_16x16x32_bf16 v[164:167],v[88:91],v[136:139],v[164:167]
ds_read_u16_d16_hi v150,v150
v_lshlrev_b32_sdwa v154,1,v154 dst_sel:DWORD dst_unused:UNUSED_PAD src0_sel:DWORD src1_sel:WORD_1
v_pk_fma_f16 v120,v120,s55,s55
v_lshlrev_b32_sdwa v153,1,v153 dst_sel:DWORD dst_unused:UNUSED_PAD src0_sel:DWORD src1_sel:WORD_1
v_mfma_f32_16x16x32_bf16 v[172:175],v[92:95],v[136:139],v[172:175]
ds_read_u16 v149,v149
v_rcp_f16 v121,v120
v_mfma_f32_16x16x32_bf16 v[180:183],v[96:99],v[136:139],v[180:183]
ds_read_u16_d16_hi v151,v151
v_lshlrev_b32_sdwa v155,1,v155 dst_sel:DWORD dst_unused:UNUSED_PAD src0_sel:DWORD src1_sel:WORD_1
v_rcp_f16_sdwa v121,v120 dst_sel:WORD_1 dst_unused:UNUSED_PRESERVE src0_sel:WORD_1
v_mfma_f32_16x16x32_bf16 v[188:191],v[100:103],v[136:139],v[188:191]
ds_read_u16 v152,v152
v_lshlrev_b32_sdwa v156,1,v156 dst_sel:DWORD dst_unused:UNUSED_PAD src0_sel:DWORD src1_sel:WORD_1
v_fma_mix_f32 v157,v157,v121,0 op_sel_hi:[0,1,0]
v_fma_mix_f32 v159,v159,v121,0 op_sel:[0,1,0] op_sel_hi:[0,1,0]
v_mfma_f32_16x16x32_bf16 v[196:199],v[104:107],v[136:139],v[196:199]
ds_read_u16_d16_hi v154,v154
v_lshlrev_b32_sdwa v158,1,v158 dst_sel:DWORD dst_unused:UNUSED_PAD src0_sel:DWORD src1_sel:WORD_1
s_waitcnt lgkmcnt(6)
v_lshl_add_u64 v[144:145],v[144:145],0,v[146:147]
s_waitcnt lgkmcnt(2)
v_lshl_add_u64 v[146:147],v[148:149],0,v[150:151]
v_mfma_f32_16x16x32_bf16 v[204:207],v[108:111],v[136:139],v[204:207]
ds_read_u16 v153,v153
ds_read_u16_d16_hi v155,v155
s_waitcnt lgkmcnt(0)
v_lshl_add_u64 v[152:153],v[152:153],0,v[154:155]
ds_read_u16 v156,v156
ds_read_u16_d16_hi v158,v158
s_waitcnt lgkmcnt(0)
v_or_b32 v154,v158,v156
v_cvt_pk_bf16_f32 v155,v157,v159
v_mfma_f32_16x16x32_bf16 v[208:211],v[88:91],v[144:147],v[208:211]
v_xor_b32 v15,64,v10
v_mul_f32 v160,v160,v64
v_mul_f32 v161,v161,v65
v_mul_f32 v162,v162,v66
v_mul_f32 v163,v163,v67
v_mfma_f32_16x16x32_bf16 v[216:219],v[92:95],v[144:147],v[216:219]
v_mul_f32 v168,v168,v68
v_mul_f32 v169,v169,v69
v_mul_f32 v170,v170,v70
v_mul_f32 v171,v171,v71
v_cvt_pk_bf16_f32 v16,v160,v161
v_mfma_f32_16x16x32_bf16 v[224:227],v[96:99],v[144:147],v[224:227]
v_cvt_pk_bf16_f32 v17,v162,v163
v_cvt_pk_bf16_f32 v18,v168,v169
v_cvt_pk_bf16_f32 v19,v170,v171
v_mul_f32_dpp v176,v176,v72 quad_perm:[1,0,3,2] row_mask:0xf bank_mask:0xf
v_mfma_f32_16x16x32_bf16 v[232:235],v[100:103],v[144:147],v[232:235]
v_mul_f32_dpp v177,v177,v73 quad_perm:[1,0,3,2] row_mask:0xf bank_mask:0xf
v_mul_f32_dpp v178,v178,v74 quad_perm:[1,0,3,2] row_mask:0xf bank_mask:0xf
v_mul_f32_dpp v179,v179,v75 quad_perm:[1,0,3,2] row_mask:0xf bank_mask:0xf
v_mul_f32_dpp v184,v184,v76 quad_perm:[1,0,3,2] row_mask:0xf bank_mask:0xf
v_mul_f32_dpp v185,v185,v77 quad_perm:[1,0,3,2] row_mask:0xf bank_mask:0xf
v_mfma_f32_16x16x32_bf16 v[240:243],v[104:107],v[144:147],v[240:243]
v_mul_f32_dpp v186,v186,v78 quad_perm:[1,0,3,2] row_mask:0xf bank_mask:0xf
v_mul_f32_dpp v187,v187,v79 quad_perm:[1,0,3,2] row_mask:0xf bank_mask:0xf
v_cvt_pk_bf16_f32 v24,v176,v177
v_cvt_pk_bf16_f32 v25,v178,v179
v_mfma_f32_16x16x32_bf16 v[248:251],v[108:111],v[144:147],v[248:251]
v_cvt_pk_bf16_f32 v26,v184,v185
v_cvt_pk_bf16_f32 v27,v186,v187
s_mov_b64 exec,s[12:13]
v_swap_b32 v16,v24
v_swap_b32 v17,v25
v_swap_b32 v18,v26
v_swap_b32 v19,v27
s_mov_b64 exec,-1
s_add_u32 s65,s61,4718592
buffer_store_dwordx4 v[16:19],v10,s[40:43],s65 offen offset:0 sc1
s_add_u32 s65,s61,4718592
buffer_store_dwordx4 v[24:27],v15,s[40:43],s65 offen offset:384 sc1
v_mfma_f32_16x16x32_bf16 v[212:215],v[88:91],v[152:155],v[212:215]
v_mfma_f32_16x16x32_bf16 v[220:223],v[92:95],v[152:155],v[220:223]
v_mul_f32 v192,v192,v80
v_mul_f32 v193,v193,v81
v_mul_f32 v194,v194,v82
v_mul_f32 v195,v195,v83
v_mfma_f32_16x16x32_bf16 v[228:231],v[96:99],v[152:155],v[228:231]
v_mul_f32 v200,v200,v84
v_mul_f32 v201,v201,v85
v_mul_f32 v202,v202,v86
v_mul_f32 v203,v203,v87
v_mfma_f32_16x16x32_bf16 v[236:239],v[100:103],v[152:155],v[236:239]
v_cvt_pk_bf16_f32 v32,v192,v193
v_cvt_pk_bf16_f32 v33,v194,v195
v_cvt_pk_bf16_f32 v34,v200,v201
v_cvt_pk_bf16_f32 v35,v202,v203
s_nop 0
s_mov_b32 s86,0xbe801d5a
v_mul_f32_dpp v164,v164,v64 quad_perm:[1,0,3,2] row_mask:0xf bank_mask:0xf
v_mfma_f32_16x16x32_bf16 v[244:247],v[104:107],v[152:155],v[244:247]
v_mul_f32_dpp v165,v165,v65 quad_perm:[1,0,3,2] row_mask:0xf bank_mask:0xf
v_mul_f32_dpp v166,v166,v66 quad_perm:[1,0,3,2] row_mask:0xf bank_mask:0xf
v_mul_f32_dpp v167,v167,v67 quad_perm:[1,0,3,2] row_mask:0xf bank_mask:0xf
v_mul_f32_dpp v172,v172,v68 quad_perm:[1,0,3,2] row_mask:0xf bank_mask:0xf
v_mfma_f32_16x16x32_bf16 v[252:255],v[108:111],v[152:155],v[252:255]
v_mul_f32_dpp v173,v173,v69 quad_perm:[1,0,3,2] row_mask:0xf bank_mask:0xf
v_mul_f32_dpp v174,v174,v70 quad_perm:[1,0,3,2] row_mask:0xf bank_mask:0xf
v_mul_f32_dpp v175,v175,v71 quad_perm:[1,0,3,2] row_mask:0xf bank_mask:0xf
v_cvt_pk_bf16_f32 v20,v164,v165
v_cvt_pk_bf16_f32 v21,v166,v167
v_cvt_pk_bf16_f32 v22,v172,v173
v_cvt_pk_bf16_f32 v23,v174,v175
s_mov_b64 exec,s[12:13]
v_swap_b32 v32,v20
v_swap_b32 v33,v21
v_swap_b32 v34,v22
v_swap_b32 v35,v23
s_mov_b64 exec,-1
s_add_u32 s65,s61,4718592
buffer_store_dwordx4 v[32:35],v10,s[40:43],s65 offen offset:128 sc1
s_add_u32 s65,s61,4718592
buffer_store_dwordx4 v[20:23],v15,s[40:43],s65 offen offset:512 sc1
v_mul_f32 v180,v180,v72
v_mul_f32 v181,v181,v73
v_mul_f32 v182,v182,v74
v_mul_f32 v183,v183,v75
v_mul_f32 v188,v188,v76
v_mul_f32 v189,v189,v77
v_mul_f32 v190,v190,v78
v_mul_f32 v191,v191,v79
v_cvt_pk_bf16_f32 v28,v180,v181
v_cvt_pk_bf16_f32 v29,v182,v183
v_cvt_pk_bf16_f32 v30,v188,v189
v_cvt_pk_bf16_f32 v31,v190,v191
v_mul_f32_dpp v196,v196,v80 quad_perm:[1,0,3,2] row_mask:0xf bank_mask:0xf
v_mul_f32_dpp v197,v197,v81 quad_perm:[1,0,3,2] row_mask:0xf bank_mask:0xf
v_mul_f32_dpp v198,v198,v82 quad_perm:[1,0,3,2] row_mask:0xf bank_mask:0xf
v_mul_f32_dpp v199,v199,v83 quad_perm:[1,0,3,2] row_mask:0xf bank_mask:0xf
v_mul_f32_dpp v204,v204,v84 quad_perm:[1,0,3,2] row_mask:0xf bank_mask:0xf
v_mul_f32_dpp v205,v205,v85 quad_perm:[1,0,3,2] row_mask:0xf bank_mask:0xf
v_mul_f32_dpp v206,v206,v86 quad_perm:[1,0,3,2] row_mask:0xf bank_mask:0xf
v_mul_f32_dpp v207,v207,v87 quad_perm:[1,0,3,2] row_mask:0xf bank_mask:0xf
v_cvt_pk_bf16_f32 v36,v196,v197
v_cvt_pk_bf16_f32 v37,v198,v199
v_cvt_pk_bf16_f32 v38,v204,v205
v_cvt_pk_bf16_f32 v39,v206,v207
s_mov_b64 exec,s[12:13]
v_swap_b32 v28,v36
v_swap_b32 v29,v37
v_swap_b32 v30,v38
v_swap_b32 v31,v39
s_mov_b64 exec,-1
s_add_u32 s65,s61,4718592
buffer_store_dwordx4 v[28:31],v10,s[40:43],s65 offen offset:256 sc1
s_add_u32 s65,s61,4718592
buffer_store_dwordx4 v[36:39],v15,s[40:43],s65 offen offset:640 sc1
v_mul_f32 v208,v208,v64
v_mul_f32 v209,v209,v65
v_mul_f32 v210,v210,v66
v_mul_f32 v211,v211,v67
v_mul_f32 v216,v216,v68
v_mul_f32 v217,v217,v69
v_mul_f32 v218,v218,v70
v_mul_f32 v219,v219,v71
v_cvt_pk_bf16_f32 v40,v208,v209
v_cvt_pk_bf16_f32 v41,v210,v211
v_cvt_pk_bf16_f32 v42,v216,v217
v_cvt_pk_bf16_f32 v43,v218,v219
v_mul_f32_dpp v224,v224,v72 quad_perm:[1,0,3,2] row_mask:0xf bank_mask:0xf
v_mul_f32_dpp v225,v225,v73 quad_perm:[1,0,3,2] row_mask:0xf bank_mask:0xf
v_mul_f32_dpp v226,v226,v74 quad_perm:[1,0,3,2] row_mask:0xf bank_mask:0xf
v_mul_f32_dpp v227,v227,v75 quad_perm:[1,0,3,2] row_mask:0xf bank_mask:0xf
v_mul_f32_dpp v232,v232,v76 quad_perm:[1,0,3,2] row_mask:0xf bank_mask:0xf
v_mul_f32_dpp v233,v233,v77 quad_perm:[1,0,3,2] row_mask:0xf bank_mask:0xf
v_mul_f32_dpp v234,v234,v78 quad_perm:[1,0,3,2] row_mask:0xf bank_mask:0xf
v_mul_f32_dpp v235,v235,v79 quad_perm:[1,0,3,2] row_mask:0xf bank_mask:0xf
v_cvt_pk_bf16_f32 v48,v224,v225
v_cvt_pk_bf16_f32 v49,v226,v227
v_cvt_pk_bf16_f32 v50,v232,v233
v_cvt_pk_bf16_f32 v51,v234,v235
s_mov_b64 exec,s[12:13]
v_swap_b32 v40,v48
v_swap_b32 v41,v49
v_swap_b32 v42,v50
v_swap_b32 v43,v51
s_mov_b64 exec,-1
s_add_u32 s65,s61,4816896
buffer_store_dwordx4 v[40:43],v10,s[40:43],s65 offen offset:0 sc1
s_add_u32 s65,s61,4816896
buffer_store_dwordx4 v[48:51],v15,s[40:43],s65 offen offset:384 sc1
v_mul_f32 v240,v240,v80
v_mul_f32 v241,v241,v81
v_mul_f32 v242,v242,v82
v_mul_f32 v243,v243,v83
v_mul_f32 v248,v248,v84
v_mul_f32 v249,v249,v85
v_mul_f32 v250,v250,v86
v_mul_f32 v251,v251,v87
v_cvt_pk_bf16_f32 v56,v240,v241
v_cvt_pk_bf16_f32 v57,v242,v243
v_cvt_pk_bf16_f32 v58,v248,v249
v_cvt_pk_bf16_f32 v59,v250,v251
v_mul_f32_dpp v212,v212,v64 quad_perm:[1,0,3,2] row_mask:0xf bank_mask:0xf
v_mul_f32_dpp v213,v213,v65 quad_perm:[1,0,3,2] row_mask:0xf bank_mask:0xf
v_mul_f32_dpp v214,v214,v66 quad_perm:[1,0,3,2] row_mask:0xf bank_mask:0xf
v_mul_f32_dpp v215,v215,v67 quad_perm:[1,0,3,2] row_mask:0xf bank_mask:0xf
v_mul_f32_dpp v220,v220,v68 quad_perm:[1,0,3,2] row_mask:0xf bank_mask:0xf
v_mul_f32_dpp v221,v221,v69 quad_perm:[1,0,3,2] row_mask:0xf bank_mask:0xf
v_mul_f32_dpp v222,v222,v70 quad_perm:[1,0,3,2] row_mask:0xf bank_mask:0xf
v_mul_f32_dpp v223,v223,v71 quad_perm:[1,0,3,2] row_mask:0xf bank_mask:0xf
v_cvt_pk_bf16_f32 v44,v212,v213
v_cvt_pk_bf16_f32 v45,v214,v215
v_cvt_pk_bf16_f32 v46,v220,v221
v_cvt_pk_bf16_f32 v47,v222,v223
s_mov_b64 exec,s[12:13]
v_swap_b32 v56,v44
v_swap_b32 v57,v45
v_swap_b32 v58,v46
v_swap_b32 v59,v47
s_mov_b64 exec,-1
s_add_u32 s65,s61,4816896
buffer_store_dwordx4 v[56:59],v10,s[40:43],s65 offen offset:128 sc1
s_add_u32 s65,s61,4816896
buffer_store_dwordx4 v[44:47],v15,s[40:43],s65 offen offset:512 sc1
v_mul_f32 v228,v228,v72
v_mul_f32 v229,v229,v73
v_mul_f32 v230,v230,v74
v_mul_f32 v231,v231,v75
v_mul_f32 v236,v236,v76
v_mul_f32 v237,v237,v77
v_mul_f32 v238,v238,v78
v_mul_f32 v239,v239,v79
v_cvt_pk_bf16_f32 v52,v228,v229
v_cvt_pk_bf16_f32 v53,v230,v231
v_cvt_pk_bf16_f32 v54,v236,v237
v_cvt_pk_bf16_f32 v55,v238,v239
v_mul_f32_dpp v244,v244,v80 quad_perm:[1,0,3,2] row_mask:0xf bank_mask:0xf
v_mul_f32_dpp v245,v245,v81 quad_perm:[1,0,3,2] row_mask:0xf bank_mask:0xf
v_mul_f32_dpp v246,v246,v82 quad_perm:[1,0,3,2] row_mask:0xf bank_mask:0xf
v_mul_f32_dpp v247,v247,v83 quad_perm:[1,0,3,2] row_mask:0xf bank_mask:0xf
v_mul_f32_dpp v252,v252,v84 quad_perm:[1,0,3,2] row_mask:0xf bank_mask:0xf
v_mul_f32_dpp v253,v253,v85 quad_perm:[1,0,3,2] row_mask:0xf bank_mask:0xf
v_mul_f32_dpp v254,v254,v86 quad_perm:[1,0,3,2] row_mask:0xf bank_mask:0xf
v_mul_f32_dpp v255,v255,v87 quad_perm:[1,0,3,2] row_mask:0xf bank_mask:0xf
v_cvt_pk_bf16_f32 v60,v244,v245
v_cvt_pk_bf16_f32 v61,v246,v247
v_cvt_pk_bf16_f32 v62,v252,v253
v_cvt_pk_bf16_f32 v63,v254,v255
s_mov_b64 exec,s[12:13]
v_swap_b32 v52,v60
v_swap_b32 v53,v61
v_swap_b32 v54,v62
v_swap_b32 v55,v63
s_mov_b64 exec,-1
s_add_u32 s65,s61,4816896
buffer_store_dwordx4 v[52:55],v10,s[40:43],s65 offen offset:256 sc1
s_add_u32 s65,s61,4816896
buffer_store_dwordx4 v[60:63],v15,s[40:43],s65 offen offset:640 sc1
v_and_b32 v1,0x7c007c00,v1
v_lshrrev_b32 v2,16,v1
v_and_b32 v1,0xffff,v1
v_max_u32 v1,v1,v2
v_cmp_eq_u32 vcc,0x7c00,v1
s_or_b32 s64,vcc_lo,vcc_hi
v_mov_b32 v1,s64
v_mov_b32 v2,8192
s_mov_b64 exec,1
ds_or_b32 v2,v1 offset:3456
s_mov_b64 exec,-1
s_waitcnt lgkmcnt(0)
s_barrier
ds_read_b32 v1,v2 offset:3456
s_waitcnt lgkmcnt(0)
v_readfirstlane_b32 s64,v1
s_cmp_lg_u32 s64,0
s_cbranch_scc1 N_fallback_%=
s_endpgm
N_mainend_%=:
N_fallback_%=:
s_setprio 0
s_waitcnt vmcnt(0) lgkmcnt(0)
s_barrier
v_lshrrev_b32 v237,6,%1
v_and_b32 v232,63,%1
v_readfirstlane_b32 s52,v237
v_lshlrev_b32 v248,4,v232
v_min_u32 v244,383,%1
v_lshlrev_b32 v238,1,v244
global_load_ushort v244,v238,s[26:27]
v_min_u32 v245,95,%1
v_lshlrev_b32 v239,1,v245
global_load_ushort v245,v239,s[30:31]
global_load_ushort v246,v239,s[32:33]
s_mov_b64 s[92:93],s[22:23]
s_and_b32 s93,s93,0xffff
s_mov_b32 s94,1728
s_mov_b32 s95,0x00020000
s_lshl_b32 s71,s52,11
s_add_u32 s71,s71,62208
s_mov_b32 m0,s71
s_movk_i32 s64,1024
buffer_load_dwordx4 v248,s[92:95],0 offen lds
s_add_u32 m0,s71,1024
s_nop 0
buffer_load_dwordx4 v248,s[92:95],s64 offen lds
v_mov_b32 v250,s71
s_mov_b64 s[36:37],s[20:21]
s_mov_b64 s[40:41],s[34:35]
s_mov_b64 s[44:45],s[24:25]
s_mov_b64 s[48:49],s[28:29]
s_sub_u32 s36,s36,192
s_subb_u32 s37,s37,0
s_and_b32 s37,s37,0xffff
s_mov_b32 s38,50331840
s_mov_b32 s39,0x00020000
s_and_b32 s41,s41,0xffff
s_mov_b32 s42,50331648
s_mov_b32 s43,0x00020000
s_and_b32 s45,s45,0xffff
s_mov_b32 s46,73728
s_mov_b32 s47,0x00020000
s_and_b32 s49,s49,0xffff
s_mov_b32 s50,73728
s_mov_b32 s51,0x00020000
s_mov_b32 s60,-1
s_mov_b32 s61,0xffff
s_mov_b32 s62,-1
s_mov_b32 s63,0x3fffff
s_and_b32 s64,%0,7
s_lshr_b32 s70,%0,3
s_lshl_b32 s56,s64,6
s_mul_i32 s57,s70,3072
s_and_b32 s66,s52,3
s_mul_i32 s68,s66,864
s_mul_i32 s67,s66,54
s_mul_i32 s58,s66,1536
v_add_u32 v237,s67,v232
v_mul_u32_u24 v238,0xaaab,v237
v_lshrrev_b32 v238,19,v238
v_mul_u32_u24 v239,12,v238
v_sub_u32 v239,v237,v239
v_bfe_u32 v237,v238,2,2
v_xor_b32 v239,v239,v237
v_lshlrev_b32 v239,4,v239
v_mul_u32_u24 v237,192,v238
v_add_u32 v247,v237,v239
s_cmp_eq_u32 s70,0
s_cselect_b32 s71,0,99
s_cmp_eq_u32 s70,31
s_cselect_b32 s71,17,s71
v_cmp_eq_u32 vcc,s71,v238
v_mov_b32 v237,0x7fff0000
s_sub_u32 s67,s56,1
s_mul_i32 s59,s67,98304
s_add_u32 s59,s59,s57
v_cndmask_b32 v247,v247,v237,vcc
s_lshr_b32 s66,s52,2
s_mul_i32 s64,s66,3456
s_add_u32 s64,s64,s68
s_mul_i32 s65,s66,98304
s_add_u32 s65,s65,s59
s_add_u32 s67,s67,s66
s_cmp_lt_i32 s67,0
s_cselect_b32 s71,0x70000000,s65
s_mov_b64 exec,s[62:63]
s_add_u32 m0,s64,0
s_nop 0
buffer_load_dwordx4 v247,s[36:39],s71 offen lds
s_add_u32 m0,s64,6912
s_add_u32 s71,s65,196608
buffer_load_dwordx4 v247,s[36:39],s71 offen lds
s_add_u32 m0,s64,13824
s_add_u32 s71,s65,393216
buffer_load_dwordx4 v247,s[36:39],s71 offen lds
s_add_u32 m0,s64,20736
s_add_u32 s71,s65,589824
buffer_load_dwordx4 v247,s[36:39],s71 offen lds
s_add_u32 m0,s64,27648
s_add_u32 s71,s65,786432
buffer_load_dwordx4 v247,s[36:39],s71 offen lds
s_add_u32 m0,s64,34560
s_add_u32 s71,s65,983040
buffer_load_dwordx4 v247,s[36:39],s71 offen lds
s_add_u32 m0,s64,41472
s_add_u32 s71,s65,1179648
buffer_load_dwordx4 v247,s[36:39],s71 offen lds
s_add_u32 m0,s64,48384
s_add_u32 s71,s65,1376256
buffer_load_dwordx4 v247,s[36:39],s71 offen lds
s_add_u32 m0,s64,55296
s_add_u32 s71,s65,1572864
buffer_load_dwordx4 v247,s[36:39],s71 offen lds
s_mov_b64 exec,-1
v_lshrrev_b32 v237,2,v232
v_mul_u32_u24 v237,768,v237
v_and_b32 v238,3,v232
v_lshl_add_u32 v237,v238,4,v237
s_and_b32 s66,s52,3
s_mul_i32 s71,s66,18432
v_add_u32 v27,s71,v237
v_add_u32 v28,s58,v248
s_mov_b32 s54,62208
s_mov_b32 s53,0
s_mov_b32 s96,0x70000000
s_add_u32 s68,s68,s54
s_add_u32 s59,s59,1572864
s_lshl_b32 s66,s52,1
s_add_u32 s66,s66,s56
s_mul_i32 s69,s66,98304
s_add_u32 s69,s69,s57
s_cmp_ge_u32 s52,4
s_cbranch_scc1 L_noW_%=
s_mov_b64 exec,s[60:61]
s_add_u32 m0,s58,124416
s_movk_i32 s64,0
buffer_load_dwordx4 v27,s[44:47],s64 offen lds
s_add_u32 m0,s58,125184
s_movk_i32 s65,9216
buffer_load_dwordx4 v27,s[44:47],s65 offen lds
s_mov_b64 exec,-1
s_mov_b64 exec,s[60:61]
s_add_u32 m0,s58,142848
s_mov_b32 s64,0
buffer_load_dwordx4 v28,s[48:51],s64 offen lds
s_add_u32 m0,s58,143616
s_mov_b32 s65,768
buffer_load_dwordx4 v28,s[48:51],s65 offen lds
s_mov_b64 exec,-1
s_mov_b64 exec,s[60:61]
s_add_u32 m0,s58,130560
s_movk_i32 s64,64
buffer_load_dwordx4 v27,s[44:47],s64 offen lds
s_add_u32 m0,s58,131328
s_movk_i32 s65,9280
buffer_load_dwordx4 v27,s[44:47],s65 offen lds
s_mov_b64 exec,-1
s_mov_b64 exec,s[60:61]
s_add_u32 m0,s58,136704
s_movk_i32 s64,128
buffer_load_dwordx4 v27,s[44:47],s64 offen lds
s_add_u32 m0,s58,137472
s_movk_i32 s65,9344
buffer_load_dwordx4 v27,s[44:47],s65 offen lds
s_mov_b64 exec,-1
s_branch L_W_%=
L_noW_%=:
v_mov_b32 v27,v247
s_mul_i32 s64,s70,2304
s_and_b32 s65,s52,3
s_mul_i32 s65,s65,576
s_add_u32 s64,s64,s65
v_min_u32 v237,560,v248
buffer_load_dwordx4 v[252:255],v237,s[44:47],s64 offen
buffer_load_dwordx4 v[252:255],v237,s[48:51],s64 offen
L_W_%=:
v_and_b32 v233,15,v232
v_lshrrev_b32 v234,4,v232
v_lshrrev_b32 v235,5,v232
v_and_b32 v237,1,v234
v_lshlrev_b32 v237,1,v237
v_or_b32 v236,v237,v235
v_lshrrev_b32 v237,3,v233
v_lshlrev_b32 v237,2,v237
v_and_b32 v238,3,v233
v_or_b32 v241,v237,v238
v_lshrrev_b32 v237,2,v233
v_and_b32 v242,1,v237
v_sub_u32 v237,v236,v242
v_cmp_gt_u32 vcc,3,v237
v_mov_b32 v238,0
v_mov_b32 v239,-1
v_cndmask_b32 v237,v238,v237,vcc
v_cndmask_b32 v249,v238,v239,vcc
v_mul_u32_u24 v237,576,v237
v_lshl_add_u32 v237,v241,1,v237
v_add_u32 v250,v250,v237
v_lshrrev_b32 v237,1,v241
v_cmp_eq_u32 vcc,0,v237
s_nop 1
v_cndmask_b32 v32,v238,v249,vcc
v_cmp_eq_u32 vcc,1,v237
s_nop 1
v_cndmask_b32 v33,v238,v249,vcc
v_cmp_eq_u32 vcc,2,v237
s_nop 1
v_cndmask_b32 v34,v238,v249,vcc
v_cmp_eq_u32 vcc,3,v237
s_nop 1
v_cndmask_b32 v35,v238,v249,vcc
v_and_b32 v243,1,v241
v_lshlrev_b32 v243,4,v243
s_mul_i32 s71,s52,6912
v_add_u32 v237,0,v233
v_bfe_u32 v238,v237,2,2
v_mul_u32_u24 v239,18,v236
v_add_u32 v239,v239,v237
v_mul_u32_u24 v239,192,v239
v_add_u32 v239,s71,v239
v_xor_b32 v240,0,v238
v_lshl_add_u32 v4,v240,4,v239
v_xor_b32 v240,1,v238
v_lshl_add_u32 v5,v240,4,v239
v_xor_b32 v240,2,v238
v_lshl_add_u32 v6,v240,4,v239
v_xor_b32 v240,3,v238
v_lshl_add_u32 v7,v240,4,v239
v_add_u32 v237,1,v233
v_bfe_u32 v238,v237,2,2
v_mul_u32_u24 v239,18,v236
v_add_u32 v239,v239,v237
v_mul_u32_u24 v239,192,v239
v_add_u32 v239,s71,v239
v_xor_b32 v240,0,v238
v_lshl_add_u32 v8,v240,4,v239
v_xor_b32 v240,1,v238
v_lshl_add_u32 v9,v240,4,v239
v_xor_b32 v240,2,v238
v_lshl_add_u32 v10,v240,4,v239
v_xor_b32 v240,3,v238
v_lshl_add_u32 v11,v240,4,v239
v_add_u32 v237,2,v233
v_bfe_u32 v238,v237,2,2
v_mul_u32_u24 v239,18,v236
v_add_u32 v239,v239,v237
v_mul_u32_u24 v239,192,v239
v_add_u32 v239,s71,v239
v_xor_b32 v240,0,v238
v_lshl_add_u32 v12,v240,4,v239
v_xor_b32 v240,1,v238
v_lshl_add_u32 v13,v240,4,v239
v_xor_b32 v240,2,v238
v_lshl_add_u32 v14,v240,4,v239
v_xor_b32 v240,3,v238
v_lshl_add_u32 v15,v240,4,v239
v_lshrrev_b32 v237,2,v233
v_lshl_add_u32 v237,v235,2,v237
v_and_b32 v238,1,v234
v_lshlrev_b32 v238,5,v238
v_and_b32 v239,3,v233
v_lshl_add_u32 v238,v239,3,v238
v_add_u32 v238,124416,v238
v_lshl_add_u32 v16,v237,6,v238
v_mul_u32_u24 v237,192,v237
v_add_u32 v17,v237,v238
v_add_u32 v17,18432,v17
v_lshlrev_b32 v237,4,v235
v_add_u32 v18,161280,v237
v_add_u32 v237,s71,v248
v_add_u32 v19,3648,v237
v_and_b32 v237,1,v234
v_add_u32 v237,1,v237
v_mul_u32_u24 v237,18,v237
v_add_u32 v238,1,v233
v_add_u32 v237,v237,v238
v_mul_u32_u24 v237,192,v237
v_add_u32 v237,s71,v237
v_lshl_add_u32 v237,v235,3,v237
v_bfe_u32 v238,v238,2,2
v_xor_b32 v240,0,v238
v_lshl_add_u32 v20,v240,4,v237
v_xor_b32 v240,1,v238
v_lshl_add_u32 v21,v240,4,v237
v_xor_b32 v240,2,v238
v_lshl_add_u32 v22,v240,4,v237
v_xor_b32 v240,3,v238
v_lshl_add_u32 v23,v240,4,v237
v_add_u32 v237,0,v232
v_mul_u32_u24 v238,0xaaab,v237
v_lshrrev_b32 v238,19,v238
v_mul_u32_u24 v239,12,v238
v_sub_u32 v239,v237,v239
v_add_u32 v237,1,v238
v_bfe_u32 v237,v237,2,2
v_xor_b32 v239,v239,v237
v_lshlrev_b32 v239,4,v239
v_mul_u32_u24 v237,192,v238
v_add_u32 v24,v237,v239
v_add_u32 v237,64,v232
v_mul_u32_u24 v238,0xaaab,v237
v_lshrrev_b32 v238,19,v238
v_mul_u32_u24 v239,12,v238
v_sub_u32 v239,v237,v239
v_add_u32 v237,1,v238
v_bfe_u32 v237,v237,2,2
v_xor_b32 v239,v239,v237
v_lshlrev_b32 v239,4,v239
v_mul_u32_u24 v237,192,v238
v_add_u32 v25,v237,v239
v_add_u32 v237,128,v232
v_mul_u32_u24 v238,0xaaab,v237
v_lshrrev_b32 v238,19,v238
v_mul_u32_u24 v239,12,v238
v_sub_u32 v239,v237,v239
v_add_u32 v237,1,v238
v_bfe_u32 v237,v237,2,2
v_xor_b32 v239,v239,v237
v_lshlrev_b32 v239,4,v239
v_mul_u32_u24 v237,192,v238
v_add_u32 v26,v237,v239
s_cmp_ge_u32 s52,4
s_cbranch_scc1 L_wB_%=
s_waitcnt vmcnt(17)
s_branch L_wA_%=
L_wB_%=:
s_waitcnt vmcnt(11)
L_wA_%=:
ds_read_u16 v36,v250 offset:0
ds_read_u16 v37,v250 offset:16
ds_read_u16 v38,v250 offset:32
ds_read_u16 v39,v250 offset:48
ds_read_u16 v40,v250 offset:64
ds_read_u16 v41,v250 offset:80
ds_read_u16 v42,v250 offset:96
ds_read_u16 v43,v250 offset:112
ds_read_u16 v44,v250 offset:128
ds_read_u16 v45,v250 offset:144
ds_read_u16 v46,v250 offset:160
ds_read_u16 v47,v250 offset:176
ds_read_u16 v48,v250 offset:192
ds_read_u16 v49,v250 offset:208
ds_read_u16 v50,v250 offset:224
ds_read_u16 v51,v250 offset:240
ds_read_u16 v52,v250 offset:256
ds_read_u16 v53,v250 offset:272
ds_read_u16 v54,v250 offset:288
ds_read_u16 v55,v250 offset:304
ds_read_u16 v56,v250 offset:320
ds_read_u16 v57,v250 offset:336
ds_read_u16 v58,v250 offset:352
ds_read_u16 v59,v250 offset:368
ds_read_u16 v60,v250 offset:384
ds_read_u16 v61,v250 offset:400
ds_read_u16 v62,v250 offset:416
ds_read_u16 v63,v250 offset:432
ds_read_u16 v64,v250 offset:448
ds_read_u16 v65,v250 offset:464
ds_read_u16 v66,v250 offset:480
ds_read_u16 v67,v250 offset:496
ds_read_u16 v68,v250 offset:512
ds_read_u16 v69,v250 offset:528
ds_read_u16 v70,v250 offset:544
ds_read_u16 v71,v250 offset:560
v_lshlrev_b32 v244,16,v244
v_lshlrev_b32 v245,16,v245
v_lshlrev_b32 v246,16,v246
v_mul_f32 v244,0xbeeff558,v244
v_mul_f32 v245,0xbeeff558,v245
v_mul_f32 v246,0xc0088e99,v246
v_min_u32 v237,383,%1
v_lshlrev_b32 v237,2,v237
v_add_u32 v237,161280,v237
ds_write_b32 v237,v244 offset:0
v_min_u32 v237,95,%1
v_lshlrev_b32 v237,2,v237
v_add_u32 v237,161280,v237
ds_write_b32 v237,v245 offset:1920
ds_write_b32 v237,v246 offset:1536
s_waitcnt lgkmcnt(0)
v_lshlrev_b32 v36,v243,v36
v_lshlrev_b32 v37,v243,v37
v_lshlrev_b32 v38,v243,v38
v_lshlrev_b32 v39,v243,v39
v_lshlrev_b32 v40,v243,v40
v_lshlrev_b32 v41,v243,v41
v_lshlrev_b32 v42,v243,v42
v_lshlrev_b32 v43,v243,v43
v_lshlrev_b32 v44,v243,v44
v_lshlrev_b32 v45,v243,v45
v_lshlrev_b32 v46,v243,v46
v_lshlrev_b32 v47,v243,v47
v_lshlrev_b32 v48,v243,v48
v_lshlrev_b32 v49,v243,v49
v_lshlrev_b32 v50,v243,v50
v_lshlrev_b32 v51,v243,v51
v_lshlrev_b32 v52,v243,v52
v_lshlrev_b32 v53,v243,v53
v_lshlrev_b32 v54,v243,v54
v_lshlrev_b32 v55,v243,v55
v_lshlrev_b32 v56,v243,v56
v_lshlrev_b32 v57,v243,v57
v_lshlrev_b32 v58,v243,v58
v_lshlrev_b32 v59,v243,v59
v_lshlrev_b32 v60,v243,v60
v_lshlrev_b32 v61,v243,v61
v_lshlrev_b32 v62,v243,v62
v_lshlrev_b32 v63,v243,v63
v_lshlrev_b32 v64,v243,v64
v_lshlrev_b32 v65,v243,v65
v_lshlrev_b32 v66,v243,v66
v_lshlrev_b32 v67,v243,v67
v_lshlrev_b32 v68,v243,v68
v_lshlrev_b32 v69,v243,v69
v_lshlrev_b32 v70,v243,v70
v_lshlrev_b32 v71,v243,v71
s_cmp_ge_u32 s52,4
s_cbranch_scc1 L_fB_%=
s_waitcnt vmcnt(6)
s_branch L_fA_%=
L_fB_%=:
s_waitcnt vmcnt(2)
L_fA_%=:
s_cmp_ge_u32 s52,4
s_cbranch_scc1 L_pass1_%=
L_pass0_%=:
s_barrier
ds_read_b128 v[200:203],v4 offset:0
ds_read_b128 v[204:207],v8 offset:0
ds_read_b128 v[208:211],v12 offset:0
ds_read_b128 v[212:215],v5 offset:0
ds_read_b128 v[216:219],v9 offset:0
ds_read_b128 v[220:223],v13 offset:0
ds_read_b128 v[96:99],v18 offset:1920
ds_read_b128 v[100:103],v18 offset:1952
ds_read_b128 v[104:107],v18 offset:1984
ds_read_b128 v[108:111],v18 offset:2016
ds_read_b128 v[112:115],v18 offset:2048
ds_read_b128 v[116:119],v18 offset:2080
ds_read_b128 v[120:123],v18 offset:2112
ds_read_b128 v[124:127],v18 offset:2144
ds_read_b128 v[128:131],v18 offset:2176
ds_read_b128 v[132:135],v18 offset:2208
ds_read_b128 v[136:139],v18 offset:2240
ds_read_b128 v[140:143],v18 offset:2272
v_and_b32 v72,v32,v36
v_and_b32 v73,v33,v36
v_and_b32 v74,v34,v36
v_and_b32 v75,v35,v36
v_and_b32 v76,v32,v48
v_and_b32 v77,v33,v48
v_and_b32 v78,v34,v48
v_and_b32 v79,v35,v48
v_and_b32 v80,v32,v60
v_and_b32 v81,v33,v60
v_and_b32 v82,v34,v60
v_and_b32 v83,v35,v60
v_and_b32 v84,v32,v37
v_and_b32 v85,v33,v37
v_and_b32 v86,v34,v37
v_and_b32 v87,v35,v37
v_and_b32 v88,v32,v49
v_and_b32 v89,v33,v49
v_and_b32 v90,v34,v49
v_and_b32 v91,v35,v49
v_and_b32 v92,v32,v61
v_and_b32 v93,v33,v61
v_and_b32 v94,v34,v61
v_and_b32 v95,v35,v61
s_setprio 1
s_waitcnt lgkmcnt(15)
v_mfma_f32_16x16x32_bf16 v[144:147],v[72:75],v[200:203],0
v_mfma_f32_16x16x32_bf16 v[144:147],v[76:79],v[204:207],v[144:147]
v_mfma_f32_16x16x32_bf16 v[144:147],v[80:83],v[208:211],v[144:147]
ds_read_b128 v[200:203],v6 offset:0
ds_read_b128 v[204:207],v10 offset:0
ds_read_b128 v[208:211],v14 offset:0
s_setprio 0
v_and_b32 v72,v32,v38
v_and_b32 v73,v33,v38
v_and_b32 v74,v34,v38
v_and_b32 v75,v35,v38
v_and_b32 v76,v32,v50
v_and_b32 v77,v33,v50
v_and_b32 v78,v34,v50
v_and_b32 v79,v35,v50
v_and_b32 v80,v32,v62
v_and_b32 v81,v33,v62
v_and_b32 v82,v34,v62
v_and_b32 v83,v35,v62
v_mul_f32 v237,v144,v144
v_mul_f32 v236,v145,v145
v_mul_f32 v239,v146,v146
v_mul_f32 v238,v147,v147
s_setprio 1
s_waitcnt lgkmcnt(15)
v_mfma_f32_16x16x32_bf16 v[148:151],v[84:87],v[212:215],0
v_mfma_f32_16x16x32_bf16 v[148:151],v[88:91],v[216:219],v[148:151]
v_mfma_f32_16x16x32_bf16 v[148:151],v[92:95],v[220:223],v[148:151]
ds_read_b128 v[212:215],v7 offset:0
ds_read_b128 v[216:219],v11 offset:0
ds_read_b128 v[220:223],v15 offset:0
s_setprio 0
v_and_b32 v84,v32,v39
v_and_b32 v85,v33,v39
v_and_b32 v86,v34,v39
v_and_b32 v87,v35,v39
v_and_b32 v88,v32,v51
v_and_b32 v89,v33,v51
v_and_b32 v90,v34,v51
v_and_b32 v91,v35,v51
v_and_b32 v92,v32,v63
v_and_b32 v93,v33,v63
v_and_b32 v94,v34,v63
v_and_b32 v95,v35,v63
v_add_f32 v232,v144,v148
v_fmac_f32 v237,v148,v148
v_add_f32 v233,v145,v149
v_fmac_f32 v236,v149,v149
v_add_f32 v234,v146,v150
v_fmac_f32 v239,v150,v150
v_add_f32 v235,v147,v151
v_fmac_f32 v238,v151,v151
s_setprio 1
s_waitcnt lgkmcnt(3)
v_mfma_f32_16x16x32_bf16 v[152:155],v[72:75],v[200:203],0
v_mfma_f32_16x16x32_bf16 v[152:155],v[76:79],v[204:207],v[152:155]
v_mfma_f32_16x16x32_bf16 v[152:155],v[80:83],v[208:211],v[152:155]
ds_read_b128 v[200:203],v4 offset:64
ds_read_b128 v[204:207],v8 offset:64
ds_read_b128 v[208:211],v12 offset:64
s_setprio 0
v_and_b32 v72,v32,v40
v_and_b32 v73,v33,v40
v_and_b32 v74,v34,v40
v_and_b32 v75,v35,v40
v_and_b32 v76,v32,v52
v_and_b32 v77,v33,v52
v_and_b32 v78,v34,v52
v_and_b32 v79,v35,v52
v_and_b32 v80,v32,v64
v_and_b32 v81,v33,v64
v_and_b32 v82,v34,v64
v_and_b32 v83,v35,v64
v_add_f32 v232,v232,v152
v_fmac_f32 v237,v152,v152
v_add_f32 v233,v233,v153
v_fmac_f32 v236,v153,v153
v_add_f32 v234,v234,v154
v_fmac_f32 v239,v154,v154
v_add_f32 v235,v235,v155
v_fmac_f32 v238,v155,v155
s_setprio 1
s_waitcnt lgkmcnt(3)
v_mfma_f32_16x16x32_bf16 v[156:159],v[84:87],v[212:215],0
v_mfma_f32_16x16x32_bf16 v[156:159],v[88:91],v[216:219],v[156:159]
v_mfma_f32_16x16x32_bf16 v[156:159],v[92:95],v[220:223],v[156:159]
ds_read_b128 v[212:215],v5 offset:64
ds_read_b128 v[216:219],v9 offset:64
ds_read_b128 v[220:223],v13 offset:64
s_setprio 0
v_and_b32 v84,v32,v41
v_and_b32 v85,v33,v41
v_and_b32 v86,v34,v41
v_and_b32 v87,v35,v41
v_and_b32 v88,v32,v53
v_and_b32 v89,v33,v53
v_and_b32 v90,v34,v53
v_and_b32 v91,v35,v53
v_and_b32 v92,v32,v65
v_and_b32 v93,v33,v65
v_and_b32 v94,v34,v65
v_and_b32 v95,v35,v65
v_add_f32 v232,v232,v156
v_fmac_f32 v237,v156,v156
v_add_f32 v233,v233,v157
v_fmac_f32 v236,v157,v157
v_add_f32 v234,v234,v158
v_fmac_f32 v239,v158,v158
v_add_f32 v235,v235,v159
v_fmac_f32 v238,v159,v159
s_setprio 1
s_waitcnt lgkmcnt(3)
v_mfma_f32_16x16x32_bf16 v[160:163],v[72:75],v[200:203],0
v_mfma_f32_16x16x32_bf16 v[160:163],v[76:79],v[204:207],v[160:163]
v_mfma_f32_16x16x32_bf16 v[160:163],v[80:83],v[208:211],v[160:163]
ds_read_b128 v[200:203],v6 offset:64
ds_read_b128 v[204:207],v10 offset:64
ds_read_b128 v[208:211],v14 offset:64
s_setprio 0
v_and_b32 v72,v32,v42
v_and_b32 v73,v33,v42
v_and_b32 v74,v34,v42
v_and_b32 v75,v35,v42
v_and_b32 v76,v32,v54
v_and_b32 v77,v33,v54
v_and_b32 v78,v34,v54
v_and_b32 v79,v35,v54
v_and_b32 v80,v32,v66
v_and_b32 v81,v33,v66
v_and_b32 v82,v34,v66
v_and_b32 v83,v35,v66
v_add_f32 v232,v232,v160
v_fmac_f32 v237,v160,v160
v_add_f32 v233,v233,v161
v_fmac_f32 v236,v161,v161
v_add_f32 v234,v234,v162
v_fmac_f32 v239,v162,v162
v_add_f32 v235,v235,v163
v_fmac_f32 v238,v163,v163
s_setprio 1
s_waitcnt lgkmcnt(3)
v_mfma_f32_16x16x32_bf16 v[164:167],v[84:87],v[212:215],0
v_mfma_f32_16x16x32_bf16 v[164:167],v[88:91],v[216:219],v[164:167]
v_mfma_f32_16x16x32_bf16 v[164:167],v[92:95],v[220:223],v[164:167]
ds_read_b128 v[212:215],v7 offset:64
ds_read_b128 v[216:219],v11 offset:64
ds_read_b128 v[220:223],v15 offset:64
s_setprio 0
v_and_b32 v84,v32,v43
v_and_b32 v85,v33,v43
v_and_b32 v86,v34,v43
v_and_b32 v87,v35,v43
v_and_b32 v88,v32,v55
v_and_b32 v89,v33,v55
v_and_b32 v90,v34,v55
v_and_b32 v91,v35,v55
v_and_b32 v92,v32,v67
v_and_b32 v93,v33,v67
v_and_b32 v94,v34,v67
v_and_b32 v95,v35,v67
v_add_f32 v232,v232,v164
v_fmac_f32 v237,v164,v164
v_add_f32 v233,v233,v165
v_fmac_f32 v236,v165,v165
v_add_f32 v234,v234,v166
v_fmac_f32 v239,v166,v166
v_add_f32 v235,v235,v167
v_fmac_f32 v238,v167,v167
s_setprio 1
s_waitcnt lgkmcnt(3)
v_mfma_f32_16x16x32_bf16 v[168:171],v[72:75],v[200:203],0
v_mfma_f32_16x16x32_bf16 v[168:171],v[76:79],v[204:207],v[168:171]
v_mfma_f32_16x16x32_bf16 v[168:171],v[80:83],v[208:211],v[168:171]
ds_read_b128 v[200:203],v4 offset:128
ds_read_b128 v[204:207],v8 offset:128
ds_read_b128 v[208:211],v12 offset:128
s_setprio 0
v_and_b32 v72,v32,v44
v_and_b32 v73,v33,v44
v_and_b32 v74,v34,v44
v_and_b32 v75,v35,v44
v_and_b32 v76,v32,v56
v_and_b32 v77,v33,v56
v_and_b32 v78,v34,v56
v_and_b32 v79,v35,v56
v_and_b32 v80,v32,v68
v_and_b32 v81,v33,v68
v_and_b32 v82,v34,v68
v_and_b32 v83,v35,v68
v_add_f32 v232,v232,v168
v_fmac_f32 v237,v168,v168
v_add_f32 v233,v233,v169
v_fmac_f32 v236,v169,v169
v_add_f32 v234,v234,v170
v_fmac_f32 v239,v170,v170
v_add_f32 v235,v235,v171
v_fmac_f32 v238,v171,v171
s_setprio 1
s_waitcnt lgkmcnt(3)
v_mfma_f32_16x16x32_bf16 v[172:175],v[84:87],v[212:215],0
v_mfma_f32_16x16x32_bf16 v[172:175],v[88:91],v[216:219],v[172:175]
v_mfma_f32_16x16x32_bf16 v[172:175],v[92:95],v[220:223],v[172:175]
ds_read_b128 v[212:215],v5 offset:128
ds_read_b128 v[216:219],v9 offset:128
ds_read_b128 v[220:223],v13 offset:128
s_setprio 0
v_and_b32 v84,v32,v45
v_and_b32 v85,v33,v45
v_and_b32 v86,v34,v45
v_and_b32 v87,v35,v45
v_and_b32 v88,v32,v57
v_and_b32 v89,v33,v57
v_and_b32 v90,v34,v57
v_and_b32 v91,v35,v57
v_and_b32 v92,v32,v69
v_and_b32 v93,v33,v69
v_and_b32 v94,v34,v69
v_and_b32 v95,v35,v69
v_add_f32 v232,v232,v172
v_fmac_f32 v237,v172,v172
v_add_f32 v233,v233,v173
v_fmac_f32 v236,v173,v173
v_add_f32 v234,v234,v174
v_fmac_f32 v239,v174,v174
v_add_f32 v235,v235,v175
v_fmac_f32 v238,v175,v175
s_setprio 1
s_waitcnt lgkmcnt(3)
v_mfma_f32_16x16x32_bf16 v[176:179],v[72:75],v[200:203],0
v_mfma_f32_16x16x32_bf16 v[176:179],v[76:79],v[204:207],v[176:179]
v_mfma_f32_16x16x32_bf16 v[176:179],v[80:83],v[208:211],v[176:179]
ds_read_b128 v[200:203],v6 offset:128
ds_read_b128 v[204:207],v10 offset:128
ds_read_b128 v[208:211],v14 offset:128
s_setprio 0
v_and_b32 v72,v32,v46
v_and_b32 v73,v33,v46
v_and_b32 v74,v34,v46
v_and_b32 v75,v35,v46
v_and_b32 v76,v32,v58
v_and_b32 v77,v33,v58
v_and_b32 v78,v34,v58
v_and_b32 v79,v35,v58
v_and_b32 v80,v32,v70
v_and_b32 v81,v33,v70
v_and_b32 v82,v34,v70
v_and_b32 v83,v35,v70
v_add_f32 v232,v232,v176
v_fmac_f32 v237,v176,v176
v_add_f32 v233,v233,v177
v_fmac_f32 v236,v177,v177
v_add_f32 v234,v234,v178
v_fmac_f32 v239,v178,v178
v_add_f32 v235,v235,v179
v_fmac_f32 v238,v179,v179
s_setprio 1
s_waitcnt lgkmcnt(3)
v_mfma_f32_16x16x32_bf16 v[180:183],v[84:87],v[212:215],0
v_mfma_f32_16x16x32_bf16 v[180:183],v[88:91],v[216:219],v[180:183]
v_mfma_f32_16x16x32_bf16 v[180:183],v[92:95],v[220:223],v[180:183]
ds_read_b128 v[212:215],v7 offset:128
ds_read_b128 v[216:219],v11 offset:128
ds_read_b128 v[220:223],v15 offset:128
s_setprio 0
v_and_b32 v84,v32,v47
v_and_b32 v85,v33,v47
v_and_b32 v86,v34,v47
v_and_b32 v87,v35,v47
v_and_b32 v88,v32,v59
v_and_b32 v89,v33,v59
v_and_b32 v90,v34,v59
v_and_b32 v91,v35,v59
v_and_b32 v92,v32,v71
v_and_b32 v93,v33,v71
v_and_b32 v94,v34,v71
v_and_b32 v95,v35,v71
v_add_f32 v232,v232,v180
v_fmac_f32 v237,v180,v180
v_add_f32 v233,v233,v181
v_fmac_f32 v236,v181,v181
v_add_f32 v234,v234,v182
v_fmac_f32 v239,v182,v182
v_add_f32 v235,v235,v183
v_fmac_f32 v238,v183,v183
s_setprio 1
s_waitcnt lgkmcnt(3)
v_mfma_f32_16x16x32_bf16 v[184:187],v[72:75],v[200:203],0
v_mfma_f32_16x16x32_bf16 v[184:187],v[76:79],v[204:207],v[184:187]
v_mfma_f32_16x16x32_bf16 v[184:187],v[80:83],v[208:211],v[184:187]
s_setprio 0
s_nop 7
v_add_f32 v232,v232,v184
v_fmac_f32 v237,v184,v184
v_add_f32 v233,v233,v185
v_fmac_f32 v236,v185,v185
v_add_f32 v234,v234,v186
v_fmac_f32 v239,v186,v186
v_add_f32 v235,v235,v187
v_fmac_f32 v238,v187,v187
s_setprio 1
s_waitcnt lgkmcnt(0)
v_mfma_f32_16x16x32_bf16 v[188:191],v[84:87],v[212:215],0
v_mfma_f32_16x16x32_bf16 v[188:191],v[88:91],v[216:219],v[188:191]
v_mfma_f32_16x16x32_bf16 v[188:191],v[92:95],v[220:223],v[188:191]
s_setprio 0
ds_read_b64_tr_b16 v[200:201],v16 offset:0
ds_read_b64_tr_b16 v[202:203],v16 offset:512
ds_read_b64_tr_b16 v[204:205],v16 offset:1024
ds_read_b64_tr_b16 v[206:207],v16 offset:1536
ds_read_b64_tr_b16 v[208:209],v16 offset:2048
ds_read_b64_tr_b16 v[210:211],v16 offset:2560
ds_read_b64_tr_b16 v[212:213],v16 offset:3072
ds_read_b64_tr_b16 v[214:215],v16 offset:3584
ds_read_b64_tr_b16 v[216:217],v16 offset:4096
ds_read_b64_tr_b16 v[218:219],v16 offset:4608
ds_read_b64_tr_b16 v[220:221],v16 offset:5120
ds_read_b64_tr_b16 v[222:223],v16 offset:5632
v_add_f32 v232,v232,v188
v_fmac_f32 v237,v188,v188
v_add_f32 v233,v233,v189
v_fmac_f32 v236,v189,v189
v_add_f32 v234,v234,v190
v_fmac_f32 v239,v190,v190
v_add_f32 v235,v235,v191
v_fmac_f32 v238,v191,v191
v_add_f32 v232,v232,v233
v_add_f32 v234,v234,v235
v_add_f32 v237,v237,v236
v_add_f32 v239,v239,v238
v_add_f32 v240,v232,v234
v_add_f32 v241,v237,v239
s_nop 1
v_permlane32_swap_b32 v240,v241
v_add_f32 v240,v240,v241
v_mov_b32 v241,v240
s_nop 1
v_permlane32_swap_b32 v240,v241
v_mul_f32 v240,0x3c2aaaab,v240
v_mul_f32 v241,0x3c2aaaab,v241
v_fma_f32 v241,-v240,v240,v241
v_max_f32 v241,0,v241
v_add_f32 v241,0x3727c5ac,v241
v_rsq_f32 v241,v241
s_nop 0
v_mul_f32 v242,0xbeeff558,v241
v_mul_f32 v243,0x3eeff558,v241
v_mul_f32 v243,v240,v243
v_fma_f32 v144,v144,v242,v243
v_fma_f32 v145,v145,v242,v243
v_fma_f32 v146,v146,v242,v243
v_fma_f32 v147,v147,v242,v243
v_fma_f32 v148,v148,v242,v243
v_fma_f32 v149,v149,v242,v243
v_fma_f32 v150,v150,v242,v243
v_fma_f32 v151,v151,v242,v243
v_fma_f32 v152,v152,v242,v243
v_fma_f32 v153,v153,v242,v243
v_fma_f32 v154,v154,v242,v243
v_fma_f32 v155,v155,v242,v243
v_fma_f32 v156,v156,v242,v243
v_fma_f32 v157,v157,v242,v243
v_fma_f32 v158,v158,v242,v243
v_fma_f32 v159,v159,v242,v243
v_fma_f32 v160,v160,v242,v243
v_fma_f32 v161,v161,v242,v243
v_fma_f32 v162,v162,v242,v243
v_fma_f32 v163,v163,v242,v243
v_fma_f32 v164,v164,v242,v243
v_fma_f32 v165,v165,v242,v243
v_fma_f32 v166,v166,v242,v243
v_fma_f32 v167,v167,v242,v243
v_fma_f32 v168,v168,v242,v243
v_fma_f32 v169,v169,v242,v243
v_fma_f32 v170,v170,v242,v243
v_fma_f32 v171,v171,v242,v243
v_fma_f32 v172,v172,v242,v243
v_fma_f32 v173,v173,v242,v243
v_fma_f32 v174,v174,v242,v243
v_fma_f32 v175,v175,v242,v243
v_fma_f32 v176,v176,v242,v243
v_fma_f32 v177,v177,v242,v243
v_fma_f32 v178,v178,v242,v243
v_fma_f32 v179,v179,v242,v243
v_fma_f32 v180,v180,v242,v243
v_fma_f32 v181,v181,v242,v243
v_fma_f32 v182,v182,v242,v243
v_fma_f32 v183,v183,v242,v243
v_fma_f32 v184,v184,v242,v243
v_fma_f32 v185,v185,v242,v243
v_fma_f32 v186,v186,v242,v243
v_fma_f32 v187,v187,v242,v243
v_fma_f32 v188,v188,v242,v243
v_fma_f32 v189,v189,v242,v243
v_fma_f32 v190,v190,v242,v243
v_fma_f32 v191,v191,v242,v243
s_setprio 1
v_cvt_pk_bf16_f32 v72,v144,v145
v_cvt_pk_bf16_f32 v73,v146,v147
v_cvt_pk_bf16_f32 v74,v148,v149
v_cvt_pk_bf16_f32 v75,v150,v151
v_cvt_pk_bf16_f32 v76,v152,v153
v_cvt_pk_bf16_f32 v77,v154,v155
v_cvt_pk_bf16_f32 v78,v156,v157
v_cvt_pk_bf16_f32 v79,v158,v159
ds_read_b128 v[144:147],v18 offset:0
ds_read_b128 v[148:151],v18 offset:32
ds_read_b128 v[152:155],v18 offset:64
ds_read_b128 v[156:159],v18 offset:96
v_cvt_pk_bf16_f32 v80,v160,v161
v_cvt_pk_bf16_f32 v81,v162,v163
v_cvt_pk_bf16_f32 v82,v164,v165
v_cvt_pk_bf16_f32 v83,v166,v167
v_cvt_pk_bf16_f32 v84,v168,v169
v_cvt_pk_bf16_f32 v85,v170,v171
v_cvt_pk_bf16_f32 v86,v172,v173
v_cvt_pk_bf16_f32 v87,v174,v175
s_waitcnt lgkmcnt(0)
v_mfma_f32_32x32x16_bf16 v[144:159],v[200:203],v[72:75],v[144:159]
v_cvt_pk_bf16_f32 v88,v176,v177
v_cvt_pk_bf16_f32 v89,v178,v179
v_cvt_pk_bf16_f32 v90,v180,v181
v_cvt_pk_bf16_f32 v91,v182,v183
v_mfma_f32_32x32x16_bf16 v[144:159],v[204:207],v[76:79],v[144:159]
v_cvt_pk_bf16_f32 v92,v184,v185
v_cvt_pk_bf16_f32 v93,v186,v187
v_cvt_pk_bf16_f32 v94,v188,v189
v_cvt_pk_bf16_f32 v95,v190,v191
v_mfma_f32_32x32x16_bf16 v[144:159],v[208:211],v[80:83],v[144:159]
v_mfma_f32_32x32x16_bf16 v[144:159],v[212:215],v[84:87],v[144:159]
v_mfma_f32_32x32x16_bf16 v[144:159],v[216:219],v[88:91],v[144:159]
v_mfma_f32_32x32x16_bf16 v[144:159],v[220:223],v[92:95],v[144:159]
s_setprio 0
s_nop 7
s_nop 1
s_waitcnt vmcnt(2)
s_barrier
v_fmaak_f32 v176,v144,v144,0x409d30fc
v_fmaak_f32 v177,v145,v145,0x409d30fc
v_fmaak_f32 v178,v146,v146,0x409d30fc
v_fmaak_f32 v179,v147,v147,0x409d30fc
v_fmaak_f32 v180,v148,v148,0x409d30fc
v_fmaak_f32 v181,v149,v149,0x409d30fc
v_fmaak_f32 v182,v150,v150,0x409d30fc
v_fmaak_f32 v183,v151,v151,0x409d30fc
v_fmaak_f32 v184,v152,v152,0x409d30fc
v_fmaak_f32 v185,v153,v153,0x409d30fc
v_fmaak_f32 v186,v154,v154,0x409d30fc
v_fmaak_f32 v187,v155,v155,0x409d30fc
v_fmaak_f32 v188,v156,v156,0x409d30fc
v_fmaak_f32 v189,v157,v157,0x409d30fc
v_fmaak_f32 v190,v158,v158,0x409d30fc
v_fmaak_f32 v191,v159,v159,0x409d30fc
v_mul_f32 v176,v144,v176
v_mul_f32 v177,v145,v177
v_mul_f32 v178,v146,v178
v_mul_f32 v179,v147,v179
v_mul_f32 v180,v148,v180
v_mul_f32 v181,v149,v181
v_mul_f32 v182,v150,v182
v_mul_f32 v183,v151,v183
v_mul_f32 v184,v152,v184
v_mul_f32 v185,v153,v185
v_mul_f32 v186,v154,v186
v_mul_f32 v187,v155,v187
v_mul_f32 v188,v156,v188
v_mul_f32 v189,v157,v189
v_mul_f32 v190,v158,v190
v_mul_f32 v191,v159,v191
s_setprio 1
v_exp_f32 v176,v176
v_exp_f32 v177,v177
v_exp_f32 v178,v178
v_exp_f32 v179,v179
v_exp_f32 v180,v180
v_exp_f32 v181,v181
v_exp_f32 v182,v182
v_exp_f32 v183,v183
v_exp_f32 v184,v184
v_exp_f32 v185,v185
v_exp_f32 v186,v186
v_exp_f32 v187,v187
v_exp_f32 v188,v188
v_exp_f32 v189,v189
v_exp_f32 v190,v190
v_exp_f32 v191,v191
ds_read_b64_tr_b16 v[200:201],v16 offset:6144
ds_read_b64_tr_b16 v[202:203],v16 offset:6656
ds_read_b64_tr_b16 v[204:205],v16 offset:7168
ds_read_b64_tr_b16 v[206:207],v16 offset:7680
ds_read_b64_tr_b16 v[208:209],v16 offset:8192
ds_read_b64_tr_b16 v[210:211],v16 offset:8704
ds_read_b64_tr_b16 v[212:213],v16 offset:9216
ds_read_b64_tr_b16 v[214:215],v16 offset:9728
ds_read_b64_tr_b16 v[216:217],v16 offset:10240
ds_read_b64_tr_b16 v[218:219],v16 offset:10752
ds_read_b64_tr_b16 v[220:221],v16 offset:11264
ds_read_b64_tr_b16 v[222:223],v16 offset:11776
ds_read_b128 v[160:163],v18 offset:128
ds_read_b128 v[164:167],v18 offset:160
ds_read_b128 v[168:171],v18 offset:192
ds_read_b128 v[172:175],v18 offset:224
s_mov_b64 exec,s[60:61]
s_add_u32 m0,s58,148992
s_mov_b32 s64,6144
buffer_load_dwordx4 v28,s[48:51],s64 offen lds
s_add_u32 m0,s58,149760
s_mov_b32 s65,6912
buffer_load_dwordx4 v28,s[48:51],s65 offen lds
s_mov_b64 exec,-1
s_setprio 0
v_add_f32 v176,1.0,v176
v_add_f32 v177,1.0,v177
v_add_f32 v178,1.0,v178
v_add_f32 v179,1.0,v179
v_add_f32 v180,1.0,v180
v_add_f32 v181,1.0,v181
v_add_f32 v182,1.0,v182
v_add_f32 v183,1.0,v183
v_add_f32 v184,1.0,v184
v_add_f32 v185,1.0,v185
v_add_f32 v186,1.0,v186
v_add_f32 v187,1.0,v187
v_add_f32 v188,1.0,v188
v_add_f32 v189,1.0,v189
v_add_f32 v190,1.0,v190
v_add_f32 v191,1.0,v191
s_setprio 1
s_waitcnt lgkmcnt(0)
v_mfma_f32_32x32x16_bf16 v[160:175],v[200:203],v[72:75],v[160:175]
v_rcp_f32 v176,v176
v_rcp_f32 v177,v177
v_rcp_f32 v178,v178
v_mfma_f32_32x32x16_bf16 v[160:175],v[204:207],v[76:79],v[160:175]
v_rcp_f32 v179,v179
v_rcp_f32 v180,v180
v_rcp_f32 v181,v181
v_mfma_f32_32x32x16_bf16 v[160:175],v[208:211],v[80:83],v[160:175]
v_rcp_f32 v182,v182
v_rcp_f32 v183,v183
v_rcp_f32 v184,v184
v_mfma_f32_32x32x16_bf16 v[160:175],v[212:215],v[84:87],v[160:175]
v_rcp_f32 v185,v185
v_rcp_f32 v186,v186
v_rcp_f32 v187,v187
v_mfma_f32_32x32x16_bf16 v[160:175],v[216:219],v[88:91],v[160:175]
v_rcp_f32 v188,v188
v_rcp_f32 v189,v189
v_mfma_f32_32x32x16_bf16 v[160:175],v[220:223],v[92:95],v[160:175]
v_rcp_f32 v190,v190
v_rcp_f32 v191,v191
ds_read_b64_tr_b16 v[200:201],v17 offset:0
ds_read_b64_tr_b16 v[202:203],v17 offset:1536
ds_read_b64_tr_b16 v[204:205],v17 offset:3072
ds_read_b64_tr_b16 v[206:207],v17 offset:4608
ds_read_b64_tr_b16 v[208:209],v17 offset:64
ds_read_b64_tr_b16 v[210:211],v17 offset:1600
ds_read_b64_tr_b16 v[212:213],v17 offset:3136
ds_read_b64_tr_b16 v[214:215],v17 offset:4672
ds_read_b64_tr_b16 v[216:217],v17 offset:128
ds_read_b64_tr_b16 v[218:219],v17 offset:1664
ds_read_b64_tr_b16 v[220:221],v17 offset:3200
ds_read_b64_tr_b16 v[222:223],v17 offset:4736
s_mov_b64 exec,s[60:61]
s_add_u32 m0,s58,124416
s_movk_i32 s64,192
buffer_load_dwordx4 v27,s[44:47],s64 offen lds
s_add_u32 m0,s58,125184
s_movk_i32 s65,9408
buffer_load_dwordx4 v27,s[44:47],s65 offen lds
s_mov_b64 exec,-1
s_setprio 0
v_mul_f32 v176,v144,v176
v_mul_f32 v177,v145,v177
v_mul_f32 v178,v146,v178
v_mul_f32 v179,v147,v179
v_mul_f32 v180,v148,v180
v_mul_f32 v181,v149,v181
v_mul_f32 v182,v150,v182
v_mul_f32 v183,v151,v183
v_mul_f32 v184,v152,v184
v_mul_f32 v185,v153,v185
v_mul_f32 v186,v154,v186
v_mul_f32 v187,v155,v187
v_mul_f32 v188,v156,v188
v_mul_f32 v189,v157,v189
v_mul_f32 v190,v158,v190
v_mul_f32 v191,v159,v191
v_cvt_pk_bf16_f32 v192,v176,v177
v_cvt_pk_bf16_f32 v193,v178,v179
v_cvt_pk_bf16_f32 v194,v180,v181
v_cvt_pk_bf16_f32 v195,v182,v183
v_cvt_pk_bf16_f32 v196,v184,v185
v_cvt_pk_bf16_f32 v197,v186,v187
v_cvt_pk_bf16_f32 v198,v188,v189
v_cvt_pk_bf16_f32 v199,v190,v191
s_waitcnt lgkmcnt(0)
s_waitcnt vmcnt(2)
s_barrier
v_fmaak_f32 v176,v160,v160,0x409d30fc
v_fmaak_f32 v177,v161,v161,0x409d30fc
v_fmaak_f32 v178,v162,v162,0x409d30fc
v_fmaak_f32 v179,v163,v163,0x409d30fc
v_fmaak_f32 v180,v164,v164,0x409d30fc
v_fmaak_f32 v181,v165,v165,0x409d30fc
v_fmaak_f32 v182,v166,v166,0x409d30fc
v_fmaak_f32 v183,v167,v167,0x409d30fc
v_fmaak_f32 v184,v168,v168,0x409d30fc
v_fmaak_f32 v185,v169,v169,0x409d30fc
v_fmaak_f32 v186,v170,v170,0x409d30fc
v_fmaak_f32 v187,v171,v171,0x409d30fc
v_fmaak_f32 v188,v172,v172,0x409d30fc
v_fmaak_f32 v189,v173,v173,0x409d30fc
v_fmaak_f32 v190,v174,v174,0x409d30fc
v_fmaak_f32 v191,v175,v175,0x409d30fc
v_mul_f32 v176,v160,v176
v_mul_f32 v177,v161,v177
v_mul_f32 v178,v162,v178
v_mul_f32 v179,v163,v179
v_mul_f32 v180,v164,v180
v_mul_f32 v181,v165,v181
v_mul_f32 v182,v166,v182
v_mul_f32 v183,v167,v183
v_mul_f32 v184,v168,v184
v_mul_f32 v185,v169,v185
v_mul_f32 v186,v170,v186
v_mul_f32 v187,v171,v187
v_mul_f32 v188,v172,v188
v_mul_f32 v189,v173,v189
v_mul_f32 v190,v174,v190
v_mul_f32 v191,v175,v191
s_setprio 1
v_mfma_f32_32x32x16_bf16 v[96:111],v[200:203],v[192:195],v[96:111]
v_exp_f32 v176,v176
v_exp_f32 v177,v177
v_exp_f32 v178,v178
v_mfma_f32_32x32x16_bf16 v[96:111],v[204:207],v[196:199],v[96:111]
v_exp_f32 v179,v179
v_exp_f32 v180,v180
v_exp_f32 v181,v181
v_mfma_f32_32x32x16_bf16 v[112:127],v[208:211],v[192:195],v[112:127]
v_exp_f32 v182,v182
v_exp_f32 v183,v183
v_exp_f32 v184,v184
v_mfma_f32_32x32x16_bf16 v[112:127],v[212:215],v[196:199],v[112:127]
v_exp_f32 v185,v185
v_exp_f32 v186,v186
v_exp_f32 v187,v187
v_mfma_f32_32x32x16_bf16 v[128:143],v[216:219],v[192:195],v[128:143]
v_exp_f32 v188,v188
v_exp_f32 v189,v189
v_mfma_f32_32x32x16_bf16 v[128:143],v[220:223],v[196:199],v[128:143]
v_exp_f32 v190,v190
v_exp_f32 v191,v191
ds_read_b64_tr_b16 v[200:201],v16 offset:12288
ds_read_b64_tr_b16 v[202:203],v16 offset:12800
ds_read_b64_tr_b16 v[204:205],v16 offset:13312
ds_read_b64_tr_b16 v[206:207],v16 offset:13824
ds_read_b64_tr_b16 v[208:209],v16 offset:14336
ds_read_b64_tr_b16 v[210:211],v16 offset:14848
ds_read_b64_tr_b16 v[212:213],v16 offset:15360
ds_read_b64_tr_b16 v[214:215],v16 offset:15872
ds_read_b64_tr_b16 v[216:217],v16 offset:16384
ds_read_b64_tr_b16 v[218:219],v16 offset:16896
ds_read_b64_tr_b16 v[220:221],v16 offset:17408
ds_read_b64_tr_b16 v[222:223],v16 offset:17920
ds_read_b128 v[144:147],v18 offset:256
ds_read_b128 v[148:151],v18 offset:288
ds_read_b128 v[152:155],v18 offset:320
ds_read_b128 v[156:159],v18 offset:352
s_mov_b64 exec,s[60:61]
s_add_u32 m0,s58,155136
s_mov_b32 s64,12288
buffer_load_dwordx4 v28,s[48:51],s64 offen lds
s_add_u32 m0,s58,155904
s_mov_b32 s65,13056
buffer_load_dwordx4 v28,s[48:51],s65 offen lds
s_mov_b64 exec,-1
s_setprio 0
v_add_f32 v176,1.0,v176
v_add_f32 v177,1.0,v177
v_add_f32 v178,1.0,v178
v_add_f32 v179,1.0,v179
v_add_f32 v180,1.0,v180
v_add_f32 v181,1.0,v181
v_add_f32 v182,1.0,v182
v_add_f32 v183,1.0,v183
v_add_f32 v184,1.0,v184
v_add_f32 v185,1.0,v185
v_add_f32 v186,1.0,v186
v_add_f32 v187,1.0,v187
v_add_f32 v188,1.0,v188
v_add_f32 v189,1.0,v189
v_add_f32 v190,1.0,v190
v_add_f32 v191,1.0,v191
s_setprio 1
s_waitcnt lgkmcnt(0)
v_mfma_f32_32x32x16_bf16 v[144:159],v[200:203],v[72:75],v[144:159]
v_rcp_f32 v176,v176
v_rcp_f32 v177,v177
v_rcp_f32 v178,v178
v_mfma_f32_32x32x16_bf16 v[144:159],v[204:207],v[76:79],v[144:159]
v_rcp_f32 v179,v179
v_rcp_f32 v180,v180
v_rcp_f32 v181,v181
v_mfma_f32_32x32x16_bf16 v[144:159],v[208:211],v[80:83],v[144:159]
v_rcp_f32 v182,v182
v_rcp_f32 v183,v183
v_rcp_f32 v184,v184
v_mfma_f32_32x32x16_bf16 v[144:159],v[212:215],v[84:87],v[144:159]
v_rcp_f32 v185,v185
v_rcp_f32 v186,v186
v_rcp_f32 v187,v187
v_mfma_f32_32x32x16_bf16 v[144:159],v[216:219],v[88:91],v[144:159]
v_rcp_f32 v188,v188
v_rcp_f32 v189,v189
v_mfma_f32_32x32x16_bf16 v[144:159],v[220:223],v[92:95],v[144:159]
v_rcp_f32 v190,v190
v_rcp_f32 v191,v191
ds_read_b64_tr_b16 v[200:201],v17 offset:6144
ds_read_b64_tr_b16 v[202:203],v17 offset:7680
ds_read_b64_tr_b16 v[204:205],v17 offset:9216
ds_read_b64_tr_b16 v[206:207],v17 offset:10752
ds_read_b64_tr_b16 v[208:209],v17 offset:6208
ds_read_b64_tr_b16 v[210:211],v17 offset:7744
ds_read_b64_tr_b16 v[212:213],v17 offset:9280
ds_read_b64_tr_b16 v[214:215],v17 offset:10816
ds_read_b64_tr_b16 v[216:217],v17 offset:6272
ds_read_b64_tr_b16 v[218:219],v17 offset:7808
ds_read_b64_tr_b16 v[220:221],v17 offset:9344
ds_read_b64_tr_b16 v[222:223],v17 offset:10880
s_mov_b64 exec,s[60:61]
s_add_u32 m0,s58,130560
s_movk_i32 s64,256
buffer_load_dwordx4 v27,s[44:47],s64 offen lds
s_add_u32 m0,s58,131328
s_movk_i32 s65,9472
buffer_load_dwordx4 v27,s[44:47],s65 offen lds
s_mov_b64 exec,-1
s_setprio 0
v_mul_f32 v176,v160,v176
v_mul_f32 v177,v161,v177
v_mul_f32 v178,v162,v178
v_mul_f32 v179,v163,v179
v_mul_f32 v180,v164,v180
v_mul_f32 v181,v165,v181
v_mul_f32 v182,v166,v182
v_mul_f32 v183,v167,v183
v_mul_f32 v184,v168,v184
v_mul_f32 v185,v169,v185
v_mul_f32 v186,v170,v186
v_mul_f32 v187,v171,v187
v_mul_f32 v188,v172,v188
v_mul_f32 v189,v173,v189
v_mul_f32 v190,v174,v190
v_mul_f32 v191,v175,v191
v_cvt_pk_bf16_f32 v192,v176,v177
v_cvt_pk_bf16_f32 v193,v178,v179
v_cvt_pk_bf16_f32 v194,v180,v181
v_cvt_pk_bf16_f32 v195,v182,v183
v_cvt_pk_bf16_f32 v196,v184,v185
v_cvt_pk_bf16_f32 v197,v186,v187
v_cvt_pk_bf16_f32 v198,v188,v189
v_cvt_pk_bf16_f32 v199,v190,v191
s_waitcnt lgkmcnt(0)
s_waitcnt vmcnt(2)
s_barrier
v_fmaak_f32 v176,v144,v144,0x409d30fc
v_fmaak_f32 v177,v145,v145,0x409d30fc
v_fmaak_f32 v178,v146,v146,0x409d30fc
v_fmaak_f32 v179,v147,v147,0x409d30fc
v_fmaak_f32 v180,v148,v148,0x409d30fc
v_fmaak_f32 v181,v149,v149,0x409d30fc
v_fmaak_f32 v182,v150,v150,0x409d30fc
v_fmaak_f32 v183,v151,v151,0x409d30fc
v_fmaak_f32 v184,v152,v152,0x409d30fc
v_fmaak_f32 v185,v153,v153,0x409d30fc
v_fmaak_f32 v186,v154,v154,0x409d30fc
v_fmaak_f32 v187,v155,v155,0x409d30fc
v_fmaak_f32 v188,v156,v156,0x409d30fc
v_fmaak_f32 v189,v157,v157,0x409d30fc
v_fmaak_f32 v190,v158,v158,0x409d30fc
v_fmaak_f32 v191,v159,v159,0x409d30fc
v_mul_f32 v176,v144,v176
v_mul_f32 v177,v145,v177
v_mul_f32 v178,v146,v178
v_mul_f32 v179,v147,v179
v_mul_f32 v180,v148,v180
v_mul_f32 v181,v149,v181
v_mul_f32 v182,v150,v182
v_mul_f32 v183,v151,v183
v_mul_f32 v184,v152,v184
v_mul_f32 v185,v153,v185
v_mul_f32 v186,v154,v186
v_mul_f32 v187,v155,v187
v_mul_f32 v188,v156,v188
v_mul_f32 v189,v157,v189
v_mul_f32 v190,v158,v190
v_mul_f32 v191,v159,v191
s_setprio 1
v_mfma_f32_32x32x16_bf16 v[96:111],v[200:203],v[192:195],v[96:111]
v_exp_f32 v176,v176
v_exp_f32 v177,v177
v_exp_f32 v178,v178
v_mfma_f32_32x32x16_bf16 v[96:111],v[204:207],v[196:199],v[96:111]
v_exp_f32 v179,v179
v_exp_f32 v180,v180
v_exp_f32 v181,v181
v_mfma_f32_32x32x16_bf16 v[112:127],v[208:211],v[192:195],v[112:127]
v_exp_f32 v182,v182
v_exp_f32 v183,v183
v_exp_f32 v184,v184
v_mfma_f32_32x32x16_bf16 v[112:127],v[212:215],v[196:199],v[112:127]
v_exp_f32 v185,v185
v_exp_f32 v186,v186
v_exp_f32 v187,v187
v_mfma_f32_32x32x16_bf16 v[128:143],v[216:219],v[192:195],v[128:143]
v_exp_f32 v188,v188
v_exp_f32 v189,v189
v_mfma_f32_32x32x16_bf16 v[128:143],v[220:223],v[196:199],v[128:143]
v_exp_f32 v190,v190
v_exp_f32 v191,v191
ds_read_b64_tr_b16 v[200:201],v16 offset:0
ds_read_b64_tr_b16 v[202:203],v16 offset:512
ds_read_b64_tr_b16 v[204:205],v16 offset:1024
ds_read_b64_tr_b16 v[206:207],v16 offset:1536
ds_read_b64_tr_b16 v[208:209],v16 offset:2048
ds_read_b64_tr_b16 v[210:211],v16 offset:2560
ds_read_b64_tr_b16 v[212:213],v16 offset:3072
ds_read_b64_tr_b16 v[214:215],v16 offset:3584
ds_read_b64_tr_b16 v[216:217],v16 offset:4096
ds_read_b64_tr_b16 v[218:219],v16 offset:4608
ds_read_b64_tr_b16 v[220:221],v16 offset:5120
ds_read_b64_tr_b16 v[222:223],v16 offset:5632
ds_read_b128 v[160:163],v18 offset:384
ds_read_b128 v[164:167],v18 offset:416
ds_read_b128 v[168:171],v18 offset:448
ds_read_b128 v[172:175],v18 offset:480
s_mov_b64 exec,s[60:61]
s_add_u32 m0,s58,142848
s_mov_b32 s64,18432
buffer_load_dwordx4 v28,s[48:51],s64 offen lds
s_add_u32 m0,s58,143616
s_mov_b32 s65,19200
buffer_load_dwordx4 v28,s[48:51],s65 offen lds
s_mov_b64 exec,-1
s_setprio 0
v_add_f32 v176,1.0,v176
v_add_f32 v177,1.0,v177
v_add_f32 v178,1.0,v178
v_add_f32 v179,1.0,v179
v_add_f32 v180,1.0,v180
v_add_f32 v181,1.0,v181
v_add_f32 v182,1.0,v182
v_add_f32 v183,1.0,v183
v_add_f32 v184,1.0,v184
v_add_f32 v185,1.0,v185
v_add_f32 v186,1.0,v186
v_add_f32 v187,1.0,v187
v_add_f32 v188,1.0,v188
v_add_f32 v189,1.0,v189
v_add_f32 v190,1.0,v190
v_add_f32 v191,1.0,v191
s_setprio 1
s_waitcnt lgkmcnt(0)
v_mfma_f32_32x32x16_bf16 v[160:175],v[200:203],v[72:75],v[160:175]
v_rcp_f32 v176,v176
v_rcp_f32 v177,v177
v_rcp_f32 v178,v178
v_mfma_f32_32x32x16_bf16 v[160:175],v[204:207],v[76:79],v[160:175]
v_rcp_f32 v179,v179
v_rcp_f32 v180,v180
v_rcp_f32 v181,v181
v_mfma_f32_32x32x16_bf16 v[160:175],v[208:211],v[80:83],v[160:175]
v_rcp_f32 v182,v182
v_rcp_f32 v183,v183
v_rcp_f32 v184,v184
v_mfma_f32_32x32x16_bf16 v[160:175],v[212:215],v[84:87],v[160:175]
v_rcp_f32 v185,v185
v_rcp_f32 v186,v186
v_rcp_f32 v187,v187
v_mfma_f32_32x32x16_bf16 v[160:175],v[216:219],v[88:91],v[160:175]
v_rcp_f32 v188,v188
v_rcp_f32 v189,v189
v_mfma_f32_32x32x16_bf16 v[160:175],v[220:223],v[92:95],v[160:175]
v_rcp_f32 v190,v190
v_rcp_f32 v191,v191
ds_read_b64_tr_b16 v[200:201],v17 offset:12288
ds_read_b64_tr_b16 v[202:203],v17 offset:13824
ds_read_b64_tr_b16 v[204:205],v17 offset:15360
ds_read_b64_tr_b16 v[206:207],v17 offset:16896
ds_read_b64_tr_b16 v[208:209],v17 offset:12352
ds_read_b64_tr_b16 v[210:211],v17 offset:13888
ds_read_b64_tr_b16 v[212:213],v17 offset:15424
ds_read_b64_tr_b16 v[214:215],v17 offset:16960
ds_read_b64_tr_b16 v[216:217],v17 offset:12416
ds_read_b64_tr_b16 v[218:219],v17 offset:13952
ds_read_b64_tr_b16 v[220:221],v17 offset:15488
ds_read_b64_tr_b16 v[222:223],v17 offset:17024
s_mov_b64 exec,s[60:61]
s_add_u32 m0,s58,136704
s_movk_i32 s64,320
buffer_load_dwordx4 v27,s[44:47],s64 offen lds
s_add_u32 m0,s58,137472
s_movk_i32 s65,9536
buffer_load_dwordx4 v27,s[44:47],s65 offen lds
s_mov_b64 exec,-1
s_setprio 0
v_mul_f32 v176,v144,v176
v_mul_f32 v177,v145,v177
v_mul_f32 v178,v146,v178
v_mul_f32 v179,v147,v179
v_mul_f32 v180,v148,v180
v_mul_f32 v181,v149,v181
v_mul_f32 v182,v150,v182
v_mul_f32 v183,v151,v183
v_mul_f32 v184,v152,v184
v_mul_f32 v185,v153,v185
v_mul_f32 v186,v154,v186
v_mul_f32 v187,v155,v187
v_mul_f32 v188,v156,v188
v_mul_f32 v189,v157,v189
v_mul_f32 v190,v158,v190
v_mul_f32 v191,v159,v191
v_cvt_pk_bf16_f32 v192,v176,v177
v_cvt_pk_bf16_f32 v193,v178,v179
v_cvt_pk_bf16_f32 v194,v180,v181
v_cvt_pk_bf16_f32 v195,v182,v183
v_cvt_pk_bf16_f32 v196,v184,v185
v_cvt_pk_bf16_f32 v197,v186,v187
v_cvt_pk_bf16_f32 v198,v188,v189
v_cvt_pk_bf16_f32 v199,v190,v191
s_waitcnt lgkmcnt(0)
s_waitcnt vmcnt(2)
s_barrier
v_fmaak_f32 v176,v160,v160,0x409d30fc
v_fmaak_f32 v177,v161,v161,0x409d30fc
v_fmaak_f32 v178,v162,v162,0x409d30fc
v_fmaak_f32 v179,v163,v163,0x409d30fc
v_fmaak_f32 v180,v164,v164,0x409d30fc
v_fmaak_f32 v181,v165,v165,0x409d30fc
v_fmaak_f32 v182,v166,v166,0x409d30fc
v_fmaak_f32 v183,v167,v167,0x409d30fc
v_fmaak_f32 v184,v168,v168,0x409d30fc
v_fmaak_f32 v185,v169,v169,0x409d30fc
v_fmaak_f32 v186,v170,v170,0x409d30fc
v_fmaak_f32 v187,v171,v171,0x409d30fc
v_fmaak_f32 v188,v172,v172,0x409d30fc
v_fmaak_f32 v189,v173,v173,0x409d30fc
v_fmaak_f32 v190,v174,v174,0x409d30fc
v_fmaak_f32 v191,v175,v175,0x409d30fc
v_mul_f32 v176,v160,v176
v_mul_f32 v177,v161,v177
v_mul_f32 v178,v162,v178
v_mul_f32 v179,v163,v179
v_mul_f32 v180,v164,v180
v_mul_f32 v181,v165,v181
v_mul_f32 v182,v166,v182
v_mul_f32 v183,v167,v183
v_mul_f32 v184,v168,v184
v_mul_f32 v185,v169,v185
v_mul_f32 v186,v170,v186
v_mul_f32 v187,v171,v187
v_mul_f32 v188,v172,v188
v_mul_f32 v189,v173,v189
v_mul_f32 v190,v174,v190
v_mul_f32 v191,v175,v191
s_setprio 1
v_mfma_f32_32x32x16_bf16 v[96:111],v[200:203],v[192:195],v[96:111]
v_exp_f32 v176,v176
v_exp_f32 v177,v177
v_exp_f32 v178,v178
v_mfma_f32_32x32x16_bf16 v[96:111],v[204:207],v[196:199],v[96:111]
v_exp_f32 v179,v179
v_exp_f32 v180,v180
v_exp_f32 v181,v181
v_mfma_f32_32x32x16_bf16 v[112:127],v[208:211],v[192:195],v[112:127]
v_exp_f32 v182,v182
v_exp_f32 v183,v183
v_exp_f32 v184,v184
v_mfma_f32_32x32x16_bf16 v[112:127],v[212:215],v[196:199],v[112:127]
v_exp_f32 v185,v185
v_exp_f32 v186,v186
v_exp_f32 v187,v187
v_mfma_f32_32x32x16_bf16 v[128:143],v[216:219],v[192:195],v[128:143]
v_exp_f32 v188,v188
v_exp_f32 v189,v189
v_mfma_f32_32x32x16_bf16 v[128:143],v[220:223],v[196:199],v[128:143]
v_exp_f32 v190,v190
v_exp_f32 v191,v191
ds_read_b64_tr_b16 v[200:201],v16 offset:6144
ds_read_b64_tr_b16 v[202:203],v16 offset:6656
ds_read_b64_tr_b16 v[204:205],v16 offset:7168
ds_read_b64_tr_b16 v[206:207],v16 offset:7680
ds_read_b64_tr_b16 v[208:209],v16 offset:8192
ds_read_b64_tr_b16 v[210:211],v16 offset:8704
ds_read_b64_tr_b16 v[212:213],v16 offset:9216
ds_read_b64_tr_b16 v[214:215],v16 offset:9728
ds_read_b64_tr_b16 v[216:217],v16 offset:10240
ds_read_b64_tr_b16 v[218:219],v16 offset:10752
ds_read_b64_tr_b16 v[220:221],v16 offset:11264
ds_read_b64_tr_b16 v[222:223],v16 offset:11776
ds_read_b128 v[144:147],v18 offset:512
ds_read_b128 v[148:151],v18 offset:544
ds_read_b128 v[152:155],v18 offset:576
ds_read_b128 v[156:159],v18 offset:608
s_mov_b64 exec,s[60:61]
s_add_u32 m0,s58,148992
s_mov_b32 s64,24576
buffer_load_dwordx4 v28,s[48:51],s64 offen lds
s_add_u32 m0,s58,149760
s_mov_b32 s65,25344
buffer_load_dwordx4 v28,s[48:51],s65 offen lds
s_mov_b64 exec,-1
s_setprio 0
v_add_f32 v176,1.0,v176
v_add_f32 v177,1.0,v177
v_add_f32 v178,1.0,v178
v_add_f32 v179,1.0,v179
v_add_f32 v180,1.0,v180
v_add_f32 v181,1.0,v181
v_add_f32 v182,1.0,v182
v_add_f32 v183,1.0,v183
v_add_f32 v184,1.0,v184
v_add_f32 v185,1.0,v185
v_add_f32 v186,1.0,v186
v_add_f32 v187,1.0,v187
v_add_f32 v188,1.0,v188
v_add_f32 v189,1.0,v189
v_add_f32 v190,1.0,v190
v_add_f32 v191,1.0,v191
s_setprio 1
s_waitcnt lgkmcnt(0)
v_mfma_f32_32x32x16_bf16 v[144:159],v[200:203],v[72:75],v[144:159]
v_rcp_f32 v176,v176
v_rcp_f32 v177,v177
v_rcp_f32 v178,v178
v_mfma_f32_32x32x16_bf16 v[144:159],v[204:207],v[76:79],v[144:159]
v_rcp_f32 v179,v179
v_rcp_f32 v180,v180
v_rcp_f32 v181,v181
v_mfma_f32_32x32x16_bf16 v[144:159],v[208:211],v[80:83],v[144:159]
v_rcp_f32 v182,v182
v_rcp_f32 v183,v183
v_rcp_f32 v184,v184
v_mfma_f32_32x32x16_bf16 v[144:159],v[212:215],v[84:87],v[144:159]
v_rcp_f32 v185,v185
v_rcp_f32 v186,v186
v_rcp_f32 v187,v187
v_mfma_f32_32x32x16_bf16 v[144:159],v[216:219],v[88:91],v[144:159]
v_rcp_f32 v188,v188
v_rcp_f32 v189,v189
v_mfma_f32_32x32x16_bf16 v[144:159],v[220:223],v[92:95],v[144:159]
v_rcp_f32 v190,v190
v_rcp_f32 v191,v191
ds_read_b64_tr_b16 v[200:201],v17 offset:0
ds_read_b64_tr_b16 v[202:203],v17 offset:1536
ds_read_b64_tr_b16 v[204:205],v17 offset:3072
ds_read_b64_tr_b16 v[206:207],v17 offset:4608
ds_read_b64_tr_b16 v[208:209],v17 offset:64
ds_read_b64_tr_b16 v[210:211],v17 offset:1600
ds_read_b64_tr_b16 v[212:213],v17 offset:3136
ds_read_b64_tr_b16 v[214:215],v17 offset:4672
ds_read_b64_tr_b16 v[216:217],v17 offset:128
ds_read_b64_tr_b16 v[218:219],v17 offset:1664
ds_read_b64_tr_b16 v[220:221],v17 offset:3200
ds_read_b64_tr_b16 v[222:223],v17 offset:4736
s_mov_b64 exec,s[60:61]
s_add_u32 m0,s58,124416
s_movk_i32 s64,384
buffer_load_dwordx4 v27,s[44:47],s64 offen lds
s_add_u32 m0,s58,125184
s_movk_i32 s65,9600
buffer_load_dwordx4 v27,s[44:47],s65 offen lds
s_mov_b64 exec,-1
s_setprio 0
v_mul_f32 v176,v160,v176
v_mul_f32 v177,v161,v177
v_mul_f32 v178,v162,v178
v_mul_f32 v179,v163,v179
v_mul_f32 v180,v164,v180
v_mul_f32 v181,v165,v181
v_mul_f32 v182,v166,v182
v_mul_f32 v183,v167,v183
v_mul_f32 v184,v168,v184
v_mul_f32 v185,v169,v185
v_mul_f32 v186,v170,v186
v_mul_f32 v187,v171,v187
v_mul_f32 v188,v172,v188
v_mul_f32 v189,v173,v189
v_mul_f32 v190,v174,v190
v_mul_f32 v191,v175,v191
v_cvt_pk_bf16_f32 v192,v176,v177
v_cvt_pk_bf16_f32 v193,v178,v179
v_cvt_pk_bf16_f32 v194,v180,v181
v_cvt_pk_bf16_f32 v195,v182,v183
v_cvt_pk_bf16_f32 v196,v184,v185
v_cvt_pk_bf16_f32 v197,v186,v187
v_cvt_pk_bf16_f32 v198,v188,v189
v_cvt_pk_bf16_f32 v199,v190,v191
s_waitcnt lgkmcnt(0)
s_waitcnt vmcnt(2)
s_barrier
v_fmaak_f32 v176,v144,v144,0x409d30fc
v_fmaak_f32 v177,v145,v145,0x409d30fc
v_fmaak_f32 v178,v146,v146,0x409d30fc
v_fmaak_f32 v179,v147,v147,0x409d30fc
v_fmaak_f32 v180,v148,v148,0x409d30fc
v_fmaak_f32 v181,v149,v149,0x409d30fc
v_fmaak_f32 v182,v150,v150,0x409d30fc
v_fmaak_f32 v183,v151,v151,0x409d30fc
v_fmaak_f32 v184,v152,v152,0x409d30fc
v_fmaak_f32 v185,v153,v153,0x409d30fc
v_fmaak_f32 v186,v154,v154,0x409d30fc
v_fmaak_f32 v187,v155,v155,0x409d30fc
v_fmaak_f32 v188,v156,v156,0x409d30fc
v_fmaak_f32 v189,v157,v157,0x409d30fc
v_fmaak_f32 v190,v158,v158,0x409d30fc
v_fmaak_f32 v191,v159,v159,0x409d30fc
v_mul_f32 v176,v144,v176
v_mul_f32 v177,v145,v177
v_mul_f32 v178,v146,v178
v_mul_f32 v179,v147,v179
v_mul_f32 v180,v148,v180
v_mul_f32 v181,v149,v181
v_mul_f32 v182,v150,v182
v_mul_f32 v183,v151,v183
v_mul_f32 v184,v152,v184
v_mul_f32 v185,v153,v185
v_mul_f32 v186,v154,v186
v_mul_f32 v187,v155,v187
v_mul_f32 v188,v156,v188
v_mul_f32 v189,v157,v189
v_mul_f32 v190,v158,v190
v_mul_f32 v191,v159,v191
s_setprio 1
v_mfma_f32_32x32x16_bf16 v[96:111],v[200:203],v[192:195],v[96:111]
v_exp_f32 v176,v176
v_exp_f32 v177,v177
v_exp_f32 v178,v178
v_mfma_f32_32x32x16_bf16 v[96:111],v[204:207],v[196:199],v[96:111]
v_exp_f32 v179,v179
v_exp_f32 v180,v180
v_exp_f32 v181,v181
v_mfma_f32_32x32x16_bf16 v[112:127],v[208:211],v[192:195],v[112:127]
v_exp_f32 v182,v182
v_exp_f32 v183,v183
v_exp_f32 v184,v184
v_mfma_f32_32x32x16_bf16 v[112:127],v[212:215],v[196:199],v[112:127]
v_exp_f32 v185,v185
v_exp_f32 v186,v186
v_exp_f32 v187,v187
v_mfma_f32_32x32x16_bf16 v[128:143],v[216:219],v[192:195],v[128:143]
v_exp_f32 v188,v188
v_exp_f32 v189,v189
v_mfma_f32_32x32x16_bf16 v[128:143],v[220:223],v[196:199],v[128:143]
v_exp_f32 v190,v190
v_exp_f32 v191,v191
ds_read_b64_tr_b16 v[200:201],v16 offset:12288
ds_read_b64_tr_b16 v[202:203],v16 offset:12800
ds_read_b64_tr_b16 v[204:205],v16 offset:13312
ds_read_b64_tr_b16 v[206:207],v16 offset:13824
ds_read_b64_tr_b16 v[208:209],v16 offset:14336
ds_read_b64_tr_b16 v[210:211],v16 offset:14848
ds_read_b64_tr_b16 v[212:213],v16 offset:15360
ds_read_b64_tr_b16 v[214:215],v16 offset:15872
ds_read_b64_tr_b16 v[216:217],v16 offset:16384
ds_read_b64_tr_b16 v[218:219],v16 offset:16896
ds_read_b64_tr_b16 v[220:221],v16 offset:17408
ds_read_b64_tr_b16 v[222:223],v16 offset:17920
ds_read_b128 v[160:163],v18 offset:640
ds_read_b128 v[164:167],v18 offset:672
ds_read_b128 v[168:171],v18 offset:704
ds_read_b128 v[172:175],v18 offset:736
s_mov_b64 exec,s[60:61]
s_add_u32 m0,s58,155136
s_mov_b32 s64,30720
buffer_load_dwordx4 v28,s[48:51],s64 offen lds
s_add_u32 m0,s58,155904
s_mov_b32 s65,31488
buffer_load_dwordx4 v28,s[48:51],s65 offen lds
s_mov_b64 exec,-1
s_setprio 0
v_add_f32 v176,1.0,v176
v_add_f32 v177,1.0,v177
v_add_f32 v178,1.0,v178
v_add_f32 v179,1.0,v179
v_add_f32 v180,1.0,v180
v_add_f32 v181,1.0,v181
v_add_f32 v182,1.0,v182
v_add_f32 v183,1.0,v183
v_add_f32 v184,1.0,v184
v_add_f32 v185,1.0,v185
v_add_f32 v186,1.0,v186
v_add_f32 v187,1.0,v187
v_add_f32 v188,1.0,v188
v_add_f32 v189,1.0,v189
v_add_f32 v190,1.0,v190
v_add_f32 v191,1.0,v191
s_setprio 1
s_waitcnt lgkmcnt(0)
v_mfma_f32_32x32x16_bf16 v[160:175],v[200:203],v[72:75],v[160:175]
v_rcp_f32 v176,v176
v_rcp_f32 v177,v177
v_rcp_f32 v178,v178
v_mfma_f32_32x32x16_bf16 v[160:175],v[204:207],v[76:79],v[160:175]
v_rcp_f32 v179,v179
v_rcp_f32 v180,v180
v_rcp_f32 v181,v181
v_mfma_f32_32x32x16_bf16 v[160:175],v[208:211],v[80:83],v[160:175]
v_rcp_f32 v182,v182
v_rcp_f32 v183,v183
v_rcp_f32 v184,v184
v_mfma_f32_32x32x16_bf16 v[160:175],v[212:215],v[84:87],v[160:175]
v_rcp_f32 v185,v185
v_rcp_f32 v186,v186
v_rcp_f32 v187,v187
v_mfma_f32_32x32x16_bf16 v[160:175],v[216:219],v[88:91],v[160:175]
v_rcp_f32 v188,v188
v_rcp_f32 v189,v189
v_mfma_f32_32x32x16_bf16 v[160:175],v[220:223],v[92:95],v[160:175]
v_rcp_f32 v190,v190
v_rcp_f32 v191,v191
ds_read_b64_tr_b16 v[200:201],v17 offset:6144
ds_read_b64_tr_b16 v[202:203],v17 offset:7680
ds_read_b64_tr_b16 v[204:205],v17 offset:9216
ds_read_b64_tr_b16 v[206:207],v17 offset:10752
ds_read_b64_tr_b16 v[208:209],v17 offset:6208
ds_read_b64_tr_b16 v[210:211],v17 offset:7744
ds_read_b64_tr_b16 v[212:213],v17 offset:9280
ds_read_b64_tr_b16 v[214:215],v17 offset:10816
ds_read_b64_tr_b16 v[216:217],v17 offset:6272
ds_read_b64_tr_b16 v[218:219],v17 offset:7808
ds_read_b64_tr_b16 v[220:221],v17 offset:9344
ds_read_b64_tr_b16 v[222:223],v17 offset:10880
s_mov_b64 exec,s[60:61]
s_add_u32 m0,s58,130560
s_movk_i32 s64,448
buffer_load_dwordx4 v27,s[44:47],s64 offen lds
s_add_u32 m0,s58,131328
s_movk_i32 s65,9664
buffer_load_dwordx4 v27,s[44:47],s65 offen lds
s_mov_b64 exec,-1
s_setprio 0
v_mul_f32 v176,v144,v176
v_mul_f32 v177,v145,v177
v_mul_f32 v178,v146,v178
v_mul_f32 v179,v147,v179
v_mul_f32 v180,v148,v180
v_mul_f32 v181,v149,v181
v_mul_f32 v182,v150,v182
v_mul_f32 v183,v151,v183
v_mul_f32 v184,v152,v184
v_mul_f32 v185,v153,v185
v_mul_f32 v186,v154,v186
v_mul_f32 v187,v155,v187
v_mul_f32 v188,v156,v188
v_mul_f32 v189,v157,v189
v_mul_f32 v190,v158,v190
v_mul_f32 v191,v159,v191
v_cvt_pk_bf16_f32 v192,v176,v177
v_cvt_pk_bf16_f32 v193,v178,v179
v_cvt_pk_bf16_f32 v194,v180,v181
v_cvt_pk_bf16_f32 v195,v182,v183
v_cvt_pk_bf16_f32 v196,v184,v185
v_cvt_pk_bf16_f32 v197,v186,v187
v_cvt_pk_bf16_f32 v198,v188,v189
v_cvt_pk_bf16_f32 v199,v190,v191
s_waitcnt lgkmcnt(0)
s_waitcnt vmcnt(2)
s_barrier
v_fmaak_f32 v176,v160,v160,0x409d30fc
v_fmaak_f32 v177,v161,v161,0x409d30fc
v_fmaak_f32 v178,v162,v162,0x409d30fc
v_fmaak_f32 v179,v163,v163,0x409d30fc
v_fmaak_f32 v180,v164,v164,0x409d30fc
v_fmaak_f32 v181,v165,v165,0x409d30fc
v_fmaak_f32 v182,v166,v166,0x409d30fc
v_fmaak_f32 v183,v167,v167,0x409d30fc
v_fmaak_f32 v184,v168,v168,0x409d30fc
v_fmaak_f32 v185,v169,v169,0x409d30fc
v_fmaak_f32 v186,v170,v170,0x409d30fc
v_fmaak_f32 v187,v171,v171,0x409d30fc
v_fmaak_f32 v188,v172,v172,0x409d30fc
v_fmaak_f32 v189,v173,v173,0x409d30fc
v_fmaak_f32 v190,v174,v174,0x409d30fc
v_fmaak_f32 v191,v175,v175,0x409d30fc
v_mul_f32 v176,v160,v176
v_mul_f32 v177,v161,v177
v_mul_f32 v178,v162,v178
v_mul_f32 v179,v163,v179
v_mul_f32 v180,v164,v180
v_mul_f32 v181,v165,v181
v_mul_f32 v182,v166,v182
v_mul_f32 v183,v167,v183
v_mul_f32 v184,v168,v184
v_mul_f32 v185,v169,v185
v_mul_f32 v186,v170,v186
v_mul_f32 v187,v171,v187
v_mul_f32 v188,v172,v188
v_mul_f32 v189,v173,v189
v_mul_f32 v190,v174,v190
v_mul_f32 v191,v175,v191
s_setprio 1
v_mfma_f32_32x32x16_bf16 v[96:111],v[200:203],v[192:195],v[96:111]
v_exp_f32 v176,v176
v_exp_f32 v177,v177
v_exp_f32 v178,v178
v_mfma_f32_32x32x16_bf16 v[96:111],v[204:207],v[196:199],v[96:111]
v_exp_f32 v179,v179
v_exp_f32 v180,v180
v_exp_f32 v181,v181
v_mfma_f32_32x32x16_bf16 v[112:127],v[208:211],v[192:195],v[112:127]
v_exp_f32 v182,v182
v_exp_f32 v183,v183
v_exp_f32 v184,v184
v_mfma_f32_32x32x16_bf16 v[112:127],v[212:215],v[196:199],v[112:127]
v_exp_f32 v185,v185
v_exp_f32 v186,v186
v_exp_f32 v187,v187
v_mfma_f32_32x32x16_bf16 v[128:143],v[216:219],v[192:195],v[128:143]
v_exp_f32 v188,v188
v_exp_f32 v189,v189
v_mfma_f32_32x32x16_bf16 v[128:143],v[220:223],v[196:199],v[128:143]
v_exp_f32 v190,v190
v_exp_f32 v191,v191
ds_read_b64_tr_b16 v[200:201],v16 offset:0
ds_read_b64_tr_b16 v[202:203],v16 offset:512
ds_read_b64_tr_b16 v[204:205],v16 offset:1024
ds_read_b64_tr_b16 v[206:207],v16 offset:1536
ds_read_b64_tr_b16 v[208:209],v16 offset:2048
ds_read_b64_tr_b16 v[210:211],v16 offset:2560
ds_read_b64_tr_b16 v[212:213],v16 offset:3072
ds_read_b64_tr_b16 v[214:215],v16 offset:3584
ds_read_b64_tr_b16 v[216:217],v16 offset:4096
ds_read_b64_tr_b16 v[218:219],v16 offset:4608
ds_read_b64_tr_b16 v[220:221],v16 offset:5120
ds_read_b64_tr_b16 v[222:223],v16 offset:5632
ds_read_b128 v[144:147],v18 offset:768
ds_read_b128 v[148:151],v18 offset:800
ds_read_b128 v[152:155],v18 offset:832
ds_read_b128 v[156:159],v18 offset:864
s_mov_b64 exec,s[60:61]
s_add_u32 m0,s58,142848
s_mov_b32 s64,36864
buffer_load_dwordx4 v28,s[48:51],s64 offen lds
s_add_u32 m0,s58,143616
s_mov_b32 s65,37632
buffer_load_dwordx4 v28,s[48:51],s65 offen lds
s_mov_b64 exec,-1
s_setprio 0
v_add_f32 v176,1.0,v176
v_add_f32 v177,1.0,v177
v_add_f32 v178,1.0,v178
v_add_f32 v179,1.0,v179
v_add_f32 v180,1.0,v180
v_add_f32 v181,1.0,v181
v_add_f32 v182,1.0,v182
v_add_f32 v183,1.0,v183
v_add_f32 v184,1.0,v184
v_add_f32 v185,1.0,v185
v_add_f32 v186,1.0,v186
v_add_f32 v187,1.0,v187
v_add_f32 v188,1.0,v188
v_add_f32 v189,1.0,v189
v_add_f32 v190,1.0,v190
v_add_f32 v191,1.0,v191
s_setprio 1
s_waitcnt lgkmcnt(0)
v_mfma_f32_32x32x16_bf16 v[144:159],v[200:203],v[72:75],v[144:159]
v_rcp_f32 v176,v176
v_rcp_f32 v177,v177
v_rcp_f32 v178,v178
v_mfma_f32_32x32x16_bf16 v[144:159],v[204:207],v[76:79],v[144:159]
v_rcp_f32 v179,v179
v_rcp_f32 v180,v180
v_rcp_f32 v181,v181
v_mfma_f32_32x32x16_bf16 v[144:159],v[208:211],v[80:83],v[144:159]
v_rcp_f32 v182,v182
v_rcp_f32 v183,v183
v_rcp_f32 v184,v184
v_mfma_f32_32x32x16_bf16 v[144:159],v[212:215],v[84:87],v[144:159]
v_rcp_f32 v185,v185
v_rcp_f32 v186,v186
v_rcp_f32 v187,v187
v_mfma_f32_32x32x16_bf16 v[144:159],v[216:219],v[88:91],v[144:159]
v_rcp_f32 v188,v188
v_rcp_f32 v189,v189
v_mfma_f32_32x32x16_bf16 v[144:159],v[220:223],v[92:95],v[144:159]
v_rcp_f32 v190,v190
v_rcp_f32 v191,v191
ds_read_b64_tr_b16 v[200:201],v17 offset:12288
ds_read_b64_tr_b16 v[202:203],v17 offset:13824
ds_read_b64_tr_b16 v[204:205],v17 offset:15360
ds_read_b64_tr_b16 v[206:207],v17 offset:16896
ds_read_b64_tr_b16 v[208:209],v17 offset:12352
ds_read_b64_tr_b16 v[210:211],v17 offset:13888
ds_read_b64_tr_b16 v[212:213],v17 offset:15424
ds_read_b64_tr_b16 v[214:215],v17 offset:16960
ds_read_b64_tr_b16 v[216:217],v17 offset:12416
ds_read_b64_tr_b16 v[218:219],v17 offset:13952
ds_read_b64_tr_b16 v[220:221],v17 offset:15488
ds_read_b64_tr_b16 v[222:223],v17 offset:17024
s_mov_b64 exec,s[60:61]
s_add_u32 m0,s58,136704
s_movk_i32 s64,512
buffer_load_dwordx4 v27,s[44:47],s64 offen lds
s_add_u32 m0,s58,137472
s_movk_i32 s65,9728
buffer_load_dwordx4 v27,s[44:47],s65 offen lds
s_mov_b64 exec,-1
s_setprio 0
v_mul_f32 v176,v160,v176
v_mul_f32 v177,v161,v177
v_mul_f32 v178,v162,v178
v_mul_f32 v179,v163,v179
v_mul_f32 v180,v164,v180
v_mul_f32 v181,v165,v181
v_mul_f32 v182,v166,v182
v_mul_f32 v183,v167,v183
v_mul_f32 v184,v168,v184
v_mul_f32 v185,v169,v185
v_mul_f32 v186,v170,v186
v_mul_f32 v187,v171,v187
v_mul_f32 v188,v172,v188
v_mul_f32 v189,v173,v189
v_mul_f32 v190,v174,v190
v_mul_f32 v191,v175,v191
v_cvt_pk_bf16_f32 v192,v176,v177
v_cvt_pk_bf16_f32 v193,v178,v179
v_cvt_pk_bf16_f32 v194,v180,v181
v_cvt_pk_bf16_f32 v195,v182,v183
v_cvt_pk_bf16_f32 v196,v184,v185
v_cvt_pk_bf16_f32 v197,v186,v187
v_cvt_pk_bf16_f32 v198,v188,v189
v_cvt_pk_bf16_f32 v199,v190,v191
s_waitcnt lgkmcnt(0)
s_waitcnt vmcnt(2)
s_barrier
v_fmaak_f32 v176,v144,v144,0x409d30fc
v_fmaak_f32 v177,v145,v145,0x409d30fc
v_fmaak_f32 v178,v146,v146,0x409d30fc
v_fmaak_f32 v179,v147,v147,0x409d30fc
v_fmaak_f32 v180,v148,v148,0x409d30fc
v_fmaak_f32 v181,v149,v149,0x409d30fc
v_fmaak_f32 v182,v150,v150,0x409d30fc
v_fmaak_f32 v183,v151,v151,0x409d30fc
v_fmaak_f32 v184,v152,v152,0x409d30fc
v_fmaak_f32 v185,v153,v153,0x409d30fc
v_fmaak_f32 v186,v154,v154,0x409d30fc
v_fmaak_f32 v187,v155,v155,0x409d30fc
v_fmaak_f32 v188,v156,v156,0x409d30fc
v_fmaak_f32 v189,v157,v157,0x409d30fc
v_fmaak_f32 v190,v158,v158,0x409d30fc
v_fmaak_f32 v191,v159,v159,0x409d30fc
v_mul_f32 v176,v144,v176
v_mul_f32 v177,v145,v177
v_mul_f32 v178,v146,v178
v_mul_f32 v179,v147,v179
v_mul_f32 v180,v148,v180
v_mul_f32 v181,v149,v181
v_mul_f32 v182,v150,v182
v_mul_f32 v183,v151,v183
v_mul_f32 v184,v152,v184
v_mul_f32 v185,v153,v185
v_mul_f32 v186,v154,v186
v_mul_f32 v187,v155,v187
v_mul_f32 v188,v156,v188
v_mul_f32 v189,v157,v189
v_mul_f32 v190,v158,v190
v_mul_f32 v191,v159,v191
s_setprio 1
v_mfma_f32_32x32x16_bf16 v[96:111],v[200:203],v[192:195],v[96:111]
v_exp_f32 v176,v176
v_exp_f32 v177,v177
v_exp_f32 v178,v178
v_mfma_f32_32x32x16_bf16 v[96:111],v[204:207],v[196:199],v[96:111]
v_exp_f32 v179,v179
v_exp_f32 v180,v180
v_exp_f32 v181,v181
v_mfma_f32_32x32x16_bf16 v[112:127],v[208:211],v[192:195],v[112:127]
v_exp_f32 v182,v182
v_exp_f32 v183,v183
v_exp_f32 v184,v184
v_mfma_f32_32x32x16_bf16 v[112:127],v[212:215],v[196:199],v[112:127]
v_exp_f32 v185,v185
v_exp_f32 v186,v186
v_exp_f32 v187,v187
v_mfma_f32_32x32x16_bf16 v[128:143],v[216:219],v[192:195],v[128:143]
v_exp_f32 v188,v188
v_exp_f32 v189,v189
v_mfma_f32_32x32x16_bf16 v[128:143],v[220:223],v[196:199],v[128:143]
v_exp_f32 v190,v190
v_exp_f32 v191,v191
ds_read_b64_tr_b16 v[200:201],v16 offset:6144
ds_read_b64_tr_b16 v[202:203],v16 offset:6656
ds_read_b64_tr_b16 v[204:205],v16 offset:7168
ds_read_b64_tr_b16 v[206:207],v16 offset:7680
ds_read_b64_tr_b16 v[208:209],v16 offset:8192
ds_read_b64_tr_b16 v[210:211],v16 offset:8704
ds_read_b64_tr_b16 v[212:213],v16 offset:9216
ds_read_b64_tr_b16 v[214:215],v16 offset:9728
ds_read_b64_tr_b16 v[216:217],v16 offset:10240
ds_read_b64_tr_b16 v[218:219],v16 offset:10752
ds_read_b64_tr_b16 v[220:221],v16 offset:11264
ds_read_b64_tr_b16 v[222:223],v16 offset:11776
ds_read_b128 v[160:163],v18 offset:896
ds_read_b128 v[164:167],v18 offset:928
ds_read_b128 v[168:171],v18 offset:960
ds_read_b128 v[172:175],v18 offset:992
s_mov_b64 exec,s[60:61]
s_add_u32 m0,s58,148992
s_mov_b32 s64,43008
buffer_load_dwordx4 v28,s[48:51],s64 offen lds
s_add_u32 m0,s58,149760
s_mov_b32 s65,43776
buffer_load_dwordx4 v28,s[48:51],s65 offen lds
s_mov_b64 exec,-1
s_setprio 0
v_add_f32 v176,1.0,v176
v_add_f32 v177,1.0,v177
v_add_f32 v178,1.0,v178
v_add_f32 v179,1.0,v179
v_add_f32 v180,1.0,v180
v_add_f32 v181,1.0,v181
v_add_f32 v182,1.0,v182
v_add_f32 v183,1.0,v183
v_add_f32 v184,1.0,v184
v_add_f32 v185,1.0,v185
v_add_f32 v186,1.0,v186
v_add_f32 v187,1.0,v187
v_add_f32 v188,1.0,v188
v_add_f32 v189,1.0,v189
v_add_f32 v190,1.0,v190
v_add_f32 v191,1.0,v191
s_setprio 1
s_waitcnt lgkmcnt(0)
v_mfma_f32_32x32x16_bf16 v[160:175],v[200:203],v[72:75],v[160:175]
v_rcp_f32 v176,v176
v_rcp_f32 v177,v177
v_rcp_f32 v178,v178
v_mfma_f32_32x32x16_bf16 v[160:175],v[204:207],v[76:79],v[160:175]
v_rcp_f32 v179,v179
v_rcp_f32 v180,v180
v_rcp_f32 v181,v181
v_mfma_f32_32x32x16_bf16 v[160:175],v[208:211],v[80:83],v[160:175]
v_rcp_f32 v182,v182
v_rcp_f32 v183,v183
v_rcp_f32 v184,v184
v_mfma_f32_32x32x16_bf16 v[160:175],v[212:215],v[84:87],v[160:175]
v_rcp_f32 v185,v185
v_rcp_f32 v186,v186
v_rcp_f32 v187,v187
v_mfma_f32_32x32x16_bf16 v[160:175],v[216:219],v[88:91],v[160:175]
v_rcp_f32 v188,v188
v_rcp_f32 v189,v189
v_mfma_f32_32x32x16_bf16 v[160:175],v[220:223],v[92:95],v[160:175]
v_rcp_f32 v190,v190
v_rcp_f32 v191,v191
ds_read_b64_tr_b16 v[200:201],v17 offset:0
ds_read_b64_tr_b16 v[202:203],v17 offset:1536
ds_read_b64_tr_b16 v[204:205],v17 offset:3072
ds_read_b64_tr_b16 v[206:207],v17 offset:4608
ds_read_b64_tr_b16 v[208:209],v17 offset:64
ds_read_b64_tr_b16 v[210:211],v17 offset:1600
ds_read_b64_tr_b16 v[212:213],v17 offset:3136
ds_read_b64_tr_b16 v[214:215],v17 offset:4672
ds_read_b64_tr_b16 v[216:217],v17 offset:128
ds_read_b64_tr_b16 v[218:219],v17 offset:1664
ds_read_b64_tr_b16 v[220:221],v17 offset:3200
ds_read_b64_tr_b16 v[222:223],v17 offset:4736
s_mov_b64 exec,s[60:61]
s_add_u32 m0,s58,124416
s_movk_i32 s64,576
buffer_load_dwordx4 v27,s[44:47],s64 offen lds
s_add_u32 m0,s58,125184
s_movk_i32 s65,9792
buffer_load_dwordx4 v27,s[44:47],s65 offen lds
s_mov_b64 exec,-1
s_setprio 0
v_mul_f32 v176,v144,v176
v_mul_f32 v177,v145,v177
v_mul_f32 v178,v146,v178
v_mul_f32 v179,v147,v179
v_mul_f32 v180,v148,v180
v_mul_f32 v181,v149,v181
v_mul_f32 v182,v150,v182
v_mul_f32 v183,v151,v183
v_mul_f32 v184,v152,v184
v_mul_f32 v185,v153,v185
v_mul_f32 v186,v154,v186
v_mul_f32 v187,v155,v187
v_mul_f32 v188,v156,v188
v_mul_f32 v189,v157,v189
v_mul_f32 v190,v158,v190
v_mul_f32 v191,v159,v191
v_cvt_pk_bf16_f32 v192,v176,v177
v_cvt_pk_bf16_f32 v193,v178,v179
v_cvt_pk_bf16_f32 v194,v180,v181
v_cvt_pk_bf16_f32 v195,v182,v183
v_cvt_pk_bf16_f32 v196,v184,v185
v_cvt_pk_bf16_f32 v197,v186,v187
v_cvt_pk_bf16_f32 v198,v188,v189
v_cvt_pk_bf16_f32 v199,v190,v191
s_waitcnt lgkmcnt(0)
s_waitcnt vmcnt(2)
s_barrier
v_fmaak_f32 v176,v160,v160,0x409d30fc
v_fmaak_f32 v177,v161,v161,0x409d30fc
v_fmaak_f32 v178,v162,v162,0x409d30fc
v_fmaak_f32 v179,v163,v163,0x409d30fc
v_fmaak_f32 v180,v164,v164,0x409d30fc
v_fmaak_f32 v181,v165,v165,0x409d30fc
v_fmaak_f32 v182,v166,v166,0x409d30fc
v_fmaak_f32 v183,v167,v167,0x409d30fc
v_fmaak_f32 v184,v168,v168,0x409d30fc
v_fmaak_f32 v185,v169,v169,0x409d30fc
v_fmaak_f32 v186,v170,v170,0x409d30fc
v_fmaak_f32 v187,v171,v171,0x409d30fc
v_fmaak_f32 v188,v172,v172,0x409d30fc
v_fmaak_f32 v189,v173,v173,0x409d30fc
v_fmaak_f32 v190,v174,v174,0x409d30fc
v_fmaak_f32 v191,v175,v175,0x409d30fc
v_mul_f32 v176,v160,v176
v_mul_f32 v177,v161,v177
v_mul_f32 v178,v162,v178
v_mul_f32 v179,v163,v179
v_mul_f32 v180,v164,v180
v_mul_f32 v181,v165,v181
v_mul_f32 v182,v166,v182
v_mul_f32 v183,v167,v183
v_mul_f32 v184,v168,v184
v_mul_f32 v185,v169,v185
v_mul_f32 v186,v170,v186
v_mul_f32 v187,v171,v187
v_mul_f32 v188,v172,v188
v_mul_f32 v189,v173,v189
v_mul_f32 v190,v174,v190
v_mul_f32 v191,v175,v191
s_setprio 1
v_mfma_f32_32x32x16_bf16 v[96:111],v[200:203],v[192:195],v[96:111]
v_exp_f32 v176,v176
v_exp_f32 v177,v177
v_exp_f32 v178,v178
v_mfma_f32_32x32x16_bf16 v[96:111],v[204:207],v[196:199],v[96:111]
v_exp_f32 v179,v179
v_exp_f32 v180,v180
v_exp_f32 v181,v181
v_mfma_f32_32x32x16_bf16 v[112:127],v[208:211],v[192:195],v[112:127]
v_exp_f32 v182,v182
v_exp_f32 v183,v183
v_exp_f32 v184,v184
v_mfma_f32_32x32x16_bf16 v[112:127],v[212:215],v[196:199],v[112:127]
v_exp_f32 v185,v185
v_exp_f32 v186,v186
v_exp_f32 v187,v187
v_mfma_f32_32x32x16_bf16 v[128:143],v[216:219],v[192:195],v[128:143]
v_exp_f32 v188,v188
v_exp_f32 v189,v189
v_mfma_f32_32x32x16_bf16 v[128:143],v[220:223],v[196:199],v[128:143]
v_exp_f32 v190,v190
v_exp_f32 v191,v191
ds_read_b64_tr_b16 v[200:201],v16 offset:12288
ds_read_b64_tr_b16 v[202:203],v16 offset:12800
ds_read_b64_tr_b16 v[204:205],v16 offset:13312
ds_read_b64_tr_b16 v[206:207],v16 offset:13824
ds_read_b64_tr_b16 v[208:209],v16 offset:14336
ds_read_b64_tr_b16 v[210:211],v16 offset:14848
ds_read_b64_tr_b16 v[212:213],v16 offset:15360
ds_read_b64_tr_b16 v[214:215],v16 offset:15872
ds_read_b64_tr_b16 v[216:217],v16 offset:16384
ds_read_b64_tr_b16 v[218:219],v16 offset:16896
ds_read_b64_tr_b16 v[220:221],v16 offset:17408
ds_read_b64_tr_b16 v[222:223],v16 offset:17920
ds_read_b128 v[144:147],v18 offset:1024
ds_read_b128 v[148:151],v18 offset:1056
ds_read_b128 v[152:155],v18 offset:1088
ds_read_b128 v[156:159],v18 offset:1120
s_mov_b64 exec,s[60:61]
s_add_u32 m0,s58,155136
s_mov_b32 s64,49152
buffer_load_dwordx4 v28,s[48:51],s64 offen lds
s_add_u32 m0,s58,155904
s_mov_b32 s65,49920
buffer_load_dwordx4 v28,s[48:51],s65 offen lds
s_mov_b64 exec,-1
s_setprio 0
v_add_f32 v176,1.0,v176
v_add_f32 v177,1.0,v177
v_add_f32 v178,1.0,v178
v_add_f32 v179,1.0,v179
v_add_f32 v180,1.0,v180
v_add_f32 v181,1.0,v181
v_add_f32 v182,1.0,v182
v_add_f32 v183,1.0,v183
v_add_f32 v184,1.0,v184
v_add_f32 v185,1.0,v185
v_add_f32 v186,1.0,v186
v_add_f32 v187,1.0,v187
v_add_f32 v188,1.0,v188
v_add_f32 v189,1.0,v189
v_add_f32 v190,1.0,v190
v_add_f32 v191,1.0,v191
s_setprio 1
s_waitcnt lgkmcnt(0)
v_mfma_f32_32x32x16_bf16 v[144:159],v[200:203],v[72:75],v[144:159]
v_rcp_f32 v176,v176
v_rcp_f32 v177,v177
v_rcp_f32 v178,v178
v_mfma_f32_32x32x16_bf16 v[144:159],v[204:207],v[76:79],v[144:159]
v_rcp_f32 v179,v179
v_rcp_f32 v180,v180
v_rcp_f32 v181,v181
v_mfma_f32_32x32x16_bf16 v[144:159],v[208:211],v[80:83],v[144:159]
v_rcp_f32 v182,v182
v_rcp_f32 v183,v183
v_rcp_f32 v184,v184
v_mfma_f32_32x32x16_bf16 v[144:159],v[212:215],v[84:87],v[144:159]
v_rcp_f32 v185,v185
v_rcp_f32 v186,v186
v_rcp_f32 v187,v187
v_mfma_f32_32x32x16_bf16 v[144:159],v[216:219],v[88:91],v[144:159]
v_rcp_f32 v188,v188
v_rcp_f32 v189,v189
v_mfma_f32_32x32x16_bf16 v[144:159],v[220:223],v[92:95],v[144:159]
v_rcp_f32 v190,v190
v_rcp_f32 v191,v191
ds_read_b64_tr_b16 v[200:201],v17 offset:6144
ds_read_b64_tr_b16 v[202:203],v17 offset:7680
ds_read_b64_tr_b16 v[204:205],v17 offset:9216
ds_read_b64_tr_b16 v[206:207],v17 offset:10752
ds_read_b64_tr_b16 v[208:209],v17 offset:6208
ds_read_b64_tr_b16 v[210:211],v17 offset:7744
ds_read_b64_tr_b16 v[212:213],v17 offset:9280
ds_read_b64_tr_b16 v[214:215],v17 offset:10816
ds_read_b64_tr_b16 v[216:217],v17 offset:6272
ds_read_b64_tr_b16 v[218:219],v17 offset:7808
ds_read_b64_tr_b16 v[220:221],v17 offset:9344
ds_read_b64_tr_b16 v[222:223],v17 offset:10880
s_mov_b64 exec,s[60:61]
s_add_u32 m0,s58,130560
s_movk_i32 s64,640
buffer_load_dwordx4 v27,s[44:47],s64 offen lds
s_add_u32 m0,s58,131328
s_movk_i32 s65,9856
buffer_load_dwordx4 v27,s[44:47],s65 offen lds
s_mov_b64 exec,-1
s_setprio 0
v_mul_f32 v176,v160,v176
v_mul_f32 v177,v161,v177
v_mul_f32 v178,v162,v178
v_mul_f32 v179,v163,v179
v_mul_f32 v180,v164,v180
v_mul_f32 v181,v165,v181
v_mul_f32 v182,v166,v182
v_mul_f32 v183,v167,v183
v_mul_f32 v184,v168,v184
v_mul_f32 v185,v169,v185
v_mul_f32 v186,v170,v186
v_mul_f32 v187,v171,v187
v_mul_f32 v188,v172,v188
v_mul_f32 v189,v173,v189
v_mul_f32 v190,v174,v190
v_mul_f32 v191,v175,v191
v_cvt_pk_bf16_f32 v192,v176,v177
v_cvt_pk_bf16_f32 v193,v178,v179
v_cvt_pk_bf16_f32 v194,v180,v181
v_cvt_pk_bf16_f32 v195,v182,v183
v_cvt_pk_bf16_f32 v196,v184,v185
v_cvt_pk_bf16_f32 v197,v186,v187
v_cvt_pk_bf16_f32 v198,v188,v189
v_cvt_pk_bf16_f32 v199,v190,v191
s_waitcnt lgkmcnt(0)
s_waitcnt vmcnt(2)
s_barrier
v_fmaak_f32 v176,v144,v144,0x409d30fc
v_fmaak_f32 v177,v145,v145,0x409d30fc
v_fmaak_f32 v178,v146,v146,0x409d30fc
v_fmaak_f32 v179,v147,v147,0x409d30fc
v_fmaak_f32 v180,v148,v148,0x409d30fc
v_fmaak_f32 v181,v149,v149,0x409d30fc
v_fmaak_f32 v182,v150,v150,0x409d30fc
v_fmaak_f32 v183,v151,v151,0x409d30fc
v_fmaak_f32 v184,v152,v152,0x409d30fc
v_fmaak_f32 v185,v153,v153,0x409d30fc
v_fmaak_f32 v186,v154,v154,0x409d30fc
v_fmaak_f32 v187,v155,v155,0x409d30fc
v_fmaak_f32 v188,v156,v156,0x409d30fc
v_fmaak_f32 v189,v157,v157,0x409d30fc
v_fmaak_f32 v190,v158,v158,0x409d30fc
v_fmaak_f32 v191,v159,v159,0x409d30fc
v_mul_f32 v176,v144,v176
v_mul_f32 v177,v145,v177
v_mul_f32 v178,v146,v178
v_mul_f32 v179,v147,v179
v_mul_f32 v180,v148,v180
v_mul_f32 v181,v149,v181
v_mul_f32 v182,v150,v182
v_mul_f32 v183,v151,v183
v_mul_f32 v184,v152,v184
v_mul_f32 v185,v153,v185
v_mul_f32 v186,v154,v186
v_mul_f32 v187,v155,v187
v_mul_f32 v188,v156,v188
v_mul_f32 v189,v157,v189
v_mul_f32 v190,v158,v190
v_mul_f32 v191,v159,v191
s_setprio 1
v_mfma_f32_32x32x16_bf16 v[96:111],v[200:203],v[192:195],v[96:111]
v_exp_f32 v176,v176
v_exp_f32 v177,v177
v_exp_f32 v178,v178
v_mfma_f32_32x32x16_bf16 v[96:111],v[204:207],v[196:199],v[96:111]
v_exp_f32 v179,v179
v_exp_f32 v180,v180
v_exp_f32 v181,v181
v_mfma_f32_32x32x16_bf16 v[112:127],v[208:211],v[192:195],v[112:127]
v_exp_f32 v182,v182
v_exp_f32 v183,v183
v_exp_f32 v184,v184
v_mfma_f32_32x32x16_bf16 v[112:127],v[212:215],v[196:199],v[112:127]
v_exp_f32 v185,v185
v_exp_f32 v186,v186
v_exp_f32 v187,v187
v_mfma_f32_32x32x16_bf16 v[128:143],v[216:219],v[192:195],v[128:143]
v_exp_f32 v188,v188
v_exp_f32 v189,v189
v_mfma_f32_32x32x16_bf16 v[128:143],v[220:223],v[196:199],v[128:143]
v_exp_f32 v190,v190
v_exp_f32 v191,v191
ds_read_b64_tr_b16 v[200:201],v16 offset:0
ds_read_b64_tr_b16 v[202:203],v16 offset:512
ds_read_b64_tr_b16 v[204:205],v16 offset:1024
ds_read_b64_tr_b16 v[206:207],v16 offset:1536
ds_read_b64_tr_b16 v[208:209],v16 offset:2048
ds_read_b64_tr_b16 v[210:211],v16 offset:2560
ds_read_b64_tr_b16 v[212:213],v16 offset:3072
ds_read_b64_tr_b16 v[214:215],v16 offset:3584
ds_read_b64_tr_b16 v[216:217],v16 offset:4096
ds_read_b64_tr_b16 v[218:219],v16 offset:4608
ds_read_b64_tr_b16 v[220:221],v16 offset:5120
ds_read_b64_tr_b16 v[222:223],v16 offset:5632
ds_read_b128 v[160:163],v18 offset:1152
ds_read_b128 v[164:167],v18 offset:1184
ds_read_b128 v[168:171],v18 offset:1216
ds_read_b128 v[172:175],v18 offset:1248
s_mov_b64 exec,s[60:61]
s_add_u32 m0,s58,142848
s_mov_b32 s64,55296
buffer_load_dwordx4 v28,s[48:51],s64 offen lds
s_add_u32 m0,s58,143616
s_mov_b32 s65,56064
buffer_load_dwordx4 v28,s[48:51],s65 offen lds
s_mov_b64 exec,-1
s_setprio 0
v_add_f32 v176,1.0,v176
v_add_f32 v177,1.0,v177
v_add_f32 v178,1.0,v178
v_add_f32 v179,1.0,v179
v_add_f32 v180,1.0,v180
v_add_f32 v181,1.0,v181
v_add_f32 v182,1.0,v182
v_add_f32 v183,1.0,v183
v_add_f32 v184,1.0,v184
v_add_f32 v185,1.0,v185
v_add_f32 v186,1.0,v186
v_add_f32 v187,1.0,v187
v_add_f32 v188,1.0,v188
v_add_f32 v189,1.0,v189
v_add_f32 v190,1.0,v190
v_add_f32 v191,1.0,v191
s_setprio 1
s_waitcnt lgkmcnt(0)
v_mfma_f32_32x32x16_bf16 v[160:175],v[200:203],v[72:75],v[160:175]
v_rcp_f32 v176,v176
v_rcp_f32 v177,v177
v_rcp_f32 v178,v178
v_mfma_f32_32x32x16_bf16 v[160:175],v[204:207],v[76:79],v[160:175]
v_rcp_f32 v179,v179
v_rcp_f32 v180,v180
v_rcp_f32 v181,v181
v_mfma_f32_32x32x16_bf16 v[160:175],v[208:211],v[80:83],v[160:175]
v_rcp_f32 v182,v182
v_rcp_f32 v183,v183
v_rcp_f32 v184,v184
v_mfma_f32_32x32x16_bf16 v[160:175],v[212:215],v[84:87],v[160:175]
v_rcp_f32 v185,v185
v_rcp_f32 v186,v186
v_rcp_f32 v187,v187
v_mfma_f32_32x32x16_bf16 v[160:175],v[216:219],v[88:91],v[160:175]
v_rcp_f32 v188,v188
v_rcp_f32 v189,v189
v_mfma_f32_32x32x16_bf16 v[160:175],v[220:223],v[92:95],v[160:175]
v_rcp_f32 v190,v190
v_rcp_f32 v191,v191
ds_read_b64_tr_b16 v[200:201],v17 offset:12288
ds_read_b64_tr_b16 v[202:203],v17 offset:13824
ds_read_b64_tr_b16 v[204:205],v17 offset:15360
ds_read_b64_tr_b16 v[206:207],v17 offset:16896
ds_read_b64_tr_b16 v[208:209],v17 offset:12352
ds_read_b64_tr_b16 v[210:211],v17 offset:13888
ds_read_b64_tr_b16 v[212:213],v17 offset:15424
ds_read_b64_tr_b16 v[214:215],v17 offset:16960
ds_read_b64_tr_b16 v[216:217],v17 offset:12416
ds_read_b64_tr_b16 v[218:219],v17 offset:13952
ds_read_b64_tr_b16 v[220:221],v17 offset:15488
ds_read_b64_tr_b16 v[222:223],v17 offset:17024
s_mov_b64 exec,s[60:61]
s_add_u32 m0,s58,136704
s_movk_i32 s64,704
buffer_load_dwordx4 v27,s[44:47],s64 offen lds
s_add_u32 m0,s58,137472
s_movk_i32 s65,9920
buffer_load_dwordx4 v27,s[44:47],s65 offen lds
s_mov_b64 exec,-1
s_setprio 0
v_mul_f32 v176,v144,v176
v_mul_f32 v177,v145,v177
v_mul_f32 v178,v146,v178
v_mul_f32 v179,v147,v179
v_mul_f32 v180,v148,v180
v_mul_f32 v181,v149,v181
v_mul_f32 v182,v150,v182
v_mul_f32 v183,v151,v183
v_mul_f32 v184,v152,v184
v_mul_f32 v185,v153,v185
v_mul_f32 v186,v154,v186
v_mul_f32 v187,v155,v187
v_mul_f32 v188,v156,v188
v_mul_f32 v189,v157,v189
v_mul_f32 v190,v158,v190
v_mul_f32 v191,v159,v191
v_cvt_pk_bf16_f32 v192,v176,v177
v_cvt_pk_bf16_f32 v193,v178,v179
v_cvt_pk_bf16_f32 v194,v180,v181
v_cvt_pk_bf16_f32 v195,v182,v183
v_cvt_pk_bf16_f32 v196,v184,v185
v_cvt_pk_bf16_f32 v197,v186,v187
v_cvt_pk_bf16_f32 v198,v188,v189
v_cvt_pk_bf16_f32 v199,v190,v191
s_waitcnt lgkmcnt(0)
s_waitcnt vmcnt(2)
s_barrier
v_fmaak_f32 v176,v160,v160,0x409d30fc
v_fmaak_f32 v177,v161,v161,0x409d30fc
v_fmaak_f32 v178,v162,v162,0x409d30fc
v_fmaak_f32 v179,v163,v163,0x409d30fc
v_fmaak_f32 v180,v164,v164,0x409d30fc
v_fmaak_f32 v181,v165,v165,0x409d30fc
v_fmaak_f32 v182,v166,v166,0x409d30fc
v_fmaak_f32 v183,v167,v167,0x409d30fc
v_fmaak_f32 v184,v168,v168,0x409d30fc
v_fmaak_f32 v185,v169,v169,0x409d30fc
v_fmaak_f32 v186,v170,v170,0x409d30fc
v_fmaak_f32 v187,v171,v171,0x409d30fc
v_fmaak_f32 v188,v172,v172,0x409d30fc
v_fmaak_f32 v189,v173,v173,0x409d30fc
v_fmaak_f32 v190,v174,v174,0x409d30fc
v_fmaak_f32 v191,v175,v175,0x409d30fc
v_mul_f32 v176,v160,v176
v_mul_f32 v177,v161,v177
v_mul_f32 v178,v162,v178
v_mul_f32 v179,v163,v179
v_mul_f32 v180,v164,v180
v_mul_f32 v181,v165,v181
v_mul_f32 v182,v166,v182
v_mul_f32 v183,v167,v183
v_mul_f32 v184,v168,v184
v_mul_f32 v185,v169,v185
v_mul_f32 v186,v170,v186
v_mul_f32 v187,v171,v187
v_mul_f32 v188,v172,v188
v_mul_f32 v189,v173,v189
v_mul_f32 v190,v174,v190
v_mul_f32 v191,v175,v191
s_setprio 1
v_mfma_f32_32x32x16_bf16 v[96:111],v[200:203],v[192:195],v[96:111]
v_exp_f32 v176,v176
v_exp_f32 v177,v177
v_exp_f32 v178,v178
v_mfma_f32_32x32x16_bf16 v[96:111],v[204:207],v[196:199],v[96:111]
v_exp_f32 v179,v179
v_exp_f32 v180,v180
v_exp_f32 v181,v181
v_mfma_f32_32x32x16_bf16 v[112:127],v[208:211],v[192:195],v[112:127]
v_exp_f32 v182,v182
v_exp_f32 v183,v183
v_exp_f32 v184,v184
v_mfma_f32_32x32x16_bf16 v[112:127],v[212:215],v[196:199],v[112:127]
v_exp_f32 v185,v185
v_exp_f32 v186,v186
v_exp_f32 v187,v187
v_mfma_f32_32x32x16_bf16 v[128:143],v[216:219],v[192:195],v[128:143]
v_exp_f32 v188,v188
v_exp_f32 v189,v189
v_mfma_f32_32x32x16_bf16 v[128:143],v[220:223],v[196:199],v[128:143]
v_exp_f32 v190,v190
v_exp_f32 v191,v191
ds_read_b64_tr_b16 v[200:201],v16 offset:6144
ds_read_b64_tr_b16 v[202:203],v16 offset:6656
ds_read_b64_tr_b16 v[204:205],v16 offset:7168
ds_read_b64_tr_b16 v[206:207],v16 offset:7680
ds_read_b64_tr_b16 v[208:209],v16 offset:8192
ds_read_b64_tr_b16 v[210:211],v16 offset:8704
ds_read_b64_tr_b16 v[212:213],v16 offset:9216
ds_read_b64_tr_b16 v[214:215],v16 offset:9728
ds_read_b64_tr_b16 v[216:217],v16 offset:10240
ds_read_b64_tr_b16 v[218:219],v16 offset:10752
ds_read_b64_tr_b16 v[220:221],v16 offset:11264
ds_read_b64_tr_b16 v[222:223],v16 offset:11776
ds_read_b128 v[144:147],v18 offset:1280
ds_read_b128 v[148:151],v18 offset:1312
ds_read_b128 v[152:155],v18 offset:1344
ds_read_b128 v[156:159],v18 offset:1376
s_mov_b64 exec,s[60:61]
s_add_u32 m0,s58,148992
s_mov_b32 s64,61440
buffer_load_dwordx4 v28,s[48:51],s64 offen lds
s_add_u32 m0,s58,149760
s_mov_b32 s65,62208
buffer_load_dwordx4 v28,s[48:51],s65 offen lds
s_mov_b64 exec,-1
s_setprio 0
v_add_f32 v176,1.0,v176
v_add_f32 v177,1.0,v177
v_add_f32 v178,1.0,v178
v_add_f32 v179,1.0,v179
v_add_f32 v180,1.0,v180
v_add_f32 v181,1.0,v181
v_add_f32 v182,1.0,v182
v_add_f32 v183,1.0,v183
v_add_f32 v184,1.0,v184
v_add_f32 v185,1.0,v185
v_add_f32 v186,1.0,v186
v_add_f32 v187,1.0,v187
v_add_f32 v188,1.0,v188
v_add_f32 v189,1.0,v189
v_add_f32 v190,1.0,v190
v_add_f32 v191,1.0,v191
s_setprio 1
s_waitcnt lgkmcnt(0)
v_mfma_f32_32x32x16_bf16 v[144:159],v[200:203],v[72:75],v[144:159]
v_rcp_f32 v176,v176
v_rcp_f32 v177,v177
v_rcp_f32 v178,v178
v_mfma_f32_32x32x16_bf16 v[144:159],v[204:207],v[76:79],v[144:159]
v_rcp_f32 v179,v179
v_rcp_f32 v180,v180
v_rcp_f32 v181,v181
v_mfma_f32_32x32x16_bf16 v[144:159],v[208:211],v[80:83],v[144:159]
v_rcp_f32 v182,v182
v_rcp_f32 v183,v183
v_rcp_f32 v184,v184
v_mfma_f32_32x32x16_bf16 v[144:159],v[212:215],v[84:87],v[144:159]
v_rcp_f32 v185,v185
v_rcp_f32 v186,v186
v_rcp_f32 v187,v187
v_mfma_f32_32x32x16_bf16 v[144:159],v[216:219],v[88:91],v[144:159]
v_rcp_f32 v188,v188
v_rcp_f32 v189,v189
v_mfma_f32_32x32x16_bf16 v[144:159],v[220:223],v[92:95],v[144:159]
v_rcp_f32 v190,v190
v_rcp_f32 v191,v191
ds_read_b64_tr_b16 v[200:201],v17 offset:0
ds_read_b64_tr_b16 v[202:203],v17 offset:1536
ds_read_b64_tr_b16 v[204:205],v17 offset:3072
ds_read_b64_tr_b16 v[206:207],v17 offset:4608
ds_read_b64_tr_b16 v[208:209],v17 offset:64
ds_read_b64_tr_b16 v[210:211],v17 offset:1600
ds_read_b64_tr_b16 v[212:213],v17 offset:3136
ds_read_b64_tr_b16 v[214:215],v17 offset:4672
ds_read_b64_tr_b16 v[216:217],v17 offset:128
ds_read_b64_tr_b16 v[218:219],v17 offset:1664
ds_read_b64_tr_b16 v[220:221],v17 offset:3200
ds_read_b64_tr_b16 v[222:223],v17 offset:4736
s_mov_b64 exec,s[60:61]
s_add_u32 m0,s58,124416
s_movk_i32 s64,0
buffer_load_dwordx4 v27,s[44:47],s64 offen lds
s_add_u32 m0,s58,125184
s_movk_i32 s65,9216
buffer_load_dwordx4 v27,s[44:47],s65 offen lds
s_mov_b64 exec,-1
s_setprio 0
v_mul_f32 v176,v160,v176
v_mul_f32 v177,v161,v177
v_mul_f32 v178,v162,v178
v_mul_f32 v179,v163,v179
v_mul_f32 v180,v164,v180
v_mul_f32 v181,v165,v181
v_mul_f32 v182,v166,v182
v_mul_f32 v183,v167,v183
v_mul_f32 v184,v168,v184
v_mul_f32 v185,v169,v185
v_mul_f32 v186,v170,v186
v_mul_f32 v187,v171,v187
v_mul_f32 v188,v172,v188
v_mul_f32 v189,v173,v189
v_mul_f32 v190,v174,v190
v_mul_f32 v191,v175,v191
v_cvt_pk_bf16_f32 v192,v176,v177
v_cvt_pk_bf16_f32 v193,v178,v179
v_cvt_pk_bf16_f32 v194,v180,v181
v_cvt_pk_bf16_f32 v195,v182,v183
v_cvt_pk_bf16_f32 v196,v184,v185
v_cvt_pk_bf16_f32 v197,v186,v187
v_cvt_pk_bf16_f32 v198,v188,v189
v_cvt_pk_bf16_f32 v199,v190,v191
s_waitcnt lgkmcnt(0)
s_waitcnt vmcnt(2)
s_barrier
v_fmaak_f32 v176,v144,v144,0x409d30fc
v_fmaak_f32 v177,v145,v145,0x409d30fc
v_fmaak_f32 v178,v146,v146,0x409d30fc
v_fmaak_f32 v179,v147,v147,0x409d30fc
v_fmaak_f32 v180,v148,v148,0x409d30fc
v_fmaak_f32 v181,v149,v149,0x409d30fc
v_fmaak_f32 v182,v150,v150,0x409d30fc
v_fmaak_f32 v183,v151,v151,0x409d30fc
v_fmaak_f32 v184,v152,v152,0x409d30fc
v_fmaak_f32 v185,v153,v153,0x409d30fc
v_fmaak_f32 v186,v154,v154,0x409d30fc
v_fmaak_f32 v187,v155,v155,0x409d30fc
v_fmaak_f32 v188,v156,v156,0x409d30fc
v_fmaak_f32 v189,v157,v157,0x409d30fc
v_fmaak_f32 v190,v158,v158,0x409d30fc
v_fmaak_f32 v191,v159,v159,0x409d30fc
v_mul_f32 v176,v144,v176
v_mul_f32 v177,v145,v177
v_mul_f32 v178,v146,v178
v_mul_f32 v179,v147,v179
v_mul_f32 v180,v148,v180
v_mul_f32 v181,v149,v181
v_mul_f32 v182,v150,v182
v_mul_f32 v183,v151,v183
v_mul_f32 v184,v152,v184
v_mul_f32 v185,v153,v185
v_mul_f32 v186,v154,v186
v_mul_f32 v187,v155,v187
v_mul_f32 v188,v156,v188
v_mul_f32 v189,v157,v189
v_mul_f32 v190,v158,v190
v_mul_f32 v191,v159,v191
s_setprio 1
v_mfma_f32_32x32x16_bf16 v[96:111],v[200:203],v[192:195],v[96:111]
v_exp_f32 v176,v176
v_exp_f32 v177,v177
v_exp_f32 v178,v178
v_mfma_f32_32x32x16_bf16 v[96:111],v[204:207],v[196:199],v[96:111]
v_exp_f32 v179,v179
v_exp_f32 v180,v180
v_exp_f32 v181,v181
v_mfma_f32_32x32x16_bf16 v[112:127],v[208:211],v[192:195],v[112:127]
v_exp_f32 v182,v182
v_exp_f32 v183,v183
v_exp_f32 v184,v184
v_mfma_f32_32x32x16_bf16 v[112:127],v[212:215],v[196:199],v[112:127]
v_exp_f32 v185,v185
v_exp_f32 v186,v186
v_exp_f32 v187,v187
v_mfma_f32_32x32x16_bf16 v[128:143],v[216:219],v[192:195],v[128:143]
v_exp_f32 v188,v188
v_exp_f32 v189,v189
v_mfma_f32_32x32x16_bf16 v[128:143],v[220:223],v[196:199],v[128:143]
v_exp_f32 v190,v190
v_exp_f32 v191,v191
ds_read_b64_tr_b16 v[200:201],v16 offset:12288
ds_read_b64_tr_b16 v[202:203],v16 offset:12800
ds_read_b64_tr_b16 v[204:205],v16 offset:13312
ds_read_b64_tr_b16 v[206:207],v16 offset:13824
ds_read_b64_tr_b16 v[208:209],v16 offset:14336
ds_read_b64_tr_b16 v[210:211],v16 offset:14848
ds_read_b64_tr_b16 v[212:213],v16 offset:15360
ds_read_b64_tr_b16 v[214:215],v16 offset:15872
ds_read_b64_tr_b16 v[216:217],v16 offset:16384
ds_read_b64_tr_b16 v[218:219],v16 offset:16896
ds_read_b64_tr_b16 v[220:221],v16 offset:17408
ds_read_b64_tr_b16 v[222:223],v16 offset:17920
ds_read_b128 v[160:163],v18 offset:1408
ds_read_b128 v[164:167],v18 offset:1440
ds_read_b128 v[168:171],v18 offset:1472
ds_read_b128 v[172:175],v18 offset:1504
s_mov_b64 exec,s[60:61]
s_add_u32 m0,s58,155136
s_mov_b32 s64,67584
buffer_load_dwordx4 v28,s[48:51],s64 offen lds
s_add_u32 m0,s58,155904
s_mov_b32 s65,68352
buffer_load_dwordx4 v28,s[48:51],s65 offen lds
s_mov_b64 exec,-1
s_setprio 0
v_add_f32 v176,1.0,v176
v_add_f32 v177,1.0,v177
v_add_f32 v178,1.0,v178
v_add_f32 v179,1.0,v179
v_add_f32 v180,1.0,v180
v_add_f32 v181,1.0,v181
v_add_f32 v182,1.0,v182
v_add_f32 v183,1.0,v183
v_add_f32 v184,1.0,v184
v_add_f32 v185,1.0,v185
v_add_f32 v186,1.0,v186
v_add_f32 v187,1.0,v187
v_add_f32 v188,1.0,v188
v_add_f32 v189,1.0,v189
v_add_f32 v190,1.0,v190
v_add_f32 v191,1.0,v191
s_setprio 1
s_waitcnt lgkmcnt(0)
v_mfma_f32_32x32x16_bf16 v[160:175],v[200:203],v[72:75],v[160:175]
v_rcp_f32 v176,v176
v_rcp_f32 v177,v177
v_rcp_f32 v178,v178
v_mfma_f32_32x32x16_bf16 v[160:175],v[204:207],v[76:79],v[160:175]
v_rcp_f32 v179,v179
v_rcp_f32 v180,v180
v_rcp_f32 v181,v181
v_mfma_f32_32x32x16_bf16 v[160:175],v[208:211],v[80:83],v[160:175]
v_rcp_f32 v182,v182
v_rcp_f32 v183,v183
v_rcp_f32 v184,v184
v_mfma_f32_32x32x16_bf16 v[160:175],v[212:215],v[84:87],v[160:175]
v_rcp_f32 v185,v185
v_rcp_f32 v186,v186
v_rcp_f32 v187,v187
v_mfma_f32_32x32x16_bf16 v[160:175],v[216:219],v[88:91],v[160:175]
v_rcp_f32 v188,v188
v_rcp_f32 v189,v189
v_mfma_f32_32x32x16_bf16 v[160:175],v[220:223],v[92:95],v[160:175]
v_rcp_f32 v190,v190
v_rcp_f32 v191,v191
ds_read_b64_tr_b16 v[200:201],v17 offset:6144
ds_read_b64_tr_b16 v[202:203],v17 offset:7680
ds_read_b64_tr_b16 v[204:205],v17 offset:9216
ds_read_b64_tr_b16 v[206:207],v17 offset:10752
ds_read_b64_tr_b16 v[208:209],v17 offset:6208
ds_read_b64_tr_b16 v[210:211],v17 offset:7744
ds_read_b64_tr_b16 v[212:213],v17 offset:9280
ds_read_b64_tr_b16 v[214:215],v17 offset:10816
ds_read_b64_tr_b16 v[216:217],v17 offset:6272
ds_read_b64_tr_b16 v[218:219],v17 offset:7808
ds_read_b64_tr_b16 v[220:221],v17 offset:9344
ds_read_b64_tr_b16 v[222:223],v17 offset:10880
s_mov_b64 exec,s[60:61]
s_add_u32 m0,s58,130560
s_movk_i32 s64,64
buffer_load_dwordx4 v27,s[44:47],s64 offen lds
s_add_u32 m0,s58,131328
s_movk_i32 s65,9280
buffer_load_dwordx4 v27,s[44:47],s65 offen lds
s_mov_b64 exec,-1
s_setprio 0
v_mul_f32 v176,v144,v176
v_mul_f32 v177,v145,v177
v_mul_f32 v178,v146,v178
v_mul_f32 v179,v147,v179
v_mul_f32 v180,v148,v180
v_mul_f32 v181,v149,v181
v_mul_f32 v182,v150,v182
v_mul_f32 v183,v151,v183
v_mul_f32 v184,v152,v184
v_mul_f32 v185,v153,v185
v_mul_f32 v186,v154,v186
v_mul_f32 v187,v155,v187
v_mul_f32 v188,v156,v188
v_mul_f32 v189,v157,v189
v_mul_f32 v190,v158,v190
v_mul_f32 v191,v159,v191
v_cvt_pk_bf16_f32 v192,v176,v177
v_cvt_pk_bf16_f32 v193,v178,v179
v_cvt_pk_bf16_f32 v194,v180,v181
v_cvt_pk_bf16_f32 v195,v182,v183
v_cvt_pk_bf16_f32 v196,v184,v185
v_cvt_pk_bf16_f32 v197,v186,v187
v_cvt_pk_bf16_f32 v198,v188,v189
v_cvt_pk_bf16_f32 v199,v190,v191
s_waitcnt lgkmcnt(0)
s_waitcnt vmcnt(2)
s_barrier
v_fmaak_f32 v176,v160,v160,0x409d30fc
v_fmaak_f32 v177,v161,v161,0x409d30fc
v_fmaak_f32 v178,v162,v162,0x409d30fc
v_fmaak_f32 v179,v163,v163,0x409d30fc
v_fmaak_f32 v180,v164,v164,0x409d30fc
v_fmaak_f32 v181,v165,v165,0x409d30fc
v_fmaak_f32 v182,v166,v166,0x409d30fc
v_fmaak_f32 v183,v167,v167,0x409d30fc
v_fmaak_f32 v184,v168,v168,0x409d30fc
v_fmaak_f32 v185,v169,v169,0x409d30fc
v_fmaak_f32 v186,v170,v170,0x409d30fc
v_fmaak_f32 v187,v171,v171,0x409d30fc
v_fmaak_f32 v188,v172,v172,0x409d30fc
v_fmaak_f32 v189,v173,v173,0x409d30fc
v_fmaak_f32 v190,v174,v174,0x409d30fc
v_fmaak_f32 v191,v175,v175,0x409d30fc
v_mul_f32 v176,v160,v176
v_mul_f32 v177,v161,v177
v_mul_f32 v178,v162,v178
v_mul_f32 v179,v163,v179
v_mul_f32 v180,v164,v180
v_mul_f32 v181,v165,v181
v_mul_f32 v182,v166,v182
v_mul_f32 v183,v167,v183
v_mul_f32 v184,v168,v184
v_mul_f32 v185,v169,v185
v_mul_f32 v186,v170,v186
v_mul_f32 v187,v171,v187
v_mul_f32 v188,v172,v188
v_mul_f32 v189,v173,v189
v_mul_f32 v190,v174,v190
v_mul_f32 v191,v175,v191
s_setprio 1
v_mfma_f32_32x32x16_bf16 v[96:111],v[200:203],v[192:195],v[96:111]
v_exp_f32 v176,v176
v_exp_f32 v177,v177
v_exp_f32 v178,v178
v_mfma_f32_32x32x16_bf16 v[96:111],v[204:207],v[196:199],v[96:111]
v_exp_f32 v179,v179
v_exp_f32 v180,v180
v_exp_f32 v181,v181
v_mfma_f32_32x32x16_bf16 v[112:127],v[208:211],v[192:195],v[112:127]
v_exp_f32 v182,v182
v_exp_f32 v183,v183
v_exp_f32 v184,v184
v_mfma_f32_32x32x16_bf16 v[112:127],v[212:215],v[196:199],v[112:127]
v_exp_f32 v185,v185
v_exp_f32 v186,v186
v_exp_f32 v187,v187
v_mfma_f32_32x32x16_bf16 v[128:143],v[216:219],v[192:195],v[128:143]
v_exp_f32 v188,v188
v_exp_f32 v189,v189
v_mfma_f32_32x32x16_bf16 v[128:143],v[220:223],v[196:199],v[128:143]
v_exp_f32 v190,v190
v_exp_f32 v191,v191
ds_read_b64 v[72:73],v20 offset:0
ds_read_b64 v[74:75],v21 offset:0
ds_read_b64 v[76:77],v22 offset:0
ds_read_b64 v[78:79],v23 offset:0
ds_read_b64 v[80:81],v20 offset:64
ds_read_b64 v[82:83],v21 offset:64
ds_read_b64 v[84:85],v22 offset:64
ds_read_b64 v[86:87],v23 offset:64
ds_read_b64 v[88:89],v20 offset:128
ds_read_b64 v[90:91],v21 offset:128
ds_read_b64 v[92:93],v22 offset:128
ds_read_b64 v[94:95],v23 offset:128
s_mov_b64 exec,s[60:61]
s_add_u32 m0,s58,142848
s_mov_b32 s64,0
buffer_load_dwordx4 v28,s[48:51],s64 offen lds
s_add_u32 m0,s58,143616
s_mov_b32 s65,768
buffer_load_dwordx4 v28,s[48:51],s65 offen lds
s_mov_b64 exec,-1
s_setprio 0
v_add_f32 v176,1.0,v176
v_add_f32 v177,1.0,v177
v_add_f32 v178,1.0,v178
v_add_f32 v179,1.0,v179
v_add_f32 v180,1.0,v180
v_add_f32 v181,1.0,v181
v_add_f32 v182,1.0,v182
v_add_f32 v183,1.0,v183
v_add_f32 v184,1.0,v184
v_add_f32 v185,1.0,v185
v_add_f32 v186,1.0,v186
v_add_f32 v187,1.0,v187
v_add_f32 v188,1.0,v188
v_add_f32 v189,1.0,v189
v_add_f32 v190,1.0,v190
v_add_f32 v191,1.0,v191
s_setprio 1
s_waitcnt lgkmcnt(0)
v_rcp_f32 v176,v176
v_rcp_f32 v177,v177
v_rcp_f32 v178,v178
v_rcp_f32 v179,v179
v_rcp_f32 v180,v180
v_rcp_f32 v181,v181
v_rcp_f32 v182,v182
v_rcp_f32 v183,v183
v_rcp_f32 v184,v184
v_rcp_f32 v185,v185
v_rcp_f32 v186,v186
v_rcp_f32 v187,v187
v_rcp_f32 v188,v188
v_rcp_f32 v189,v189
v_rcp_f32 v190,v190
v_rcp_f32 v191,v191
ds_read_b64_tr_b16 v[200:201],v17 offset:12288
ds_read_b64_tr_b16 v[202:203],v17 offset:13824
ds_read_b64_tr_b16 v[204:205],v17 offset:15360
ds_read_b64_tr_b16 v[206:207],v17 offset:16896
ds_read_b64_tr_b16 v[208:209],v17 offset:12352
ds_read_b64_tr_b16 v[210:211],v17 offset:13888
ds_read_b64_tr_b16 v[212:213],v17 offset:15424
ds_read_b64_tr_b16 v[214:215],v17 offset:16960
ds_read_b64_tr_b16 v[216:217],v17 offset:12416
ds_read_b64_tr_b16 v[218:219],v17 offset:13952
ds_read_b64_tr_b16 v[220:221],v17 offset:15488
ds_read_b64_tr_b16 v[222:223],v17 offset:17024
s_mov_b64 exec,s[60:61]
s_add_u32 m0,s58,136704
s_movk_i32 s64,128
buffer_load_dwordx4 v27,s[44:47],s64 offen lds
s_add_u32 m0,s58,137472
s_movk_i32 s65,9344
buffer_load_dwordx4 v27,s[44:47],s65 offen lds
s_mov_b64 exec,-1
s_setprio 0
v_mul_f32 v176,v160,v176
v_mul_f32 v177,v161,v177
v_mul_f32 v178,v162,v178
v_mul_f32 v179,v163,v179
v_mul_f32 v180,v164,v180
v_mul_f32 v181,v165,v181
v_mul_f32 v182,v166,v182
v_mul_f32 v183,v167,v183
v_mul_f32 v184,v168,v184
v_mul_f32 v185,v169,v185
v_mul_f32 v186,v170,v186
v_mul_f32 v187,v171,v187
v_mul_f32 v188,v172,v188
v_mul_f32 v189,v173,v189
v_mul_f32 v190,v174,v190
v_mul_f32 v191,v175,v191
v_cvt_pk_bf16_f32 v192,v176,v177
v_cvt_pk_bf16_f32 v193,v178,v179
v_cvt_pk_bf16_f32 v194,v180,v181
v_cvt_pk_bf16_f32 v195,v182,v183
v_cvt_pk_bf16_f32 v196,v184,v185
v_cvt_pk_bf16_f32 v197,v186,v187
v_cvt_pk_bf16_f32 v198,v188,v189
v_cvt_pk_bf16_f32 v199,v190,v191
ds_read_b128 v[144:147],v18 offset:1536
ds_read_b128 v[148:151],v18 offset:1568
ds_read_b128 v[152:155],v18 offset:1600
ds_read_b128 v[156:159],v18 offset:1632
ds_read_b128 v[160:163],v18 offset:1664
ds_read_b128 v[164:167],v18 offset:1696
ds_read_b128 v[168:171],v18 offset:1728
ds_read_b128 v[172:175],v18 offset:1760
ds_read_b128 v[176:179],v18 offset:1792
ds_read_b128 v[180:183],v18 offset:1824
ds_read_b128 v[184:187],v18 offset:1856
ds_read_b128 v[188:191],v18 offset:1888
s_waitcnt lgkmcnt(12)
s_setprio 1
v_mfma_f32_32x32x16_bf16 v[96:111],v[200:203],v[192:195],v[96:111]
v_lshlrev_b32 v232,16,v72
v_lshlrev_b32 v233,16,v73
v_lshlrev_b32 v234,16,v74
v_lshlrev_b32 v235,16,v75
v_mfma_f32_32x32x16_bf16 v[96:111],v[204:207],v[196:199],v[96:111]
v_lshlrev_b32 v236,16,v76
v_lshlrev_b32 v237,16,v77
v_lshlrev_b32 v238,16,v78
v_lshlrev_b32 v239,16,v79
v_mfma_f32_32x32x16_bf16 v[112:127],v[208:211],v[192:195],v[112:127]
v_lshlrev_b32 v240,16,v80
v_lshlrev_b32 v241,16,v81
v_lshlrev_b32 v242,16,v82
v_lshlrev_b32 v243,16,v83
v_mfma_f32_32x32x16_bf16 v[112:127],v[212:215],v[196:199],v[112:127]
v_lshlrev_b32 v244,16,v84
v_lshlrev_b32 v245,16,v85
v_lshlrev_b32 v246,16,v86
v_lshlrev_b32 v247,16,v87
v_mfma_f32_32x32x16_bf16 v[128:143],v[216:219],v[192:195],v[128:143]
v_lshlrev_b32 v248,16,v88
v_lshlrev_b32 v249,16,v89
v_lshlrev_b32 v250,16,v90
v_lshlrev_b32 v251,16,v91
v_mfma_f32_32x32x16_bf16 v[128:143],v[220:223],v[196:199],v[128:143]
v_lshlrev_b32 v252,16,v92
v_lshlrev_b32 v253,16,v93
v_lshlrev_b32 v254,16,v94
v_lshlrev_b32 v255,16,v95
s_setprio 0
s_waitcnt lgkmcnt(0)
v_and_b32 v72,0xffff0000,v72
v_and_b32 v73,0xffff0000,v73
v_and_b32 v74,0xffff0000,v74
v_and_b32 v75,0xffff0000,v75
v_and_b32 v76,0xffff0000,v76
v_and_b32 v77,0xffff0000,v77
v_and_b32 v78,0xffff0000,v78
v_and_b32 v79,0xffff0000,v79
v_and_b32 v80,0xffff0000,v80
v_and_b32 v81,0xffff0000,v81
v_and_b32 v82,0xffff0000,v82
v_and_b32 v83,0xffff0000,v83
v_and_b32 v84,0xffff0000,v84
v_and_b32 v85,0xffff0000,v85
v_and_b32 v86,0xffff0000,v86
v_and_b32 v87,0xffff0000,v87
v_and_b32 v88,0xffff0000,v88
v_and_b32 v89,0xffff0000,v89
v_and_b32 v90,0xffff0000,v90
v_and_b32 v91,0xffff0000,v91
v_and_b32 v92,0xffff0000,v92
v_and_b32 v93,0xffff0000,v93
v_and_b32 v94,0xffff0000,v94
v_and_b32 v95,0xffff0000,v95
v_fma_f32 v232,v96,v144,v232
v_fma_f32 v72,v97,v145,v72
v_fma_f32 v233,v98,v146,v233
v_fma_f32 v73,v99,v147,v73
v_fma_f32 v234,v100,v148,v234
v_fma_f32 v74,v101,v149,v74
v_fma_f32 v235,v102,v150,v235
v_fma_f32 v75,v103,v151,v75
v_fma_f32 v236,v104,v152,v236
v_fma_f32 v76,v105,v153,v76
v_fma_f32 v237,v106,v154,v237
v_fma_f32 v77,v107,v155,v77
v_fma_f32 v238,v108,v156,v238
v_fma_f32 v78,v109,v157,v78
v_fma_f32 v239,v110,v158,v239
v_fma_f32 v79,v111,v159,v79
v_fma_f32 v240,v112,v160,v240
v_fma_f32 v80,v113,v161,v80
v_fma_f32 v241,v114,v162,v241
v_fma_f32 v81,v115,v163,v81
v_fma_f32 v242,v116,v164,v242
v_fma_f32 v82,v117,v165,v82
v_fma_f32 v243,v118,v166,v243
v_fma_f32 v83,v119,v167,v83
v_fma_f32 v244,v120,v168,v244
v_fma_f32 v84,v121,v169,v84
v_fma_f32 v245,v122,v170,v245
v_fma_f32 v85,v123,v171,v85
v_fma_f32 v246,v124,v172,v246
v_fma_f32 v86,v125,v173,v86
v_fma_f32 v247,v126,v174,v247
v_fma_f32 v87,v127,v175,v87
v_fma_f32 v248,v128,v176,v248
v_fma_f32 v88,v129,v177,v88
v_fma_f32 v249,v130,v178,v249
v_fma_f32 v89,v131,v179,v89
v_fma_f32 v250,v132,v180,v250
v_fma_f32 v90,v133,v181,v90
v_fma_f32 v251,v134,v182,v251
v_fma_f32 v91,v135,v183,v91
v_fma_f32 v252,v136,v184,v252
v_fma_f32 v92,v137,v185,v92
v_fma_f32 v253,v138,v186,v253
v_fma_f32 v93,v139,v187,v93
v_fma_f32 v254,v140,v188,v254
v_fma_f32 v94,v141,v189,v94
v_fma_f32 v255,v142,v190,v255
v_fma_f32 v95,v143,v191,v95
v_add_u32 v4,s54,v4
v_add_u32 v5,s54,v5
v_add_u32 v6,s54,v6
v_add_u32 v7,s54,v7
v_add_u32 v8,s54,v8
v_add_u32 v9,s54,v9
v_add_u32 v10,s54,v10
v_add_u32 v11,s54,v11
v_add_u32 v12,s54,v12
v_add_u32 v13,s54,v13
v_add_u32 v14,s54,v14
v_add_u32 v15,s54,v15
v_cvt_pk_bf16_f32 v200,v232,v72
v_cvt_pk_bf16_f32 v201,v233,v73
v_cvt_pk_bf16_f32 v202,v234,v74
v_cvt_pk_bf16_f32 v203,v235,v75
v_cvt_pk_bf16_f32 v204,v236,v76
v_cvt_pk_bf16_f32 v205,v237,v77
v_cvt_pk_bf16_f32 v206,v238,v78
v_cvt_pk_bf16_f32 v207,v239,v79
v_cvt_pk_bf16_f32 v208,v240,v80
v_cvt_pk_bf16_f32 v209,v241,v81
v_cvt_pk_bf16_f32 v210,v242,v82
v_cvt_pk_bf16_f32 v211,v243,v83
v_cvt_pk_bf16_f32 v212,v244,v84
v_cvt_pk_bf16_f32 v213,v245,v85
v_cvt_pk_bf16_f32 v214,v246,v86
v_cvt_pk_bf16_f32 v215,v247,v87
v_cvt_pk_bf16_f32 v216,v248,v88
v_cvt_pk_bf16_f32 v217,v249,v89
v_cvt_pk_bf16_f32 v218,v250,v90
v_cvt_pk_bf16_f32 v219,v251,v91
v_cvt_pk_bf16_f32 v220,v252,v92
v_cvt_pk_bf16_f32 v221,v253,v93
v_cvt_pk_bf16_f32 v222,v254,v94
v_cvt_pk_bf16_f32 v223,v255,v95
ds_write_b64 v20,v[200:201] offset:0
ds_write_b64 v21,v[202:203] offset:0
ds_write_b64 v22,v[204:205] offset:0
ds_write_b64 v23,v[206:207] offset:0
ds_write_b64 v20,v[208:209] offset:64
ds_write_b64 v21,v[210:211] offset:64
ds_write_b64 v22,v[212:213] offset:64
ds_write_b64 v23,v[214:215] offset:64
ds_write_b64 v20,v[216:217] offset:128
ds_write_b64 v21,v[218:219] offset:128
ds_write_b64 v22,v[220:221] offset:128
ds_write_b64 v23,v[222:223] offset:128
ds_read_b128 v[72:75],v19 offset:0
ds_read_b128 v[76:79],v19 offset:1024
ds_read_b128 v[80:83],v19 offset:2048
ds_read_b128 v[84:87],v19 offset:3456
ds_read_b128 v[88:91],v19 offset:4480
ds_read_b128 v[92:95],v19 offset:5504
v_add_u32 v20,s54,v20
v_add_u32 v21,s54,v21
v_add_u32 v22,s54,v22
v_add_u32 v23,s54,v23
s_sub_u32 s68,s68,s54
s_add_u32 s59,s59,1572864
s_add_u32 s53,s53,1
s_cmp_eq_u32 s53,3
s_cselect_b32 s59,0x70000000,s59
s_add_u32 s67,s69,98304
s_waitcnt lgkmcnt(5)
buffer_store_dwordx4 v[72:75],v24,s[40:43],s69 offen nt
s_waitcnt lgkmcnt(4)
buffer_store_dwordx4 v[76:79],v25,s[40:43],s69 offen nt
s_waitcnt lgkmcnt(3)
buffer_store_dwordx4 v[80:83],v26,s[40:43],s69 offen nt
s_waitcnt lgkmcnt(2)
buffer_store_dwordx4 v[84:87],v24,s[40:43],s67 offen nt
s_waitcnt lgkmcnt(1)
buffer_store_dwordx4 v[88:91],v25,s[40:43],s67 offen nt
s_waitcnt lgkmcnt(0)
buffer_store_dwordx4 v[92:95],v26,s[40:43],s67 offen nt
v_add_u32 v19,s54,v19
s_add_u32 s69,s69,1572864
s_sub_u32 s54,0,s54
s_cmp_lt_u32 s53,4
s_cbranch_scc1 L_pass0_%=
s_branch L_end_%=
L_pass1_%=:
s_waitcnt vmcnt(6)
s_barrier
ds_read_b128 v[200:203],v4 offset:0
ds_read_b128 v[204:207],v8 offset:0
ds_read_b128 v[208:211],v12 offset:0
ds_read_b128 v[212:215],v5 offset:0
ds_read_b128 v[216:219],v9 offset:0
ds_read_b128 v[220:223],v13 offset:0
ds_read_b128 v[96:99],v18 offset:1920
ds_read_b128 v[100:103],v18 offset:1952
ds_read_b128 v[104:107],v18 offset:1984
ds_read_b128 v[108:111],v18 offset:2016
ds_read_b128 v[112:115],v18 offset:2048
ds_read_b128 v[116:119],v18 offset:2080
ds_read_b128 v[120:123],v18 offset:2112
ds_read_b128 v[124:127],v18 offset:2144
ds_read_b128 v[128:131],v18 offset:2176
ds_read_b128 v[132:135],v18 offset:2208
ds_read_b128 v[136:139],v18 offset:2240
ds_read_b128 v[140:143],v18 offset:2272
v_and_b32 v72,v32,v36
v_and_b32 v73,v33,v36
v_and_b32 v74,v34,v36
v_and_b32 v75,v35,v36
v_and_b32 v76,v32,v48
v_and_b32 v77,v33,v48
v_and_b32 v78,v34,v48
v_and_b32 v79,v35,v48
v_and_b32 v80,v32,v60
v_and_b32 v81,v33,v60
v_and_b32 v82,v34,v60
v_and_b32 v83,v35,v60
v_and_b32 v84,v32,v37
v_and_b32 v85,v33,v37
v_and_b32 v86,v34,v37
v_and_b32 v87,v35,v37
v_and_b32 v88,v32,v49
v_and_b32 v89,v33,v49
v_and_b32 v90,v34,v49
v_and_b32 v91,v35,v49
v_and_b32 v92,v32,v61
v_and_b32 v93,v33,v61
v_and_b32 v94,v34,v61
v_and_b32 v95,v35,v61
s_setprio 1
s_waitcnt lgkmcnt(15)
v_mfma_f32_16x16x32_bf16 v[144:147],v[72:75],v[200:203],0
v_mfma_f32_16x16x32_bf16 v[144:147],v[76:79],v[204:207],v[144:147]
v_mfma_f32_16x16x32_bf16 v[144:147],v[80:83],v[208:211],v[144:147]
ds_read_b128 v[200:203],v6 offset:0
ds_read_b128 v[204:207],v10 offset:0
ds_read_b128 v[208:211],v14 offset:0
s_setprio 0
v_and_b32 v72,v32,v38
v_and_b32 v73,v33,v38
v_and_b32 v74,v34,v38
v_and_b32 v75,v35,v38
v_and_b32 v76,v32,v50
v_and_b32 v77,v33,v50
v_and_b32 v78,v34,v50
v_and_b32 v79,v35,v50
v_and_b32 v80,v32,v62
v_and_b32 v81,v33,v62
v_and_b32 v82,v34,v62
v_and_b32 v83,v35,v62
v_mul_f32 v237,v144,v144
v_mul_f32 v236,v145,v145
v_mul_f32 v239,v146,v146
v_mul_f32 v238,v147,v147
s_setprio 1
s_waitcnt lgkmcnt(15)
v_mfma_f32_16x16x32_bf16 v[148:151],v[84:87],v[212:215],0
v_mfma_f32_16x16x32_bf16 v[148:151],v[88:91],v[216:219],v[148:151]
v_mfma_f32_16x16x32_bf16 v[148:151],v[92:95],v[220:223],v[148:151]
ds_read_b128 v[212:215],v7 offset:0
ds_read_b128 v[216:219],v11 offset:0
ds_read_b128 v[220:223],v15 offset:0
s_setprio 0
v_and_b32 v84,v32,v39
v_and_b32 v85,v33,v39
v_and_b32 v86,v34,v39
v_and_b32 v87,v35,v39
v_and_b32 v88,v32,v51
v_and_b32 v89,v33,v51
v_and_b32 v90,v34,v51
v_and_b32 v91,v35,v51
v_and_b32 v92,v32,v63
v_and_b32 v93,v33,v63
v_and_b32 v94,v34,v63
v_and_b32 v95,v35,v63
v_add_f32 v232,v144,v148
v_fmac_f32 v237,v148,v148
v_add_f32 v233,v145,v149
v_fmac_f32 v236,v149,v149
v_add_f32 v234,v146,v150
v_fmac_f32 v239,v150,v150
v_add_f32 v235,v147,v151
v_fmac_f32 v238,v151,v151
s_setprio 1
s_waitcnt lgkmcnt(3)
v_mfma_f32_16x16x32_bf16 v[152:155],v[72:75],v[200:203],0
v_mfma_f32_16x16x32_bf16 v[152:155],v[76:79],v[204:207],v[152:155]
v_mfma_f32_16x16x32_bf16 v[152:155],v[80:83],v[208:211],v[152:155]
ds_read_b128 v[200:203],v4 offset:64
ds_read_b128 v[204:207],v8 offset:64
ds_read_b128 v[208:211],v12 offset:64
s_setprio 0
v_and_b32 v72,v32,v40
v_and_b32 v73,v33,v40
v_and_b32 v74,v34,v40
v_and_b32 v75,v35,v40
v_and_b32 v76,v32,v52
v_and_b32 v77,v33,v52
v_and_b32 v78,v34,v52
v_and_b32 v79,v35,v52
v_and_b32 v80,v32,v64
v_and_b32 v81,v33,v64
v_and_b32 v82,v34,v64
v_and_b32 v83,v35,v64
v_add_f32 v232,v232,v152
v_fmac_f32 v237,v152,v152
v_add_f32 v233,v233,v153
v_fmac_f32 v236,v153,v153
v_add_f32 v234,v234,v154
v_fmac_f32 v239,v154,v154
v_add_f32 v235,v235,v155
v_fmac_f32 v238,v155,v155
s_setprio 1
s_waitcnt lgkmcnt(3)
v_mfma_f32_16x16x32_bf16 v[156:159],v[84:87],v[212:215],0
v_mfma_f32_16x16x32_bf16 v[156:159],v[88:91],v[216:219],v[156:159]
v_mfma_f32_16x16x32_bf16 v[156:159],v[92:95],v[220:223],v[156:159]
ds_read_b128 v[212:215],v5 offset:64
ds_read_b128 v[216:219],v9 offset:64
ds_read_b128 v[220:223],v13 offset:64
s_setprio 0
v_and_b32 v84,v32,v41
v_and_b32 v85,v33,v41
v_and_b32 v86,v34,v41
v_and_b32 v87,v35,v41
v_and_b32 v88,v32,v53
v_and_b32 v89,v33,v53
v_and_b32 v90,v34,v53
v_and_b32 v91,v35,v53
v_and_b32 v92,v32,v65
v_and_b32 v93,v33,v65
v_and_b32 v94,v34,v65
v_and_b32 v95,v35,v65
v_add_f32 v232,v232,v156
v_fmac_f32 v237,v156,v156
v_add_f32 v233,v233,v157
v_fmac_f32 v236,v157,v157
v_add_f32 v234,v234,v158
v_fmac_f32 v239,v158,v158
v_add_f32 v235,v235,v159
v_fmac_f32 v238,v159,v159
s_setprio 1
s_waitcnt lgkmcnt(3)
v_mfma_f32_16x16x32_bf16 v[160:163],v[72:75],v[200:203],0
v_mfma_f32_16x16x32_bf16 v[160:163],v[76:79],v[204:207],v[160:163]
v_mfma_f32_16x16x32_bf16 v[160:163],v[80:83],v[208:211],v[160:163]
ds_read_b128 v[200:203],v6 offset:64
ds_read_b128 v[204:207],v10 offset:64
ds_read_b128 v[208:211],v14 offset:64
s_setprio 0
v_and_b32 v72,v32,v42
v_and_b32 v73,v33,v42
v_and_b32 v74,v34,v42
v_and_b32 v75,v35,v42
v_and_b32 v76,v32,v54
v_and_b32 v77,v33,v54
v_and_b32 v78,v34,v54
v_and_b32 v79,v35,v54
v_and_b32 v80,v32,v66
v_and_b32 v81,v33,v66
v_and_b32 v82,v34,v66
v_and_b32 v83,v35,v66
v_add_f32 v232,v232,v160
v_fmac_f32 v237,v160,v160
v_add_f32 v233,v233,v161
v_fmac_f32 v236,v161,v161
v_add_f32 v234,v234,v162
v_fmac_f32 v239,v162,v162
v_add_f32 v235,v235,v163
v_fmac_f32 v238,v163,v163
s_setprio 1
s_waitcnt lgkmcnt(3)
v_mfma_f32_16x16x32_bf16 v[164:167],v[84:87],v[212:215],0
v_mfma_f32_16x16x32_bf16 v[164:167],v[88:91],v[216:219],v[164:167]
v_mfma_f32_16x16x32_bf16 v[164:167],v[92:95],v[220:223],v[164:167]
ds_read_b128 v[212:215],v7 offset:64
ds_read_b128 v[216:219],v11 offset:64
ds_read_b128 v[220:223],v15 offset:64
s_setprio 0
v_and_b32 v84,v32,v43
v_and_b32 v85,v33,v43
v_and_b32 v86,v34,v43
v_and_b32 v87,v35,v43
v_and_b32 v88,v32,v55
v_and_b32 v89,v33,v55
v_and_b32 v90,v34,v55
v_and_b32 v91,v35,v55
v_and_b32 v92,v32,v67
v_and_b32 v93,v33,v67
v_and_b32 v94,v34,v67
v_and_b32 v95,v35,v67
v_add_f32 v232,v232,v164
v_fmac_f32 v237,v164,v164
v_add_f32 v233,v233,v165
v_fmac_f32 v236,v165,v165
v_add_f32 v234,v234,v166
v_fmac_f32 v239,v166,v166
v_add_f32 v235,v235,v167
v_fmac_f32 v238,v167,v167
s_setprio 1
s_waitcnt lgkmcnt(3)
v_mfma_f32_16x16x32_bf16 v[168:171],v[72:75],v[200:203],0
v_mfma_f32_16x16x32_bf16 v[168:171],v[76:79],v[204:207],v[168:171]
v_mfma_f32_16x16x32_bf16 v[168:171],v[80:83],v[208:211],v[168:171]
ds_read_b128 v[200:203],v4 offset:128
ds_read_b128 v[204:207],v8 offset:128
ds_read_b128 v[208:211],v12 offset:128
s_setprio 0
v_and_b32 v72,v32,v44
v_and_b32 v73,v33,v44
v_and_b32 v74,v34,v44
v_and_b32 v75,v35,v44
v_and_b32 v76,v32,v56
v_and_b32 v77,v33,v56
v_and_b32 v78,v34,v56
v_and_b32 v79,v35,v56
v_and_b32 v80,v32,v68
v_and_b32 v81,v33,v68
v_and_b32 v82,v34,v68
v_and_b32 v83,v35,v68
v_add_f32 v232,v232,v168
v_fmac_f32 v237,v168,v168
v_add_f32 v233,v233,v169
v_fmac_f32 v236,v169,v169
v_add_f32 v234,v234,v170
v_fmac_f32 v239,v170,v170
v_add_f32 v235,v235,v171
v_fmac_f32 v238,v171,v171
s_setprio 1
s_waitcnt lgkmcnt(3)
v_mfma_f32_16x16x32_bf16 v[172:175],v[84:87],v[212:215],0
v_mfma_f32_16x16x32_bf16 v[172:175],v[88:91],v[216:219],v[172:175]
v_mfma_f32_16x16x32_bf16 v[172:175],v[92:95],v[220:223],v[172:175]
ds_read_b128 v[212:215],v5 offset:128
ds_read_b128 v[216:219],v9 offset:128
ds_read_b128 v[220:223],v13 offset:128
s_setprio 0
v_and_b32 v84,v32,v45
v_and_b32 v85,v33,v45
v_and_b32 v86,v34,v45
v_and_b32 v87,v35,v45
v_and_b32 v88,v32,v57
v_and_b32 v89,v33,v57
v_and_b32 v90,v34,v57
v_and_b32 v91,v35,v57
v_and_b32 v92,v32,v69
v_and_b32 v93,v33,v69
v_and_b32 v94,v34,v69
v_and_b32 v95,v35,v69
v_add_f32 v232,v232,v172
v_fmac_f32 v237,v172,v172
v_add_f32 v233,v233,v173
v_fmac_f32 v236,v173,v173
v_add_f32 v234,v234,v174
v_fmac_f32 v239,v174,v174
v_add_f32 v235,v235,v175
v_fmac_f32 v238,v175,v175
s_setprio 1
s_waitcnt lgkmcnt(3)
v_mfma_f32_16x16x32_bf16 v[176:179],v[72:75],v[200:203],0
v_mfma_f32_16x16x32_bf16 v[176:179],v[76:79],v[204:207],v[176:179]
v_mfma_f32_16x16x32_bf16 v[176:179],v[80:83],v[208:211],v[176:179]
ds_read_b128 v[200:203],v6 offset:128
ds_read_b128 v[204:207],v10 offset:128
ds_read_b128 v[208:211],v14 offset:128
s_setprio 0
v_and_b32 v72,v32,v46
v_and_b32 v73,v33,v46
v_and_b32 v74,v34,v46
v_and_b32 v75,v35,v46
v_and_b32 v76,v32,v58
v_and_b32 v77,v33,v58
v_and_b32 v78,v34,v58
v_and_b32 v79,v35,v58
v_and_b32 v80,v32,v70
v_and_b32 v81,v33,v70
v_and_b32 v82,v34,v70
v_and_b32 v83,v35,v70
v_add_f32 v232,v232,v176
v_fmac_f32 v237,v176,v176
v_add_f32 v233,v233,v177
v_fmac_f32 v236,v177,v177
v_add_f32 v234,v234,v178
v_fmac_f32 v239,v178,v178
v_add_f32 v235,v235,v179
v_fmac_f32 v238,v179,v179
s_setprio 1
s_waitcnt lgkmcnt(3)
v_mfma_f32_16x16x32_bf16 v[180:183],v[84:87],v[212:215],0
v_mfma_f32_16x16x32_bf16 v[180:183],v[88:91],v[216:219],v[180:183]
v_mfma_f32_16x16x32_bf16 v[180:183],v[92:95],v[220:223],v[180:183]
ds_read_b128 v[212:215],v7 offset:128
ds_read_b128 v[216:219],v11 offset:128
ds_read_b128 v[220:223],v15 offset:128
s_setprio 0
v_and_b32 v84,v32,v47
v_and_b32 v85,v33,v47
v_and_b32 v86,v34,v47
v_and_b32 v87,v35,v47
v_and_b32 v88,v32,v59
v_and_b32 v89,v33,v59
v_and_b32 v90,v34,v59
v_and_b32 v91,v35,v59
v_and_b32 v92,v32,v71
v_and_b32 v93,v33,v71
v_and_b32 v94,v34,v71
v_and_b32 v95,v35,v71
v_add_f32 v232,v232,v180
v_fmac_f32 v237,v180,v180
v_add_f32 v233,v233,v181
v_fmac_f32 v236,v181,v181
v_add_f32 v234,v234,v182
v_fmac_f32 v239,v182,v182
v_add_f32 v235,v235,v183
v_fmac_f32 v238,v183,v183
s_setprio 1
s_waitcnt lgkmcnt(3)
v_mfma_f32_16x16x32_bf16 v[184:187],v[72:75],v[200:203],0
v_mfma_f32_16x16x32_bf16 v[184:187],v[76:79],v[204:207],v[184:187]
v_mfma_f32_16x16x32_bf16 v[184:187],v[80:83],v[208:211],v[184:187]
s_setprio 0
s_nop 7
v_add_f32 v232,v232,v184
v_fmac_f32 v237,v184,v184
v_add_f32 v233,v233,v185
v_fmac_f32 v236,v185,v185
v_add_f32 v234,v234,v186
v_fmac_f32 v239,v186,v186
v_add_f32 v235,v235,v187
v_fmac_f32 v238,v187,v187
s_setprio 1
s_waitcnt lgkmcnt(0)
v_mfma_f32_16x16x32_bf16 v[188:191],v[84:87],v[212:215],0
v_mfma_f32_16x16x32_bf16 v[188:191],v[88:91],v[216:219],v[188:191]
v_mfma_f32_16x16x32_bf16 v[188:191],v[92:95],v[220:223],v[188:191]
s_setprio 0
ds_read_b64_tr_b16 v[200:201],v16 offset:0
ds_read_b64_tr_b16 v[202:203],v16 offset:512
ds_read_b64_tr_b16 v[204:205],v16 offset:1024
ds_read_b64_tr_b16 v[206:207],v16 offset:1536
ds_read_b64_tr_b16 v[208:209],v16 offset:2048
ds_read_b64_tr_b16 v[210:211],v16 offset:2560
ds_read_b64_tr_b16 v[212:213],v16 offset:3072
ds_read_b64_tr_b16 v[214:215],v16 offset:3584
ds_read_b64_tr_b16 v[216:217],v16 offset:4096
ds_read_b64_tr_b16 v[218:219],v16 offset:4608
ds_read_b64_tr_b16 v[220:221],v16 offset:5120
ds_read_b64_tr_b16 v[222:223],v16 offset:5632
v_add_f32 v232,v232,v188
v_fmac_f32 v237,v188,v188
v_add_f32 v233,v233,v189
v_fmac_f32 v236,v189,v189
v_add_f32 v234,v234,v190
v_fmac_f32 v239,v190,v190
v_add_f32 v235,v235,v191
v_fmac_f32 v238,v191,v191
v_add_f32 v232,v232,v233
v_add_f32 v234,v234,v235
v_add_f32 v237,v237,v236
v_add_f32 v239,v239,v238
v_add_f32 v240,v232,v234
v_add_f32 v241,v237,v239
s_nop 1
v_permlane32_swap_b32 v240,v241
v_add_f32 v240,v240,v241
v_mov_b32 v241,v240
s_nop 1
v_permlane32_swap_b32 v240,v241
v_mul_f32 v240,0x3c2aaaab,v240
v_mul_f32 v241,0x3c2aaaab,v241
v_fma_f32 v241,-v240,v240,v241
v_max_f32 v241,0,v241
v_add_f32 v241,0x3727c5ac,v241
v_rsq_f32 v241,v241
s_nop 0
v_mul_f32 v242,0xbeeff558,v241
v_mul_f32 v243,0x3eeff558,v241
v_mul_f32 v243,v240,v243
v_fma_f32 v144,v144,v242,v243
v_fma_f32 v145,v145,v242,v243
v_fma_f32 v146,v146,v242,v243
v_fma_f32 v147,v147,v242,v243
v_fma_f32 v148,v148,v242,v243
v_fma_f32 v149,v149,v242,v243
v_fma_f32 v150,v150,v242,v243
v_fma_f32 v151,v151,v242,v243
v_fma_f32 v152,v152,v242,v243
v_fma_f32 v153,v153,v242,v243
v_fma_f32 v154,v154,v242,v243
v_fma_f32 v155,v155,v242,v243
v_fma_f32 v156,v156,v242,v243
v_fma_f32 v157,v157,v242,v243
v_fma_f32 v158,v158,v242,v243
v_fma_f32 v159,v159,v242,v243
v_fma_f32 v160,v160,v242,v243
v_fma_f32 v161,v161,v242,v243
v_fma_f32 v162,v162,v242,v243
v_fma_f32 v163,v163,v242,v243
v_fma_f32 v164,v164,v242,v243
v_fma_f32 v165,v165,v242,v243
v_fma_f32 v166,v166,v242,v243
v_fma_f32 v167,v167,v242,v243
v_fma_f32 v168,v168,v242,v243
v_fma_f32 v169,v169,v242,v243
v_fma_f32 v170,v170,v242,v243
v_fma_f32 v171,v171,v242,v243
v_fma_f32 v172,v172,v242,v243
v_fma_f32 v173,v173,v242,v243
v_fma_f32 v174,v174,v242,v243
v_fma_f32 v175,v175,v242,v243
v_fma_f32 v176,v176,v242,v243
v_fma_f32 v177,v177,v242,v243
v_fma_f32 v178,v178,v242,v243
v_fma_f32 v179,v179,v242,v243
v_fma_f32 v180,v180,v242,v243
v_fma_f32 v181,v181,v242,v243
v_fma_f32 v182,v182,v242,v243
v_fma_f32 v183,v183,v242,v243
v_fma_f32 v184,v184,v242,v243
v_fma_f32 v185,v185,v242,v243
v_fma_f32 v186,v186,v242,v243
v_fma_f32 v187,v187,v242,v243
v_fma_f32 v188,v188,v242,v243
v_fma_f32 v189,v189,v242,v243
v_fma_f32 v190,v190,v242,v243
v_fma_f32 v191,v191,v242,v243
s_setprio 1
v_cvt_pk_bf16_f32 v72,v144,v145
v_cvt_pk_bf16_f32 v73,v146,v147
v_cvt_pk_bf16_f32 v74,v148,v149
v_cvt_pk_bf16_f32 v75,v150,v151
v_cvt_pk_bf16_f32 v76,v152,v153
v_cvt_pk_bf16_f32 v77,v154,v155
v_cvt_pk_bf16_f32 v78,v156,v157
v_cvt_pk_bf16_f32 v79,v158,v159
ds_read_b128 v[144:147],v18 offset:0
ds_read_b128 v[148:151],v18 offset:32
ds_read_b128 v[152:155],v18 offset:64
ds_read_b128 v[156:159],v18 offset:96
v_cvt_pk_bf16_f32 v80,v160,v161
v_cvt_pk_bf16_f32 v81,v162,v163
v_cvt_pk_bf16_f32 v82,v164,v165
v_cvt_pk_bf16_f32 v83,v166,v167
v_cvt_pk_bf16_f32 v84,v168,v169
v_cvt_pk_bf16_f32 v85,v170,v171
v_cvt_pk_bf16_f32 v86,v172,v173
v_cvt_pk_bf16_f32 v87,v174,v175
s_waitcnt lgkmcnt(0)
v_mfma_f32_32x32x16_bf16 v[144:159],v[200:203],v[72:75],v[144:159]
v_cvt_pk_bf16_f32 v88,v176,v177
v_cvt_pk_bf16_f32 v89,v178,v179
v_cvt_pk_bf16_f32 v90,v180,v181
v_cvt_pk_bf16_f32 v91,v182,v183
v_mfma_f32_32x32x16_bf16 v[144:159],v[204:207],v[76:79],v[144:159]
v_cvt_pk_bf16_f32 v92,v184,v185
v_cvt_pk_bf16_f32 v93,v186,v187
v_cvt_pk_bf16_f32 v94,v188,v189
v_cvt_pk_bf16_f32 v95,v190,v191
v_mfma_f32_32x32x16_bf16 v[144:159],v[208:211],v[80:83],v[144:159]
v_mfma_f32_32x32x16_bf16 v[144:159],v[212:215],v[84:87],v[144:159]
v_mfma_f32_32x32x16_bf16 v[144:159],v[216:219],v[88:91],v[144:159]
v_mfma_f32_32x32x16_bf16 v[144:159],v[220:223],v[92:95],v[144:159]
s_setprio 0
s_nop 7
s_nop 1
s_waitcnt vmcnt(0)
s_barrier
v_fmaak_f32 v176,v144,v144,0x409d30fc
v_fmaak_f32 v177,v145,v145,0x409d30fc
v_fmaak_f32 v178,v146,v146,0x409d30fc
v_fmaak_f32 v179,v147,v147,0x409d30fc
v_fmaak_f32 v180,v148,v148,0x409d30fc
v_fmaak_f32 v181,v149,v149,0x409d30fc
v_fmaak_f32 v182,v150,v150,0x409d30fc
v_fmaak_f32 v183,v151,v151,0x409d30fc
v_fmaak_f32 v184,v152,v152,0x409d30fc
v_fmaak_f32 v185,v153,v153,0x409d30fc
v_fmaak_f32 v186,v154,v154,0x409d30fc
v_fmaak_f32 v187,v155,v155,0x409d30fc
v_fmaak_f32 v188,v156,v156,0x409d30fc
v_fmaak_f32 v189,v157,v157,0x409d30fc
v_fmaak_f32 v190,v158,v158,0x409d30fc
v_fmaak_f32 v191,v159,v159,0x409d30fc
v_mul_f32 v176,v144,v176
v_mul_f32 v177,v145,v177
v_mul_f32 v178,v146,v178
v_mul_f32 v179,v147,v179
v_mul_f32 v180,v148,v180
v_mul_f32 v181,v149,v181
v_mul_f32 v182,v150,v182
v_mul_f32 v183,v151,v183
v_mul_f32 v184,v152,v184
v_mul_f32 v185,v153,v185
v_mul_f32 v186,v154,v186
v_mul_f32 v187,v155,v187
v_mul_f32 v188,v156,v188
v_mul_f32 v189,v157,v189
v_mul_f32 v190,v158,v190
v_mul_f32 v191,v159,v191
s_setprio 1
v_exp_f32 v176,v176
v_exp_f32 v177,v177
v_exp_f32 v178,v178
v_exp_f32 v179,v179
v_exp_f32 v180,v180
v_exp_f32 v181,v181
v_exp_f32 v182,v182
v_exp_f32 v183,v183
v_exp_f32 v184,v184
v_exp_f32 v185,v185
v_exp_f32 v186,v186
v_exp_f32 v187,v187
v_exp_f32 v188,v188
v_exp_f32 v189,v189
v_exp_f32 v190,v190
v_exp_f32 v191,v191
ds_read_b64_tr_b16 v[200:201],v16 offset:6144
ds_read_b64_tr_b16 v[202:203],v16 offset:6656
ds_read_b64_tr_b16 v[204:205],v16 offset:7168
ds_read_b64_tr_b16 v[206:207],v16 offset:7680
ds_read_b64_tr_b16 v[208:209],v16 offset:8192
ds_read_b64_tr_b16 v[210:211],v16 offset:8704
ds_read_b64_tr_b16 v[212:213],v16 offset:9216
ds_read_b64_tr_b16 v[214:215],v16 offset:9728
ds_read_b64_tr_b16 v[216:217],v16 offset:10240
ds_read_b64_tr_b16 v[218:219],v16 offset:10752
ds_read_b64_tr_b16 v[220:221],v16 offset:11264
ds_read_b64_tr_b16 v[222:223],v16 offset:11776
ds_read_b128 v[160:163],v18 offset:128
ds_read_b128 v[164:167],v18 offset:160
ds_read_b128 v[168:171],v18 offset:192
ds_read_b128 v[172:175],v18 offset:224
s_mov_b64 exec,s[62:63]
s_add_u32 m0,s68,0
s_add_u32 s64,s59,0
buffer_load_dwordx4 v27,s[36:39],s64 offen lds
s_mov_b64 exec,-1
s_setprio 0
v_add_f32 v176,1.0,v176
v_add_f32 v177,1.0,v177
v_add_f32 v178,1.0,v178
v_add_f32 v179,1.0,v179
v_add_f32 v180,1.0,v180
v_add_f32 v181,1.0,v181
v_add_f32 v182,1.0,v182
v_add_f32 v183,1.0,v183
v_add_f32 v184,1.0,v184
v_add_f32 v185,1.0,v185
v_add_f32 v186,1.0,v186
v_add_f32 v187,1.0,v187
v_add_f32 v188,1.0,v188
v_add_f32 v189,1.0,v189
v_add_f32 v190,1.0,v190
v_add_f32 v191,1.0,v191
s_setprio 1
s_waitcnt lgkmcnt(0)
v_mfma_f32_32x32x16_bf16 v[160:175],v[200:203],v[72:75],v[160:175]
v_rcp_f32 v176,v176
v_rcp_f32 v177,v177
v_rcp_f32 v178,v178
v_mfma_f32_32x32x16_bf16 v[160:175],v[204:207],v[76:79],v[160:175]
v_rcp_f32 v179,v179
v_rcp_f32 v180,v180
v_rcp_f32 v181,v181
v_mfma_f32_32x32x16_bf16 v[160:175],v[208:211],v[80:83],v[160:175]
v_rcp_f32 v182,v182
v_rcp_f32 v183,v183
v_rcp_f32 v184,v184
v_mfma_f32_32x32x16_bf16 v[160:175],v[212:215],v[84:87],v[160:175]
v_rcp_f32 v185,v185
v_rcp_f32 v186,v186
v_rcp_f32 v187,v187
v_mfma_f32_32x32x16_bf16 v[160:175],v[216:219],v[88:91],v[160:175]
v_rcp_f32 v188,v188
v_rcp_f32 v189,v189
v_mfma_f32_32x32x16_bf16 v[160:175],v[220:223],v[92:95],v[160:175]
v_rcp_f32 v190,v190
v_rcp_f32 v191,v191
ds_read_b64_tr_b16 v[200:201],v17 offset:0
ds_read_b64_tr_b16 v[202:203],v17 offset:1536
ds_read_b64_tr_b16 v[204:205],v17 offset:3072
ds_read_b64_tr_b16 v[206:207],v17 offset:4608
ds_read_b64_tr_b16 v[208:209],v17 offset:64
ds_read_b64_tr_b16 v[210:211],v17 offset:1600
ds_read_b64_tr_b16 v[212:213],v17 offset:3136
ds_read_b64_tr_b16 v[214:215],v17 offset:4672
ds_read_b64_tr_b16 v[216:217],v17 offset:128
ds_read_b64_tr_b16 v[218:219],v17 offset:1664
ds_read_b64_tr_b16 v[220:221],v17 offset:3200
ds_read_b64_tr_b16 v[222:223],v17 offset:4736
s_mov_b64 exec,s[62:63]
s_add_u32 m0,s68,3456
s_add_u32 s64,s59,98304
buffer_load_dwordx4 v27,s[36:39],s64 offen lds
s_mov_b64 exec,-1
s_setprio 0
v_mul_f32 v176,v144,v176
v_mul_f32 v177,v145,v177
v_mul_f32 v178,v146,v178
v_mul_f32 v179,v147,v179
v_mul_f32 v180,v148,v180
v_mul_f32 v181,v149,v181
v_mul_f32 v182,v150,v182
v_mul_f32 v183,v151,v183
v_mul_f32 v184,v152,v184
v_mul_f32 v185,v153,v185
v_mul_f32 v186,v154,v186
v_mul_f32 v187,v155,v187
v_mul_f32 v188,v156,v188
v_mul_f32 v189,v157,v189
v_mul_f32 v190,v158,v190
v_mul_f32 v191,v159,v191
v_cvt_pk_bf16_f32 v192,v176,v177
v_cvt_pk_bf16_f32 v193,v178,v179
v_cvt_pk_bf16_f32 v194,v180,v181
v_cvt_pk_bf16_f32 v195,v182,v183
v_cvt_pk_bf16_f32 v196,v184,v185
v_cvt_pk_bf16_f32 v197,v186,v187
v_cvt_pk_bf16_f32 v198,v188,v189
v_cvt_pk_bf16_f32 v199,v190,v191
s_waitcnt lgkmcnt(0)
s_barrier
v_fmaak_f32 v176,v160,v160,0x409d30fc
v_fmaak_f32 v177,v161,v161,0x409d30fc
v_fmaak_f32 v178,v162,v162,0x409d30fc
v_fmaak_f32 v179,v163,v163,0x409d30fc
v_fmaak_f32 v180,v164,v164,0x409d30fc
v_fmaak_f32 v181,v165,v165,0x409d30fc
v_fmaak_f32 v182,v166,v166,0x409d30fc
v_fmaak_f32 v183,v167,v167,0x409d30fc
v_fmaak_f32 v184,v168,v168,0x409d30fc
v_fmaak_f32 v185,v169,v169,0x409d30fc
v_fmaak_f32 v186,v170,v170,0x409d30fc
v_fmaak_f32 v187,v171,v171,0x409d30fc
v_fmaak_f32 v188,v172,v172,0x409d30fc
v_fmaak_f32 v189,v173,v173,0x409d30fc
v_fmaak_f32 v190,v174,v174,0x409d30fc
v_fmaak_f32 v191,v175,v175,0x409d30fc
v_mul_f32 v176,v160,v176
v_mul_f32 v177,v161,v177
v_mul_f32 v178,v162,v178
v_mul_f32 v179,v163,v179
v_mul_f32 v180,v164,v180
v_mul_f32 v181,v165,v181
v_mul_f32 v182,v166,v182
v_mul_f32 v183,v167,v183
v_mul_f32 v184,v168,v184
v_mul_f32 v185,v169,v185
v_mul_f32 v186,v170,v186
v_mul_f32 v187,v171,v187
v_mul_f32 v188,v172,v188
v_mul_f32 v189,v173,v189
v_mul_f32 v190,v174,v190
v_mul_f32 v191,v175,v191
s_setprio 1
v_mfma_f32_32x32x16_bf16 v[96:111],v[200:203],v[192:195],v[96:111]
v_exp_f32 v176,v176
v_exp_f32 v177,v177
v_exp_f32 v178,v178
v_mfma_f32_32x32x16_bf16 v[96:111],v[204:207],v[196:199],v[96:111]
v_exp_f32 v179,v179
v_exp_f32 v180,v180
v_exp_f32 v181,v181
v_mfma_f32_32x32x16_bf16 v[112:127],v[208:211],v[192:195],v[112:127]
v_exp_f32 v182,v182
v_exp_f32 v183,v183
v_exp_f32 v184,v184
v_mfma_f32_32x32x16_bf16 v[112:127],v[212:215],v[196:199],v[112:127]
v_exp_f32 v185,v185
v_exp_f32 v186,v186
v_exp_f32 v187,v187
v_mfma_f32_32x32x16_bf16 v[128:143],v[216:219],v[192:195],v[128:143]
v_exp_f32 v188,v188
v_exp_f32 v189,v189
v_mfma_f32_32x32x16_bf16 v[128:143],v[220:223],v[196:199],v[128:143]
v_exp_f32 v190,v190
v_exp_f32 v191,v191
ds_read_b64_tr_b16 v[200:201],v16 offset:12288
ds_read_b64_tr_b16 v[202:203],v16 offset:12800
ds_read_b64_tr_b16 v[204:205],v16 offset:13312
ds_read_b64_tr_b16 v[206:207],v16 offset:13824
ds_read_b64_tr_b16 v[208:209],v16 offset:14336
ds_read_b64_tr_b16 v[210:211],v16 offset:14848
ds_read_b64_tr_b16 v[212:213],v16 offset:15360
ds_read_b64_tr_b16 v[214:215],v16 offset:15872
ds_read_b64_tr_b16 v[216:217],v16 offset:16384
ds_read_b64_tr_b16 v[218:219],v16 offset:16896
ds_read_b64_tr_b16 v[220:221],v16 offset:17408
ds_read_b64_tr_b16 v[222:223],v16 offset:17920
ds_read_b128 v[144:147],v18 offset:256
ds_read_b128 v[148:151],v18 offset:288
ds_read_b128 v[152:155],v18 offset:320
ds_read_b128 v[156:159],v18 offset:352
s_mov_b64 exec,s[62:63]
s_add_u32 m0,s68,6912
s_add_u32 s64,s59,196608
buffer_load_dwordx4 v27,s[36:39],s64 offen lds
s_mov_b64 exec,-1
s_setprio 0
v_add_f32 v176,1.0,v176
v_add_f32 v177,1.0,v177
v_add_f32 v178,1.0,v178
v_add_f32 v179,1.0,v179
v_add_f32 v180,1.0,v180
v_add_f32 v181,1.0,v181
v_add_f32 v182,1.0,v182
v_add_f32 v183,1.0,v183
v_add_f32 v184,1.0,v184
v_add_f32 v185,1.0,v185
v_add_f32 v186,1.0,v186
v_add_f32 v187,1.0,v187
v_add_f32 v188,1.0,v188
v_add_f32 v189,1.0,v189
v_add_f32 v190,1.0,v190
v_add_f32 v191,1.0,v191
s_setprio 1
s_waitcnt lgkmcnt(0)
v_mfma_f32_32x32x16_bf16 v[144:159],v[200:203],v[72:75],v[144:159]
v_rcp_f32 v176,v176
v_rcp_f32 v177,v177
v_rcp_f32 v178,v178
v_mfma_f32_32x32x16_bf16 v[144:159],v[204:207],v[76:79],v[144:159]
v_rcp_f32 v179,v179
v_rcp_f32 v180,v180
v_rcp_f32 v181,v181
v_mfma_f32_32x32x16_bf16 v[144:159],v[208:211],v[80:83],v[144:159]
v_rcp_f32 v182,v182
v_rcp_f32 v183,v183
v_rcp_f32 v184,v184
v_mfma_f32_32x32x16_bf16 v[144:159],v[212:215],v[84:87],v[144:159]
v_rcp_f32 v185,v185
v_rcp_f32 v186,v186
v_rcp_f32 v187,v187
v_mfma_f32_32x32x16_bf16 v[144:159],v[216:219],v[88:91],v[144:159]
v_rcp_f32 v188,v188
v_rcp_f32 v189,v189
v_mfma_f32_32x32x16_bf16 v[144:159],v[220:223],v[92:95],v[144:159]
v_rcp_f32 v190,v190
v_rcp_f32 v191,v191
ds_read_b64_tr_b16 v[200:201],v17 offset:6144
ds_read_b64_tr_b16 v[202:203],v17 offset:7680
ds_read_b64_tr_b16 v[204:205],v17 offset:9216
ds_read_b64_tr_b16 v[206:207],v17 offset:10752
ds_read_b64_tr_b16 v[208:209],v17 offset:6208
ds_read_b64_tr_b16 v[210:211],v17 offset:7744
ds_read_b64_tr_b16 v[212:213],v17 offset:9280
ds_read_b64_tr_b16 v[214:215],v17 offset:10816
ds_read_b64_tr_b16 v[216:217],v17 offset:6272
ds_read_b64_tr_b16 v[218:219],v17 offset:7808
ds_read_b64_tr_b16 v[220:221],v17 offset:9344
ds_read_b64_tr_b16 v[222:223],v17 offset:10880
s_mov_b64 exec,s[62:63]
s_add_u32 m0,s68,10368
s_add_u32 s64,s59,294912
buffer_load_dwordx4 v27,s[36:39],s64 offen lds
s_mov_b64 exec,-1
s_setprio 0
v_mul_f32 v176,v160,v176
v_mul_f32 v177,v161,v177
v_mul_f32 v178,v162,v178
v_mul_f32 v179,v163,v179
v_mul_f32 v180,v164,v180
v_mul_f32 v181,v165,v181
v_mul_f32 v182,v166,v182
v_mul_f32 v183,v167,v183
v_mul_f32 v184,v168,v184
v_mul_f32 v185,v169,v185
v_mul_f32 v186,v170,v186
v_mul_f32 v187,v171,v187
v_mul_f32 v188,v172,v188
v_mul_f32 v189,v173,v189
v_mul_f32 v190,v174,v190
v_mul_f32 v191,v175,v191
v_cvt_pk_bf16_f32 v192,v176,v177
v_cvt_pk_bf16_f32 v193,v178,v179
v_cvt_pk_bf16_f32 v194,v180,v181
v_cvt_pk_bf16_f32 v195,v182,v183
v_cvt_pk_bf16_f32 v196,v184,v185
v_cvt_pk_bf16_f32 v197,v186,v187
v_cvt_pk_bf16_f32 v198,v188,v189
v_cvt_pk_bf16_f32 v199,v190,v191
s_waitcnt lgkmcnt(0)
s_barrier
v_fmaak_f32 v176,v144,v144,0x409d30fc
v_fmaak_f32 v177,v145,v145,0x409d30fc
v_fmaak_f32 v178,v146,v146,0x409d30fc
v_fmaak_f32 v179,v147,v147,0x409d30fc
v_fmaak_f32 v180,v148,v148,0x409d30fc
v_fmaak_f32 v181,v149,v149,0x409d30fc
v_fmaak_f32 v182,v150,v150,0x409d30fc
v_fmaak_f32 v183,v151,v151,0x409d30fc
v_fmaak_f32 v184,v152,v152,0x409d30fc
v_fmaak_f32 v185,v153,v153,0x409d30fc
v_fmaak_f32 v186,v154,v154,0x409d30fc
v_fmaak_f32 v187,v155,v155,0x409d30fc
v_fmaak_f32 v188,v156,v156,0x409d30fc
v_fmaak_f32 v189,v157,v157,0x409d30fc
v_fmaak_f32 v190,v158,v158,0x409d30fc
v_fmaak_f32 v191,v159,v159,0x409d30fc
v_mul_f32 v176,v144,v176
v_mul_f32 v177,v145,v177
v_mul_f32 v178,v146,v178
v_mul_f32 v179,v147,v179
v_mul_f32 v180,v148,v180
v_mul_f32 v181,v149,v181
v_mul_f32 v182,v150,v182
v_mul_f32 v183,v151,v183
v_mul_f32 v184,v152,v184
v_mul_f32 v185,v153,v185
v_mul_f32 v186,v154,v186
v_mul_f32 v187,v155,v187
v_mul_f32 v188,v156,v188
v_mul_f32 v189,v157,v189
v_mul_f32 v190,v158,v190
v_mul_f32 v191,v159,v191
s_setprio 1
v_mfma_f32_32x32x16_bf16 v[96:111],v[200:203],v[192:195],v[96:111]
v_exp_f32 v176,v176
v_exp_f32 v177,v177
v_exp_f32 v178,v178
v_mfma_f32_32x32x16_bf16 v[96:111],v[204:207],v[196:199],v[96:111]
v_exp_f32 v179,v179
v_exp_f32 v180,v180
v_exp_f32 v181,v181
v_mfma_f32_32x32x16_bf16 v[112:127],v[208:211],v[192:195],v[112:127]
v_exp_f32 v182,v182
v_exp_f32 v183,v183
v_exp_f32 v184,v184
v_mfma_f32_32x32x16_bf16 v[112:127],v[212:215],v[196:199],v[112:127]
v_exp_f32 v185,v185
v_exp_f32 v186,v186
v_exp_f32 v187,v187
v_mfma_f32_32x32x16_bf16 v[128:143],v[216:219],v[192:195],v[128:143]
v_exp_f32 v188,v188
v_exp_f32 v189,v189
v_mfma_f32_32x32x16_bf16 v[128:143],v[220:223],v[196:199],v[128:143]
v_exp_f32 v190,v190
v_exp_f32 v191,v191
ds_read_b64_tr_b16 v[200:201],v16 offset:0
ds_read_b64_tr_b16 v[202:203],v16 offset:512
ds_read_b64_tr_b16 v[204:205],v16 offset:1024
ds_read_b64_tr_b16 v[206:207],v16 offset:1536
ds_read_b64_tr_b16 v[208:209],v16 offset:2048
ds_read_b64_tr_b16 v[210:211],v16 offset:2560
ds_read_b64_tr_b16 v[212:213],v16 offset:3072
ds_read_b64_tr_b16 v[214:215],v16 offset:3584
ds_read_b64_tr_b16 v[216:217],v16 offset:4096
ds_read_b64_tr_b16 v[218:219],v16 offset:4608
ds_read_b64_tr_b16 v[220:221],v16 offset:5120
ds_read_b64_tr_b16 v[222:223],v16 offset:5632
ds_read_b128 v[160:163],v18 offset:384
ds_read_b128 v[164:167],v18 offset:416
ds_read_b128 v[168:171],v18 offset:448
ds_read_b128 v[172:175],v18 offset:480
s_mov_b64 exec,s[62:63]
s_add_u32 m0,s68,13824
s_add_u32 s64,s59,393216
buffer_load_dwordx4 v27,s[36:39],s64 offen lds
s_mov_b64 exec,-1
s_setprio 0
v_add_f32 v176,1.0,v176
v_add_f32 v177,1.0,v177
v_add_f32 v178,1.0,v178
v_add_f32 v179,1.0,v179
v_add_f32 v180,1.0,v180
v_add_f32 v181,1.0,v181
v_add_f32 v182,1.0,v182
v_add_f32 v183,1.0,v183
v_add_f32 v184,1.0,v184
v_add_f32 v185,1.0,v185
v_add_f32 v186,1.0,v186
v_add_f32 v187,1.0,v187
v_add_f32 v188,1.0,v188
v_add_f32 v189,1.0,v189
v_add_f32 v190,1.0,v190
v_add_f32 v191,1.0,v191
s_setprio 1
s_waitcnt lgkmcnt(0)
v_mfma_f32_32x32x16_bf16 v[160:175],v[200:203],v[72:75],v[160:175]
v_rcp_f32 v176,v176
v_rcp_f32 v177,v177
v_rcp_f32 v178,v178
v_mfma_f32_32x32x16_bf16 v[160:175],v[204:207],v[76:79],v[160:175]
v_rcp_f32 v179,v179
v_rcp_f32 v180,v180
v_rcp_f32 v181,v181
v_mfma_f32_32x32x16_bf16 v[160:175],v[208:211],v[80:83],v[160:175]
v_rcp_f32 v182,v182
v_rcp_f32 v183,v183
v_rcp_f32 v184,v184
v_mfma_f32_32x32x16_bf16 v[160:175],v[212:215],v[84:87],v[160:175]
v_rcp_f32 v185,v185
v_rcp_f32 v186,v186
v_rcp_f32 v187,v187
v_mfma_f32_32x32x16_bf16 v[160:175],v[216:219],v[88:91],v[160:175]
v_rcp_f32 v188,v188
v_rcp_f32 v189,v189
v_mfma_f32_32x32x16_bf16 v[160:175],v[220:223],v[92:95],v[160:175]
v_rcp_f32 v190,v190
v_rcp_f32 v191,v191
ds_read_b64_tr_b16 v[200:201],v17 offset:12288
ds_read_b64_tr_b16 v[202:203],v17 offset:13824
ds_read_b64_tr_b16 v[204:205],v17 offset:15360
ds_read_b64_tr_b16 v[206:207],v17 offset:16896
ds_read_b64_tr_b16 v[208:209],v17 offset:12352
ds_read_b64_tr_b16 v[210:211],v17 offset:13888
ds_read_b64_tr_b16 v[212:213],v17 offset:15424
ds_read_b64_tr_b16 v[214:215],v17 offset:16960
ds_read_b64_tr_b16 v[216:217],v17 offset:12416
ds_read_b64_tr_b16 v[218:219],v17 offset:13952
ds_read_b64_tr_b16 v[220:221],v17 offset:15488
ds_read_b64_tr_b16 v[222:223],v17 offset:17024
s_mov_b64 exec,s[62:63]
s_add_u32 m0,s68,17280
s_add_u32 s64,s59,491520
buffer_load_dwordx4 v27,s[36:39],s64 offen lds
s_mov_b64 exec,-1
s_setprio 0
v_mul_f32 v176,v144,v176
v_mul_f32 v177,v145,v177
v_mul_f32 v178,v146,v178
v_mul_f32 v179,v147,v179
v_mul_f32 v180,v148,v180
v_mul_f32 v181,v149,v181
v_mul_f32 v182,v150,v182
v_mul_f32 v183,v151,v183
v_mul_f32 v184,v152,v184
v_mul_f32 v185,v153,v185
v_mul_f32 v186,v154,v186
v_mul_f32 v187,v155,v187
v_mul_f32 v188,v156,v188
v_mul_f32 v189,v157,v189
v_mul_f32 v190,v158,v190
v_mul_f32 v191,v159,v191
v_cvt_pk_bf16_f32 v192,v176,v177
v_cvt_pk_bf16_f32 v193,v178,v179
v_cvt_pk_bf16_f32 v194,v180,v181
v_cvt_pk_bf16_f32 v195,v182,v183
v_cvt_pk_bf16_f32 v196,v184,v185
v_cvt_pk_bf16_f32 v197,v186,v187
v_cvt_pk_bf16_f32 v198,v188,v189
v_cvt_pk_bf16_f32 v199,v190,v191
s_waitcnt lgkmcnt(0)
s_barrier
v_fmaak_f32 v176,v160,v160,0x409d30fc
v_fmaak_f32 v177,v161,v161,0x409d30fc
v_fmaak_f32 v178,v162,v162,0x409d30fc
v_fmaak_f32 v179,v163,v163,0x409d30fc
v_fmaak_f32 v180,v164,v164,0x409d30fc
v_fmaak_f32 v181,v165,v165,0x409d30fc
v_fmaak_f32 v182,v166,v166,0x409d30fc
v_fmaak_f32 v183,v167,v167,0x409d30fc
v_fmaak_f32 v184,v168,v168,0x409d30fc
v_fmaak_f32 v185,v169,v169,0x409d30fc
v_fmaak_f32 v186,v170,v170,0x409d30fc
v_fmaak_f32 v187,v171,v171,0x409d30fc
v_fmaak_f32 v188,v172,v172,0x409d30fc
v_fmaak_f32 v189,v173,v173,0x409d30fc
v_fmaak_f32 v190,v174,v174,0x409d30fc
v_fmaak_f32 v191,v175,v175,0x409d30fc
v_mul_f32 v176,v160,v176
v_mul_f32 v177,v161,v177
v_mul_f32 v178,v162,v178
v_mul_f32 v179,v163,v179
v_mul_f32 v180,v164,v180
v_mul_f32 v181,v165,v181
v_mul_f32 v182,v166,v182
v_mul_f32 v183,v167,v183
v_mul_f32 v184,v168,v184
v_mul_f32 v185,v169,v185
v_mul_f32 v186,v170,v186
v_mul_f32 v187,v171,v187
v_mul_f32 v188,v172,v188
v_mul_f32 v189,v173,v189
v_mul_f32 v190,v174,v190
v_mul_f32 v191,v175,v191
s_setprio 1
v_mfma_f32_32x32x16_bf16 v[96:111],v[200:203],v[192:195],v[96:111]
v_exp_f32 v176,v176
v_exp_f32 v177,v177
v_exp_f32 v178,v178
v_mfma_f32_32x32x16_bf16 v[96:111],v[204:207],v[196:199],v[96:111]
v_exp_f32 v179,v179
v_exp_f32 v180,v180
v_exp_f32 v181,v181
v_mfma_f32_32x32x16_bf16 v[112:127],v[208:211],v[192:195],v[112:127]
v_exp_f32 v182,v182
v_exp_f32 v183,v183
v_exp_f32 v184,v184
v_mfma_f32_32x32x16_bf16 v[112:127],v[212:215],v[196:199],v[112:127]
v_exp_f32 v185,v185
v_exp_f32 v186,v186
v_exp_f32 v187,v187
v_mfma_f32_32x32x16_bf16 v[128:143],v[216:219],v[192:195],v[128:143]
v_exp_f32 v188,v188
v_exp_f32 v189,v189
v_mfma_f32_32x32x16_bf16 v[128:143],v[220:223],v[196:199],v[128:143]
v_exp_f32 v190,v190
v_exp_f32 v191,v191
ds_read_b64_tr_b16 v[200:201],v16 offset:6144
ds_read_b64_tr_b16 v[202:203],v16 offset:6656
ds_read_b64_tr_b16 v[204:205],v16 offset:7168
ds_read_b64_tr_b16 v[206:207],v16 offset:7680
ds_read_b64_tr_b16 v[208:209],v16 offset:8192
ds_read_b64_tr_b16 v[210:211],v16 offset:8704
ds_read_b64_tr_b16 v[212:213],v16 offset:9216
ds_read_b64_tr_b16 v[214:215],v16 offset:9728
ds_read_b64_tr_b16 v[216:217],v16 offset:10240
ds_read_b64_tr_b16 v[218:219],v16 offset:10752
ds_read_b64_tr_b16 v[220:221],v16 offset:11264
ds_read_b64_tr_b16 v[222:223],v16 offset:11776
ds_read_b128 v[144:147],v18 offset:512
ds_read_b128 v[148:151],v18 offset:544
ds_read_b128 v[152:155],v18 offset:576
ds_read_b128 v[156:159],v18 offset:608
s_mov_b64 exec,s[62:63]
s_add_u32 m0,s68,20736
s_add_u32 s64,s59,589824
buffer_load_dwordx4 v27,s[36:39],s64 offen lds
s_mov_b64 exec,-1
s_setprio 0
v_add_f32 v176,1.0,v176
v_add_f32 v177,1.0,v177
v_add_f32 v178,1.0,v178
v_add_f32 v179,1.0,v179
v_add_f32 v180,1.0,v180
v_add_f32 v181,1.0,v181
v_add_f32 v182,1.0,v182
v_add_f32 v183,1.0,v183
v_add_f32 v184,1.0,v184
v_add_f32 v185,1.0,v185
v_add_f32 v186,1.0,v186
v_add_f32 v187,1.0,v187
v_add_f32 v188,1.0,v188
v_add_f32 v189,1.0,v189
v_add_f32 v190,1.0,v190
v_add_f32 v191,1.0,v191
s_setprio 1
s_waitcnt lgkmcnt(0)
v_mfma_f32_32x32x16_bf16 v[144:159],v[200:203],v[72:75],v[144:159]
v_rcp_f32 v176,v176
v_rcp_f32 v177,v177
v_rcp_f32 v178,v178
v_mfma_f32_32x32x16_bf16 v[144:159],v[204:207],v[76:79],v[144:159]
v_rcp_f32 v179,v179
v_rcp_f32 v180,v180
v_rcp_f32 v181,v181
v_mfma_f32_32x32x16_bf16 v[144:159],v[208:211],v[80:83],v[144:159]
v_rcp_f32 v182,v182
v_rcp_f32 v183,v183
v_rcp_f32 v184,v184
v_mfma_f32_32x32x16_bf16 v[144:159],v[212:215],v[84:87],v[144:159]
v_rcp_f32 v185,v185
v_rcp_f32 v186,v186
v_rcp_f32 v187,v187
v_mfma_f32_32x32x16_bf16 v[144:159],v[216:219],v[88:91],v[144:159]
v_rcp_f32 v188,v188
v_rcp_f32 v189,v189
v_mfma_f32_32x32x16_bf16 v[144:159],v[220:223],v[92:95],v[144:159]
v_rcp_f32 v190,v190
v_rcp_f32 v191,v191
ds_read_b64_tr_b16 v[200:201],v17 offset:0
ds_read_b64_tr_b16 v[202:203],v17 offset:1536
ds_read_b64_tr_b16 v[204:205],v17 offset:3072
ds_read_b64_tr_b16 v[206:207],v17 offset:4608
ds_read_b64_tr_b16 v[208:209],v17 offset:64
ds_read_b64_tr_b16 v[210:211],v17 offset:1600
ds_read_b64_tr_b16 v[212:213],v17 offset:3136
ds_read_b64_tr_b16 v[214:215],v17 offset:4672
ds_read_b64_tr_b16 v[216:217],v17 offset:128
ds_read_b64_tr_b16 v[218:219],v17 offset:1664
ds_read_b64_tr_b16 v[220:221],v17 offset:3200
ds_read_b64_tr_b16 v[222:223],v17 offset:4736
s_mov_b64 exec,s[62:63]
s_add_u32 m0,s68,24192
s_add_u32 s64,s59,688128
buffer_load_dwordx4 v27,s[36:39],s64 offen lds
s_mov_b64 exec,-1
s_setprio 0
v_mul_f32 v176,v160,v176
v_mul_f32 v177,v161,v177
v_mul_f32 v178,v162,v178
v_mul_f32 v179,v163,v179
v_mul_f32 v180,v164,v180
v_mul_f32 v181,v165,v181
v_mul_f32 v182,v166,v182
v_mul_f32 v183,v167,v183
v_mul_f32 v184,v168,v184
v_mul_f32 v185,v169,v185
v_mul_f32 v186,v170,v186
v_mul_f32 v187,v171,v187
v_mul_f32 v188,v172,v188
v_mul_f32 v189,v173,v189
v_mul_f32 v190,v174,v190
v_mul_f32 v191,v175,v191
v_cvt_pk_bf16_f32 v192,v176,v177
v_cvt_pk_bf16_f32 v193,v178,v179
v_cvt_pk_bf16_f32 v194,v180,v181
v_cvt_pk_bf16_f32 v195,v182,v183
v_cvt_pk_bf16_f32 v196,v184,v185
v_cvt_pk_bf16_f32 v197,v186,v187
v_cvt_pk_bf16_f32 v198,v188,v189
v_cvt_pk_bf16_f32 v199,v190,v191
s_waitcnt lgkmcnt(0)
s_barrier
v_fmaak_f32 v176,v144,v144,0x409d30fc
v_fmaak_f32 v177,v145,v145,0x409d30fc
v_fmaak_f32 v178,v146,v146,0x409d30fc
v_fmaak_f32 v179,v147,v147,0x409d30fc
v_fmaak_f32 v180,v148,v148,0x409d30fc
v_fmaak_f32 v181,v149,v149,0x409d30fc
v_fmaak_f32 v182,v150,v150,0x409d30fc
v_fmaak_f32 v183,v151,v151,0x409d30fc
v_fmaak_f32 v184,v152,v152,0x409d30fc
v_fmaak_f32 v185,v153,v153,0x409d30fc
v_fmaak_f32 v186,v154,v154,0x409d30fc
v_fmaak_f32 v187,v155,v155,0x409d30fc
v_fmaak_f32 v188,v156,v156,0x409d30fc
v_fmaak_f32 v189,v157,v157,0x409d30fc
v_fmaak_f32 v190,v158,v158,0x409d30fc
v_fmaak_f32 v191,v159,v159,0x409d30fc
v_mul_f32 v176,v144,v176
v_mul_f32 v177,v145,v177
v_mul_f32 v178,v146,v178
v_mul_f32 v179,v147,v179
v_mul_f32 v180,v148,v180
v_mul_f32 v181,v149,v181
v_mul_f32 v182,v150,v182
v_mul_f32 v183,v151,v183
v_mul_f32 v184,v152,v184
v_mul_f32 v185,v153,v185
v_mul_f32 v186,v154,v186
v_mul_f32 v187,v155,v187
v_mul_f32 v188,v156,v188
v_mul_f32 v189,v157,v189
v_mul_f32 v190,v158,v190
v_mul_f32 v191,v159,v191
s_setprio 1
v_mfma_f32_32x32x16_bf16 v[96:111],v[200:203],v[192:195],v[96:111]
v_exp_f32 v176,v176
v_exp_f32 v177,v177
v_exp_f32 v178,v178
v_mfma_f32_32x32x16_bf16 v[96:111],v[204:207],v[196:199],v[96:111]
v_exp_f32 v179,v179
v_exp_f32 v180,v180
v_exp_f32 v181,v181
v_mfma_f32_32x32x16_bf16 v[112:127],v[208:211],v[192:195],v[112:127]
v_exp_f32 v182,v182
v_exp_f32 v183,v183
v_exp_f32 v184,v184
v_mfma_f32_32x32x16_bf16 v[112:127],v[212:215],v[196:199],v[112:127]
v_exp_f32 v185,v185
v_exp_f32 v186,v186
v_exp_f32 v187,v187
v_mfma_f32_32x32x16_bf16 v[128:143],v[216:219],v[192:195],v[128:143]
v_exp_f32 v188,v188
v_exp_f32 v189,v189
v_mfma_f32_32x32x16_bf16 v[128:143],v[220:223],v[196:199],v[128:143]
v_exp_f32 v190,v190
v_exp_f32 v191,v191
ds_read_b64_tr_b16 v[200:201],v16 offset:12288
ds_read_b64_tr_b16 v[202:203],v16 offset:12800
ds_read_b64_tr_b16 v[204:205],v16 offset:13312
ds_read_b64_tr_b16 v[206:207],v16 offset:13824
ds_read_b64_tr_b16 v[208:209],v16 offset:14336
ds_read_b64_tr_b16 v[210:211],v16 offset:14848
ds_read_b64_tr_b16 v[212:213],v16 offset:15360
ds_read_b64_tr_b16 v[214:215],v16 offset:15872
ds_read_b64_tr_b16 v[216:217],v16 offset:16384
ds_read_b64_tr_b16 v[218:219],v16 offset:16896
ds_read_b64_tr_b16 v[220:221],v16 offset:17408
ds_read_b64_tr_b16 v[222:223],v16 offset:17920
ds_read_b128 v[160:163],v18 offset:640
ds_read_b128 v[164:167],v18 offset:672
ds_read_b128 v[168:171],v18 offset:704
ds_read_b128 v[172:175],v18 offset:736
s_mov_b64 exec,s[62:63]
s_add_u32 m0,s68,27648
s_add_u32 s64,s59,786432
buffer_load_dwordx4 v27,s[36:39],s64 offen lds
s_mov_b64 exec,-1
s_setprio 0
v_add_f32 v176,1.0,v176
v_add_f32 v177,1.0,v177
v_add_f32 v178,1.0,v178
v_add_f32 v179,1.0,v179
v_add_f32 v180,1.0,v180
v_add_f32 v181,1.0,v181
v_add_f32 v182,1.0,v182
v_add_f32 v183,1.0,v183
v_add_f32 v184,1.0,v184
v_add_f32 v185,1.0,v185
v_add_f32 v186,1.0,v186
v_add_f32 v187,1.0,v187
v_add_f32 v188,1.0,v188
v_add_f32 v189,1.0,v189
v_add_f32 v190,1.0,v190
v_add_f32 v191,1.0,v191
s_setprio 1
s_waitcnt lgkmcnt(0)
v_mfma_f32_32x32x16_bf16 v[160:175],v[200:203],v[72:75],v[160:175]
v_rcp_f32 v176,v176
v_rcp_f32 v177,v177
v_rcp_f32 v178,v178
v_mfma_f32_32x32x16_bf16 v[160:175],v[204:207],v[76:79],v[160:175]
v_rcp_f32 v179,v179
v_rcp_f32 v180,v180
v_rcp_f32 v181,v181
v_mfma_f32_32x32x16_bf16 v[160:175],v[208:211],v[80:83],v[160:175]
v_rcp_f32 v182,v182
v_rcp_f32 v183,v183
v_rcp_f32 v184,v184
v_mfma_f32_32x32x16_bf16 v[160:175],v[212:215],v[84:87],v[160:175]
v_rcp_f32 v185,v185
v_rcp_f32 v186,v186
v_rcp_f32 v187,v187
v_mfma_f32_32x32x16_bf16 v[160:175],v[216:219],v[88:91],v[160:175]
v_rcp_f32 v188,v188
v_rcp_f32 v189,v189
v_mfma_f32_32x32x16_bf16 v[160:175],v[220:223],v[92:95],v[160:175]
v_rcp_f32 v190,v190
v_rcp_f32 v191,v191
ds_read_b64_tr_b16 v[200:201],v17 offset:6144
ds_read_b64_tr_b16 v[202:203],v17 offset:7680
ds_read_b64_tr_b16 v[204:205],v17 offset:9216
ds_read_b64_tr_b16 v[206:207],v17 offset:10752
ds_read_b64_tr_b16 v[208:209],v17 offset:6208
ds_read_b64_tr_b16 v[210:211],v17 offset:7744
ds_read_b64_tr_b16 v[212:213],v17 offset:9280
ds_read_b64_tr_b16 v[214:215],v17 offset:10816
ds_read_b64_tr_b16 v[216:217],v17 offset:6272
ds_read_b64_tr_b16 v[218:219],v17 offset:7808
ds_read_b64_tr_b16 v[220:221],v17 offset:9344
ds_read_b64_tr_b16 v[222:223],v17 offset:10880
s_mov_b64 exec,s[62:63]
s_add_u32 m0,s68,31104
s_add_u32 s64,s59,884736
buffer_load_dwordx4 v27,s[36:39],s64 offen lds
s_mov_b64 exec,-1
s_setprio 0
v_mul_f32 v176,v144,v176
v_mul_f32 v177,v145,v177
v_mul_f32 v178,v146,v178
v_mul_f32 v179,v147,v179
v_mul_f32 v180,v148,v180
v_mul_f32 v181,v149,v181
v_mul_f32 v182,v150,v182
v_mul_f32 v183,v151,v183
v_mul_f32 v184,v152,v184
v_mul_f32 v185,v153,v185
v_mul_f32 v186,v154,v186
v_mul_f32 v187,v155,v187
v_mul_f32 v188,v156,v188
v_mul_f32 v189,v157,v189
v_mul_f32 v190,v158,v190
v_mul_f32 v191,v159,v191
v_cvt_pk_bf16_f32 v192,v176,v177
v_cvt_pk_bf16_f32 v193,v178,v179
v_cvt_pk_bf16_f32 v194,v180,v181
v_cvt_pk_bf16_f32 v195,v182,v183
v_cvt_pk_bf16_f32 v196,v184,v185
v_cvt_pk_bf16_f32 v197,v186,v187
v_cvt_pk_bf16_f32 v198,v188,v189
v_cvt_pk_bf16_f32 v199,v190,v191
s_waitcnt lgkmcnt(0)
s_barrier
v_fmaak_f32 v176,v160,v160,0x409d30fc
v_fmaak_f32 v177,v161,v161,0x409d30fc
v_fmaak_f32 v178,v162,v162,0x409d30fc
v_fmaak_f32 v179,v163,v163,0x409d30fc
v_fmaak_f32 v180,v164,v164,0x409d30fc
v_fmaak_f32 v181,v165,v165,0x409d30fc
v_fmaak_f32 v182,v166,v166,0x409d30fc
v_fmaak_f32 v183,v167,v167,0x409d30fc
v_fmaak_f32 v184,v168,v168,0x409d30fc
v_fmaak_f32 v185,v169,v169,0x409d30fc
v_fmaak_f32 v186,v170,v170,0x409d30fc
v_fmaak_f32 v187,v171,v171,0x409d30fc
v_fmaak_f32 v188,v172,v172,0x409d30fc
v_fmaak_f32 v189,v173,v173,0x409d30fc
v_fmaak_f32 v190,v174,v174,0x409d30fc
v_fmaak_f32 v191,v175,v175,0x409d30fc
v_mul_f32 v176,v160,v176
v_mul_f32 v177,v161,v177
v_mul_f32 v178,v162,v178
v_mul_f32 v179,v163,v179
v_mul_f32 v180,v164,v180
v_mul_f32 v181,v165,v181
v_mul_f32 v182,v166,v182
v_mul_f32 v183,v167,v183
v_mul_f32 v184,v168,v184
v_mul_f32 v185,v169,v185
v_mul_f32 v186,v170,v186
v_mul_f32 v187,v171,v187
v_mul_f32 v188,v172,v188
v_mul_f32 v189,v173,v189
v_mul_f32 v190,v174,v190
v_mul_f32 v191,v175,v191
s_setprio 1
v_mfma_f32_32x32x16_bf16 v[96:111],v[200:203],v[192:195],v[96:111]
v_exp_f32 v176,v176
v_exp_f32 v177,v177
v_exp_f32 v178,v178
v_mfma_f32_32x32x16_bf16 v[96:111],v[204:207],v[196:199],v[96:111]
v_exp_f32 v179,v179
v_exp_f32 v180,v180
v_exp_f32 v181,v181
v_mfma_f32_32x32x16_bf16 v[112:127],v[208:211],v[192:195],v[112:127]
v_exp_f32 v182,v182
v_exp_f32 v183,v183
v_exp_f32 v184,v184
v_mfma_f32_32x32x16_bf16 v[112:127],v[212:215],v[196:199],v[112:127]
v_exp_f32 v185,v185
v_exp_f32 v186,v186
v_exp_f32 v187,v187
v_mfma_f32_32x32x16_bf16 v[128:143],v[216:219],v[192:195],v[128:143]
v_exp_f32 v188,v188
v_exp_f32 v189,v189
v_mfma_f32_32x32x16_bf16 v[128:143],v[220:223],v[196:199],v[128:143]
v_exp_f32 v190,v190
v_exp_f32 v191,v191
ds_read_b64_tr_b16 v[200:201],v16 offset:0
ds_read_b64_tr_b16 v[202:203],v16 offset:512
ds_read_b64_tr_b16 v[204:205],v16 offset:1024
ds_read_b64_tr_b16 v[206:207],v16 offset:1536
ds_read_b64_tr_b16 v[208:209],v16 offset:2048
ds_read_b64_tr_b16 v[210:211],v16 offset:2560
ds_read_b64_tr_b16 v[212:213],v16 offset:3072
ds_read_b64_tr_b16 v[214:215],v16 offset:3584
ds_read_b64_tr_b16 v[216:217],v16 offset:4096
ds_read_b64_tr_b16 v[218:219],v16 offset:4608
ds_read_b64_tr_b16 v[220:221],v16 offset:5120
ds_read_b64_tr_b16 v[222:223],v16 offset:5632
ds_read_b128 v[144:147],v18 offset:768
ds_read_b128 v[148:151],v18 offset:800
ds_read_b128 v[152:155],v18 offset:832
ds_read_b128 v[156:159],v18 offset:864
s_mov_b64 exec,s[62:63]
s_add_u32 m0,s68,34560
s_add_u32 s64,s59,983040
buffer_load_dwordx4 v27,s[36:39],s64 offen lds
s_mov_b64 exec,-1
s_setprio 0
v_add_f32 v176,1.0,v176
v_add_f32 v177,1.0,v177
v_add_f32 v178,1.0,v178
v_add_f32 v179,1.0,v179
v_add_f32 v180,1.0,v180
v_add_f32 v181,1.0,v181
v_add_f32 v182,1.0,v182
v_add_f32 v183,1.0,v183
v_add_f32 v184,1.0,v184
v_add_f32 v185,1.0,v185
v_add_f32 v186,1.0,v186
v_add_f32 v187,1.0,v187
v_add_f32 v188,1.0,v188
v_add_f32 v189,1.0,v189
v_add_f32 v190,1.0,v190
v_add_f32 v191,1.0,v191
s_setprio 1
s_waitcnt lgkmcnt(0)
v_mfma_f32_32x32x16_bf16 v[144:159],v[200:203],v[72:75],v[144:159]
v_rcp_f32 v176,v176
v_rcp_f32 v177,v177
v_rcp_f32 v178,v178
v_mfma_f32_32x32x16_bf16 v[144:159],v[204:207],v[76:79],v[144:159]
v_rcp_f32 v179,v179
v_rcp_f32 v180,v180
v_rcp_f32 v181,v181
v_mfma_f32_32x32x16_bf16 v[144:159],v[208:211],v[80:83],v[144:159]
v_rcp_f32 v182,v182
v_rcp_f32 v183,v183
v_rcp_f32 v184,v184
v_mfma_f32_32x32x16_bf16 v[144:159],v[212:215],v[84:87],v[144:159]
v_rcp_f32 v185,v185
v_rcp_f32 v186,v186
v_rcp_f32 v187,v187
v_mfma_f32_32x32x16_bf16 v[144:159],v[216:219],v[88:91],v[144:159]
v_rcp_f32 v188,v188
v_rcp_f32 v189,v189
v_mfma_f32_32x32x16_bf16 v[144:159],v[220:223],v[92:95],v[144:159]
v_rcp_f32 v190,v190
v_rcp_f32 v191,v191
ds_read_b64_tr_b16 v[200:201],v17 offset:12288
ds_read_b64_tr_b16 v[202:203],v17 offset:13824
ds_read_b64_tr_b16 v[204:205],v17 offset:15360
ds_read_b64_tr_b16 v[206:207],v17 offset:16896
ds_read_b64_tr_b16 v[208:209],v17 offset:12352
ds_read_b64_tr_b16 v[210:211],v17 offset:13888
ds_read_b64_tr_b16 v[212:213],v17 offset:15424
ds_read_b64_tr_b16 v[214:215],v17 offset:16960
ds_read_b64_tr_b16 v[216:217],v17 offset:12416
ds_read_b64_tr_b16 v[218:219],v17 offset:13952
ds_read_b64_tr_b16 v[220:221],v17 offset:15488
ds_read_b64_tr_b16 v[222:223],v17 offset:17024
s_mov_b64 exec,s[62:63]
s_add_u32 m0,s68,38016
s_add_u32 s64,s59,1081344
buffer_load_dwordx4 v27,s[36:39],s64 offen lds
s_mov_b64 exec,-1
s_setprio 0
v_mul_f32 v176,v160,v176
v_mul_f32 v177,v161,v177
v_mul_f32 v178,v162,v178
v_mul_f32 v179,v163,v179
v_mul_f32 v180,v164,v180
v_mul_f32 v181,v165,v181
v_mul_f32 v182,v166,v182
v_mul_f32 v183,v167,v183
v_mul_f32 v184,v168,v184
v_mul_f32 v185,v169,v185
v_mul_f32 v186,v170,v186
v_mul_f32 v187,v171,v187
v_mul_f32 v188,v172,v188
v_mul_f32 v189,v173,v189
v_mul_f32 v190,v174,v190
v_mul_f32 v191,v175,v191
v_cvt_pk_bf16_f32 v192,v176,v177
v_cvt_pk_bf16_f32 v193,v178,v179
v_cvt_pk_bf16_f32 v194,v180,v181
v_cvt_pk_bf16_f32 v195,v182,v183
v_cvt_pk_bf16_f32 v196,v184,v185
v_cvt_pk_bf16_f32 v197,v186,v187
v_cvt_pk_bf16_f32 v198,v188,v189
v_cvt_pk_bf16_f32 v199,v190,v191
s_waitcnt lgkmcnt(0)
s_barrier
v_fmaak_f32 v176,v144,v144,0x409d30fc
v_fmaak_f32 v177,v145,v145,0x409d30fc
v_fmaak_f32 v178,v146,v146,0x409d30fc
v_fmaak_f32 v179,v147,v147,0x409d30fc
v_fmaak_f32 v180,v148,v148,0x409d30fc
v_fmaak_f32 v181,v149,v149,0x409d30fc
v_fmaak_f32 v182,v150,v150,0x409d30fc
v_fmaak_f32 v183,v151,v151,0x409d30fc
v_fmaak_f32 v184,v152,v152,0x409d30fc
v_fmaak_f32 v185,v153,v153,0x409d30fc
v_fmaak_f32 v186,v154,v154,0x409d30fc
v_fmaak_f32 v187,v155,v155,0x409d30fc
v_fmaak_f32 v188,v156,v156,0x409d30fc
v_fmaak_f32 v189,v157,v157,0x409d30fc
v_fmaak_f32 v190,v158,v158,0x409d30fc
v_fmaak_f32 v191,v159,v159,0x409d30fc
v_mul_f32 v176,v144,v176
v_mul_f32 v177,v145,v177
v_mul_f32 v178,v146,v178
v_mul_f32 v179,v147,v179
v_mul_f32 v180,v148,v180
v_mul_f32 v181,v149,v181
v_mul_f32 v182,v150,v182
v_mul_f32 v183,v151,v183
v_mul_f32 v184,v152,v184
v_mul_f32 v185,v153,v185
v_mul_f32 v186,v154,v186
v_mul_f32 v187,v155,v187
v_mul_f32 v188,v156,v188
v_mul_f32 v189,v157,v189
v_mul_f32 v190,v158,v190
v_mul_f32 v191,v159,v191
s_setprio 1
v_mfma_f32_32x32x16_bf16 v[96:111],v[200:203],v[192:195],v[96:111]
v_exp_f32 v176,v176
v_exp_f32 v177,v177
v_exp_f32 v178,v178
v_mfma_f32_32x32x16_bf16 v[96:111],v[204:207],v[196:199],v[96:111]
v_exp_f32 v179,v179
v_exp_f32 v180,v180
v_exp_f32 v181,v181
v_mfma_f32_32x32x16_bf16 v[112:127],v[208:211],v[192:195],v[112:127]
v_exp_f32 v182,v182
v_exp_f32 v183,v183
v_exp_f32 v184,v184
v_mfma_f32_32x32x16_bf16 v[112:127],v[212:215],v[196:199],v[112:127]
v_exp_f32 v185,v185
v_exp_f32 v186,v186
v_exp_f32 v187,v187
v_mfma_f32_32x32x16_bf16 v[128:143],v[216:219],v[192:195],v[128:143]
v_exp_f32 v188,v188
v_exp_f32 v189,v189
v_mfma_f32_32x32x16_bf16 v[128:143],v[220:223],v[196:199],v[128:143]
v_exp_f32 v190,v190
v_exp_f32 v191,v191
ds_read_b64_tr_b16 v[200:201],v16 offset:6144
ds_read_b64_tr_b16 v[202:203],v16 offset:6656
ds_read_b64_tr_b16 v[204:205],v16 offset:7168
ds_read_b64_tr_b16 v[206:207],v16 offset:7680
ds_read_b64_tr_b16 v[208:209],v16 offset:8192
ds_read_b64_tr_b16 v[210:211],v16 offset:8704
ds_read_b64_tr_b16 v[212:213],v16 offset:9216
ds_read_b64_tr_b16 v[214:215],v16 offset:9728
ds_read_b64_tr_b16 v[216:217],v16 offset:10240
ds_read_b64_tr_b16 v[218:219],v16 offset:10752
ds_read_b64_tr_b16 v[220:221],v16 offset:11264
ds_read_b64_tr_b16 v[222:223],v16 offset:11776
ds_read_b128 v[160:163],v18 offset:896
ds_read_b128 v[164:167],v18 offset:928
ds_read_b128 v[168:171],v18 offset:960
ds_read_b128 v[172:175],v18 offset:992
s_mov_b64 exec,s[62:63]
s_add_u32 m0,s68,41472
s_add_u32 s64,s59,1179648
buffer_load_dwordx4 v27,s[36:39],s64 offen lds
s_mov_b64 exec,-1
s_setprio 0
v_add_f32 v176,1.0,v176
v_add_f32 v177,1.0,v177
v_add_f32 v178,1.0,v178
v_add_f32 v179,1.0,v179
v_add_f32 v180,1.0,v180
v_add_f32 v181,1.0,v181
v_add_f32 v182,1.0,v182
v_add_f32 v183,1.0,v183
v_add_f32 v184,1.0,v184
v_add_f32 v185,1.0,v185
v_add_f32 v186,1.0,v186
v_add_f32 v187,1.0,v187
v_add_f32 v188,1.0,v188
v_add_f32 v189,1.0,v189
v_add_f32 v190,1.0,v190
v_add_f32 v191,1.0,v191
s_setprio 1
s_waitcnt lgkmcnt(0)
v_mfma_f32_32x32x16_bf16 v[160:175],v[200:203],v[72:75],v[160:175]
v_rcp_f32 v176,v176
v_rcp_f32 v177,v177
v_rcp_f32 v178,v178
v_mfma_f32_32x32x16_bf16 v[160:175],v[204:207],v[76:79],v[160:175]
v_rcp_f32 v179,v179
v_rcp_f32 v180,v180
v_rcp_f32 v181,v181
v_mfma_f32_32x32x16_bf16 v[160:175],v[208:211],v[80:83],v[160:175]
v_rcp_f32 v182,v182
v_rcp_f32 v183,v183
v_rcp_f32 v184,v184
v_mfma_f32_32x32x16_bf16 v[160:175],v[212:215],v[84:87],v[160:175]
v_rcp_f32 v185,v185
v_rcp_f32 v186,v186
v_rcp_f32 v187,v187
v_mfma_f32_32x32x16_bf16 v[160:175],v[216:219],v[88:91],v[160:175]
v_rcp_f32 v188,v188
v_rcp_f32 v189,v189
v_mfma_f32_32x32x16_bf16 v[160:175],v[220:223],v[92:95],v[160:175]
v_rcp_f32 v190,v190
v_rcp_f32 v191,v191
ds_read_b64_tr_b16 v[200:201],v17 offset:0
ds_read_b64_tr_b16 v[202:203],v17 offset:1536
ds_read_b64_tr_b16 v[204:205],v17 offset:3072
ds_read_b64_tr_b16 v[206:207],v17 offset:4608
ds_read_b64_tr_b16 v[208:209],v17 offset:64
ds_read_b64_tr_b16 v[210:211],v17 offset:1600
ds_read_b64_tr_b16 v[212:213],v17 offset:3136
ds_read_b64_tr_b16 v[214:215],v17 offset:4672
ds_read_b64_tr_b16 v[216:217],v17 offset:128
ds_read_b64_tr_b16 v[218:219],v17 offset:1664
ds_read_b64_tr_b16 v[220:221],v17 offset:3200
ds_read_b64_tr_b16 v[222:223],v17 offset:4736
s_mov_b64 exec,s[62:63]
s_add_u32 m0,s68,44928
s_add_u32 s64,s59,1277952
buffer_load_dwordx4 v27,s[36:39],s64 offen lds
s_mov_b64 exec,-1
s_setprio 0
v_mul_f32 v176,v144,v176
v_mul_f32 v177,v145,v177
v_mul_f32 v178,v146,v178
v_mul_f32 v179,v147,v179
v_mul_f32 v180,v148,v180
v_mul_f32 v181,v149,v181
v_mul_f32 v182,v150,v182
v_mul_f32 v183,v151,v183
v_mul_f32 v184,v152,v184
v_mul_f32 v185,v153,v185
v_mul_f32 v186,v154,v186
v_mul_f32 v187,v155,v187
v_mul_f32 v188,v156,v188
v_mul_f32 v189,v157,v189
v_mul_f32 v190,v158,v190
v_mul_f32 v191,v159,v191
v_cvt_pk_bf16_f32 v192,v176,v177
v_cvt_pk_bf16_f32 v193,v178,v179
v_cvt_pk_bf16_f32 v194,v180,v181
v_cvt_pk_bf16_f32 v195,v182,v183
v_cvt_pk_bf16_f32 v196,v184,v185
v_cvt_pk_bf16_f32 v197,v186,v187
v_cvt_pk_bf16_f32 v198,v188,v189
v_cvt_pk_bf16_f32 v199,v190,v191
s_waitcnt lgkmcnt(0)
s_barrier
v_fmaak_f32 v176,v160,v160,0x409d30fc
v_fmaak_f32 v177,v161,v161,0x409d30fc
v_fmaak_f32 v178,v162,v162,0x409d30fc
v_fmaak_f32 v179,v163,v163,0x409d30fc
v_fmaak_f32 v180,v164,v164,0x409d30fc
v_fmaak_f32 v181,v165,v165,0x409d30fc
v_fmaak_f32 v182,v166,v166,0x409d30fc
v_fmaak_f32 v183,v167,v167,0x409d30fc
v_fmaak_f32 v184,v168,v168,0x409d30fc
v_fmaak_f32 v185,v169,v169,0x409d30fc
v_fmaak_f32 v186,v170,v170,0x409d30fc
v_fmaak_f32 v187,v171,v171,0x409d30fc
v_fmaak_f32 v188,v172,v172,0x409d30fc
v_fmaak_f32 v189,v173,v173,0x409d30fc
v_fmaak_f32 v190,v174,v174,0x409d30fc
v_fmaak_f32 v191,v175,v175,0x409d30fc
v_mul_f32 v176,v160,v176
v_mul_f32 v177,v161,v177
v_mul_f32 v178,v162,v178
v_mul_f32 v179,v163,v179
v_mul_f32 v180,v164,v180
v_mul_f32 v181,v165,v181
v_mul_f32 v182,v166,v182
v_mul_f32 v183,v167,v183
v_mul_f32 v184,v168,v184
v_mul_f32 v185,v169,v185
v_mul_f32 v186,v170,v186
v_mul_f32 v187,v171,v187
v_mul_f32 v188,v172,v188
v_mul_f32 v189,v173,v189
v_mul_f32 v190,v174,v190
v_mul_f32 v191,v175,v191
s_setprio 1
v_mfma_f32_32x32x16_bf16 v[96:111],v[200:203],v[192:195],v[96:111]
v_exp_f32 v176,v176
v_exp_f32 v177,v177
v_exp_f32 v178,v178
v_mfma_f32_32x32x16_bf16 v[96:111],v[204:207],v[196:199],v[96:111]
v_exp_f32 v179,v179
v_exp_f32 v180,v180
v_exp_f32 v181,v181
v_mfma_f32_32x32x16_bf16 v[112:127],v[208:211],v[192:195],v[112:127]
v_exp_f32 v182,v182
v_exp_f32 v183,v183
v_exp_f32 v184,v184
v_mfma_f32_32x32x16_bf16 v[112:127],v[212:215],v[196:199],v[112:127]
v_exp_f32 v185,v185
v_exp_f32 v186,v186
v_exp_f32 v187,v187
v_mfma_f32_32x32x16_bf16 v[128:143],v[216:219],v[192:195],v[128:143]
v_exp_f32 v188,v188
v_exp_f32 v189,v189
v_mfma_f32_32x32x16_bf16 v[128:143],v[220:223],v[196:199],v[128:143]
v_exp_f32 v190,v190
v_exp_f32 v191,v191
ds_read_b64_tr_b16 v[200:201],v16 offset:12288
ds_read_b64_tr_b16 v[202:203],v16 offset:12800
ds_read_b64_tr_b16 v[204:205],v16 offset:13312
ds_read_b64_tr_b16 v[206:207],v16 offset:13824
ds_read_b64_tr_b16 v[208:209],v16 offset:14336
ds_read_b64_tr_b16 v[210:211],v16 offset:14848
ds_read_b64_tr_b16 v[212:213],v16 offset:15360
ds_read_b64_tr_b16 v[214:215],v16 offset:15872
ds_read_b64_tr_b16 v[216:217],v16 offset:16384
ds_read_b64_tr_b16 v[218:219],v16 offset:16896
ds_read_b64_tr_b16 v[220:221],v16 offset:17408
ds_read_b64_tr_b16 v[222:223],v16 offset:17920
ds_read_b128 v[144:147],v18 offset:1024
ds_read_b128 v[148:151],v18 offset:1056
ds_read_b128 v[152:155],v18 offset:1088
ds_read_b128 v[156:159],v18 offset:1120
s_mov_b64 exec,s[62:63]
s_add_u32 m0,s68,48384
s_add_u32 s64,s59,1376256
buffer_load_dwordx4 v27,s[36:39],s64 offen lds
s_mov_b64 exec,-1
s_setprio 0
v_add_f32 v176,1.0,v176
v_add_f32 v177,1.0,v177
v_add_f32 v178,1.0,v178
v_add_f32 v179,1.0,v179
v_add_f32 v180,1.0,v180
v_add_f32 v181,1.0,v181
v_add_f32 v182,1.0,v182
v_add_f32 v183,1.0,v183
v_add_f32 v184,1.0,v184
v_add_f32 v185,1.0,v185
v_add_f32 v186,1.0,v186
v_add_f32 v187,1.0,v187
v_add_f32 v188,1.0,v188
v_add_f32 v189,1.0,v189
v_add_f32 v190,1.0,v190
v_add_f32 v191,1.0,v191
s_setprio 1
s_waitcnt lgkmcnt(0)
v_mfma_f32_32x32x16_bf16 v[144:159],v[200:203],v[72:75],v[144:159]
v_rcp_f32 v176,v176
v_rcp_f32 v177,v177
v_rcp_f32 v178,v178
v_mfma_f32_32x32x16_bf16 v[144:159],v[204:207],v[76:79],v[144:159]
v_rcp_f32 v179,v179
v_rcp_f32 v180,v180
v_rcp_f32 v181,v181
v_mfma_f32_32x32x16_bf16 v[144:159],v[208:211],v[80:83],v[144:159]
v_rcp_f32 v182,v182
v_rcp_f32 v183,v183
v_rcp_f32 v184,v184
v_mfma_f32_32x32x16_bf16 v[144:159],v[212:215],v[84:87],v[144:159]
v_rcp_f32 v185,v185
v_rcp_f32 v186,v186
v_rcp_f32 v187,v187
v_mfma_f32_32x32x16_bf16 v[144:159],v[216:219],v[88:91],v[144:159]
v_rcp_f32 v188,v188
v_rcp_f32 v189,v189
v_mfma_f32_32x32x16_bf16 v[144:159],v[220:223],v[92:95],v[144:159]
v_rcp_f32 v190,v190
v_rcp_f32 v191,v191
ds_read_b64_tr_b16 v[200:201],v17 offset:6144
ds_read_b64_tr_b16 v[202:203],v17 offset:7680
ds_read_b64_tr_b16 v[204:205],v17 offset:9216
ds_read_b64_tr_b16 v[206:207],v17 offset:10752
ds_read_b64_tr_b16 v[208:209],v17 offset:6208
ds_read_b64_tr_b16 v[210:211],v17 offset:7744
ds_read_b64_tr_b16 v[212:213],v17 offset:9280
ds_read_b64_tr_b16 v[214:215],v17 offset:10816
ds_read_b64_tr_b16 v[216:217],v17 offset:6272
ds_read_b64_tr_b16 v[218:219],v17 offset:7808
ds_read_b64_tr_b16 v[220:221],v17 offset:9344
ds_read_b64_tr_b16 v[222:223],v17 offset:10880
s_mov_b64 exec,s[62:63]
s_add_u32 m0,s68,51840
s_add_u32 s64,s59,1474560
buffer_load_dwordx4 v27,s[36:39],s64 offen lds
s_mov_b64 exec,-1
s_setprio 0
v_mul_f32 v176,v160,v176
v_mul_f32 v177,v161,v177
v_mul_f32 v178,v162,v178
v_mul_f32 v179,v163,v179
v_mul_f32 v180,v164,v180
v_mul_f32 v181,v165,v181
v_mul_f32 v182,v166,v182
v_mul_f32 v183,v167,v183
v_mul_f32 v184,v168,v184
v_mul_f32 v185,v169,v185
v_mul_f32 v186,v170,v186
v_mul_f32 v187,v171,v187
v_mul_f32 v188,v172,v188
v_mul_f32 v189,v173,v189
v_mul_f32 v190,v174,v190
v_mul_f32 v191,v175,v191
v_cvt_pk_bf16_f32 v192,v176,v177
v_cvt_pk_bf16_f32 v193,v178,v179
v_cvt_pk_bf16_f32 v194,v180,v181
v_cvt_pk_bf16_f32 v195,v182,v183
v_cvt_pk_bf16_f32 v196,v184,v185
v_cvt_pk_bf16_f32 v197,v186,v187
v_cvt_pk_bf16_f32 v198,v188,v189
v_cvt_pk_bf16_f32 v199,v190,v191
s_waitcnt lgkmcnt(0)
s_barrier
v_fmaak_f32 v176,v144,v144,0x409d30fc
v_fmaak_f32 v177,v145,v145,0x409d30fc
v_fmaak_f32 v178,v146,v146,0x409d30fc
v_fmaak_f32 v179,v147,v147,0x409d30fc
v_fmaak_f32 v180,v148,v148,0x409d30fc
v_fmaak_f32 v181,v149,v149,0x409d30fc
v_fmaak_f32 v182,v150,v150,0x409d30fc
v_fmaak_f32 v183,v151,v151,0x409d30fc
v_fmaak_f32 v184,v152,v152,0x409d30fc
v_fmaak_f32 v185,v153,v153,0x409d30fc
v_fmaak_f32 v186,v154,v154,0x409d30fc
v_fmaak_f32 v187,v155,v155,0x409d30fc
v_fmaak_f32 v188,v156,v156,0x409d30fc
v_fmaak_f32 v189,v157,v157,0x409d30fc
v_fmaak_f32 v190,v158,v158,0x409d30fc
v_fmaak_f32 v191,v159,v159,0x409d30fc
v_mul_f32 v176,v144,v176
v_mul_f32 v177,v145,v177
v_mul_f32 v178,v146,v178
v_mul_f32 v179,v147,v179
v_mul_f32 v180,v148,v180
v_mul_f32 v181,v149,v181
v_mul_f32 v182,v150,v182
v_mul_f32 v183,v151,v183
v_mul_f32 v184,v152,v184
v_mul_f32 v185,v153,v185
v_mul_f32 v186,v154,v186
v_mul_f32 v187,v155,v187
v_mul_f32 v188,v156,v188
v_mul_f32 v189,v157,v189
v_mul_f32 v190,v158,v190
v_mul_f32 v191,v159,v191
s_setprio 1
v_mfma_f32_32x32x16_bf16 v[96:111],v[200:203],v[192:195],v[96:111]
v_exp_f32 v176,v176
v_exp_f32 v177,v177
v_exp_f32 v178,v178
v_mfma_f32_32x32x16_bf16 v[96:111],v[204:207],v[196:199],v[96:111]
v_exp_f32 v179,v179
v_exp_f32 v180,v180
v_exp_f32 v181,v181
v_mfma_f32_32x32x16_bf16 v[112:127],v[208:211],v[192:195],v[112:127]
v_exp_f32 v182,v182
v_exp_f32 v183,v183
v_exp_f32 v184,v184
v_mfma_f32_32x32x16_bf16 v[112:127],v[212:215],v[196:199],v[112:127]
v_exp_f32 v185,v185
v_exp_f32 v186,v186
v_exp_f32 v187,v187
v_mfma_f32_32x32x16_bf16 v[128:143],v[216:219],v[192:195],v[128:143]
v_exp_f32 v188,v188
v_exp_f32 v189,v189
v_mfma_f32_32x32x16_bf16 v[128:143],v[220:223],v[196:199],v[128:143]
v_exp_f32 v190,v190
v_exp_f32 v191,v191
ds_read_b64_tr_b16 v[200:201],v16 offset:0
ds_read_b64_tr_b16 v[202:203],v16 offset:512
ds_read_b64_tr_b16 v[204:205],v16 offset:1024
ds_read_b64_tr_b16 v[206:207],v16 offset:1536
ds_read_b64_tr_b16 v[208:209],v16 offset:2048
ds_read_b64_tr_b16 v[210:211],v16 offset:2560
ds_read_b64_tr_b16 v[212:213],v16 offset:3072
ds_read_b64_tr_b16 v[214:215],v16 offset:3584
ds_read_b64_tr_b16 v[216:217],v16 offset:4096
ds_read_b64_tr_b16 v[218:219],v16 offset:4608
ds_read_b64_tr_b16 v[220:221],v16 offset:5120
ds_read_b64_tr_b16 v[222:223],v16 offset:5632
ds_read_b128 v[160:163],v18 offset:1152
ds_read_b128 v[164:167],v18 offset:1184
ds_read_b128 v[168:171],v18 offset:1216
ds_read_b128 v[172:175],v18 offset:1248
s_mov_b64 exec,s[62:63]
s_add_u32 m0,s68,55296
s_add_u32 s64,s59,1572864
buffer_load_dwordx4 v27,s[36:39],s64 offen lds
s_mov_b64 exec,-1
s_setprio 0
v_add_f32 v176,1.0,v176
v_add_f32 v177,1.0,v177
v_add_f32 v178,1.0,v178
v_add_f32 v179,1.0,v179
v_add_f32 v180,1.0,v180
v_add_f32 v181,1.0,v181
v_add_f32 v182,1.0,v182
v_add_f32 v183,1.0,v183
v_add_f32 v184,1.0,v184
v_add_f32 v185,1.0,v185
v_add_f32 v186,1.0,v186
v_add_f32 v187,1.0,v187
v_add_f32 v188,1.0,v188
v_add_f32 v189,1.0,v189
v_add_f32 v190,1.0,v190
v_add_f32 v191,1.0,v191
s_setprio 1
s_waitcnt lgkmcnt(0)
v_mfma_f32_32x32x16_bf16 v[160:175],v[200:203],v[72:75],v[160:175]
v_rcp_f32 v176,v176
v_rcp_f32 v177,v177
v_rcp_f32 v178,v178
v_mfma_f32_32x32x16_bf16 v[160:175],v[204:207],v[76:79],v[160:175]
v_rcp_f32 v179,v179
v_rcp_f32 v180,v180
v_rcp_f32 v181,v181
v_mfma_f32_32x32x16_bf16 v[160:175],v[208:211],v[80:83],v[160:175]
v_rcp_f32 v182,v182
v_rcp_f32 v183,v183
v_rcp_f32 v184,v184
v_mfma_f32_32x32x16_bf16 v[160:175],v[212:215],v[84:87],v[160:175]
v_rcp_f32 v185,v185
v_rcp_f32 v186,v186
v_rcp_f32 v187,v187
v_mfma_f32_32x32x16_bf16 v[160:175],v[216:219],v[88:91],v[160:175]
v_rcp_f32 v188,v188
v_rcp_f32 v189,v189
v_mfma_f32_32x32x16_bf16 v[160:175],v[220:223],v[92:95],v[160:175]
v_rcp_f32 v190,v190
v_rcp_f32 v191,v191
ds_read_b64_tr_b16 v[200:201],v17 offset:12288
ds_read_b64_tr_b16 v[202:203],v17 offset:13824
ds_read_b64_tr_b16 v[204:205],v17 offset:15360
ds_read_b64_tr_b16 v[206:207],v17 offset:16896
ds_read_b64_tr_b16 v[208:209],v17 offset:12352
ds_read_b64_tr_b16 v[210:211],v17 offset:13888
ds_read_b64_tr_b16 v[212:213],v17 offset:15424
ds_read_b64_tr_b16 v[214:215],v17 offset:16960
ds_read_b64_tr_b16 v[216:217],v17 offset:12416
ds_read_b64_tr_b16 v[218:219],v17 offset:13952
ds_read_b64_tr_b16 v[220:221],v17 offset:15488
ds_read_b64_tr_b16 v[222:223],v17 offset:17024
s_mov_b64 exec,s[62:63]
s_add_u32 m0,s68,58752
s_add_u32 s64,s59,1671168
buffer_load_dwordx4 v27,s[36:39],s64 offen lds
s_mov_b64 exec,-1
s_setprio 0
v_mul_f32 v176,v144,v176
v_mul_f32 v177,v145,v177
v_mul_f32 v178,v146,v178
v_mul_f32 v179,v147,v179
v_mul_f32 v180,v148,v180
v_mul_f32 v181,v149,v181
v_mul_f32 v182,v150,v182
v_mul_f32 v183,v151,v183
v_mul_f32 v184,v152,v184
v_mul_f32 v185,v153,v185
v_mul_f32 v186,v154,v186
v_mul_f32 v187,v155,v187
v_mul_f32 v188,v156,v188
v_mul_f32 v189,v157,v189
v_mul_f32 v190,v158,v190
v_mul_f32 v191,v159,v191
v_cvt_pk_bf16_f32 v192,v176,v177
v_cvt_pk_bf16_f32 v193,v178,v179
v_cvt_pk_bf16_f32 v194,v180,v181
v_cvt_pk_bf16_f32 v195,v182,v183
v_cvt_pk_bf16_f32 v196,v184,v185
v_cvt_pk_bf16_f32 v197,v186,v187
v_cvt_pk_bf16_f32 v198,v188,v189
v_cvt_pk_bf16_f32 v199,v190,v191
s_waitcnt lgkmcnt(0)
s_barrier
v_fmaak_f32 v176,v160,v160,0x409d30fc
v_fmaak_f32 v177,v161,v161,0x409d30fc
v_fmaak_f32 v178,v162,v162,0x409d30fc
v_fmaak_f32 v179,v163,v163,0x409d30fc
v_fmaak_f32 v180,v164,v164,0x409d30fc
v_fmaak_f32 v181,v165,v165,0x409d30fc
v_fmaak_f32 v182,v166,v166,0x409d30fc
v_fmaak_f32 v183,v167,v167,0x409d30fc
v_fmaak_f32 v184,v168,v168,0x409d30fc
v_fmaak_f32 v185,v169,v169,0x409d30fc
v_fmaak_f32 v186,v170,v170,0x409d30fc
v_fmaak_f32 v187,v171,v171,0x409d30fc
v_fmaak_f32 v188,v172,v172,0x409d30fc
v_fmaak_f32 v189,v173,v173,0x409d30fc
v_fmaak_f32 v190,v174,v174,0x409d30fc
v_fmaak_f32 v191,v175,v175,0x409d30fc
v_mul_f32 v176,v160,v176
v_mul_f32 v177,v161,v177
v_mul_f32 v178,v162,v178
v_mul_f32 v179,v163,v179
v_mul_f32 v180,v164,v180
v_mul_f32 v181,v165,v181
v_mul_f32 v182,v166,v182
v_mul_f32 v183,v167,v183
v_mul_f32 v184,v168,v184
v_mul_f32 v185,v169,v185
v_mul_f32 v186,v170,v186
v_mul_f32 v187,v171,v187
v_mul_f32 v188,v172,v188
v_mul_f32 v189,v173,v189
v_mul_f32 v190,v174,v190
v_mul_f32 v191,v175,v191
s_setprio 1
v_mfma_f32_32x32x16_bf16 v[96:111],v[200:203],v[192:195],v[96:111]
v_exp_f32 v176,v176
v_exp_f32 v177,v177
v_exp_f32 v178,v178
v_mfma_f32_32x32x16_bf16 v[96:111],v[204:207],v[196:199],v[96:111]
v_exp_f32 v179,v179
v_exp_f32 v180,v180
v_exp_f32 v181,v181
v_mfma_f32_32x32x16_bf16 v[112:127],v[208:211],v[192:195],v[112:127]
v_exp_f32 v182,v182
v_exp_f32 v183,v183
v_exp_f32 v184,v184
v_mfma_f32_32x32x16_bf16 v[112:127],v[212:215],v[196:199],v[112:127]
v_exp_f32 v185,v185
v_exp_f32 v186,v186
v_exp_f32 v187,v187
v_mfma_f32_32x32x16_bf16 v[128:143],v[216:219],v[192:195],v[128:143]
v_exp_f32 v188,v188
v_exp_f32 v189,v189
v_mfma_f32_32x32x16_bf16 v[128:143],v[220:223],v[196:199],v[128:143]
v_exp_f32 v190,v190
v_exp_f32 v191,v191
ds_read_b64_tr_b16 v[200:201],v16 offset:6144
ds_read_b64_tr_b16 v[202:203],v16 offset:6656
ds_read_b64_tr_b16 v[204:205],v16 offset:7168
ds_read_b64_tr_b16 v[206:207],v16 offset:7680
ds_read_b64_tr_b16 v[208:209],v16 offset:8192
ds_read_b64_tr_b16 v[210:211],v16 offset:8704
ds_read_b64_tr_b16 v[212:213],v16 offset:9216
ds_read_b64_tr_b16 v[214:215],v16 offset:9728
ds_read_b64_tr_b16 v[216:217],v16 offset:10240
ds_read_b64_tr_b16 v[218:219],v16 offset:10752
ds_read_b64_tr_b16 v[220:221],v16 offset:11264
ds_read_b64_tr_b16 v[222:223],v16 offset:11776
ds_read_b128 v[144:147],v18 offset:1280
ds_read_b128 v[148:151],v18 offset:1312
ds_read_b128 v[152:155],v18 offset:1344
ds_read_b128 v[156:159],v18 offset:1376
s_setprio 0
v_add_f32 v176,1.0,v176
v_add_f32 v177,1.0,v177
v_add_f32 v178,1.0,v178
v_add_f32 v179,1.0,v179
v_add_f32 v180,1.0,v180
v_add_f32 v181,1.0,v181
v_add_f32 v182,1.0,v182
v_add_f32 v183,1.0,v183
v_add_f32 v184,1.0,v184
v_add_f32 v185,1.0,v185
v_add_f32 v186,1.0,v186
v_add_f32 v187,1.0,v187
v_add_f32 v188,1.0,v188
v_add_f32 v189,1.0,v189
v_add_f32 v190,1.0,v190
v_add_f32 v191,1.0,v191
s_setprio 1
s_waitcnt lgkmcnt(0)
v_mfma_f32_32x32x16_bf16 v[144:159],v[200:203],v[72:75],v[144:159]
v_rcp_f32 v176,v176
v_rcp_f32 v177,v177
v_rcp_f32 v178,v178
v_mfma_f32_32x32x16_bf16 v[144:159],v[204:207],v[76:79],v[144:159]
v_rcp_f32 v179,v179
v_rcp_f32 v180,v180
v_rcp_f32 v181,v181
v_mfma_f32_32x32x16_bf16 v[144:159],v[208:211],v[80:83],v[144:159]
v_rcp_f32 v182,v182
v_rcp_f32 v183,v183
v_rcp_f32 v184,v184
v_mfma_f32_32x32x16_bf16 v[144:159],v[212:215],v[84:87],v[144:159]
v_rcp_f32 v185,v185
v_rcp_f32 v186,v186
v_rcp_f32 v187,v187
v_mfma_f32_32x32x16_bf16 v[144:159],v[216:219],v[88:91],v[144:159]
v_rcp_f32 v188,v188
v_rcp_f32 v189,v189
v_mfma_f32_32x32x16_bf16 v[144:159],v[220:223],v[92:95],v[144:159]
v_rcp_f32 v190,v190
v_rcp_f32 v191,v191
ds_read_b64_tr_b16 v[200:201],v17 offset:0
ds_read_b64_tr_b16 v[202:203],v17 offset:1536
ds_read_b64_tr_b16 v[204:205],v17 offset:3072
ds_read_b64_tr_b16 v[206:207],v17 offset:4608
ds_read_b64_tr_b16 v[208:209],v17 offset:64
ds_read_b64_tr_b16 v[210:211],v17 offset:1600
ds_read_b64_tr_b16 v[212:213],v17 offset:3136
ds_read_b64_tr_b16 v[214:215],v17 offset:4672
ds_read_b64_tr_b16 v[216:217],v17 offset:128
ds_read_b64_tr_b16 v[218:219],v17 offset:1664
ds_read_b64_tr_b16 v[220:221],v17 offset:3200
ds_read_b64_tr_b16 v[222:223],v17 offset:4736
s_setprio 0
v_mul_f32 v176,v160,v176
v_mul_f32 v177,v161,v177
v_mul_f32 v178,v162,v178
v_mul_f32 v179,v163,v179
v_mul_f32 v180,v164,v180
v_mul_f32 v181,v165,v181
v_mul_f32 v182,v166,v182
v_mul_f32 v183,v167,v183
v_mul_f32 v184,v168,v184
v_mul_f32 v185,v169,v185
v_mul_f32 v186,v170,v186
v_mul_f32 v187,v171,v187
v_mul_f32 v188,v172,v188
v_mul_f32 v189,v173,v189
v_mul_f32 v190,v174,v190
v_mul_f32 v191,v175,v191
v_cvt_pk_bf16_f32 v192,v176,v177
v_cvt_pk_bf16_f32 v193,v178,v179
v_cvt_pk_bf16_f32 v194,v180,v181
v_cvt_pk_bf16_f32 v195,v182,v183
v_cvt_pk_bf16_f32 v196,v184,v185
v_cvt_pk_bf16_f32 v197,v186,v187
v_cvt_pk_bf16_f32 v198,v188,v189
v_cvt_pk_bf16_f32 v199,v190,v191
s_waitcnt lgkmcnt(0)
s_barrier
v_fmaak_f32 v176,v144,v144,0x409d30fc
v_fmaak_f32 v177,v145,v145,0x409d30fc
v_fmaak_f32 v178,v146,v146,0x409d30fc
v_fmaak_f32 v179,v147,v147,0x409d30fc
v_fmaak_f32 v180,v148,v148,0x409d30fc
v_fmaak_f32 v181,v149,v149,0x409d30fc
v_fmaak_f32 v182,v150,v150,0x409d30fc
v_fmaak_f32 v183,v151,v151,0x409d30fc
v_fmaak_f32 v184,v152,v152,0x409d30fc
v_fmaak_f32 v185,v153,v153,0x409d30fc
v_fmaak_f32 v186,v154,v154,0x409d30fc
v_fmaak_f32 v187,v155,v155,0x409d30fc
v_fmaak_f32 v188,v156,v156,0x409d30fc
v_fmaak_f32 v189,v157,v157,0x409d30fc
v_fmaak_f32 v190,v158,v158,0x409d30fc
v_fmaak_f32 v191,v159,v159,0x409d30fc
v_mul_f32 v176,v144,v176
v_mul_f32 v177,v145,v177
v_mul_f32 v178,v146,v178
v_mul_f32 v179,v147,v179
v_mul_f32 v180,v148,v180
v_mul_f32 v181,v149,v181
v_mul_f32 v182,v150,v182
v_mul_f32 v183,v151,v183
v_mul_f32 v184,v152,v184
v_mul_f32 v185,v153,v185
v_mul_f32 v186,v154,v186
v_mul_f32 v187,v155,v187
v_mul_f32 v188,v156,v188
v_mul_f32 v189,v157,v189
v_mul_f32 v190,v158,v190
v_mul_f32 v191,v159,v191
s_setprio 1
v_mfma_f32_32x32x16_bf16 v[96:111],v[200:203],v[192:195],v[96:111]
v_exp_f32 v176,v176
v_exp_f32 v177,v177
v_exp_f32 v178,v178
v_mfma_f32_32x32x16_bf16 v[96:111],v[204:207],v[196:199],v[96:111]
v_exp_f32 v179,v179
v_exp_f32 v180,v180
v_exp_f32 v181,v181
v_mfma_f32_32x32x16_bf16 v[112:127],v[208:211],v[192:195],v[112:127]
v_exp_f32 v182,v182
v_exp_f32 v183,v183
v_exp_f32 v184,v184
v_mfma_f32_32x32x16_bf16 v[112:127],v[212:215],v[196:199],v[112:127]
v_exp_f32 v185,v185
v_exp_f32 v186,v186
v_exp_f32 v187,v187
v_mfma_f32_32x32x16_bf16 v[128:143],v[216:219],v[192:195],v[128:143]
v_exp_f32 v188,v188
v_exp_f32 v189,v189
v_mfma_f32_32x32x16_bf16 v[128:143],v[220:223],v[196:199],v[128:143]
v_exp_f32 v190,v190
v_exp_f32 v191,v191
ds_read_b64_tr_b16 v[200:201],v16 offset:12288
ds_read_b64_tr_b16 v[202:203],v16 offset:12800
ds_read_b64_tr_b16 v[204:205],v16 offset:13312
ds_read_b64_tr_b16 v[206:207],v16 offset:13824
ds_read_b64_tr_b16 v[208:209],v16 offset:14336
ds_read_b64_tr_b16 v[210:211],v16 offset:14848
ds_read_b64_tr_b16 v[212:213],v16 offset:15360
ds_read_b64_tr_b16 v[214:215],v16 offset:15872
ds_read_b64_tr_b16 v[216:217],v16 offset:16384
ds_read_b64_tr_b16 v[218:219],v16 offset:16896
ds_read_b64_tr_b16 v[220:221],v16 offset:17408
ds_read_b64_tr_b16 v[222:223],v16 offset:17920
ds_read_b128 v[160:163],v18 offset:1408
ds_read_b128 v[164:167],v18 offset:1440
ds_read_b128 v[168:171],v18 offset:1472
ds_read_b128 v[172:175],v18 offset:1504
s_setprio 0
v_add_f32 v176,1.0,v176
v_add_f32 v177,1.0,v177
v_add_f32 v178,1.0,v178
v_add_f32 v179,1.0,v179
v_add_f32 v180,1.0,v180
v_add_f32 v181,1.0,v181
v_add_f32 v182,1.0,v182
v_add_f32 v183,1.0,v183
v_add_f32 v184,1.0,v184
v_add_f32 v185,1.0,v185
v_add_f32 v186,1.0,v186
v_add_f32 v187,1.0,v187
v_add_f32 v188,1.0,v188
v_add_f32 v189,1.0,v189
v_add_f32 v190,1.0,v190
v_add_f32 v191,1.0,v191
s_setprio 1
s_waitcnt lgkmcnt(0)
v_mfma_f32_32x32x16_bf16 v[160:175],v[200:203],v[72:75],v[160:175]
v_rcp_f32 v176,v176
v_rcp_f32 v177,v177
v_rcp_f32 v178,v178
v_mfma_f32_32x32x16_bf16 v[160:175],v[204:207],v[76:79],v[160:175]
v_rcp_f32 v179,v179
v_rcp_f32 v180,v180
v_rcp_f32 v181,v181
v_mfma_f32_32x32x16_bf16 v[160:175],v[208:211],v[80:83],v[160:175]
v_rcp_f32 v182,v182
v_rcp_f32 v183,v183
v_rcp_f32 v184,v184
v_mfma_f32_32x32x16_bf16 v[160:175],v[212:215],v[84:87],v[160:175]
v_rcp_f32 v185,v185
v_rcp_f32 v186,v186
v_rcp_f32 v187,v187
v_mfma_f32_32x32x16_bf16 v[160:175],v[216:219],v[88:91],v[160:175]
v_rcp_f32 v188,v188
v_rcp_f32 v189,v189
v_mfma_f32_32x32x16_bf16 v[160:175],v[220:223],v[92:95],v[160:175]
v_rcp_f32 v190,v190
v_rcp_f32 v191,v191
ds_read_b64_tr_b16 v[200:201],v17 offset:6144
ds_read_b64_tr_b16 v[202:203],v17 offset:7680
ds_read_b64_tr_b16 v[204:205],v17 offset:9216
ds_read_b64_tr_b16 v[206:207],v17 offset:10752
ds_read_b64_tr_b16 v[208:209],v17 offset:6208
ds_read_b64_tr_b16 v[210:211],v17 offset:7744
ds_read_b64_tr_b16 v[212:213],v17 offset:9280
ds_read_b64_tr_b16 v[214:215],v17 offset:10816
ds_read_b64_tr_b16 v[216:217],v17 offset:6272
ds_read_b64_tr_b16 v[218:219],v17 offset:7808
ds_read_b64_tr_b16 v[220:221],v17 offset:9344
ds_read_b64_tr_b16 v[222:223],v17 offset:10880
s_setprio 0
v_mul_f32 v176,v144,v176
v_mul_f32 v177,v145,v177
v_mul_f32 v178,v146,v178
v_mul_f32 v179,v147,v179
v_mul_f32 v180,v148,v180
v_mul_f32 v181,v149,v181
v_mul_f32 v182,v150,v182
v_mul_f32 v183,v151,v183
v_mul_f32 v184,v152,v184
v_mul_f32 v185,v153,v185
v_mul_f32 v186,v154,v186
v_mul_f32 v187,v155,v187
v_mul_f32 v188,v156,v188
v_mul_f32 v189,v157,v189
v_mul_f32 v190,v158,v190
v_mul_f32 v191,v159,v191
v_cvt_pk_bf16_f32 v192,v176,v177
v_cvt_pk_bf16_f32 v193,v178,v179
v_cvt_pk_bf16_f32 v194,v180,v181
v_cvt_pk_bf16_f32 v195,v182,v183
v_cvt_pk_bf16_f32 v196,v184,v185
v_cvt_pk_bf16_f32 v197,v186,v187
v_cvt_pk_bf16_f32 v198,v188,v189
v_cvt_pk_bf16_f32 v199,v190,v191
s_waitcnt lgkmcnt(0)
s_barrier
v_fmaak_f32 v176,v160,v160,0x409d30fc
v_fmaak_f32 v177,v161,v161,0x409d30fc
v_fmaak_f32 v178,v162,v162,0x409d30fc
v_fmaak_f32 v179,v163,v163,0x409d30fc
v_fmaak_f32 v180,v164,v164,0x409d30fc
v_fmaak_f32 v181,v165,v165,0x409d30fc
v_fmaak_f32 v182,v166,v166,0x409d30fc
v_fmaak_f32 v183,v167,v167,0x409d30fc
v_fmaak_f32 v184,v168,v168,0x409d30fc
v_fmaak_f32 v185,v169,v169,0x409d30fc
v_fmaak_f32 v186,v170,v170,0x409d30fc
v_fmaak_f32 v187,v171,v171,0x409d30fc
v_fmaak_f32 v188,v172,v172,0x409d30fc
v_fmaak_f32 v189,v173,v173,0x409d30fc
v_fmaak_f32 v190,v174,v174,0x409d30fc
v_fmaak_f32 v191,v175,v175,0x409d30fc
v_mul_f32 v176,v160,v176
v_mul_f32 v177,v161,v177
v_mul_f32 v178,v162,v178
v_mul_f32 v179,v163,v179
v_mul_f32 v180,v164,v180
v_mul_f32 v181,v165,v181
v_mul_f32 v182,v166,v182
v_mul_f32 v183,v167,v183
v_mul_f32 v184,v168,v184
v_mul_f32 v185,v169,v185
v_mul_f32 v186,v170,v186
v_mul_f32 v187,v171,v187
v_mul_f32 v188,v172,v188
v_mul_f32 v189,v173,v189
v_mul_f32 v190,v174,v190
v_mul_f32 v191,v175,v191
s_setprio 1
v_mfma_f32_32x32x16_bf16 v[96:111],v[200:203],v[192:195],v[96:111]
v_exp_f32 v176,v176
v_exp_f32 v177,v177
v_exp_f32 v178,v178
v_mfma_f32_32x32x16_bf16 v[96:111],v[204:207],v[196:199],v[96:111]
v_exp_f32 v179,v179
v_exp_f32 v180,v180
v_exp_f32 v181,v181
v_mfma_f32_32x32x16_bf16 v[112:127],v[208:211],v[192:195],v[112:127]
v_exp_f32 v182,v182
v_exp_f32 v183,v183
v_exp_f32 v184,v184
v_mfma_f32_32x32x16_bf16 v[112:127],v[212:215],v[196:199],v[112:127]
v_exp_f32 v185,v185
v_exp_f32 v186,v186
v_exp_f32 v187,v187
v_mfma_f32_32x32x16_bf16 v[128:143],v[216:219],v[192:195],v[128:143]
v_exp_f32 v188,v188
v_exp_f32 v189,v189
v_mfma_f32_32x32x16_bf16 v[128:143],v[220:223],v[196:199],v[128:143]
v_exp_f32 v190,v190
v_exp_f32 v191,v191
ds_read_b64 v[72:73],v20 offset:0
ds_read_b64 v[74:75],v21 offset:0
ds_read_b64 v[76:77],v22 offset:0
ds_read_b64 v[78:79],v23 offset:0
ds_read_b64 v[80:81],v20 offset:64
ds_read_b64 v[82:83],v21 offset:64
ds_read_b64 v[84:85],v22 offset:64
ds_read_b64 v[86:87],v23 offset:64
ds_read_b64 v[88:89],v20 offset:128
ds_read_b64 v[90:91],v21 offset:128
ds_read_b64 v[92:93],v22 offset:128
ds_read_b64 v[94:95],v23 offset:128
s_setprio 0
v_add_f32 v176,1.0,v176
v_add_f32 v177,1.0,v177
v_add_f32 v178,1.0,v178
v_add_f32 v179,1.0,v179
v_add_f32 v180,1.0,v180
v_add_f32 v181,1.0,v181
v_add_f32 v182,1.0,v182
v_add_f32 v183,1.0,v183
v_add_f32 v184,1.0,v184
v_add_f32 v185,1.0,v185
v_add_f32 v186,1.0,v186
v_add_f32 v187,1.0,v187
v_add_f32 v188,1.0,v188
v_add_f32 v189,1.0,v189
v_add_f32 v190,1.0,v190
v_add_f32 v191,1.0,v191
s_setprio 1
s_waitcnt lgkmcnt(0)
v_rcp_f32 v176,v176
v_rcp_f32 v177,v177
v_rcp_f32 v178,v178
v_rcp_f32 v179,v179
v_rcp_f32 v180,v180
v_rcp_f32 v181,v181
v_rcp_f32 v182,v182
v_rcp_f32 v183,v183
v_rcp_f32 v184,v184
v_rcp_f32 v185,v185
v_rcp_f32 v186,v186
v_rcp_f32 v187,v187
v_rcp_f32 v188,v188
v_rcp_f32 v189,v189
v_rcp_f32 v190,v190
v_rcp_f32 v191,v191
ds_read_b64_tr_b16 v[200:201],v17 offset:12288
ds_read_b64_tr_b16 v[202:203],v17 offset:13824
ds_read_b64_tr_b16 v[204:205],v17 offset:15360
ds_read_b64_tr_b16 v[206:207],v17 offset:16896
ds_read_b64_tr_b16 v[208:209],v17 offset:12352
ds_read_b64_tr_b16 v[210:211],v17 offset:13888
ds_read_b64_tr_b16 v[212:213],v17 offset:15424
ds_read_b64_tr_b16 v[214:215],v17 offset:16960
ds_read_b64_tr_b16 v[216:217],v17 offset:12416
ds_read_b64_tr_b16 v[218:219],v17 offset:13952
ds_read_b64_tr_b16 v[220:221],v17 offset:15488
ds_read_b64_tr_b16 v[222:223],v17 offset:17024
s_setprio 0
v_mul_f32 v176,v160,v176
v_mul_f32 v177,v161,v177
v_mul_f32 v178,v162,v178
v_mul_f32 v179,v163,v179
v_mul_f32 v180,v164,v180
v_mul_f32 v181,v165,v181
v_mul_f32 v182,v166,v182
v_mul_f32 v183,v167,v183
v_mul_f32 v184,v168,v184
v_mul_f32 v185,v169,v185
v_mul_f32 v186,v170,v186
v_mul_f32 v187,v171,v187
v_mul_f32 v188,v172,v188
v_mul_f32 v189,v173,v189
v_mul_f32 v190,v174,v190
v_mul_f32 v191,v175,v191
v_cvt_pk_bf16_f32 v192,v176,v177
v_cvt_pk_bf16_f32 v193,v178,v179
v_cvt_pk_bf16_f32 v194,v180,v181
v_cvt_pk_bf16_f32 v195,v182,v183
v_cvt_pk_bf16_f32 v196,v184,v185
v_cvt_pk_bf16_f32 v197,v186,v187
v_cvt_pk_bf16_f32 v198,v188,v189
v_cvt_pk_bf16_f32 v199,v190,v191
ds_read_b128 v[144:147],v18 offset:1536
ds_read_b128 v[148:151],v18 offset:1568
ds_read_b128 v[152:155],v18 offset:1600
ds_read_b128 v[156:159],v18 offset:1632
ds_read_b128 v[160:163],v18 offset:1664
ds_read_b128 v[164:167],v18 offset:1696
ds_read_b128 v[168:171],v18 offset:1728
ds_read_b128 v[172:175],v18 offset:1760
ds_read_b128 v[176:179],v18 offset:1792
ds_read_b128 v[180:183],v18 offset:1824
ds_read_b128 v[184:187],v18 offset:1856
ds_read_b128 v[188:191],v18 offset:1888
s_waitcnt lgkmcnt(12)
s_setprio 1
v_mfma_f32_32x32x16_bf16 v[96:111],v[200:203],v[192:195],v[96:111]
v_lshlrev_b32 v232,16,v72
v_lshlrev_b32 v233,16,v73
v_lshlrev_b32 v234,16,v74
v_lshlrev_b32 v235,16,v75
v_mfma_f32_32x32x16_bf16 v[96:111],v[204:207],v[196:199],v[96:111]
v_lshlrev_b32 v236,16,v76
v_lshlrev_b32 v237,16,v77
v_lshlrev_b32 v238,16,v78
v_lshlrev_b32 v239,16,v79
v_mfma_f32_32x32x16_bf16 v[112:127],v[208:211],v[192:195],v[112:127]
v_lshlrev_b32 v240,16,v80
v_lshlrev_b32 v241,16,v81
v_lshlrev_b32 v242,16,v82
v_lshlrev_b32 v243,16,v83
v_mfma_f32_32x32x16_bf16 v[112:127],v[212:215],v[196:199],v[112:127]
v_lshlrev_b32 v244,16,v84
v_lshlrev_b32 v245,16,v85
v_lshlrev_b32 v246,16,v86
v_lshlrev_b32 v247,16,v87
v_mfma_f32_32x32x16_bf16 v[128:143],v[216:219],v[192:195],v[128:143]
v_lshlrev_b32 v248,16,v88
v_lshlrev_b32 v249,16,v89
v_lshlrev_b32 v250,16,v90
v_lshlrev_b32 v251,16,v91
v_mfma_f32_32x32x16_bf16 v[128:143],v[220:223],v[196:199],v[128:143]
v_lshlrev_b32 v252,16,v92
v_lshlrev_b32 v253,16,v93
v_lshlrev_b32 v254,16,v94
v_lshlrev_b32 v255,16,v95
s_setprio 0
s_waitcnt lgkmcnt(0)
v_and_b32 v72,0xffff0000,v72
v_and_b32 v73,0xffff0000,v73
v_and_b32 v74,0xffff0000,v74
v_and_b32 v75,0xffff0000,v75
v_and_b32 v76,0xffff0000,v76
v_and_b32 v77,0xffff0000,v77
v_and_b32 v78,0xffff0000,v78
v_and_b32 v79,0xffff0000,v79
v_and_b32 v80,0xffff0000,v80
v_and_b32 v81,0xffff0000,v81
v_and_b32 v82,0xffff0000,v82
v_and_b32 v83,0xffff0000,v83
v_and_b32 v84,0xffff0000,v84
v_and_b32 v85,0xffff0000,v85
v_and_b32 v86,0xffff0000,v86
v_and_b32 v87,0xffff0000,v87
v_and_b32 v88,0xffff0000,v88
v_and_b32 v89,0xffff0000,v89
v_and_b32 v90,0xffff0000,v90
v_and_b32 v91,0xffff0000,v91
v_and_b32 v92,0xffff0000,v92
v_and_b32 v93,0xffff0000,v93
v_and_b32 v94,0xffff0000,v94
v_and_b32 v95,0xffff0000,v95
v_fma_f32 v232,v96,v144,v232
v_fma_f32 v72,v97,v145,v72
v_fma_f32 v233,v98,v146,v233
v_fma_f32 v73,v99,v147,v73
v_fma_f32 v234,v100,v148,v234
v_fma_f32 v74,v101,v149,v74
v_fma_f32 v235,v102,v150,v235
v_fma_f32 v75,v103,v151,v75
v_fma_f32 v236,v104,v152,v236
v_fma_f32 v76,v105,v153,v76
v_fma_f32 v237,v106,v154,v237
v_fma_f32 v77,v107,v155,v77
v_fma_f32 v238,v108,v156,v238
v_fma_f32 v78,v109,v157,v78
v_fma_f32 v239,v110,v158,v239
v_fma_f32 v79,v111,v159,v79
v_fma_f32 v240,v112,v160,v240
v_fma_f32 v80,v113,v161,v80
v_fma_f32 v241,v114,v162,v241
v_fma_f32 v81,v115,v163,v81
v_fma_f32 v242,v116,v164,v242
v_fma_f32 v82,v117,v165,v82
v_fma_f32 v243,v118,v166,v243
v_fma_f32 v83,v119,v167,v83
v_fma_f32 v244,v120,v168,v244
v_fma_f32 v84,v121,v169,v84
v_fma_f32 v245,v122,v170,v245
v_fma_f32 v85,v123,v171,v85
v_fma_f32 v246,v124,v172,v246
v_fma_f32 v86,v125,v173,v86
v_fma_f32 v247,v126,v174,v247
v_fma_f32 v87,v127,v175,v87
v_fma_f32 v248,v128,v176,v248
v_fma_f32 v88,v129,v177,v88
v_fma_f32 v249,v130,v178,v249
v_fma_f32 v89,v131,v179,v89
v_fma_f32 v250,v132,v180,v250
v_fma_f32 v90,v133,v181,v90
v_fma_f32 v251,v134,v182,v251
v_fma_f32 v91,v135,v183,v91
v_fma_f32 v252,v136,v184,v252
v_fma_f32 v92,v137,v185,v92
v_fma_f32 v253,v138,v186,v253
v_fma_f32 v93,v139,v187,v93
v_fma_f32 v254,v140,v188,v254
v_fma_f32 v94,v141,v189,v94
v_fma_f32 v255,v142,v190,v255
v_fma_f32 v95,v143,v191,v95
v_add_u32 v4,s54,v4
v_add_u32 v5,s54,v5
v_add_u32 v6,s54,v6
v_add_u32 v7,s54,v7
v_add_u32 v8,s54,v8
v_add_u32 v9,s54,v9
v_add_u32 v10,s54,v10
v_add_u32 v11,s54,v11
v_add_u32 v12,s54,v12
v_add_u32 v13,s54,v13
v_add_u32 v14,s54,v14
v_add_u32 v15,s54,v15
v_cvt_pk_bf16_f32 v200,v232,v72
v_cvt_pk_bf16_f32 v201,v233,v73
v_cvt_pk_bf16_f32 v202,v234,v74
v_cvt_pk_bf16_f32 v203,v235,v75
v_cvt_pk_bf16_f32 v204,v236,v76
v_cvt_pk_bf16_f32 v205,v237,v77
v_cvt_pk_bf16_f32 v206,v238,v78
v_cvt_pk_bf16_f32 v207,v239,v79
v_cvt_pk_bf16_f32 v208,v240,v80
v_cvt_pk_bf16_f32 v209,v241,v81
v_cvt_pk_bf16_f32 v210,v242,v82
v_cvt_pk_bf16_f32 v211,v243,v83
v_cvt_pk_bf16_f32 v212,v244,v84
v_cvt_pk_bf16_f32 v213,v245,v85
v_cvt_pk_bf16_f32 v214,v246,v86
v_cvt_pk_bf16_f32 v215,v247,v87
v_cvt_pk_bf16_f32 v216,v248,v88
v_cvt_pk_bf16_f32 v217,v249,v89
v_cvt_pk_bf16_f32 v218,v250,v90
v_cvt_pk_bf16_f32 v219,v251,v91
v_cvt_pk_bf16_f32 v220,v252,v92
v_cvt_pk_bf16_f32 v221,v253,v93
v_cvt_pk_bf16_f32 v222,v254,v94
v_cvt_pk_bf16_f32 v223,v255,v95
ds_write_b64 v20,v[200:201] offset:0
ds_write_b64 v21,v[202:203] offset:0
ds_write_b64 v22,v[204:205] offset:0
ds_write_b64 v23,v[206:207] offset:0
ds_write_b64 v20,v[208:209] offset:64
ds_write_b64 v21,v[210:211] offset:64
ds_write_b64 v22,v[212:213] offset:64
ds_write_b64 v23,v[214:215] offset:64
ds_write_b64 v20,v[216:217] offset:128
ds_write_b64 v21,v[218:219] offset:128
ds_write_b64 v22,v[220:221] offset:128
ds_write_b64 v23,v[222:223] offset:128
ds_read_b128 v[72:75],v19 offset:0
ds_read_b128 v[76:79],v19 offset:1024
ds_read_b128 v[80:83],v19 offset:2048
ds_read_b128 v[84:87],v19 offset:3456
ds_read_b128 v[88:91],v19 offset:4480
ds_read_b128 v[92:95],v19 offset:5504
v_add_u32 v20,s54,v20
v_add_u32 v21,s54,v21
v_add_u32 v22,s54,v22
v_add_u32 v23,s54,v23
s_sub_u32 s68,s68,s54
s_add_u32 s59,s59,1572864
s_add_u32 s53,s53,1
s_cmp_eq_u32 s53,3
s_cselect_b32 s59,0x70000000,s59
s_add_u32 s67,s69,98304
s_waitcnt lgkmcnt(5)
buffer_store_dwordx4 v[72:75],v24,s[40:43],s69 offen nt
s_waitcnt lgkmcnt(4)
buffer_store_dwordx4 v[76:79],v25,s[40:43],s69 offen nt
s_waitcnt lgkmcnt(3)
buffer_store_dwordx4 v[80:83],v26,s[40:43],s69 offen nt
s_waitcnt lgkmcnt(2)
buffer_store_dwordx4 v[84:87],v24,s[40:43],s67 offen nt
s_waitcnt lgkmcnt(1)
buffer_store_dwordx4 v[88:91],v25,s[40:43],s67 offen nt
s_waitcnt lgkmcnt(0)
buffer_store_dwordx4 v[92:95],v26,s[40:43],s67 offen nt
v_add_u32 v19,s54,v19
s_add_u32 s69,s69,1572864
s_sub_u32 s54,0,s54
s_cmp_lt_u32 s53,4
s_cbranch_scc1 L_pass1_%=
L_end_%=:
s_waitcnt vmcnt(0) lgkmcnt(0)
s_endpgm
.p2align 12
)ASM"
    :: "s"(blockIdx.x), "v"(tid), "s"((unsigned long long)__builtin_amdgcn_kernarg_segment_ptr())
    : "v1", "v2", "v3", "v4", "v5", "v6", "v7", "v8", "v9", "v10", "v11", "v12", "v13", "v14", "v15", "v16", "v17", "v18", "v19", "v20", "v21", "v22", "v23", "v24", "v25", "v26", "v27", "v28", "v29", "v30", "v31", "v32", "v33", "v34", "v35", "v36", "v37", "v38", "v39", "v40", "v41", "v42", "v43", "v44", "v45", "v46", "v47", "v48", "v49", "v50", "v51", "v52", "v53", "v54", "v55", "v56", "v57", "v58", "v59", "v60", "v61", "v62", "v63", "v64", "v65", "v66", "v67", "v68", "v69", "v70", "v71", "v72", "v73", "v74", "v75", "v76", "v77", "v78", "v79", "v80", "v81", "v82", "v83", "v84", "v85", "v86", "v87", "v88", "v89", "v90", "v91", "v92", "v93", "v94", "v95", "v96", "v97", "v98", "v99", "v100", "v101", "v102", "v103", "v104", "v105", "v106", "v107", "v108", "v109", "v110", "v111", "v112", "v113", "v114", "v115", "v116", "v117", "v118", "v119", "v120", "v121", "v122", "v123", "v124", "v125", "v126", "v127", "v128", "v129", "v130", "v131", "v132", "v133", "v134", "v135", "v136", "v137", "v138", "v139", "v140", "v141", "v142", "v143", "v144", "v145", "v146", "v147", "v148", "v149", "v150", "v151", "v152", "v153", "v154", "v155", "v156", "v157", "v158", "v159", "v160", "v161", "v162", "v163", "v164", "v165", "v166", "v167", "v168", "v169", "v170", "v171", "v172", "v173", "v174", "v175", "v176", "v177", "v178", "v179", "v180", "v181", "v182", "v183", "v184", "v185", "v186", "v187", "v188", "v189", "v190", "v191", "v192", "v193", "v194", "v195", "v196", "v197", "v198", "v199", "v200", "v201", "v202", "v203", "v204", "v205", "v206", "v207", "v208", "v209", "v210", "v211", "v212", "v213", "v214", "v215", "v216", "v217", "v218", "v219", "v220", "v221", "v222", "v223", "v224", "v225", "v226", "v227", "v228", "v229", "v230", "v231", "v232", "v233", "v234", "v235", "v236", "v237", "v238", "v239", "v240", "v241", "v242", "v243", "v244", "v245", "v246", "v247", "v248", "v249", "v250", "v251", "v252", "v253", "v254", "v255", "s4", "s5", "s6", "s7", "s8", "s9", "s10", "s11", "s12", "s13", "s14", "s15", "s16", "s17", "s18", "s19", "s20", "s21", "s22", "s23", "s24", "s25", "s26", "s27", "s28", "s29", "s30", "s31", "s32", "s33", "s34", "s35", "s36", "s37", "s38", "s39", "s40", "s41", "s42", "s43", "s44", "s45", "s46", "s47", "s48", "s49", "s50", "s51", "s52", "s53", "s54", "s55", "s56", "s57", "s58", "s59", "s60", "s61", "s62", "s63", "s64", "s65", "s66", "s67", "s68", "s69", "s70", "s71", "s72", "s73", "s74", "s75", "s76", "s77", "s78", "s79", "s80", "s81", "s82", "s83", "s84", "s85", "s86", "s87", "s88", "s89", "s90", "s91", "s92", "s93", "s94", "s95", "s96", "s97", "s98", "s99", "s100", "s101", "vcc", "scc", "memory");
}

extern "C" void kernel_launch(void* const* d_in, const int* in_sizes, int n_in,
                              void* d_out, int out_size, void* d_ws, size_t ws_size,
                              hipStream_t stream) {
    (void)hipFuncSetAttribute((const void*)convnext_kernel, hipFuncAttributeMaxDynamicSharedMemorySize, LDS_BYTES);
    convnext_kernel<<<256, 512, LDS_BYTES, stream>>>(
        (const unsigned short*)d_in[0], (const unsigned short*)d_in[1], (const unsigned short*)d_in[2], (const unsigned short*)d_in[3],
        (const unsigned short*)d_in[4], (const unsigned short*)d_in[5], (const unsigned short*)d_in[6], (unsigned short*)d_out);
}
```

```cpp
#include <hip/hip_runtime.h>
#include <stdint.h>

constexpr int LDS_BYTES = 163584;

__global__ __launch_bounds__(512, 2) void convnext_kernel(
    const unsigned short* __restrict__ x, const unsigned short* __restrict__ dw, const unsigned short* __restrict__ w1,
    const unsigned short* __restrict__ b1, const unsigned short* __restrict__ w2, const unsigned short* __restrict__ b2,
    const unsigned short* __restrict__ gamma, unsigned short* __restrict__ out) {
    const unsigned tid = threadIdx.x;
    asm volatile(
R"ASM(
N_main_%=:
s_load_dwordx16 s[20:35],%2,0
v_lshrrev_b32 v236,6,%1
v_and_b32 v232,63,%1
v_readfirstlane_b32 s52,v236
s_getpc_b64 s[14:15]
N_ep_%=:
v_lshlrev_b32 v252,7,v232
v_lshrrev_b32 v253,8,%1
v_mul_u32_u24 v253,N_B_%=-N_ep_%=,v253
v_add_u32 v252,v252,v253
global_load_dword v254,v252,s[14:15]
v_mov_b32 v228,0
v_mov_b32 v229,0
v_mov_b32 v230,0
v_mov_b32 v231,0
s_and_b32 s64,%0,7
s_lshr_b32 s70,%0,3
s_lshl_b32 s71,s64,6
s_mul_i32 s68,s70,3072
s_and_b32 s64,s52,3
s_mul_i32 s67,s64,54
s_cmp_eq_u32 s70,0
s_cselect_b32 s66,0,99
s_cmp_eq_u32 s70,31
s_cselect_b32 s66,17,s66
v_add_u32 v236,s67,v232
s_mul_i32 s57,s64,864
s_add_u32 s57,s57,73728
v_mul_u32_u24 v237,0xaaab,v236
s_sub_u32 s65,s71,1
v_lshrrev_b32 v237,19,v237
s_mul_i32 s56,s65,98304
v_mul_u32_u24 v238,12,v237
s_add_u32 s56,s56,s68
v_sub_u32 v238,v236,v238
s_cmp_lt_i32 s65,0
s_cselect_b32 s101,0x70000000,s56
v_add_u32 v236,1,v237
s_mov_b32 s62,-1
s_mov_b32 s63,0x3fffff
v_bfe_u32 v236,v236,2,2
s_mov_b32 s38,50331840
s_mov_b32 s39,0x00020000
v_xor_b32 v238,v238,v236
s_mov_b32 s96,0x8befe000
s_mov_b32 s99,0x72800000
v_mul_u32_u24 v236,192,v237
s_mov_b32 s98,0xffff0000
s_bitcmp1_b32 s52,1
s_cselect_b64 vcc,-1,0
v_lshl_add_u32 v241,v238,4,v236
v_cmp_eq_u32_e64 s[92:93],s66,v237
v_mov_b32 v236,0x7fff0000
s_nop 0
v_cndmask_b32_e64 v241,v241,v236,s[92:93]
s_cmp_ge_u32 s52,4
s_cbranch_scc1 N_B_%=
s_setprio 3
s_waitcnt lgkmcnt(0)
s_sub_u32 s36,s20,192
s_subb_u32 s37,s21,0
s_and_b32 s37,s37,0xffff
s_bitcmp0_b32 s52,0
s_cselect_b64 s[44:45],s[24:25],s[28:29]
s_and_b32 s45,s45,0xffff
s_mov_b32 s40,s34
s_and_b32 s41,s35,0xffff
s_mov_b64 exec,s[62:63]
s_add_u32 m0,s57,0
s_nop 0
buffer_load_dwordx4 v241,s[36:39],s101 offen lds
s_add_u32 m0,s57,3488
s_add_u32 s101,s56,98304
buffer_load_dwordx4 v241,s[36:39],s101 offen lds
s_add_u32 m0,s57,6976
s_add_u32 s101,s56,196608
buffer_load_dwordx4 v241,s[36:39],s101 offen lds
s_add_u32 m0,s57,10464
s_add_u32 s101,s56,294912
buffer_load_dwordx4 v241,s[36:39],s101 offen lds
s_mov_b64 exec,-1
s_and_b32 s66,s52,3
s_cmp_eq_u32 s66,3
s_cselect_b32 s14,1,s66
v_min_u32 v236,47,v232
s_mul_i32 s64,s14,192
s_lshl_b32 s65,s14,7
v_lshl_add_u32 v200,v236,2,s64
s_add_u32 s65,s65,144256
v_and_b32 v199,255,%1
v_min_u32 v237,191,v199
v_lshlrev_b32 v201,2,v237
v_min_u32 v249,95,v199
v_lshlrev_b32 v202,1,v249
s_lshl_b32 s64,s66,3
s_lshr_b32 s67,0x30011100,s64
s_and_b32 s67,s67,0xff
s_lshl_b32 s67,s67,3
s_add_u32 s67,s67,143488
v_lshrrev_b32 v238,2,v236
v_and_b32 v239,3,v236
v_mul_u32_u24 v238,1152,v238
v_lshl_add_u32 v238,v239,5,v238
s_and_b32 s66,s66,1
s_lshl_b32 s64,s66,3
s_cmp_eq_u32 s66,0
s_movk_i32 s66,128
s_cselect_b32 s66,640,s66
v_add_u32 v203,s65,v238
v_add_u32 v209,s67,v238
v_lshl_add_u32 v204,v239,2,v203
v_add_u32 v215,s66,v209
v_xor_b32 v237,s64,v209
v_lshrrev_b32 v239,1,v239
v_xor_b32 v238,s64,v215
ds_write_b128 v203,v[228:231] offset:0
ds_write_b128 v203,v[228:231] offset:16
ds_write2_b64 v209,v[228:229],v[228:229] offset0:0 offset1:2
ds_write2_b64 v215,v[228:229],v[228:229] offset0:0 offset1:2
ds_write2_b64 v237,v[228:229],v[228:229] offset0:0 offset1:2
ds_write2_b64 v238,v[228:229],v[228:229] offset0:0 offset1:2
v_lshl_add_u32 v209,v239,2,v209
v_lshl_add_u32 v215,v239,2,v215
ds_write_b32 v228,v228 offset:11648
global_load_dword v242,v200,s[22:23] offset:0
global_load_dword v243,v200,s[22:23] offset:576
global_load_dword v244,v200,s[22:23] offset:1152
global_load_ushort v247,v202,s[32:33]
global_load_dword v245,v201,s[26:27]
global_load_ushort v246,v202,s[30:31]
s_mov_b64 exec,s[62:63]
s_add_u32 m0,s57,13952
s_add_u32 s101,s56,393216
buffer_load_dwordx4 v241,s[36:39],s101 offen lds
s_add_u32 m0,s57,17440
s_add_u32 s101,s56,491520
buffer_load_dwordx4 v241,s[36:39],s101 offen lds
s_add_u32 m0,s57,20928
s_add_u32 s101,s56,589824
buffer_load_dwordx4 v241,s[36:39],s101 offen lds
s_add_u32 m0,s57,24416
s_add_u32 s101,s56,688128
buffer_load_dwordx4 v241,s[36:39],s101 offen lds
s_add_u32 m0,s57,27904
s_add_u32 s101,s56,786432
buffer_load_dwordx4 v241,s[36:39],s101 offen lds
s_add_u32 m0,s57,31392
s_add_u32 s101,s56,884736
buffer_load_dwordx4 v241,s[36:39],s101 offen lds
s_mov_b64 exec,-1
s_and_b32 s64,s52,3
s_lshl_b32 s10,s64,2
s_add_u32 s64,s10,s71
s_mul_i32 s61,s64,98304
s_add_u32 s61,s61,s68
s_mov_b32 s54,0x44ea44ea
s_mov_b32 s55,0x877f877f
s_mov_b32 s8,0x0ff00ff0
s_mov_b32 s9,0x0ff00ff0
s_and_b32 s64,s52,3
s_mul_i32 s67,s64,54
s_cmp_eq_u32 s70,31
s_cselect_b32 s66,17,99
s_mov_b32 s12,0xaaaaaaaa
s_mov_b32 s13,0xaaaaaaaa
s_mov_b32 s42,50331648
s_mov_b32 s43,0x00020000
s_bitcmp0_b32 s52,0
s_mov_b32 s58,40896
s_cselect_b32 s58,16256,s58
s_cselect_b32 s59,64,6144
s_movk_i32 s53,1024
s_cselect_b32 s53,6144,s53
s_cselect_b32 s60,0,67584
s_cselect_b32 s97,64,0
s_cselect_b32 s69,0,0x70000000
s_mov_b32 s100,73728
s_cselect_b32 s100,110688,s100
s_cselect_b32 s64,16,0
s_movk_i32 s50,1032
s_cselect_b32 s50,1024,s50
s_cselect_b32 s51,s53,-72704
s_mov_b32 s46,73728
s_mov_b32 s47,0x00020000
v_mov_b32 v1,0
v_lshlrev_b32 v233,4,v232
v_lshlrev_b32 v234,3,%1
v_add_u32 v233,157312,v233
v_add_u32 v234,157312,v234
v_lshrrev_b32 v236,5,v249
v_bfe_u32 v237,v249,2,1
v_and_b32 v238,3,v249
v_lshlrev_b32 v206,7,v236
v_lshl_add_u32 v206,v237,5,v206
v_lshl_add_u32 v206,v238,2,v206
v_bfe_u32 v239,v249,3,1
v_lshl_add_u32 v206,v239,6,v206
v_bfe_u32 v239,v249,4,1
v_lshl_add_u32 v206,v239,4,v206
v_add_u32 v206,9728,v206
v_lshl_add_u32 v207,v236,1,v237
v_lshlrev_b32 v207,4,v207
v_bfe_u32 v239,v249,3,2
v_lshl_add_u32 v207,v239,2,v207
v_add_u32 v207,v207,v238
v_lshlrev_b32 v207,4,v207
v_add_u32 v207,10112,v207
v_lshlrev_b32 v208,4,v238
v_cmp_eq_u32_e64 s[48:49],1,v237
v_lshlrev_b32 v205,1,v201
v_add_u32 v205,8192,v205
v_mov_b32 v221,0
v_mov_b32 v222,0x3f800000
s_waitcnt vmcnt(6)
v_and_b32 v212,0xffff,v242
v_and_b32 v242,s98,v242
ds_write2_b32 v209,v212,v242 offset0:0 offset1:4
v_and_b32 v213,0xffff,v243
v_and_b32 v243,s98,v243
ds_write2_b32 v215,v213,v243 offset0:0 offset1:4
v_and_b32 v214,0xffff,v244
v_and_b32 v244,s98,v244
ds_write2_b32 v204,v214,v244 offset0:0 offset1:4
v_lshlrev_b32 v247,16,v247
v_lshlrev_b32 v246,16,v246
v_mul_f32 v247,0x6c800000,v247
v_mul_f32 v246,0x12800000,v246
v_rcp_f32 v224,v247
v_lshlrev_b32 v210,16,v245
v_and_b32 v211,s98,v245
v_med3_f32 v224,v224,v222,-v222
v_pk_mul_f32 v[210:211],v[210:211],s[96:97] op_sel_hi:[1,0]
v_cvt_pk_bf16_f32 v220,v224,v228
ds_write_b64 v205,v[210:211]
ds_write_b32 v206,v246 offset:2048
v_lshlrev_b32 v222,16,v220
v_rcp_f32 v222,v222
v_lshlrev_b64 v[220:221],v208,v[220:221]
v_cndmask_b32_e64 v224,v220,0,s[48:49]
v_cndmask_b32_e64 v226,0,v220,s[48:49]
v_cndmask_b32_e64 v225,v221,0,s[48:49]
v_cndmask_b32_e64 v227,0,v221,s[48:49]
ds_write_b128 v207,v[224:227]
ds_write_b32 v206,v222
s_waitcnt lgkmcnt(0)
s_lshl_b32 s4,s52,1
s_and_b32 s5,s52,1
s_sub_u32 s4,s4,s5
s_sub_u32 s6,s4,s10
s_mul_i32 s4,s4,3488
s_mul_i32 s6,s6,98304
s_add_u32 s49,s61,s6
v_mov_b32 v124,s4
s_mov_b64 exec,s[8:9]
v_add_u32 v124,6976,v124
s_mov_b64 exec,-1
s_waitcnt vmcnt(0)
s_barrier
s_setprio 3
ds_read_b128 v[12:15],v233 offset:2048
ds_read_b128 v[8:11],v233 offset:1024
ds_read_b128 v[4:7],v233 offset:0
ds_read_b64 v[2:3],v234 offset:3072
s_waitcnt lgkmcnt(2)
v_xad_u32 v125,v15,16,v124
v_xad_u32 v126,v15,32,v124
v_xad_u32 v127,v15,48,v124
v_add_u32 v124,v15,v124
ds_read_b128 v[88:91],v11 offset:768
ds_read_b128 v[92:95],v12 offset:0
ds_read_b128 v[96:99],v11 offset:1920
ds_read_b128 v[100:103],v12 offset:1152
ds_read_b128 v[48:51],v124 offset:6976
ds_read_b128 v[40:43],v124 offset:0
ds_read_b128 v[44:47],v124 offset:3488
ds_read_b128 v[60:63],v125 offset:6976
ds_read_b128 v[52:55],v125 offset:0
ds_read_b128 v[56:59],v125 offset:3488
s_waitcnt lgkmcnt(5)
v_mfma_f32_16x16x32_bf16 v[160:163],v[88:91],v[48:51],0
s_waitcnt lgkmcnt(3)
ds_read_b128 v[48:51],v124 offset:7040
ds_read_b128 v[88:91],v11 offset:5376
v_smfmac_f32_16x16x64_bf16 v[160:163],v[92:95],v[40:47],v13
s_waitcnt lgkmcnt(4)
ds_read_b128 v[40:43],v124 offset:64
ds_read_b128 v[44:47],v124 offset:3552
ds_read_b128 v[92:95],v12 offset:4608
v_mfma_f32_16x16x32_bf16 v[164:167],v[96:99],v[60:63],0
s_waitcnt lgkmcnt(5)
ds_read_b128 v[60:63],v125 offset:7040
ds_read_b128 v[96:99],v11 offset:6528
v_smfmac_f32_16x16x64_bf16 v[164:167],v[100:103],v[52:59],v13
s_waitcnt lgkmcnt(5)
ds_read_b128 v[52:55],v125 offset:64
ds_read_b128 v[56:59],v125 offset:3552
ds_read_b128 v[100:103],v12 offset:5760
v_mfma_f32_16x16x32_bf16 v[176:179],v[88:91],v[48:51],0
s_waitcnt lgkmcnt(5)
ds_read_b128 v[48:51],v124 offset:7104
ds_read_b128 v[88:91],v11 offset:9984
v_add_f32 v120,v160,v161
v_mul_f32 v122,v160,v160
v_fmac_f32 v122,v161,v161
v_smfmac_f32_16x16x64_bf16 v[176:179],v[92:95],v[40:47],v13
s_waitcnt lgkmcnt(5)
ds_read_b128 v[40:43],v124 offset:128
ds_read_b128 v[44:47],v124 offset:3616
ds_read_b128 v[92:95],v12 offset:9216
v_add_f32 v121,v162,v163
v_mul_f32 v123,v162,v162
v_fmac_f32 v123,v163,v163
v_mfma_f32_16x16x32_bf16 v[180:183],v[96:99],v[60:63],0
s_waitcnt lgkmcnt(5)
ds_read_b128 v[60:63],v125 offset:7104
ds_read_b128 v[96:99],v11 offset:11136
v_add_f32 v120,v120,v164
v_fmac_f32 v122,v164,v164
v_add_f32 v120,v120,v165
v_fmac_f32 v122,v165,v165
v_smfmac_f32_16x16x64_bf16 v[180:183],v[100:103],v[52:59],v13
s_waitcnt lgkmcnt(5)
ds_read_b128 v[52:55],v125 offset:128
ds_read_b128 v[56:59],v125 offset:3616
ds_read_b128 v[100:103],v12 offset:10368
v_add_f32 v121,v121,v166
v_fmac_f32 v123,v166,v166
v_add_f32 v121,v121,v167
v_fmac_f32 v123,v167,v167
v_mfma_f32_16x16x32_bf16 v[192:195],v[88:91],v[48:51],0
s_waitcnt lgkmcnt(5)
ds_read_b128 v[48:51],v126 offset:6976
ds_read_b128 v[88:91],v11 offset:3072
v_add_f32 v120,v120,v176
v_fmac_f32 v122,v176,v176
v_add_f32 v120,v120,v177
v_fmac_f32 v122,v177,v177
v_smfmac_f32_16x16x64_bf16 v[192:195],v[92:95],v[40:47],v13
s_waitcnt lgkmcnt(5)
ds_read_b128 v[40:43],v126 offset:0
ds_read_b128 v[44:47],v126 offset:3488
ds_read_b128 v[92:95],v12 offset:2304
v_add_f32 v121,v121,v178
v_fmac_f32 v123,v178,v178
v_add_f32 v121,v121,v179
v_fmac_f32 v123,v179,v179
v_mfma_f32_16x16x32_bf16 v[196:199],v[96:99],v[60:63],0
s_waitcnt lgkmcnt(5)
ds_read_b128 v[60:63],v127 offset:6976
ds_read_b128 v[96:99],v11 offset:4224
v_add_f32 v120,v120,v180
v_fmac_f32 v122,v180,v180
v_add_f32 v120,v120,v181
v_fmac_f32 v122,v181,v181
v_smfmac_f32_16x16x64_bf16 v[196:199],v[100:103],v[52:59],v13
s_waitcnt lgkmcnt(5)
ds_read_b128 v[52:55],v127 offset:0
ds_read_b128 v[56:59],v127 offset:3488
ds_read_b128 v[100:103],v12 offset:3456
v_add_f32 v121,v121,v182
v_fmac_f32 v123,v182,v182
v_add_f32 v121,v121,v183
v_fmac_f32 v123,v183,v183
v_mfma_f32_16x16x32_bf16 v[168:171],v[88:91],v[48:51],0
s_waitcnt lgkmcnt(5)
ds_read_b128 v[48:51],v126 offset:7040
ds_read_b128 v[88:91],v11 offset:7680
v_add_f32 v120,v120,v192
v_fmac_f32 v122,v192,v192
v_add_f32 v120,v120,v193
v_fmac_f32 v122,v193,v193
v_smfmac_f32_16x16x64_bf16 v[168:171],v[92:95],v[40:47],v13
s_waitcnt lgkmcnt(5)
ds_read_b128 v[40:43],v126 offset:64
ds_read_b128 v[44:47],v126 offset:3552
ds_read_b128 v[92:95],v12 offset:6912
v_add_f32 v121,v121,v194
v_fmac_f32 v123,v194,v194
v_add_f32 v121,v121,v195
v_fmac_f32 v123,v195,v195
v_mfma_f32_16x16x32_bf16 v[172:175],v[96:99],v[60:63],0
s_waitcnt lgkmcnt(5)
ds_read_b128 v[60:63],v127 offset:7040
ds_read_b128 v[96:99],v11 offset:8832
v_add_f32 v120,v120,v196
v_fmac_f32 v122,v196,v196
v_add_f32 v120,v120,v197
v_fmac_f32 v122,v197,v197
v_add_f32 v121,v121,v198
v_smfmac_f32_16x16x64_bf16 v[172:175],v[100:103],v[52:59],v13
s_waitcnt lgkmcnt(5)
ds_read_b128 v[52:55],v127 offset:64
ds_read_b128 v[56:59],v127 offset:3552
ds_read_b128 v[100:103],v12 offset:8064
v_fmac_f32 v123,v198,v198
v_add_f32 v121,v121,v199
v_fmac_f32 v123,v199,v199
v_add_u32 v124,v124,v8
v_mfma_f32_16x16x32_bf16 v[184:187],v[88:91],v[48:51],0
s_waitcnt lgkmcnt(5)
ds_read_b128 v[48:51],v126 offset:7104
ds_read_b128 v[88:91],v11 offset:12288
v_add_f32 v120,v120,v168
v_fmac_f32 v122,v168,v168
v_add_f32 v120,v120,v169
v_fmac_f32 v122,v169,v169
v_smfmac_f32_16x16x64_bf16 v[184:187],v[92:95],v[40:47],v13
s_waitcnt lgkmcnt(5)
ds_read_b128 v[40:43],v126 offset:128
ds_read_b128 v[44:47],v126 offset:3616
ds_read_b128 v[92:95],v12 offset:11520
v_add_f32 v121,v121,v170
v_fmac_f32 v123,v170,v170
v_add_f32 v121,v121,v171
v_fmac_f32 v123,v171,v171
v_mfma_f32_16x16x32_bf16 v[188:191],v[96:99],v[60:63],0
s_waitcnt lgkmcnt(5)
ds_read_b128 v[60:63],v127 offset:7104
ds_read_b128 v[96:99],v11 offset:13440
v_add_f32 v120,v120,v172
v_fmac_f32 v122,v172,v172
v_add_f32 v120,v120,v173
v_fmac_f32 v122,v173,v173
v_smfmac_f32_16x16x64_bf16 v[188:191],v[100:103],v[52:59],v13
s_waitcnt lgkmcnt(5)
ds_read_b128 v[52:55],v127 offset:128
ds_read_b128 v[56:59],v127 offset:3616
ds_read_b128 v[100:103],v12 offset:12672
v_add_f32 v121,v121,v174
v_fmac_f32 v123,v174,v174
v_add_f32 v121,v121,v175
v_fmac_f32 v123,v175,v175
v_mfma_f32_16x16x32_bf16 v[200:203],v[88:91],v[48:51],0
s_waitcnt lgkmcnt(5)
ds_read_b128 v[144:147],v124 offset:3488
ds_read_b128 v[148:151],v124 offset:3680
ds_read_b128 v[88:91],v124 offset:3552
v_add_f32 v120,v120,v184
v_fmac_f32 v122,v184,v184
v_add_f32 v120,v120,v185
v_fmac_f32 v122,v185,v185
v_smfmac_f32_16x16x64_bf16 v[200:203],v[92:95],v[40:47],v13
s_waitcnt lgkmcnt(6)
ds_read_b128 v[64:67],v14 offset:512
ds_read_b128 v[68:71],v7 offset:3712
ds_read_b128 v[92:95],v124 offset:3744
v_add_f32 v121,v121,v186
v_fmac_f32 v123,v186,v186
v_add_f32 v121,v121,v187
v_fmac_f32 v123,v187,v187
v_mfma_f32_16x16x32_bf16 v[204:207],v[96:99],v[60:63],0
s_waitcnt lgkmcnt(6)
ds_read_b128 v[104:107],v14 offset:0
ds_read_b128 v[108:111],v7 offset:3584
ds_read_b128 v[80:83],v14 offset:1024
v_add_f32 v120,v120,v188
v_fmac_f32 v122,v188,v188
v_add_f32 v120,v120,v189
v_fmac_f32 v122,v189,v189
v_smfmac_f32_16x16x64_bf16 v[204:207],v[100:103],v[52:59],v13
ds_read_b128 v[84:87],v7 offset:3840
ds_read_b128 v[96:99],v124 offset:3616
ds_read_b128 v[72:75],v14 offset:768
v_add_f32 v121,v121,v190
v_fmac_f32 v123,v190,v190
v_add_f32 v121,v121,v191
v_fmac_f32 v123,v191,v191
ds_read_b128 v[76:79],v7 offset:3744
ds_read_b128 v[100:103],v124 offset:3808
ds_read_b128 v[152:155],v14 offset:256
ds_read_b128 v[156:159],v7 offset:3616
ds_read_b128 v[128:131],v14 offset:1280
ds_read_b128 v[132:135],v7 offset:3872
v_add_f32 v120,v120,v200
v_fmac_f32 v122,v200,v200
v_add_f32 v120,v120,v201
v_fmac_f32 v122,v201,v201
v_add_f32 v121,v121,v202
v_fmac_f32 v123,v202,v202
v_add_f32 v121,v121,v203
v_fmac_f32 v123,v203,v203
v_add_f32 v120,v120,v204
v_fmac_f32 v122,v204,v204
v_add_f32 v120,v120,v205
v_fmac_f32 v122,v205,v205
v_add_f32 v121,v121,v206
v_fmac_f32 v123,v206,v206
v_add_f32 v121,v121,v207
v_fmac_f32 v123,v207,v207
v_sub_u32 v124,v124,v8
v_permlane32_swap_b32 v120,v121
v_permlane32_swap_b32 v122,v123
v_add_f32 v120,v120,v121
v_add_f32 v122,v122,v123
s_nop 1
v_permlane16_swap_b32 v120,v122
v_add_f32 v61,v120,v122
v_add_f32 v62,v120,v122
s_nop 1
v_permlane16_swap_b32 v61,v62
v_mul_f32 v61,0x3c2aaaab,v61
v_mul_f32 v62,0x3c2aaaab,v62
v_mul_f32 v60,0x5e000000,v62
v_fma_f32 v62,-v61,v61,v62
v_pk_maximum3_f16 v1,v1,v60,v60 op_sel:[0,1,1] op_sel_hi:[1,1,1]
v_max_f32 v62,0,v62
v_add_f32 v62,0x3727c5ac,v62
v_rsq_f32 v62,v62
s_nop 0
v_mul_f32 v120,0x8befe000,v62
v_mul_f32 v121,0x8befe000,v62
v_mul_f32 v122,-v61,v120
v_mul_f32 v123,-v61,v120
v_permlane32_swap_b32 v120,v121
s_nop 0
v_permlane32_swap_b32 v122,v123
v_pk_fma_f32 v[160:161],v[160:161],v[120:121],v[122:123] op_sel:[0,0,0] op_sel_hi:[1,0,0]
v_pk_fma_f32 v[162:163],v[162:163],v[120:121],v[122:123] op_sel:[0,1,1] op_sel_hi:[1,1,1]
v_pk_fma_f32 v[164:165],v[164:165],v[120:121],v[122:123] op_sel:[0,0,0] op_sel_hi:[1,0,0]
v_pk_fma_f32 v[166:167],v[166:167],v[120:121],v[122:123] op_sel:[0,1,1] op_sel_hi:[1,1,1]
v_pk_fma_f32 v[168:169],v[168:169],v[120:121],v[122:123] op_sel:[0,0,0] op_sel_hi:[1,0,0]
v_pk_fma_f32 v[170:171],v[170:171],v[120:121],v[122:123] op_sel:[0,1,1] op_sel_hi:[1,1,1]
v_pk_fma_f32 v[172:173],v[172:173],v[120:121],v[122:123] op_sel:[0,0,0] op_sel_hi:[1,0,0]
v_pk_fma_f32 v[174:175],v[174:175],v[120:121],v[122:123] op_sel:[0,1,1] op_sel_hi:[1,1,1]
v_pk_fma_f32 v[176:177],v[176:177],v[120:121],v[122:123] op_sel:[0,0,0] op_sel_hi:[1,0,0]
v_pk_fma_f32 v[178:179],v[178:179],v[120:121],v[122:123] op_sel:[0,1,1] op_sel_hi:[1,1,1]
v_pk_fma_f32 v[180:181],v[180:181],v[120:121],v[122:123] op_sel:[0,0,0] op_sel_hi:[1,0,0]
v_pk_fma_f32 v[182:183],v[182:183],v[120:121],v[122:123] op_sel:[0,1,1] op_sel_hi:[1,1,1]
v_pk_fma_f32 v[184:185],v[184:185],v[120:121],v[122:123] op_sel:[0,0,0] op_sel_hi:[1,0,0]
v_pk_fma_f32 v[186:187],v[186:187],v[120:121],v[122:123] op_sel:[0,1,1] op_sel_hi:[1,1,1]
v_pk_fma_f32 v[188:189],v[188:189],v[120:121],v[122:123] op_sel:[0,0,0] op_sel_hi:[1,0,0]
v_pk_fma_f32 v[190:191],v[190:191],v[120:121],v[122:123] op_sel:[0,1,1] op_sel_hi:[1,1,1]
v_pk_fma_f32 v[192:193],v[192:193],v[120:121],v[122:123] op_sel:[0,0,0] op_sel_hi:[1,0,0]
v_pk_fma_f32 v[194:195],v[194:195],v[120:121],v[122:123] op_sel:[0,1,1] op_sel_hi:[1,1,1]
v_pk_fma_f32 v[196:197],v[196:197],v[120:121],v[122:123] op_sel:[0,0,0] op_sel_hi:[1,0,0]
v_pk_fma_f32 v[198:199],v[198:199],v[120:121],v[122:123] op_sel:[0,1,1] op_sel_hi:[1,1,1]
v_pk_fma_f32 v[200:201],v[200:201],v[120:121],v[122:123] op_sel:[0,0,0] op_sel_hi:[1,0,0]
v_pk_fma_f32 v[202:203],v[202:203],v[120:121],v[122:123] op_sel:[0,1,1] op_sel_hi:[1,1,1]
v_pk_fma_f32 v[204:205],v[204:205],v[120:121],v[122:123] op_sel:[0,0,0] op_sel_hi:[1,0,0]
v_pk_fma_f32 v[206:207],v[206:207],v[120:121],v[122:123] op_sel:[0,1,1] op_sel_hi:[1,1,1]
v_cvt_pk_bf16_f32 v16,v160,v161
v_cvt_pk_bf16_f32 v20,v162,v163
v_cvt_pk_bf16_f32 v17,v164,v165
v_cvt_pk_bf16_f32 v21,v166,v167
v_cvt_pk_bf16_f32 v18,v168,v169
v_cvt_pk_bf16_f32 v22,v170,v171
v_cvt_pk_bf16_f32 v19,v172,v173
v_cvt_pk_bf16_f32 v23,v174,v175
v_cvt_pk_bf16_f32 v24,v176,v177
s_waitcnt lgkmcnt(10)
v_mfma_f32_16x16x32_bf16 v[160:163],v[104:107],v[144:147],v[108:111]
v_cvt_pk_bf16_f32 v28,v178,v179
v_cvt_pk_bf16_f32 v25,v180,v181
v_mfma_f32_16x16x32_bf16 v[164:167],v[104:107],v[148:151],v[108:111]
s_waitcnt lgkmcnt(2)
v_cvt_pk_bf16_f32 v29,v182,v183
v_cvt_pk_bf16_f32 v26,v184,v185
v_mfma_f32_16x16x32_bf16 v[168:171],v[152:155],v[144:147],v[156:159]
v_cvt_pk_bf16_f32 v30,v186,v187
v_cvt_pk_bf16_f32 v27,v188,v189
v_mfma_f32_16x16x32_bf16 v[172:175],v[152:155],v[148:151],v[156:159]
v_cvt_pk_bf16_f32 v31,v190,v191
v_cvt_pk_bf16_f32 v32,v192,v193
v_mfma_f32_16x16x32_bf16 v[176:179],v[64:67],v[88:91],v[68:71]
v_cvt_pk_bf16_f32 v36,v194,v195
v_cvt_pk_bf16_f32 v33,v196,v197
v_mfma_f32_16x16x32_bf16 v[180:183],v[64:67],v[92:95],v[68:71]
v_cvt_pk_bf16_f32 v37,v198,v199
v_cvt_pk_bf16_f32 v34,v200,v201
v_mfma_f32_16x16x32_bf16 v[184:187],v[72:75],v[88:91],v[76:79]
v_cvt_pk_bf16_f32 v38,v202,v203
v_cvt_pk_bf16_f32 v35,v204,v205
v_mfma_f32_16x16x32_bf16 v[188:191],v[72:75],v[92:95],v[76:79]
v_cvt_pk_bf16_f32 v39,v206,v207
v_mfma_f32_16x16x32_bf16 v[192:195],v[80:83],v[96:99],v[84:87]
v_mfma_f32_16x16x32_bf16 v[196:199],v[80:83],v[100:103],v[84:87]
s_waitcnt lgkmcnt(0)
v_mfma_f32_16x16x32_bf16 v[200:203],v[128:131],v[96:99],v[132:135]
v_mfma_f32_16x16x32_bf16 v[204:207],v[128:131],v[100:103],v[132:135]
ds_read_b128 v[88:91],v11 offset:768
ds_read_b128 v[92:95],v12 offset:0
ds_read_b128 v[96:99],v11 offset:1920
ds_read_b128 v[100:103],v12 offset:1152
s_waitcnt lgkmcnt(0)
s_barrier
ds_read_b128 v[48:51],v124 offset:34880
ds_read_b128 v[40:43],v124 offset:27904
ds_read_b128 v[44:47],v124 offset:31392
ds_read_b128 v[60:63],v125 offset:34880
ds_read_b128 v[52:55],v125 offset:27904
ds_read_b128 v[56:59],v125 offset:31392
ds_read_b64_tr_b16 v[64:65],v4 offset:12832
ds_read_b64_tr_b16 v[66:67],v4 offset:14880
ds_read_b128 v[112:115],v7 offset:128
ds_read_b64_tr_b16 v[68:69],v5 offset:12832
ds_read_b64_tr_b16 v[70:71],v5 offset:14880
ds_read_b128 v[116:119],v7 offset:160
ds_read_b64_tr_b16 v[72:73],v4 offset:16928
ds_read_b64_tr_b16 v[74:75],v4 offset:18976
ds_read_b64_tr_b16 v[76:77],v5 offset:16928
ds_read_b64_tr_b16 v[78:79],v5 offset:18976
ds_read_b64_tr_b16 v[80:81],v4 offset:21024
ds_read_b64_tr_b16 v[82:83],v4 offset:23072
ds_read_b64_tr_b16 v[84:85],v5 offset:21024
ds_read_b64_tr_b16 v[86:87],v5 offset:23072
s_waitcnt lgkmcnt(15)
v_mfma_f32_16x16x32_bf16 v[208:211],v[88:91],v[48:51],0
ds_read_b128 v[48:51],v124 offset:34944
ds_read_b128 v[88:91],v11 offset:5376
v_smfmac_f32_16x16x64_bf16 v[208:211],v[92:95],v[40:47],v13
ds_read_b128 v[40:43],v124 offset:27968
ds_read_b128 v[44:47],v124 offset:31456
ds_read_b128 v[92:95],v12 offset:4608
v_mfma_f32_16x16x32_bf16 v[212:215],v[96:99],v[60:63],0
s_waitcnt lgkmcnt(15)
ds_read_b128 v[60:63],v125 offset:34944
ds_read_b128 v[96:99],v11 offset:6528
v_smfmac_f32_16x16x64_bf16 v[212:215],v[100:103],v[52:59],v13
s_waitcnt lgkmcnt(5)
ds_read_b128 v[52:55],v125 offset:27968
ds_read_b128 v[56:59],v125 offset:31456
ds_read_b128 v[100:103],v12 offset:5760
v_mfma_f32_16x16x32_bf16 v[224:227],v[88:91],v[48:51],0
s_waitcnt lgkmcnt(5)
ds_read_b128 v[48:51],v124 offset:35008
ds_read_b128 v[88:91],v11 offset:9984
v_add_f32 v120,v208,v209
v_mul_f32 v122,v208,v208
v_smfmac_f32_16x16x64_bf16 v[224:227],v[92:95],v[40:47],v13
ds_read_b128 v[40:43],v124 offset:28032
ds_read_b128 v[44:47],v124 offset:31520
ds_read_b128 v[92:95],v12 offset:9216
v_fmac_f32 v122,v209,v209
v_mfma_f32_16x16x32_bf16 v[128:131],v[64:67],v[16:19],v[112:115]
v_add_f32 v121,v210,v211
v_mul_f32 v123,v210,v210
v_mfma_f32_16x16x32_bf16 v[136:139],v[64:67],v[20:23],v[112:115]
s_waitcnt lgkmcnt(8)
v_fmac_f32 v123,v211,v211
v_mfma_f32_16x16x32_bf16 v[228:231],v[96:99],v[60:63],0
s_waitcnt lgkmcnt(5)
ds_read_b128 v[60:63],v125 offset:35008
ds_read_b128 v[96:99],v11 offset:11136
v_add_f32 v120,v120,v212
v_fmac_f32 v122,v212,v212
v_smfmac_f32_16x16x64_bf16 v[228:231],v[100:103],v[52:59],v13
ds_read_b128 v[52:55],v125 offset:28032
ds_read_b128 v[56:59],v125 offset:31520
ds_read_b128 v[100:103],v12 offset:10368
v_add_f32 v120,v120,v213
s_mov_b32 s86,0xbe801d5a
v_fmac_f32 v122,v213,v213
v_mfma_f32_16x16x32_bf16 v[140:143],v[68:71],v[20:23],v[116:119]
v_add_f32 v121,v121,v214
v_fmac_f32 v123,v214,v214
v_mfma_f32_16x16x32_bf16 v[132:135],v[68:71],v[16:19],v[116:119]
s_waitcnt lgkmcnt(8)
v_add_f32 v121,v121,v215
v_fmac_f32 v123,v215,v215
v_mfma_f32_16x16x32_bf16 v[240:243],v[88:91],v[48:51],0
s_waitcnt lgkmcnt(5)
ds_read_b128 v[48:51],v126 offset:34880
ds_read_b128 v[88:91],v11 offset:3072
v_add_f32 v120,v120,v224
v_fmac_f32 v122,v224,v224
v_add_f32 v120,v120,v225
v_smfmac_f32_16x16x64_bf16 v[240:243],v[92:95],v[40:47],v13
ds_read_b128 v[40:43],v126 offset:27904
ds_read_b128 v[44:47],v126 offset:31392
ds_read_b128 v[92:95],v12 offset:2304
v_fmac_f32 v122,v225,v225
v_add_f32 v121,v121,v226
v_fmac_f32 v123,v226,v226
v_mfma_f32_16x16x32_bf16 v[128:131],v[72:75],v[24:27],v[128:131]
s_waitcnt lgkmcnt(8)
v_add_f32 v121,v121,v227
v_fmac_f32 v123,v227,v227
v_mfma_f32_16x16x32_bf16 v[244:247],v[96:99],v[60:63],0
s_waitcnt lgkmcnt(5)
ds_read_b128 v[60:63],v127 offset:34880
ds_read_b128 v[96:99],v11 offset:4224
v_add_f32 v120,v120,v228
v_fmac_f32 v122,v228,v228
v_add_f32 v120,v120,v229
v_smfmac_f32_16x16x64_bf16 v[244:247],v[100:103],v[52:59],v13
ds_read_b128 v[52:55],v127 offset:27904
ds_read_b128 v[56:59],v127 offset:31392
ds_read_b128 v[100:103],v12 offset:3456
v_fmac_f32 v122,v229,v229
v_add_f32 v121,v121,v230
v_fmac_f32 v123,v230,v230
v_mfma_f32_16x16x32_bf16 v[136:139],v[72:75],v[28:31],v[136:139]
s_waitcnt lgkmcnt(8)
v_add_f32 v121,v121,v231
v_fmac_f32 v123,v231,v231
v_mfma_f32_16x16x32_bf16 v[216:219],v[88:91],v[48:51],0
s_waitcnt lgkmcnt(5)
ds_read_b128 v[48:51],v126 offset:34944
ds_read_b128 v[88:91],v11 offset:7680
v_add_f32 v120,v120,v240
v_fmac_f32 v122,v240,v240
v_add_f32 v120,v120,v241
v_smfmac_f32_16x16x64_bf16 v[216:219],v[92:95],v[40:47],v13
ds_read_b128 v[40:43],v126 offset:27968
ds_read_b128 v[44:47],v126 offset:31456
ds_read_b128 v[92:95],v12 offset:6912
v_fmac_f32 v122,v241,v241
v_add_f32 v121,v121,v242
v_fmac_f32 v123,v242,v242
v_mfma_f32_16x16x32_bf16 v[140:143],v[76:79],v[28:31],v[140:143]
s_waitcnt lgkmcnt(8)
v_add_f32 v121,v121,v243
v_fmac_f32 v123,v243,v243
v_mfma_f32_16x16x32_bf16 v[220:223],v[96:99],v[60:63],0
s_waitcnt lgkmcnt(5)
ds_read_b128 v[60:63],v127 offset:34944
ds_read_b128 v[96:99],v11 offset:8832
v_add_f32 v120,v120,v244
v_fmac_f32 v122,v244,v244
v_add_f32 v120,v120,v245
v_fmac_f32 v122,v245,v245
v_smfmac_f32_16x16x64_bf16 v[220:223],v[100:103],v[52:59],v13
ds_read_b128 v[52:55],v127 offset:27968
ds_read_b128 v[56:59],v127 offset:31456
ds_read_b128 v[100:103],v12 offset:8064
v_add_f32 v121,v121,v246
v_fmac_f32 v123,v246,v246
v_add_f32 v121,v121,v247
v_mfma_f32_16x16x32_bf16 v[132:135],v[76:79],v[24:27],v[132:135]
s_waitcnt lgkmcnt(8)
v_fmac_f32 v123,v247,v247
v_add_u32 v124,v124,v8
v_mfma_f32_16x16x32_bf16 v[232:235],v[88:91],v[48:51],0
s_waitcnt lgkmcnt(5)
ds_read_b128 v[48:51],v126 offset:35008
ds_read_b128 v[88:91],v11 offset:12288
v_add_f32 v120,v120,v216
v_fmac_f32 v122,v216,v216
v_add_f32 v120,v120,v217
v_smfmac_f32_16x16x64_bf16 v[232:235],v[92:95],v[40:47],v13
ds_read_b128 v[40:43],v126 offset:28032
ds_read_b128 v[44:47],v126 offset:31520
ds_read_b128 v[92:95],v12 offset:11520
v_fmac_f32 v122,v217,v217
v_add_f32 v121,v121,v218
v_fmac_f32 v123,v218,v218
v_mfma_f32_16x16x32_bf16 v[128:131],v[80:83],v[32:35],v[128:131]
s_waitcnt lgkmcnt(8)
v_add_f32 v121,v121,v219
v_fmac_f32 v123,v219,v219
v_mfma_f32_16x16x32_bf16 v[236:239],v[96:99],v[60:63],0
s_waitcnt lgkmcnt(5)
ds_read_b128 v[60:63],v127 offset:35008
ds_read_b128 v[96:99],v11 offset:13440
v_add_f32 v120,v120,v220
v_fmac_f32 v122,v220,v220
v_add_f32 v120,v120,v221
v_smfmac_f32_16x16x64_bf16 v[236:239],v[100:103],v[52:59],v13
ds_read_b128 v[52:55],v127 offset:28032
ds_read_b128 v[56:59],v127 offset:31520
ds_read_b128 v[100:103],v12 offset:12672
v_fmac_f32 v122,v221,v221
v_add_f32 v121,v121,v222
v_fmac_f32 v123,v222,v222
v_mfma_f32_16x16x32_bf16 v[136:139],v[80:83],v[36:39],v[136:139]
s_waitcnt lgkmcnt(8)
ds_read_b128 v[144:147],v124 offset:31392
ds_read_b128 v[148:151],v124 offset:31584
v_add_f32 v121,v121,v223
v_fmac_f32 v123,v223,v223
v_mfma_f32_16x16x32_bf16 v[248:251],v[88:91],v[48:51],0
s_waitcnt lgkmcnt(7)
ds_read_b128 v[88:91],v124 offset:31456
v_add_f32 v120,v120,v232
v_fmac_f32 v122,v232,v232
v_add_f32 v120,v120,v233
v_smfmac_f32_16x16x64_bf16 v[248:251],v[92:95],v[40:47],v13
ds_read_b128 v[92:95],v124 offset:31648
ds_read_b128 v[104:107],v14 offset:0
ds_read_b128 v[108:111],v7 offset:3584
v_fmac_f32 v122,v233,v233
v_add_f32 v121,v121,v234
v_fmac_f32 v123,v234,v234
v_mfma_f32_16x16x32_bf16 v[140:143],v[84:87],v[36:39],v[140:143]
s_waitcnt lgkmcnt(9)
v_add_f32 v121,v121,v235
v_fmac_f32 v123,v235,v235
v_mfma_f32_16x16x32_bf16 v[252:255],v[96:99],v[60:63],0
s_waitcnt lgkmcnt(6)
ds_read_b128 v[96:99],v124 offset:31520
v_add_f32 v120,v120,v236
v_fmac_f32 v122,v236,v236
v_add_f32 v120,v120,v237
v_smfmac_f32_16x16x64_bf16 v[252:255],v[100:103],v[52:59],v13
ds_read_b128 v[100:103],v124 offset:31712
ds_read_b128 v[152:155],v14 offset:256
ds_read_b128 v[156:159],v7 offset:3616
v_fmac_f32 v122,v237,v237
v_add_f32 v121,v121,v238
v_fmac_f32 v123,v238,v238
v_mfma_f32_16x16x32_bf16 v[132:135],v[84:87],v[32:35],v[132:135]
v_add_f32 v121,v121,v239
v_fmac_f32 v123,v239,v239
v_add_f32 v120,v120,v248
v_fmac_f32 v122,v248,v248
v_add_f32 v120,v120,v249
v_fmac_f32 v122,v249,v249
v_add_f32 v121,v121,v250
v_fmac_f32 v123,v250,v250
v_add_f32 v121,v121,v251
v_fmac_f32 v123,v251,v251
s_waitcnt lgkmcnt(2)
s_barrier
v_add_f32 v120,v120,v252
v_fmac_f32 v122,v252,v252
v_add_f32 v120,v120,v253
v_fmac_f32 v122,v253,v253
v_add_f32 v121,v121,v254
v_fmac_f32 v123,v254,v254
v_add_f32 v121,v121,v255
v_fmac_f32 v123,v255,v255
s_nop 0
v_permlane32_swap_b32 v120,v121
v_permlane32_swap_b32 v122,v123
v_add_f32 v120,v120,v121
v_add_f32 v122,v122,v123
s_nop 1
v_permlane16_swap_b32 v120,v122
v_add_f32 v61,v120,v122
v_add_f32 v62,v120,v122
s_nop 1
v_permlane16_swap_b32 v61,v62
v_mul_f32 v61,0x3c2aaaab,v61
v_mul_f32 v62,0x3c2aaaab,v62
v_mul_f32 v60,0x5e000000,v62
v_fma_f32 v62,-v61,v61,v62
v_pk_maximum3_f16 v1,v1,v60,v60 op_sel:[0,1,1] op_sel_hi:[1,1,1]
v_max_f32 v62,0,v62
v_add_f32 v62,0x3727c5ac,v62
v_rsq_f32 v62,v62
s_nop 0
v_mul_f32 v120,0x8befe000,v62
v_mul_f32 v121,0x8befe000,v62
v_mul_f32 v122,-v61,v120
v_mul_f32 v123,-v61,v120
v_permlane32_swap_b32 v120,v121
s_nop 0
v_permlane32_swap_b32 v122,v123
v_pk_fma_f32 v[208:209],v[208:209],v[120:121],v[122:123] op_sel:[0,0,0] op_sel_hi:[1,0,0]
v_pk_fma_f32 v[210:211],v[210:211],v[120:121],v[122:123] op_sel:[0,1,1] op_sel_hi:[1,1,1]
v_pk_fma_f32 v[212:213],v[212:213],v[120:121],v[122:123] op_sel:[0,0,0] op_sel_hi:[1,0,0]
v_pk_fma_f32 v[214:215],v[214:215],v[120:121],v[122:123] op_sel:[0,1,1] op_sel_hi:[1,1,1]
v_pk_fma_f32 v[216:217],v[216:217],v[120:121],v[122:123] op_sel:[0,0,0] op_sel_hi:[1,0,0]
v_pk_fma_f32 v[218:219],v[218:219],v[120:121],v[122:123] op_sel:[0,1,1] op_sel_hi:[1,1,1]
v_pk_fma_f32 v[220:221],v[220:221],v[120:121],v[122:123] op_sel:[0,0,0] op_sel_hi:[1,0,0]
v_pk_fma_f32 v[222:223],v[222:223],v[120:121],v[122:123] op_sel:[0,1,1] op_sel_hi:[1,1,1]
v_pk_fma_f32 v[224:225],v[224:225],v[120:121],v[122:123] op_sel:[0,0,0] op_sel_hi:[1,0,0]
v_pk_fma_f32 v[226:227],v[226:227],v[120:121],v[122:123] op_sel:[0,1,1] op_sel_hi:[1,1,1]
v_pk_fma_f32 v[228:229],v[228:229],v[120:121],v[122:123] op_sel:[0,0,0] op_sel_hi:[1,0,0]
v_pk_fma_f32 v[230:231],v[230:231],v[120:121],v[122:123] op_sel:[0,1,1] op_sel_hi:[1,1,1]
v_pk_fma_f32 v[232:233],v[232:233],v[120:121],v[122:123] op_sel:[0,0,0] op_sel_hi:[1,0,0]
v_pk_fma_f32 v[234:235],v[234:235],v[120:121],v[122:123] op_sel:[0,1,1] op_sel_hi:[1,1,1]
v_pk_fma_f32 v[236:237],v[236:237],v[120:121],v[122:123] op_sel:[0,0,0] op_sel_hi:[1,0,0]
v_pk_fma_f32 v[238:239],v[238:239],v[120:121],v[122:123] op_sel:[0,1,1] op_sel_hi:[1,1,1]
v_pk_fma_f32 v[240:241],v[240:241],v[120:121],v[122:123] op_sel:[0,0,0] op_sel_hi:[1,0,0]
v_pk_fma_f32 v[242:243],v[242:243],v[120:121],v[122:123] op_sel:[0,1,1] op_sel_hi:[1,1,1]
v_pk_fma_f32 v[244:245],v[244:245],v[120:121],v[122:123] op_sel:[0,0,0] op_sel_hi:[1,0,0]
v_pk_fma_f32 v[246:247],v[246:247],v[120:121],v[122:123] op_sel:[0,1,1] op_sel_hi:[1,1,1]
v_pk_fma_f32 v[248:249],v[248:249],v[120:121],v[122:123] op_sel:[0,0,0] op_sel_hi:[1,0,0]
v_pk_fma_f32 v[250:251],v[250:251],v[120:121],v[122:123] op_sel:[0,1,1] op_sel_hi:[1,1,1]
v_pk_fma_f32 v[252:253],v[252:253],v[120:121],v[122:123] op_sel:[0,0,0] op_sel_hi:[1,0,0]
v_pk_fma_f32 v[254:255],v[254:255],v[120:121],v[122:123] op_sel:[0,1,1] op_sel_hi:[1,1,1]
v_cvt_pk_bf16_f32 v40,v208,v209
v_cvt_pk_bf16_f32 v44,v210,v211
v_cvt_pk_bf16_f32 v41,v212,v213
v_cvt_pk_bf16_f32 v45,v214,v215
v_cvt_pk_bf16_f32 v42,v216,v217
v_cvt_pk_bf16_f32 v46,v218,v219
v_cvt_pk_bf16_f32 v43,v220,v221
v_cvt_pk_bf16_f32 v47,v222,v223
v_cvt_pk_bf16_f32 v48,v224,v225
v_mfma_f32_16x16x32_bf16 v[208:211],v[104:107],v[144:147],v[108:111]
v_cvt_pk_bf16_f32 v52,v226,v227
v_cvt_pk_bf16_f32 v49,v228,v229
v_mfma_f32_16x16x32_bf16 v[212:215],v[104:107],v[148:151],v[108:111]
s_waitcnt lgkmcnt(0)
ds_read_b128 v[104:107],v14 offset:512
ds_read_b128 v[108:111],v7 offset:3712
v_cvt_pk_bf16_f32 v53,v230,v231
v_cvt_pk_bf16_f32 v50,v232,v233
v_mfma_f32_16x16x32_bf16 v[216:219],v[152:155],v[144:147],v[156:159]
v_cvt_pk_bf16_f32 v54,v234,v235
v_cvt_pk_bf16_f32 v51,v236,v237
v_mfma_f32_16x16x32_bf16 v[220:223],v[152:155],v[148:151],v[156:159]
ds_read_b128 v[152:155],v14 offset:768
ds_read_b128 v[156:159],v7 offset:3744
v_cvt_pk_bf16_f32 v55,v238,v239
v_cvt_pk_bf16_f32 v56,v240,v241
s_waitcnt lgkmcnt(2)
v_mfma_f32_16x16x32_bf16 v[224:227],v[104:107],v[88:91],v[108:111]
v_cvt_pk_bf16_f32 v60,v242,v243
v_cvt_pk_bf16_f32 v57,v244,v245
v_mfma_f32_16x16x32_bf16 v[228:231],v[104:107],v[92:95],v[108:111]
s_waitcnt lgkmcnt(0)
ds_read_b128 v[104:107],v14 offset:1024
ds_read_b128 v[108:111],v7 offset:3840
v_cvt_pk_bf16_f32 v61,v246,v247
v_cvt_pk_bf16_f32 v58,v248,v249
v_mfma_f32_16x16x32_bf16 v[232:235],v[152:155],v[88:91],v[156:159]
v_cvt_pk_bf16_f32 v62,v250,v251
v_cvt_pk_bf16_f32 v59,v252,v253
v_mfma_f32_16x16x32_bf16 v[236:239],v[152:155],v[92:95],v[156:159]
ds_read_b128 v[152:155],v14 offset:1280
ds_read_b128 v[156:159],v7 offset:3872
v_cvt_pk_bf16_f32 v63,v254,v255
s_waitcnt lgkmcnt(2)
v_mfma_f32_16x16x32_bf16 v[240:243],v[104:107],v[96:99],v[108:111]
s_barrier
s_setprio 2
ds_read_b64_tr_b16 v[88:89],v6 offset:0
v_lshlrev_b32_sdwa v128,1,v128 dst_sel:DWORD dst_unused:UNUSED_PAD src0_sel:DWORD src1_sel:WORD_1
v_lshlrev_b32_sdwa v130,1,v130 dst_sel:DWORD dst_unused:UNUSED_PAD src0_sel:DWORD src1_sel:WORD_1
v_mfma_f32_16x16x32_bf16 v[244:247],v[104:107],v[100:103],v[108:111]
s_waitcnt lgkmcnt(1)
ds_read_u16 v128,v128
ds_read_b64_tr_b16 v[90:91],v6 offset:1536
v_lshlrev_b32_sdwa v129,1,v129 dst_sel:DWORD dst_unused:UNUSED_PAD src0_sel:DWORD src1_sel:WORD_1
v_lshlrev_b32_sdwa v131,1,v131 dst_sel:DWORD dst_unused:UNUSED_PAD src0_sel:DWORD src1_sel:WORD_1
v_mfma_f32_16x16x32_bf16 v[248:251],v[152:155],v[96:99],v[156:159]
ds_read_u16_d16_hi v130,v130
ds_read_b64_tr_b16 v[92:93],v6 offset:8
v_fma_mixlo_f16 v120,v141,s99,0
v_lshlrev_b32_sdwa v132,1,v132 dst_sel:DWORD dst_unused:UNUSED_PAD src0_sel:DWORD src1_sel:WORD_1
v_mfma_f32_16x16x32_bf16 v[252:255],v[152:155],v[100:103],v[156:159]
ds_read_u16 v129,v129
ds_read_b64_tr_b16 v[94:95],v6 offset:1544
v_fma_mixhi_f16 v120,v143,s99,0
v_lshlrev_b32_sdwa v134,1,v134 dst_sel:DWORD dst_unused:UNUSED_PAD src0_sel:DWORD src1_sel:WORD_1
v_pk_fma_f16 v121,v120,v120,s54
v_mfma_f32_16x16x32_bf16 v[144:147],v[64:67],v[40:43],v[112:115]
ds_read_u16_d16_hi v131,v131
ds_read_b64_tr_b16 v[104:105],v6 offset:128
v_lshlrev_b32_sdwa v133,1,v133 dst_sel:DWORD dst_unused:UNUSED_PAD src0_sel:DWORD src1_sel:WORD_1
v_pk_mul_f16 v121,v120,v121
v_mfma_f32_16x16x32_bf16 v[152:155],v[64:67],v[44:47],v[112:115]
ds_read_u16 v132,v132
ds_read_b64_tr_b16 v[106:107],v6 offset:1664
v_lshlrev_b32_sdwa v135,1,v135 dst_sel:DWORD dst_unused:UNUSED_PAD src0_sel:DWORD src1_sel:WORD_1
v_exp_f16 v120,v121
v_mfma_f32_16x16x32_bf16 v[156:159],v[68:71],v[44:47],v[116:119]
ds_read_u16_d16_hi v134,v134
ds_read_b64_tr_b16 v[108:109],v6 offset:136
v_lshlrev_b32_sdwa v136,1,v136 dst_sel:DWORD dst_unused:UNUSED_PAD src0_sel:DWORD src1_sel:WORD_1
v_mfma_f32_16x16x32_bf16 v[148:151],v[68:71],v[40:43],v[116:119]
ds_read_u16 v133,v133
ds_read_b64_tr_b16 v[110:111],v6 offset:1672
v_exp_f16_sdwa v120,v121 dst_sel:WORD_1 dst_unused:UNUSED_PRESERVE src0_sel:WORD_1
v_mfma_f32_16x16x32_bf16 v[144:147],v[72:75],v[48:51],v[144:147]
ds_read_u16_d16_hi v135,v135
ds_read_b64_tr_b16 v[96:97],v6 offset:64
v_lshlrev_b32_sdwa v138,1,v138 dst_sel:DWORD dst_unused:UNUSED_PAD src0_sel:DWORD src1_sel:WORD_1
v_mfma_f32_16x16x32_bf16 v[152:155],v[72:75],v[52:55],v[152:155]
ds_read_u16 v136,v136
ds_read_b64_tr_b16 v[98:99],v6 offset:1600
v_pk_fma_f16 v120,v120,s55,s55
v_lshlrev_b32_sdwa v137,1,v137 dst_sel:DWORD dst_unused:UNUSED_PAD src0_sel:DWORD src1_sel:WORD_1
v_mfma_f32_16x16x32_bf16 v[156:159],v[76:79],v[52:55],v[156:159]
ds_read_u16_d16_hi v138,v138
ds_read_b64_tr_b16 v[100:101],v6 offset:72
v_rcp_f16 v121,v120
v_mfma_f32_16x16x32_bf16 v[148:151],v[76:79],v[48:51],v[148:151]
ds_read_u16 v137,v137
ds_read_b64_tr_b16 v[102:103],v6 offset:1608
v_lshlrev_b32_sdwa v139,1,v139 dst_sel:DWORD dst_unused:UNUSED_PAD src0_sel:DWORD src1_sel:WORD_1
v_rcp_f16_sdwa v121,v120 dst_sel:WORD_1 dst_unused:UNUSED_PRESERVE src0_sel:WORD_1
v_mfma_f32_16x16x32_bf16 v[144:147],v[80:83],v[56:59],v[144:147]
ds_read_u16_d16_hi v139,v139
ds_read_b64_tr_b16 v[64:65],v4 offset:0
v_lshlrev_b32_sdwa v140,1,v140 dst_sel:DWORD dst_unused:UNUSED_PAD src0_sel:DWORD src1_sel:WORD_1
v_mfma_f32_16x16x32_bf16 v[152:155],v[80:83],v[60:63],v[152:155]
ds_read_u16 v140,v140
ds_read_b64_tr_b16 v[66:67],v4 offset:2048
v_fma_mix_f32 v141,v141,v121,0 op_sel_hi:[0,1,0]
v_lshlrev_b32_sdwa v142,1,v142 dst_sel:DWORD dst_unused:UNUSED_PAD src0_sel:DWORD src1_sel:WORD_1
v_mfma_f32_16x16x32_bf16 v[156:159],v[84:87],v[60:63],v[156:159]
ds_read_u16_d16_hi v142,v142
ds_read_b128 v[112:115],v7 offset:256
v_fma_mix_f32 v143,v143,v121,0 op_sel:[0,1,0] op_sel_hi:[0,1,0]
s_waitcnt lgkmcnt(15)
v_lshl_add_u64 v[128:129],v[128:129],0,v[130:131]
v_mfma_f32_16x16x32_bf16 v[148:151],v[84:87],v[56:59],v[148:151]
ds_read_b64_tr_b16 v[68:69],v5 offset:0
s_waitcnt lgkmcnt(14)
v_lshl_add_u64 v[130:131],v[132:133],0,v[134:135]
v_lshlrev_b32_sdwa v144,1,v144 dst_sel:DWORD dst_unused:UNUSED_PAD src0_sel:DWORD src1_sel:WORD_1
v_lshlrev_b32_sdwa v146,1,v146 dst_sel:DWORD dst_unused:UNUSED_PAD src0_sel:DWORD src1_sel:WORD_1
v_mfma_f32_16x16x32_bf16 v[160:163],v[88:91],v[128:131],v[160:163]
ds_read_u16 v144,v144
v_lshlrev_b32_sdwa v145,1,v145 dst_sel:DWORD dst_unused:UNUSED_PAD src0_sel:DWORD src1_sel:WORD_1
v_lshlrev_b32_sdwa v147,1,v147 dst_sel:DWORD dst_unused:UNUSED_PAD src0_sel:DWORD src1_sel:WORD_1
v_mfma_f32_16x16x32_bf16 v[168:171],v[92:95],v[128:131],v[168:171]
s_waitcnt lgkmcnt(12)
ds_read_b64_tr_b16 v[70:71],v5 offset:2048
ds_read_b128 v[116:119],v7 offset:288
v_fma_mixlo_f16 v120,v157,s99,0
v_mfma_f32_16x16x32_bf16 v[176:179],v[96:99],v[128:131],v[176:179]
s_waitcnt lgkmcnt(9)
ds_read_u16_d16_hi v146,v146
v_lshlrev_b32_sdwa v148,1,v148 dst_sel:DWORD dst_unused:UNUSED_PAD src0_sel:DWORD src1_sel:WORD_1
v_lshl_add_u64 v[136:137],v[136:137],0,v[138:139]
v_fma_mixhi_f16 v120,v159,s99,0
v_lshlrev_b32_sdwa v150,1,v150 dst_sel:DWORD dst_unused:UNUSED_PAD src0_sel:DWORD src1_sel:WORD_1
v_mfma_f32_16x16x32_bf16 v[184:187],v[100:103],v[128:131],v[184:187]
ds_read_u16 v145,v145
v_mfma_f32_16x16x32_bf16 v[192:195],v[104:107],v[128:131],v[192:195]
s_waitcnt lgkmcnt(7)
ds_read_b64_tr_b16 v[72:73],v4 offset:4096
ds_read_b64_tr_b16 v[74:75],v4 offset:6144
v_or_b32 v138,v142,v140
v_cvt_pk_bf16_f32 v139,v141,v143
v_mfma_f32_16x16x32_bf16 v[200:203],v[108:111],v[128:131],v[200:203]
ds_read_b64_tr_b16 v[76:77],v5 offset:4096
ds_read_b64_tr_b16 v[78:79],v5 offset:6144
v_pk_fma_f16 v121,v120,v120,s54
v_lshlrev_b32_sdwa v149,1,v149 dst_sel:DWORD dst_unused:UNUSED_PAD src0_sel:DWORD src1_sel:WORD_1
v_mfma_f32_16x16x32_bf16 v[164:167],v[88:91],v[136:139],v[164:167]
ds_read_u16_d16_hi v147,v147
v_pk_mul_f16 v121,v120,v121
v_lshlrev_b32_sdwa v151,1,v151 dst_sel:DWORD dst_unused:UNUSED_PAD src0_sel:DWORD src1_sel:WORD_1
v_mfma_f32_16x16x32_bf16 v[172:175],v[92:95],v[136:139],v[172:175]
ds_read_u16 v148,v148
v_lshlrev_b32_sdwa v152,1,v152 dst_sel:DWORD dst_unused:UNUSED_PAD src0_sel:DWORD src1_sel:WORD_1
v_exp_f16 v120,v121
v_mfma_f32_16x16x32_bf16 v[180:183],v[96:99],v[136:139],v[180:183]
ds_read_b64_tr_b16 v[80:81],v4 offset:8192
ds_read_b64_tr_b16 v[82:83],v4 offset:10240
v_mfma_f32_16x16x32_bf16 v[188:191],v[100:103],v[136:139],v[188:191]
v_exp_f16_sdwa v120,v121 dst_sel:WORD_1 dst_unused:UNUSED_PRESERVE src0_sel:WORD_1
v_mfma_f32_16x16x32_bf16 v[196:199],v[104:107],v[136:139],v[196:199]
ds_read_u16_d16_hi v150,v150
ds_read_u16 v149,v149
v_lshlrev_b32_sdwa v154,1,v154 dst_sel:DWORD dst_unused:UNUSED_PAD src0_sel:DWORD src1_sel:WORD_1
v_mfma_f32_16x16x32_bf16 v[204:207],v[108:111],v[136:139],v[204:207]
s_waitcnt lgkmcnt(15)
v_pk_fma_f16 v120,v120,s55,s55
v_lshlrev_b32_sdwa v153,1,v153 dst_sel:DWORD dst_unused:UNUSED_PAD src0_sel:DWORD src1_sel:WORD_1
v_mfma_f32_16x16x32_bf16 v[128:131],v[64:67],v[16:19],v[112:115]
ds_read_u16_d16_hi v151,v151
ds_read_b64_tr_b16 v[84:85],v5 offset:8192
v_rcp_f16 v121,v120
v_mfma_f32_16x16x32_bf16 v[136:139],v[64:67],v[20:23],v[112:115]
s_waitcnt lgkmcnt(14)
ds_read_b64_tr_b16 v[86:87],v5 offset:10240
v_lshlrev_b32_sdwa v155,1,v155 dst_sel:DWORD dst_unused:UNUSED_PAD src0_sel:DWORD src1_sel:WORD_1
v_mfma_f32_16x16x32_bf16 v[140:143],v[68:71],v[20:23],v[116:119]
ds_read_u16 v152,v152
v_rcp_f16_sdwa v121,v120 dst_sel:WORD_1 dst_unused:UNUSED_PRESERVE src0_sel:WORD_1
v_mfma_f32_16x16x32_bf16 v[132:135],v[68:71],v[16:19],v[116:119]
s_waitcnt lgkmcnt(12)
ds_read_u16_d16_hi v154,v154
v_lshlrev_b32_sdwa v156,1,v156 dst_sel:DWORD dst_unused:UNUSED_PAD src0_sel:DWORD src1_sel:WORD_1
v_mfma_f32_16x16x32_bf16 v[128:131],v[72:75],v[24:27],v[128:131]
v_fma_mix_f32 v157,v157,v121,0 op_sel_hi:[0,1,0]
v_mfma_f32_16x16x32_bf16 v[136:139],v[72:75],v[28:31],v[136:139]
s_waitcnt lgkmcnt(11)
ds_read_u16 v153,v153
v_fma_mix_f32 v159,v159,v121,0 op_sel:[0,1,0] op_sel_hi:[0,1,0]
v_mfma_f32_16x16x32_bf16 v[140:143],v[76:79],v[28:31],v[140:143]
v_mfma_f32_16x16x32_bf16 v[132:135],v[76:79],v[24:27],v[132:135]
s_waitcnt lgkmcnt(8)
ds_read_u16_d16_hi v155,v155
v_lshlrev_b32_sdwa v158,1,v158 dst_sel:DWORD dst_unused:UNUSED_PAD src0_sel:DWORD src1_sel:WORD_1
v_mfma_f32_16x16x32_bf16 v[128:131],v[80:83],v[32:35],v[128:131]
ds_read_u16 v156,v156
v_mfma_f32_16x16x32_bf16 v[136:139],v[80:83],v[36:39],v[136:139]
s_waitcnt lgkmcnt(5)
v_lshl_add_u64 v[144:145],v[144:145],0,v[146:147]
v_mfma_f32_16x16x32_bf16 v[140:143],v[84:87],v[36:39],v[140:143]
ds_read_u16_d16_hi v158,v158
v_lshl_add_u64 v[146:147],v[148:149],0,v[150:151]
v_mfma_f32_16x16x32_bf16 v[132:135],v[84:87],v[32:35],v[132:135]
v_lshlrev_b32_sdwa v128,1,v128 dst_sel:DWORD dst_unused:UNUSED_PAD src0_sel:DWORD src1_sel:WORD_1
v_lshlrev_b32_sdwa v130,1,v130 dst_sel:DWORD dst_unused:UNUSED_PAD src0_sel:DWORD src1_sel:WORD_1
v_mfma_f32_16x16x32_bf16 v[208:211],v[88:91],v[144:147],v[208:211]
ds_read_u16 v128,v128
v_lshlrev_b32_sdwa v129,1,v129 dst_sel:DWORD dst_unused:UNUSED_PAD src0_sel:DWORD src1_sel:WORD_1
v_lshlrev_b32_sdwa v131,1,v131 dst_sel:DWORD dst_unused:UNUSED_PAD src0_sel:DWORD src1_sel:WORD_1
v_mfma_f32_16x16x32_bf16 v[216:219],v[92:95],v[144:147],v[216:219]
v_fma_mixlo_f16 v120,v141,s99,0
v_lshlrev_b32_sdwa v132,1,v132 dst_sel:DWORD dst_unused:UNUSED_PAD src0_sel:DWORD src1_sel:WORD_1
v_mfma_f32_16x16x32_bf16 v[224:227],v[96:99],v[144:147],v[224:227]
ds_read_u16_d16_hi v130,v130
v_fma_mixhi_f16 v120,v143,s99,0
v_lshlrev_b32_sdwa v134,1,v134 dst_sel:DWORD dst_unused:UNUSED_PAD src0_sel:DWORD src1_sel:WORD_1
v_mfma_f32_16x16x32_bf16 v[232:235],v[100:103],v[144:147],v[232:235]
s_waitcnt lgkmcnt(4)
ds_read_u16 v129,v129
v_lshl_add_u64 v[152:153],v[152:153],0,v[154:155]
v_mfma_f32_16x16x32_bf16 v[240:243],v[104:107],v[144:147],v[240:243]
s_waitcnt lgkmcnt(3)
ds_read_u16_d16_hi v131,v131
v_or_b32 v154,v158,v156
v_cvt_pk_bf16_f32 v155,v157,v159
v_mfma_f32_16x16x32_bf16 v[248:251],v[108:111],v[144:147],v[248:251]
v_pk_fma_f16 v121,v120,v120,s54
v_lshlrev_b32_sdwa v133,1,v133 dst_sel:DWORD dst_unused:UNUSED_PAD src0_sel:DWORD src1_sel:WORD_1
v_mfma_f32_16x16x32_bf16 v[212:215],v[88:91],v[152:155],v[212:215]
ds_read_u16 v132,v132
ds_read_b64_tr_b16 v[88:89],v6 offset:6160
v_pk_mul_f16 v121,v120,v121
v_lshlrev_b32_sdwa v135,1,v135 dst_sel:DWORD dst_unused:UNUSED_PAD src0_sel:DWORD src1_sel:WORD_1
v_mfma_f32_16x16x32_bf16 v[220:223],v[92:95],v[152:155],v[220:223]
ds_read_u16_d16_hi v134,v134
v_exp_f16 v120,v121
v_mfma_f32_16x16x32_bf16 v[228:231],v[96:99],v[152:155],v[228:231]
ds_read_b64_tr_b16 v[90:91],v6 offset:7696
ds_read_b64_tr_b16 v[92:93],v6 offset:6168
v_lshlrev_b32_sdwa v136,1,v136 dst_sel:DWORD dst_unused:UNUSED_PAD src0_sel:DWORD src1_sel:WORD_1
v_mfma_f32_16x16x32_bf16 v[236:239],v[100:103],v[152:155],v[236:239]
ds_read_u16 v133,v133
v_exp_f16_sdwa v120,v121 dst_sel:WORD_1 dst_unused:UNUSED_PRESERVE src0_sel:WORD_1
v_mfma_f32_16x16x32_bf16 v[244:247],v[104:107],v[152:155],v[244:247]
v_lshlrev_b32_sdwa v138,1,v138 dst_sel:DWORD dst_unused:UNUSED_PAD src0_sel:DWORD src1_sel:WORD_1
v_mfma_f32_16x16x32_bf16 v[252:255],v[108:111],v[152:155],v[252:255]
ds_read_b64_tr_b16 v[94:95],v6 offset:7704
ds_read_b64_tr_b16 v[96:97],v6 offset:6224
v_pk_fma_f16 v120,v120,s55,s55
v_lshlrev_b32_sdwa v137,1,v137 dst_sel:DWORD dst_unused:UNUSED_PAD src0_sel:DWORD src1_sel:WORD_1
v_mfma_f32_16x16x32_bf16 v[144:147],v[64:67],v[40:43],v[112:115]
ds_read_u16_d16_hi v135,v135
v_rcp_f16 v121,v120
v_mfma_f32_16x16x32_bf16 v[152:155],v[64:67],v[44:47],v[112:115]
ds_read_b64_tr_b16 v[98:99],v6 offset:7760
ds_read_b64_tr_b16 v[100:101],v6 offset:6232
v_lshlrev_b32_sdwa v139,1,v139 dst_sel:DWORD dst_unused:UNUSED_PAD src0_sel:DWORD src1_sel:WORD_1
v_mfma_f32_16x16x32_bf16 v[156:159],v[68:71],v[44:47],v[116:119]
ds_read_u16 v136,v136
v_rcp_f16_sdwa v121,v120 dst_sel:WORD_1 dst_unused:UNUSED_PRESERVE src0_sel:WORD_1
v_mfma_f32_16x16x32_bf16 v[148:151],v[68:71],v[40:43],v[116:119]
ds_read_u16_d16_hi v138,v138
v_lshlrev_b32_sdwa v140,1,v140 dst_sel:DWORD dst_unused:UNUSED_PAD src0_sel:DWORD src1_sel:WORD_1
v_mfma_f32_16x16x32_bf16 v[144:147],v[72:75],v[48:51],v[144:147]
ds_read_b64_tr_b16 v[102:103],v6 offset:7768
ds_read_b64_tr_b16 v[104:105],v6 offset:6288
v_fma_mix_f32 v141,v141,v121,0 op_sel_hi:[0,1,0]
v_lshlrev_b32_sdwa v142,1,v142 dst_sel:DWORD dst_unused:UNUSED_PAD src0_sel:DWORD src1_sel:WORD_1
v_mfma_f32_16x16x32_bf16 v[152:155],v[72:75],v[52:55],v[152:155]
ds_read_u16 v137,v137
v_fma_mix_f32 v143,v143,v121,0 op_sel:[0,1,0] op_sel_hi:[0,1,0]
v_mfma_f32_16x16x32_bf16 v[156:159],v[76:79],v[52:55],v[156:159]
ds_read_b64_tr_b16 v[106:107],v6 offset:7824
ds_read_b64_tr_b16 v[108:109],v6 offset:6296
v_mfma_f32_16x16x32_bf16 v[148:151],v[76:79],v[48:51],v[148:151]
ds_read_u16_d16_hi v139,v139
v_mfma_f32_16x16x32_bf16 v[144:147],v[80:83],v[56:59],v[144:147]
ds_read_b64_tr_b16 v[110:111],v6 offset:7832
ds_read_b64_tr_b16 v[64:65],v4 offset:512
v_mfma_f32_16x16x32_bf16 v[152:155],v[80:83],v[60:63],v[152:155]
s_waitcnt lgkmcnt(12)
ds_read_u16 v140,v140
v_lshl_add_u64 v[128:129],v[128:129],0,v[130:131]
v_lshl_add_u64 v[130:131],v[132:133],0,v[134:135]
v_mfma_f32_16x16x32_bf16 v[156:159],v[84:87],v[60:63],v[156:159]
ds_read_u16_d16_hi v142,v142
v_mfma_f32_16x16x32_bf16 v[148:151],v[84:87],v[56:59],v[148:151]
ds_read_b64_tr_b16 v[66:67],v4 offset:2560
ds_read_b128 v[112:115],v7 offset:384
v_lshlrev_b32_sdwa v144,1,v144 dst_sel:DWORD dst_unused:UNUSED_PAD src0_sel:DWORD src1_sel:WORD_1
v_lshlrev_b32_sdwa v146,1,v146 dst_sel:DWORD dst_unused:UNUSED_PAD src0_sel:DWORD src1_sel:WORD_1
v_mfma_f32_16x16x32_bf16 v[160:163],v[88:91],v[128:131],v[160:163]
ds_read_u16 v144,v144
v_lshlrev_b32_sdwa v145,1,v145 dst_sel:DWORD dst_unused:UNUSED_PAD src0_sel:DWORD src1_sel:WORD_1
v_lshlrev_b32_sdwa v147,1,v147 dst_sel:DWORD dst_unused:UNUSED_PAD src0_sel:DWORD src1_sel:WORD_1
v_mfma_f32_16x16x32_bf16 v[168:171],v[92:95],v[128:131],v[168:171]
s_waitcnt lgkmcnt(15)
ds_read_b64_tr_b16 v[68:69],v5 offset:512
ds_read_b64_tr_b16 v[70:71],v5 offset:2560
v_fma_mixlo_f16 v120,v157,s99,0
v_mfma_f32_16x16x32_bf16 v[176:179],v[96:99],v[128:131],v[176:179]
s_waitcnt lgkmcnt(9)
ds_read_u16_d16_hi v146,v146
v_lshlrev_b32_sdwa v148,1,v148 dst_sel:DWORD dst_unused:UNUSED_PAD src0_sel:DWORD src1_sel:WORD_1
v_lshl_add_u64 v[136:137],v[136:137],0,v[138:139]
v_fma_mixhi_f16 v120,v159,s99,0
v_lshlrev_b32_sdwa v150,1,v150 dst_sel:DWORD dst_unused:UNUSED_PAD src0_sel:DWORD src1_sel:WORD_1
v_mfma_f32_16x16x32_bf16 v[184:187],v[100:103],v[128:131],v[184:187]
ds_read_u16 v145,v145
v_mfma_f32_16x16x32_bf16 v[192:195],v[104:107],v[128:131],v[192:195]
s_waitcnt lgkmcnt(7)
ds_read_b128 v[116:119],v7 offset:416
ds_read_b64_tr_b16 v[72:73],v4 offset:4608
v_or_b32 v138,v142,v140
v_cvt_pk_bf16_f32 v139,v141,v143
v_mfma_f32_16x16x32_bf16 v[200:203],v[108:111],v[128:131],v[200:203]
ds_read_b64_tr_b16 v[74:75],v4 offset:6656
ds_read_b64_tr_b16 v[76:77],v5 offset:4608
v_pk_fma_f16 v121,v120,v120,s54
v_lshlrev_b32_sdwa v149,1,v149 dst_sel:DWORD dst_unused:UNUSED_PAD src0_sel:DWORD src1_sel:WORD_1
v_mfma_f32_16x16x32_bf16 v[164:167],v[88:91],v[136:139],v[164:167]
ds_read_u16_d16_hi v147,v147
v_pk_mul_f16 v121,v120,v121
v_lshlrev_b32_sdwa v151,1,v151 dst_sel:DWORD dst_unused:UNUSED_PAD src0_sel:DWORD src1_sel:WORD_1
v_mfma_f32_16x16x32_bf16 v[172:175],v[92:95],v[136:139],v[172:175]
ds_read_u16 v148,v148
v_lshlrev_b32_sdwa v152,1,v152 dst_sel:DWORD dst_unused:UNUSED_PAD src0_sel:DWORD src1_sel:WORD_1
v_exp_f16 v120,v121
v_mfma_f32_16x16x32_bf16 v[180:183],v[96:99],v[136:139],v[180:183]
ds_read_b64_tr_b16 v[78:79],v5 offset:6656
ds_read_b64_tr_b16 v[80:81],v4 offset:8704
v_mfma_f32_16x16x32_bf16 v[188:191],v[100:103],v[136:139],v[188:191]
v_exp_f16_sdwa v120,v121 dst_sel:WORD_1 dst_unused:UNUSED_PRESERVE src0_sel:WORD_1
v_mfma_f32_16x16x32_bf16 v[196:199],v[104:107],v[136:139],v[196:199]
ds_read_u16_d16_hi v150,v150
ds_read_u16 v149,v149
v_lshlrev_b32_sdwa v154,1,v154 dst_sel:DWORD dst_unused:UNUSED_PAD src0_sel:DWORD src1_sel:WORD_1
v_mfma_f32_16x16x32_bf16 v[204:207],v[108:111],v[136:139],v[204:207]
s_waitcnt lgkmcnt(15)
v_pk_fma_f16 v120,v120,s55,s55
v_lshlrev_b32_sdwa v153,1,v153 dst_sel:DWORD dst_unused:UNUSED_PAD src0_sel:DWORD src1_sel:WORD_1
v_mfma_f32_16x16x32_bf16 v[128:131],v[64:67],v[16:19],v[112:115]
ds_read_u16_d16_hi v151,v151
ds_read_b64_tr_b16 v[82:83],v4 offset:10752
v_rcp_f16 v121,v120
v_mfma_f32_16x16x32_bf16 v[136:139],v[64:67],v[20:23],v[112:115]
s_waitcnt lgkmcnt(11)
ds_read_b64_tr_b16 v[84:85],v5 offset:8704
v_lshlrev_b32_sdwa v155,1,v155 dst_sel:DWORD dst_unused:UNUSED_PAD src0_sel:DWORD src1_sel:WORD_1
v_mfma_f32_16x16x32_bf16 v[140:143],v[68:71],v[20:23],v[116:119]
ds_read_u16 v152,v152
v_rcp_f16_sdwa v121,v120 dst_sel:WORD_1 dst_unused:UNUSED_PRESERVE src0_sel:WORD_1
v_mfma_f32_16x16x32_bf16 v[132:135],v[68:71],v[16:19],v[116:119]
s_waitcnt lgkmcnt(11)
ds_read_u16_d16_hi v154,v154
v_lshlrev_b32_sdwa v156,1,v156 dst_sel:DWORD dst_unused:UNUSED_PAD src0_sel:DWORD src1_sel:WORD_1
v_mfma_f32_16x16x32_bf16 v[128:131],v[72:75],v[24:27],v[128:131]
ds_read_b64_tr_b16 v[86:87],v5 offset:10752
v_fma_mix_f32 v157,v157,v121,0 op_sel_hi:[0,1,0]
v_mfma_f32_16x16x32_bf16 v[136:139],v[72:75],v[28:31],v[136:139]
s_waitcnt lgkmcnt(9)
ds_read_u16 v153,v153
v_fma_mix_f32 v159,v159,v121,0 op_sel:[0,1,0] op_sel_hi:[0,1,0]
v_mfma_f32_16x16x32_bf16 v[140:143],v[76:79],v[28:31],v[140:143]
s_mov_b32 s86,0xbe801d5a
v_mfma_f32_16x16x32_bf16 v[132:135],v[76:79],v[24:27],v[132:135]
s_waitcnt lgkmcnt(5)
ds_read_u16_d16_hi v155,v155
v_lshlrev_b32_sdwa v158,1,v158 dst_sel:DWORD dst_unused:UNUSED_PAD src0_sel:DWORD src1_sel:WORD_1
v_mfma_f32_16x16x32_bf16 v[128:131],v[80:83],v[32:35],v[128:131]
ds_read_u16 v156,v156
v_mfma_f32_16x16x32_bf16 v[136:139],v[80:83],v[36:39],v[136:139]
s_waitcnt lgkmcnt(3)
v_lshl_add_u64 v[144:145],v[144:145],0,v[146:147]
v_mfma_f32_16x16x32_bf16 v[140:143],v[84:87],v[36:39],v[140:143]
ds_read_u16_d16_hi v158,v158
v_lshl_add_u64 v[146:147],v[148:149],0,v[150:151]
v_mfma_f32_16x16x32_bf16 v[132:135],v[84:87],v[32:35],v[132:135]
v_lshlrev_b32_sdwa v128,1,v128 dst_sel:DWORD dst_unused:UNUSED_PAD src0_sel:DWORD src1_sel:WORD_1
v_lshlrev_b32_sdwa v130,1,v130 dst_sel:DWORD dst_unused:UNUSED_PAD src0_sel:DWORD src1_sel:WORD_1
v_mfma_f32_16x16x32_bf16 v[208:211],v[88:91],v[144:147],v[208:211]
s_barrier
ds_read_u16 v128,v128
v_lshlrev_b32_sdwa v129,1,v129 dst_sel:DWORD dst_unused:UNUSED_PAD src0_sel:DWORD src1_sel:WORD_1
v_lshlrev_b32_sdwa v131,1,v131 dst_sel:DWORD dst_unused:UNUSED_PAD src0_sel:DWORD src1_sel:WORD_1
v_mfma_f32_16x16x32_bf16 v[216:219],v[92:95],v[144:147],v[216:219]
v_fma_mixlo_f16 v120,v141,s99,0
v_lshlrev_b32_sdwa v132,1,v132 dst_sel:DWORD dst_unused:UNUSED_PAD src0_sel:DWORD src1_sel:WORD_1
v_mfma_f32_16x16x32_bf16 v[224:227],v[96:99],v[144:147],v[224:227]
ds_read_u16_d16_hi v130,v130
v_fma_mixhi_f16 v120,v143,s99,0
v_lshlrev_b32_sdwa v134,1,v134 dst_sel:DWORD dst_unused:UNUSED_PAD src0_sel:DWORD src1_sel:WORD_1
v_mfma_f32_16x16x32_bf16 v[232:235],v[100:103],v[144:147],v[232:235]
s_waitcnt lgkmcnt(4)
ds_read_u16 v129,v129
v_lshl_add_u64 v[152:153],v[152:153],0,v[154:155]
v_mfma_f32_16x16x32_bf16 v[240:243],v[104:107],v[144:147],v[240:243]
s_waitcnt lgkmcnt(3)
ds_read_u16_d16_hi v131,v131
v_or_b32 v154,v158,v156
v_cvt_pk_bf16_f32 v155,v157,v159
v_mfma_f32_16x16x32_bf16 v[248:251],v[108:111],v[144:147],v[248:251]
v_pk_fma_f16 v121,v120,v120,s54
v_lshlrev_b32_sdwa v133,1,v133 dst_sel:DWORD dst_unused:UNUSED_PAD src0_sel:DWORD src1_sel:WORD_1
v_mfma_f32_16x16x32_bf16 v[212:215],v[88:91],v[152:155],v[212:215]
ds_read_u16 v132,v132
ds_read_b64_tr_b16 v[88:89],v6 offset:12320
v_pk_mul_f16 v121,v120,v121
v_lshlrev_b32_sdwa v135,1,v135 dst_sel:DWORD dst_unused:UNUSED_PAD src0_sel:DWORD src1_sel:WORD_1
v_mfma_f32_16x16x32_bf16 v[220:223],v[92:95],v[152:155],v[220:223]
ds_read_u16_d16_hi v134,v134
v_exp_f16 v120,v121
v_mfma_f32_16x16x32_bf16 v[228:231],v[96:99],v[152:155],v[228:231]
ds_read_b64_tr_b16 v[90:91],v6 offset:13856
ds_read_b64_tr_b16 v[92:93],v6 offset:12328
v_lshlrev_b32_sdwa v136,1,v136 dst_sel:DWORD dst_unused:UNUSED_PAD src0_sel:DWORD src1_sel:WORD_1
v_mfma_f32_16x16x32_bf16 v[236:239],v[100:103],v[152:155],v[236:239]
ds_read_u16 v133,v133
v_exp_f16_sdwa v120,v121 dst_sel:WORD_1 dst_unused:UNUSED_PRESERVE src0_sel:WORD_1
v_mfma_f32_16x16x32_bf16 v[244:247],v[104:107],v[152:155],v[244:247]
v_lshlrev_b32_sdwa v138,1,v138 dst_sel:DWORD dst_unused:UNUSED_PAD src0_sel:DWORD src1_sel:WORD_1
v_mfma_f32_16x16x32_bf16 v[252:255],v[108:111],v[152:155],v[252:255]
ds_read_b64_tr_b16 v[94:95],v6 offset:13864
ds_read_b64_tr_b16 v[96:97],v6 offset:12384
v_pk_fma_f16 v120,v120,s55,s55
v_lshlrev_b32_sdwa v137,1,v137 dst_sel:DWORD dst_unused:UNUSED_PAD src0_sel:DWORD src1_sel:WORD_1
v_mfma_f32_16x16x32_bf16 v[144:147],v[64:67],v[40:43],v[112:115]
ds_read_u16_d16_hi v135,v135
v_rcp_f16 v121,v120
v_mfma_f32_16x16x32_bf16 v[152:155],v[64:67],v[44:47],v[112:115]
ds_read_b64_tr_b16 v[98:99],v6 offset:13920
ds_read_b64_tr_b16 v[100:101],v6 offset:12392
v_lshlrev_b32_sdwa v139,1,v139 dst_sel:DWORD dst_unused:UNUSED_PAD src0_sel:DWORD src1_sel:WORD_1
v_mfma_f32_16x16x32_bf16 v[156:159],v[68:71],v[44:47],v[116:119]
ds_read_u16 v136,v136
v_rcp_f16_sdwa v121,v120 dst_sel:WORD_1 dst_unused:UNUSED_PRESERVE src0_sel:WORD_1
v_mfma_f32_16x16x32_bf16 v[148:151],v[68:71],v[40:43],v[116:119]
ds_read_u16_d16_hi v138,v138
v_lshlrev_b32_sdwa v140,1,v140 dst_sel:DWORD dst_unused:UNUSED_PAD src0_sel:DWORD src1_sel:WORD_1
v_mfma_f32_16x16x32_bf16 v[144:147],v[72:75],v[48:51],v[144:147]
ds_read_b64_tr_b16 v[102:103],v6 offset:13928
ds_read_b64_tr_b16 v[104:105],v6 offset:12448
v_fma_mix_f32 v141,v141,v121,0 op_sel_hi:[0,1,0]
v_lshlrev_b32_sdwa v142,1,v142 dst_sel:DWORD dst_unused:UNUSED_PAD src0_sel:DWORD src1_sel:WORD_1
v_mfma_f32_16x16x32_bf16 v[152:155],v[72:75],v[52:55],v[152:155]
ds_read_u16 v137,v137
v_fma_mix_f32 v143,v143,v121,0 op_sel:[0,1,0] op_sel_hi:[0,1,0]
v_mfma_f32_16x16x32_bf16 v[156:159],v[76:79],v[52:55],v[156:159]
ds_read_b64_tr_b16 v[106:107],v6 offset:13984
ds_read_b64_tr_b16 v[108:109],v6 offset:12456
v_mfma_f32_16x16x32_bf16 v[148:151],v[76:79],v[48:51],v[148:151]
ds_read_u16_d16_hi v139,v139
v_mfma_f32_16x16x32_bf16 v[144:147],v[80:83],v[56:59],v[144:147]
ds_read_b64_tr_b16 v[110:111],v6 offset:13992
ds_read_b64_tr_b16 v[64:65],v4 offset:12320
v_mfma_f32_16x16x32_bf16 v[152:155],v[80:83],v[60:63],v[152:155]
s_waitcnt lgkmcnt(12)
ds_read_u16 v140,v140
v_lshl_add_u64 v[128:129],v[128:129],0,v[130:131]
v_lshl_add_u64 v[130:131],v[132:133],0,v[134:135]
v_mfma_f32_16x16x32_bf16 v[156:159],v[84:87],v[60:63],v[156:159]
ds_read_u16_d16_hi v142,v142
v_mfma_f32_16x16x32_bf16 v[148:151],v[84:87],v[56:59],v[148:151]
ds_read_b64_tr_b16 v[66:67],v4 offset:14368
ds_read_b128 v[112:115],v7 offset:512
v_lshlrev_b32_sdwa v144,1,v144 dst_sel:DWORD dst_unused:UNUSED_PAD src0_sel:DWORD src1_sel:WORD_1
v_lshlrev_b32_sdwa v146,1,v146 dst_sel:DWORD dst_unused:UNUSED_PAD src0_sel:DWORD src1_sel:WORD_1
v_mfma_f32_16x16x32_bf16 v[160:163],v[88:91],v[128:131],v[160:163]
ds_read_u16 v144,v144
v_lshlrev_b32_sdwa v145,1,v145 dst_sel:DWORD dst_unused:UNUSED_PAD src0_sel:DWORD src1_sel:WORD_1
v_lshlrev_b32_sdwa v147,1,v147 dst_sel:DWORD dst_unused:UNUSED_PAD src0_sel:DWORD src1_sel:WORD_1
v_mfma_f32_16x16x32_bf16 v[168:171],v[92:95],v[128:131],v[168:171]
s_waitcnt lgkmcnt(15)
ds_read_b64_tr_b16 v[68:69],v5 offset:12320
ds_read_b64_tr_b16 v[70:71],v5 offset:14368
v_fma_mixlo_f16 v120,v157,s99,0
v_mfma_f32_16x16x32_bf16 v[176:179],v[96:99],v[128:131],v[176:179]
s_waitcnt lgkmcnt(9)
ds_read_u16_d16_hi v146,v146
v_lshlrev_b32_sdwa v148,1,v148 dst_sel:DWORD dst_unused:UNUSED_PAD src0_sel:DWORD src1_sel:WORD_1
v_lshl_add_u64 v[136:137],v[136:137],0,v[138:139]
v_fma_mixhi_f16 v120,v159,s99,0
v_lshlrev_b32_sdwa v150,1,v150 dst_sel:DWORD dst_unused:UNUSED_PAD src0_sel:DWORD src1_sel:WORD_1
v_mfma_f32_16x16x32_bf16 v[184:187],v[100:103],v[128:131],v[184:187]
ds_read_u16 v145,v145
v_mfma_f32_16x16x32_bf16 v[192:195],v[104:107],v[128:131],v[192:195]
s_waitcnt lgkmcnt(7)
ds_read_b128 v[116:119],v7 offset:544
ds_read_b64_tr_b16 v[72:73],v4 offset:16416
v_or_b32 v138,v142,v140
v_cvt_pk_bf16_f32 v139,v141,v143
v_mfma_f32_16x16x32_bf16 v[200:203],v[108:111],v[128:131],v[200:203]
ds_read_b64_tr_b16 v[74:75],v4 offset:18464
ds_read_b64_tr_b16 v[76:77],v5 offset:16416
v_pk_fma_f16 v121,v120,v120,s54
v_lshlrev_b32_sdwa v149,1,v149 dst_sel:DWORD dst_unused:UNUSED_PAD src0_sel:DWORD src1_sel:WORD_1
v_mfma_f32_16x16x32_bf16 v[164:167],v[88:91],v[136:139],v[164:167]
ds_read_u16_d16_hi v147,v147
v_pk_mul_f16 v121,v120,v121
v_lshlrev_b32_sdwa v151,1,v151 dst_sel:DWORD dst_unused:UNUSED_PAD src0_sel:DWORD src1_sel:WORD_1
v_mfma_f32_16x16x32_bf16 v[172:175],v[92:95],v[136:139],v[172:175]
ds_read_u16 v148,v148
v_lshlrev_b32_sdwa v152,1,v152 dst_sel:DWORD dst_unused:UNUSED_PAD src0_sel:DWORD src1_sel:WORD_1
v_exp_f16 v120,v121
v_mfma_f32_16x16x32_bf16 v[180:183],v[96:99],v[136:139],v[180:183]
ds_read_b64_tr_b16 v[78:79],v5 offset:18464
ds_read_b64_tr_b16 v[80:81],v4 offset:20512
v_mfma_f32_16x16x32_bf16 v[188:191],v[100:103],v[136:139],v[188:191]
v_exp_f16_sdwa v120,v121 dst_sel:WORD_1 dst_unused:UNUSED_PRESERVE src0_sel:WORD_1
v_mfma_f32_16x16x32_bf16 v[196:199],v[104:107],v[136:139],v[196:199]
ds_read_u16_d16_hi v150,v150
ds_read_u16 v149,v149
v_lshlrev_b32_sdwa v154,1,v154 dst_sel:DWORD dst_unused:UNUSED_PAD src0_sel:DWORD src1_sel:WORD_1
v_mfma_f32_16x16x32_bf16 v[204:207],v[108:111],v[136:139],v[204:207]
s_waitcnt lgkmcnt(15)
v_pk_fma_f16 v120,v120,s55,s55
v_lshlrev_b32_sdwa v153,1,v153 dst_sel:DWORD dst_unused:UNUSED_PAD src0_sel:DWORD src1_sel:WORD_1
v_mfma_f32_16x16x32_bf16 v[128:131],v[64:67],v[16:19],v[112:115]
ds_read_u16_d16_hi v151,v151
ds_read_b64_tr_b16 v[82:83],v4 offset:22560
v_rcp_f16 v121,v120
v_mfma_f32_16x16x32_bf16 v[136:139],v[64:67],v[20:23],v[112:115]
s_waitcnt lgkmcnt(11)
ds_read_b64_tr_b16 v[84:85],v5 offset:20512
v_lshlrev_b32_sdwa v155,1,v155 dst_sel:DWORD dst_unused:UNUSED_PAD src0_sel:DWORD src1_sel:WORD_1
v_mfma_f32_16x16x32_bf16 v[140:143],v[68:71],v[20:23],v[116:119]
ds_read_u16 v152,v152
v_rcp_f16_sdwa v121,v120 dst_sel:WORD_1 dst_unused:UNUSED_PRESERVE src0_sel:WORD_1
v_mfma_f32_16x16x32_bf16 v[132:135],v[68:71],v[16:19],v[116:119]
s_waitcnt lgkmcnt(11)
ds_read_u16_d16_hi v154,v154
v_lshlrev_b32_sdwa v156,1,v156 dst_sel:DWORD dst_unused:UNUSED_PAD src0_sel:DWORD src1_sel:WORD_1
v_mfma_f32_16x16x32_bf16 v[128:131],v[72:75],v[24:27],v[128:131]
ds_read_b64_tr_b16 v[86:87],v5 offset:22560
v_fma_mix_f32 v157,v157,v121,0 op_sel_hi:[0,1,0]
v_mfma_f32_16x16x32_bf16 v[136:139],v[72:75],v[28:31],v[136:139]
s_waitcnt lgkmcnt(9)
ds_read_u16 v153,v153
v_fma_mix_f32 v159,v159,v121,0 op_sel:[0,1,0] op_sel_hi:[0,1,0]
v_mfma_f32_16x16x32_bf16 v[140:143],v[76:79],v[28:31],v[140:143]
v_mfma_f32_16x16x32_bf16 v[132:135],v[76:79],v[24:27],v[132:135]
s_waitcnt lgkmcnt(5)
ds_read_u16_d16_hi v155,v155
v_lshlrev_b32_sdwa v158,1,v158 dst_sel:DWORD dst_unused:UNUSED_PAD src0_sel:DWORD src1_sel:WORD_1
v_mfma_f32_16x16x32_bf16 v[128:131],v[80:83],v[32:35],v[128:131]
ds_read_u16 v156,v156
v_mfma_f32_16x16x32_bf16 v[136:139],v[80:83],v[36:39],v[136:139]
s_waitcnt lgkmcnt(3)
v_lshl_add_u64 v[144:145],v[144:145],0,v[146:147]
v_mfma_f32_16x16x32_bf16 v[140:143],v[84:87],v[36:39],v[140:143]
ds_read_u16_d16_hi v158,v158
v_lshl_add_u64 v[146:147],v[148:149],0,v[150:151]
v_mfma_f32_16x16x32_bf16 v[132:135],v[84:87],v[32:35],v[132:135]
v_lshlrev_b32_sdwa v128,1,v128 dst_sel:DWORD dst_unused:UNUSED_PAD src0_sel:DWORD src1_sel:WORD_1
v_lshlrev_b32_sdwa v130,1,v130 dst_sel:DWORD dst_unused:UNUSED_PAD src0_sel:DWORD src1_sel:WORD_1
v_mfma_f32_16x16x32_bf16 v[208:211],v[88:91],v[144:147],v[208:211]
ds_read_u16 v128,v128
v_lshlrev_b32_sdwa v129,1,v129 dst_sel:DWORD dst_unused:UNUSED_PAD src0_sel:DWORD src1_sel:WORD_1
v_lshlrev_b32_sdwa v131,1,v131 dst_sel:DWORD dst_unused:UNUSED_PAD src0_sel:DWORD src1_sel:WORD_1
v_mfma_f32_16x16x32_bf16 v[216:219],v[92:95],v[144:147],v[216:219]
v_fma_mixlo_f16 v120,v141,s99,0
v_lshlrev_b32_sdwa v132,1,v132 dst_sel:DWORD dst_unused:UNUSED_PAD src0_sel:DWORD src1_sel:WORD_1
v_mfma_f32_16x16x32_bf16 v[224:227],v[96:99],v[144:147],v[224:227]
ds_read_u16_d16_hi v130,v130
v_fma_mixhi_f16 v120,v143,s99,0
v_lshlrev_b32_sdwa v134,1,v134 dst_sel:DWORD dst_unused:UNUSED_PAD src0_sel:DWORD src1_sel:WORD_1
v_mfma_f32_16x16x32_bf16 v[232:235],v[100:103],v[144:147],v[232:235]
s_waitcnt lgkmcnt(4)
ds_read_u16 v129,v129
v_lshl_add_u64 v[152:153],v[152:153],0,v[154:155]
v_mfma_f32_16x16x32_bf16 v[240:243],v[104:107],v[144:147],v[240:243]
s_waitcnt lgkmcnt(3)
ds_read_u16_d16_hi v131,v131
v_or_b32 v154,v158,v156
v_cvt_pk_bf16_f32 v155,v157,v159
v_mfma_f32_16x16x32_bf16 v[248:251],v[108:111],v[144:147],v[248:251]
v_pk_fma_f16 v121,v120,v120,s54
v_lshlrev_b32_sdwa v133,1,v133 dst_sel:DWORD dst_unused:UNUSED_PAD src0_sel:DWORD src1_sel:WORD_1
v_mfma_f32_16x16x32_bf16 v[212:215],v[88:91],v[152:155],v[212:215]
ds_read_u16 v132,v132
ds_read_b64_tr_b16 v[88:89],v6 offset:18480
v_pk_mul_f16 v121,v120,v121
v_lshlrev_b32_sdwa v135,1,v135 dst_sel:DWORD dst_unused:UNUSED_PAD src0_sel:DWORD src1_sel:WORD_1
v_mfma_f32_16x16x32_bf16 v[220:223],v[92:95],v[152:155],v[220:223]
ds_read_u16_d16_hi v134,v134
v_exp_f16 v120,v121
v_mfma_f32_16x16x32_bf16 v[228:231],v[96:99],v[152:155],v[228:231]
ds_read_b64_tr_b16 v[90:91],v6 offset:20016
ds_read_b64_tr_b16 v[92:93],v6 offset:18488
v_lshlrev_b32_sdwa v136,1,v136 dst_sel:DWORD dst_unused:UNUSED_PAD src0_sel:DWORD src1_sel:WORD_1
v_mfma_f32_16x16x32_bf16 v[236:239],v[100:103],v[152:155],v[236:239]
ds_read_u16 v133,v133
v_exp_f16_sdwa v120,v121 dst_sel:WORD_1 dst_unused:UNUSED_PRESERVE src0_sel:WORD_1
v_mfma_f32_16x16x32_bf16 v[244:247],v[104:107],v[152:155],v[244:247]
v_lshlrev_b32_sdwa v138,1,v138 dst_sel:DWORD dst_unused:UNUSED_PAD src0_sel:DWORD src1_sel:WORD_1
v_mfma_f32_16x16x32_bf16 v[252:255],v[108:111],v[152:155],v[252:255]
ds_read_b64_tr_b16 v[94:95],v6 offset:20024
ds_read_b64_tr_b16 v[96:97],v6 offset:18544
v_pk_fma_f16 v120,v120,s55,s55
v_lshlrev_b32_sdwa v137,1,v137 dst_sel:DWORD dst_unused:UNUSED_PAD src0_sel:DWORD src1_sel:WORD_1
v_mfma_f32_16x16x32_bf16 v[144:147],v[64:67],v[40:43],v[112:115]
ds_read_u16_d16_hi v135,v135
v_rcp_f16 v121,v120
v_mfma_f32_16x16x32_bf16 v[152:155],v[64:67],v[44:47],v[112:115]
ds_read_b64_tr_b16 v[98:99],v6 offset:20080
ds_read_b64_tr_b16 v[100:101],v6 offset:18552
v_lshlrev_b32_sdwa v139,1,v139 dst_sel:DWORD dst_unused:UNUSED_PAD src0_sel:DWORD src1_sel:WORD_1
v_mfma_f32_16x16x32_bf16 v[156:159],v[68:71],v[44:47],v[116:119]
ds_read_u16 v136,v136
v_rcp_f16_sdwa v121,v120 dst_sel:WORD_1 dst_unused:UNUSED_PRESERVE src0_sel:WORD_1
v_mfma_f32_16x16x32_bf16 v[148:151],v[68:71],v[40:43],v[116:119]
ds_read_u16_d16_hi v138,v138
v_lshlrev_b32_sdwa v140,1,v140 dst_sel:DWORD dst_unused:UNUSED_PAD src0_sel:DWORD src1_sel:WORD_1
v_mfma_f32_16x16x32_bf16 v[144:147],v[72:75],v[48:51],v[144:147]
ds_read_b64_tr_b16 v[102:103],v6 offset:20088
ds_read_b64_tr_b16 v[104:105],v6 offset:18608
v_fma_mix_f32 v141,v141,v121,0 op_sel_hi:[0,1,0]
v_lshlrev_b32_sdwa v142,1,v142 dst_sel:DWORD dst_unused:UNUSED_PAD src0_sel:DWORD src1_sel:WORD_1
v_mfma_f32_16x16x32_bf16 v[152:155],v[72:75],v[52:55],v[152:155]
ds_read_u16 v137,v137
v_fma_mix_f32 v143,v143,v121,0 op_sel:[0,1,0] op_sel_hi:[0,1,0]
v_mfma_f32_16x16x32_bf16 v[156:159],v[76:79],v[52:55],v[156:159]
ds_read_b64_tr_b16 v[106:107],v6 offset:20144
ds_read_b64_tr_b16 v[108:109],v6 offset:18616
v_mfma_f32_16x16x32_bf16 v[148:151],v[76:79],v[48:51],v[148:151]
ds_read_u16_d16_hi v139,v139
v_mfma_f32_16x16x32_bf16 v[144:147],v[80:83],v[56:59],v[144:147]
ds_read_b64_tr_b16 v[110:111],v6 offset:20152
ds_read_b64_tr_b16 v[64:65],v4 offset:12832
v_mfma_f32_16x16x32_bf16 v[152:155],v[80:83],v[60:63],v[152:155]
s_waitcnt lgkmcnt(12)
ds_read_u16 v140,v140
v_lshl_add_u64 v[128:129],v[128:129],0,v[130:131]
v_lshl_add_u64 v[130:131],v[132:133],0,v[134:135]
v_mfma_f32_16x16x32_bf16 v[156:159],v[84:87],v[60:63],v[156:159]
ds_read_u16_d16_hi v142,v142
v_mfma_f32_16x16x32_bf16 v[148:151],v[84:87],v[56:59],v[148:151]
ds_read_b64_tr_b16 v[66:67],v4 offset:14880
ds_read_b128 v[112:115],v7 offset:640
v_lshlrev_b32_sdwa v144,1,v144 dst_sel:DWORD dst_unused:UNUSED_PAD src0_sel:DWORD src1_sel:WORD_1
v_lshlrev_b32_sdwa v146,1,v146 dst_sel:DWORD dst_unused:UNUSED_PAD src0_sel:DWORD src1_sel:WORD_1
v_mfma_f32_16x16x32_bf16 v[160:163],v[88:91],v[128:131],v[160:163]
ds_read_u16 v144,v144
v_lshlrev_b32_sdwa v145,1,v145 dst_sel:DWORD dst_unused:UNUSED_PAD src0_sel:DWORD src1_sel:WORD_1
v_lshlrev_b32_sdwa v147,1,v147 dst_sel:DWORD dst_unused:UNUSED_PAD src0_sel:DWORD src1_sel:WORD_1
v_mfma_f32_16x16x32_bf16 v[168:171],v[92:95],v[128:131],v[168:171]
s_waitcnt lgkmcnt(15)
ds_read_b64_tr_b16 v[68:69],v5 offset:12832
ds_read_b64_tr_b16 v[70:71],v5 offset:14880
v_fma_mixlo_f16 v120,v157,s99,0
v_mfma_f32_16x16x32_bf16 v[176:179],v[96:99],v[128:131],v[176:179]
s_waitcnt lgkmcnt(9)
ds_read_u16_d16_hi v146,v146
v_lshlrev_b32_sdwa v148,1,v148 dst_sel:DWORD dst_unused:UNUSED_PAD src0_sel:DWORD src1_sel:WORD_1
v_lshl_add_u64 v[136:137],v[136:137],0,v[138:139]
v_fma_mixhi_f16 v120,v159,s99,0
v_lshlrev_b32_sdwa v150,1,v150 dst_sel:DWORD dst_unused:UNUSED_PAD src0_sel:DWORD src1_sel:WORD_1
v_mfma_f32_16x16x32_bf16 v[184:187],v[100:103],v[128:131],v[184:187]
ds_read_u16 v145,v145
v_mfma_f32_16x16x32_bf16 v[192:195],v[104:107],v[128:131],v[192:195]
s_waitcnt lgkmcnt(7)
ds_read_b128 v[116:119],v7 offset:672
ds_read_b64_tr_b16 v[72:73],v4 offset:16928
v_or_b32 v138,v142,v140
v_cvt_pk_bf16_f32 v139,v141,v143
v_mfma_f32_16x16x32_bf16 v[200:203],v[108:111],v[128:131],v[200:203]
ds_read_b64_tr_b16 v[74:75],v4 offset:18976
ds_read_b64_tr_b16 v[76:77],v5 offset:16928
v_pk_fma_f16 v121,v120,v120,s54
v_lshlrev_b32_sdwa v149,1,v149 dst_sel:DWORD dst_unused:UNUSED_PAD src0_sel:DWORD src1_sel:WORD_1
v_mfma_f32_16x16x32_bf16 v[164:167],v[88:91],v[136:139],v[164:167]
ds_read_u16_d16_hi v147,v147
v_pk_mul_f16 v121,v120,v121
v_lshlrev_b32_sdwa v151,1,v151 dst_sel:DWORD dst_unused:UNUSED_PAD src0_sel:DWORD src1_sel:WORD_1
v_mfma_f32_16x16x32_bf16 v[172:175],v[92:95],v[136:139],v[172:175]
ds_read_u16 v148,v148
v_lshlrev_b32_sdwa v152,1,v152 dst_sel:DWORD dst_unused:UNUSED_PAD src0_sel:DWORD src1_sel:WORD_1
v_exp_f16 v120,v121
v_mfma_f32_16x16x32_bf16 v[180:183],v[96:99],v[136:139],v[180:183]
ds_read_b64_tr_b16 v[78:79],v5 offset:18976
ds_read_b64_tr_b16 v[80:81],v4 offset:21024
v_mfma_f32_16x16x32_bf16 v[188:191],v[100:103],v[136:139],v[188:191]
v_exp_f16_sdwa v120,v121 dst_sel:WORD_1 dst_unused:UNUSED_PRESERVE src0_sel:WORD_1
v_mfma_f32_16x16x32_bf16 v[196:199],v[104:107],v[136:139],v[196:199]
ds_read_u16_d16_hi v150,v150
ds_read_u16 v149,v149
v_lshlrev_b32_sdwa v154,1,v154 dst_sel:DWORD dst_unused:UNUSED_PAD src0_sel:DWORD src1_sel:WORD_1
v_mfma_f32_16x16x32_bf16 v[204:207],v[108:111],v[136:139],v[204:207]
s_waitcnt lgkmcnt(15)
v_pk_fma_f16 v120,v120,s55,s55
v_lshlrev_b32_sdwa v153,1,v153 dst_sel:DWORD dst_unused:UNUSED_PAD src0_sel:DWORD src1_sel:WORD_1
v_mfma_f32_16x16x32_bf16 v[128:131],v[64:67],v[16:19],v[112:115]
ds_read_u16_d16_hi v151,v151
ds_read_b64_tr_b16 v[82:83],v4 offset:23072
v_rcp_f16 v121,v120
v_mfma_f32_16x16x32_bf16 v[136:139],v[64:67],v[20:23],v[112:115]
s_waitcnt lgkmcnt(11)
ds_read_b64_tr_b16 v[84:85],v5 offset:21024
v_lshlrev_b32_sdwa v155,1,v155 dst_sel:DWORD dst_unused:UNUSED_PAD src0_sel:DWORD src1_sel:WORD_1
v_mfma_f32_16x16x32_bf16 v[140:143],v[68:71],v[20:23],v[116:119]
ds_read_u16 v152,v152
v_rcp_f16_sdwa v121,v120 dst_sel:WORD_1 dst_unused:UNUSED_PRESERVE src0_sel:WORD_1
v_mfma_f32_16x16x32_bf16 v[132:135],v[68:71],v[16:19],v[116:119]
s_waitcnt lgkmcnt(11)
ds_read_u16_d16_hi v154,v154
v_lshlrev_b32_sdwa v156,1,v156 dst_sel:DWORD dst_unused:UNUSED_PAD src0_sel:DWORD src1_sel:WORD_1
v_mfma_f32_16x16x32_bf16 v[128:131],v[72:75],v[24:27],v[128:131]
ds_read_b64_tr_b16 v[86:87],v5 offset:23072
v_fma_mix_f32 v157,v157,v121,0 op_sel_hi:[0,1,0]
v_mfma_f32_16x16x32_bf16 v[136:139],v[72:75],v[28:31],v[136:139]
s_waitcnt lgkmcnt(9)
ds_read_u16 v153,v153
v_fma_mix_f32 v159,v159,v121,0 op_sel:[0,1,0] op_sel_hi:[0,1,0]
v_mfma_f32_16x16x32_bf16 v[140:143],v[76:79],v[28:31],v[140:143]
v_mfma_f32_16x16x32_bf16 v[132:135],v[76:79],v[24:27],v[132:135]
s_waitcnt lgkmcnt(5)
ds_read_u16_d16_hi v155,v155
v_lshlrev_b32_sdwa v158,1,v158 dst_sel:DWORD dst_unused:UNUSED_PAD src0_sel:DWORD src1_sel:WORD_1
v_mfma_f32_16x16x32_bf16 v[128:131],v[80:83],v[32:35],v[128:131]
ds_read_u16 v156,v156
v_mfma_f32_16x16x32_bf16 v[136:139],v[80:83],v[36:39],v[136:139]
s_waitcnt lgkmcnt(3)
v_lshl_add_u64 v[144:145],v[144:145],0,v[146:147]
v_mfma_f32_16x16x32_bf16 v[140:143],v[84:87],v[36:39],v[140:143]
ds_read_u16_d16_hi v158,v158
v_lshl_add_u64 v[146:147],v[148:149],0,v[150:151]
v_mfma_f32_16x16x32_bf16 v[132:135],v[84:87],v[32:35],v[132:135]
v_lshlrev_b32_sdwa v128,1,v128 dst_sel:DWORD dst_unused:UNUSED_PAD src0_sel:DWORD src1_sel:WORD_1
v_lshlrev_b32_sdwa v130,1,v130 dst_sel:DWORD dst_unused:UNUSED_PAD src0_sel:DWORD src1_sel:WORD_1
v_mfma_f32_16x16x32_bf16 v[208:211],v[88:91],v[144:147],v[208:211]
s_barrier
s_setprio 3
ds_read_u16 v128,v128
v_fma_mixlo_f16 v120,v140,s99,0
v_lshlrev_b32_sdwa v129,1,v129 dst_sel:DWORD dst_unused:UNUSED_PAD src0_sel:DWORD src1_sel:WORD_1
v_lshlrev_b32_sdwa v131,1,v131 dst_sel:DWORD dst_unused:UNUSED_PAD src0_sel:DWORD src1_sel:WORD_1
v_mfma_f32_16x16x32_bf16 v[216:219],v[92:95],v[144:147],v[216:219]
s_cbranch_vccnz RA1_%=
s_mov_b64 exec,s[62:63]
s_add_u32 m0,s57,48832
s_add_u32 s64,s56,3342336
buffer_load_dwordx4 v2,s[36:39],s64 offen lds
s_add_u32 m0,s57,50560
s_nop 0
buffer_load_dwordx4 v3,s[36:39],s64 offen lds
s_mov_b64 exec,-1
RA1_%=:
v_fma_mixlo_f16 v122,v141,s99,0
v_lshlrev_b32_sdwa v132,1,v132 dst_sel:DWORD dst_unused:UNUSED_PAD src0_sel:DWORD src1_sel:WORD_1
v_mfma_f32_16x16x32_bf16 v[224:227],v[96:99],v[144:147],v[224:227]
v_lshlrev_b32_sdwa v134,1,v134 dst_sel:DWORD dst_unused:UNUSED_PAD src0_sel:DWORD src1_sel:WORD_1
v_mfma_f32_16x16x32_bf16 v[232:235],v[100:103],v[144:147],v[232:235]
s_waitcnt lgkmcnt(3)
ds_read_u16_d16_hi v130,v130
v_lshl_add_u64 v[152:153],v[152:153],0,v[154:155]
v_mfma_f32_16x16x32_bf16 v[240:243],v[104:107],v[144:147],v[240:243]
s_waitcnt lgkmcnt(2)
ds_read_u16 v129,v129
v_or_b32 v154,v158,v156
v_cvt_pk_bf16_f32 v155,v157,v159
v_mfma_f32_16x16x32_bf16 v[248:251],v[108:111],v[144:147],v[248:251]
v_fma_mixhi_f16 v120,v142,s99,0
v_fma_mixhi_f16 v122,v143,s99,0
v_mfma_f32_16x16x32_bf16 v[212:215],v[88:91],v[152:155],v[212:215]
ds_read_u16_d16_hi v131,v131
v_lshlrev_b32_sdwa v133,1,v133 dst_sel:DWORD dst_unused:UNUSED_PAD src0_sel:DWORD src1_sel:WORD_1
v_lshlrev_b32_sdwa v135,1,v135 dst_sel:DWORD dst_unused:UNUSED_PAD src0_sel:DWORD src1_sel:WORD_1
v_mfma_f32_16x16x32_bf16 v[220:223],v[92:95],v[152:155],v[220:223]
v_pk_fma_f16 v121,v120,v120,s54
v_pk_fma_f16 v123,v122,v122,s54
v_lshlrev_b32_sdwa v136,1,v136 dst_sel:DWORD dst_unused:UNUSED_PAD src0_sel:DWORD src1_sel:WORD_1
v_mfma_f32_16x16x32_bf16 v[228:231],v[96:99],v[152:155],v[228:231]
s_cbranch_vccnz RA2_%=
s_mov_b64 exec,s[62:63]
s_add_u32 m0,s57,52320
s_add_u32 s64,s56,3440640
buffer_load_dwordx4 v2,s[36:39],s64 offen lds
s_add_u32 m0,s57,54048
s_nop 0
buffer_load_dwordx4 v3,s[36:39],s64 offen lds
s_mov_b64 exec,-1
RA2_%=:
ds_read_u16 v132,v132
v_pk_mul_f16 v121,v120,v121
v_pk_mul_f16 v123,v122,v123
v_mfma_f32_16x16x32_bf16 v[236:239],v[100:103],v[152:155],v[236:239]
ds_read_b64_tr_b16 v[88:89],v6 offset:0
ds_read_b64_tr_b16 v[90:91],v6 offset:1536
v_mfma_f32_16x16x32_bf16 v[244:247],v[104:107],v[152:155],v[244:247]
ds_read_u16_d16_hi v134,v134
v_exp_f16 v120,v121
v_mfma_f32_16x16x32_bf16 v[252:255],v[108:111],v[152:155],v[252:255]
ds_read_b64_tr_b16 v[92:93],v6 offset:8
v_lshlrev_b32_sdwa v138,1,v138 dst_sel:DWORD dst_unused:UNUSED_PAD src0_sel:DWORD src1_sel:WORD_1
v_exp_f16 v122,v123
v_mfma_f32_16x16x32_bf16 v[144:147],v[64:67],v[40:43],v[112:115]
ds_read_u16 v133,v133
v_lshlrev_b32_sdwa v137,1,v137 dst_sel:DWORD dst_unused:UNUSED_PAD src0_sel:DWORD src1_sel:WORD_1
v_lshlrev_b32_sdwa v139,1,v139 dst_sel:DWORD dst_unused:UNUSED_PAD src0_sel:DWORD src1_sel:WORD_1
v_mfma_f32_16x16x32_bf16 v[152:155],v[64:67],v[44:47],v[112:115]
ds_read_b64_tr_b16 v[94:95],v6 offset:1544
v_exp_f16_sdwa v120,v121 dst_sel:WORD_1 dst_unused:UNUSED_PRESERVE src0_sel:WORD_1
v_mfma_f32_16x16x32_bf16 v[156:159],v[68:71],v[44:47],v[116:119]
ds_read_u16_d16_hi v135,v135
v_exp_f16_sdwa v122,v123 dst_sel:WORD_1 dst_unused:UNUSED_PRESERVE src0_sel:WORD_1
v_mfma_f32_16x16x32_bf16 v[148:151],v[68:71],v[40:43],v[116:119]
ds_read_u16 v136,v136
ds_read_b64_tr_b16 v[96:97],v6 offset:64
v_pk_fma_f16 v120,v120,s55,s55
v_mfma_f32_16x16x32_bf16 v[144:147],v[72:75],v[48:51],v[144:147]
v_rcp_f16 v121,v120
v_pk_fma_f16 v122,v122,s55,s55
v_mfma_f32_16x16x32_bf16 v[152:155],v[72:75],v[52:55],v[152:155]
ds_read_b64_tr_b16 v[98:99],v6 offset:1600
v_rcp_f16 v123,v122
v_mfma_f32_16x16x32_bf16 v[156:159],v[76:79],v[52:55],v[156:159]
ds_read_u16_d16_hi v138,v138
v_rcp_f16_sdwa v121,v120 dst_sel:WORD_1 dst_unused:UNUSED_PRESERVE src0_sel:WORD_1
v_mfma_f32_16x16x32_bf16 v[148:151],v[76:79],v[48:51],v[148:151]
ds_read_b64_tr_b16 v[100:101],v6 offset:72
ds_read_b64_tr_b16 v[102:103],v6 offset:1608
v_rcp_f16_sdwa v123,v122 dst_sel:WORD_1 dst_unused:UNUSED_PRESERVE src0_sel:WORD_1
v_mfma_f32_16x16x32_bf16 v[144:147],v[80:83],v[56:59],v[144:147]
s_waitcnt lgkmcnt(14)
ds_read_u16 v137,v137
v_lshl_add_u64 v[128:129],v[128:129],0,v[130:131]
v_fma_mix_f32 v140,v140,v121,0 op_sel_hi:[0,1,0]
v_fma_mix_f32 v142,v142,v121,0 op_sel:[0,1,0] op_sel_hi:[0,1,0]
v_mfma_f32_16x16x32_bf16 v[152:155],v[80:83],v[60:63],v[152:155]
ds_read_b64_tr_b16 v[104:105],v6 offset:128
ds_read_b64_tr_b16 v[106:107],v6 offset:1664
v_fma_mix_f32 v141,v141,v123,0 op_sel_hi:[0,1,0]
v_mfma_f32_16x16x32_bf16 v[156:159],v[84:87],v[60:63],v[156:159]
s_waitcnt lgkmcnt(9)
ds_read_u16_d16_hi v139,v139
v_lshl_add_u64 v[130:131],v[132:133],0,v[134:135]
v_fma_mix_f32 v143,v143,v123,0 op_sel:[0,1,0] op_sel_hi:[0,1,0]
v_mfma_f32_16x16x32_bf16 v[148:151],v[84:87],v[56:59],v[148:151]
ds_read_b64_tr_b16 v[108:109],v6 offset:136
v_lshlrev_b32_sdwa v144,1,v144 dst_sel:DWORD dst_unused:UNUSED_PAD src0_sel:DWORD src1_sel:WORD_1
v_lshlrev_b32_sdwa v146,1,v146 dst_sel:DWORD dst_unused:UNUSED_PAD src0_sel:DWORD src1_sel:WORD_1
v_mfma_f32_16x16x32_bf16 v[160:163],v[88:91],v[128:131],v[160:163]
ds_read_b64_tr_b16 v[110:111],v6 offset:1672
ds_read_b64_tr_b16 v[64:65],v4 offset:0
v_lshlrev_b32_sdwa v145,1,v145 dst_sel:DWORD dst_unused:UNUSED_PAD src0_sel:DWORD src1_sel:WORD_1
v_lshlrev_b32_sdwa v147,1,v147 dst_sel:DWORD dst_unused:UNUSED_PAD src0_sel:DWORD src1_sel:WORD_1
v_mfma_f32_16x16x32_bf16 v[168:171],v[92:95],v[128:131],v[168:171]
s_waitcnt lgkmcnt(10)
ds_read_b64_tr_b16 v[66:67],v4 offset:2048
ds_read_b128 v[112:115],v7 offset:768
v_fma_mixlo_f16 v120,v156,s99,0
v_mfma_f32_16x16x32_bf16 v[176:179],v[96:99],v[128:131],v[176:179]
s_waitcnt lgkmcnt(9)
ds_read_b64_tr_b16 v[68:69],v5 offset:0
ds_read_u16 v144,v144
v_fma_mixlo_f16 v122,v157,s99,0
v_lshlrev_b32_sdwa v148,1,v148 dst_sel:DWORD dst_unused:UNUSED_PAD src0_sel:DWORD src1_sel:WORD_1
v_lshlrev_b32_sdwa v150,1,v150 dst_sel:DWORD dst_unused:UNUSED_PAD src0_sel:DWORD src1_sel:WORD_1
v_mfma_f32_16x16x32_bf16 v[184:187],v[100:103],v[128:131],v[184:187]
s_waitcnt lgkmcnt(7)
ds_read_u16_d16_hi v146,v146
v_lshl_add_u64 v[136:137],v[136:137],0,v[138:139]
v_mfma_f32_16x16x32_bf16 v[192:195],v[104:107],v[128:131],v[192:195]
s_waitcnt lgkmcnt(6)
ds_read_b64_tr_b16 v[70:71],v5 offset:2048
v_fma_mixhi_f16 v120,v158,s99,0
v_cvt_pk_bf16_f32 v138,v140,v142
v_cvt_pk_bf16_f32 v139,v141,v143
v_mfma_f32_16x16x32_bf16 v[200:203],v[108:111],v[128:131],v[200:203]
ds_read_b128 v[116:119],v7 offset:800
ds_read_b64_tr_b16 v[72:73],v4 offset:4096
v_fma_mixhi_f16 v122,v159,s99,0
v_mfma_f32_16x16x32_bf16 v[164:167],v[88:91],v[136:139],v[164:167]
v_lshlrev_b32_sdwa v149,1,v149 dst_sel:DWORD dst_unused:UNUSED_PAD src0_sel:DWORD src1_sel:WORD_1
v_lshlrev_b32_sdwa v151,1,v151 dst_sel:DWORD dst_unused:UNUSED_PAD src0_sel:DWORD src1_sel:WORD_1
v_mfma_f32_16x16x32_bf16 v[172:175],v[92:95],v[136:139],v[172:175]
ds_read_b64_tr_b16 v[74:75],v4 offset:6144
ds_read_b64_tr_b16 v[76:77],v5 offset:4096
v_pk_fma_f16 v123,v122,v122,s54
v_pk_fma_f16 v121,v120,v120,s54
v_mfma_f32_16x16x32_bf16 v[180:183],v[96:99],v[136:139],v[180:183]
ds_read_u16 v145,v145
v_pk_mul_f16 v121,v120,v121
v_pk_mul_f16 v123,v122,v123
v_mfma_f32_16x16x32_bf16 v[188:191],v[100:103],v[136:139],v[188:191]
ds_read_b64_tr_b16 v[78:79],v5 offset:6144
v_lshlrev_b32_sdwa v152,1,v152 dst_sel:DWORD dst_unused:UNUSED_PAD src0_sel:DWORD src1_sel:WORD_1
v_mfma_f32_16x16x32_bf16 v[196:199],v[104:107],v[136:139],v[196:199]
ds_read_u16_d16_hi v147,v147
v_lshlrev_b32_sdwa v154,1,v154 dst_sel:DWORD dst_unused:UNUSED_PAD src0_sel:DWORD src1_sel:WORD_1
v_exp_f16 v120,v121
v_mfma_f32_16x16x32_bf16 v[204:207],v[108:111],v[136:139],v[204:207]
s_waitcnt lgkmcnt(11)
ds_read_b64_tr_b16 v[80:81],v4 offset:8192
v_exp_f16 v122,v123
v_mfma_f32_16x16x32_bf16 v[128:131],v[64:67],v[16:19],v[112:115]
ds_read_u16 v148,v148
ds_read_u16_d16_hi v150,v150
v_lshlrev_b32_sdwa v153,1,v153 dst_sel:DWORD dst_unused:UNUSED_PAD src0_sel:DWORD src1_sel:WORD_1
v_exp_f16_sdwa v122,v123 dst_sel:WORD_1 dst_unused:UNUSED_PRESERVE src0_sel:WORD_1
v_mfma_f32_16x16x32_bf16 v[136:139],v[64:67],v[20:23],v[112:115]
s_waitcnt lgkmcnt(9)
v_lshlrev_b32_sdwa v155,1,v155 dst_sel:DWORD dst_unused:UNUSED_PAD src0_sel:DWORD src1_sel:WORD_1
v_exp_f16_sdwa v120,v121 dst_sel:WORD_1 dst_unused:UNUSED_PRESERVE src0_sel:WORD_1
v_mfma_f32_16x16x32_bf16 v[140:143],v[68:71],v[20:23],v[116:119]
ds_read_b64_tr_b16 v[82:83],v4 offset:10240
ds_read_b64_tr_b16 v[84:85],v5 offset:8192
v_pk_fma_f16 v120,v120,s55,s55
v_mfma_f32_16x16x32_bf16 v[132:135],v[68:71],v[16:19],v[116:119]
s_waitcnt lgkmcnt(9)
ds_read_u16 v149,v149
s_nop 0
s_mov_b32 s86,0xbe801d5a
v_pk_fma_f16 v122,v122,s55,s55
v_mfma_f32_16x16x32_bf16 v[128:131],v[72:75],v[24:27],v[128:131]
ds_read_b64_tr_b16 v[86:87],v5 offset:10240
ds_read_u16_d16_hi v151,v151
v_rcp_f16 v121,v120
v_mfma_f32_16x16x32_bf16 v[136:139],v[72:75],v[28:31],v[136:139]
s_waitcnt lgkmcnt(9)
v_rcp_f16 v123,v122
v_mfma_f32_16x16x32_bf16 v[140:143],v[76:79],v[28:31],v[140:143]
ds_read_u16 v152,v152
v_rcp_f16_sdwa v121,v120 dst_sel:WORD_1 dst_unused:UNUSED_PRESERVE src0_sel:WORD_1
v_mfma_f32_16x16x32_bf16 v[132:135],v[76:79],v[24:27],v[132:135]
s_waitcnt lgkmcnt(5)
v_rcp_f16_sdwa v123,v122 dst_sel:WORD_1 dst_unused:UNUSED_PRESERVE src0_sel:WORD_1
v_mfma_f32_16x16x32_bf16 v[128:131],v[80:83],v[32:35],v[128:131]
ds_read_u16_d16_hi v154,v154
v_fma_mix_f32 v156,v156,v121,0 op_sel_hi:[0,1,0]
v_mfma_f32_16x16x32_bf16 v[136:139],v[80:83],v[36:39],v[136:139]
s_waitcnt lgkmcnt(2)
v_lshl_add_u64 v[144:145],v[144:145],0,v[146:147]
v_fma_mix_f32 v158,v158,v121,0 op_sel:[0,1,0] op_sel_hi:[0,1,0]
v_lshl_add_u64 v[146:147],v[148:149],0,v[150:151]
v_mfma_f32_16x16x32_bf16 v[140:143],v[84:87],v[36:39],v[140:143]
ds_read_u16 v153,v153
v_fma_mix_f32 v157,v157,v123,0 op_sel_hi:[0,1,0]
v_fma_mix_f32 v159,v159,v123,0 op_sel:[0,1,0] op_sel_hi:[0,1,0]
v_mfma_f32_16x16x32_bf16 v[132:135],v[84:87],v[32:35],v[132:135]
v_lshlrev_b32_sdwa v128,1,v128 dst_sel:DWORD dst_unused:UNUSED_PAD src0_sel:DWORD src1_sel:WORD_1
v_lshlrev_b32_sdwa v130,1,v130 dst_sel:DWORD dst_unused:UNUSED_PAD src0_sel:DWORD src1_sel:WORD_1
v_mfma_f32_16x16x32_bf16 v[208:211],v[88:91],v[144:147],v[208:211]
ds_read_u16_d16_hi v155,v155
v_fma_mixlo_f16 v120,v140,s99,0
v_lshlrev_b32_sdwa v129,1,v129 dst_sel:DWORD dst_unused:UNUSED_PAD src0_sel:DWORD src1_sel:WORD_1
v_lshlrev_b32_sdwa v131,1,v131 dst_sel:DWORD dst_unused:UNUSED_PAD src0_sel:DWORD src1_sel:WORD_1
v_mfma_f32_16x16x32_bf16 v[216:219],v[92:95],v[144:147],v[216:219]
v_fma_mixlo_f16 v122,v141,s99,0
v_lshlrev_b32_sdwa v132,1,v132 dst_sel:DWORD dst_unused:UNUSED_PAD src0_sel:DWORD src1_sel:WORD_1
v_mfma_f32_16x16x32_bf16 v[224:227],v[96:99],v[144:147],v[224:227]
v_lshlrev_b32_sdwa v134,1,v134 dst_sel:DWORD dst_unused:UNUSED_PAD src0_sel:DWORD src1_sel:WORD_1
v_mfma_f32_16x16x32_bf16 v[232:235],v[100:103],v[144:147],v[232:235]
s_waitcnt lgkmcnt(0)
ds_read_u16 v128,v128
v_lshl_add_u64 v[152:153],v[152:153],0,v[154:155]
v_mfma_f32_16x16x32_bf16 v[240:243],v[104:107],v[144:147],v[240:243]
ds_read_u16_d16_hi v130,v130
v_cvt_pk_bf16_f32 v155,v157,v159
v_cvt_pk_bf16_f32 v154,v156,v158
v_mfma_f32_16x16x32_bf16 v[248:251],v[108:111],v[144:147],v[248:251]
s_setprio 2
v_fma_mixhi_f16 v120,v142,s99,0
v_fma_mixhi_f16 v122,v143,s99,0
v_mfma_f32_16x16x32_bf16 v[212:215],v[88:91],v[152:155],v[212:215]
ds_read_u16 v129,v129
v_lshlrev_b32_sdwa v133,1,v133 dst_sel:DWORD dst_unused:UNUSED_PAD src0_sel:DWORD src1_sel:WORD_1
v_lshlrev_b32_sdwa v135,1,v135 dst_sel:DWORD dst_unused:UNUSED_PAD src0_sel:DWORD src1_sel:WORD_1
v_mfma_f32_16x16x32_bf16 v[220:223],v[92:95],v[152:155],v[220:223]
v_pk_fma_f16 v121,v120,v120,s54
v_pk_fma_f16 v123,v122,v122,s54
v_lshlrev_b32_sdwa v136,1,v136 dst_sel:DWORD dst_unused:UNUSED_PAD src0_sel:DWORD src1_sel:WORD_1
v_mfma_f32_16x16x32_bf16 v[228:231],v[96:99],v[152:155],v[228:231]
ds_read_u16_d16_hi v131,v131
v_pk_mul_f16 v121,v120,v121
v_pk_mul_f16 v123,v122,v123
v_mfma_f32_16x16x32_bf16 v[236:239],v[100:103],v[152:155],v[236:239]
ds_read_b64_tr_b16 v[88:89],v6 offset:6160
ds_read_b64_tr_b16 v[90:91],v6 offset:7696
v_mfma_f32_16x16x32_bf16 v[244:247],v[104:107],v[152:155],v[244:247]
ds_read_u16 v132,v132
v_exp_f16 v120,v121
v_mfma_f32_16x16x32_bf16 v[252:255],v[108:111],v[152:155],v[252:255]
ds_read_b64_tr_b16 v[92:93],v6 offset:6168
v_lshlrev_b32_sdwa v138,1,v138 dst_sel:DWORD dst_unused:UNUSED_PAD src0_sel:DWORD src1_sel:WORD_1
v_exp_f16 v122,v123
v_mfma_f32_16x16x32_bf16 v[144:147],v[64:67],v[40:43],v[112:115]
ds_read_u16_d16_hi v134,v134
v_lshlrev_b32_sdwa v137,1,v137 dst_sel:DWORD dst_unused:UNUSED_PAD src0_sel:DWORD src1_sel:WORD_1
v_lshlrev_b32_sdwa v139,1,v139 dst_sel:DWORD dst_unused:UNUSED_PAD src0_sel:DWORD src1_sel:WORD_1
v_mfma_f32_16x16x32_bf16 v[152:155],v[64:67],v[44:47],v[112:115]
ds_read_b64_tr_b16 v[94:95],v6 offset:7704
v_exp_f16_sdwa v120,v121 dst_sel:WORD_1 dst_unused:UNUSED_PRESERVE src0_sel:WORD_1
v_mfma_f32_16x16x32_bf16 v[156:159],v[68:71],v[44:47],v[116:119]
ds_read_u16 v133,v133
v_exp_f16_sdwa v122,v123 dst_sel:WORD_1 dst_unused:UNUSED_PRESERVE src0_sel:WORD_1
v_mfma_f32_16x16x32_bf16 v[148:151],v[68:71],v[40:43],v[116:119]
ds_read_u16_d16_hi v135,v135
ds_read_b64_tr_b16 v[96:97],v6 offset:6224
v_pk_fma_f16 v120,v120,s55,s55
v_mfma_f32_16x16x32_bf16 v[144:147],v[72:75],v[48:51],v[144:147]
v_rcp_f16 v121,v120
v_pk_fma_f16 v122,v122,s55,s55
v_mfma_f32_16x16x32_bf16 v[152:155],v[72:75],v[52:55],v[152:155]
ds_read_b64_tr_b16 v[98:99],v6 offset:7760
v_rcp_f16 v123,v122
v_mfma_f32_16x16x32_bf16 v[156:159],v[76:79],v[52:55],v[156:159]
ds_read_u16 v136,v136
v_rcp_f16_sdwa v121,v120 dst_sel:WORD_1 dst_unused:UNUSED_PRESERVE src0_sel:WORD_1
v_mfma_f32_16x16x32_bf16 v[148:151],v[76:79],v[48:51],v[148:151]
ds_read_b64_tr_b16 v[100:101],v6 offset:6232
ds_read_b64_tr_b16 v[102:103],v6 offset:7768
v_rcp_f16_sdwa v123,v122 dst_sel:WORD_1 dst_unused:UNUSED_PRESERVE src0_sel:WORD_1
v_mfma_f32_16x16x32_bf16 v[144:147],v[80:83],v[56:59],v[144:147]
s_waitcnt lgkmcnt(13)
ds_read_u16_d16_hi v138,v138
v_lshl_add_u64 v[128:129],v[128:129],0,v[130:131]
v_fma_mix_f32 v140,v140,v121,0 op_sel_hi:[0,1,0]
v_fma_mix_f32 v142,v142,v121,0 op_sel:[0,1,0] op_sel_hi:[0,1,0]
v_mfma_f32_16x16x32_bf16 v[152:155],v[80:83],v[60:63],v[152:155]
ds_read_b64_tr_b16 v[104:105],v6 offset:6288
ds_read_b64_tr_b16 v[106:107],v6 offset:7824
v_fma_mix_f32 v141,v141,v123,0 op_sel_hi:[0,1,0]
v_mfma_f32_16x16x32_bf16 v[156:159],v[84:87],v[60:63],v[156:159]
s_waitcnt lgkmcnt(8)
ds_read_u16 v137,v137
v_lshl_add_u64 v[130:131],v[132:133],0,v[134:135]
v_fma_mix_f32 v143,v143,v123,0 op_sel:[0,1,0] op_sel_hi:[0,1,0]
v_mfma_f32_16x16x32_bf16 v[148:151],v[84:87],v[56:59],v[148:151]
ds_read_u16_d16_hi v139,v139
v_lshlrev_b32_sdwa v144,1,v144 dst_sel:DWORD dst_unused:UNUSED_PAD src0_sel:DWORD src1_sel:WORD_1
v_lshlrev_b32_sdwa v146,1,v146 dst_sel:DWORD dst_unused:UNUSED_PAD src0_sel:DWORD src1_sel:WORD_1
v_mfma_f32_16x16x32_bf16 v[160:163],v[88:91],v[128:131],v[160:163]
ds_read_b64_tr_b16 v[108:109],v6 offset:6296
ds_read_b64_tr_b16 v[110:111],v6 offset:7832
v_lshlrev_b32_sdwa v145,1,v145 dst_sel:DWORD dst_unused:UNUSED_PAD src0_sel:DWORD src1_sel:WORD_1
v_lshlrev_b32_sdwa v147,1,v147 dst_sel:DWORD dst_unused:UNUSED_PAD src0_sel:DWORD src1_sel:WORD_1
v_mfma_f32_16x16x32_bf16 v[168:171],v[92:95],v[128:131],v[168:171]
s_waitcnt lgkmcnt(10)
ds_read_b64_tr_b16 v[64:65],v4 offset:512
ds_read_b64_tr_b16 v[66:67],v4 offset:2560
v_fma_mixlo_f16 v120,v156,s99,0
v_mfma_f32_16x16x32_bf16 v[176:179],v[96:99],v[128:131],v[176:179]
s_waitcnt lgkmcnt(9)
ds_read_b128 v[112:115],v7 offset:896
ds_read_u16 v144,v144
v_fma_mixlo_f16 v122,v157,s99,0
v_lshlrev_b32_sdwa v148,1,v148 dst_sel:DWORD dst_unused:UNUSED_PAD src0_sel:DWORD src1_sel:WORD_1
v_lshlrev_b32_sdwa v150,1,v150 dst_sel:DWORD dst_unused:UNUSED_PAD src0_sel:DWORD src1_sel:WORD_1
v_mfma_f32_16x16x32_bf16 v[184:187],v[100:103],v[128:131],v[184:187]
s_waitcnt lgkmcnt(6)
s_setprio 1
ds_read_u16_d16_hi v146,v146
v_lshl_add_u64 v[136:137],v[136:137],0,v[138:139]
v_mfma_f32_16x16x32_bf16 v[192:195],v[104:107],v[128:131],v[192:195]
s_waitcnt lgkmcnt(5)
ds_read_b64_tr_b16 v[68:69],v5 offset:512
v_fma_mixhi_f16 v120,v158,s99,0
v_cvt_pk_bf16_f32 v138,v140,v142
v_cvt_pk_bf16_f32 v139,v141,v143
v_mfma_f32_16x16x32_bf16 v[200:203],v[108:111],v[128:131],v[200:203]
ds_read_b64_tr_b16 v[70:71],v5 offset:2560
ds_read_b128 v[116:119],v7 offset:928
v_fma_mixhi_f16 v122,v159,s99,0
v_mfma_f32_16x16x32_bf16 v[164:167],v[88:91],v[136:139],v[164:167]
v_lshlrev_b32_sdwa v149,1,v149 dst_sel:DWORD dst_unused:UNUSED_PAD src0_sel:DWORD src1_sel:WORD_1
v_lshlrev_b32_sdwa v151,1,v151 dst_sel:DWORD dst_unused:UNUSED_PAD src0_sel:DWORD src1_sel:WORD_1
v_mfma_f32_16x16x32_bf16 v[172:175],v[92:95],v[136:139],v[172:175]
ds_read_b64_tr_b16 v[72:73],v4 offset:4608
ds_read_b64_tr_b16 v[74:75],v4 offset:6656
v_pk_fma_f16 v123,v122,v122,s54
v_pk_fma_f16 v121,v120,v120,s54
v_mfma_f32_16x16x32_bf16 v[180:183],v[96:99],v[136:139],v[180:183]
ds_read_u16 v145,v145
v_pk_mul_f16 v121,v120,v121
v_pk_mul_f16 v123,v122,v123
v_mfma_f32_16x16x32_bf16 v[188:191],v[100:103],v[136:139],v[188:191]
ds_read_b64_tr_b16 v[76:77],v5 offset:4608
v_lshlrev_b32_sdwa v152,1,v152 dst_sel:DWORD dst_unused:UNUSED_PAD src0_sel:DWORD src1_sel:WORD_1
v_mfma_f32_16x16x32_bf16 v[196:199],v[104:107],v[136:139],v[196:199]
ds_read_u16_d16_hi v147,v147
v_lshlrev_b32_sdwa v154,1,v154 dst_sel:DWORD dst_unused:UNUSED_PAD src0_sel:DWORD src1_sel:WORD_1
v_exp_f16 v120,v121
v_mfma_f32_16x16x32_bf16 v[204:207],v[108:111],v[136:139],v[204:207]
s_waitcnt lgkmcnt(10)
ds_read_b64_tr_b16 v[78:79],v5 offset:6656
v_exp_f16 v122,v123
v_mfma_f32_16x16x32_bf16 v[128:131],v[64:67],v[16:19],v[112:115]
ds_read_u16 v148,v148
ds_read_u16_d16_hi v150,v150
v_lshlrev_b32_sdwa v153,1,v153 dst_sel:DWORD dst_unused:UNUSED_PAD src0_sel:DWORD src1_sel:WORD_1
v_exp_f16_sdwa v122,v123 dst_sel:WORD_1 dst_unused:UNUSED_PRESERVE src0_sel:WORD_1
v_mfma_f32_16x16x32_bf16 v[136:139],v[64:67],v[20:23],v[112:115]
s_waitcnt lgkmcnt(8)
s_setprio 0
v_lshlrev_b32_sdwa v155,1,v155 dst_sel:DWORD dst_unused:UNUSED_PAD src0_sel:DWORD src1_sel:WORD_1
v_exp_f16_sdwa v120,v121 dst_sel:WORD_1 dst_unused:UNUSED_PRESERVE src0_sel:WORD_1
v_mfma_f32_16x16x32_bf16 v[140:143],v[68:71],v[20:23],v[116:119]
ds_read_b64_tr_b16 v[80:81],v4 offset:8704
ds_read_b64_tr_b16 v[82:83],v4 offset:10752
v_pk_fma_f16 v120,v120,s55,s55
v_mfma_f32_16x16x32_bf16 v[132:135],v[68:71],v[16:19],v[116:119]
s_waitcnt lgkmcnt(8)
ds_read_u16 v149,v149
v_pk_fma_f16 v122,v122,s55,s55
v_mfma_f32_16x16x32_bf16 v[128:131],v[72:75],v[24:27],v[128:131]
ds_read_b64_tr_b16 v[84:85],v5 offset:8704
ds_read_u16_d16_hi v151,v151
v_rcp_f16 v121,v120
v_mfma_f32_16x16x32_bf16 v[136:139],v[72:75],v[28:31],v[136:139]
s_waitcnt lgkmcnt(7)
ds_read_b64_tr_b16 v[86:87],v5 offset:10752
v_rcp_f16 v123,v122
v_mfma_f32_16x16x32_bf16 v[140:143],v[76:79],v[28:31],v[140:143]
ds_read_u16 v152,v152
v_rcp_f16_sdwa v121,v120 dst_sel:WORD_1 dst_unused:UNUSED_PRESERVE src0_sel:WORD_1
v_mfma_f32_16x16x32_bf16 v[132:135],v[76:79],v[24:27],v[132:135]
s_waitcnt lgkmcnt(5)
v_rcp_f16_sdwa v123,v122 dst_sel:WORD_1 dst_unused:UNUSED_PRESERVE src0_sel:WORD_1
v_mfma_f32_16x16x32_bf16 v[128:131],v[80:83],v[32:35],v[128:131]
ds_read_u16_d16_hi v154,v154
v_fma_mix_f32 v156,v156,v121,0 op_sel_hi:[0,1,0]
v_mfma_f32_16x16x32_bf16 v[136:139],v[80:83],v[36:39],v[136:139]
s_waitcnt lgkmcnt(2)
v_lshl_add_u64 v[144:145],v[144:145],0,v[146:147]
v_fma_mix_f32 v158,v158,v121,0 op_sel:[0,1,0] op_sel_hi:[0,1,0]
v_lshl_add_u64 v[146:147],v[148:149],0,v[150:151]
v_mfma_f32_16x16x32_bf16 v[140:143],v[84:87],v[36:39],v[140:143]
ds_read_u16 v153,v153
v_fma_mix_f32 v157,v157,v123,0 op_sel_hi:[0,1,0]
v_fma_mix_f32 v159,v159,v123,0 op_sel:[0,1,0] op_sel_hi:[0,1,0]
v_mfma_f32_16x16x32_bf16 v[132:135],v[84:87],v[32:35],v[132:135]
v_lshlrev_b32_sdwa v128,1,v128 dst_sel:DWORD dst_unused:UNUSED_PAD src0_sel:DWORD src1_sel:WORD_1
v_lshlrev_b32_sdwa v130,1,v130 dst_sel:DWORD dst_unused:UNUSED_PAD src0_sel:DWORD src1_sel:WORD_1
v_mfma_f32_16x16x32_bf16 v[208:211],v[88:91],v[144:147],v[208:211]
s_barrier
s_setprio 2
v_lshlrev_b32_sdwa v129,1,v129 dst_sel:DWORD dst_unused:UNUSED_PAD src0_sel:DWORD src1_sel:WORD_1
v_lshlrev_b32_sdwa v131,1,v131 dst_sel:DWORD dst_unused:UNUSED_PAD src0_sel:DWORD src1_sel:WORD_1
v_mfma_f32_16x16x32_bf16 v[216:219],v[92:95],v[144:147],v[216:219]
s_cbranch_vccnz RA3_%=
s_mov_b64 exec,s[62:63]
s_add_u32 m0,s57,55808
s_add_u32 s64,s56,3538944
buffer_load_dwordx4 v2,s[36:39],s64 offen lds
s_add_u32 m0,s57,57536
s_nop 0
buffer_load_dwordx4 v3,s[36:39],s64 offen lds
s_mov_b64 exec,-1
RA3_%=:
ds_read_u16_d16_hi v155,v155
v_mfma_f32_16x16x32_bf16 v[224:227],v[96:99],v[144:147],v[224:227]
v_lshlrev_b32_sdwa v132,1,v132 dst_sel:DWORD dst_unused:UNUSED_PAD src0_sel:DWORD src1_sel:WORD_1
v_mfma_f32_16x16x32_bf16 v[232:235],v[100:103],v[144:147],v[232:235]
s_waitcnt lgkmcnt(0)
ds_read_u16 v128,v128
v_lshl_add_u64 v[152:153],v[152:153],0,v[154:155]
v_cvt_pk_bf16_f32 v155,v157,v159
v_lshlrev_b32_sdwa v134,1,v134 dst_sel:DWORD dst_unused:UNUSED_PAD src0_sel:DWORD src1_sel:WORD_1
v_cvt_pk_bf16_f32 v154,v156,v158
v_mfma_f32_16x16x32_bf16 v[240:243],v[104:107],v[144:147],v[240:243]
ds_read_u16_d16_hi v130,v130
v_mfma_f32_16x16x32_bf16 v[248:251],v[108:111],v[144:147],v[248:251]
ds_read_u16 v129,v129
v_lshlrev_b32_sdwa v133,1,v133 dst_sel:DWORD dst_unused:UNUSED_PAD src0_sel:DWORD src1_sel:WORD_1
v_mfma_f32_16x16x32_bf16 v[212:215],v[88:91],v[152:155],v[212:215]
v_lshlrev_b32_sdwa v135,1,v135 dst_sel:DWORD dst_unused:UNUSED_PAD src0_sel:DWORD src1_sel:WORD_1
v_mfma_f32_16x16x32_bf16 v[220:223],v[92:95],v[152:155],v[220:223]
ds_read_u16_d16_hi v131,v131
v_lshlrev_b32_sdwa v136,1,v136 dst_sel:DWORD dst_unused:UNUSED_PAD src0_sel:DWORD src1_sel:WORD_1
v_mfma_f32_16x16x32_bf16 v[228:231],v[96:99],v[152:155],v[228:231]
ds_read_b64_tr_b16 v[88:89],v6 offset:12320
ds_read_b64_tr_b16 v[90:91],v6 offset:13856
v_lshlrev_b32_sdwa v138,1,v138 dst_sel:DWORD dst_unused:UNUSED_PAD src0_sel:DWORD src1_sel:WORD_1
v_mfma_f32_16x16x32_bf16 v[236:239],v[100:103],v[152:155],v[236:239]
ds_read_u16 v132,v132
v_mfma_f32_16x16x32_bf16 v[244:247],v[104:107],v[152:155],v[244:247]
ds_read_u16_d16_hi v134,v134
v_lshlrev_b32_sdwa v137,1,v137 dst_sel:DWORD dst_unused:UNUSED_PAD src0_sel:DWORD src1_sel:WORD_1
v_lshlrev_b32_sdwa v139,1,v139 dst_sel:DWORD dst_unused:UNUSED_PAD src0_sel:DWORD src1_sel:WORD_1
v_mfma_f32_16x16x32_bf16 v[252:255],v[108:111],v[152:155],v[252:255]
ds_read_b64_tr_b16 v[92:93],v6 offset:12328
ds_read_b64_tr_b16 v[94:95],v6 offset:13864
v_lshlrev_b32_sdwa v140,1,v140 dst_sel:DWORD dst_unused:UNUSED_PAD src0_sel:DWORD src1_sel:WORD_1
v_mfma_f32_16x16x32_bf16 v[144:147],v[64:67],v[40:43],v[112:115]
ds_read_u16 v133,v133
v_lshlrev_b32_sdwa v142,1,v142 dst_sel:DWORD dst_unused:UNUSED_PAD src0_sel:DWORD src1_sel:WORD_1
v_mfma_f32_16x16x32_bf16 v[152:155],v[64:67],v[44:47],v[112:115]
ds_read_b64_tr_b16 v[96:97],v6 offset:12384
v_lshlrev_b32_sdwa v141,1,v141 dst_sel:DWORD dst_unused:UNUSED_PAD src0_sel:DWORD src1_sel:WORD_1
v_lshlrev_b32_sdwa v143,1,v143 dst_sel:DWORD dst_unused:UNUSED_PAD src0_sel:DWORD src1_sel:WORD_1
v_mfma_f32_16x16x32_bf16 v[156:159],v[68:71],v[44:47],v[116:119]
ds_read_u16_d16_hi v135,v135
v_mfma_f32_16x16x32_bf16 v[148:151],v[68:71],v[40:43],v[116:119]
ds_read_u16 v136,v136
ds_read_b64_tr_b16 v[98:99],v6 offset:13920
v_mfma_f32_16x16x32_bf16 v[144:147],v[72:75],v[48:51],v[144:147]
ds_read_u16_d16_hi v138,v138
v_mfma_f32_16x16x32_bf16 v[152:155],v[72:75],v[52:55],v[152:155]
ds_read_b64_tr_b16 v[100:101],v6 offset:12392
ds_read_b64_tr_b16 v[102:103],v6 offset:13928
v_mfma_f32_16x16x32_bf16 v[156:159],v[76:79],v[52:55],v[156:159]
ds_read_u16 v137,v137
v_mfma_f32_16x16x32_bf16 v[148:151],v[76:79],v[48:51],v[148:151]
s_waitcnt lgkmcnt(6)
ds_read_u16_d16_hi v139,v139
v_lshl_add_u64 v[128:129],v[128:129],0,v[130:131]
v_lshl_add_u64 v[130:131],v[132:133],0,v[134:135]
v_mfma_f32_16x16x32_bf16 v[144:147],v[80:83],v[56:59],v[144:147]
ds_read_b64_tr_b16 v[104:105],v6 offset:12448
ds_read_b64_tr_b16 v[106:107],v6 offset:13984
v_mfma_f32_16x16x32_bf16 v[152:155],v[80:83],v[60:63],v[152:155]
s_cbranch_vccnz RA4_%=
s_mov_b64 exec,s[62:63]
s_add_u32 m0,s57,59296
s_add_u32 s64,s56,3637248
buffer_load_dwordx4 v2,s[36:39],s64 offen lds
s_add_u32 m0,s57,61024
s_nop 0
buffer_load_dwordx4 v3,s[36:39],s64 offen lds
s_mov_b64 exec,-1
RA4_%=:
ds_read_u16 v140,v140
v_mfma_f32_16x16x32_bf16 v[156:159],v[84:87],v[60:63],v[156:159]
ds_read_u16_d16_hi v142,v142
s_nop 0
v_lshlrev_b32_sdwa v144,1,v144 dst_sel:DWORD dst_unused:UNUSED_PAD src0_sel:DWORD src1_sel:WORD_1
v_mfma_f32_16x16x32_bf16 v[148:151],v[84:87],v[56:59],v[148:151]
ds_read_b64_tr_b16 v[108:109],v6 offset:12456
ds_read_b64_tr_b16 v[110:111],v6 offset:13992
v_lshlrev_b32_sdwa v146,1,v146 dst_sel:DWORD dst_unused:UNUSED_PAD src0_sel:DWORD src1_sel:WORD_1
v_mfma_f32_16x16x32_bf16 v[160:163],v[88:91],v[128:131],v[160:163]
ds_read_b64_tr_b16 v[64:65],v4 offset:12320
ds_read_u16 v141,v141
v_lshlrev_b32_sdwa v145,1,v145 dst_sel:DWORD dst_unused:UNUSED_PAD src0_sel:DWORD src1_sel:WORD_1
v_mfma_f32_16x16x32_bf16 v[168:171],v[92:95],v[128:131],v[168:171]
s_waitcnt lgkmcnt(13)
ds_read_u16_d16_hi v143,v143
v_lshlrev_b32_sdwa v147,1,v147 dst_sel:DWORD dst_unused:UNUSED_PAD src0_sel:DWORD src1_sel:WORD_1
v_mfma_f32_16x16x32_bf16 v[176:179],v[96:99],v[128:131],v[176:179]
s_waitcnt lgkmcnt(11)
ds_read_b64_tr_b16 v[66:67],v4 offset:14368
v_lshlrev_b32_sdwa v148,1,v148 dst_sel:DWORD dst_unused:UNUSED_PAD src0_sel:DWORD src1_sel:WORD_1
v_mfma_f32_16x16x32_bf16 v[184:187],v[100:103],v[128:131],v[184:187]
s_waitcnt lgkmcnt(8)
ds_read_u16 v144,v144
v_lshl_add_u64 v[136:137],v[136:137],0,v[138:139]
v_lshlrev_b32_sdwa v150,1,v150 dst_sel:DWORD dst_unused:UNUSED_PAD src0_sel:DWORD src1_sel:WORD_1
v_mfma_f32_16x16x32_bf16 v[192:195],v[104:107],v[128:131],v[192:195]
s_waitcnt lgkmcnt(2)
ds_read_u16_d16_hi v146,v146
v_lshl_add_u64 v[138:139],v[140:141],0,v[142:143]
v_lshlrev_b32_sdwa v149,1,v149 dst_sel:DWORD dst_unused:UNUSED_PAD src0_sel:DWORD src1_sel:WORD_1
v_mfma_f32_16x16x32_bf16 v[200:203],v[108:111],v[128:131],v[200:203]
ds_read_b128 v[112:115],v7 offset:1024
v_mfma_f32_16x16x32_bf16 v[164:167],v[88:91],v[136:139],v[164:167]
ds_read_u16 v145,v145
v_lshlrev_b32_sdwa v151,1,v151 dst_sel:DWORD dst_unused:UNUSED_PAD src0_sel:DWORD src1_sel:WORD_1
v_mfma_f32_16x16x32_bf16 v[172:175],v[92:95],v[136:139],v[172:175]
ds_read_u16_d16_hi v147,v147
ds_read_b64_tr_b16 v[68:69],v5 offset:12320
v_lshlrev_b32_sdwa v152,1,v152 dst_sel:DWORD dst_unused:UNUSED_PAD src0_sel:DWORD src1_sel:WORD_1
v_mfma_f32_16x16x32_bf16 v[180:183],v[96:99],v[136:139],v[180:183]
ds_read_b64_tr_b16 v[70:71],v5 offset:14368
ds_read_b128 v[116:119],v7 offset:1056
v_lshlrev_b32_sdwa v154,1,v154 dst_sel:DWORD dst_unused:UNUSED_PAD src0_sel:DWORD src1_sel:WORD_1
v_mfma_f32_16x16x32_bf16 v[188:191],v[100:103],v[136:139],v[188:191]
ds_read_u16 v148,v148
ds_read_b64_tr_b16 v[72:73],v4 offset:16416
v_lshlrev_b32_sdwa v153,1,v153 dst_sel:DWORD dst_unused:UNUSED_PAD src0_sel:DWORD src1_sel:WORD_1
v_mfma_f32_16x16x32_bf16 v[196:199],v[104:107],v[136:139],v[196:199]
ds_read_u16_d16_hi v150,v150
v_lshlrev_b32_sdwa v155,1,v155 dst_sel:DWORD dst_unused:UNUSED_PAD src0_sel:DWORD src1_sel:WORD_1
v_mfma_f32_16x16x32_bf16 v[204:207],v[108:111],v[136:139],v[204:207]
s_waitcnt lgkmcnt(8)
ds_read_b64_tr_b16 v[74:75],v4 offset:18464
ds_read_b64_tr_b16 v[76:77],v5 offset:16416
v_mfma_f32_16x16x32_bf16 v[128:131],v[64:67],v[16:19],v[112:115]
ds_read_u16 v149,v149
v_lshlrev_b32_sdwa v156,1,v156 dst_sel:DWORD dst_unused:UNUSED_PAD src0_sel:DWORD src1_sel:WORD_1
v_lshlrev_b32_sdwa v158,1,v158 dst_sel:DWORD dst_unused:UNUSED_PAD src0_sel:DWORD src1_sel:WORD_1
v_mfma_f32_16x16x32_bf16 v[136:139],v[64:67],v[20:23],v[112:115]
s_waitcnt lgkmcnt(6)
ds_read_b64_tr_b16 v[78:79],v5 offset:18464
ds_read_b64_tr_b16 v[80:81],v4 offset:20512
v_lshlrev_b32_sdwa v157,1,v157 dst_sel:DWORD dst_unused:UNUSED_PAD src0_sel:DWORD src1_sel:WORD_1
v_mfma_f32_16x16x32_bf16 v[140:143],v[68:71],v[20:23],v[116:119]
ds_read_u16_d16_hi v151,v151
v_lshlrev_b32_sdwa v159,1,v159 dst_sel:DWORD dst_unused:UNUSED_PAD src0_sel:DWORD src1_sel:WORD_1
v_mfma_f32_16x16x32_bf16 v[132:135],v[68:71],v[16:19],v[116:119]
s_waitcnt lgkmcnt(5)
ds_read_u16 v152,v152
ds_read_b64_tr_b16 v[82:83],v4 offset:22560
v_mfma_f32_16x16x32_bf16 v[128:131],v[72:75],v[24:27],v[128:131]
s_cbranch_vccnz RA5_%=
s_mov_b64 exec,s[62:63]
s_add_u32 m0,s57,62784
s_add_u32 s64,s56,3735552
buffer_load_dwordx4 v2,s[36:39],s64 offen lds
s_add_u32 m0,s57,64512
s_nop 0
buffer_load_dwordx4 v3,s[36:39],s64 offen lds
s_mov_b64 exec,-1
RA5_%=:
ds_read_b64_tr_b16 v[84:85],v5 offset:20512
ds_read_b64_tr_b16 v[86:87],v5 offset:22560
v_mfma_f32_16x16x32_bf16 v[136:139],v[72:75],v[28:31],v[136:139]
s_waitcnt lgkmcnt(6)
ds_read_u16_d16_hi v154,v154
v_mfma_f32_16x16x32_bf16 v[140:143],v[76:79],v[28:31],v[140:143]
ds_read_u16 v153,v153
v_mfma_f32_16x16x32_bf16 v[132:135],v[76:79],v[24:27],v[132:135]
s_waitcnt lgkmcnt(4)
ds_read_u16_d16_hi v155,v155
v_lshl_add_u64 v[144:145],v[144:145],0,v[146:147]
v_mfma_f32_16x16x32_bf16 v[128:131],v[80:83],v[32:35],v[128:131]
v_lshl_add_u64 v[146:147],v[148:149],0,v[150:151]
v_mfma_f32_16x16x32_bf16 v[136:139],v[80:83],v[36:39],v[136:139]
s_waitcnt lgkmcnt(3)
ds_read_u16 v156,v156
v_mfma_f32_16x16x32_bf16 v[140:143],v[84:87],v[36:39],v[140:143]
ds_read_u16_d16_hi v158,v158
v_mfma_f32_16x16x32_bf16 v[132:135],v[84:87],v[32:35],v[132:135]
ds_read_u16 v157,v157
v_lshlrev_b32_sdwa v128,1,v128 dst_sel:DWORD dst_unused:UNUSED_PAD src0_sel:DWORD src1_sel:WORD_1
v_lshlrev_b32_sdwa v130,1,v130 dst_sel:DWORD dst_unused:UNUSED_PAD src0_sel:DWORD src1_sel:WORD_1
v_mfma_f32_16x16x32_bf16 v[208:211],v[88:91],v[144:147],v[208:211]
v_lshlrev_b32_sdwa v129,1,v129 dst_sel:DWORD dst_unused:UNUSED_PAD src0_sel:DWORD src1_sel:WORD_1
v_lshlrev_b32_sdwa v131,1,v131 dst_sel:DWORD dst_unused:UNUSED_PAD src0_sel:DWORD src1_sel:WORD_1
v_mfma_f32_16x16x32_bf16 v[216:219],v[92:95],v[144:147],v[216:219]
ds_read_u16_d16_hi v159,v159
v_mfma_f32_16x16x32_bf16 v[224:227],v[96:99],v[144:147],v[224:227]
v_lshlrev_b32_sdwa v132,1,v132 dst_sel:DWORD dst_unused:UNUSED_PAD src0_sel:DWORD src1_sel:WORD_1
v_mfma_f32_16x16x32_bf16 v[232:235],v[100:103],v[144:147],v[232:235]
s_waitcnt lgkmcnt(4)
ds_read_u16 v128,v128
v_lshl_add_u64 v[152:153],v[152:153],0,v[154:155]
v_lshlrev_b32_sdwa v134,1,v134 dst_sel:DWORD dst_unused:UNUSED_PAD src0_sel:DWORD src1_sel:WORD_1
v_mfma_f32_16x16x32_bf16 v[240:243],v[104:107],v[144:147],v[240:243]
s_waitcnt lgkmcnt(1)
ds_read_u16_d16_hi v130,v130
v_lshl_add_u64 v[154:155],v[156:157],0,v[158:159]
v_mfma_f32_16x16x32_bf16 v[248:251],v[108:111],v[144:147],v[248:251]
ds_read_u16 v129,v129
v_lshlrev_b32_sdwa v133,1,v133 dst_sel:DWORD dst_unused:UNUSED_PAD src0_sel:DWORD src1_sel:WORD_1
v_mfma_f32_16x16x32_bf16 v[212:215],v[88:91],v[152:155],v[212:215]
v_lshlrev_b32_sdwa v135,1,v135 dst_sel:DWORD dst_unused:UNUSED_PAD src0_sel:DWORD src1_sel:WORD_1
v_mfma_f32_16x16x32_bf16 v[220:223],v[92:95],v[152:155],v[220:223]
ds_read_u16_d16_hi v131,v131
v_lshlrev_b32_sdwa v136,1,v136 dst_sel:DWORD dst_unused:UNUSED_PAD src0_sel:DWORD src1_sel:WORD_1
v_mfma_f32_16x16x32_bf16 v[228:231],v[96:99],v[152:155],v[228:231]
ds_read_b64_tr_b16 v[88:89],v6 offset:18480
ds_read_b64_tr_b16 v[90:91],v6 offset:20016
v_lshlrev_b32_sdwa v138,1,v138 dst_sel:DWORD dst_unused:UNUSED_PAD src0_sel:DWORD src1_sel:WORD_1
v_mfma_f32_16x16x32_bf16 v[236:239],v[100:103],v[152:155],v[236:239]
ds_read_u16 v132,v132
v_mfma_f32_16x16x32_bf16 v[244:247],v[104:107],v[152:155],v[244:247]
ds_read_u16_d16_hi v134,v134
v_lshlrev_b32_sdwa v137,1,v137 dst_sel:DWORD dst_unused:UNUSED_PAD src0_sel:DWORD src1_sel:WORD_1
v_lshlrev_b32_sdwa v139,1,v139 dst_sel:DWORD dst_unused:UNUSED_PAD src0_sel:DWORD src1_sel:WORD_1
v_mfma_f32_16x16x32_bf16 v[252:255],v[108:111],v[152:155],v[252:255]
ds_read_b64_tr_b16 v[92:93],v6 offset:18488
ds_read_b64_tr_b16 v[94:95],v6 offset:20024
v_lshlrev_b32_sdwa v140,1,v140 dst_sel:DWORD dst_unused:UNUSED_PAD src0_sel:DWORD src1_sel:WORD_1
v_mfma_f32_16x16x32_bf16 v[144:147],v[64:67],v[40:43],v[112:115]
s_cbranch_vccnz RA6_%=
s_mov_b64 exec,s[62:63]
s_add_u32 m0,s57,66272
s_add_u32 s64,s56,3833856
buffer_load_dwordx4 v2,s[36:39],s64 offen lds
s_add_u32 m0,s57,68000
s_nop 0
buffer_load_dwordx4 v3,s[36:39],s64 offen lds
s_mov_b64 exec,-1
RA6_%=:
ds_read_u16 v133,v133
v_lshlrev_b32_sdwa v142,1,v142 dst_sel:DWORD dst_unused:UNUSED_PAD src0_sel:DWORD src1_sel:WORD_1
v_mfma_f32_16x16x32_bf16 v[152:155],v[64:67],v[44:47],v[112:115]
ds_read_b64_tr_b16 v[96:97],v6 offset:18544
v_lshlrev_b32_sdwa v141,1,v141 dst_sel:DWORD dst_unused:UNUSED_PAD src0_sel:DWORD src1_sel:WORD_1
v_lshlrev_b32_sdwa v143,1,v143 dst_sel:DWORD dst_unused:UNUSED_PAD src0_sel:DWORD src1_sel:WORD_1
v_mfma_f32_16x16x32_bf16 v[156:159],v[68:71],v[44:47],v[116:119]
ds_read_u16_d16_hi v135,v135
v_mfma_f32_16x16x32_bf16 v[148:151],v[68:71],v[40:43],v[116:119]
ds_read_u16 v136,v136
ds_read_b64_tr_b16 v[98:99],v6 offset:20080
v_mfma_f32_16x16x32_bf16 v[144:147],v[72:75],v[48:51],v[144:147]
ds_read_u16_d16_hi v138,v138
v_mfma_f32_16x16x32_bf16 v[152:155],v[72:75],v[52:55],v[152:155]
ds_read_b64_tr_b16 v[100:101],v6 offset:18552
ds_read_b64_tr_b16 v[102:103],v6 offset:20088
v_mfma_f32_16x16x32_bf16 v[156:159],v[76:79],v[52:55],v[156:159]
ds_read_u16 v137,v137
v_mfma_f32_16x16x32_bf16 v[148:151],v[76:79],v[48:51],v[148:151]
s_waitcnt lgkmcnt(6)
ds_read_u16_d16_hi v139,v139
v_lshl_add_u64 v[128:129],v[128:129],0,v[130:131]
v_lshl_add_u64 v[130:131],v[132:133],0,v[134:135]
v_mfma_f32_16x16x32_bf16 v[144:147],v[80:83],v[56:59],v[144:147]
ds_read_b64_tr_b16 v[104:105],v6 offset:18608
ds_read_b64_tr_b16 v[106:107],v6 offset:20144
v_mfma_f32_16x16x32_bf16 v[152:155],v[80:83],v[60:63],v[152:155]
ds_read_u16 v140,v140
v_mfma_f32_16x16x32_bf16 v[156:159],v[84:87],v[60:63],v[156:159]
ds_read_u16_d16_hi v142,v142
s_nop 1
v_lshlrev_b32_sdwa v144,1,v144 dst_sel:DWORD dst_unused:UNUSED_PAD src0_sel:DWORD src1_sel:WORD_1
v_mfma_f32_16x16x32_bf16 v[148:151],v[84:87],v[56:59],v[148:151]
ds_read_b64_tr_b16 v[108:109],v6 offset:18616
ds_read_b64_tr_b16 v[110:111],v6 offset:20152
v_lshlrev_b32_sdwa v146,1,v146 dst_sel:DWORD dst_unused:UNUSED_PAD src0_sel:DWORD src1_sel:WORD_1
v_mfma_f32_16x16x32_bf16 v[160:163],v[88:91],v[128:131],v[160:163]
ds_read_b64_tr_b16 v[64:65],v4 offset:12832
ds_read_u16 v141,v141
v_lshlrev_b32_sdwa v145,1,v145 dst_sel:DWORD dst_unused:UNUSED_PAD src0_sel:DWORD src1_sel:WORD_1
v_mfma_f32_16x16x32_bf16 v[168:171],v[92:95],v[128:131],v[168:171]
s_waitcnt lgkmcnt(13)
ds_read_u16_d16_hi v143,v143
v_lshlrev_b32_sdwa v147,1,v147 dst_sel:DWORD dst_unused:UNUSED_PAD src0_sel:DWORD src1_sel:WORD_1
v_mfma_f32_16x16x32_bf16 v[176:179],v[96:99],v[128:131],v[176:179]
s_waitcnt lgkmcnt(11)
ds_read_b64_tr_b16 v[66:67],v4 offset:14880
v_lshlrev_b32_sdwa v148,1,v148 dst_sel:DWORD dst_unused:UNUSED_PAD src0_sel:DWORD src1_sel:WORD_1
v_mfma_f32_16x16x32_bf16 v[184:187],v[100:103],v[128:131],v[184:187]
s_waitcnt lgkmcnt(8)
ds_read_u16 v144,v144
v_lshl_add_u64 v[136:137],v[136:137],0,v[138:139]
v_lshlrev_b32_sdwa v150,1,v150 dst_sel:DWORD dst_unused:UNUSED_PAD src0_sel:DWORD src1_sel:WORD_1
v_mfma_f32_16x16x32_bf16 v[192:195],v[104:107],v[128:131],v[192:195]
s_waitcnt lgkmcnt(2)
ds_read_u16_d16_hi v146,v146
v_lshl_add_u64 v[138:139],v[140:141],0,v[142:143]
v_lshlrev_b32_sdwa v149,1,v149 dst_sel:DWORD dst_unused:UNUSED_PAD src0_sel:DWORD src1_sel:WORD_1
v_mfma_f32_16x16x32_bf16 v[200:203],v[108:111],v[128:131],v[200:203]
ds_read_b128 v[112:115],v7 offset:1152
v_mfma_f32_16x16x32_bf16 v[164:167],v[88:91],v[136:139],v[164:167]
ds_read_u16 v145,v145
v_lshlrev_b32_sdwa v151,1,v151 dst_sel:DWORD dst_unused:UNUSED_PAD src0_sel:DWORD src1_sel:WORD_1
v_mfma_f32_16x16x32_bf16 v[172:175],v[92:95],v[136:139],v[172:175]
ds_read_u16_d16_hi v147,v147
ds_read_b64_tr_b16 v[68:69],v5 offset:12832
v_lshlrev_b32_sdwa v152,1,v152 dst_sel:DWORD dst_unused:UNUSED_PAD src0_sel:DWORD src1_sel:WORD_1
v_mfma_f32_16x16x32_bf16 v[180:183],v[96:99],v[136:139],v[180:183]
ds_read_b64_tr_b16 v[70:71],v5 offset:14880
ds_read_b128 v[116:119],v7 offset:1184
v_lshlrev_b32_sdwa v154,1,v154 dst_sel:DWORD dst_unused:UNUSED_PAD src0_sel:DWORD src1_sel:WORD_1
v_mfma_f32_16x16x32_bf16 v[188:191],v[100:103],v[136:139],v[188:191]
s_cbranch_vccnz RA7_%=
s_mov_b64 exec,s[62:63]
s_add_u32 m0,s57,0
s_add_u32 s64,s56,3932160
buffer_load_dwordx4 v2,s[36:39],s64 offen lds
s_add_u32 m0,s57,1728
s_nop 0
buffer_load_dwordx4 v3,s[36:39],s64 offen lds
s_mov_b64 exec,-1
RA7_%=:
ds_read_u16 v148,v148
ds_read_b64_tr_b16 v[72:73],v4 offset:16928
v_lshlrev_b32_sdwa v153,1,v153 dst_sel:DWORD dst_unused:UNUSED_PAD src0_sel:DWORD src1_sel:WORD_1
v_mfma_f32_16x16x32_bf16 v[196:199],v[104:107],v[136:139],v[196:199]
ds_read_u16_d16_hi v150,v150
v_lshlrev_b32_sdwa v155,1,v155 dst_sel:DWORD dst_unused:UNUSED_PAD src0_sel:DWORD src1_sel:WORD_1
v_mfma_f32_16x16x32_bf16 v[204:207],v[108:111],v[136:139],v[204:207]
s_waitcnt lgkmcnt(8)
ds_read_b64_tr_b16 v[74:75],v4 offset:18976
s_mov_b32 s86,0xbe801d5a
ds_read_b64_tr_b16 v[76:77],v5 offset:16928
v_mfma_f32_16x16x32_bf16 v[128:131],v[64:67],v[16:19],v[112:115]
ds_read_u16 v149,v149
v_lshlrev_b32_sdwa v156,1,v156 dst_sel:DWORD dst_unused:UNUSED_PAD src0_sel:DWORD src1_sel:WORD_1
v_lshlrev_b32_sdwa v158,1,v158 dst_sel:DWORD dst_unused:UNUSED_PAD src0_sel:DWORD src1_sel:WORD_1
v_mfma_f32_16x16x32_bf16 v[136:139],v[64:67],v[20:23],v[112:115]
s_waitcnt lgkmcnt(6)
ds_read_b64_tr_b16 v[78:79],v5 offset:18976
ds_read_b64_tr_b16 v[80:81],v4 offset:21024
v_lshlrev_b32_sdwa v157,1,v157 dst_sel:DWORD dst_unused:UNUSED_PAD src0_sel:DWORD src1_sel:WORD_1
v_mfma_f32_16x16x32_bf16 v[140:143],v[68:71],v[20:23],v[116:119]
ds_read_u16_d16_hi v151,v151
v_lshlrev_b32_sdwa v159,1,v159 dst_sel:DWORD dst_unused:UNUSED_PAD src0_sel:DWORD src1_sel:WORD_1
v_mfma_f32_16x16x32_bf16 v[132:135],v[68:71],v[16:19],v[116:119]
s_waitcnt lgkmcnt(5)
ds_read_u16 v152,v152
ds_read_b64_tr_b16 v[82:83],v4 offset:23072
v_mfma_f32_16x16x32_bf16 v[128:131],v[72:75],v[24:27],v[128:131]
ds_read_b64_tr_b16 v[84:85],v5 offset:21024
ds_read_b64_tr_b16 v[86:87],v5 offset:23072
v_mfma_f32_16x16x32_bf16 v[136:139],v[72:75],v[28:31],v[136:139]
s_waitcnt lgkmcnt(6)
ds_read_u16_d16_hi v154,v154
v_mfma_f32_16x16x32_bf16 v[140:143],v[76:79],v[28:31],v[140:143]
ds_read_u16 v153,v153
v_mfma_f32_16x16x32_bf16 v[132:135],v[76:79],v[24:27],v[132:135]
s_waitcnt lgkmcnt(4)
ds_read_u16_d16_hi v155,v155
v_lshl_add_u64 v[144:145],v[144:145],0,v[146:147]
v_mfma_f32_16x16x32_bf16 v[128:131],v[80:83],v[32:35],v[128:131]
v_lshl_add_u64 v[146:147],v[148:149],0,v[150:151]
v_mfma_f32_16x16x32_bf16 v[136:139],v[80:83],v[36:39],v[136:139]
s_waitcnt lgkmcnt(3)
ds_read_u16 v156,v156
v_mfma_f32_16x16x32_bf16 v[140:143],v[84:87],v[36:39],v[140:143]
ds_read_u16_d16_hi v158,v158
v_mfma_f32_16x16x32_bf16 v[132:135],v[84:87],v[32:35],v[132:135]
ds_read_u16 v157,v157
v_lshlrev_b32_sdwa v128,1,v128 dst_sel:DWORD dst_unused:UNUSED_PAD src0_sel:DWORD src1_sel:WORD_1
v_lshlrev_b32_sdwa v130,1,v130 dst_sel:DWORD dst_unused:UNUSED_PAD src0_sel:DWORD src1_sel:WORD_1
v_mfma_f32_16x16x32_bf16 v[208:211],v[88:91],v[144:147],v[208:211]
s_barrier
s_setprio 3
v_lshlrev_b32_sdwa v129,1,v129 dst_sel:DWORD dst_unused:UNUSED_PAD src0_sel:DWORD src1_sel:WORD_1
v_lshlrev_b32_sdwa v131,1,v131 dst_sel:DWORD dst_unused:UNUSED_PAD src0_sel:DWORD src1_sel:WORD_1
v_mfma_f32_16x16x32_bf16 v[216:219],v[92:95],v[144:147],v[216:219]
ds_read_u16_d16_hi v159,v159
ds_read_u16 v128,v128
v_fma_mixlo_f16 v120,v140,s99,0
v_fma_mixlo_f16 v122,v141,s99,0
v_mfma_f32_16x16x32_bf16 v[224:227],v[96:99],v[144:147],v[224:227]
s_cbranch_vccz RA8_%=
s_add_u32 m0,s58,12320
s_mov_b32 s69,s60
buffer_load_dwordx4 v2,s[44:47],s69 offen lds
s_add_u32 m0,m0,1024
s_add_u32 s69,s69,s53
buffer_load_dwordx4 v3,s[44:47],s69 offen lds
s_add_u32 m0,m0,1024
s_add_u32 s69,s69,s53
buffer_load_dwordx4 v2,s[44:47],s69 offen lds
s_add_u32 m0,m0,s50
s_add_u32 s69,s69,s53
buffer_load_dwordx4 v3,s[44:47],s69 offen lds
RA8_%=:
ds_read_u16_d16_hi v130,v130
v_lshlrev_b32_sdwa v132,1,v132 dst_sel:DWORD dst_unused:UNUSED_PAD src0_sel:DWORD src1_sel:WORD_1
v_lshlrev_b32_sdwa v134,1,v134 dst_sel:DWORD dst_unused:UNUSED_PAD src0_sel:DWORD src1_sel:WORD_1
v_mfma_f32_16x16x32_bf16 v[232:235],v[100:103],v[144:147],v[232:235]
s_waitcnt lgkmcnt(6)
v_lshl_add_u64 v[152:153],v[152:153],0,v[154:155]
v_mfma_f32_16x16x32_bf16 v[240:243],v[104:107],v[144:147],v[240:243]
s_waitcnt lgkmcnt(2)
s_cbranch_vccnz RA9_%=
s_mov_b64 exec,s[62:63]
s_add_u32 m0,s57,3488
s_add_u32 s64,s56,4030464
buffer_load_dwordx4 v2,s[36:39],s64 offen lds
s_add_u32 m0,s57,5216
s_nop 0
buffer_load_dwordx4 v3,s[36:39],s64 offen lds
s_mov_b64 exec,-1
RA9_%=:
v_lshl_add_u64 v[154:155],v[156:157],0,v[158:159]
v_fma_mixhi_f16 v120,v142,s99,0
v_mfma_f32_16x16x32_bf16 v[248:251],v[108:111],v[144:147],v[248:251]
ds_read_u16 v129,v129
v_fma_mixhi_f16 v122,v143,s99,0
v_mfma_f32_16x16x32_bf16 v[212:215],v[88:91],v[152:155],v[212:215]
s_cbranch_vccz RA10_%=
s_add_u32 m0,m0,1024
s_add_u32 s69,s69,s53
buffer_load_dwordx4 v2,s[44:47],s69 offen lds
s_add_u32 m0,m0,1024
s_add_u32 s69,s69,s53
buffer_load_dwordx4 v3,s[44:47],s69 offen lds
s_add_u32 m0,m0,s50
s_add_u32 s69,s69,s51
buffer_load_dwordx4 v2,s[44:47],s69 offen lds
s_add_u32 m0,m0,1024
s_add_u32 s69,s69,s53
buffer_load_dwordx4 v3,s[44:47],s69 offen lds
RA10_%=:
v_lshlrev_b32_sdwa v133,1,v133 dst_sel:DWORD dst_unused:UNUSED_PAD src0_sel:DWORD src1_sel:WORD_1
v_pk_fma_f16 v121,v120,v120,s54
v_lshlrev_b32_sdwa v135,1,v135 dst_sel:DWORD dst_unused:UNUSED_PAD src0_sel:DWORD src1_sel:WORD_1
v_mfma_f32_16x16x32_bf16 v[220:223],v[92:95],v[152:155],v[220:223]
ds_read_u16_d16_hi v131,v131
ds_read_u16 v132,v132
v_pk_fma_f16 v123,v122,v122,s54
v_mfma_f32_16x16x32_bf16 v[228:231],v[96:99],v[152:155],v[228:231]
ds_read_b64_tr_b16 v[88:89],v6 offset:0
v_pk_mul_f16 v121,v120,v121
v_lshlrev_b32_sdwa v136,1,v136 dst_sel:DWORD dst_unused:UNUSED_PAD src0_sel:DWORD src1_sel:WORD_1
v_pk_mul_f16 v123,v122,v123
v_mfma_f32_16x16x32_bf16 v[236:239],v[100:103],v[152:155],v[236:239]
s_cbranch_vccz RA11_%=
s_add_u32 m0,m0,1024
s_add_u32 s69,s69,s53
buffer_load_dwordx4 v2,s[44:47],s69 offen lds
s_add_u32 m0,m0,s50
s_add_u32 s69,s69,s53
buffer_load_dwordx4 v3,s[44:47],s69 offen lds
s_add_u32 m0,m0,1024
s_add_u32 s69,s69,s53
buffer_load_dwordx4 v2,s[44:47],s69 offen lds
s_add_u32 m0,m0,1024
s_add_u32 s69,s69,s53
buffer_load_dwordx4 v3,s[44:47],s69 offen lds
RA11_%=:
ds_read_b64_tr_b16 v[90:91],v6 offset:1536
v_lshlrev_b32_sdwa v138,1,v138 dst_sel:DWORD dst_unused:UNUSED_PAD src0_sel:DWORD src1_sel:WORD_1
v_mfma_f32_16x16x32_bf16 v[244:247],v[104:107],v[152:155],v[244:247]
ds_read_u16_d16_hi v134,v134
v_exp_f16 v120,v121
v_mfma_f32_16x16x32_bf16 v[252:255],v[108:111],v[152:155],v[252:255]
ds_read_b64_tr_b16 v[92:93],v6 offset:8
v_lshlrev_b32_sdwa v137,1,v137 dst_sel:DWORD dst_unused:UNUSED_PAD src0_sel:DWORD src1_sel:WORD_1
v_mfma_f32_16x16x32_bf16 v[144:147],v[64:67],v[40:43],v[112:115]
ds_read_b64_tr_b16 v[94:95],v6 offset:1544
v_lshlrev_b32_sdwa v139,1,v139 dst_sel:DWORD dst_unused:UNUSED_PAD src0_sel:DWORD src1_sel:WORD_1
v_exp_f16 v122,v123
v_mfma_f32_16x16x32_bf16 v[152:155],v[64:67],v[44:47],v[112:115]
ds_read_u16 v133,v133
ds_read_b64_tr_b16 v[96:97],v6 offset:64
v_mfma_f32_16x16x32_bf16 v[156:159],v[68:71],v[44:47],v[116:119]
ds_read_u16_d16_hi v135,v135
v_exp_f16_sdwa v122,v123 dst_sel:WORD_1 dst_unused:UNUSED_PRESERVE src0_sel:WORD_1
v_exp_f16_sdwa v120,v121 dst_sel:WORD_1 dst_unused:UNUSED_PRESERVE src0_sel:WORD_1
v_mfma_f32_16x16x32_bf16 v[148:151],v[68:71],v[40:43],v[116:119]
ds_read_b64_tr_b16 v[98:99],v6 offset:1600
v_pk_fma_f16 v120,v120,s55,s55
v_pk_fma_f16 v122,v122,s55,s55
v_mfma_f32_16x16x32_bf16 v[144:147],v[72:75],v[48:51],v[144:147]
v_rcp_f16 v121,v120
v_mfma_f32_16x16x32_bf16 v[152:155],v[72:75],v[52:55],v[152:155]
ds_read_b64_tr_b16 v[100:101],v6 offset:72
v_rcp_f16 v123,v122
v_mfma_f32_16x16x32_bf16 v[156:159],v[76:79],v[52:55],v[156:159]
ds_read_u16 v136,v136
v_rcp_f16_sdwa v121,v120 dst_sel:WORD_1 dst_unused:UNUSED_PRESERVE src0_sel:WORD_1
v_mfma_f32_16x16x32_bf16 v[148:151],v[76:79],v[48:51],v[148:151]
s_cbranch_vccnz RA12_%=
s_mov_b64 exec,s[62:63]
s_add_u32 m0,s57,6976
s_add_u32 s64,s56,4128768
buffer_load_dwordx4 v2,s[36:39],s64 offen lds
s_add_u32 m0,s57,8704
s_nop 0
buffer_load_dwordx4 v3,s[36:39],s64 offen lds
s_mov_b64 exec,-1
RA12_%=:
ds_read_b64_tr_b16 v[102:103],v6 offset:1608
ds_read_b64_tr_b16 v[104:105],v6 offset:128
v_rcp_f16_sdwa v123,v122 dst_sel:WORD_1 dst_unused:UNUSED_PRESERVE src0_sel:WORD_1
v_mfma_f32_16x16x32_bf16 v[144:147],v[80:83],v[56:59],v[144:147]
ds_read_u16_d16_hi v138,v138
v_fma_mix_f32 v142,v142,v121,0 op_sel:[0,1,0] op_sel_hi:[0,1,0]
v_mfma_f32_16x16x32_bf16 v[152:155],v[80:83],v[60:63],v[152:155]
s_waitcnt lgkmcnt(6)
ds_read_b64_tr_b16 v[106:107],v6 offset:1664
ds_read_b64_tr_b16 v[108:109],v6 offset:136
v_fma_mix_f32 v140,v140,v121,0 op_sel_hi:[0,1,0]
v_lshl_add_u64 v[128:129],v[128:129],0,v[130:131]
v_lshl_add_u64 v[130:131],v[132:133],0,v[134:135]
v_mfma_f32_16x16x32_bf16 v[156:159],v[84:87],v[60:63],v[156:159]
ds_read_u16 v137,v137
v_fma_mix_f32 v141,v141,v123,0 op_sel_hi:[0,1,0]
v_fma_mix_f32 v143,v143,v123,0 op_sel:[0,1,0] op_sel_hi:[0,1,0]
v_mfma_f32_16x16x32_bf16 v[148:151],v[84:87],v[56:59],v[148:151]
ds_read_b64_tr_b16 v[110:111],v6 offset:1672
ds_read_b64_tr_b16 v[64:65],v4 offset:0
v_lshlrev_b32_sdwa v144,1,v144 dst_sel:DWORD dst_unused:UNUSED_PAD src0_sel:DWORD src1_sel:WORD_1
v_lshlrev_b32_sdwa v146,1,v146 dst_sel:DWORD dst_unused:UNUSED_PAD src0_sel:DWORD src1_sel:WORD_1
v_mfma_f32_16x16x32_bf16 v[160:163],v[88:91],v[128:131],v[160:163]
ds_read_u16_d16_hi v139,v139
v_lshlrev_b32_sdwa v145,1,v145 dst_sel:DWORD dst_unused:UNUSED_PAD src0_sel:DWORD src1_sel:WORD_1
v_lshlrev_b32_sdwa v147,1,v147 dst_sel:DWORD dst_unused:UNUSED_PAD src0_sel:DWORD src1_sel:WORD_1
v_fma_mixlo_f16 v122,v157,s99,0
v_mfma_f32_16x16x32_bf16 v[168:171],v[92:95],v[128:131],v[168:171]
s_waitcnt lgkmcnt(11)
ds_read_b64_tr_b16 v[66:67],v4 offset:2048
ds_read_b128 v[112:115],v7 offset:1280
v_fma_mixlo_f16 v120,v156,s99,0
v_mfma_f32_16x16x32_bf16 v[176:179],v[96:99],v[128:131],v[176:179]
s_waitcnt lgkmcnt(10)
ds_read_u16 v144,v144
v_lshlrev_b32_sdwa v148,1,v148 dst_sel:DWORD dst_unused:UNUSED_PAD src0_sel:DWORD src1_sel:WORD_1
v_lshlrev_b32_sdwa v150,1,v150 dst_sel:DWORD dst_unused:UNUSED_PAD src0_sel:DWORD src1_sel:WORD_1
v_mfma_f32_16x16x32_bf16 v[184:187],v[100:103],v[128:131],v[184:187]
s_waitcnt lgkmcnt(3)
ds_read_b64_tr_b16 v[68:69],v5 offset:0
ds_read_b64_tr_b16 v[70:71],v5 offset:2048
v_lshl_add_u64 v[136:137],v[136:137],0,v[138:139]
v_fma_mixhi_f16 v120,v158,s99,0
v_mfma_f32_16x16x32_bf16 v[192:195],v[104:107],v[128:131],v[192:195]
ds_read_u16_d16_hi v146,v146
v_cvt_pk_bf16_f32 v138,v140,v142
v_cvt_pk_bf16_f32 v139,v141,v143
v_mfma_f32_16x16x32_bf16 v[200:203],v[108:111],v[128:131],v[200:203]
v_fma_mixhi_f16 v122,v159,s99,0
v_mfma_f32_16x16x32_bf16 v[164:167],v[88:91],v[136:139],v[164:167]
ds_read_b128 v[116:119],v7 offset:1312
ds_read_b64_tr_b16 v[72:73],v4 offset:4096
v_lshlrev_b32_sdwa v149,1,v149 dst_sel:DWORD dst_unused:UNUSED_PAD src0_sel:DWORD src1_sel:WORD_1
v_lshlrev_b32_sdwa v151,1,v151 dst_sel:DWORD dst_unused:UNUSED_PAD src0_sel:DWORD src1_sel:WORD_1
v_mfma_f32_16x16x32_bf16 v[172:175],v[92:95],v[136:139],v[172:175]
ds_read_b64_tr_b16 v[74:75],v4 offset:6144
ds_read_b64_tr_b16 v[76:77],v5 offset:4096
v_pk_fma_f16 v121,v120,v120,s54
v_pk_fma_f16 v123,v122,v122,s54
v_mfma_f32_16x16x32_bf16 v[180:183],v[96:99],v[136:139],v[180:183]
ds_read_u16 v145,v145
v_pk_mul_f16 v121,v120,v121
v_pk_mul_f16 v123,v122,v123
v_mfma_f32_16x16x32_bf16 v[188:191],v[100:103],v[136:139],v[188:191]
s_cbranch_vccnz RA13_%=
s_mov_b64 exec,s[62:63]
s_add_u32 m0,s57,10464
s_add_u32 s64,s56,4227072
buffer_load_dwordx4 v2,s[36:39],s64 offen lds
s_add_u32 m0,s57,12192
s_nop 0
buffer_load_dwordx4 v3,s[36:39],s64 offen lds
s_mov_b64 exec,-1
RA13_%=:
ds_read_u16_d16_hi v147,v147
v_lshlrev_b32_sdwa v152,1,v152 dst_sel:DWORD dst_unused:UNUSED_PAD src0_sel:DWORD src1_sel:WORD_1
v_lshlrev_b32_sdwa v154,1,v154 dst_sel:DWORD dst_unused:UNUSED_PAD src0_sel:DWORD src1_sel:WORD_1
v_mfma_f32_16x16x32_bf16 v[196:199],v[104:107],v[136:139],v[196:199]
v_exp_f16 v120,v121
v_mfma_f32_16x16x32_bf16 v[204:207],v[108:111],v[136:139],v[204:207]
s_waitcnt lgkmcnt(10)
ds_read_b64_tr_b16 v[78:79],v5 offset:6144
ds_read_b64_tr_b16 v[80:81],v4 offset:8192
v_exp_f16 v122,v123
v_mfma_f32_16x16x32_bf16 v[128:131],v[64:67],v[16:19],v[112:115]
v_lshlrev_b32_sdwa v153,1,v153 dst_sel:DWORD dst_unused:UNUSED_PAD src0_sel:DWORD src1_sel:WORD_1
v_lshlrev_b32_sdwa v155,1,v155 dst_sel:DWORD dst_unused:UNUSED_PAD src0_sel:DWORD src1_sel:WORD_1
v_mfma_f32_16x16x32_bf16 v[136:139],v[64:67],v[20:23],v[112:115]
s_waitcnt lgkmcnt(7)
ds_read_u16 v148,v148
ds_read_u16_d16_hi v150,v150
v_exp_f16_sdwa v120,v121 dst_sel:WORD_1 dst_unused:UNUSED_PRESERVE src0_sel:WORD_1
v_mfma_f32_16x16x32_bf16 v[140:143],v[68:71],v[20:23],v[116:119]
v_exp_f16_sdwa v122,v123 dst_sel:WORD_1 dst_unused:UNUSED_PRESERVE src0_sel:WORD_1
v_mfma_f32_16x16x32_bf16 v[132:135],v[68:71],v[16:19],v[116:119]
s_waitcnt lgkmcnt(7)
ds_read_b64_tr_b16 v[82:83],v4 offset:10240
v_pk_fma_f16 v120,v120,s55,s55
v_mfma_f32_16x16x32_bf16 v[128:131],v[72:75],v[24:27],v[128:131]
ds_read_b64_tr_b16 v[84:85],v5 offset:8192
v_pk_fma_f16 v122,v122,s55,s55
v_rcp_f16 v121,v120
v_mfma_f32_16x16x32_bf16 v[136:139],v[72:75],v[28:31],v[136:139]
s_waitcnt lgkmcnt(5)
ds_read_b64_tr_b16 v[86:87],v5 offset:10240
ds_read_u16 v149,v149
v_rcp_f16 v123,v122
v_mfma_f32_16x16x32_bf16 v[140:143],v[76:79],v[28:31],v[140:143]
ds_read_u16_d16_hi v151,v151
ds_read_u16 v152,v152
v_rcp_f16_sdwa v121,v120 dst_sel:WORD_1 dst_unused:UNUSED_PRESERVE src0_sel:WORD_1
v_mfma_f32_16x16x32_bf16 v[132:135],v[76:79],v[24:27],v[132:135]
s_waitcnt lgkmcnt(5)
v_rcp_f16_sdwa v123,v122 dst_sel:WORD_1 dst_unused:UNUSED_PRESERVE src0_sel:WORD_1
v_mfma_f32_16x16x32_bf16 v[128:131],v[80:83],v[32:35],v[128:131]
ds_read_u16_d16_hi v154,v154
v_fma_mix_f32 v156,v156,v121,0 op_sel_hi:[0,1,0]
v_fma_mix_f32 v158,v158,v121,0 op_sel:[0,1,0] op_sel_hi:[0,1,0]
v_mfma_f32_16x16x32_bf16 v[136:139],v[80:83],v[36:39],v[136:139]
s_waitcnt lgkmcnt(2)
v_lshl_add_u64 v[144:145],v[144:145],0,v[146:147]
v_lshl_add_u64 v[146:147],v[148:149],0,v[150:151]
v_mfma_f32_16x16x32_bf16 v[140:143],v[84:87],v[36:39],v[140:143]
ds_read_u16 v153,v153
v_fma_mix_f32 v157,v157,v123,0 op_sel_hi:[0,1,0]
v_fma_mix_f32 v159,v159,v123,0 op_sel:[0,1,0] op_sel_hi:[0,1,0]
v_mfma_f32_16x16x32_bf16 v[132:135],v[84:87],v[32:35],v[132:135]
v_lshlrev_b32_sdwa v128,1,v128 dst_sel:DWORD dst_unused:UNUSED_PAD src0_sel:DWORD src1_sel:WORD_1
v_lshlrev_b32_sdwa v130,1,v130 dst_sel:DWORD dst_unused:UNUSED_PAD src0_sel:DWORD src1_sel:WORD_1
v_mfma_f32_16x16x32_bf16 v[208:211],v[88:91],v[144:147],v[208:211]
s_cbranch_vccnz RA14_%=
s_mov_b64 exec,s[62:63]
s_add_u32 m0,s57,13952
s_add_u32 s64,s56,4325376
buffer_load_dwordx4 v2,s[36:39],s64 offen lds
s_add_u32 m0,s57,15680
s_nop 0
buffer_load_dwordx4 v3,s[36:39],s64 offen lds
s_mov_b64 exec,-1
RA14_%=:
v_lshlrev_b32_sdwa v129,1,v129 dst_sel:DWORD dst_unused:UNUSED_PAD src0_sel:DWORD src1_sel:WORD_1
v_lshlrev_b32_sdwa v131,1,v131 dst_sel:DWORD dst_unused:UNUSED_PAD src0_sel:DWORD src1_sel:WORD_1
v_mfma_f32_16x16x32_bf16 v[216:219],v[92:95],v[144:147],v[216:219]
ds_read_u16_d16_hi v155,v155
ds_read_u16 v128,v128
v_fma_mixlo_f16 v120,v140,s99,0
v_fma_mixlo_f16 v122,v141,s99,0
v_mfma_f32_16x16x32_bf16 v[224:227],v[96:99],v[144:147],v[224:227]
ds_read_u16_d16_hi v130,v130
v_lshlrev_b32_sdwa v132,1,v132 dst_sel:DWORD dst_unused:UNUSED_PAD src0_sel:DWORD src1_sel:WORD_1
v_lshlrev_b32_sdwa v134,1,v134 dst_sel:DWORD dst_unused:UNUSED_PAD src0_sel:DWORD src1_sel:WORD_1
v_mfma_f32_16x16x32_bf16 v[232:235],v[100:103],v[144:147],v[232:235]
s_waitcnt lgkmcnt(2)
v_lshl_add_u64 v[152:153],v[152:153],0,v[154:155]
v_cvt_pk_bf16_f32 v154,v156,v158
v_mfma_f32_16x16x32_bf16 v[240:243],v[104:107],v[144:147],v[240:243]
v_fma_mixhi_f16 v120,v142,s99,0
v_cvt_pk_bf16_f32 v155,v157,v159
v_mfma_f32_16x16x32_bf16 v[248:251],v[108:111],v[144:147],v[248:251]
s_setprio 2
ds_read_u16 v129,v129
v_fma_mixhi_f16 v122,v143,s99,0
v_mfma_f32_16x16x32_bf16 v[212:215],v[88:91],v[152:155],v[212:215]
v_lshlrev_b32_sdwa v133,1,v133 dst_sel:DWORD dst_unused:UNUSED_PAD src0_sel:DWORD src1_sel:WORD_1
v_pk_fma_f16 v121,v120,v120,s54
v_lshlrev_b32_sdwa v135,1,v135 dst_sel:DWORD dst_unused:UNUSED_PAD src0_sel:DWORD src1_sel:WORD_1
v_mfma_f32_16x16x32_bf16 v[220:223],v[92:95],v[152:155],v[220:223]
ds_read_u16_d16_hi v131,v131
ds_read_u16 v132,v132
v_pk_fma_f16 v123,v122,v122,s54
v_mfma_f32_16x16x32_bf16 v[228:231],v[96:99],v[152:155],v[228:231]
ds_read_b64_tr_b16 v[88:89],v6 offset:6160
v_pk_mul_f16 v121,v120,v121
v_lshlrev_b32_sdwa v136,1,v136 dst_sel:DWORD dst_unused:UNUSED_PAD src0_sel:DWORD src1_sel:WORD_1
v_pk_mul_f16 v123,v122,v123
v_mfma_f32_16x16x32_bf16 v[236:239],v[100:103],v[152:155],v[236:239]
ds_read_b64_tr_b16 v[90:91],v6 offset:7696
v_lshlrev_b32_sdwa v138,1,v138 dst_sel:DWORD dst_unused:UNUSED_PAD src0_sel:DWORD src1_sel:WORD_1
v_mfma_f32_16x16x32_bf16 v[244:247],v[104:107],v[152:155],v[244:247]
ds_read_u16_d16_hi v134,v134
v_exp_f16 v120,v121
v_mfma_f32_16x16x32_bf16 v[252:255],v[108:111],v[152:155],v[252:255]
ds_read_b64_tr_b16 v[92:93],v6 offset:6168
v_lshlrev_b32_sdwa v137,1,v137 dst_sel:DWORD dst_unused:UNUSED_PAD src0_sel:DWORD src1_sel:WORD_1
v_mfma_f32_16x16x32_bf16 v[144:147],v[64:67],v[40:43],v[112:115]
ds_read_b64_tr_b16 v[94:95],v6 offset:7704
v_lshlrev_b32_sdwa v139,1,v139 dst_sel:DWORD dst_unused:UNUSED_PAD src0_sel:DWORD src1_sel:WORD_1
v_exp_f16 v122,v123
v_mfma_f32_16x16x32_bf16 v[152:155],v[64:67],v[44:47],v[112:115]
ds_read_u16 v133,v133
ds_read_b64_tr_b16 v[96:97],v6 offset:6224
v_mfma_f32_16x16x32_bf16 v[156:159],v[68:71],v[44:47],v[116:119]
ds_read_u16_d16_hi v135,v135
v_exp_f16_sdwa v122,v123 dst_sel:WORD_1 dst_unused:UNUSED_PRESERVE src0_sel:WORD_1
v_exp_f16_sdwa v120,v121 dst_sel:WORD_1 dst_unused:UNUSED_PRESERVE src0_sel:WORD_1
v_mfma_f32_16x16x32_bf16 v[148:151],v[68:71],v[40:43],v[116:119]
s_cbranch_vccnz RA15_%=
s_mov_b64 exec,s[62:63]
s_add_u32 m0,s57,17440
s_add_u32 s64,s56,4423680
buffer_load_dwordx4 v2,s[36:39],s64 offen lds
s_add_u32 m0,s57,19168
s_nop 0
buffer_load_dwordx4 v3,s[36:39],s64 offen lds
s_mov_b64 exec,-1
RA15_%=:
ds_read_b64_tr_b16 v[98:99],v6 offset:7760
v_pk_fma_f16 v120,v120,s55,s55
v_pk_fma_f16 v122,v122,s55,s55
v_mfma_f32_16x16x32_bf16 v[144:147],v[72:75],v[48:51],v[144:147]
v_rcp_f16 v121,v120
v_mfma_f32_16x16x32_bf16 v[152:155],v[72:75],v[52:55],v[152:155]
ds_read_b64_tr_b16 v[100:101],v6 offset:6232
v_rcp_f16 v123,v122
v_mfma_f32_16x16x32_bf16 v[156:159],v[76:79],v[52:55],v[156:159]
ds_read_u16 v136,v136
v_rcp_f16_sdwa v121,v120 dst_sel:WORD_1 dst_unused:UNUSED_PRESERVE src0_sel:WORD_1
v_mfma_f32_16x16x32_bf16 v[148:151],v[76:79],v[48:51],v[148:151]
ds_read_b64_tr_b16 v[102:103],v6 offset:7768
ds_read_b64_tr_b16 v[104:105],v6 offset:6288
v_rcp_f16_sdwa v123,v122 dst_sel:WORD_1 dst_unused:UNUSED_PRESERVE src0_sel:WORD_1
v_mfma_f32_16x16x32_bf16 v[144:147],v[80:83],v[56:59],v[144:147]
ds_read_u16_d16_hi v138,v138
v_fma_mix_f32 v142,v142,v121,0 op_sel:[0,1,0] op_sel_hi:[0,1,0]
v_mfma_f32_16x16x32_bf16 v[152:155],v[80:83],v[60:63],v[152:155]
s_waitcnt lgkmcnt(6)
ds_read_b64_tr_b16 v[106:107],v6 offset:7824
ds_read_b64_tr_b16 v[108:109],v6 offset:6296
v_fma_mix_f32 v140,v140,v121,0 op_sel_hi:[0,1,0]
v_lshl_add_u64 v[128:129],v[128:129],0,v[130:131]
v_lshl_add_u64 v[130:131],v[132:133],0,v[134:135]
v_mfma_f32_16x16x32_bf16 v[156:159],v[84:87],v[60:63],v[156:159]
ds_read_u16 v137,v137
v_fma_mix_f32 v141,v141,v123,0 op_sel_hi:[0,1,0]
v_fma_mix_f32 v143,v143,v123,0 op_sel:[0,1,0] op_sel_hi:[0,1,0]
v_mfma_f32_16x16x32_bf16 v[148:151],v[84:87],v[56:59],v[148:151]
ds_read_b64_tr_b16 v[110:111],v6 offset:7832
ds_read_b64_tr_b16 v[64:65],v4 offset:512
v_lshlrev_b32_sdwa v144,1,v144 dst_sel:DWORD dst_unused:UNUSED_PAD src0_sel:DWORD src1_sel:WORD_1
v_lshlrev_b32_sdwa v146,1,v146 dst_sel:DWORD dst_unused:UNUSED_PAD src0_sel:DWORD src1_sel:WORD_1
v_mfma_f32_16x16x32_bf16 v[160:163],v[88:91],v[128:131],v[160:163]
ds_read_u16_d16_hi v139,v139
v_lshlrev_b32_sdwa v145,1,v145 dst_sel:DWORD dst_unused:UNUSED_PAD src0_sel:DWORD src1_sel:WORD_1
v_lshlrev_b32_sdwa v147,1,v147 dst_sel:DWORD dst_unused:UNUSED_PAD src0_sel:DWORD src1_sel:WORD_1
v_fma_mixlo_f16 v122,v157,s99,0
v_mfma_f32_16x16x32_bf16 v[168:171],v[92:95],v[128:131],v[168:171]
s_waitcnt lgkmcnt(11)
ds_read_b64_tr_b16 v[66:67],v4 offset:2560
ds_read_b128 v[112:115],v7 offset:1408
v_fma_mixlo_f16 v120,v156,s99,0
v_mfma_f32_16x16x32_bf16 v[176:179],v[96:99],v[128:131],v[176:179]
s_waitcnt lgkmcnt(10)
ds_read_u16 v144,v144
v_lshlrev_b32_sdwa v148,1,v148 dst_sel:DWORD dst_unused:UNUSED_PAD src0_sel:DWORD src1_sel:WORD_1
v_lshlrev_b32_sdwa v150,1,v150 dst_sel:DWORD dst_unused:UNUSED_PAD src0_sel:DWORD src1_sel:WORD_1
v_mfma_f32_16x16x32_bf16 v[184:187],v[100:103],v[128:131],v[184:187]
s_waitcnt lgkmcnt(3)
ds_read_b64_tr_b16 v[68:69],v5 offset:512
ds_read_b64_tr_b16 v[70:71],v5 offset:2560
v_lshl_add_u64 v[136:137],v[136:137],0,v[138:139]
v_fma_mixhi_f16 v120,v158,s99,0
v_mfma_f32_16x16x32_bf16 v[192:195],v[104:107],v[128:131],v[192:195]
s_cbranch_vccnz RA16_%=
s_mov_b64 exec,s[62:63]
s_add_u32 m0,s57,20928
s_add_u32 s64,s56,4521984
buffer_load_dwordx4 v2,s[36:39],s64 offen lds
s_add_u32 m0,s57,22656
s_nop 0
buffer_load_dwordx4 v3,s[36:39],s64 offen lds
s_mov_b64 exec,-1
RA16_%=:
ds_read_u16_d16_hi v146,v146
v_cvt_pk_bf16_f32 v138,v140,v142
v_cvt_pk_bf16_f32 v139,v141,v143
v_mfma_f32_16x16x32_bf16 v[200:203],v[108:111],v[128:131],v[200:203]
v_fma_mixhi_f16 v122,v159,s99,0
v_mfma_f32_16x16x32_bf16 v[164:167],v[88:91],v[136:139],v[164:167]
ds_read_b128 v[116:119],v7 offset:1440
ds_read_b64_tr_b16 v[72:73],v4 offset:4608
v_lshlrev_b32_sdwa v149,1,v149 dst_sel:DWORD dst_unused:UNUSED_PAD src0_sel:DWORD src1_sel:WORD_1
v_lshlrev_b32_sdwa v151,1,v151 dst_sel:DWORD dst_unused:UNUSED_PAD src0_sel:DWORD src1_sel:WORD_1
v_mfma_f32_16x16x32_bf16 v[172:175],v[92:95],v[136:139],v[172:175]
s_setprio 1
ds_read_b64_tr_b16 v[74:75],v4 offset:6656
ds_read_b64_tr_b16 v[76:77],v5 offset:4608
v_pk_fma_f16 v121,v120,v120,s54
v_pk_fma_f16 v123,v122,v122,s54
v_mfma_f32_16x16x32_bf16 v[180:183],v[96:99],v[136:139],v[180:183]
ds_read_u16 v145,v145
v_pk_mul_f16 v121,v120,v121
v_pk_mul_f16 v123,v122,v123
v_mfma_f32_16x16x32_bf16 v[188:191],v[100:103],v[136:139],v[188:191]
ds_read_u16_d16_hi v147,v147
v_lshlrev_b32_sdwa v152,1,v152 dst_sel:DWORD dst_unused:UNUSED_PAD src0_sel:DWORD src1_sel:WORD_1
v_lshlrev_b32_sdwa v154,1,v154 dst_sel:DWORD dst_unused:UNUSED_PAD src0_sel:DWORD src1_sel:WORD_1
v_mfma_f32_16x16x32_bf16 v[196:199],v[104:107],v[136:139],v[196:199]
v_exp_f16 v120,v121
v_mfma_f32_16x16x32_bf16 v[204:207],v[108:111],v[136:139],v[204:207]
s_waitcnt lgkmcnt(10)
ds_read_b64_tr_b16 v[78:79],v5 offset:6656
ds_read_b64_tr_b16 v[80:81],v4 offset:8704
v_exp_f16 v122,v123
v_mfma_f32_16x16x32_bf16 v[128:131],v[64:67],v[16:19],v[112:115]
v_lshlrev_b32_sdwa v153,1,v153 dst_sel:DWORD dst_unused:UNUSED_PAD src0_sel:DWORD src1_sel:WORD_1
v_lshlrev_b32_sdwa v155,1,v155 dst_sel:DWORD dst_unused:UNUSED_PAD src0_sel:DWORD src1_sel:WORD_1
v_mfma_f32_16x16x32_bf16 v[136:139],v[64:67],v[20:23],v[112:115]
s_waitcnt lgkmcnt(7)
ds_read_u16 v148,v148
ds_read_u16_d16_hi v150,v150
v_exp_f16_sdwa v120,v121 dst_sel:WORD_1 dst_unused:UNUSED_PRESERVE src0_sel:WORD_1
v_mfma_f32_16x16x32_bf16 v[140:143],v[68:71],v[20:23],v[116:119]
v_exp_f16_sdwa v122,v123 dst_sel:WORD_1 dst_unused:UNUSED_PRESERVE src0_sel:WORD_1
v_mfma_f32_16x16x32_bf16 v[132:135],v[68:71],v[16:19],v[116:119]
s_waitcnt lgkmcnt(7)
ds_read_b64_tr_b16 v[82:83],v4 offset:10752
v_pk_fma_f16 v120,v120,s55,s55
v_mfma_f32_16x16x32_bf16 v[128:131],v[72:75],v[24:27],v[128:131]
ds_read_b64_tr_b16 v[84:85],v5 offset:8704
v_pk_fma_f16 v122,v122,s55,s55
v_rcp_f16 v121,v120
v_mfma_f32_16x16x32_bf16 v[136:139],v[72:75],v[28:31],v[136:139]
s_waitcnt lgkmcnt(5)
ds_read_b64_tr_b16 v[86:87],v5 offset:10752
ds_read_u16 v149,v149
v_rcp_f16 v123,v122
v_mfma_f32_16x16x32_bf16 v[140:143],v[76:79],v[28:31],v[140:143]
ds_read_u16_d16_hi v151,v151
ds_read_u16 v152,v152
v_rcp_f16_sdwa v121,v120 dst_sel:WORD_1 dst_unused:UNUSED_PRESERVE src0_sel:WORD_1
v_mfma_f32_16x16x32_bf16 v[132:135],v[76:79],v[24:27],v[132:135]
s_waitcnt lgkmcnt(5)
s_cbranch_vccnz RA17_%=
s_mov_b64 exec,s[62:63]
s_add_u32 m0,s57,24416
s_add_u32 s64,s56,4620288
buffer_load_dwordx4 v2,s[36:39],s64 offen lds
s_add_u32 m0,s57,26144
s_nop 0
buffer_load_dwordx4 v3,s[36:39],s64 offen lds
s_mov_b64 exec,-1
RA17_%=:
v_rcp_f16_sdwa v123,v122 dst_sel:WORD_1 dst_unused:UNUSED_PRESERVE src0_sel:WORD_1
v_mfma_f32_16x16x32_bf16 v[128:131],v[80:83],v[32:35],v[128:131]
ds_read_u16_d16_hi v154,v154
v_fma_mix_f32 v156,v156,v121,0 op_sel_hi:[0,1,0]
v_fma_mix_f32 v158,v158,v121,0 op_sel:[0,1,0] op_sel_hi:[0,1,0]
v_mfma_f32_16x16x32_bf16 v[136:139],v[80:83],v[36:39],v[136:139]
s_waitcnt lgkmcnt(2)
s_setprio 0
v_lshl_add_u64 v[144:145],v[144:145],0,v[146:147]
v_lshl_add_u64 v[146:147],v[148:149],0,v[150:151]
v_mfma_f32_16x16x32_bf16 v[140:143],v[84:87],v[36:39],v[140:143]
ds_read_u16 v153,v153
v_fma_mix_f32 v157,v157,v123,0 op_sel_hi:[0,1,0]
v_fma_mix_f32 v159,v159,v123,0 op_sel:[0,1,0] op_sel_hi:[0,1,0]
v_mfma_f32_16x16x32_bf16 v[132:135],v[84:87],v[32:35],v[132:135]
v_lshlrev_b32_sdwa v128,1,v128 dst_sel:DWORD dst_unused:UNUSED_PAD src0_sel:DWORD src1_sel:WORD_1
v_lshlrev_b32_sdwa v130,1,v130 dst_sel:DWORD dst_unused:UNUSED_PAD src0_sel:DWORD src1_sel:WORD_1
v_mfma_f32_16x16x32_bf16 v[208:211],v[88:91],v[144:147],v[208:211]
s_cbranch_vccz RA18_%=
s_waitcnt vmcnt(0)
RA18_%=:
s_barrier
s_setprio 3
v_lshlrev_b32_sdwa v129,1,v129 dst_sel:DWORD dst_unused:UNUSED_PAD src0_sel:DWORD src1_sel:WORD_1
v_lshlrev_b32_sdwa v131,1,v131 dst_sel:DWORD dst_unused:UNUSED_PAD src0_sel:DWORD src1_sel:WORD_1
v_mfma_f32_16x16x32_bf16 v[216:219],v[92:95],v[144:147],v[216:219]
ds_read_u16_d16_hi v155,v155
ds_read_u16 v128,v128
v_fma_mixlo_f16 v120,v140,s99,0
v_fma_mixlo_f16 v122,v141,s99,0
v_mfma_f32_16x16x32_bf16 v[224:227],v[96:99],v[144:147],v[224:227]
s_cbranch_vccz RA19_%=
s_add_u32 m0,s58,0
s_mul_i32 s69,s59,1
s_add_u32 s69,s69,s97
buffer_load_dwordx4 v2,s[44:47],s69 offen lds
s_add_u32 m0,m0,1024
s_add_u32 s69,s69,s53
buffer_load_dwordx4 v3,s[44:47],s69 offen lds
s_add_u32 m0,m0,1024
s_add_u32 s69,s69,s53
buffer_load_dwordx4 v2,s[44:47],s69 offen lds
s_add_u32 m0,m0,s50
s_add_u32 s69,s69,s53
buffer_load_dwordx4 v3,s[44:47],s69 offen lds
RA19_%=:
ds_read_u16_d16_hi v130,v130
v_lshlrev_b32_sdwa v132,1,v132 dst_sel:DWORD dst_unused:UNUSED_PAD src0_sel:DWORD src1_sel:WORD_1
v_lshlrev_b32_sdwa v134,1,v134 dst_sel:DWORD dst_unused:UNUSED_PAD src0_sel:DWORD src1_sel:WORD_1
v_mfma_f32_16x16x32_bf16 v[232:235],v[100:103],v[144:147],v[232:235]
s_waitcnt lgkmcnt(2)
v_lshl_add_u64 v[152:153],v[152:153],0,v[154:155]
v_cvt_pk_bf16_f32 v154,v156,v158
v_mfma_f32_16x16x32_bf16 v[240:243],v[104:107],v[144:147],v[240:243]
v_fma_mixhi_f16 v120,v142,s99,0
v_cvt_pk_bf16_f32 v155,v157,v159
v_mfma_f32_16x16x32_bf16 v[248:251],v[108:111],v[144:147],v[248:251]
s_cbranch_vccz RA20_%=
s_add_u32 m0,m0,1024
s_add_u32 s69,s69,s53
buffer_load_dwordx4 v2,s[44:47],s69 offen lds
s_add_u32 m0,m0,1024
s_add_u32 s69,s69,s53
buffer_load_dwordx4 v3,s[44:47],s69 offen lds
s_add_u32 m0,m0,s50
s_mov_b32 s86,0xbe801d5a
s_add_u32 s69,s69,s53
buffer_load_dwordx4 v2,s[44:47],s69 offen lds
s_add_u32 m0,m0,1024
s_add_u32 s69,s69,s53
buffer_load_dwordx4 v3,s[44:47],s69 offen lds
RA20_%=:
ds_read_u16 v129,v129
v_fma_mixhi_f16 v122,v143,s99,0
v_mfma_f32_16x16x32_bf16 v[212:215],v[88:91],v[152:155],v[212:215]
v_lshlrev_b32_sdwa v133,1,v133 dst_sel:DWORD dst_unused:UNUSED_PAD src0_sel:DWORD src1_sel:WORD_1
v_pk_fma_f16 v121,v120,v120,s54
v_lshlrev_b32_sdwa v135,1,v135 dst_sel:DWORD dst_unused:UNUSED_PAD src0_sel:DWORD src1_sel:WORD_1
v_mfma_f32_16x16x32_bf16 v[220:223],v[92:95],v[152:155],v[220:223]
ds_read_u16_d16_hi v131,v131
ds_read_u16 v132,v132
v_pk_fma_f16 v123,v122,v122,s54
v_mfma_f32_16x16x32_bf16 v[228:231],v[96:99],v[152:155],v[228:231]
ds_read_b64_tr_b16 v[88:89],v6 offset:12320
v_pk_mul_f16 v121,v120,v121
v_lshlrev_b32_sdwa v136,1,v136 dst_sel:DWORD dst_unused:UNUSED_PAD src0_sel:DWORD src1_sel:WORD_1
v_pk_mul_f16 v123,v122,v123
v_mfma_f32_16x16x32_bf16 v[236:239],v[100:103],v[152:155],v[236:239]
s_cbranch_vccnz RA21_%=
s_mov_b64 exec,s[62:63]
s_add_u32 m0,s57,27904
s_add_u32 s64,s56,4718592
buffer_load_dwordx4 v2,s[36:39],s64 offen lds
s_add_u32 m0,s57,29632
s_nop 0
buffer_load_dwordx4 v3,s[36:39],s64 offen lds
s_mov_b64 exec,-1
RA21_%=:
ds_read_b64_tr_b16 v[90:91],v6 offset:13856
v_lshlrev_b32_sdwa v138,1,v138 dst_sel:DWORD dst_unused:UNUSED_PAD src0_sel:DWORD src1_sel:WORD_1
v_mfma_f32_16x16x32_bf16 v[244:247],v[104:107],v[152:155],v[244:247]
s_cbranch_vccz RA22_%=
s_add_u32 m0,m0,1024
s_add_u32 s69,s69,s53
buffer_load_dwordx4 v2,s[44:47],s69 offen lds
s_add_u32 m0,m0,s50
s_add_u32 s69,s69,s53
buffer_load_dwordx4 v3,s[44:47],s69 offen lds
s_add_u32 m0,m0,1024
s_add_u32 s69,s69,s53
buffer_load_dwordx4 v2,s[44:47],s69 offen lds
s_add_u32 m0,m0,1024
s_add_u32 s69,s69,s53
buffer_load_dwordx4 v3,s[44:47],s69 offen lds
RA22_%=:
ds_read_u16_d16_hi v134,v134
v_exp_f16 v120,v121
v_mfma_f32_16x16x32_bf16 v[252:255],v[108:111],v[152:155],v[252:255]
ds_read_b64_tr_b16 v[92:93],v6 offset:12328
v_lshlrev_b32_sdwa v137,1,v137 dst_sel:DWORD dst_unused:UNUSED_PAD src0_sel:DWORD src1_sel:WORD_1
v_mfma_f32_16x16x32_bf16 v[144:147],v[64:67],v[40:43],v[112:115]
ds_read_b64_tr_b16 v[94:95],v6 offset:13864
v_lshlrev_b32_sdwa v139,1,v139 dst_sel:DWORD dst_unused:UNUSED_PAD src0_sel:DWORD src1_sel:WORD_1
v_exp_f16 v122,v123
v_mfma_f32_16x16x32_bf16 v[152:155],v[64:67],v[44:47],v[112:115]
ds_read_u16 v133,v133
ds_read_b64_tr_b16 v[96:97],v6 offset:12384
v_mfma_f32_16x16x32_bf16 v[156:159],v[68:71],v[44:47],v[116:119]
ds_read_u16_d16_hi v135,v135
v_exp_f16_sdwa v122,v123 dst_sel:WORD_1 dst_unused:UNUSED_PRESERVE src0_sel:WORD_1
v_exp_f16_sdwa v120,v121 dst_sel:WORD_1 dst_unused:UNUSED_PRESERVE src0_sel:WORD_1
v_mfma_f32_16x16x32_bf16 v[148:151],v[68:71],v[40:43],v[116:119]
ds_read_b64_tr_b16 v[98:99],v6 offset:13920
v_pk_fma_f16 v120,v120,s55,s55
v_pk_fma_f16 v122,v122,s55,s55
v_mfma_f32_16x16x32_bf16 v[144:147],v[72:75],v[48:51],v[144:147]
v_rcp_f16 v121,v120
v_mfma_f32_16x16x32_bf16 v[152:155],v[72:75],v[52:55],v[152:155]
ds_read_b64_tr_b16 v[100:101],v6 offset:12392
v_rcp_f16 v123,v122
v_mfma_f32_16x16x32_bf16 v[156:159],v[76:79],v[52:55],v[156:159]
ds_read_u16 v136,v136
v_rcp_f16_sdwa v121,v120 dst_sel:WORD_1 dst_unused:UNUSED_PRESERVE src0_sel:WORD_1
v_mfma_f32_16x16x32_bf16 v[148:151],v[76:79],v[48:51],v[148:151]
ds_read_b64_tr_b16 v[102:103],v6 offset:13928
ds_read_b64_tr_b16 v[104:105],v6 offset:12448
v_rcp_f16_sdwa v123,v122 dst_sel:WORD_1 dst_unused:UNUSED_PRESERVE src0_sel:WORD_1
v_mfma_f32_16x16x32_bf16 v[144:147],v[80:83],v[56:59],v[144:147]
ds_read_u16_d16_hi v138,v138
v_fma_mix_f32 v142,v142,v121,0 op_sel:[0,1,0] op_sel_hi:[0,1,0]
v_mfma_f32_16x16x32_bf16 v[152:155],v[80:83],v[60:63],v[152:155]
s_waitcnt lgkmcnt(6)
ds_read_b64_tr_b16 v[106:107],v6 offset:13984
ds_read_b64_tr_b16 v[108:109],v6 offset:12456
v_fma_mix_f32 v140,v140,v121,0 op_sel_hi:[0,1,0]
v_lshl_add_u64 v[128:129],v[128:129],0,v[130:131]
v_lshl_add_u64 v[130:131],v[132:133],0,v[134:135]
v_mfma_f32_16x16x32_bf16 v[156:159],v[84:87],v[60:63],v[156:159]
ds_read_u16 v137,v137
v_fma_mix_f32 v141,v141,v123,0 op_sel_hi:[0,1,0]
v_fma_mix_f32 v143,v143,v123,0 op_sel:[0,1,0] op_sel_hi:[0,1,0]
v_mfma_f32_16x16x32_bf16 v[148:151],v[84:87],v[56:59],v[148:151]
ds_read_b64_tr_b16 v[110:111],v6 offset:13992
ds_read_b64_tr_b16 v[64:65],v4 offset:12320
v_lshlrev_b32_sdwa v144,1,v144 dst_sel:DWORD dst_unused:UNUSED_PAD src0_sel:DWORD src1_sel:WORD_1
v_lshlrev_b32_sdwa v146,1,v146 dst_sel:DWORD dst_unused:UNUSED_PAD src0_sel:DWORD src1_sel:WORD_1
v_mfma_f32_16x16x32_bf16 v[160:163],v[88:91],v[128:131],v[160:163]
s_cbranch_vccnz RA23_%=
s_mov_b64 exec,s[62:63]
s_add_u32 m0,s57,31392
s_add_u32 s64,s56,4816896
buffer_load_dwordx4 v2,s[36:39],s64 offen lds
s_add_u32 m0,s57,33120
s_nop 0
buffer_load_dwordx4 v3,s[36:39],s64 offen lds
s_mov_b64 exec,-1
RA23_%=:
ds_read_u16_d16_hi v139,v139
v_lshlrev_b32_sdwa v145,1,v145 dst_sel:DWORD dst_unused:UNUSED_PAD src0_sel:DWORD src1_sel:WORD_1
v_lshlrev_b32_sdwa v147,1,v147 dst_sel:DWORD dst_unused:UNUSED_PAD src0_sel:DWORD src1_sel:WORD_1
v_fma_mixlo_f16 v122,v157,s99,0
v_mfma_f32_16x16x32_bf16 v[168:171],v[92:95],v[128:131],v[168:171]
s_waitcnt lgkmcnt(11)
ds_read_b64_tr_b16 v[66:67],v4 offset:14368
ds_read_b128 v[112:115],v7 offset:0
v_fma_mixlo_f16 v120,v156,s99,0
v_mfma_f32_16x16x32_bf16 v[176:179],v[96:99],v[128:131],v[176:179]
s_waitcnt lgkmcnt(10)
ds_read_u16 v144,v144
v_lshlrev_b32_sdwa v148,1,v148 dst_sel:DWORD dst_unused:UNUSED_PAD src0_sel:DWORD src1_sel:WORD_1
v_lshlrev_b32_sdwa v150,1,v150 dst_sel:DWORD dst_unused:UNUSED_PAD src0_sel:DWORD src1_sel:WORD_1
v_mfma_f32_16x16x32_bf16 v[184:187],v[100:103],v[128:131],v[184:187]
s_waitcnt lgkmcnt(3)
ds_read_b64_tr_b16 v[68:69],v5 offset:12320
ds_read_b64_tr_b16 v[70:71],v5 offset:14368
v_lshl_add_u64 v[136:137],v[136:137],0,v[138:139]
v_fma_mixhi_f16 v120,v158,s99,0
v_mfma_f32_16x16x32_bf16 v[192:195],v[104:107],v[128:131],v[192:195]
ds_read_u16_d16_hi v146,v146
v_cvt_pk_bf16_f32 v138,v140,v142
v_cvt_pk_bf16_f32 v139,v141,v143
v_mfma_f32_16x16x32_bf16 v[200:203],v[108:111],v[128:131],v[200:203]
v_fma_mixhi_f16 v122,v159,s99,0
v_mfma_f32_16x16x32_bf16 v[164:167],v[88:91],v[136:139],v[164:167]
ds_read_b128 v[116:119],v7 offset:32
ds_read_b64_tr_b16 v[72:73],v4 offset:16416
v_lshlrev_b32_sdwa v149,1,v149 dst_sel:DWORD dst_unused:UNUSED_PAD src0_sel:DWORD src1_sel:WORD_1
v_lshlrev_b32_sdwa v151,1,v151 dst_sel:DWORD dst_unused:UNUSED_PAD src0_sel:DWORD src1_sel:WORD_1
v_mfma_f32_16x16x32_bf16 v[172:175],v[92:95],v[136:139],v[172:175]
ds_read_b64_tr_b16 v[74:75],v4 offset:18464
ds_read_b64_tr_b16 v[76:77],v5 offset:16416
v_pk_fma_f16 v121,v120,v120,s54
v_pk_fma_f16 v123,v122,v122,s54
v_mfma_f32_16x16x32_bf16 v[180:183],v[96:99],v[136:139],v[180:183]
ds_read_u16 v145,v145
v_pk_mul_f16 v121,v120,v121
v_pk_mul_f16 v123,v122,v123
v_mfma_f32_16x16x32_bf16 v[188:191],v[100:103],v[136:139],v[188:191]
ds_read_u16_d16_hi v147,v147
v_lshlrev_b32_sdwa v152,1,v152 dst_sel:DWORD dst_unused:UNUSED_PAD src0_sel:DWORD src1_sel:WORD_1
v_lshlrev_b32_sdwa v154,1,v154 dst_sel:DWORD dst_unused:UNUSED_PAD src0_sel:DWORD src1_sel:WORD_1
v_mfma_f32_16x16x32_bf16 v[196:199],v[104:107],v[136:139],v[196:199]
v_exp_f16 v120,v121
v_mfma_f32_16x16x32_bf16 v[204:207],v[108:111],v[136:139],v[204:207]
s_waitcnt lgkmcnt(10)
ds_read_b64_tr_b16 v[78:79],v5 offset:18464
ds_read_b64_tr_b16 v[80:81],v4 offset:20512
v_exp_f16 v122,v123
v_mfma_f32_16x16x32_bf16 v[128:131],v[64:67],v[16:19],v[112:115]
v_lshlrev_b32_sdwa v153,1,v153 dst_sel:DWORD dst_unused:UNUSED_PAD src0_sel:DWORD src1_sel:WORD_1
v_lshlrev_b32_sdwa v155,1,v155 dst_sel:DWORD dst_unused:UNUSED_PAD src0_sel:DWORD src1_sel:WORD_1
v_mfma_f32_16x16x32_bf16 v[136:139],v[64:67],v[20:23],v[112:115]
s_waitcnt lgkmcnt(7)
ds_read_u16 v148,v148
ds_read_u16_d16_hi v150,v150
v_exp_f16_sdwa v120,v121 dst_sel:WORD_1 dst_unused:UNUSED_PRESERVE src0_sel:WORD_1
v_mfma_f32_16x16x32_bf16 v[140:143],v[68:71],v[20:23],v[116:119]
v_exp_f16_sdwa v122,v123 dst_sel:WORD_1 dst_unused:UNUSED_PRESERVE src0_sel:WORD_1
v_mfma_f32_16x16x32_bf16 v[132:135],v[68:71],v[16:19],v[116:119]
s_waitcnt lgkmcnt(7)
ds_read_b64_tr_b16 v[82:83],v4 offset:22560
v_pk_fma_f16 v120,v120,s55,s55
v_mfma_f32_16x16x32_bf16 v[128:131],v[72:75],v[24:27],v[128:131]
ds_read_b64_tr_b16 v[84:85],v5 offset:20512
v_pk_fma_f16 v122,v122,s55,s55
v_rcp_f16 v121,v120
v_mfma_f32_16x16x32_bf16 v[136:139],v[72:75],v[28:31],v[136:139]
s_waitcnt lgkmcnt(5)
ds_read_b64_tr_b16 v[86:87],v5 offset:22560
ds_read_u16 v149,v149
v_rcp_f16 v123,v122
v_mfma_f32_16x16x32_bf16 v[140:143],v[76:79],v[28:31],v[140:143]
ds_read_u16_d16_hi v151,v151
ds_read_u16 v152,v152
v_rcp_f16_sdwa v121,v120 dst_sel:WORD_1 dst_unused:UNUSED_PRESERVE src0_sel:WORD_1
v_mfma_f32_16x16x32_bf16 v[132:135],v[76:79],v[24:27],v[132:135]
s_waitcnt lgkmcnt(5)
v_rcp_f16_sdwa v123,v122 dst_sel:WORD_1 dst_unused:UNUSED_PRESERVE src0_sel:WORD_1
v_mfma_f32_16x16x32_bf16 v[128:131],v[80:83],v[32:35],v[128:131]
ds_read_u16_d16_hi v154,v154
v_fma_mix_f32 v156,v156,v121,0 op_sel_hi:[0,1,0]
v_fma_mix_f32 v158,v158,v121,0 op_sel:[0,1,0] op_sel_hi:[0,1,0]
v_mfma_f32_16x16x32_bf16 v[136:139],v[80:83],v[36:39],v[136:139]
s_waitcnt lgkmcnt(2)
v_lshl_add_u64 v[144:145],v[144:145],0,v[146:147]
v_lshl_add_u64 v[146:147],v[148:149],0,v[150:151]
v_mfma_f32_16x16x32_bf16 v[140:143],v[84:87],v[36:39],v[140:143]
ds_read_u16 v153,v153
v_fma_mix_f32 v157,v157,v123,0 op_sel_hi:[0,1,0]
v_fma_mix_f32 v159,v159,v123,0 op_sel:[0,1,0] op_sel_hi:[0,1,0]
v_mfma_f32_16x16x32_bf16 v[132:135],v[84:87],v[32:35],v[132:135]
v_lshlrev_b32_sdwa v128,1,v128 dst_sel:DWORD dst_unused:UNUSED_PAD src0_sel:DWORD src1_sel:WORD_1
v_lshlrev_b32_sdwa v130,1,v130 dst_sel:DWORD dst_unused:UNUSED_PAD src0_sel:DWORD src1_sel:WORD_1
v_mfma_f32_16x16x32_bf16 v[208:211],v[88:91],v[144:147],v[208:211]
v_lshlrev_b32_sdwa v129,1,v129 dst_sel:DWORD dst_unused:UNUSED_PAD src0_sel:DWORD src1_sel:WORD_1
v_lshlrev_b32_sdwa v131,1,v131 dst_sel:DWORD dst_unused:UNUSED_PAD src0_sel:DWORD src1_sel:WORD_1
v_mfma_f32_16x16x32_bf16 v[216:219],v[92:95],v[144:147],v[216:219]
ds_read_u16_d16_hi v155,v155
ds_read_u16 v128,v128
v_fma_mixlo_f16 v120,v140,s99,0
v_fma_mixlo_f16 v122,v141,s99,0
v_mfma_f32_16x16x32_bf16 v[224:227],v[96:99],v[144:147],v[224:227]
s_setprio 2
ds_read_u16_d16_hi v130,v130
v_lshlrev_b32_sdwa v132,1,v132 dst_sel:DWORD dst_unused:UNUSED_PAD src0_sel:DWORD src1_sel:WORD_1
v_lshlrev_b32_sdwa v134,1,v134 dst_sel:DWORD dst_unused:UNUSED_PAD src0_sel:DWORD src1_sel:WORD_1
v_mfma_f32_16x16x32_bf16 v[232:235],v[100:103],v[144:147],v[232:235]
s_waitcnt lgkmcnt(2)
v_lshl_add_u64 v[152:153],v[152:153],0,v[154:155]
v_cvt_pk_bf16_f32 v154,v156,v158
v_mfma_f32_16x16x32_bf16 v[240:243],v[104:107],v[144:147],v[240:243]
v_fma_mixhi_f16 v120,v142,s99,0
v_cvt_pk_bf16_f32 v155,v157,v159
v_mfma_f32_16x16x32_bf16 v[248:251],v[108:111],v[144:147],v[248:251]
ds_read_u16 v129,v129
v_fma_mixhi_f16 v122,v143,s99,0
v_mfma_f32_16x16x32_bf16 v[212:215],v[88:91],v[152:155],v[212:215]
v_lshlrev_b32_sdwa v133,1,v133 dst_sel:DWORD dst_unused:UNUSED_PAD src0_sel:DWORD src1_sel:WORD_1
v_pk_fma_f16 v121,v120,v120,s54
v_lshlrev_b32_sdwa v135,1,v135 dst_sel:DWORD dst_unused:UNUSED_PAD src0_sel:DWORD src1_sel:WORD_1
v_mfma_f32_16x16x32_bf16 v[220:223],v[92:95],v[152:155],v[220:223]
ds_read_u16_d16_hi v131,v131
ds_read_u16 v132,v132
v_pk_fma_f16 v123,v122,v122,s54
v_mfma_f32_16x16x32_bf16 v[228:231],v[96:99],v[152:155],v[228:231]
ds_read_b64_tr_b16 v[88:89],v6 offset:18480
v_pk_mul_f16 v121,v120,v121
v_lshlrev_b32_sdwa v136,1,v136 dst_sel:DWORD dst_unused:UNUSED_PAD src0_sel:DWORD src1_sel:WORD_1
v_pk_mul_f16 v123,v122,v123
v_mfma_f32_16x16x32_bf16 v[236:239],v[100:103],v[152:155],v[236:239]
ds_read_b64_tr_b16 v[90:91],v6 offset:20016
v_lshlrev_b32_sdwa v138,1,v138 dst_sel:DWORD dst_unused:UNUSED_PAD src0_sel:DWORD src1_sel:WORD_1
v_mfma_f32_16x16x32_bf16 v[244:247],v[104:107],v[152:155],v[244:247]
ds_read_u16_d16_hi v134,v134
v_exp_f16 v120,v121
v_mfma_f32_16x16x32_bf16 v[252:255],v[108:111],v[152:155],v[252:255]
ds_read_b64_tr_b16 v[92:93],v6 offset:18488
v_lshlrev_b32_sdwa v137,1,v137 dst_sel:DWORD dst_unused:UNUSED_PAD src0_sel:DWORD src1_sel:WORD_1
v_mfma_f32_16x16x32_bf16 v[144:147],v[64:67],v[40:43],v[112:115]
ds_read_b64_tr_b16 v[94:95],v6 offset:20024
v_lshlrev_b32_sdwa v139,1,v139 dst_sel:DWORD dst_unused:UNUSED_PAD src0_sel:DWORD src1_sel:WORD_1
v_exp_f16 v122,v123
v_mfma_f32_16x16x32_bf16 v[152:155],v[64:67],v[44:47],v[112:115]
ds_read_u16 v133,v133
ds_read_b64_tr_b16 v[96:97],v6 offset:18544
v_mfma_f32_16x16x32_bf16 v[156:159],v[68:71],v[44:47],v[116:119]
ds_read_u16_d16_hi v135,v135
v_exp_f16_sdwa v122,v123 dst_sel:WORD_1 dst_unused:UNUSED_PRESERVE src0_sel:WORD_1
v_exp_f16_sdwa v120,v121 dst_sel:WORD_1 dst_unused:UNUSED_PRESERVE src0_sel:WORD_1
v_mfma_f32_16x16x32_bf16 v[148:151],v[68:71],v[40:43],v[116:119]
ds_read_b64_tr_b16 v[98:99],v6 offset:20080
v_pk_fma_f16 v120,v120,s55,s55
v_pk_fma_f16 v122,v122,s55,s55
v_mfma_f32_16x16x32_bf16 v[144:147],v[72:75],v[48:51],v[144:147]
v_rcp_f16 v121,v120
v_mfma_f32_16x16x32_bf16 v[152:155],v[72:75],v[52:55],v[152:155]
ds_read_b64_tr_b16 v[100:101],v6 offset:18552
v_rcp_f16 v123,v122
v_mfma_f32_16x16x32_bf16 v[156:159],v[76:79],v[52:55],v[156:159]
ds_read_u16 v136,v136
v_rcp_f16_sdwa v121,v120 dst_sel:WORD_1 dst_unused:UNUSED_PRESERVE src0_sel:WORD_1
v_mfma_f32_16x16x32_bf16 v[148:151],v[76:79],v[48:51],v[148:151]
ds_read_b64_tr_b16 v[102:103],v6 offset:20088
ds_read_b64_tr_b16 v[104:105],v6 offset:18608
v_rcp_f16_sdwa v123,v122 dst_sel:WORD_1 dst_unused:UNUSED_PRESERVE src0_sel:WORD_1
v_mfma_f32_16x16x32_bf16 v[144:147],v[80:83],v[56:59],v[144:147]
ds_read_u16_d16_hi v138,v138
v_fma_mix_f32 v142,v142,v121,0 op_sel:[0,1,0] op_sel_hi:[0,1,0]
v_mfma_f32_16x16x32_bf16 v[152:155],v[80:83],v[60:63],v[152:155]
s_waitcnt lgkmcnt(6)
ds_read_b64_tr_b16 v[106:107],v6 offset:20144
ds_read_b64_tr_b16 v[108:109],v6 offset:18616
v_fma_mix_f32 v140,v140,v121,0 op_sel_hi:[0,1,0]
v_lshl_add_u64 v[128:129],v[128:129],0,v[130:131]
v_lshl_add_u64 v[130:131],v[132:133],0,v[134:135]
v_mfma_f32_16x16x32_bf16 v[156:159],v[84:87],v[60:63],v[156:159]
ds_read_u16 v137,v137
v_fma_mix_f32 v141,v141,v123,0 op_sel_hi:[0,1,0]
v_fma_mix_f32 v143,v143,v123,0 op_sel:[0,1,0] op_sel_hi:[0,1,0]
v_mfma_f32_16x16x32_bf16 v[148:151],v[84:87],v[56:59],v[148:151]
ds_read_b64_tr_b16 v[110:111],v6 offset:20152
ds_read_b128 v[64:67],v7 offset:1536
v_lshlrev_b32_sdwa v144,1,v144 dst_sel:DWORD dst_unused:UNUSED_PAD src0_sel:DWORD src1_sel:WORD_1
v_lshlrev_b32_sdwa v146,1,v146 dst_sel:DWORD dst_unused:UNUSED_PAD src0_sel:DWORD src1_sel:WORD_1
v_mfma_f32_16x16x32_bf16 v[160:163],v[88:91],v[128:131],v[160:163]
ds_read_u16_d16_hi v139,v139
ds_read_b128 v[68:71],v7 offset:1568
v_lshlrev_b32_sdwa v145,1,v145 dst_sel:DWORD dst_unused:UNUSED_PAD src0_sel:DWORD src1_sel:WORD_1
v_lshlrev_b32_sdwa v147,1,v147 dst_sel:DWORD dst_unused:UNUSED_PAD src0_sel:DWORD src1_sel:WORD_1
v_fma_mixlo_f16 v122,v157,s99,0
v_fma_mixlo_f16 v120,v156,s99,0
v_mfma_f32_16x16x32_bf16 v[168:171],v[92:95],v[128:131],v[168:171]
s_waitcnt lgkmcnt(12)
ds_read_u16 v144,v144
ds_read_b128 v[72:75],v7 offset:1664
v_lshlrev_b32_sdwa v148,1,v148 dst_sel:DWORD dst_unused:UNUSED_PAD src0_sel:DWORD src1_sel:WORD_1
v_lshlrev_b32_sdwa v150,1,v150 dst_sel:DWORD dst_unused:UNUSED_PAD src0_sel:DWORD src1_sel:WORD_1
s_waitcnt lgkmcnt(3)
v_lshl_add_u64 v[136:137],v[136:137],0,v[138:139]
v_fma_mixhi_f16 v120,v158,s99,0
v_mfma_f32_16x16x32_bf16 v[176:179],v[96:99],v[128:131],v[176:179]
s_setprio 1
ds_read_u16_d16_hi v146,v146
ds_read_b128 v[76:79],v7 offset:1696
v_cvt_pk_bf16_f32 v138,v140,v142
v_cvt_pk_bf16_f32 v139,v141,v143
v_fma_mixhi_f16 v122,v159,s99,0
v_lshlrev_b32_sdwa v149,1,v149 dst_sel:DWORD dst_unused:UNUSED_PAD src0_sel:DWORD src1_sel:WORD_1
v_mfma_f32_16x16x32_bf16 v[184:187],v[100:103],v[128:131],v[184:187]
ds_read_u16 v145,v145
ds_read_b128 v[80:83],v7 offset:1792
v_lshlrev_b32_sdwa v151,1,v151 dst_sel:DWORD dst_unused:UNUSED_PAD src0_sel:DWORD src1_sel:WORD_1
v_pk_fma_f16 v121,v120,v120,s54
v_pk_fma_f16 v123,v122,v122,s54
v_pk_mul_f16 v121,v120,v121
v_mfma_f32_16x16x32_bf16 v[192:195],v[104:107],v[128:131],v[192:195]
ds_read_u16_d16_hi v147,v147
ds_read_b128 v[84:87],v7 offset:1824
v_pk_mul_f16 v123,v122,v123
v_lshlrev_b32_sdwa v152,1,v152 dst_sel:DWORD dst_unused:UNUSED_PAD src0_sel:DWORD src1_sel:WORD_1
v_lshlrev_b32_sdwa v154,1,v154 dst_sel:DWORD dst_unused:UNUSED_PAD src0_sel:DWORD src1_sel:WORD_1
v_exp_f16 v120,v121
v_mfma_f32_16x16x32_bf16 v[200:203],v[108:111],v[128:131],v[200:203]
ds_read_u16 v148,v148
v_exp_f16 v122,v123
v_lshlrev_b32_sdwa v153,1,v153 dst_sel:DWORD dst_unused:UNUSED_PAD src0_sel:DWORD src1_sel:WORD_1
v_mfma_f32_16x16x32_bf16 v[164:167],v[88:91],v[136:139],v[164:167]
ds_read_u16_d16_hi v150,v150
v_lshlrev_b32_sdwa v155,1,v155 dst_sel:DWORD dst_unused:UNUSED_PAD src0_sel:DWORD src1_sel:WORD_1
v_exp_f16_sdwa v120,v121 dst_sel:WORD_1 dst_unused:UNUSED_PRESERVE src0_sel:WORD_1
v_exp_f16_sdwa v122,v123 dst_sel:WORD_1 dst_unused:UNUSED_PRESERVE src0_sel:WORD_1
v_mfma_f32_16x16x32_bf16 v[172:175],v[92:95],v[136:139],v[172:175]
ds_read_u16 v149,v149
v_pk_fma_f16 v120,v120,s55,s55
v_pk_fma_f16 v122,v122,s55,s55
v_rcp_f16 v121,v120
v_mfma_f32_16x16x32_bf16 v[180:183],v[96:99],v[136:139],v[180:183]
ds_read_u16_d16_hi v151,v151
v_rcp_f16 v123,v122
v_rcp_f16_sdwa v121,v120 dst_sel:WORD_1 dst_unused:UNUSED_PRESERVE src0_sel:WORD_1
v_mfma_f32_16x16x32_bf16 v[188:191],v[100:103],v[136:139],v[188:191]
ds_read_u16 v152,v152
v_rcp_f16_sdwa v123,v122 dst_sel:WORD_1 dst_unused:UNUSED_PRESERVE src0_sel:WORD_1
v_fma_mix_f32 v156,v156,v121,0 op_sel_hi:[0,1,0]
v_mfma_f32_16x16x32_bf16 v[196:199],v[104:107],v[136:139],v[196:199]
s_setprio 0
ds_read_u16_d16_hi v154,v154
v_fma_mix_f32 v158,v158,v121,0 op_sel:[0,1,0] op_sel_hi:[0,1,0]
s_waitcnt lgkmcnt(7)
v_lshl_add_u64 v[144:145],v[144:145],0,v[146:147]
s_waitcnt lgkmcnt(2)
v_lshl_add_u64 v[146:147],v[148:149],0,v[150:151]
v_fma_mix_f32 v157,v157,v123,0 op_sel_hi:[0,1,0]
v_mfma_f32_16x16x32_bf16 v[204:207],v[108:111],v[136:139],v[204:207]
ds_read_u16 v153,v153
v_fma_mix_f32 v159,v159,v123,0 op_sel:[0,1,0] op_sel_hi:[0,1,0]
ds_read_u16_d16_hi v155,v155
s_waitcnt lgkmcnt(0)
v_lshl_add_u64 v[152:153],v[152:153],0,v[154:155]
v_cvt_pk_bf16_f32 v154,v156,v158
v_cvt_pk_bf16_f32 v155,v157,v159
v_mfma_f32_16x16x32_bf16 v[208:211],v[88:91],v[144:147],v[208:211]
s_cbranch_vccz RA24_%=
s_waitcnt vmcnt(0)
RA24_%=:
s_barrier
s_setprio 0
v_xor_b32 v9,64,v10
v_mul_f32 v160,v160,v64
v_mul_f32 v161,v161,v65
v_mul_f32 v162,v162,v66
v_mul_f32 v163,v163,v67
v_mfma_f32_16x16x32_bf16 v[216:219],v[92:95],v[144:147],v[216:219]
v_mul_f32 v168,v168,v68
v_mul_f32 v169,v169,v69
v_mul_f32 v170,v170,v70
v_mul_f32 v171,v171,v71
v_cvt_pk_bf16_f32 v16,v160,v161
v_mfma_f32_16x16x32_bf16 v[224:227],v[96:99],v[144:147],v[224:227]
v_cvt_pk_bf16_f32 v17,v162,v163
v_cvt_pk_bf16_f32 v18,v168,v169
v_cvt_pk_bf16_f32 v19,v170,v171
v_mul_f32_dpp v176,v176,v72 quad_perm:[1,0,3,2] row_mask:0xf bank_mask:0xf
v_mfma_f32_16x16x32_bf16 v[232:235],v[100:103],v[144:147],v[232:235]
v_mul_f32_dpp v177,v177,v73 quad_perm:[1,0,3,2] row_mask:0xf bank_mask:0xf
v_mul_f32_dpp v178,v178,v74 quad_perm:[1,0,3,2] row_mask:0xf bank_mask:0xf
v_mul_f32_dpp v179,v179,v75 quad_perm:[1,0,3,2] row_mask:0xf bank_mask:0xf
v_mul_f32_dpp v184,v184,v76 quad_perm:[1,0,3,2] row_mask:0xf bank_mask:0xf
v_mul_f32_dpp v185,v185,v77 quad_perm:[1,0,3,2] row_mask:0xf bank_mask:0xf
v_mfma_f32_16x16x32_bf16 v[240:243],v[104:107],v[144:147],v[240:243]
v_mul_f32_dpp v186,v186,v78 quad_perm:[1,0,3,2] row_mask:0xf bank_mask:0xf
v_mul_f32_dpp v187,v187,v79 quad_perm:[1,0,3,2] row_mask:0xf bank_mask:0xf
v_cvt_pk_bf16_f32 v24,v176,v177
v_cvt_pk_bf16_f32 v25,v178,v179
v_mfma_f32_16x16x32_bf16 v[248:251],v[108:111],v[144:147],v[248:251]
v_cvt_pk_bf16_f32 v26,v184,v185
v_cvt_pk_bf16_f32 v27,v186,v187
s_mov_b64 exec,s[12:13]
v_swap_b32 v16,v24
v_swap_b32 v17,v25
v_swap_b32 v18,v26
v_swap_b32 v19,v27
s_mov_b64 exec,-1
v_mfma_f32_16x16x32_bf16 v[212:215],v[88:91],v[152:155],v[212:215]
s_add_u32 s65,s49,0
buffer_store_dwordx4 v[16:19],v10,s[40:43],s65 offen offset:0 sc1
v_mfma_f32_16x16x32_bf16 v[220:223],v[92:95],v[152:155],v[220:223]
v_mul_f32 v192,v192,v80
v_mul_f32 v193,v193,v81
v_mul_f32 v194,v194,v82
v_mul_f32 v195,v195,v83
v_mfma_f32_16x16x32_bf16 v[228:231],v[96:99],v[152:155],v[228:231]
v_mul_f32 v200,v200,v84
v_mul_f32 v201,v201,v85
v_mul_f32 v202,v202,v86
v_mul_f32 v203,v203,v87
v_mfma_f32_16x16x32_bf16 v[236:239],v[100:103],v[152:155],v[236:239]
v_cvt_pk_bf16_f32 v32,v192,v193
v_cvt_pk_bf16_f32 v33,v194,v195
v_cvt_pk_bf16_f32 v34,v200,v201
v_cvt_pk_bf16_f32 v35,v202,v203
v_mul_f32_dpp v164,v164,v64 quad_perm:[1,0,3,2] row_mask:0xf bank_mask:0xf
v_mfma_f32_16x16x32_bf16 v[244:247],v[104:107],v[152:155],v[244:247]
v_mul_f32_dpp v165,v165,v65 quad_perm:[1,0,3,2] row_mask:0xf bank_mask:0xf
v_mul_f32_dpp v166,v166,v66 quad_perm:[1,0,3,2] row_mask:0xf bank_mask:0xf
v_mul_f32_dpp v167,v167,v67 quad_perm:[1,0,3,2] row_mask:0xf bank_mask:0xf
v_mul_f32_dpp v172,v172,v68 quad_perm:[1,0,3,2] row_mask:0xf bank_mask:0xf
v_mfma_f32_16x16x32_bf16 v[252:255],v[108:111],v[152:155],v[252:255]
v_mul_f32_dpp v173,v173,v69 quad_perm:[1,0,3,2] row_mask:0xf bank_mask:0xf
s_add_u32 s65,s49,0
buffer_store_dwordx4 v[24:27],v9,s[40:43],s65 offen offset:384 sc1
v_mul_f32_dpp v174,v174,v70 quad_perm:[1,0,3,2] row_mask:0xf bank_mask:0xf
v_mul_f32_dpp v175,v175,v71 quad_perm:[1,0,3,2] row_mask:0xf bank_mask:0xf
v_cvt_pk_bf16_f32 v20,v164,v165
v_cvt_pk_bf16_f32 v21,v166,v167
v_cvt_pk_bf16_f32 v22,v172,v173
v_cvt_pk_bf16_f32 v23,v174,v175
s_mov_b64 exec,s[12:13]
v_swap_b32 v32,v20
v_swap_b32 v33,v21
v_swap_b32 v34,v22
v_swap_b32 v35,v23
s_mov_b64 exec,-1
v_mul_f32 v180,v180,v72
v_mul_f32 v181,v181,v73
v_mul_f32 v182,v182,v74
v_mul_f32 v183,v183,v75
v_mul_f32 v188,v188,v76
v_mul_f32 v189,v189,v77
v_mul_f32 v190,v190,v78
v_mul_f32 v191,v191,v79
v_cvt_pk_bf16_f32 v28,v180,v181
v_cvt_pk_bf16_f32 v29,v182,v183
v_cvt_pk_bf16_f32 v30,v188,v189
v_cvt_pk_bf16_f32 v31,v190,v191
v_mul_f32_dpp v196,v196,v80 quad_perm:[1,0,3,2] row_mask:0xf bank_mask:0xf
v_mul_f32_dpp v197,v197,v81 quad_perm:[1,0,3,2] row_mask:0xf bank_mask:0xf
v_mul_f32_dpp v198,v198,v82 quad_perm:[1,0,3,2] row_mask:0xf bank_mask:0xf
v_mul_f32_dpp v199,v199,v83 quad_perm:[1,0,3,2] row_mask:0xf bank_mask:0xf
s_add_u32 s65,s49,0
buffer_store_dwordx4 v[32:35],v10,s[40:43],s65 offen offset:128 sc1
v_mul_f32_dpp v204,v204,v84 quad_perm:[1,0,3,2] row_mask:0xf bank_mask:0xf
v_mul_f32_dpp v205,v205,v85 quad_perm:[1,0,3,2] row_mask:0xf bank_mask:0xf
v_mul_f32_dpp v206,v206,v86 quad_perm:[1,0,3,2] row_mask:0xf bank_mask:0xf
v_mul_f32_dpp v207,v207,v87 quad_perm:[1,0,3,2] row_mask:0xf bank_mask:0xf
v_cvt_pk_bf16_f32 v36,v196,v197
v_cvt_pk_bf16_f32 v37,v198,v199
v_cvt_pk_bf16_f32 v38,v204,v205
v_cvt_pk_bf16_f32 v39,v206,v207
s_mov_b64 exec,s[12:13]
v_swap_b32 v28,v36
v_swap_b32 v29,v37
v_swap_b32 v30,v38
v_swap_b32 v31,v39
s_mov_b64 exec,-1
v_mul_f32 v208,v208,v64
v_mul_f32 v209,v209,v65
v_mul_f32 v210,v210,v66
v_mul_f32 v211,v211,v67
v_mul_f32 v216,v216,v68
v_mul_f32 v217,v217,v69
v_mul_f32 v218,v218,v70
v_mul_f32 v219,v219,v71
v_cvt_pk_bf16_f32 v40,v208,v209
v_cvt_pk_bf16_f32 v41,v210,v211
v_cvt_pk_bf16_f32 v42,v216,v217
v_cvt_pk_bf16_f32 v43,v218,v219
v_mul_f32_dpp v224,v224,v72 quad_perm:[1,0,3,2] row_mask:0xf bank_mask:0xf
v_mul_f32_dpp v225,v225,v73 quad_perm:[1,0,3,2] row_mask:0xf bank_mask:0xf
s_add_u32 s65,s49,0
buffer_store_dwordx4 v[20:23],v9,s[40:43],s65 offen offset:512 sc1
v_mul_f32_dpp v226,v226,v74 quad_perm:[1,0,3,2] row_mask:0xf bank_mask:0xf
v_mul_f32_dpp v227,v227,v75 quad_perm:[1,0,3,2] row_mask:0xf bank_mask:0xf
v_mul_f32_dpp v232,v232,v76 quad_perm:[1,0,3,2] row_mask:0xf bank_mask:0xf
v_mul_f32_dpp v233,v233,v77 quad_perm:[1,0,3,2] row_mask:0xf bank_mask:0xf
v_mul_f32_dpp v234,v234,v78 quad_perm:[1,0,3,2] row_mask:0xf bank_mask:0xf
v_mul_f32_dpp v235,v235,v79 quad_perm:[1,0,3,2] row_mask:0xf bank_mask:0xf
v_cvt_pk_bf16_f32 v48,v224,v225
v_cvt_pk_bf16_f32 v49,v226,v227
v_cvt_pk_bf16_f32 v50,v232,v233
v_cvt_pk_bf16_f32 v51,v234,v235
s_mov_b64 exec,s[12:13]
v_swap_b32 v40,v48
v_swap_b32 v41,v49
v_swap_b32 v42,v50
v_swap_b32 v43,v51
s_mov_b64 exec,-1
v_mul_f32 v240,v240,v80
v_mul_f32 v241,v241,v81
v_mul_f32 v242,v242,v82
v_mul_f32 v243,v243,v83
v_mul_f32 v248,v248,v84
v_mul_f32 v249,v249,v85
v_mul_f32 v250,v250,v86
v_mul_f32 v251,v251,v87
v_cvt_pk_bf16_f32 v56,v240,v241
v_cvt_pk_bf16_f32 v57,v242,v243
v_cvt_pk_bf16_f32 v58,v248,v249
v_cvt_pk_bf16_f32 v59,v250,v251
s_add_u32 s65,s49,0
buffer_store_dwordx4 v[28:31],v10,s[40:43],s65 offen offset:256 sc1
v_mul_f32_dpp v212,v212,v64 quad_perm:[1,0,3,2] row_mask:0xf bank_mask:0xf
v_mul_f32_dpp v213,v213,v65 quad_perm:[1,0,3,2] row_mask:0xf bank_mask:0xf
v_mul_f32_dpp v214,v214,v66 quad_perm:[1,0,3,2] row_mask:0xf bank_mask:0xf
v_mul_f32_dpp v215,v215,v67 quad_perm:[1,0,3,2] row_mask:0xf bank_mask:0xf
v_mul_f32_dpp v220,v220,v68 quad_perm:[1,0,3,2] row_mask:0xf bank_mask:0xf
v_mul_f32_dpp v221,v221,v69 quad_perm:[1,0,3,2] row_mask:0xf bank_mask:0xf
v_mul_f32_dpp v222,v222,v70 quad_perm:[1,0,3,2] row_mask:0xf bank_mask:0xf
v_mul_f32_dpp v223,v223,v71 quad_perm:[1,0,3,2] row_mask:0xf bank_mask:0xf
v_cvt_pk_bf16_f32 v44,v212,v213
v_cvt_pk_bf16_f32 v45,v214,v215
v_cvt_pk_bf16_f32 v46,v220,v221
v_cvt_pk_bf16_f32 v47,v222,v223
s_mov_b64 exec,s[12:13]
v_swap_b32 v56,v44
v_swap_b32 v57,v45
v_swap_b32 v58,v46
v_swap_b32 v59,v47
s_mov_b64 exec,-1
v_mul_f32 v228,v228,v72
v_mul_f32 v229,v229,v73
v_mul_f32 v230,v230,v74
v_mul_f32 v231,v231,v75
v_mul_f32 v236,v236,v76
v_mul_f32 v237,v237,v77
v_mul_f32 v238,v238,v78
v_mul_f32 v239,v239,v79
v_cvt_pk_bf16_f32 v52,v228,v229
v_cvt_pk_bf16_f32 v53,v230,v231
s_add_u32 s65,s49,0
buffer_store_dwordx4 v[36:39],v9,s[40:43],s65 offen offset:640 sc1
v_cvt_pk_bf16_f32 v54,v236,v237
v_cvt_pk_bf16_f32 v55,v238,v239
v_mul_f32_dpp v244,v244,v80 quad_perm:[1,0,3,2] row_mask:0xf bank_mask:0xf
v_mul_f32_dpp v245,v245,v81 quad_perm:[1,0,3,2] row_mask:0xf bank_mask:0xf
v_mul_f32_dpp v246,v246,v82 quad_perm:[1,0,3,2] row_mask:0xf bank_mask:0xf
v_mul_f32_dpp v247,v247,v83 quad_perm:[1,0,3,2] row_mask:0xf bank_mask:0xf
v_mul_f32_dpp v252,v252,v84 quad_perm:[1,0,3,2] row_mask:0xf bank_mask:0xf
v_mul_f32_dpp v253,v253,v85 quad_perm:[1,0,3,2] row_mask:0xf bank_mask:0xf
v_mul_f32_dpp v254,v254,v86 quad_perm:[1,0,3,2] row_mask:0xf bank_mask:0xf
v_mul_f32_dpp v255,v255,v87 quad_perm:[1,0,3,2] row_mask:0xf bank_mask:0xf
v_cvt_pk_bf16_f32 v60,v244,v245
v_cvt_pk_bf16_f32 v61,v246,v247
v_cvt_pk_bf16_f32 v62,v252,v253
v_cvt_pk_bf16_f32 v63,v254,v255
s_mov_b64 exec,s[12:13]
v_swap_b32 v52,v60
v_swap_b32 v53,v61
v_swap_b32 v54,v62
v_swap_b32 v55,v63
s_mov_b64 exec,-1
s_cbranch_vccnz RA26_%=
v_and_b32 v112,63,%1
s_bfe_u32 s64,%0,0x20003
s_lshl_b32 s64,s64,13
s_bitcmp1_b32 s52,0
s_mov_b32 s66,28672
s_nop 0
s_mov_b32 s86,0xbe801d5a
s_cselect_b32 s66,69632,s66
s_add_u32 s64,s64,s66
v_lshl_add_u32 v112,v112,7,s64
s_getpc_b64 s[14:15]
N_pgA25_%=:
s_sub_u32 s14,s14,N_pgA25_%=-N_main_%=
s_subb_u32 s15,s15,0
s_mov_b32 m0,157312
v_min_u32 v112,N_mainend_%=-N_main_%=,v112
global_load_lds_dword v112,s[14:15]
RA26_%=:
s_add_u32 s4,s10,12
s_cmp_ge_u32 s4,20
s_cselect_b32 s7,20,0
s_sub_u32 s4,s4,s7
s_mul_i32 s4,s4,3488
s_add_u32 s5,s10,14
s_cmp_ge_u32 s5,20
s_cselect_b32 s7,20,0
s_sub_u32 s5,s5,s7
s_mul_i32 s5,s5,3488
s_add_u32 s6,s10,16
s_cmp_ge_u32 s6,20
s_cselect_b32 s7,20,0
s_sub_u32 s6,s6,s7
s_mul_i32 s6,s6,3488
v_add_u32 v112,s4,v15
v_add_u32 v116,s5,v15
s_mov_b64 exec,s[8:9]
v_add_u32 v112,s5,v15
v_add_u32 v116,s6,v15
s_mov_b64 exec,-1
v_xor_b32 v113,16,v112
v_xor_b32 v117,16,v116
v_xor_b32 v114,32,v112
v_xor_b32 v118,32,v116
v_xor_b32 v115,48,v112
v_xor_b32 v119,48,v116
s_waitcnt vmcnt(7)
s_barrier
s_setprio 3
ds_read_b128 v[136:139],v116 offset:0
ds_read_b128 v[88:91],v11 offset:768
ds_read_b128 v[140:143],v116 offset:3488
ds_read_b128 v[128:131],v112 offset:0
ds_read_b128 v[132:135],v112 offset:3488
ds_read_b128 v[92:95],v12 offset:0
ds_read_b128 v[152:155],v117 offset:0
ds_read_b128 v[96:99],v11 offset:1920
ds_read_b128 v[156:159],v117 offset:3488
ds_read_b128 v[144:147],v113 offset:0
ds_read_b128 v[148:151],v113 offset:3488
ds_read_b128 v[100:103],v12 offset:1152
s_waitcnt lgkmcnt(10)
v_mfma_f32_16x16x32_bf16 v[160:163],v[88:91],v[136:139],0
s_waitcnt lgkmcnt(9)
s_add_u32 s65,s49,786432
buffer_store_dwordx4 v[40:43],v10,s[40:43],s65 offen offset:0 sc1
v_mfma_f32_16x16x32_bf16 v[184:187],v[88:91],v[140:143],0
s_waitcnt lgkmcnt(6)
ds_read_b128 v[140:143],v116 offset:3552
ds_read_b128 v[88:91],v11 offset:5376
v_smfmac_f32_16x16x64_bf16 v[160:163],v[92:95],v[128:135],v13
ds_read_b128 v[128:131],v112 offset:64
v_smfmac_f32_16x16x64_bf16 v[184:187],v[92:95],v[132:139],v13
s_waitcnt lgkmcnt(7)
ds_read_b128 v[136:139],v116 offset:64
ds_read_b128 v[132:135],v112 offset:3552
v_mfma_f32_16x16x32_bf16 v[164:167],v[96:99],v[152:155],0
s_waitcnt lgkmcnt(8)
ds_read_b128 v[92:95],v12 offset:4608
v_add_f32 v120,v160,v161
v_mul_f32 v124,v160,v160
v_fmac_f32 v124,v161,v161
v_mfma_f32_16x16x32_bf16 v[188:191],v[96:99],v[156:159],0
s_waitcnt lgkmcnt(6)
ds_read_b128 v[156:159],v117 offset:3552
ds_read_b128 v[96:99],v11 offset:6528
v_add_f32 v121,v162,v163
v_mul_f32 v125,v162,v162
v_fmac_f32 v125,v163,v163
v_smfmac_f32_16x16x64_bf16 v[164:167],v[100:103],v[144:151],v13
ds_read_b128 v[144:147],v113 offset:64
v_add_f32 v122,v184,v185
v_mul_f32 v126,v184,v184
v_fmac_f32 v126,v185,v185
v_smfmac_f32_16x16x64_bf16 v[188:191],v[100:103],v[148:155],v13
s_waitcnt lgkmcnt(5)
s_add_u32 s65,s49,786432
buffer_store_dwordx4 v[48:51],v9,s[40:43],s65 offen offset:384 sc1
ds_read_b128 v[152:155],v117 offset:64
ds_read_b128 v[148:151],v113 offset:3552
v_add_f32 v123,v186,v187
v_mul_f32 v127,v186,v186
v_fmac_f32 v127,v187,v187
v_mfma_f32_16x16x32_bf16 v[168:171],v[88:91],v[136:139],0
ds_read_b128 v[100:103],v12 offset:5760
v_add_f32 v120,v120,v164
v_fmac_f32 v124,v164,v164
v_add_f32 v120,v120,v165
v_fmac_f32 v124,v165,v165
v_mfma_f32_16x16x32_bf16 v[192:195],v[88:91],v[140:143],0
s_waitcnt lgkmcnt(6)
ds_read_b128 v[140:143],v116 offset:3616
ds_read_b128 v[88:91],v11 offset:9984
v_add_f32 v121,v121,v166
v_fmac_f32 v125,v166,v166
v_add_f32 v121,v121,v167
v_fmac_f32 v125,v167,v167
v_smfmac_f32_16x16x64_bf16 v[168:171],v[92:95],v[128:135],v13
ds_read_b128 v[128:131],v112 offset:128
v_add_f32 v122,v122,v188
v_fmac_f32 v126,v188,v188
v_add_f32 v122,v122,v189
v_fmac_f32 v126,v189,v189
v_smfmac_f32_16x16x64_bf16 v[192:195],v[92:95],v[132:139],v13
s_waitcnt lgkmcnt(5)
ds_read_b128 v[136:139],v116 offset:128
ds_read_b128 v[132:135],v112 offset:3616
v_add_f32 v123,v123,v190
v_fmac_f32 v127,v190,v190
v_add_f32 v123,v123,v191
v_fmac_f32 v127,v191,v191
v_mfma_f32_16x16x32_bf16 v[172:175],v[96:99],v[152:155],0
s_add_u32 s65,s49,786432
buffer_store_dwordx4 v[56:59],v10,s[40:43],s65 offen offset:128 sc1
ds_read_b128 v[92:95],v12 offset:9216
v_add_f32 v120,v120,v168
v_fmac_f32 v124,v168,v168
v_add_f32 v120,v120,v169
v_fmac_f32 v124,v169,v169
v_mfma_f32_16x16x32_bf16 v[196:199],v[96:99],v[156:159],0
s_waitcnt lgkmcnt(6)
ds_read_b128 v[156:159],v117 offset:3616
ds_read_b128 v[96:99],v11 offset:11136
v_add_f32 v121,v121,v170
v_fmac_f32 v125,v170,v170
v_add_f32 v121,v121,v171
v_fmac_f32 v125,v171,v171
v_smfmac_f32_16x16x64_bf16 v[172:175],v[100:103],v[144:151],v13
ds_read_b128 v[144:147],v113 offset:128
v_add_f32 v122,v122,v192
v_fmac_f32 v126,v192,v192
v_add_f32 v122,v122,v193
v_fmac_f32 v126,v193,v193
v_smfmac_f32_16x16x64_bf16 v[196:199],v[100:103],v[148:155],v13
s_waitcnt lgkmcnt(5)
ds_read_b128 v[152:155],v117 offset:128
ds_read_b128 v[148:151],v113 offset:3616
v_add_f32 v123,v123,v194
v_fmac_f32 v127,v194,v194
v_add_f32 v123,v123,v195
v_fmac_f32 v127,v195,v195
v_mfma_f32_16x16x32_bf16 v[176:179],v[88:91],v[136:139],0
s_add_u32 s65,s49,786432
buffer_store_dwordx4 v[44:47],v9,s[40:43],s65 offen offset:512 sc1
ds_read_b128 v[100:103],v12 offset:10368
v_add_f32 v120,v120,v172
v_fmac_f32 v124,v172,v172
v_add_f32 v120,v120,v173
v_fmac_f32 v124,v173,v173
v_mfma_f32_16x16x32_bf16 v[200:203],v[88:91],v[140:143],0
s_waitcnt lgkmcnt(6)
ds_read_b128 v[140:143],v118 offset:3488
ds_read_b128 v[88:91],v11 offset:3072
v_add_f32 v121,v121,v174
v_fmac_f32 v125,v174,v174
v_add_f32 v121,v121,v175
v_fmac_f32 v125,v175,v175
v_smfmac_f32_16x16x64_bf16 v[176:179],v[92:95],v[128:135],v13
ds_read_b128 v[128:131],v114 offset:0
v_add_f32 v122,v122,v196
v_fmac_f32 v126,v196,v196
v_add_f32 v122,v122,v197
v_fmac_f32 v126,v197,v197
v_smfmac_f32_16x16x64_bf16 v[200:203],v[92:95],v[132:139],v13
s_waitcnt lgkmcnt(5)
ds_read_b128 v[136:139],v118 offset:0
ds_read_b128 v[132:135],v114 offset:3488
v_add_f32 v123,v123,v198
v_fmac_f32 v127,v198,v198
v_add_f32 v123,v123,v199
v_fmac_f32 v127,v199,v199
v_mfma_f32_16x16x32_bf16 v[180:183],v[96:99],v[152:155],0
ds_read_b128 v[92:95],v12 offset:2304
v_add_f32 v120,v120,v176
v_fmac_f32 v124,v176,v176
v_add_f32 v120,v120,v177
v_fmac_f32 v124,v177,v177
v_mfma_f32_16x16x32_bf16 v[204:207],v[96:99],v[156:159],0
s_waitcnt lgkmcnt(6)
s_add_u32 s65,s49,786432
buffer_store_dwordx4 v[52:55],v10,s[40:43],s65 offen offset:256 sc1
ds_read_b128 v[156:159],v119 offset:3488
ds_read_b128 v[96:99],v11 offset:4224
v_add_f32 v121,v121,v178
v_fmac_f32 v125,v178,v178
v_add_f32 v121,v121,v179
v_fmac_f32 v125,v179,v179
v_smfmac_f32_16x16x64_bf16 v[180:183],v[100:103],v[144:151],v13
ds_read_b128 v[144:147],v115 offset:0
v_add_f32 v122,v122,v200
v_fmac_f32 v126,v200,v200
v_add_f32 v122,v122,v201
v_fmac_f32 v126,v201,v201
v_smfmac_f32_16x16x64_bf16 v[204:207],v[100:103],v[148:155],v13
s_waitcnt lgkmcnt(5)
ds_read_b128 v[152:155],v119 offset:0
ds_read_b128 v[148:151],v115 offset:3488
v_add_f32 v123,v123,v202
v_fmac_f32 v127,v202,v202
v_add_f32 v123,v123,v203
v_fmac_f32 v127,v203,v203
v_mfma_f32_16x16x32_bf16 v[208:211],v[88:91],v[136:139],0
ds_read_b128 v[100:103],v12 offset:3456
v_add_f32 v120,v120,v180
v_fmac_f32 v124,v180,v180
v_add_f32 v120,v120,v181
v_fmac_f32 v124,v181,v181
v_mfma_f32_16x16x32_bf16 v[232:235],v[88:91],v[140:143],0
s_waitcnt lgkmcnt(6)
ds_read_b128 v[140:143],v118 offset:3552
ds_read_b128 v[88:91],v11 offset:7680
v_add_f32 v121,v121,v182
v_fmac_f32 v125,v182,v182
v_add_f32 v121,v121,v183
v_fmac_f32 v125,v183,v183
v_smfmac_f32_16x16x64_bf16 v[208:211],v[92:95],v[128:135],v13
s_add_u32 s65,s49,786432
buffer_store_dwordx4 v[60:63],v9,s[40:43],s65 offen offset:640 sc1
ds_read_b128 v[128:131],v114 offset:64
v_add_f32 v122,v122,v204
v_fmac_f32 v126,v204,v204
v_add_f32 v122,v122,v205
v_fmac_f32 v126,v205,v205
v_smfmac_f32_16x16x64_bf16 v[232:235],v[92:95],v[132:139],v13
s_waitcnt lgkmcnt(5)
ds_read_b128 v[136:139],v118 offset:64
ds_read_b128 v[132:135],v114 offset:3552
v_add_f32 v123,v123,v206
v_fmac_f32 v127,v206,v206
v_add_f32 v123,v123,v207
v_fmac_f32 v127,v207,v207
v_mfma_f32_16x16x32_bf16 v[212:215],v[96:99],v[152:155],0
ds_read_b128 v[92:95],v12 offset:6912
v_add_f32 v120,v120,v208
v_fmac_f32 v124,v208,v208
v_add_f32 v120,v120,v209
v_fmac_f32 v124,v209,v209
v_mfma_f32_16x16x32_bf16 v[236:239],v[96:99],v[156:159],0
s_waitcnt lgkmcnt(6)
s_setprio 2
ds_read_b128 v[156:159],v119 offset:3552
ds_read_b128 v[96:99],v11 offset:8832
v_add_f32 v121,v121,v210
v_fmac_f32 v125,v210,v210
v_add_f32 v121,v121,v211
v_fmac_f32 v125,v211,v211
v_smfmac_f32_16x16x64_bf16 v[212:215],v[100:103],v[144:151],v13
ds_read_b128 v[144:147],v115 offset:64
v_add_f32 v122,v122,v232
v_fmac_f32 v126,v232,v232
v_add_f32 v122,v122,v233
v_fmac_f32 v126,v233,v233
v_smfmac_f32_16x16x64_bf16 v[236:239],v[100:103],v[148:155],v13
s_waitcnt lgkmcnt(5)
ds_read_b128 v[152:155],v119 offset:64
ds_read_b128 v[148:151],v115 offset:3552
v_add_f32 v123,v123,v234
v_fmac_f32 v127,v234,v234
v_add_f32 v123,v123,v235
v_fmac_f32 v127,v235,v235
v_mfma_f32_16x16x32_bf16 v[216:219],v[88:91],v[136:139],0
ds_read_b128 v[100:103],v12 offset:8064
v_add_f32 v120,v120,v212
v_fmac_f32 v124,v212,v212
v_add_f32 v120,v120,v213
v_fmac_f32 v124,v213,v213
v_add_f32 v121,v121,v214
v_mfma_f32_16x16x32_bf16 v[240:243],v[88:91],v[140:143],0
s_waitcnt lgkmcnt(6)
ds_read_b128 v[140:143],v118 offset:3616
ds_read_b128 v[88:91],v11 offset:12288
v_fmac_f32 v125,v214,v214
v_add_f32 v121,v121,v215
v_fmac_f32 v125,v215,v215
v_add_f32 v122,v122,v236
v_smfmac_f32_16x16x64_bf16 v[216:219],v[92:95],v[128:135],v13
ds_read_b128 v[128:131],v114 offset:128
v_fmac_f32 v126,v236,v236
v_add_f32 v122,v122,v237
v_fmac_f32 v126,v237,v237
v_add_f32 v123,v123,v238
v_fmac_f32 v127,v238,v238
v_smfmac_f32_16x16x64_bf16 v[240:243],v[92:95],v[132:139],v13
s_waitcnt lgkmcnt(5)
ds_read_b128 v[136:139],v118 offset:128
ds_read_b128 v[132:135],v114 offset:3616
v_add_f32 v123,v123,v239
v_fmac_f32 v127,v239,v239
v_add_u32 v112,v112,v8
v_add_u32 v116,v116,v8
v_mfma_f32_16x16x32_bf16 v[220:223],v[96:99],v[152:155],0
ds_read_b128 v[92:95],v12 offset:11520
ds_read_b128 v[104:107],v14 offset:0
v_add_f32 v120,v120,v216
v_fmac_f32 v124,v216,v216
v_add_f32 v120,v120,v217
v_fmac_f32 v124,v217,v217
v_mfma_f32_16x16x32_bf16 v[244:247],v[96:99],v[156:159],0
s_waitcnt lgkmcnt(7)
ds_read_b128 v[108:111],v7 offset:3584
ds_read_b128 v[156:159],v119 offset:3616
v_add_f32 v121,v121,v218
v_fmac_f32 v125,v218,v218
v_add_f32 v121,v121,v219
v_fmac_f32 v125,v219,v219
v_smfmac_f32_16x16x64_bf16 v[220:223],v[100:103],v[144:151],v13
ds_read_b128 v[96:99],v11 offset:13440
ds_read_b128 v[144:147],v115 offset:128
v_add_f32 v122,v122,v240
v_fmac_f32 v126,v240,v240
v_add_f32 v122,v122,v241
v_fmac_f32 v126,v241,v241
v_smfmac_f32_16x16x64_bf16 v[244:247],v[100:103],v[148:155],v13
s_waitcnt lgkmcnt(7)
ds_read_b128 v[152:155],v119 offset:128
ds_read_b128 v[148:151],v115 offset:3616
v_add_f32 v123,v123,v242
v_fmac_f32 v127,v242,v242
v_add_f32 v123,v123,v243
v_fmac_f32 v127,v243,v243
v_mfma_f32_16x16x32_bf16 v[224:227],v[88:91],v[136:139],0
ds_read_b128 v[100:103],v12 offset:12672
v_add_f32 v120,v120,v220
v_fmac_f32 v124,v220,v220
v_add_f32 v120,v120,v221
v_fmac_f32 v124,v221,v221
v_mfma_f32_16x16x32_bf16 v[248:251],v[88:91],v[140:143],0
s_waitcnt lgkmcnt(8)
s_setprio 1
ds_read_b128 v[140:143],v116 offset:192
ds_read_b128 v[88:91],v112 offset:3616
v_add_f32 v121,v121,v222
v_fmac_f32 v125,v222,v222
v_add_f32 v121,v121,v223
v_fmac_f32 v125,v223,v223
v_smfmac_f32_16x16x64_bf16 v[224:227],v[92:95],v[128:135],v13
ds_read_b128 v[128:131],v112 offset:3488
v_add_f32 v122,v122,v244
v_fmac_f32 v126,v244,v244
v_add_f32 v122,v122,v245
v_fmac_f32 v126,v245,v245
v_smfmac_f32_16x16x64_bf16 v[248:251],v[92:95],v[132:139],v13
s_waitcnt lgkmcnt(5)
ds_read_b128 v[136:139],v116 offset:0
ds_read_b128 v[132:135],v112 offset:3680
v_add_f32 v123,v123,v246
v_fmac_f32 v127,v246,v246
v_add_f32 v123,v123,v247
v_fmac_f32 v127,v247,v247
v_mfma_f32_16x16x32_bf16 v[228:231],v[96:99],v[152:155],0
ds_read_b128 v[92:95],v112 offset:3808
v_add_f32 v120,v120,v224
v_fmac_f32 v124,v224,v224
v_add_f32 v120,v120,v225
v_fmac_f32 v124,v225,v225
v_mfma_f32_16x16x32_bf16 v[252:255],v[96:99],v[156:159],0
s_waitcnt lgkmcnt(6)
s_cbranch_vccnz RA27_%=
s_mov_b64 exec,s[62:63]
s_add_u32 m0,s57,34880
s_add_u32 s64,s56,4915200
buffer_load_dwordx4 v2,s[36:39],s64 offen lds
s_add_u32 m0,s57,36608
s_nop 0
buffer_load_dwordx4 v3,s[36:39],s64 offen lds
s_mov_b64 exec,-1
RA27_%=:
ds_read_b128 v[156:159],v116 offset:256
ds_read_b128 v[96:99],v116 offset:128
v_add_f32 v121,v121,v226
v_fmac_f32 v125,v226,v226
v_add_f32 v121,v121,v227
v_fmac_f32 v125,v227,v227
v_smfmac_f32_16x16x64_bf16 v[228:231],v[100:103],v[144:151],v13
ds_read_b128 v[144:147],v112 offset:3552
v_add_f32 v122,v122,v248
v_fmac_f32 v126,v248,v248
v_add_f32 v122,v122,v249
v_fmac_f32 v126,v249,v249
v_smfmac_f32_16x16x64_bf16 v[252:255],v[100:103],v[148:155],v13
s_setprio 0
ds_read_b128 v[152:155],v116 offset:64
ds_read_b128 v[148:151],v112 offset:3744
v_add_f32 v123,v123,v250
v_fmac_f32 v127,v250,v250
v_add_f32 v123,v123,v251
v_fmac_f32 v127,v251,v251
ds_read_b128 v[100:103],v116 offset:320
v_add_f32 v120,v120,v228
v_fmac_f32 v124,v228,v228
v_add_f32 v120,v120,v229
v_fmac_f32 v124,v229,v229
v_add_f32 v121,v121,v230
v_fmac_f32 v125,v230,v230
v_add_f32 v121,v121,v231
v_fmac_f32 v125,v231,v231
s_waitcnt lgkmcnt(0)
s_barrier
s_setprio 0
v_add_f32 v122,v122,v252
s_cbranch_vccnz RA28_%=
s_mov_b64 exec,s[62:63]
s_add_u32 m0,s57,38368
s_add_u32 s64,s56,5013504
buffer_load_dwordx4 v2,s[36:39],s64 offen lds
s_add_u32 m0,s57,40096
s_nop 0
buffer_load_dwordx4 v3,s[36:39],s64 offen lds
s_mov_b64 exec,-1
RA28_%=:
v_fmac_f32 v126,v252,v252
v_add_f32 v122,v122,v253
s_cbranch_vccnz RA29_%=
s_mov_b64 exec,s[62:63]
s_add_u32 m0,s57,41856
s_add_u32 s64,s56,5111808
buffer_load_dwordx4 v2,s[36:39],s64 offen lds
s_add_u32 m0,s57,43584
s_nop 0
buffer_load_dwordx4 v3,s[36:39],s64 offen lds
s_mov_b64 exec,-1
RA29_%=:
v_fmac_f32 v126,v253,v253
v_add_f32 v123,v123,v254
v_fmac_f32 v127,v254,v254
v_add_f32 v123,v123,v255
v_fmac_f32 v127,v255,v255
s_setprio 3
ds_read_b128 v[56:59],v14 offset:256
ds_read_b128 v[60:63],v7 offset:3616
s_setprio 0
v_permlane32_swap_b32 v120,v122
v_permlane32_swap_b32 v121,v123
v_permlane32_swap_b32 v124,v126
v_permlane32_swap_b32 v125,v127
v_add_f32 v120,v120,v122
v_add_f32 v121,v121,v123
v_add_f32 v124,v124,v126
v_add_f32 v125,v125,v127
v_permlane16_swap_b32 v120,v121
s_nop 0
v_permlane16_swap_b32 v124,v125
v_add_f32 v113,v120,v121
v_add_f32 v114,v124,v125
v_mul_f32 v113,0x3c2aaaab,v113
v_mul_f32 v114,0x3c2aaaab,v114
v_mul_f32 v115,0x5e000000,v114
v_fma_f32 v114,-v113,v113,v114
v_pk_maximum3_f16 v1,v1,v115,v115 op_sel:[0,1,1] op_sel_hi:[1,1,1]
v_max_f32 v114,0,v114
v_add_f32 v114,0x3727c5ac,v114
v_rsq_f32 v114,v114
s_nop 0
v_mul_f32 v120,0x8befe000,v114
v_mul_f32 v121,0x8befe000,v114
v_mul_f32 v124,-v113,v120
v_mul_f32 v125,-v113,v120
v_permlane16_swap_b32 v120,v121
s_nop 0
v_permlane16_swap_b32 v124,v125
v_mov_b32 v122,v120
v_mov_b32 v123,v121
s_cbranch_vccnz RA30_%=
s_mov_b64 exec,s[62:63]
s_add_u32 m0,s57,45344
s_add_u32 s64,s56,5210112
buffer_load_dwordx4 v2,s[36:39],s64 offen lds
s_add_u32 m0,s57,47072
s_nop 0
buffer_load_dwordx4 v3,s[36:39],s64 offen lds
s_mov_b64 exec,-1
RA30_%=:
v_mov_b32 v126,v124
v_mov_b32 v127,v125
v_permlane32_swap_b32 v120,v122
v_permlane32_swap_b32 v124,v126
v_permlane32_swap_b32 v121,v123
v_permlane32_swap_b32 v125,v127
v_add_u32 v8,94432,v4
v_add_u32 v9,94432,v5
v_fma_f32 v160,v160,v120,v124
s_setprio 3
ds_read_b64_tr_b16 v[64:65],v4 offset:512
s_setprio 0
v_fma_f32 v161,v161,v120,v124
s_setprio 3
ds_read_b64_tr_b16 v[66:67],v4 offset:2560
s_setprio 0
v_fma_f32 v162,v162,v121,v125
s_setprio 3
ds_read_b128 v[112:115],v7 offset:896
s_setprio 0
v_fma_f32 v163,v163,v121,v125
s_setprio 3
ds_read_b64_tr_b16 v[68:69],v5 offset:512
s_setprio 0
v_fma_f32 v164,v164,v120,v124
s_setprio 3
ds_read_b64_tr_b16 v[70:71],v5 offset:2560
s_setprio 0
v_fma_f32 v165,v165,v120,v124
s_setprio 3
ds_read_b128 v[116:119],v7 offset:928
s_setprio 0
v_fma_f32 v166,v166,v121,v125
s_setprio 3
ds_read_b64_tr_b16 v[72:73],v4 offset:4608
s_setprio 0
v_fma_f32 v167,v167,v121,v125
s_setprio 3
ds_read_b64_tr_b16 v[74:75],v4 offset:6656
s_setprio 0
v_fma_f32 v208,v208,v120,v124
s_setprio 3
ds_read_b64_tr_b16 v[76:77],v5 offset:4608
s_setprio 0
v_fma_f32 v209,v209,v120,v124
s_setprio 3
ds_read_b64_tr_b16 v[78:79],v5 offset:6656
s_setprio 0
v_fma_f32 v210,v210,v121,v125
s_setprio 3
ds_read_b64_tr_b16 v[80:81],v4 offset:8704
s_setprio 0
v_fma_f32 v211,v211,v121,v125
s_setprio 3
ds_read_b64_tr_b16 v[82:83],v4 offset:10752
s_setprio 0
v_fma_f32 v212,v212,v120,v124
s_setprio 3
ds_read_b64_tr_b16 v[84:85],v5 offset:8704
s_setprio 0
v_fma_f32 v213,v213,v120,v124
s_setprio 3
ds_read_b64_tr_b16 v[86:87],v5 offset:10752
s_setprio 0
v_fma_f32 v214,v214,v121,v125
v_fma_f32 v215,v215,v121,v125
v_cvt_pk_bf16_f32 v16,v160,v161
v_cvt_pk_bf16_f32 v20,v162,v163
v_cvt_pk_bf16_f32 v17,v164,v165
v_cvt_pk_bf16_f32 v21,v166,v167
v_cvt_pk_bf16_f32 v18,v208,v209
v_cvt_pk_bf16_f32 v22,v210,v211
v_cvt_pk_bf16_f32 v19,v212,v213
v_cvt_pk_bf16_f32 v23,v214,v215
v_fma_f32 v168,v168,v120,v124
v_fma_f32 v169,v169,v120,v124
v_mfma_f32_16x16x32_bf16 v[160:163],v[104:107],v[128:131],v[108:111]
v_fma_f32 v170,v170,v121,v125
v_fma_f32 v171,v171,v121,v125
v_fma_f32 v172,v172,v120,v124
v_fma_f32 v173,v173,v120,v124
v_fma_f32 v174,v174,v121,v125
v_fma_f32 v175,v175,v121,v125
v_mfma_f32_16x16x32_bf16 v[164:167],v[104:107],v[132:135],v[108:111]
s_cbranch_vccnz RA31_%=
s_mov_b64 exec,s[62:63]
s_add_u32 m0,s57,48832
s_add_u32 s64,s56,5308416
buffer_load_dwordx4 v2,s[36:39],s64 offen lds
s_add_u32 m0,s57,50560
s_nop 0
buffer_load_dwordx4 v3,s[36:39],s64 offen lds
s_mov_b64 exec,-1
RA31_%=:
v_fma_f32 v216,v216,v120,v124
v_fma_f32 v217,v217,v120,v124
v_fma_f32 v218,v218,v121,v125
v_fma_f32 v219,v219,v121,v125
v_fma_f32 v220,v220,v120,v124
v_fma_f32 v221,v221,v120,v124
v_mfma_f32_16x16x32_bf16 v[208:211],v[104:107],v[136:139],v[108:111]
v_fma_f32 v222,v222,v121,v125
v_fma_f32 v223,v223,v121,v125
v_cvt_pk_bf16_f32 v24,v168,v169
v_cvt_pk_bf16_f32 v28,v170,v171
v_cvt_pk_bf16_f32 v25,v172,v173
v_cvt_pk_bf16_f32 v29,v174,v175
s_setprio 3
v_mfma_f32_16x16x32_bf16 v[212:215],v[104:107],v[140:143],v[108:111]
ds_read_b128 v[104:107],v14 offset:512
s_setprio 0
v_cvt_pk_bf16_f32 v26,v216,v217
v_cvt_pk_bf16_f32 v30,v218,v219
v_cvt_pk_bf16_f32 v27,v220,v221
v_cvt_pk_bf16_f32 v31,v222,v223
v_fma_f32 v176,v176,v120,v124
v_fma_f32 v177,v177,v120,v124
s_waitcnt lgkmcnt(15)
s_setprio 3
v_mfma_f32_16x16x32_bf16 v[168:171],v[56:59],v[128:131],v[60:63]
ds_read_b128 v[108:111],v7 offset:3712
s_setprio 0
v_fma_f32 v178,v178,v121,v125
v_fma_f32 v179,v179,v121,v125
v_fma_f32 v180,v180,v120,v124
v_fma_f32 v181,v181,v120,v124
v_fma_f32 v182,v182,v121,v125
v_fma_f32 v183,v183,v121,v125
v_mfma_f32_16x16x32_bf16 v[172:175],v[56:59],v[132:135],v[60:63]
v_fma_f32 v224,v224,v120,v124
v_fma_f32 v225,v225,v120,v124
v_fma_f32 v226,v226,v121,v125
v_fma_f32 v227,v227,v121,v125
v_fma_f32 v228,v228,v120,v124
v_fma_f32 v229,v229,v120,v124
v_mfma_f32_16x16x32_bf16 v[216:219],v[56:59],v[136:139],v[60:63]
s_cbranch_vccnz RA32_%=
s_mov_b64 exec,s[62:63]
s_add_u32 m0,s57,52320
s_add_u32 s64,s56,5406720
buffer_load_dwordx4 v2,s[36:39],s64 offen lds
s_add_u32 m0,s57,54048
s_nop 0
buffer_load_dwordx4 v3,s[36:39],s64 offen lds
s_mov_b64 exec,-1
RA32_%=:
v_fma_f32 v230,v230,v121,v125
v_fma_f32 v231,v231,v121,v125
v_cvt_pk_bf16_f32 v32,v176,v177
v_cvt_pk_bf16_f32 v36,v178,v179
v_cvt_pk_bf16_f32 v33,v180,v181
v_cvt_pk_bf16_f32 v37,v182,v183
s_setprio 3
v_mfma_f32_16x16x32_bf16 v[220:223],v[56:59],v[140:143],v[60:63]
ds_read_b128 v[56:59],v14 offset:768
s_setprio 0
v_cvt_pk_bf16_f32 v34,v224,v225
v_cvt_pk_bf16_f32 v38,v226,v227
v_cvt_pk_bf16_f32 v35,v228,v229
v_cvt_pk_bf16_f32 v39,v230,v231
v_fma_f32 v184,v184,v122,v126
s_waitcnt lgkmcnt(1)
s_setprio 3
v_mfma_f32_16x16x32_bf16 v[176:179],v[104:107],v[144:147],v[108:111]
ds_read_b128 v[60:63],v7 offset:3744
s_setprio 0
v_fma_f32 v185,v185,v122,v126
v_fma_f32 v186,v186,v123,v127
v_fma_f32 v187,v187,v123,v127
v_mfma_f32_16x16x32_bf16 v[128:131],v[64:67],v[16:19],v[112:115]
v_fma_f32 v188,v188,v122,v126
v_fma_f32 v189,v189,v122,v126
v_fma_f32 v190,v190,v123,v127
v_mfma_f32_16x16x32_bf16 v[180:183],v[104:107],v[148:151],v[108:111]
v_fma_f32 v191,v191,v123,v127
v_fma_f32 v232,v232,v122,v126
v_fma_f32 v233,v233,v122,v126
v_mfma_f32_16x16x32_bf16 v[136:139],v[64:67],v[20:23],v[112:115]
v_fma_f32 v234,v234,v123,v127
v_fma_f32 v235,v235,v123,v127
v_fma_f32 v236,v236,v122,v126
v_mfma_f32_16x16x32_bf16 v[224:227],v[104:107],v[152:155],v[108:111]
v_fma_f32 v237,v237,v122,v126
v_fma_f32 v238,v238,v123,v127
v_fma_f32 v239,v239,v123,v127
v_mfma_f32_16x16x32_bf16 v[140:143],v[68:71],v[20:23],v[116:119]
s_cbranch_vccnz RA33_%=
s_mov_b64 exec,s[62:63]
s_add_u32 m0,s57,55808
s_add_u32 s64,s56,5505024
buffer_load_dwordx4 v2,s[36:39],s64 offen lds
s_add_u32 m0,s57,57536
s_nop 0
buffer_load_dwordx4 v3,s[36:39],s64 offen lds
s_mov_b64 exec,-1
RA33_%=:
v_cvt_pk_bf16_f32 v40,v184,v185
v_cvt_pk_bf16_f32 v44,v186,v187
v_cvt_pk_bf16_f32 v41,v188,v189
s_setprio 3
v_mfma_f32_16x16x32_bf16 v[228:231],v[104:107],v[156:159],v[108:111]
ds_read_b128 v[104:107],v14 offset:1024
s_setprio 0
v_cvt_pk_bf16_f32 v45,v190,v191
v_cvt_pk_bf16_f32 v42,v232,v233
v_cvt_pk_bf16_f32 v46,v234,v235
s_setprio 3
v_mfma_f32_16x16x32_bf16 v[132:135],v[68:71],v[16:19],v[116:119]
ds_read_b128 v[108:111],v7 offset:3840
s_setprio 0
v_cvt_pk_bf16_f32 v43,v236,v237
s_nop 0
s_mov_b32 s86,0xbe801d5a
v_cvt_pk_bf16_f32 v47,v238,v239
v_fma_f32 v192,v192,v122,v126
s_waitcnt lgkmcnt(2)
v_mfma_f32_16x16x32_bf16 v[184:187],v[56:59],v[144:147],v[60:63]
v_fma_f32 v193,v193,v122,v126
v_fma_f32 v194,v194,v123,v127
v_mfma_f32_16x16x32_bf16 v[128:131],v[72:75],v[24:27],v[128:131]
v_fma_f32 v195,v195,v123,v127
v_fma_f32 v196,v196,v122,v126
v_mfma_f32_16x16x32_bf16 v[188:191],v[56:59],v[148:151],v[60:63]
v_fma_f32 v197,v197,v122,v126
v_fma_f32 v198,v198,v123,v127
v_mfma_f32_16x16x32_bf16 v[136:139],v[72:75],v[28:31],v[136:139]
v_fma_f32 v199,v199,v123,v127
v_fma_f32 v240,v240,v122,v126
v_mfma_f32_16x16x32_bf16 v[232:235],v[56:59],v[152:155],v[60:63]
v_fma_f32 v241,v241,v122,v126
v_fma_f32 v242,v242,v123,v127
v_mfma_f32_16x16x32_bf16 v[140:143],v[76:79],v[28:31],v[140:143]
v_fma_f32 v243,v243,v123,v127
v_fma_f32 v244,v244,v122,v126
v_mfma_f32_16x16x32_bf16 v[236:239],v[56:59],v[156:159],v[60:63]
s_cbranch_vccnz RA34_%=
s_mov_b64 exec,s[62:63]
s_add_u32 m0,s57,59296
s_add_u32 s64,s56,5603328
buffer_load_dwordx4 v2,s[36:39],s64 offen lds
s_add_u32 m0,s57,61024
s_nop 0
buffer_load_dwordx4 v3,s[36:39],s64 offen lds
s_mov_b64 exec,-1
RA34_%=:
s_setprio 3
ds_read_b128 v[144:147],v14 offset:1280
s_setprio 0
v_fma_f32 v245,v245,v122,v126
v_fma_f32 v246,v246,v123,v127
s_setprio 3
v_mfma_f32_16x16x32_bf16 v[132:135],v[76:79],v[24:27],v[132:135]
ds_read_b128 v[148:151],v7 offset:3872
s_setprio 0
v_fma_f32 v247,v247,v123,v127
v_cvt_pk_bf16_f32 v48,v192,v193
v_cvt_pk_bf16_f32 v52,v194,v195
v_cvt_pk_bf16_f32 v49,v196,v197
v_cvt_pk_bf16_f32 v53,v198,v199
v_cvt_pk_bf16_f32 v50,v240,v241
v_cvt_pk_bf16_f32 v54,v242,v243
v_cvt_pk_bf16_f32 v51,v244,v245
v_cvt_pk_bf16_f32 v55,v246,v247
v_fma_f32 v200,v200,v122,v126
s_waitcnt lgkmcnt(2)
v_mfma_f32_16x16x32_bf16 v[192:195],v[104:107],v[88:91],v[108:111]
v_fma_f32 v201,v201,v122,v126
v_fma_f32 v202,v202,v123,v127
v_fma_f32 v203,v203,v123,v127
v_mfma_f32_16x16x32_bf16 v[128:131],v[80:83],v[32:35],v[128:131]
v_fma_f32 v204,v204,v122,v126
v_fma_f32 v205,v205,v122,v126
v_fma_f32 v206,v206,v123,v127
v_mfma_f32_16x16x32_bf16 v[196:199],v[104:107],v[92:95],v[108:111]
v_fma_f32 v207,v207,v123,v127
v_fma_f32 v248,v248,v122,v126
v_fma_f32 v249,v249,v122,v126
v_mfma_f32_16x16x32_bf16 v[136:139],v[80:83],v[36:39],v[136:139]
v_fma_f32 v250,v250,v123,v127
v_fma_f32 v251,v251,v123,v127
v_fma_f32 v252,v252,v122,v126
v_mfma_f32_16x16x32_bf16 v[240:243],v[104:107],v[96:99],v[108:111]
v_fma_f32 v253,v253,v122,v126
v_fma_f32 v254,v254,v123,v127
v_fma_f32 v255,v255,v123,v127
v_mfma_f32_16x16x32_bf16 v[140:143],v[84:87],v[36:39],v[140:143]
s_cbranch_vccnz RA35_%=
s_mov_b64 exec,s[62:63]
s_add_u32 m0,s57,62784
s_add_u32 s64,s56,5701632
buffer_load_dwordx4 v2,s[36:39],s64 offen lds
s_add_u32 m0,s57,64512
s_nop 0
buffer_load_dwordx4 v3,s[36:39],s64 offen lds
s_mov_b64 exec,-1
RA35_%=:
v_cvt_pk_bf16_f32 v56,v200,v201
v_cvt_pk_bf16_f32 v60,v202,v203
v_cvt_pk_bf16_f32 v57,v204,v205
v_mfma_f32_16x16x32_bf16 v[244:247],v[104:107],v[100:103],v[108:111]
v_cvt_pk_bf16_f32 v61,v206,v207
v_cvt_pk_bf16_f32 v58,v248,v249
v_cvt_pk_bf16_f32 v62,v250,v251
v_mfma_f32_16x16x32_bf16 v[132:135],v[84:87],v[32:35],v[132:135]
v_cvt_pk_bf16_f32 v59,v252,v253
v_cvt_pk_bf16_f32 v63,v254,v255
s_waitcnt lgkmcnt(0)
v_mfma_f32_16x16x32_bf16 v[200:203],v[144:147],v[88:91],v[148:151]
s_barrier
s_setprio 3
ds_read_b64_tr_b16 v[104:105],v6 offset:12448
v_lshlrev_b32_sdwa v128,1,v128 dst_sel:DWORD dst_unused:UNUSED_PAD src0_sel:DWORD src1_sel:WORD_1
v_lshlrev_b32_sdwa v130,1,v130 dst_sel:DWORD dst_unused:UNUSED_PAD src0_sel:DWORD src1_sel:WORD_1
v_lshlrev_b32_sdwa v129,1,v129 dst_sel:DWORD dst_unused:UNUSED_PAD src0_sel:DWORD src1_sel:WORD_1
v_mfma_f32_16x16x32_bf16 v[204:207],v[144:147],v[92:95],v[148:151]
s_cbranch_vccz RA36_%=
s_add_u32 m0,s58,0
s_mul_i32 s69,s59,9
s_add_u32 s69,s69,s97
buffer_load_dwordx4 v2,s[44:47],s69 offen lds
s_add_u32 m0,m0,1024
s_add_u32 s69,s69,s53
buffer_load_dwordx4 v3,s[44:47],s69 offen lds
s_add_u32 m0,m0,1024
s_add_u32 s69,s69,s53
buffer_load_dwordx4 v2,s[44:47],s69 offen lds
s_add_u32 m0,m0,s50
s_add_u32 s69,s69,s53
buffer_load_dwordx4 v3,s[44:47],s69 offen lds
RA36_%=:
ds_read_u16 v128,v128
ds_read_b64_tr_b16 v[106:107],v6 offset:13984
v_lshlrev_b32_sdwa v131,1,v131 dst_sel:DWORD dst_unused:UNUSED_PAD src0_sel:DWORD src1_sel:WORD_1
v_fma_mixlo_f16 v120,v140,s99,0
v_fma_mixlo_f16 v122,v141,s99,0
v_mfma_f32_16x16x32_bf16 v[248:251],v[144:147],v[96:99],v[148:151]
ds_read_u16_d16_hi v130,v130
ds_read_b64_tr_b16 v[108:109],v6 offset:12456
v_lshlrev_b32_sdwa v132,1,v132 dst_sel:DWORD dst_unused:UNUSED_PAD src0_sel:DWORD src1_sel:WORD_1
v_lshlrev_b32_sdwa v134,1,v134 dst_sel:DWORD dst_unused:UNUSED_PAD src0_sel:DWORD src1_sel:WORD_1
v_mfma_f32_16x16x32_bf16 v[252:255],v[144:147],v[100:103],v[148:151]
ds_read_u16 v129,v129
ds_read_b64_tr_b16 v[110:111],v6 offset:13992
v_fma_mixhi_f16 v120,v142,s99,0
v_fma_mixhi_f16 v122,v143,s99,0
v_lshlrev_b32_sdwa v133,1,v133 dst_sel:DWORD dst_unused:UNUSED_PAD src0_sel:DWORD src1_sel:WORD_1
v_pk_fma_f16 v121,v120,v120,s54
v_mfma_f32_16x16x32_bf16 v[144:147],v[64:67],v[40:43],v[112:115]
s_cbranch_vccz RA37_%=
s_add_u32 m0,m0,1024
s_add_u32 s69,s69,s53
buffer_load_dwordx4 v2,s[44:47],s69 offen lds
s_add_u32 m0,m0,1024
s_add_u32 s69,s69,s53
buffer_load_dwordx4 v3,s[44:47],s69 offen lds
s_add_u32 m0,m0,s50
s_add_u32 s69,s69,s53
buffer_load_dwordx4 v2,s[44:47],s69 offen lds
s_add_u32 m0,m0,1024
s_add_u32 s69,s69,s53
buffer_load_dwordx4 v3,s[44:47],s69 offen lds
RA37_%=:
ds_read_u16_d16_hi v131,v131
ds_read_b64_tr_b16 v[88:89],v6 offset:12320
v_lshlrev_b32_sdwa v135,1,v135 dst_sel:DWORD dst_unused:UNUSED_PAD src0_sel:DWORD src1_sel:WORD_1
v_pk_fma_f16 v123,v122,v122,s54
v_pk_mul_f16 v121,v120,v121
v_mfma_f32_16x16x32_bf16 v[152:155],v[64:67],v[44:47],v[112:115]
ds_read_u16 v132,v132
ds_read_b64_tr_b16 v[90:91],v6 offset:13856
v_lshlrev_b32_sdwa v136,1,v136 dst_sel:DWORD dst_unused:UNUSED_PAD src0_sel:DWORD src1_sel:WORD_1
v_pk_mul_f16 v123,v122,v123
v_mfma_f32_16x16x32_bf16 v[156:159],v[68:71],v[44:47],v[116:119]
s_cbranch_vccnz RA38_%=
s_mov_b64 exec,s[62:63]
s_add_u32 m0,s57,66272
s_add_u32 s64,s56,5799936
buffer_load_dwordx4 v2,s[36:39],s64 offen lds
s_add_u32 m0,s57,68000
s_nop 0
buffer_load_dwordx4 v3,s[36:39],s64 offen lds
s_mov_b64 exec,-1
RA38_%=:
ds_read_u16_d16_hi v134,v134
ds_read_b64_tr_b16 v[92:93],v6 offset:12328
v_lshlrev_b32_sdwa v138,1,v138 dst_sel:DWORD dst_unused:UNUSED_PAD src0_sel:DWORD src1_sel:WORD_1
v_exp_f16 v120,v121
v_mfma_f32_16x16x32_bf16 v[148:151],v[68:71],v[40:43],v[116:119]
ds_read_u16 v133,v133
ds_read_b64_tr_b16 v[94:95],v6 offset:13864
v_lshlrev_b32_sdwa v137,1,v137 dst_sel:DWORD dst_unused:UNUSED_PAD src0_sel:DWORD src1_sel:WORD_1
v_lshlrev_b32_sdwa v139,1,v139 dst_sel:DWORD dst_unused:UNUSED_PAD src0_sel:DWORD src1_sel:WORD_1
v_exp_f16 v122,v123
v_mfma_f32_16x16x32_bf16 v[144:147],v[72:75],v[48:51],v[144:147]
s_cbranch_vccz RA39_%=
s_add_u32 m0,m0,1024
s_add_u32 s69,s69,s53
buffer_load_dwordx4 v2,s[44:47],s69 offen lds
s_add_u32 m0,m0,s50
s_add_u32 s69,s69,s53
buffer_load_dwordx4 v3,s[44:47],s69 offen lds
s_add_u32 m0,m0,1024
s_add_u32 s69,s69,s53
buffer_load_dwordx4 v2,s[44:47],s69 offen lds
s_add_u32 m0,m0,1024
s_add_u32 s69,s69,s53
buffer_load_dwordx4 v3,s[44:47],s69 offen lds
RA39_%=:
ds_read_u16_d16_hi v135,v135
ds_read_b64_tr_b16 v[96:97],v6 offset:12384
v_exp_f16_sdwa v122,v123 dst_sel:WORD_1 dst_unused:UNUSED_PRESERVE src0_sel:WORD_1
v_mfma_f32_16x16x32_bf16 v[152:155],v[72:75],v[52:55],v[152:155]
ds_read_u16 v136,v136
ds_read_b64_tr_b16 v[98:99],v6 offset:13920
v_exp_f16_sdwa v120,v121 dst_sel:WORD_1 dst_unused:UNUSED_PRESERVE src0_sel:WORD_1
v_mfma_f32_16x16x32_bf16 v[156:159],v[76:79],v[52:55],v[156:159]
ds_read_u16_d16_hi v138,v138
ds_read_b64_tr_b16 v[100:101],v6 offset:12392
v_pk_fma_f16 v120,v120,s55,s55
v_pk_fma_f16 v122,v122,s55,s55
v_rcp_f16 v121,v120
v_mfma_f32_16x16x32_bf16 v[148:151],v[76:79],v[48:51],v[148:151]
ds_read_u16 v137,v137
ds_read_b64_tr_b16 v[102:103],v6 offset:13928
v_rcp_f16 v123,v122
v_mfma_f32_16x16x32_bf16 v[144:147],v[80:83],v[56:59],v[144:147]
ds_read_u16_d16_hi v139,v139
ds_read_b64_tr_b16 v[64:65],v4 offset:12320
v_rcp_f16_sdwa v121,v120 dst_sel:WORD_1 dst_unused:UNUSED_PRESERVE src0_sel:WORD_1
v_mfma_f32_16x16x32_bf16 v[152:155],v[80:83],v[60:63],v[152:155]
ds_read_b64_tr_b16 v[66:67],v4 offset:14368
v_rcp_f16_sdwa v123,v122 dst_sel:WORD_1 dst_unused:UNUSED_PRESERVE src0_sel:WORD_1
v_fma_mix_f32 v142,v142,v121,0 op_sel:[0,1,0] op_sel_hi:[0,1,0]
v_mfma_f32_16x16x32_bf16 v[156:159],v[84:87],v[60:63],v[156:159]
ds_read_b128 v[112:115],v7 offset:1024
v_fma_mix_f32 v140,v140,v121,0 op_sel_hi:[0,1,0]
s_waitcnt lgkmcnt(15)
v_lshl_add_u64 v[128:129],v[128:129],0,v[130:131]
s_waitcnt lgkmcnt(11)
v_lshl_add_u64 v[130:131],v[132:133],0,v[134:135]
v_mfma_f32_16x16x32_bf16 v[148:151],v[84:87],v[56:59],v[148:151]
s_cbranch_vccnz RA40_%=
s_mov_b64 exec,s[62:63]
s_add_u32 m0,s57,0
s_add_u32 s64,s56,5898240
buffer_load_dwordx4 v2,s[36:39],s64 offen lds
s_add_u32 m0,s57,1728
s_nop 0
buffer_load_dwordx4 v3,s[36:39],s64 offen lds
s_mov_b64 exec,-1
RA40_%=:
ds_read_b64_tr_b16 v[68:69],v5 offset:12320
v_fma_mix_f32 v141,v141,v123,0 op_sel_hi:[0,1,0]
v_fma_mix_f32 v143,v143,v123,0 op_sel:[0,1,0] op_sel_hi:[0,1,0]
v_lshlrev_b32_sdwa v144,1,v144 dst_sel:DWORD dst_unused:UNUSED_PAD src0_sel:DWORD src1_sel:WORD_1
v_lshlrev_b32_sdwa v146,1,v146 dst_sel:DWORD dst_unused:UNUSED_PAD src0_sel:DWORD src1_sel:WORD_1
v_mfma_f32_16x16x32_bf16 v[160:163],v[88:91],v[128:131],v[160:163]
ds_read_u16 v144,v144
v_lshlrev_b32_sdwa v145,1,v145 dst_sel:DWORD dst_unused:UNUSED_PAD src0_sel:DWORD src1_sel:WORD_1
v_lshlrev_b32_sdwa v147,1,v147 dst_sel:DWORD dst_unused:UNUSED_PAD src0_sel:DWORD src1_sel:WORD_1
v_fma_mixlo_f16 v122,v157,s99,0
v_mfma_f32_16x16x32_bf16 v[168:171],v[92:95],v[128:131],v[168:171]
s_waitcnt lgkmcnt(10)
ds_read_b64_tr_b16 v[70:71],v5 offset:14368
ds_read_b128 v[116:119],v7 offset:1056
v_fma_mixlo_f16 v120,v156,s99,0
v_mfma_f32_16x16x32_bf16 v[176:179],v[96:99],v[128:131],v[176:179]
s_waitcnt lgkmcnt(8)
ds_read_u16_d16_hi v146,v146
v_lshlrev_b32_sdwa v148,1,v148 dst_sel:DWORD dst_unused:UNUSED_PAD src0_sel:DWORD src1_sel:WORD_1
v_lshlrev_b32_sdwa v150,1,v150 dst_sel:DWORD dst_unused:UNUSED_PAD src0_sel:DWORD src1_sel:WORD_1
v_mfma_f32_16x16x32_bf16 v[184:187],v[100:103],v[128:131],v[184:187]
s_waitcnt lgkmcnt(8)
ds_read_b64_tr_b16 v[72:73],v4 offset:16416
ds_read_b64_tr_b16 v[74:75],v4 offset:18464
v_lshl_add_u64 v[136:137],v[136:137],0,v[138:139]
v_fma_mixhi_f16 v120,v158,s99,0
v_mfma_f32_16x16x32_bf16 v[192:195],v[104:107],v[128:131],v[192:195]
ds_read_u16 v145,v145
v_cvt_pk_bf16_f32 v138,v140,v142
v_cvt_pk_bf16_f32 v139,v141,v143
v_mfma_f32_16x16x32_bf16 v[200:203],v[108:111],v[128:131],v[200:203]
v_fma_mixhi_f16 v122,v159,s99,0
v_mfma_f32_16x16x32_bf16 v[164:167],v[88:91],v[136:139],v[164:167]
ds_read_b64_tr_b16 v[76:77],v5 offset:16416
ds_read_b64_tr_b16 v[78:79],v5 offset:18464
v_lshlrev_b32_sdwa v149,1,v149 dst_sel:DWORD dst_unused:UNUSED_PAD src0_sel:DWORD src1_sel:WORD_1
v_lshlrev_b32_sdwa v151,1,v151 dst_sel:DWORD dst_unused:UNUSED_PAD src0_sel:DWORD src1_sel:WORD_1
v_mfma_f32_16x16x32_bf16 v[172:175],v[92:95],v[136:139],v[172:175]
ds_read_b64_tr_b16 v[80:81],v4 offset:20512
ds_read_b64_tr_b16 v[82:83],v4 offset:22560
v_pk_fma_f16 v121,v120,v120,s54
v_pk_fma_f16 v123,v122,v122,s54
v_mfma_f32_16x16x32_bf16 v[180:183],v[96:99],v[136:139],v[180:183]
ds_read_u16_d16_hi v147,v147
v_pk_mul_f16 v121,v120,v121
v_pk_mul_f16 v123,v122,v123
v_mfma_f32_16x16x32_bf16 v[188:191],v[100:103],v[136:139],v[188:191]
s_cbranch_vccnz RA41_%=
s_mov_b64 exec,s[62:63]
s_add_u32 m0,s57,3488
s_add_u32 s64,s56,5996544
buffer_load_dwordx4 v2,s[36:39],s64 offen lds
s_add_u32 m0,s57,5216
s_nop 0
buffer_load_dwordx4 v3,s[36:39],s64 offen lds
s_mov_b64 exec,-1
RA41_%=:
ds_read_u16 v148,v148
v_lshlrev_b32_sdwa v152,1,v152 dst_sel:DWORD dst_unused:UNUSED_PAD src0_sel:DWORD src1_sel:WORD_1
v_lshlrev_b32_sdwa v154,1,v154 dst_sel:DWORD dst_unused:UNUSED_PAD src0_sel:DWORD src1_sel:WORD_1
v_mfma_f32_16x16x32_bf16 v[196:199],v[104:107],v[136:139],v[196:199]
v_exp_f16 v120,v121
v_mfma_f32_16x16x32_bf16 v[204:207],v[108:111],v[136:139],v[204:207]
s_waitcnt lgkmcnt(14)
ds_read_b64_tr_b16 v[84:85],v5 offset:20512
ds_read_b64_tr_b16 v[86:87],v5 offset:22560
v_exp_f16 v122,v123
v_mfma_f32_16x16x32_bf16 v[128:131],v[64:67],v[16:19],v[112:115]
v_lshlrev_b32_sdwa v153,1,v153 dst_sel:DWORD dst_unused:UNUSED_PAD src0_sel:DWORD src1_sel:WORD_1
v_lshlrev_b32_sdwa v155,1,v155 dst_sel:DWORD dst_unused:UNUSED_PAD src0_sel:DWORD src1_sel:WORD_1
v_mfma_f32_16x16x32_bf16 v[136:139],v[64:67],v[20:23],v[112:115]
s_waitcnt lgkmcnt(12)
ds_read_u16_d16_hi v150,v150
ds_read_u16 v149,v149
v_exp_f16_sdwa v120,v121 dst_sel:WORD_1 dst_unused:UNUSED_PRESERVE src0_sel:WORD_1
v_mfma_f32_16x16x32_bf16 v[140:143],v[68:71],v[20:23],v[116:119]
v_exp_f16_sdwa v122,v123 dst_sel:WORD_1 dst_unused:UNUSED_PRESERVE src0_sel:WORD_1
v_mfma_f32_16x16x32_bf16 v[132:135],v[68:71],v[16:19],v[116:119]
s_waitcnt lgkmcnt(11)
v_pk_fma_f16 v120,v120,s55,s55
v_mfma_f32_16x16x32_bf16 v[128:131],v[72:75],v[24:27],v[128:131]
v_pk_fma_f16 v122,v122,s55,s55
v_rcp_f16 v121,v120
v_mfma_f32_16x16x32_bf16 v[136:139],v[72:75],v[28:31],v[136:139]
s_waitcnt lgkmcnt(8)
ds_read_u16_d16_hi v151,v151
v_rcp_f16 v123,v122
v_mfma_f32_16x16x32_bf16 v[140:143],v[76:79],v[28:31],v[140:143]
ds_read_u16 v152,v152
ds_read_u16_d16_hi v154,v154
v_rcp_f16_sdwa v121,v120 dst_sel:WORD_1 dst_unused:UNUSED_PRESERVE src0_sel:WORD_1
v_mfma_f32_16x16x32_bf16 v[132:135],v[76:79],v[24:27],v[132:135]
s_waitcnt lgkmcnt(9)
s_cbranch_vccnz RA42_%=
s_mov_b64 exec,s[62:63]
s_add_u32 m0,s57,6976
s_add_u32 s64,s56,6094848
buffer_load_dwordx4 v2,s[36:39],s64 offen lds
s_add_u32 m0,s57,8704
s_nop 0
buffer_load_dwordx4 v3,s[36:39],s64 offen lds
s_mov_b64 exec,-1
RA42_%=:
v_rcp_f16_sdwa v123,v122 dst_sel:WORD_1 dst_unused:UNUSED_PRESERVE src0_sel:WORD_1
v_mfma_f32_16x16x32_bf16 v[128:131],v[80:83],v[32:35],v[128:131]
ds_read_u16 v153,v153
v_fma_mix_f32 v156,v156,v121,0 op_sel_hi:[0,1,0]
v_fma_mix_f32 v158,v158,v121,0 op_sel:[0,1,0] op_sel_hi:[0,1,0]
v_mfma_f32_16x16x32_bf16 v[136:139],v[80:83],v[36:39],v[136:139]
s_waitcnt lgkmcnt(3)
v_lshl_add_u64 v[144:145],v[144:145],0,v[146:147]
v_lshl_add_u64 v[146:147],v[148:149],0,v[150:151]
v_mfma_f32_16x16x32_bf16 v[140:143],v[84:87],v[36:39],v[140:143]
ds_read_u16_d16_hi v155,v155
v_fma_mix_f32 v157,v157,v123,0 op_sel_hi:[0,1,0]
v_fma_mix_f32 v159,v159,v123,0 op_sel:[0,1,0] op_sel_hi:[0,1,0]
v_mfma_f32_16x16x32_bf16 v[132:135],v[84:87],v[32:35],v[132:135]
v_lshlrev_b32_sdwa v128,1,v128 dst_sel:DWORD dst_unused:UNUSED_PAD src0_sel:DWORD src1_sel:WORD_1
v_lshlrev_b32_sdwa v130,1,v130 dst_sel:DWORD dst_unused:UNUSED_PAD src0_sel:DWORD src1_sel:WORD_1
v_mfma_f32_16x16x32_bf16 v[208:211],v[88:91],v[144:147],v[208:211]
v_lshlrev_b32_sdwa v129,1,v129 dst_sel:DWORD dst_unused:UNUSED_PAD src0_sel:DWORD src1_sel:WORD_1
v_lshlrev_b32_sdwa v131,1,v131 dst_sel:DWORD dst_unused:UNUSED_PAD src0_sel:DWORD src1_sel:WORD_1
v_mfma_f32_16x16x32_bf16 v[216:219],v[92:95],v[144:147],v[216:219]
ds_read_u16 v128,v128
ds_read_u16_d16_hi v130,v130
v_fma_mixlo_f16 v120,v140,s99,0
v_fma_mixlo_f16 v122,v141,s99,0
v_mfma_f32_16x16x32_bf16 v[224:227],v[96:99],v[144:147],v[224:227]
ds_read_u16 v129,v129
v_lshlrev_b32_sdwa v132,1,v132 dst_sel:DWORD dst_unused:UNUSED_PAD src0_sel:DWORD src1_sel:WORD_1
v_lshlrev_b32_sdwa v134,1,v134 dst_sel:DWORD dst_unused:UNUSED_PAD src0_sel:DWORD src1_sel:WORD_1
v_mfma_f32_16x16x32_bf16 v[232:235],v[100:103],v[144:147],v[232:235]
s_waitcnt lgkmcnt(3)
v_lshl_add_u64 v[152:153],v[152:153],0,v[154:155]
v_cvt_pk_bf16_f32 v154,v156,v158
v_mfma_f32_16x16x32_bf16 v[240:243],v[104:107],v[144:147],v[240:243]
v_fma_mixhi_f16 v120,v142,s99,0
v_cvt_pk_bf16_f32 v155,v157,v159
v_mfma_f32_16x16x32_bf16 v[248:251],v[108:111],v[144:147],v[248:251]
s_cbranch_vccnz RA43_%=
s_mov_b64 exec,s[62:63]
s_add_u32 m0,s57,10464
s_add_u32 s64,s56,6193152
buffer_load_dwordx4 v2,s[36:39],s64 offen lds
s_add_u32 m0,s57,12192
s_nop 0
buffer_load_dwordx4 v3,s[36:39],s64 offen lds
s_mov_b64 exec,-1
RA43_%=:
ds_read_u16_d16_hi v131,v131
v_fma_mixhi_f16 v122,v143,s99,0
v_mfma_f32_16x16x32_bf16 v[212:215],v[88:91],v[152:155],v[212:215]
v_lshlrev_b32_sdwa v133,1,v133 dst_sel:DWORD dst_unused:UNUSED_PAD src0_sel:DWORD src1_sel:WORD_1
v_pk_fma_f16 v121,v120,v120,s54
v_lshlrev_b32_sdwa v135,1,v135 dst_sel:DWORD dst_unused:UNUSED_PAD src0_sel:DWORD src1_sel:WORD_1
v_mfma_f32_16x16x32_bf16 v[220:223],v[92:95],v[152:155],v[220:223]
s_setprio 2
ds_read_u16 v132,v132
ds_read_u16_d16_hi v134,v134
v_pk_fma_f16 v123,v122,v122,s54
v_mfma_f32_16x16x32_bf16 v[228:231],v[96:99],v[152:155],v[228:231]
ds_read_b64_tr_b16 v[88:89],v6 offset:18480
v_pk_mul_f16 v121,v120,v121
v_lshlrev_b32_sdwa v136,1,v136 dst_sel:DWORD dst_unused:UNUSED_PAD src0_sel:DWORD src1_sel:WORD_1
v_pk_mul_f16 v123,v122,v123
v_mfma_f32_16x16x32_bf16 v[236:239],v[100:103],v[152:155],v[236:239]
ds_read_b64_tr_b16 v[90:91],v6 offset:20016
v_lshlrev_b32_sdwa v138,1,v138 dst_sel:DWORD dst_unused:UNUSED_PAD src0_sel:DWORD src1_sel:WORD_1
v_mfma_f32_16x16x32_bf16 v[244:247],v[104:107],v[152:155],v[244:247]
ds_read_u16 v133,v133
v_exp_f16 v120,v121
v_mfma_f32_16x16x32_bf16 v[252:255],v[108:111],v[152:155],v[252:255]
ds_read_b64_tr_b16 v[92:93],v6 offset:18488
v_lshlrev_b32_sdwa v137,1,v137 dst_sel:DWORD dst_unused:UNUSED_PAD src0_sel:DWORD src1_sel:WORD_1
v_mfma_f32_16x16x32_bf16 v[144:147],v[64:67],v[40:43],v[112:115]
ds_read_b64_tr_b16 v[94:95],v6 offset:20024
v_lshlrev_b32_sdwa v139,1,v139 dst_sel:DWORD dst_unused:UNUSED_PAD src0_sel:DWORD src1_sel:WORD_1
v_exp_f16 v122,v123
v_mfma_f32_16x16x32_bf16 v[152:155],v[64:67],v[44:47],v[112:115]
ds_read_u16_d16_hi v135,v135
ds_read_b64_tr_b16 v[96:97],v6 offset:18544
v_mfma_f32_16x16x32_bf16 v[156:159],v[68:71],v[44:47],v[116:119]
ds_read_u16 v136,v136
v_exp_f16_sdwa v122,v123 dst_sel:WORD_1 dst_unused:UNUSED_PRESERVE src0_sel:WORD_1
v_exp_f16_sdwa v120,v121 dst_sel:WORD_1 dst_unused:UNUSED_PRESERVE src0_sel:WORD_1
v_mfma_f32_16x16x32_bf16 v[148:151],v[68:71],v[40:43],v[116:119]
ds_read_b64_tr_b16 v[98:99],v6 offset:20080
v_pk_fma_f16 v120,v120,s55,s55
v_pk_fma_f16 v122,v122,s55,s55
v_mfma_f32_16x16x32_bf16 v[144:147],v[72:75],v[48:51],v[144:147]
s_cbranch_vccnz RA44_%=
s_mov_b64 exec,s[62:63]
s_add_u32 m0,s57,13952
s_add_u32 s64,s56,6291456
buffer_load_dwordx4 v2,s[36:39],s64 offen lds
s_add_u32 m0,s57,15680
s_nop 0
buffer_load_dwordx4 v3,s[36:39],s64 offen lds
s_mov_b64 exec,-1
RA44_%=:
v_rcp_f16 v121,v120
v_mfma_f32_16x16x32_bf16 v[152:155],v[72:75],v[52:55],v[152:155]
ds_read_b64_tr_b16 v[100:101],v6 offset:18552
v_rcp_f16 v123,v122
v_mfma_f32_16x16x32_bf16 v[156:159],v[76:79],v[52:55],v[156:159]
ds_read_u16_d16_hi v138,v138
v_rcp_f16_sdwa v121,v120 dst_sel:WORD_1 dst_unused:UNUSED_PRESERVE src0_sel:WORD_1
v_mfma_f32_16x16x32_bf16 v[148:151],v[76:79],v[48:51],v[148:151]
ds_read_b64_tr_b16 v[102:103],v6 offset:20088
ds_read_b64_tr_b16 v[104:105],v6 offset:18608
v_rcp_f16_sdwa v123,v122 dst_sel:WORD_1 dst_unused:UNUSED_PRESERVE src0_sel:WORD_1
v_mfma_f32_16x16x32_bf16 v[144:147],v[80:83],v[56:59],v[144:147]
ds_read_u16 v137,v137
v_fma_mix_f32 v142,v142,v121,0 op_sel:[0,1,0] op_sel_hi:[0,1,0]
v_mfma_f32_16x16x32_bf16 v[152:155],v[80:83],v[60:63],v[152:155]
s_waitcnt lgkmcnt(8)
ds_read_b64_tr_b16 v[106:107],v6 offset:20144
ds_read_b64_tr_b16 v[108:109],v6 offset:18616
v_fma_mix_f32 v140,v140,v121,0 op_sel_hi:[0,1,0]
v_lshl_add_u64 v[128:129],v[128:129],0,v[130:131]
v_lshl_add_u64 v[130:131],v[132:133],0,v[134:135]
v_mfma_f32_16x16x32_bf16 v[156:159],v[84:87],v[60:63],v[156:159]
ds_read_u16_d16_hi v139,v139
v_fma_mix_f32 v141,v141,v123,0 op_sel_hi:[0,1,0]
v_fma_mix_f32 v143,v143,v123,0 op_sel:[0,1,0] op_sel_hi:[0,1,0]
v_mfma_f32_16x16x32_bf16 v[148:151],v[84:87],v[56:59],v[148:151]
ds_read_b64_tr_b16 v[110:111],v6 offset:20152
ds_read_b64_tr_b16 v[64:65],v4 offset:12832
v_lshlrev_b32_sdwa v144,1,v144 dst_sel:DWORD dst_unused:UNUSED_PAD src0_sel:DWORD src1_sel:WORD_1
v_lshlrev_b32_sdwa v146,1,v146 dst_sel:DWORD dst_unused:UNUSED_PAD src0_sel:DWORD src1_sel:WORD_1
v_mfma_f32_16x16x32_bf16 v[160:163],v[88:91],v[128:131],v[160:163]
ds_read_u16 v144,v144
v_lshlrev_b32_sdwa v145,1,v145 dst_sel:DWORD dst_unused:UNUSED_PAD src0_sel:DWORD src1_sel:WORD_1
v_lshlrev_b32_sdwa v147,1,v147 dst_sel:DWORD dst_unused:UNUSED_PAD src0_sel:DWORD src1_sel:WORD_1
v_fma_mixlo_f16 v122,v157,s99,0
v_mfma_f32_16x16x32_bf16 v[168:171],v[92:95],v[128:131],v[168:171]
s_waitcnt lgkmcnt(11)
ds_read_b64_tr_b16 v[66:67],v4 offset:14880
ds_read_b128 v[112:115],v7 offset:1152
v_fma_mixlo_f16 v120,v156,s99,0
v_mfma_f32_16x16x32_bf16 v[176:179],v[96:99],v[128:131],v[176:179]
s_waitcnt lgkmcnt(10)
s_cbranch_vccnz RA45_%=
s_mov_b64 exec,s[62:63]
s_add_u32 m0,s57,17440
s_add_u32 s64,s56,6389760
buffer_load_dwordx4 v2,s[36:39],s64 offen lds
s_add_u32 m0,s57,19168
s_nop 0
buffer_load_dwordx4 v3,s[36:39],s64 offen lds
s_mov_b64 exec,-1
RA45_%=:
ds_read_u16_d16_hi v146,v146
v_lshlrev_b32_sdwa v148,1,v148 dst_sel:DWORD dst_unused:UNUSED_PAD src0_sel:DWORD src1_sel:WORD_1
v_lshlrev_b32_sdwa v150,1,v150 dst_sel:DWORD dst_unused:UNUSED_PAD src0_sel:DWORD src1_sel:WORD_1
v_mfma_f32_16x16x32_bf16 v[184:187],v[100:103],v[128:131],v[184:187]
s_waitcnt lgkmcnt(6)
ds_read_b64_tr_b16 v[68:69],v5 offset:12832
ds_read_b64_tr_b16 v[70:71],v5 offset:14880
v_lshl_add_u64 v[136:137],v[136:137],0,v[138:139]
v_fma_mixhi_f16 v120,v158,s99,0
v_mfma_f32_16x16x32_bf16 v[192:195],v[104:107],v[128:131],v[192:195]
s_waitcnt lgkmcnt(7)
ds_read_u16 v145,v145
v_cvt_pk_bf16_f32 v138,v140,v142
v_cvt_pk_bf16_f32 v139,v141,v143
v_mfma_f32_16x16x32_bf16 v[200:203],v[108:111],v[128:131],v[200:203]
v_fma_mixhi_f16 v122,v159,s99,0
v_mfma_f32_16x16x32_bf16 v[164:167],v[88:91],v[136:139],v[164:167]
ds_read_b128 v[116:119],v7 offset:1184
ds_read_b64_tr_b16 v[72:73],v4 offset:16928
v_lshlrev_b32_sdwa v149,1,v149 dst_sel:DWORD dst_unused:UNUSED_PAD src0_sel:DWORD src1_sel:WORD_1
v_lshlrev_b32_sdwa v151,1,v151 dst_sel:DWORD dst_unused:UNUSED_PAD src0_sel:DWORD src1_sel:WORD_1
v_mfma_f32_16x16x32_bf16 v[172:175],v[92:95],v[136:139],v[172:175]
ds_read_b64_tr_b16 v[74:75],v4 offset:18976
ds_read_b64_tr_b16 v[76:77],v5 offset:16928
v_pk_fma_f16 v121,v120,v120,s54
v_pk_fma_f16 v123,v122,v122,s54
v_mfma_f32_16x16x32_bf16 v[180:183],v[96:99],v[136:139],v[180:183]
s_setprio 1
ds_read_u16_d16_hi v147,v147
v_pk_mul_f16 v121,v120,v121
v_pk_mul_f16 v123,v122,v123
v_mfma_f32_16x16x32_bf16 v[188:191],v[100:103],v[136:139],v[188:191]
ds_read_u16 v148,v148
v_lshlrev_b32_sdwa v152,1,v152 dst_sel:DWORD dst_unused:UNUSED_PAD src0_sel:DWORD src1_sel:WORD_1
v_lshlrev_b32_sdwa v154,1,v154 dst_sel:DWORD dst_unused:UNUSED_PAD src0_sel:DWORD src1_sel:WORD_1
v_mfma_f32_16x16x32_bf16 v[196:199],v[104:107],v[136:139],v[196:199]
v_exp_f16 v120,v121
v_mfma_f32_16x16x32_bf16 v[204:207],v[108:111],v[136:139],v[204:207]
s_waitcnt lgkmcnt(10)
ds_read_b64_tr_b16 v[78:79],v5 offset:18976
ds_read_b64_tr_b16 v[80:81],v4 offset:21024
v_exp_f16 v122,v123
v_mfma_f32_16x16x32_bf16 v[128:131],v[64:67],v[16:19],v[112:115]
v_lshlrev_b32_sdwa v153,1,v153 dst_sel:DWORD dst_unused:UNUSED_PAD src0_sel:DWORD src1_sel:WORD_1
v_lshlrev_b32_sdwa v155,1,v155 dst_sel:DWORD dst_unused:UNUSED_PAD src0_sel:DWORD src1_sel:WORD_1
v_mfma_f32_16x16x32_bf16 v[136:139],v[64:67],v[20:23],v[112:115]
s_waitcnt lgkmcnt(7)
ds_read_u16_d16_hi v150,v150
ds_read_u16 v149,v149
v_exp_f16_sdwa v120,v121 dst_sel:WORD_1 dst_unused:UNUSED_PRESERVE src0_sel:WORD_1
v_mfma_f32_16x16x32_bf16 v[140:143],v[68:71],v[20:23],v[116:119]
v_exp_f16_sdwa v122,v123 dst_sel:WORD_1 dst_unused:UNUSED_PRESERVE src0_sel:WORD_1
v_mfma_f32_16x16x32_bf16 v[132:135],v[68:71],v[16:19],v[116:119]
s_waitcnt lgkmcnt(7)
ds_read_b64_tr_b16 v[82:83],v4 offset:23072
v_pk_fma_f16 v120,v120,s55,s55
v_mfma_f32_16x16x32_bf16 v[128:131],v[72:75],v[24:27],v[128:131]
ds_read_b64_tr_b16 v[84:85],v5 offset:21024
v_pk_fma_f16 v122,v122,s55,s55
v_rcp_f16 v121,v120
v_mfma_f32_16x16x32_bf16 v[136:139],v[72:75],v[28:31],v[136:139]
s_waitcnt lgkmcnt(5)
ds_read_b64_tr_b16 v[86:87],v5 offset:23072
ds_read_u16_d16_hi v151,v151
v_rcp_f16 v123,v122
v_mfma_f32_16x16x32_bf16 v[140:143],v[76:79],v[28:31],v[140:143]
ds_read_u16 v152,v152
ds_read_u16_d16_hi v154,v154
v_rcp_f16_sdwa v121,v120 dst_sel:WORD_1 dst_unused:UNUSED_PRESERVE src0_sel:WORD_1
v_mfma_f32_16x16x32_bf16 v[132:135],v[76:79],v[24:27],v[132:135]
s_waitcnt lgkmcnt(5)
v_rcp_f16_sdwa v123,v122 dst_sel:WORD_1 dst_unused:UNUSED_PRESERVE src0_sel:WORD_1
v_mfma_f32_16x16x32_bf16 v[128:131],v[80:83],v[32:35],v[128:131]
ds_read_u16 v153,v153
v_fma_mix_f32 v156,v156,v121,0 op_sel_hi:[0,1,0]
v_fma_mix_f32 v158,v158,v121,0 op_sel:[0,1,0] op_sel_hi:[0,1,0]
v_mfma_f32_16x16x32_bf16 v[136:139],v[80:83],v[36:39],v[136:139]
s_waitcnt lgkmcnt(3)
s_setprio 0
v_lshl_add_u64 v[144:145],v[144:145],0,v[146:147]
v_lshl_add_u64 v[146:147],v[148:149],0,v[150:151]
v_mfma_f32_16x16x32_bf16 v[140:143],v[84:87],v[36:39],v[140:143]
ds_read_u16_d16_hi v155,v155
v_fma_mix_f32 v157,v157,v123,0 op_sel_hi:[0,1,0]
v_fma_mix_f32 v159,v159,v123,0 op_sel:[0,1,0] op_sel_hi:[0,1,0]
v_mfma_f32_16x16x32_bf16 v[132:135],v[84:87],v[32:35],v[132:135]
v_lshlrev_b32_sdwa v128,1,v128 dst_sel:DWORD dst_unused:UNUSED_PAD src0_sel:DWORD src1_sel:WORD_1
s_mov_b32 s86,0xbe801d5a
v_lshlrev_b32_sdwa v130,1,v130 dst_sel:DWORD dst_unused:UNUSED_PAD src0_sel:DWORD src1_sel:WORD_1
v_mfma_f32_16x16x32_bf16 v[208:211],v[88:91],v[144:147],v[208:211]
s_cbranch_vccz RA46_%=
s_waitcnt vmcnt(0)
RA46_%=:
s_barrier
s_setprio 2
ds_read_u16 v128,v128
v_lshlrev_b32_sdwa v129,1,v129 dst_sel:DWORD dst_unused:UNUSED_PAD src0_sel:DWORD src1_sel:WORD_1
v_mfma_f32_16x16x32_bf16 v[216:219],v[92:95],v[144:147],v[216:219]
ds_read_u16_d16_hi v130,v130
v_lshlrev_b32_sdwa v131,1,v131 dst_sel:DWORD dst_unused:UNUSED_PAD src0_sel:DWORD src1_sel:WORD_1
v_mfma_f32_16x16x32_bf16 v[224:227],v[96:99],v[144:147],v[224:227]
s_cbranch_vccz RA47_%=
s_add_u32 m0,s58,12320
s_mov_b32 s69,s60
buffer_load_dwordx4 v2,s[44:47],s69 offen lds
s_add_u32 m0,m0,1024
s_add_u32 s69,s69,s53
buffer_load_dwordx4 v3,s[44:47],s69 offen lds
s_add_u32 m0,m0,1024
s_add_u32 s69,s69,s53
buffer_load_dwordx4 v2,s[44:47],s69 offen lds
s_add_u32 m0,m0,s50
s_add_u32 s69,s69,s53
buffer_load_dwordx4 v3,s[44:47],s69 offen lds
RA47_%=:
v_lshlrev_b32_sdwa v132,1,v132 dst_sel:DWORD dst_unused:UNUSED_PAD src0_sel:DWORD src1_sel:WORD_1
v_mfma_f32_16x16x32_bf16 v[232:235],v[100:103],v[144:147],v[232:235]
s_waitcnt lgkmcnt(2)
ds_read_u16 v129,v129
v_lshl_add_u64 v[152:153],v[152:153],0,v[154:155]
v_lshlrev_b32_sdwa v134,1,v134 dst_sel:DWORD dst_unused:UNUSED_PAD src0_sel:DWORD src1_sel:WORD_1
v_mfma_f32_16x16x32_bf16 v[240:243],v[104:107],v[144:147],v[240:243]
v_cvt_pk_bf16_f32 v155,v157,v159
v_cvt_pk_bf16_f32 v154,v156,v158
v_mfma_f32_16x16x32_bf16 v[248:251],v[108:111],v[144:147],v[248:251]
ds_read_u16_d16_hi v131,v131
v_lshlrev_b32_sdwa v133,1,v133 dst_sel:DWORD dst_unused:UNUSED_PAD src0_sel:DWORD src1_sel:WORD_1
v_mfma_f32_16x16x32_bf16 v[212:215],v[88:91],v[152:155],v[212:215]
s_cbranch_vccz RA48_%=
s_add_u32 m0,m0,1024
s_add_u32 s69,s69,s53
buffer_load_dwordx4 v2,s[44:47],s69 offen lds
s_add_u32 m0,m0,1024
s_add_u32 s69,s69,s53
buffer_load_dwordx4 v3,s[44:47],s69 offen lds
s_add_u32 m0,m0,s50
s_add_u32 s69,s69,s51
buffer_load_dwordx4 v2,s[44:47],s69 offen lds
s_add_u32 m0,m0,1024
s_add_u32 s69,s69,s53
buffer_load_dwordx4 v3,s[44:47],s69 offen lds
RA48_%=:
ds_read_u16 v132,v132
v_lshlrev_b32_sdwa v135,1,v135 dst_sel:DWORD dst_unused:UNUSED_PAD src0_sel:DWORD src1_sel:WORD_1
v_mfma_f32_16x16x32_bf16 v[220:223],v[92:95],v[152:155],v[220:223]
ds_read_b64_tr_b16 v[88:89],v6 offset:0
ds_read_b64_tr_b16 v[90:91],v6 offset:1536
v_lshlrev_b32_sdwa v136,1,v136 dst_sel:DWORD dst_unused:UNUSED_PAD src0_sel:DWORD src1_sel:WORD_1
v_mfma_f32_16x16x32_bf16 v[228:231],v[96:99],v[152:155],v[228:231]
ds_read_u16_d16_hi v134,v134
v_lshlrev_b32_sdwa v138,1,v138 dst_sel:DWORD dst_unused:UNUSED_PAD src0_sel:DWORD src1_sel:WORD_1
v_mfma_f32_16x16x32_bf16 v[236:239],v[100:103],v[152:155],v[236:239]
ds_read_u16 v133,v133
v_lshlrev_b32_sdwa v137,1,v137 dst_sel:DWORD dst_unused:UNUSED_PAD src0_sel:DWORD src1_sel:WORD_1
v_mfma_f32_16x16x32_bf16 v[244:247],v[104:107],v[152:155],v[244:247]
ds_read_u16_d16_hi v135,v135
v_lshlrev_b32_sdwa v139,1,v139 dst_sel:DWORD dst_unused:UNUSED_PAD src0_sel:DWORD src1_sel:WORD_1
v_mfma_f32_16x16x32_bf16 v[252:255],v[108:111],v[152:155],v[252:255]
s_cbranch_vccz RA49_%=
s_add_u32 m0,m0,1024
s_add_u32 s69,s69,s53
buffer_load_dwordx4 v2,s[44:47],s69 offen lds
s_add_u32 m0,m0,s50
s_add_u32 s69,s69,s53
buffer_load_dwordx4 v3,s[44:47],s69 offen lds
s_add_u32 m0,m0,1024
s_add_u32 s69,s69,s53
buffer_load_dwordx4 v2,s[44:47],s69 offen lds
s_add_u32 m0,m0,1024
s_add_u32 s69,s69,s53
buffer_load_dwordx4 v3,s[44:47],s69 offen lds
RA49_%=:
ds_read_b64_tr_b16 v[92:93],v6 offset:8
ds_read_b64_tr_b16 v[94:95],v6 offset:1544
v_lshlrev_b32_sdwa v140,1,v140 dst_sel:DWORD dst_unused:UNUSED_PAD src0_sel:DWORD src1_sel:WORD_1
v_mfma_f32_16x16x32_bf16 v[144:147],v[64:67],v[40:43],v[112:115]
ds_read_u16 v136,v136
v_lshlrev_b32_sdwa v142,1,v142 dst_sel:DWORD dst_unused:UNUSED_PAD src0_sel:DWORD src1_sel:WORD_1
v_mfma_f32_16x16x32_bf16 v[152:155],v[64:67],v[44:47],v[112:115]
ds_read_u16_d16_hi v138,v138
ds_read_b64_tr_b16 v[96:97],v6 offset:64
v_lshlrev_b32_sdwa v141,1,v141 dst_sel:DWORD dst_unused:UNUSED_PAD src0_sel:DWORD src1_sel:WORD_1
v_mfma_f32_16x16x32_bf16 v[156:159],v[68:71],v[44:47],v[116:119]
ds_read_b64_tr_b16 v[98:99],v6 offset:1600
ds_read_b64_tr_b16 v[100:101],v6 offset:72
v_mfma_f32_16x16x32_bf16 v[148:151],v[68:71],v[40:43],v[116:119]
ds_read_u16 v137,v137
v_lshlrev_b32_sdwa v143,1,v143 dst_sel:DWORD dst_unused:UNUSED_PAD src0_sel:DWORD src1_sel:WORD_1
v_mfma_f32_16x16x32_bf16 v[144:147],v[72:75],v[48:51],v[144:147]
ds_read_u16_d16_hi v139,v139
v_mfma_f32_16x16x32_bf16 v[152:155],v[72:75],v[52:55],v[152:155]
ds_read_b64_tr_b16 v[102:103],v6 offset:1608
ds_read_b64_tr_b16 v[104:105],v6 offset:128
v_mfma_f32_16x16x32_bf16 v[156:159],v[76:79],v[52:55],v[156:159]
ds_read_u16 v140,v140
v_mfma_f32_16x16x32_bf16 v[148:151],v[76:79],v[48:51],v[148:151]
s_waitcnt lgkmcnt(15)
ds_read_u16_d16_hi v142,v142
v_lshl_add_u64 v[128:129],v[128:129],0,v[130:131]
v_mfma_f32_16x16x32_bf16 v[144:147],v[80:83],v[56:59],v[144:147]
ds_read_b64_tr_b16 v[106:107],v6 offset:1664
ds_read_b64_tr_b16 v[108:109],v6 offset:136
v_mfma_f32_16x16x32_bf16 v[152:155],v[80:83],v[60:63],v[152:155]
s_waitcnt lgkmcnt(15)
ds_read_u16 v141,v141
v_lshl_add_u64 v[130:131],v[132:133],0,v[134:135]
v_mfma_f32_16x16x32_bf16 v[156:159],v[84:87],v[60:63],v[156:159]
ds_read_u16_d16_hi v143,v143
v_mfma_f32_16x16x32_bf16 v[148:151],v[84:87],v[56:59],v[148:151]
ds_read_b64_tr_b16 v[110:111],v6 offset:1672
ds_read_b64_tr_b16 v[64:65],v4 offset:0
v_lshlrev_b32_sdwa v144,1,v144 dst_sel:DWORD dst_unused:UNUSED_PAD src0_sel:DWORD src1_sel:WORD_1
v_lshlrev_b32_sdwa v146,1,v146 dst_sel:DWORD dst_unused:UNUSED_PAD src0_sel:DWORD src1_sel:WORD_1
v_mfma_f32_16x16x32_bf16 v[160:163],v[88:91],v[128:131],v[160:163]
s_waitcnt lgkmcnt(15)
v_lshlrev_b32_sdwa v145,1,v145 dst_sel:DWORD dst_unused:UNUSED_PAD src0_sel:DWORD src1_sel:WORD_1
v_mfma_f32_16x16x32_bf16 v[168:171],v[92:95],v[128:131],v[168:171]
s_waitcnt lgkmcnt(13)
ds_read_u16 v144,v144
v_lshlrev_b32_sdwa v147,1,v147 dst_sel:DWORD dst_unused:UNUSED_PAD src0_sel:DWORD src1_sel:WORD_1
v_mfma_f32_16x16x32_bf16 v[176:179],v[96:99],v[128:131],v[176:179]
s_waitcnt lgkmcnt(10)
ds_read_b64_tr_b16 v[66:67],v4 offset:2048
ds_read_b128 v[112:115],v7 offset:1280
v_mfma_f32_16x16x32_bf16 v[184:187],v[100:103],v[128:131],v[184:187]
s_waitcnt lgkmcnt(8)
ds_read_b64_tr_b16 v[68:69],v5 offset:0
ds_read_u16_d16_hi v146,v146
v_lshlrev_b32_sdwa v148,1,v148 dst_sel:DWORD dst_unused:UNUSED_PAD src0_sel:DWORD src1_sel:WORD_1
v_lshl_add_u64 v[136:137],v[136:137],0,v[138:139]
v_lshlrev_b32_sdwa v150,1,v150 dst_sel:DWORD dst_unused:UNUSED_PAD src0_sel:DWORD src1_sel:WORD_1
v_mfma_f32_16x16x32_bf16 v[192:195],v[104:107],v[128:131],v[192:195]
s_waitcnt lgkmcnt(6)
ds_read_u16 v145,v145
ds_read_u16_d16_hi v147,v147
v_lshl_add_u64 v[138:139],v[140:141],0,v[142:143]
v_mfma_f32_16x16x32_bf16 v[200:203],v[108:111],v[128:131],v[200:203]
ds_read_b64_tr_b16 v[70:71],v5 offset:2048
ds_read_b128 v[116:119],v7 offset:1312
v_mfma_f32_16x16x32_bf16 v[164:167],v[88:91],v[136:139],v[164:167]
ds_read_u16 v148,v148
v_lshlrev_b32_sdwa v149,1,v149 dst_sel:DWORD dst_unused:UNUSED_PAD src0_sel:DWORD src1_sel:WORD_1
v_lshlrev_b32_sdwa v151,1,v151 dst_sel:DWORD dst_unused:UNUSED_PAD src0_sel:DWORD src1_sel:WORD_1
v_mfma_f32_16x16x32_bf16 v[172:175],v[92:95],v[136:139],v[172:175]
ds_read_b64_tr_b16 v[72:73],v4 offset:4096
v_lshlrev_b32_sdwa v152,1,v152 dst_sel:DWORD dst_unused:UNUSED_PAD src0_sel:DWORD src1_sel:WORD_1
v_mfma_f32_16x16x32_bf16 v[180:183],v[96:99],v[136:139],v[180:183]
ds_read_b64_tr_b16 v[74:75],v4 offset:6144
ds_read_b64_tr_b16 v[76:77],v5 offset:4096
v_lshlrev_b32_sdwa v154,1,v154 dst_sel:DWORD dst_unused:UNUSED_PAD src0_sel:DWORD src1_sel:WORD_1
v_mfma_f32_16x16x32_bf16 v[188:191],v[100:103],v[136:139],v[188:191]
ds_read_u16_d16_hi v150,v150
v_lshlrev_b32_sdwa v153,1,v153 dst_sel:DWORD dst_unused:UNUSED_PAD src0_sel:DWORD src1_sel:WORD_1
v_mfma_f32_16x16x32_bf16 v[196:199],v[104:107],v[136:139],v[196:199]
ds_read_u16 v149,v149
v_lshlrev_b32_sdwa v155,1,v155 dst_sel:DWORD dst_unused:UNUSED_PAD src0_sel:DWORD src1_sel:WORD_1
v_mfma_f32_16x16x32_bf16 v[204:207],v[108:111],v[136:139],v[204:207]
s_waitcnt lgkmcnt(12)
ds_read_b64_tr_b16 v[78:79],v5 offset:6144
ds_read_b64_tr_b16 v[80:81],v4 offset:8192
v_lshlrev_b32_sdwa v156,1,v156 dst_sel:DWORD dst_unused:UNUSED_PAD src0_sel:DWORD src1_sel:WORD_1
v_mfma_f32_16x16x32_bf16 v[128:131],v[64:67],v[16:19],v[112:115]
ds_read_u16_d16_hi v151,v151
v_lshlrev_b32_sdwa v158,1,v158 dst_sel:DWORD dst_unused:UNUSED_PAD src0_sel:DWORD src1_sel:WORD_1
v_mfma_f32_16x16x32_bf16 v[136:139],v[64:67],v[20:23],v[112:115]
s_waitcnt lgkmcnt(9)
ds_read_u16 v152,v152
ds_read_b64_tr_b16 v[82:83],v4 offset:10240
v_lshlrev_b32_sdwa v157,1,v157 dst_sel:DWORD dst_unused:UNUSED_PAD src0_sel:DWORD src1_sel:WORD_1
v_mfma_f32_16x16x32_bf16 v[140:143],v[68:71],v[20:23],v[116:119]
ds_read_b64_tr_b16 v[84:85],v5 offset:8192
ds_read_b64_tr_b16 v[86:87],v5 offset:10240
v_lshlrev_b32_sdwa v159,1,v159 dst_sel:DWORD dst_unused:UNUSED_PAD src0_sel:DWORD src1_sel:WORD_1
v_mfma_f32_16x16x32_bf16 v[132:135],v[68:71],v[16:19],v[116:119]
s_waitcnt lgkmcnt(10)
ds_read_u16_d16_hi v154,v154
v_mfma_f32_16x16x32_bf16 v[128:131],v[72:75],v[24:27],v[128:131]
ds_read_u16 v153,v153
v_mfma_f32_16x16x32_bf16 v[136:139],v[72:75],v[28:31],v[136:139]
s_waitcnt lgkmcnt(8)
v_mfma_f32_16x16x32_bf16 v[140:143],v[76:79],v[28:31],v[140:143]
ds_read_u16_d16_hi v155,v155
v_lshl_add_u64 v[144:145],v[144:145],0,v[146:147]
v_mfma_f32_16x16x32_bf16 v[132:135],v[76:79],v[24:27],v[132:135]
s_waitcnt lgkmcnt(5)
ds_read_u16 v156,v156
v_mfma_f32_16x16x32_bf16 v[128:131],v[80:83],v[32:35],v[128:131]
v_lshl_add_u64 v[146:147],v[148:149],0,v[150:151]
v_mfma_f32_16x16x32_bf16 v[136:139],v[80:83],v[36:39],v[136:139]
s_waitcnt lgkmcnt(4)
ds_read_u16_d16_hi v158,v158
v_mfma_f32_16x16x32_bf16 v[140:143],v[84:87],v[36:39],v[140:143]
ds_read_u16 v157,v157
v_mfma_f32_16x16x32_bf16 v[132:135],v[84:87],v[32:35],v[132:135]
s_nop 0
v_lshlrev_b32_sdwa v128,1,v128 dst_sel:DWORD dst_unused:UNUSED_PAD src0_sel:DWORD src1_sel:WORD_1
v_lshlrev_b32_sdwa v130,1,v130 dst_sel:DWORD dst_unused:UNUSED_PAD src0_sel:DWORD src1_sel:WORD_1
v_mfma_f32_16x16x32_bf16 v[208:211],v[88:91],v[144:147],v[208:211]
ds_read_u16_d16_hi v159,v159
v_lshlrev_b32_sdwa v129,1,v129 dst_sel:DWORD dst_unused:UNUSED_PAD src0_sel:DWORD src1_sel:WORD_1
v_mfma_f32_16x16x32_bf16 v[216:219],v[92:95],v[144:147],v[216:219]
ds_read_u16 v128,v128
v_lshlrev_b32_sdwa v131,1,v131 dst_sel:DWORD dst_unused:UNUSED_PAD src0_sel:DWORD src1_sel:WORD_1
v_mfma_f32_16x16x32_bf16 v[224:227],v[96:99],v[144:147],v[224:227]
v_lshlrev_b32_sdwa v132,1,v132 dst_sel:DWORD dst_unused:UNUSED_PAD src0_sel:DWORD src1_sel:WORD_1
v_mfma_f32_16x16x32_bf16 v[232:235],v[100:103],v[144:147],v[232:235]
s_waitcnt lgkmcnt(5)
ds_read_u16_d16_hi v130,v130
v_lshl_add_u64 v[152:153],v[152:153],0,v[154:155]
v_lshlrev_b32_sdwa v134,1,v134 dst_sel:DWORD dst_unused:UNUSED_PAD src0_sel:DWORD src1_sel:WORD_1
v_mfma_f32_16x16x32_bf16 v[240:243],v[104:107],v[144:147],v[240:243]
s_waitcnt lgkmcnt(2)
v_lshl_add_u64 v[154:155],v[156:157],0,v[158:159]
v_mfma_f32_16x16x32_bf16 v[248:251],v[108:111],v[144:147],v[248:251]
ds_read_u16 v129,v129
v_lshlrev_b32_sdwa v133,1,v133 dst_sel:DWORD dst_unused:UNUSED_PAD src0_sel:DWORD src1_sel:WORD_1
v_mfma_f32_16x16x32_bf16 v[212:215],v[88:91],v[152:155],v[212:215]
ds_read_u16_d16_hi v131,v131
v_lshlrev_b32_sdwa v135,1,v135 dst_sel:DWORD dst_unused:UNUSED_PAD src0_sel:DWORD src1_sel:WORD_1
v_mfma_f32_16x16x32_bf16 v[220:223],v[92:95],v[152:155],v[220:223]
ds_read_b64_tr_b16 v[88:89],v6 offset:6160
ds_read_b64_tr_b16 v[90:91],v6 offset:7696
v_lshlrev_b32_sdwa v136,1,v136 dst_sel:DWORD dst_unused:UNUSED_PAD src0_sel:DWORD src1_sel:WORD_1
v_mfma_f32_16x16x32_bf16 v[228:231],v[96:99],v[152:155],v[228:231]
ds_read_u16 v132,v132
v_lshlrev_b32_sdwa v138,1,v138 dst_sel:DWORD dst_unused:UNUSED_PAD src0_sel:DWORD src1_sel:WORD_1
v_mfma_f32_16x16x32_bf16 v[236:239],v[100:103],v[152:155],v[236:239]
ds_read_u16_d16_hi v134,v134
v_lshlrev_b32_sdwa v137,1,v137 dst_sel:DWORD dst_unused:UNUSED_PAD src0_sel:DWORD src1_sel:WORD_1
v_mfma_f32_16x16x32_bf16 v[244:247],v[104:107],v[152:155],v[244:247]
ds_read_u16 v133,v133
v_lshlrev_b32_sdwa v139,1,v139 dst_sel:DWORD dst_unused:UNUSED_PAD src0_sel:DWORD src1_sel:WORD_1
v_mfma_f32_16x16x32_bf16 v[252:255],v[108:111],v[152:155],v[252:255]
ds_read_b64_tr_b16 v[92:93],v6 offset:6168
ds_read_b64_tr_b16 v[94:95],v6 offset:7704
v_lshlrev_b32_sdwa v140,1,v140 dst_sel:DWORD dst_unused:UNUSED_PAD src0_sel:DWORD src1_sel:WORD_1
v_mfma_f32_16x16x32_bf16 v[144:147],v[64:67],v[40:43],v[112:115]
ds_read_u16_d16_hi v135,v135
v_lshlrev_b32_sdwa v142,1,v142 dst_sel:DWORD dst_unused:UNUSED_PAD src0_sel:DWORD src1_sel:WORD_1
v_mfma_f32_16x16x32_bf16 v[152:155],v[64:67],v[44:47],v[112:115]
ds_read_u16 v136,v136
ds_read_b64_tr_b16 v[96:97],v6 offset:6224
v_lshlrev_b32_sdwa v141,1,v141 dst_sel:DWORD dst_unused:UNUSED_PAD src0_sel:DWORD src1_sel:WORD_1
v_mfma_f32_16x16x32_bf16 v[156:159],v[68:71],v[44:47],v[116:119]
ds_read_b64_tr_b16 v[98:99],v6 offset:7760
ds_read_b64_tr_b16 v[100:101],v6 offset:6232
v_mfma_f32_16x16x32_bf16 v[148:151],v[68:71],v[40:43],v[116:119]
ds_read_u16_d16_hi v138,v138
v_lshlrev_b32_sdwa v143,1,v143 dst_sel:DWORD dst_unused:UNUSED_PAD src0_sel:DWORD src1_sel:WORD_1
v_mfma_f32_16x16x32_bf16 v[144:147],v[72:75],v[48:51],v[144:147]
ds_read_u16 v137,v137
v_mfma_f32_16x16x32_bf16 v[152:155],v[72:75],v[52:55],v[152:155]
ds_read_b64_tr_b16 v[102:103],v6 offset:7768
ds_read_b64_tr_b16 v[104:105],v6 offset:6288
v_mfma_f32_16x16x32_bf16 v[156:159],v[76:79],v[52:55],v[156:159]
ds_read_u16_d16_hi v139,v139
v_mfma_f32_16x16x32_bf16 v[148:151],v[76:79],v[48:51],v[148:151]
s_waitcnt lgkmcnt(15)
ds_read_u16 v140,v140
v_lshl_add_u64 v[128:129],v[128:129],0,v[130:131]
v_mfma_f32_16x16x32_bf16 v[144:147],v[80:83],v[56:59],v[144:147]
ds_read_b64_tr_b16 v[106:107],v6 offset:7824
ds_read_b64_tr_b16 v[108:109],v6 offset:6296
v_mfma_f32_16x16x32_bf16 v[152:155],v[80:83],v[60:63],v[152:155]
s_waitcnt lgkmcnt(12)
ds_read_u16_d16_hi v142,v142
v_lshl_add_u64 v[130:131],v[132:133],0,v[134:135]
v_mfma_f32_16x16x32_bf16 v[156:159],v[84:87],v[60:63],v[156:159]
ds_read_u16 v141,v141
ds_read_u16_d16_hi v143,v143
v_mfma_f32_16x16x32_bf16 v[148:151],v[84:87],v[56:59],v[148:151]
ds_read_b64_tr_b16 v[110:111],v6 offset:7832
ds_read_b64_tr_b16 v[64:65],v4 offset:512
v_lshlrev_b32_sdwa v144,1,v144 dst_sel:DWORD dst_unused:UNUSED_PAD src0_sel:DWORD src1_sel:WORD_1
v_lshlrev_b32_sdwa v146,1,v146 dst_sel:DWORD dst_unused:UNUSED_PAD src0_sel:DWORD src1_sel:WORD_1
v_mfma_f32_16x16x32_bf16 v[160:163],v[88:91],v[128:131],v[160:163]
v_lshlrev_b32_sdwa v145,1,v145 dst_sel:DWORD dst_unused:UNUSED_PAD src0_sel:DWORD src1_sel:WORD_1
v_mfma_f32_16x16x32_bf16 v[168:171],v[92:95],v[128:131],v[168:171]
s_waitcnt lgkmcnt(14)
ds_read_u16 v144,v144
v_lshlrev_b32_sdwa v147,1,v147 dst_sel:DWORD dst_unused:UNUSED_PAD src0_sel:DWORD src1_sel:WORD_1
v_mfma_f32_16x16x32_bf16 v[176:179],v[96:99],v[128:131],v[176:179]
s_waitcnt lgkmcnt(11)
ds_read_b64_tr_b16 v[66:67],v4 offset:2560
ds_read_b128 v[112:115],v7 offset:1408
v_mfma_f32_16x16x32_bf16 v[184:187],v[100:103],v[128:131],v[184:187]
s_waitcnt lgkmcnt(9)
ds_read_b64_tr_b16 v[68:69],v5 offset:512
ds_read_u16_d16_hi v146,v146
v_lshlrev_b32_sdwa v148,1,v148 dst_sel:DWORD dst_unused:UNUSED_PAD src0_sel:DWORD src1_sel:WORD_1
v_lshl_add_u64 v[136:137],v[136:137],0,v[138:139]
v_lshlrev_b32_sdwa v150,1,v150 dst_sel:DWORD dst_unused:UNUSED_PAD src0_sel:DWORD src1_sel:WORD_1
v_mfma_f32_16x16x32_bf16 v[192:195],v[104:107],v[128:131],v[192:195]
s_waitcnt lgkmcnt(6)
ds_read_u16 v145,v145
ds_read_u16_d16_hi v147,v147
v_lshl_add_u64 v[138:139],v[140:141],0,v[142:143]
v_mfma_f32_16x16x32_bf16 v[200:203],v[108:111],v[128:131],v[200:203]
ds_read_b64_tr_b16 v[70:71],v5 offset:2560
ds_read_b128 v[116:119],v7 offset:1440
v_mfma_f32_16x16x32_bf16 v[164:167],v[88:91],v[136:139],v[164:167]
ds_read_u16 v148,v148
v_lshlrev_b32_sdwa v149,1,v149 dst_sel:DWORD dst_unused:UNUSED_PAD src0_sel:DWORD src1_sel:WORD_1
v_lshlrev_b32_sdwa v151,1,v151 dst_sel:DWORD dst_unused:UNUSED_PAD src0_sel:DWORD src1_sel:WORD_1
v_mfma_f32_16x16x32_bf16 v[172:175],v[92:95],v[136:139],v[172:175]
ds_read_b64_tr_b16 v[72:73],v4 offset:4608
v_lshlrev_b32_sdwa v152,1,v152 dst_sel:DWORD dst_unused:UNUSED_PAD src0_sel:DWORD src1_sel:WORD_1
v_mfma_f32_16x16x32_bf16 v[180:183],v[96:99],v[136:139],v[180:183]
ds_read_b64_tr_b16 v[74:75],v4 offset:6656
ds_read_b64_tr_b16 v[76:77],v5 offset:4608
v_lshlrev_b32_sdwa v154,1,v154 dst_sel:DWORD dst_unused:UNUSED_PAD src0_sel:DWORD src1_sel:WORD_1
v_mfma_f32_16x16x32_bf16 v[188:191],v[100:103],v[136:139],v[188:191]
ds_read_u16_d16_hi v150,v150
v_lshlrev_b32_sdwa v153,1,v153 dst_sel:DWORD dst_unused:UNUSED_PAD src0_sel:DWORD src1_sel:WORD_1
v_mfma_f32_16x16x32_bf16 v[196:199],v[104:107],v[136:139],v[196:199]
ds_read_u16 v149,v149
v_lshlrev_b32_sdwa v155,1,v155 dst_sel:DWORD dst_unused:UNUSED_PAD src0_sel:DWORD src1_sel:WORD_1
v_mfma_f32_16x16x32_bf16 v[204:207],v[108:111],v[136:139],v[204:207]
s_waitcnt lgkmcnt(12)
ds_read_b64_tr_b16 v[78:79],v5 offset:6656
ds_read_b64_tr_b16 v[80:81],v4 offset:8704
v_lshlrev_b32_sdwa v156,1,v156 dst_sel:DWORD dst_unused:UNUSED_PAD src0_sel:DWORD src1_sel:WORD_1
v_mfma_f32_16x16x32_bf16 v[128:131],v[64:67],v[16:19],v[112:115]
ds_read_u16_d16_hi v151,v151
v_lshlrev_b32_sdwa v158,1,v158 dst_sel:DWORD dst_unused:UNUSED_PAD src0_sel:DWORD src1_sel:WORD_1
v_mfma_f32_16x16x32_bf16 v[136:139],v[64:67],v[20:23],v[112:115]
s_waitcnt lgkmcnt(9)
ds_read_u16 v152,v152
ds_read_b64_tr_b16 v[82:83],v4 offset:10752
v_lshlrev_b32_sdwa v157,1,v157 dst_sel:DWORD dst_unused:UNUSED_PAD src0_sel:DWORD src1_sel:WORD_1
v_mfma_f32_16x16x32_bf16 v[140:143],v[68:71],v[20:23],v[116:119]
ds_read_b64_tr_b16 v[84:85],v5 offset:8704
ds_read_b64_tr_b16 v[86:87],v5 offset:10752
v_lshlrev_b32_sdwa v159,1,v159 dst_sel:DWORD dst_unused:UNUSED_PAD src0_sel:DWORD src1_sel:WORD_1
v_mfma_f32_16x16x32_bf16 v[132:135],v[68:71],v[16:19],v[116:119]
s_waitcnt lgkmcnt(10)
ds_read_u16_d16_hi v154,v154
v_mfma_f32_16x16x32_bf16 v[128:131],v[72:75],v[24:27],v[128:131]
ds_read_u16 v153,v153
v_mfma_f32_16x16x32_bf16 v[136:139],v[72:75],v[28:31],v[136:139]
s_waitcnt lgkmcnt(8)
v_mfma_f32_16x16x32_bf16 v[140:143],v[76:79],v[28:31],v[140:143]
ds_read_u16_d16_hi v155,v155
v_lshl_add_u64 v[144:145],v[144:145],0,v[146:147]
v_mfma_f32_16x16x32_bf16 v[132:135],v[76:79],v[24:27],v[132:135]
s_waitcnt lgkmcnt(5)
ds_read_u16 v156,v156
v_mfma_f32_16x16x32_bf16 v[128:131],v[80:83],v[32:35],v[128:131]
v_lshl_add_u64 v[146:147],v[148:149],0,v[150:151]
v_mfma_f32_16x16x32_bf16 v[136:139],v[80:83],v[36:39],v[136:139]
s_waitcnt lgkmcnt(4)
ds_read_u16_d16_hi v158,v158
v_mfma_f32_16x16x32_bf16 v[140:143],v[84:87],v[36:39],v[140:143]
ds_read_u16 v157,v157
v_mfma_f32_16x16x32_bf16 v[132:135],v[84:87],v[32:35],v[132:135]
s_nop 0
v_lshlrev_b32_sdwa v128,1,v128 dst_sel:DWORD dst_unused:UNUSED_PAD src0_sel:DWORD src1_sel:WORD_1
v_lshlrev_b32_sdwa v130,1,v130 dst_sel:DWORD dst_unused:UNUSED_PAD src0_sel:DWORD src1_sel:WORD_1
v_mfma_f32_16x16x32_bf16 v[208:211],v[88:91],v[144:147],v[208:211]
s_waitcnt vmcnt(0)
s_barrier
s_setprio 3
ds_read_u16_d16_hi v159,v159
v_fma_mixlo_f16 v120,v140,s99,0
v_lshlrev_b32_sdwa v129,1,v129 dst_sel:DWORD dst_unused:UNUSED_PAD src0_sel:DWORD src1_sel:WORD_1
v_lshlrev_b32_sdwa v131,1,v131 dst_sel:DWORD dst_unused:UNUSED_PAD src0_sel:DWORD src1_sel:WORD_1
v_mfma_f32_16x16x32_bf16 v[216:219],v[92:95],v[144:147],v[216:219]
s_cbranch_vccz RA50_%=
s_add_u32 m0,s58,0
s_mul_i32 s69,s59,1
s_add_u32 s69,s69,s97
buffer_load_dwordx4 v2,s[44:47],s69 offen lds
s_add_u32 m0,m0,1024
s_add_u32 s69,s69,s53
buffer_load_dwordx4 v3,s[44:47],s69 offen lds
s_add_u32 m0,m0,1024
s_add_u32 s69,s69,s53
buffer_load_dwordx4 v2,s[44:47],s69 offen lds
s_add_u32 m0,m0,s50
s_add_u32 s69,s69,s53
buffer_load_dwordx4 v3,s[44:47],s69 offen lds
RA50_%=:
v_fma_mixlo_f16 v122,v141,s99,0
v_lshlrev_b32_sdwa v132,1,v132 dst_sel:DWORD dst_unused:UNUSED_PAD src0_sel:DWORD src1_sel:WORD_1
v_mfma_f32_16x16x32_bf16 v[224:227],v[96:99],v[144:147],v[224:227]
v_lshlrev_b32_sdwa v134,1,v134 dst_sel:DWORD dst_unused:UNUSED_PAD src0_sel:DWORD src1_sel:WORD_1
v_mfma_f32_16x16x32_bf16 v[232:235],v[100:103],v[144:147],v[232:235]
s_waitcnt lgkmcnt(4)
ds_read_u16 v128,v128
v_lshl_add_u64 v[152:153],v[152:153],0,v[154:155]
v_mfma_f32_16x16x32_bf16 v[240:243],v[104:107],v[144:147],v[240:243]
s_waitcnt lgkmcnt(1)
ds_read_u16_d16_hi v130,v130
v_lshl_add_u64 v[154:155],v[156:157],0,v[158:159]
v_mfma_f32_16x16x32_bf16 v[248:251],v[108:111],v[144:147],v[248:251]
v_fma_mixhi_f16 v120,v142,s99,0
v_fma_mixhi_f16 v122,v143,s99,0
v_mfma_f32_16x16x32_bf16 v[212:215],v[88:91],v[152:155],v[212:215]
s_cbranch_vccz RA51_%=
s_add_u32 m0,m0,1024
s_add_u32 s69,s69,s53
buffer_load_dwordx4 v2,s[44:47],s69 offen lds
s_add_u32 m0,m0,1024
s_add_u32 s69,s69,s53
buffer_load_dwordx4 v3,s[44:47],s69 offen lds
s_add_u32 m0,m0,s50
s_add_u32 s69,s69,s53
buffer_load_dwordx4 v2,s[44:47],s69 offen lds
s_add_u32 m0,m0,1024
s_add_u32 s69,s69,s53
buffer_load_dwordx4 v3,s[44:47],s69 offen lds
RA51_%=:
ds_read_u16 v129,v129
v_lshlrev_b32_sdwa v133,1,v133 dst_sel:DWORD dst_unused:UNUSED_PAD src0_sel:DWORD src1_sel:WORD_1
v_lshlrev_b32_sdwa v135,1,v135 dst_sel:DWORD dst_unused:UNUSED_PAD src0_sel:DWORD src1_sel:WORD_1
v_mfma_f32_16x16x32_bf16 v[220:223],v[92:95],v[152:155],v[220:223]
v_pk_fma_f16 v121,v120,v120,s54
v_pk_fma_f16 v123,v122,v122,s54
v_lshlrev_b32_sdwa v136,1,v136 dst_sel:DWORD dst_unused:UNUSED_PAD src0_sel:DWORD src1_sel:WORD_1
v_mfma_f32_16x16x32_bf16 v[228:231],v[96:99],v[152:155],v[228:231]
ds_read_u16_d16_hi v131,v131
v_pk_mul_f16 v121,v120,v121
v_pk_mul_f16 v123,v122,v123
v_mfma_f32_16x16x32_bf16 v[236:239],v[100:103],v[152:155],v[236:239]
s_cbranch_vccz RA52_%=
s_add_u32 m0,m0,1024
s_add_u32 s69,s69,s53
buffer_load_dwordx4 v2,s[44:47],s69 offen lds
s_add_u32 m0,m0,s50
s_add_u32 s69,s69,s53
buffer_load_dwordx4 v3,s[44:47],s69 offen lds
s_add_u32 m0,m0,1024
s_add_u32 s69,s69,s53
buffer_load_dwordx4 v2,s[44:47],s69 offen lds
s_add_u32 m0,m0,1024
s_add_u32 s69,s69,s53
buffer_load_dwordx4 v3,s[44:47],s69 offen lds
RA52_%=:
ds_read_b64_tr_b16 v[88:89],v6 offset:12320
ds_read_b64_tr_b16 v[90:91],v6 offset:13856
v_mfma_f32_16x16x32_bf16 v[244:247],v[104:107],v[152:155],v[244:247]
ds_read_u16 v132,v132
v_exp_f16 v120,v121
v_mfma_f32_16x16x32_bf16 v[252:255],v[108:111],v[152:155],v[252:255]
ds_read_b64_tr_b16 v[92:93],v6 offset:12328
v_lshlrev_b32_sdwa v138,1,v138 dst_sel:DWORD dst_unused:UNUSED_PAD src0_sel:DWORD src1_sel:WORD_1
v_exp_f16 v122,v123
v_mfma_f32_16x16x32_bf16 v[144:147],v[64:67],v[40:43],v[112:115]
ds_read_u16_d16_hi v134,v134
v_lshlrev_b32_sdwa v137,1,v137 dst_sel:DWORD dst_unused:UNUSED_PAD src0_sel:DWORD src1_sel:WORD_1
v_lshlrev_b32_sdwa v139,1,v139 dst_sel:DWORD dst_unused:UNUSED_PAD src0_sel:DWORD src1_sel:WORD_1
v_mfma_f32_16x16x32_bf16 v[152:155],v[64:67],v[44:47],v[112:115]
ds_read_b64_tr_b16 v[94:95],v6 offset:13864
v_exp_f16_sdwa v120,v121 dst_sel:WORD_1 dst_unused:UNUSED_PRESERVE src0_sel:WORD_1
v_mfma_f32_16x16x32_bf16 v[156:159],v[68:71],v[44:47],v[116:119]
ds_read_u16 v133,v133
v_exp_f16_sdwa v122,v123 dst_sel:WORD_1 dst_unused:UNUSED_PRESERVE src0_sel:WORD_1
v_mfma_f32_16x16x32_bf16 v[148:151],v[68:71],v[40:43],v[116:119]
ds_read_u16_d16_hi v135,v135
ds_read_b64_tr_b16 v[96:97],v6 offset:12384
v_pk_fma_f16 v120,v120,s55,s55
v_mfma_f32_16x16x32_bf16 v[144:147],v[72:75],v[48:51],v[144:147]
v_rcp_f16 v121,v120
v_pk_fma_f16 v122,v122,s55,s55
v_mfma_f32_16x16x32_bf16 v[152:155],v[72:75],v[52:55],v[152:155]
ds_read_b64_tr_b16 v[98:99],v6 offset:13920
v_rcp_f16 v123,v122
v_mfma_f32_16x16x32_bf16 v[156:159],v[76:79],v[52:55],v[156:159]
ds_read_u16 v136,v136
v_rcp_f16_sdwa v121,v120 dst_sel:WORD_1 dst_unused:UNUSED_PRESERVE src0_sel:WORD_1
v_mfma_f32_16x16x32_bf16 v[148:151],v[76:79],v[48:51],v[148:151]
ds_read_b64_tr_b16 v[100:101],v6 offset:12392
ds_read_b64_tr_b16 v[102:103],v6 offset:13928
v_rcp_f16_sdwa v123,v122 dst_sel:WORD_1 dst_unused:UNUSED_PRESERVE src0_sel:WORD_1
v_mfma_f32_16x16x32_bf16 v[144:147],v[80:83],v[56:59],v[144:147]
s_waitcnt lgkmcnt(13)
ds_read_u16_d16_hi v138,v138
v_lshl_add_u64 v[128:129],v[128:129],0,v[130:131]
v_fma_mix_f32 v140,v140,v121,0 op_sel_hi:[0,1,0]
v_fma_mix_f32 v142,v142,v121,0 op_sel:[0,1,0] op_sel_hi:[0,1,0]
v_mfma_f32_16x16x32_bf16 v[152:155],v[80:83],v[60:63],v[152:155]
ds_read_b64_tr_b16 v[104:105],v6 offset:12448
ds_read_b64_tr_b16 v[106:107],v6 offset:13984
v_fma_mix_f32 v141,v141,v123,0 op_sel_hi:[0,1,0]
v_mfma_f32_16x16x32_bf16 v[156:159],v[84:87],v[60:63],v[156:159]
s_waitcnt lgkmcnt(8)
ds_read_u16 v137,v137
v_lshl_add_u64 v[130:131],v[132:133],0,v[134:135]
v_fma_mix_f32 v143,v143,v123,0 op_sel:[0,1,0] op_sel_hi:[0,1,0]
v_mfma_f32_16x16x32_bf16 v[148:151],v[84:87],v[56:59],v[148:151]
ds_read_u16_d16_hi v139,v139
v_lshlrev_b32_sdwa v144,1,v144 dst_sel:DWORD dst_unused:UNUSED_PAD src0_sel:DWORD src1_sel:WORD_1
v_lshlrev_b32_sdwa v146,1,v146 dst_sel:DWORD dst_unused:UNUSED_PAD src0_sel:DWORD src1_sel:WORD_1
v_mfma_f32_16x16x32_bf16 v[160:163],v[88:91],v[128:131],v[160:163]
ds_read_b64_tr_b16 v[108:109],v6 offset:12456
ds_read_b64_tr_b16 v[110:111],v6 offset:13992
v_lshlrev_b32_sdwa v145,1,v145 dst_sel:DWORD dst_unused:UNUSED_PAD src0_sel:DWORD src1_sel:WORD_1
v_lshlrev_b32_sdwa v147,1,v147 dst_sel:DWORD dst_unused:UNUSED_PAD src0_sel:DWORD src1_sel:WORD_1
v_mfma_f32_16x16x32_bf16 v[168:171],v[92:95],v[128:131],v[168:171]
s_waitcnt lgkmcnt(10)
ds_read_b64_tr_b16 v[64:65],v4 offset:12320
ds_read_b64_tr_b16 v[66:67],v4 offset:14368
v_fma_mixlo_f16 v120,v156,s99,0
v_mfma_f32_16x16x32_bf16 v[176:179],v[96:99],v[128:131],v[176:179]
s_waitcnt lgkmcnt(9)
ds_read_b128 v[112:115],v7 offset:0
ds_read_u16 v144,v144
v_fma_mixlo_f16 v122,v157,s99,0
v_lshlrev_b32_sdwa v148,1,v148 dst_sel:DWORD dst_unused:UNUSED_PAD src0_sel:DWORD src1_sel:WORD_1
v_lshlrev_b32_sdwa v150,1,v150 dst_sel:DWORD dst_unused:UNUSED_PAD src0_sel:DWORD src1_sel:WORD_1
v_mfma_f32_16x16x32_bf16 v[184:187],v[100:103],v[128:131],v[184:187]
s_waitcnt lgkmcnt(6)
ds_read_u16_d16_hi v146,v146
v_lshl_add_u64 v[136:137],v[136:137],0,v[138:139]
v_mfma_f32_16x16x32_bf16 v[192:195],v[104:107],v[128:131],v[192:195]
s_waitcnt lgkmcnt(5)
ds_read_b64_tr_b16 v[68:69],v5 offset:12320
v_fma_mixhi_f16 v120,v158,s99,0
v_cvt_pk_bf16_f32 v138,v140,v142
v_cvt_pk_bf16_f32 v139,v141,v143
v_mfma_f32_16x16x32_bf16 v[200:203],v[108:111],v[128:131],v[200:203]
ds_read_b64_tr_b16 v[70:71],v5 offset:14368
ds_read_b128 v[116:119],v7 offset:32
v_fma_mixhi_f16 v122,v159,s99,0
v_mfma_f32_16x16x32_bf16 v[164:167],v[88:91],v[136:139],v[164:167]
v_lshlrev_b32_sdwa v149,1,v149 dst_sel:DWORD dst_unused:UNUSED_PAD src0_sel:DWORD src1_sel:WORD_1
v_lshlrev_b32_sdwa v151,1,v151 dst_sel:DWORD dst_unused:UNUSED_PAD src0_sel:DWORD src1_sel:WORD_1
v_mfma_f32_16x16x32_bf16 v[172:175],v[92:95],v[136:139],v[172:175]
ds_read_b64_tr_b16 v[72:73],v4 offset:16416
ds_read_b64_tr_b16 v[74:75],v4 offset:18464
v_pk_fma_f16 v123,v122,v122,s54
v_pk_fma_f16 v121,v120,v120,s54
v_mfma_f32_16x16x32_bf16 v[180:183],v[96:99],v[136:139],v[180:183]
ds_read_u16 v145,v145
v_pk_mul_f16 v121,v120,v121
v_pk_mul_f16 v123,v122,v123
v_mfma_f32_16x16x32_bf16 v[188:191],v[100:103],v[136:139],v[188:191]
ds_read_b64_tr_b16 v[76:77],v5 offset:16416
v_lshlrev_b32_sdwa v152,1,v152 dst_sel:DWORD dst_unused:UNUSED_PAD src0_sel:DWORD src1_sel:WORD_1
v_mfma_f32_16x16x32_bf16 v[196:199],v[104:107],v[136:139],v[196:199]
ds_read_u16_d16_hi v147,v147
v_lshlrev_b32_sdwa v154,1,v154 dst_sel:DWORD dst_unused:UNUSED_PAD src0_sel:DWORD src1_sel:WORD_1
v_exp_f16 v120,v121
v_mfma_f32_16x16x32_bf16 v[204:207],v[108:111],v[136:139],v[204:207]
s_waitcnt lgkmcnt(10)
ds_read_b64_tr_b16 v[78:79],v5 offset:18464
v_exp_f16 v122,v123
v_mfma_f32_16x16x32_bf16 v[128:131],v[64:67],v[16:19],v[112:115]
ds_read_u16 v148,v148
ds_read_u16_d16_hi v150,v150
v_lshlrev_b32_sdwa v153,1,v153 dst_sel:DWORD dst_unused:UNUSED_PAD src0_sel:DWORD src1_sel:WORD_1
s_mov_b32 s86,0xbe801d5a
v_exp_f16_sdwa v122,v123 dst_sel:WORD_1 dst_unused:UNUSED_PRESERVE src0_sel:WORD_1
v_mfma_f32_16x16x32_bf16 v[136:139],v[64:67],v[20:23],v[112:115]
s_waitcnt lgkmcnt(8)
v_lshlrev_b32_sdwa v155,1,v155 dst_sel:DWORD dst_unused:UNUSED_PAD src0_sel:DWORD src1_sel:WORD_1
v_exp_f16_sdwa v120,v121 dst_sel:WORD_1 dst_unused:UNUSED_PRESERVE src0_sel:WORD_1
v_mfma_f32_16x16x32_bf16 v[140:143],v[68:71],v[20:23],v[116:119]
ds_read_b64_tr_b16 v[80:81],v4 offset:20512
ds_read_b64_tr_b16 v[82:83],v4 offset:22560
v_pk_fma_f16 v120,v120,s55,s55
v_mfma_f32_16x16x32_bf16 v[132:135],v[68:71],v[16:19],v[116:119]
s_waitcnt lgkmcnt(8)
ds_read_u16 v149,v149
v_pk_fma_f16 v122,v122,s55,s55
v_mfma_f32_16x16x32_bf16 v[128:131],v[72:75],v[24:27],v[128:131]
ds_read_b64_tr_b16 v[84:85],v5 offset:20512
ds_read_u16_d16_hi v151,v151
v_rcp_f16 v121,v120
v_mfma_f32_16x16x32_bf16 v[136:139],v[72:75],v[28:31],v[136:139]
s_waitcnt lgkmcnt(7)
ds_read_b64_tr_b16 v[86:87],v5 offset:22560
v_rcp_f16 v123,v122
v_mfma_f32_16x16x32_bf16 v[140:143],v[76:79],v[28:31],v[140:143]
ds_read_u16 v152,v152
v_rcp_f16_sdwa v121,v120 dst_sel:WORD_1 dst_unused:UNUSED_PRESERVE src0_sel:WORD_1
v_mfma_f32_16x16x32_bf16 v[132:135],v[76:79],v[24:27],v[132:135]
s_waitcnt lgkmcnt(5)
v_rcp_f16_sdwa v123,v122 dst_sel:WORD_1 dst_unused:UNUSED_PRESERVE src0_sel:WORD_1
v_mfma_f32_16x16x32_bf16 v[128:131],v[80:83],v[32:35],v[128:131]
ds_read_u16_d16_hi v154,v154
v_fma_mix_f32 v156,v156,v121,0 op_sel_hi:[0,1,0]
v_mfma_f32_16x16x32_bf16 v[136:139],v[80:83],v[36:39],v[136:139]
s_waitcnt lgkmcnt(2)
v_lshl_add_u64 v[144:145],v[144:145],0,v[146:147]
v_fma_mix_f32 v158,v158,v121,0 op_sel:[0,1,0] op_sel_hi:[0,1,0]
v_lshl_add_u64 v[146:147],v[148:149],0,v[150:151]
v_mfma_f32_16x16x32_bf16 v[140:143],v[84:87],v[36:39],v[140:143]
ds_read_u16 v153,v153
v_fma_mix_f32 v157,v157,v123,0 op_sel_hi:[0,1,0]
v_fma_mix_f32 v159,v159,v123,0 op_sel:[0,1,0] op_sel_hi:[0,1,0]
v_mfma_f32_16x16x32_bf16 v[132:135],v[84:87],v[32:35],v[132:135]
v_lshlrev_b32_sdwa v128,1,v128 dst_sel:DWORD dst_unused:UNUSED_PAD src0_sel:DWORD src1_sel:WORD_1
v_lshlrev_b32_sdwa v130,1,v130 dst_sel:DWORD dst_unused:UNUSED_PAD src0_sel:DWORD src1_sel:WORD_1
v_mfma_f32_16x16x32_bf16 v[208:211],v[88:91],v[144:147],v[208:211]
ds_read_u16_d16_hi v155,v155
v_fma_mixlo_f16 v120,v140,s99,0
v_lshlrev_b32_sdwa v129,1,v129 dst_sel:DWORD dst_unused:UNUSED_PAD src0_sel:DWORD src1_sel:WORD_1
v_lshlrev_b32_sdwa v131,1,v131 dst_sel:DWORD dst_unused:UNUSED_PAD src0_sel:DWORD src1_sel:WORD_1
v_mfma_f32_16x16x32_bf16 v[216:219],v[92:95],v[144:147],v[216:219]
v_fma_mixlo_f16 v122,v141,s99,0
v_lshlrev_b32_sdwa v132,1,v132 dst_sel:DWORD dst_unused:UNUSED_PAD src0_sel:DWORD src1_sel:WORD_1
v_mfma_f32_16x16x32_bf16 v[224:227],v[96:99],v[144:147],v[224:227]
v_lshlrev_b32_sdwa v134,1,v134 dst_sel:DWORD dst_unused:UNUSED_PAD src0_sel:DWORD src1_sel:WORD_1
v_mfma_f32_16x16x32_bf16 v[232:235],v[100:103],v[144:147],v[232:235]
s_waitcnt lgkmcnt(0)
ds_read_u16 v128,v128
v_lshl_add_u64 v[152:153],v[152:153],0,v[154:155]
v_mfma_f32_16x16x32_bf16 v[240:243],v[104:107],v[144:147],v[240:243]
ds_read_u16_d16_hi v130,v130
v_cvt_pk_bf16_f32 v155,v157,v159
v_cvt_pk_bf16_f32 v154,v156,v158
v_mfma_f32_16x16x32_bf16 v[248:251],v[108:111],v[144:147],v[248:251]
v_fma_mixhi_f16 v120,v142,s99,0
v_fma_mixhi_f16 v122,v143,s99,0
v_mfma_f32_16x16x32_bf16 v[212:215],v[88:91],v[152:155],v[212:215]
s_setprio 2
ds_read_u16 v129,v129
v_lshlrev_b32_sdwa v133,1,v133 dst_sel:DWORD dst_unused:UNUSED_PAD src0_sel:DWORD src1_sel:WORD_1
v_lshlrev_b32_sdwa v135,1,v135 dst_sel:DWORD dst_unused:UNUSED_PAD src0_sel:DWORD src1_sel:WORD_1
v_mfma_f32_16x16x32_bf16 v[220:223],v[92:95],v[152:155],v[220:223]
v_pk_fma_f16 v121,v120,v120,s54
v_pk_fma_f16 v123,v122,v122,s54
v_lshlrev_b32_sdwa v136,1,v136 dst_sel:DWORD dst_unused:UNUSED_PAD src0_sel:DWORD src1_sel:WORD_1
v_mfma_f32_16x16x32_bf16 v[228:231],v[96:99],v[152:155],v[228:231]
ds_read_u16_d16_hi v131,v131
v_pk_mul_f16 v121,v120,v121
v_pk_mul_f16 v123,v122,v123
v_mfma_f32_16x16x32_bf16 v[236:239],v[100:103],v[152:155],v[236:239]
ds_read_b64_tr_b16 v[88:89],v6 offset:18480
ds_read_b64_tr_b16 v[90:91],v6 offset:20016
v_mfma_f32_16x16x32_bf16 v[244:247],v[104:107],v[152:155],v[244:247]
ds_read_u16 v132,v132
v_exp_f16 v120,v121
v_mfma_f32_16x16x32_bf16 v[252:255],v[108:111],v[152:155],v[252:255]
ds_read_b64_tr_b16 v[92:93],v6 offset:18488
v_lshlrev_b32_sdwa v138,1,v138 dst_sel:DWORD dst_unused:UNUSED_PAD src0_sel:DWORD src1_sel:WORD_1
v_exp_f16 v122,v123
v_mfma_f32_16x16x32_bf16 v[144:147],v[64:67],v[40:43],v[112:115]
ds_read_u16_d16_hi v134,v134
v_lshlrev_b32_sdwa v137,1,v137 dst_sel:DWORD dst_unused:UNUSED_PAD src0_sel:DWORD src1_sel:WORD_1
v_lshlrev_b32_sdwa v139,1,v139 dst_sel:DWORD dst_unused:UNUSED_PAD src0_sel:DWORD src1_sel:WORD_1
v_mfma_f32_16x16x32_bf16 v[152:155],v[64:67],v[44:47],v[112:115]
ds_read_b64_tr_b16 v[94:95],v6 offset:20024
v_exp_f16_sdwa v120,v121 dst_sel:WORD_1 dst_unused:UNUSED_PRESERVE src0_sel:WORD_1
v_mfma_f32_16x16x32_bf16 v[156:159],v[68:71],v[44:47],v[116:119]
ds_read_u16 v133,v133
v_exp_f16_sdwa v122,v123 dst_sel:WORD_1 dst_unused:UNUSED_PRESERVE src0_sel:WORD_1
v_mfma_f32_16x16x32_bf16 v[148:151],v[68:71],v[40:43],v[116:119]
ds_read_u16_d16_hi v135,v135
ds_read_b64_tr_b16 v[96:97],v6 offset:18544
v_pk_fma_f16 v120,v120,s55,s55
v_mfma_f32_16x16x32_bf16 v[144:147],v[72:75],v[48:51],v[144:147]
v_rcp_f16 v121,v120
v_pk_fma_f16 v122,v122,s55,s55
v_mfma_f32_16x16x32_bf16 v[152:155],v[72:75],v[52:55],v[152:155]
ds_read_b64_tr_b16 v[98:99],v6 offset:20080
v_rcp_f16 v123,v122
v_mfma_f32_16x16x32_bf16 v[156:159],v[76:79],v[52:55],v[156:159]
ds_read_u16 v136,v136
v_rcp_f16_sdwa v121,v120 dst_sel:WORD_1 dst_unused:UNUSED_PRESERVE src0_sel:WORD_1
v_mfma_f32_16x16x32_bf16 v[148:151],v[76:79],v[48:51],v[148:151]
ds_read_b64_tr_b16 v[100:101],v6 offset:18552
ds_read_b64_tr_b16 v[102:103],v6 offset:20088
v_rcp_f16_sdwa v123,v122 dst_sel:WORD_1 dst_unused:UNUSED_PRESERVE src0_sel:WORD_1
v_mfma_f32_16x16x32_bf16 v[144:147],v[80:83],v[56:59],v[144:147]
s_waitcnt lgkmcnt(13)
ds_read_u16_d16_hi v138,v138
v_lshl_add_u64 v[128:129],v[128:129],0,v[130:131]
v_fma_mix_f32 v140,v140,v121,0 op_sel_hi:[0,1,0]
v_fma_mix_f32 v142,v142,v121,0 op_sel:[0,1,0] op_sel_hi:[0,1,0]
v_mfma_f32_16x16x32_bf16 v[152:155],v[80:83],v[60:63],v[152:155]
ds_read_b64_tr_b16 v[104:105],v6 offset:18608
ds_read_b64_tr_b16 v[106:107],v6 offset:20144
v_fma_mix_f32 v141,v141,v123,0 op_sel_hi:[0,1,0]
v_mfma_f32_16x16x32_bf16 v[156:159],v[84:87],v[60:63],v[156:159]
s_waitcnt lgkmcnt(8)
ds_read_u16 v137,v137
v_lshl_add_u64 v[130:131],v[132:133],0,v[134:135]
v_fma_mix_f32 v143,v143,v123,0 op_sel:[0,1,0] op_sel_hi:[0,1,0]
v_mfma_f32_16x16x32_bf16 v[148:151],v[84:87],v[56:59],v[148:151]
ds_read_u16_d16_hi v139,v139
v_lshlrev_b32_sdwa v144,1,v144 dst_sel:DWORD dst_unused:UNUSED_PAD src0_sel:DWORD src1_sel:WORD_1
v_lshlrev_b32_sdwa v146,1,v146 dst_sel:DWORD dst_unused:UNUSED_PAD src0_sel:DWORD src1_sel:WORD_1
v_mfma_f32_16x16x32_bf16 v[160:163],v[88:91],v[128:131],v[160:163]
ds_read_b64_tr_b16 v[108:109],v6 offset:18616
ds_read_b64_tr_b16 v[110:111],v6 offset:20152
v_lshlrev_b32_sdwa v145,1,v145 dst_sel:DWORD dst_unused:UNUSED_PAD src0_sel:DWORD src1_sel:WORD_1
v_lshlrev_b32_sdwa v147,1,v147 dst_sel:DWORD dst_unused:UNUSED_PAD src0_sel:DWORD src1_sel:WORD_1
v_mfma_f32_16x16x32_bf16 v[168:171],v[92:95],v[128:131],v[168:171]
s_waitcnt lgkmcnt(10)
ds_read_b64_tr_b16 v[64:65],v4 offset:12832
ds_read_b64_tr_b16 v[66:67],v4 offset:14880
v_fma_mixlo_f16 v120,v156,s99,0
v_mfma_f32_16x16x32_bf16 v[176:179],v[96:99],v[128:131],v[176:179]
s_waitcnt lgkmcnt(9)
ds_read_b128 v[112:115],v7 offset:128
ds_read_u16 v144,v144
v_fma_mixlo_f16 v122,v157,s99,0
v_lshlrev_b32_sdwa v148,1,v148 dst_sel:DWORD dst_unused:UNUSED_PAD src0_sel:DWORD src1_sel:WORD_1
v_lshlrev_b32_sdwa v150,1,v150 dst_sel:DWORD dst_unused:UNUSED_PAD src0_sel:DWORD src1_sel:WORD_1
v_mfma_f32_16x16x32_bf16 v[184:187],v[100:103],v[128:131],v[184:187]
s_waitcnt lgkmcnt(6)
s_setprio 1
ds_read_u16_d16_hi v146,v146
v_lshl_add_u64 v[136:137],v[136:137],0,v[138:139]
v_mfma_f32_16x16x32_bf16 v[192:195],v[104:107],v[128:131],v[192:195]
s_waitcnt lgkmcnt(5)
ds_read_b64_tr_b16 v[68:69],v5 offset:12832
v_fma_mixhi_f16 v120,v158,s99,0
v_cvt_pk_bf16_f32 v138,v140,v142
v_cvt_pk_bf16_f32 v139,v141,v143
v_mfma_f32_16x16x32_bf16 v[200:203],v[108:111],v[128:131],v[200:203]
ds_read_b64_tr_b16 v[70:71],v5 offset:14880
ds_read_b128 v[116:119],v7 offset:160
v_fma_mixhi_f16 v122,v159,s99,0
v_mfma_f32_16x16x32_bf16 v[164:167],v[88:91],v[136:139],v[164:167]
v_lshlrev_b32_sdwa v149,1,v149 dst_sel:DWORD dst_unused:UNUSED_PAD src0_sel:DWORD src1_sel:WORD_1
v_lshlrev_b32_sdwa v151,1,v151 dst_sel:DWORD dst_unused:UNUSED_PAD src0_sel:DWORD src1_sel:WORD_1
v_mfma_f32_16x16x32_bf16 v[172:175],v[92:95],v[136:139],v[172:175]
ds_read_b64_tr_b16 v[72:73],v4 offset:16928
ds_read_b64_tr_b16 v[74:75],v4 offset:18976
v_pk_fma_f16 v123,v122,v122,s54
v_pk_fma_f16 v121,v120,v120,s54
v_mfma_f32_16x16x32_bf16 v[180:183],v[96:99],v[136:139],v[180:183]
ds_read_u16 v145,v145
v_pk_mul_f16 v121,v120,v121
v_pk_mul_f16 v123,v122,v123
v_mfma_f32_16x16x32_bf16 v[188:191],v[100:103],v[136:139],v[188:191]
ds_read_b64_tr_b16 v[76:77],v5 offset:16928
v_lshlrev_b32_sdwa v152,1,v152 dst_sel:DWORD dst_unused:UNUSED_PAD src0_sel:DWORD src1_sel:WORD_1
v_mfma_f32_16x16x32_bf16 v[196:199],v[104:107],v[136:139],v[196:199]
ds_read_u16_d16_hi v147,v147
v_lshlrev_b32_sdwa v154,1,v154 dst_sel:DWORD dst_unused:UNUSED_PAD src0_sel:DWORD src1_sel:WORD_1
v_exp_f16 v120,v121
v_mfma_f32_16x16x32_bf16 v[204:207],v[108:111],v[136:139],v[204:207]
s_waitcnt lgkmcnt(10)
ds_read_b64_tr_b16 v[78:79],v5 offset:18976
v_exp_f16 v122,v123
v_mfma_f32_16x16x32_bf16 v[128:131],v[64:67],v[16:19],v[112:115]
ds_read_u16 v148,v148
ds_read_u16_d16_hi v150,v150
v_lshlrev_b32_sdwa v153,1,v153 dst_sel:DWORD dst_unused:UNUSED_PAD src0_sel:DWORD src1_sel:WORD_1
v_exp_f16_sdwa v122,v123 dst_sel:WORD_1 dst_unused:UNUSED_PRESERVE src0_sel:WORD_1
v_mfma_f32_16x16x32_bf16 v[136:139],v[64:67],v[20:23],v[112:115]
s_waitcnt lgkmcnt(8)
s_setprio 0
v_lshlrev_b32_sdwa v155,1,v155 dst_sel:DWORD dst_unused:UNUSED_PAD src0_sel:DWORD src1_sel:WORD_1
v_exp_f16_sdwa v120,v121 dst_sel:WORD_1 dst_unused:UNUSED_PRESERVE src0_sel:WORD_1
v_mfma_f32_16x16x32_bf16 v[140:143],v[68:71],v[20:23],v[116:119]
ds_read_b64_tr_b16 v[80:81],v4 offset:21024
ds_read_b64_tr_b16 v[82:83],v4 offset:23072
v_pk_fma_f16 v120,v120,s55,s55
v_mfma_f32_16x16x32_bf16 v[132:135],v[68:71],v[16:19],v[116:119]
s_waitcnt lgkmcnt(8)
ds_read_u16 v149,v149
v_pk_fma_f16 v122,v122,s55,s55
v_mfma_f32_16x16x32_bf16 v[128:131],v[72:75],v[24:27],v[128:131]
ds_read_b64_tr_b16 v[84:85],v5 offset:21024
ds_read_u16_d16_hi v151,v151
v_rcp_f16 v121,v120
v_mfma_f32_16x16x32_bf16 v[136:139],v[72:75],v[28:31],v[136:139]
s_waitcnt lgkmcnt(7)
ds_read_b64_tr_b16 v[86:87],v5 offset:23072
v_rcp_f16 v123,v122
v_mfma_f32_16x16x32_bf16 v[140:143],v[76:79],v[28:31],v[140:143]
ds_read_u16 v152,v152
v_rcp_f16_sdwa v121,v120 dst_sel:WORD_1 dst_unused:UNUSED_PRESERVE src0_sel:WORD_1
v_mfma_f32_16x16x32_bf16 v[132:135],v[76:79],v[24:27],v[132:135]
s_waitcnt lgkmcnt(5)
v_rcp_f16_sdwa v123,v122 dst_sel:WORD_1 dst_unused:UNUSED_PRESERVE src0_sel:WORD_1
v_mfma_f32_16x16x32_bf16 v[128:131],v[80:83],v[32:35],v[128:131]
ds_read_u16_d16_hi v154,v154
v_fma_mix_f32 v156,v156,v121,0 op_sel_hi:[0,1,0]
v_mfma_f32_16x16x32_bf16 v[136:139],v[80:83],v[36:39],v[136:139]
s_waitcnt lgkmcnt(2)
v_lshl_add_u64 v[144:145],v[144:145],0,v[146:147]
v_fma_mix_f32 v158,v158,v121,0 op_sel:[0,1,0] op_sel_hi:[0,1,0]
v_lshl_add_u64 v[146:147],v[148:149],0,v[150:151]
v_mfma_f32_16x16x32_bf16 v[140:143],v[84:87],v[36:39],v[140:143]
ds_read_u16 v153,v153
v_fma_mix_f32 v157,v157,v123,0 op_sel_hi:[0,1,0]
v_fma_mix_f32 v159,v159,v123,0 op_sel:[0,1,0] op_sel_hi:[0,1,0]
v_mfma_f32_16x16x32_bf16 v[132:135],v[84:87],v[32:35],v[132:135]
v_lshlrev_b32_sdwa v128,1,v128 dst_sel:DWORD dst_unused:UNUSED_PAD src0_sel:DWORD src1_sel:WORD_1
v_lshlrev_b32_sdwa v130,1,v130 dst_sel:DWORD dst_unused:UNUSED_PAD src0_sel:DWORD src1_sel:WORD_1
v_mfma_f32_16x16x32_bf16 v[208:211],v[88:91],v[144:147],v[208:211]
s_waitcnt vmcnt(0)
s_barrier
s_setprio 2
v_lshlrev_b32_sdwa v129,1,v129 dst_sel:DWORD dst_unused:UNUSED_PAD src0_sel:DWORD src1_sel:WORD_1
v_lshlrev_b32_sdwa v131,1,v131 dst_sel:DWORD dst_unused:UNUSED_PAD src0_sel:DWORD src1_sel:WORD_1
v_mfma_f32_16x16x32_bf16 v[216:219],v[92:95],v[144:147],v[216:219]
ds_read_u16_d16_hi v155,v155
v_mfma_f32_16x16x32_bf16 v[224:227],v[96:99],v[144:147],v[224:227]
s_cbranch_vccz RA53_%=
s_add_u32 m0,s58,12320
s_mul_i32 s69,s59,3
s_add_u32 s69,s69,s97
buffer_load_dwordx4 v2,s[44:47],s69 offen lds
s_add_u32 m0,m0,1024
s_add_u32 s69,s69,s53
buffer_load_dwordx4 v3,s[44:47],s69 offen lds
s_add_u32 m0,m0,1024
s_add_u32 s69,s69,s53
buffer_load_dwordx4 v2,s[44:47],s69 offen lds
s_add_u32 m0,m0,s50
s_add_u32 s69,s69,s53
buffer_load_dwordx4 v3,s[44:47],s69 offen lds
RA53_%=:
v_lshlrev_b32_sdwa v132,1,v132 dst_sel:DWORD dst_unused:UNUSED_PAD src0_sel:DWORD src1_sel:WORD_1
v_mfma_f32_16x16x32_bf16 v[232:235],v[100:103],v[144:147],v[232:235]
s_waitcnt lgkmcnt(0)
ds_read_u16 v128,v128
v_lshl_add_u64 v[152:153],v[152:153],0,v[154:155]
v_cvt_pk_bf16_f32 v155,v157,v159
v_lshlrev_b32_sdwa v134,1,v134 dst_sel:DWORD dst_unused:UNUSED_PAD src0_sel:DWORD src1_sel:WORD_1
v_cvt_pk_bf16_f32 v154,v156,v158
v_mfma_f32_16x16x32_bf16 v[240:243],v[104:107],v[144:147],v[240:243]
ds_read_u16_d16_hi v130,v130
v_mfma_f32_16x16x32_bf16 v[248:251],v[108:111],v[144:147],v[248:251]
ds_read_u16 v129,v129
v_lshlrev_b32_sdwa v133,1,v133 dst_sel:DWORD dst_unused:UNUSED_PAD src0_sel:DWORD src1_sel:WORD_1
v_mfma_f32_16x16x32_bf16 v[212:215],v[88:91],v[152:155],v[212:215]
s_cbranch_vccz RA54_%=
s_add_u32 m0,m0,1024
s_add_u32 s69,s69,s53
buffer_load_dwordx4 v2,s[44:47],s69 offen lds
s_add_u32 m0,m0,1024
s_add_u32 s69,s69,s53
buffer_load_dwordx4 v3,s[44:47],s69 offen lds
s_add_u32 m0,m0,s50
s_add_u32 s69,s69,s53
buffer_load_dwordx4 v2,s[44:47],s69 offen lds
s_add_u32 m0,m0,1024
s_add_u32 s69,s69,s53
buffer_load_dwordx4 v3,s[44:47],s69 offen lds
RA54_%=:
v_lshlrev_b32_sdwa v135,1,v135 dst_sel:DWORD dst_unused:UNUSED_PAD src0_sel:DWORD src1_sel:WORD_1
v_mfma_f32_16x16x32_bf16 v[220:223],v[92:95],v[152:155],v[220:223]
ds_read_u16_d16_hi v131,v131
v_lshlrev_b32_sdwa v136,1,v136 dst_sel:DWORD dst_unused:UNUSED_PAD src0_sel:DWORD src1_sel:WORD_1
v_mfma_f32_16x16x32_bf16 v[228:231],v[96:99],v[152:155],v[228:231]
ds_read_b64_tr_b16 v[88:89],v6 offset:0
ds_read_b64_tr_b16 v[90:91],v6 offset:1536
v_lshlrev_b32_sdwa v138,1,v138 dst_sel:DWORD dst_unused:UNUSED_PAD src0_sel:DWORD src1_sel:WORD_1
v_mfma_f32_16x16x32_bf16 v[236:239],v[100:103],v[152:155],v[236:239]
ds_read_u16 v132,v132
v_mfma_f32_16x16x32_bf16 v[244:247],v[104:107],v[152:155],v[244:247]
ds_read_u16_d16_hi v134,v134
v_lshlrev_b32_sdwa v137,1,v137 dst_sel:DWORD dst_unused:UNUSED_PAD src0_sel:DWORD src1_sel:WORD_1
v_lshlrev_b32_sdwa v139,1,v139 dst_sel:DWORD dst_unused:UNUSED_PAD src0_sel:DWORD src1_sel:WORD_1
v_mfma_f32_16x16x32_bf16 v[252:255],v[108:111],v[152:155],v[252:255]
s_cbranch_vccz RA55_%=
s_add_u32 m0,m0,1024
s_add_u32 s69,s69,s53
buffer_load_dwordx4 v2,s[44:47],s69 offen lds
s_add_u32 m0,m0,s50
s_add_u32 s69,s69,s53
buffer_load_dwordx4 v3,s[44:47],s69 offen lds
s_add_u32 m0,m0,1024
s_add_u32 s69,s69,s53
buffer_load_dwordx4 v2,s[44:47],s69 offen lds
s_add_u32 m0,m0,1024
s_add_u32 s69,s69,s53
buffer_load_dwordx4 v3,s[44:47],s69 offen lds
RA55_%=:
ds_read_b64_tr_b16 v[92:93],v6 offset:8
ds_read_b64_tr_b16 v[94:95],v6 offset:1544
v_lshlrev_b32_sdwa v140,1,v140 dst_sel:DWORD dst_unused:UNUSED_PAD src0_sel:DWORD src1_sel:WORD_1
v_mfma_f32_16x16x32_bf16 v[144:147],v[64:67],v[40:43],v[112:115]
ds_read_u16 v133,v133
v_lshlrev_b32_sdwa v142,1,v142 dst_sel:DWORD dst_unused:UNUSED_PAD src0_sel:DWORD src1_sel:WORD_1
v_mfma_f32_16x16x32_bf16 v[152:155],v[64:67],v[44:47],v[112:115]
ds_read_b64_tr_b16 v[96:97],v6 offset:64
v_lshlrev_b32_sdwa v141,1,v141 dst_sel:DWORD dst_unused:UNUSED_PAD src0_sel:DWORD src1_sel:WORD_1
v_lshlrev_b32_sdwa v143,1,v143 dst_sel:DWORD dst_unused:UNUSED_PAD src0_sel:DWORD src1_sel:WORD_1
v_mfma_f32_16x16x32_bf16 v[156:159],v[68:71],v[44:47],v[116:119]
ds_read_u16_d16_hi v135,v135
v_mfma_f32_16x16x32_bf16 v[148:151],v[68:71],v[40:43],v[116:119]
ds_read_u16 v136,v136
ds_read_b64_tr_b16 v[98:99],v6 offset:1600
v_mfma_f32_16x16x32_bf16 v[144:147],v[72:75],v[48:51],v[144:147]
ds_read_u16_d16_hi v138,v138
v_mfma_f32_16x16x32_bf16 v[152:155],v[72:75],v[52:55],v[152:155]
s_cbranch_vccz RA56_%=
s_add_u32 m0,s100,0
s_mul_i32 s69,s59,9
s_add_u32 s69,s69,s97
buffer_load_dwordx4 v2,s[44:47],s69 offen lds
s_add_u32 m0,m0,1024
s_add_u32 s69,s69,s53
buffer_load_dwordx4 v3,s[44:47],s69 offen lds
s_add_u32 m0,m0,1024
s_add_u32 s69,s69,s53
buffer_load_dwordx4 v2,s[44:47],s69 offen lds
s_add_u32 m0,m0,s50
s_add_u32 s69,s69,s53
buffer_load_dwordx4 v3,s[44:47],s69 offen lds
RA56_%=:
ds_read_b64_tr_b16 v[100:101],v6 offset:72
ds_read_b64_tr_b16 v[102:103],v6 offset:1608
v_mfma_f32_16x16x32_bf16 v[156:159],v[76:79],v[52:55],v[156:159]
ds_read_u16 v137,v137
v_mfma_f32_16x16x32_bf16 v[148:151],v[76:79],v[48:51],v[148:151]
s_waitcnt lgkmcnt(6)
ds_read_u16_d16_hi v139,v139
v_lshl_add_u64 v[128:129],v[128:129],0,v[130:131]
v_lshl_add_u64 v[130:131],v[132:133],0,v[134:135]
v_mfma_f32_16x16x32_bf16 v[144:147],v[80:83],v[56:59],v[144:147]
ds_read_b64_tr_b16 v[104:105],v6 offset:128
ds_read_b64_tr_b16 v[106:107],v6 offset:1664
v_mfma_f32_16x16x32_bf16 v[152:155],v[80:83],v[60:63],v[152:155]
ds_read_u16 v140,v140
v_mfma_f32_16x16x32_bf16 v[156:159],v[84:87],v[60:63],v[156:159]
ds_read_u16_d16_hi v142,v142
s_nop 1
v_lshlrev_b32_sdwa v144,1,v144 dst_sel:DWORD dst_unused:UNUSED_PAD src0_sel:DWORD src1_sel:WORD_1
v_mfma_f32_16x16x32_bf16 v[148:151],v[84:87],v[56:59],v[148:151]
s_cbranch_vccz RA57_%=
s_add_u32 m0,m0,1024
s_add_u32 s69,s69,s53
buffer_load_dwordx4 v2,s[44:47],s69 offen lds
s_add_u32 m0,m0,1024
s_add_u32 s69,s69,s53
buffer_load_dwordx4 v3,s[44:47],s69 offen lds
s_add_u32 m0,m0,s50
s_add_u32 s69,s69,s53
buffer_load_dwordx4 v2,s[44:47],s69 offen lds
s_add_u32 m0,m0,1024
s_add_u32 s69,s69,s53
buffer_load_dwordx4 v3,s[44:47],s69 offen lds
RA57_%=:
ds_read_b64_tr_b16 v[108:109],v6 offset:136
ds_read_b64_tr_b16 v[110:111],v6 offset:1672
v_lshlrev_b32_sdwa v146,1,v146 dst_sel:DWORD dst_unused:UNUSED_PAD src0_sel:DWORD src1_sel:WORD_1
v_mfma_f32_16x16x32_bf16 v[160:163],v[88:91],v[128:131],v[160:163]
ds_read_b64_tr_b16 v[64:65],v4 offset:0
ds_read_u16 v141,v141
v_lshlrev_b32_sdwa v145,1,v145 dst_sel:DWORD dst_unused:UNUSED_PAD src0_sel:DWORD src1_sel:WORD_1
v_mfma_f32_16x16x32_bf16 v[168:171],v[92:95],v[128:131],v[168:171]
s_waitcnt lgkmcnt(13)
ds_read_u16_d16_hi v143,v143
v_lshlrev_b32_sdwa v147,1,v147 dst_sel:DWORD dst_unused:UNUSED_PAD src0_sel:DWORD src1_sel:WORD_1
v_mfma_f32_16x16x32_bf16 v[176:179],v[96:99],v[128:131],v[176:179]
s_waitcnt lgkmcnt(11)
ds_read_b64_tr_b16 v[66:67],v4 offset:2048
v_lshlrev_b32_sdwa v148,1,v148 dst_sel:DWORD dst_unused:UNUSED_PAD src0_sel:DWORD src1_sel:WORD_1
v_mfma_f32_16x16x32_bf16 v[184:187],v[100:103],v[128:131],v[184:187]
s_waitcnt lgkmcnt(8)
ds_read_u16 v144,v144
v_lshl_add_u64 v[136:137],v[136:137],0,v[138:139]
v_lshlrev_b32_sdwa v150,1,v150 dst_sel:DWORD dst_unused:UNUSED_PAD src0_sel:DWORD src1_sel:WORD_1
v_mfma_f32_16x16x32_bf16 v[192:195],v[104:107],v[128:131],v[192:195]
s_waitcnt lgkmcnt(2)
s_cbranch_vccz RA58_%=
s_add_u32 m0,m0,1024
s_add_u32 s69,s69,s53
buffer_load_dwordx4 v2,s[44:47],s69 offen lds
s_add_u32 m0,m0,s50
s_add_u32 s69,s69,s53
buffer_load_dwordx4 v3,s[44:47],s69 offen lds
s_add_u32 m0,m0,1024
s_add_u32 s69,s69,s53
buffer_load_dwordx4 v2,s[44:47],s69 offen lds
s_add_u32 m0,m0,1024
s_add_u32 s69,s69,s53
buffer_load_dwordx4 v3,s[44:47],s69 offen lds
RA58_%=:
ds_read_u16_d16_hi v146,v146
v_lshl_add_u64 v[138:139],v[140:141],0,v[142:143]
v_lshlrev_b32_sdwa v149,1,v149 dst_sel:DWORD dst_unused:UNUSED_PAD src0_sel:DWORD src1_sel:WORD_1
v_mfma_f32_16x16x32_bf16 v[200:203],v[108:111],v[128:131],v[200:203]
ds_read_b128 v[112:115],v7 offset:256
v_mfma_f32_16x16x32_bf16 v[164:167],v[88:91],v[136:139],v[164:167]
ds_read_u16 v145,v145
v_lshlrev_b32_sdwa v151,1,v151 dst_sel:DWORD dst_unused:UNUSED_PAD src0_sel:DWORD src1_sel:WORD_1
v_mfma_f32_16x16x32_bf16 v[172:175],v[92:95],v[136:139],v[172:175]
ds_read_u16_d16_hi v147,v147
ds_read_b64_tr_b16 v[68:69],v5 offset:0
v_lshlrev_b32_sdwa v152,1,v152 dst_sel:DWORD dst_unused:UNUSED_PAD src0_sel:DWORD src1_sel:WORD_1
v_mfma_f32_16x16x32_bf16 v[180:183],v[96:99],v[136:139],v[180:183]
ds_read_b64_tr_b16 v[70:71],v5 offset:2048
ds_read_b128 v[116:119],v7 offset:288
v_lshlrev_b32_sdwa v154,1,v154 dst_sel:DWORD dst_unused:UNUSED_PAD src0_sel:DWORD src1_sel:WORD_1
v_mfma_f32_16x16x32_bf16 v[188:191],v[100:103],v[136:139],v[188:191]
ds_read_u16 v148,v148
ds_read_b64_tr_b16 v[72:73],v4 offset:4096
v_lshlrev_b32_sdwa v153,1,v153 dst_sel:DWORD dst_unused:UNUSED_PAD src0_sel:DWORD src1_sel:WORD_1
v_mfma_f32_16x16x32_bf16 v[196:199],v[104:107],v[136:139],v[196:199]
ds_read_u16_d16_hi v150,v150
v_lshlrev_b32_sdwa v155,1,v155 dst_sel:DWORD dst_unused:UNUSED_PAD src0_sel:DWORD src1_sel:WORD_1
v_mfma_f32_16x16x32_bf16 v[204:207],v[108:111],v[136:139],v[204:207]
s_waitcnt lgkmcnt(8)
ds_read_b64_tr_b16 v[74:75],v4 offset:6144
ds_read_b64_tr_b16 v[76:77],v5 offset:4096
v_mfma_f32_16x16x32_bf16 v[128:131],v[64:67],v[16:19],v[112:115]
ds_read_u16 v149,v149
v_lshlrev_b32_sdwa v156,1,v156 dst_sel:DWORD dst_unused:UNUSED_PAD src0_sel:DWORD src1_sel:WORD_1
v_lshlrev_b32_sdwa v158,1,v158 dst_sel:DWORD dst_unused:UNUSED_PAD src0_sel:DWORD src1_sel:WORD_1
v_mfma_f32_16x16x32_bf16 v[136:139],v[64:67],v[20:23],v[112:115]
s_waitcnt lgkmcnt(6)
ds_read_b64_tr_b16 v[78:79],v5 offset:6144
ds_read_b64_tr_b16 v[80:81],v4 offset:8192
v_lshlrev_b32_sdwa v157,1,v157 dst_sel:DWORD dst_unused:UNUSED_PAD src0_sel:DWORD src1_sel:WORD_1
v_mfma_f32_16x16x32_bf16 v[140:143],v[68:71],v[20:23],v[116:119]
ds_read_u16_d16_hi v151,v151
v_lshlrev_b32_sdwa v159,1,v159 dst_sel:DWORD dst_unused:UNUSED_PAD src0_sel:DWORD src1_sel:WORD_1
v_mfma_f32_16x16x32_bf16 v[132:135],v[68:71],v[16:19],v[116:119]
s_waitcnt lgkmcnt(5)
ds_read_u16 v152,v152
ds_read_b64_tr_b16 v[82:83],v4 offset:10240
v_mfma_f32_16x16x32_bf16 v[128:131],v[72:75],v[24:27],v[128:131]
ds_read_b64_tr_b16 v[84:85],v5 offset:8192
ds_read_b64_tr_b16 v[86:87],v5 offset:10240
v_mfma_f32_16x16x32_bf16 v[136:139],v[72:75],v[28:31],v[136:139]
s_waitcnt lgkmcnt(6)
ds_read_u16_d16_hi v154,v154
v_mfma_f32_16x16x32_bf16 v[140:143],v[76:79],v[28:31],v[140:143]
ds_read_u16 v153,v153
v_mfma_f32_16x16x32_bf16 v[132:135],v[76:79],v[24:27],v[132:135]
s_waitcnt lgkmcnt(4)
ds_read_u16_d16_hi v155,v155
v_lshl_add_u64 v[144:145],v[144:145],0,v[146:147]
v_mfma_f32_16x16x32_bf16 v[128:131],v[80:83],v[32:35],v[128:131]
v_lshl_add_u64 v[146:147],v[148:149],0,v[150:151]
v_mfma_f32_16x16x32_bf16 v[136:139],v[80:83],v[36:39],v[136:139]
s_waitcnt lgkmcnt(3)
ds_read_u16 v156,v156
v_mfma_f32_16x16x32_bf16 v[140:143],v[84:87],v[36:39],v[140:143]
ds_read_u16_d16_hi v158,v158
v_mfma_f32_16x16x32_bf16 v[132:135],v[84:87],v[32:35],v[132:135]
ds_read_u16 v157,v157
v_lshlrev_b32_sdwa v128,1,v128 dst_sel:DWORD dst_unused:UNUSED_PAD src0_sel:DWORD src1_sel:WORD_1
v_lshlrev_b32_sdwa v130,1,v130 dst_sel:DWORD dst_unused:UNUSED_PAD src0_sel:DWORD src1_sel:WORD_1
v_mfma_f32_16x16x32_bf16 v[208:211],v[88:91],v[144:147],v[208:211]
v_lshlrev_b32_sdwa v129,1,v129 dst_sel:DWORD dst_unused:UNUSED_PAD src0_sel:DWORD src1_sel:WORD_1
v_lshlrev_b32_sdwa v131,1,v131 dst_sel:DWORD dst_unused:UNUSED_PAD src0_sel:DWORD src1_sel:WORD_1
v_mfma_f32_16x16x32_bf16 v[216:219],v[92:95],v[144:147],v[216:219]
ds_read_u16_d16_hi v159,v159
v_mfma_f32_16x16x32_bf16 v[224:227],v[96:99],v[144:147],v[224:227]
v_lshlrev_b32_sdwa v132,1,v132 dst_sel:DWORD dst_unused:UNUSED_PAD src0_sel:DWORD src1_sel:WORD_1
v_mfma_f32_16x16x32_bf16 v[232:235],v[100:103],v[144:147],v[232:235]
s_waitcnt lgkmcnt(4)
ds_read_u16 v128,v128
v_lshl_add_u64 v[152:153],v[152:153],0,v[154:155]
v_lshlrev_b32_sdwa v134,1,v134 dst_sel:DWORD dst_unused:UNUSED_PAD src0_sel:DWORD src1_sel:WORD_1
v_mfma_f32_16x16x32_bf16 v[240:243],v[104:107],v[144:147],v[240:243]
s_waitcnt lgkmcnt(1)
ds_read_u16_d16_hi v130,v130
v_lshl_add_u64 v[154:155],v[156:157],0,v[158:159]
v_mfma_f32_16x16x32_bf16 v[248:251],v[108:111],v[144:147],v[248:251]
ds_read_u16 v129,v129
v_lshlrev_b32_sdwa v133,1,v133 dst_sel:DWORD dst_unused:UNUSED_PAD src0_sel:DWORD src1_sel:WORD_1
v_mfma_f32_16x16x32_bf16 v[212:215],v[88:91],v[152:155],v[212:215]
v_lshlrev_b32_sdwa v135,1,v135 dst_sel:DWORD dst_unused:UNUSED_PAD src0_sel:DWORD src1_sel:WORD_1
v_mfma_f32_16x16x32_bf16 v[220:223],v[92:95],v[152:155],v[220:223]
ds_read_u16_d16_hi v131,v131
v_lshlrev_b32_sdwa v136,1,v136 dst_sel:DWORD dst_unused:UNUSED_PAD src0_sel:DWORD src1_sel:WORD_1
v_mfma_f32_16x16x32_bf16 v[228:231],v[96:99],v[152:155],v[228:231]
ds_read_b64_tr_b16 v[88:89],v6 offset:6160
ds_read_b64_tr_b16 v[90:91],v6 offset:7696
v_lshlrev_b32_sdwa v138,1,v138 dst_sel:DWORD dst_unused:UNUSED_PAD src0_sel:DWORD src1_sel:WORD_1
v_mfma_f32_16x16x32_bf16 v[236:239],v[100:103],v[152:155],v[236:239]
ds_read_u16 v132,v132
v_mfma_f32_16x16x32_bf16 v[244:247],v[104:107],v[152:155],v[244:247]
ds_read_u16_d16_hi v134,v134
v_lshlrev_b32_sdwa v137,1,v137 dst_sel:DWORD dst_unused:UNUSED_PAD src0_sel:DWORD src1_sel:WORD_1
v_lshlrev_b32_sdwa v139,1,v139 dst_sel:DWORD dst_unused:UNUSED_PAD src0_sel:DWORD src1_sel:WORD_1
v_mfma_f32_16x16x32_bf16 v[252:255],v[108:111],v[152:155],v[252:255]
ds_read_b64_tr_b16 v[92:93],v6 offset:6168
ds_read_b64_tr_b16 v[94:95],v6 offset:7704
v_lshlrev_b32_sdwa v140,1,v140 dst_sel:DWORD dst_unused:UNUSED_PAD src0_sel:DWORD src1_sel:WORD_1
v_mfma_f32_16x16x32_bf16 v[144:147],v[64:67],v[40:43],v[112:115]
ds_read_u16 v133,v133
v_lshlrev_b32_sdwa v142,1,v142 dst_sel:DWORD dst_unused:UNUSED_PAD src0_sel:DWORD src1_sel:WORD_1
v_mfma_f32_16x16x32_bf16 v[152:155],v[64:67],v[44:47],v[112:115]
ds_read_b64_tr_b16 v[96:97],v6 offset:6224
v_lshlrev_b32_sdwa v141,1,v141 dst_sel:DWORD dst_unused:UNUSED_PAD src0_sel:DWORD src1_sel:WORD_1
v_lshlrev_b32_sdwa v143,1,v143 dst_sel:DWORD dst_unused:UNUSED_PAD src0_sel:DWORD src1_sel:WORD_1
v_mfma_f32_16x16x32_bf16 v[156:159],v[68:71],v[44:47],v[116:119]
ds_read_u16_d16_hi v135,v135
v_mfma_f32_16x16x32_bf16 v[148:151],v[68:71],v[40:43],v[116:119]
ds_read_u16 v136,v136
ds_read_b64_tr_b16 v[98:99],v6 offset:7760
v_mfma_f32_16x16x32_bf16 v[144:147],v[72:75],v[48:51],v[144:147]
ds_read_u16_d16_hi v138,v138
v_mfma_f32_16x16x32_bf16 v[152:155],v[72:75],v[52:55],v[152:155]
ds_read_b64_tr_b16 v[100:101],v6 offset:6232
ds_read_b64_tr_b16 v[102:103],v6 offset:7768
v_mfma_f32_16x16x32_bf16 v[156:159],v[76:79],v[52:55],v[156:159]
ds_read_u16 v137,v137
v_mfma_f32_16x16x32_bf16 v[148:151],v[76:79],v[48:51],v[148:151]
s_waitcnt lgkmcnt(6)
ds_read_u16_d16_hi v139,v139
v_lshl_add_u64 v[128:129],v[128:129],0,v[130:131]
v_lshl_add_u64 v[130:131],v[132:133],0,v[134:135]
v_mfma_f32_16x16x32_bf16 v[144:147],v[80:83],v[56:59],v[144:147]
ds_read_b64_tr_b16 v[104:105],v6 offset:6288
ds_read_b64_tr_b16 v[106:107],v6 offset:7824
v_mfma_f32_16x16x32_bf16 v[152:155],v[80:83],v[60:63],v[152:155]
ds_read_u16 v140,v140
v_mfma_f32_16x16x32_bf16 v[156:159],v[84:87],v[60:63],v[156:159]
ds_read_u16_d16_hi v142,v142
s_nop 1
v_lshlrev_b32_sdwa v144,1,v144 dst_sel:DWORD dst_unused:UNUSED_PAD src0_sel:DWORD src1_sel:WORD_1
v_mfma_f32_16x16x32_bf16 v[148:151],v[84:87],v[56:59],v[148:151]
ds_read_b64_tr_b16 v[108:109],v6 offset:6296
ds_read_b64_tr_b16 v[110:111],v6 offset:7832
v_lshlrev_b32_sdwa v146,1,v146 dst_sel:DWORD dst_unused:UNUSED_PAD src0_sel:DWORD src1_sel:WORD_1
v_mfma_f32_16x16x32_bf16 v[160:163],v[88:91],v[128:131],v[160:163]
ds_read_b64_tr_b16 v[64:65],v4 offset:512
ds_read_u16 v141,v141
v_lshlrev_b32_sdwa v145,1,v145 dst_sel:DWORD dst_unused:UNUSED_PAD src0_sel:DWORD src1_sel:WORD_1
s_mov_b32 s86,0xbe801d5a
v_mfma_f32_16x16x32_bf16 v[168:171],v[92:95],v[128:131],v[168:171]
s_waitcnt lgkmcnt(13)
ds_read_u16_d16_hi v143,v143
v_lshlrev_b32_sdwa v147,1,v147 dst_sel:DWORD dst_unused:UNUSED_PAD src0_sel:DWORD src1_sel:WORD_1
v_mfma_f32_16x16x32_bf16 v[176:179],v[96:99],v[128:131],v[176:179]
s_waitcnt lgkmcnt(11)
ds_read_b64_tr_b16 v[66:67],v4 offset:2560
v_lshlrev_b32_sdwa v148,1,v148 dst_sel:DWORD dst_unused:UNUSED_PAD src0_sel:DWORD src1_sel:WORD_1
v_mfma_f32_16x16x32_bf16 v[184:187],v[100:103],v[128:131],v[184:187]
s_waitcnt lgkmcnt(8)
ds_read_u16 v144,v144
v_lshl_add_u64 v[136:137],v[136:137],0,v[138:139]
v_lshlrev_b32_sdwa v150,1,v150 dst_sel:DWORD dst_unused:UNUSED_PAD src0_sel:DWORD src1_sel:WORD_1
v_mfma_f32_16x16x32_bf16 v[192:195],v[104:107],v[128:131],v[192:195]
s_waitcnt lgkmcnt(2)
ds_read_u16_d16_hi v146,v146
v_lshl_add_u64 v[138:139],v[140:141],0,v[142:143]
v_lshlrev_b32_sdwa v149,1,v149 dst_sel:DWORD dst_unused:UNUSED_PAD src0_sel:DWORD src1_sel:WORD_1
v_mfma_f32_16x16x32_bf16 v[200:203],v[108:111],v[128:131],v[200:203]
ds_read_b128 v[112:115],v7 offset:384
v_mfma_f32_16x16x32_bf16 v[164:167],v[88:91],v[136:139],v[164:167]
ds_read_u16 v145,v145
v_lshlrev_b32_sdwa v151,1,v151 dst_sel:DWORD dst_unused:UNUSED_PAD src0_sel:DWORD src1_sel:WORD_1
v_mfma_f32_16x16x32_bf16 v[172:175],v[92:95],v[136:139],v[172:175]
ds_read_u16_d16_hi v147,v147
ds_read_b64_tr_b16 v[68:69],v5 offset:512
v_lshlrev_b32_sdwa v152,1,v152 dst_sel:DWORD dst_unused:UNUSED_PAD src0_sel:DWORD src1_sel:WORD_1
v_mfma_f32_16x16x32_bf16 v[180:183],v[96:99],v[136:139],v[180:183]
ds_read_b64_tr_b16 v[70:71],v5 offset:2560
ds_read_b128 v[116:119],v7 offset:416
v_lshlrev_b32_sdwa v154,1,v154 dst_sel:DWORD dst_unused:UNUSED_PAD src0_sel:DWORD src1_sel:WORD_1
v_mfma_f32_16x16x32_bf16 v[188:191],v[100:103],v[136:139],v[188:191]
ds_read_u16 v148,v148
ds_read_b64_tr_b16 v[72:73],v4 offset:4608
v_lshlrev_b32_sdwa v153,1,v153 dst_sel:DWORD dst_unused:UNUSED_PAD src0_sel:DWORD src1_sel:WORD_1
v_mfma_f32_16x16x32_bf16 v[196:199],v[104:107],v[136:139],v[196:199]
ds_read_u16_d16_hi v150,v150
v_lshlrev_b32_sdwa v155,1,v155 dst_sel:DWORD dst_unused:UNUSED_PAD src0_sel:DWORD src1_sel:WORD_1
v_mfma_f32_16x16x32_bf16 v[204:207],v[108:111],v[136:139],v[204:207]
s_waitcnt lgkmcnt(8)
ds_read_b64_tr_b16 v[74:75],v4 offset:6656
ds_read_b64_tr_b16 v[76:77],v5 offset:4608
v_mfma_f32_16x16x32_bf16 v[128:131],v[64:67],v[16:19],v[112:115]
ds_read_u16 v149,v149
v_lshlrev_b32_sdwa v156,1,v156 dst_sel:DWORD dst_unused:UNUSED_PAD src0_sel:DWORD src1_sel:WORD_1
v_lshlrev_b32_sdwa v158,1,v158 dst_sel:DWORD dst_unused:UNUSED_PAD src0_sel:DWORD src1_sel:WORD_1
v_mfma_f32_16x16x32_bf16 v[136:139],v[64:67],v[20:23],v[112:115]
s_waitcnt lgkmcnt(6)
ds_read_b64_tr_b16 v[78:79],v5 offset:6656
ds_read_b64_tr_b16 v[80:81],v4 offset:8704
v_lshlrev_b32_sdwa v157,1,v157 dst_sel:DWORD dst_unused:UNUSED_PAD src0_sel:DWORD src1_sel:WORD_1
v_mfma_f32_16x16x32_bf16 v[140:143],v[68:71],v[20:23],v[116:119]
ds_read_u16_d16_hi v151,v151
v_lshlrev_b32_sdwa v159,1,v159 dst_sel:DWORD dst_unused:UNUSED_PAD src0_sel:DWORD src1_sel:WORD_1
v_mfma_f32_16x16x32_bf16 v[132:135],v[68:71],v[16:19],v[116:119]
s_waitcnt lgkmcnt(5)
ds_read_u16 v152,v152
ds_read_b64_tr_b16 v[82:83],v4 offset:10752
v_mfma_f32_16x16x32_bf16 v[128:131],v[72:75],v[24:27],v[128:131]
ds_read_b64_tr_b16 v[84:85],v5 offset:8704
ds_read_b64_tr_b16 v[86:87],v5 offset:10752
v_mfma_f32_16x16x32_bf16 v[136:139],v[72:75],v[28:31],v[136:139]
s_waitcnt lgkmcnt(6)
ds_read_u16_d16_hi v154,v154
v_mfma_f32_16x16x32_bf16 v[140:143],v[76:79],v[28:31],v[140:143]
ds_read_u16 v153,v153
v_mfma_f32_16x16x32_bf16 v[132:135],v[76:79],v[24:27],v[132:135]
s_waitcnt lgkmcnt(4)
ds_read_u16_d16_hi v155,v155
v_lshl_add_u64 v[144:145],v[144:145],0,v[146:147]
v_mfma_f32_16x16x32_bf16 v[128:131],v[80:83],v[32:35],v[128:131]
v_lshl_add_u64 v[146:147],v[148:149],0,v[150:151]
v_mfma_f32_16x16x32_bf16 v[136:139],v[80:83],v[36:39],v[136:139]
s_waitcnt lgkmcnt(3)
ds_read_u16 v156,v156
v_mfma_f32_16x16x32_bf16 v[140:143],v[84:87],v[36:39],v[140:143]
ds_read_u16_d16_hi v158,v158
v_mfma_f32_16x16x32_bf16 v[132:135],v[84:87],v[32:35],v[132:135]
ds_read_u16 v157,v157
v_lshlrev_b32_sdwa v128,1,v128 dst_sel:DWORD dst_unused:UNUSED_PAD src0_sel:DWORD src1_sel:WORD_1
v_lshlrev_b32_sdwa v130,1,v130 dst_sel:DWORD dst_unused:UNUSED_PAD src0_sel:DWORD src1_sel:WORD_1
v_mfma_f32_16x16x32_bf16 v[208:211],v[88:91],v[144:147],v[208:211]
s_waitcnt vmcnt(12)
s_barrier
s_setprio 3
v_lshlrev_b32_sdwa v129,1,v129 dst_sel:DWORD dst_unused:UNUSED_PAD src0_sel:DWORD src1_sel:WORD_1
v_lshlrev_b32_sdwa v131,1,v131 dst_sel:DWORD dst_unused:UNUSED_PAD src0_sel:DWORD src1_sel:WORD_1
v_mfma_f32_16x16x32_bf16 v[216:219],v[92:95],v[144:147],v[216:219]
ds_read_u16_d16_hi v159,v159
ds_read_u16 v128,v128
v_fma_mixlo_f16 v120,v140,s99,0
v_fma_mixlo_f16 v122,v141,s99,0
v_mfma_f32_16x16x32_bf16 v[224:227],v[96:99],v[144:147],v[224:227]
s_cbranch_vccz RA59_%=
s_add_u32 m0,s58,0
s_mul_i32 s69,s59,5
s_add_u32 s69,s69,s97
buffer_load_dwordx4 v2,s[44:47],s69 offen lds
s_add_u32 m0,m0,1024
s_add_u32 s69,s69,s53
buffer_load_dwordx4 v3,s[44:47],s69 offen lds
s_add_u32 m0,m0,1024
s_add_u32 s69,s69,s53
buffer_load_dwordx4 v2,s[44:47],s69 offen lds
s_add_u32 m0,m0,s50
s_add_u32 s69,s69,s53
buffer_load_dwordx4 v3,s[44:47],s69 offen lds
RA59_%=:
ds_read_u16_d16_hi v130,v130
v_lshlrev_b32_sdwa v132,1,v132 dst_sel:DWORD dst_unused:UNUSED_PAD src0_sel:DWORD src1_sel:WORD_1
v_lshlrev_b32_sdwa v134,1,v134 dst_sel:DWORD dst_unused:UNUSED_PAD src0_sel:DWORD src1_sel:WORD_1
v_mfma_f32_16x16x32_bf16 v[232:235],v[100:103],v[144:147],v[232:235]
s_waitcnt lgkmcnt(6)
v_lshl_add_u64 v[152:153],v[152:153],0,v[154:155]
v_mfma_f32_16x16x32_bf16 v[240:243],v[104:107],v[144:147],v[240:243]
s_waitcnt lgkmcnt(2)
v_lshl_add_u64 v[154:155],v[156:157],0,v[158:159]
v_fma_mixhi_f16 v120,v142,s99,0
v_mfma_f32_16x16x32_bf16 v[248:251],v[108:111],v[144:147],v[248:251]
ds_read_u16 v129,v129
v_fma_mixhi_f16 v122,v143,s99,0
v_mfma_f32_16x16x32_bf16 v[212:215],v[88:91],v[152:155],v[212:215]
s_cbranch_vccz RA60_%=
s_add_u32 m0,m0,1024
s_add_u32 s69,s69,s53
buffer_load_dwordx4 v2,s[44:47],s69 offen lds
s_add_u32 m0,m0,1024
s_add_u32 s69,s69,s53
buffer_load_dwordx4 v3,s[44:47],s69 offen lds
s_add_u32 m0,m0,s50
s_add_u32 s69,s69,s53
buffer_load_dwordx4 v2,s[44:47],s69 offen lds
s_add_u32 m0,m0,1024
s_add_u32 s69,s69,s53
buffer_load_dwordx4 v3,s[44:47],s69 offen lds
RA60_%=:
v_lshlrev_b32_sdwa v133,1,v133 dst_sel:DWORD dst_unused:UNUSED_PAD src0_sel:DWORD src1_sel:WORD_1
v_pk_fma_f16 v121,v120,v120,s54
v_lshlrev_b32_sdwa v135,1,v135 dst_sel:DWORD dst_unused:UNUSED_PAD src0_sel:DWORD src1_sel:WORD_1
v_mfma_f32_16x16x32_bf16 v[220:223],v[92:95],v[152:155],v[220:223]
ds_read_u16_d16_hi v131,v131
ds_read_u16 v132,v132
v_pk_fma_f16 v123,v122,v122,s54
v_mfma_f32_16x16x32_bf16 v[228:231],v[96:99],v[152:155],v[228:231]
ds_read_b64_tr_b16 v[88:89],v6 offset:12320
v_pk_mul_f16 v121,v120,v121
v_lshlrev_b32_sdwa v136,1,v136 dst_sel:DWORD dst_unused:UNUSED_PAD src0_sel:DWORD src1_sel:WORD_1
v_pk_mul_f16 v123,v122,v123
v_mfma_f32_16x16x32_bf16 v[236:239],v[100:103],v[152:155],v[236:239]
s_cbranch_vccz RA61_%=
s_add_u32 m0,m0,1024
s_add_u32 s69,s69,s53
buffer_load_dwordx4 v2,s[44:47],s69 offen lds
s_add_u32 m0,m0,s50
s_add_u32 s69,s69,s53
buffer_load_dwordx4 v3,s[44:47],s69 offen lds
s_add_u32 m0,m0,1024
s_add_u32 s69,s69,s53
buffer_load_dwordx4 v2,s[44:47],s69 offen lds
s_add_u32 m0,m0,1024
s_add_u32 s69,s69,s53
buffer_load_dwordx4 v3,s[44:47],s69 offen lds
RA61_%=:
ds_read_b64_tr_b16 v[90:91],v6 offset:13856
v_lshlrev_b32_sdwa v138,1,v138 dst_sel:DWORD dst_unused:UNUSED_PAD src0_sel:DWORD src1_sel:WORD_1
v_mfma_f32_16x16x32_bf16 v[244:247],v[104:107],v[152:155],v[244:247]
ds_read_u16_d16_hi v134,v134
v_exp_f16 v120,v121
v_mfma_f32_16x16x32_bf16 v[252:255],v[108:111],v[152:155],v[252:255]
ds_read_b64_tr_b16 v[92:93],v6 offset:12328
v_lshlrev_b32_sdwa v137,1,v137 dst_sel:DWORD dst_unused:UNUSED_PAD src0_sel:DWORD src1_sel:WORD_1
v_mfma_f32_16x16x32_bf16 v[144:147],v[64:67],v[40:43],v[112:115]
ds_read_b64_tr_b16 v[94:95],v6 offset:13864
v_lshlrev_b32_sdwa v139,1,v139 dst_sel:DWORD dst_unused:UNUSED_PAD src0_sel:DWORD src1_sel:WORD_1
v_exp_f16 v122,v123
v_mfma_f32_16x16x32_bf16 v[152:155],v[64:67],v[44:47],v[112:115]
s_cbranch_vccz RA62_%=
s_add_u32 m0,s100,12320
s_mov_b32 s69,s60
buffer_load_dwordx4 v2,s[44:47],s69 offen lds
s_add_u32 m0,m0,1024
s_add_u32 s69,s69,s53
buffer_load_dwordx4 v3,s[44:47],s69 offen lds
s_add_u32 m0,m0,1024
s_add_u32 s69,s69,s53
buffer_load_dwordx4 v2,s[44:47],s69 offen lds
s_add_u32 m0,m0,s50
s_add_u32 s69,s69,s53
buffer_load_dwordx4 v3,s[44:47],s69 offen lds
RA62_%=:
ds_read_u16 v133,v133
ds_read_b64_tr_b16 v[96:97],v6 offset:12384
v_mfma_f32_16x16x32_bf16 v[156:159],v[68:71],v[44:47],v[116:119]
ds_read_u16_d16_hi v135,v135
v_exp_f16_sdwa v122,v123 dst_sel:WORD_1 dst_unused:UNUSED_PRESERVE src0_sel:WORD_1
v_exp_f16_sdwa v120,v121 dst_sel:WORD_1 dst_unused:UNUSED_PRESERVE src0_sel:WORD_1
v_mfma_f32_16x16x32_bf16 v[148:151],v[68:71],v[40:43],v[116:119]
ds_read_b64_tr_b16 v[98:99],v6 offset:13920
v_pk_fma_f16 v120,v120,s55,s55
v_pk_fma_f16 v122,v122,s55,s55
v_mfma_f32_16x16x32_bf16 v[144:147],v[72:75],v[48:51],v[144:147]
v_rcp_f16 v121,v120
v_mfma_f32_16x16x32_bf16 v[152:155],v[72:75],v[52:55],v[152:155]
s_cbranch_vccz RA63_%=
s_add_u32 m0,m0,1024
s_add_u32 s69,s69,s53
buffer_load_dwordx4 v2,s[44:47],s69 offen lds
s_add_u32 m0,m0,1024
s_add_u32 s69,s69,s53
buffer_load_dwordx4 v3,s[44:47],s69 offen lds
s_add_u32 m0,m0,s50
s_add_u32 s69,s69,s51
buffer_load_dwordx4 v2,s[44:47],s69 offen lds
s_add_u32 m0,m0,1024
s_add_u32 s69,s69,s53
buffer_load_dwordx4 v3,s[44:47],s69 offen lds
RA63_%=:
ds_read_b64_tr_b16 v[100:101],v6 offset:12392
v_rcp_f16 v123,v122
v_mfma_f32_16x16x32_bf16 v[156:159],v[76:79],v[52:55],v[156:159]
ds_read_u16 v136,v136
v_rcp_f16_sdwa v121,v120 dst_sel:WORD_1 dst_unused:UNUSED_PRESERVE src0_sel:WORD_1
v_mfma_f32_16x16x32_bf16 v[148:151],v[76:79],v[48:51],v[148:151]
ds_read_b64_tr_b16 v[102:103],v6 offset:13928
ds_read_b64_tr_b16 v[104:105],v6 offset:12448
v_rcp_f16_sdwa v123,v122 dst_sel:WORD_1 dst_unused:UNUSED_PRESERVE src0_sel:WORD_1
v_mfma_f32_16x16x32_bf16 v[144:147],v[80:83],v[56:59],v[144:147]
ds_read_u16_d16_hi v138,v138
v_fma_mix_f32 v142,v142,v121,0 op_sel:[0,1,0] op_sel_hi:[0,1,0]
v_mfma_f32_16x16x32_bf16 v[152:155],v[80:83],v[60:63],v[152:155]
s_waitcnt lgkmcnt(6)
s_cbranch_vccz RA64_%=
s_add_u32 m0,m0,1024
s_add_u32 s69,s69,s53
buffer_load_dwordx4 v2,s[44:47],s69 offen lds
s_add_u32 m0,m0,s50
s_add_u32 s69,s69,s53
buffer_load_dwordx4 v3,s[44:47],s69 offen lds
s_add_u32 m0,m0,1024
s_add_u32 s69,s69,s53
buffer_load_dwordx4 v2,s[44:47],s69 offen lds
s_add_u32 m0,m0,1024
s_add_u32 s69,s69,s53
buffer_load_dwordx4 v3,s[44:47],s69 offen lds
RA64_%=:
ds_read_b64_tr_b16 v[106:107],v6 offset:13984
ds_read_b64_tr_b16 v[108:109],v6 offset:12456
v_fma_mix_f32 v140,v140,v121,0 op_sel_hi:[0,1,0]
v_lshl_add_u64 v[128:129],v[128:129],0,v[130:131]
v_lshl_add_u64 v[130:131],v[132:133],0,v[134:135]
v_mfma_f32_16x16x32_bf16 v[156:159],v[84:87],v[60:63],v[156:159]
ds_read_u16 v137,v137
v_fma_mix_f32 v141,v141,v123,0 op_sel_hi:[0,1,0]
v_fma_mix_f32 v143,v143,v123,0 op_sel:[0,1,0] op_sel_hi:[0,1,0]
v_mfma_f32_16x16x32_bf16 v[148:151],v[84:87],v[56:59],v[148:151]
ds_read_b64_tr_b16 v[110:111],v6 offset:13992
ds_read_b64_tr_b16 v[64:65],v4 offset:12320
v_lshlrev_b32_sdwa v144,1,v144 dst_sel:DWORD dst_unused:UNUSED_PAD src0_sel:DWORD src1_sel:WORD_1
v_lshlrev_b32_sdwa v146,1,v146 dst_sel:DWORD dst_unused:UNUSED_PAD src0_sel:DWORD src1_sel:WORD_1
v_mfma_f32_16x16x32_bf16 v[160:163],v[88:91],v[128:131],v[160:163]
ds_read_u16_d16_hi v139,v139
v_lshlrev_b32_sdwa v145,1,v145 dst_sel:DWORD dst_unused:UNUSED_PAD src0_sel:DWORD src1_sel:WORD_1
v_lshlrev_b32_sdwa v147,1,v147 dst_sel:DWORD dst_unused:UNUSED_PAD src0_sel:DWORD src1_sel:WORD_1
v_fma_mixlo_f16 v122,v157,s99,0
v_mfma_f32_16x16x32_bf16 v[168:171],v[92:95],v[128:131],v[168:171]
s_waitcnt lgkmcnt(11)
ds_read_b64_tr_b16 v[66:67],v4 offset:14368
ds_read_b128 v[112:115],v7 offset:512
v_fma_mixlo_f16 v120,v156,s99,0
v_mfma_f32_16x16x32_bf16 v[176:179],v[96:99],v[128:131],v[176:179]
s_waitcnt lgkmcnt(10)
ds_read_u16 v144,v144
v_lshlrev_b32_sdwa v148,1,v148 dst_sel:DWORD dst_unused:UNUSED_PAD src0_sel:DWORD src1_sel:WORD_1
v_lshlrev_b32_sdwa v150,1,v150 dst_sel:DWORD dst_unused:UNUSED_PAD src0_sel:DWORD src1_sel:WORD_1
v_mfma_f32_16x16x32_bf16 v[184:187],v[100:103],v[128:131],v[184:187]
s_waitcnt lgkmcnt(3)
ds_read_b64_tr_b16 v[68:69],v5 offset:12320
ds_read_b64_tr_b16 v[70:71],v5 offset:14368
v_lshl_add_u64 v[136:137],v[136:137],0,v[138:139]
v_fma_mixhi_f16 v120,v158,s99,0
v_mfma_f32_16x16x32_bf16 v[192:195],v[104:107],v[128:131],v[192:195]
ds_read_u16_d16_hi v146,v146
v_cvt_pk_bf16_f32 v138,v140,v142
v_cvt_pk_bf16_f32 v139,v141,v143
v_mfma_f32_16x16x32_bf16 v[200:203],v[108:111],v[128:131],v[200:203]
v_fma_mixhi_f16 v122,v159,s99,0
v_mfma_f32_16x16x32_bf16 v[164:167],v[88:91],v[136:139],v[164:167]
ds_read_b128 v[116:119],v7 offset:544
ds_read_b64_tr_b16 v[72:73],v4 offset:16416
v_lshlrev_b32_sdwa v149,1,v149 dst_sel:DWORD dst_unused:UNUSED_PAD src0_sel:DWORD src1_sel:WORD_1
v_lshlrev_b32_sdwa v151,1,v151 dst_sel:DWORD dst_unused:UNUSED_PAD src0_sel:DWORD src1_sel:WORD_1
v_mfma_f32_16x16x32_bf16 v[172:175],v[92:95],v[136:139],v[172:175]
ds_read_b64_tr_b16 v[74:75],v4 offset:18464
ds_read_b64_tr_b16 v[76:77],v5 offset:16416
v_pk_fma_f16 v121,v120,v120,s54
v_pk_fma_f16 v123,v122,v122,s54
v_mfma_f32_16x16x32_bf16 v[180:183],v[96:99],v[136:139],v[180:183]
ds_read_u16 v145,v145
v_pk_mul_f16 v121,v120,v121
v_pk_mul_f16 v123,v122,v123
v_mfma_f32_16x16x32_bf16 v[188:191],v[100:103],v[136:139],v[188:191]
ds_read_u16_d16_hi v147,v147
v_lshlrev_b32_sdwa v152,1,v152 dst_sel:DWORD dst_unused:UNUSED_PAD src0_sel:DWORD src1_sel:WORD_1
v_lshlrev_b32_sdwa v154,1,v154 dst_sel:DWORD dst_unused:UNUSED_PAD src0_sel:DWORD src1_sel:WORD_1
v_mfma_f32_16x16x32_bf16 v[196:199],v[104:107],v[136:139],v[196:199]
v_exp_f16 v120,v121
v_mfma_f32_16x16x32_bf16 v[204:207],v[108:111],v[136:139],v[204:207]
s_waitcnt lgkmcnt(10)
ds_read_b64_tr_b16 v[78:79],v5 offset:18464
ds_read_b64_tr_b16 v[80:81],v4 offset:20512
v_exp_f16 v122,v123
v_mfma_f32_16x16x32_bf16 v[128:131],v[64:67],v[16:19],v[112:115]
v_lshlrev_b32_sdwa v153,1,v153 dst_sel:DWORD dst_unused:UNUSED_PAD src0_sel:DWORD src1_sel:WORD_1
v_lshlrev_b32_sdwa v155,1,v155 dst_sel:DWORD dst_unused:UNUSED_PAD src0_sel:DWORD src1_sel:WORD_1
v_mfma_f32_16x16x32_bf16 v[136:139],v[64:67],v[20:23],v[112:115]
s_waitcnt lgkmcnt(7)
ds_read_u16 v148,v148
ds_read_u16_d16_hi v150,v150
v_exp_f16_sdwa v120,v121 dst_sel:WORD_1 dst_unused:UNUSED_PRESERVE src0_sel:WORD_1
v_mfma_f32_16x16x32_bf16 v[140:143],v[68:71],v[20:23],v[116:119]
v_exp_f16_sdwa v122,v123 dst_sel:WORD_1 dst_unused:UNUSED_PRESERVE src0_sel:WORD_1
v_mfma_f32_16x16x32_bf16 v[132:135],v[68:71],v[16:19],v[116:119]
s_waitcnt lgkmcnt(7)
ds_read_b64_tr_b16 v[82:83],v4 offset:22560
v_pk_fma_f16 v120,v120,s55,s55
v_mfma_f32_16x16x32_bf16 v[128:131],v[72:75],v[24:27],v[128:131]
ds_read_b64_tr_b16 v[84:85],v5 offset:20512
v_pk_fma_f16 v122,v122,s55,s55
v_rcp_f16 v121,v120
v_mfma_f32_16x16x32_bf16 v[136:139],v[72:75],v[28:31],v[136:139]
s_waitcnt lgkmcnt(5)
ds_read_b64_tr_b16 v[86:87],v5 offset:22560
ds_read_u16 v149,v149
v_rcp_f16 v123,v122
v_mfma_f32_16x16x32_bf16 v[140:143],v[76:79],v[28:31],v[140:143]
ds_read_u16_d16_hi v151,v151
ds_read_u16 v152,v152
v_rcp_f16_sdwa v121,v120 dst_sel:WORD_1 dst_unused:UNUSED_PRESERVE src0_sel:WORD_1
v_mfma_f32_16x16x32_bf16 v[132:135],v[76:79],v[24:27],v[132:135]
s_waitcnt lgkmcnt(5)
v_rcp_f16_sdwa v123,v122 dst_sel:WORD_1 dst_unused:UNUSED_PRESERVE src0_sel:WORD_1
v_mfma_f32_16x16x32_bf16 v[128:131],v[80:83],v[32:35],v[128:131]
ds_read_u16_d16_hi v154,v154
v_fma_mix_f32 v156,v156,v121,0 op_sel_hi:[0,1,0]
v_fma_mix_f32 v158,v158,v121,0 op_sel:[0,1,0] op_sel_hi:[0,1,0]
v_mfma_f32_16x16x32_bf16 v[136:139],v[80:83],v[36:39],v[136:139]
s_waitcnt lgkmcnt(2)
v_lshl_add_u64 v[144:145],v[144:145],0,v[146:147]
v_lshl_add_u64 v[146:147],v[148:149],0,v[150:151]
v_mfma_f32_16x16x32_bf16 v[140:143],v[84:87],v[36:39],v[140:143]
ds_read_u16 v153,v153
v_fma_mix_f32 v157,v157,v123,0 op_sel_hi:[0,1,0]
v_fma_mix_f32 v159,v159,v123,0 op_sel:[0,1,0] op_sel_hi:[0,1,0]
v_mfma_f32_16x16x32_bf16 v[132:135],v[84:87],v[32:35],v[132:135]
v_lshlrev_b32_sdwa v128,1,v128 dst_sel:DWORD dst_unused:UNUSED_PAD src0_sel:DWORD src1_sel:WORD_1
v_lshlrev_b32_sdwa v130,1,v130 dst_sel:DWORD dst_unused:UNUSED_PAD src0_sel:DWORD src1_sel:WORD_1
v_mfma_f32_16x16x32_bf16 v[208:211],v[88:91],v[144:147],v[208:211]
v_lshlrev_b32_sdwa v129,1,v129 dst_sel:DWORD dst_unused:UNUSED_PAD src0_sel:DWORD src1_sel:WORD_1
v_lshlrev_b32_sdwa v131,1,v131 dst_sel:DWORD dst_unused:UNUSED_PAD src0_sel:DWORD src1_sel:WORD_1
v_mfma_f32_16x16x32_bf16 v[216:219],v[92:95],v[144:147],v[216:219]
ds_read_u16_d16_hi v155,v155
ds_read_u16 v128,v128
v_fma_mixlo_f16 v120,v140,s99,0
v_fma_mixlo_f16 v122,v141,s99,0
v_mfma_f32_16x16x32_bf16 v[224:227],v[96:99],v[144:147],v[224:227]
ds_read_u16_d16_hi v130,v130
v_lshlrev_b32_sdwa v132,1,v132 dst_sel:DWORD dst_unused:UNUSED_PAD src0_sel:DWORD src1_sel:WORD_1
v_lshlrev_b32_sdwa v134,1,v134 dst_sel:DWORD dst_unused:UNUSED_PAD src0_sel:DWORD src1_sel:WORD_1
v_mfma_f32_16x16x32_bf16 v[232:235],v[100:103],v[144:147],v[232:235]
s_waitcnt lgkmcnt(2)
v_lshl_add_u64 v[152:153],v[152:153],0,v[154:155]
v_cvt_pk_bf16_f32 v154,v156,v158
v_mfma_f32_16x16x32_bf16 v[240:243],v[104:107],v[144:147],v[240:243]
v_fma_mixhi_f16 v120,v142,s99,0
v_cvt_pk_bf16_f32 v155,v157,v159
v_mfma_f32_16x16x32_bf16 v[248:251],v[108:111],v[144:147],v[248:251]
s_setprio 2
ds_read_u16 v129,v129
v_fma_mixhi_f16 v122,v143,s99,0
v_mfma_f32_16x16x32_bf16 v[212:215],v[88:91],v[152:155],v[212:215]
v_lshlrev_b32_sdwa v133,1,v133 dst_sel:DWORD dst_unused:UNUSED_PAD src0_sel:DWORD src1_sel:WORD_1
v_pk_fma_f16 v121,v120,v120,s54
v_lshlrev_b32_sdwa v135,1,v135 dst_sel:DWORD dst_unused:UNUSED_PAD src0_sel:DWORD src1_sel:WORD_1
v_mfma_f32_16x16x32_bf16 v[220:223],v[92:95],v[152:155],v[220:223]
ds_read_u16_d16_hi v131,v131
ds_read_u16 v132,v132
v_pk_fma_f16 v123,v122,v122,s54
v_mfma_f32_16x16x32_bf16 v[228:231],v[96:99],v[152:155],v[228:231]
ds_read_b64_tr_b16 v[88:89],v6 offset:18480
v_pk_mul_f16 v121,v120,v121
v_lshlrev_b32_sdwa v136,1,v136 dst_sel:DWORD dst_unused:UNUSED_PAD src0_sel:DWORD src1_sel:WORD_1
v_pk_mul_f16 v123,v122,v123
v_mfma_f32_16x16x32_bf16 v[236:239],v[100:103],v[152:155],v[236:239]
ds_read_b64_tr_b16 v[90:91],v6 offset:20016
v_lshlrev_b32_sdwa v138,1,v138 dst_sel:DWORD dst_unused:UNUSED_PAD src0_sel:DWORD src1_sel:WORD_1
v_mfma_f32_16x16x32_bf16 v[244:247],v[104:107],v[152:155],v[244:247]
ds_read_u16_d16_hi v134,v134
v_exp_f16 v120,v121
v_mfma_f32_16x16x32_bf16 v[252:255],v[108:111],v[152:155],v[252:255]
ds_read_b64_tr_b16 v[92:93],v6 offset:18488
v_lshlrev_b32_sdwa v137,1,v137 dst_sel:DWORD dst_unused:UNUSED_PAD src0_sel:DWORD src1_sel:WORD_1
v_mfma_f32_16x16x32_bf16 v[144:147],v[64:67],v[40:43],v[112:115]
ds_read_b64_tr_b16 v[94:95],v6 offset:20024
v_lshlrev_b32_sdwa v139,1,v139 dst_sel:DWORD dst_unused:UNUSED_PAD src0_sel:DWORD src1_sel:WORD_1
v_exp_f16 v122,v123
v_mfma_f32_16x16x32_bf16 v[152:155],v[64:67],v[44:47],v[112:115]
ds_read_u16 v133,v133
ds_read_b64_tr_b16 v[96:97],v6 offset:18544
v_mfma_f32_16x16x32_bf16 v[156:159],v[68:71],v[44:47],v[116:119]
ds_read_u16_d16_hi v135,v135
v_exp_f16_sdwa v122,v123 dst_sel:WORD_1 dst_unused:UNUSED_PRESERVE src0_sel:WORD_1
v_exp_f16_sdwa v120,v121 dst_sel:WORD_1 dst_unused:UNUSED_PRESERVE src0_sel:WORD_1
v_mfma_f32_16x16x32_bf16 v[148:151],v[68:71],v[40:43],v[116:119]
ds_read_b64_tr_b16 v[98:99],v6 offset:20080
v_pk_fma_f16 v120,v120,s55,s55
v_pk_fma_f16 v122,v122,s55,s55
v_mfma_f32_16x16x32_bf16 v[144:147],v[72:75],v[48:51],v[144:147]
v_rcp_f16 v121,v120
v_mfma_f32_16x16x32_bf16 v[152:155],v[72:75],v[52:55],v[152:155]
ds_read_b64_tr_b16 v[100:101],v6 offset:18552
v_rcp_f16 v123,v122
v_mfma_f32_16x16x32_bf16 v[156:159],v[76:79],v[52:55],v[156:159]
ds_read_u16 v136,v136
v_rcp_f16_sdwa v121,v120 dst_sel:WORD_1 dst_unused:UNUSED_PRESERVE src0_sel:WORD_1
v_mfma_f32_16x16x32_bf16 v[148:151],v[76:79],v[48:51],v[148:151]
ds_read_b64_tr_b16 v[102:103],v6 offset:20088
ds_read_b64_tr_b16 v[104:105],v6 offset:18608
v_rcp_f16_sdwa v123,v122 dst_sel:WORD_1 dst_unused:UNUSED_PRESERVE src0_sel:WORD_1
v_mfma_f32_16x16x32_bf16 v[144:147],v[80:83],v[56:59],v[144:147]
ds_read_u16_d16_hi v138,v138
v_fma_mix_f32 v142,v142,v121,0 op_sel:[0,1,0] op_sel_hi:[0,1,0]
v_mfma_f32_16x16x32_bf16 v[152:155],v[80:83],v[60:63],v[152:155]
s_waitcnt lgkmcnt(6)
ds_read_b64_tr_b16 v[106:107],v6 offset:20144
ds_read_b64_tr_b16 v[108:109],v6 offset:18616
v_fma_mix_f32 v140,v140,v121,0 op_sel_hi:[0,1,0]
v_lshl_add_u64 v[128:129],v[128:129],0,v[130:131]
v_lshl_add_u64 v[130:131],v[132:133],0,v[134:135]
v_mfma_f32_16x16x32_bf16 v[156:159],v[84:87],v[60:63],v[156:159]
ds_read_u16 v137,v137
v_fma_mix_f32 v141,v141,v123,0 op_sel_hi:[0,1,0]
v_fma_mix_f32 v143,v143,v123,0 op_sel:[0,1,0] op_sel_hi:[0,1,0]
v_mfma_f32_16x16x32_bf16 v[148:151],v[84:87],v[56:59],v[148:151]
ds_read_b64_tr_b16 v[110:111],v6 offset:20152
ds_read_b64_tr_b16 v[64:65],v4 offset:12832
v_lshlrev_b32_sdwa v144,1,v144 dst_sel:DWORD dst_unused:UNUSED_PAD src0_sel:DWORD src1_sel:WORD_1
v_lshlrev_b32_sdwa v146,1,v146 dst_sel:DWORD dst_unused:UNUSED_PAD src0_sel:DWORD src1_sel:WORD_1
v_mfma_f32_16x16x32_bf16 v[160:163],v[88:91],v[128:131],v[160:163]
ds_read_u16_d16_hi v139,v139
v_lshlrev_b32_sdwa v145,1,v145 dst_sel:DWORD dst_unused:UNUSED_PAD src0_sel:DWORD src1_sel:WORD_1
v_lshlrev_b32_sdwa v147,1,v147 dst_sel:DWORD dst_unused:UNUSED_PAD src0_sel:DWORD src1_sel:WORD_1
v_fma_mixlo_f16 v122,v157,s99,0
v_mfma_f32_16x16x32_bf16 v[168:171],v[92:95],v[128:131],v[168:171]
s_waitcnt lgkmcnt(11)
ds_read_b64_tr_b16 v[66:67],v4 offset:14880
ds_read_b128 v[112:115],v7 offset:640
v_fma_mixlo_f16 v120,v156,s99,0
v_mfma_f32_16x16x32_bf16 v[176:179],v[96:99],v[128:131],v[176:179]
s_waitcnt lgkmcnt(10)
ds_read_u16 v144,v144
v_lshlrev_b32_sdwa v148,1,v148 dst_sel:DWORD dst_unused:UNUSED_PAD src0_sel:DWORD src1_sel:WORD_1
v_lshlrev_b32_sdwa v150,1,v150 dst_sel:DWORD dst_unused:UNUSED_PAD src0_sel:DWORD src1_sel:WORD_1
v_mfma_f32_16x16x32_bf16 v[184:187],v[100:103],v[128:131],v[184:187]
s_waitcnt lgkmcnt(3)
ds_read_b64_tr_b16 v[68:69],v5 offset:12832
ds_read_b64_tr_b16 v[70:71],v5 offset:14880
v_lshl_add_u64 v[136:137],v[136:137],0,v[138:139]
v_fma_mixhi_f16 v120,v158,s99,0
v_mfma_f32_16x16x32_bf16 v[192:195],v[104:107],v[128:131],v[192:195]
ds_read_u16_d16_hi v146,v146
v_cvt_pk_bf16_f32 v138,v140,v142
v_cvt_pk_bf16_f32 v139,v141,v143
v_mfma_f32_16x16x32_bf16 v[200:203],v[108:111],v[128:131],v[200:203]
v_fma_mixhi_f16 v122,v159,s99,0
v_mfma_f32_16x16x32_bf16 v[164:167],v[88:91],v[136:139],v[164:167]
ds_read_b128 v[116:119],v7 offset:672
ds_read_b64_tr_b16 v[72:73],v4 offset:16928
v_lshlrev_b32_sdwa v149,1,v149 dst_sel:DWORD dst_unused:UNUSED_PAD src0_sel:DWORD src1_sel:WORD_1
v_lshlrev_b32_sdwa v151,1,v151 dst_sel:DWORD dst_unused:UNUSED_PAD src0_sel:DWORD src1_sel:WORD_1
v_mfma_f32_16x16x32_bf16 v[172:175],v[92:95],v[136:139],v[172:175]
s_setprio 1
ds_read_b64_tr_b16 v[74:75],v4 offset:18976
ds_read_b64_tr_b16 v[76:77],v5 offset:16928
v_pk_fma_f16 v121,v120,v120,s54
v_pk_fma_f16 v123,v122,v122,s54
v_mfma_f32_16x16x32_bf16 v[180:183],v[96:99],v[136:139],v[180:183]
ds_read_u16 v145,v145
v_pk_mul_f16 v121,v120,v121
v_pk_mul_f16 v123,v122,v123
v_mfma_f32_16x16x32_bf16 v[188:191],v[100:103],v[136:139],v[188:191]
ds_read_u16_d16_hi v147,v147
v_lshlrev_b32_sdwa v152,1,v152 dst_sel:DWORD dst_unused:UNUSED_PAD src0_sel:DWORD src1_sel:WORD_1
v_lshlrev_b32_sdwa v154,1,v154 dst_sel:DWORD dst_unused:UNUSED_PAD src0_sel:DWORD src1_sel:WORD_1
v_mfma_f32_16x16x32_bf16 v[196:199],v[104:107],v[136:139],v[196:199]
v_exp_f16 v120,v121
v_mfma_f32_16x16x32_bf16 v[204:207],v[108:111],v[136:139],v[204:207]
s_waitcnt lgkmcnt(10)
ds_read_b64_tr_b16 v[78:79],v5 offset:18976
ds_read_b64_tr_b16 v[80:81],v4 offset:21024
v_exp_f16 v122,v123
v_mfma_f32_16x16x32_bf16 v[128:131],v[64:67],v[16:19],v[112:115]
v_lshlrev_b32_sdwa v153,1,v153 dst_sel:DWORD dst_unused:UNUSED_PAD src0_sel:DWORD src1_sel:WORD_1
v_lshlrev_b32_sdwa v155,1,v155 dst_sel:DWORD dst_unused:UNUSED_PAD src0_sel:DWORD src1_sel:WORD_1
v_mfma_f32_16x16x32_bf16 v[136:139],v[64:67],v[20:23],v[112:115]
s_waitcnt lgkmcnt(7)
ds_read_u16 v148,v148
ds_read_u16_d16_hi v150,v150
v_exp_f16_sdwa v120,v121 dst_sel:WORD_1 dst_unused:UNUSED_PRESERVE src0_sel:WORD_1
v_mfma_f32_16x16x32_bf16 v[140:143],v[68:71],v[20:23],v[116:119]
v_exp_f16_sdwa v122,v123 dst_sel:WORD_1 dst_unused:UNUSED_PRESERVE src0_sel:WORD_1
v_mfma_f32_16x16x32_bf16 v[132:135],v[68:71],v[16:19],v[116:119]
s_waitcnt lgkmcnt(7)
ds_read_b64_tr_b16 v[82:83],v4 offset:23072
v_pk_fma_f16 v120,v120,s55,s55
v_mfma_f32_16x16x32_bf16 v[128:131],v[72:75],v[24:27],v[128:131]
ds_read_b64_tr_b16 v[84:85],v5 offset:21024
v_pk_fma_f16 v122,v122,s55,s55
v_rcp_f16 v121,v120
v_mfma_f32_16x16x32_bf16 v[136:139],v[72:75],v[28:31],v[136:139]
s_waitcnt lgkmcnt(5)
ds_read_b64_tr_b16 v[86:87],v5 offset:23072
ds_read_u16 v149,v149
v_rcp_f16 v123,v122
v_mfma_f32_16x16x32_bf16 v[140:143],v[76:79],v[28:31],v[140:143]
ds_read_u16_d16_hi v151,v151
ds_read_u16 v152,v152
v_rcp_f16_sdwa v121,v120 dst_sel:WORD_1 dst_unused:UNUSED_PRESERVE src0_sel:WORD_1
v_mfma_f32_16x16x32_bf16 v[132:135],v[76:79],v[24:27],v[132:135]
s_waitcnt lgkmcnt(5)
v_rcp_f16_sdwa v123,v122 dst_sel:WORD_1 dst_unused:UNUSED_PRESERVE src0_sel:WORD_1
v_mfma_f32_16x16x32_bf16 v[128:131],v[80:83],v[32:35],v[128:131]
ds_read_u16_d16_hi v154,v154
v_fma_mix_f32 v156,v156,v121,0 op_sel_hi:[0,1,0]
v_fma_mix_f32 v158,v158,v121,0 op_sel:[0,1,0] op_sel_hi:[0,1,0]
v_mfma_f32_16x16x32_bf16 v[136:139],v[80:83],v[36:39],v[136:139]
s_waitcnt lgkmcnt(2)
s_setprio 0
v_lshl_add_u64 v[144:145],v[144:145],0,v[146:147]
v_lshl_add_u64 v[146:147],v[148:149],0,v[150:151]
v_mfma_f32_16x16x32_bf16 v[140:143],v[84:87],v[36:39],v[140:143]
ds_read_u16 v153,v153
v_fma_mix_f32 v157,v157,v123,0 op_sel_hi:[0,1,0]
v_fma_mix_f32 v159,v159,v123,0 op_sel:[0,1,0] op_sel_hi:[0,1,0]
v_mfma_f32_16x16x32_bf16 v[132:135],v[84:87],v[32:35],v[132:135]
v_lshlrev_b32_sdwa v128,1,v128 dst_sel:DWORD dst_unused:UNUSED_PAD src0_sel:DWORD src1_sel:WORD_1
v_lshlrev_b32_sdwa v130,1,v130 dst_sel:DWORD dst_unused:UNUSED_PAD src0_sel:DWORD src1_sel:WORD_1
v_mfma_f32_16x16x32_bf16 v[208:211],v[88:91],v[144:147],v[208:211]
s_waitcnt vmcnt(12)
s_barrier
s_setprio 3
v_lshlrev_b32_sdwa v129,1,v129 dst_sel:DWORD dst_unused:UNUSED_PAD src0_sel:DWORD src1_sel:WORD_1
v_lshlrev_b32_sdwa v131,1,v131 dst_sel:DWORD dst_unused:UNUSED_PAD src0_sel:DWORD src1_sel:WORD_1
v_mfma_f32_16x16x32_bf16 v[216:219],v[92:95],v[144:147],v[216:219]
ds_read_u16_d16_hi v155,v155
ds_read_u16 v128,v128
v_fma_mixlo_f16 v120,v140,s99,0
v_fma_mixlo_f16 v122,v141,s99,0
v_mfma_f32_16x16x32_bf16 v[224:227],v[96:99],v[144:147],v[224:227]
s_cbranch_vccz RA65_%=
s_add_u32 m0,s58,12320
s_mul_i32 s69,s59,7
s_add_u32 s69,s69,s97
buffer_load_dwordx4 v2,s[44:47],s69 offen lds
s_add_u32 m0,m0,1024
s_add_u32 s69,s69,s53
buffer_load_dwordx4 v3,s[44:47],s69 offen lds
s_add_u32 m0,m0,1024
s_mov_b32 s86,0xbe801d5a
s_add_u32 s69,s69,s53
buffer_load_dwordx4 v2,s[44:47],s69 offen lds
s_add_u32 m0,m0,s50
s_add_u32 s69,s69,s53
buffer_load_dwordx4 v3,s[44:47],s69 offen lds
RA65_%=:
ds_read_u16_d16_hi v130,v130
v_lshlrev_b32_sdwa v132,1,v132 dst_sel:DWORD dst_unused:UNUSED_PAD src0_sel:DWORD src1_sel:WORD_1
v_lshlrev_b32_sdwa v134,1,v134 dst_sel:DWORD dst_unused:UNUSED_PAD src0_sel:DWORD src1_sel:WORD_1
v_mfma_f32_16x16x32_bf16 v[232:235],v[100:103],v[144:147],v[232:235]
s_waitcnt lgkmcnt(2)
v_lshl_add_u64 v[152:153],v[152:153],0,v[154:155]
v_cvt_pk_bf16_f32 v154,v156,v158
v_mfma_f32_16x16x32_bf16 v[240:243],v[104:107],v[144:147],v[240:243]
v_fma_mixhi_f16 v120,v142,s99,0
v_cvt_pk_bf16_f32 v155,v157,v159
v_mfma_f32_16x16x32_bf16 v[248:251],v[108:111],v[144:147],v[248:251]
s_cbranch_vccz RA66_%=
s_add_u32 m0,m0,1024
s_add_u32 s69,s69,s53
buffer_load_dwordx4 v2,s[44:47],s69 offen lds
s_add_u32 m0,m0,1024
s_add_u32 s69,s69,s53
buffer_load_dwordx4 v3,s[44:47],s69 offen lds
s_add_u32 m0,m0,s50
s_add_u32 s69,s69,s53
buffer_load_dwordx4 v2,s[44:47],s69 offen lds
s_add_u32 m0,m0,1024
s_add_u32 s69,s69,s53
buffer_load_dwordx4 v3,s[44:47],s69 offen lds
RA66_%=:
ds_read_u16 v129,v129
v_fma_mixhi_f16 v122,v143,s99,0
v_mfma_f32_16x16x32_bf16 v[212:215],v[88:91],v[152:155],v[212:215]
v_lshlrev_b32_sdwa v133,1,v133 dst_sel:DWORD dst_unused:UNUSED_PAD src0_sel:DWORD src1_sel:WORD_1
v_pk_fma_f16 v121,v120,v120,s54
v_lshlrev_b32_sdwa v135,1,v135 dst_sel:DWORD dst_unused:UNUSED_PAD src0_sel:DWORD src1_sel:WORD_1
v_mfma_f32_16x16x32_bf16 v[220:223],v[92:95],v[152:155],v[220:223]
ds_read_u16_d16_hi v131,v131
ds_read_u16 v132,v132
v_pk_fma_f16 v123,v122,v122,s54
v_mfma_f32_16x16x32_bf16 v[228:231],v[96:99],v[152:155],v[228:231]
ds_read_b64_tr_b16 v[88:89],v6 offset:0
v_pk_mul_f16 v121,v120,v121
v_lshlrev_b32_sdwa v136,1,v136 dst_sel:DWORD dst_unused:UNUSED_PAD src0_sel:DWORD src1_sel:WORD_1
v_pk_mul_f16 v123,v122,v123
v_mfma_f32_16x16x32_bf16 v[236:239],v[100:103],v[152:155],v[236:239]
s_cbranch_vccz RA67_%=
s_add_u32 m0,m0,1024
s_add_u32 s69,s69,s53
buffer_load_dwordx4 v2,s[44:47],s69 offen lds
s_add_u32 m0,m0,s50
s_add_u32 s69,s69,s53
buffer_load_dwordx4 v3,s[44:47],s69 offen lds
s_add_u32 m0,m0,1024
s_add_u32 s69,s69,s53
buffer_load_dwordx4 v2,s[44:47],s69 offen lds
s_add_u32 m0,m0,1024
s_add_u32 s69,s69,s53
buffer_load_dwordx4 v3,s[44:47],s69 offen lds
RA67_%=:
ds_read_b64_tr_b16 v[90:91],v6 offset:1536
v_lshlrev_b32_sdwa v138,1,v138 dst_sel:DWORD dst_unused:UNUSED_PAD src0_sel:DWORD src1_sel:WORD_1
v_mfma_f32_16x16x32_bf16 v[244:247],v[104:107],v[152:155],v[244:247]
ds_read_u16_d16_hi v134,v134
v_exp_f16 v120,v121
v_mfma_f32_16x16x32_bf16 v[252:255],v[108:111],v[152:155],v[252:255]
ds_read_b64_tr_b16 v[92:93],v6 offset:8
v_lshlrev_b32_sdwa v137,1,v137 dst_sel:DWORD dst_unused:UNUSED_PAD src0_sel:DWORD src1_sel:WORD_1
v_mfma_f32_16x16x32_bf16 v[144:147],v[64:67],v[40:43],v[112:115]
ds_read_b64_tr_b16 v[94:95],v6 offset:1544
v_lshlrev_b32_sdwa v139,1,v139 dst_sel:DWORD dst_unused:UNUSED_PAD src0_sel:DWORD src1_sel:WORD_1
v_exp_f16 v122,v123
v_mfma_f32_16x16x32_bf16 v[152:155],v[64:67],v[44:47],v[112:115]
s_cbranch_vccz RA68_%=
s_add_u32 m0,s100,24640
s_mul_i32 s69,s59,1
s_add_u32 s69,s69,s97
buffer_load_dwordx4 v2,s[44:47],s69 offen lds
s_add_u32 m0,m0,1024
s_add_u32 s69,s69,s53
buffer_load_dwordx4 v3,s[44:47],s69 offen lds
s_add_u32 m0,m0,1024
s_add_u32 s69,s69,s53
buffer_load_dwordx4 v2,s[44:47],s69 offen lds
s_add_u32 m0,m0,s50
s_add_u32 s69,s69,s53
buffer_load_dwordx4 v3,s[44:47],s69 offen lds
RA68_%=:
ds_read_u16 v133,v133
ds_read_b64_tr_b16 v[96:97],v6 offset:64
v_mfma_f32_16x16x32_bf16 v[156:159],v[68:71],v[44:47],v[116:119]
ds_read_u16_d16_hi v135,v135
v_exp_f16_sdwa v122,v123 dst_sel:WORD_1 dst_unused:UNUSED_PRESERVE src0_sel:WORD_1
v_exp_f16_sdwa v120,v121 dst_sel:WORD_1 dst_unused:UNUSED_PRESERVE src0_sel:WORD_1
v_mfma_f32_16x16x32_bf16 v[148:151],v[68:71],v[40:43],v[116:119]
ds_read_b64_tr_b16 v[98:99],v6 offset:1600
v_pk_fma_f16 v120,v120,s55,s55
v_pk_fma_f16 v122,v122,s55,s55
v_mfma_f32_16x16x32_bf16 v[144:147],v[72:75],v[48:51],v[144:147]
v_rcp_f16 v121,v120
v_mfma_f32_16x16x32_bf16 v[152:155],v[72:75],v[52:55],v[152:155]
s_cbranch_vccz RA69_%=
s_add_u32 m0,m0,1024
s_add_u32 s69,s69,s53
buffer_load_dwordx4 v2,s[44:47],s69 offen lds
s_add_u32 m0,m0,1024
s_add_u32 s69,s69,s53
buffer_load_dwordx4 v3,s[44:47],s69 offen lds
s_add_u32 m0,m0,s50
s_add_u32 s69,s69,s53
buffer_load_dwordx4 v2,s[44:47],s69 offen lds
s_add_u32 m0,m0,1024
s_add_u32 s69,s69,s53
buffer_load_dwordx4 v3,s[44:47],s69 offen lds
RA69_%=:
ds_read_b64_tr_b16 v[100:101],v6 offset:72
v_rcp_f16 v123,v122
v_mfma_f32_16x16x32_bf16 v[156:159],v[76:79],v[52:55],v[156:159]
ds_read_u16 v136,v136
v_rcp_f16_sdwa v121,v120 dst_sel:WORD_1 dst_unused:UNUSED_PRESERVE src0_sel:WORD_1
v_mfma_f32_16x16x32_bf16 v[148:151],v[76:79],v[48:51],v[148:151]
ds_read_b64_tr_b16 v[102:103],v6 offset:1608
ds_read_b64_tr_b16 v[104:105],v6 offset:128
v_rcp_f16_sdwa v123,v122 dst_sel:WORD_1 dst_unused:UNUSED_PRESERVE src0_sel:WORD_1
v_mfma_f32_16x16x32_bf16 v[144:147],v[80:83],v[56:59],v[144:147]
ds_read_u16_d16_hi v138,v138
v_fma_mix_f32 v142,v142,v121,0 op_sel:[0,1,0] op_sel_hi:[0,1,0]
v_mfma_f32_16x16x32_bf16 v[152:155],v[80:83],v[60:63],v[152:155]
s_waitcnt lgkmcnt(6)
s_cbranch_vccz RA70_%=
s_add_u32 m0,m0,1024
s_add_u32 s69,s69,s53
buffer_load_dwordx4 v2,s[44:47],s69 offen lds
s_add_u32 m0,m0,s50
s_add_u32 s69,s69,s53
buffer_load_dwordx4 v3,s[44:47],s69 offen lds
s_add_u32 m0,m0,1024
s_add_u32 s69,s69,s53
buffer_load_dwordx4 v2,s[44:47],s69 offen lds
s_add_u32 m0,m0,1024
s_add_u32 s69,s69,s53
buffer_load_dwordx4 v3,s[44:47],s69 offen lds
RA70_%=:
ds_read_b64_tr_b16 v[106:107],v6 offset:1664
ds_read_b64_tr_b16 v[108:109],v6 offset:136
v_fma_mix_f32 v140,v140,v121,0 op_sel_hi:[0,1,0]
v_lshl_add_u64 v[128:129],v[128:129],0,v[130:131]
v_lshl_add_u64 v[130:131],v[132:133],0,v[134:135]
v_mfma_f32_16x16x32_bf16 v[156:159],v[84:87],v[60:63],v[156:159]
ds_read_u16 v137,v137
v_fma_mix_f32 v141,v141,v123,0 op_sel_hi:[0,1,0]
v_fma_mix_f32 v143,v143,v123,0 op_sel:[0,1,0] op_sel_hi:[0,1,0]
v_mfma_f32_16x16x32_bf16 v[148:151],v[84:87],v[56:59],v[148:151]
ds_read_b64_tr_b16 v[110:111],v6 offset:1672
ds_read_b64_tr_b16 v[64:65],v4 offset:0
v_lshlrev_b32_sdwa v144,1,v144 dst_sel:DWORD dst_unused:UNUSED_PAD src0_sel:DWORD src1_sel:WORD_1
v_lshlrev_b32_sdwa v146,1,v146 dst_sel:DWORD dst_unused:UNUSED_PAD src0_sel:DWORD src1_sel:WORD_1
v_mfma_f32_16x16x32_bf16 v[160:163],v[88:91],v[128:131],v[160:163]
ds_read_u16_d16_hi v139,v139
v_lshlrev_b32_sdwa v145,1,v145 dst_sel:DWORD dst_unused:UNUSED_PAD src0_sel:DWORD src1_sel:WORD_1
v_lshlrev_b32_sdwa v147,1,v147 dst_sel:DWORD dst_unused:UNUSED_PAD src0_sel:DWORD src1_sel:WORD_1
v_fma_mixlo_f16 v122,v157,s99,0
v_mfma_f32_16x16x32_bf16 v[168:171],v[92:95],v[128:131],v[168:171]
s_waitcnt lgkmcnt(11)
ds_read_b64_tr_b16 v[66:67],v4 offset:2048
ds_read_b128 v[112:115],v7 offset:768
v_fma_mixlo_f16 v120,v156,s99,0
v_mfma_f32_16x16x32_bf16 v[176:179],v[96:99],v[128:131],v[176:179]
s_waitcnt lgkmcnt(10)
ds_read_u16 v144,v144
v_lshlrev_b32_sdwa v148,1,v148 dst_sel:DWORD dst_unused:UNUSED_PAD src0_sel:DWORD src1_sel:WORD_1
v_lshlrev_b32_sdwa v150,1,v150 dst_sel:DWORD dst_unused:UNUSED_PAD src0_sel:DWORD src1_sel:WORD_1
v_mfma_f32_16x16x32_bf16 v[184:187],v[100:103],v[128:131],v[184:187]
s_waitcnt lgkmcnt(3)
ds_read_b64_tr_b16 v[68:69],v5 offset:0
ds_read_b64_tr_b16 v[70:71],v5 offset:2048
v_lshl_add_u64 v[136:137],v[136:137],0,v[138:139]
v_fma_mixhi_f16 v120,v158,s99,0
v_mfma_f32_16x16x32_bf16 v[192:195],v[104:107],v[128:131],v[192:195]
ds_read_u16_d16_hi v146,v146
v_cvt_pk_bf16_f32 v138,v140,v142
v_cvt_pk_bf16_f32 v139,v141,v143
v_mfma_f32_16x16x32_bf16 v[200:203],v[108:111],v[128:131],v[200:203]
v_fma_mixhi_f16 v122,v159,s99,0
v_mfma_f32_16x16x32_bf16 v[164:167],v[88:91],v[136:139],v[164:167]
ds_read_b128 v[116:119],v7 offset:800
ds_read_b64_tr_b16 v[72:73],v4 offset:4096
v_lshlrev_b32_sdwa v149,1,v149 dst_sel:DWORD dst_unused:UNUSED_PAD src0_sel:DWORD src1_sel:WORD_1
v_lshlrev_b32_sdwa v151,1,v151 dst_sel:DWORD dst_unused:UNUSED_PAD src0_sel:DWORD src1_sel:WORD_1
v_mfma_f32_16x16x32_bf16 v[172:175],v[92:95],v[136:139],v[172:175]
ds_read_b64_tr_b16 v[74:75],v4 offset:6144
ds_read_b64_tr_b16 v[76:77],v5 offset:4096
v_pk_fma_f16 v121,v120,v120,s54
v_pk_fma_f16 v123,v122,v122,s54
v_mfma_f32_16x16x32_bf16 v[180:183],v[96:99],v[136:139],v[180:183]
ds_read_u16 v145,v145
v_pk_mul_f16 v121,v120,v121
v_pk_mul_f16 v123,v122,v123
v_mfma_f32_16x16x32_bf16 v[188:191],v[100:103],v[136:139],v[188:191]
ds_read_u16_d16_hi v147,v147
v_lshlrev_b32_sdwa v152,1,v152 dst_sel:DWORD dst_unused:UNUSED_PAD src0_sel:DWORD src1_sel:WORD_1
v_lshlrev_b32_sdwa v154,1,v154 dst_sel:DWORD dst_unused:UNUSED_PAD src0_sel:DWORD src1_sel:WORD_1
v_mfma_f32_16x16x32_bf16 v[196:199],v[104:107],v[136:139],v[196:199]
v_exp_f16 v120,v121
v_mfma_f32_16x16x32_bf16 v[204:207],v[108:111],v[136:139],v[204:207]
s_waitcnt lgkmcnt(10)
ds_read_b64_tr_b16 v[78:79],v5 offset:6144
ds_read_b64_tr_b16 v[80:81],v4 offset:8192
v_exp_f16 v122,v123
v_mfma_f32_16x16x32_bf16 v[128:131],v[64:67],v[16:19],v[112:115]
v_lshlrev_b32_sdwa v153,1,v153 dst_sel:DWORD dst_unused:UNUSED_PAD src0_sel:DWORD src1_sel:WORD_1
v_lshlrev_b32_sdwa v155,1,v155 dst_sel:DWORD dst_unused:UNUSED_PAD src0_sel:DWORD src1_sel:WORD_1
v_mfma_f32_16x16x32_bf16 v[136:139],v[64:67],v[20:23],v[112:115]
s_waitcnt lgkmcnt(7)
ds_read_u16 v148,v148
ds_read_u16_d16_hi v150,v150
v_exp_f16_sdwa v120,v121 dst_sel:WORD_1 dst_unused:UNUSED_PRESERVE src0_sel:WORD_1
v_mfma_f32_16x16x32_bf16 v[140:143],v[68:71],v[20:23],v[116:119]
v_exp_f16_sdwa v122,v123 dst_sel:WORD_1 dst_unused:UNUSED_PRESERVE src0_sel:WORD_1
v_mfma_f32_16x16x32_bf16 v[132:135],v[68:71],v[16:19],v[116:119]
s_waitcnt lgkmcnt(7)
ds_read_b64_tr_b16 v[82:83],v4 offset:10240
v_pk_fma_f16 v120,v120,s55,s55
v_mfma_f32_16x16x32_bf16 v[128:131],v[72:75],v[24:27],v[128:131]
ds_read_b64_tr_b16 v[84:85],v5 offset:8192
v_pk_fma_f16 v122,v122,s55,s55
v_rcp_f16 v121,v120
v_mfma_f32_16x16x32_bf16 v[136:139],v[72:75],v[28:31],v[136:139]
s_waitcnt lgkmcnt(5)
ds_read_b64_tr_b16 v[86:87],v5 offset:10240
ds_read_u16 v149,v149
v_rcp_f16 v123,v122
v_mfma_f32_16x16x32_bf16 v[140:143],v[76:79],v[28:31],v[140:143]
ds_read_u16_d16_hi v151,v151
ds_read_u16 v152,v152
v_rcp_f16_sdwa v121,v120 dst_sel:WORD_1 dst_unused:UNUSED_PRESERVE src0_sel:WORD_1
v_mfma_f32_16x16x32_bf16 v[132:135],v[76:79],v[24:27],v[132:135]
s_waitcnt lgkmcnt(5)
v_rcp_f16_sdwa v123,v122 dst_sel:WORD_1 dst_unused:UNUSED_PRESERVE src0_sel:WORD_1
v_mfma_f32_16x16x32_bf16 v[128:131],v[80:83],v[32:35],v[128:131]
ds_read_u16_d16_hi v154,v154
v_fma_mix_f32 v156,v156,v121,0 op_sel_hi:[0,1,0]
v_fma_mix_f32 v158,v158,v121,0 op_sel:[0,1,0] op_sel_hi:[0,1,0]
v_mfma_f32_16x16x32_bf16 v[136:139],v[80:83],v[36:39],v[136:139]
s_waitcnt lgkmcnt(2)
v_lshl_add_u64 v[144:145],v[144:145],0,v[146:147]
v_lshl_add_u64 v[146:147],v[148:149],0,v[150:151]
v_mfma_f32_16x16x32_bf16 v[140:143],v[84:87],v[36:39],v[140:143]
ds_read_u16 v153,v153
v_fma_mix_f32 v157,v157,v123,0 op_sel_hi:[0,1,0]
v_fma_mix_f32 v159,v159,v123,0 op_sel:[0,1,0] op_sel_hi:[0,1,0]
v_mfma_f32_16x16x32_bf16 v[132:135],v[84:87],v[32:35],v[132:135]
v_lshlrev_b32_sdwa v128,1,v128 dst_sel:DWORD dst_unused:UNUSED_PAD src0_sel:DWORD src1_sel:WORD_1
v_lshlrev_b32_sdwa v130,1,v130 dst_sel:DWORD dst_unused:UNUSED_PAD src0_sel:DWORD src1_sel:WORD_1
v_mfma_f32_16x16x32_bf16 v[208:211],v[88:91],v[144:147],v[208:211]
v_lshlrev_b32_sdwa v129,1,v129 dst_sel:DWORD dst_unused:UNUSED_PAD src0_sel:DWORD src1_sel:WORD_1
v_lshlrev_b32_sdwa v131,1,v131 dst_sel:DWORD dst_unused:UNUSED_PAD src0_sel:DWORD src1_sel:WORD_1
v_mfma_f32_16x16x32_bf16 v[216:219],v[92:95],v[144:147],v[216:219]
ds_read_u16_d16_hi v155,v155
ds_read_u16 v128,v128
v_fma_mixlo_f16 v120,v140,s99,0
v_fma_mixlo_f16 v122,v141,s99,0
v_mfma_f32_16x16x32_bf16 v[224:227],v[96:99],v[144:147],v[224:227]
s_setprio 2
ds_read_u16_d16_hi v130,v130
v_lshlrev_b32_sdwa v132,1,v132 dst_sel:DWORD dst_unused:UNUSED_PAD src0_sel:DWORD src1_sel:WORD_1
v_lshlrev_b32_sdwa v134,1,v134 dst_sel:DWORD dst_unused:UNUSED_PAD src0_sel:DWORD src1_sel:WORD_1
v_mfma_f32_16x16x32_bf16 v[232:235],v[100:103],v[144:147],v[232:235]
s_waitcnt lgkmcnt(2)
v_lshl_add_u64 v[152:153],v[152:153],0,v[154:155]
v_cvt_pk_bf16_f32 v154,v156,v158
v_mfma_f32_16x16x32_bf16 v[240:243],v[104:107],v[144:147],v[240:243]
v_fma_mixhi_f16 v120,v142,s99,0
v_cvt_pk_bf16_f32 v155,v157,v159
v_mfma_f32_16x16x32_bf16 v[248:251],v[108:111],v[144:147],v[248:251]
ds_read_u16 v129,v129
v_fma_mixhi_f16 v122,v143,s99,0
v_mfma_f32_16x16x32_bf16 v[212:215],v[88:91],v[152:155],v[212:215]
v_lshlrev_b32_sdwa v133,1,v133 dst_sel:DWORD dst_unused:UNUSED_PAD src0_sel:DWORD src1_sel:WORD_1
v_pk_fma_f16 v121,v120,v120,s54
v_lshlrev_b32_sdwa v135,1,v135 dst_sel:DWORD dst_unused:UNUSED_PAD src0_sel:DWORD src1_sel:WORD_1
v_mfma_f32_16x16x32_bf16 v[220:223],v[92:95],v[152:155],v[220:223]
ds_read_u16_d16_hi v131,v131
ds_read_u16 v132,v132
v_pk_fma_f16 v123,v122,v122,s54
v_mfma_f32_16x16x32_bf16 v[228:231],v[96:99],v[152:155],v[228:231]
ds_read_b64_tr_b16 v[88:89],v6 offset:6160
v_pk_mul_f16 v121,v120,v121
v_lshlrev_b32_sdwa v136,1,v136 dst_sel:DWORD dst_unused:UNUSED_PAD src0_sel:DWORD src1_sel:WORD_1
v_pk_mul_f16 v123,v122,v123
v_mfma_f32_16x16x32_bf16 v[236:239],v[100:103],v[152:155],v[236:239]
ds_read_b64_tr_b16 v[90:91],v6 offset:7696
v_lshlrev_b32_sdwa v138,1,v138 dst_sel:DWORD dst_unused:UNUSED_PAD src0_sel:DWORD src1_sel:WORD_1
v_mfma_f32_16x16x32_bf16 v[244:247],v[104:107],v[152:155],v[244:247]
ds_read_u16_d16_hi v134,v134
v_exp_f16 v120,v121
v_mfma_f32_16x16x32_bf16 v[252:255],v[108:111],v[152:155],v[252:255]
ds_read_b64_tr_b16 v[92:93],v6 offset:6168
v_lshlrev_b32_sdwa v137,1,v137 dst_sel:DWORD dst_unused:UNUSED_PAD src0_sel:DWORD src1_sel:WORD_1
v_mfma_f32_16x16x32_bf16 v[144:147],v[64:67],v[40:43],v[112:115]
ds_read_b64_tr_b16 v[94:95],v6 offset:7704
v_lshlrev_b32_sdwa v139,1,v139 dst_sel:DWORD dst_unused:UNUSED_PAD src0_sel:DWORD src1_sel:WORD_1
v_exp_f16 v122,v123
v_mfma_f32_16x16x32_bf16 v[152:155],v[64:67],v[44:47],v[112:115]
ds_read_u16 v133,v133
ds_read_b64_tr_b16 v[96:97],v6 offset:6224
v_mfma_f32_16x16x32_bf16 v[156:159],v[68:71],v[44:47],v[116:119]
ds_read_u16_d16_hi v135,v135
v_exp_f16_sdwa v122,v123 dst_sel:WORD_1 dst_unused:UNUSED_PRESERVE src0_sel:WORD_1
v_exp_f16_sdwa v120,v121 dst_sel:WORD_1 dst_unused:UNUSED_PRESERVE src0_sel:WORD_1
v_mfma_f32_16x16x32_bf16 v[148:151],v[68:71],v[40:43],v[116:119]
ds_read_b64_tr_b16 v[98:99],v6 offset:7760
v_pk_fma_f16 v120,v120,s55,s55
v_pk_fma_f16 v122,v122,s55,s55
v_mfma_f32_16x16x32_bf16 v[144:147],v[72:75],v[48:51],v[144:147]
v_rcp_f16 v121,v120
v_mfma_f32_16x16x32_bf16 v[152:155],v[72:75],v[52:55],v[152:155]
ds_read_b64_tr_b16 v[100:101],v6 offset:6232
v_rcp_f16 v123,v122
v_mfma_f32_16x16x32_bf16 v[156:159],v[76:79],v[52:55],v[156:159]
ds_read_u16 v136,v136
v_rcp_f16_sdwa v121,v120 dst_sel:WORD_1 dst_unused:UNUSED_PRESERVE src0_sel:WORD_1
v_mfma_f32_16x16x32_bf16 v[148:151],v[76:79],v[48:51],v[148:151]
ds_read_b64_tr_b16 v[102:103],v6 offset:7768
ds_read_b64_tr_b16 v[104:105],v6 offset:6288
v_rcp_f16_sdwa v123,v122 dst_sel:WORD_1 dst_unused:UNUSED_PRESERVE src0_sel:WORD_1
v_mfma_f32_16x16x32_bf16 v[144:147],v[80:83],v[56:59],v[144:147]
ds_read_u16_d16_hi v138,v138
v_fma_mix_f32 v142,v142,v121,0 op_sel:[0,1,0] op_sel_hi:[0,1,0]
v_mfma_f32_16x16x32_bf16 v[152:155],v[80:83],v[60:63],v[152:155]
s_waitcnt lgkmcnt(6)
ds_read_b64_tr_b16 v[106:107],v6 offset:7824
ds_read_b64_tr_b16 v[108:109],v6 offset:6296
v_fma_mix_f32 v140,v140,v121,0 op_sel_hi:[0,1,0]
v_lshl_add_u64 v[128:129],v[128:129],0,v[130:131]
v_lshl_add_u64 v[130:131],v[132:133],0,v[134:135]
v_mfma_f32_16x16x32_bf16 v[156:159],v[84:87],v[60:63],v[156:159]
ds_read_u16 v137,v137
v_fma_mix_f32 v141,v141,v123,0 op_sel_hi:[0,1,0]
v_fma_mix_f32 v143,v143,v123,0 op_sel:[0,1,0] op_sel_hi:[0,1,0]
v_mfma_f32_16x16x32_bf16 v[148:151],v[84:87],v[56:59],v[148:151]
ds_read_b64_tr_b16 v[110:111],v6 offset:7832
ds_read_b128 v[64:67],v7 offset:1536
v_lshlrev_b32_sdwa v144,1,v144 dst_sel:DWORD dst_unused:UNUSED_PAD src0_sel:DWORD src1_sel:WORD_1
v_lshlrev_b32_sdwa v146,1,v146 dst_sel:DWORD dst_unused:UNUSED_PAD src0_sel:DWORD src1_sel:WORD_1
v_mfma_f32_16x16x32_bf16 v[160:163],v[88:91],v[128:131],v[160:163]
ds_read_u16_d16_hi v139,v139
ds_read_b128 v[68:71],v7 offset:1568
v_lshlrev_b32_sdwa v145,1,v145 dst_sel:DWORD dst_unused:UNUSED_PAD src0_sel:DWORD src1_sel:WORD_1
v_lshlrev_b32_sdwa v147,1,v147 dst_sel:DWORD dst_unused:UNUSED_PAD src0_sel:DWORD src1_sel:WORD_1
v_fma_mixlo_f16 v122,v157,s99,0
v_fma_mixlo_f16 v120,v156,s99,0
v_mfma_f32_16x16x32_bf16 v[168:171],v[92:95],v[128:131],v[168:171]
s_waitcnt lgkmcnt(12)
ds_read_u16 v144,v144
ds_read_b128 v[72:75],v7 offset:1664
v_lshlrev_b32_sdwa v148,1,v148 dst_sel:DWORD dst_unused:UNUSED_PAD src0_sel:DWORD src1_sel:WORD_1
v_lshlrev_b32_sdwa v150,1,v150 dst_sel:DWORD dst_unused:UNUSED_PAD src0_sel:DWORD src1_sel:WORD_1
s_waitcnt lgkmcnt(3)
v_lshl_add_u64 v[136:137],v[136:137],0,v[138:139]
v_fma_mixhi_f16 v120,v158,s99,0
v_mfma_f32_16x16x32_bf16 v[176:179],v[96:99],v[128:131],v[176:179]
s_setprio 1
ds_read_u16_d16_hi v146,v146
ds_read_b128 v[76:79],v7 offset:1696
v_cvt_pk_bf16_f32 v138,v140,v142
v_cvt_pk_bf16_f32 v139,v141,v143
v_fma_mixhi_f16 v122,v159,s99,0
v_lshlrev_b32_sdwa v149,1,v149 dst_sel:DWORD dst_unused:UNUSED_PAD src0_sel:DWORD src1_sel:WORD_1
v_mfma_f32_16x16x32_bf16 v[184:187],v[100:103],v[128:131],v[184:187]
ds_read_u16 v145,v145
ds_read_b128 v[80:83],v7 offset:1792
v_lshlrev_b32_sdwa v151,1,v151 dst_sel:DWORD dst_unused:UNUSED_PAD src0_sel:DWORD src1_sel:WORD_1
v_pk_fma_f16 v121,v120,v120,s54
v_pk_fma_f16 v123,v122,v122,s54
v_pk_mul_f16 v121,v120,v121
v_mfma_f32_16x16x32_bf16 v[192:195],v[104:107],v[128:131],v[192:195]
ds_read_u16_d16_hi v147,v147
ds_read_b128 v[84:87],v7 offset:1824
v_pk_mul_f16 v123,v122,v123
v_lshlrev_b32_sdwa v152,1,v152 dst_sel:DWORD dst_unused:UNUSED_PAD src0_sel:DWORD src1_sel:WORD_1
v_lshlrev_b32_sdwa v154,1,v154 dst_sel:DWORD dst_unused:UNUSED_PAD src0_sel:DWORD src1_sel:WORD_1
v_exp_f16 v120,v121
v_mfma_f32_16x16x32_bf16 v[200:203],v[108:111],v[128:131],v[200:203]
ds_read_u16 v148,v148
v_exp_f16 v122,v123
v_lshlrev_b32_sdwa v153,1,v153 dst_sel:DWORD dst_unused:UNUSED_PAD src0_sel:DWORD src1_sel:WORD_1
v_mfma_f32_16x16x32_bf16 v[164:167],v[88:91],v[136:139],v[164:167]
ds_read_u16_d16_hi v150,v150
v_lshlrev_b32_sdwa v155,1,v155 dst_sel:DWORD dst_unused:UNUSED_PAD src0_sel:DWORD src1_sel:WORD_1
v_exp_f16_sdwa v120,v121 dst_sel:WORD_1 dst_unused:UNUSED_PRESERVE src0_sel:WORD_1
v_exp_f16_sdwa v122,v123 dst_sel:WORD_1 dst_unused:UNUSED_PRESERVE src0_sel:WORD_1
v_mfma_f32_16x16x32_bf16 v[172:175],v[92:95],v[136:139],v[172:175]
ds_read_u16 v149,v149
v_pk_fma_f16 v120,v120,s55,s55
v_pk_fma_f16 v122,v122,s55,s55
v_rcp_f16 v121,v120
v_mfma_f32_16x16x32_bf16 v[180:183],v[96:99],v[136:139],v[180:183]
ds_read_u16_d16_hi v151,v151
v_rcp_f16 v123,v122
v_rcp_f16_sdwa v121,v120 dst_sel:WORD_1 dst_unused:UNUSED_PRESERVE src0_sel:WORD_1
v_mfma_f32_16x16x32_bf16 v[188:191],v[100:103],v[136:139],v[188:191]
ds_read_u16 v152,v152
v_rcp_f16_sdwa v123,v122 dst_sel:WORD_1 dst_unused:UNUSED_PRESERVE src0_sel:WORD_1
v_fma_mix_f32 v156,v156,v121,0 op_sel_hi:[0,1,0]
v_mfma_f32_16x16x32_bf16 v[196:199],v[104:107],v[136:139],v[196:199]
s_setprio 0
ds_read_u16_d16_hi v154,v154
v_fma_mix_f32 v158,v158,v121,0 op_sel:[0,1,0] op_sel_hi:[0,1,0]
s_waitcnt lgkmcnt(7)
v_lshl_add_u64 v[144:145],v[144:145],0,v[146:147]
s_waitcnt lgkmcnt(2)
v_lshl_add_u64 v[146:147],v[148:149],0,v[150:151]
v_fma_mix_f32 v157,v157,v123,0 op_sel_hi:[0,1,0]
v_mfma_f32_16x16x32_bf16 v[204:207],v[108:111],v[136:139],v[204:207]
ds_read_u16 v153,v153
v_fma_mix_f32 v159,v159,v123,0 op_sel:[0,1,0] op_sel_hi:[0,1,0]
ds_read_u16_d16_hi v155,v155
s_waitcnt lgkmcnt(0)
v_lshl_add_u64 v[152:153],v[152:153],0,v[154:155]
v_cvt_pk_bf16_f32 v154,v156,v158
v_cvt_pk_bf16_f32 v155,v157,v159
v_mfma_f32_16x16x32_bf16 v[208:211],v[88:91],v[144:147],v[208:211]
s_waitcnt vmcnt(12)
s_barrier
s_setprio 0
v_xor_b32 v15,64,v10
v_mul_f32 v160,v160,v64
v_mul_f32 v161,v161,v65
v_mul_f32 v162,v162,v66
v_mul_f32 v163,v163,v67
v_mfma_f32_16x16x32_bf16 v[216:219],v[92:95],v[144:147],v[216:219]
v_mul_f32 v168,v168,v68
v_mul_f32 v169,v169,v69
v_mul_f32 v170,v170,v70
v_mul_f32 v171,v171,v71
v_cvt_pk_bf16_f32 v16,v160,v161
v_mfma_f32_16x16x32_bf16 v[224:227],v[96:99],v[144:147],v[224:227]
v_cvt_pk_bf16_f32 v17,v162,v163
v_cvt_pk_bf16_f32 v18,v168,v169
v_cvt_pk_bf16_f32 v19,v170,v171
v_mul_f32_dpp v176,v176,v72 quad_perm:[1,0,3,2] row_mask:0xf bank_mask:0xf
v_mfma_f32_16x16x32_bf16 v[232:235],v[100:103],v[144:147],v[232:235]
v_mul_f32_dpp v177,v177,v73 quad_perm:[1,0,3,2] row_mask:0xf bank_mask:0xf
v_mul_f32_dpp v178,v178,v74 quad_perm:[1,0,3,2] row_mask:0xf bank_mask:0xf
v_mul_f32_dpp v179,v179,v75 quad_perm:[1,0,3,2] row_mask:0xf bank_mask:0xf
v_mul_f32_dpp v184,v184,v76 quad_perm:[1,0,3,2] row_mask:0xf bank_mask:0xf
v_mul_f32_dpp v185,v185,v77 quad_perm:[1,0,3,2] row_mask:0xf bank_mask:0xf
v_mfma_f32_16x16x32_bf16 v[240:243],v[104:107],v[144:147],v[240:243]
v_mul_f32_dpp v186,v186,v78 quad_perm:[1,0,3,2] row_mask:0xf bank_mask:0xf
v_mul_f32_dpp v187,v187,v79 quad_perm:[1,0,3,2] row_mask:0xf bank_mask:0xf
v_cvt_pk_bf16_f32 v24,v176,v177
v_cvt_pk_bf16_f32 v25,v178,v179
v_mfma_f32_16x16x32_bf16 v[248:251],v[108:111],v[144:147],v[248:251]
v_cvt_pk_bf16_f32 v26,v184,v185
v_cvt_pk_bf16_f32 v27,v186,v187
s_mov_b64 exec,s[12:13]
v_swap_b32 v16,v24
v_swap_b32 v17,v25
v_swap_b32 v18,v26
v_swap_b32 v19,v27
s_mov_b64 exec,-1
v_mfma_f32_16x16x32_bf16 v[212:215],v[88:91],v[152:155],v[212:215]
s_add_u32 s65,s61,3145728
buffer_store_dwordx4 v[16:19],v10,s[40:43],s65 offen offset:0 sc1
v_mfma_f32_16x16x32_bf16 v[220:223],v[92:95],v[152:155],v[220:223]
v_mul_f32 v192,v192,v80
v_mul_f32 v193,v193,v81
v_mul_f32 v194,v194,v82
v_mul_f32 v195,v195,v83
v_mfma_f32_16x16x32_bf16 v[228:231],v[96:99],v[152:155],v[228:231]
v_mul_f32 v200,v200,v84
v_mul_f32 v201,v201,v85
v_mul_f32 v202,v202,v86
v_mul_f32 v203,v203,v87
v_mfma_f32_16x16x32_bf16 v[236:239],v[100:103],v[152:155],v[236:239]
v_cvt_pk_bf16_f32 v32,v192,v193
v_cvt_pk_bf16_f32 v33,v194,v195
v_cvt_pk_bf16_f32 v34,v200,v201
v_cvt_pk_bf16_f32 v35,v202,v203
v_mul_f32_dpp v164,v164,v64 quad_perm:[1,0,3,2] row_mask:0xf bank_mask:0xf
v_mfma_f32_16x16x32_bf16 v[244:247],v[104:107],v[152:155],v[244:247]
v_mul_f32_dpp v165,v165,v65 quad_perm:[1,0,3,2] row_mask:0xf bank_mask:0xf
v_mul_f32_dpp v166,v166,v66 quad_perm:[1,0,3,2] row_mask:0xf bank_mask:0xf
v_mul_f32_dpp v167,v167,v67 quad_perm:[1,0,3,2] row_mask:0xf bank_mask:0xf
v_mul_f32_dpp v172,v172,v68 quad_perm:[1,0,3,2] row_mask:0xf bank_mask:0xf
v_mfma_f32_16x16x32_bf16 v[252:255],v[108:111],v[152:155],v[252:255]
v_mul_f32_dpp v173,v173,v69 quad_perm:[1,0,3,2] row_mask:0xf bank_mask:0xf
v_mul_f32_dpp v174,v174,v70 quad_perm:[1,0,3,2] row_mask:0xf bank_mask:0xf
v_mul_f32_dpp v175,v175,v71 quad_perm:[1,0,3,2] row_mask:0xf bank_mask:0xf
v_cvt_pk_bf16_f32 v20,v164,v165
v_cvt_pk_bf16_f32 v21,v166,v167
v_cvt_pk_bf16_f32 v22,v172,v173
v_cvt_pk_bf16_f32 v23,v174,v175
s_mov_b64 exec,s[12:13]
v_swap_b32 v32,v20
v_swap_b32 v33,v21
v_swap_b32 v34,v22
v_swap_b32 v35,v23
s_mov_b64 exec,-1
v_mul_f32 v180,v180,v72
v_mul_f32 v181,v181,v73
v_mul_f32 v182,v182,v74
v_mul_f32 v183,v183,v75
v_mul_f32 v188,v188,v76
v_mul_f32 v189,v189,v77
v_mul_f32 v190,v190,v78
v_mul_f32 v191,v191,v79
v_cvt_pk_bf16_f32 v28,v180,v181
s_add_u32 s65,s61,3145728
buffer_store_dwordx4 v[24:27],v15,s[40:43],s65 offen offset:384 sc1
v_cvt_pk_bf16_f32 v29,v182,v183
v_cvt_pk_bf16_f32 v30,v188,v189
v_cvt_pk_bf16_f32 v31,v190,v191
v_mul_f32_dpp v196,v196,v80 quad_perm:[1,0,3,2] row_mask:0xf bank_mask:0xf
v_mul_f32_dpp v197,v197,v81 quad_perm:[1,0,3,2] row_mask:0xf bank_mask:0xf
v_mul_f32_dpp v198,v198,v82 quad_perm:[1,0,3,2] row_mask:0xf bank_mask:0xf
v_mul_f32_dpp v199,v199,v83 quad_perm:[1,0,3,2] row_mask:0xf bank_mask:0xf
v_mul_f32_dpp v204,v204,v84 quad_perm:[1,0,3,2] row_mask:0xf bank_mask:0xf
v_mul_f32_dpp v205,v205,v85 quad_perm:[1,0,3,2] row_mask:0xf bank_mask:0xf
v_mul_f32_dpp v206,v206,v86 quad_perm:[1,0,3,2] row_mask:0xf bank_mask:0xf
v_mul_f32_dpp v207,v207,v87 quad_perm:[1,0,3,2] row_mask:0xf bank_mask:0xf
v_cvt_pk_bf16_f32 v36,v196,v197
v_cvt_pk_bf16_f32 v37,v198,v199
v_cvt_pk_bf16_f32 v38,v204,v205
v_cvt_pk_bf16_f32 v39,v206,v207
s_mov_b64 exec,s[12:13]
v_swap_b32 v28,v36
v_swap_b32 v29,v37
v_swap_b32 v30,v38
v_swap_b32 v31,v39
s_mov_b64 exec,-1
v_mul_f32 v208,v208,v64
v_mul_f32 v209,v209,v65
v_mul_f32 v210,v210,v66
v_mul_f32 v211,v211,v67
v_mul_f32 v216,v216,v68
v_mul_f32 v217,v217,v69
v_mul_f32 v218,v218,v70
v_mul_f32 v219,v219,v71
v_cvt_pk_bf16_f32 v40,v208,v209
v_cvt_pk_bf16_f32 v41,v210,v211
v_cvt_pk_bf16_f32 v42,v216,v217
v_cvt_pk_bf16_f32 v43,v218,v219
v_mul_f32_dpp v224,v224,v72 quad_perm:[1,0,3,2] row_mask:0xf bank_mask:0xf
v_mul_f32_dpp v225,v225,v73 quad_perm:[1,0,3,2] row_mask:0xf bank_mask:0xf
v_mul_f32_dpp v226,v226,v74 quad_perm:[1,0,3,2] row_mask:0xf bank_mask:0xf
v_mul_f32_dpp v227,v227,v75 quad_perm:[1,0,3,2] row_mask:0xf bank_mask:0xf
v_mul_f32_dpp v232,v232,v76 quad_perm:[1,0,3,2] row_mask:0xf bank_mask:0xf
v_mul_f32_dpp v233,v233,v77 quad_perm:[1,0,3,2] row_mask:0xf bank_mask:0xf
v_mul_f32_dpp v234,v234,v78 quad_perm:[1,0,3,2] row_mask:0xf bank_mask:0xf
v_mul_f32_dpp v235,v235,v79 quad_perm:[1,0,3,2] row_mask:0xf bank_mask:0xf
v_cvt_pk_bf16_f32 v48,v224,v225
v_cvt_pk_bf16_f32 v49,v226,v227
v_cvt_pk_bf16_f32 v50,v232,v233
v_cvt_pk_bf16_f32 v51,v234,v235
s_mov_b64 exec,s[12:13]
v_swap_b32 v40,v48
v_swap_b32 v41,v49
v_swap_b32 v42,v50
v_swap_b32 v43,v51
s_mov_b64 exec,-1
s_add_u32 s65,s61,3145728
buffer_store_dwordx4 v[32:35],v10,s[40:43],s65 offen offset:128 sc1
v_mul_f32 v240,v240,v80
v_mul_f32 v241,v241,v81
v_mul_f32 v242,v242,v82
v_mul_f32 v243,v243,v83
v_mul_f32 v248,v248,v84
v_mul_f32 v249,v249,v85
v_mul_f32 v250,v250,v86
v_mul_f32 v251,v251,v87
v_cvt_pk_bf16_f32 v56,v240,v241
v_cvt_pk_bf16_f32 v57,v242,v243
v_cvt_pk_bf16_f32 v58,v248,v249
v_cvt_pk_bf16_f32 v59,v250,v251
v_mul_f32_dpp v212,v212,v64 quad_perm:[1,0,3,2] row_mask:0xf bank_mask:0xf
v_mul_f32_dpp v213,v213,v65 quad_perm:[1,0,3,2] row_mask:0xf bank_mask:0xf
v_mul_f32_dpp v214,v214,v66 quad_perm:[1,0,3,2] row_mask:0xf bank_mask:0xf
v_mul_f32_dpp v215,v215,v67 quad_perm:[1,0,3,2] row_mask:0xf bank_mask:0xf
v_mul_f32_dpp v220,v220,v68 quad_perm:[1,0,3,2] row_mask:0xf bank_mask:0xf
v_mul_f32_dpp v221,v221,v69 quad_perm:[1,0,3,2] row_mask:0xf bank_mask:0xf
v_mul_f32_dpp v222,v222,v70 quad_perm:[1,0,3,2] row_mask:0xf bank_mask:0xf
v_mul_f32_dpp v223,v223,v71 quad_perm:[1,0,3,2] row_mask:0xf bank_mask:0xf
v_cvt_pk_bf16_f32 v44,v212,v213
s_nop 0
s_mov_b32 s86,0xbe801d5a
v_cvt_pk_bf16_f32 v45,v214,v215
v_cvt_pk_bf16_f32 v46,v220,v221
v_cvt_pk_bf16_f32 v47,v222,v223
s_mov_b64 exec,s[12:13]
v_swap_b32 v56,v44
v_swap_b32 v57,v45
v_swap_b32 v58,v46
v_swap_b32 v59,v47
s_mov_b64 exec,-1
v_mul_f32 v228,v228,v72
v_mul_f32 v229,v229,v73
v_mul_f32 v230,v230,v74
v_mul_f32 v231,v231,v75
v_mul_f32 v236,v236,v76
v_mul_f32 v237,v237,v77
v_mul_f32 v238,v238,v78
v_mul_f32 v239,v239,v79
v_cvt_pk_bf16_f32 v52,v228,v229
v_cvt_pk_bf16_f32 v53,v230,v231
v_cvt_pk_bf16_f32 v54,v236,v237
v_cvt_pk_bf16_f32 v55,v238,v239
v_mul_f32_dpp v244,v244,v80 quad_perm:[1,0,3,2] row_mask:0xf bank_mask:0xf
v_mul_f32_dpp v245,v245,v81 quad_perm:[1,0,3,2] row_mask:0xf bank_mask:0xf
v_mul_f32_dpp v246,v246,v82 quad_perm:[1,0,3,2] row_mask:0xf bank_mask:0xf
v_mul_f32_dpp v247,v247,v83 quad_perm:[1,0,3,2] row_mask:0xf bank_mask:0xf
v_mul_f32_dpp v252,v252,v84 quad_perm:[1,0,3,2] row_mask:0xf bank_mask:0xf
v_mul_f32_dpp v253,v253,v85 quad_perm:[1,0,3,2] row_mask:0xf bank_mask:0xf
s_add_u32 s65,s61,3145728
buffer_store_dwordx4 v[20:23],v15,s[40:43],s65 offen offset:512 sc1
v_mul_f32_dpp v254,v254,v86 quad_perm:[1,0,3,2] row_mask:0xf bank_mask:0xf
v_mul_f32_dpp v255,v255,v87 quad_perm:[1,0,3,2] row_mask:0xf bank_mask:0xf
v_cvt_pk_bf16_f32 v60,v244,v245
v_cvt_pk_bf16_f32 v61,v246,v247
v_cvt_pk_bf16_f32 v62,v252,v253
v_cvt_pk_bf16_f32 v63,v254,v255
s_mov_b64 exec,s[12:13]
v_swap_b32 v52,v60
v_swap_b32 v53,v61
v_swap_b32 v54,v62
v_swap_b32 v55,v63
s_mov_b64 exec,-1
s_waitcnt vmcnt(4)
s_barrier
s_sleep 2
s_add_u32 s65,s61,3145728
buffer_store_dwordx4 v[28:31],v10,s[40:43],s65 offen offset:256 sc1
s_sleep 3
s_add_u32 s65,s61,3145728
buffer_store_dwordx4 v[36:39],v15,s[40:43],s65 offen offset:640 sc1
s_sleep 3
s_add_u32 s65,s61,3244032
buffer_store_dwordx4 v[40:43],v10,s[40:43],s65 offen offset:0 sc1
s_sleep 3
s_add_u32 s65,s61,3244032
buffer_store_dwordx4 v[48:51],v15,s[40:43],s65 offen offset:384 sc1
s_sleep 3
s_add_u32 s65,s61,3244032
buffer_store_dwordx4 v[56:59],v10,s[40:43],s65 offen offset:128 sc1
s_sleep 3
s_add_u32 s65,s61,3244032
buffer_store_dwordx4 v[44:47],v15,s[40:43],s65 offen offset:512 sc1
s_sleep 3
s_add_u32 s65,s61,3244032
buffer_store_dwordx4 v[52:55],v10,s[40:43],s65 offen offset:256 sc1
s_sleep 3
s_add_u32 s65,s61,3244032
buffer_store_dwordx4 v[60:63],v15,s[40:43],s65 offen offset:640 sc1
v_and_b32 v232,63,%1
s_getpc_b64 s[88:89]
N_itA71_%=:
s_lshl_b32 s87,s10,10
s_add_u32 s87,s87,((N_mainend_%=-N_main_%=-16)&0xff000)-12280
s_sub_u32 s88,s88,N_itA71_%=-N_main_%=
s_subb_u32 s89,s89,0
s_add_u32 s88,s88,s87
s_addc_u32 s89,s89,0
s_swappc_b64 s[90:91],s[88:89]
s_cmp_lg_u32 s52,1
s_cbranch_scc1 N_s0_%=
v_min_u32 v236,47,v232
v_lshlrev_b32 v152,4,v236
v_lshlrev_b32 v153,2,v236
v_min_u32 v236,59,v232
v_mul_u32_u24 v237,0xaaab,v236
v_lshrrev_b32 v237,19,v237
v_mul_u32_u24 v237,14592,v237
v_lshl_add_u32 v148,v236,4,v237
s_add_u32 s64,s24,0
s_addc_u32 s65,s25,0
global_load_dwordx4 v[112:115],v152,s[64:65]
s_add_u32 s64,s24,9216
s_addc_u32 s65,s25,0
global_load_dwordx4 v[116:119],v152,s[64:65]
s_add_u32 s64,s24,18432
s_addc_u32 s65,s25,0
global_load_dwordx4 v[120:123],v152,s[64:65]
s_add_u32 s64,s24,27648
s_addc_u32 s65,s25,0
global_load_dwordx4 v[124:127],v152,s[64:65]
s_add_u32 s64,s24,36864
s_addc_u32 s65,s25,0
global_load_dwordx4 v[128:131],v152,s[64:65]
s_add_u32 s64,s24,46080
s_addc_u32 s65,s25,0
global_load_dwordx4 v[132:135],v152,s[64:65]
s_add_u32 s64,s24,55296
s_addc_u32 s65,s25,0
global_load_dwordx4 v[136:139],v152,s[64:65]
s_add_u32 s64,s24,64512
s_addc_u32 s65,s25,0
global_load_dwordx4 v[140:143],v152,s[64:65]
global_load_dwordx4 v[144:147],v148,s[28:29]
global_load_dwordx4 v[148:151],v148,s[28:29] offset:3648
global_load_dwordx4 v[156:159],v152,s[26:27]
global_load_dword v152,v153,s[32:33]
global_load_dword v153,v153,s[30:31]
N_s0_%=:
s_and_b32 s64,s52,3
s_mul_i32 s64,s64,18432
s_add_u32 s64,s64,4096
v_lshl_add_u32 v255,v232,4,s64
global_load_dwordx4 v[160:163],v255,s[24:25] offset:-4096
global_load_dwordx4 v[164:167],v255,s[24:25] offset:-3072
global_load_dwordx4 v[168:171],v255,s[24:25] offset:-2048
global_load_dwordx4 v[172:175],v255,s[24:25] offset:-1024
global_load_dwordx4 v[176:179],v255,s[24:25] offset:0
global_load_dwordx4 v[180:183],v255,s[24:25] offset:1024
global_load_dwordx4 v[184:187],v255,s[24:25] offset:2048
global_load_dwordx4 v[188:191],v255,s[24:25] offset:3072
v_add_u32 v255,8192,v255
global_load_dwordx4 v[192:195],v255,s[24:25] offset:-4096
global_load_dwordx4 v[196:199],v255,s[24:25] offset:-3072
global_load_dwordx4 v[200:203],v255,s[24:25] offset:-2048
global_load_dwordx4 v[204:207],v255,s[24:25] offset:-1024
global_load_dwordx4 v[208:211],v255,s[24:25] offset:0
global_load_dwordx4 v[212:215],v255,s[24:25] offset:1024
global_load_dwordx4 v[216:219],v255,s[24:25] offset:2048
global_load_dwordx4 v[220:223],v255,s[24:25] offset:3072
v_add_u32 v255,8192,v255
global_load_dwordx4 v[224:227],v255,s[24:25] offset:-4096
global_load_dwordx4 v[228:231],v255,s[24:25] offset:-3072
s_waitcnt vmcnt(0)
v_or3_b32 v160,v160,v164,v165
v_or3_b32 v162,v162,v166,v167
v_or3_b32 v160,v160,v168,v169
v_or3_b32 v162,v162,v170,v171
v_or3_b32 v160,v160,v172,v173
v_or3_b32 v162,v162,v174,v175
v_or3_b32 v160,v160,v176,v177
v_or3_b32 v162,v162,v178,v179
v_or3_b32 v160,v160,v180,v181
v_or3_b32 v162,v162,v182,v183
v_or3_b32 v160,v160,v184,v185
v_or3_b32 v162,v162,v186,v187
v_or3_b32 v160,v160,v188,v189
v_or3_b32 v162,v162,v190,v191
v_or3_b32 v160,v160,v192,v193
v_or3_b32 v162,v162,v194,v195
v_or3_b32 v160,v160,v196,v197
v_or3_b32 v162,v162,v198,v199
v_or3_b32 v160,v160,v200,v201
v_or3_b32 v162,v162,v202,v203
v_or3_b32 v160,v160,v204,v205
v_or3_b32 v162,v162,v206,v207
v_or3_b32 v160,v160,v208,v209
v_or3_b32 v162,v162,v210,v211
v_or3_b32 v160,v160,v212,v213
v_or3_b32 v162,v162,v214,v215
v_or3_b32 v160,v160,v216,v217
v_or3_b32 v162,v162,v218,v219
v_or3_b32 v160,v160,v220,v221
v_or3_b32 v162,v162,v222,v223
v_or3_b32 v160,v160,v224,v225
v_or3_b32 v162,v162,v226,v227
v_or3_b32 v160,v160,v228,v229
v_or3_b32 v162,v162,v230,v231
v_or3_b32 v160,v160,v161,v162
v_or_b32 v160,v160,v163
s_mov_b32 s64,0x7bff7bff
v_and_b32 v160,0x40004000,v160
v_pk_mul_f16 v160,v160,s64
v_or_b32 v1,v1,v160
s_cmp_lg_u32 s52,1
s_cbranch_scc1 N_s1_%=
s_mov_b32 s65,0x7fff7fff
v_and_b32 v112,s65,v112
v_and_b32 v113,s65,v113
v_and_b32 v114,s65,v114
v_and_b32 v115,s65,v115
v_and_b32 v116,s65,v116
v_and_b32 v117,s65,v117
v_and_b32 v118,s65,v118
v_and_b32 v119,s65,v119
v_and_b32 v120,s65,v120
v_and_b32 v121,s65,v121
v_and_b32 v122,s65,v122
v_and_b32 v123,s65,v123
v_and_b32 v124,s65,v124
v_and_b32 v125,s65,v125
v_and_b32 v126,s65,v126
v_and_b32 v127,s65,v127
v_and_b32 v128,s65,v128
v_and_b32 v129,s65,v129
v_and_b32 v130,s65,v130
v_and_b32 v131,s65,v131
v_and_b32 v132,s65,v132
v_and_b32 v133,s65,v133
v_and_b32 v134,s65,v134
v_and_b32 v135,s65,v135
v_and_b32 v136,s65,v136
v_and_b32 v137,s65,v137
v_and_b32 v138,s65,v138
v_and_b32 v139,s65,v139
v_and_b32 v140,s65,v140
v_and_b32 v141,s65,v141
v_and_b32 v142,s65,v142
v_and_b32 v143,s65,v143
v_pk_max_u16 v100,v112,v116
v_pk_max_u16 v101,v113,v117
v_pk_max_u16 v102,v114,v118
v_pk_max_u16 v103,v115,v119
v_pk_max_u16 v100,v100,v120
v_pk_max_u16 v101,v101,v121
v_pk_max_u16 v102,v102,v122
v_pk_max_u16 v103,v103,v123
v_pk_max_u16 v100,v100,v124
v_pk_max_u16 v101,v101,v125
v_pk_max_u16 v102,v102,v126
v_pk_max_u16 v103,v103,v127
v_pk_max_u16 v100,v100,v128
v_pk_max_u16 v101,v101,v129
v_pk_max_u16 v102,v102,v130
v_pk_max_u16 v103,v103,v131
v_pk_max_u16 v100,v100,v132
v_pk_max_u16 v101,v101,v133
v_pk_max_u16 v102,v102,v134
v_pk_max_u16 v103,v103,v135
v_pk_max_u16 v100,v100,v136
v_pk_max_u16 v101,v101,v137
v_pk_max_u16 v102,v102,v138
v_pk_max_u16 v103,v103,v139
v_pk_max_u16 v100,v100,v140
v_pk_max_u16 v101,v101,v141
v_pk_max_u16 v102,v102,v142
v_pk_max_u16 v103,v103,v143
v_pk_min_u16 v100,v100,v101
v_pk_min_u16 v102,v102,v103
v_pk_min_u16 v100,v100,v102
v_lshrrev_b32 v101,16,v100
v_and_b32 v100,0xffff,v100
v_min_u32 v100,v100,v101
v_mov_b32 v101,0x3a00
v_cmp_lt_u32_e64 s[92:93],v100,v101
s_nop 0
s_or_b32 s64,s92,s93
v_and_b32 v152,s65,v152
v_lshrrev_b32 v101,16,v152
v_and_b32 v152,0xffff,v152
v_max_u32 v152,v152,v101
v_mov_b32 v101,0x4580
v_cmp_le_u32_e64 s[92:93],v101,v152
s_nop 0
s_or_b32 s64,s64,s92
s_or_b32 s64,s64,s93
v_and_b32 v153,s65,v153
v_and_b32 v144,s65,v144
v_pk_max_u16 v153,v153,v144
v_and_b32 v145,s65,v145
v_pk_max_u16 v153,v153,v145
v_and_b32 v146,s65,v146
v_pk_max_u16 v153,v153,v146
v_and_b32 v147,s65,v147
v_pk_max_u16 v153,v153,v147
v_and_b32 v148,s65,v148
v_pk_max_u16 v153,v153,v148
v_and_b32 v149,s65,v149
v_pk_max_u16 v153,v153,v149
v_and_b32 v150,s65,v150
v_pk_max_u16 v153,v153,v150
v_and_b32 v151,s65,v151
v_pk_max_u16 v153,v153,v151
v_lshrrev_b32 v101,16,v153
v_and_b32 v153,0xffff,v153
v_max_u32 v153,v153,v101
v_mov_b32 v101,0x5380
v_cmp_le_u32_e64 s[92:93],v101,v153
s_nop 0
s_or_b32 s64,s64,s92
s_or_b32 s64,s64,s93
v_and_b32 v156,s65,v156
v_and_b32 v157,s65,v157
v_and_b32 v158,s65,v158
v_and_b32 v159,s65,v159
v_pk_max_u16 v156,v156,v157
v_pk_max_u16 v156,v156,v158
v_pk_max_u16 v156,v156,v159
v_lshrrev_b32 v101,16,v156
v_and_b32 v156,0xffff,v156
v_max_u32 v156,v156,v101
v_mov_b32 v101,0x4280
v_cmp_le_u32_e64 s[92:93],v101,v156
s_nop 0
s_or_b32 s64,s64,s92
s_or_b32 s64,s64,s93
s_cmp_eq_u32 s64,0
s_cbranch_scc1 N_s1_%=
v_mov_b32 v1,0x7c007c00
N_s1_%=:
v_and_b32 v1,0x7c007c00,v1
v_lshrrev_b32 v2,16,v1
v_and_b32 v1,0xffff,v1
v_max_u32 v1,v1,v2
v_cmp_eq_u32 vcc,0x7c00,v1
s_or_b32 s64,vcc_lo,vcc_hi
v_mov_b32 v1,s64
v_mov_b32 v2,8192
s_mov_b64 exec,1
ds_or_b32 v2,v1 offset:3456
s_mov_b64 exec,-1
s_waitcnt lgkmcnt(0)
s_barrier
s_setprio 3
ds_read_b32 v1,v2 offset:3456
s_waitcnt lgkmcnt(0)
s_setprio 0
v_readfirstlane_b32 s64,v1
s_cmp_lg_u32 s64,0
s_cbranch_scc1 N_fallback_%=
s_endpgm
s_nop 4
N_B_%=:
v_mov_b32 v192,0x409d3ef8
v_and_b32 v248,255,%1
v_lshlrev_b32 v198,4,v248
v_lshlrev_b32 v248,19,v248
v_cmp_eq_u32_e64 s[94:95],0,v248
v_add_u32 v199,0x8000,v198
s_mov_b32 s4,0x3f000000
s_mov_b32 s5,0x3fc00000
s_mov_b32 s6,0x40200000
s_mov_b32 s7,0x40600000
s_mov_b32 s8,0x40900000
s_mov_b32 s9,0x40b00000
s_mov_b32 s10,0x40d00000
s_mov_b32 s11,0x40f00000
s_mov_b32 s12,0x3f80222e
s_mov_b32 s13,0x13089abf
s_mov_b32 s96,0xebefe000
s_mov_b32 s15,0
N_tl_%=:
v_add_u32 v237,0x10000,v248
v_mul_f32 v194,s99,v248
v_fma_f32 v196,v237,s99,-v194
v_pk_fma_f32 v[160:161],v[196:197],s[4:5],v[194:195] op_sel_hi:[0,1,0]
v_pk_fma_f32 v[162:163],v[196:197],s[6:7],v[194:195] op_sel_hi:[0,1,0]
v_pk_fma_f32 v[164:165],v[196:197],s[8:9],v[194:195] op_sel_hi:[0,1,0]
v_pk_fma_f32 v[166:167],v[196:197],s[10:11],v[194:195] op_sel_hi:[0,1,0]
v_cndmask_b32_e64 v160,v160,0,s[94:95]
v_pk_mul_f32 v[168:169],v[160:161],v[160:161]
v_pk_mul_f32 v[170:171],v[162:163],v[162:163]
v_pk_mul_f32 v[172:173],v[164:165],v[164:165]
v_pk_mul_f32 v[174:175],v[166:167],v[166:167]
v_pk_fma_f32 v[168:169],v[168:169],s[12:13],v[192:193] op_sel_hi:[1,0,0]
v_pk_fma_f32 v[170:171],v[170:171],s[12:13],v[192:193] op_sel_hi:[1,0,0]
v_pk_fma_f32 v[172:173],v[172:173],s[12:13],v[192:193] op_sel_hi:[1,0,0]
v_pk_fma_f32 v[174:175],v[174:175],s[12:13],v[192:193] op_sel_hi:[1,0,0]
v_pk_mul_f32 v[168:169],v[168:169],v[160:161]
v_pk_mul_f32 v[170:171],v[170:171],v[162:163]
v_pk_mul_f32 v[172:173],v[172:173],v[164:165]
v_pk_mul_f32 v[174:175],v[174:175],v[166:167]
v_exp_f32 v168,v168
v_exp_f32 v169,v169
v_exp_f32 v170,v170
v_exp_f32 v171,v171
v_exp_f32 v172,v172
v_exp_f32 v173,v173
v_exp_f32 v174,v174
v_exp_f32 v175,v175
v_pk_fma_f32 v[168:169],v[168:169],s[96:97],s[96:97] op_sel_hi:[1,0,0]
v_pk_fma_f32 v[170:171],v[170:171],s[96:97],s[96:97] op_sel_hi:[1,0,0]
v_pk_fma_f32 v[172:173],v[172:173],s[96:97],s[96:97] op_sel_hi:[1,0,0]
v_pk_fma_f32 v[174:175],v[174:175],s[96:97],s[96:97] op_sel_hi:[1,0,0]
v_rcp_f32 v168,v168
v_rcp_f32 v169,v169
v_rcp_f32 v170,v170
v_rcp_f32 v171,v171
v_rcp_f32 v172,v172
v_rcp_f32 v173,v173
v_rcp_f32 v174,v174
v_rcp_f32 v175,v175
v_pk_mul_f32 v[168:169],v[168:169],v[160:161]
v_pk_mul_f32 v[170:171],v[170:171],v[162:163]
v_pk_mul_f32 v[172:173],v[172:173],v[164:165]
v_pk_mul_f32 v[174:175],v[174:175],v[166:167]
v_pk_fma_f32 v[160:161],v[160:161],s[12:13],v[168:169] op_sel:[0,1,0] op_sel_hi:[1,1,1]
v_pk_fma_f32 v[162:163],v[162:163],s[12:13],v[170:171] op_sel:[0,1,0] op_sel_hi:[1,1,1]
v_pk_fma_f32 v[164:165],v[164:165],s[12:13],v[172:173] op_sel:[0,1,0] op_sel_hi:[1,1,1]
v_pk_fma_f32 v[166:167],v[166:167],s[12:13],v[174:175] op_sel:[0,1,0] op_sel_hi:[1,1,1]
v_cvt_pk_bf16_f32 v176,v168,v169
v_cvt_pk_bf16_f32 v177,v170,v171
v_cvt_pk_bf16_f32 v178,v172,v173
v_cvt_pk_bf16_f32 v179,v174,v175
v_cvt_pk_bf16_f32 v180,v160,v161
v_cvt_pk_bf16_f32 v181,v162,v163
v_cvt_pk_bf16_f32 v182,v164,v165
v_cvt_pk_bf16_f32 v183,v166,v167
ds_write_b128 v198,v[176:179] offset:0
ds_write_b128 v199,v[180:183] offset:32768
s_mov_b64 s[94:95],0
v_add_u32 v248,0x8000000,v248
v_add_u32 v198,4096,v198
v_add_u32 v199,4096,v199
s_add_u32 s15,s15,1
s_cmp_lt_u32 s15,2
s_cbranch_scc1 N_tl_%=
s_and_b32 s64,s52,3
s_lshl_b32 s10,s64,2
s_mov_b32 s8,0x0ff00ff0
s_mov_b32 s9,0x0ff00ff0
s_and_b32 s64,s52,3
s_mov_b32 s12,0xaaaaaaaa
s_mov_b32 s13,0xaaaaaaaa
s_and_b32 s64,s52,3
s_lshl_b32 s10,s64,2
s_add_u32 s64,s10,s71
v_and_b32 v233,15,v232
v_lshrrev_b32 v234,4,v232
s_mul_i32 s61,s64,98304
s_add_u32 s61,s61,s68
v_and_b32 v236,1,v234
v_lshlrev_b32 v236,1,v236
v_lshrrev_b32 v237,5,v232
v_or_b32 v235,v236,v237
s_mov_b32 s54,0x44ea44ea
s_mov_b32 s55,0x877f877f
s_mov_b32 s8,0x0ff00ff0
s_mov_b32 s9,0x0ff00ff0
v_mov_b32 v1,0
v_mov_b32 v239,0x70000
v_bfe_u32 v236,v233,1,1
v_sub_u32 v236,v235,v236
v_cmp_gt_u32_e64 s[92:93],3,v236
v_lshrrev_b32 v237,2,v233
v_lshlrev_b32 v237,1,v237
v_and_b32 v238,1,v233
v_or_b32 v237,v237,v238
v_lshl_add_u32 v236,v236,3,v237
v_lshlrev_b32 v236,4,v236
v_add_u32 v236,143488,v236
v_cndmask_b32_e64 v11,v239,v236,s[92:93]
v_and_b32 v13,3,v237
v_lshlrev_b32 v236,1,v233
v_and_b32 v236,4,v236
v_lshl_add_u32 v236,v234,3,v236
v_mov_b32 v238,0x54250310
v_mul_u32_u24 v13,0x5555,v13
v_bfe_u32 v236,v238,v236,3
v_lshl_add_u32 v236,v236,3,v237
v_lshlrev_b32 v236,4,v236
v_add_u32 v12,143488,v236
v_lshrrev_b32 v237,2,v233
v_cmp_eq_u32_e64 s[92:93],v234,v237
v_lshlrev_b32 v236,4,v233
v_add_u32 v236,10112,v236
v_cndmask_b32_e64 v14,v239,v236,s[92:93]
v_and_b32 v239,7,v233
v_lshl_add_u32 v236,v239,1,v235
v_add_u32 v237,1,v236
v_bfe_u32 v237,v237,2,2
v_mul_u32_u24 v236,192,v236
v_lshl_add_u32 v236,v237,4,v236
v_add_u32 v237,1,v239
v_bfe_u32 v237,v237,1,2
v_xor_b32 v237,v237,v234
v_mul_u32_u24 v239,384,v239
v_lshl_add_u32 v237,v237,4,v239
v_sub_u32 v237,v237,v236
v_add_u32 v8,192,v237
v_add_u32 v15,73728,v236
v_and_b32 v238,1,v234
v_lshrrev_b32 v237,1,v234
v_lshlrev_b32 v238,6,v238
v_lshl_add_u32 v238,v237,4,v238
v_add_u32 v7,8192,v238
s_and_b32 s64,s52,3
s_mul_i32 s67,s64,54
s_cmp_eq_u32 s70,31
s_cselect_b32 s66,17,99
v_add_u32 v236,s67,v232
v_add_u32 v236,108,v236
v_mul_u32_u24 v237,0xaaab,v236
v_lshrrev_b32 v237,19,v237
v_mul_u32_u24 v238,12,v237
v_sub_u32 v238,v236,v238
v_add_u32 v236,1,v237
v_bfe_u32 v236,v236,2,2
v_xor_b32 v238,v238,v236
v_mul_u32_u24 v236,192,v237
v_lshl_add_u32 v239,v238,4,v236
v_cmp_eq_u32_e64 s[92:93],s66,v237
v_mov_b32 v236,0x7fff0000
s_nop 0
v_cndmask_b32_e64 v239,v239,v236,s[92:93]
v_cndmask_b32_e64 v3,v239,v3,vcc
v_and_b32 v239,7,v233
v_mul_u32_u24 v239,384,v239
v_and_b32 v238,1,v233
v_mul_u32_u24 v238,320,v238
v_sub_u32 v239,v239,v238
s_mov_b32 s12,0xaaaaaaaa
s_mov_b32 s13,0xaaaaaaaa
v_lshl_add_u32 v10,v234,4,v239
s_mov_b64 exec,s[8:9]
v_add_u32 v10,196608,v10
s_mov_b64 exec,-1
v_lshrrev_b32 v236,3,v233
v_bfe_u32 v237,v233,2,1
v_lshl_add_u32 v237,v236,4,v237
v_lshl_add_u32 v237,v234,1,v237
v_lshlrev_b32 v237,6,v237
v_lshl_add_u32 v237,v236,4,v237
v_and_b32 v238,1,v233
v_lshl_add_u32 v237,v238,5,v237
v_bfe_u32 v238,v233,1,1
v_lshl_add_u32 v237,v238,3,v237
v_xor_b32 v238,16,v237
v_add_u32 v4,16256,v237
v_add_u32 v5,16256,v238
v_lshrrev_b32 v236,3,v233
v_bfe_u32 v237,v233,2,1
v_lshl_add_u32 v236,v237,1,v236
v_lshrrev_b32 v237,1,v234
v_lshl_add_u32 v236,v237,2,v236
v_mul_u32_u24 v236,192,v236
v_and_b32 v238,1,v234
v_mul_u32_u24 v239,3080,v238
v_add_u32 v236,v236,v239
v_and_b32 v239,3,v233
v_lshl_add_u32 v236,v239,4,v236
v_add_u32 v6,40896,v236
s_mov_b32 s42,50331648
s_mov_b32 s43,0x00020000
v_bfe_u32 v236,v232,2,3
v_mul_u32_u24 v236,768,v236
v_and_b32 v237,3,v232
v_lshrrev_b32 v238,5,v232
v_lshl_add_u32 v237,v238,2,v237
v_lshl_add_u32 v236,v237,4,v236
v_lshlrev_b32 v237,4,v232
s_bitcmp0_b32 s52,0
s_mov_b32 s58,40896
s_cselect_b32 s58,16256,s58
s_cselect_b64 s[92:93],-1,0
s_cselect_b32 s59,64,6144
s_movk_i32 s53,1024
s_cselect_b32 s53,6144,s53
s_cselect_b32 s60,0,67584
s_cselect_b32 s97,64,0
s_cselect_b32 s69,0,0x70000000
s_mov_b32 s100,73728
s_cselect_b32 s100,110688,s100
s_cselect_b32 s64,16,0
s_movk_i32 s50,1032
s_cselect_b32 s50,1024,s50
s_cselect_b32 s51,s53,-72704
v_cndmask_b32_e64 v236,v237,v236,s[92:93]
v_cndmask_b32_e64 v2,v241,v236,vcc
v_xor_b32 v236,s64,v236
v_cndmask_b32_e64 v3,v3,v236,vcc
s_mov_b32 s46,73728
s_mov_b32 s47,0x00020000
v_lshlrev_b32 v236,4,v232
v_and_b32 v237,255,%1
v_add_u32 v236,157312,v236
v_lshlrev_b32 v237,3,v237
v_add_u32 v237,157312,v237
ds_write_b128 v236,v[4:7] offset:0
ds_write_b128 v236,v[8:11] offset:1024
ds_write_b128 v236,v[12:15] offset:2048
ds_write_b64 v237,v[2:3] offset:3072
s_waitcnt lgkmcnt(0)
s_sub_u32 s36,s20,192
s_subb_u32 s37,s21,0
s_and_b32 s37,s37,0xffff
s_bitcmp0_b32 s52,0
s_cselect_b64 s[44:45],s[24:25],s[28:29]
s_and_b32 s45,s45,0xffff
s_mov_b32 s40,s34
s_and_b32 s41,s35,0xffff
s_cbranch_vccz RB1_%=
s_add_u32 m0,s58,12320
s_nop 0
buffer_load_dwordx4 v2,s[44:47],s69 offen lds
s_add_u32 m0,m0,1024
s_add_u32 s69,s69,s53
buffer_load_dwordx4 v3,s[44:47],s69 offen lds
s_add_u32 m0,m0,1024
s_add_u32 s69,s69,s53
buffer_load_dwordx4 v2,s[44:47],s69 offen lds
s_add_u32 m0,m0,s50
s_add_u32 s69,s69,s53
buffer_load_dwordx4 v3,s[44:47],s69 offen lds
s_add_u32 m0,m0,1024
s_add_u32 s69,s69,s53
buffer_load_dwordx4 v2,s[44:47],s69 offen lds
s_add_u32 m0,m0,1024
s_add_u32 s69,s69,s53
buffer_load_dwordx4 v3,s[44:47],s69 offen lds
s_add_u32 m0,m0,s50
s_add_u32 s69,s69,s51
buffer_load_dwordx4 v2,s[44:47],s69 offen lds
s_add_u32 m0,m0,1024
s_add_u32 s69,s69,s53
buffer_load_dwordx4 v3,s[44:47],s69 offen lds
s_add_u32 m0,m0,1024
s_add_u32 s69,s69,s53
buffer_load_dwordx4 v2,s[44:47],s69 offen lds
s_add_u32 m0,m0,s50
s_add_u32 s69,s69,s53
buffer_load_dwordx4 v3,s[44:47],s69 offen lds
s_add_u32 m0,m0,1024
s_add_u32 s69,s69,s53
buffer_load_dwordx4 v2,s[44:47],s69 offen lds
s_add_u32 m0,m0,1024
s_add_u32 s69,s69,s53
buffer_load_dwordx4 v3,s[44:47],s69 offen lds
RB1_%=:
s_mov_b64 exec,s[62:63]
s_add_u32 m0,s57,34880
s_add_u32 s101,s56,983040
buffer_load_dwordx4 v241,s[36:39],s101 offen lds
s_add_u32 m0,s57,38368
s_add_u32 s101,s56,1081344
buffer_load_dwordx4 v241,s[36:39],s101 offen lds
s_add_u32 m0,s57,41856
s_add_u32 s101,s56,1179648
buffer_load_dwordx4 v241,s[36:39],s101 offen lds
s_add_u32 m0,s57,45344
s_add_u32 s101,s56,1277952
buffer_load_dwordx4 v241,s[36:39],s101 offen lds
s_add_u32 m0,s57,48832
s_add_u32 s101,s56,1376256
buffer_load_dwordx4 v241,s[36:39],s101 offen lds
s_add_u32 m0,s57,52320
s_add_u32 s101,s56,1474560
buffer_load_dwordx4 v241,s[36:39],s101 offen lds
s_nop 0
s_mov_b32 s86,0xbe801d5a
s_add_u32 m0,s57,55808
s_add_u32 s101,s56,1572864
buffer_load_dwordx4 v241,s[36:39],s101 offen lds
s_add_u32 m0,s57,59296
s_add_u32 s101,s56,1671168
buffer_load_dwordx4 v241,s[36:39],s101 offen lds
s_mov_b64 exec,-1
s_waitcnt lgkmcnt(0)
s_barrier
s_setprio 0
s_cbranch_vccz RB2_%=
s_add_u32 m0,s58,0
s_mul_i32 s69,s59,1
s_add_u32 s69,s69,s97
buffer_load_dwordx4 v2,s[44:47],s69 offen lds
s_add_u32 m0,m0,1024
s_add_u32 s69,s69,s53
buffer_load_dwordx4 v3,s[44:47],s69 offen lds
s_add_u32 m0,m0,1024
s_add_u32 s69,s69,s53
buffer_load_dwordx4 v2,s[44:47],s69 offen lds
s_add_u32 m0,m0,s50
s_add_u32 s69,s69,s53
buffer_load_dwordx4 v3,s[44:47],s69 offen lds
s_add_u32 m0,m0,1024
s_add_u32 s69,s69,s53
buffer_load_dwordx4 v2,s[44:47],s69 offen lds
s_add_u32 m0,m0,1024
s_add_u32 s69,s69,s53
buffer_load_dwordx4 v3,s[44:47],s69 offen lds
s_add_u32 m0,m0,s50
s_add_u32 s69,s69,s53
buffer_load_dwordx4 v2,s[44:47],s69 offen lds
s_add_u32 m0,m0,1024
s_add_u32 s69,s69,s53
buffer_load_dwordx4 v3,s[44:47],s69 offen lds
s_add_u32 m0,m0,1024
s_add_u32 s69,s69,s53
buffer_load_dwordx4 v2,s[44:47],s69 offen lds
s_add_u32 m0,m0,s50
s_add_u32 s69,s69,s53
buffer_load_dwordx4 v3,s[44:47],s69 offen lds
s_add_u32 m0,m0,1024
s_add_u32 s69,s69,s53
buffer_load_dwordx4 v2,s[44:47],s69 offen lds
s_add_u32 m0,m0,1024
s_add_u32 s69,s69,s53
buffer_load_dwordx4 v3,s[44:47],s69 offen lds
RB2_%=:
s_getpc_b64 s[88:89]
N_itB3_%=:
s_lshl_b32 s87,s10,10
s_add_u32 s87,s87,4104
s_sub_u32 s88,s88,N_itB3_%=-N_main_%=
s_subb_u32 s89,s89,0
s_add_u32 s88,s88,s87
s_addc_u32 s89,s89,0
s_swappc_b64 s[90:91],s[88:89]
s_waitcnt lgkmcnt(0)
s_cbranch_vccnz RB4_%=
s_waitcnt vmcnt(0)
RB4_%=:
s_cbranch_vccz RB5_%=
s_waitcnt vmcnt(12)
RB5_%=:
s_barrier
v_lshlrev_b32 v253,7,v232
s_bfe_u32 s64,%0,0x20003
s_and_b32 s65,s52,3
s_lshl_b32 s65,s65,2
s_add_u32 s64,s64,s65
s_lshl_b32 s65,s64,13
v_add_u32 v252,s65,v253
s_getpc_b64 s[14:15]
N_pf_%=:
s_sub_u32 s14,s14,N_pf_%=-N_main_%=
s_subb_u32 s15,s15,0
v_min_u32 v252,N_mainend_%=-N_main_%=,v252
global_load_dword v254,v252,s[14:15]
s_cbranch_vccnz RB6_%=
s_mov_b64 exec,s[62:63]
s_add_u32 m0,s57,62784
s_add_u32 s64,s56,1769472
buffer_load_dwordx4 v2,s[36:39],s64 offen lds
s_add_u32 m0,s57,64512
s_nop 0
buffer_load_dwordx4 v3,s[36:39],s64 offen lds
s_mov_b64 exec,-1
RB6_%=:
s_cbranch_vccnz RB7_%=
s_mov_b64 exec,s[62:63]
s_add_u32 m0,s57,0
s_add_u32 s64,s56,1966080
buffer_load_dwordx4 v2,s[36:39],s64 offen lds
s_add_u32 m0,s57,1728
s_nop 0
buffer_load_dwordx4 v3,s[36:39],s64 offen lds
s_mov_b64 exec,-1
RB7_%=:
s_cbranch_vccnz RB8_%=
s_mov_b64 exec,s[62:63]
s_add_u32 m0,s57,3488
s_add_u32 s64,s56,2064384
buffer_load_dwordx4 v2,s[36:39],s64 offen lds
s_add_u32 m0,s57,5216
s_nop 0
buffer_load_dwordx4 v3,s[36:39],s64 offen lds
s_mov_b64 exec,-1
RB8_%=:
s_cbranch_vccnz RB9_%=
s_mov_b64 exec,s[62:63]
s_add_u32 m0,s57,6976
s_add_u32 s64,s56,2162688
buffer_load_dwordx4 v2,s[36:39],s64 offen lds
s_add_u32 m0,s57,8704
s_nop 0
buffer_load_dwordx4 v3,s[36:39],s64 offen lds
s_mov_b64 exec,-1
RB9_%=:
s_cbranch_vccnz RB10_%=
s_mov_b64 exec,s[62:63]
s_add_u32 m0,s57,66272
s_add_u32 s64,s56,1867776
buffer_load_dwordx4 v2,s[36:39],s64 offen lds
s_add_u32 m0,s57,68000
s_nop 0
buffer_load_dwordx4 v3,s[36:39],s64 offen lds
s_mov_b64 exec,-1
RB10_%=:
s_barrier
s_cbranch_vccz RB11_%=
s_add_u32 m0,s58,12320
s_mul_i32 s69,s59,3
s_add_u32 s69,s69,s97
buffer_load_dwordx4 v2,s[44:47],s69 offen lds
s_add_u32 m0,m0,1024
s_add_u32 s69,s69,s53
buffer_load_dwordx4 v3,s[44:47],s69 offen lds
s_add_u32 m0,m0,1024
s_add_u32 s69,s69,s53
buffer_load_dwordx4 v2,s[44:47],s69 offen lds
s_add_u32 m0,m0,s50
s_add_u32 s69,s69,s53
buffer_load_dwordx4 v3,s[44:47],s69 offen lds
RB11_%=:
s_cbranch_vccz RB12_%=
s_add_u32 m0,m0,1024
s_add_u32 s69,s69,s53
buffer_load_dwordx4 v2,s[44:47],s69 offen lds
s_add_u32 m0,m0,1024
s_add_u32 s69,s69,s53
buffer_load_dwordx4 v3,s[44:47],s69 offen lds
s_add_u32 m0,m0,s50
s_add_u32 s69,s69,s53
buffer_load_dwordx4 v2,s[44:47],s69 offen lds
s_add_u32 m0,m0,1024
s_add_u32 s69,s69,s53
buffer_load_dwordx4 v3,s[44:47],s69 offen lds
RB12_%=:
s_cbranch_vccz RB13_%=
s_add_u32 m0,m0,1024
s_add_u32 s69,s69,s53
buffer_load_dwordx4 v2,s[44:47],s69 offen lds
s_add_u32 m0,m0,s50
s_add_u32 s69,s69,s53
buffer_load_dwordx4 v3,s[44:47],s69 offen lds
s_add_u32 m0,m0,1024
s_add_u32 s69,s69,s53
buffer_load_dwordx4 v2,s[44:47],s69 offen lds
s_add_u32 m0,m0,1024
s_add_u32 s69,s69,s53
buffer_load_dwordx4 v3,s[44:47],s69 offen lds
RB13_%=:
s_waitcnt vmcnt(13)
s_barrier
s_cbranch_vccnz RB14_%=
s_mov_b64 exec,s[62:63]
s_add_u32 m0,s57,10464
s_add_u32 s64,s56,2260992
buffer_load_dwordx4 v2,s[36:39],s64 offen lds
s_add_u32 m0,s57,12192
s_nop 0
buffer_load_dwordx4 v3,s[36:39],s64 offen lds
s_mov_b64 exec,-1
RB14_%=:
s_cbranch_vccnz RB15_%=
s_mov_b64 exec,s[62:63]
s_add_u32 m0,s57,13952
s_add_u32 s64,s56,2359296
buffer_load_dwordx4 v2,s[36:39],s64 offen lds
s_add_u32 m0,s57,15680
s_nop 0
buffer_load_dwordx4 v3,s[36:39],s64 offen lds
s_mov_b64 exec,-1
RB15_%=:
s_cbranch_vccnz RB16_%=
s_mov_b64 exec,s[62:63]
s_add_u32 m0,s57,17440
s_add_u32 s64,s56,2457600
buffer_load_dwordx4 v2,s[36:39],s64 offen lds
s_add_u32 m0,s57,19168
s_nop 0
buffer_load_dwordx4 v3,s[36:39],s64 offen lds
s_mov_b64 exec,-1
RB16_%=:
s_cbranch_vccnz RB17_%=
s_mov_b64 exec,s[62:63]
s_add_u32 m0,s57,20928
s_add_u32 s64,s56,2555904
buffer_load_dwordx4 v2,s[36:39],s64 offen lds
s_add_u32 m0,s57,22656
s_nop 0
buffer_load_dwordx4 v3,s[36:39],s64 offen lds
s_mov_b64 exec,-1
RB17_%=:
s_cbranch_vccnz RB18_%=
s_mov_b64 exec,s[62:63]
s_add_u32 m0,s57,24416
s_add_u32 s64,s56,2654208
buffer_load_dwordx4 v2,s[36:39],s64 offen lds
s_add_u32 m0,s57,26144
s_nop 0
buffer_load_dwordx4 v3,s[36:39],s64 offen lds
s_mov_b64 exec,-1
RB18_%=:
s_cbranch_vccnz RB19_%=
s_mov_b64 exec,s[62:63]
s_add_u32 m0,s57,27904
s_add_u32 s64,s56,2752512
buffer_load_dwordx4 v2,s[36:39],s64 offen lds
s_add_u32 m0,s57,29632
s_nop 0
buffer_load_dwordx4 v3,s[36:39],s64 offen lds
s_mov_b64 exec,-1
RB19_%=:
s_cbranch_vccnz RB20_%=
s_mov_b64 exec,s[62:63]
s_add_u32 m0,s57,31392
s_add_u32 s64,s56,2850816
buffer_load_dwordx4 v2,s[36:39],s64 offen lds
s_add_u32 m0,s57,33120
s_nop 0
buffer_load_dwordx4 v3,s[36:39],s64 offen lds
s_mov_b64 exec,-1
RB20_%=:
s_cbranch_vccnz RB21_%=
s_mov_b64 exec,s[62:63]
s_add_u32 m0,s57,34880
s_add_u32 s64,s56,2949120
buffer_load_dwordx4 v2,s[36:39],s64 offen lds
s_add_u32 m0,s57,36608
s_nop 0
buffer_load_dwordx4 v3,s[36:39],s64 offen lds
s_mov_b64 exec,-1
RB21_%=:
s_cbranch_vccnz RB22_%=
s_mov_b64 exec,s[62:63]
s_add_u32 m0,s57,38368
s_add_u32 s64,s56,3047424
buffer_load_dwordx4 v2,s[36:39],s64 offen lds
s_add_u32 m0,s57,40096
s_nop 0
buffer_load_dwordx4 v3,s[36:39],s64 offen lds
s_mov_b64 exec,-1
RB22_%=:
s_cbranch_vccnz RB23_%=
s_mov_b64 exec,s[62:63]
s_add_u32 m0,s57,41856
s_add_u32 s64,s56,3145728
buffer_load_dwordx4 v2,s[36:39],s64 offen lds
s_add_u32 m0,s57,43584
s_nop 0
buffer_load_dwordx4 v3,s[36:39],s64 offen lds
s_mov_b64 exec,-1
RB23_%=:
s_cbranch_vccnz RB24_%=
s_mov_b64 exec,s[62:63]
s_add_u32 m0,s57,45344
s_add_u32 s64,s56,3244032
buffer_load_dwordx4 v2,s[36:39],s64 offen lds
s_add_u32 m0,s57,47072
s_nop 0
buffer_load_dwordx4 v3,s[36:39],s64 offen lds
s_mov_b64 exec,-1
RB24_%=:
s_cbranch_vccz RB25_%=
s_waitcnt vmcnt(0)
RB25_%=:
s_barrier
s_cbranch_vccz RB26_%=
s_add_u32 m0,s58,0
s_mul_i32 s69,s59,5
s_add_u32 s69,s69,s97
buffer_load_dwordx4 v2,s[44:47],s69 offen lds
s_add_u32 m0,m0,1024
s_add_u32 s69,s69,s53
buffer_load_dwordx4 v3,s[44:47],s69 offen lds
s_add_u32 m0,m0,1024
s_add_u32 s69,s69,s53
buffer_load_dwordx4 v2,s[44:47],s69 offen lds
s_add_u32 m0,m0,s50
s_add_u32 s69,s69,s53
buffer_load_dwordx4 v3,s[44:47],s69 offen lds
RB26_%=:
s_cbranch_vccz RB27_%=
s_add_u32 m0,m0,1024
s_add_u32 s69,s69,s53
buffer_load_dwordx4 v2,s[44:47],s69 offen lds
s_add_u32 m0,m0,1024
s_add_u32 s69,s69,s53
buffer_load_dwordx4 v3,s[44:47],s69 offen lds
s_add_u32 m0,m0,s50
s_add_u32 s69,s69,s53
buffer_load_dwordx4 v2,s[44:47],s69 offen lds
s_add_u32 m0,m0,1024
s_add_u32 s69,s69,s53
buffer_load_dwordx4 v3,s[44:47],s69 offen lds
RB27_%=:
s_cbranch_vccz RB28_%=
s_add_u32 m0,m0,1024
s_add_u32 s69,s69,s53
buffer_load_dwordx4 v2,s[44:47],s69 offen lds
s_add_u32 m0,m0,s50
s_add_u32 s69,s69,s53
buffer_load_dwordx4 v3,s[44:47],s69 offen lds
s_add_u32 m0,m0,1024
s_add_u32 s69,s69,s53
buffer_load_dwordx4 v2,s[44:47],s69 offen lds
s_add_u32 m0,m0,1024
s_add_u32 s69,s69,s53
buffer_load_dwordx4 v3,s[44:47],s69 offen lds
RB28_%=:
s_add_u32 s4,s10,16
s_cmp_ge_u32 s4,20
s_cselect_b32 s7,20,0
s_sub_u32 s4,s4,s7
s_mul_i32 s4,s4,3488
s_add_u32 s5,s10,18
s_cmp_ge_u32 s5,20
s_cselect_b32 s7,20,0
s_sub_u32 s5,s5,s7
s_mul_i32 s5,s5,3488
s_add_u32 s6,s10,0
s_cmp_ge_u32 s6,20
s_cselect_b32 s7,20,0
s_sub_u32 s6,s6,s7
s_mul_i32 s6,s6,3488
v_add_u32 v112,s4,v15
v_add_u32 v116,s5,v15
s_mov_b64 exec,s[8:9]
v_add_u32 v112,s5,v15
v_add_u32 v116,s6,v15
s_mov_b64 exec,-1
v_xor_b32 v113,16,v112
v_xor_b32 v117,16,v116
v_xor_b32 v114,32,v112
v_xor_b32 v118,32,v116
v_xor_b32 v115,48,v112
v_xor_b32 v119,48,v116
s_waitcnt vmcnt(0)
s_barrier
s_setprio 3
ds_read_b128 v[136:139],v116 offset:0
ds_read_b128 v[88:91],v11 offset:768
ds_read_b128 v[140:143],v116 offset:3488
ds_read_b128 v[128:131],v112 offset:0
ds_read_b128 v[132:135],v112 offset:3488
ds_read_b128 v[92:95],v12 offset:0
ds_read_b128 v[152:155],v117 offset:0
ds_read_b128 v[96:99],v11 offset:1920
ds_read_b128 v[156:159],v117 offset:3488
ds_read_b128 v[144:147],v113 offset:0
ds_read_b128 v[148:151],v113 offset:3488
ds_read_b128 v[100:103],v12 offset:1152
s_waitcnt lgkmcnt(10)
v_mfma_f32_16x16x32_bf16 v[160:163],v[88:91],v[136:139],0
s_waitcnt lgkmcnt(9)
v_mfma_f32_16x16x32_bf16 v[184:187],v[88:91],v[140:143],0
s_waitcnt lgkmcnt(6)
ds_read_b128 v[140:143],v116 offset:3552
ds_read_b128 v[88:91],v11 offset:5376
v_smfmac_f32_16x16x64_bf16 v[160:163],v[92:95],v[128:135],v13
s_cbranch_vccz RB29_%=
s_add_u32 m0,s58,12320
s_mul_i32 s69,s59,7
s_add_u32 s69,s69,s97
buffer_load_dwordx4 v2,s[44:47],s69 offen lds
s_add_u32 m0,m0,1024
s_add_u32 s69,s69,s53
buffer_load_dwordx4 v3,s[44:47],s69 offen lds
s_add_u32 m0,m0,1024
s_add_u32 s69,s69,s53
buffer_load_dwordx4 v2,s[44:47],s69 offen lds
s_add_u32 m0,m0,s50
s_add_u32 s69,s69,s53
buffer_load_dwordx4 v3,s[44:47],s69 offen lds
RB29_%=:
ds_read_b128 v[128:131],v112 offset:64
v_smfmac_f32_16x16x64_bf16 v[184:187],v[92:95],v[132:139],v13
s_waitcnt lgkmcnt(7)
ds_read_b128 v[136:139],v116 offset:64
ds_read_b128 v[132:135],v112 offset:3552
v_mfma_f32_16x16x32_bf16 v[164:167],v[96:99],v[152:155],0
s_waitcnt lgkmcnt(8)
ds_read_b128 v[92:95],v12 offset:4608
v_add_f32 v120,v160,v161
v_mul_f32 v124,v160,v160
v_fmac_f32 v124,v161,v161
v_mfma_f32_16x16x32_bf16 v[188:191],v[96:99],v[156:159],0
s_waitcnt lgkmcnt(6)
ds_read_b128 v[156:159],v117 offset:3552
ds_read_b128 v[96:99],v11 offset:6528
v_add_f32 v121,v162,v163
v_mul_f32 v125,v162,v162
v_fmac_f32 v125,v163,v163
v_smfmac_f32_16x16x64_bf16 v[164:167],v[100:103],v[144:151],v13
s_cbranch_vccz RB30_%=
s_add_u32 m0,m0,1024
s_add_u32 s69,s69,s53
buffer_load_dwordx4 v2,s[44:47],s69 offen lds
s_add_u32 m0,m0,1024
s_add_u32 s69,s69,s53
buffer_load_dwordx4 v3,s[44:47],s69 offen lds
s_add_u32 m0,m0,s50
s_add_u32 s69,s69,s53
buffer_load_dwordx4 v2,s[44:47],s69 offen lds
s_add_u32 m0,m0,1024
s_add_u32 s69,s69,s53
buffer_load_dwordx4 v3,s[44:47],s69 offen lds
RB30_%=:
ds_read_b128 v[144:147],v113 offset:64
v_add_f32 v122,v184,v185
v_mul_f32 v126,v184,v184
v_fmac_f32 v126,v185,v185
v_smfmac_f32_16x16x64_bf16 v[188:191],v[100:103],v[148:155],v13
s_waitcnt lgkmcnt(5)
ds_read_b128 v[152:155],v117 offset:64
ds_read_b128 v[148:151],v113 offset:3552
v_add_f32 v123,v186,v187
v_mul_f32 v127,v186,v186
v_fmac_f32 v127,v187,v187
v_mfma_f32_16x16x32_bf16 v[168:171],v[88:91],v[136:139],0
ds_read_b128 v[100:103],v12 offset:5760
v_add_f32 v120,v120,v164
v_fmac_f32 v124,v164,v164
v_add_f32 v120,v120,v165
v_fmac_f32 v124,v165,v165
v_mfma_f32_16x16x32_bf16 v[192:195],v[88:91],v[140:143],0
s_waitcnt lgkmcnt(6)
s_cbranch_vccz RB31_%=
s_add_u32 m0,m0,1024
s_add_u32 s69,s69,s53
buffer_load_dwordx4 v2,s[44:47],s69 offen lds
s_add_u32 m0,m0,s50
s_add_u32 s69,s69,s53
buffer_load_dwordx4 v3,s[44:47],s69 offen lds
s_add_u32 m0,m0,1024
s_add_u32 s69,s69,s53
buffer_load_dwordx4 v2,s[44:47],s69 offen lds
s_add_u32 m0,m0,1024
s_add_u32 s69,s69,s53
buffer_load_dwordx4 v3,s[44:47],s69 offen lds
RB31_%=:
ds_read_b128 v[140:143],v116 offset:3616
ds_read_b128 v[88:91],v11 offset:9984
v_add_f32 v121,v121,v166
v_fmac_f32 v125,v166,v166
v_add_f32 v121,v121,v167
v_fmac_f32 v125,v167,v167
v_smfmac_f32_16x16x64_bf16 v[168:171],v[92:95],v[128:135],v13
ds_read_b128 v[128:131],v112 offset:128
v_add_f32 v122,v122,v188
v_fmac_f32 v126,v188,v188
v_add_f32 v122,v122,v189
v_fmac_f32 v126,v189,v189
v_smfmac_f32_16x16x64_bf16 v[192:195],v[92:95],v[132:139],v13
s_waitcnt lgkmcnt(5)
ds_read_b128 v[136:139],v116 offset:128
ds_read_b128 v[132:135],v112 offset:3616
v_add_f32 v123,v123,v190
v_fmac_f32 v127,v190,v190
v_add_f32 v123,v123,v191
v_fmac_f32 v127,v191,v191
v_mfma_f32_16x16x32_bf16 v[172:175],v[96:99],v[152:155],0
ds_read_b128 v[92:95],v12 offset:9216
v_add_f32 v120,v120,v168
v_fmac_f32 v124,v168,v168
v_add_f32 v120,v120,v169
v_fmac_f32 v124,v169,v169
v_mfma_f32_16x16x32_bf16 v[196:199],v[96:99],v[156:159],0
s_waitcnt lgkmcnt(6)
ds_read_b128 v[156:159],v117 offset:3616
ds_read_b128 v[96:99],v11 offset:11136
v_add_f32 v121,v121,v170
v_fmac_f32 v125,v170,v170
v_add_f32 v121,v121,v171
v_fmac_f32 v125,v171,v171
v_smfmac_f32_16x16x64_bf16 v[172:175],v[100:103],v[144:151],v13
ds_read_b128 v[144:147],v113 offset:128
v_add_f32 v122,v122,v192
v_fmac_f32 v126,v192,v192
v_add_f32 v122,v122,v193
v_fmac_f32 v126,v193,v193
v_smfmac_f32_16x16x64_bf16 v[196:199],v[100:103],v[148:155],v13
s_waitcnt lgkmcnt(5)
ds_read_b128 v[152:155],v117 offset:128
ds_read_b128 v[148:151],v113 offset:3616
v_add_f32 v123,v123,v194
v_fmac_f32 v127,v194,v194
v_add_f32 v123,v123,v195
v_fmac_f32 v127,v195,v195
v_mfma_f32_16x16x32_bf16 v[176:179],v[88:91],v[136:139],0
ds_read_b128 v[100:103],v12 offset:10368
v_add_f32 v120,v120,v172
v_fmac_f32 v124,v172,v172
v_add_f32 v120,v120,v173
v_fmac_f32 v124,v173,v173
v_mfma_f32_16x16x32_bf16 v[200:203],v[88:91],v[140:143],0
s_waitcnt lgkmcnt(6)
ds_read_b128 v[140:143],v118 offset:3488
ds_read_b128 v[88:91],v11 offset:3072
v_add_f32 v121,v121,v174
v_fmac_f32 v125,v174,v174
v_add_f32 v121,v121,v175
v_fmac_f32 v125,v175,v175
v_smfmac_f32_16x16x64_bf16 v[176:179],v[92:95],v[128:135],v13
ds_read_b128 v[128:131],v114 offset:0
v_add_f32 v122,v122,v196
v_fmac_f32 v126,v196,v196
v_add_f32 v122,v122,v197
v_fmac_f32 v126,v197,v197
v_smfmac_f32_16x16x64_bf16 v[200:203],v[92:95],v[132:139],v13
s_waitcnt lgkmcnt(5)
ds_read_b128 v[136:139],v118 offset:0
ds_read_b128 v[132:135],v114 offset:3488
v_add_f32 v123,v123,v198
v_fmac_f32 v127,v198,v198
v_add_f32 v123,v123,v199
v_fmac_f32 v127,v199,v199
v_mfma_f32_16x16x32_bf16 v[180:183],v[96:99],v[152:155],0
ds_read_b128 v[92:95],v12 offset:2304
v_add_f32 v120,v120,v176
v_fmac_f32 v124,v176,v176
v_add_f32 v120,v120,v177
v_fmac_f32 v124,v177,v177
v_mfma_f32_16x16x32_bf16 v[204:207],v[96:99],v[156:159],0
s_waitcnt lgkmcnt(6)
ds_read_b128 v[156:159],v119 offset:3488
ds_read_b128 v[96:99],v11 offset:4224
v_add_f32 v121,v121,v178
v_fmac_f32 v125,v178,v178
v_add_f32 v121,v121,v179
v_fmac_f32 v125,v179,v179
v_smfmac_f32_16x16x64_bf16 v[180:183],v[100:103],v[144:151],v13
ds_read_b128 v[144:147],v115 offset:0
v_add_f32 v122,v122,v200
v_fmac_f32 v126,v200,v200
v_add_f32 v122,v122,v201
v_fmac_f32 v126,v201,v201
v_smfmac_f32_16x16x64_bf16 v[204:207],v[100:103],v[148:155],v13
s_waitcnt lgkmcnt(5)
ds_read_b128 v[152:155],v119 offset:0
ds_read_b128 v[148:151],v115 offset:3488
v_add_f32 v123,v123,v202
v_fmac_f32 v127,v202,v202
v_add_f32 v123,v123,v203
v_fmac_f32 v127,v203,v203
v_mfma_f32_16x16x32_bf16 v[208:211],v[88:91],v[136:139],0
ds_read_b128 v[100:103],v12 offset:3456
v_add_f32 v120,v120,v180
v_fmac_f32 v124,v180,v180
v_add_f32 v120,v120,v181
v_fmac_f32 v124,v181,v181
v_mfma_f32_16x16x32_bf16 v[232:235],v[88:91],v[140:143],0
s_waitcnt lgkmcnt(6)
ds_read_b128 v[140:143],v118 offset:3552
ds_read_b128 v[88:91],v11 offset:7680
v_add_f32 v121,v121,v182
v_fmac_f32 v125,v182,v182
v_add_f32 v121,v121,v183
v_fmac_f32 v125,v183,v183
v_smfmac_f32_16x16x64_bf16 v[208:211],v[92:95],v[128:135],v13
ds_read_b128 v[128:131],v114 offset:64
v_add_f32 v122,v122,v204
v_fmac_f32 v126,v204,v204
v_add_f32 v122,v122,v205
v_fmac_f32 v126,v205,v205
v_smfmac_f32_16x16x64_bf16 v[232:235],v[92:95],v[132:139],v13
s_waitcnt lgkmcnt(5)
ds_read_b128 v[136:139],v118 offset:64
ds_read_b128 v[132:135],v114 offset:3552
v_add_f32 v123,v123,v206
v_fmac_f32 v127,v206,v206
v_add_f32 v123,v123,v207
v_fmac_f32 v127,v207,v207
v_mfma_f32_16x16x32_bf16 v[212:215],v[96:99],v[152:155],0
ds_read_b128 v[92:95],v12 offset:6912
v_add_f32 v120,v120,v208
v_fmac_f32 v124,v208,v208
v_add_f32 v120,v120,v209
v_fmac_f32 v124,v209,v209
v_mfma_f32_16x16x32_bf16 v[236:239],v[96:99],v[156:159],0
s_waitcnt lgkmcnt(6)
s_setprio 2
ds_read_b128 v[156:159],v119 offset:3552
ds_read_b128 v[96:99],v11 offset:8832
v_add_f32 v121,v121,v210
v_fmac_f32 v125,v210,v210
v_add_f32 v121,v121,v211
v_fmac_f32 v125,v211,v211
v_smfmac_f32_16x16x64_bf16 v[212:215],v[100:103],v[144:151],v13
ds_read_b128 v[144:147],v115 offset:64
v_add_f32 v122,v122,v232
v_fmac_f32 v126,v232,v232
v_add_f32 v122,v122,v233
v_fmac_f32 v126,v233,v233
v_smfmac_f32_16x16x64_bf16 v[236:239],v[100:103],v[148:155],v13
s_waitcnt lgkmcnt(5)
ds_read_b128 v[152:155],v119 offset:64
ds_read_b128 v[148:151],v115 offset:3552
v_add_f32 v123,v123,v234
v_fmac_f32 v127,v234,v234
v_add_f32 v123,v123,v235
v_fmac_f32 v127,v235,v235
v_mfma_f32_16x16x32_bf16 v[216:219],v[88:91],v[136:139],0
ds_read_b128 v[100:103],v12 offset:8064
v_add_f32 v120,v120,v212
v_fmac_f32 v124,v212,v212
v_add_f32 v120,v120,v213
v_fmac_f32 v124,v213,v213
v_add_f32 v121,v121,v214
v_mfma_f32_16x16x32_bf16 v[240:243],v[88:91],v[140:143],0
s_waitcnt lgkmcnt(6)
ds_read_b128 v[140:143],v118 offset:3616
ds_read_b128 v[88:91],v11 offset:12288
v_fmac_f32 v125,v214,v214
v_add_f32 v121,v121,v215
v_fmac_f32 v125,v215,v215
v_add_f32 v122,v122,v236
v_smfmac_f32_16x16x64_bf16 v[216:219],v[92:95],v[128:135],v13
ds_read_b128 v[128:131],v114 offset:128
v_fmac_f32 v126,v236,v236
v_add_f32 v122,v122,v237
v_fmac_f32 v126,v237,v237
v_add_f32 v123,v123,v238
v_fmac_f32 v127,v238,v238
v_smfmac_f32_16x16x64_bf16 v[240:243],v[92:95],v[132:139],v13
s_waitcnt lgkmcnt(5)
ds_read_b128 v[136:139],v118 offset:128
ds_read_b128 v[132:135],v114 offset:3616
v_add_f32 v123,v123,v239
v_fmac_f32 v127,v239,v239
v_add_u32 v112,v112,v8
v_add_u32 v116,v116,v8
v_mfma_f32_16x16x32_bf16 v[220:223],v[96:99],v[152:155],0
ds_read_b128 v[92:95],v12 offset:11520
ds_read_b128 v[104:107],v14 offset:0
v_add_f32 v120,v120,v216
v_fmac_f32 v124,v216,v216
v_add_f32 v120,v120,v217
v_fmac_f32 v124,v217,v217
v_mfma_f32_16x16x32_bf16 v[244:247],v[96:99],v[156:159],0
s_waitcnt lgkmcnt(7)
ds_read_b128 v[108:111],v7 offset:3584
ds_read_b128 v[156:159],v119 offset:3616
v_add_f32 v121,v121,v218
v_fmac_f32 v125,v218,v218
v_add_f32 v121,v121,v219
v_fmac_f32 v125,v219,v219
v_smfmac_f32_16x16x64_bf16 v[220:223],v[100:103],v[144:151],v13
ds_read_b128 v[96:99],v11 offset:13440
ds_read_b128 v[144:147],v115 offset:128
v_add_f32 v122,v122,v240
v_fmac_f32 v126,v240,v240
v_add_f32 v122,v122,v241
v_fmac_f32 v126,v241,v241
v_smfmac_f32_16x16x64_bf16 v[244:247],v[100:103],v[148:155],v13
s_waitcnt lgkmcnt(7)
ds_read_b128 v[152:155],v119 offset:128
ds_read_b128 v[148:151],v115 offset:3616
v_add_f32 v123,v123,v242
v_fmac_f32 v127,v242,v242
v_add_f32 v123,v123,v243
v_fmac_f32 v127,v243,v243
v_mfma_f32_16x16x32_bf16 v[224:227],v[88:91],v[136:139],0
ds_read_b128 v[100:103],v12 offset:12672
v_add_f32 v120,v120,v220
v_fmac_f32 v124,v220,v220
v_add_f32 v120,v120,v221
v_fmac_f32 v124,v221,v221
v_mfma_f32_16x16x32_bf16 v[248:251],v[88:91],v[140:143],0
s_waitcnt lgkmcnt(8)
s_setprio 1
ds_read_b128 v[140:143],v116 offset:192
ds_read_b128 v[88:91],v112 offset:3616
v_add_f32 v121,v121,v222
v_fmac_f32 v125,v222,v222
v_add_f32 v121,v121,v223
v_fmac_f32 v125,v223,v223
v_smfmac_f32_16x16x64_bf16 v[224:227],v[92:95],v[128:135],v13
ds_read_b128 v[128:131],v112 offset:3488
v_add_f32 v122,v122,v244
v_fmac_f32 v126,v244,v244
v_add_f32 v122,v122,v245
v_fmac_f32 v126,v245,v245
v_smfmac_f32_16x16x64_bf16 v[248:251],v[92:95],v[132:139],v13
s_waitcnt lgkmcnt(5)
ds_read_b128 v[136:139],v116 offset:0
ds_read_b128 v[132:135],v112 offset:3680
v_add_f32 v123,v123,v246
v_fmac_f32 v127,v246,v246
v_add_f32 v123,v123,v247
v_fmac_f32 v127,v247,v247
v_mfma_f32_16x16x32_bf16 v[228:231],v[96:99],v[152:155],0
ds_read_b128 v[92:95],v112 offset:3808
v_add_f32 v120,v120,v224
v_fmac_f32 v124,v224,v224
v_add_f32 v120,v120,v225
v_fmac_f32 v124,v225,v225
v_mfma_f32_16x16x32_bf16 v[252:255],v[96:99],v[156:159],0
s_waitcnt lgkmcnt(6)
ds_read_b128 v[156:159],v116 offset:256
ds_read_b128 v[96:99],v116 offset:128
v_add_f32 v121,v121,v226
v_fmac_f32 v125,v226,v226
v_add_f32 v121,v121,v227
v_fmac_f32 v125,v227,v227
v_smfmac_f32_16x16x64_bf16 v[228:231],v[100:103],v[144:151],v13
ds_read_b128 v[144:147],v112 offset:3552
v_add_f32 v122,v122,v248
v_fmac_f32 v126,v248,v248
v_add_f32 v122,v122,v249
v_fmac_f32 v126,v249,v249
v_smfmac_f32_16x16x64_bf16 v[252:255],v[100:103],v[148:155],v13
s_setprio 0
ds_read_b128 v[152:155],v116 offset:64
ds_read_b128 v[148:151],v112 offset:3744
v_add_f32 v123,v123,v250
v_fmac_f32 v127,v250,v250
v_add_f32 v123,v123,v251
v_fmac_f32 v127,v251,v251
ds_read_b128 v[100:103],v116 offset:320
v_add_f32 v120,v120,v228
s_mov_b32 s86,0xbe801d5a
v_fmac_f32 v124,v228,v228
v_add_f32 v120,v120,v229
v_fmac_f32 v124,v229,v229
v_add_f32 v121,v121,v230
v_fmac_f32 v125,v230,v230
v_add_f32 v121,v121,v231
v_fmac_f32 v125,v231,v231
s_waitcnt lgkmcnt(0)
s_waitcnt vmcnt(0)
s_barrier
s_setprio 0
v_add_f32 v122,v122,v252
v_fmac_f32 v126,v252,v252
v_add_f32 v122,v122,v253
v_fmac_f32 v126,v253,v253
v_add_f32 v123,v123,v254
s_cbranch_vccz RB32_%=
s_add_u32 m0,s58,0
s_mul_i32 s69,s59,9
s_add_u32 s69,s69,s97
buffer_load_dwordx4 v2,s[44:47],s69 offen lds
s_add_u32 m0,m0,1024
s_add_u32 s69,s69,s53
buffer_load_dwordx4 v3,s[44:47],s69 offen lds
s_add_u32 m0,m0,1024
s_add_u32 s69,s69,s53
buffer_load_dwordx4 v2,s[44:47],s69 offen lds
s_add_u32 m0,m0,s50
s_add_u32 s69,s69,s53
buffer_load_dwordx4 v3,s[44:47],s69 offen lds
RB32_%=:
v_fmac_f32 v127,v254,v254
v_add_f32 v123,v123,v255
v_fmac_f32 v127,v255,v255
s_setprio 3
ds_read_b128 v[56:59],v14 offset:256
ds_read_b128 v[60:63],v7 offset:3616
s_setprio 0
v_permlane32_swap_b32 v120,v122
v_permlane32_swap_b32 v121,v123
v_permlane32_swap_b32 v124,v126
v_permlane32_swap_b32 v125,v127
v_add_f32 v120,v120,v122
v_add_f32 v121,v121,v123
v_add_f32 v124,v124,v126
v_add_f32 v125,v125,v127
s_cbranch_vccz RB33_%=
s_add_u32 m0,m0,1024
s_add_u32 s69,s69,s53
buffer_load_dwordx4 v2,s[44:47],s69 offen lds
s_add_u32 m0,m0,1024
s_add_u32 s69,s69,s53
buffer_load_dwordx4 v3,s[44:47],s69 offen lds
s_add_u32 m0,m0,s50
s_add_u32 s69,s69,s53
buffer_load_dwordx4 v2,s[44:47],s69 offen lds
s_add_u32 m0,m0,1024
s_add_u32 s69,s69,s53
buffer_load_dwordx4 v3,s[44:47],s69 offen lds
RB33_%=:
v_permlane16_swap_b32 v120,v121
v_permlane16_swap_b32 v124,v125
v_add_f32 v113,v120,v121
v_add_f32 v114,v124,v125
v_mul_f32 v113,0x3c2aaaab,v113
v_mul_f32 v114,0x3c2aaaab,v114
v_mul_f32 v115,0x5e000000,v114
v_fma_f32 v114,-v113,v113,v114
v_pk_maximum3_f16 v1,v1,v115,v115 op_sel:[0,1,1] op_sel_hi:[1,1,1]
v_max_f32 v114,0,v114
v_add_f32 v114,0x3727c5ac,v114
v_rsq_f32 v114,v114
s_cbranch_vccz RB34_%=
s_add_u32 m0,m0,1024
s_add_u32 s69,s69,s53
buffer_load_dwordx4 v2,s[44:47],s69 offen lds
s_add_u32 m0,m0,s50
s_add_u32 s69,s69,s53
buffer_load_dwordx4 v3,s[44:47],s69 offen lds
s_add_u32 m0,m0,1024
s_add_u32 s69,s69,s53
buffer_load_dwordx4 v2,s[44:47],s69 offen lds
s_add_u32 m0,m0,1024
s_add_u32 s69,s69,s53
buffer_load_dwordx4 v3,s[44:47],s69 offen lds
RB34_%=:
v_mul_f32 v120,0x8befe000,v114
v_mul_f32 v121,0x8befe000,v114
v_mul_f32 v124,-v113,v120
v_mul_f32 v125,-v113,v120
v_permlane16_swap_b32 v120,v121
s_nop 0
v_permlane16_swap_b32 v124,v125
v_mov_b32 v122,v120
v_mov_b32 v123,v121
v_mov_b32 v126,v124
v_mov_b32 v127,v125
v_permlane32_swap_b32 v120,v122
v_permlane32_swap_b32 v124,v126
v_permlane32_swap_b32 v121,v123
v_permlane32_swap_b32 v125,v127
v_fma_f32 v160,v160,v120,v124
s_setprio 3
ds_read_b64_tr_b16 v[64:65],v4 offset:12832
s_setprio 0
v_fma_f32 v161,v161,v120,v124
s_setprio 3
ds_read_b64_tr_b16 v[66:67],v4 offset:14880
s_setprio 0
v_fma_f32 v162,v162,v121,v125
s_setprio 3
ds_read_b128 v[112:115],v7 offset:1152
s_setprio 0
v_fma_f32 v163,v163,v121,v125
s_setprio 3
ds_read_b64_tr_b16 v[68:69],v5 offset:12832
s_setprio 0
v_fma_f32 v164,v164,v120,v124
s_setprio 3
ds_read_b64_tr_b16 v[70:71],v5 offset:14880
s_setprio 0
v_fma_f32 v165,v165,v120,v124
s_setprio 3
ds_read_b128 v[116:119],v7 offset:1184
s_setprio 0
v_fma_f32 v166,v166,v121,v125
s_setprio 3
ds_read_b64_tr_b16 v[72:73],v4 offset:16928
s_setprio 0
v_fma_f32 v167,v167,v121,v125
s_setprio 3
ds_read_b64_tr_b16 v[74:75],v4 offset:18976
s_setprio 0
v_fma_f32 v208,v208,v120,v124
s_setprio 3
ds_read_b64_tr_b16 v[76:77],v5 offset:16928
s_setprio 0
v_fma_f32 v209,v209,v120,v124
s_setprio 3
ds_read_b64_tr_b16 v[78:79],v5 offset:18976
s_setprio 0
v_fma_f32 v210,v210,v121,v125
s_setprio 3
ds_read_b64_tr_b16 v[80:81],v4 offset:21024
s_setprio 0
v_fma_f32 v211,v211,v121,v125
s_setprio 3
ds_read_b64_tr_b16 v[82:83],v4 offset:23072
s_setprio 0
v_fma_f32 v212,v212,v120,v124
s_setprio 3
ds_read_b64_tr_b16 v[84:85],v5 offset:21024
s_setprio 0
v_fma_f32 v213,v213,v120,v124
s_setprio 3
ds_read_b64_tr_b16 v[86:87],v5 offset:23072
s_setprio 0
v_fma_f32 v214,v214,v121,v125
v_fma_f32 v215,v215,v121,v125
v_cvt_pk_bf16_f32 v16,v160,v161
v_cvt_pk_bf16_f32 v20,v162,v163
v_cvt_pk_bf16_f32 v17,v164,v165
v_cvt_pk_bf16_f32 v21,v166,v167
v_cvt_pk_bf16_f32 v18,v208,v209
v_cvt_pk_bf16_f32 v22,v210,v211
v_cvt_pk_bf16_f32 v19,v212,v213
v_cvt_pk_bf16_f32 v23,v214,v215
v_fma_f32 v168,v168,v120,v124
v_fma_f32 v169,v169,v120,v124
v_mfma_f32_16x16x32_bf16 v[160:163],v[104:107],v[128:131],v[108:111]
v_fma_f32 v170,v170,v121,v125
v_fma_f32 v171,v171,v121,v125
v_fma_f32 v172,v172,v120,v124
v_fma_f32 v173,v173,v120,v124
v_fma_f32 v174,v174,v121,v125
v_fma_f32 v175,v175,v121,v125
v_mfma_f32_16x16x32_bf16 v[164:167],v[104:107],v[132:135],v[108:111]
v_fma_f32 v216,v216,v120,v124
v_fma_f32 v217,v217,v120,v124
v_fma_f32 v218,v218,v121,v125
v_fma_f32 v219,v219,v121,v125
v_fma_f32 v220,v220,v120,v124
v_fma_f32 v221,v221,v120,v124
v_mfma_f32_16x16x32_bf16 v[208:211],v[104:107],v[136:139],v[108:111]
v_fma_f32 v222,v222,v121,v125
v_fma_f32 v223,v223,v121,v125
v_cvt_pk_bf16_f32 v24,v168,v169
v_cvt_pk_bf16_f32 v28,v170,v171
v_cvt_pk_bf16_f32 v25,v172,v173
v_cvt_pk_bf16_f32 v29,v174,v175
s_setprio 3
v_mfma_f32_16x16x32_bf16 v[212:215],v[104:107],v[140:143],v[108:111]
ds_read_b128 v[104:107],v14 offset:512
s_setprio 0
v_cvt_pk_bf16_f32 v26,v216,v217
v_cvt_pk_bf16_f32 v30,v218,v219
v_cvt_pk_bf16_f32 v27,v220,v221
v_cvt_pk_bf16_f32 v31,v222,v223
v_fma_f32 v176,v176,v120,v124
v_fma_f32 v177,v177,v120,v124
s_waitcnt lgkmcnt(15)
s_setprio 3
v_mfma_f32_16x16x32_bf16 v[168:171],v[56:59],v[128:131],v[60:63]
ds_read_b128 v[108:111],v7 offset:3712
s_setprio 0
v_fma_f32 v178,v178,v121,v125
v_fma_f32 v179,v179,v121,v125
v_fma_f32 v180,v180,v120,v124
v_fma_f32 v181,v181,v120,v124
v_fma_f32 v182,v182,v121,v125
v_fma_f32 v183,v183,v121,v125
v_mfma_f32_16x16x32_bf16 v[172:175],v[56:59],v[132:135],v[60:63]
v_fma_f32 v224,v224,v120,v124
v_fma_f32 v225,v225,v120,v124
v_fma_f32 v226,v226,v121,v125
v_fma_f32 v227,v227,v121,v125
v_fma_f32 v228,v228,v120,v124
v_fma_f32 v229,v229,v120,v124
v_mfma_f32_16x16x32_bf16 v[216:219],v[56:59],v[136:139],v[60:63]
v_fma_f32 v230,v230,v121,v125
v_fma_f32 v231,v231,v121,v125
v_cvt_pk_bf16_f32 v32,v176,v177
v_cvt_pk_bf16_f32 v36,v178,v179
v_cvt_pk_bf16_f32 v33,v180,v181
v_cvt_pk_bf16_f32 v37,v182,v183
s_setprio 3
v_mfma_f32_16x16x32_bf16 v[220:223],v[56:59],v[140:143],v[60:63]
ds_read_b128 v[56:59],v14 offset:768
s_setprio 0
v_cvt_pk_bf16_f32 v34,v224,v225
v_cvt_pk_bf16_f32 v38,v226,v227
v_cvt_pk_bf16_f32 v35,v228,v229
v_cvt_pk_bf16_f32 v39,v230,v231
v_fma_f32 v184,v184,v122,v126
s_waitcnt lgkmcnt(1)
s_setprio 3
v_mfma_f32_16x16x32_bf16 v[176:179],v[104:107],v[144:147],v[108:111]
ds_read_b128 v[60:63],v7 offset:3744
s_setprio 0
v_fma_f32 v185,v185,v122,v126
v_fma_f32 v186,v186,v123,v127
v_fma_f32 v187,v187,v123,v127
v_mfma_f32_16x16x32_bf16 v[128:131],v[64:67],v[16:19],v[112:115]
v_fma_f32 v188,v188,v122,v126
v_fma_f32 v189,v189,v122,v126
v_fma_f32 v190,v190,v123,v127
v_mfma_f32_16x16x32_bf16 v[180:183],v[104:107],v[148:151],v[108:111]
v_fma_f32 v191,v191,v123,v127
v_fma_f32 v232,v232,v122,v126
v_fma_f32 v233,v233,v122,v126
v_mfma_f32_16x16x32_bf16 v[136:139],v[64:67],v[20:23],v[112:115]
v_fma_f32 v234,v234,v123,v127
v_fma_f32 v235,v235,v123,v127
v_fma_f32 v236,v236,v122,v126
v_mfma_f32_16x16x32_bf16 v[224:227],v[104:107],v[152:155],v[108:111]
v_fma_f32 v237,v237,v122,v126
v_fma_f32 v238,v238,v123,v127
v_fma_f32 v239,v239,v123,v127
v_mfma_f32_16x16x32_bf16 v[140:143],v[68:71],v[20:23],v[116:119]
v_cvt_pk_bf16_f32 v40,v184,v185
v_cvt_pk_bf16_f32 v44,v186,v187
v_cvt_pk_bf16_f32 v41,v188,v189
s_setprio 3
v_mfma_f32_16x16x32_bf16 v[228:231],v[104:107],v[156:159],v[108:111]
ds_read_b128 v[104:107],v14 offset:1024
s_setprio 0
v_cvt_pk_bf16_f32 v45,v190,v191
v_cvt_pk_bf16_f32 v42,v232,v233
v_cvt_pk_bf16_f32 v46,v234,v235
s_setprio 3
v_mfma_f32_16x16x32_bf16 v[132:135],v[68:71],v[16:19],v[116:119]
ds_read_b128 v[108:111],v7 offset:3840
s_setprio 0
v_cvt_pk_bf16_f32 v43,v236,v237
v_cvt_pk_bf16_f32 v47,v238,v239
v_fma_f32 v192,v192,v122,v126
s_waitcnt lgkmcnt(2)
v_mfma_f32_16x16x32_bf16 v[184:187],v[56:59],v[144:147],v[60:63]
v_fma_f32 v193,v193,v122,v126
v_fma_f32 v194,v194,v123,v127
v_mfma_f32_16x16x32_bf16 v[128:131],v[72:75],v[24:27],v[128:131]
v_fma_f32 v195,v195,v123,v127
v_fma_f32 v196,v196,v122,v126
v_mfma_f32_16x16x32_bf16 v[188:191],v[56:59],v[148:151],v[60:63]
v_fma_f32 v197,v197,v122,v126
v_fma_f32 v198,v198,v123,v127
v_mfma_f32_16x16x32_bf16 v[136:139],v[72:75],v[28:31],v[136:139]
v_fma_f32 v199,v199,v123,v127
v_fma_f32 v240,v240,v122,v126
v_mfma_f32_16x16x32_bf16 v[232:235],v[56:59],v[152:155],v[60:63]
v_fma_f32 v241,v241,v122,v126
v_fma_f32 v242,v242,v123,v127
v_mfma_f32_16x16x32_bf16 v[140:143],v[76:79],v[28:31],v[140:143]
v_fma_f32 v243,v243,v123,v127
v_fma_f32 v244,v244,v122,v126
s_setprio 3
v_mfma_f32_16x16x32_bf16 v[236:239],v[56:59],v[156:159],v[60:63]
ds_read_b128 v[144:147],v14 offset:1280
s_setprio 0
v_fma_f32 v245,v245,v122,v126
v_fma_f32 v246,v246,v123,v127
s_setprio 3
v_mfma_f32_16x16x32_bf16 v[132:135],v[76:79],v[24:27],v[132:135]
ds_read_b128 v[148:151],v7 offset:3872
s_setprio 0
v_fma_f32 v247,v247,v123,v127
v_cvt_pk_bf16_f32 v48,v192,v193
v_cvt_pk_bf16_f32 v52,v194,v195
v_cvt_pk_bf16_f32 v49,v196,v197
v_cvt_pk_bf16_f32 v53,v198,v199
v_cvt_pk_bf16_f32 v50,v240,v241
v_cvt_pk_bf16_f32 v54,v242,v243
v_cvt_pk_bf16_f32 v51,v244,v245
v_cvt_pk_bf16_f32 v55,v246,v247
v_fma_f32 v200,v200,v122,v126
s_waitcnt lgkmcnt(2)
v_mfma_f32_16x16x32_bf16 v[192:195],v[104:107],v[88:91],v[108:111]
v_fma_f32 v201,v201,v122,v126
v_fma_f32 v202,v202,v123,v127
v_fma_f32 v203,v203,v123,v127
v_mfma_f32_16x16x32_bf16 v[128:131],v[80:83],v[32:35],v[128:131]
v_fma_f32 v204,v204,v122,v126
v_fma_f32 v205,v205,v122,v126
v_fma_f32 v206,v206,v123,v127
v_mfma_f32_16x16x32_bf16 v[196:199],v[104:107],v[92:95],v[108:111]
v_fma_f32 v207,v207,v123,v127
v_fma_f32 v248,v248,v122,v126
v_fma_f32 v249,v249,v122,v126
v_mfma_f32_16x16x32_bf16 v[136:139],v[80:83],v[36:39],v[136:139]
v_fma_f32 v250,v250,v123,v127
v_fma_f32 v251,v251,v123,v127
v_fma_f32 v252,v252,v122,v126
v_mfma_f32_16x16x32_bf16 v[240:243],v[104:107],v[96:99],v[108:111]
v_fma_f32 v253,v253,v122,v126
v_fma_f32 v254,v254,v123,v127
v_fma_f32 v255,v255,v123,v127
v_mfma_f32_16x16x32_bf16 v[140:143],v[84:87],v[36:39],v[140:143]
v_cvt_pk_bf16_f32 v56,v200,v201
v_cvt_pk_bf16_f32 v60,v202,v203
v_cvt_pk_bf16_f32 v57,v204,v205
v_mfma_f32_16x16x32_bf16 v[244:247],v[104:107],v[100:103],v[108:111]
v_cvt_pk_bf16_f32 v61,v206,v207
v_cvt_pk_bf16_f32 v58,v248,v249
v_cvt_pk_bf16_f32 v62,v250,v251
v_mfma_f32_16x16x32_bf16 v[132:135],v[84:87],v[32:35],v[132:135]
v_cvt_pk_bf16_f32 v59,v252,v253
v_cvt_pk_bf16_f32 v63,v254,v255
s_waitcnt lgkmcnt(0)
v_mfma_f32_16x16x32_bf16 v[200:203],v[144:147],v[88:91],v[148:151]
s_waitcnt vmcnt(0)
s_barrier
s_setprio 3
ds_read_b64_tr_b16 v[104:105],v6 offset:128
v_lshlrev_b32_sdwa v128,1,v128 dst_sel:DWORD dst_unused:UNUSED_PAD src0_sel:DWORD src1_sel:WORD_1
v_lshlrev_b32_sdwa v130,1,v130 dst_sel:DWORD dst_unused:UNUSED_PAD src0_sel:DWORD src1_sel:WORD_1
v_lshlrev_b32_sdwa v129,1,v129 dst_sel:DWORD dst_unused:UNUSED_PAD src0_sel:DWORD src1_sel:WORD_1
v_mfma_f32_16x16x32_bf16 v[204:207],v[144:147],v[92:95],v[148:151]
ds_read_u16 v128,v128
ds_read_b64_tr_b16 v[106:107],v6 offset:1664
v_lshlrev_b32_sdwa v131,1,v131 dst_sel:DWORD dst_unused:UNUSED_PAD src0_sel:DWORD src1_sel:WORD_1
v_fma_mixlo_f16 v120,v140,s99,0
v_fma_mixlo_f16 v122,v141,s99,0
v_mfma_f32_16x16x32_bf16 v[248:251],v[144:147],v[96:99],v[148:151]
ds_read_u16_d16_hi v130,v130
ds_read_b64_tr_b16 v[108:109],v6 offset:136
v_lshlrev_b32_sdwa v132,1,v132 dst_sel:DWORD dst_unused:UNUSED_PAD src0_sel:DWORD src1_sel:WORD_1
v_lshlrev_b32_sdwa v134,1,v134 dst_sel:DWORD dst_unused:UNUSED_PAD src0_sel:DWORD src1_sel:WORD_1
v_mfma_f32_16x16x32_bf16 v[252:255],v[144:147],v[100:103],v[148:151]
ds_read_u16 v129,v129
ds_read_b64_tr_b16 v[110:111],v6 offset:1672
v_fma_mixhi_f16 v120,v142,s99,0
v_fma_mixhi_f16 v122,v143,s99,0
v_lshlrev_b32_sdwa v133,1,v133 dst_sel:DWORD dst_unused:UNUSED_PAD src0_sel:DWORD src1_sel:WORD_1
v_pk_fma_f16 v121,v120,v120,s54
v_mfma_f32_16x16x32_bf16 v[144:147],v[64:67],v[40:43],v[112:115]
ds_read_u16_d16_hi v131,v131
ds_read_b64_tr_b16 v[88:89],v6 offset:0
v_lshlrev_b32_sdwa v135,1,v135 dst_sel:DWORD dst_unused:UNUSED_PAD src0_sel:DWORD src1_sel:WORD_1
v_pk_fma_f16 v123,v122,v122,s54
v_pk_mul_f16 v121,v120,v121
v_mfma_f32_16x16x32_bf16 v[152:155],v[64:67],v[44:47],v[112:115]
ds_read_u16 v132,v132
ds_read_b64_tr_b16 v[90:91],v6 offset:1536
v_lshlrev_b32_sdwa v136,1,v136 dst_sel:DWORD dst_unused:UNUSED_PAD src0_sel:DWORD src1_sel:WORD_1
v_pk_mul_f16 v123,v122,v123
v_mfma_f32_16x16x32_bf16 v[156:159],v[68:71],v[44:47],v[116:119]
ds_read_u16_d16_hi v134,v134
ds_read_b64_tr_b16 v[92:93],v6 offset:8
v_lshlrev_b32_sdwa v138,1,v138 dst_sel:DWORD dst_unused:UNUSED_PAD src0_sel:DWORD src1_sel:WORD_1
v_exp_f16 v120,v121
v_mfma_f32_16x16x32_bf16 v[148:151],v[68:71],v[40:43],v[116:119]
ds_read_u16 v133,v133
ds_read_b64_tr_b16 v[94:95],v6 offset:1544
v_lshlrev_b32_sdwa v137,1,v137 dst_sel:DWORD dst_unused:UNUSED_PAD src0_sel:DWORD src1_sel:WORD_1
v_lshlrev_b32_sdwa v139,1,v139 dst_sel:DWORD dst_unused:UNUSED_PAD src0_sel:DWORD src1_sel:WORD_1
v_exp_f16 v122,v123
v_mfma_f32_16x16x32_bf16 v[144:147],v[72:75],v[48:51],v[144:147]
ds_read_u16_d16_hi v135,v135
ds_read_b64_tr_b16 v[96:97],v6 offset:64
v_exp_f16_sdwa v122,v123 dst_sel:WORD_1 dst_unused:UNUSED_PRESERVE src0_sel:WORD_1
v_mfma_f32_16x16x32_bf16 v[152:155],v[72:75],v[52:55],v[152:155]
ds_read_u16 v136,v136
ds_read_b64_tr_b16 v[98:99],v6 offset:1600
v_exp_f16_sdwa v120,v121 dst_sel:WORD_1 dst_unused:UNUSED_PRESERVE src0_sel:WORD_1
v_mfma_f32_16x16x32_bf16 v[156:159],v[76:79],v[52:55],v[156:159]
ds_read_u16_d16_hi v138,v138
ds_read_b64_tr_b16 v[100:101],v6 offset:72
v_pk_fma_f16 v120,v120,s55,s55
v_pk_fma_f16 v122,v122,s55,s55
v_rcp_f16 v121,v120
v_mfma_f32_16x16x32_bf16 v[148:151],v[76:79],v[48:51],v[148:151]
ds_read_u16 v137,v137
ds_read_b64_tr_b16 v[102:103],v6 offset:1608
v_rcp_f16 v123,v122
v_mfma_f32_16x16x32_bf16 v[144:147],v[80:83],v[56:59],v[144:147]
ds_read_u16_d16_hi v139,v139
ds_read_b64_tr_b16 v[64:65],v4 offset:0
v_rcp_f16_sdwa v121,v120 dst_sel:WORD_1 dst_unused:UNUSED_PRESERVE src0_sel:WORD_1
v_mfma_f32_16x16x32_bf16 v[152:155],v[80:83],v[60:63],v[152:155]
ds_read_b64_tr_b16 v[66:67],v4 offset:2048
v_rcp_f16_sdwa v123,v122 dst_sel:WORD_1 dst_unused:UNUSED_PRESERVE src0_sel:WORD_1
v_fma_mix_f32 v142,v142,v121,0 op_sel:[0,1,0] op_sel_hi:[0,1,0]
v_mfma_f32_16x16x32_bf16 v[156:159],v[84:87],v[60:63],v[156:159]
ds_read_b128 v[112:115],v7 offset:1280
v_fma_mix_f32 v140,v140,v121,0 op_sel_hi:[0,1,0]
s_waitcnt lgkmcnt(15)
v_lshl_add_u64 v[128:129],v[128:129],0,v[130:131]
s_waitcnt lgkmcnt(11)
v_lshl_add_u64 v[130:131],v[132:133],0,v[134:135]
v_mfma_f32_16x16x32_bf16 v[148:151],v[84:87],v[56:59],v[148:151]
ds_read_b64_tr_b16 v[68:69],v5 offset:0
v_fma_mix_f32 v141,v141,v123,0 op_sel_hi:[0,1,0]
v_fma_mix_f32 v143,v143,v123,0 op_sel:[0,1,0] op_sel_hi:[0,1,0]
v_lshlrev_b32_sdwa v144,1,v144 dst_sel:DWORD dst_unused:UNUSED_PAD src0_sel:DWORD src1_sel:WORD_1
v_lshlrev_b32_sdwa v146,1,v146 dst_sel:DWORD dst_unused:UNUSED_PAD src0_sel:DWORD src1_sel:WORD_1
v_mfma_f32_16x16x32_bf16 v[160:163],v[88:91],v[128:131],v[160:163]
ds_read_u16 v144,v144
v_lshlrev_b32_sdwa v145,1,v145 dst_sel:DWORD dst_unused:UNUSED_PAD src0_sel:DWORD src1_sel:WORD_1
v_lshlrev_b32_sdwa v147,1,v147 dst_sel:DWORD dst_unused:UNUSED_PAD src0_sel:DWORD src1_sel:WORD_1
v_fma_mixlo_f16 v122,v157,s99,0
v_mfma_f32_16x16x32_bf16 v[168:171],v[92:95],v[128:131],v[168:171]
s_waitcnt lgkmcnt(10)
ds_read_b64_tr_b16 v[70:71],v5 offset:2048
ds_read_b128 v[116:119],v7 offset:1312
v_fma_mixlo_f16 v120,v156,s99,0
v_mfma_f32_16x16x32_bf16 v[176:179],v[96:99],v[128:131],v[176:179]
s_waitcnt lgkmcnt(8)
ds_read_u16_d16_hi v146,v146
v_lshlrev_b32_sdwa v148,1,v148 dst_sel:DWORD dst_unused:UNUSED_PAD src0_sel:DWORD src1_sel:WORD_1
v_lshlrev_b32_sdwa v150,1,v150 dst_sel:DWORD dst_unused:UNUSED_PAD src0_sel:DWORD src1_sel:WORD_1
v_mfma_f32_16x16x32_bf16 v[184:187],v[100:103],v[128:131],v[184:187]
s_waitcnt lgkmcnt(8)
ds_read_b64_tr_b16 v[72:73],v4 offset:4096
ds_read_b64_tr_b16 v[74:75],v4 offset:6144
v_lshl_add_u64 v[136:137],v[136:137],0,v[138:139]
v_fma_mixhi_f16 v120,v158,s99,0
v_mfma_f32_16x16x32_bf16 v[192:195],v[104:107],v[128:131],v[192:195]
ds_read_u16 v145,v145
v_cvt_pk_bf16_f32 v138,v140,v142
v_cvt_pk_bf16_f32 v139,v141,v143
v_mfma_f32_16x16x32_bf16 v[200:203],v[108:111],v[128:131],v[200:203]
v_fma_mixhi_f16 v122,v159,s99,0
v_mfma_f32_16x16x32_bf16 v[164:167],v[88:91],v[136:139],v[164:167]
ds_read_b64_tr_b16 v[76:77],v5 offset:4096
ds_read_b64_tr_b16 v[78:79],v5 offset:6144
v_lshlrev_b32_sdwa v149,1,v149 dst_sel:DWORD dst_unused:UNUSED_PAD src0_sel:DWORD src1_sel:WORD_1
v_lshlrev_b32_sdwa v151,1,v151 dst_sel:DWORD dst_unused:UNUSED_PAD src0_sel:DWORD src1_sel:WORD_1
v_mfma_f32_16x16x32_bf16 v[172:175],v[92:95],v[136:139],v[172:175]
ds_read_b64_tr_b16 v[80:81],v4 offset:8192
ds_read_b64_tr_b16 v[82:83],v4 offset:10240
v_pk_fma_f16 v121,v120,v120,s54
v_pk_fma_f16 v123,v122,v122,s54
v_mfma_f32_16x16x32_bf16 v[180:183],v[96:99],v[136:139],v[180:183]
ds_read_u16_d16_hi v147,v147
v_pk_mul_f16 v121,v120,v121
v_pk_mul_f16 v123,v122,v123
v_mfma_f32_16x16x32_bf16 v[188:191],v[100:103],v[136:139],v[188:191]
ds_read_u16 v148,v148
v_lshlrev_b32_sdwa v152,1,v152 dst_sel:DWORD dst_unused:UNUSED_PAD src0_sel:DWORD src1_sel:WORD_1
v_lshlrev_b32_sdwa v154,1,v154 dst_sel:DWORD dst_unused:UNUSED_PAD src0_sel:DWORD src1_sel:WORD_1
v_mfma_f32_16x16x32_bf16 v[196:199],v[104:107],v[136:139],v[196:199]
v_exp_f16 v120,v121
v_mfma_f32_16x16x32_bf16 v[204:207],v[108:111],v[136:139],v[204:207]
s_waitcnt lgkmcnt(14)
ds_read_b64_tr_b16 v[84:85],v5 offset:8192
ds_read_b64_tr_b16 v[86:87],v5 offset:10240
v_exp_f16 v122,v123
v_mfma_f32_16x16x32_bf16 v[128:131],v[64:67],v[16:19],v[112:115]
v_lshlrev_b32_sdwa v153,1,v153 dst_sel:DWORD dst_unused:UNUSED_PAD src0_sel:DWORD src1_sel:WORD_1
v_lshlrev_b32_sdwa v155,1,v155 dst_sel:DWORD dst_unused:UNUSED_PAD src0_sel:DWORD src1_sel:WORD_1
v_mfma_f32_16x16x32_bf16 v[136:139],v[64:67],v[20:23],v[112:115]
s_waitcnt lgkmcnt(12)
ds_read_u16_d16_hi v150,v150
ds_read_u16 v149,v149
v_exp_f16_sdwa v120,v121 dst_sel:WORD_1 dst_unused:UNUSED_PRESERVE src0_sel:WORD_1
v_mfma_f32_16x16x32_bf16 v[140:143],v[68:71],v[20:23],v[116:119]
v_exp_f16_sdwa v122,v123 dst_sel:WORD_1 dst_unused:UNUSED_PRESERVE src0_sel:WORD_1
v_mfma_f32_16x16x32_bf16 v[132:135],v[68:71],v[16:19],v[116:119]
s_waitcnt lgkmcnt(11)
v_pk_fma_f16 v120,v120,s55,s55
v_mfma_f32_16x16x32_bf16 v[128:131],v[72:75],v[24:27],v[128:131]
v_pk_fma_f16 v122,v122,s55,s55
v_rcp_f16 v121,v120
v_mfma_f32_16x16x32_bf16 v[136:139],v[72:75],v[28:31],v[136:139]
s_waitcnt lgkmcnt(8)
ds_read_u16_d16_hi v151,v151
v_rcp_f16 v123,v122
v_mfma_f32_16x16x32_bf16 v[140:143],v[76:79],v[28:31],v[140:143]
ds_read_u16 v152,v152
ds_read_u16_d16_hi v154,v154
v_rcp_f16_sdwa v121,v120 dst_sel:WORD_1 dst_unused:UNUSED_PRESERVE src0_sel:WORD_1
v_mfma_f32_16x16x32_bf16 v[132:135],v[76:79],v[24:27],v[132:135]
s_waitcnt lgkmcnt(9)
v_rcp_f16_sdwa v123,v122 dst_sel:WORD_1 dst_unused:UNUSED_PRESERVE src0_sel:WORD_1
v_mfma_f32_16x16x32_bf16 v[128:131],v[80:83],v[32:35],v[128:131]
ds_read_u16 v153,v153
v_fma_mix_f32 v156,v156,v121,0 op_sel_hi:[0,1,0]
v_fma_mix_f32 v158,v158,v121,0 op_sel:[0,1,0] op_sel_hi:[0,1,0]
v_mfma_f32_16x16x32_bf16 v[136:139],v[80:83],v[36:39],v[136:139]
s_waitcnt lgkmcnt(3)
v_lshl_add_u64 v[144:145],v[144:145],0,v[146:147]
v_lshl_add_u64 v[146:147],v[148:149],0,v[150:151]
v_mfma_f32_16x16x32_bf16 v[140:143],v[84:87],v[36:39],v[140:143]
ds_read_u16_d16_hi v155,v155
v_fma_mix_f32 v157,v157,v123,0 op_sel_hi:[0,1,0]
v_fma_mix_f32 v159,v159,v123,0 op_sel:[0,1,0] op_sel_hi:[0,1,0]
v_mfma_f32_16x16x32_bf16 v[132:135],v[84:87],v[32:35],v[132:135]
v_lshlrev_b32_sdwa v128,1,v128 dst_sel:DWORD dst_unused:UNUSED_PAD src0_sel:DWORD src1_sel:WORD_1
v_lshlrev_b32_sdwa v130,1,v130 dst_sel:DWORD dst_unused:UNUSED_PAD src0_sel:DWORD src1_sel:WORD_1
v_mfma_f32_16x16x32_bf16 v[208:211],v[88:91],v[144:147],v[208:211]
v_lshlrev_b32_sdwa v129,1,v129 dst_sel:DWORD dst_unused:UNUSED_PAD src0_sel:DWORD src1_sel:WORD_1
v_lshlrev_b32_sdwa v131,1,v131 dst_sel:DWORD dst_unused:UNUSED_PAD src0_sel:DWORD src1_sel:WORD_1
v_mfma_f32_16x16x32_bf16 v[216:219],v[92:95],v[144:147],v[216:219]
ds_read_u16 v128,v128
ds_read_u16_d16_hi v130,v130
v_fma_mixlo_f16 v120,v140,s99,0
v_fma_mixlo_f16 v122,v141,s99,0
v_mfma_f32_16x16x32_bf16 v[224:227],v[96:99],v[144:147],v[224:227]
ds_read_u16 v129,v129
v_lshlrev_b32_sdwa v132,1,v132 dst_sel:DWORD dst_unused:UNUSED_PAD src0_sel:DWORD src1_sel:WORD_1
v_lshlrev_b32_sdwa v134,1,v134 dst_sel:DWORD dst_unused:UNUSED_PAD src0_sel:DWORD src1_sel:WORD_1
v_mfma_f32_16x16x32_bf16 v[232:235],v[100:103],v[144:147],v[232:235]
s_waitcnt lgkmcnt(3)
v_lshl_add_u64 v[152:153],v[152:153],0,v[154:155]
v_cvt_pk_bf16_f32 v154,v156,v158
v_mfma_f32_16x16x32_bf16 v[240:243],v[104:107],v[144:147],v[240:243]
v_fma_mixhi_f16 v120,v142,s99,0
v_cvt_pk_bf16_f32 v155,v157,v159
v_mfma_f32_16x16x32_bf16 v[248:251],v[108:111],v[144:147],v[248:251]
ds_read_u16_d16_hi v131,v131
v_fma_mixhi_f16 v122,v143,s99,0
v_mfma_f32_16x16x32_bf16 v[212:215],v[88:91],v[152:155],v[212:215]
v_lshlrev_b32_sdwa v133,1,v133 dst_sel:DWORD dst_unused:UNUSED_PAD src0_sel:DWORD src1_sel:WORD_1
v_pk_fma_f16 v121,v120,v120,s54
v_lshlrev_b32_sdwa v135,1,v135 dst_sel:DWORD dst_unused:UNUSED_PAD src0_sel:DWORD src1_sel:WORD_1
v_mfma_f32_16x16x32_bf16 v[220:223],v[92:95],v[152:155],v[220:223]
s_setprio 2
ds_read_u16 v132,v132
ds_read_u16_d16_hi v134,v134
v_pk_fma_f16 v123,v122,v122,s54
v_mfma_f32_16x16x32_bf16 v[228:231],v[96:99],v[152:155],v[228:231]
ds_read_b64_tr_b16 v[88:89],v6 offset:6160
v_pk_mul_f16 v121,v120,v121
v_lshlrev_b32_sdwa v136,1,v136 dst_sel:DWORD dst_unused:UNUSED_PAD src0_sel:DWORD src1_sel:WORD_1
v_pk_mul_f16 v123,v122,v123
v_mfma_f32_16x16x32_bf16 v[236:239],v[100:103],v[152:155],v[236:239]
ds_read_b64_tr_b16 v[90:91],v6 offset:7696
v_lshlrev_b32_sdwa v138,1,v138 dst_sel:DWORD dst_unused:UNUSED_PAD src0_sel:DWORD src1_sel:WORD_1
v_mfma_f32_16x16x32_bf16 v[244:247],v[104:107],v[152:155],v[244:247]
ds_read_u16 v133,v133
v_exp_f16 v120,v121
v_mfma_f32_16x16x32_bf16 v[252:255],v[108:111],v[152:155],v[252:255]
ds_read_b64_tr_b16 v[92:93],v6 offset:6168
v_lshlrev_b32_sdwa v137,1,v137 dst_sel:DWORD dst_unused:UNUSED_PAD src0_sel:DWORD src1_sel:WORD_1
v_mfma_f32_16x16x32_bf16 v[144:147],v[64:67],v[40:43],v[112:115]
ds_read_b64_tr_b16 v[94:95],v6 offset:7704
v_lshlrev_b32_sdwa v139,1,v139 dst_sel:DWORD dst_unused:UNUSED_PAD src0_sel:DWORD src1_sel:WORD_1
v_exp_f16 v122,v123
v_mfma_f32_16x16x32_bf16 v[152:155],v[64:67],v[44:47],v[112:115]
ds_read_u16_d16_hi v135,v135
ds_read_b64_tr_b16 v[96:97],v6 offset:6224
v_mfma_f32_16x16x32_bf16 v[156:159],v[68:71],v[44:47],v[116:119]
ds_read_u16 v136,v136
v_exp_f16_sdwa v122,v123 dst_sel:WORD_1 dst_unused:UNUSED_PRESERVE src0_sel:WORD_1
s_nop 0
s_mov_b32 s86,0xbe801d5a
v_exp_f16_sdwa v120,v121 dst_sel:WORD_1 dst_unused:UNUSED_PRESERVE src0_sel:WORD_1
v_mfma_f32_16x16x32_bf16 v[148:151],v[68:71],v[40:43],v[116:119]
ds_read_b64_tr_b16 v[98:99],v6 offset:7760
v_pk_fma_f16 v120,v120,s55,s55
v_pk_fma_f16 v122,v122,s55,s55
v_mfma_f32_16x16x32_bf16 v[144:147],v[72:75],v[48:51],v[144:147]
v_rcp_f16 v121,v120
v_mfma_f32_16x16x32_bf16 v[152:155],v[72:75],v[52:55],v[152:155]
ds_read_b64_tr_b16 v[100:101],v6 offset:6232
v_rcp_f16 v123,v122
v_mfma_f32_16x16x32_bf16 v[156:159],v[76:79],v[52:55],v[156:159]
ds_read_u16_d16_hi v138,v138
v_rcp_f16_sdwa v121,v120 dst_sel:WORD_1 dst_unused:UNUSED_PRESERVE src0_sel:WORD_1
v_mfma_f32_16x16x32_bf16 v[148:151],v[76:79],v[48:51],v[148:151]
ds_read_b64_tr_b16 v[102:103],v6 offset:7768
ds_read_b64_tr_b16 v[104:105],v6 offset:6288
v_rcp_f16_sdwa v123,v122 dst_sel:WORD_1 dst_unused:UNUSED_PRESERVE src0_sel:WORD_1
v_mfma_f32_16x16x32_bf16 v[144:147],v[80:83],v[56:59],v[144:147]
ds_read_u16 v137,v137
v_fma_mix_f32 v142,v142,v121,0 op_sel:[0,1,0] op_sel_hi:[0,1,0]
v_mfma_f32_16x16x32_bf16 v[152:155],v[80:83],v[60:63],v[152:155]
s_waitcnt lgkmcnt(8)
ds_read_b64_tr_b16 v[106:107],v6 offset:7824
ds_read_b64_tr_b16 v[108:109],v6 offset:6296
v_fma_mix_f32 v140,v140,v121,0 op_sel_hi:[0,1,0]
v_lshl_add_u64 v[128:129],v[128:129],0,v[130:131]
v_lshl_add_u64 v[130:131],v[132:133],0,v[134:135]
v_mfma_f32_16x16x32_bf16 v[156:159],v[84:87],v[60:63],v[156:159]
ds_read_u16_d16_hi v139,v139
v_fma_mix_f32 v141,v141,v123,0 op_sel_hi:[0,1,0]
v_fma_mix_f32 v143,v143,v123,0 op_sel:[0,1,0] op_sel_hi:[0,1,0]
v_mfma_f32_16x16x32_bf16 v[148:151],v[84:87],v[56:59],v[148:151]
ds_read_b64_tr_b16 v[110:111],v6 offset:7832
ds_read_b64_tr_b16 v[64:65],v4 offset:512
v_lshlrev_b32_sdwa v144,1,v144 dst_sel:DWORD dst_unused:UNUSED_PAD src0_sel:DWORD src1_sel:WORD_1
v_lshlrev_b32_sdwa v146,1,v146 dst_sel:DWORD dst_unused:UNUSED_PAD src0_sel:DWORD src1_sel:WORD_1
v_mfma_f32_16x16x32_bf16 v[160:163],v[88:91],v[128:131],v[160:163]
ds_read_u16 v144,v144
v_lshlrev_b32_sdwa v145,1,v145 dst_sel:DWORD dst_unused:UNUSED_PAD src0_sel:DWORD src1_sel:WORD_1
v_lshlrev_b32_sdwa v147,1,v147 dst_sel:DWORD dst_unused:UNUSED_PAD src0_sel:DWORD src1_sel:WORD_1
v_fma_mixlo_f16 v122,v157,s99,0
v_mfma_f32_16x16x32_bf16 v[168:171],v[92:95],v[128:131],v[168:171]
s_waitcnt lgkmcnt(11)
ds_read_b64_tr_b16 v[66:67],v4 offset:2560
ds_read_b128 v[112:115],v7 offset:1408
v_fma_mixlo_f16 v120,v156,s99,0
v_mfma_f32_16x16x32_bf16 v[176:179],v[96:99],v[128:131],v[176:179]
s_waitcnt lgkmcnt(10)
ds_read_u16_d16_hi v146,v146
v_lshlrev_b32_sdwa v148,1,v148 dst_sel:DWORD dst_unused:UNUSED_PAD src0_sel:DWORD src1_sel:WORD_1
v_lshlrev_b32_sdwa v150,1,v150 dst_sel:DWORD dst_unused:UNUSED_PAD src0_sel:DWORD src1_sel:WORD_1
v_mfma_f32_16x16x32_bf16 v[184:187],v[100:103],v[128:131],v[184:187]
s_waitcnt lgkmcnt(6)
ds_read_b64_tr_b16 v[68:69],v5 offset:512
ds_read_b64_tr_b16 v[70:71],v5 offset:2560
v_lshl_add_u64 v[136:137],v[136:137],0,v[138:139]
v_fma_mixhi_f16 v120,v158,s99,0
v_mfma_f32_16x16x32_bf16 v[192:195],v[104:107],v[128:131],v[192:195]
s_waitcnt lgkmcnt(7)
ds_read_u16 v145,v145
v_cvt_pk_bf16_f32 v138,v140,v142
v_cvt_pk_bf16_f32 v139,v141,v143
v_mfma_f32_16x16x32_bf16 v[200:203],v[108:111],v[128:131],v[200:203]
v_fma_mixhi_f16 v122,v159,s99,0
v_mfma_f32_16x16x32_bf16 v[164:167],v[88:91],v[136:139],v[164:167]
ds_read_b128 v[116:119],v7 offset:1440
ds_read_b64_tr_b16 v[72:73],v4 offset:4608
v_lshlrev_b32_sdwa v149,1,v149 dst_sel:DWORD dst_unused:UNUSED_PAD src0_sel:DWORD src1_sel:WORD_1
v_lshlrev_b32_sdwa v151,1,v151 dst_sel:DWORD dst_unused:UNUSED_PAD src0_sel:DWORD src1_sel:WORD_1
v_mfma_f32_16x16x32_bf16 v[172:175],v[92:95],v[136:139],v[172:175]
ds_read_b64_tr_b16 v[74:75],v4 offset:6656
ds_read_b64_tr_b16 v[76:77],v5 offset:4608
v_pk_fma_f16 v121,v120,v120,s54
v_pk_fma_f16 v123,v122,v122,s54
v_mfma_f32_16x16x32_bf16 v[180:183],v[96:99],v[136:139],v[180:183]
s_setprio 1
ds_read_u16_d16_hi v147,v147
v_pk_mul_f16 v121,v120,v121
v_pk_mul_f16 v123,v122,v123
v_mfma_f32_16x16x32_bf16 v[188:191],v[100:103],v[136:139],v[188:191]
ds_read_u16 v148,v148
v_lshlrev_b32_sdwa v152,1,v152 dst_sel:DWORD dst_unused:UNUSED_PAD src0_sel:DWORD src1_sel:WORD_1
v_lshlrev_b32_sdwa v154,1,v154 dst_sel:DWORD dst_unused:UNUSED_PAD src0_sel:DWORD src1_sel:WORD_1
v_mfma_f32_16x16x32_bf16 v[196:199],v[104:107],v[136:139],v[196:199]
v_exp_f16 v120,v121
v_mfma_f32_16x16x32_bf16 v[204:207],v[108:111],v[136:139],v[204:207]
s_waitcnt lgkmcnt(10)
ds_read_b64_tr_b16 v[78:79],v5 offset:6656
ds_read_b64_tr_b16 v[80:81],v4 offset:8704
v_exp_f16 v122,v123
v_mfma_f32_16x16x32_bf16 v[128:131],v[64:67],v[16:19],v[112:115]
v_lshlrev_b32_sdwa v153,1,v153 dst_sel:DWORD dst_unused:UNUSED_PAD src0_sel:DWORD src1_sel:WORD_1
v_lshlrev_b32_sdwa v155,1,v155 dst_sel:DWORD dst_unused:UNUSED_PAD src0_sel:DWORD src1_sel:WORD_1
v_mfma_f32_16x16x32_bf16 v[136:139],v[64:67],v[20:23],v[112:115]
s_waitcnt lgkmcnt(7)
ds_read_u16_d16_hi v150,v150
ds_read_u16 v149,v149
v_exp_f16_sdwa v120,v121 dst_sel:WORD_1 dst_unused:UNUSED_PRESERVE src0_sel:WORD_1
v_mfma_f32_16x16x32_bf16 v[140:143],v[68:71],v[20:23],v[116:119]
v_exp_f16_sdwa v122,v123 dst_sel:WORD_1 dst_unused:UNUSED_PRESERVE src0_sel:WORD_1
v_mfma_f32_16x16x32_bf16 v[132:135],v[68:71],v[16:19],v[116:119]
s_waitcnt lgkmcnt(7)
ds_read_b64_tr_b16 v[82:83],v4 offset:10752
v_pk_fma_f16 v120,v120,s55,s55
v_mfma_f32_16x16x32_bf16 v[128:131],v[72:75],v[24:27],v[128:131]
ds_read_b64_tr_b16 v[84:85],v5 offset:8704
v_pk_fma_f16 v122,v122,s55,s55
v_rcp_f16 v121,v120
v_mfma_f32_16x16x32_bf16 v[136:139],v[72:75],v[28:31],v[136:139]
s_waitcnt lgkmcnt(5)
ds_read_b64_tr_b16 v[86:87],v5 offset:10752
ds_read_u16_d16_hi v151,v151
v_rcp_f16 v123,v122
v_mfma_f32_16x16x32_bf16 v[140:143],v[76:79],v[28:31],v[140:143]
ds_read_u16 v152,v152
ds_read_u16_d16_hi v154,v154
v_rcp_f16_sdwa v121,v120 dst_sel:WORD_1 dst_unused:UNUSED_PRESERVE src0_sel:WORD_1
v_mfma_f32_16x16x32_bf16 v[132:135],v[76:79],v[24:27],v[132:135]
s_waitcnt lgkmcnt(5)
v_rcp_f16_sdwa v123,v122 dst_sel:WORD_1 dst_unused:UNUSED_PRESERVE src0_sel:WORD_1
v_mfma_f32_16x16x32_bf16 v[128:131],v[80:83],v[32:35],v[128:131]
ds_read_u16 v153,v153
v_fma_mix_f32 v156,v156,v121,0 op_sel_hi:[0,1,0]
v_fma_mix_f32 v158,v158,v121,0 op_sel:[0,1,0] op_sel_hi:[0,1,0]
v_mfma_f32_16x16x32_bf16 v[136:139],v[80:83],v[36:39],v[136:139]
s_waitcnt lgkmcnt(3)
s_setprio 0
v_lshl_add_u64 v[144:145],v[144:145],0,v[146:147]
v_lshl_add_u64 v[146:147],v[148:149],0,v[150:151]
v_mfma_f32_16x16x32_bf16 v[140:143],v[84:87],v[36:39],v[140:143]
ds_read_u16_d16_hi v155,v155
v_fma_mix_f32 v157,v157,v123,0 op_sel_hi:[0,1,0]
v_fma_mix_f32 v159,v159,v123,0 op_sel:[0,1,0] op_sel_hi:[0,1,0]
v_mfma_f32_16x16x32_bf16 v[132:135],v[84:87],v[32:35],v[132:135]
v_lshlrev_b32_sdwa v128,1,v128 dst_sel:DWORD dst_unused:UNUSED_PAD src0_sel:DWORD src1_sel:WORD_1
v_lshlrev_b32_sdwa v130,1,v130 dst_sel:DWORD dst_unused:UNUSED_PAD src0_sel:DWORD src1_sel:WORD_1
v_mfma_f32_16x16x32_bf16 v[208:211],v[88:91],v[144:147],v[208:211]
s_barrier
s_setprio 3
v_lshlrev_b32_sdwa v129,1,v129 dst_sel:DWORD dst_unused:UNUSED_PAD src0_sel:DWORD src1_sel:WORD_1
v_lshlrev_b32_sdwa v131,1,v131 dst_sel:DWORD dst_unused:UNUSED_PAD src0_sel:DWORD src1_sel:WORD_1
v_mfma_f32_16x16x32_bf16 v[216:219],v[92:95],v[144:147],v[216:219]
ds_read_u16 v128,v128
ds_read_u16_d16_hi v130,v130
v_fma_mixlo_f16 v120,v140,s99,0
v_fma_mixlo_f16 v122,v141,s99,0
v_mfma_f32_16x16x32_bf16 v[224:227],v[96:99],v[144:147],v[224:227]
ds_read_u16 v129,v129
v_lshlrev_b32_sdwa v132,1,v132 dst_sel:DWORD dst_unused:UNUSED_PAD src0_sel:DWORD src1_sel:WORD_1
v_lshlrev_b32_sdwa v134,1,v134 dst_sel:DWORD dst_unused:UNUSED_PAD src0_sel:DWORD src1_sel:WORD_1
v_mfma_f32_16x16x32_bf16 v[232:235],v[100:103],v[144:147],v[232:235]
s_waitcnt lgkmcnt(3)
v_lshl_add_u64 v[152:153],v[152:153],0,v[154:155]
v_cvt_pk_bf16_f32 v154,v156,v158
v_mfma_f32_16x16x32_bf16 v[240:243],v[104:107],v[144:147],v[240:243]
v_fma_mixhi_f16 v120,v142,s99,0
v_cvt_pk_bf16_f32 v155,v157,v159
v_mfma_f32_16x16x32_bf16 v[248:251],v[108:111],v[144:147],v[248:251]
ds_read_u16_d16_hi v131,v131
v_fma_mixhi_f16 v122,v143,s99,0
v_mfma_f32_16x16x32_bf16 v[212:215],v[88:91],v[152:155],v[212:215]
v_lshlrev_b32_sdwa v133,1,v133 dst_sel:DWORD dst_unused:UNUSED_PAD src0_sel:DWORD src1_sel:WORD_1
v_pk_fma_f16 v121,v120,v120,s54
v_lshlrev_b32_sdwa v135,1,v135 dst_sel:DWORD dst_unused:UNUSED_PAD src0_sel:DWORD src1_sel:WORD_1
v_mfma_f32_16x16x32_bf16 v[220:223],v[92:95],v[152:155],v[220:223]
ds_read_u16 v132,v132
ds_read_u16_d16_hi v134,v134
v_pk_fma_f16 v123,v122,v122,s54
v_mfma_f32_16x16x32_bf16 v[228:231],v[96:99],v[152:155],v[228:231]
ds_read_b64_tr_b16 v[88:89],v6 offset:12320
v_pk_mul_f16 v121,v120,v121
v_lshlrev_b32_sdwa v136,1,v136 dst_sel:DWORD dst_unused:UNUSED_PAD src0_sel:DWORD src1_sel:WORD_1
v_pk_mul_f16 v123,v122,v123
v_mfma_f32_16x16x32_bf16 v[236:239],v[100:103],v[152:155],v[236:239]
ds_read_b64_tr_b16 v[90:91],v6 offset:13856
v_lshlrev_b32_sdwa v138,1,v138 dst_sel:DWORD dst_unused:UNUSED_PAD src0_sel:DWORD src1_sel:WORD_1
v_mfma_f32_16x16x32_bf16 v[244:247],v[104:107],v[152:155],v[244:247]
ds_read_u16 v133,v133
v_exp_f16 v120,v121
v_mfma_f32_16x16x32_bf16 v[252:255],v[108:111],v[152:155],v[252:255]
ds_read_b64_tr_b16 v[92:93],v6 offset:12328
v_lshlrev_b32_sdwa v137,1,v137 dst_sel:DWORD dst_unused:UNUSED_PAD src0_sel:DWORD src1_sel:WORD_1
v_mfma_f32_16x16x32_bf16 v[144:147],v[64:67],v[40:43],v[112:115]
ds_read_b64_tr_b16 v[94:95],v6 offset:13864
v_lshlrev_b32_sdwa v139,1,v139 dst_sel:DWORD dst_unused:UNUSED_PAD src0_sel:DWORD src1_sel:WORD_1
v_exp_f16 v122,v123
v_mfma_f32_16x16x32_bf16 v[152:155],v[64:67],v[44:47],v[112:115]
ds_read_u16_d16_hi v135,v135
ds_read_b64_tr_b16 v[96:97],v6 offset:12384
v_mfma_f32_16x16x32_bf16 v[156:159],v[68:71],v[44:47],v[116:119]
ds_read_u16 v136,v136
v_exp_f16_sdwa v122,v123 dst_sel:WORD_1 dst_unused:UNUSED_PRESERVE src0_sel:WORD_1
v_exp_f16_sdwa v120,v121 dst_sel:WORD_1 dst_unused:UNUSED_PRESERVE src0_sel:WORD_1
v_mfma_f32_16x16x32_bf16 v[148:151],v[68:71],v[40:43],v[116:119]
ds_read_b64_tr_b16 v[98:99],v6 offset:13920
v_pk_fma_f16 v120,v120,s55,s55
v_pk_fma_f16 v122,v122,s55,s55
v_mfma_f32_16x16x32_bf16 v[144:147],v[72:75],v[48:51],v[144:147]
v_rcp_f16 v121,v120
v_mfma_f32_16x16x32_bf16 v[152:155],v[72:75],v[52:55],v[152:155]
ds_read_b64_tr_b16 v[100:101],v6 offset:12392
v_rcp_f16 v123,v122
v_mfma_f32_16x16x32_bf16 v[156:159],v[76:79],v[52:55],v[156:159]
ds_read_u16_d16_hi v138,v138
v_rcp_f16_sdwa v121,v120 dst_sel:WORD_1 dst_unused:UNUSED_PRESERVE src0_sel:WORD_1
v_mfma_f32_16x16x32_bf16 v[148:151],v[76:79],v[48:51],v[148:151]
ds_read_b64_tr_b16 v[102:103],v6 offset:13928
ds_read_b64_tr_b16 v[104:105],v6 offset:12448
v_rcp_f16_sdwa v123,v122 dst_sel:WORD_1 dst_unused:UNUSED_PRESERVE src0_sel:WORD_1
v_mfma_f32_16x16x32_bf16 v[144:147],v[80:83],v[56:59],v[144:147]
ds_read_u16 v137,v137
v_fma_mix_f32 v142,v142,v121,0 op_sel:[0,1,0] op_sel_hi:[0,1,0]
v_mfma_f32_16x16x32_bf16 v[152:155],v[80:83],v[60:63],v[152:155]
s_waitcnt lgkmcnt(8)
ds_read_b64_tr_b16 v[106:107],v6 offset:13984
ds_read_b64_tr_b16 v[108:109],v6 offset:12456
v_fma_mix_f32 v140,v140,v121,0 op_sel_hi:[0,1,0]
v_lshl_add_u64 v[128:129],v[128:129],0,v[130:131]
v_lshl_add_u64 v[130:131],v[132:133],0,v[134:135]
v_mfma_f32_16x16x32_bf16 v[156:159],v[84:87],v[60:63],v[156:159]
ds_read_u16_d16_hi v139,v139
v_fma_mix_f32 v141,v141,v123,0 op_sel_hi:[0,1,0]
v_fma_mix_f32 v143,v143,v123,0 op_sel:[0,1,0] op_sel_hi:[0,1,0]
v_mfma_f32_16x16x32_bf16 v[148:151],v[84:87],v[56:59],v[148:151]
ds_read_b64_tr_b16 v[110:111],v6 offset:13992
ds_read_b64_tr_b16 v[64:65],v4 offset:12320
v_lshlrev_b32_sdwa v144,1,v144 dst_sel:DWORD dst_unused:UNUSED_PAD src0_sel:DWORD src1_sel:WORD_1
v_lshlrev_b32_sdwa v146,1,v146 dst_sel:DWORD dst_unused:UNUSED_PAD src0_sel:DWORD src1_sel:WORD_1
v_mfma_f32_16x16x32_bf16 v[160:163],v[88:91],v[128:131],v[160:163]
ds_read_u16 v144,v144
v_lshlrev_b32_sdwa v145,1,v145 dst_sel:DWORD dst_unused:UNUSED_PAD src0_sel:DWORD src1_sel:WORD_1
v_lshlrev_b32_sdwa v147,1,v147 dst_sel:DWORD dst_unused:UNUSED_PAD src0_sel:DWORD src1_sel:WORD_1
v_fma_mixlo_f16 v122,v157,s99,0
v_mfma_f32_16x16x32_bf16 v[168:171],v[92:95],v[128:131],v[168:171]
s_waitcnt lgkmcnt(11)
ds_read_b64_tr_b16 v[66:67],v4 offset:14368
ds_read_b128 v[112:115],v7 offset:0
v_fma_mixlo_f16 v120,v156,s99,0
v_mfma_f32_16x16x32_bf16 v[176:179],v[96:99],v[128:131],v[176:179]
s_waitcnt lgkmcnt(10)
ds_read_u16_d16_hi v146,v146
v_lshlrev_b32_sdwa v148,1,v148 dst_sel:DWORD dst_unused:UNUSED_PAD src0_sel:DWORD src1_sel:WORD_1
v_lshlrev_b32_sdwa v150,1,v150 dst_sel:DWORD dst_unused:UNUSED_PAD src0_sel:DWORD src1_sel:WORD_1
v_mfma_f32_16x16x32_bf16 v[184:187],v[100:103],v[128:131],v[184:187]
s_waitcnt lgkmcnt(6)
ds_read_b64_tr_b16 v[68:69],v5 offset:12320
ds_read_b64_tr_b16 v[70:71],v5 offset:14368
v_lshl_add_u64 v[136:137],v[136:137],0,v[138:139]
v_fma_mixhi_f16 v120,v158,s99,0
v_mfma_f32_16x16x32_bf16 v[192:195],v[104:107],v[128:131],v[192:195]
s_waitcnt lgkmcnt(7)
ds_read_u16 v145,v145
v_cvt_pk_bf16_f32 v138,v140,v142
v_cvt_pk_bf16_f32 v139,v141,v143
v_mfma_f32_16x16x32_bf16 v[200:203],v[108:111],v[128:131],v[200:203]
v_fma_mixhi_f16 v122,v159,s99,0
v_mfma_f32_16x16x32_bf16 v[164:167],v[88:91],v[136:139],v[164:167]
ds_read_b128 v[116:119],v7 offset:32
ds_read_b64_tr_b16 v[72:73],v4 offset:16416
v_lshlrev_b32_sdwa v149,1,v149 dst_sel:DWORD dst_unused:UNUSED_PAD src0_sel:DWORD src1_sel:WORD_1
v_lshlrev_b32_sdwa v151,1,v151 dst_sel:DWORD dst_unused:UNUSED_PAD src0_sel:DWORD src1_sel:WORD_1
v_mfma_f32_16x16x32_bf16 v[172:175],v[92:95],v[136:139],v[172:175]
ds_read_b64_tr_b16 v[74:75],v4 offset:18464
ds_read_b64_tr_b16 v[76:77],v5 offset:16416
v_pk_fma_f16 v121,v120,v120,s54
v_pk_fma_f16 v123,v122,v122,s54
v_mfma_f32_16x16x32_bf16 v[180:183],v[96:99],v[136:139],v[180:183]
ds_read_u16_d16_hi v147,v147
v_pk_mul_f16 v121,v120,v121
v_pk_mul_f16 v123,v122,v123
v_mfma_f32_16x16x32_bf16 v[188:191],v[100:103],v[136:139],v[188:191]
ds_read_u16 v148,v148
v_lshlrev_b32_sdwa v152,1,v152 dst_sel:DWORD dst_unused:UNUSED_PAD src0_sel:DWORD src1_sel:WORD_1
v_lshlrev_b32_sdwa v154,1,v154 dst_sel:DWORD dst_unused:UNUSED_PAD src0_sel:DWORD src1_sel:WORD_1
v_mfma_f32_16x16x32_bf16 v[196:199],v[104:107],v[136:139],v[196:199]
v_exp_f16 v120,v121
v_mfma_f32_16x16x32_bf16 v[204:207],v[108:111],v[136:139],v[204:207]
s_waitcnt lgkmcnt(10)
ds_read_b64_tr_b16 v[78:79],v5 offset:18464
ds_read_b64_tr_b16 v[80:81],v4 offset:20512
v_exp_f16 v122,v123
v_mfma_f32_16x16x32_bf16 v[128:131],v[64:67],v[16:19],v[112:115]
v_lshlrev_b32_sdwa v153,1,v153 dst_sel:DWORD dst_unused:UNUSED_PAD src0_sel:DWORD src1_sel:WORD_1
v_lshlrev_b32_sdwa v155,1,v155 dst_sel:DWORD dst_unused:UNUSED_PAD src0_sel:DWORD src1_sel:WORD_1
v_mfma_f32_16x16x32_bf16 v[136:139],v[64:67],v[20:23],v[112:115]
s_waitcnt lgkmcnt(7)
ds_read_u16_d16_hi v150,v150
ds_read_u16 v149,v149
v_exp_f16_sdwa v120,v121 dst_sel:WORD_1 dst_unused:UNUSED_PRESERVE src0_sel:WORD_1
v_mfma_f32_16x16x32_bf16 v[140:143],v[68:71],v[20:23],v[116:119]
v_exp_f16_sdwa v122,v123 dst_sel:WORD_1 dst_unused:UNUSED_PRESERVE src0_sel:WORD_1
v_mfma_f32_16x16x32_bf16 v[132:135],v[68:71],v[16:19],v[116:119]
s_waitcnt lgkmcnt(7)
ds_read_b64_tr_b16 v[82:83],v4 offset:22560
v_pk_fma_f16 v120,v120,s55,s55
v_mfma_f32_16x16x32_bf16 v[128:131],v[72:75],v[24:27],v[128:131]
ds_read_b64_tr_b16 v[84:85],v5 offset:20512
v_pk_fma_f16 v122,v122,s55,s55
v_rcp_f16 v121,v120
v_mfma_f32_16x16x32_bf16 v[136:139],v[72:75],v[28:31],v[136:139]
s_waitcnt lgkmcnt(5)
ds_read_b64_tr_b16 v[86:87],v5 offset:22560
ds_read_u16_d16_hi v151,v151
v_rcp_f16 v123,v122
v_mfma_f32_16x16x32_bf16 v[140:143],v[76:79],v[28:31],v[140:143]
ds_read_u16 v152,v152
ds_read_u16_d16_hi v154,v154
v_rcp_f16_sdwa v121,v120 dst_sel:WORD_1 dst_unused:UNUSED_PRESERVE src0_sel:WORD_1
v_mfma_f32_16x16x32_bf16 v[132:135],v[76:79],v[24:27],v[132:135]
s_waitcnt lgkmcnt(5)
v_rcp_f16_sdwa v123,v122 dst_sel:WORD_1 dst_unused:UNUSED_PRESERVE src0_sel:WORD_1
v_mfma_f32_16x16x32_bf16 v[128:131],v[80:83],v[32:35],v[128:131]
ds_read_u16 v153,v153
v_fma_mix_f32 v156,v156,v121,0 op_sel_hi:[0,1,0]
v_fma_mix_f32 v158,v158,v121,0 op_sel:[0,1,0] op_sel_hi:[0,1,0]
v_mfma_f32_16x16x32_bf16 v[136:139],v[80:83],v[36:39],v[136:139]
s_waitcnt lgkmcnt(3)
v_lshl_add_u64 v[144:145],v[144:145],0,v[146:147]
v_lshl_add_u64 v[146:147],v[148:149],0,v[150:151]
v_mfma_f32_16x16x32_bf16 v[140:143],v[84:87],v[36:39],v[140:143]
ds_read_u16_d16_hi v155,v155
v_fma_mix_f32 v157,v157,v123,0 op_sel_hi:[0,1,0]
v_fma_mix_f32 v159,v159,v123,0 op_sel:[0,1,0] op_sel_hi:[0,1,0]
v_mfma_f32_16x16x32_bf16 v[132:135],v[84:87],v[32:35],v[132:135]
v_lshlrev_b32_sdwa v128,1,v128 dst_sel:DWORD dst_unused:UNUSED_PAD src0_sel:DWORD src1_sel:WORD_1
v_lshlrev_b32_sdwa v130,1,v130 dst_sel:DWORD dst_unused:UNUSED_PAD src0_sel:DWORD src1_sel:WORD_1
v_mfma_f32_16x16x32_bf16 v[208:211],v[88:91],v[144:147],v[208:211]
v_lshlrev_b32_sdwa v129,1,v129 dst_sel:DWORD dst_unused:UNUSED_PAD src0_sel:DWORD src1_sel:WORD_1
v_lshlrev_b32_sdwa v131,1,v131 dst_sel:DWORD dst_unused:UNUSED_PAD src0_sel:DWORD src1_sel:WORD_1
v_mfma_f32_16x16x32_bf16 v[216:219],v[92:95],v[144:147],v[216:219]
ds_read_u16 v128,v128
ds_read_u16_d16_hi v130,v130
v_fma_mixlo_f16 v120,v140,s99,0
v_fma_mixlo_f16 v122,v141,s99,0
v_mfma_f32_16x16x32_bf16 v[224:227],v[96:99],v[144:147],v[224:227]
ds_read_u16 v129,v129
v_lshlrev_b32_sdwa v132,1,v132 dst_sel:DWORD dst_unused:UNUSED_PAD src0_sel:DWORD src1_sel:WORD_1
v_lshlrev_b32_sdwa v134,1,v134 dst_sel:DWORD dst_unused:UNUSED_PAD src0_sel:DWORD src1_sel:WORD_1
v_mfma_f32_16x16x32_bf16 v[232:235],v[100:103],v[144:147],v[232:235]
s_waitcnt lgkmcnt(3)
v_lshl_add_u64 v[152:153],v[152:153],0,v[154:155]
v_cvt_pk_bf16_f32 v154,v156,v158
v_mfma_f32_16x16x32_bf16 v[240:243],v[104:107],v[144:147],v[240:243]
v_fma_mixhi_f16 v120,v142,s99,0
v_cvt_pk_bf16_f32 v155,v157,v159
v_mfma_f32_16x16x32_bf16 v[248:251],v[108:111],v[144:147],v[248:251]
s_setprio 2
ds_read_u16_d16_hi v131,v131
v_fma_mixhi_f16 v122,v143,s99,0
v_mfma_f32_16x16x32_bf16 v[212:215],v[88:91],v[152:155],v[212:215]
v_lshlrev_b32_sdwa v133,1,v133 dst_sel:DWORD dst_unused:UNUSED_PAD src0_sel:DWORD src1_sel:WORD_1
v_pk_fma_f16 v121,v120,v120,s54
v_lshlrev_b32_sdwa v135,1,v135 dst_sel:DWORD dst_unused:UNUSED_PAD src0_sel:DWORD src1_sel:WORD_1
v_mfma_f32_16x16x32_bf16 v[220:223],v[92:95],v[152:155],v[220:223]
ds_read_u16 v132,v132
ds_read_u16_d16_hi v134,v134
v_pk_fma_f16 v123,v122,v122,s54
v_mfma_f32_16x16x32_bf16 v[228:231],v[96:99],v[152:155],v[228:231]
ds_read_b64_tr_b16 v[88:89],v6 offset:18480
v_pk_mul_f16 v121,v120,v121
v_lshlrev_b32_sdwa v136,1,v136 dst_sel:DWORD dst_unused:UNUSED_PAD src0_sel:DWORD src1_sel:WORD_1
v_pk_mul_f16 v123,v122,v123
v_mfma_f32_16x16x32_bf16 v[236:239],v[100:103],v[152:155],v[236:239]
ds_read_b64_tr_b16 v[90:91],v6 offset:20016
v_lshlrev_b32_sdwa v138,1,v138 dst_sel:DWORD dst_unused:UNUSED_PAD src0_sel:DWORD src1_sel:WORD_1
v_mfma_f32_16x16x32_bf16 v[244:247],v[104:107],v[152:155],v[244:247]
ds_read_u16 v133,v133
v_exp_f16 v120,v121
v_mfma_f32_16x16x32_bf16 v[252:255],v[108:111],v[152:155],v[252:255]
ds_read_b64_tr_b16 v[92:93],v6 offset:18488
v_lshlrev_b32_sdwa v137,1,v137 dst_sel:DWORD dst_unused:UNUSED_PAD src0_sel:DWORD src1_sel:WORD_1
v_mfma_f32_16x16x32_bf16 v[144:147],v[64:67],v[40:43],v[112:115]
ds_read_b64_tr_b16 v[94:95],v6 offset:20024
v_lshlrev_b32_sdwa v139,1,v139 dst_sel:DWORD dst_unused:UNUSED_PAD src0_sel:DWORD src1_sel:WORD_1
v_exp_f16 v122,v123
v_mfma_f32_16x16x32_bf16 v[152:155],v[64:67],v[44:47],v[112:115]
ds_read_u16_d16_hi v135,v135
ds_read_b64_tr_b16 v[96:97],v6 offset:18544
v_mfma_f32_16x16x32_bf16 v[156:159],v[68:71],v[44:47],v[116:119]
ds_read_u16 v136,v136
v_exp_f16_sdwa v122,v123 dst_sel:WORD_1 dst_unused:UNUSED_PRESERVE src0_sel:WORD_1
v_exp_f16_sdwa v120,v121 dst_sel:WORD_1 dst_unused:UNUSED_PRESERVE src0_sel:WORD_1
v_mfma_f32_16x16x32_bf16 v[148:151],v[68:71],v[40:43],v[116:119]
ds_read_b64_tr_b16 v[98:99],v6 offset:20080
v_pk_fma_f16 v120,v120,s55,s55
v_pk_fma_f16 v122,v122,s55,s55
v_mfma_f32_16x16x32_bf16 v[144:147],v[72:75],v[48:51],v[144:147]
v_rcp_f16 v121,v120
v_mfma_f32_16x16x32_bf16 v[152:155],v[72:75],v[52:55],v[152:155]
ds_read_b64_tr_b16 v[100:101],v6 offset:18552
v_rcp_f16 v123,v122
v_mfma_f32_16x16x32_bf16 v[156:159],v[76:79],v[52:55],v[156:159]
ds_read_u16_d16_hi v138,v138
v_rcp_f16_sdwa v121,v120 dst_sel:WORD_1 dst_unused:UNUSED_PRESERVE src0_sel:WORD_1
v_mfma_f32_16x16x32_bf16 v[148:151],v[76:79],v[48:51],v[148:151]
ds_read_b64_tr_b16 v[102:103],v6 offset:20088
ds_read_b64_tr_b16 v[104:105],v6 offset:18608
v_rcp_f16_sdwa v123,v122 dst_sel:WORD_1 dst_unused:UNUSED_PRESERVE src0_sel:WORD_1
v_mfma_f32_16x16x32_bf16 v[144:147],v[80:83],v[56:59],v[144:147]
ds_read_u16 v137,v137
v_fma_mix_f32 v142,v142,v121,0 op_sel:[0,1,0] op_sel_hi:[0,1,0]
v_mfma_f32_16x16x32_bf16 v[152:155],v[80:83],v[60:63],v[152:155]
s_waitcnt lgkmcnt(8)
ds_read_b64_tr_b16 v[106:107],v6 offset:20144
ds_read_b64_tr_b16 v[108:109],v6 offset:18616
v_fma_mix_f32 v140,v140,v121,0 op_sel_hi:[0,1,0]
v_lshl_add_u64 v[128:129],v[128:129],0,v[130:131]
v_lshl_add_u64 v[130:131],v[132:133],0,v[134:135]
v_mfma_f32_16x16x32_bf16 v[156:159],v[84:87],v[60:63],v[156:159]
ds_read_u16_d16_hi v139,v139
v_fma_mix_f32 v141,v141,v123,0 op_sel_hi:[0,1,0]
v_fma_mix_f32 v143,v143,v123,0 op_sel:[0,1,0] op_sel_hi:[0,1,0]
v_mfma_f32_16x16x32_bf16 v[148:151],v[84:87],v[56:59],v[148:151]
ds_read_b64_tr_b16 v[110:111],v6 offset:20152
ds_read_b64_tr_b16 v[64:65],v4 offset:12832
v_lshlrev_b32_sdwa v144,1,v144 dst_sel:DWORD dst_unused:UNUSED_PAD src0_sel:DWORD src1_sel:WORD_1
v_lshlrev_b32_sdwa v146,1,v146 dst_sel:DWORD dst_unused:UNUSED_PAD src0_sel:DWORD src1_sel:WORD_1
v_mfma_f32_16x16x32_bf16 v[160:163],v[88:91],v[128:131],v[160:163]
ds_read_u16 v144,v144
v_lshlrev_b32_sdwa v145,1,v145 dst_sel:DWORD dst_unused:UNUSED_PAD src0_sel:DWORD src1_sel:WORD_1
v_lshlrev_b32_sdwa v147,1,v147 dst_sel:DWORD dst_unused:UNUSED_PAD src0_sel:DWORD src1_sel:WORD_1
v_fma_mixlo_f16 v122,v157,s99,0
v_mfma_f32_16x16x32_bf16 v[168:171],v[92:95],v[128:131],v[168:171]
s_waitcnt lgkmcnt(11)
ds_read_b64_tr_b16 v[66:67],v4 offset:14880
ds_read_b128 v[112:115],v7 offset:128
v_fma_mixlo_f16 v120,v156,s99,0
v_mfma_f32_16x16x32_bf16 v[176:179],v[96:99],v[128:131],v[176:179]
s_waitcnt lgkmcnt(10)
ds_read_u16_d16_hi v146,v146
v_lshlrev_b32_sdwa v148,1,v148 dst_sel:DWORD dst_unused:UNUSED_PAD src0_sel:DWORD src1_sel:WORD_1
v_lshlrev_b32_sdwa v150,1,v150 dst_sel:DWORD dst_unused:UNUSED_PAD src0_sel:DWORD src1_sel:WORD_1
v_mfma_f32_16x16x32_bf16 v[184:187],v[100:103],v[128:131],v[184:187]
s_waitcnt lgkmcnt(6)
ds_read_b64_tr_b16 v[68:69],v5 offset:12832
ds_read_b64_tr_b16 v[70:71],v5 offset:14880
v_lshl_add_u64 v[136:137],v[136:137],0,v[138:139]
v_fma_mixhi_f16 v120,v158,s99,0
v_mfma_f32_16x16x32_bf16 v[192:195],v[104:107],v[128:131],v[192:195]
s_waitcnt lgkmcnt(7)
ds_read_u16 v145,v145
v_cvt_pk_bf16_f32 v138,v140,v142
v_cvt_pk_bf16_f32 v139,v141,v143
v_mfma_f32_16x16x32_bf16 v[200:203],v[108:111],v[128:131],v[200:203]
v_fma_mixhi_f16 v122,v159,s99,0
v_mfma_f32_16x16x32_bf16 v[164:167],v[88:91],v[136:139],v[164:167]
ds_read_b128 v[116:119],v7 offset:160
ds_read_b64_tr_b16 v[72:73],v4 offset:16928
v_lshlrev_b32_sdwa v149,1,v149 dst_sel:DWORD dst_unused:UNUSED_PAD src0_sel:DWORD src1_sel:WORD_1
v_lshlrev_b32_sdwa v151,1,v151 dst_sel:DWORD dst_unused:UNUSED_PAD src0_sel:DWORD src1_sel:WORD_1
v_mfma_f32_16x16x32_bf16 v[172:175],v[92:95],v[136:139],v[172:175]
s_setprio 1
ds_read_b64_tr_b16 v[74:75],v4 offset:18976
ds_read_b64_tr_b16 v[76:77],v5 offset:16928
v_pk_fma_f16 v121,v120,v120,s54
v_pk_fma_f16 v123,v122,v122,s54
v_mfma_f32_16x16x32_bf16 v[180:183],v[96:99],v[136:139],v[180:183]
ds_read_u16_d16_hi v147,v147
v_pk_mul_f16 v121,v120,v121
v_pk_mul_f16 v123,v122,v123
v_mfma_f32_16x16x32_bf16 v[188:191],v[100:103],v[136:139],v[188:191]
ds_read_u16 v148,v148
v_lshlrev_b32_sdwa v152,1,v152 dst_sel:DWORD dst_unused:UNUSED_PAD src0_sel:DWORD src1_sel:WORD_1
v_lshlrev_b32_sdwa v154,1,v154 dst_sel:DWORD dst_unused:UNUSED_PAD src0_sel:DWORD src1_sel:WORD_1
v_mfma_f32_16x16x32_bf16 v[196:199],v[104:107],v[136:139],v[196:199]
v_exp_f16 v120,v121
v_mfma_f32_16x16x32_bf16 v[204:207],v[108:111],v[136:139],v[204:207]
s_waitcnt lgkmcnt(10)
ds_read_b64_tr_b16 v[78:79],v5 offset:18976
ds_read_b64_tr_b16 v[80:81],v4 offset:21024
v_exp_f16 v122,v123
v_mfma_f32_16x16x32_bf16 v[128:131],v[64:67],v[16:19],v[112:115]
v_lshlrev_b32_sdwa v153,1,v153 dst_sel:DWORD dst_unused:UNUSED_PAD src0_sel:DWORD src1_sel:WORD_1
v_lshlrev_b32_sdwa v155,1,v155 dst_sel:DWORD dst_unused:UNUSED_PAD src0_sel:DWORD src1_sel:WORD_1
v_mfma_f32_16x16x32_bf16 v[136:139],v[64:67],v[20:23],v[112:115]
s_waitcnt lgkmcnt(7)
ds_read_u16_d16_hi v150,v150
ds_read_u16 v149,v149
v_exp_f16_sdwa v120,v121 dst_sel:WORD_1 dst_unused:UNUSED_PRESERVE src0_sel:WORD_1
v_mfma_f32_16x16x32_bf16 v[140:143],v[68:71],v[20:23],v[116:119]
v_exp_f16_sdwa v122,v123 dst_sel:WORD_1 dst_unused:UNUSED_PRESERVE src0_sel:WORD_1
v_mfma_f32_16x16x32_bf16 v[132:135],v[68:71],v[16:19],v[116:119]
s_waitcnt lgkmcnt(7)
ds_read_b64_tr_b16 v[82:83],v4 offset:23072
v_pk_fma_f16 v120,v120,s55,s55
v_mfma_f32_16x16x32_bf16 v[128:131],v[72:75],v[24:27],v[128:131]
ds_read_b64_tr_b16 v[84:85],v5 offset:21024
v_pk_fma_f16 v122,v122,s55,s55
v_rcp_f16 v121,v120
v_mfma_f32_16x16x32_bf16 v[136:139],v[72:75],v[28:31],v[136:139]
s_waitcnt lgkmcnt(5)
ds_read_b64_tr_b16 v[86:87],v5 offset:23072
ds_read_u16_d16_hi v151,v151
v_rcp_f16 v123,v122
v_mfma_f32_16x16x32_bf16 v[140:143],v[76:79],v[28:31],v[140:143]
ds_read_u16 v152,v152
ds_read_u16_d16_hi v154,v154
v_rcp_f16_sdwa v121,v120 dst_sel:WORD_1 dst_unused:UNUSED_PRESERVE src0_sel:WORD_1
v_mfma_f32_16x16x32_bf16 v[132:135],v[76:79],v[24:27],v[132:135]
s_waitcnt lgkmcnt(5)
v_rcp_f16_sdwa v123,v122 dst_sel:WORD_1 dst_unused:UNUSED_PRESERVE src0_sel:WORD_1
v_mfma_f32_16x16x32_bf16 v[128:131],v[80:83],v[32:35],v[128:131]
ds_read_u16 v153,v153
v_fma_mix_f32 v156,v156,v121,0 op_sel_hi:[0,1,0]
v_fma_mix_f32 v158,v158,v121,0 op_sel:[0,1,0] op_sel_hi:[0,1,0]
v_mfma_f32_16x16x32_bf16 v[136:139],v[80:83],v[36:39],v[136:139]
s_waitcnt lgkmcnt(3)
s_setprio 0
v_lshl_add_u64 v[144:145],v[144:145],0,v[146:147]
v_lshl_add_u64 v[146:147],v[148:149],0,v[150:151]
v_mfma_f32_16x16x32_bf16 v[140:143],v[84:87],v[36:39],v[140:143]
ds_read_u16_d16_hi v155,v155
v_fma_mix_f32 v157,v157,v123,0 op_sel_hi:[0,1,0]
v_fma_mix_f32 v159,v159,v123,0 op_sel:[0,1,0] op_sel_hi:[0,1,0]
v_mfma_f32_16x16x32_bf16 v[132:135],v[84:87],v[32:35],v[132:135]
v_lshlrev_b32_sdwa v128,1,v128 dst_sel:DWORD dst_unused:UNUSED_PAD src0_sel:DWORD src1_sel:WORD_1
v_lshlrev_b32_sdwa v130,1,v130 dst_sel:DWORD dst_unused:UNUSED_PAD src0_sel:DWORD src1_sel:WORD_1
v_mfma_f32_16x16x32_bf16 v[208:211],v[88:91],v[144:147],v[208:211]
s_barrier
s_setprio 2
ds_read_u16 v128,v128
v_lshlrev_b32_sdwa v129,1,v129 dst_sel:DWORD dst_unused:UNUSED_PAD src0_sel:DWORD src1_sel:WORD_1
v_mfma_f32_16x16x32_bf16 v[216:219],v[92:95],v[144:147],v[216:219]
ds_read_u16_d16_hi v130,v130
v_lshlrev_b32_sdwa v131,1,v131 dst_sel:DWORD dst_unused:UNUSED_PAD src0_sel:DWORD src1_sel:WORD_1
v_mfma_f32_16x16x32_bf16 v[224:227],v[96:99],v[144:147],v[224:227]
s_cbranch_vccz RB35_%=
s_add_u32 m0,s58,12320
s_mul_i32 s69,s59,3
s_add_u32 s69,s69,s97
buffer_load_dwordx4 v2,s[44:47],s69 offen lds
s_add_u32 m0,m0,1024
s_add_u32 s69,s69,s53
buffer_load_dwordx4 v3,s[44:47],s69 offen lds
s_add_u32 m0,m0,1024
s_add_u32 s69,s69,s53
buffer_load_dwordx4 v2,s[44:47],s69 offen lds
s_add_u32 m0,m0,s50
s_add_u32 s69,s69,s53
buffer_load_dwordx4 v3,s[44:47],s69 offen lds
RB35_%=:
v_lshlrev_b32_sdwa v132,1,v132 dst_sel:DWORD dst_unused:UNUSED_PAD src0_sel:DWORD src1_sel:WORD_1
v_mfma_f32_16x16x32_bf16 v[232:235],v[100:103],v[144:147],v[232:235]
s_waitcnt lgkmcnt(2)
ds_read_u16 v129,v129
v_lshl_add_u64 v[152:153],v[152:153],0,v[154:155]
v_lshlrev_b32_sdwa v134,1,v134 dst_sel:DWORD dst_unused:UNUSED_PAD src0_sel:DWORD src1_sel:WORD_1
v_mfma_f32_16x16x32_bf16 v[240:243],v[104:107],v[144:147],v[240:243]
v_cvt_pk_bf16_f32 v155,v157,v159
s_mov_b32 s86,0xbe801d5a
v_cvt_pk_bf16_f32 v154,v156,v158
v_mfma_f32_16x16x32_bf16 v[248:251],v[108:111],v[144:147],v[248:251]
ds_read_u16_d16_hi v131,v131
v_lshlrev_b32_sdwa v133,1,v133 dst_sel:DWORD dst_unused:UNUSED_PAD src0_sel:DWORD src1_sel:WORD_1
v_mfma_f32_16x16x32_bf16 v[212:215],v[88:91],v[152:155],v[212:215]
s_cbranch_vccz RB36_%=
s_add_u32 m0,m0,1024
s_add_u32 s69,s69,s53
buffer_load_dwordx4 v2,s[44:47],s69 offen lds
s_add_u32 m0,m0,1024
s_add_u32 s69,s69,s53
buffer_load_dwordx4 v3,s[44:47],s69 offen lds
s_add_u32 m0,m0,s50
s_add_u32 s69,s69,s53
buffer_load_dwordx4 v2,s[44:47],s69 offen lds
s_add_u32 m0,m0,1024
s_add_u32 s69,s69,s53
buffer_load_dwordx4 v3,s[44:47],s69 offen lds
RB36_%=:
ds_read_u16 v132,v132
v_lshlrev_b32_sdwa v135,1,v135 dst_sel:DWORD dst_unused:UNUSED_PAD src0_sel:DWORD src1_sel:WORD_1
v_mfma_f32_16x16x32_bf16 v[220:223],v[92:95],v[152:155],v[220:223]
ds_read_b64_tr_b16 v[88:89],v6 offset:0
ds_read_b64_tr_b16 v[90:91],v6 offset:1536
v_lshlrev_b32_sdwa v136,1,v136 dst_sel:DWORD dst_unused:UNUSED_PAD src0_sel:DWORD src1_sel:WORD_1
v_mfma_f32_16x16x32_bf16 v[228:231],v[96:99],v[152:155],v[228:231]
ds_read_u16_d16_hi v134,v134
v_lshlrev_b32_sdwa v138,1,v138 dst_sel:DWORD dst_unused:UNUSED_PAD src0_sel:DWORD src1_sel:WORD_1
v_mfma_f32_16x16x32_bf16 v[236:239],v[100:103],v[152:155],v[236:239]
ds_read_u16 v133,v133
v_lshlrev_b32_sdwa v137,1,v137 dst_sel:DWORD dst_unused:UNUSED_PAD src0_sel:DWORD src1_sel:WORD_1
v_mfma_f32_16x16x32_bf16 v[244:247],v[104:107],v[152:155],v[244:247]
ds_read_u16_d16_hi v135,v135
v_lshlrev_b32_sdwa v139,1,v139 dst_sel:DWORD dst_unused:UNUSED_PAD src0_sel:DWORD src1_sel:WORD_1
v_mfma_f32_16x16x32_bf16 v[252:255],v[108:111],v[152:155],v[252:255]
s_cbranch_vccz RB37_%=
s_add_u32 m0,m0,1024
s_add_u32 s69,s69,s53
buffer_load_dwordx4 v2,s[44:47],s69 offen lds
s_add_u32 m0,m0,s50
s_add_u32 s69,s69,s53
buffer_load_dwordx4 v3,s[44:47],s69 offen lds
s_add_u32 m0,m0,1024
s_add_u32 s69,s69,s53
buffer_load_dwordx4 v2,s[44:47],s69 offen lds
s_add_u32 m0,m0,1024
s_add_u32 s69,s69,s53
buffer_load_dwordx4 v3,s[44:47],s69 offen lds
RB37_%=:
ds_read_b64_tr_b16 v[92:93],v6 offset:8
ds_read_b64_tr_b16 v[94:95],v6 offset:1544
v_lshlrev_b32_sdwa v140,1,v140 dst_sel:DWORD dst_unused:UNUSED_PAD src0_sel:DWORD src1_sel:WORD_1
v_mfma_f32_16x16x32_bf16 v[144:147],v[64:67],v[40:43],v[112:115]
ds_read_u16 v136,v136
v_lshlrev_b32_sdwa v142,1,v142 dst_sel:DWORD dst_unused:UNUSED_PAD src0_sel:DWORD src1_sel:WORD_1
v_mfma_f32_16x16x32_bf16 v[152:155],v[64:67],v[44:47],v[112:115]
ds_read_u16_d16_hi v138,v138
ds_read_b64_tr_b16 v[96:97],v6 offset:64
v_lshlrev_b32_sdwa v141,1,v141 dst_sel:DWORD dst_unused:UNUSED_PAD src0_sel:DWORD src1_sel:WORD_1
v_mfma_f32_16x16x32_bf16 v[156:159],v[68:71],v[44:47],v[116:119]
ds_read_b64_tr_b16 v[98:99],v6 offset:1600
ds_read_b64_tr_b16 v[100:101],v6 offset:72
v_mfma_f32_16x16x32_bf16 v[148:151],v[68:71],v[40:43],v[116:119]
ds_read_u16 v137,v137
v_lshlrev_b32_sdwa v143,1,v143 dst_sel:DWORD dst_unused:UNUSED_PAD src0_sel:DWORD src1_sel:WORD_1
v_mfma_f32_16x16x32_bf16 v[144:147],v[72:75],v[48:51],v[144:147]
ds_read_u16_d16_hi v139,v139
v_mfma_f32_16x16x32_bf16 v[152:155],v[72:75],v[52:55],v[152:155]
ds_read_b64_tr_b16 v[102:103],v6 offset:1608
ds_read_b64_tr_b16 v[104:105],v6 offset:128
v_mfma_f32_16x16x32_bf16 v[156:159],v[76:79],v[52:55],v[156:159]
ds_read_u16 v140,v140
v_mfma_f32_16x16x32_bf16 v[148:151],v[76:79],v[48:51],v[148:151]
s_waitcnt lgkmcnt(15)
ds_read_u16_d16_hi v142,v142
v_lshl_add_u64 v[128:129],v[128:129],0,v[130:131]
v_mfma_f32_16x16x32_bf16 v[144:147],v[80:83],v[56:59],v[144:147]
ds_read_b64_tr_b16 v[106:107],v6 offset:1664
ds_read_b64_tr_b16 v[108:109],v6 offset:136
v_mfma_f32_16x16x32_bf16 v[152:155],v[80:83],v[60:63],v[152:155]
s_waitcnt lgkmcnt(15)
ds_read_u16 v141,v141
v_lshl_add_u64 v[130:131],v[132:133],0,v[134:135]
v_mfma_f32_16x16x32_bf16 v[156:159],v[84:87],v[60:63],v[156:159]
ds_read_u16_d16_hi v143,v143
v_mfma_f32_16x16x32_bf16 v[148:151],v[84:87],v[56:59],v[148:151]
ds_read_b64_tr_b16 v[110:111],v6 offset:1672
ds_read_b64_tr_b16 v[64:65],v4 offset:0
v_lshlrev_b32_sdwa v144,1,v144 dst_sel:DWORD dst_unused:UNUSED_PAD src0_sel:DWORD src1_sel:WORD_1
v_lshlrev_b32_sdwa v146,1,v146 dst_sel:DWORD dst_unused:UNUSED_PAD src0_sel:DWORD src1_sel:WORD_1
v_mfma_f32_16x16x32_bf16 v[160:163],v[88:91],v[128:131],v[160:163]
s_waitcnt lgkmcnt(15)
v_lshlrev_b32_sdwa v145,1,v145 dst_sel:DWORD dst_unused:UNUSED_PAD src0_sel:DWORD src1_sel:WORD_1
v_mfma_f32_16x16x32_bf16 v[168:171],v[92:95],v[128:131],v[168:171]
s_waitcnt lgkmcnt(13)
ds_read_u16 v144,v144
v_lshlrev_b32_sdwa v147,1,v147 dst_sel:DWORD dst_unused:UNUSED_PAD src0_sel:DWORD src1_sel:WORD_1
v_mfma_f32_16x16x32_bf16 v[176:179],v[96:99],v[128:131],v[176:179]
s_waitcnt lgkmcnt(10)
ds_read_b64_tr_b16 v[66:67],v4 offset:2048
ds_read_b128 v[112:115],v7 offset:256
v_mfma_f32_16x16x32_bf16 v[184:187],v[100:103],v[128:131],v[184:187]
s_waitcnt lgkmcnt(8)
ds_read_b64_tr_b16 v[68:69],v5 offset:0
ds_read_u16_d16_hi v146,v146
v_lshlrev_b32_sdwa v148,1,v148 dst_sel:DWORD dst_unused:UNUSED_PAD src0_sel:DWORD src1_sel:WORD_1
v_lshl_add_u64 v[136:137],v[136:137],0,v[138:139]
v_lshlrev_b32_sdwa v150,1,v150 dst_sel:DWORD dst_unused:UNUSED_PAD src0_sel:DWORD src1_sel:WORD_1
v_mfma_f32_16x16x32_bf16 v[192:195],v[104:107],v[128:131],v[192:195]
s_waitcnt lgkmcnt(6)
ds_read_u16 v145,v145
ds_read_u16_d16_hi v147,v147
v_lshl_add_u64 v[138:139],v[140:141],0,v[142:143]
v_mfma_f32_16x16x32_bf16 v[200:203],v[108:111],v[128:131],v[200:203]
ds_read_b64_tr_b16 v[70:71],v5 offset:2048
ds_read_b128 v[116:119],v7 offset:288
v_mfma_f32_16x16x32_bf16 v[164:167],v[88:91],v[136:139],v[164:167]
ds_read_u16 v148,v148
v_lshlrev_b32_sdwa v149,1,v149 dst_sel:DWORD dst_unused:UNUSED_PAD src0_sel:DWORD src1_sel:WORD_1
v_lshlrev_b32_sdwa v151,1,v151 dst_sel:DWORD dst_unused:UNUSED_PAD src0_sel:DWORD src1_sel:WORD_1
v_mfma_f32_16x16x32_bf16 v[172:175],v[92:95],v[136:139],v[172:175]
ds_read_b64_tr_b16 v[72:73],v4 offset:4096
v_lshlrev_b32_sdwa v152,1,v152 dst_sel:DWORD dst_unused:UNUSED_PAD src0_sel:DWORD src1_sel:WORD_1
v_mfma_f32_16x16x32_bf16 v[180:183],v[96:99],v[136:139],v[180:183]
ds_read_b64_tr_b16 v[74:75],v4 offset:6144
ds_read_b64_tr_b16 v[76:77],v5 offset:4096
v_lshlrev_b32_sdwa v154,1,v154 dst_sel:DWORD dst_unused:UNUSED_PAD src0_sel:DWORD src1_sel:WORD_1
v_mfma_f32_16x16x32_bf16 v[188:191],v[100:103],v[136:139],v[188:191]
ds_read_u16_d16_hi v150,v150
v_lshlrev_b32_sdwa v153,1,v153 dst_sel:DWORD dst_unused:UNUSED_PAD src0_sel:DWORD src1_sel:WORD_1
v_mfma_f32_16x16x32_bf16 v[196:199],v[104:107],v[136:139],v[196:199]
ds_read_u16 v149,v149
v_lshlrev_b32_sdwa v155,1,v155 dst_sel:DWORD dst_unused:UNUSED_PAD src0_sel:DWORD src1_sel:WORD_1
v_mfma_f32_16x16x32_bf16 v[204:207],v[108:111],v[136:139],v[204:207]
s_waitcnt lgkmcnt(12)
ds_read_b64_tr_b16 v[78:79],v5 offset:6144
ds_read_b64_tr_b16 v[80:81],v4 offset:8192
v_lshlrev_b32_sdwa v156,1,v156 dst_sel:DWORD dst_unused:UNUSED_PAD src0_sel:DWORD src1_sel:WORD_1
v_mfma_f32_16x16x32_bf16 v[128:131],v[64:67],v[16:19],v[112:115]
ds_read_u16_d16_hi v151,v151
v_lshlrev_b32_sdwa v158,1,v158 dst_sel:DWORD dst_unused:UNUSED_PAD src0_sel:DWORD src1_sel:WORD_1
v_mfma_f32_16x16x32_bf16 v[136:139],v[64:67],v[20:23],v[112:115]
s_waitcnt lgkmcnt(9)
ds_read_u16 v152,v152
ds_read_b64_tr_b16 v[82:83],v4 offset:10240
v_lshlrev_b32_sdwa v157,1,v157 dst_sel:DWORD dst_unused:UNUSED_PAD src0_sel:DWORD src1_sel:WORD_1
v_mfma_f32_16x16x32_bf16 v[140:143],v[68:71],v[20:23],v[116:119]
ds_read_b64_tr_b16 v[84:85],v5 offset:8192
ds_read_b64_tr_b16 v[86:87],v5 offset:10240
v_lshlrev_b32_sdwa v159,1,v159 dst_sel:DWORD dst_unused:UNUSED_PAD src0_sel:DWORD src1_sel:WORD_1
v_mfma_f32_16x16x32_bf16 v[132:135],v[68:71],v[16:19],v[116:119]
s_waitcnt lgkmcnt(10)
ds_read_u16_d16_hi v154,v154
v_mfma_f32_16x16x32_bf16 v[128:131],v[72:75],v[24:27],v[128:131]
ds_read_u16 v153,v153
v_mfma_f32_16x16x32_bf16 v[136:139],v[72:75],v[28:31],v[136:139]
s_waitcnt lgkmcnt(8)
v_mfma_f32_16x16x32_bf16 v[140:143],v[76:79],v[28:31],v[140:143]
ds_read_u16_d16_hi v155,v155
v_lshl_add_u64 v[144:145],v[144:145],0,v[146:147]
v_mfma_f32_16x16x32_bf16 v[132:135],v[76:79],v[24:27],v[132:135]
s_waitcnt lgkmcnt(5)
ds_read_u16 v156,v156
v_mfma_f32_16x16x32_bf16 v[128:131],v[80:83],v[32:35],v[128:131]
v_lshl_add_u64 v[146:147],v[148:149],0,v[150:151]
v_mfma_f32_16x16x32_bf16 v[136:139],v[80:83],v[36:39],v[136:139]
s_waitcnt lgkmcnt(4)
ds_read_u16_d16_hi v158,v158
v_mfma_f32_16x16x32_bf16 v[140:143],v[84:87],v[36:39],v[140:143]
ds_read_u16 v157,v157
v_mfma_f32_16x16x32_bf16 v[132:135],v[84:87],v[32:35],v[132:135]
s_nop 0
v_lshlrev_b32_sdwa v128,1,v128 dst_sel:DWORD dst_unused:UNUSED_PAD src0_sel:DWORD src1_sel:WORD_1
v_lshlrev_b32_sdwa v130,1,v130 dst_sel:DWORD dst_unused:UNUSED_PAD src0_sel:DWORD src1_sel:WORD_1
v_mfma_f32_16x16x32_bf16 v[208:211],v[88:91],v[144:147],v[208:211]
ds_read_u16_d16_hi v159,v159
v_lshlrev_b32_sdwa v129,1,v129 dst_sel:DWORD dst_unused:UNUSED_PAD src0_sel:DWORD src1_sel:WORD_1
v_mfma_f32_16x16x32_bf16 v[216:219],v[92:95],v[144:147],v[216:219]
ds_read_u16 v128,v128
v_lshlrev_b32_sdwa v131,1,v131 dst_sel:DWORD dst_unused:UNUSED_PAD src0_sel:DWORD src1_sel:WORD_1
v_mfma_f32_16x16x32_bf16 v[224:227],v[96:99],v[144:147],v[224:227]
v_lshlrev_b32_sdwa v132,1,v132 dst_sel:DWORD dst_unused:UNUSED_PAD src0_sel:DWORD src1_sel:WORD_1
v_mfma_f32_16x16x32_bf16 v[232:235],v[100:103],v[144:147],v[232:235]
s_waitcnt lgkmcnt(5)
ds_read_u16_d16_hi v130,v130
v_lshl_add_u64 v[152:153],v[152:153],0,v[154:155]
v_lshlrev_b32_sdwa v134,1,v134 dst_sel:DWORD dst_unused:UNUSED_PAD src0_sel:DWORD src1_sel:WORD_1
v_mfma_f32_16x16x32_bf16 v[240:243],v[104:107],v[144:147],v[240:243]
s_waitcnt lgkmcnt(2)
v_lshl_add_u64 v[154:155],v[156:157],0,v[158:159]
v_mfma_f32_16x16x32_bf16 v[248:251],v[108:111],v[144:147],v[248:251]
ds_read_u16 v129,v129
v_lshlrev_b32_sdwa v133,1,v133 dst_sel:DWORD dst_unused:UNUSED_PAD src0_sel:DWORD src1_sel:WORD_1
v_mfma_f32_16x16x32_bf16 v[212:215],v[88:91],v[152:155],v[212:215]
ds_read_u16_d16_hi v131,v131
v_lshlrev_b32_sdwa v135,1,v135 dst_sel:DWORD dst_unused:UNUSED_PAD src0_sel:DWORD src1_sel:WORD_1
v_mfma_f32_16x16x32_bf16 v[220:223],v[92:95],v[152:155],v[220:223]
ds_read_b64_tr_b16 v[88:89],v6 offset:6160
ds_read_b64_tr_b16 v[90:91],v6 offset:7696
v_lshlrev_b32_sdwa v136,1,v136 dst_sel:DWORD dst_unused:UNUSED_PAD src0_sel:DWORD src1_sel:WORD_1
v_mfma_f32_16x16x32_bf16 v[228:231],v[96:99],v[152:155],v[228:231]
ds_read_u16 v132,v132
v_lshlrev_b32_sdwa v138,1,v138 dst_sel:DWORD dst_unused:UNUSED_PAD src0_sel:DWORD src1_sel:WORD_1
v_mfma_f32_16x16x32_bf16 v[236:239],v[100:103],v[152:155],v[236:239]
ds_read_u16_d16_hi v134,v134
v_lshlrev_b32_sdwa v137,1,v137 dst_sel:DWORD dst_unused:UNUSED_PAD src0_sel:DWORD src1_sel:WORD_1
v_mfma_f32_16x16x32_bf16 v[244:247],v[104:107],v[152:155],v[244:247]
ds_read_u16 v133,v133
v_lshlrev_b32_sdwa v139,1,v139 dst_sel:DWORD dst_unused:UNUSED_PAD src0_sel:DWORD src1_sel:WORD_1
v_mfma_f32_16x16x32_bf16 v[252:255],v[108:111],v[152:155],v[252:255]
ds_read_b64_tr_b16 v[92:93],v6 offset:6168
ds_read_b64_tr_b16 v[94:95],v6 offset:7704
v_lshlrev_b32_sdwa v140,1,v140 dst_sel:DWORD dst_unused:UNUSED_PAD src0_sel:DWORD src1_sel:WORD_1
v_mfma_f32_16x16x32_bf16 v[144:147],v[64:67],v[40:43],v[112:115]
ds_read_u16_d16_hi v135,v135
v_lshlrev_b32_sdwa v142,1,v142 dst_sel:DWORD dst_unused:UNUSED_PAD src0_sel:DWORD src1_sel:WORD_1
v_mfma_f32_16x16x32_bf16 v[152:155],v[64:67],v[44:47],v[112:115]
ds_read_u16 v136,v136
ds_read_b64_tr_b16 v[96:97],v6 offset:6224
v_lshlrev_b32_sdwa v141,1,v141 dst_sel:DWORD dst_unused:UNUSED_PAD src0_sel:DWORD src1_sel:WORD_1
v_mfma_f32_16x16x32_bf16 v[156:159],v[68:71],v[44:47],v[116:119]
ds_read_b64_tr_b16 v[98:99],v6 offset:7760
ds_read_b64_tr_b16 v[100:101],v6 offset:6232
v_mfma_f32_16x16x32_bf16 v[148:151],v[68:71],v[40:43],v[116:119]
ds_read_u16_d16_hi v138,v138
v_lshlrev_b32_sdwa v143,1,v143 dst_sel:DWORD dst_unused:UNUSED_PAD src0_sel:DWORD src1_sel:WORD_1
v_mfma_f32_16x16x32_bf16 v[144:147],v[72:75],v[48:51],v[144:147]
ds_read_u16 v137,v137
v_mfma_f32_16x16x32_bf16 v[152:155],v[72:75],v[52:55],v[152:155]
ds_read_b64_tr_b16 v[102:103],v6 offset:7768
ds_read_b64_tr_b16 v[104:105],v6 offset:6288
v_mfma_f32_16x16x32_bf16 v[156:159],v[76:79],v[52:55],v[156:159]
ds_read_u16_d16_hi v139,v139
v_mfma_f32_16x16x32_bf16 v[148:151],v[76:79],v[48:51],v[148:151]
s_waitcnt lgkmcnt(15)
ds_read_u16 v140,v140
v_lshl_add_u64 v[128:129],v[128:129],0,v[130:131]
v_mfma_f32_16x16x32_bf16 v[144:147],v[80:83],v[56:59],v[144:147]
ds_read_b64_tr_b16 v[106:107],v6 offset:7824
ds_read_b64_tr_b16 v[108:109],v6 offset:6296
v_mfma_f32_16x16x32_bf16 v[152:155],v[80:83],v[60:63],v[152:155]
s_waitcnt lgkmcnt(12)
ds_read_u16_d16_hi v142,v142
v_lshl_add_u64 v[130:131],v[132:133],0,v[134:135]
v_mfma_f32_16x16x32_bf16 v[156:159],v[84:87],v[60:63],v[156:159]
ds_read_u16 v141,v141
ds_read_u16_d16_hi v143,v143
v_mfma_f32_16x16x32_bf16 v[148:151],v[84:87],v[56:59],v[148:151]
ds_read_b64_tr_b16 v[110:111],v6 offset:7832
ds_read_b64_tr_b16 v[64:65],v4 offset:512
v_lshlrev_b32_sdwa v144,1,v144 dst_sel:DWORD dst_unused:UNUSED_PAD src0_sel:DWORD src1_sel:WORD_1
v_lshlrev_b32_sdwa v146,1,v146 dst_sel:DWORD dst_unused:UNUSED_PAD src0_sel:DWORD src1_sel:WORD_1
v_mfma_f32_16x16x32_bf16 v[160:163],v[88:91],v[128:131],v[160:163]
v_lshlrev_b32_sdwa v145,1,v145 dst_sel:DWORD dst_unused:UNUSED_PAD src0_sel:DWORD src1_sel:WORD_1
v_mfma_f32_16x16x32_bf16 v[168:171],v[92:95],v[128:131],v[168:171]
s_waitcnt lgkmcnt(14)
ds_read_u16 v144,v144
v_lshlrev_b32_sdwa v147,1,v147 dst_sel:DWORD dst_unused:UNUSED_PAD src0_sel:DWORD src1_sel:WORD_1
v_mfma_f32_16x16x32_bf16 v[176:179],v[96:99],v[128:131],v[176:179]
s_waitcnt lgkmcnt(11)
ds_read_b64_tr_b16 v[66:67],v4 offset:2560
ds_read_b128 v[112:115],v7 offset:384
v_mfma_f32_16x16x32_bf16 v[184:187],v[100:103],v[128:131],v[184:187]
s_waitcnt lgkmcnt(9)
ds_read_b64_tr_b16 v[68:69],v5 offset:512
ds_read_u16_d16_hi v146,v146
v_lshlrev_b32_sdwa v148,1,v148 dst_sel:DWORD dst_unused:UNUSED_PAD src0_sel:DWORD src1_sel:WORD_1
v_lshl_add_u64 v[136:137],v[136:137],0,v[138:139]
v_lshlrev_b32_sdwa v150,1,v150 dst_sel:DWORD dst_unused:UNUSED_PAD src0_sel:DWORD src1_sel:WORD_1
v_mfma_f32_16x16x32_bf16 v[192:195],v[104:107],v[128:131],v[192:195]
s_waitcnt lgkmcnt(6)
ds_read_u16 v145,v145
ds_read_u16_d16_hi v147,v147
v_lshl_add_u64 v[138:139],v[140:141],0,v[142:143]
v_mfma_f32_16x16x32_bf16 v[200:203],v[108:111],v[128:131],v[200:203]
ds_read_b64_tr_b16 v[70:71],v5 offset:2560
ds_read_b128 v[116:119],v7 offset:416
v_mfma_f32_16x16x32_bf16 v[164:167],v[88:91],v[136:139],v[164:167]
ds_read_u16 v148,v148
v_lshlrev_b32_sdwa v149,1,v149 dst_sel:DWORD dst_unused:UNUSED_PAD src0_sel:DWORD src1_sel:WORD_1
v_lshlrev_b32_sdwa v151,1,v151 dst_sel:DWORD dst_unused:UNUSED_PAD src0_sel:DWORD src1_sel:WORD_1
v_mfma_f32_16x16x32_bf16 v[172:175],v[92:95],v[136:139],v[172:175]
ds_read_b64_tr_b16 v[72:73],v4 offset:4608
v_lshlrev_b32_sdwa v152,1,v152 dst_sel:DWORD dst_unused:UNUSED_PAD src0_sel:DWORD src1_sel:WORD_1
v_mfma_f32_16x16x32_bf16 v[180:183],v[96:99],v[136:139],v[180:183]
ds_read_b64_tr_b16 v[74:75],v4 offset:6656
ds_read_b64_tr_b16 v[76:77],v5 offset:4608
v_lshlrev_b32_sdwa v154,1,v154 dst_sel:DWORD dst_unused:UNUSED_PAD src0_sel:DWORD src1_sel:WORD_1
v_mfma_f32_16x16x32_bf16 v[188:191],v[100:103],v[136:139],v[188:191]
ds_read_u16_d16_hi v150,v150
v_lshlrev_b32_sdwa v153,1,v153 dst_sel:DWORD dst_unused:UNUSED_PAD src0_sel:DWORD src1_sel:WORD_1
v_mfma_f32_16x16x32_bf16 v[196:199],v[104:107],v[136:139],v[196:199]
ds_read_u16 v149,v149
v_lshlrev_b32_sdwa v155,1,v155 dst_sel:DWORD dst_unused:UNUSED_PAD src0_sel:DWORD src1_sel:WORD_1
v_mfma_f32_16x16x32_bf16 v[204:207],v[108:111],v[136:139],v[204:207]
s_waitcnt lgkmcnt(12)
ds_read_b64_tr_b16 v[78:79],v5 offset:6656
ds_read_b64_tr_b16 v[80:81],v4 offset:8704
v_lshlrev_b32_sdwa v156,1,v156 dst_sel:DWORD dst_unused:UNUSED_PAD src0_sel:DWORD src1_sel:WORD_1
v_mfma_f32_16x16x32_bf16 v[128:131],v[64:67],v[16:19],v[112:115]
ds_read_u16_d16_hi v151,v151
v_lshlrev_b32_sdwa v158,1,v158 dst_sel:DWORD dst_unused:UNUSED_PAD src0_sel:DWORD src1_sel:WORD_1
v_mfma_f32_16x16x32_bf16 v[136:139],v[64:67],v[20:23],v[112:115]
s_waitcnt lgkmcnt(9)
ds_read_u16 v152,v152
ds_read_b64_tr_b16 v[82:83],v4 offset:10752
v_lshlrev_b32_sdwa v157,1,v157 dst_sel:DWORD dst_unused:UNUSED_PAD src0_sel:DWORD src1_sel:WORD_1
v_mfma_f32_16x16x32_bf16 v[140:143],v[68:71],v[20:23],v[116:119]
ds_read_b64_tr_b16 v[84:85],v5 offset:8704
ds_read_b64_tr_b16 v[86:87],v5 offset:10752
v_lshlrev_b32_sdwa v159,1,v159 dst_sel:DWORD dst_unused:UNUSED_PAD src0_sel:DWORD src1_sel:WORD_1
v_mfma_f32_16x16x32_bf16 v[132:135],v[68:71],v[16:19],v[116:119]
s_waitcnt lgkmcnt(10)
ds_read_u16_d16_hi v154,v154
v_mfma_f32_16x16x32_bf16 v[128:131],v[72:75],v[24:27],v[128:131]
ds_read_u16 v153,v153
v_mfma_f32_16x16x32_bf16 v[136:139],v[72:75],v[28:31],v[136:139]
s_waitcnt lgkmcnt(8)
v_mfma_f32_16x16x32_bf16 v[140:143],v[76:79],v[28:31],v[140:143]
ds_read_u16_d16_hi v155,v155
v_lshl_add_u64 v[144:145],v[144:145],0,v[146:147]
v_mfma_f32_16x16x32_bf16 v[132:135],v[76:79],v[24:27],v[132:135]
s_waitcnt lgkmcnt(5)
ds_read_u16 v156,v156
v_mfma_f32_16x16x32_bf16 v[128:131],v[80:83],v[32:35],v[128:131]
v_lshl_add_u64 v[146:147],v[148:149],0,v[150:151]
v_mfma_f32_16x16x32_bf16 v[136:139],v[80:83],v[36:39],v[136:139]
s_waitcnt lgkmcnt(4)
ds_read_u16_d16_hi v158,v158
v_mfma_f32_16x16x32_bf16 v[140:143],v[84:87],v[36:39],v[140:143]
ds_read_u16 v157,v157
v_mfma_f32_16x16x32_bf16 v[132:135],v[84:87],v[32:35],v[132:135]
s_nop 0
v_lshlrev_b32_sdwa v128,1,v128 dst_sel:DWORD dst_unused:UNUSED_PAD src0_sel:DWORD src1_sel:WORD_1
v_lshlrev_b32_sdwa v130,1,v130 dst_sel:DWORD dst_unused:UNUSED_PAD src0_sel:DWORD src1_sel:WORD_1
v_mfma_f32_16x16x32_bf16 v[208:211],v[88:91],v[144:147],v[208:211]
s_waitcnt vmcnt(0)
s_barrier
s_setprio 3
ds_read_u16_d16_hi v159,v159
v_fma_mixlo_f16 v120,v140,s99,0
v_lshlrev_b32_sdwa v129,1,v129 dst_sel:DWORD dst_unused:UNUSED_PAD src0_sel:DWORD src1_sel:WORD_1
v_lshlrev_b32_sdwa v131,1,v131 dst_sel:DWORD dst_unused:UNUSED_PAD src0_sel:DWORD src1_sel:WORD_1
v_mfma_f32_16x16x32_bf16 v[216:219],v[92:95],v[144:147],v[216:219]
s_cbranch_vccz RB38_%=
s_add_u32 m0,s58,0
s_mul_i32 s69,s59,5
s_add_u32 s69,s69,s97
buffer_load_dwordx4 v2,s[44:47],s69 offen lds
s_add_u32 m0,m0,1024
s_add_u32 s69,s69,s53
buffer_load_dwordx4 v3,s[44:47],s69 offen lds
s_add_u32 m0,m0,1024
s_add_u32 s69,s69,s53
buffer_load_dwordx4 v2,s[44:47],s69 offen lds
s_add_u32 m0,m0,s50
s_add_u32 s69,s69,s53
buffer_load_dwordx4 v3,s[44:47],s69 offen lds
RB38_%=:
v_fma_mixlo_f16 v122,v141,s99,0
v_lshlrev_b32_sdwa v132,1,v132 dst_sel:DWORD dst_unused:UNUSED_PAD src0_sel:DWORD src1_sel:WORD_1
v_mfma_f32_16x16x32_bf16 v[224:227],v[96:99],v[144:147],v[224:227]
v_lshlrev_b32_sdwa v134,1,v134 dst_sel:DWORD dst_unused:UNUSED_PAD src0_sel:DWORD src1_sel:WORD_1
v_mfma_f32_16x16x32_bf16 v[232:235],v[100:103],v[144:147],v[232:235]
s_waitcnt lgkmcnt(4)
ds_read_u16 v128,v128
v_lshl_add_u64 v[152:153],v[152:153],0,v[154:155]
v_mfma_f32_16x16x32_bf16 v[240:243],v[104:107],v[144:147],v[240:243]
s_waitcnt lgkmcnt(1)
ds_read_u16_d16_hi v130,v130
v_lshl_add_u64 v[154:155],v[156:157],0,v[158:159]
v_mfma_f32_16x16x32_bf16 v[248:251],v[108:111],v[144:147],v[248:251]
v_fma_mixhi_f16 v120,v142,s99,0
v_fma_mixhi_f16 v122,v143,s99,0
v_mfma_f32_16x16x32_bf16 v[212:215],v[88:91],v[152:155],v[212:215]
s_cbranch_vccz RB39_%=
s_add_u32 m0,m0,1024
s_add_u32 s69,s69,s53
buffer_load_dwordx4 v2,s[44:47],s69 offen lds
s_add_u32 m0,m0,1024
s_add_u32 s69,s69,s53
buffer_load_dwordx4 v3,s[44:47],s69 offen lds
s_add_u32 m0,m0,s50
s_add_u32 s69,s69,s53
buffer_load_dwordx4 v2,s[44:47],s69 offen lds
s_add_u32 m0,m0,1024
s_add_u32 s69,s69,s53
buffer_load_dwordx4 v3,s[44:47],s69 offen lds
RB39_%=:
ds_read_u16 v129,v129
v_lshlrev_b32_sdwa v133,1,v133 dst_sel:DWORD dst_unused:UNUSED_PAD src0_sel:DWORD src1_sel:WORD_1
v_lshlrev_b32_sdwa v135,1,v135 dst_sel:DWORD dst_unused:UNUSED_PAD src0_sel:DWORD src1_sel:WORD_1
v_mfma_f32_16x16x32_bf16 v[220:223],v[92:95],v[152:155],v[220:223]
v_pk_fma_f16 v121,v120,v120,s54
v_pk_fma_f16 v123,v122,v122,s54
v_lshlrev_b32_sdwa v136,1,v136 dst_sel:DWORD dst_unused:UNUSED_PAD src0_sel:DWORD src1_sel:WORD_1
v_mfma_f32_16x16x32_bf16 v[228:231],v[96:99],v[152:155],v[228:231]
ds_read_u16_d16_hi v131,v131
v_pk_mul_f16 v121,v120,v121
v_pk_mul_f16 v123,v122,v123
v_mfma_f32_16x16x32_bf16 v[236:239],v[100:103],v[152:155],v[236:239]
s_cbranch_vccz RB40_%=
s_add_u32 m0,m0,1024
s_add_u32 s69,s69,s53
buffer_load_dwordx4 v2,s[44:47],s69 offen lds
s_add_u32 m0,m0,s50
s_add_u32 s69,s69,s53
buffer_load_dwordx4 v3,s[44:47],s69 offen lds
s_add_u32 m0,m0,1024
s_add_u32 s69,s69,s53
buffer_load_dwordx4 v2,s[44:47],s69 offen lds
s_add_u32 m0,m0,1024
s_add_u32 s69,s69,s53
buffer_load_dwordx4 v3,s[44:47],s69 offen lds
RB40_%=:
ds_read_b64_tr_b16 v[88:89],v6 offset:12320
ds_read_b64_tr_b16 v[90:91],v6 offset:13856
v_mfma_f32_16x16x32_bf16 v[244:247],v[104:107],v[152:155],v[244:247]
ds_read_u16 v132,v132
v_exp_f16 v120,v121
v_mfma_f32_16x16x32_bf16 v[252:255],v[108:111],v[152:155],v[252:255]
ds_read_b64_tr_b16 v[92:93],v6 offset:12328
v_lshlrev_b32_sdwa v138,1,v138 dst_sel:DWORD dst_unused:UNUSED_PAD src0_sel:DWORD src1_sel:WORD_1
v_exp_f16 v122,v123
v_mfma_f32_16x16x32_bf16 v[144:147],v[64:67],v[40:43],v[112:115]
ds_read_u16_d16_hi v134,v134
v_lshlrev_b32_sdwa v137,1,v137 dst_sel:DWORD dst_unused:UNUSED_PAD src0_sel:DWORD src1_sel:WORD_1
v_lshlrev_b32_sdwa v139,1,v139 dst_sel:DWORD dst_unused:UNUSED_PAD src0_sel:DWORD src1_sel:WORD_1
v_mfma_f32_16x16x32_bf16 v[152:155],v[64:67],v[44:47],v[112:115]
ds_read_b64_tr_b16 v[94:95],v6 offset:13864
v_exp_f16_sdwa v120,v121 dst_sel:WORD_1 dst_unused:UNUSED_PRESERVE src0_sel:WORD_1
v_mfma_f32_16x16x32_bf16 v[156:159],v[68:71],v[44:47],v[116:119]
ds_read_u16 v133,v133
v_exp_f16_sdwa v122,v123 dst_sel:WORD_1 dst_unused:UNUSED_PRESERVE src0_sel:WORD_1
v_mfma_f32_16x16x32_bf16 v[148:151],v[68:71],v[40:43],v[116:119]
ds_read_u16_d16_hi v135,v135
ds_read_b64_tr_b16 v[96:97],v6 offset:12384
v_pk_fma_f16 v120,v120,s55,s55
v_mfma_f32_16x16x32_bf16 v[144:147],v[72:75],v[48:51],v[144:147]
v_rcp_f16 v121,v120
v_pk_fma_f16 v122,v122,s55,s55
v_mfma_f32_16x16x32_bf16 v[152:155],v[72:75],v[52:55],v[152:155]
ds_read_b64_tr_b16 v[98:99],v6 offset:13920
v_rcp_f16 v123,v122
v_mfma_f32_16x16x32_bf16 v[156:159],v[76:79],v[52:55],v[156:159]
ds_read_u16 v136,v136
v_rcp_f16_sdwa v121,v120 dst_sel:WORD_1 dst_unused:UNUSED_PRESERVE src0_sel:WORD_1
v_mfma_f32_16x16x32_bf16 v[148:151],v[76:79],v[48:51],v[148:151]
ds_read_b64_tr_b16 v[100:101],v6 offset:12392
ds_read_b64_tr_b16 v[102:103],v6 offset:13928
v_rcp_f16_sdwa v123,v122 dst_sel:WORD_1 dst_unused:UNUSED_PRESERVE src0_sel:WORD_1
v_mfma_f32_16x16x32_bf16 v[144:147],v[80:83],v[56:59],v[144:147]
s_waitcnt lgkmcnt(13)
ds_read_u16_d16_hi v138,v138
v_lshl_add_u64 v[128:129],v[128:129],0,v[130:131]
v_fma_mix_f32 v140,v140,v121,0 op_sel_hi:[0,1,0]
v_fma_mix_f32 v142,v142,v121,0 op_sel:[0,1,0] op_sel_hi:[0,1,0]
v_mfma_f32_16x16x32_bf16 v[152:155],v[80:83],v[60:63],v[152:155]
ds_read_b64_tr_b16 v[104:105],v6 offset:12448
ds_read_b64_tr_b16 v[106:107],v6 offset:13984
v_fma_mix_f32 v141,v141,v123,0 op_sel_hi:[0,1,0]
v_mfma_f32_16x16x32_bf16 v[156:159],v[84:87],v[60:63],v[156:159]
s_waitcnt lgkmcnt(8)
ds_read_u16 v137,v137
v_lshl_add_u64 v[130:131],v[132:133],0,v[134:135]
v_fma_mix_f32 v143,v143,v123,0 op_sel:[0,1,0] op_sel_hi:[0,1,0]
v_mfma_f32_16x16x32_bf16 v[148:151],v[84:87],v[56:59],v[148:151]
ds_read_u16_d16_hi v139,v139
v_lshlrev_b32_sdwa v144,1,v144 dst_sel:DWORD dst_unused:UNUSED_PAD src0_sel:DWORD src1_sel:WORD_1
v_lshlrev_b32_sdwa v146,1,v146 dst_sel:DWORD dst_unused:UNUSED_PAD src0_sel:DWORD src1_sel:WORD_1
v_mfma_f32_16x16x32_bf16 v[160:163],v[88:91],v[128:131],v[160:163]
ds_read_b64_tr_b16 v[108:109],v6 offset:12456
ds_read_b64_tr_b16 v[110:111],v6 offset:13992
v_lshlrev_b32_sdwa v145,1,v145 dst_sel:DWORD dst_unused:UNUSED_PAD src0_sel:DWORD src1_sel:WORD_1
v_lshlrev_b32_sdwa v147,1,v147 dst_sel:DWORD dst_unused:UNUSED_PAD src0_sel:DWORD src1_sel:WORD_1
v_mfma_f32_16x16x32_bf16 v[168:171],v[92:95],v[128:131],v[168:171]
s_waitcnt lgkmcnt(10)
ds_read_b64_tr_b16 v[64:65],v4 offset:12320
ds_read_b64_tr_b16 v[66:67],v4 offset:14368
v_fma_mixlo_f16 v120,v156,s99,0
v_mfma_f32_16x16x32_bf16 v[176:179],v[96:99],v[128:131],v[176:179]
s_waitcnt lgkmcnt(9)
ds_read_b128 v[112:115],v7 offset:512
ds_read_u16 v144,v144
v_fma_mixlo_f16 v122,v157,s99,0
v_lshlrev_b32_sdwa v148,1,v148 dst_sel:DWORD dst_unused:UNUSED_PAD src0_sel:DWORD src1_sel:WORD_1
v_lshlrev_b32_sdwa v150,1,v150 dst_sel:DWORD dst_unused:UNUSED_PAD src0_sel:DWORD src1_sel:WORD_1
v_mfma_f32_16x16x32_bf16 v[184:187],v[100:103],v[128:131],v[184:187]
s_waitcnt lgkmcnt(6)
ds_read_u16_d16_hi v146,v146
v_lshl_add_u64 v[136:137],v[136:137],0,v[138:139]
v_mfma_f32_16x16x32_bf16 v[192:195],v[104:107],v[128:131],v[192:195]
s_waitcnt lgkmcnt(5)
ds_read_b64_tr_b16 v[68:69],v5 offset:12320
v_fma_mixhi_f16 v120,v158,s99,0
v_cvt_pk_bf16_f32 v138,v140,v142
v_cvt_pk_bf16_f32 v139,v141,v143
v_mfma_f32_16x16x32_bf16 v[200:203],v[108:111],v[128:131],v[200:203]
ds_read_b64_tr_b16 v[70:71],v5 offset:14368
ds_read_b128 v[116:119],v7 offset:544
v_fma_mixhi_f16 v122,v159,s99,0
v_mfma_f32_16x16x32_bf16 v[164:167],v[88:91],v[136:139],v[164:167]
v_lshlrev_b32_sdwa v149,1,v149 dst_sel:DWORD dst_unused:UNUSED_PAD src0_sel:DWORD src1_sel:WORD_1
v_lshlrev_b32_sdwa v151,1,v151 dst_sel:DWORD dst_unused:UNUSED_PAD src0_sel:DWORD src1_sel:WORD_1
v_mfma_f32_16x16x32_bf16 v[172:175],v[92:95],v[136:139],v[172:175]
ds_read_b64_tr_b16 v[72:73],v4 offset:16416
ds_read_b64_tr_b16 v[74:75],v4 offset:18464
v_pk_fma_f16 v123,v122,v122,s54
v_pk_fma_f16 v121,v120,v120,s54
v_mfma_f32_16x16x32_bf16 v[180:183],v[96:99],v[136:139],v[180:183]
ds_read_u16 v145,v145
v_pk_mul_f16 v121,v120,v121
v_pk_mul_f16 v123,v122,v123
v_mfma_f32_16x16x32_bf16 v[188:191],v[100:103],v[136:139],v[188:191]
ds_read_b64_tr_b16 v[76:77],v5 offset:16416
v_lshlrev_b32_sdwa v152,1,v152 dst_sel:DWORD dst_unused:UNUSED_PAD src0_sel:DWORD src1_sel:WORD_1
v_mfma_f32_16x16x32_bf16 v[196:199],v[104:107],v[136:139],v[196:199]
ds_read_u16_d16_hi v147,v147
v_lshlrev_b32_sdwa v154,1,v154 dst_sel:DWORD dst_unused:UNUSED_PAD src0_sel:DWORD src1_sel:WORD_1
v_exp_f16 v120,v121
v_mfma_f32_16x16x32_bf16 v[204:207],v[108:111],v[136:139],v[204:207]
s_waitcnt lgkmcnt(10)
ds_read_b64_tr_b16 v[78:79],v5 offset:18464
v_exp_f16 v122,v123
v_mfma_f32_16x16x32_bf16 v[128:131],v[64:67],v[16:19],v[112:115]
ds_read_u16 v148,v148
ds_read_u16_d16_hi v150,v150
v_lshlrev_b32_sdwa v153,1,v153 dst_sel:DWORD dst_unused:UNUSED_PAD src0_sel:DWORD src1_sel:WORD_1
v_exp_f16_sdwa v122,v123 dst_sel:WORD_1 dst_unused:UNUSED_PRESERVE src0_sel:WORD_1
v_mfma_f32_16x16x32_bf16 v[136:139],v[64:67],v[20:23],v[112:115]
s_waitcnt lgkmcnt(8)
v_lshlrev_b32_sdwa v155,1,v155 dst_sel:DWORD dst_unused:UNUSED_PAD src0_sel:DWORD src1_sel:WORD_1
v_exp_f16_sdwa v120,v121 dst_sel:WORD_1 dst_unused:UNUSED_PRESERVE src0_sel:WORD_1
v_mfma_f32_16x16x32_bf16 v[140:143],v[68:71],v[20:23],v[116:119]
ds_read_b64_tr_b16 v[80:81],v4 offset:20512
ds_read_b64_tr_b16 v[82:83],v4 offset:22560
v_pk_fma_f16 v120,v120,s55,s55
v_mfma_f32_16x16x32_bf16 v[132:135],v[68:71],v[16:19],v[116:119]
s_waitcnt lgkmcnt(8)
ds_read_u16 v149,v149
v_pk_fma_f16 v122,v122,s55,s55
v_mfma_f32_16x16x32_bf16 v[128:131],v[72:75],v[24:27],v[128:131]
ds_read_b64_tr_b16 v[84:85],v5 offset:20512
ds_read_u16_d16_hi v151,v151
v_rcp_f16 v121,v120
v_mfma_f32_16x16x32_bf16 v[136:139],v[72:75],v[28:31],v[136:139]
s_waitcnt lgkmcnt(7)
ds_read_b64_tr_b16 v[86:87],v5 offset:22560
v_rcp_f16 v123,v122
v_mfma_f32_16x16x32_bf16 v[140:143],v[76:79],v[28:31],v[140:143]
ds_read_u16 v152,v152
v_rcp_f16_sdwa v121,v120 dst_sel:WORD_1 dst_unused:UNUSED_PRESERVE src0_sel:WORD_1
v_mfma_f32_16x16x32_bf16 v[132:135],v[76:79],v[24:27],v[132:135]
s_waitcnt lgkmcnt(5)
v_rcp_f16_sdwa v123,v122 dst_sel:WORD_1 dst_unused:UNUSED_PRESERVE src0_sel:WORD_1
v_mfma_f32_16x16x32_bf16 v[128:131],v[80:83],v[32:35],v[128:131]
ds_read_u16_d16_hi v154,v154
v_fma_mix_f32 v156,v156,v121,0 op_sel_hi:[0,1,0]
s_nop 0
s_mov_b32 s86,0xbe801d5a
v_mfma_f32_16x16x32_bf16 v[136:139],v[80:83],v[36:39],v[136:139]
s_waitcnt lgkmcnt(2)
v_lshl_add_u64 v[144:145],v[144:145],0,v[146:147]
v_fma_mix_f32 v158,v158,v121,0 op_sel:[0,1,0] op_sel_hi:[0,1,0]
v_lshl_add_u64 v[146:147],v[148:149],0,v[150:151]
v_mfma_f32_16x16x32_bf16 v[140:143],v[84:87],v[36:39],v[140:143]
ds_read_u16 v153,v153
v_fma_mix_f32 v157,v157,v123,0 op_sel_hi:[0,1,0]
v_fma_mix_f32 v159,v159,v123,0 op_sel:[0,1,0] op_sel_hi:[0,1,0]
v_mfma_f32_16x16x32_bf16 v[132:135],v[84:87],v[32:35],v[132:135]
v_lshlrev_b32_sdwa v128,1,v128 dst_sel:DWORD dst_unused:UNUSED_PAD src0_sel:DWORD src1_sel:WORD_1
v_lshlrev_b32_sdwa v130,1,v130 dst_sel:DWORD dst_unused:UNUSED_PAD src0_sel:DWORD src1_sel:WORD_1
v_mfma_f32_16x16x32_bf16 v[208:211],v[88:91],v[144:147],v[208:211]
ds_read_u16_d16_hi v155,v155
v_fma_mixlo_f16 v120,v140,s99,0
v_lshlrev_b32_sdwa v129,1,v129 dst_sel:DWORD dst_unused:UNUSED_PAD src0_sel:DWORD src1_sel:WORD_1
v_lshlrev_b32_sdwa v131,1,v131 dst_sel:DWORD dst_unused:UNUSED_PAD src0_sel:DWORD src1_sel:WORD_1
v_mfma_f32_16x16x32_bf16 v[216:219],v[92:95],v[144:147],v[216:219]
v_fma_mixlo_f16 v122,v141,s99,0
v_lshlrev_b32_sdwa v132,1,v132 dst_sel:DWORD dst_unused:UNUSED_PAD src0_sel:DWORD src1_sel:WORD_1
v_mfma_f32_16x16x32_bf16 v[224:227],v[96:99],v[144:147],v[224:227]
v_lshlrev_b32_sdwa v134,1,v134 dst_sel:DWORD dst_unused:UNUSED_PAD src0_sel:DWORD src1_sel:WORD_1
v_mfma_f32_16x16x32_bf16 v[232:235],v[100:103],v[144:147],v[232:235]
s_waitcnt lgkmcnt(0)
ds_read_u16 v128,v128
v_lshl_add_u64 v[152:153],v[152:153],0,v[154:155]
v_mfma_f32_16x16x32_bf16 v[240:243],v[104:107],v[144:147],v[240:243]
ds_read_u16_d16_hi v130,v130
v_cvt_pk_bf16_f32 v155,v157,v159
v_cvt_pk_bf16_f32 v154,v156,v158
v_mfma_f32_16x16x32_bf16 v[248:251],v[108:111],v[144:147],v[248:251]
v_fma_mixhi_f16 v120,v142,s99,0
v_fma_mixhi_f16 v122,v143,s99,0
v_mfma_f32_16x16x32_bf16 v[212:215],v[88:91],v[152:155],v[212:215]
s_setprio 2
ds_read_u16 v129,v129
v_lshlrev_b32_sdwa v133,1,v133 dst_sel:DWORD dst_unused:UNUSED_PAD src0_sel:DWORD src1_sel:WORD_1
v_lshlrev_b32_sdwa v135,1,v135 dst_sel:DWORD dst_unused:UNUSED_PAD src0_sel:DWORD src1_sel:WORD_1
v_mfma_f32_16x16x32_bf16 v[220:223],v[92:95],v[152:155],v[220:223]
v_pk_fma_f16 v121,v120,v120,s54
v_pk_fma_f16 v123,v122,v122,s54
v_lshlrev_b32_sdwa v136,1,v136 dst_sel:DWORD dst_unused:UNUSED_PAD src0_sel:DWORD src1_sel:WORD_1
v_mfma_f32_16x16x32_bf16 v[228:231],v[96:99],v[152:155],v[228:231]
ds_read_u16_d16_hi v131,v131
v_pk_mul_f16 v121,v120,v121
v_pk_mul_f16 v123,v122,v123
v_mfma_f32_16x16x32_bf16 v[236:239],v[100:103],v[152:155],v[236:239]
ds_read_b64_tr_b16 v[88:89],v6 offset:18480
ds_read_b64_tr_b16 v[90:91],v6 offset:20016
v_mfma_f32_16x16x32_bf16 v[244:247],v[104:107],v[152:155],v[244:247]
ds_read_u16 v132,v132
v_exp_f16 v120,v121
v_mfma_f32_16x16x32_bf16 v[252:255],v[108:111],v[152:155],v[252:255]
ds_read_b64_tr_b16 v[92:93],v6 offset:18488
v_lshlrev_b32_sdwa v138,1,v138 dst_sel:DWORD dst_unused:UNUSED_PAD src0_sel:DWORD src1_sel:WORD_1
v_exp_f16 v122,v123
v_mfma_f32_16x16x32_bf16 v[144:147],v[64:67],v[40:43],v[112:115]
ds_read_u16_d16_hi v134,v134
v_lshlrev_b32_sdwa v137,1,v137 dst_sel:DWORD dst_unused:UNUSED_PAD src0_sel:DWORD src1_sel:WORD_1
v_lshlrev_b32_sdwa v139,1,v139 dst_sel:DWORD dst_unused:UNUSED_PAD src0_sel:DWORD src1_sel:WORD_1
v_mfma_f32_16x16x32_bf16 v[152:155],v[64:67],v[44:47],v[112:115]
ds_read_b64_tr_b16 v[94:95],v6 offset:20024
v_exp_f16_sdwa v120,v121 dst_sel:WORD_1 dst_unused:UNUSED_PRESERVE src0_sel:WORD_1
v_mfma_f32_16x16x32_bf16 v[156:159],v[68:71],v[44:47],v[116:119]
ds_read_u16 v133,v133
v_exp_f16_sdwa v122,v123 dst_sel:WORD_1 dst_unused:UNUSED_PRESERVE src0_sel:WORD_1
v_mfma_f32_16x16x32_bf16 v[148:151],v[68:71],v[40:43],v[116:119]
ds_read_u16_d16_hi v135,v135
ds_read_b64_tr_b16 v[96:97],v6 offset:18544
v_pk_fma_f16 v120,v120,s55,s55
v_mfma_f32_16x16x32_bf16 v[144:147],v[72:75],v[48:51],v[144:147]
v_rcp_f16 v121,v120
v_pk_fma_f16 v122,v122,s55,s55
v_mfma_f32_16x16x32_bf16 v[152:155],v[72:75],v[52:55],v[152:155]
ds_read_b64_tr_b16 v[98:99],v6 offset:20080
v_rcp_f16 v123,v122
v_mfma_f32_16x16x32_bf16 v[156:159],v[76:79],v[52:55],v[156:159]
ds_read_u16 v136,v136
v_rcp_f16_sdwa v121,v120 dst_sel:WORD_1 dst_unused:UNUSED_PRESERVE src0_sel:WORD_1
v_mfma_f32_16x16x32_bf16 v[148:151],v[76:79],v[48:51],v[148:151]
ds_read_b64_tr_b16 v[100:101],v6 offset:18552
ds_read_b64_tr_b16 v[102:103],v6 offset:20088
v_rcp_f16_sdwa v123,v122 dst_sel:WORD_1 dst_unused:UNUSED_PRESERVE src0_sel:WORD_1
v_mfma_f32_16x16x32_bf16 v[144:147],v[80:83],v[56:59],v[144:147]
s_waitcnt lgkmcnt(13)
ds_read_u16_d16_hi v138,v138
v_lshl_add_u64 v[128:129],v[128:129],0,v[130:131]
v_fma_mix_f32 v140,v140,v121,0 op_sel_hi:[0,1,0]
v_fma_mix_f32 v142,v142,v121,0 op_sel:[0,1,0] op_sel_hi:[0,1,0]
v_mfma_f32_16x16x32_bf16 v[152:155],v[80:83],v[60:63],v[152:155]
ds_read_b64_tr_b16 v[104:105],v6 offset:18608
ds_read_b64_tr_b16 v[106:107],v6 offset:20144
v_fma_mix_f32 v141,v141,v123,0 op_sel_hi:[0,1,0]
v_mfma_f32_16x16x32_bf16 v[156:159],v[84:87],v[60:63],v[156:159]
s_waitcnt lgkmcnt(8)
ds_read_u16 v137,v137
v_lshl_add_u64 v[130:131],v[132:133],0,v[134:135]
v_fma_mix_f32 v143,v143,v123,0 op_sel:[0,1,0] op_sel_hi:[0,1,0]
v_mfma_f32_16x16x32_bf16 v[148:151],v[84:87],v[56:59],v[148:151]
ds_read_u16_d16_hi v139,v139
v_lshlrev_b32_sdwa v144,1,v144 dst_sel:DWORD dst_unused:UNUSED_PAD src0_sel:DWORD src1_sel:WORD_1
v_lshlrev_b32_sdwa v146,1,v146 dst_sel:DWORD dst_unused:UNUSED_PAD src0_sel:DWORD src1_sel:WORD_1
v_mfma_f32_16x16x32_bf16 v[160:163],v[88:91],v[128:131],v[160:163]
ds_read_b64_tr_b16 v[108:109],v6 offset:18616
ds_read_b64_tr_b16 v[110:111],v6 offset:20152
v_lshlrev_b32_sdwa v145,1,v145 dst_sel:DWORD dst_unused:UNUSED_PAD src0_sel:DWORD src1_sel:WORD_1
v_lshlrev_b32_sdwa v147,1,v147 dst_sel:DWORD dst_unused:UNUSED_PAD src0_sel:DWORD src1_sel:WORD_1
v_mfma_f32_16x16x32_bf16 v[168:171],v[92:95],v[128:131],v[168:171]
s_waitcnt lgkmcnt(10)
ds_read_b64_tr_b16 v[64:65],v4 offset:12832
ds_read_b64_tr_b16 v[66:67],v4 offset:14880
v_fma_mixlo_f16 v120,v156,s99,0
v_mfma_f32_16x16x32_bf16 v[176:179],v[96:99],v[128:131],v[176:179]
s_waitcnt lgkmcnt(9)
ds_read_b128 v[112:115],v7 offset:640
ds_read_u16 v144,v144
v_fma_mixlo_f16 v122,v157,s99,0
v_lshlrev_b32_sdwa v148,1,v148 dst_sel:DWORD dst_unused:UNUSED_PAD src0_sel:DWORD src1_sel:WORD_1
v_lshlrev_b32_sdwa v150,1,v150 dst_sel:DWORD dst_unused:UNUSED_PAD src0_sel:DWORD src1_sel:WORD_1
v_mfma_f32_16x16x32_bf16 v[184:187],v[100:103],v[128:131],v[184:187]
s_waitcnt lgkmcnt(6)
s_setprio 1
ds_read_u16_d16_hi v146,v146
v_lshl_add_u64 v[136:137],v[136:137],0,v[138:139]
v_mfma_f32_16x16x32_bf16 v[192:195],v[104:107],v[128:131],v[192:195]
s_waitcnt lgkmcnt(5)
ds_read_b64_tr_b16 v[68:69],v5 offset:12832
v_fma_mixhi_f16 v120,v158,s99,0
v_cvt_pk_bf16_f32 v138,v140,v142
v_cvt_pk_bf16_f32 v139,v141,v143
v_mfma_f32_16x16x32_bf16 v[200:203],v[108:111],v[128:131],v[200:203]
ds_read_b64_tr_b16 v[70:71],v5 offset:14880
ds_read_b128 v[116:119],v7 offset:672
v_fma_mixhi_f16 v122,v159,s99,0
v_mfma_f32_16x16x32_bf16 v[164:167],v[88:91],v[136:139],v[164:167]
v_lshlrev_b32_sdwa v149,1,v149 dst_sel:DWORD dst_unused:UNUSED_PAD src0_sel:DWORD src1_sel:WORD_1
v_lshlrev_b32_sdwa v151,1,v151 dst_sel:DWORD dst_unused:UNUSED_PAD src0_sel:DWORD src1_sel:WORD_1
v_mfma_f32_16x16x32_bf16 v[172:175],v[92:95],v[136:139],v[172:175]
ds_read_b64_tr_b16 v[72:73],v4 offset:16928
ds_read_b64_tr_b16 v[74:75],v4 offset:18976
v_pk_fma_f16 v123,v122,v122,s54
v_pk_fma_f16 v121,v120,v120,s54
v_mfma_f32_16x16x32_bf16 v[180:183],v[96:99],v[136:139],v[180:183]
ds_read_u16 v145,v145
v_pk_mul_f16 v121,v120,v121
v_pk_mul_f16 v123,v122,v123
v_mfma_f32_16x16x32_bf16 v[188:191],v[100:103],v[136:139],v[188:191]
ds_read_b64_tr_b16 v[76:77],v5 offset:16928
v_lshlrev_b32_sdwa v152,1,v152 dst_sel:DWORD dst_unused:UNUSED_PAD src0_sel:DWORD src1_sel:WORD_1
v_mfma_f32_16x16x32_bf16 v[196:199],v[104:107],v[136:139],v[196:199]
ds_read_u16_d16_hi v147,v147
v_lshlrev_b32_sdwa v154,1,v154 dst_sel:DWORD dst_unused:UNUSED_PAD src0_sel:DWORD src1_sel:WORD_1
v_exp_f16 v120,v121
v_mfma_f32_16x16x32_bf16 v[204:207],v[108:111],v[136:139],v[204:207]
s_waitcnt lgkmcnt(10)
ds_read_b64_tr_b16 v[78:79],v5 offset:18976
v_exp_f16 v122,v123
v_mfma_f32_16x16x32_bf16 v[128:131],v[64:67],v[16:19],v[112:115]
ds_read_u16 v148,v148
ds_read_u16_d16_hi v150,v150
v_lshlrev_b32_sdwa v153,1,v153 dst_sel:DWORD dst_unused:UNUSED_PAD src0_sel:DWORD src1_sel:WORD_1
v_exp_f16_sdwa v122,v123 dst_sel:WORD_1 dst_unused:UNUSED_PRESERVE src0_sel:WORD_1
v_mfma_f32_16x16x32_bf16 v[136:139],v[64:67],v[20:23],v[112:115]
s_waitcnt lgkmcnt(8)
s_setprio 0
v_lshlrev_b32_sdwa v155,1,v155 dst_sel:DWORD dst_unused:UNUSED_PAD src0_sel:DWORD src1_sel:WORD_1
v_exp_f16_sdwa v120,v121 dst_sel:WORD_1 dst_unused:UNUSED_PRESERVE src0_sel:WORD_1
v_mfma_f32_16x16x32_bf16 v[140:143],v[68:71],v[20:23],v[116:119]
ds_read_b64_tr_b16 v[80:81],v4 offset:21024
ds_read_b64_tr_b16 v[82:83],v4 offset:23072
v_pk_fma_f16 v120,v120,s55,s55
v_mfma_f32_16x16x32_bf16 v[132:135],v[68:71],v[16:19],v[116:119]
s_waitcnt lgkmcnt(8)
ds_read_u16 v149,v149
v_pk_fma_f16 v122,v122,s55,s55
v_mfma_f32_16x16x32_bf16 v[128:131],v[72:75],v[24:27],v[128:131]
ds_read_b64_tr_b16 v[84:85],v5 offset:21024
ds_read_u16_d16_hi v151,v151
v_rcp_f16 v121,v120
v_mfma_f32_16x16x32_bf16 v[136:139],v[72:75],v[28:31],v[136:139]
s_waitcnt lgkmcnt(7)
ds_read_b64_tr_b16 v[86:87],v5 offset:23072
v_rcp_f16 v123,v122
v_mfma_f32_16x16x32_bf16 v[140:143],v[76:79],v[28:31],v[140:143]
ds_read_u16 v152,v152
v_rcp_f16_sdwa v121,v120 dst_sel:WORD_1 dst_unused:UNUSED_PRESERVE src0_sel:WORD_1
v_mfma_f32_16x16x32_bf16 v[132:135],v[76:79],v[24:27],v[132:135]
s_waitcnt lgkmcnt(5)
v_rcp_f16_sdwa v123,v122 dst_sel:WORD_1 dst_unused:UNUSED_PRESERVE src0_sel:WORD_1
v_mfma_f32_16x16x32_bf16 v[128:131],v[80:83],v[32:35],v[128:131]
ds_read_u16_d16_hi v154,v154
v_fma_mix_f32 v156,v156,v121,0 op_sel_hi:[0,1,0]
v_mfma_f32_16x16x32_bf16 v[136:139],v[80:83],v[36:39],v[136:139]
s_waitcnt lgkmcnt(2)
v_lshl_add_u64 v[144:145],v[144:145],0,v[146:147]
v_fma_mix_f32 v158,v158,v121,0 op_sel:[0,1,0] op_sel_hi:[0,1,0]
v_lshl_add_u64 v[146:147],v[148:149],0,v[150:151]
v_mfma_f32_16x16x32_bf16 v[140:143],v[84:87],v[36:39],v[140:143]
ds_read_u16 v153,v153
v_fma_mix_f32 v157,v157,v123,0 op_sel_hi:[0,1,0]
v_fma_mix_f32 v159,v159,v123,0 op_sel:[0,1,0] op_sel_hi:[0,1,0]
v_mfma_f32_16x16x32_bf16 v[132:135],v[84:87],v[32:35],v[132:135]
v_lshlrev_b32_sdwa v128,1,v128 dst_sel:DWORD dst_unused:UNUSED_PAD src0_sel:DWORD src1_sel:WORD_1
v_lshlrev_b32_sdwa v130,1,v130 dst_sel:DWORD dst_unused:UNUSED_PAD src0_sel:DWORD src1_sel:WORD_1
v_mfma_f32_16x16x32_bf16 v[208:211],v[88:91],v[144:147],v[208:211]
s_waitcnt vmcnt(0)
s_barrier
s_setprio 2
v_lshlrev_b32_sdwa v129,1,v129 dst_sel:DWORD dst_unused:UNUSED_PAD src0_sel:DWORD src1_sel:WORD_1
v_lshlrev_b32_sdwa v131,1,v131 dst_sel:DWORD dst_unused:UNUSED_PAD src0_sel:DWORD src1_sel:WORD_1
v_mfma_f32_16x16x32_bf16 v[216:219],v[92:95],v[144:147],v[216:219]
ds_read_u16_d16_hi v155,v155
v_mfma_f32_16x16x32_bf16 v[224:227],v[96:99],v[144:147],v[224:227]
s_cbranch_vccz RB41_%=
s_add_u32 m0,s58,12320
s_mul_i32 s69,s59,7
s_add_u32 s69,s69,s97
buffer_load_dwordx4 v2,s[44:47],s69 offen lds
s_add_u32 m0,m0,1024
s_add_u32 s69,s69,s53
buffer_load_dwordx4 v3,s[44:47],s69 offen lds
s_add_u32 m0,m0,1024
s_add_u32 s69,s69,s53
buffer_load_dwordx4 v2,s[44:47],s69 offen lds
s_add_u32 m0,m0,s50
s_add_u32 s69,s69,s53
buffer_load_dwordx4 v3,s[44:47],s69 offen lds
RB41_%=:
v_lshlrev_b32_sdwa v132,1,v132 dst_sel:DWORD dst_unused:UNUSED_PAD src0_sel:DWORD src1_sel:WORD_1
v_mfma_f32_16x16x32_bf16 v[232:235],v[100:103],v[144:147],v[232:235]
s_waitcnt lgkmcnt(0)
ds_read_u16 v128,v128
v_lshl_add_u64 v[152:153],v[152:153],0,v[154:155]
v_cvt_pk_bf16_f32 v155,v157,v159
v_lshlrev_b32_sdwa v134,1,v134 dst_sel:DWORD dst_unused:UNUSED_PAD src0_sel:DWORD src1_sel:WORD_1
v_cvt_pk_bf16_f32 v154,v156,v158
v_mfma_f32_16x16x32_bf16 v[240:243],v[104:107],v[144:147],v[240:243]
ds_read_u16_d16_hi v130,v130
v_mfma_f32_16x16x32_bf16 v[248:251],v[108:111],v[144:147],v[248:251]
ds_read_u16 v129,v129
v_lshlrev_b32_sdwa v133,1,v133 dst_sel:DWORD dst_unused:UNUSED_PAD src0_sel:DWORD src1_sel:WORD_1
v_mfma_f32_16x16x32_bf16 v[212:215],v[88:91],v[152:155],v[212:215]
s_cbranch_vccz RB42_%=
s_add_u32 m0,m0,1024
s_add_u32 s69,s69,s53
buffer_load_dwordx4 v2,s[44:47],s69 offen lds
s_add_u32 m0,m0,1024
s_add_u32 s69,s69,s53
buffer_load_dwordx4 v3,s[44:47],s69 offen lds
s_add_u32 m0,m0,s50
s_add_u32 s69,s69,s53
buffer_load_dwordx4 v2,s[44:47],s69 offen lds
s_add_u32 m0,m0,1024
s_add_u32 s69,s69,s53
buffer_load_dwordx4 v3,s[44:47],s69 offen lds
RB42_%=:
v_lshlrev_b32_sdwa v135,1,v135 dst_sel:DWORD dst_unused:UNUSED_PAD src0_sel:DWORD src1_sel:WORD_1
v_mfma_f32_16x16x32_bf16 v[220:223],v[92:95],v[152:155],v[220:223]
ds_read_u16_d16_hi v131,v131
v_lshlrev_b32_sdwa v136,1,v136 dst_sel:DWORD dst_unused:UNUSED_PAD src0_sel:DWORD src1_sel:WORD_1
v_mfma_f32_16x16x32_bf16 v[228:231],v[96:99],v[152:155],v[228:231]
ds_read_b64_tr_b16 v[88:89],v6 offset:0
ds_read_b64_tr_b16 v[90:91],v6 offset:1536
v_lshlrev_b32_sdwa v138,1,v138 dst_sel:DWORD dst_unused:UNUSED_PAD src0_sel:DWORD src1_sel:WORD_1
v_mfma_f32_16x16x32_bf16 v[236:239],v[100:103],v[152:155],v[236:239]
ds_read_u16 v132,v132
v_mfma_f32_16x16x32_bf16 v[244:247],v[104:107],v[152:155],v[244:247]
ds_read_u16_d16_hi v134,v134
v_lshlrev_b32_sdwa v137,1,v137 dst_sel:DWORD dst_unused:UNUSED_PAD src0_sel:DWORD src1_sel:WORD_1
v_lshlrev_b32_sdwa v139,1,v139 dst_sel:DWORD dst_unused:UNUSED_PAD src0_sel:DWORD src1_sel:WORD_1
v_mfma_f32_16x16x32_bf16 v[252:255],v[108:111],v[152:155],v[252:255]
s_cbranch_vccz RB43_%=
s_add_u32 m0,m0,1024
s_add_u32 s69,s69,s53
buffer_load_dwordx4 v2,s[44:47],s69 offen lds
s_add_u32 m0,m0,s50
s_add_u32 s69,s69,s53
buffer_load_dwordx4 v3,s[44:47],s69 offen lds
s_add_u32 m0,m0,1024
s_add_u32 s69,s69,s53
buffer_load_dwordx4 v2,s[44:47],s69 offen lds
s_add_u32 m0,m0,1024
s_add_u32 s69,s69,s53
buffer_load_dwordx4 v3,s[44:47],s69 offen lds
RB43_%=:
ds_read_b64_tr_b16 v[92:93],v6 offset:8
ds_read_b64_tr_b16 v[94:95],v6 offset:1544
v_lshlrev_b32_sdwa v140,1,v140 dst_sel:DWORD dst_unused:UNUSED_PAD src0_sel:DWORD src1_sel:WORD_1
v_mfma_f32_16x16x32_bf16 v[144:147],v[64:67],v[40:43],v[112:115]
ds_read_u16 v133,v133
v_lshlrev_b32_sdwa v142,1,v142 dst_sel:DWORD dst_unused:UNUSED_PAD src0_sel:DWORD src1_sel:WORD_1
v_mfma_f32_16x16x32_bf16 v[152:155],v[64:67],v[44:47],v[112:115]
ds_read_b64_tr_b16 v[96:97],v6 offset:64
v_lshlrev_b32_sdwa v141,1,v141 dst_sel:DWORD dst_unused:UNUSED_PAD src0_sel:DWORD src1_sel:WORD_1
v_lshlrev_b32_sdwa v143,1,v143 dst_sel:DWORD dst_unused:UNUSED_PAD src0_sel:DWORD src1_sel:WORD_1
v_mfma_f32_16x16x32_bf16 v[156:159],v[68:71],v[44:47],v[116:119]
ds_read_u16_d16_hi v135,v135
v_mfma_f32_16x16x32_bf16 v[148:151],v[68:71],v[40:43],v[116:119]
ds_read_u16 v136,v136
ds_read_b64_tr_b16 v[98:99],v6 offset:1600
v_mfma_f32_16x16x32_bf16 v[144:147],v[72:75],v[48:51],v[144:147]
ds_read_u16_d16_hi v138,v138
v_mfma_f32_16x16x32_bf16 v[152:155],v[72:75],v[52:55],v[152:155]
ds_read_b64_tr_b16 v[100:101],v6 offset:72
ds_read_b64_tr_b16 v[102:103],v6 offset:1608
v_mfma_f32_16x16x32_bf16 v[156:159],v[76:79],v[52:55],v[156:159]
ds_read_u16 v137,v137
v_mfma_f32_16x16x32_bf16 v[148:151],v[76:79],v[48:51],v[148:151]
s_waitcnt lgkmcnt(6)
ds_read_u16_d16_hi v139,v139
v_lshl_add_u64 v[128:129],v[128:129],0,v[130:131]
v_lshl_add_u64 v[130:131],v[132:133],0,v[134:135]
v_mfma_f32_16x16x32_bf16 v[144:147],v[80:83],v[56:59],v[144:147]
ds_read_b64_tr_b16 v[104:105],v6 offset:128
ds_read_b64_tr_b16 v[106:107],v6 offset:1664
v_mfma_f32_16x16x32_bf16 v[152:155],v[80:83],v[60:63],v[152:155]
ds_read_u16 v140,v140
v_mfma_f32_16x16x32_bf16 v[156:159],v[84:87],v[60:63],v[156:159]
ds_read_u16_d16_hi v142,v142
s_nop 1
v_lshlrev_b32_sdwa v144,1,v144 dst_sel:DWORD dst_unused:UNUSED_PAD src0_sel:DWORD src1_sel:WORD_1
v_mfma_f32_16x16x32_bf16 v[148:151],v[84:87],v[56:59],v[148:151]
ds_read_b64_tr_b16 v[108:109],v6 offset:136
ds_read_b64_tr_b16 v[110:111],v6 offset:1672
v_lshlrev_b32_sdwa v146,1,v146 dst_sel:DWORD dst_unused:UNUSED_PAD src0_sel:DWORD src1_sel:WORD_1
v_mfma_f32_16x16x32_bf16 v[160:163],v[88:91],v[128:131],v[160:163]
ds_read_b64_tr_b16 v[64:65],v4 offset:0
ds_read_u16 v141,v141
v_lshlrev_b32_sdwa v145,1,v145 dst_sel:DWORD dst_unused:UNUSED_PAD src0_sel:DWORD src1_sel:WORD_1
v_mfma_f32_16x16x32_bf16 v[168:171],v[92:95],v[128:131],v[168:171]
s_waitcnt lgkmcnt(13)
ds_read_u16_d16_hi v143,v143
v_lshlrev_b32_sdwa v147,1,v147 dst_sel:DWORD dst_unused:UNUSED_PAD src0_sel:DWORD src1_sel:WORD_1
v_mfma_f32_16x16x32_bf16 v[176:179],v[96:99],v[128:131],v[176:179]
s_waitcnt lgkmcnt(11)
ds_read_b64_tr_b16 v[66:67],v4 offset:2048
v_lshlrev_b32_sdwa v148,1,v148 dst_sel:DWORD dst_unused:UNUSED_PAD src0_sel:DWORD src1_sel:WORD_1
v_mfma_f32_16x16x32_bf16 v[184:187],v[100:103],v[128:131],v[184:187]
s_waitcnt lgkmcnt(8)
ds_read_u16 v144,v144
v_lshl_add_u64 v[136:137],v[136:137],0,v[138:139]
v_lshlrev_b32_sdwa v150,1,v150 dst_sel:DWORD dst_unused:UNUSED_PAD src0_sel:DWORD src1_sel:WORD_1
v_mfma_f32_16x16x32_bf16 v[192:195],v[104:107],v[128:131],v[192:195]
s_waitcnt lgkmcnt(2)
ds_read_u16_d16_hi v146,v146
v_lshl_add_u64 v[138:139],v[140:141],0,v[142:143]
v_lshlrev_b32_sdwa v149,1,v149 dst_sel:DWORD dst_unused:UNUSED_PAD src0_sel:DWORD src1_sel:WORD_1
v_mfma_f32_16x16x32_bf16 v[200:203],v[108:111],v[128:131],v[200:203]
ds_read_b128 v[112:115],v7 offset:768
v_mfma_f32_16x16x32_bf16 v[164:167],v[88:91],v[136:139],v[164:167]
ds_read_u16 v145,v145
v_lshlrev_b32_sdwa v151,1,v151 dst_sel:DWORD dst_unused:UNUSED_PAD src0_sel:DWORD src1_sel:WORD_1
v_mfma_f32_16x16x32_bf16 v[172:175],v[92:95],v[136:139],v[172:175]
ds_read_u16_d16_hi v147,v147
ds_read_b64_tr_b16 v[68:69],v5 offset:0
v_lshlrev_b32_sdwa v152,1,v152 dst_sel:DWORD dst_unused:UNUSED_PAD src0_sel:DWORD src1_sel:WORD_1
v_mfma_f32_16x16x32_bf16 v[180:183],v[96:99],v[136:139],v[180:183]
ds_read_b64_tr_b16 v[70:71],v5 offset:2048
ds_read_b128 v[116:119],v7 offset:800
v_lshlrev_b32_sdwa v154,1,v154 dst_sel:DWORD dst_unused:UNUSED_PAD src0_sel:DWORD src1_sel:WORD_1
v_mfma_f32_16x16x32_bf16 v[188:191],v[100:103],v[136:139],v[188:191]
ds_read_u16 v148,v148
ds_read_b64_tr_b16 v[72:73],v4 offset:4096
v_lshlrev_b32_sdwa v153,1,v153 dst_sel:DWORD dst_unused:UNUSED_PAD src0_sel:DWORD src1_sel:WORD_1
v_mfma_f32_16x16x32_bf16 v[196:199],v[104:107],v[136:139],v[196:199]
ds_read_u16_d16_hi v150,v150
v_lshlrev_b32_sdwa v155,1,v155 dst_sel:DWORD dst_unused:UNUSED_PAD src0_sel:DWORD src1_sel:WORD_1
v_mfma_f32_16x16x32_bf16 v[204:207],v[108:111],v[136:139],v[204:207]
s_waitcnt lgkmcnt(8)
ds_read_b64_tr_b16 v[74:75],v4 offset:6144
ds_read_b64_tr_b16 v[76:77],v5 offset:4096
v_mfma_f32_16x16x32_bf16 v[128:131],v[64:67],v[16:19],v[112:115]
ds_read_u16 v149,v149
v_lshlrev_b32_sdwa v156,1,v156 dst_sel:DWORD dst_unused:UNUSED_PAD src0_sel:DWORD src1_sel:WORD_1
v_lshlrev_b32_sdwa v158,1,v158 dst_sel:DWORD dst_unused:UNUSED_PAD src0_sel:DWORD src1_sel:WORD_1
v_mfma_f32_16x16x32_bf16 v[136:139],v[64:67],v[20:23],v[112:115]
s_waitcnt lgkmcnt(6)
ds_read_b64_tr_b16 v[78:79],v5 offset:6144
ds_read_b64_tr_b16 v[80:81],v4 offset:8192
v_lshlrev_b32_sdwa v157,1,v157 dst_sel:DWORD dst_unused:UNUSED_PAD src0_sel:DWORD src1_sel:WORD_1
v_mfma_f32_16x16x32_bf16 v[140:143],v[68:71],v[20:23],v[116:119]
ds_read_u16_d16_hi v151,v151
v_lshlrev_b32_sdwa v159,1,v159 dst_sel:DWORD dst_unused:UNUSED_PAD src0_sel:DWORD src1_sel:WORD_1
v_mfma_f32_16x16x32_bf16 v[132:135],v[68:71],v[16:19],v[116:119]
s_waitcnt lgkmcnt(5)
ds_read_u16 v152,v152
ds_read_b64_tr_b16 v[82:83],v4 offset:10240
v_mfma_f32_16x16x32_bf16 v[128:131],v[72:75],v[24:27],v[128:131]
ds_read_b64_tr_b16 v[84:85],v5 offset:8192
ds_read_b64_tr_b16 v[86:87],v5 offset:10240
v_mfma_f32_16x16x32_bf16 v[136:139],v[72:75],v[28:31],v[136:139]
s_waitcnt lgkmcnt(6)
ds_read_u16_d16_hi v154,v154
v_mfma_f32_16x16x32_bf16 v[140:143],v[76:79],v[28:31],v[140:143]
ds_read_u16 v153,v153
v_mfma_f32_16x16x32_bf16 v[132:135],v[76:79],v[24:27],v[132:135]
s_waitcnt lgkmcnt(4)
ds_read_u16_d16_hi v155,v155
v_lshl_add_u64 v[144:145],v[144:145],0,v[146:147]
v_mfma_f32_16x16x32_bf16 v[128:131],v[80:83],v[32:35],v[128:131]
v_lshl_add_u64 v[146:147],v[148:149],0,v[150:151]
v_mfma_f32_16x16x32_bf16 v[136:139],v[80:83],v[36:39],v[136:139]
s_waitcnt lgkmcnt(3)
ds_read_u16 v156,v156
v_mfma_f32_16x16x32_bf16 v[140:143],v[84:87],v[36:39],v[140:143]
ds_read_u16_d16_hi v158,v158
v_mfma_f32_16x16x32_bf16 v[132:135],v[84:87],v[32:35],v[132:135]
ds_read_u16 v157,v157
v_lshlrev_b32_sdwa v128,1,v128 dst_sel:DWORD dst_unused:UNUSED_PAD src0_sel:DWORD src1_sel:WORD_1
v_lshlrev_b32_sdwa v130,1,v130 dst_sel:DWORD dst_unused:UNUSED_PAD src0_sel:DWORD src1_sel:WORD_1
v_mfma_f32_16x16x32_bf16 v[208:211],v[88:91],v[144:147],v[208:211]
v_lshlrev_b32_sdwa v129,1,v129 dst_sel:DWORD dst_unused:UNUSED_PAD src0_sel:DWORD src1_sel:WORD_1
v_lshlrev_b32_sdwa v131,1,v131 dst_sel:DWORD dst_unused:UNUSED_PAD src0_sel:DWORD src1_sel:WORD_1
v_mfma_f32_16x16x32_bf16 v[216:219],v[92:95],v[144:147],v[216:219]
ds_read_u16_d16_hi v159,v159
v_mfma_f32_16x16x32_bf16 v[224:227],v[96:99],v[144:147],v[224:227]
v_lshlrev_b32_sdwa v132,1,v132 dst_sel:DWORD dst_unused:UNUSED_PAD src0_sel:DWORD src1_sel:WORD_1
v_mfma_f32_16x16x32_bf16 v[232:235],v[100:103],v[144:147],v[232:235]
s_waitcnt lgkmcnt(4)
ds_read_u16 v128,v128
v_lshl_add_u64 v[152:153],v[152:153],0,v[154:155]
v_lshlrev_b32_sdwa v134,1,v134 dst_sel:DWORD dst_unused:UNUSED_PAD src0_sel:DWORD src1_sel:WORD_1
v_mfma_f32_16x16x32_bf16 v[240:243],v[104:107],v[144:147],v[240:243]
s_waitcnt lgkmcnt(1)
ds_read_u16_d16_hi v130,v130
v_lshl_add_u64 v[154:155],v[156:157],0,v[158:159]
v_mfma_f32_16x16x32_bf16 v[248:251],v[108:111],v[144:147],v[248:251]
ds_read_u16 v129,v129
v_lshlrev_b32_sdwa v133,1,v133 dst_sel:DWORD dst_unused:UNUSED_PAD src0_sel:DWORD src1_sel:WORD_1
v_mfma_f32_16x16x32_bf16 v[212:215],v[88:91],v[152:155],v[212:215]
v_lshlrev_b32_sdwa v135,1,v135 dst_sel:DWORD dst_unused:UNUSED_PAD src0_sel:DWORD src1_sel:WORD_1
v_mfma_f32_16x16x32_bf16 v[220:223],v[92:95],v[152:155],v[220:223]
ds_read_u16_d16_hi v131,v131
v_lshlrev_b32_sdwa v136,1,v136 dst_sel:DWORD dst_unused:UNUSED_PAD src0_sel:DWORD src1_sel:WORD_1
v_mfma_f32_16x16x32_bf16 v[228:231],v[96:99],v[152:155],v[228:231]
ds_read_b64_tr_b16 v[88:89],v6 offset:6160
ds_read_b64_tr_b16 v[90:91],v6 offset:7696
v_lshlrev_b32_sdwa v138,1,v138 dst_sel:DWORD dst_unused:UNUSED_PAD src0_sel:DWORD src1_sel:WORD_1
v_mfma_f32_16x16x32_bf16 v[236:239],v[100:103],v[152:155],v[236:239]
ds_read_u16 v132,v132
v_mfma_f32_16x16x32_bf16 v[244:247],v[104:107],v[152:155],v[244:247]
ds_read_u16_d16_hi v134,v134
v_lshlrev_b32_sdwa v137,1,v137 dst_sel:DWORD dst_unused:UNUSED_PAD src0_sel:DWORD src1_sel:WORD_1
v_lshlrev_b32_sdwa v139,1,v139 dst_sel:DWORD dst_unused:UNUSED_PAD src0_sel:DWORD src1_sel:WORD_1
v_mfma_f32_16x16x32_bf16 v[252:255],v[108:111],v[152:155],v[252:255]
ds_read_b64_tr_b16 v[92:93],v6 offset:6168
ds_read_b64_tr_b16 v[94:95],v6 offset:7704
v_lshlrev_b32_sdwa v140,1,v140 dst_sel:DWORD dst_unused:UNUSED_PAD src0_sel:DWORD src1_sel:WORD_1
v_mfma_f32_16x16x32_bf16 v[144:147],v[64:67],v[40:43],v[112:115]
ds_read_u16 v133,v133
v_lshlrev_b32_sdwa v142,1,v142 dst_sel:DWORD dst_unused:UNUSED_PAD src0_sel:DWORD src1_sel:WORD_1
v_mfma_f32_16x16x32_bf16 v[152:155],v[64:67],v[44:47],v[112:115]
ds_read_b64_tr_b16 v[96:97],v6 offset:6224
v_lshlrev_b32_sdwa v141,1,v141 dst_sel:DWORD dst_unused:UNUSED_PAD src0_sel:DWORD src1_sel:WORD_1
v_lshlrev_b32_sdwa v143,1,v143 dst_sel:DWORD dst_unused:UNUSED_PAD src0_sel:DWORD src1_sel:WORD_1
v_mfma_f32_16x16x32_bf16 v[156:159],v[68:71],v[44:47],v[116:119]
ds_read_u16_d16_hi v135,v135
v_mfma_f32_16x16x32_bf16 v[148:151],v[68:71],v[40:43],v[116:119]
ds_read_u16 v136,v136
ds_read_b64_tr_b16 v[98:99],v6 offset:7760
v_mfma_f32_16x16x32_bf16 v[144:147],v[72:75],v[48:51],v[144:147]
ds_read_u16_d16_hi v138,v138
v_mfma_f32_16x16x32_bf16 v[152:155],v[72:75],v[52:55],v[152:155]
ds_read_b64_tr_b16 v[100:101],v6 offset:6232
ds_read_b64_tr_b16 v[102:103],v6 offset:7768
v_mfma_f32_16x16x32_bf16 v[156:159],v[76:79],v[52:55],v[156:159]
ds_read_u16 v137,v137
v_mfma_f32_16x16x32_bf16 v[148:151],v[76:79],v[48:51],v[148:151]
s_waitcnt lgkmcnt(6)
ds_read_u16_d16_hi v139,v139
v_lshl_add_u64 v[128:129],v[128:129],0,v[130:131]
v_lshl_add_u64 v[130:131],v[132:133],0,v[134:135]
v_mfma_f32_16x16x32_bf16 v[144:147],v[80:83],v[56:59],v[144:147]
ds_read_b64_tr_b16 v[104:105],v6 offset:6288
ds_read_b64_tr_b16 v[106:107],v6 offset:7824
v_mfma_f32_16x16x32_bf16 v[152:155],v[80:83],v[60:63],v[152:155]
ds_read_u16 v140,v140
v_mfma_f32_16x16x32_bf16 v[156:159],v[84:87],v[60:63],v[156:159]
ds_read_u16_d16_hi v142,v142
s_nop 1
v_lshlrev_b32_sdwa v144,1,v144 dst_sel:DWORD dst_unused:UNUSED_PAD src0_sel:DWORD src1_sel:WORD_1
v_mfma_f32_16x16x32_bf16 v[148:151],v[84:87],v[56:59],v[148:151]
ds_read_b64_tr_b16 v[108:109],v6 offset:6296
ds_read_b64_tr_b16 v[110:111],v6 offset:7832
v_lshlrev_b32_sdwa v146,1,v146 dst_sel:DWORD dst_unused:UNUSED_PAD src0_sel:DWORD src1_sel:WORD_1
v_mfma_f32_16x16x32_bf16 v[160:163],v[88:91],v[128:131],v[160:163]
ds_read_b64_tr_b16 v[64:65],v4 offset:512
ds_read_u16 v141,v141
v_lshlrev_b32_sdwa v145,1,v145 dst_sel:DWORD dst_unused:UNUSED_PAD src0_sel:DWORD src1_sel:WORD_1
v_mfma_f32_16x16x32_bf16 v[168:171],v[92:95],v[128:131],v[168:171]
s_waitcnt lgkmcnt(13)
ds_read_u16_d16_hi v143,v143
v_lshlrev_b32_sdwa v147,1,v147 dst_sel:DWORD dst_unused:UNUSED_PAD src0_sel:DWORD src1_sel:WORD_1
v_mfma_f32_16x16x32_bf16 v[176:179],v[96:99],v[128:131],v[176:179]
s_waitcnt lgkmcnt(11)
ds_read_b64_tr_b16 v[66:67],v4 offset:2560
v_lshlrev_b32_sdwa v148,1,v148 dst_sel:DWORD dst_unused:UNUSED_PAD src0_sel:DWORD src1_sel:WORD_1
v_mfma_f32_16x16x32_bf16 v[184:187],v[100:103],v[128:131],v[184:187]
s_waitcnt lgkmcnt(8)
ds_read_u16 v144,v144
v_lshl_add_u64 v[136:137],v[136:137],0,v[138:139]
v_lshlrev_b32_sdwa v150,1,v150 dst_sel:DWORD dst_unused:UNUSED_PAD src0_sel:DWORD src1_sel:WORD_1
v_mfma_f32_16x16x32_bf16 v[192:195],v[104:107],v[128:131],v[192:195]
s_waitcnt lgkmcnt(2)
ds_read_u16_d16_hi v146,v146
v_lshl_add_u64 v[138:139],v[140:141],0,v[142:143]
v_lshlrev_b32_sdwa v149,1,v149 dst_sel:DWORD dst_unused:UNUSED_PAD src0_sel:DWORD src1_sel:WORD_1
v_mfma_f32_16x16x32_bf16 v[200:203],v[108:111],v[128:131],v[200:203]
ds_read_b128 v[112:115],v7 offset:896
v_mfma_f32_16x16x32_bf16 v[164:167],v[88:91],v[136:139],v[164:167]
ds_read_u16 v145,v145
v_lshlrev_b32_sdwa v151,1,v151 dst_sel:DWORD dst_unused:UNUSED_PAD src0_sel:DWORD src1_sel:WORD_1
v_mfma_f32_16x16x32_bf16 v[172:175],v[92:95],v[136:139],v[172:175]
ds_read_u16_d16_hi v147,v147
ds_read_b64_tr_b16 v[68:69],v5 offset:512
v_lshlrev_b32_sdwa v152,1,v152 dst_sel:DWORD dst_unused:UNUSED_PAD src0_sel:DWORD src1_sel:WORD_1
v_mfma_f32_16x16x32_bf16 v[180:183],v[96:99],v[136:139],v[180:183]
ds_read_b64_tr_b16 v[70:71],v5 offset:2560
ds_read_b128 v[116:119],v7 offset:928
v_lshlrev_b32_sdwa v154,1,v154 dst_sel:DWORD dst_unused:UNUSED_PAD src0_sel:DWORD src1_sel:WORD_1
v_mfma_f32_16x16x32_bf16 v[188:191],v[100:103],v[136:139],v[188:191]
ds_read_u16 v148,v148
ds_read_b64_tr_b16 v[72:73],v4 offset:4608
v_lshlrev_b32_sdwa v153,1,v153 dst_sel:DWORD dst_unused:UNUSED_PAD src0_sel:DWORD src1_sel:WORD_1
v_mfma_f32_16x16x32_bf16 v[196:199],v[104:107],v[136:139],v[196:199]
ds_read_u16_d16_hi v150,v150
v_lshlrev_b32_sdwa v155,1,v155 dst_sel:DWORD dst_unused:UNUSED_PAD src0_sel:DWORD src1_sel:WORD_1
v_mfma_f32_16x16x32_bf16 v[204:207],v[108:111],v[136:139],v[204:207]
s_waitcnt lgkmcnt(8)
ds_read_b64_tr_b16 v[74:75],v4 offset:6656
ds_read_b64_tr_b16 v[76:77],v5 offset:4608
v_mfma_f32_16x16x32_bf16 v[128:131],v[64:67],v[16:19],v[112:115]
ds_read_u16 v149,v149
v_lshlrev_b32_sdwa v156,1,v156 dst_sel:DWORD dst_unused:UNUSED_PAD src0_sel:DWORD src1_sel:WORD_1
v_lshlrev_b32_sdwa v158,1,v158 dst_sel:DWORD dst_unused:UNUSED_PAD src0_sel:DWORD src1_sel:WORD_1
v_mfma_f32_16x16x32_bf16 v[136:139],v[64:67],v[20:23],v[112:115]
s_waitcnt lgkmcnt(6)
ds_read_b64_tr_b16 v[78:79],v5 offset:6656
ds_read_b64_tr_b16 v[80:81],v4 offset:8704
v_lshlrev_b32_sdwa v157,1,v157 dst_sel:DWORD dst_unused:UNUSED_PAD src0_sel:DWORD src1_sel:WORD_1
v_mfma_f32_16x16x32_bf16 v[140:143],v[68:71],v[20:23],v[116:119]
ds_read_u16_d16_hi v151,v151
v_lshlrev_b32_sdwa v159,1,v159 dst_sel:DWORD dst_unused:UNUSED_PAD src0_sel:DWORD src1_sel:WORD_1
v_mfma_f32_16x16x32_bf16 v[132:135],v[68:71],v[16:19],v[116:119]
s_waitcnt lgkmcnt(5)
ds_read_u16 v152,v152
ds_read_b64_tr_b16 v[82:83],v4 offset:10752
v_mfma_f32_16x16x32_bf16 v[128:131],v[72:75],v[24:27],v[128:131]
ds_read_b64_tr_b16 v[84:85],v5 offset:8704
s_nop 0
s_mov_b32 s86,0xbe801d5a
ds_read_b64_tr_b16 v[86:87],v5 offset:10752
v_mfma_f32_16x16x32_bf16 v[136:139],v[72:75],v[28:31],v[136:139]
s_waitcnt lgkmcnt(6)
ds_read_u16_d16_hi v154,v154
v_mfma_f32_16x16x32_bf16 v[140:143],v[76:79],v[28:31],v[140:143]
ds_read_u16 v153,v153
v_mfma_f32_16x16x32_bf16 v[132:135],v[76:79],v[24:27],v[132:135]
s_waitcnt lgkmcnt(4)
ds_read_u16_d16_hi v155,v155
v_lshl_add_u64 v[144:145],v[144:145],0,v[146:147]
v_mfma_f32_16x16x32_bf16 v[128:131],v[80:83],v[32:35],v[128:131]
v_lshl_add_u64 v[146:147],v[148:149],0,v[150:151]
v_mfma_f32_16x16x32_bf16 v[136:139],v[80:83],v[36:39],v[136:139]
s_waitcnt lgkmcnt(3)
ds_read_u16 v156,v156
v_mfma_f32_16x16x32_bf16 v[140:143],v[84:87],v[36:39],v[140:143]
ds_read_u16_d16_hi v158,v158
v_mfma_f32_16x16x32_bf16 v[132:135],v[84:87],v[32:35],v[132:135]
ds_read_u16 v157,v157
v_lshlrev_b32_sdwa v128,1,v128 dst_sel:DWORD dst_unused:UNUSED_PAD src0_sel:DWORD src1_sel:WORD_1
v_lshlrev_b32_sdwa v130,1,v130 dst_sel:DWORD dst_unused:UNUSED_PAD src0_sel:DWORD src1_sel:WORD_1
v_mfma_f32_16x16x32_bf16 v[208:211],v[88:91],v[144:147],v[208:211]
s_waitcnt vmcnt(0)
s_barrier
s_setprio 3
v_lshlrev_b32_sdwa v129,1,v129 dst_sel:DWORD dst_unused:UNUSED_PAD src0_sel:DWORD src1_sel:WORD_1
v_lshlrev_b32_sdwa v131,1,v131 dst_sel:DWORD dst_unused:UNUSED_PAD src0_sel:DWORD src1_sel:WORD_1
v_mfma_f32_16x16x32_bf16 v[216:219],v[92:95],v[144:147],v[216:219]
ds_read_u16_d16_hi v159,v159
ds_read_u16 v128,v128
v_fma_mixlo_f16 v120,v140,s99,0
v_fma_mixlo_f16 v122,v141,s99,0
v_mfma_f32_16x16x32_bf16 v[224:227],v[96:99],v[144:147],v[224:227]
ds_read_u16_d16_hi v130,v130
v_lshlrev_b32_sdwa v132,1,v132 dst_sel:DWORD dst_unused:UNUSED_PAD src0_sel:DWORD src1_sel:WORD_1
v_lshlrev_b32_sdwa v134,1,v134 dst_sel:DWORD dst_unused:UNUSED_PAD src0_sel:DWORD src1_sel:WORD_1
v_mfma_f32_16x16x32_bf16 v[232:235],v[100:103],v[144:147],v[232:235]
s_waitcnt lgkmcnt(6)
v_lshl_add_u64 v[152:153],v[152:153],0,v[154:155]
v_mfma_f32_16x16x32_bf16 v[240:243],v[104:107],v[144:147],v[240:243]
s_waitcnt lgkmcnt(2)
v_lshl_add_u64 v[154:155],v[156:157],0,v[158:159]
v_fma_mixhi_f16 v120,v142,s99,0
v_mfma_f32_16x16x32_bf16 v[248:251],v[108:111],v[144:147],v[248:251]
ds_read_u16 v129,v129
v_fma_mixhi_f16 v122,v143,s99,0
v_mfma_f32_16x16x32_bf16 v[212:215],v[88:91],v[152:155],v[212:215]
v_lshlrev_b32_sdwa v133,1,v133 dst_sel:DWORD dst_unused:UNUSED_PAD src0_sel:DWORD src1_sel:WORD_1
v_pk_fma_f16 v121,v120,v120,s54
v_lshlrev_b32_sdwa v135,1,v135 dst_sel:DWORD dst_unused:UNUSED_PAD src0_sel:DWORD src1_sel:WORD_1
v_mfma_f32_16x16x32_bf16 v[220:223],v[92:95],v[152:155],v[220:223]
ds_read_u16_d16_hi v131,v131
ds_read_u16 v132,v132
v_pk_fma_f16 v123,v122,v122,s54
v_mfma_f32_16x16x32_bf16 v[228:231],v[96:99],v[152:155],v[228:231]
ds_read_b64_tr_b16 v[88:89],v6 offset:12320
v_pk_mul_f16 v121,v120,v121
v_lshlrev_b32_sdwa v136,1,v136 dst_sel:DWORD dst_unused:UNUSED_PAD src0_sel:DWORD src1_sel:WORD_1
v_pk_mul_f16 v123,v122,v123
v_mfma_f32_16x16x32_bf16 v[236:239],v[100:103],v[152:155],v[236:239]
ds_read_b64_tr_b16 v[90:91],v6 offset:13856
v_lshlrev_b32_sdwa v138,1,v138 dst_sel:DWORD dst_unused:UNUSED_PAD src0_sel:DWORD src1_sel:WORD_1
v_mfma_f32_16x16x32_bf16 v[244:247],v[104:107],v[152:155],v[244:247]
ds_read_u16_d16_hi v134,v134
v_exp_f16 v120,v121
v_mfma_f32_16x16x32_bf16 v[252:255],v[108:111],v[152:155],v[252:255]
ds_read_b64_tr_b16 v[92:93],v6 offset:12328
v_lshlrev_b32_sdwa v137,1,v137 dst_sel:DWORD dst_unused:UNUSED_PAD src0_sel:DWORD src1_sel:WORD_1
v_mfma_f32_16x16x32_bf16 v[144:147],v[64:67],v[40:43],v[112:115]
ds_read_b64_tr_b16 v[94:95],v6 offset:13864
v_lshlrev_b32_sdwa v139,1,v139 dst_sel:DWORD dst_unused:UNUSED_PAD src0_sel:DWORD src1_sel:WORD_1
v_exp_f16 v122,v123
v_mfma_f32_16x16x32_bf16 v[152:155],v[64:67],v[44:47],v[112:115]
ds_read_u16 v133,v133
ds_read_b64_tr_b16 v[96:97],v6 offset:12384
v_mfma_f32_16x16x32_bf16 v[156:159],v[68:71],v[44:47],v[116:119]
ds_read_u16_d16_hi v135,v135
v_exp_f16_sdwa v122,v123 dst_sel:WORD_1 dst_unused:UNUSED_PRESERVE src0_sel:WORD_1
v_exp_f16_sdwa v120,v121 dst_sel:WORD_1 dst_unused:UNUSED_PRESERVE src0_sel:WORD_1
v_mfma_f32_16x16x32_bf16 v[148:151],v[68:71],v[40:43],v[116:119]
ds_read_b64_tr_b16 v[98:99],v6 offset:13920
v_pk_fma_f16 v120,v120,s55,s55
v_pk_fma_f16 v122,v122,s55,s55
v_mfma_f32_16x16x32_bf16 v[144:147],v[72:75],v[48:51],v[144:147]
v_rcp_f16 v121,v120
v_mfma_f32_16x16x32_bf16 v[152:155],v[72:75],v[52:55],v[152:155]
ds_read_b64_tr_b16 v[100:101],v6 offset:12392
v_rcp_f16 v123,v122
v_mfma_f32_16x16x32_bf16 v[156:159],v[76:79],v[52:55],v[156:159]
ds_read_u16 v136,v136
v_rcp_f16_sdwa v121,v120 dst_sel:WORD_1 dst_unused:UNUSED_PRESERVE src0_sel:WORD_1
v_mfma_f32_16x16x32_bf16 v[148:151],v[76:79],v[48:51],v[148:151]
ds_read_b64_tr_b16 v[102:103],v6 offset:13928
ds_read_b64_tr_b16 v[104:105],v6 offset:12448
v_rcp_f16_sdwa v123,v122 dst_sel:WORD_1 dst_unused:UNUSED_PRESERVE src0_sel:WORD_1
v_mfma_f32_16x16x32_bf16 v[144:147],v[80:83],v[56:59],v[144:147]
ds_read_u16_d16_hi v138,v138
v_fma_mix_f32 v142,v142,v121,0 op_sel:[0,1,0] op_sel_hi:[0,1,0]
v_mfma_f32_16x16x32_bf16 v[152:155],v[80:83],v[60:63],v[152:155]
s_waitcnt lgkmcnt(6)
ds_read_b64_tr_b16 v[106:107],v6 offset:13984
ds_read_b64_tr_b16 v[108:109],v6 offset:12456
v_fma_mix_f32 v140,v140,v121,0 op_sel_hi:[0,1,0]
v_lshl_add_u64 v[128:129],v[128:129],0,v[130:131]
v_lshl_add_u64 v[130:131],v[132:133],0,v[134:135]
v_mfma_f32_16x16x32_bf16 v[156:159],v[84:87],v[60:63],v[156:159]
ds_read_u16 v137,v137
v_fma_mix_f32 v141,v141,v123,0 op_sel_hi:[0,1,0]
v_fma_mix_f32 v143,v143,v123,0 op_sel:[0,1,0] op_sel_hi:[0,1,0]
v_mfma_f32_16x16x32_bf16 v[148:151],v[84:87],v[56:59],v[148:151]
ds_read_b64_tr_b16 v[110:111],v6 offset:13992
ds_read_b64_tr_b16 v[64:65],v4 offset:12320
v_lshlrev_b32_sdwa v144,1,v144 dst_sel:DWORD dst_unused:UNUSED_PAD src0_sel:DWORD src1_sel:WORD_1
v_lshlrev_b32_sdwa v146,1,v146 dst_sel:DWORD dst_unused:UNUSED_PAD src0_sel:DWORD src1_sel:WORD_1
v_mfma_f32_16x16x32_bf16 v[160:163],v[88:91],v[128:131],v[160:163]
ds_read_u16_d16_hi v139,v139
v_lshlrev_b32_sdwa v145,1,v145 dst_sel:DWORD dst_unused:UNUSED_PAD src0_sel:DWORD src1_sel:WORD_1
v_lshlrev_b32_sdwa v147,1,v147 dst_sel:DWORD dst_unused:UNUSED_PAD src0_sel:DWORD src1_sel:WORD_1
v_fma_mixlo_f16 v122,v157,s99,0
v_mfma_f32_16x16x32_bf16 v[168:171],v[92:95],v[128:131],v[168:171]
s_waitcnt lgkmcnt(11)
ds_read_b64_tr_b16 v[66:67],v4 offset:14368
ds_read_b128 v[112:115],v7 offset:1024
v_fma_mixlo_f16 v120,v156,s99,0
v_mfma_f32_16x16x32_bf16 v[176:179],v[96:99],v[128:131],v[176:179]
s_waitcnt lgkmcnt(10)
ds_read_u16 v144,v144
v_lshlrev_b32_sdwa v148,1,v148 dst_sel:DWORD dst_unused:UNUSED_PAD src0_sel:DWORD src1_sel:WORD_1
v_lshlrev_b32_sdwa v150,1,v150 dst_sel:DWORD dst_unused:UNUSED_PAD src0_sel:DWORD src1_sel:WORD_1
v_mfma_f32_16x16x32_bf16 v[184:187],v[100:103],v[128:131],v[184:187]
s_waitcnt lgkmcnt(3)
ds_read_b64_tr_b16 v[68:69],v5 offset:12320
ds_read_b64_tr_b16 v[70:71],v5 offset:14368
v_lshl_add_u64 v[136:137],v[136:137],0,v[138:139]
v_fma_mixhi_f16 v120,v158,s99,0
v_mfma_f32_16x16x32_bf16 v[192:195],v[104:107],v[128:131],v[192:195]
ds_read_u16_d16_hi v146,v146
v_cvt_pk_bf16_f32 v138,v140,v142
v_cvt_pk_bf16_f32 v139,v141,v143
v_mfma_f32_16x16x32_bf16 v[200:203],v[108:111],v[128:131],v[200:203]
v_fma_mixhi_f16 v122,v159,s99,0
v_mfma_f32_16x16x32_bf16 v[164:167],v[88:91],v[136:139],v[164:167]
ds_read_b128 v[116:119],v7 offset:1056
ds_read_b64_tr_b16 v[72:73],v4 offset:16416
v_lshlrev_b32_sdwa v149,1,v149 dst_sel:DWORD dst_unused:UNUSED_PAD src0_sel:DWORD src1_sel:WORD_1
v_lshlrev_b32_sdwa v151,1,v151 dst_sel:DWORD dst_unused:UNUSED_PAD src0_sel:DWORD src1_sel:WORD_1
v_mfma_f32_16x16x32_bf16 v[172:175],v[92:95],v[136:139],v[172:175]
ds_read_b64_tr_b16 v[74:75],v4 offset:18464
ds_read_b64_tr_b16 v[76:77],v5 offset:16416
v_pk_fma_f16 v121,v120,v120,s54
v_pk_fma_f16 v123,v122,v122,s54
v_mfma_f32_16x16x32_bf16 v[180:183],v[96:99],v[136:139],v[180:183]
ds_read_u16 v145,v145
v_pk_mul_f16 v121,v120,v121
v_pk_mul_f16 v123,v122,v123
v_mfma_f32_16x16x32_bf16 v[188:191],v[100:103],v[136:139],v[188:191]
ds_read_u16_d16_hi v147,v147
v_lshlrev_b32_sdwa v152,1,v152 dst_sel:DWORD dst_unused:UNUSED_PAD src0_sel:DWORD src1_sel:WORD_1
v_lshlrev_b32_sdwa v154,1,v154 dst_sel:DWORD dst_unused:UNUSED_PAD src0_sel:DWORD src1_sel:WORD_1
v_mfma_f32_16x16x32_bf16 v[196:199],v[104:107],v[136:139],v[196:199]
v_exp_f16 v120,v121
v_mfma_f32_16x16x32_bf16 v[204:207],v[108:111],v[136:139],v[204:207]
s_waitcnt lgkmcnt(10)
ds_read_b64_tr_b16 v[78:79],v5 offset:18464
ds_read_b64_tr_b16 v[80:81],v4 offset:20512
v_exp_f16 v122,v123
v_mfma_f32_16x16x32_bf16 v[128:131],v[64:67],v[16:19],v[112:115]
v_lshlrev_b32_sdwa v153,1,v153 dst_sel:DWORD dst_unused:UNUSED_PAD src0_sel:DWORD src1_sel:WORD_1
v_lshlrev_b32_sdwa v155,1,v155 dst_sel:DWORD dst_unused:UNUSED_PAD src0_sel:DWORD src1_sel:WORD_1
v_mfma_f32_16x16x32_bf16 v[136:139],v[64:67],v[20:23],v[112:115]
s_waitcnt lgkmcnt(7)
ds_read_u16 v148,v148
ds_read_u16_d16_hi v150,v150
v_exp_f16_sdwa v120,v121 dst_sel:WORD_1 dst_unused:UNUSED_PRESERVE src0_sel:WORD_1
v_mfma_f32_16x16x32_bf16 v[140:143],v[68:71],v[20:23],v[116:119]
v_exp_f16_sdwa v122,v123 dst_sel:WORD_1 dst_unused:UNUSED_PRESERVE src0_sel:WORD_1
v_mfma_f32_16x16x32_bf16 v[132:135],v[68:71],v[16:19],v[116:119]
s_waitcnt lgkmcnt(7)
ds_read_b64_tr_b16 v[82:83],v4 offset:22560
v_pk_fma_f16 v120,v120,s55,s55
v_mfma_f32_16x16x32_bf16 v[128:131],v[72:75],v[24:27],v[128:131]
ds_read_b64_tr_b16 v[84:85],v5 offset:20512
v_pk_fma_f16 v122,v122,s55,s55
v_rcp_f16 v121,v120
v_mfma_f32_16x16x32_bf16 v[136:139],v[72:75],v[28:31],v[136:139]
s_waitcnt lgkmcnt(5)
ds_read_b64_tr_b16 v[86:87],v5 offset:22560
ds_read_u16 v149,v149
v_rcp_f16 v123,v122
v_mfma_f32_16x16x32_bf16 v[140:143],v[76:79],v[28:31],v[140:143]
ds_read_u16_d16_hi v151,v151
ds_read_u16 v152,v152
v_rcp_f16_sdwa v121,v120 dst_sel:WORD_1 dst_unused:UNUSED_PRESERVE src0_sel:WORD_1
v_mfma_f32_16x16x32_bf16 v[132:135],v[76:79],v[24:27],v[132:135]
s_waitcnt lgkmcnt(5)
v_rcp_f16_sdwa v123,v122 dst_sel:WORD_1 dst_unused:UNUSED_PRESERVE src0_sel:WORD_1
v_mfma_f32_16x16x32_bf16 v[128:131],v[80:83],v[32:35],v[128:131]
ds_read_u16_d16_hi v154,v154
v_fma_mix_f32 v156,v156,v121,0 op_sel_hi:[0,1,0]
v_fma_mix_f32 v158,v158,v121,0 op_sel:[0,1,0] op_sel_hi:[0,1,0]
v_mfma_f32_16x16x32_bf16 v[136:139],v[80:83],v[36:39],v[136:139]
s_waitcnt lgkmcnt(2)
v_lshl_add_u64 v[144:145],v[144:145],0,v[146:147]
v_lshl_add_u64 v[146:147],v[148:149],0,v[150:151]
v_mfma_f32_16x16x32_bf16 v[140:143],v[84:87],v[36:39],v[140:143]
ds_read_u16 v153,v153
v_fma_mix_f32 v157,v157,v123,0 op_sel_hi:[0,1,0]
v_fma_mix_f32 v159,v159,v123,0 op_sel:[0,1,0] op_sel_hi:[0,1,0]
v_mfma_f32_16x16x32_bf16 v[132:135],v[84:87],v[32:35],v[132:135]
v_lshlrev_b32_sdwa v128,1,v128 dst_sel:DWORD dst_unused:UNUSED_PAD src0_sel:DWORD src1_sel:WORD_1
v_lshlrev_b32_sdwa v130,1,v130 dst_sel:DWORD dst_unused:UNUSED_PAD src0_sel:DWORD src1_sel:WORD_1
v_mfma_f32_16x16x32_bf16 v[208:211],v[88:91],v[144:147],v[208:211]
v_lshlrev_b32_sdwa v129,1,v129 dst_sel:DWORD dst_unused:UNUSED_PAD src0_sel:DWORD src1_sel:WORD_1
v_lshlrev_b32_sdwa v131,1,v131 dst_sel:DWORD dst_unused:UNUSED_PAD src0_sel:DWORD src1_sel:WORD_1
v_mfma_f32_16x16x32_bf16 v[216:219],v[92:95],v[144:147],v[216:219]
ds_read_u16_d16_hi v155,v155
ds_read_u16 v128,v128
v_fma_mixlo_f16 v120,v140,s99,0
v_fma_mixlo_f16 v122,v141,s99,0
v_mfma_f32_16x16x32_bf16 v[224:227],v[96:99],v[144:147],v[224:227]
s_setprio 2
ds_read_u16_d16_hi v130,v130
v_lshlrev_b32_sdwa v132,1,v132 dst_sel:DWORD dst_unused:UNUSED_PAD src0_sel:DWORD src1_sel:WORD_1
v_lshlrev_b32_sdwa v134,1,v134 dst_sel:DWORD dst_unused:UNUSED_PAD src0_sel:DWORD src1_sel:WORD_1
v_mfma_f32_16x16x32_bf16 v[232:235],v[100:103],v[144:147],v[232:235]
s_waitcnt lgkmcnt(2)
v_lshl_add_u64 v[152:153],v[152:153],0,v[154:155]
v_cvt_pk_bf16_f32 v154,v156,v158
v_mfma_f32_16x16x32_bf16 v[240:243],v[104:107],v[144:147],v[240:243]
v_fma_mixhi_f16 v120,v142,s99,0
v_cvt_pk_bf16_f32 v155,v157,v159
v_mfma_f32_16x16x32_bf16 v[248:251],v[108:111],v[144:147],v[248:251]
ds_read_u16 v129,v129
v_fma_mixhi_f16 v122,v143,s99,0
v_mfma_f32_16x16x32_bf16 v[212:215],v[88:91],v[152:155],v[212:215]
v_lshlrev_b32_sdwa v133,1,v133 dst_sel:DWORD dst_unused:UNUSED_PAD src0_sel:DWORD src1_sel:WORD_1
v_pk_fma_f16 v121,v120,v120,s54
v_lshlrev_b32_sdwa v135,1,v135 dst_sel:DWORD dst_unused:UNUSED_PAD src0_sel:DWORD src1_sel:WORD_1
v_mfma_f32_16x16x32_bf16 v[220:223],v[92:95],v[152:155],v[220:223]
ds_read_u16_d16_hi v131,v131
ds_read_u16 v132,v132
v_pk_fma_f16 v123,v122,v122,s54
v_mfma_f32_16x16x32_bf16 v[228:231],v[96:99],v[152:155],v[228:231]
ds_read_b64_tr_b16 v[88:89],v6 offset:18480
v_pk_mul_f16 v121,v120,v121
v_lshlrev_b32_sdwa v136,1,v136 dst_sel:DWORD dst_unused:UNUSED_PAD src0_sel:DWORD src1_sel:WORD_1
v_pk_mul_f16 v123,v122,v123
v_mfma_f32_16x16x32_bf16 v[236:239],v[100:103],v[152:155],v[236:239]
ds_read_b64_tr_b16 v[90:91],v6 offset:20016
v_lshlrev_b32_sdwa v138,1,v138 dst_sel:DWORD dst_unused:UNUSED_PAD src0_sel:DWORD src1_sel:WORD_1
v_mfma_f32_16x16x32_bf16 v[244:247],v[104:107],v[152:155],v[244:247]
ds_read_u16_d16_hi v134,v134
v_exp_f16 v120,v121
v_mfma_f32_16x16x32_bf16 v[252:255],v[108:111],v[152:155],v[252:255]
ds_read_b64_tr_b16 v[92:93],v6 offset:18488
v_lshlrev_b32_sdwa v137,1,v137 dst_sel:DWORD dst_unused:UNUSED_PAD src0_sel:DWORD src1_sel:WORD_1
v_mfma_f32_16x16x32_bf16 v[144:147],v[64:67],v[40:43],v[112:115]
ds_read_b64_tr_b16 v[94:95],v6 offset:20024
v_lshlrev_b32_sdwa v139,1,v139 dst_sel:DWORD dst_unused:UNUSED_PAD src0_sel:DWORD src1_sel:WORD_1
v_exp_f16 v122,v123
v_mfma_f32_16x16x32_bf16 v[152:155],v[64:67],v[44:47],v[112:115]
ds_read_u16 v133,v133
ds_read_b64_tr_b16 v[96:97],v6 offset:18544
v_mfma_f32_16x16x32_bf16 v[156:159],v[68:71],v[44:47],v[116:119]
ds_read_u16_d16_hi v135,v135
v_exp_f16_sdwa v122,v123 dst_sel:WORD_1 dst_unused:UNUSED_PRESERVE src0_sel:WORD_1
v_exp_f16_sdwa v120,v121 dst_sel:WORD_1 dst_unused:UNUSED_PRESERVE src0_sel:WORD_1
v_mfma_f32_16x16x32_bf16 v[148:151],v[68:71],v[40:43],v[116:119]
ds_read_b64_tr_b16 v[98:99],v6 offset:20080
v_pk_fma_f16 v120,v120,s55,s55
v_pk_fma_f16 v122,v122,s55,s55
v_mfma_f32_16x16x32_bf16 v[144:147],v[72:75],v[48:51],v[144:147]
v_rcp_f16 v121,v120
v_mfma_f32_16x16x32_bf16 v[152:155],v[72:75],v[52:55],v[152:155]
ds_read_b64_tr_b16 v[100:101],v6 offset:18552
v_rcp_f16 v123,v122
v_mfma_f32_16x16x32_bf16 v[156:159],v[76:79],v[52:55],v[156:159]
ds_read_u16 v136,v136
v_rcp_f16_sdwa v121,v120 dst_sel:WORD_1 dst_unused:UNUSED_PRESERVE src0_sel:WORD_1
v_mfma_f32_16x16x32_bf16 v[148:151],v[76:79],v[48:51],v[148:151]
ds_read_b64_tr_b16 v[102:103],v6 offset:20088
ds_read_b64_tr_b16 v[104:105],v6 offset:18608
v_rcp_f16_sdwa v123,v122 dst_sel:WORD_1 dst_unused:UNUSED_PRESERVE src0_sel:WORD_1
v_mfma_f32_16x16x32_bf16 v[144:147],v[80:83],v[56:59],v[144:147]
ds_read_u16_d16_hi v138,v138
v_fma_mix_f32 v142,v142,v121,0 op_sel:[0,1,0] op_sel_hi:[0,1,0]
v_mfma_f32_16x16x32_bf16 v[152:155],v[80:83],v[60:63],v[152:155]
s_waitcnt lgkmcnt(6)
ds_read_b64_tr_b16 v[106:107],v6 offset:20144
ds_read_b64_tr_b16 v[108:109],v6 offset:18616
v_fma_mix_f32 v140,v140,v121,0 op_sel_hi:[0,1,0]
v_lshl_add_u64 v[128:129],v[128:129],0,v[130:131]
v_lshl_add_u64 v[130:131],v[132:133],0,v[134:135]
v_mfma_f32_16x16x32_bf16 v[156:159],v[84:87],v[60:63],v[156:159]
ds_read_u16 v137,v137
v_fma_mix_f32 v141,v141,v123,0 op_sel_hi:[0,1,0]
v_fma_mix_f32 v143,v143,v123,0 op_sel:[0,1,0] op_sel_hi:[0,1,0]
v_mfma_f32_16x16x32_bf16 v[148:151],v[84:87],v[56:59],v[148:151]
ds_read_b64_tr_b16 v[110:111],v6 offset:20152
ds_read_b128 v[64:67],v7 offset:1536
v_lshlrev_b32_sdwa v144,1,v144 dst_sel:DWORD dst_unused:UNUSED_PAD src0_sel:DWORD src1_sel:WORD_1
v_lshlrev_b32_sdwa v146,1,v146 dst_sel:DWORD dst_unused:UNUSED_PAD src0_sel:DWORD src1_sel:WORD_1
v_mfma_f32_16x16x32_bf16 v[160:163],v[88:91],v[128:131],v[160:163]
ds_read_u16_d16_hi v139,v139
ds_read_b128 v[68:71],v7 offset:1568
v_lshlrev_b32_sdwa v145,1,v145 dst_sel:DWORD dst_unused:UNUSED_PAD src0_sel:DWORD src1_sel:WORD_1
v_lshlrev_b32_sdwa v147,1,v147 dst_sel:DWORD dst_unused:UNUSED_PAD src0_sel:DWORD src1_sel:WORD_1
v_fma_mixlo_f16 v122,v157,s99,0
v_fma_mixlo_f16 v120,v156,s99,0
v_mfma_f32_16x16x32_bf16 v[168:171],v[92:95],v[128:131],v[168:171]
s_waitcnt lgkmcnt(12)
ds_read_u16 v144,v144
ds_read_b128 v[72:75],v7 offset:1664
v_lshlrev_b32_sdwa v148,1,v148 dst_sel:DWORD dst_unused:UNUSED_PAD src0_sel:DWORD src1_sel:WORD_1
v_lshlrev_b32_sdwa v150,1,v150 dst_sel:DWORD dst_unused:UNUSED_PAD src0_sel:DWORD src1_sel:WORD_1
s_waitcnt lgkmcnt(3)
v_lshl_add_u64 v[136:137],v[136:137],0,v[138:139]
v_fma_mixhi_f16 v120,v158,s99,0
v_mfma_f32_16x16x32_bf16 v[176:179],v[96:99],v[128:131],v[176:179]
s_setprio 1
ds_read_u16_d16_hi v146,v146
ds_read_b128 v[76:79],v7 offset:1696
v_cvt_pk_bf16_f32 v138,v140,v142
v_cvt_pk_bf16_f32 v139,v141,v143
v_fma_mixhi_f16 v122,v159,s99,0
v_lshlrev_b32_sdwa v149,1,v149 dst_sel:DWORD dst_unused:UNUSED_PAD src0_sel:DWORD src1_sel:WORD_1
v_mfma_f32_16x16x32_bf16 v[184:187],v[100:103],v[128:131],v[184:187]
ds_read_u16 v145,v145
ds_read_b128 v[80:83],v7 offset:1792
v_lshlrev_b32_sdwa v151,1,v151 dst_sel:DWORD dst_unused:UNUSED_PAD src0_sel:DWORD src1_sel:WORD_1
v_pk_fma_f16 v121,v120,v120,s54
v_pk_fma_f16 v123,v122,v122,s54
v_pk_mul_f16 v121,v120,v121
v_mfma_f32_16x16x32_bf16 v[192:195],v[104:107],v[128:131],v[192:195]
ds_read_u16_d16_hi v147,v147
ds_read_b128 v[84:87],v7 offset:1824
v_pk_mul_f16 v123,v122,v123
v_lshlrev_b32_sdwa v152,1,v152 dst_sel:DWORD dst_unused:UNUSED_PAD src0_sel:DWORD src1_sel:WORD_1
v_lshlrev_b32_sdwa v154,1,v154 dst_sel:DWORD dst_unused:UNUSED_PAD src0_sel:DWORD src1_sel:WORD_1
v_exp_f16 v120,v121
v_mfma_f32_16x16x32_bf16 v[200:203],v[108:111],v[128:131],v[200:203]
ds_read_u16 v148,v148
v_exp_f16 v122,v123
v_lshlrev_b32_sdwa v153,1,v153 dst_sel:DWORD dst_unused:UNUSED_PAD src0_sel:DWORD src1_sel:WORD_1
v_mfma_f32_16x16x32_bf16 v[164:167],v[88:91],v[136:139],v[164:167]
ds_read_u16_d16_hi v150,v150
v_lshlrev_b32_sdwa v155,1,v155 dst_sel:DWORD dst_unused:UNUSED_PAD src0_sel:DWORD src1_sel:WORD_1
v_exp_f16_sdwa v120,v121 dst_sel:WORD_1 dst_unused:UNUSED_PRESERVE src0_sel:WORD_1
v_exp_f16_sdwa v122,v123 dst_sel:WORD_1 dst_unused:UNUSED_PRESERVE src0_sel:WORD_1
v_mfma_f32_16x16x32_bf16 v[172:175],v[92:95],v[136:139],v[172:175]
ds_read_u16 v149,v149
v_pk_fma_f16 v120,v120,s55,s55
v_pk_fma_f16 v122,v122,s55,s55
v_rcp_f16 v121,v120
v_mfma_f32_16x16x32_bf16 v[180:183],v[96:99],v[136:139],v[180:183]
ds_read_u16_d16_hi v151,v151
v_rcp_f16 v123,v122
v_rcp_f16_sdwa v121,v120 dst_sel:WORD_1 dst_unused:UNUSED_PRESERVE src0_sel:WORD_1
v_mfma_f32_16x16x32_bf16 v[188:191],v[100:103],v[136:139],v[188:191]
ds_read_u16 v152,v152
v_rcp_f16_sdwa v123,v122 dst_sel:WORD_1 dst_unused:UNUSED_PRESERVE src0_sel:WORD_1
v_fma_mix_f32 v156,v156,v121,0 op_sel_hi:[0,1,0]
v_mfma_f32_16x16x32_bf16 v[196:199],v[104:107],v[136:139],v[196:199]
s_setprio 0
ds_read_u16_d16_hi v154,v154
v_fma_mix_f32 v158,v158,v121,0 op_sel:[0,1,0] op_sel_hi:[0,1,0]
s_waitcnt lgkmcnt(7)
v_lshl_add_u64 v[144:145],v[144:145],0,v[146:147]
s_waitcnt lgkmcnt(2)
v_lshl_add_u64 v[146:147],v[148:149],0,v[150:151]
v_fma_mix_f32 v157,v157,v123,0 op_sel_hi:[0,1,0]
v_mfma_f32_16x16x32_bf16 v[204:207],v[108:111],v[136:139],v[204:207]
ds_read_u16 v153,v153
v_fma_mix_f32 v159,v159,v123,0 op_sel:[0,1,0] op_sel_hi:[0,1,0]
ds_read_u16_d16_hi v155,v155
s_waitcnt lgkmcnt(0)
v_lshl_add_u64 v[152:153],v[152:153],0,v[154:155]
v_cvt_pk_bf16_f32 v154,v156,v158
v_cvt_pk_bf16_f32 v155,v157,v159
v_mfma_f32_16x16x32_bf16 v[208:211],v[88:91],v[144:147],v[208:211]
s_barrier
s_setprio 0
v_xor_b32 v9,64,v10
v_mul_f32 v160,v160,v64
v_mul_f32 v161,v161,v65
v_mul_f32 v162,v162,v66
v_mul_f32 v163,v163,v67
v_mfma_f32_16x16x32_bf16 v[216:219],v[92:95],v[144:147],v[216:219]
v_mul_f32 v168,v168,v68
v_mul_f32 v169,v169,v69
v_mul_f32 v170,v170,v70
v_mul_f32 v171,v171,v71
v_cvt_pk_bf16_f32 v16,v160,v161
v_mfma_f32_16x16x32_bf16 v[224:227],v[96:99],v[144:147],v[224:227]
v_cvt_pk_bf16_f32 v17,v162,v163
v_cvt_pk_bf16_f32 v18,v168,v169
v_cvt_pk_bf16_f32 v19,v170,v171
v_mul_f32_dpp v176,v176,v72 quad_perm:[1,0,3,2] row_mask:0xf bank_mask:0xf
v_mfma_f32_16x16x32_bf16 v[232:235],v[100:103],v[144:147],v[232:235]
v_mul_f32_dpp v177,v177,v73 quad_perm:[1,0,3,2] row_mask:0xf bank_mask:0xf
v_mul_f32_dpp v178,v178,v74 quad_perm:[1,0,3,2] row_mask:0xf bank_mask:0xf
v_mul_f32_dpp v179,v179,v75 quad_perm:[1,0,3,2] row_mask:0xf bank_mask:0xf
v_mul_f32_dpp v184,v184,v76 quad_perm:[1,0,3,2] row_mask:0xf bank_mask:0xf
v_mul_f32_dpp v185,v185,v77 quad_perm:[1,0,3,2] row_mask:0xf bank_mask:0xf
v_mfma_f32_16x16x32_bf16 v[240:243],v[104:107],v[144:147],v[240:243]
v_mul_f32_dpp v186,v186,v78 quad_perm:[1,0,3,2] row_mask:0xf bank_mask:0xf
v_mul_f32_dpp v187,v187,v79 quad_perm:[1,0,3,2] row_mask:0xf bank_mask:0xf
v_cvt_pk_bf16_f32 v24,v176,v177
v_cvt_pk_bf16_f32 v25,v178,v179
v_mfma_f32_16x16x32_bf16 v[248:251],v[108:111],v[144:147],v[248:251]
v_cvt_pk_bf16_f32 v26,v184,v185
v_cvt_pk_bf16_f32 v27,v186,v187
s_mov_b64 exec,s[12:13]
v_swap_b32 v16,v24
v_swap_b32 v17,v25
v_swap_b32 v18,v26
v_swap_b32 v19,v27
s_mov_b64 exec,-1
v_mfma_f32_16x16x32_bf16 v[212:215],v[88:91],v[152:155],v[212:215]
s_add_u32 s65,s61,1572864
buffer_store_dwordx4 v[16:19],v10,s[40:43],s65 offen offset:0 sc1
v_mfma_f32_16x16x32_bf16 v[220:223],v[92:95],v[152:155],v[220:223]
v_mul_f32 v192,v192,v80
v_mul_f32 v193,v193,v81
v_mul_f32 v194,v194,v82
v_mul_f32 v195,v195,v83
v_mfma_f32_16x16x32_bf16 v[228:231],v[96:99],v[152:155],v[228:231]
v_mul_f32 v200,v200,v84
v_mul_f32 v201,v201,v85
v_mul_f32 v202,v202,v86
v_mul_f32 v203,v203,v87
v_mfma_f32_16x16x32_bf16 v[236:239],v[100:103],v[152:155],v[236:239]
v_cvt_pk_bf16_f32 v32,v192,v193
v_cvt_pk_bf16_f32 v33,v194,v195
v_cvt_pk_bf16_f32 v34,v200,v201
v_cvt_pk_bf16_f32 v35,v202,v203
v_mul_f32_dpp v164,v164,v64 quad_perm:[1,0,3,2] row_mask:0xf bank_mask:0xf
v_mfma_f32_16x16x32_bf16 v[244:247],v[104:107],v[152:155],v[244:247]
v_mul_f32_dpp v165,v165,v65 quad_perm:[1,0,3,2] row_mask:0xf bank_mask:0xf
v_mul_f32_dpp v166,v166,v66 quad_perm:[1,0,3,2] row_mask:0xf bank_mask:0xf
v_mul_f32_dpp v167,v167,v67 quad_perm:[1,0,3,2] row_mask:0xf bank_mask:0xf
v_mul_f32_dpp v172,v172,v68 quad_perm:[1,0,3,2] row_mask:0xf bank_mask:0xf
v_mfma_f32_16x16x32_bf16 v[252:255],v[108:111],v[152:155],v[252:255]
v_mul_f32_dpp v173,v173,v69 quad_perm:[1,0,3,2] row_mask:0xf bank_mask:0xf
s_add_u32 s65,s61,1572864
buffer_store_dwordx4 v[24:27],v9,s[40:43],s65 offen offset:384 sc1
v_mul_f32_dpp v174,v174,v70 quad_perm:[1,0,3,2] row_mask:0xf bank_mask:0xf
v_mul_f32_dpp v175,v175,v71 quad_perm:[1,0,3,2] row_mask:0xf bank_mask:0xf
v_cvt_pk_bf16_f32 v20,v164,v165
v_cvt_pk_bf16_f32 v21,v166,v167
v_cvt_pk_bf16_f32 v22,v172,v173
v_cvt_pk_bf16_f32 v23,v174,v175
s_mov_b64 exec,s[12:13]
v_swap_b32 v32,v20
v_swap_b32 v33,v21
v_swap_b32 v34,v22
v_swap_b32 v35,v23
s_mov_b64 exec,-1
v_mul_f32 v180,v180,v72
v_mul_f32 v181,v181,v73
v_mul_f32 v182,v182,v74
v_mul_f32 v183,v183,v75
v_mul_f32 v188,v188,v76
v_mul_f32 v189,v189,v77
v_mul_f32 v190,v190,v78
v_mul_f32 v191,v191,v79
v_cvt_pk_bf16_f32 v28,v180,v181
v_cvt_pk_bf16_f32 v29,v182,v183
v_cvt_pk_bf16_f32 v30,v188,v189
v_cvt_pk_bf16_f32 v31,v190,v191
v_mul_f32_dpp v196,v196,v80 quad_perm:[1,0,3,2] row_mask:0xf bank_mask:0xf
v_mul_f32_dpp v197,v197,v81 quad_perm:[1,0,3,2] row_mask:0xf bank_mask:0xf
v_mul_f32_dpp v198,v198,v82 quad_perm:[1,0,3,2] row_mask:0xf bank_mask:0xf
v_mul_f32_dpp v199,v199,v83 quad_perm:[1,0,3,2] row_mask:0xf bank_mask:0xf
s_add_u32 s65,s61,1572864
buffer_store_dwordx4 v[32:35],v10,s[40:43],s65 offen offset:128 sc1
v_mul_f32_dpp v204,v204,v84 quad_perm:[1,0,3,2] row_mask:0xf bank_mask:0xf
v_mul_f32_dpp v205,v205,v85 quad_perm:[1,0,3,2] row_mask:0xf bank_mask:0xf
v_mul_f32_dpp v206,v206,v86 quad_perm:[1,0,3,2] row_mask:0xf bank_mask:0xf
v_mul_f32_dpp v207,v207,v87 quad_perm:[1,0,3,2] row_mask:0xf bank_mask:0xf
v_cvt_pk_bf16_f32 v36,v196,v197
v_cvt_pk_bf16_f32 v37,v198,v199
v_cvt_pk_bf16_f32 v38,v204,v205
v_cvt_pk_bf16_f32 v39,v206,v207
s_mov_b64 exec,s[12:13]
v_swap_b32 v28,v36
v_swap_b32 v29,v37
v_swap_b32 v30,v38
v_swap_b32 v31,v39
s_mov_b64 exec,-1
v_mul_f32 v208,v208,v64
v_mul_f32 v209,v209,v65
v_mul_f32 v210,v210,v66
v_mul_f32 v211,v211,v67
v_mul_f32 v216,v216,v68
v_mul_f32 v217,v217,v69
v_mul_f32 v218,v218,v70
v_mul_f32 v219,v219,v71
v_cvt_pk_bf16_f32 v40,v208,v209
v_cvt_pk_bf16_f32 v41,v210,v211
v_cvt_pk_bf16_f32 v42,v216,v217
v_cvt_pk_bf16_f32 v43,v218,v219
v_mul_f32_dpp v224,v224,v72 quad_perm:[1,0,3,2] row_mask:0xf bank_mask:0xf
v_mul_f32_dpp v225,v225,v73 quad_perm:[1,0,3,2] row_mask:0xf bank_mask:0xf
s_add_u32 s65,s61,1572864
buffer_store_dwordx4 v[20:23],v9,s[40:43],s65 offen offset:512 sc1
v_mul_f32_dpp v226,v226,v74 quad_perm:[1,0,3,2] row_mask:0xf bank_mask:0xf
v_mul_f32_dpp v227,v227,v75 quad_perm:[1,0,3,2] row_mask:0xf bank_mask:0xf
v_mul_f32_dpp v232,v232,v76 quad_perm:[1,0,3,2] row_mask:0xf bank_mask:0xf
v_mul_f32_dpp v233,v233,v77 quad_perm:[1,0,3,2] row_mask:0xf bank_mask:0xf
v_mul_f32_dpp v234,v234,v78 quad_perm:[1,0,3,2] row_mask:0xf bank_mask:0xf
v_mul_f32_dpp v235,v235,v79 quad_perm:[1,0,3,2] row_mask:0xf bank_mask:0xf
v_cvt_pk_bf16_f32 v48,v224,v225
v_cvt_pk_bf16_f32 v49,v226,v227
v_cvt_pk_bf16_f32 v50,v232,v233
v_cvt_pk_bf16_f32 v51,v234,v235
s_mov_b64 exec,s[12:13]
v_swap_b32 v40,v48
v_swap_b32 v41,v49
v_swap_b32 v42,v50
v_swap_b32 v43,v51
s_mov_b64 exec,-1
v_mul_f32 v240,v240,v80
v_mul_f32 v241,v241,v81
v_mul_f32 v242,v242,v82
v_mul_f32 v243,v243,v83
v_mul_f32 v248,v248,v84
v_mul_f32 v249,v249,v85
v_mul_f32 v250,v250,v86
v_mul_f32 v251,v251,v87
v_cvt_pk_bf16_f32 v56,v240,v241
v_cvt_pk_bf16_f32 v57,v242,v243
v_cvt_pk_bf16_f32 v58,v248,v249
v_cvt_pk_bf16_f32 v59,v250,v251
s_add_u32 s65,s61,1572864
buffer_store_dwordx4 v[28:31],v10,s[40:43],s65 offen offset:256 sc1
v_mul_f32_dpp v212,v212,v64 quad_perm:[1,0,3,2] row_mask:0xf bank_mask:0xf
v_mul_f32_dpp v213,v213,v65 quad_perm:[1,0,3,2] row_mask:0xf bank_mask:0xf
v_mul_f32_dpp v214,v214,v66 quad_perm:[1,0,3,2] row_mask:0xf bank_mask:0xf
v_mul_f32_dpp v215,v215,v67 quad_perm:[1,0,3,2] row_mask:0xf bank_mask:0xf
v_mul_f32_dpp v220,v220,v68 quad_perm:[1,0,3,2] row_mask:0xf bank_mask:0xf
v_mul_f32_dpp v221,v221,v69 quad_perm:[1,0,3,2] row_mask:0xf bank_mask:0xf
v_mul_f32_dpp v222,v222,v70 quad_perm:[1,0,3,2] row_mask:0xf bank_mask:0xf
v_mul_f32_dpp v223,v223,v71 quad_perm:[1,0,3,2] row_mask:0xf bank_mask:0xf
v_cvt_pk_bf16_f32 v44,v212,v213
v_cvt_pk_bf16_f32 v45,v214,v215
v_cvt_pk_bf16_f32 v46,v220,v221
v_cvt_pk_bf16_f32 v47,v222,v223
s_mov_b64 exec,s[12:13]
v_swap_b32 v56,v44
v_swap_b32 v57,v45
v_swap_b32 v58,v46
v_swap_b32 v59,v47
s_mov_b64 exec,-1
v_mul_f32 v228,v228,v72
v_mul_f32 v229,v229,v73
v_mul_f32 v230,v230,v74
v_mul_f32 v231,v231,v75
v_mul_f32 v236,v236,v76
v_mul_f32 v237,v237,v77
v_mul_f32 v238,v238,v78
v_mul_f32 v239,v239,v79
v_cvt_pk_bf16_f32 v52,v228,v229
v_cvt_pk_bf16_f32 v53,v230,v231
s_add_u32 s65,s61,1572864
buffer_store_dwordx4 v[36:39],v9,s[40:43],s65 offen offset:640 sc1
v_cvt_pk_bf16_f32 v54,v236,v237
v_cvt_pk_bf16_f32 v55,v238,v239
v_mul_f32_dpp v244,v244,v80 quad_perm:[1,0,3,2] row_mask:0xf bank_mask:0xf
v_mul_f32_dpp v245,v245,v81 quad_perm:[1,0,3,2] row_mask:0xf bank_mask:0xf
v_mul_f32_dpp v246,v246,v82 quad_perm:[1,0,3,2] row_mask:0xf bank_mask:0xf
v_mul_f32_dpp v247,v247,v83 quad_perm:[1,0,3,2] row_mask:0xf bank_mask:0xf
v_mul_f32_dpp v252,v252,v84 quad_perm:[1,0,3,2] row_mask:0xf bank_mask:0xf
s_nop 0
s_mov_b32 s86,0xbe801d5a
v_mul_f32_dpp v253,v253,v85 quad_perm:[1,0,3,2] row_mask:0xf bank_mask:0xf
v_mul_f32_dpp v254,v254,v86 quad_perm:[1,0,3,2] row_mask:0xf bank_mask:0xf
v_mul_f32_dpp v255,v255,v87 quad_perm:[1,0,3,2] row_mask:0xf bank_mask:0xf
v_cvt_pk_bf16_f32 v60,v244,v245
v_cvt_pk_bf16_f32 v61,v246,v247
v_cvt_pk_bf16_f32 v62,v252,v253
v_cvt_pk_bf16_f32 v63,v254,v255
s_mov_b64 exec,s[12:13]
v_swap_b32 v52,v60
v_swap_b32 v53,v61
v_swap_b32 v54,v62
v_swap_b32 v55,v63
s_mov_b64 exec,-1
s_add_u32 s4,s10,8
s_cmp_ge_u32 s4,20
s_cselect_b32 s7,20,0
s_sub_u32 s4,s4,s7
s_mul_i32 s4,s4,3488
s_add_u32 s5,s10,10
s_cmp_ge_u32 s5,20
s_cselect_b32 s7,20,0
s_sub_u32 s5,s5,s7
s_mul_i32 s5,s5,3488
s_add_u32 s6,s10,12
s_cmp_ge_u32 s6,20
s_cselect_b32 s7,20,0
s_sub_u32 s6,s6,s7
s_mul_i32 s6,s6,3488
v_add_u32 v112,s4,v15
v_add_u32 v116,s5,v15
s_mov_b64 exec,s[8:9]
v_add_u32 v112,s5,v15
v_add_u32 v116,s6,v15
s_mov_b64 exec,-1
v_xor_b32 v113,16,v112
v_xor_b32 v117,16,v116
v_xor_b32 v114,32,v112
v_xor_b32 v118,32,v116
v_xor_b32 v115,48,v112
v_xor_b32 v119,48,v116
s_barrier
s_setprio 3
ds_read_b128 v[136:139],v116 offset:0
ds_read_b128 v[88:91],v11 offset:768
ds_read_b128 v[140:143],v116 offset:3488
ds_read_b128 v[128:131],v112 offset:0
ds_read_b128 v[132:135],v112 offset:3488
ds_read_b128 v[92:95],v12 offset:0
ds_read_b128 v[152:155],v117 offset:0
ds_read_b128 v[96:99],v11 offset:1920
ds_read_b128 v[156:159],v117 offset:3488
ds_read_b128 v[144:147],v113 offset:0
ds_read_b128 v[148:151],v113 offset:3488
ds_read_b128 v[100:103],v12 offset:1152
s_waitcnt lgkmcnt(10)
v_mfma_f32_16x16x32_bf16 v[160:163],v[88:91],v[136:139],0
s_waitcnt lgkmcnt(9)
s_add_u32 s65,s61,1671168
buffer_store_dwordx4 v[40:43],v10,s[40:43],s65 offen offset:0 sc1
v_mfma_f32_16x16x32_bf16 v[184:187],v[88:91],v[140:143],0
s_waitcnt lgkmcnt(6)
ds_read_b128 v[140:143],v116 offset:3552
ds_read_b128 v[88:91],v11 offset:5376
v_smfmac_f32_16x16x64_bf16 v[160:163],v[92:95],v[128:135],v13
ds_read_b128 v[128:131],v112 offset:64
v_smfmac_f32_16x16x64_bf16 v[184:187],v[92:95],v[132:139],v13
s_waitcnt lgkmcnt(7)
ds_read_b128 v[136:139],v116 offset:64
ds_read_b128 v[132:135],v112 offset:3552
v_mfma_f32_16x16x32_bf16 v[164:167],v[96:99],v[152:155],0
s_waitcnt lgkmcnt(8)
ds_read_b128 v[92:95],v12 offset:4608
v_add_f32 v120,v160,v161
v_mul_f32 v124,v160,v160
v_fmac_f32 v124,v161,v161
v_mfma_f32_16x16x32_bf16 v[188:191],v[96:99],v[156:159],0
s_waitcnt lgkmcnt(6)
ds_read_b128 v[156:159],v117 offset:3552
ds_read_b128 v[96:99],v11 offset:6528
v_add_f32 v121,v162,v163
v_mul_f32 v125,v162,v162
v_fmac_f32 v125,v163,v163
v_smfmac_f32_16x16x64_bf16 v[164:167],v[100:103],v[144:151],v13
ds_read_b128 v[144:147],v113 offset:64
v_add_f32 v122,v184,v185
v_mul_f32 v126,v184,v184
v_fmac_f32 v126,v185,v185
v_smfmac_f32_16x16x64_bf16 v[188:191],v[100:103],v[148:155],v13
s_waitcnt lgkmcnt(5)
s_add_u32 s65,s61,1671168
buffer_store_dwordx4 v[48:51],v9,s[40:43],s65 offen offset:384 sc1
ds_read_b128 v[152:155],v117 offset:64
ds_read_b128 v[148:151],v113 offset:3552
v_add_f32 v123,v186,v187
v_mul_f32 v127,v186,v186
v_fmac_f32 v127,v187,v187
v_mfma_f32_16x16x32_bf16 v[168:171],v[88:91],v[136:139],0
ds_read_b128 v[100:103],v12 offset:5760
v_add_f32 v120,v120,v164
v_fmac_f32 v124,v164,v164
v_add_f32 v120,v120,v165
v_fmac_f32 v124,v165,v165
v_mfma_f32_16x16x32_bf16 v[192:195],v[88:91],v[140:143],0
s_waitcnt lgkmcnt(6)
ds_read_b128 v[140:143],v116 offset:3616
ds_read_b128 v[88:91],v11 offset:9984
v_add_f32 v121,v121,v166
v_fmac_f32 v125,v166,v166
v_add_f32 v121,v121,v167
v_fmac_f32 v125,v167,v167
v_smfmac_f32_16x16x64_bf16 v[168:171],v[92:95],v[128:135],v13
ds_read_b128 v[128:131],v112 offset:128
v_add_f32 v122,v122,v188
v_fmac_f32 v126,v188,v188
v_add_f32 v122,v122,v189
v_fmac_f32 v126,v189,v189
v_smfmac_f32_16x16x64_bf16 v[192:195],v[92:95],v[132:139],v13
s_waitcnt lgkmcnt(5)
ds_read_b128 v[136:139],v116 offset:128
ds_read_b128 v[132:135],v112 offset:3616
v_add_f32 v123,v123,v190
v_fmac_f32 v127,v190,v190
v_add_f32 v123,v123,v191
v_fmac_f32 v127,v191,v191
v_mfma_f32_16x16x32_bf16 v[172:175],v[96:99],v[152:155],0
s_add_u32 s65,s61,1671168
buffer_store_dwordx4 v[56:59],v10,s[40:43],s65 offen offset:128 sc1
ds_read_b128 v[92:95],v12 offset:9216
v_add_f32 v120,v120,v168
v_fmac_f32 v124,v168,v168
v_add_f32 v120,v120,v169
v_fmac_f32 v124,v169,v169
v_mfma_f32_16x16x32_bf16 v[196:199],v[96:99],v[156:159],0
s_waitcnt lgkmcnt(6)
ds_read_b128 v[156:159],v117 offset:3616
ds_read_b128 v[96:99],v11 offset:11136
v_add_f32 v121,v121,v170
v_fmac_f32 v125,v170,v170
v_add_f32 v121,v121,v171
v_fmac_f32 v125,v171,v171
v_smfmac_f32_16x16x64_bf16 v[172:175],v[100:103],v[144:151],v13
ds_read_b128 v[144:147],v113 offset:128
v_add_f32 v122,v122,v192
v_fmac_f32 v126,v192,v192
v_add_f32 v122,v122,v193
v_fmac_f32 v126,v193,v193
v_smfmac_f32_16x16x64_bf16 v[196:199],v[100:103],v[148:155],v13
s_waitcnt lgkmcnt(5)
ds_read_b128 v[152:155],v117 offset:128
ds_read_b128 v[148:151],v113 offset:3616
v_add_f32 v123,v123,v194
v_fmac_f32 v127,v194,v194
v_add_f32 v123,v123,v195
v_fmac_f32 v127,v195,v195
v_mfma_f32_16x16x32_bf16 v[176:179],v[88:91],v[136:139],0
ds_read_b128 v[100:103],v12 offset:10368
v_add_f32 v120,v120,v172
v_fmac_f32 v124,v172,v172
v_add_f32 v120,v120,v173
v_fmac_f32 v124,v173,v173
v_mfma_f32_16x16x32_bf16 v[200:203],v[88:91],v[140:143],0
s_waitcnt lgkmcnt(6)
s_add_u32 s65,s61,1671168
buffer_store_dwordx4 v[44:47],v9,s[40:43],s65 offen offset:512 sc1
ds_read_b128 v[140:143],v118 offset:3488
ds_read_b128 v[88:91],v11 offset:3072
v_add_f32 v121,v121,v174
v_fmac_f32 v125,v174,v174
v_add_f32 v121,v121,v175
v_fmac_f32 v125,v175,v175
v_smfmac_f32_16x16x64_bf16 v[176:179],v[92:95],v[128:135],v13
ds_read_b128 v[128:131],v114 offset:0
v_add_f32 v122,v122,v196
v_fmac_f32 v126,v196,v196
v_add_f32 v122,v122,v197
v_fmac_f32 v126,v197,v197
v_smfmac_f32_16x16x64_bf16 v[200:203],v[92:95],v[132:139],v13
s_waitcnt lgkmcnt(5)
ds_read_b128 v[136:139],v118 offset:0
ds_read_b128 v[132:135],v114 offset:3488
v_add_f32 v123,v123,v198
v_fmac_f32 v127,v198,v198
v_add_f32 v123,v123,v199
v_fmac_f32 v127,v199,v199
v_mfma_f32_16x16x32_bf16 v[180:183],v[96:99],v[152:155],0
ds_read_b128 v[92:95],v12 offset:2304
v_add_f32 v120,v120,v176
v_fmac_f32 v124,v176,v176
v_add_f32 v120,v120,v177
v_fmac_f32 v124,v177,v177
v_mfma_f32_16x16x32_bf16 v[204:207],v[96:99],v[156:159],0
s_waitcnt lgkmcnt(6)
s_add_u32 s65,s61,1671168
buffer_store_dwordx4 v[52:55],v10,s[40:43],s65 offen offset:256 sc1
ds_read_b128 v[156:159],v119 offset:3488
ds_read_b128 v[96:99],v11 offset:4224
v_add_f32 v121,v121,v178
v_fmac_f32 v125,v178,v178
v_add_f32 v121,v121,v179
v_fmac_f32 v125,v179,v179
v_smfmac_f32_16x16x64_bf16 v[180:183],v[100:103],v[144:151],v13
ds_read_b128 v[144:147],v115 offset:0
v_add_f32 v122,v122,v200
v_fmac_f32 v126,v200,v200
v_add_f32 v122,v122,v201
v_fmac_f32 v126,v201,v201
v_smfmac_f32_16x16x64_bf16 v[204:207],v[100:103],v[148:155],v13
s_waitcnt lgkmcnt(5)
ds_read_b128 v[152:155],v119 offset:0
ds_read_b128 v[148:151],v115 offset:3488
v_add_f32 v123,v123,v202
v_fmac_f32 v127,v202,v202
v_add_f32 v123,v123,v203
v_fmac_f32 v127,v203,v203
v_mfma_f32_16x16x32_bf16 v[208:211],v[88:91],v[136:139],0
ds_read_b128 v[100:103],v12 offset:3456
v_add_f32 v120,v120,v180
v_fmac_f32 v124,v180,v180
v_add_f32 v120,v120,v181
v_fmac_f32 v124,v181,v181
v_mfma_f32_16x16x32_bf16 v[232:235],v[88:91],v[140:143],0
s_waitcnt lgkmcnt(6)
ds_read_b128 v[140:143],v118 offset:3552
ds_read_b128 v[88:91],v11 offset:7680
v_add_f32 v121,v121,v182
v_fmac_f32 v125,v182,v182
v_add_f32 v121,v121,v183
v_fmac_f32 v125,v183,v183
v_smfmac_f32_16x16x64_bf16 v[208:211],v[92:95],v[128:135],v13
s_add_u32 s65,s61,1671168
buffer_store_dwordx4 v[60:63],v9,s[40:43],s65 offen offset:640 sc1
ds_read_b128 v[128:131],v114 offset:64
v_add_f32 v122,v122,v204
v_fmac_f32 v126,v204,v204
v_add_f32 v122,v122,v205
v_fmac_f32 v126,v205,v205
v_smfmac_f32_16x16x64_bf16 v[232:235],v[92:95],v[132:139],v13
s_waitcnt lgkmcnt(5)
ds_read_b128 v[136:139],v118 offset:64
ds_read_b128 v[132:135],v114 offset:3552
v_add_f32 v123,v123,v206
v_fmac_f32 v127,v206,v206
v_add_f32 v123,v123,v207
v_fmac_f32 v127,v207,v207
v_mfma_f32_16x16x32_bf16 v[212:215],v[96:99],v[152:155],0
ds_read_b128 v[92:95],v12 offset:6912
v_add_f32 v120,v120,v208
v_fmac_f32 v124,v208,v208
v_add_f32 v120,v120,v209
v_fmac_f32 v124,v209,v209
v_mfma_f32_16x16x32_bf16 v[236:239],v[96:99],v[156:159],0
s_waitcnt lgkmcnt(6)
s_setprio 2
ds_read_b128 v[156:159],v119 offset:3552
ds_read_b128 v[96:99],v11 offset:8832
v_add_f32 v121,v121,v210
v_fmac_f32 v125,v210,v210
v_add_f32 v121,v121,v211
v_fmac_f32 v125,v211,v211
v_smfmac_f32_16x16x64_bf16 v[212:215],v[100:103],v[144:151],v13
ds_read_b128 v[144:147],v115 offset:64
v_add_f32 v122,v122,v232
v_fmac_f32 v126,v232,v232
v_add_f32 v122,v122,v233
v_fmac_f32 v126,v233,v233
v_smfmac_f32_16x16x64_bf16 v[236:239],v[100:103],v[148:155],v13
s_waitcnt lgkmcnt(5)
ds_read_b128 v[152:155],v119 offset:64
ds_read_b128 v[148:151],v115 offset:3552
v_add_f32 v123,v123,v234
v_fmac_f32 v127,v234,v234
v_add_f32 v123,v123,v235
v_fmac_f32 v127,v235,v235
v_mfma_f32_16x16x32_bf16 v[216:219],v[88:91],v[136:139],0
ds_read_b128 v[100:103],v12 offset:8064
v_add_f32 v120,v120,v212
v_fmac_f32 v124,v212,v212
v_add_f32 v120,v120,v213
v_fmac_f32 v124,v213,v213
v_add_f32 v121,v121,v214
v_mfma_f32_16x16x32_bf16 v[240:243],v[88:91],v[140:143],0
s_waitcnt lgkmcnt(6)
ds_read_b128 v[140:143],v118 offset:3616
ds_read_b128 v[88:91],v11 offset:12288
v_fmac_f32 v125,v214,v214
v_add_f32 v121,v121,v215
v_fmac_f32 v125,v215,v215
v_add_f32 v122,v122,v236
v_smfmac_f32_16x16x64_bf16 v[216:219],v[92:95],v[128:135],v13
ds_read_b128 v[128:131],v114 offset:128
v_fmac_f32 v126,v236,v236
v_add_f32 v122,v122,v237
v_fmac_f32 v126,v237,v237
v_add_f32 v123,v123,v238
v_fmac_f32 v127,v238,v238
v_smfmac_f32_16x16x64_bf16 v[240:243],v[92:95],v[132:139],v13
s_waitcnt lgkmcnt(5)
ds_read_b128 v[136:139],v118 offset:128
ds_read_b128 v[132:135],v114 offset:3616
v_add_f32 v123,v123,v239
v_fmac_f32 v127,v239,v239
v_add_u32 v112,v112,v8
v_add_u32 v116,v116,v8
v_mfma_f32_16x16x32_bf16 v[220:223],v[96:99],v[152:155],0
ds_read_b128 v[92:95],v12 offset:11520
ds_read_b128 v[104:107],v14 offset:0
v_add_f32 v120,v120,v216
v_fmac_f32 v124,v216,v216
v_add_f32 v120,v120,v217
v_fmac_f32 v124,v217,v217
v_mfma_f32_16x16x32_bf16 v[244:247],v[96:99],v[156:159],0
s_waitcnt lgkmcnt(7)
ds_read_b128 v[108:111],v7 offset:3584
ds_read_b128 v[156:159],v119 offset:3616
v_add_f32 v121,v121,v218
v_fmac_f32 v125,v218,v218
v_add_f32 v121,v121,v219
v_fmac_f32 v125,v219,v219
v_smfmac_f32_16x16x64_bf16 v[220:223],v[100:103],v[144:151],v13
ds_read_b128 v[96:99],v11 offset:13440
ds_read_b128 v[144:147],v115 offset:128
v_add_f32 v122,v122,v240
v_fmac_f32 v126,v240,v240
v_add_f32 v122,v122,v241
v_fmac_f32 v126,v241,v241
v_smfmac_f32_16x16x64_bf16 v[244:247],v[100:103],v[148:155],v13
s_waitcnt lgkmcnt(7)
ds_read_b128 v[152:155],v119 offset:128
ds_read_b128 v[148:151],v115 offset:3616
v_add_f32 v123,v123,v242
v_fmac_f32 v127,v242,v242
v_add_f32 v123,v123,v243
v_fmac_f32 v127,v243,v243
v_mfma_f32_16x16x32_bf16 v[224:227],v[88:91],v[136:139],0
ds_read_b128 v[100:103],v12 offset:12672
v_add_f32 v120,v120,v220
v_fmac_f32 v124,v220,v220
v_add_f32 v120,v120,v221
v_fmac_f32 v124,v221,v221
v_mfma_f32_16x16x32_bf16 v[248:251],v[88:91],v[140:143],0
s_waitcnt lgkmcnt(8)
s_setprio 1
ds_read_b128 v[140:143],v116 offset:192
ds_read_b128 v[88:91],v112 offset:3616
v_add_f32 v121,v121,v222
v_fmac_f32 v125,v222,v222
v_add_f32 v121,v121,v223
v_fmac_f32 v125,v223,v223
v_smfmac_f32_16x16x64_bf16 v[224:227],v[92:95],v[128:135],v13
ds_read_b128 v[128:131],v112 offset:3488
v_add_f32 v122,v122,v244
v_fmac_f32 v126,v244,v244
v_add_f32 v122,v122,v245
v_fmac_f32 v126,v245,v245
v_smfmac_f32_16x16x64_bf16 v[248:251],v[92:95],v[132:139],v13
s_waitcnt lgkmcnt(5)
ds_read_b128 v[136:139],v116 offset:0
ds_read_b128 v[132:135],v112 offset:3680
v_add_f32 v123,v123,v246
v_fmac_f32 v127,v246,v246
v_add_f32 v123,v123,v247
v_fmac_f32 v127,v247,v247
v_mfma_f32_16x16x32_bf16 v[228:231],v[96:99],v[152:155],0
ds_read_b128 v[92:95],v112 offset:3808
v_add_f32 v120,v120,v224
v_fmac_f32 v124,v224,v224
v_add_f32 v120,v120,v225
v_fmac_f32 v124,v225,v225
v_mfma_f32_16x16x32_bf16 v[252:255],v[96:99],v[156:159],0
s_waitcnt lgkmcnt(6)
ds_read_b128 v[156:159],v116 offset:256
ds_read_b128 v[96:99],v116 offset:128
v_add_f32 v121,v121,v226
v_fmac_f32 v125,v226,v226
v_add_f32 v121,v121,v227
v_fmac_f32 v125,v227,v227
v_smfmac_f32_16x16x64_bf16 v[228:231],v[100:103],v[144:151],v13
ds_read_b128 v[144:147],v112 offset:3552
v_add_f32 v122,v122,v248
v_fmac_f32 v126,v248,v248
v_add_f32 v122,v122,v249
v_fmac_f32 v126,v249,v249
v_smfmac_f32_16x16x64_bf16 v[252:255],v[100:103],v[148:155],v13
s_setprio 0
ds_read_b128 v[152:155],v116 offset:64
ds_read_b128 v[148:151],v112 offset:3744
v_add_f32 v123,v123,v250
v_fmac_f32 v127,v250,v250
v_add_f32 v123,v123,v251
v_fmac_f32 v127,v251,v251
ds_read_b128 v[100:103],v116 offset:320
v_add_f32 v120,v120,v228
v_fmac_f32 v124,v228,v228
v_add_f32 v120,v120,v229
v_fmac_f32 v124,v229,v229
v_add_f32 v121,v121,v230
v_fmac_f32 v125,v230,v230
v_add_f32 v121,v121,v231
v_fmac_f32 v125,v231,v231
s_waitcnt lgkmcnt(0)
s_barrier
s_setprio 0
v_add_f32 v122,v122,v252
v_fmac_f32 v126,v252,v252
v_add_f32 v122,v122,v253
v_fmac_f32 v126,v253,v253
v_add_f32 v123,v123,v254
v_fmac_f32 v127,v254,v254
v_add_f32 v123,v123,v255
v_fmac_f32 v127,v255,v255
s_setprio 3
ds_read_b128 v[56:59],v14 offset:256
ds_read_b128 v[60:63],v7 offset:3616
s_setprio 0
v_permlane32_swap_b32 v120,v122
v_permlane32_swap_b32 v121,v123
v_permlane32_swap_b32 v124,v126
v_permlane32_swap_b32 v125,v127
v_add_f32 v120,v120,v122
v_add_f32 v121,v121,v123
v_add_f32 v124,v124,v126
v_add_f32 v125,v125,v127
v_permlane16_swap_b32 v120,v121
s_nop 0
v_permlane16_swap_b32 v124,v125
v_add_f32 v113,v120,v121
v_add_f32 v114,v124,v125
v_mul_f32 v113,0x3c2aaaab,v113
v_mul_f32 v114,0x3c2aaaab,v114
v_mul_f32 v115,0x5e000000,v114
v_fma_f32 v114,-v113,v113,v114
v_pk_maximum3_f16 v1,v1,v115,v115 op_sel:[0,1,1] op_sel_hi:[1,1,1]
v_max_f32 v114,0,v114
v_add_f32 v114,0x3727c5ac,v114
v_rsq_f32 v114,v114
s_nop 0
v_mul_f32 v120,0x8befe000,v114
v_mul_f32 v121,0x8befe000,v114
v_mul_f32 v124,-v113,v120
v_mul_f32 v125,-v113,v120
v_permlane16_swap_b32 v120,v121
s_nop 0
v_permlane16_swap_b32 v124,v125
v_mov_b32 v122,v120
v_mov_b32 v123,v121
v_mov_b32 v126,v124
v_mov_b32 v127,v125
v_permlane32_swap_b32 v120,v122
v_permlane32_swap_b32 v124,v126
v_permlane32_swap_b32 v121,v123
v_permlane32_swap_b32 v125,v127
v_add_u32 v8,94432,v4
v_add_u32 v9,94432,v5
v_fma_f32 v160,v160,v120,v124
s_setprio 3
ds_read_b64_tr_b16 v[64:65],v4 offset:512
s_setprio 0
v_fma_f32 v161,v161,v120,v124
s_setprio 3
ds_read_b64_tr_b16 v[66:67],v4 offset:2560
s_setprio 0
v_fma_f32 v162,v162,v121,v125
s_setprio 3
ds_read_b128 v[112:115],v7 offset:384
s_setprio 0
v_fma_f32 v163,v163,v121,v125
s_setprio 3
ds_read_b64_tr_b16 v[68:69],v5 offset:512
s_setprio 0
v_fma_f32 v164,v164,v120,v124
s_setprio 3
ds_read_b64_tr_b16 v[70:71],v5 offset:2560
s_setprio 0
v_fma_f32 v165,v165,v120,v124
s_setprio 3
ds_read_b128 v[116:119],v7 offset:416
s_setprio 0
v_fma_f32 v166,v166,v121,v125
s_setprio 3
ds_read_b64_tr_b16 v[72:73],v4 offset:4608
s_setprio 0
v_fma_f32 v167,v167,v121,v125
s_setprio 3
ds_read_b64_tr_b16 v[74:75],v4 offset:6656
s_setprio 0
v_fma_f32 v208,v208,v120,v124
s_setprio 3
ds_read_b64_tr_b16 v[76:77],v5 offset:4608
s_setprio 0
v_fma_f32 v209,v209,v120,v124
s_setprio 3
ds_read_b64_tr_b16 v[78:79],v5 offset:6656
s_setprio 0
v_fma_f32 v210,v210,v121,v125
s_setprio 3
ds_read_b64_tr_b16 v[80:81],v4 offset:8704
s_setprio 0
v_fma_f32 v211,v211,v121,v125
s_setprio 3
ds_read_b64_tr_b16 v[82:83],v4 offset:10752
s_setprio 0
v_fma_f32 v212,v212,v120,v124
s_setprio 3
ds_read_b64_tr_b16 v[84:85],v5 offset:8704
s_setprio 0
v_fma_f32 v213,v213,v120,v124
s_setprio 3
ds_read_b64_tr_b16 v[86:87],v5 offset:10752
s_setprio 0
v_fma_f32 v214,v214,v121,v125
v_fma_f32 v215,v215,v121,v125
v_cvt_pk_bf16_f32 v16,v160,v161
v_cvt_pk_bf16_f32 v20,v162,v163
v_cvt_pk_bf16_f32 v17,v164,v165
v_cvt_pk_bf16_f32 v21,v166,v167
v_cvt_pk_bf16_f32 v18,v208,v209
v_cvt_pk_bf16_f32 v22,v210,v211
v_cvt_pk_bf16_f32 v19,v212,v213
v_cvt_pk_bf16_f32 v23,v214,v215
v_fma_f32 v168,v168,v120,v124
v_fma_f32 v169,v169,v120,v124
v_mfma_f32_16x16x32_bf16 v[160:163],v[104:107],v[128:131],v[108:111]
v_fma_f32 v170,v170,v121,v125
v_fma_f32 v171,v171,v121,v125
v_fma_f32 v172,v172,v120,v124
v_fma_f32 v173,v173,v120,v124
v_fma_f32 v174,v174,v121,v125
v_fma_f32 v175,v175,v121,v125
v_mfma_f32_16x16x32_bf16 v[164:167],v[104:107],v[132:135],v[108:111]
v_fma_f32 v216,v216,v120,v124
v_fma_f32 v217,v217,v120,v124
v_fma_f32 v218,v218,v121,v125
v_fma_f32 v219,v219,v121,v125
v_fma_f32 v220,v220,v120,v124
v_fma_f32 v221,v221,v120,v124
v_mfma_f32_16x16x32_bf16 v[208:211],v[104:107],v[136:139],v[108:111]
v_fma_f32 v222,v222,v121,v125
v_fma_f32 v223,v223,v121,v125
v_cvt_pk_bf16_f32 v24,v168,v169
v_cvt_pk_bf16_f32 v28,v170,v171
v_cvt_pk_bf16_f32 v25,v172,v173
v_cvt_pk_bf16_f32 v29,v174,v175
s_setprio 3
v_mfma_f32_16x16x32_bf16 v[212:215],v[104:107],v[140:143],v[108:111]
ds_read_b128 v[104:107],v14 offset:512
s_setprio 0
v_cvt_pk_bf16_f32 v26,v216,v217
v_cvt_pk_bf16_f32 v30,v218,v219
v_cvt_pk_bf16_f32 v27,v220,v221
v_cvt_pk_bf16_f32 v31,v222,v223
v_fma_f32 v176,v176,v120,v124
v_fma_f32 v177,v177,v120,v124
s_waitcnt lgkmcnt(15)
s_setprio 3
v_mfma_f32_16x16x32_bf16 v[168:171],v[56:59],v[128:131],v[60:63]
ds_read_b128 v[108:111],v7 offset:3712
s_setprio 0
v_fma_f32 v178,v178,v121,v125
v_fma_f32 v179,v179,v121,v125
v_fma_f32 v180,v180,v120,v124
v_fma_f32 v181,v181,v120,v124
v_fma_f32 v182,v182,v121,v125
v_fma_f32 v183,v183,v121,v125
v_mfma_f32_16x16x32_bf16 v[172:175],v[56:59],v[132:135],v[60:63]
v_fma_f32 v224,v224,v120,v124
v_fma_f32 v225,v225,v120,v124
v_fma_f32 v226,v226,v121,v125
v_fma_f32 v227,v227,v121,v125
v_fma_f32 v228,v228,v120,v124
v_fma_f32 v229,v229,v120,v124
v_mfma_f32_16x16x32_bf16 v[216:219],v[56:59],v[136:139],v[60:63]
v_fma_f32 v230,v230,v121,v125
v_fma_f32 v231,v231,v121,v125
v_cvt_pk_bf16_f32 v32,v176,v177
v_cvt_pk_bf16_f32 v36,v178,v179
v_cvt_pk_bf16_f32 v33,v180,v181
v_cvt_pk_bf16_f32 v37,v182,v183
s_setprio 3
v_mfma_f32_16x16x32_bf16 v[220:223],v[56:59],v[140:143],v[60:63]
ds_read_b128 v[56:59],v14 offset:768
s_setprio 0
v_cvt_pk_bf16_f32 v34,v224,v225
v_cvt_pk_bf16_f32 v38,v226,v227
v_cvt_pk_bf16_f32 v35,v228,v229
v_cvt_pk_bf16_f32 v39,v230,v231
v_fma_f32 v184,v184,v122,v126
s_waitcnt lgkmcnt(1)
s_setprio 3
v_mfma_f32_16x16x32_bf16 v[176:179],v[104:107],v[144:147],v[108:111]
ds_read_b128 v[60:63],v7 offset:3744
s_setprio 0
v_fma_f32 v185,v185,v122,v126
v_fma_f32 v186,v186,v123,v127
v_fma_f32 v187,v187,v123,v127
v_mfma_f32_16x16x32_bf16 v[128:131],v[64:67],v[16:19],v[112:115]
v_fma_f32 v188,v188,v122,v126
v_fma_f32 v189,v189,v122,v126
v_fma_f32 v190,v190,v123,v127
v_mfma_f32_16x16x32_bf16 v[180:183],v[104:107],v[148:151],v[108:111]
v_fma_f32 v191,v191,v123,v127
v_fma_f32 v232,v232,v122,v126
v_fma_f32 v233,v233,v122,v126
v_mfma_f32_16x16x32_bf16 v[136:139],v[64:67],v[20:23],v[112:115]
v_fma_f32 v234,v234,v123,v127
v_fma_f32 v235,v235,v123,v127
v_fma_f32 v236,v236,v122,v126
v_mfma_f32_16x16x32_bf16 v[224:227],v[104:107],v[152:155],v[108:111]
v_fma_f32 v237,v237,v122,v126
v_fma_f32 v238,v238,v123,v127
v_fma_f32 v239,v239,v123,v127
v_mfma_f32_16x16x32_bf16 v[140:143],v[68:71],v[20:23],v[116:119]
v_cvt_pk_bf16_f32 v40,v184,v185
v_cvt_pk_bf16_f32 v44,v186,v187
v_cvt_pk_bf16_f32 v41,v188,v189
s_setprio 3
v_mfma_f32_16x16x32_bf16 v[228:231],v[104:107],v[156:159],v[108:111]
ds_read_b128 v[104:107],v14 offset:1024
s_setprio 0
v_cvt_pk_bf16_f32 v45,v190,v191
v_cvt_pk_bf16_f32 v42,v232,v233
v_cvt_pk_bf16_f32 v46,v234,v235
s_setprio 3
v_mfma_f32_16x16x32_bf16 v[132:135],v[68:71],v[16:19],v[116:119]
ds_read_b128 v[108:111],v7 offset:3840
s_setprio 0
v_cvt_pk_bf16_f32 v43,v236,v237
v_cvt_pk_bf16_f32 v47,v238,v239
v_fma_f32 v192,v192,v122,v126
s_waitcnt lgkmcnt(2)
v_mfma_f32_16x16x32_bf16 v[184:187],v[56:59],v[144:147],v[60:63]
v_fma_f32 v193,v193,v122,v126
v_fma_f32 v194,v194,v123,v127
v_mfma_f32_16x16x32_bf16 v[128:131],v[72:75],v[24:27],v[128:131]
v_fma_f32 v195,v195,v123,v127
v_fma_f32 v196,v196,v122,v126
v_mfma_f32_16x16x32_bf16 v[188:191],v[56:59],v[148:151],v[60:63]
v_fma_f32 v197,v197,v122,v126
v_fma_f32 v198,v198,v123,v127
v_mfma_f32_16x16x32_bf16 v[136:139],v[72:75],v[28:31],v[136:139]
v_fma_f32 v199,v199,v123,v127
v_fma_f32 v240,v240,v122,v126
v_mfma_f32_16x16x32_bf16 v[232:235],v[56:59],v[152:155],v[60:63]
v_fma_f32 v241,v241,v122,v126
v_fma_f32 v242,v242,v123,v127
v_mfma_f32_16x16x32_bf16 v[140:143],v[76:79],v[28:31],v[140:143]
v_fma_f32 v243,v243,v123,v127
v_fma_f32 v244,v244,v122,v126
s_setprio 3
v_mfma_f32_16x16x32_bf16 v[236:239],v[56:59],v[156:159],v[60:63]
ds_read_b128 v[144:147],v14 offset:1280
s_setprio 0
v_fma_f32 v245,v245,v122,v126
v_fma_f32 v246,v246,v123,v127
s_setprio 3
v_mfma_f32_16x16x32_bf16 v[132:135],v[76:79],v[24:27],v[132:135]
ds_read_b128 v[148:151],v7 offset:3872
s_setprio 0
v_fma_f32 v247,v247,v123,v127
v_cvt_pk_bf16_f32 v48,v192,v193
v_cvt_pk_bf16_f32 v52,v194,v195
v_cvt_pk_bf16_f32 v49,v196,v197
v_cvt_pk_bf16_f32 v53,v198,v199
v_cvt_pk_bf16_f32 v50,v240,v241
v_cvt_pk_bf16_f32 v54,v242,v243
v_cvt_pk_bf16_f32 v51,v244,v245
v_cvt_pk_bf16_f32 v55,v246,v247
v_fma_f32 v200,v200,v122,v126
s_waitcnt lgkmcnt(2)
v_mfma_f32_16x16x32_bf16 v[192:195],v[104:107],v[88:91],v[108:111]
v_fma_f32 v201,v201,v122,v126
v_fma_f32 v202,v202,v123,v127
v_fma_f32 v203,v203,v123,v127
v_mfma_f32_16x16x32_bf16 v[128:131],v[80:83],v[32:35],v[128:131]
v_fma_f32 v204,v204,v122,v126
v_fma_f32 v205,v205,v122,v126
v_fma_f32 v206,v206,v123,v127
s_nop 0
s_mov_b32 s86,0xbe801d5a
v_mfma_f32_16x16x32_bf16 v[196:199],v[104:107],v[92:95],v[108:111]
v_fma_f32 v207,v207,v123,v127
v_fma_f32 v248,v248,v122,v126
v_fma_f32 v249,v249,v122,v126
v_mfma_f32_16x16x32_bf16 v[136:139],v[80:83],v[36:39],v[136:139]
v_fma_f32 v250,v250,v123,v127
v_fma_f32 v251,v251,v123,v127
v_fma_f32 v252,v252,v122,v126
v_mfma_f32_16x16x32_bf16 v[240:243],v[104:107],v[96:99],v[108:111]
v_fma_f32 v253,v253,v122,v126
v_fma_f32 v254,v254,v123,v127
v_fma_f32 v255,v255,v123,v127
v_mfma_f32_16x16x32_bf16 v[140:143],v[84:87],v[36:39],v[140:143]
v_cvt_pk_bf16_f32 v56,v200,v201
v_cvt_pk_bf16_f32 v60,v202,v203
v_cvt_pk_bf16_f32 v57,v204,v205
v_mfma_f32_16x16x32_bf16 v[244:247],v[104:107],v[100:103],v[108:111]
v_cvt_pk_bf16_f32 v61,v206,v207
v_cvt_pk_bf16_f32 v58,v248,v249
v_cvt_pk_bf16_f32 v62,v250,v251
v_mfma_f32_16x16x32_bf16 v[132:135],v[84:87],v[32:35],v[132:135]
v_cvt_pk_bf16_f32 v59,v252,v253
v_cvt_pk_bf16_f32 v63,v254,v255
s_waitcnt lgkmcnt(0)
v_mfma_f32_16x16x32_bf16 v[200:203],v[144:147],v[88:91],v[148:151]
s_barrier
s_setprio 3
ds_read_b64_tr_b16 v[104:105],v6 offset:12448
v_lshlrev_b32_sdwa v128,1,v128 dst_sel:DWORD dst_unused:UNUSED_PAD src0_sel:DWORD src1_sel:WORD_1
v_lshlrev_b32_sdwa v130,1,v130 dst_sel:DWORD dst_unused:UNUSED_PAD src0_sel:DWORD src1_sel:WORD_1
v_lshlrev_b32_sdwa v129,1,v129 dst_sel:DWORD dst_unused:UNUSED_PAD src0_sel:DWORD src1_sel:WORD_1
v_mfma_f32_16x16x32_bf16 v[204:207],v[144:147],v[92:95],v[148:151]
ds_read_u16 v128,v128
ds_read_b64_tr_b16 v[106:107],v6 offset:13984
v_lshlrev_b32_sdwa v131,1,v131 dst_sel:DWORD dst_unused:UNUSED_PAD src0_sel:DWORD src1_sel:WORD_1
v_fma_mixlo_f16 v120,v140,s99,0
v_fma_mixlo_f16 v122,v141,s99,0
v_mfma_f32_16x16x32_bf16 v[248:251],v[144:147],v[96:99],v[148:151]
ds_read_u16_d16_hi v130,v130
ds_read_b64_tr_b16 v[108:109],v6 offset:12456
v_lshlrev_b32_sdwa v132,1,v132 dst_sel:DWORD dst_unused:UNUSED_PAD src0_sel:DWORD src1_sel:WORD_1
v_lshlrev_b32_sdwa v134,1,v134 dst_sel:DWORD dst_unused:UNUSED_PAD src0_sel:DWORD src1_sel:WORD_1
v_mfma_f32_16x16x32_bf16 v[252:255],v[144:147],v[100:103],v[148:151]
ds_read_u16 v129,v129
ds_read_b64_tr_b16 v[110:111],v6 offset:13992
v_fma_mixhi_f16 v120,v142,s99,0
v_fma_mixhi_f16 v122,v143,s99,0
v_lshlrev_b32_sdwa v133,1,v133 dst_sel:DWORD dst_unused:UNUSED_PAD src0_sel:DWORD src1_sel:WORD_1
v_pk_fma_f16 v121,v120,v120,s54
v_mfma_f32_16x16x32_bf16 v[144:147],v[64:67],v[40:43],v[112:115]
ds_read_u16_d16_hi v131,v131
ds_read_b64_tr_b16 v[88:89],v6 offset:12320
v_lshlrev_b32_sdwa v135,1,v135 dst_sel:DWORD dst_unused:UNUSED_PAD src0_sel:DWORD src1_sel:WORD_1
v_pk_fma_f16 v123,v122,v122,s54
v_pk_mul_f16 v121,v120,v121
v_mfma_f32_16x16x32_bf16 v[152:155],v[64:67],v[44:47],v[112:115]
ds_read_u16 v132,v132
ds_read_b64_tr_b16 v[90:91],v6 offset:13856
v_lshlrev_b32_sdwa v136,1,v136 dst_sel:DWORD dst_unused:UNUSED_PAD src0_sel:DWORD src1_sel:WORD_1
v_pk_mul_f16 v123,v122,v123
v_mfma_f32_16x16x32_bf16 v[156:159],v[68:71],v[44:47],v[116:119]
ds_read_u16_d16_hi v134,v134
ds_read_b64_tr_b16 v[92:93],v6 offset:12328
v_lshlrev_b32_sdwa v138,1,v138 dst_sel:DWORD dst_unused:UNUSED_PAD src0_sel:DWORD src1_sel:WORD_1
v_exp_f16 v120,v121
v_mfma_f32_16x16x32_bf16 v[148:151],v[68:71],v[40:43],v[116:119]
ds_read_u16 v133,v133
ds_read_b64_tr_b16 v[94:95],v6 offset:13864
v_lshlrev_b32_sdwa v137,1,v137 dst_sel:DWORD dst_unused:UNUSED_PAD src0_sel:DWORD src1_sel:WORD_1
v_lshlrev_b32_sdwa v139,1,v139 dst_sel:DWORD dst_unused:UNUSED_PAD src0_sel:DWORD src1_sel:WORD_1
v_exp_f16 v122,v123
v_mfma_f32_16x16x32_bf16 v[144:147],v[72:75],v[48:51],v[144:147]
ds_read_u16_d16_hi v135,v135
ds_read_b64_tr_b16 v[96:97],v6 offset:12384
v_exp_f16_sdwa v122,v123 dst_sel:WORD_1 dst_unused:UNUSED_PRESERVE src0_sel:WORD_1
v_mfma_f32_16x16x32_bf16 v[152:155],v[72:75],v[52:55],v[152:155]
ds_read_u16 v136,v136
ds_read_b64_tr_b16 v[98:99],v6 offset:13920
v_exp_f16_sdwa v120,v121 dst_sel:WORD_1 dst_unused:UNUSED_PRESERVE src0_sel:WORD_1
v_mfma_f32_16x16x32_bf16 v[156:159],v[76:79],v[52:55],v[156:159]
ds_read_u16_d16_hi v138,v138
ds_read_b64_tr_b16 v[100:101],v6 offset:12392
v_pk_fma_f16 v120,v120,s55,s55
v_pk_fma_f16 v122,v122,s55,s55
v_rcp_f16 v121,v120
v_mfma_f32_16x16x32_bf16 v[148:151],v[76:79],v[48:51],v[148:151]
ds_read_u16 v137,v137
ds_read_b64_tr_b16 v[102:103],v6 offset:13928
v_rcp_f16 v123,v122
v_mfma_f32_16x16x32_bf16 v[144:147],v[80:83],v[56:59],v[144:147]
ds_read_u16_d16_hi v139,v139
ds_read_b64_tr_b16 v[64:65],v4 offset:12320
v_rcp_f16_sdwa v121,v120 dst_sel:WORD_1 dst_unused:UNUSED_PRESERVE src0_sel:WORD_1
v_mfma_f32_16x16x32_bf16 v[152:155],v[80:83],v[60:63],v[152:155]
ds_read_b64_tr_b16 v[66:67],v4 offset:14368
v_rcp_f16_sdwa v123,v122 dst_sel:WORD_1 dst_unused:UNUSED_PRESERVE src0_sel:WORD_1
v_fma_mix_f32 v142,v142,v121,0 op_sel:[0,1,0] op_sel_hi:[0,1,0]
v_mfma_f32_16x16x32_bf16 v[156:159],v[84:87],v[60:63],v[156:159]
ds_read_b128 v[112:115],v7 offset:512
v_fma_mix_f32 v140,v140,v121,0 op_sel_hi:[0,1,0]
s_waitcnt lgkmcnt(15)
v_lshl_add_u64 v[128:129],v[128:129],0,v[130:131]
s_waitcnt lgkmcnt(11)
v_lshl_add_u64 v[130:131],v[132:133],0,v[134:135]
v_mfma_f32_16x16x32_bf16 v[148:151],v[84:87],v[56:59],v[148:151]
ds_read_b64_tr_b16 v[68:69],v5 offset:12320
v_fma_mix_f32 v141,v141,v123,0 op_sel_hi:[0,1,0]
v_fma_mix_f32 v143,v143,v123,0 op_sel:[0,1,0] op_sel_hi:[0,1,0]
v_lshlrev_b32_sdwa v144,1,v144 dst_sel:DWORD dst_unused:UNUSED_PAD src0_sel:DWORD src1_sel:WORD_1
v_lshlrev_b32_sdwa v146,1,v146 dst_sel:DWORD dst_unused:UNUSED_PAD src0_sel:DWORD src1_sel:WORD_1
v_mfma_f32_16x16x32_bf16 v[160:163],v[88:91],v[128:131],v[160:163]
ds_read_u16 v144,v144
v_lshlrev_b32_sdwa v145,1,v145 dst_sel:DWORD dst_unused:UNUSED_PAD src0_sel:DWORD src1_sel:WORD_1
v_lshlrev_b32_sdwa v147,1,v147 dst_sel:DWORD dst_unused:UNUSED_PAD src0_sel:DWORD src1_sel:WORD_1
v_fma_mixlo_f16 v122,v157,s99,0
v_mfma_f32_16x16x32_bf16 v[168:171],v[92:95],v[128:131],v[168:171]
s_waitcnt lgkmcnt(10)
ds_read_b64_tr_b16 v[70:71],v5 offset:14368
ds_read_b128 v[116:119],v7 offset:544
v_fma_mixlo_f16 v120,v156,s99,0
v_mfma_f32_16x16x32_bf16 v[176:179],v[96:99],v[128:131],v[176:179]
s_waitcnt lgkmcnt(8)
ds_read_u16_d16_hi v146,v146
v_lshlrev_b32_sdwa v148,1,v148 dst_sel:DWORD dst_unused:UNUSED_PAD src0_sel:DWORD src1_sel:WORD_1
v_lshlrev_b32_sdwa v150,1,v150 dst_sel:DWORD dst_unused:UNUSED_PAD src0_sel:DWORD src1_sel:WORD_1
v_mfma_f32_16x16x32_bf16 v[184:187],v[100:103],v[128:131],v[184:187]
s_waitcnt lgkmcnt(8)
ds_read_b64_tr_b16 v[72:73],v4 offset:16416
ds_read_b64_tr_b16 v[74:75],v4 offset:18464
v_lshl_add_u64 v[136:137],v[136:137],0,v[138:139]
v_fma_mixhi_f16 v120,v158,s99,0
v_mfma_f32_16x16x32_bf16 v[192:195],v[104:107],v[128:131],v[192:195]
ds_read_u16 v145,v145
v_cvt_pk_bf16_f32 v138,v140,v142
v_cvt_pk_bf16_f32 v139,v141,v143
v_mfma_f32_16x16x32_bf16 v[200:203],v[108:111],v[128:131],v[200:203]
v_fma_mixhi_f16 v122,v159,s99,0
v_mfma_f32_16x16x32_bf16 v[164:167],v[88:91],v[136:139],v[164:167]
ds_read_b64_tr_b16 v[76:77],v5 offset:16416
ds_read_b64_tr_b16 v[78:79],v5 offset:18464
v_lshlrev_b32_sdwa v149,1,v149 dst_sel:DWORD dst_unused:UNUSED_PAD src0_sel:DWORD src1_sel:WORD_1
v_lshlrev_b32_sdwa v151,1,v151 dst_sel:DWORD dst_unused:UNUSED_PAD src0_sel:DWORD src1_sel:WORD_1
v_mfma_f32_16x16x32_bf16 v[172:175],v[92:95],v[136:139],v[172:175]
ds_read_b64_tr_b16 v[80:81],v4 offset:20512
ds_read_b64_tr_b16 v[82:83],v4 offset:22560
v_pk_fma_f16 v121,v120,v120,s54
v_pk_fma_f16 v123,v122,v122,s54
v_mfma_f32_16x16x32_bf16 v[180:183],v[96:99],v[136:139],v[180:183]
ds_read_u16_d16_hi v147,v147
v_pk_mul_f16 v121,v120,v121
v_pk_mul_f16 v123,v122,v123
v_mfma_f32_16x16x32_bf16 v[188:191],v[100:103],v[136:139],v[188:191]
ds_read_u16 v148,v148
v_lshlrev_b32_sdwa v152,1,v152 dst_sel:DWORD dst_unused:UNUSED_PAD src0_sel:DWORD src1_sel:WORD_1
v_lshlrev_b32_sdwa v154,1,v154 dst_sel:DWORD dst_unused:UNUSED_PAD src0_sel:DWORD src1_sel:WORD_1
v_mfma_f32_16x16x32_bf16 v[196:199],v[104:107],v[136:139],v[196:199]
v_exp_f16 v120,v121
v_mfma_f32_16x16x32_bf16 v[204:207],v[108:111],v[136:139],v[204:207]
s_waitcnt lgkmcnt(14)
ds_read_b64_tr_b16 v[84:85],v5 offset:20512
ds_read_b64_tr_b16 v[86:87],v5 offset:22560
v_exp_f16 v122,v123
v_mfma_f32_16x16x32_bf16 v[128:131],v[64:67],v[16:19],v[112:115]
v_lshlrev_b32_sdwa v153,1,v153 dst_sel:DWORD dst_unused:UNUSED_PAD src0_sel:DWORD src1_sel:WORD_1
v_lshlrev_b32_sdwa v155,1,v155 dst_sel:DWORD dst_unused:UNUSED_PAD src0_sel:DWORD src1_sel:WORD_1
v_mfma_f32_16x16x32_bf16 v[136:139],v[64:67],v[20:23],v[112:115]
s_waitcnt lgkmcnt(12)
ds_read_u16_d16_hi v150,v150
ds_read_u16 v149,v149
v_exp_f16_sdwa v120,v121 dst_sel:WORD_1 dst_unused:UNUSED_PRESERVE src0_sel:WORD_1
v_mfma_f32_16x16x32_bf16 v[140:143],v[68:71],v[20:23],v[116:119]
v_exp_f16_sdwa v122,v123 dst_sel:WORD_1 dst_unused:UNUSED_PRESERVE src0_sel:WORD_1
v_mfma_f32_16x16x32_bf16 v[132:135],v[68:71],v[16:19],v[116:119]
s_waitcnt lgkmcnt(11)
v_pk_fma_f16 v120,v120,s55,s55
v_mfma_f32_16x16x32_bf16 v[128:131],v[72:75],v[24:27],v[128:131]
v_pk_fma_f16 v122,v122,s55,s55
v_rcp_f16 v121,v120
v_mfma_f32_16x16x32_bf16 v[136:139],v[72:75],v[28:31],v[136:139]
s_waitcnt lgkmcnt(8)
ds_read_u16_d16_hi v151,v151
v_rcp_f16 v123,v122
v_mfma_f32_16x16x32_bf16 v[140:143],v[76:79],v[28:31],v[140:143]
ds_read_u16 v152,v152
ds_read_u16_d16_hi v154,v154
v_rcp_f16_sdwa v121,v120 dst_sel:WORD_1 dst_unused:UNUSED_PRESERVE src0_sel:WORD_1
v_mfma_f32_16x16x32_bf16 v[132:135],v[76:79],v[24:27],v[132:135]
s_waitcnt lgkmcnt(9)
v_rcp_f16_sdwa v123,v122 dst_sel:WORD_1 dst_unused:UNUSED_PRESERVE src0_sel:WORD_1
v_mfma_f32_16x16x32_bf16 v[128:131],v[80:83],v[32:35],v[128:131]
ds_read_u16 v153,v153
v_fma_mix_f32 v156,v156,v121,0 op_sel_hi:[0,1,0]
v_fma_mix_f32 v158,v158,v121,0 op_sel:[0,1,0] op_sel_hi:[0,1,0]
v_mfma_f32_16x16x32_bf16 v[136:139],v[80:83],v[36:39],v[136:139]
s_waitcnt lgkmcnt(3)
v_lshl_add_u64 v[144:145],v[144:145],0,v[146:147]
v_lshl_add_u64 v[146:147],v[148:149],0,v[150:151]
v_mfma_f32_16x16x32_bf16 v[140:143],v[84:87],v[36:39],v[140:143]
ds_read_u16_d16_hi v155,v155
v_fma_mix_f32 v157,v157,v123,0 op_sel_hi:[0,1,0]
v_fma_mix_f32 v159,v159,v123,0 op_sel:[0,1,0] op_sel_hi:[0,1,0]
v_mfma_f32_16x16x32_bf16 v[132:135],v[84:87],v[32:35],v[132:135]
v_lshlrev_b32_sdwa v128,1,v128 dst_sel:DWORD dst_unused:UNUSED_PAD src0_sel:DWORD src1_sel:WORD_1
v_lshlrev_b32_sdwa v130,1,v130 dst_sel:DWORD dst_unused:UNUSED_PAD src0_sel:DWORD src1_sel:WORD_1
v_mfma_f32_16x16x32_bf16 v[208:211],v[88:91],v[144:147],v[208:211]
v_lshlrev_b32_sdwa v129,1,v129 dst_sel:DWORD dst_unused:UNUSED_PAD src0_sel:DWORD src1_sel:WORD_1
v_lshlrev_b32_sdwa v131,1,v131 dst_sel:DWORD dst_unused:UNUSED_PAD src0_sel:DWORD src1_sel:WORD_1
v_mfma_f32_16x16x32_bf16 v[216:219],v[92:95],v[144:147],v[216:219]
ds_read_u16 v128,v128
ds_read_u16_d16_hi v130,v130
v_fma_mixlo_f16 v120,v140,s99,0
v_fma_mixlo_f16 v122,v141,s99,0
v_mfma_f32_16x16x32_bf16 v[224:227],v[96:99],v[144:147],v[224:227]
ds_read_u16 v129,v129
v_lshlrev_b32_sdwa v132,1,v132 dst_sel:DWORD dst_unused:UNUSED_PAD src0_sel:DWORD src1_sel:WORD_1
v_lshlrev_b32_sdwa v134,1,v134 dst_sel:DWORD dst_unused:UNUSED_PAD src0_sel:DWORD src1_sel:WORD_1
v_mfma_f32_16x16x32_bf16 v[232:235],v[100:103],v[144:147],v[232:235]
s_waitcnt lgkmcnt(3)
v_lshl_add_u64 v[152:153],v[152:153],0,v[154:155]
v_cvt_pk_bf16_f32 v154,v156,v158
v_mfma_f32_16x16x32_bf16 v[240:243],v[104:107],v[144:147],v[240:243]
v_fma_mixhi_f16 v120,v142,s99,0
v_cvt_pk_bf16_f32 v155,v157,v159
v_mfma_f32_16x16x32_bf16 v[248:251],v[108:111],v[144:147],v[248:251]
ds_read_u16_d16_hi v131,v131
v_fma_mixhi_f16 v122,v143,s99,0
v_mfma_f32_16x16x32_bf16 v[212:215],v[88:91],v[152:155],v[212:215]
v_lshlrev_b32_sdwa v133,1,v133 dst_sel:DWORD dst_unused:UNUSED_PAD src0_sel:DWORD src1_sel:WORD_1
v_pk_fma_f16 v121,v120,v120,s54
v_lshlrev_b32_sdwa v135,1,v135 dst_sel:DWORD dst_unused:UNUSED_PAD src0_sel:DWORD src1_sel:WORD_1
v_mfma_f32_16x16x32_bf16 v[220:223],v[92:95],v[152:155],v[220:223]
s_setprio 2
ds_read_u16 v132,v132
ds_read_u16_d16_hi v134,v134
v_pk_fma_f16 v123,v122,v122,s54
v_mfma_f32_16x16x32_bf16 v[228:231],v[96:99],v[152:155],v[228:231]
ds_read_b64_tr_b16 v[88:89],v6 offset:18480
v_pk_mul_f16 v121,v120,v121
v_lshlrev_b32_sdwa v136,1,v136 dst_sel:DWORD dst_unused:UNUSED_PAD src0_sel:DWORD src1_sel:WORD_1
v_pk_mul_f16 v123,v122,v123
v_mfma_f32_16x16x32_bf16 v[236:239],v[100:103],v[152:155],v[236:239]
ds_read_b64_tr_b16 v[90:91],v6 offset:20016
v_lshlrev_b32_sdwa v138,1,v138 dst_sel:DWORD dst_unused:UNUSED_PAD src0_sel:DWORD src1_sel:WORD_1
v_mfma_f32_16x16x32_bf16 v[244:247],v[104:107],v[152:155],v[244:247]
ds_read_u16 v133,v133
v_exp_f16 v120,v121
v_mfma_f32_16x16x32_bf16 v[252:255],v[108:111],v[152:155],v[252:255]
ds_read_b64_tr_b16 v[92:93],v6 offset:18488
v_lshlrev_b32_sdwa v137,1,v137 dst_sel:DWORD dst_unused:UNUSED_PAD src0_sel:DWORD src1_sel:WORD_1
v_mfma_f32_16x16x32_bf16 v[144:147],v[64:67],v[40:43],v[112:115]
ds_read_b64_tr_b16 v[94:95],v6 offset:20024
v_lshlrev_b32_sdwa v139,1,v139 dst_sel:DWORD dst_unused:UNUSED_PAD src0_sel:DWORD src1_sel:WORD_1
v_exp_f16 v122,v123
v_mfma_f32_16x16x32_bf16 v[152:155],v[64:67],v[44:47],v[112:115]
ds_read_u16_d16_hi v135,v135
ds_read_b64_tr_b16 v[96:97],v6 offset:18544
v_mfma_f32_16x16x32_bf16 v[156:159],v[68:71],v[44:47],v[116:119]
ds_read_u16 v136,v136
v_exp_f16_sdwa v122,v123 dst_sel:WORD_1 dst_unused:UNUSED_PRESERVE src0_sel:WORD_1
v_exp_f16_sdwa v120,v121 dst_sel:WORD_1 dst_unused:UNUSED_PRESERVE src0_sel:WORD_1
v_mfma_f32_16x16x32_bf16 v[148:151],v[68:71],v[40:43],v[116:119]
ds_read_b64_tr_b16 v[98:99],v6 offset:20080
v_pk_fma_f16 v120,v120,s55,s55
v_pk_fma_f16 v122,v122,s55,s55
v_mfma_f32_16x16x32_bf16 v[144:147],v[72:75],v[48:51],v[144:147]
v_rcp_f16 v121,v120
v_mfma_f32_16x16x32_bf16 v[152:155],v[72:75],v[52:55],v[152:155]
ds_read_b64_tr_b16 v[100:101],v6 offset:18552
v_rcp_f16 v123,v122
v_mfma_f32_16x16x32_bf16 v[156:159],v[76:79],v[52:55],v[156:159]
ds_read_u16_d16_hi v138,v138
v_rcp_f16_sdwa v121,v120 dst_sel:WORD_1 dst_unused:UNUSED_PRESERVE src0_sel:WORD_1
v_mfma_f32_16x16x32_bf16 v[148:151],v[76:79],v[48:51],v[148:151]
ds_read_b64_tr_b16 v[102:103],v6 offset:20088
ds_read_b64_tr_b16 v[104:105],v6 offset:18608
v_rcp_f16_sdwa v123,v122 dst_sel:WORD_1 dst_unused:UNUSED_PRESERVE src0_sel:WORD_1
v_mfma_f32_16x16x32_bf16 v[144:147],v[80:83],v[56:59],v[144:147]
ds_read_u16 v137,v137
v_fma_mix_f32 v142,v142,v121,0 op_sel:[0,1,0] op_sel_hi:[0,1,0]
v_mfma_f32_16x16x32_bf16 v[152:155],v[80:83],v[60:63],v[152:155]
s_waitcnt lgkmcnt(8)
ds_read_b64_tr_b16 v[106:107],v6 offset:20144
ds_read_b64_tr_b16 v[108:109],v6 offset:18616
v_fma_mix_f32 v140,v140,v121,0 op_sel_hi:[0,1,0]
v_lshl_add_u64 v[128:129],v[128:129],0,v[130:131]
v_lshl_add_u64 v[130:131],v[132:133],0,v[134:135]
v_mfma_f32_16x16x32_bf16 v[156:159],v[84:87],v[60:63],v[156:159]
ds_read_u16_d16_hi v139,v139
v_fma_mix_f32 v141,v141,v123,0 op_sel_hi:[0,1,0]
v_fma_mix_f32 v143,v143,v123,0 op_sel:[0,1,0] op_sel_hi:[0,1,0]
v_mfma_f32_16x16x32_bf16 v[148:151],v[84:87],v[56:59],v[148:151]
ds_read_b64_tr_b16 v[110:111],v6 offset:20152
ds_read_b64_tr_b16 v[64:65],v4 offset:12832
v_lshlrev_b32_sdwa v144,1,v144 dst_sel:DWORD dst_unused:UNUSED_PAD src0_sel:DWORD src1_sel:WORD_1
v_lshlrev_b32_sdwa v146,1,v146 dst_sel:DWORD dst_unused:UNUSED_PAD src0_sel:DWORD src1_sel:WORD_1
v_mfma_f32_16x16x32_bf16 v[160:163],v[88:91],v[128:131],v[160:163]
ds_read_u16 v144,v144
v_lshlrev_b32_sdwa v145,1,v145 dst_sel:DWORD dst_unused:UNUSED_PAD src0_sel:DWORD src1_sel:WORD_1
v_lshlrev_b32_sdwa v147,1,v147 dst_sel:DWORD dst_unused:UNUSED_PAD src0_sel:DWORD src1_sel:WORD_1
v_fma_mixlo_f16 v122,v157,s99,0
v_mfma_f32_16x16x32_bf16 v[168:171],v[92:95],v[128:131],v[168:171]
s_waitcnt lgkmcnt(11)
ds_read_b64_tr_b16 v[66:67],v4 offset:14880
ds_read_b128 v[112:115],v7 offset:640
v_fma_mixlo_f16 v120,v156,s99,0
v_mfma_f32_16x16x32_bf16 v[176:179],v[96:99],v[128:131],v[176:179]
s_waitcnt lgkmcnt(10)
ds_read_u16_d16_hi v146,v146
v_lshlrev_b32_sdwa v148,1,v148 dst_sel:DWORD dst_unused:UNUSED_PAD src0_sel:DWORD src1_sel:WORD_1
v_lshlrev_b32_sdwa v150,1,v150 dst_sel:DWORD dst_unused:UNUSED_PAD src0_sel:DWORD src1_sel:WORD_1
v_mfma_f32_16x16x32_bf16 v[184:187],v[100:103],v[128:131],v[184:187]
s_waitcnt lgkmcnt(6)
ds_read_b64_tr_b16 v[68:69],v5 offset:12832
ds_read_b64_tr_b16 v[70:71],v5 offset:14880
v_lshl_add_u64 v[136:137],v[136:137],0,v[138:139]
v_fma_mixhi_f16 v120,v158,s99,0
v_mfma_f32_16x16x32_bf16 v[192:195],v[104:107],v[128:131],v[192:195]
s_waitcnt lgkmcnt(7)
ds_read_u16 v145,v145
v_cvt_pk_bf16_f32 v138,v140,v142
v_cvt_pk_bf16_f32 v139,v141,v143
v_mfma_f32_16x16x32_bf16 v[200:203],v[108:111],v[128:131],v[200:203]
v_fma_mixhi_f16 v122,v159,s99,0
v_mfma_f32_16x16x32_bf16 v[164:167],v[88:91],v[136:139],v[164:167]
ds_read_b128 v[116:119],v7 offset:672
ds_read_b64_tr_b16 v[72:73],v4 offset:16928
v_lshlrev_b32_sdwa v149,1,v149 dst_sel:DWORD dst_unused:UNUSED_PAD src0_sel:DWORD src1_sel:WORD_1
v_lshlrev_b32_sdwa v151,1,v151 dst_sel:DWORD dst_unused:UNUSED_PAD src0_sel:DWORD src1_sel:WORD_1
v_mfma_f32_16x16x32_bf16 v[172:175],v[92:95],v[136:139],v[172:175]
ds_read_b64_tr_b16 v[74:75],v4 offset:18976
ds_read_b64_tr_b16 v[76:77],v5 offset:16928
v_pk_fma_f16 v121,v120,v120,s54
v_pk_fma_f16 v123,v122,v122,s54
v_mfma_f32_16x16x32_bf16 v[180:183],v[96:99],v[136:139],v[180:183]
s_setprio 1
ds_read_u16_d16_hi v147,v147
v_pk_mul_f16 v121,v120,v121
v_pk_mul_f16 v123,v122,v123
v_mfma_f32_16x16x32_bf16 v[188:191],v[100:103],v[136:139],v[188:191]
ds_read_u16 v148,v148
v_lshlrev_b32_sdwa v152,1,v152 dst_sel:DWORD dst_unused:UNUSED_PAD src0_sel:DWORD src1_sel:WORD_1
v_lshlrev_b32_sdwa v154,1,v154 dst_sel:DWORD dst_unused:UNUSED_PAD src0_sel:DWORD src1_sel:WORD_1
v_mfma_f32_16x16x32_bf16 v[196:199],v[104:107],v[136:139],v[196:199]
v_exp_f16 v120,v121
v_mfma_f32_16x16x32_bf16 v[204:207],v[108:111],v[136:139],v[204:207]
s_waitcnt lgkmcnt(10)
ds_read_b64_tr_b16 v[78:79],v5 offset:18976
ds_read_b64_tr_b16 v[80:81],v4 offset:21024
v_exp_f16 v122,v123
v_mfma_f32_16x16x32_bf16 v[128:131],v[64:67],v[16:19],v[112:115]
v_lshlrev_b32_sdwa v153,1,v153 dst_sel:DWORD dst_unused:UNUSED_PAD src0_sel:DWORD src1_sel:WORD_1
v_lshlrev_b32_sdwa v155,1,v155 dst_sel:DWORD dst_unused:UNUSED_PAD src0_sel:DWORD src1_sel:WORD_1
v_mfma_f32_16x16x32_bf16 v[136:139],v[64:67],v[20:23],v[112:115]
s_waitcnt lgkmcnt(7)
ds_read_u16_d16_hi v150,v150
ds_read_u16 v149,v149
v_exp_f16_sdwa v120,v121 dst_sel:WORD_1 dst_unused:UNUSED_PRESERVE src0_sel:WORD_1
v_mfma_f32_16x16x32_bf16 v[140:143],v[68:71],v[20:23],v[116:119]
v_exp_f16_sdwa v122,v123 dst_sel:WORD_1 dst_unused:UNUSED_PRESERVE src0_sel:WORD_1
v_mfma_f32_16x16x32_bf16 v[132:135],v[68:71],v[16:19],v[116:119]
s_waitcnt lgkmcnt(7)
ds_read_b64_tr_b16 v[82:83],v4 offset:23072
v_pk_fma_f16 v120,v120,s55,s55
v_mfma_f32_16x16x32_bf16 v[128:131],v[72:75],v[24:27],v[128:131]
ds_read_b64_tr_b16 v[84:85],v5 offset:21024
v_pk_fma_f16 v122,v122,s55,s55
v_rcp_f16 v121,v120
v_mfma_f32_16x16x32_bf16 v[136:139],v[72:75],v[28:31],v[136:139]
s_waitcnt lgkmcnt(5)
ds_read_b64_tr_b16 v[86:87],v5 offset:23072
ds_read_u16_d16_hi v151,v151
v_rcp_f16 v123,v122
v_mfma_f32_16x16x32_bf16 v[140:143],v[76:79],v[28:31],v[140:143]
ds_read_u16 v152,v152
ds_read_u16_d16_hi v154,v154
v_rcp_f16_sdwa v121,v120 dst_sel:WORD_1 dst_unused:UNUSED_PRESERVE src0_sel:WORD_1
v_mfma_f32_16x16x32_bf16 v[132:135],v[76:79],v[24:27],v[132:135]
s_waitcnt lgkmcnt(5)
v_rcp_f16_sdwa v123,v122 dst_sel:WORD_1 dst_unused:UNUSED_PRESERVE src0_sel:WORD_1
v_mfma_f32_16x16x32_bf16 v[128:131],v[80:83],v[32:35],v[128:131]
ds_read_u16 v153,v153
v_fma_mix_f32 v156,v156,v121,0 op_sel_hi:[0,1,0]
v_fma_mix_f32 v158,v158,v121,0 op_sel:[0,1,0] op_sel_hi:[0,1,0]
v_mfma_f32_16x16x32_bf16 v[136:139],v[80:83],v[36:39],v[136:139]
s_waitcnt lgkmcnt(3)
s_setprio 0
v_lshl_add_u64 v[144:145],v[144:145],0,v[146:147]
v_lshl_add_u64 v[146:147],v[148:149],0,v[150:151]
v_mfma_f32_16x16x32_bf16 v[140:143],v[84:87],v[36:39],v[140:143]
ds_read_u16_d16_hi v155,v155
v_fma_mix_f32 v157,v157,v123,0 op_sel_hi:[0,1,0]
v_fma_mix_f32 v159,v159,v123,0 op_sel:[0,1,0] op_sel_hi:[0,1,0]
v_mfma_f32_16x16x32_bf16 v[132:135],v[84:87],v[32:35],v[132:135]
v_lshlrev_b32_sdwa v128,1,v128 dst_sel:DWORD dst_unused:UNUSED_PAD src0_sel:DWORD src1_sel:WORD_1
v_lshlrev_b32_sdwa v130,1,v130 dst_sel:DWORD dst_unused:UNUSED_PAD src0_sel:DWORD src1_sel:WORD_1
v_mfma_f32_16x16x32_bf16 v[208:211],v[88:91],v[144:147],v[208:211]
s_barrier
s_setprio 3
v_lshlrev_b32_sdwa v129,1,v129 dst_sel:DWORD dst_unused:UNUSED_PAD src0_sel:DWORD src1_sel:WORD_1
v_lshlrev_b32_sdwa v131,1,v131 dst_sel:DWORD dst_unused:UNUSED_PAD src0_sel:DWORD src1_sel:WORD_1
v_mfma_f32_16x16x32_bf16 v[216:219],v[92:95],v[144:147],v[216:219]
ds_read_u16 v128,v128
ds_read_u16_d16_hi v130,v130
v_fma_mixlo_f16 v120,v140,s99,0
v_fma_mixlo_f16 v122,v141,s99,0
v_mfma_f32_16x16x32_bf16 v[224:227],v[96:99],v[144:147],v[224:227]
ds_read_u16 v129,v129
v_lshlrev_b32_sdwa v132,1,v132 dst_sel:DWORD dst_unused:UNUSED_PAD src0_sel:DWORD src1_sel:WORD_1
v_lshlrev_b32_sdwa v134,1,v134 dst_sel:DWORD dst_unused:UNUSED_PAD src0_sel:DWORD src1_sel:WORD_1
v_mfma_f32_16x16x32_bf16 v[232:235],v[100:103],v[144:147],v[232:235]
s_waitcnt lgkmcnt(3)
v_lshl_add_u64 v[152:153],v[152:153],0,v[154:155]
v_cvt_pk_bf16_f32 v154,v156,v158
v_mfma_f32_16x16x32_bf16 v[240:243],v[104:107],v[144:147],v[240:243]
v_fma_mixhi_f16 v120,v142,s99,0
v_cvt_pk_bf16_f32 v155,v157,v159
v_mfma_f32_16x16x32_bf16 v[248:251],v[108:111],v[144:147],v[248:251]
ds_read_u16_d16_hi v131,v131
v_fma_mixhi_f16 v122,v143,s99,0
v_mfma_f32_16x16x32_bf16 v[212:215],v[88:91],v[152:155],v[212:215]
v_lshlrev_b32_sdwa v133,1,v133 dst_sel:DWORD dst_unused:UNUSED_PAD src0_sel:DWORD src1_sel:WORD_1
v_pk_fma_f16 v121,v120,v120,s54
v_lshlrev_b32_sdwa v135,1,v135 dst_sel:DWORD dst_unused:UNUSED_PAD src0_sel:DWORD src1_sel:WORD_1
v_mfma_f32_16x16x32_bf16 v[220:223],v[92:95],v[152:155],v[220:223]
ds_read_u16 v132,v132
ds_read_u16_d16_hi v134,v134
v_pk_fma_f16 v123,v122,v122,s54
v_mfma_f32_16x16x32_bf16 v[228:231],v[96:99],v[152:155],v[228:231]
ds_read_b64_tr_b16 v[88:89],v6 offset:0
v_pk_mul_f16 v121,v120,v121
v_lshlrev_b32_sdwa v136,1,v136 dst_sel:DWORD dst_unused:UNUSED_PAD src0_sel:DWORD src1_sel:WORD_1
v_pk_mul_f16 v123,v122,v123
v_mfma_f32_16x16x32_bf16 v[236:239],v[100:103],v[152:155],v[236:239]
ds_read_b64_tr_b16 v[90:91],v6 offset:1536
v_lshlrev_b32_sdwa v138,1,v138 dst_sel:DWORD dst_unused:UNUSED_PAD src0_sel:DWORD src1_sel:WORD_1
v_mfma_f32_16x16x32_bf16 v[244:247],v[104:107],v[152:155],v[244:247]
ds_read_u16 v133,v133
v_exp_f16 v120,v121
v_mfma_f32_16x16x32_bf16 v[252:255],v[108:111],v[152:155],v[252:255]
ds_read_b64_tr_b16 v[92:93],v6 offset:8
v_lshlrev_b32_sdwa v137,1,v137 dst_sel:DWORD dst_unused:UNUSED_PAD src0_sel:DWORD src1_sel:WORD_1
v_mfma_f32_16x16x32_bf16 v[144:147],v[64:67],v[40:43],v[112:115]
ds_read_b64_tr_b16 v[94:95],v6 offset:1544
v_lshlrev_b32_sdwa v139,1,v139 dst_sel:DWORD dst_unused:UNUSED_PAD src0_sel:DWORD src1_sel:WORD_1
v_exp_f16 v122,v123
v_mfma_f32_16x16x32_bf16 v[152:155],v[64:67],v[44:47],v[112:115]
ds_read_u16_d16_hi v135,v135
ds_read_b64_tr_b16 v[96:97],v6 offset:64
v_mfma_f32_16x16x32_bf16 v[156:159],v[68:71],v[44:47],v[116:119]
ds_read_u16 v136,v136
v_exp_f16_sdwa v122,v123 dst_sel:WORD_1 dst_unused:UNUSED_PRESERVE src0_sel:WORD_1
v_exp_f16_sdwa v120,v121 dst_sel:WORD_1 dst_unused:UNUSED_PRESERVE src0_sel:WORD_1
v_mfma_f32_16x16x32_bf16 v[148:151],v[68:71],v[40:43],v[116:119]
ds_read_b64_tr_b16 v[98:99],v6 offset:1600
v_pk_fma_f16 v120,v120,s55,s55
v_pk_fma_f16 v122,v122,s55,s55
v_mfma_f32_16x16x32_bf16 v[144:147],v[72:75],v[48:51],v[144:147]
v_rcp_f16 v121,v120
v_mfma_f32_16x16x32_bf16 v[152:155],v[72:75],v[52:55],v[152:155]
ds_read_b64_tr_b16 v[100:101],v6 offset:72
v_rcp_f16 v123,v122
v_mfma_f32_16x16x32_bf16 v[156:159],v[76:79],v[52:55],v[156:159]
ds_read_u16_d16_hi v138,v138
v_rcp_f16_sdwa v121,v120 dst_sel:WORD_1 dst_unused:UNUSED_PRESERVE src0_sel:WORD_1
v_mfma_f32_16x16x32_bf16 v[148:151],v[76:79],v[48:51],v[148:151]
ds_read_b64_tr_b16 v[102:103],v6 offset:1608
ds_read_b64_tr_b16 v[104:105],v6 offset:128
v_rcp_f16_sdwa v123,v122 dst_sel:WORD_1 dst_unused:UNUSED_PRESERVE src0_sel:WORD_1
v_mfma_f32_16x16x32_bf16 v[144:147],v[80:83],v[56:59],v[144:147]
ds_read_u16 v137,v137
v_fma_mix_f32 v142,v142,v121,0 op_sel:[0,1,0] op_sel_hi:[0,1,0]
v_mfma_f32_16x16x32_bf16 v[152:155],v[80:83],v[60:63],v[152:155]
s_waitcnt lgkmcnt(8)
ds_read_b64_tr_b16 v[106:107],v6 offset:1664
ds_read_b64_tr_b16 v[108:109],v6 offset:136
v_fma_mix_f32 v140,v140,v121,0 op_sel_hi:[0,1,0]
v_lshl_add_u64 v[128:129],v[128:129],0,v[130:131]
v_lshl_add_u64 v[130:131],v[132:133],0,v[134:135]
v_mfma_f32_16x16x32_bf16 v[156:159],v[84:87],v[60:63],v[156:159]
ds_read_u16_d16_hi v139,v139
v_fma_mix_f32 v141,v141,v123,0 op_sel_hi:[0,1,0]
v_fma_mix_f32 v143,v143,v123,0 op_sel:[0,1,0] op_sel_hi:[0,1,0]
v_mfma_f32_16x16x32_bf16 v[148:151],v[84:87],v[56:59],v[148:151]
ds_read_b64_tr_b16 v[110:111],v6 offset:1672
ds_read_b64_tr_b16 v[64:65],v4 offset:0
v_lshlrev_b32_sdwa v144,1,v144 dst_sel:DWORD dst_unused:UNUSED_PAD src0_sel:DWORD src1_sel:WORD_1
v_lshlrev_b32_sdwa v146,1,v146 dst_sel:DWORD dst_unused:UNUSED_PAD src0_sel:DWORD src1_sel:WORD_1
v_mfma_f32_16x16x32_bf16 v[160:163],v[88:91],v[128:131],v[160:163]
ds_read_u16 v144,v144
v_lshlrev_b32_sdwa v145,1,v145 dst_sel:DWORD dst_unused:UNUSED_PAD src0_sel:DWORD src1_sel:WORD_1
v_lshlrev_b32_sdwa v147,1,v147 dst_sel:DWORD dst_unused:UNUSED_PAD src0_sel:DWORD src1_sel:WORD_1
v_fma_mixlo_f16 v122,v157,s99,0
v_mfma_f32_16x16x32_bf16 v[168:171],v[92:95],v[128:131],v[168:171]
s_waitcnt lgkmcnt(11)
ds_read_b64_tr_b16 v[66:67],v4 offset:2048
ds_read_b128 v[112:115],v7 offset:768
v_fma_mixlo_f16 v120,v156,s99,0
v_mfma_f32_16x16x32_bf16 v[176:179],v[96:99],v[128:131],v[176:179]
s_waitcnt lgkmcnt(10)
ds_read_u16_d16_hi v146,v146
v_lshlrev_b32_sdwa v148,1,v148 dst_sel:DWORD dst_unused:UNUSED_PAD src0_sel:DWORD src1_sel:WORD_1
v_lshlrev_b32_sdwa v150,1,v150 dst_sel:DWORD dst_unused:UNUSED_PAD src0_sel:DWORD src1_sel:WORD_1
v_mfma_f32_16x16x32_bf16 v[184:187],v[100:103],v[128:131],v[184:187]
s_waitcnt lgkmcnt(6)
ds_read_b64_tr_b16 v[68:69],v5 offset:0
ds_read_b64_tr_b16 v[70:71],v5 offset:2048
v_lshl_add_u64 v[136:137],v[136:137],0,v[138:139]
v_fma_mixhi_f16 v120,v158,s99,0
v_mfma_f32_16x16x32_bf16 v[192:195],v[104:107],v[128:131],v[192:195]
s_waitcnt lgkmcnt(7)
ds_read_u16 v145,v145
v_cvt_pk_bf16_f32 v138,v140,v142
v_cvt_pk_bf16_f32 v139,v141,v143
v_mfma_f32_16x16x32_bf16 v[200:203],v[108:111],v[128:131],v[200:203]
v_fma_mixhi_f16 v122,v159,s99,0
v_mfma_f32_16x16x32_bf16 v[164:167],v[88:91],v[136:139],v[164:167]
ds_read_b128 v[116:119],v7 offset:800
ds_read_b64_tr_b16 v[72:73],v4 offset:4096
v_lshlrev_b32_sdwa v149,1,v149 dst_sel:DWORD dst_unused:UNUSED_PAD src0_sel:DWORD src1_sel:WORD_1
v_lshlrev_b32_sdwa v151,1,v151 dst_sel:DWORD dst_unused:UNUSED_PAD src0_sel:DWORD src1_sel:WORD_1
v_mfma_f32_16x16x32_bf16 v[172:175],v[92:95],v[136:139],v[172:175]
ds_read_b64_tr_b16 v[74:75],v4 offset:6144
ds_read_b64_tr_b16 v[76:77],v5 offset:4096
v_pk_fma_f16 v121,v120,v120,s54
v_pk_fma_f16 v123,v122,v122,s54
v_mfma_f32_16x16x32_bf16 v[180:183],v[96:99],v[136:139],v[180:183]
ds_read_u16_d16_hi v147,v147
v_pk_mul_f16 v121,v120,v121
v_pk_mul_f16 v123,v122,v123
v_mfma_f32_16x16x32_bf16 v[188:191],v[100:103],v[136:139],v[188:191]
ds_read_u16 v148,v148
v_lshlrev_b32_sdwa v152,1,v152 dst_sel:DWORD dst_unused:UNUSED_PAD src0_sel:DWORD src1_sel:WORD_1
v_lshlrev_b32_sdwa v154,1,v154 dst_sel:DWORD dst_unused:UNUSED_PAD src0_sel:DWORD src1_sel:WORD_1
v_mfma_f32_16x16x32_bf16 v[196:199],v[104:107],v[136:139],v[196:199]
v_exp_f16 v120,v121
v_mfma_f32_16x16x32_bf16 v[204:207],v[108:111],v[136:139],v[204:207]
s_waitcnt lgkmcnt(10)
ds_read_b64_tr_b16 v[78:79],v5 offset:6144
ds_read_b64_tr_b16 v[80:81],v4 offset:8192
v_exp_f16 v122,v123
v_mfma_f32_16x16x32_bf16 v[128:131],v[64:67],v[16:19],v[112:115]
s_mov_b32 s86,0xbe801d5a
v_lshlrev_b32_sdwa v153,1,v153 dst_sel:DWORD dst_unused:UNUSED_PAD src0_sel:DWORD src1_sel:WORD_1
v_lshlrev_b32_sdwa v155,1,v155 dst_sel:DWORD dst_unused:UNUSED_PAD src0_sel:DWORD src1_sel:WORD_1
v_mfma_f32_16x16x32_bf16 v[136:139],v[64:67],v[20:23],v[112:115]
s_waitcnt lgkmcnt(7)
ds_read_u16_d16_hi v150,v150
ds_read_u16 v149,v149
v_exp_f16_sdwa v120,v121 dst_sel:WORD_1 dst_unused:UNUSED_PRESERVE src0_sel:WORD_1
v_mfma_f32_16x16x32_bf16 v[140:143],v[68:71],v[20:23],v[116:119]
v_exp_f16_sdwa v122,v123 dst_sel:WORD_1 dst_unused:UNUSED_PRESERVE src0_sel:WORD_1
v_mfma_f32_16x16x32_bf16 v[132:135],v[68:71],v[16:19],v[116:119]
s_waitcnt lgkmcnt(7)
ds_read_b64_tr_b16 v[82:83],v4 offset:10240
v_pk_fma_f16 v120,v120,s55,s55
v_mfma_f32_16x16x32_bf16 v[128:131],v[72:75],v[24:27],v[128:131]
ds_read_b64_tr_b16 v[84:85],v5 offset:8192
v_pk_fma_f16 v122,v122,s55,s55
v_rcp_f16 v121,v120
v_mfma_f32_16x16x32_bf16 v[136:139],v[72:75],v[28:31],v[136:139]
s_waitcnt lgkmcnt(5)
ds_read_b64_tr_b16 v[86:87],v5 offset:10240
ds_read_u16_d16_hi v151,v151
v_rcp_f16 v123,v122
v_mfma_f32_16x16x32_bf16 v[140:143],v[76:79],v[28:31],v[140:143]
ds_read_u16 v152,v152
ds_read_u16_d16_hi v154,v154
v_rcp_f16_sdwa v121,v120 dst_sel:WORD_1 dst_unused:UNUSED_PRESERVE src0_sel:WORD_1
v_mfma_f32_16x16x32_bf16 v[132:135],v[76:79],v[24:27],v[132:135]
s_waitcnt lgkmcnt(5)
v_rcp_f16_sdwa v123,v122 dst_sel:WORD_1 dst_unused:UNUSED_PRESERVE src0_sel:WORD_1
v_mfma_f32_16x16x32_bf16 v[128:131],v[80:83],v[32:35],v[128:131]
ds_read_u16 v153,v153
v_fma_mix_f32 v156,v156,v121,0 op_sel_hi:[0,1,0]
v_fma_mix_f32 v158,v158,v121,0 op_sel:[0,1,0] op_sel_hi:[0,1,0]
v_mfma_f32_16x16x32_bf16 v[136:139],v[80:83],v[36:39],v[136:139]
s_waitcnt lgkmcnt(3)
v_lshl_add_u64 v[144:145],v[144:145],0,v[146:147]
v_lshl_add_u64 v[146:147],v[148:149],0,v[150:151]
v_mfma_f32_16x16x32_bf16 v[140:143],v[84:87],v[36:39],v[140:143]
ds_read_u16_d16_hi v155,v155
v_fma_mix_f32 v157,v157,v123,0 op_sel_hi:[0,1,0]
v_fma_mix_f32 v159,v159,v123,0 op_sel:[0,1,0] op_sel_hi:[0,1,0]
v_mfma_f32_16x16x32_bf16 v[132:135],v[84:87],v[32:35],v[132:135]
v_lshlrev_b32_sdwa v128,1,v128 dst_sel:DWORD dst_unused:UNUSED_PAD src0_sel:DWORD src1_sel:WORD_1
v_lshlrev_b32_sdwa v130,1,v130 dst_sel:DWORD dst_unused:UNUSED_PAD src0_sel:DWORD src1_sel:WORD_1
v_mfma_f32_16x16x32_bf16 v[208:211],v[88:91],v[144:147],v[208:211]
v_lshlrev_b32_sdwa v129,1,v129 dst_sel:DWORD dst_unused:UNUSED_PAD src0_sel:DWORD src1_sel:WORD_1
v_lshlrev_b32_sdwa v131,1,v131 dst_sel:DWORD dst_unused:UNUSED_PAD src0_sel:DWORD src1_sel:WORD_1
v_mfma_f32_16x16x32_bf16 v[216:219],v[92:95],v[144:147],v[216:219]
ds_read_u16 v128,v128
ds_read_u16_d16_hi v130,v130
v_fma_mixlo_f16 v120,v140,s99,0
v_fma_mixlo_f16 v122,v141,s99,0
v_mfma_f32_16x16x32_bf16 v[224:227],v[96:99],v[144:147],v[224:227]
ds_read_u16 v129,v129
v_lshlrev_b32_sdwa v132,1,v132 dst_sel:DWORD dst_unused:UNUSED_PAD src0_sel:DWORD src1_sel:WORD_1
v_lshlrev_b32_sdwa v134,1,v134 dst_sel:DWORD dst_unused:UNUSED_PAD src0_sel:DWORD src1_sel:WORD_1
v_mfma_f32_16x16x32_bf16 v[232:235],v[100:103],v[144:147],v[232:235]
s_waitcnt lgkmcnt(3)
v_lshl_add_u64 v[152:153],v[152:153],0,v[154:155]
v_cvt_pk_bf16_f32 v154,v156,v158
v_mfma_f32_16x16x32_bf16 v[240:243],v[104:107],v[144:147],v[240:243]
v_fma_mixhi_f16 v120,v142,s99,0
v_cvt_pk_bf16_f32 v155,v157,v159
v_mfma_f32_16x16x32_bf16 v[248:251],v[108:111],v[144:147],v[248:251]
s_setprio 2
ds_read_u16_d16_hi v131,v131
v_fma_mixhi_f16 v122,v143,s99,0
v_mfma_f32_16x16x32_bf16 v[212:215],v[88:91],v[152:155],v[212:215]
v_lshlrev_b32_sdwa v133,1,v133 dst_sel:DWORD dst_unused:UNUSED_PAD src0_sel:DWORD src1_sel:WORD_1
v_pk_fma_f16 v121,v120,v120,s54
v_lshlrev_b32_sdwa v135,1,v135 dst_sel:DWORD dst_unused:UNUSED_PAD src0_sel:DWORD src1_sel:WORD_1
v_mfma_f32_16x16x32_bf16 v[220:223],v[92:95],v[152:155],v[220:223]
ds_read_u16 v132,v132
ds_read_u16_d16_hi v134,v134
v_pk_fma_f16 v123,v122,v122,s54
v_mfma_f32_16x16x32_bf16 v[228:231],v[96:99],v[152:155],v[228:231]
ds_read_b64_tr_b16 v[88:89],v6 offset:6160
v_pk_mul_f16 v121,v120,v121
v_lshlrev_b32_sdwa v136,1,v136 dst_sel:DWORD dst_unused:UNUSED_PAD src0_sel:DWORD src1_sel:WORD_1
v_pk_mul_f16 v123,v122,v123
v_mfma_f32_16x16x32_bf16 v[236:239],v[100:103],v[152:155],v[236:239]
ds_read_b64_tr_b16 v[90:91],v6 offset:7696
v_lshlrev_b32_sdwa v138,1,v138 dst_sel:DWORD dst_unused:UNUSED_PAD src0_sel:DWORD src1_sel:WORD_1
v_mfma_f32_16x16x32_bf16 v[244:247],v[104:107],v[152:155],v[244:247]
ds_read_u16 v133,v133
v_exp_f16 v120,v121
v_mfma_f32_16x16x32_bf16 v[252:255],v[108:111],v[152:155],v[252:255]
ds_read_b64_tr_b16 v[92:93],v6 offset:6168
v_lshlrev_b32_sdwa v137,1,v137 dst_sel:DWORD dst_unused:UNUSED_PAD src0_sel:DWORD src1_sel:WORD_1
v_mfma_f32_16x16x32_bf16 v[144:147],v[64:67],v[40:43],v[112:115]
ds_read_b64_tr_b16 v[94:95],v6 offset:7704
v_lshlrev_b32_sdwa v139,1,v139 dst_sel:DWORD dst_unused:UNUSED_PAD src0_sel:DWORD src1_sel:WORD_1
v_exp_f16 v122,v123
v_mfma_f32_16x16x32_bf16 v[152:155],v[64:67],v[44:47],v[112:115]
ds_read_u16_d16_hi v135,v135
ds_read_b64_tr_b16 v[96:97],v6 offset:6224
v_mfma_f32_16x16x32_bf16 v[156:159],v[68:71],v[44:47],v[116:119]
ds_read_u16 v136,v136
v_exp_f16_sdwa v122,v123 dst_sel:WORD_1 dst_unused:UNUSED_PRESERVE src0_sel:WORD_1
v_exp_f16_sdwa v120,v121 dst_sel:WORD_1 dst_unused:UNUSED_PRESERVE src0_sel:WORD_1
v_mfma_f32_16x16x32_bf16 v[148:151],v[68:71],v[40:43],v[116:119]
ds_read_b64_tr_b16 v[98:99],v6 offset:7760
v_pk_fma_f16 v120,v120,s55,s55
v_pk_fma_f16 v122,v122,s55,s55
v_mfma_f32_16x16x32_bf16 v[144:147],v[72:75],v[48:51],v[144:147]
v_rcp_f16 v121,v120
v_mfma_f32_16x16x32_bf16 v[152:155],v[72:75],v[52:55],v[152:155]
ds_read_b64_tr_b16 v[100:101],v6 offset:6232
v_rcp_f16 v123,v122
v_mfma_f32_16x16x32_bf16 v[156:159],v[76:79],v[52:55],v[156:159]
ds_read_u16_d16_hi v138,v138
v_rcp_f16_sdwa v121,v120 dst_sel:WORD_1 dst_unused:UNUSED_PRESERVE src0_sel:WORD_1
v_mfma_f32_16x16x32_bf16 v[148:151],v[76:79],v[48:51],v[148:151]
ds_read_b64_tr_b16 v[102:103],v6 offset:7768
ds_read_b64_tr_b16 v[104:105],v6 offset:6288
v_rcp_f16_sdwa v123,v122 dst_sel:WORD_1 dst_unused:UNUSED_PRESERVE src0_sel:WORD_1
v_mfma_f32_16x16x32_bf16 v[144:147],v[80:83],v[56:59],v[144:147]
ds_read_u16 v137,v137
v_fma_mix_f32 v142,v142,v121,0 op_sel:[0,1,0] op_sel_hi:[0,1,0]
v_mfma_f32_16x16x32_bf16 v[152:155],v[80:83],v[60:63],v[152:155]
s_waitcnt lgkmcnt(8)
ds_read_b64_tr_b16 v[106:107],v6 offset:7824
ds_read_b64_tr_b16 v[108:109],v6 offset:6296
v_fma_mix_f32 v140,v140,v121,0 op_sel_hi:[0,1,0]
v_lshl_add_u64 v[128:129],v[128:129],0,v[130:131]
v_lshl_add_u64 v[130:131],v[132:133],0,v[134:135]
v_mfma_f32_16x16x32_bf16 v[156:159],v[84:87],v[60:63],v[156:159]
ds_read_u16_d16_hi v139,v139
v_fma_mix_f32 v141,v141,v123,0 op_sel_hi:[0,1,0]
v_fma_mix_f32 v143,v143,v123,0 op_sel:[0,1,0] op_sel_hi:[0,1,0]
v_mfma_f32_16x16x32_bf16 v[148:151],v[84:87],v[56:59],v[148:151]
ds_read_b64_tr_b16 v[110:111],v6 offset:7832
ds_read_b64_tr_b16 v[64:65],v4 offset:512
v_lshlrev_b32_sdwa v144,1,v144 dst_sel:DWORD dst_unused:UNUSED_PAD src0_sel:DWORD src1_sel:WORD_1
v_lshlrev_b32_sdwa v146,1,v146 dst_sel:DWORD dst_unused:UNUSED_PAD src0_sel:DWORD src1_sel:WORD_1
v_mfma_f32_16x16x32_bf16 v[160:163],v[88:91],v[128:131],v[160:163]
ds_read_u16 v144,v144
v_lshlrev_b32_sdwa v145,1,v145 dst_sel:DWORD dst_unused:UNUSED_PAD src0_sel:DWORD src1_sel:WORD_1
v_lshlrev_b32_sdwa v147,1,v147 dst_sel:DWORD dst_unused:UNUSED_PAD src0_sel:DWORD src1_sel:WORD_1
v_fma_mixlo_f16 v122,v157,s99,0
v_mfma_f32_16x16x32_bf16 v[168:171],v[92:95],v[128:131],v[168:171]
s_waitcnt lgkmcnt(11)
ds_read_b64_tr_b16 v[66:67],v4 offset:2560
ds_read_b128 v[112:115],v7 offset:896
v_fma_mixlo_f16 v120,v156,s99,0
v_mfma_f32_16x16x32_bf16 v[176:179],v[96:99],v[128:131],v[176:179]
s_waitcnt lgkmcnt(10)
ds_read_u16_d16_hi v146,v146
v_lshlrev_b32_sdwa v148,1,v148 dst_sel:DWORD dst_unused:UNUSED_PAD src0_sel:DWORD src1_sel:WORD_1
v_lshlrev_b32_sdwa v150,1,v150 dst_sel:DWORD dst_unused:UNUSED_PAD src0_sel:DWORD src1_sel:WORD_1
v_mfma_f32_16x16x32_bf16 v[184:187],v[100:103],v[128:131],v[184:187]
s_waitcnt lgkmcnt(6)
ds_read_b64_tr_b16 v[68:69],v5 offset:512
ds_read_b64_tr_b16 v[70:71],v5 offset:2560
v_lshl_add_u64 v[136:137],v[136:137],0,v[138:139]
v_fma_mixhi_f16 v120,v158,s99,0
v_mfma_f32_16x16x32_bf16 v[192:195],v[104:107],v[128:131],v[192:195]
s_waitcnt lgkmcnt(7)
ds_read_u16 v145,v145
v_cvt_pk_bf16_f32 v138,v140,v142
v_cvt_pk_bf16_f32 v139,v141,v143
v_mfma_f32_16x16x32_bf16 v[200:203],v[108:111],v[128:131],v[200:203]
v_fma_mixhi_f16 v122,v159,s99,0
v_mfma_f32_16x16x32_bf16 v[164:167],v[88:91],v[136:139],v[164:167]
ds_read_b128 v[116:119],v7 offset:928
ds_read_b64_tr_b16 v[72:73],v4 offset:4608
v_lshlrev_b32_sdwa v149,1,v149 dst_sel:DWORD dst_unused:UNUSED_PAD src0_sel:DWORD src1_sel:WORD_1
v_lshlrev_b32_sdwa v151,1,v151 dst_sel:DWORD dst_unused:UNUSED_PAD src0_sel:DWORD src1_sel:WORD_1
v_mfma_f32_16x16x32_bf16 v[172:175],v[92:95],v[136:139],v[172:175]
s_setprio 1
ds_read_b64_tr_b16 v[74:75],v4 offset:6656
ds_read_b64_tr_b16 v[76:77],v5 offset:4608
v_pk_fma_f16 v121,v120,v120,s54
v_pk_fma_f16 v123,v122,v122,s54
v_mfma_f32_16x16x32_bf16 v[180:183],v[96:99],v[136:139],v[180:183]
ds_read_u16_d16_hi v147,v147
v_pk_mul_f16 v121,v120,v121
v_pk_mul_f16 v123,v122,v123
v_mfma_f32_16x16x32_bf16 v[188:191],v[100:103],v[136:139],v[188:191]
ds_read_u16 v148,v148
v_lshlrev_b32_sdwa v152,1,v152 dst_sel:DWORD dst_unused:UNUSED_PAD src0_sel:DWORD src1_sel:WORD_1
v_lshlrev_b32_sdwa v154,1,v154 dst_sel:DWORD dst_unused:UNUSED_PAD src0_sel:DWORD src1_sel:WORD_1
v_mfma_f32_16x16x32_bf16 v[196:199],v[104:107],v[136:139],v[196:199]
v_exp_f16 v120,v121
v_mfma_f32_16x16x32_bf16 v[204:207],v[108:111],v[136:139],v[204:207]
s_waitcnt lgkmcnt(10)
ds_read_b64_tr_b16 v[78:79],v5 offset:6656
ds_read_b64_tr_b16 v[80:81],v4 offset:8704
v_exp_f16 v122,v123
v_mfma_f32_16x16x32_bf16 v[128:131],v[64:67],v[16:19],v[112:115]
v_lshlrev_b32_sdwa v153,1,v153 dst_sel:DWORD dst_unused:UNUSED_PAD src0_sel:DWORD src1_sel:WORD_1
v_lshlrev_b32_sdwa v155,1,v155 dst_sel:DWORD dst_unused:UNUSED_PAD src0_sel:DWORD src1_sel:WORD_1
v_mfma_f32_16x16x32_bf16 v[136:139],v[64:67],v[20:23],v[112:115]
s_waitcnt lgkmcnt(7)
ds_read_u16_d16_hi v150,v150
ds_read_u16 v149,v149
v_exp_f16_sdwa v120,v121 dst_sel:WORD_1 dst_unused:UNUSED_PRESERVE src0_sel:WORD_1
v_mfma_f32_16x16x32_bf16 v[140:143],v[68:71],v[20:23],v[116:119]
v_exp_f16_sdwa v122,v123 dst_sel:WORD_1 dst_unused:UNUSED_PRESERVE src0_sel:WORD_1
v_mfma_f32_16x16x32_bf16 v[132:135],v[68:71],v[16:19],v[116:119]
s_waitcnt lgkmcnt(7)
ds_read_b64_tr_b16 v[82:83],v4 offset:10752
v_pk_fma_f16 v120,v120,s55,s55
v_mfma_f32_16x16x32_bf16 v[128:131],v[72:75],v[24:27],v[128:131]
ds_read_b64_tr_b16 v[84:85],v5 offset:8704
v_pk_fma_f16 v122,v122,s55,s55
v_rcp_f16 v121,v120
v_mfma_f32_16x16x32_bf16 v[136:139],v[72:75],v[28:31],v[136:139]
s_waitcnt lgkmcnt(5)
ds_read_b64_tr_b16 v[86:87],v5 offset:10752
ds_read_u16_d16_hi v151,v151
v_rcp_f16 v123,v122
v_mfma_f32_16x16x32_bf16 v[140:143],v[76:79],v[28:31],v[140:143]
ds_read_u16 v152,v152
ds_read_u16_d16_hi v154,v154
v_rcp_f16_sdwa v121,v120 dst_sel:WORD_1 dst_unused:UNUSED_PRESERVE src0_sel:WORD_1
v_mfma_f32_16x16x32_bf16 v[132:135],v[76:79],v[24:27],v[132:135]
s_waitcnt lgkmcnt(5)
v_rcp_f16_sdwa v123,v122 dst_sel:WORD_1 dst_unused:UNUSED_PRESERVE src0_sel:WORD_1
v_mfma_f32_16x16x32_bf16 v[128:131],v[80:83],v[32:35],v[128:131]
ds_read_u16 v153,v153
v_fma_mix_f32 v156,v156,v121,0 op_sel_hi:[0,1,0]
v_fma_mix_f32 v158,v158,v121,0 op_sel:[0,1,0] op_sel_hi:[0,1,0]
v_mfma_f32_16x16x32_bf16 v[136:139],v[80:83],v[36:39],v[136:139]
s_waitcnt lgkmcnt(3)
s_setprio 0
v_lshl_add_u64 v[144:145],v[144:145],0,v[146:147]
v_lshl_add_u64 v[146:147],v[148:149],0,v[150:151]
v_mfma_f32_16x16x32_bf16 v[140:143],v[84:87],v[36:39],v[140:143]
ds_read_u16_d16_hi v155,v155
v_fma_mix_f32 v157,v157,v123,0 op_sel_hi:[0,1,0]
v_fma_mix_f32 v159,v159,v123,0 op_sel:[0,1,0] op_sel_hi:[0,1,0]
v_mfma_f32_16x16x32_bf16 v[132:135],v[84:87],v[32:35],v[132:135]
v_lshlrev_b32_sdwa v128,1,v128 dst_sel:DWORD dst_unused:UNUSED_PAD src0_sel:DWORD src1_sel:WORD_1
v_lshlrev_b32_sdwa v130,1,v130 dst_sel:DWORD dst_unused:UNUSED_PAD src0_sel:DWORD src1_sel:WORD_1
v_mfma_f32_16x16x32_bf16 v[208:211],v[88:91],v[144:147],v[208:211]
s_barrier
s_setprio 2
ds_read_u16 v128,v128
v_lshlrev_b32_sdwa v129,1,v129 dst_sel:DWORD dst_unused:UNUSED_PAD src0_sel:DWORD src1_sel:WORD_1
v_mfma_f32_16x16x32_bf16 v[216:219],v[92:95],v[144:147],v[216:219]
ds_read_u16_d16_hi v130,v130
v_lshlrev_b32_sdwa v131,1,v131 dst_sel:DWORD dst_unused:UNUSED_PAD src0_sel:DWORD src1_sel:WORD_1
v_mfma_f32_16x16x32_bf16 v[224:227],v[96:99],v[144:147],v[224:227]
v_lshlrev_b32_sdwa v132,1,v132 dst_sel:DWORD dst_unused:UNUSED_PAD src0_sel:DWORD src1_sel:WORD_1
v_mfma_f32_16x16x32_bf16 v[232:235],v[100:103],v[144:147],v[232:235]
s_waitcnt lgkmcnt(2)
ds_read_u16 v129,v129
v_lshl_add_u64 v[152:153],v[152:153],0,v[154:155]
v_lshlrev_b32_sdwa v134,1,v134 dst_sel:DWORD dst_unused:UNUSED_PAD src0_sel:DWORD src1_sel:WORD_1
v_mfma_f32_16x16x32_bf16 v[240:243],v[104:107],v[144:147],v[240:243]
v_cvt_pk_bf16_f32 v155,v157,v159
v_cvt_pk_bf16_f32 v154,v156,v158
v_mfma_f32_16x16x32_bf16 v[248:251],v[108:111],v[144:147],v[248:251]
ds_read_u16_d16_hi v131,v131
v_lshlrev_b32_sdwa v133,1,v133 dst_sel:DWORD dst_unused:UNUSED_PAD src0_sel:DWORD src1_sel:WORD_1
v_mfma_f32_16x16x32_bf16 v[212:215],v[88:91],v[152:155],v[212:215]
ds_read_u16 v132,v132
v_lshlrev_b32_sdwa v135,1,v135 dst_sel:DWORD dst_unused:UNUSED_PAD src0_sel:DWORD src1_sel:WORD_1
v_mfma_f32_16x16x32_bf16 v[220:223],v[92:95],v[152:155],v[220:223]
ds_read_b64_tr_b16 v[88:89],v6 offset:12320
ds_read_b64_tr_b16 v[90:91],v6 offset:13856
v_lshlrev_b32_sdwa v136,1,v136 dst_sel:DWORD dst_unused:UNUSED_PAD src0_sel:DWORD src1_sel:WORD_1
v_mfma_f32_16x16x32_bf16 v[228:231],v[96:99],v[152:155],v[228:231]
ds_read_u16_d16_hi v134,v134
v_lshlrev_b32_sdwa v138,1,v138 dst_sel:DWORD dst_unused:UNUSED_PAD src0_sel:DWORD src1_sel:WORD_1
v_mfma_f32_16x16x32_bf16 v[236:239],v[100:103],v[152:155],v[236:239]
ds_read_u16 v133,v133
v_lshlrev_b32_sdwa v137,1,v137 dst_sel:DWORD dst_unused:UNUSED_PAD src0_sel:DWORD src1_sel:WORD_1
v_mfma_f32_16x16x32_bf16 v[244:247],v[104:107],v[152:155],v[244:247]
ds_read_u16_d16_hi v135,v135
v_lshlrev_b32_sdwa v139,1,v139 dst_sel:DWORD dst_unused:UNUSED_PAD src0_sel:DWORD src1_sel:WORD_1
v_mfma_f32_16x16x32_bf16 v[252:255],v[108:111],v[152:155],v[252:255]
ds_read_b64_tr_b16 v[92:93],v6 offset:12328
ds_read_b64_tr_b16 v[94:95],v6 offset:13864
v_lshlrev_b32_sdwa v140,1,v140 dst_sel:DWORD dst_unused:UNUSED_PAD src0_sel:DWORD src1_sel:WORD_1
v_mfma_f32_16x16x32_bf16 v[144:147],v[64:67],v[40:43],v[112:115]
ds_read_u16 v136,v136
v_lshlrev_b32_sdwa v142,1,v142 dst_sel:DWORD dst_unused:UNUSED_PAD src0_sel:DWORD src1_sel:WORD_1
v_mfma_f32_16x16x32_bf16 v[152:155],v[64:67],v[44:47],v[112:115]
ds_read_u16_d16_hi v138,v138
ds_read_b64_tr_b16 v[96:97],v6 offset:12384
v_lshlrev_b32_sdwa v141,1,v141 dst_sel:DWORD dst_unused:UNUSED_PAD src0_sel:DWORD src1_sel:WORD_1
v_mfma_f32_16x16x32_bf16 v[156:159],v[68:71],v[44:47],v[116:119]
ds_read_b64_tr_b16 v[98:99],v6 offset:13920
ds_read_b64_tr_b16 v[100:101],v6 offset:12392
v_mfma_f32_16x16x32_bf16 v[148:151],v[68:71],v[40:43],v[116:119]
ds_read_u16 v137,v137
v_lshlrev_b32_sdwa v143,1,v143 dst_sel:DWORD dst_unused:UNUSED_PAD src0_sel:DWORD src1_sel:WORD_1
v_mfma_f32_16x16x32_bf16 v[144:147],v[72:75],v[48:51],v[144:147]
ds_read_u16_d16_hi v139,v139
v_mfma_f32_16x16x32_bf16 v[152:155],v[72:75],v[52:55],v[152:155]
ds_read_b64_tr_b16 v[102:103],v6 offset:13928
ds_read_b64_tr_b16 v[104:105],v6 offset:12448
v_mfma_f32_16x16x32_bf16 v[156:159],v[76:79],v[52:55],v[156:159]
ds_read_u16 v140,v140
v_mfma_f32_16x16x32_bf16 v[148:151],v[76:79],v[48:51],v[148:151]
s_waitcnt lgkmcnt(15)
ds_read_u16_d16_hi v142,v142
v_lshl_add_u64 v[128:129],v[128:129],0,v[130:131]
v_mfma_f32_16x16x32_bf16 v[144:147],v[80:83],v[56:59],v[144:147]
ds_read_b64_tr_b16 v[106:107],v6 offset:13984
ds_read_b64_tr_b16 v[108:109],v6 offset:12456
v_mfma_f32_16x16x32_bf16 v[152:155],v[80:83],v[60:63],v[152:155]
s_waitcnt lgkmcnt(15)
ds_read_u16 v141,v141
v_lshl_add_u64 v[130:131],v[132:133],0,v[134:135]
v_mfma_f32_16x16x32_bf16 v[156:159],v[84:87],v[60:63],v[156:159]
ds_read_u16_d16_hi v143,v143
v_mfma_f32_16x16x32_bf16 v[148:151],v[84:87],v[56:59],v[148:151]
ds_read_b64_tr_b16 v[110:111],v6 offset:13992
ds_read_b64_tr_b16 v[64:65],v4 offset:12320
v_lshlrev_b32_sdwa v144,1,v144 dst_sel:DWORD dst_unused:UNUSED_PAD src0_sel:DWORD src1_sel:WORD_1
v_lshlrev_b32_sdwa v146,1,v146 dst_sel:DWORD dst_unused:UNUSED_PAD src0_sel:DWORD src1_sel:WORD_1
v_mfma_f32_16x16x32_bf16 v[160:163],v[88:91],v[128:131],v[160:163]
s_waitcnt lgkmcnt(15)
v_lshlrev_b32_sdwa v145,1,v145 dst_sel:DWORD dst_unused:UNUSED_PAD src0_sel:DWORD src1_sel:WORD_1
v_mfma_f32_16x16x32_bf16 v[168:171],v[92:95],v[128:131],v[168:171]
s_waitcnt lgkmcnt(13)
ds_read_u16 v144,v144
v_lshlrev_b32_sdwa v147,1,v147 dst_sel:DWORD dst_unused:UNUSED_PAD src0_sel:DWORD src1_sel:WORD_1
v_mfma_f32_16x16x32_bf16 v[176:179],v[96:99],v[128:131],v[176:179]
s_waitcnt lgkmcnt(10)
ds_read_b64_tr_b16 v[66:67],v4 offset:14368
ds_read_b128 v[112:115],v7 offset:1024
v_mfma_f32_16x16x32_bf16 v[184:187],v[100:103],v[128:131],v[184:187]
s_waitcnt lgkmcnt(8)
ds_read_b64_tr_b16 v[68:69],v5 offset:12320
ds_read_u16_d16_hi v146,v146
v_lshlrev_b32_sdwa v148,1,v148 dst_sel:DWORD dst_unused:UNUSED_PAD src0_sel:DWORD src1_sel:WORD_1
v_lshl_add_u64 v[136:137],v[136:137],0,v[138:139]
v_lshlrev_b32_sdwa v150,1,v150 dst_sel:DWORD dst_unused:UNUSED_PAD src0_sel:DWORD src1_sel:WORD_1
v_mfma_f32_16x16x32_bf16 v[192:195],v[104:107],v[128:131],v[192:195]
s_waitcnt lgkmcnt(6)
ds_read_u16 v145,v145
ds_read_u16_d16_hi v147,v147
v_lshl_add_u64 v[138:139],v[140:141],0,v[142:143]
v_mfma_f32_16x16x32_bf16 v[200:203],v[108:111],v[128:131],v[200:203]
ds_read_b64_tr_b16 v[70:71],v5 offset:14368
ds_read_b128 v[116:119],v7 offset:1056
v_mfma_f32_16x16x32_bf16 v[164:167],v[88:91],v[136:139],v[164:167]
ds_read_u16 v148,v148
v_lshlrev_b32_sdwa v149,1,v149 dst_sel:DWORD dst_unused:UNUSED_PAD src0_sel:DWORD src1_sel:WORD_1
v_lshlrev_b32_sdwa v151,1,v151 dst_sel:DWORD dst_unused:UNUSED_PAD src0_sel:DWORD src1_sel:WORD_1
v_mfma_f32_16x16x32_bf16 v[172:175],v[92:95],v[136:139],v[172:175]
ds_read_b64_tr_b16 v[72:73],v4 offset:16416
v_lshlrev_b32_sdwa v152,1,v152 dst_sel:DWORD dst_unused:UNUSED_PAD src0_sel:DWORD src1_sel:WORD_1
v_mfma_f32_16x16x32_bf16 v[180:183],v[96:99],v[136:139],v[180:183]
ds_read_b64_tr_b16 v[74:75],v4 offset:18464
ds_read_b64_tr_b16 v[76:77],v5 offset:16416
v_lshlrev_b32_sdwa v154,1,v154 dst_sel:DWORD dst_unused:UNUSED_PAD src0_sel:DWORD src1_sel:WORD_1
v_mfma_f32_16x16x32_bf16 v[188:191],v[100:103],v[136:139],v[188:191]
ds_read_u16_d16_hi v150,v150
v_lshlrev_b32_sdwa v153,1,v153 dst_sel:DWORD dst_unused:UNUSED_PAD src0_sel:DWORD src1_sel:WORD_1
v_mfma_f32_16x16x32_bf16 v[196:199],v[104:107],v[136:139],v[196:199]
ds_read_u16 v149,v149
v_lshlrev_b32_sdwa v155,1,v155 dst_sel:DWORD dst_unused:UNUSED_PAD src0_sel:DWORD src1_sel:WORD_1
v_mfma_f32_16x16x32_bf16 v[204:207],v[108:111],v[136:139],v[204:207]
s_waitcnt lgkmcnt(12)
ds_read_b64_tr_b16 v[78:79],v5 offset:18464
ds_read_b64_tr_b16 v[80:81],v4 offset:20512
v_lshlrev_b32_sdwa v156,1,v156 dst_sel:DWORD dst_unused:UNUSED_PAD src0_sel:DWORD src1_sel:WORD_1
v_mfma_f32_16x16x32_bf16 v[128:131],v[64:67],v[16:19],v[112:115]
ds_read_u16_d16_hi v151,v151
v_lshlrev_b32_sdwa v158,1,v158 dst_sel:DWORD dst_unused:UNUSED_PAD src0_sel:DWORD src1_sel:WORD_1
v_mfma_f32_16x16x32_bf16 v[136:139],v[64:67],v[20:23],v[112:115]
s_waitcnt lgkmcnt(9)
ds_read_u16 v152,v152
ds_read_b64_tr_b16 v[82:83],v4 offset:22560
v_lshlrev_b32_sdwa v157,1,v157 dst_sel:DWORD dst_unused:UNUSED_PAD src0_sel:DWORD src1_sel:WORD_1
v_mfma_f32_16x16x32_bf16 v[140:143],v[68:71],v[20:23],v[116:119]
ds_read_b64_tr_b16 v[84:85],v5 offset:20512
ds_read_b64_tr_b16 v[86:87],v5 offset:22560
v_lshlrev_b32_sdwa v159,1,v159 dst_sel:DWORD dst_unused:UNUSED_PAD src0_sel:DWORD src1_sel:WORD_1
v_mfma_f32_16x16x32_bf16 v[132:135],v[68:71],v[16:19],v[116:119]
s_waitcnt lgkmcnt(10)
ds_read_u16_d16_hi v154,v154
v_mfma_f32_16x16x32_bf16 v[128:131],v[72:75],v[24:27],v[128:131]
ds_read_u16 v153,v153
v_mfma_f32_16x16x32_bf16 v[136:139],v[72:75],v[28:31],v[136:139]
s_waitcnt lgkmcnt(8)
v_mfma_f32_16x16x32_bf16 v[140:143],v[76:79],v[28:31],v[140:143]
ds_read_u16_d16_hi v155,v155
v_lshl_add_u64 v[144:145],v[144:145],0,v[146:147]
v_mfma_f32_16x16x32_bf16 v[132:135],v[76:79],v[24:27],v[132:135]
s_waitcnt lgkmcnt(5)
ds_read_u16 v156,v156
v_mfma_f32_16x16x32_bf16 v[128:131],v[80:83],v[32:35],v[128:131]
v_lshl_add_u64 v[146:147],v[148:149],0,v[150:151]
v_mfma_f32_16x16x32_bf16 v[136:139],v[80:83],v[36:39],v[136:139]
s_waitcnt lgkmcnt(4)
ds_read_u16_d16_hi v158,v158
v_mfma_f32_16x16x32_bf16 v[140:143],v[84:87],v[36:39],v[140:143]
ds_read_u16 v157,v157
v_mfma_f32_16x16x32_bf16 v[132:135],v[84:87],v[32:35],v[132:135]
s_nop 0
v_lshlrev_b32_sdwa v128,1,v128 dst_sel:DWORD dst_unused:UNUSED_PAD src0_sel:DWORD src1_sel:WORD_1
v_lshlrev_b32_sdwa v130,1,v130 dst_sel:DWORD dst_unused:UNUSED_PAD src0_sel:DWORD src1_sel:WORD_1
v_mfma_f32_16x16x32_bf16 v[208:211],v[88:91],v[144:147],v[208:211]
ds_read_u16_d16_hi v159,v159
v_lshlrev_b32_sdwa v129,1,v129 dst_sel:DWORD dst_unused:UNUSED_PAD src0_sel:DWORD src1_sel:WORD_1
v_mfma_f32_16x16x32_bf16 v[216:219],v[92:95],v[144:147],v[216:219]
ds_read_u16 v128,v128
v_lshlrev_b32_sdwa v131,1,v131 dst_sel:DWORD dst_unused:UNUSED_PAD src0_sel:DWORD src1_sel:WORD_1
v_mfma_f32_16x16x32_bf16 v[224:227],v[96:99],v[144:147],v[224:227]
v_lshlrev_b32_sdwa v132,1,v132 dst_sel:DWORD dst_unused:UNUSED_PAD src0_sel:DWORD src1_sel:WORD_1
v_mfma_f32_16x16x32_bf16 v[232:235],v[100:103],v[144:147],v[232:235]
s_waitcnt lgkmcnt(5)
ds_read_u16_d16_hi v130,v130
v_lshl_add_u64 v[152:153],v[152:153],0,v[154:155]
v_lshlrev_b32_sdwa v134,1,v134 dst_sel:DWORD dst_unused:UNUSED_PAD src0_sel:DWORD src1_sel:WORD_1
v_mfma_f32_16x16x32_bf16 v[240:243],v[104:107],v[144:147],v[240:243]
s_waitcnt lgkmcnt(2)
v_lshl_add_u64 v[154:155],v[156:157],0,v[158:159]
v_mfma_f32_16x16x32_bf16 v[248:251],v[108:111],v[144:147],v[248:251]
ds_read_u16 v129,v129
v_lshlrev_b32_sdwa v133,1,v133 dst_sel:DWORD dst_unused:UNUSED_PAD src0_sel:DWORD src1_sel:WORD_1
v_mfma_f32_16x16x32_bf16 v[212:215],v[88:91],v[152:155],v[212:215]
ds_read_u16_d16_hi v131,v131
v_lshlrev_b32_sdwa v135,1,v135 dst_sel:DWORD dst_unused:UNUSED_PAD src0_sel:DWORD src1_sel:WORD_1
v_mfma_f32_16x16x32_bf16 v[220:223],v[92:95],v[152:155],v[220:223]
ds_read_b64_tr_b16 v[88:89],v6 offset:18480
ds_read_b64_tr_b16 v[90:91],v6 offset:20016
v_lshlrev_b32_sdwa v136,1,v136 dst_sel:DWORD dst_unused:UNUSED_PAD src0_sel:DWORD src1_sel:WORD_1
v_mfma_f32_16x16x32_bf16 v[228:231],v[96:99],v[152:155],v[228:231]
ds_read_u16 v132,v132
v_lshlrev_b32_sdwa v138,1,v138 dst_sel:DWORD dst_unused:UNUSED_PAD src0_sel:DWORD src1_sel:WORD_1
v_mfma_f32_16x16x32_bf16 v[236:239],v[100:103],v[152:155],v[236:239]
ds_read_u16_d16_hi v134,v134
v_lshlrev_b32_sdwa v137,1,v137 dst_sel:DWORD dst_unused:UNUSED_PAD src0_sel:DWORD src1_sel:WORD_1
v_mfma_f32_16x16x32_bf16 v[244:247],v[104:107],v[152:155],v[244:247]
ds_read_u16 v133,v133
v_lshlrev_b32_sdwa v139,1,v139 dst_sel:DWORD dst_unused:UNUSED_PAD src0_sel:DWORD src1_sel:WORD_1
v_mfma_f32_16x16x32_bf16 v[252:255],v[108:111],v[152:155],v[252:255]
ds_read_b64_tr_b16 v[92:93],v6 offset:18488
ds_read_b64_tr_b16 v[94:95],v6 offset:20024
v_lshlrev_b32_sdwa v140,1,v140 dst_sel:DWORD dst_unused:UNUSED_PAD src0_sel:DWORD src1_sel:WORD_1
v_mfma_f32_16x16x32_bf16 v[144:147],v[64:67],v[40:43],v[112:115]
ds_read_u16_d16_hi v135,v135
v_lshlrev_b32_sdwa v142,1,v142 dst_sel:DWORD dst_unused:UNUSED_PAD src0_sel:DWORD src1_sel:WORD_1
v_mfma_f32_16x16x32_bf16 v[152:155],v[64:67],v[44:47],v[112:115]
ds_read_u16 v136,v136
ds_read_b64_tr_b16 v[96:97],v6 offset:18544
v_lshlrev_b32_sdwa v141,1,v141 dst_sel:DWORD dst_unused:UNUSED_PAD src0_sel:DWORD src1_sel:WORD_1
v_mfma_f32_16x16x32_bf16 v[156:159],v[68:71],v[44:47],v[116:119]
ds_read_b64_tr_b16 v[98:99],v6 offset:20080
ds_read_b64_tr_b16 v[100:101],v6 offset:18552
v_mfma_f32_16x16x32_bf16 v[148:151],v[68:71],v[40:43],v[116:119]
ds_read_u16_d16_hi v138,v138
v_lshlrev_b32_sdwa v143,1,v143 dst_sel:DWORD dst_unused:UNUSED_PAD src0_sel:DWORD src1_sel:WORD_1
v_mfma_f32_16x16x32_bf16 v[144:147],v[72:75],v[48:51],v[144:147]
ds_read_u16 v137,v137
v_mfma_f32_16x16x32_bf16 v[152:155],v[72:75],v[52:55],v[152:155]
ds_read_b64_tr_b16 v[102:103],v6 offset:20088
ds_read_b64_tr_b16 v[104:105],v6 offset:18608
v_mfma_f32_16x16x32_bf16 v[156:159],v[76:79],v[52:55],v[156:159]
ds_read_u16_d16_hi v139,v139
v_mfma_f32_16x16x32_bf16 v[148:151],v[76:79],v[48:51],v[148:151]
s_waitcnt lgkmcnt(15)
ds_read_u16 v140,v140
v_lshl_add_u64 v[128:129],v[128:129],0,v[130:131]
v_mfma_f32_16x16x32_bf16 v[144:147],v[80:83],v[56:59],v[144:147]
ds_read_b64_tr_b16 v[106:107],v6 offset:20144
ds_read_b64_tr_b16 v[108:109],v6 offset:18616
v_mfma_f32_16x16x32_bf16 v[152:155],v[80:83],v[60:63],v[152:155]
s_waitcnt lgkmcnt(12)
ds_read_u16_d16_hi v142,v142
v_lshl_add_u64 v[130:131],v[132:133],0,v[134:135]
v_mfma_f32_16x16x32_bf16 v[156:159],v[84:87],v[60:63],v[156:159]
ds_read_u16 v141,v141
ds_read_u16_d16_hi v143,v143
v_mfma_f32_16x16x32_bf16 v[148:151],v[84:87],v[56:59],v[148:151]
ds_read_b64_tr_b16 v[110:111],v6 offset:20152
ds_read_b64_tr_b16 v[64:65],v4 offset:12832
v_lshlrev_b32_sdwa v144,1,v144 dst_sel:DWORD dst_unused:UNUSED_PAD src0_sel:DWORD src1_sel:WORD_1
v_lshlrev_b32_sdwa v146,1,v146 dst_sel:DWORD dst_unused:UNUSED_PAD src0_sel:DWORD src1_sel:WORD_1
v_mfma_f32_16x16x32_bf16 v[160:163],v[88:91],v[128:131],v[160:163]
v_lshlrev_b32_sdwa v145,1,v145 dst_sel:DWORD dst_unused:UNUSED_PAD src0_sel:DWORD src1_sel:WORD_1
v_mfma_f32_16x16x32_bf16 v[168:171],v[92:95],v[128:131],v[168:171]
s_waitcnt lgkmcnt(14)
ds_read_u16 v144,v144
v_lshlrev_b32_sdwa v147,1,v147 dst_sel:DWORD dst_unused:UNUSED_PAD src0_sel:DWORD src1_sel:WORD_1
v_mfma_f32_16x16x32_bf16 v[176:179],v[96:99],v[128:131],v[176:179]
s_waitcnt lgkmcnt(11)
ds_read_b64_tr_b16 v[66:67],v4 offset:14880
ds_read_b128 v[112:115],v7 offset:1152
v_mfma_f32_16x16x32_bf16 v[184:187],v[100:103],v[128:131],v[184:187]
s_waitcnt lgkmcnt(9)
ds_read_b64_tr_b16 v[68:69],v5 offset:12832
ds_read_u16_d16_hi v146,v146
v_lshlrev_b32_sdwa v148,1,v148 dst_sel:DWORD dst_unused:UNUSED_PAD src0_sel:DWORD src1_sel:WORD_1
v_lshl_add_u64 v[136:137],v[136:137],0,v[138:139]
v_lshlrev_b32_sdwa v150,1,v150 dst_sel:DWORD dst_unused:UNUSED_PAD src0_sel:DWORD src1_sel:WORD_1
v_mfma_f32_16x16x32_bf16 v[192:195],v[104:107],v[128:131],v[192:195]
s_waitcnt lgkmcnt(6)
ds_read_u16 v145,v145
ds_read_u16_d16_hi v147,v147
v_lshl_add_u64 v[138:139],v[140:141],0,v[142:143]
v_mfma_f32_16x16x32_bf16 v[200:203],v[108:111],v[128:131],v[200:203]
ds_read_b64_tr_b16 v[70:71],v5 offset:14880
ds_read_b128 v[116:119],v7 offset:1184
v_mfma_f32_16x16x32_bf16 v[164:167],v[88:91],v[136:139],v[164:167]
ds_read_u16 v148,v148
v_lshlrev_b32_sdwa v149,1,v149 dst_sel:DWORD dst_unused:UNUSED_PAD src0_sel:DWORD src1_sel:WORD_1
v_lshlrev_b32_sdwa v151,1,v151 dst_sel:DWORD dst_unused:UNUSED_PAD src0_sel:DWORD src1_sel:WORD_1
v_mfma_f32_16x16x32_bf16 v[172:175],v[92:95],v[136:139],v[172:175]
ds_read_b64_tr_b16 v[72:73],v4 offset:16928
v_lshlrev_b32_sdwa v152,1,v152 dst_sel:DWORD dst_unused:UNUSED_PAD src0_sel:DWORD src1_sel:WORD_1
v_mfma_f32_16x16x32_bf16 v[180:183],v[96:99],v[136:139],v[180:183]
ds_read_b64_tr_b16 v[74:75],v4 offset:18976
ds_read_b64_tr_b16 v[76:77],v5 offset:16928
v_lshlrev_b32_sdwa v154,1,v154 dst_sel:DWORD dst_unused:UNUSED_PAD src0_sel:DWORD src1_sel:WORD_1
v_mfma_f32_16x16x32_bf16 v[188:191],v[100:103],v[136:139],v[188:191]
ds_read_u16_d16_hi v150,v150
v_lshlrev_b32_sdwa v153,1,v153 dst_sel:DWORD dst_unused:UNUSED_PAD src0_sel:DWORD src1_sel:WORD_1
v_mfma_f32_16x16x32_bf16 v[196:199],v[104:107],v[136:139],v[196:199]
ds_read_u16 v149,v149
v_lshlrev_b32_sdwa v155,1,v155 dst_sel:DWORD dst_unused:UNUSED_PAD src0_sel:DWORD src1_sel:WORD_1
v_mfma_f32_16x16x32_bf16 v[204:207],v[108:111],v[136:139],v[204:207]
s_waitcnt lgkmcnt(12)
ds_read_b64_tr_b16 v[78:79],v5 offset:18976
ds_read_b64_tr_b16 v[80:81],v4 offset:21024
v_lshlrev_b32_sdwa v156,1,v156 dst_sel:DWORD dst_unused:UNUSED_PAD src0_sel:DWORD src1_sel:WORD_1
v_mfma_f32_16x16x32_bf16 v[128:131],v[64:67],v[16:19],v[112:115]
ds_read_u16_d16_hi v151,v151
v_lshlrev_b32_sdwa v158,1,v158 dst_sel:DWORD dst_unused:UNUSED_PAD src0_sel:DWORD src1_sel:WORD_1
v_mfma_f32_16x16x32_bf16 v[136:139],v[64:67],v[20:23],v[112:115]
s_waitcnt lgkmcnt(9)
ds_read_u16 v152,v152
ds_read_b64_tr_b16 v[82:83],v4 offset:23072
v_lshlrev_b32_sdwa v157,1,v157 dst_sel:DWORD dst_unused:UNUSED_PAD src0_sel:DWORD src1_sel:WORD_1
v_mfma_f32_16x16x32_bf16 v[140:143],v[68:71],v[20:23],v[116:119]
ds_read_b64_tr_b16 v[84:85],v5 offset:21024
ds_read_b64_tr_b16 v[86:87],v5 offset:23072
v_lshlrev_b32_sdwa v159,1,v159 dst_sel:DWORD dst_unused:UNUSED_PAD src0_sel:DWORD src1_sel:WORD_1
v_mfma_f32_16x16x32_bf16 v[132:135],v[68:71],v[16:19],v[116:119]
s_waitcnt lgkmcnt(10)
ds_read_u16_d16_hi v154,v154
v_mfma_f32_16x16x32_bf16 v[128:131],v[72:75],v[24:27],v[128:131]
ds_read_u16 v153,v153
s_mov_b32 s86,0xbe801d5a
v_mfma_f32_16x16x32_bf16 v[136:139],v[72:75],v[28:31],v[136:139]
s_waitcnt lgkmcnt(8)
v_mfma_f32_16x16x32_bf16 v[140:143],v[76:79],v[28:31],v[140:143]
ds_read_u16_d16_hi v155,v155
v_lshl_add_u64 v[144:145],v[144:145],0,v[146:147]
v_mfma_f32_16x16x32_bf16 v[132:135],v[76:79],v[24:27],v[132:135]
s_waitcnt lgkmcnt(5)
ds_read_u16 v156,v156
v_mfma_f32_16x16x32_bf16 v[128:131],v[80:83],v[32:35],v[128:131]
v_lshl_add_u64 v[146:147],v[148:149],0,v[150:151]
v_mfma_f32_16x16x32_bf16 v[136:139],v[80:83],v[36:39],v[136:139]
s_waitcnt lgkmcnt(4)
ds_read_u16_d16_hi v158,v158
v_mfma_f32_16x16x32_bf16 v[140:143],v[84:87],v[36:39],v[140:143]
ds_read_u16 v157,v157
v_mfma_f32_16x16x32_bf16 v[132:135],v[84:87],v[32:35],v[132:135]
s_nop 0
v_lshlrev_b32_sdwa v128,1,v128 dst_sel:DWORD dst_unused:UNUSED_PAD src0_sel:DWORD src1_sel:WORD_1
v_lshlrev_b32_sdwa v130,1,v130 dst_sel:DWORD dst_unused:UNUSED_PAD src0_sel:DWORD src1_sel:WORD_1
v_mfma_f32_16x16x32_bf16 v[208:211],v[88:91],v[144:147],v[208:211]
s_barrier
ds_read_u16_d16_hi v159,v159
v_lshlrev_b32_sdwa v129,1,v129 dst_sel:DWORD dst_unused:UNUSED_PAD src0_sel:DWORD src1_sel:WORD_1
v_lshlrev_b32_sdwa v131,1,v131 dst_sel:DWORD dst_unused:UNUSED_PAD src0_sel:DWORD src1_sel:WORD_1
v_mfma_f32_16x16x32_bf16 v[216:219],v[92:95],v[144:147],v[216:219]
v_fma_mixlo_f16 v120,v141,s99,0
v_lshlrev_b32_sdwa v132,1,v132 dst_sel:DWORD dst_unused:UNUSED_PAD src0_sel:DWORD src1_sel:WORD_1
v_mfma_f32_16x16x32_bf16 v[224:227],v[96:99],v[144:147],v[224:227]
ds_read_u16 v128,v128
v_fma_mixhi_f16 v120,v143,s99,0
v_lshlrev_b32_sdwa v134,1,v134 dst_sel:DWORD dst_unused:UNUSED_PAD src0_sel:DWORD src1_sel:WORD_1
v_mfma_f32_16x16x32_bf16 v[232:235],v[100:103],v[144:147],v[232:235]
s_waitcnt lgkmcnt(5)
ds_read_u16_d16_hi v130,v130
v_lshl_add_u64 v[152:153],v[152:153],0,v[154:155]
v_mfma_f32_16x16x32_bf16 v[240:243],v[104:107],v[144:147],v[240:243]
s_waitcnt lgkmcnt(2)
ds_read_u16 v129,v129
v_lshl_add_u64 v[154:155],v[156:157],0,v[158:159]
v_mfma_f32_16x16x32_bf16 v[248:251],v[108:111],v[144:147],v[248:251]
v_pk_fma_f16 v121,v120,v120,s54
v_lshlrev_b32_sdwa v133,1,v133 dst_sel:DWORD dst_unused:UNUSED_PAD src0_sel:DWORD src1_sel:WORD_1
v_mfma_f32_16x16x32_bf16 v[212:215],v[88:91],v[152:155],v[212:215]
ds_read_u16_d16_hi v131,v131
ds_read_b64_tr_b16 v[88:89],v6 offset:32832
v_pk_mul_f16 v121,v120,v121
v_lshlrev_b32_sdwa v135,1,v135 dst_sel:DWORD dst_unused:UNUSED_PAD src0_sel:DWORD src1_sel:WORD_1
v_mfma_f32_16x16x32_bf16 v[220:223],v[92:95],v[152:155],v[220:223]
ds_read_u16 v132,v132
v_exp_f16 v120,v121
v_mfma_f32_16x16x32_bf16 v[228:231],v[96:99],v[152:155],v[228:231]
ds_read_b64_tr_b16 v[90:91],v6 offset:34368
ds_read_b64_tr_b16 v[92:93],v6 offset:32840
v_lshlrev_b32_sdwa v136,1,v136 dst_sel:DWORD dst_unused:UNUSED_PAD src0_sel:DWORD src1_sel:WORD_1
v_mfma_f32_16x16x32_bf16 v[236:239],v[100:103],v[152:155],v[236:239]
ds_read_u16_d16_hi v134,v134
v_exp_f16_sdwa v120,v121 dst_sel:WORD_1 dst_unused:UNUSED_PRESERVE src0_sel:WORD_1
v_mfma_f32_16x16x32_bf16 v[244:247],v[104:107],v[152:155],v[244:247]
v_lshlrev_b32_sdwa v138,1,v138 dst_sel:DWORD dst_unused:UNUSED_PAD src0_sel:DWORD src1_sel:WORD_1
v_mfma_f32_16x16x32_bf16 v[252:255],v[108:111],v[152:155],v[252:255]
ds_read_b64_tr_b16 v[94:95],v6 offset:34376
ds_read_b64_tr_b16 v[96:97],v6 offset:32896
v_pk_fma_f16 v120,v120,s55,s55
v_lshlrev_b32_sdwa v137,1,v137 dst_sel:DWORD dst_unused:UNUSED_PAD src0_sel:DWORD src1_sel:WORD_1
v_mfma_f32_16x16x32_bf16 v[144:147],v[64:67],v[40:43],v[112:115]
ds_read_u16 v133,v133
v_rcp_f16 v121,v120
v_mfma_f32_16x16x32_bf16 v[152:155],v[64:67],v[44:47],v[112:115]
ds_read_b64_tr_b16 v[98:99],v6 offset:34432
ds_read_b64_tr_b16 v[100:101],v6 offset:32904
v_lshlrev_b32_sdwa v139,1,v139 dst_sel:DWORD dst_unused:UNUSED_PAD src0_sel:DWORD src1_sel:WORD_1
v_mfma_f32_16x16x32_bf16 v[156:159],v[68:71],v[44:47],v[116:119]
ds_read_u16_d16_hi v135,v135
v_rcp_f16_sdwa v121,v120 dst_sel:WORD_1 dst_unused:UNUSED_PRESERVE src0_sel:WORD_1
v_mfma_f32_16x16x32_bf16 v[148:151],v[68:71],v[40:43],v[116:119]
ds_read_u16 v136,v136
v_lshlrev_b32_sdwa v140,1,v140 dst_sel:DWORD dst_unused:UNUSED_PAD src0_sel:DWORD src1_sel:WORD_1
v_mfma_f32_16x16x32_bf16 v[144:147],v[72:75],v[48:51],v[144:147]
ds_read_b64_tr_b16 v[102:103],v6 offset:34440
ds_read_b64_tr_b16 v[104:105],v6 offset:32960
v_fma_mix_f32 v141,v141,v121,0 op_sel_hi:[0,1,0]
v_lshlrev_b32_sdwa v142,1,v142 dst_sel:DWORD dst_unused:UNUSED_PAD src0_sel:DWORD src1_sel:WORD_1
v_mfma_f32_16x16x32_bf16 v[152:155],v[72:75],v[52:55],v[152:155]
ds_read_u16_d16_hi v138,v138
v_fma_mix_f32 v143,v143,v121,0 op_sel:[0,1,0] op_sel_hi:[0,1,0]
v_mfma_f32_16x16x32_bf16 v[156:159],v[76:79],v[52:55],v[156:159]
ds_read_b64_tr_b16 v[106:107],v6 offset:34496
ds_read_b64_tr_b16 v[108:109],v6 offset:32968
v_mfma_f32_16x16x32_bf16 v[148:151],v[76:79],v[48:51],v[148:151]
ds_read_u16 v137,v137
v_mfma_f32_16x16x32_bf16 v[144:147],v[80:83],v[56:59],v[144:147]
ds_read_b64_tr_b16 v[110:111],v6 offset:34504
ds_read_b64_tr_b16 v[64:65],v8 offset:0
v_mfma_f32_16x16x32_bf16 v[152:155],v[80:83],v[60:63],v[152:155]
s_waitcnt lgkmcnt(9)
ds_read_u16_d16_hi v139,v139
v_lshl_add_u64 v[128:129],v[128:129],0,v[130:131]
v_lshl_add_u64 v[130:131],v[132:133],0,v[134:135]
v_mfma_f32_16x16x32_bf16 v[156:159],v[84:87],v[60:63],v[156:159]
ds_read_u16 v140,v140
v_mfma_f32_16x16x32_bf16 v[148:151],v[84:87],v[56:59],v[148:151]
ds_read_b64_tr_b16 v[66:67],v8 offset:2048
ds_read_b128 v[112:115],v7 offset:1280
v_lshlrev_b32_sdwa v144,1,v144 dst_sel:DWORD dst_unused:UNUSED_PAD src0_sel:DWORD src1_sel:WORD_1
v_lshlrev_b32_sdwa v146,1,v146 dst_sel:DWORD dst_unused:UNUSED_PAD src0_sel:DWORD src1_sel:WORD_1
v_mfma_f32_16x16x32_bf16 v[160:163],v[88:91],v[128:131],v[160:163]
ds_read_u16_d16_hi v142,v142
v_lshlrev_b32_sdwa v145,1,v145 dst_sel:DWORD dst_unused:UNUSED_PAD src0_sel:DWORD src1_sel:WORD_1
v_lshlrev_b32_sdwa v147,1,v147 dst_sel:DWORD dst_unused:UNUSED_PAD src0_sel:DWORD src1_sel:WORD_1
v_mfma_f32_16x16x32_bf16 v[168:171],v[92:95],v[128:131],v[168:171]
ds_read_b64_tr_b16 v[68:69],v9 offset:0
ds_read_b64_tr_b16 v[70:71],v9 offset:2048
v_fma_mixlo_f16 v120,v157,s99,0
v_mfma_f32_16x16x32_bf16 v[176:179],v[96:99],v[128:131],v[176:179]
s_waitcnt lgkmcnt(6)
ds_read_u16 v144,v144
v_lshlrev_b32_sdwa v148,1,v148 dst_sel:DWORD dst_unused:UNUSED_PAD src0_sel:DWORD src1_sel:WORD_1
v_lshl_add_u64 v[136:137],v[136:137],0,v[138:139]
v_fma_mixhi_f16 v120,v159,s99,0
v_lshlrev_b32_sdwa v150,1,v150 dst_sel:DWORD dst_unused:UNUSED_PAD src0_sel:DWORD src1_sel:WORD_1
v_mfma_f32_16x16x32_bf16 v[184:187],v[100:103],v[128:131],v[184:187]
ds_read_u16_d16_hi v146,v146
v_mfma_f32_16x16x32_bf16 v[192:195],v[104:107],v[128:131],v[192:195]
s_waitcnt lgkmcnt(4)
ds_read_b128 v[116:119],v7 offset:1312
ds_read_b64_tr_b16 v[72:73],v8 offset:4096
v_or_b32 v138,v142,v140
v_cvt_pk_bf16_f32 v139,v141,v143
v_mfma_f32_16x16x32_bf16 v[200:203],v[108:111],v[128:131],v[200:203]
ds_read_b64_tr_b16 v[74:75],v8 offset:6144
ds_read_b64_tr_b16 v[76:77],v9 offset:4096
v_pk_fma_f16 v121,v120,v120,s54
v_lshlrev_b32_sdwa v149,1,v149 dst_sel:DWORD dst_unused:UNUSED_PAD src0_sel:DWORD src1_sel:WORD_1
v_mfma_f32_16x16x32_bf16 v[164:167],v[88:91],v[136:139],v[164:167]
ds_read_u16 v145,v145
v_pk_mul_f16 v121,v120,v121
v_lshlrev_b32_sdwa v151,1,v151 dst_sel:DWORD dst_unused:UNUSED_PAD src0_sel:DWORD src1_sel:WORD_1
v_mfma_f32_16x16x32_bf16 v[172:175],v[92:95],v[136:139],v[172:175]
ds_read_u16_d16_hi v147,v147
v_lshlrev_b32_sdwa v152,1,v152 dst_sel:DWORD dst_unused:UNUSED_PAD src0_sel:DWORD src1_sel:WORD_1
v_exp_f16 v120,v121
v_mfma_f32_16x16x32_bf16 v[180:183],v[96:99],v[136:139],v[180:183]
ds_read_b64_tr_b16 v[78:79],v9 offset:6144
ds_read_b64_tr_b16 v[80:81],v8 offset:8192
v_mfma_f32_16x16x32_bf16 v[188:191],v[100:103],v[136:139],v[188:191]
v_exp_f16_sdwa v120,v121 dst_sel:WORD_1 dst_unused:UNUSED_PRESERVE src0_sel:WORD_1
v_mfma_f32_16x16x32_bf16 v[196:199],v[104:107],v[136:139],v[196:199]
ds_read_u16 v148,v148
ds_read_u16_d16_hi v150,v150
v_lshlrev_b32_sdwa v154,1,v154 dst_sel:DWORD dst_unused:UNUSED_PAD src0_sel:DWORD src1_sel:WORD_1
v_mfma_f32_16x16x32_bf16 v[204:207],v[108:111],v[136:139],v[204:207]
v_pk_fma_f16 v120,v120,s55,s55
v_lshlrev_b32_sdwa v153,1,v153 dst_sel:DWORD dst_unused:UNUSED_PAD src0_sel:DWORD src1_sel:WORD_1
v_mfma_f32_16x16x32_bf16 v[128:131],v[64:67],v[16:19],v[112:115]
ds_read_u16 v149,v149
ds_read_b64_tr_b16 v[82:83],v8 offset:10240
v_rcp_f16 v121,v120
v_mfma_f32_16x16x32_bf16 v[136:139],v[64:67],v[20:23],v[112:115]
s_waitcnt lgkmcnt(11)
ds_read_b64_tr_b16 v[84:85],v9 offset:8192
v_lshlrev_b32_sdwa v155,1,v155 dst_sel:DWORD dst_unused:UNUSED_PAD src0_sel:DWORD src1_sel:WORD_1
v_mfma_f32_16x16x32_bf16 v[140:143],v[68:71],v[20:23],v[116:119]
ds_read_u16_d16_hi v151,v151
v_rcp_f16_sdwa v121,v120 dst_sel:WORD_1 dst_unused:UNUSED_PRESERVE src0_sel:WORD_1
v_mfma_f32_16x16x32_bf16 v[132:135],v[68:71],v[16:19],v[116:119]
s_waitcnt lgkmcnt(11)
ds_read_u16 v152,v152
v_lshlrev_b32_sdwa v156,1,v156 dst_sel:DWORD dst_unused:UNUSED_PAD src0_sel:DWORD src1_sel:WORD_1
v_mfma_f32_16x16x32_bf16 v[128:131],v[72:75],v[24:27],v[128:131]
ds_read_b64_tr_b16 v[86:87],v9 offset:10240
v_fma_mix_f32 v157,v157,v121,0 op_sel_hi:[0,1,0]
v_mfma_f32_16x16x32_bf16 v[136:139],v[72:75],v[28:31],v[136:139]
s_waitcnt lgkmcnt(9)
ds_read_u16_d16_hi v154,v154
v_fma_mix_f32 v159,v159,v121,0 op_sel:[0,1,0] op_sel_hi:[0,1,0]
v_mfma_f32_16x16x32_bf16 v[140:143],v[76:79],v[28:31],v[140:143]
v_mfma_f32_16x16x32_bf16 v[132:135],v[76:79],v[24:27],v[132:135]
s_waitcnt lgkmcnt(5)
ds_read_u16 v153,v153
v_lshlrev_b32_sdwa v158,1,v158 dst_sel:DWORD dst_unused:UNUSED_PAD src0_sel:DWORD src1_sel:WORD_1
v_mfma_f32_16x16x32_bf16 v[128:131],v[80:83],v[32:35],v[128:131]
ds_read_u16_d16_hi v155,v155
v_mfma_f32_16x16x32_bf16 v[136:139],v[80:83],v[36:39],v[136:139]
s_waitcnt lgkmcnt(3)
v_lshl_add_u64 v[144:145],v[144:145],0,v[146:147]
v_mfma_f32_16x16x32_bf16 v[140:143],v[84:87],v[36:39],v[140:143]
ds_read_u16 v156,v156
v_lshl_add_u64 v[146:147],v[148:149],0,v[150:151]
v_mfma_f32_16x16x32_bf16 v[132:135],v[84:87],v[32:35],v[132:135]
v_lshlrev_b32_sdwa v128,1,v128 dst_sel:DWORD dst_unused:UNUSED_PAD src0_sel:DWORD src1_sel:WORD_1
v_lshlrev_b32_sdwa v130,1,v130 dst_sel:DWORD dst_unused:UNUSED_PAD src0_sel:DWORD src1_sel:WORD_1
v_mfma_f32_16x16x32_bf16 v[208:211],v[88:91],v[144:147],v[208:211]
ds_read_u16_d16_hi v158,v158
v_lshlrev_b32_sdwa v129,1,v129 dst_sel:DWORD dst_unused:UNUSED_PAD src0_sel:DWORD src1_sel:WORD_1
v_lshlrev_b32_sdwa v131,1,v131 dst_sel:DWORD dst_unused:UNUSED_PAD src0_sel:DWORD src1_sel:WORD_1
v_mfma_f32_16x16x32_bf16 v[216:219],v[92:95],v[144:147],v[216:219]
v_fma_mixlo_f16 v120,v141,s99,0
v_lshlrev_b32_sdwa v132,1,v132 dst_sel:DWORD dst_unused:UNUSED_PAD src0_sel:DWORD src1_sel:WORD_1
v_mfma_f32_16x16x32_bf16 v[224:227],v[96:99],v[144:147],v[224:227]
ds_read_u16 v128,v128
v_fma_mixhi_f16 v120,v143,s99,0
v_lshlrev_b32_sdwa v134,1,v134 dst_sel:DWORD dst_unused:UNUSED_PAD src0_sel:DWORD src1_sel:WORD_1
v_mfma_f32_16x16x32_bf16 v[232:235],v[100:103],v[144:147],v[232:235]
s_waitcnt lgkmcnt(3)
ds_read_u16_d16_hi v130,v130
v_lshl_add_u64 v[152:153],v[152:153],0,v[154:155]
v_mfma_f32_16x16x32_bf16 v[240:243],v[104:107],v[144:147],v[240:243]
s_waitcnt lgkmcnt(2)
ds_read_u16 v129,v129
v_or_b32 v154,v158,v156
v_cvt_pk_bf16_f32 v155,v157,v159
v_mfma_f32_16x16x32_bf16 v[248:251],v[108:111],v[144:147],v[248:251]
v_pk_fma_f16 v121,v120,v120,s54
v_lshlrev_b32_sdwa v133,1,v133 dst_sel:DWORD dst_unused:UNUSED_PAD src0_sel:DWORD src1_sel:WORD_1
v_mfma_f32_16x16x32_bf16 v[212:215],v[88:91],v[152:155],v[212:215]
ds_read_u16_d16_hi v131,v131
ds_read_b64_tr_b16 v[88:89],v6 offset:38992
v_pk_mul_f16 v121,v120,v121
v_lshlrev_b32_sdwa v135,1,v135 dst_sel:DWORD dst_unused:UNUSED_PAD src0_sel:DWORD src1_sel:WORD_1
v_mfma_f32_16x16x32_bf16 v[220:223],v[92:95],v[152:155],v[220:223]
ds_read_u16 v132,v132
v_exp_f16 v120,v121
v_mfma_f32_16x16x32_bf16 v[228:231],v[96:99],v[152:155],v[228:231]
ds_read_b64_tr_b16 v[90:91],v6 offset:40528
ds_read_b64_tr_b16 v[92:93],v6 offset:39000
v_lshlrev_b32_sdwa v136,1,v136 dst_sel:DWORD dst_unused:UNUSED_PAD src0_sel:DWORD src1_sel:WORD_1
v_mfma_f32_16x16x32_bf16 v[236:239],v[100:103],v[152:155],v[236:239]
ds_read_u16_d16_hi v134,v134
v_exp_f16_sdwa v120,v121 dst_sel:WORD_1 dst_unused:UNUSED_PRESERVE src0_sel:WORD_1
v_mfma_f32_16x16x32_bf16 v[244:247],v[104:107],v[152:155],v[244:247]
v_lshlrev_b32_sdwa v138,1,v138 dst_sel:DWORD dst_unused:UNUSED_PAD src0_sel:DWORD src1_sel:WORD_1
v_mfma_f32_16x16x32_bf16 v[252:255],v[108:111],v[152:155],v[252:255]
ds_read_b64_tr_b16 v[94:95],v6 offset:40536
ds_read_b64_tr_b16 v[96:97],v6 offset:39056
v_pk_fma_f16 v120,v120,s55,s55
v_lshlrev_b32_sdwa v137,1,v137 dst_sel:DWORD dst_unused:UNUSED_PAD src0_sel:DWORD src1_sel:WORD_1
v_mfma_f32_16x16x32_bf16 v[144:147],v[64:67],v[40:43],v[112:115]
ds_read_u16 v133,v133
v_rcp_f16 v121,v120
v_mfma_f32_16x16x32_bf16 v[152:155],v[64:67],v[44:47],v[112:115]
ds_read_b64_tr_b16 v[98:99],v6 offset:40592
ds_read_b64_tr_b16 v[100:101],v6 offset:39064
v_lshlrev_b32_sdwa v139,1,v139 dst_sel:DWORD dst_unused:UNUSED_PAD src0_sel:DWORD src1_sel:WORD_1
v_mfma_f32_16x16x32_bf16 v[156:159],v[68:71],v[44:47],v[116:119]
ds_read_u16_d16_hi v135,v135
v_rcp_f16_sdwa v121,v120 dst_sel:WORD_1 dst_unused:UNUSED_PRESERVE src0_sel:WORD_1
v_mfma_f32_16x16x32_bf16 v[148:151],v[68:71],v[40:43],v[116:119]
ds_read_u16 v136,v136
v_lshlrev_b32_sdwa v140,1,v140 dst_sel:DWORD dst_unused:UNUSED_PAD src0_sel:DWORD src1_sel:WORD_1
v_mfma_f32_16x16x32_bf16 v[144:147],v[72:75],v[48:51],v[144:147]
ds_read_b64_tr_b16 v[102:103],v6 offset:40600
ds_read_b64_tr_b16 v[104:105],v6 offset:39120
v_fma_mix_f32 v141,v141,v121,0 op_sel_hi:[0,1,0]
v_lshlrev_b32_sdwa v142,1,v142 dst_sel:DWORD dst_unused:UNUSED_PAD src0_sel:DWORD src1_sel:WORD_1
v_mfma_f32_16x16x32_bf16 v[152:155],v[72:75],v[52:55],v[152:155]
ds_read_u16_d16_hi v138,v138
v_fma_mix_f32 v143,v143,v121,0 op_sel:[0,1,0] op_sel_hi:[0,1,0]
v_mfma_f32_16x16x32_bf16 v[156:159],v[76:79],v[52:55],v[156:159]
ds_read_b64_tr_b16 v[106:107],v6 offset:40656
ds_read_b64_tr_b16 v[108:109],v6 offset:39128
v_mfma_f32_16x16x32_bf16 v[148:151],v[76:79],v[48:51],v[148:151]
ds_read_u16 v137,v137
v_mfma_f32_16x16x32_bf16 v[144:147],v[80:83],v[56:59],v[144:147]
ds_read_b64_tr_b16 v[110:111],v6 offset:40664
ds_read_b64_tr_b16 v[64:65],v8 offset:512
v_mfma_f32_16x16x32_bf16 v[152:155],v[80:83],v[60:63],v[152:155]
s_waitcnt lgkmcnt(9)
ds_read_u16_d16_hi v139,v139
v_lshl_add_u64 v[128:129],v[128:129],0,v[130:131]
v_lshl_add_u64 v[130:131],v[132:133],0,v[134:135]
v_mfma_f32_16x16x32_bf16 v[156:159],v[84:87],v[60:63],v[156:159]
ds_read_u16 v140,v140
v_mfma_f32_16x16x32_bf16 v[148:151],v[84:87],v[56:59],v[148:151]
ds_read_b64_tr_b16 v[66:67],v8 offset:2560
ds_read_b128 v[112:115],v7 offset:1408
v_lshlrev_b32_sdwa v144,1,v144 dst_sel:DWORD dst_unused:UNUSED_PAD src0_sel:DWORD src1_sel:WORD_1
v_lshlrev_b32_sdwa v146,1,v146 dst_sel:DWORD dst_unused:UNUSED_PAD src0_sel:DWORD src1_sel:WORD_1
v_mfma_f32_16x16x32_bf16 v[160:163],v[88:91],v[128:131],v[160:163]
ds_read_u16_d16_hi v142,v142
v_lshlrev_b32_sdwa v145,1,v145 dst_sel:DWORD dst_unused:UNUSED_PAD src0_sel:DWORD src1_sel:WORD_1
v_lshlrev_b32_sdwa v147,1,v147 dst_sel:DWORD dst_unused:UNUSED_PAD src0_sel:DWORD src1_sel:WORD_1
v_mfma_f32_16x16x32_bf16 v[168:171],v[92:95],v[128:131],v[168:171]
ds_read_b64_tr_b16 v[68:69],v9 offset:512
ds_read_b64_tr_b16 v[70:71],v9 offset:2560
v_fma_mixlo_f16 v120,v157,s99,0
v_mfma_f32_16x16x32_bf16 v[176:179],v[96:99],v[128:131],v[176:179]
s_waitcnt lgkmcnt(6)
ds_read_u16 v144,v144
v_lshlrev_b32_sdwa v148,1,v148 dst_sel:DWORD dst_unused:UNUSED_PAD src0_sel:DWORD src1_sel:WORD_1
v_lshl_add_u64 v[136:137],v[136:137],0,v[138:139]
v_fma_mixhi_f16 v120,v159,s99,0
v_lshlrev_b32_sdwa v150,1,v150 dst_sel:DWORD dst_unused:UNUSED_PAD src0_sel:DWORD src1_sel:WORD_1
v_mfma_f32_16x16x32_bf16 v[184:187],v[100:103],v[128:131],v[184:187]
ds_read_u16_d16_hi v146,v146
v_mfma_f32_16x16x32_bf16 v[192:195],v[104:107],v[128:131],v[192:195]
s_waitcnt lgkmcnt(4)
ds_read_b128 v[116:119],v7 offset:1440
ds_read_b64_tr_b16 v[72:73],v8 offset:4608
v_or_b32 v138,v142,v140
v_cvt_pk_bf16_f32 v139,v141,v143
v_mfma_f32_16x16x32_bf16 v[200:203],v[108:111],v[128:131],v[200:203]
ds_read_b64_tr_b16 v[74:75],v8 offset:6656
ds_read_b64_tr_b16 v[76:77],v9 offset:4608
v_pk_fma_f16 v121,v120,v120,s54
v_lshlrev_b32_sdwa v149,1,v149 dst_sel:DWORD dst_unused:UNUSED_PAD src0_sel:DWORD src1_sel:WORD_1
v_mfma_f32_16x16x32_bf16 v[164:167],v[88:91],v[136:139],v[164:167]
ds_read_u16 v145,v145
v_pk_mul_f16 v121,v120,v121
v_lshlrev_b32_sdwa v151,1,v151 dst_sel:DWORD dst_unused:UNUSED_PAD src0_sel:DWORD src1_sel:WORD_1
v_mfma_f32_16x16x32_bf16 v[172:175],v[92:95],v[136:139],v[172:175]
ds_read_u16_d16_hi v147,v147
v_lshlrev_b32_sdwa v152,1,v152 dst_sel:DWORD dst_unused:UNUSED_PAD src0_sel:DWORD src1_sel:WORD_1
v_exp_f16 v120,v121
v_mfma_f32_16x16x32_bf16 v[180:183],v[96:99],v[136:139],v[180:183]
ds_read_b64_tr_b16 v[78:79],v9 offset:6656
ds_read_b64_tr_b16 v[80:81],v8 offset:8704
v_mfma_f32_16x16x32_bf16 v[188:191],v[100:103],v[136:139],v[188:191]
v_exp_f16_sdwa v120,v121 dst_sel:WORD_1 dst_unused:UNUSED_PRESERVE src0_sel:WORD_1
v_mfma_f32_16x16x32_bf16 v[196:199],v[104:107],v[136:139],v[196:199]
ds_read_u16 v148,v148
ds_read_u16_d16_hi v150,v150
v_lshlrev_b32_sdwa v154,1,v154 dst_sel:DWORD dst_unused:UNUSED_PAD src0_sel:DWORD src1_sel:WORD_1
v_mfma_f32_16x16x32_bf16 v[204:207],v[108:111],v[136:139],v[204:207]
v_pk_fma_f16 v120,v120,s55,s55
v_lshlrev_b32_sdwa v153,1,v153 dst_sel:DWORD dst_unused:UNUSED_PAD src0_sel:DWORD src1_sel:WORD_1
v_mfma_f32_16x16x32_bf16 v[128:131],v[64:67],v[16:19],v[112:115]
ds_read_u16 v149,v149
ds_read_b64_tr_b16 v[82:83],v8 offset:10752
v_rcp_f16 v121,v120
v_mfma_f32_16x16x32_bf16 v[136:139],v[64:67],v[20:23],v[112:115]
s_waitcnt lgkmcnt(11)
ds_read_b64_tr_b16 v[84:85],v9 offset:8704
v_lshlrev_b32_sdwa v155,1,v155 dst_sel:DWORD dst_unused:UNUSED_PAD src0_sel:DWORD src1_sel:WORD_1
v_mfma_f32_16x16x32_bf16 v[140:143],v[68:71],v[20:23],v[116:119]
ds_read_u16_d16_hi v151,v151
v_rcp_f16_sdwa v121,v120 dst_sel:WORD_1 dst_unused:UNUSED_PRESERVE src0_sel:WORD_1
v_mfma_f32_16x16x32_bf16 v[132:135],v[68:71],v[16:19],v[116:119]
s_waitcnt lgkmcnt(11)
ds_read_u16 v152,v152
v_lshlrev_b32_sdwa v156,1,v156 dst_sel:DWORD dst_unused:UNUSED_PAD src0_sel:DWORD src1_sel:WORD_1
v_mfma_f32_16x16x32_bf16 v[128:131],v[72:75],v[24:27],v[128:131]
ds_read_b64_tr_b16 v[86:87],v9 offset:10752
v_fma_mix_f32 v157,v157,v121,0 op_sel_hi:[0,1,0]
v_mfma_f32_16x16x32_bf16 v[136:139],v[72:75],v[28:31],v[136:139]
s_waitcnt lgkmcnt(9)
ds_read_u16_d16_hi v154,v154
v_fma_mix_f32 v159,v159,v121,0 op_sel:[0,1,0] op_sel_hi:[0,1,0]
v_mfma_f32_16x16x32_bf16 v[140:143],v[76:79],v[28:31],v[140:143]
v_mfma_f32_16x16x32_bf16 v[132:135],v[76:79],v[24:27],v[132:135]
s_waitcnt lgkmcnt(5)
ds_read_u16 v153,v153
v_lshlrev_b32_sdwa v158,1,v158 dst_sel:DWORD dst_unused:UNUSED_PAD src0_sel:DWORD src1_sel:WORD_1
v_mfma_f32_16x16x32_bf16 v[128:131],v[80:83],v[32:35],v[128:131]
ds_read_u16_d16_hi v155,v155
v_mfma_f32_16x16x32_bf16 v[136:139],v[80:83],v[36:39],v[136:139]
s_waitcnt lgkmcnt(3)
v_lshl_add_u64 v[144:145],v[144:145],0,v[146:147]
v_mfma_f32_16x16x32_bf16 v[140:143],v[84:87],v[36:39],v[140:143]
ds_read_u16 v156,v156
v_lshl_add_u64 v[146:147],v[148:149],0,v[150:151]
v_mfma_f32_16x16x32_bf16 v[132:135],v[84:87],v[32:35],v[132:135]
v_lshlrev_b32_sdwa v128,1,v128 dst_sel:DWORD dst_unused:UNUSED_PAD src0_sel:DWORD src1_sel:WORD_1
v_lshlrev_b32_sdwa v130,1,v130 dst_sel:DWORD dst_unused:UNUSED_PAD src0_sel:DWORD src1_sel:WORD_1
v_mfma_f32_16x16x32_bf16 v[208:211],v[88:91],v[144:147],v[208:211]
ds_read_u16_d16_hi v158,v158
v_lshlrev_b32_sdwa v129,1,v129 dst_sel:DWORD dst_unused:UNUSED_PAD src0_sel:DWORD src1_sel:WORD_1
v_lshlrev_b32_sdwa v131,1,v131 dst_sel:DWORD dst_unused:UNUSED_PAD src0_sel:DWORD src1_sel:WORD_1
v_mfma_f32_16x16x32_bf16 v[216:219],v[92:95],v[144:147],v[216:219]
v_fma_mixlo_f16 v120,v141,s99,0
v_lshlrev_b32_sdwa v132,1,v132 dst_sel:DWORD dst_unused:UNUSED_PAD src0_sel:DWORD src1_sel:WORD_1
v_mfma_f32_16x16x32_bf16 v[224:227],v[96:99],v[144:147],v[224:227]
ds_read_u16 v128,v128
v_fma_mixhi_f16 v120,v143,s99,0
v_lshlrev_b32_sdwa v134,1,v134 dst_sel:DWORD dst_unused:UNUSED_PAD src0_sel:DWORD src1_sel:WORD_1
v_mfma_f32_16x16x32_bf16 v[232:235],v[100:103],v[144:147],v[232:235]
s_waitcnt lgkmcnt(3)
ds_read_u16_d16_hi v130,v130
v_lshl_add_u64 v[152:153],v[152:153],0,v[154:155]
v_mfma_f32_16x16x32_bf16 v[240:243],v[104:107],v[144:147],v[240:243]
s_waitcnt lgkmcnt(2)
ds_read_u16 v129,v129
v_or_b32 v154,v158,v156
v_cvt_pk_bf16_f32 v155,v157,v159
v_mfma_f32_16x16x32_bf16 v[248:251],v[108:111],v[144:147],v[248:251]
v_pk_fma_f16 v121,v120,v120,s54
v_lshlrev_b32_sdwa v133,1,v133 dst_sel:DWORD dst_unused:UNUSED_PAD src0_sel:DWORD src1_sel:WORD_1
v_mfma_f32_16x16x32_bf16 v[212:215],v[88:91],v[152:155],v[212:215]
ds_read_u16_d16_hi v131,v131
ds_read_b64_tr_b16 v[88:89],v6 offset:45152
v_pk_mul_f16 v121,v120,v121
v_lshlrev_b32_sdwa v135,1,v135 dst_sel:DWORD dst_unused:UNUSED_PAD src0_sel:DWORD src1_sel:WORD_1
v_mfma_f32_16x16x32_bf16 v[220:223],v[92:95],v[152:155],v[220:223]
ds_read_u16 v132,v132
v_exp_f16 v120,v121
v_mfma_f32_16x16x32_bf16 v[228:231],v[96:99],v[152:155],v[228:231]
ds_read_b64_tr_b16 v[90:91],v6 offset:46688
ds_read_b64_tr_b16 v[92:93],v6 offset:45160
v_lshlrev_b32_sdwa v136,1,v136 dst_sel:DWORD dst_unused:UNUSED_PAD src0_sel:DWORD src1_sel:WORD_1
v_mfma_f32_16x16x32_bf16 v[236:239],v[100:103],v[152:155],v[236:239]
ds_read_u16_d16_hi v134,v134
v_exp_f16_sdwa v120,v121 dst_sel:WORD_1 dst_unused:UNUSED_PRESERVE src0_sel:WORD_1
v_mfma_f32_16x16x32_bf16 v[244:247],v[104:107],v[152:155],v[244:247]
v_lshlrev_b32_sdwa v138,1,v138 dst_sel:DWORD dst_unused:UNUSED_PAD src0_sel:DWORD src1_sel:WORD_1
v_mfma_f32_16x16x32_bf16 v[252:255],v[108:111],v[152:155],v[252:255]
ds_read_b64_tr_b16 v[94:95],v6 offset:46696
ds_read_b64_tr_b16 v[96:97],v6 offset:45216
v_pk_fma_f16 v120,v120,s55,s55
v_lshlrev_b32_sdwa v137,1,v137 dst_sel:DWORD dst_unused:UNUSED_PAD src0_sel:DWORD src1_sel:WORD_1
v_mfma_f32_16x16x32_bf16 v[144:147],v[64:67],v[40:43],v[112:115]
ds_read_u16 v133,v133
v_rcp_f16 v121,v120
v_mfma_f32_16x16x32_bf16 v[152:155],v[64:67],v[44:47],v[112:115]
ds_read_b64_tr_b16 v[98:99],v6 offset:46752
ds_read_b64_tr_b16 v[100:101],v6 offset:45224
v_lshlrev_b32_sdwa v139,1,v139 dst_sel:DWORD dst_unused:UNUSED_PAD src0_sel:DWORD src1_sel:WORD_1
v_mfma_f32_16x16x32_bf16 v[156:159],v[68:71],v[44:47],v[116:119]
ds_read_u16_d16_hi v135,v135
v_rcp_f16_sdwa v121,v120 dst_sel:WORD_1 dst_unused:UNUSED_PRESERVE src0_sel:WORD_1
v_mfma_f32_16x16x32_bf16 v[148:151],v[68:71],v[40:43],v[116:119]
ds_read_u16 v136,v136
v_lshlrev_b32_sdwa v140,1,v140 dst_sel:DWORD dst_unused:UNUSED_PAD src0_sel:DWORD src1_sel:WORD_1
v_mfma_f32_16x16x32_bf16 v[144:147],v[72:75],v[48:51],v[144:147]
ds_read_b64_tr_b16 v[102:103],v6 offset:46760
ds_read_b64_tr_b16 v[104:105],v6 offset:45280
v_fma_mix_f32 v141,v141,v121,0 op_sel_hi:[0,1,0]
v_lshlrev_b32_sdwa v142,1,v142 dst_sel:DWORD dst_unused:UNUSED_PAD src0_sel:DWORD src1_sel:WORD_1
v_mfma_f32_16x16x32_bf16 v[152:155],v[72:75],v[52:55],v[152:155]
ds_read_u16_d16_hi v138,v138
v_fma_mix_f32 v143,v143,v121,0 op_sel:[0,1,0] op_sel_hi:[0,1,0]
v_mfma_f32_16x16x32_bf16 v[156:159],v[76:79],v[52:55],v[156:159]
ds_read_b64_tr_b16 v[106:107],v6 offset:46816
ds_read_b64_tr_b16 v[108:109],v6 offset:45288
v_mfma_f32_16x16x32_bf16 v[148:151],v[76:79],v[48:51],v[148:151]
ds_read_u16 v137,v137
v_mfma_f32_16x16x32_bf16 v[144:147],v[80:83],v[56:59],v[144:147]
ds_read_b64_tr_b16 v[110:111],v6 offset:46824
ds_read_b64_tr_b16 v[64:65],v8 offset:12320
v_mfma_f32_16x16x32_bf16 v[152:155],v[80:83],v[60:63],v[152:155]
s_waitcnt lgkmcnt(9)
ds_read_u16_d16_hi v139,v139
v_lshl_add_u64 v[128:129],v[128:129],0,v[130:131]
v_lshl_add_u64 v[130:131],v[132:133],0,v[134:135]
v_mfma_f32_16x16x32_bf16 v[156:159],v[84:87],v[60:63],v[156:159]
ds_read_u16 v140,v140
v_mfma_f32_16x16x32_bf16 v[148:151],v[84:87],v[56:59],v[148:151]
ds_read_b64_tr_b16 v[66:67],v8 offset:14368
ds_read_b128 v[112:115],v7 offset:0
v_lshlrev_b32_sdwa v144,1,v144 dst_sel:DWORD dst_unused:UNUSED_PAD src0_sel:DWORD src1_sel:WORD_1
v_lshlrev_b32_sdwa v146,1,v146 dst_sel:DWORD dst_unused:UNUSED_PAD src0_sel:DWORD src1_sel:WORD_1
v_mfma_f32_16x16x32_bf16 v[160:163],v[88:91],v[128:131],v[160:163]
ds_read_u16_d16_hi v142,v142
ds_read_b64_tr_b16 v[68:69],v9 offset:12320
v_lshlrev_b32_sdwa v145,1,v145 dst_sel:DWORD dst_unused:UNUSED_PAD src0_sel:DWORD src1_sel:WORD_1
v_lshlrev_b32_sdwa v147,1,v147 dst_sel:DWORD dst_unused:UNUSED_PAD src0_sel:DWORD src1_sel:WORD_1
v_mfma_f32_16x16x32_bf16 v[168:171],v[92:95],v[128:131],v[168:171]
ds_read_b64_tr_b16 v[70:71],v9 offset:14368
v_fma_mixlo_f16 v120,v156,s99,0
v_fma_mixlo_f16 v122,v157,s99,0
v_mfma_f32_16x16x32_bf16 v[176:179],v[96:99],v[128:131],v[176:179]
s_waitcnt lgkmcnt(14)
ds_read_u16 v144,v144
ds_read_b128 v[116:119],v7 offset:32
v_lshlrev_b32_sdwa v148,1,v148 dst_sel:DWORD dst_unused:UNUSED_PAD src0_sel:DWORD src1_sel:WORD_1
v_lshlrev_b32_sdwa v150,1,v150 dst_sel:DWORD dst_unused:UNUSED_PAD src0_sel:DWORD src1_sel:WORD_1
v_mfma_f32_16x16x32_bf16 v[184:187],v[100:103],v[128:131],v[184:187]
s_waitcnt lgkmcnt(8)
ds_read_b64_tr_b16 v[72:73],v8 offset:16416
v_lshl_add_u64 v[136:137],v[136:137],0,v[138:139]
v_mfma_f32_16x16x32_bf16 v[192:195],v[104:107],v[128:131],v[192:195]
s_waitcnt lgkmcnt(5)
ds_read_u16_d16_hi v146,v146
v_or_b32 v138,v142,v140
v_cvt_pk_bf16_f32 v139,v141,v143
v_mfma_f32_16x16x32_bf16 v[200:203],v[108:111],v[128:131],v[200:203]
ds_read_u16 v145,v145
ds_read_b64_tr_b16 v[74:75],v8 offset:18464
v_fma_mixhi_f16 v120,v158,s99,0
v_fma_mixhi_f16 v122,v159,s99,0
v_mfma_f32_16x16x32_bf16 v[164:167],v[88:91],v[136:139],v[164:167]
ds_read_u16_d16_hi v147,v147
ds_read_b64_tr_b16 v[76:77],v9 offset:16416
v_lshlrev_b32_sdwa v149,1,v149 dst_sel:DWORD dst_unused:UNUSED_PAD src0_sel:DWORD src1_sel:WORD_1
v_lshlrev_b32_sdwa v151,1,v151 dst_sel:DWORD dst_unused:UNUSED_PAD src0_sel:DWORD src1_sel:WORD_1
v_mfma_f32_16x16x32_bf16 v[172:175],v[92:95],v[136:139],v[172:175]
ds_read_b64_tr_b16 v[78:79],v9 offset:18464
v_pk_fma_f16 v121,v120,v120,s54
v_pk_fma_f16 v123,v122,v122,s54
v_mfma_f32_16x16x32_bf16 v[180:183],v[96:99],v[136:139],v[180:183]
ds_read_u16 v148,v148
v_pk_mul_f16 v121,v120,v121
v_pk_mul_f16 v123,v122,v123
v_mfma_f32_16x16x32_bf16 v[188:191],v[100:103],v[136:139],v[188:191]
ds_read_b64_tr_b16 v[80:81],v8 offset:20512
ds_read_b64_tr_b16 v[82:83],v8 offset:22560
v_lshlrev_b32_sdwa v152,1,v152 dst_sel:DWORD dst_unused:UNUSED_PAD src0_sel:DWORD src1_sel:WORD_1
v_lshlrev_b32_sdwa v154,1,v154 dst_sel:DWORD dst_unused:UNUSED_PAD src0_sel:DWORD src1_sel:WORD_1
v_mfma_f32_16x16x32_bf16 v[196:199],v[104:107],v[136:139],v[196:199]
ds_read_u16_d16_hi v150,v150
ds_read_b64_tr_b16 v[84:85],v9 offset:20512
v_exp_f16 v120,v121
v_mfma_f32_16x16x32_bf16 v[204:207],v[108:111],v[136:139],v[204:207]
ds_read_b64_tr_b16 v[86:87],v9 offset:22560
v_exp_f16 v122,v123
v_mfma_f32_16x16x32_bf16 v[128:131],v[64:67],v[16:19],v[112:115]
ds_read_u16 v149,v149
v_lshlrev_b32_sdwa v153,1,v153 dst_sel:DWORD dst_unused:UNUSED_PAD src0_sel:DWORD src1_sel:WORD_1
v_lshlrev_b32_sdwa v155,1,v155 dst_sel:DWORD dst_unused:UNUSED_PAD src0_sel:DWORD src1_sel:WORD_1
v_mfma_f32_16x16x32_bf16 v[136:139],v[64:67],v[20:23],v[112:115]
s_waitcnt lgkmcnt(14)
ds_read_b64_tr_b16 v[64:65],v8 offset:12832
ds_read_b64_tr_b16 v[66:67],v8 offset:14880
v_exp_f16_sdwa v120,v121 dst_sel:WORD_1 dst_unused:UNUSED_PRESERVE src0_sel:WORD_1
v_mfma_f32_16x16x32_bf16 v[140:143],v[68:71],v[20:23],v[116:119]
ds_read_u16_d16_hi v151,v151
ds_read_b128 v[112:115],v7 offset:128
v_exp_f16_sdwa v122,v123 dst_sel:WORD_1 dst_unused:UNUSED_PRESERVE src0_sel:WORD_1
v_mfma_f32_16x16x32_bf16 v[132:135],v[68:71],v[16:19],v[116:119]
s_waitcnt lgkmcnt(14)
ds_read_b64_tr_b16 v[68:69],v9 offset:12832
v_pk_fma_f16 v120,v120,s55,s55
v_pk_fma_f16 v122,v122,s55,s55
v_mfma_f32_16x16x32_bf16 v[128:131],v[72:75],v[24:27],v[128:131]
ds_read_u16 v152,v152
v_rcp_f16 v121,v120
v_mfma_f32_16x16x32_bf16 v[136:139],v[72:75],v[28:31],v[136:139]
s_waitcnt lgkmcnt(13)
ds_read_b64_tr_b16 v[70:71],v9 offset:14880
ds_read_b128 v[116:119],v7 offset:160
v_rcp_f16 v123,v122
v_mfma_f32_16x16x32_bf16 v[140:143],v[76:79],v[28:31],v[140:143]
ds_read_u16_d16_hi v154,v154
ds_read_b64_tr_b16 v[72:73],v8 offset:16928
v_rcp_f16_sdwa v121,v120 dst_sel:WORD_1 dst_unused:UNUSED_PRESERVE src0_sel:WORD_1
v_mfma_f32_16x16x32_bf16 v[132:135],v[76:79],v[24:27],v[132:135]
s_waitcnt lgkmcnt(14)
ds_read_b64_tr_b16 v[74:75],v8 offset:18976
ds_read_b64_tr_b16 v[76:77],v9 offset:16928
v_rcp_f16_sdwa v123,v122 dst_sel:WORD_1 dst_unused:UNUSED_PRESERVE src0_sel:WORD_1
s_mov_b32 s86,0xbe801d5a
v_mfma_f32_16x16x32_bf16 v[128:131],v[80:83],v[32:35],v[128:131]
ds_read_u16 v153,v153
v_fma_mix_f32 v156,v156,v121,0 op_sel_hi:[0,1,0]
v_fma_mix_f32 v158,v158,v121,0 op_sel:[0,1,0] op_sel_hi:[0,1,0]
v_mfma_f32_16x16x32_bf16 v[136:139],v[80:83],v[36:39],v[136:139]
s_waitcnt lgkmcnt(10)
ds_read_b64_tr_b16 v[78:79],v9 offset:18976
ds_read_b64_tr_b16 v[80:81],v8 offset:21024
v_lshl_add_u64 v[144:145],v[144:145],0,v[146:147]
v_lshl_add_u64 v[146:147],v[148:149],0,v[150:151]
v_mfma_f32_16x16x32_bf16 v[140:143],v[84:87],v[36:39],v[140:143]
ds_read_u16_d16_hi v155,v155
ds_read_b64_tr_b16 v[82:83],v8 offset:23072
v_fma_mix_f32 v157,v157,v123,0 op_sel_hi:[0,1,0]
v_fma_mix_f32 v159,v159,v123,0 op_sel:[0,1,0] op_sel_hi:[0,1,0]
v_mfma_f32_16x16x32_bf16 v[132:135],v[84:87],v[32:35],v[132:135]
ds_read_b64_tr_b16 v[84:85],v9 offset:21024
ds_read_b64_tr_b16 v[86:87],v9 offset:23072
v_lshlrev_b32_sdwa v128,1,v128 dst_sel:DWORD dst_unused:UNUSED_PAD src0_sel:DWORD src1_sel:WORD_1
v_lshlrev_b32_sdwa v130,1,v130 dst_sel:DWORD dst_unused:UNUSED_PAD src0_sel:DWORD src1_sel:WORD_1
v_mfma_f32_16x16x32_bf16 v[208:211],v[88:91],v[144:147],v[208:211]
ds_read_u16 v128,v128
v_lshlrev_b32_sdwa v129,1,v129 dst_sel:DWORD dst_unused:UNUSED_PAD src0_sel:DWORD src1_sel:WORD_1
v_lshlrev_b32_sdwa v131,1,v131 dst_sel:DWORD dst_unused:UNUSED_PAD src0_sel:DWORD src1_sel:WORD_1
v_mfma_f32_16x16x32_bf16 v[216:219],v[92:95],v[144:147],v[216:219]
v_fma_mixlo_f16 v120,v140,s99,0
v_fma_mixlo_f16 v122,v141,s99,0
v_mfma_f32_16x16x32_bf16 v[224:227],v[96:99],v[144:147],v[224:227]
ds_read_u16_d16_hi v130,v130
v_lshlrev_b32_sdwa v132,1,v132 dst_sel:DWORD dst_unused:UNUSED_PAD src0_sel:DWORD src1_sel:WORD_1
v_lshlrev_b32_sdwa v134,1,v134 dst_sel:DWORD dst_unused:UNUSED_PAD src0_sel:DWORD src1_sel:WORD_1
v_mfma_f32_16x16x32_bf16 v[232:235],v[100:103],v[144:147],v[232:235]
s_waitcnt lgkmcnt(5)
v_lshl_add_u64 v[152:153],v[152:153],0,v[154:155]
v_mfma_f32_16x16x32_bf16 v[240:243],v[104:107],v[144:147],v[240:243]
ds_read_u16 v129,v129
v_cvt_pk_bf16_f32 v154,v156,v158
v_cvt_pk_bf16_f32 v155,v157,v159
v_mfma_f32_16x16x32_bf16 v[248:251],v[108:111],v[144:147],v[248:251]
v_fma_mixhi_f16 v120,v142,s99,0
v_fma_mixhi_f16 v122,v143,s99,0
v_mfma_f32_16x16x32_bf16 v[212:215],v[88:91],v[152:155],v[212:215]
ds_read_u16_d16_hi v131,v131
ds_read_b64_tr_b16 v[88:89],v6 offset:51312
v_lshlrev_b32_sdwa v133,1,v133 dst_sel:DWORD dst_unused:UNUSED_PAD src0_sel:DWORD src1_sel:WORD_1
v_lshlrev_b32_sdwa v135,1,v135 dst_sel:DWORD dst_unused:UNUSED_PAD src0_sel:DWORD src1_sel:WORD_1
v_mfma_f32_16x16x32_bf16 v[220:223],v[92:95],v[152:155],v[220:223]
ds_read_b64_tr_b16 v[90:91],v6 offset:52848
v_pk_fma_f16 v121,v120,v120,s54
v_pk_fma_f16 v123,v122,v122,s54
v_mfma_f32_16x16x32_bf16 v[228:231],v[96:99],v[152:155],v[228:231]
ds_read_u16 v132,v132
v_pk_mul_f16 v121,v120,v121
v_pk_mul_f16 v123,v122,v123
v_mfma_f32_16x16x32_bf16 v[236:239],v[100:103],v[152:155],v[236:239]
ds_read_b64_tr_b16 v[92:93],v6 offset:51320
ds_read_b64_tr_b16 v[94:95],v6 offset:52856
v_lshlrev_b32_sdwa v136,1,v136 dst_sel:DWORD dst_unused:UNUSED_PAD src0_sel:DWORD src1_sel:WORD_1
v_lshlrev_b32_sdwa v138,1,v138 dst_sel:DWORD dst_unused:UNUSED_PAD src0_sel:DWORD src1_sel:WORD_1
v_mfma_f32_16x16x32_bf16 v[244:247],v[104:107],v[152:155],v[244:247]
ds_read_u16_d16_hi v134,v134
ds_read_b64_tr_b16 v[96:97],v6 offset:51376
v_exp_f16 v120,v121
v_mfma_f32_16x16x32_bf16 v[252:255],v[108:111],v[152:155],v[252:255]
ds_read_b64_tr_b16 v[98:99],v6 offset:52912
ds_read_u16 v133,v133
v_exp_f16 v122,v123
v_mfma_f32_16x16x32_bf16 v[144:147],v[64:67],v[16:19],v[112:115]
ds_read_u16_d16_hi v135,v135
v_lshlrev_b32_sdwa v137,1,v137 dst_sel:DWORD dst_unused:UNUSED_PAD src0_sel:DWORD src1_sel:WORD_1
v_lshlrev_b32_sdwa v139,1,v139 dst_sel:DWORD dst_unused:UNUSED_PAD src0_sel:DWORD src1_sel:WORD_1
v_mfma_f32_16x16x32_bf16 v[152:155],v[64:67],v[20:23],v[112:115]
ds_read_b64_tr_b16 v[100:101],v6 offset:51384
ds_read_b64_tr_b16 v[102:103],v6 offset:52920
v_exp_f16_sdwa v120,v121 dst_sel:WORD_1 dst_unused:UNUSED_PRESERVE src0_sel:WORD_1
v_mfma_f32_16x16x32_bf16 v[156:159],v[68:71],v[20:23],v[116:119]
ds_read_u16 v136,v136
ds_read_b64_tr_b16 v[104:105],v6 offset:51440
v_exp_f16_sdwa v122,v123 dst_sel:WORD_1 dst_unused:UNUSED_PRESERVE src0_sel:WORD_1
v_mfma_f32_16x16x32_bf16 v[148:151],v[68:71],v[16:19],v[116:119]
ds_read_b64_tr_b16 v[106:107],v6 offset:52976
v_pk_fma_f16 v120,v120,s55,s55
v_pk_fma_f16 v122,v122,s55,s55
v_mfma_f32_16x16x32_bf16 v[144:147],v[72:75],v[24:27],v[144:147]
ds_read_u16_d16_hi v138,v138
v_rcp_f16 v121,v120
v_mfma_f32_16x16x32_bf16 v[152:155],v[72:75],v[28:31],v[152:155]
ds_read_b64_tr_b16 v[108:109],v6 offset:51448
ds_read_b64_tr_b16 v[110:111],v6 offset:52984
v_rcp_f16 v123,v122
v_mfma_f32_16x16x32_bf16 v[156:159],v[76:79],v[28:31],v[156:159]
ds_read_u16 v137,v137
ds_read_b64_tr_b16 v[64:65],v8 offset:24640
v_rcp_f16_sdwa v121,v120 dst_sel:WORD_1 dst_unused:UNUSED_PRESERVE src0_sel:WORD_1
v_mfma_f32_16x16x32_bf16 v[148:151],v[76:79],v[24:27],v[148:151]
s_waitcnt lgkmcnt(15)
ds_read_b64_tr_b16 v[66:67],v8 offset:26688
ds_read_b128 v[112:115],v7 offset:256
v_rcp_f16_sdwa v123,v122 dst_sel:WORD_1 dst_unused:UNUSED_PRESERVE src0_sel:WORD_1
v_mfma_f32_16x16x32_bf16 v[144:147],v[80:83],v[32:35],v[144:147]
ds_read_u16_d16_hi v139,v139
v_fma_mix_f32 v140,v140,v121,0 op_sel_hi:[0,1,0]
v_fma_mix_f32 v142,v142,v121,0 op_sel:[0,1,0] op_sel_hi:[0,1,0]
v_mfma_f32_16x16x32_bf16 v[152:155],v[80:83],v[36:39],v[152:155]
s_waitcnt lgkmcnt(13)
ds_read_b64_tr_b16 v[68:69],v9 offset:24640
ds_read_b64_tr_b16 v[70:71],v9 offset:26688
v_lshl_add_u64 v[128:129],v[128:129],0,v[130:131]
v_lshl_add_u64 v[130:131],v[132:133],0,v[134:135]
v_mfma_f32_16x16x32_bf16 v[156:159],v[84:87],v[36:39],v[156:159]
ds_read_b128 v[116:119],v7 offset:288
v_fma_mix_f32 v141,v141,v123,0 op_sel_hi:[0,1,0]
v_fma_mix_f32 v143,v143,v123,0 op_sel:[0,1,0] op_sel_hi:[0,1,0]
v_mfma_f32_16x16x32_bf16 v[148:151],v[84:87],v[32:35],v[148:151]
ds_read_b64_tr_b16 v[72:73],v8 offset:28736
ds_read_b64_tr_b16 v[74:75],v8 offset:30784
v_lshlrev_b32_sdwa v144,1,v144 dst_sel:DWORD dst_unused:UNUSED_PAD src0_sel:DWORD src1_sel:WORD_1
v_lshlrev_b32_sdwa v146,1,v146 dst_sel:DWORD dst_unused:UNUSED_PAD src0_sel:DWORD src1_sel:WORD_1
v_mfma_f32_16x16x32_bf16 v[160:163],v[88:91],v[128:131],v[160:163]
ds_read_u16 v144,v144
ds_read_b64_tr_b16 v[76:77],v9 offset:28736
v_lshlrev_b32_sdwa v145,1,v145 dst_sel:DWORD dst_unused:UNUSED_PAD src0_sel:DWORD src1_sel:WORD_1
v_lshlrev_b32_sdwa v147,1,v147 dst_sel:DWORD dst_unused:UNUSED_PAD src0_sel:DWORD src1_sel:WORD_1
v_mfma_f32_16x16x32_bf16 v[168:171],v[92:95],v[128:131],v[168:171]
ds_read_b64_tr_b16 v[78:79],v9 offset:30784
v_fma_mixlo_f16 v120,v156,s99,0
v_fma_mixlo_f16 v122,v157,s99,0
v_mfma_f32_16x16x32_bf16 v[176:179],v[96:99],v[128:131],v[176:179]
s_waitcnt lgkmcnt(15)
ds_read_u16_d16_hi v146,v146
ds_read_b64_tr_b16 v[80:81],v8 offset:32832
v_lshlrev_b32_sdwa v148,1,v148 dst_sel:DWORD dst_unused:UNUSED_PAD src0_sel:DWORD src1_sel:WORD_1
v_lshlrev_b32_sdwa v150,1,v150 dst_sel:DWORD dst_unused:UNUSED_PAD src0_sel:DWORD src1_sel:WORD_1
v_mfma_f32_16x16x32_bf16 v[184:187],v[100:103],v[128:131],v[184:187]
s_waitcnt lgkmcnt(10)
ds_read_b64_tr_b16 v[82:83],v8 offset:34880
v_lshl_add_u64 v[136:137],v[136:137],0,v[138:139]
v_mfma_f32_16x16x32_bf16 v[192:195],v[104:107],v[128:131],v[192:195]
ds_read_u16 v145,v145
v_cvt_pk_bf16_f32 v138,v140,v142
v_cvt_pk_bf16_f32 v139,v141,v143
v_mfma_f32_16x16x32_bf16 v[200:203],v[108:111],v[128:131],v[200:203]
ds_read_b64_tr_b16 v[84:85],v9 offset:32832
ds_read_b64_tr_b16 v[86:87],v9 offset:34880
v_fma_mixhi_f16 v120,v158,s99,0
v_fma_mixhi_f16 v122,v159,s99,0
v_mfma_f32_16x16x32_bf16 v[164:167],v[88:91],v[136:139],v[164:167]
ds_read_u16_d16_hi v147,v147
ds_read_b64_tr_b16 v[88:89],v6 offset:57472
v_lshlrev_b32_sdwa v149,1,v149 dst_sel:DWORD dst_unused:UNUSED_PAD src0_sel:DWORD src1_sel:WORD_1
v_lshlrev_b32_sdwa v151,1,v151 dst_sel:DWORD dst_unused:UNUSED_PAD src0_sel:DWORD src1_sel:WORD_1
v_mfma_f32_16x16x32_bf16 v[172:175],v[92:95],v[136:139],v[172:175]
ds_read_b64_tr_b16 v[90:91],v6 offset:59008
v_pk_fma_f16 v121,v120,v120,s54
v_pk_fma_f16 v123,v122,v122,s54
v_mfma_f32_16x16x32_bf16 v[180:183],v[96:99],v[136:139],v[180:183]
ds_read_u16 v148,v148
v_pk_mul_f16 v121,v120,v121
v_pk_mul_f16 v123,v122,v123
v_mfma_f32_16x16x32_bf16 v[188:191],v[100:103],v[136:139],v[188:191]
ds_read_b64_tr_b16 v[92:93],v6 offset:57480
ds_read_b64_tr_b16 v[94:95],v6 offset:59016
v_lshlrev_b32_sdwa v152,1,v152 dst_sel:DWORD dst_unused:UNUSED_PAD src0_sel:DWORD src1_sel:WORD_1
v_lshlrev_b32_sdwa v154,1,v154 dst_sel:DWORD dst_unused:UNUSED_PAD src0_sel:DWORD src1_sel:WORD_1
v_mfma_f32_16x16x32_bf16 v[196:199],v[104:107],v[136:139],v[196:199]
ds_read_u16_d16_hi v150,v150
ds_read_b64_tr_b16 v[96:97],v6 offset:57536
v_exp_f16 v120,v121
v_mfma_f32_16x16x32_bf16 v[204:207],v[108:111],v[136:139],v[204:207]
ds_read_b64_tr_b16 v[98:99],v6 offset:59072
v_exp_f16 v122,v123
v_mfma_f32_16x16x32_bf16 v[128:131],v[64:67],v[16:19],v[112:115]
ds_read_u16 v149,v149
v_lshlrev_b32_sdwa v153,1,v153 dst_sel:DWORD dst_unused:UNUSED_PAD src0_sel:DWORD src1_sel:WORD_1
v_lshlrev_b32_sdwa v155,1,v155 dst_sel:DWORD dst_unused:UNUSED_PAD src0_sel:DWORD src1_sel:WORD_1
v_exp_f16_sdwa v120,v121 dst_sel:WORD_1 dst_unused:UNUSED_PRESERVE src0_sel:WORD_1
v_mfma_f32_16x16x32_bf16 v[136:139],v[64:67],v[20:23],v[112:115]
s_waitcnt lgkmcnt(15)
ds_read_b64_tr_b16 v[100:101],v6 offset:57544
ds_read_b64_tr_b16 v[102:103],v6 offset:59080
v_mfma_f32_16x16x32_bf16 v[140:143],v[68:71],v[20:23],v[116:119]
ds_read_u16_d16_hi v151,v151
ds_read_b64_tr_b16 v[104:105],v6 offset:57600
v_exp_f16_sdwa v122,v123 dst_sel:WORD_1 dst_unused:UNUSED_PRESERVE src0_sel:WORD_1
v_mfma_f32_16x16x32_bf16 v[132:135],v[68:71],v[16:19],v[116:119]
ds_read_b64_tr_b16 v[106:107],v6 offset:59136
v_pk_fma_f16 v120,v120,s55,s55
v_pk_fma_f16 v122,v122,s55,s55
v_mfma_f32_16x16x32_bf16 v[128:131],v[72:75],v[24:27],v[128:131]
ds_read_u16 v152,v152
v_rcp_f16 v121,v120
v_mfma_f32_16x16x32_bf16 v[136:139],v[72:75],v[28:31],v[136:139]
ds_read_b64_tr_b16 v[108:109],v6 offset:57608
ds_read_b64_tr_b16 v[110:111],v6 offset:59144
v_rcp_f16 v123,v122
v_mfma_f32_16x16x32_bf16 v[140:143],v[76:79],v[28:31],v[140:143]
ds_read_u16_d16_hi v154,v154
v_rcp_f16_sdwa v121,v120 dst_sel:WORD_1 dst_unused:UNUSED_PRESERVE src0_sel:WORD_1
v_mfma_f32_16x16x32_bf16 v[132:135],v[76:79],v[24:27],v[132:135]
s_waitcnt lgkmcnt(15)
v_rcp_f16_sdwa v123,v122 dst_sel:WORD_1 dst_unused:UNUSED_PRESERVE src0_sel:WORD_1
v_mfma_f32_16x16x32_bf16 v[128:131],v[80:83],v[32:35],v[128:131]
ds_read_u16 v153,v153
v_fma_mix_f32 v156,v156,v121,0 op_sel_hi:[0,1,0]
v_fma_mix_f32 v158,v158,v121,0 op_sel:[0,1,0] op_sel_hi:[0,1,0]
v_mfma_f32_16x16x32_bf16 v[136:139],v[80:83],v[36:39],v[136:139]
s_waitcnt lgkmcnt(7)
v_lshl_add_u64 v[144:145],v[144:145],0,v[146:147]
v_lshl_add_u64 v[146:147],v[148:149],0,v[150:151]
v_mfma_f32_16x16x32_bf16 v[140:143],v[84:87],v[36:39],v[140:143]
ds_read_u16_d16_hi v155,v155
v_fma_mix_f32 v157,v157,v123,0 op_sel_hi:[0,1,0]
v_fma_mix_f32 v159,v159,v123,0 op_sel:[0,1,0] op_sel_hi:[0,1,0]
v_mfma_f32_16x16x32_bf16 v[132:135],v[84:87],v[32:35],v[132:135]
v_lshlrev_b32_sdwa v128,1,v128 dst_sel:DWORD dst_unused:UNUSED_PAD src0_sel:DWORD src1_sel:WORD_1
v_lshlrev_b32_sdwa v130,1,v130 dst_sel:DWORD dst_unused:UNUSED_PAD src0_sel:DWORD src1_sel:WORD_1
v_mfma_f32_16x16x32_bf16 v[160:163],v[88:91],v[144:147],v[160:163]
ds_read_u16 v128,v128
v_lshlrev_b32_sdwa v129,1,v129 dst_sel:DWORD dst_unused:UNUSED_PAD src0_sel:DWORD src1_sel:WORD_1
v_lshlrev_b32_sdwa v131,1,v131 dst_sel:DWORD dst_unused:UNUSED_PAD src0_sel:DWORD src1_sel:WORD_1
v_mfma_f32_16x16x32_bf16 v[168:171],v[92:95],v[144:147],v[168:171]
v_fma_mixlo_f16 v120,v140,s99,0
v_fma_mixlo_f16 v122,v141,s99,0
v_mfma_f32_16x16x32_bf16 v[176:179],v[96:99],v[144:147],v[176:179]
ds_read_u16_d16_hi v130,v130
v_lshlrev_b32_sdwa v132,1,v132 dst_sel:DWORD dst_unused:UNUSED_PAD src0_sel:DWORD src1_sel:WORD_1
v_lshlrev_b32_sdwa v134,1,v134 dst_sel:DWORD dst_unused:UNUSED_PAD src0_sel:DWORD src1_sel:WORD_1
v_mfma_f32_16x16x32_bf16 v[184:187],v[100:103],v[144:147],v[184:187]
s_waitcnt lgkmcnt(2)
ds_read_u16 v129,v129
ds_read_u16_d16_hi v131,v131
v_lshl_add_u64 v[152:153],v[152:153],0,v[154:155]
v_mfma_f32_16x16x32_bf16 v[192:195],v[104:107],v[144:147],v[192:195]
ds_read_u16 v132,v132
v_cvt_pk_bf16_f32 v154,v156,v158
v_cvt_pk_bf16_f32 v155,v157,v159
v_mfma_f32_16x16x32_bf16 v[200:203],v[108:111],v[144:147],v[200:203]
v_fma_mixhi_f16 v122,v143,s99,0
v_mfma_f32_16x16x32_bf16 v[164:167],v[88:91],v[152:155],v[164:167]
ds_read_u16_d16_hi v134,v134
ds_read_b64_tr_b16 v[88:89],v6 offset:63632
v_fma_mixhi_f16 v120,v142,s99,0
v_lshlrev_b32_sdwa v133,1,v133 dst_sel:DWORD dst_unused:UNUSED_PAD src0_sel:DWORD src1_sel:WORD_1
v_lshlrev_b32_sdwa v135,1,v135 dst_sel:DWORD dst_unused:UNUSED_PAD src0_sel:DWORD src1_sel:WORD_1
v_mfma_f32_16x16x32_bf16 v[172:175],v[92:95],v[152:155],v[172:175]
ds_read_b64_tr_b16 v[90:91],v6 offset:65168
v_pk_fma_f16 v121,v120,v120,s54
v_pk_fma_f16 v123,v122,v122,s54
v_mfma_f32_16x16x32_bf16 v[180:183],v[96:99],v[152:155],v[180:183]
ds_read_u16 v133,v133
v_pk_mul_f16 v121,v120,v121
v_pk_mul_f16 v123,v122,v123
v_mfma_f32_16x16x32_bf16 v[188:191],v[100:103],v[152:155],v[188:191]
ds_read_b64_tr_b16 v[92:93],v6 offset:63640
ds_read_b64_tr_b16 v[94:95],v6 offset:65176
v_lshlrev_b32_sdwa v136,1,v136 dst_sel:DWORD dst_unused:UNUSED_PAD src0_sel:DWORD src1_sel:WORD_1
v_lshlrev_b32_sdwa v138,1,v138 dst_sel:DWORD dst_unused:UNUSED_PAD src0_sel:DWORD src1_sel:WORD_1
v_mfma_f32_16x16x32_bf16 v[196:199],v[104:107],v[152:155],v[196:199]
ds_read_u16_d16_hi v135,v135
ds_read_b64_tr_b16 v[96:97],v6 offset:63696
v_exp_f16 v120,v121
v_mfma_f32_16x16x32_bf16 v[204:207],v[108:111],v[152:155],v[204:207]
ds_read_b64_tr_b16 v[98:99],v6 offset:65232
v_exp_f16 v122,v123
v_mfma_f32_16x16x32_bf16 v[144:147],v[64:67],v[40:43],v[112:115]
ds_read_u16 v136,v136
v_lshlrev_b32_sdwa v137,1,v137 dst_sel:DWORD dst_unused:UNUSED_PAD src0_sel:DWORD src1_sel:WORD_1
v_lshlrev_b32_sdwa v139,1,v139 dst_sel:DWORD dst_unused:UNUSED_PAD src0_sel:DWORD src1_sel:WORD_1
v_mfma_f32_16x16x32_bf16 v[152:155],v[64:67],v[44:47],v[112:115]
ds_read_b64_tr_b16 v[100:101],v6 offset:63704
ds_read_b64_tr_b16 v[102:103],v6 offset:65240
v_exp_f16_sdwa v120,v121 dst_sel:WORD_1 dst_unused:UNUSED_PRESERVE src0_sel:WORD_1
v_mfma_f32_16x16x32_bf16 v[156:159],v[68:71],v[44:47],v[116:119]
ds_read_u16_d16_hi v138,v138
ds_read_b64_tr_b16 v[104:105],v6 offset:63760
v_exp_f16_sdwa v122,v123 dst_sel:WORD_1 dst_unused:UNUSED_PRESERVE src0_sel:WORD_1
v_mfma_f32_16x16x32_bf16 v[148:151],v[68:71],v[40:43],v[116:119]
ds_read_b64_tr_b16 v[106:107],v6 offset:65296
v_pk_fma_f16 v120,v120,s55,s55
v_pk_fma_f16 v122,v122,s55,s55
v_mfma_f32_16x16x32_bf16 v[144:147],v[72:75],v[48:51],v[144:147]
ds_read_u16 v137,v137
v_rcp_f16 v121,v120
v_mfma_f32_16x16x32_bf16 v[152:155],v[72:75],v[52:55],v[152:155]
ds_read_b64_tr_b16 v[108:109],v6 offset:63768
ds_read_b64_tr_b16 v[110:111],v6 offset:65304
v_rcp_f16 v123,v122
v_mfma_f32_16x16x32_bf16 v[156:159],v[76:79],v[52:55],v[156:159]
ds_read_u16_d16_hi v139,v139
ds_read_b64_tr_b16 v[64:65],v8 offset:12832
v_rcp_f16_sdwa v121,v120 dst_sel:WORD_1 dst_unused:UNUSED_PRESERVE src0_sel:WORD_1
v_mfma_f32_16x16x32_bf16 v[148:151],v[76:79],v[48:51],v[148:151]
ds_read_b64_tr_b16 v[66:67],v8 offset:14880
ds_read_b128 v[112:115],v7 offset:128
v_rcp_f16_sdwa v123,v122 dst_sel:WORD_1 dst_unused:UNUSED_PRESERVE src0_sel:WORD_1
v_mfma_f32_16x16x32_bf16 v[144:147],v[80:83],v[56:59],v[144:147]
ds_read_b64_tr_b16 v[68:69],v9 offset:12832
v_fma_mix_f32 v140,v140,v121,0 op_sel_hi:[0,1,0]
v_fma_mix_f32 v142,v142,v121,0 op_sel:[0,1,0] op_sel_hi:[0,1,0]
v_mfma_f32_16x16x32_bf16 v[152:155],v[80:83],v[60:63],v[152:155]
s_waitcnt lgkmcnt(15)
ds_read_b64_tr_b16 v[70:71],v9 offset:14880
ds_read_b128 v[116:119],v7 offset:160
v_lshl_add_u64 v[128:129],v[128:129],0,v[130:131]
v_lshl_add_u64 v[130:131],v[132:133],0,v[134:135]
v_mfma_f32_16x16x32_bf16 v[156:159],v[84:87],v[60:63],v[156:159]
ds_read_b64_tr_b16 v[72:73],v8 offset:16928
v_fma_mix_f32 v141,v141,v123,0 op_sel_hi:[0,1,0]
v_fma_mix_f32 v143,v143,v123,0 op_sel:[0,1,0] op_sel_hi:[0,1,0]
v_mfma_f32_16x16x32_bf16 v[148:151],v[84:87],v[56:59],v[148:151]
ds_read_b64_tr_b16 v[74:75],v8 offset:18976
ds_read_b64_tr_b16 v[76:77],v9 offset:16928
v_lshlrev_b32_sdwa v144,1,v144 dst_sel:DWORD dst_unused:UNUSED_PAD src0_sel:DWORD src1_sel:WORD_1
v_lshlrev_b32_sdwa v146,1,v146 dst_sel:DWORD dst_unused:UNUSED_PAD src0_sel:DWORD src1_sel:WORD_1
v_mfma_f32_16x16x32_bf16 v[160:163],v[88:91],v[128:131],v[160:163]
ds_read_u16 v144,v144
ds_read_b128 v[20:23],v7 offset:1536
v_lshlrev_b32_sdwa v145,1,v145 dst_sel:DWORD dst_unused:UNUSED_PAD src0_sel:DWORD src1_sel:WORD_1
v_mfma_f32_16x16x32_bf16 v[168:171],v[92:95],v[128:131],v[168:171]
s_waitcnt lgkmcnt(15)
ds_read_u16_d16_hi v146,v146
ds_read_b128 v[124:127],v7 offset:1568
v_lshlrev_b32_sdwa v147,1,v147 dst_sel:DWORD dst_unused:UNUSED_PAD src0_sel:DWORD src1_sel:WORD_1
v_mfma_f32_16x16x32_bf16 v[176:179],v[96:99],v[128:131],v[176:179]
ds_read_b128 v[28:31],v7 offset:1664
ds_read_b128 v[36:39],v7 offset:1696
v_lshlrev_b32_sdwa v148,1,v148 dst_sel:DWORD dst_unused:UNUSED_PAD src0_sel:DWORD src1_sel:WORD_1
v_mfma_f32_16x16x32_bf16 v[184:187],v[100:103],v[128:131],v[184:187]
s_waitcnt lgkmcnt(15)
ds_read_u16 v145,v145
ds_read_b64_tr_b16 v[78:79],v9 offset:18976
v_lshl_add_u64 v[136:137],v[136:137],0,v[138:139]
v_lshlrev_b32_sdwa v150,1,v150 dst_sel:DWORD dst_unused:UNUSED_PAD src0_sel:DWORD src1_sel:WORD_1
v_mfma_f32_16x16x32_bf16 v[192:195],v[104:107],v[128:131],v[192:195]
ds_read_u16_d16_hi v147,v147
ds_read_b64_tr_b16 v[80:81],v8 offset:21024
v_cvt_pk_bf16_f32 v138,v140,v142
v_cvt_pk_bf16_f32 v139,v141,v143
v_mfma_f32_16x16x32_bf16 v[200:203],v[108:111],v[128:131],v[200:203]
ds_read_b64_tr_b16 v[82:83],v8 offset:23072
v_lshlrev_b32_sdwa v149,1,v149 dst_sel:DWORD dst_unused:UNUSED_PAD src0_sel:DWORD src1_sel:WORD_1
v_mfma_f32_16x16x32_bf16 v[164:167],v[88:91],v[136:139],v[164:167]
ds_read_u16 v148,v148
ds_read_b64_tr_b16 v[84:85],v9 offset:21024
v_lshlrev_b32_sdwa v151,1,v151 dst_sel:DWORD dst_unused:UNUSED_PAD src0_sel:DWORD src1_sel:WORD_1
v_mfma_f32_16x16x32_bf16 v[172:175],v[92:95],v[136:139],v[172:175]
s_waitcnt lgkmcnt(11)
ds_read_u16_d16_hi v150,v150
ds_read_b64_tr_b16 v[86:87],v9 offset:23072
v_lshlrev_b32_sdwa v152,1,v152 dst_sel:DWORD dst_unused:UNUSED_PAD src0_sel:DWORD src1_sel:WORD_1
v_xor_b32 v15,64,v10
v_mul_f32 v160,v160,v20
v_mfma_f32_16x16x32_bf16 v[180:183],v[96:99],v[136:139],v[180:183]
v_lshlrev_b32_sdwa v154,1,v154 dst_sel:DWORD dst_unused:UNUSED_PAD src0_sel:DWORD src1_sel:WORD_1
v_mul_f32 v161,v161,v21
v_mul_f32 v162,v162,v22
v_mfma_f32_16x16x32_bf16 v[188:191],v[100:103],v[136:139],v[188:191]
s_waitcnt lgkmcnt(11)
ds_read_u16 v149,v149
v_lshlrev_b32_sdwa v153,1,v153 dst_sel:DWORD dst_unused:UNUSED_PAD src0_sel:DWORD src1_sel:WORD_1
v_mul_f32 v163,v163,v23
v_mul_f32 v168,v168,v124
v_mfma_f32_16x16x32_bf16 v[196:199],v[104:107],v[136:139],v[196:199]
ds_read_u16_d16_hi v151,v151
v_lshlrev_b32_sdwa v155,1,v155 dst_sel:DWORD dst_unused:UNUSED_PAD src0_sel:DWORD src1_sel:WORD_1
v_mul_f32 v169,v169,v125
v_mul_f32 v170,v170,v126
v_mfma_f32_16x16x32_bf16 v[204:207],v[108:111],v[136:139],v[204:207]
v_lshlrev_b32_sdwa v156,1,v156 dst_sel:DWORD dst_unused:UNUSED_PAD src0_sel:DWORD src1_sel:WORD_1
v_mul_f32 v171,v171,v127
v_cvt_pk_bf16_f32 v16,v160,v161
v_mfma_f32_16x16x32_bf16 v[128:131],v[64:67],v[40:43],v[112:115]
ds_read_u16 v152,v152
v_lshlrev_b32_sdwa v158,1,v158 dst_sel:DWORD dst_unused:UNUSED_PAD src0_sel:DWORD src1_sel:WORD_1
v_cvt_pk_bf16_f32 v17,v162,v163
v_cvt_pk_bf16_f32 v18,v168,v169
v_mfma_f32_16x16x32_bf16 v[136:139],v[64:67],v[44:47],v[112:115]
s_waitcnt lgkmcnt(13)
ds_read_u16_d16_hi v154,v154
ds_read_b64_tr_b16 v[64:65],v8 offset:12320
v_lshlrev_b32_sdwa v157,1,v157 dst_sel:DWORD dst_unused:UNUSED_PAD src0_sel:DWORD src1_sel:WORD_1
v_cvt_pk_bf16_f32 v19,v170,v171
v_mul_f32_dpp v176,v176,v28 quad_perm:[1,0,3,2] row_mask:0xf bank_mask:0xf
v_mfma_f32_16x16x32_bf16 v[140:143],v[68:71],v[44:47],v[116:119]
ds_read_b64_tr_b16 v[66:67],v8 offset:14368
ds_read_b128 v[112:115],v7 offset:0
v_lshlrev_b32_sdwa v159,1,v159 dst_sel:DWORD dst_unused:UNUSED_PAD src0_sel:DWORD src1_sel:WORD_1
v_mul_f32_dpp v177,v177,v29 quad_perm:[1,0,3,2] row_mask:0xf bank_mask:0xf
v_mul_f32_dpp v178,v178,v30 quad_perm:[1,0,3,2] row_mask:0xf bank_mask:0xf
v_mfma_f32_16x16x32_bf16 v[132:135],v[68:71],v[40:43],v[116:119]
s_waitcnt lgkmcnt(15)
ds_read_u16 v153,v153
ds_read_u16_d16_hi v155,v155
v_mul_f32_dpp v179,v179,v31 quad_perm:[1,0,3,2] row_mask:0xf bank_mask:0xf
v_mul_f32_dpp v184,v184,v36 quad_perm:[1,0,3,2] row_mask:0xf bank_mask:0xf
v_mul_f32_dpp v185,v185,v37 quad_perm:[1,0,3,2] row_mask:0xf bank_mask:0xf
v_mfma_f32_16x16x32_bf16 v[128:131],v[72:75],v[48:51],v[128:131]
ds_read_u16 v156,v156
ds_read_b128 v[160:163],v7 offset:1792
v_mul_f32_dpp v186,v186,v38 quad_perm:[1,0,3,2] row_mask:0xf bank_mask:0xf
v_mul_f32_dpp v187,v187,v39 quad_perm:[1,0,3,2] row_mask:0xf bank_mask:0xf
v_cvt_pk_bf16_f32 v24,v176,v177
v_mfma_f32_16x16x32_bf16 v[136:139],v[72:75],v[52:55],v[136:139]
s_waitcnt lgkmcnt(15)
ds_read_b128 v[168:171],v7 offset:1824
v_cvt_pk_bf16_f32 v25,v178,v179
v_cvt_pk_bf16_f32 v26,v184,v185
v_cvt_pk_bf16_f32 v27,v186,v187
v_mfma_f32_16x16x32_bf16 v[140:143],v[76:79],v[52:55],v[140:143]
ds_read_u16_d16_hi v158,v158
ds_read_b64_tr_b16 v[68:69],v9 offset:12320
s_mov_b64 exec,s[12:13]
v_swap_b32 v16,v24
v_swap_b32 v17,v25
v_swap_b32 v18,v26
v_swap_b32 v19,v27
s_mov_b64 exec,-1
s_add_u32 s65,s61,4718592
buffer_store_dwordx4 v[16:19],v10,s[40:43],s65 offen offset:0 sc1
s_add_u32 s65,s61,4718592
buffer_store_dwordx4 v[24:27],v15,s[40:43],s65 offen offset:384 sc1
v_mfma_f32_16x16x32_bf16 v[132:135],v[76:79],v[48:51],v[132:135]
s_waitcnt lgkmcnt(3)
ds_read_u16 v157,v157
ds_read_b64_tr_b16 v[70:71],v9 offset:14368
v_lshl_add_u64 v[144:145],v[144:145],0,v[146:147]
v_mul_f32 v192,v192,v160
v_mul_f32 v193,v193,v161
v_mfma_f32_16x16x32_bf16 v[128:131],v[80:83],v[56:59],v[128:131]
ds_read_b128 v[116:119],v7 offset:32
ds_read_b64_tr_b16 v[72:73],v8 offset:16416
v_lshl_add_u64 v[146:147],v[148:149],0,v[150:151]
v_mul_f32 v194,v194,v162
v_mul_f32 v195,v195,v163
v_mfma_f32_16x16x32_bf16 v[136:139],v[80:83],v[60:63],v[136:139]
s_waitcnt lgkmcnt(6)
ds_read_u16_d16_hi v159,v159
ds_read_b64_tr_b16 v[74:75],v8 offset:18464
v_mul_f32 v200,v200,v168
v_mul_f32 v201,v201,v169
v_mul_f32 v202,v202,v170
v_mfma_f32_16x16x32_bf16 v[140:143],v[84:87],v[60:63],v[140:143]
ds_read_b64_tr_b16 v[76:77],v9 offset:16416
v_mul_f32 v203,v203,v171
v_cvt_pk_bf16_f32 v32,v192,v193
v_cvt_pk_bf16_f32 v33,v194,v195
v_mfma_f32_16x16x32_bf16 v[132:135],v[84:87],v[56:59],v[132:135]
ds_read_b64_tr_b16 v[78:79],v9 offset:18464
v_lshlrev_b32_sdwa v128,1,v128 dst_sel:DWORD dst_unused:UNUSED_PAD src0_sel:DWORD src1_sel:WORD_1
v_lshlrev_b32_sdwa v130,1,v130 dst_sel:DWORD dst_unused:UNUSED_PAD src0_sel:DWORD src1_sel:WORD_1
v_cvt_pk_bf16_f32 v34,v200,v201
v_mfma_f32_16x16x32_bf16 v[208:211],v[88:91],v[144:147],v[208:211]
ds_read_u16 v128,v128
ds_read_b64_tr_b16 v[80:81],v8 offset:20512
v_lshlrev_b32_sdwa v129,1,v129 dst_sel:DWORD dst_unused:UNUSED_PAD src0_sel:DWORD src1_sel:WORD_1
v_cvt_pk_bf16_f32 v35,v202,v203
v_mfma_f32_16x16x32_bf16 v[216:219],v[92:95],v[144:147],v[216:219]
ds_read_u16_d16_hi v130,v130
ds_read_b64_tr_b16 v[82:83],v8 offset:22560
v_lshlrev_b32_sdwa v131,1,v131 dst_sel:DWORD dst_unused:UNUSED_PAD src0_sel:DWORD src1_sel:WORD_1
v_mul_f32_dpp v164,v164,v20 quad_perm:[1,0,3,2] row_mask:0xf bank_mask:0xf
v_mfma_f32_16x16x32_bf16 v[224:227],v[96:99],v[144:147],v[224:227]
ds_read_b64_tr_b16 v[84:85],v9 offset:20512
ds_read_b64_tr_b16 v[86:87],v9 offset:22560
v_lshlrev_b32_sdwa v132,1,v132 dst_sel:DWORD dst_unused:UNUSED_PAD src0_sel:DWORD src1_sel:WORD_1
v_mul_f32_dpp v165,v165,v21 quad_perm:[1,0,3,2] row_mask:0xf bank_mask:0xf
v_mfma_f32_16x16x32_bf16 v[232:235],v[100:103],v[144:147],v[232:235]
ds_read_u16 v129,v129
v_lshl_add_u64 v[152:153],v[152:153],0,v[154:155]
v_lshlrev_b32_sdwa v134,1,v134 dst_sel:DWORD dst_unused:UNUSED_PAD src0_sel:DWORD src1_sel:WORD_1
v_mfma_f32_16x16x32_bf16 v[240:243],v[104:107],v[144:147],v[240:243]
s_waitcnt lgkmcnt(10)
ds_read_u16_d16_hi v131,v131
v_lshl_add_u64 v[154:155],v[156:157],0,v[158:159]
v_mul_f32_dpp v166,v166,v22 quad_perm:[1,0,3,2] row_mask:0xf bank_mask:0xf
v_mfma_f32_16x16x32_bf16 v[248:251],v[108:111],v[144:147],v[248:251]
v_lshlrev_b32_sdwa v133,1,v133 dst_sel:DWORD dst_unused:UNUSED_PAD src0_sel:DWORD src1_sel:WORD_1
v_mul_f32_dpp v167,v167,v23 quad_perm:[1,0,3,2] row_mask:0xf bank_mask:0xf
v_mfma_f32_16x16x32_bf16 v[212:215],v[88:91],v[152:155],v[212:215]
ds_read_u16 v132,v132
ds_read_b64_tr_b16 v[88:89],v6 offset:57472
v_lshlrev_b32_sdwa v135,1,v135 dst_sel:DWORD dst_unused:UNUSED_PAD src0_sel:DWORD src1_sel:WORD_1
v_mul_f32_dpp v172,v172,v124 quad_perm:[1,0,3,2] row_mask:0xf bank_mask:0xf
v_mfma_f32_16x16x32_bf16 v[220:223],v[92:95],v[152:155],v[220:223]
ds_read_u16_d16_hi v134,v134
ds_read_b64_tr_b16 v[90:91],v6 offset:59008
v_lshlrev_b32_sdwa v136,1,v136 dst_sel:DWORD dst_unused:UNUSED_PAD src0_sel:DWORD src1_sel:WORD_1
v_mul_f32_dpp v173,v173,v125 quad_perm:[1,0,3,2] row_mask:0xf bank_mask:0xf
v_mfma_f32_16x16x32_bf16 v[228:231],v[96:99],v[152:155],v[228:231]
ds_read_b64_tr_b16 v[92:93],v6 offset:57480
ds_read_b64_tr_b16 v[94:95],v6 offset:59016
v_lshlrev_b32_sdwa v138,1,v138 dst_sel:DWORD dst_unused:UNUSED_PAD src0_sel:DWORD src1_sel:WORD_1
v_mul_f32_dpp v174,v174,v126 quad_perm:[1,0,3,2] row_mask:0xf bank_mask:0xf
v_mfma_f32_16x16x32_bf16 v[236:239],v[100:103],v[152:155],v[236:239]
ds_read_u16 v133,v133
ds_read_b64_tr_b16 v[96:97],v6 offset:57536
v_lshlrev_b32_sdwa v137,1,v137 dst_sel:DWORD dst_unused:UNUSED_PAD src0_sel:DWORD src1_sel:WORD_1
v_mul_f32_dpp v175,v175,v127 quad_perm:[1,0,3,2] row_mask:0xf bank_mask:0xf
v_mfma_f32_16x16x32_bf16 v[244:247],v[104:107],v[152:155],v[244:247]
ds_read_u16_d16_hi v135,v135
ds_read_b64_tr_b16 v[98:99],v6 offset:59072
v_lshlrev_b32_sdwa v139,1,v139 dst_sel:DWORD dst_unused:UNUSED_PAD src0_sel:DWORD src1_sel:WORD_1
v_cvt_pk_bf16_f32 v20,v164,v165
v_mfma_f32_16x16x32_bf16 v[252:255],v[108:111],v[152:155],v[252:255]
ds_read_b64_tr_b16 v[100:101],v6 offset:57544
v_lshlrev_b32_sdwa v140,1,v140 dst_sel:DWORD dst_unused:UNUSED_PAD src0_sel:DWORD src1_sel:WORD_1
v_cvt_pk_bf16_f32 v21,v166,v167
v_mfma_f32_16x16x32_bf16 v[144:147],v[64:67],v[40:43],v[112:115]
ds_read_u16 v136,v136
ds_read_b64_tr_b16 v[102:103],v6 offset:59080
v_lshlrev_b32_sdwa v142,1,v142 dst_sel:DWORD dst_unused:UNUSED_PAD src0_sel:DWORD src1_sel:WORD_1
v_cvt_pk_bf16_f32 v22,v172,v173
v_mfma_f32_16x16x32_bf16 v[152:155],v[64:67],v[44:47],v[112:115]
ds_read_u16_d16_hi v138,v138
ds_read_b64_tr_b16 v[104:105],v6 offset:57600
v_lshlrev_b32_sdwa v141,1,v141 dst_sel:DWORD dst_unused:UNUSED_PAD src0_sel:DWORD src1_sel:WORD_1
v_cvt_pk_bf16_f32 v23,v174,v175
v_mfma_f32_16x16x32_bf16 v[156:159],v[68:71],v[44:47],v[116:119]
ds_read_b64_tr_b16 v[106:107],v6 offset:59136
ds_read_b64_tr_b16 v[108:109],v6 offset:57608
v_lshlrev_b32_sdwa v143,1,v143 dst_sel:DWORD dst_unused:UNUSED_PAD src0_sel:DWORD src1_sel:WORD_1
s_mov_b64 exec,s[12:13]
v_swap_b32 v32,v20
v_swap_b32 v33,v21
v_swap_b32 v34,v22
v_swap_b32 v35,v23
s_mov_b64 exec,-1
s_add_u32 s65,s61,4718592
buffer_store_dwordx4 v[32:35],v10,s[40:43],s65 offen offset:128 sc1
s_add_u32 s65,s61,4718592
buffer_store_dwordx4 v[20:23],v15,s[40:43],s65 offen offset:512 sc1
v_mfma_f32_16x16x32_bf16 v[148:151],v[68:71],v[40:43],v[116:119]
s_waitcnt lgkmcnt(15)
ds_read_u16 v137,v137
ds_read_b64_tr_b16 v[110:111],v6 offset:59144
v_mul_f32 v180,v180,v28
v_mul_f32 v181,v181,v29
v_mfma_f32_16x16x32_bf16 v[144:147],v[72:75],v[48:51],v[144:147]
ds_read_u16_d16_hi v139,v139
ds_read_b128 v[64:67],v7 offset:1536
v_mul_f32 v182,v182,v30
v_mul_f32 v183,v183,v31
v_mfma_f32_16x16x32_bf16 v[152:155],v[72:75],v[52:55],v[152:155]
ds_read_u16 v140,v140
s_nop 0
s_mov_b32 s86,0xbe801d5a
ds_read_b128 v[68:71],v7 offset:1568
v_mul_f32 v188,v188,v36
v_mul_f32 v189,v189,v37
v_mfma_f32_16x16x32_bf16 v[156:159],v[76:79],v[52:55],v[156:159]
ds_read_b128 v[72:75],v7 offset:1664
v_mul_f32 v190,v190,v38
v_mul_f32 v191,v191,v39
v_mfma_f32_16x16x32_bf16 v[148:151],v[76:79],v[48:51],v[148:151]
ds_read_u16_d16_hi v142,v142
ds_read_b128 v[76:79],v7 offset:1696
v_lshl_add_u64 v[128:129],v[128:129],0,v[130:131]
v_cvt_pk_bf16_f32 v28,v180,v181
v_mfma_f32_16x16x32_bf16 v[144:147],v[80:83],v[56:59],v[144:147]
s_waitcnt lgkmcnt(15)
v_lshl_add_u64 v[130:131],v[132:133],0,v[134:135]
v_cvt_pk_bf16_f32 v29,v182,v183
v_mfma_f32_16x16x32_bf16 v[152:155],v[80:83],v[60:63],v[152:155]
ds_read_u16 v141,v141
ds_read_b128 v[80:83],v7 offset:1792
v_cvt_pk_bf16_f32 v30,v188,v189
v_cvt_pk_bf16_f32 v31,v190,v191
v_mfma_f32_16x16x32_bf16 v[156:159],v[84:87],v[60:63],v[156:159]
ds_read_u16_d16_hi v143,v143
ds_read_b64_tr_b16 v[176:177],v6 offset:51312
v_mul_f32_dpp v196,v196,v160 quad_perm:[1,0,3,2] row_mask:0xf bank_mask:0xf
v_mul_f32_dpp v197,v197,v161 quad_perm:[1,0,3,2] row_mask:0xf bank_mask:0xf
v_mfma_f32_16x16x32_bf16 v[148:151],v[84:87],v[56:59],v[148:151]
ds_read_b64_tr_b16 v[178:179],v6 offset:52848
v_lshlrev_b32_sdwa v144,1,v144 dst_sel:DWORD dst_unused:UNUSED_PAD src0_sel:DWORD src1_sel:WORD_1
v_lshlrev_b32_sdwa v146,1,v146 dst_sel:DWORD dst_unused:UNUSED_PAD src0_sel:DWORD src1_sel:WORD_1
v_mfma_f32_16x16x32_bf16 v[208:211],v[88:91],v[128:131],v[208:211]
ds_read_u16 v144,v144
ds_read_b64_tr_b16 v[184:185],v6 offset:51320
v_lshlrev_b32_sdwa v145,1,v145 dst_sel:DWORD dst_unused:UNUSED_PAD src0_sel:DWORD src1_sel:WORD_1
v_mul_f32_dpp v198,v198,v162 quad_perm:[1,0,3,2] row_mask:0xf bank_mask:0xf
v_mul_f32_dpp v199,v199,v163 quad_perm:[1,0,3,2] row_mask:0xf bank_mask:0xf
v_mfma_f32_16x16x32_bf16 v[216:219],v[92:95],v[128:131],v[216:219]
ds_read_u16_d16_hi v146,v146
ds_read_b64_tr_b16 v[186:187],v6 offset:52856
v_mul_f32_dpp v204,v204,v168 quad_perm:[1,0,3,2] row_mask:0xf bank_mask:0xf
v_lshlrev_b32_sdwa v147,1,v147 dst_sel:DWORD dst_unused:UNUSED_PAD src0_sel:DWORD src1_sel:WORD_1
v_mul_f32_dpp v205,v205,v169 quad_perm:[1,0,3,2] row_mask:0xf bank_mask:0xf
v_mfma_f32_16x16x32_bf16 v[224:227],v[96:99],v[128:131],v[224:227]
s_waitcnt lgkmcnt(15)
ds_read_u16 v145,v145
ds_read_b64_tr_b16 v[192:193],v6 offset:51376
v_mul_f32_dpp v206,v206,v170 quad_perm:[1,0,3,2] row_mask:0xf bank_mask:0xf
v_mul_f32_dpp v207,v207,v171 quad_perm:[1,0,3,2] row_mask:0xf bank_mask:0xf
v_lshlrev_b32_sdwa v148,1,v148 dst_sel:DWORD dst_unused:UNUSED_PAD src0_sel:DWORD src1_sel:WORD_1
v_mfma_f32_16x16x32_bf16 v[232:235],v[100:103],v[128:131],v[232:235]
ds_read_u16_d16_hi v147,v147
ds_read_b64_tr_b16 v[194:195],v6 offset:52912
v_cvt_pk_bf16_f32 v36,v196,v197
v_cvt_pk_bf16_f32 v37,v198,v199
v_cvt_pk_bf16_f32 v38,v204,v205
v_mfma_f32_16x16x32_bf16 v[240:243],v[104:107],v[128:131],v[240:243]
ds_read_u16 v148,v148
ds_read_b64_tr_b16 v[200:201],v6 offset:51384
v_lshl_add_u64 v[136:137],v[136:137],0,v[138:139]
v_lshlrev_b32_sdwa v150,1,v150 dst_sel:DWORD dst_unused:UNUSED_PAD src0_sel:DWORD src1_sel:WORD_1
v_cvt_pk_bf16_f32 v39,v206,v207
v_mfma_f32_16x16x32_bf16 v[248:251],v[108:111],v[128:131],v[248:251]
ds_read_u16_d16_hi v150,v150
ds_read_b64_tr_b16 v[202:203],v6 offset:52920
s_mov_b64 exec,s[12:13]
v_swap_b32 v28,v36
v_swap_b32 v29,v37
v_swap_b32 v30,v38
v_swap_b32 v31,v39
s_mov_b64 exec,-1
s_add_u32 s65,s61,4718592
buffer_store_dwordx4 v[28:31],v10,s[40:43],s65 offen offset:256 sc1
s_add_u32 s65,s61,4718592
buffer_store_dwordx4 v[36:39],v15,s[40:43],s65 offen offset:640 sc1
s_waitcnt lgkmcnt(14)
v_lshl_add_u64 v[138:139],v[140:141],0,v[142:143]
v_lshlrev_b32_sdwa v149,1,v149 dst_sel:DWORD dst_unused:UNUSED_PAD src0_sel:DWORD src1_sel:WORD_1
v_lshlrev_b32_sdwa v151,1,v151 dst_sel:DWORD dst_unused:UNUSED_PAD src0_sel:DWORD src1_sel:WORD_1
v_mfma_f32_16x16x32_bf16 v[212:215],v[88:91],v[136:139],v[212:215]
ds_read_u16 v149,v149
ds_read_b64_tr_b16 v[164:165],v6 offset:51440
v_mfma_f32_16x16x32_bf16 v[220:223],v[92:95],v[136:139],v[220:223]
ds_read_u16_d16_hi v151,v151
ds_read_b64_tr_b16 v[166:167],v6 offset:52976
v_mfma_f32_16x16x32_bf16 v[228:231],v[96:99],v[136:139],v[228:231]
ds_read_b64_tr_b16 v[172:173],v6 offset:51448
v_lshlrev_b32_sdwa v152,1,v152 dst_sel:DWORD dst_unused:UNUSED_PAD src0_sel:DWORD src1_sel:WORD_1
v_mfma_f32_16x16x32_bf16 v[236:239],v[100:103],v[136:139],v[236:239]
ds_read_u16 v152,v152
ds_read_b64_tr_b16 v[174:175],v6 offset:52984
v_lshlrev_b32_sdwa v154,1,v154 dst_sel:DWORD dst_unused:UNUSED_PAD src0_sel:DWORD src1_sel:WORD_1
v_lshlrev_b32_sdwa v153,1,v153 dst_sel:DWORD dst_unused:UNUSED_PAD src0_sel:DWORD src1_sel:WORD_1
v_lshlrev_b32_sdwa v155,1,v155 dst_sel:DWORD dst_unused:UNUSED_PAD src0_sel:DWORD src1_sel:WORD_1
v_mfma_f32_16x16x32_bf16 v[244:247],v[104:107],v[136:139],v[244:247]
ds_read_u16_d16_hi v154,v154
ds_read_b128 v[84:87],v7 offset:1824
v_lshlrev_b32_sdwa v156,1,v156 dst_sel:DWORD dst_unused:UNUSED_PAD src0_sel:DWORD src1_sel:WORD_1
v_lshlrev_b32_sdwa v158,1,v158 dst_sel:DWORD dst_unused:UNUSED_PAD src0_sel:DWORD src1_sel:WORD_1
v_lshlrev_b32_sdwa v157,1,v157 dst_sel:DWORD dst_unused:UNUSED_PAD src0_sel:DWORD src1_sel:WORD_1
v_mfma_f32_16x16x32_bf16 v[252:255],v[108:111],v[136:139],v[252:255]
s_waitcnt lgkmcnt(15)
ds_read_u16 v153,v153
v_lshlrev_b32_sdwa v159,1,v159 dst_sel:DWORD dst_unused:UNUSED_PAD src0_sel:DWORD src1_sel:WORD_1
s_waitcnt lgkmcnt(15)
v_lshl_add_u64 v[144:145],v[144:145],0,v[146:147]
s_waitcnt lgkmcnt(7)
v_lshl_add_u64 v[146:147],v[148:149],0,v[150:151]
s_nop 1
v_mfma_f32_16x16x32_bf16 v[208:211],v[176:179],v[144:147],v[208:211]
ds_read_u16_d16_hi v155,v155
v_mfma_f32_16x16x32_bf16 v[216:219],v[184:187],v[144:147],v[216:219]
ds_read_u16 v156,v156
v_mfma_f32_16x16x32_bf16 v[224:227],v[192:195],v[144:147],v[224:227]
ds_read_u16_d16_hi v158,v158
v_mfma_f32_16x16x32_bf16 v[232:235],v[200:203],v[144:147],v[232:235]
s_waitcnt lgkmcnt(9)
ds_read_u16 v157,v157
v_mfma_f32_16x16x32_bf16 v[240:243],v[164:167],v[144:147],v[240:243]
s_waitcnt lgkmcnt(7)
ds_read_u16_d16_hi v159,v159
v_mfma_f32_16x16x32_bf16 v[248:251],v[172:175],v[144:147],v[248:251]
v_mul_f32 v208,v208,v64
s_waitcnt lgkmcnt(4)
v_lshl_add_u64 v[152:153],v[152:153],0,v[154:155]
s_waitcnt lgkmcnt(0)
v_lshl_add_u64 v[154:155],v[156:157],0,v[158:159]
v_mul_f32 v209,v209,v65
v_mul_f32 v210,v210,v66
v_mfma_f32_16x16x32_bf16 v[212:215],v[176:179],v[152:155],v[212:215]
v_mul_f32 v211,v211,v67
v_mul_f32 v216,v216,v68
v_mul_f32 v217,v217,v69
v_mfma_f32_16x16x32_bf16 v[220:223],v[184:187],v[152:155],v[220:223]
v_mul_f32 v218,v218,v70
v_mul_f32 v219,v219,v71
v_cvt_pk_bf16_f32 v40,v208,v209
v_mfma_f32_16x16x32_bf16 v[228:231],v[192:195],v[152:155],v[228:231]
v_cvt_pk_bf16_f32 v41,v210,v211
v_cvt_pk_bf16_f32 v42,v216,v217
v_cvt_pk_bf16_f32 v43,v218,v219
v_mfma_f32_16x16x32_bf16 v[236:239],v[200:203],v[152:155],v[236:239]
v_mul_f32_dpp v224,v224,v72 quad_perm:[1,0,3,2] row_mask:0xf bank_mask:0xf
v_mul_f32_dpp v225,v225,v73 quad_perm:[1,0,3,2] row_mask:0xf bank_mask:0xf
v_mul_f32_dpp v226,v226,v74 quad_perm:[1,0,3,2] row_mask:0xf bank_mask:0xf
v_mfma_f32_16x16x32_bf16 v[244:247],v[164:167],v[152:155],v[244:247]
v_mul_f32_dpp v227,v227,v75 quad_perm:[1,0,3,2] row_mask:0xf bank_mask:0xf
v_mul_f32_dpp v232,v232,v76 quad_perm:[1,0,3,2] row_mask:0xf bank_mask:0xf
v_mul_f32_dpp v233,v233,v77 quad_perm:[1,0,3,2] row_mask:0xf bank_mask:0xf
v_mfma_f32_16x16x32_bf16 v[252:255],v[172:175],v[152:155],v[252:255]
v_mul_f32_dpp v234,v234,v78 quad_perm:[1,0,3,2] row_mask:0xf bank_mask:0xf
v_mul_f32_dpp v235,v235,v79 quad_perm:[1,0,3,2] row_mask:0xf bank_mask:0xf
v_cvt_pk_bf16_f32 v48,v224,v225
v_cvt_pk_bf16_f32 v49,v226,v227
v_cvt_pk_bf16_f32 v50,v232,v233
v_cvt_pk_bf16_f32 v51,v234,v235
s_mov_b64 exec,s[12:13]
v_swap_b32 v40,v48
v_swap_b32 v41,v49
v_swap_b32 v42,v50
v_swap_b32 v43,v51
s_mov_b64 exec,-1
s_add_u32 s65,s61,4816896
buffer_store_dwordx4 v[40:43],v10,s[40:43],s65 offen offset:0 sc1
s_add_u32 s65,s61,4816896
buffer_store_dwordx4 v[48:51],v15,s[40:43],s65 offen offset:384 sc1
v_mul_f32 v240,v240,v80
v_mul_f32 v241,v241,v81
v_mul_f32 v242,v242,v82
v_mul_f32 v243,v243,v83
v_mul_f32 v248,v248,v84
v_mul_f32 v249,v249,v85
v_mul_f32 v250,v250,v86
v_mul_f32 v251,v251,v87
v_cvt_pk_bf16_f32 v56,v240,v241
v_cvt_pk_bf16_f32 v57,v242,v243
v_cvt_pk_bf16_f32 v58,v248,v249
v_cvt_pk_bf16_f32 v59,v250,v251
v_mul_f32_dpp v212,v212,v64 quad_perm:[1,0,3,2] row_mask:0xf bank_mask:0xf
v_mul_f32_dpp v213,v213,v65 quad_perm:[1,0,3,2] row_mask:0xf bank_mask:0xf
v_mul_f32_dpp v214,v214,v66 quad_perm:[1,0,3,2] row_mask:0xf bank_mask:0xf
v_mul_f32_dpp v215,v215,v67 quad_perm:[1,0,3,2] row_mask:0xf bank_mask:0xf
v_mul_f32_dpp v220,v220,v68 quad_perm:[1,0,3,2] row_mask:0xf bank_mask:0xf
v_mul_f32_dpp v221,v221,v69 quad_perm:[1,0,3,2] row_mask:0xf bank_mask:0xf
v_mul_f32_dpp v222,v222,v70 quad_perm:[1,0,3,2] row_mask:0xf bank_mask:0xf
v_mul_f32_dpp v223,v223,v71 quad_perm:[1,0,3,2] row_mask:0xf bank_mask:0xf
v_cvt_pk_bf16_f32 v44,v212,v213
v_cvt_pk_bf16_f32 v45,v214,v215
v_cvt_pk_bf16_f32 v46,v220,v221
v_cvt_pk_bf16_f32 v47,v222,v223
s_mov_b64 exec,s[12:13]
v_swap_b32 v56,v44
v_swap_b32 v57,v45
v_swap_b32 v58,v46
v_swap_b32 v59,v47
s_mov_b64 exec,-1
s_add_u32 s65,s61,4816896
buffer_store_dwordx4 v[56:59],v10,s[40:43],s65 offen offset:128 sc1
s_add_u32 s65,s61,4816896
buffer_store_dwordx4 v[44:47],v15,s[40:43],s65 offen offset:512 sc1
v_mul_f32 v228,v228,v72
v_mul_f32 v229,v229,v73
v_mul_f32 v230,v230,v74
v_mul_f32 v231,v231,v75
v_mul_f32 v236,v236,v76
v_mul_f32 v237,v237,v77
v_mul_f32 v238,v238,v78
v_mul_f32 v239,v239,v79
v_cvt_pk_bf16_f32 v52,v228,v229
v_cvt_pk_bf16_f32 v53,v230,v231
v_cvt_pk_bf16_f32 v54,v236,v237
v_cvt_pk_bf16_f32 v55,v238,v239
v_mul_f32_dpp v244,v244,v80 quad_perm:[1,0,3,2] row_mask:0xf bank_mask:0xf
v_mul_f32_dpp v245,v245,v81 quad_perm:[1,0,3,2] row_mask:0xf bank_mask:0xf
v_mul_f32_dpp v246,v246,v82 quad_perm:[1,0,3,2] row_mask:0xf bank_mask:0xf
v_mul_f32_dpp v247,v247,v83 quad_perm:[1,0,3,2] row_mask:0xf bank_mask:0xf
v_mul_f32_dpp v252,v252,v84 quad_perm:[1,0,3,2] row_mask:0xf bank_mask:0xf
v_mul_f32_dpp v253,v253,v85 quad_perm:[1,0,3,2] row_mask:0xf bank_mask:0xf
v_mul_f32_dpp v254,v254,v86 quad_perm:[1,0,3,2] row_mask:0xf bank_mask:0xf
v_mul_f32_dpp v255,v255,v87 quad_perm:[1,0,3,2] row_mask:0xf bank_mask:0xf
v_cvt_pk_bf16_f32 v60,v244,v245
v_cvt_pk_bf16_f32 v61,v246,v247
v_cvt_pk_bf16_f32 v62,v252,v253
v_cvt_pk_bf16_f32 v63,v254,v255
s_mov_b64 exec,s[12:13]
v_swap_b32 v52,v60
v_swap_b32 v53,v61
v_swap_b32 v54,v62
v_swap_b32 v55,v63
s_mov_b64 exec,-1
s_add_u32 s65,s61,4816896
buffer_store_dwordx4 v[52:55],v10,s[40:43],s65 offen offset:256 sc1
s_add_u32 s65,s61,4816896
buffer_store_dwordx4 v[60:63],v15,s[40:43],s65 offen offset:640 sc1
v_and_b32 v1,0x7c007c00,v1
v_lshrrev_b32 v2,16,v1
v_and_b32 v1,0xffff,v1
v_max_u32 v1,v1,v2
v_cmp_eq_u32 vcc,0x7c00,v1
s_or_b32 s64,vcc_lo,vcc_hi
v_mov_b32 v1,s64
v_mov_b32 v2,8192
s_mov_b64 exec,1
ds_or_b32 v2,v1 offset:3456
s_mov_b64 exec,-1
s_waitcnt lgkmcnt(0)
s_barrier
ds_read_b32 v1,v2 offset:3456
s_waitcnt lgkmcnt(0)
v_readfirstlane_b32 s64,v1
s_cmp_lg_u32 s64,0
s_cbranch_scc1 N_fallback_%=
s_endpgm
N_mainend_%=:
N_fallback_%=:
s_setprio 0
s_waitcnt vmcnt(0) lgkmcnt(0)
s_barrier
v_lshrrev_b32 v237,6,%1
v_and_b32 v232,63,%1
v_readfirstlane_b32 s52,v237
v_lshlrev_b32 v248,4,v232
v_min_u32 v244,383,%1
v_lshlrev_b32 v238,1,v244
global_load_ushort v244,v238,s[26:27]
v_min_u32 v245,95,%1
v_lshlrev_b32 v239,1,v245
global_load_ushort v245,v239,s[30:31]
global_load_ushort v246,v239,s[32:33]
s_mov_b64 s[92:93],s[22:23]
s_and_b32 s93,s93,0xffff
s_mov_b32 s94,1728
s_mov_b32 s95,0x00020000
s_lshl_b32 s71,s52,11
s_add_u32 s71,s71,62208
s_mov_b32 m0,s71
s_movk_i32 s64,1024
buffer_load_dwordx4 v248,s[92:95],0 offen lds
s_add_u32 m0,s71,1024
s_nop 0
buffer_load_dwordx4 v248,s[92:95],s64 offen lds
v_mov_b32 v250,s71
s_mov_b64 s[36:37],s[20:21]
s_mov_b64 s[40:41],s[34:35]
s_mov_b64 s[44:45],s[24:25]
s_mov_b64 s[48:49],s[28:29]
s_sub_u32 s36,s36,192
s_subb_u32 s37,s37,0
s_and_b32 s37,s37,0xffff
s_mov_b32 s38,50331840
s_mov_b32 s39,0x00020000
s_and_b32 s41,s41,0xffff
s_mov_b32 s42,50331648
s_mov_b32 s43,0x00020000
s_and_b32 s45,s45,0xffff
s_mov_b32 s46,73728
s_mov_b32 s47,0x00020000
s_and_b32 s49,s49,0xffff
s_mov_b32 s50,73728
s_mov_b32 s51,0x00020000
s_mov_b32 s60,-1
s_mov_b32 s61,0xffff
s_mov_b32 s62,-1
s_mov_b32 s63,0x3fffff
s_and_b32 s64,%0,7
s_lshr_b32 s70,%0,3
s_lshl_b32 s56,s64,6
s_mul_i32 s57,s70,3072
s_and_b32 s66,s52,3
s_mul_i32 s68,s66,864
s_mul_i32 s67,s66,54
s_mul_i32 s58,s66,1536
v_add_u32 v237,s67,v232
v_mul_u32_u24 v238,0xaaab,v237
v_lshrrev_b32 v238,19,v238
v_mul_u32_u24 v239,12,v238
v_sub_u32 v239,v237,v239
v_bfe_u32 v237,v238,2,2
v_xor_b32 v239,v239,v237
v_lshlrev_b32 v239,4,v239
v_mul_u32_u24 v237,192,v238
v_add_u32 v247,v237,v239
s_cmp_eq_u32 s70,0
s_cselect_b32 s71,0,99
s_cmp_eq_u32 s70,31
s_cselect_b32 s71,17,s71
v_cmp_eq_u32 vcc,s71,v238
v_mov_b32 v237,0x7fff0000
s_sub_u32 s67,s56,1
s_mul_i32 s59,s67,98304
s_add_u32 s59,s59,s57
v_cndmask_b32 v247,v247,v237,vcc
s_lshr_b32 s66,s52,2
s_mul_i32 s64,s66,3456
s_add_u32 s64,s64,s68
s_mul_i32 s65,s66,98304
s_add_u32 s65,s65,s59
s_add_u32 s67,s67,s66
s_cmp_lt_i32 s67,0
s_cselect_b32 s71,0x70000000,s65
s_mov_b64 exec,s[62:63]
s_add_u32 m0,s64,0
s_nop 0
buffer_load_dwordx4 v247,s[36:39],s71 offen lds
s_add_u32 m0,s64,6912
s_add_u32 s71,s65,196608
buffer_load_dwordx4 v247,s[36:39],s71 offen lds
s_add_u32 m0,s64,13824
s_add_u32 s71,s65,393216
buffer_load_dwordx4 v247,s[36:39],s71 offen lds
s_add_u32 m0,s64,20736
s_add_u32 s71,s65,589824
buffer_load_dwordx4 v247,s[36:39],s71 offen lds
s_add_u32 m0,s64,27648
s_add_u32 s71,s65,786432
buffer_load_dwordx4 v247,s[36:39],s71 offen lds
s_add_u32 m0,s64,34560
s_add_u32 s71,s65,983040
buffer_load_dwordx4 v247,s[36:39],s71 offen lds
s_add_u32 m0,s64,41472
s_add_u32 s71,s65,1179648
buffer_load_dwordx4 v247,s[36:39],s71 offen lds
s_add_u32 m0,s64,48384
s_add_u32 s71,s65,1376256
buffer_load_dwordx4 v247,s[36:39],s71 offen lds
s_add_u32 m0,s64,55296
s_add_u32 s71,s65,1572864
buffer_load_dwordx4 v247,s[36:39],s71 offen lds
s_mov_b64 exec,-1
v_lshrrev_b32 v237,2,v232
v_mul_u32_u24 v237,768,v237
v_and_b32 v238,3,v232
v_lshl_add_u32 v237,v238,4,v237
s_and_b32 s66,s52,3
s_mul_i32 s71,s66,18432
v_add_u32 v27,s71,v237
v_add_u32 v28,s58,v248
s_mov_b32 s54,62208
s_mov_b32 s53,0
s_mov_b32 s96,0x70000000
s_add_u32 s68,s68,s54
s_add_u32 s59,s59,1572864
s_lshl_b32 s66,s52,1
s_add_u32 s66,s66,s56
s_mul_i32 s69,s66,98304
s_add_u32 s69,s69,s57
s_cmp_ge_u32 s52,4
s_cbranch_scc1 L_noW_%=
s_mov_b64 exec,s[60:61]
s_add_u32 m0,s58,124416
s_movk_i32 s64,0
buffer_load_dwordx4 v27,s[44:47],s64 offen lds
s_add_u32 m0,s58,125184
s_movk_i32 s65,9216
buffer_load_dwordx4 v27,s[44:47],s65 offen lds
s_mov_b64 exec,-1
s_mov_b64 exec,s[60:61]
s_add_u32 m0,s58,142848
s_mov_b32 s64,0
buffer_load_dwordx4 v28,s[48:51],s64 offen lds
s_add_u32 m0,s58,143616
s_mov_b32 s65,768
buffer_load_dwordx4 v28,s[48:51],s65 offen lds
s_mov_b64 exec,-1
s_mov_b64 exec,s[60:61]
s_add_u32 m0,s58,130560
s_movk_i32 s64,64
buffer_load_dwordx4 v27,s[44:47],s64 offen lds
s_add_u32 m0,s58,131328
s_movk_i32 s65,9280
buffer_load_dwordx4 v27,s[44:47],s65 offen lds
s_mov_b64 exec,-1
s_mov_b64 exec,s[60:61]
s_add_u32 m0,s58,136704
s_movk_i32 s64,128
buffer_load_dwordx4 v27,s[44:47],s64 offen lds
s_add_u32 m0,s58,137472
s_movk_i32 s65,9344
buffer_load_dwordx4 v27,s[44:47],s65 offen lds
s_mov_b64 exec,-1
s_branch L_W_%=
L_noW_%=:
v_mov_b32 v27,v247
s_mul_i32 s64,s70,2304
s_and_b32 s65,s52,3
s_mul_i32 s65,s65,576
s_add_u32 s64,s64,s65
v_min_u32 v237,560,v248
buffer_load_dwordx4 v[252:255],v237,s[44:47],s64 offen
buffer_load_dwordx4 v[252:255],v237,s[48:51],s64 offen
L_W_%=:
v_and_b32 v233,15,v232
v_lshrrev_b32 v234,4,v232
v_lshrrev_b32 v235,5,v232
v_and_b32 v237,1,v234
v_lshlrev_b32 v237,1,v237
v_or_b32 v236,v237,v235
v_lshrrev_b32 v237,3,v233
v_lshlrev_b32 v237,2,v237
v_and_b32 v238,3,v233
v_or_b32 v241,v237,v238
v_lshrrev_b32 v237,2,v233
v_and_b32 v242,1,v237
v_sub_u32 v237,v236,v242
v_cmp_gt_u32 vcc,3,v237
v_mov_b32 v238,0
v_mov_b32 v239,-1
v_cndmask_b32 v237,v238,v237,vcc
v_cndmask_b32 v249,v238,v239,vcc
v_mul_u32_u24 v237,576,v237
v_lshl_add_u32 v237,v241,1,v237
v_add_u32 v250,v250,v237
v_lshrrev_b32 v237,1,v241
v_cmp_eq_u32 vcc,0,v237
s_nop 1
v_cndmask_b32 v32,v238,v249,vcc
v_cmp_eq_u32 vcc,1,v237
s_nop 1
v_cndmask_b32 v33,v238,v249,vcc
v_cmp_eq_u32 vcc,2,v237
s_nop 1
v_cndmask_b32 v34,v238,v249,vcc
v_cmp_eq_u32 vcc,3,v237
s_nop 1
v_cndmask_b32 v35,v238,v249,vcc
v_and_b32 v243,1,v241
v_lshlrev_b32 v243,4,v243
s_mul_i32 s71,s52,6912
v_add_u32 v237,0,v233
v_bfe_u32 v238,v237,2,2
v_mul_u32_u24 v239,18,v236
v_add_u32 v239,v239,v237
v_mul_u32_u24 v239,192,v239
v_add_u32 v239,s71,v239
v_xor_b32 v240,0,v238
v_lshl_add_u32 v4,v240,4,v239
v_xor_b32 v240,1,v238
v_lshl_add_u32 v5,v240,4,v239
v_xor_b32 v240,2,v238
v_lshl_add_u32 v6,v240,4,v239
v_xor_b32 v240,3,v238
v_lshl_add_u32 v7,v240,4,v239
v_add_u32 v237,1,v233
v_bfe_u32 v238,v237,2,2
v_mul_u32_u24 v239,18,v236
v_add_u32 v239,v239,v237
v_mul_u32_u24 v239,192,v239
v_add_u32 v239,s71,v239
v_xor_b32 v240,0,v238
v_lshl_add_u32 v8,v240,4,v239
v_xor_b32 v240,1,v238
v_lshl_add_u32 v9,v240,4,v239
v_xor_b32 v240,2,v238
v_lshl_add_u32 v10,v240,4,v239
v_xor_b32 v240,3,v238
v_lshl_add_u32 v11,v240,4,v239
v_add_u32 v237,2,v233
v_bfe_u32 v238,v237,2,2
v_mul_u32_u24 v239,18,v236
v_add_u32 v239,v239,v237
v_mul_u32_u24 v239,192,v239
v_add_u32 v239,s71,v239
v_xor_b32 v240,0,v238
v_lshl_add_u32 v12,v240,4,v239
v_xor_b32 v240,1,v238
v_lshl_add_u32 v13,v240,4,v239
v_xor_b32 v240,2,v238
v_lshl_add_u32 v14,v240,4,v239
v_xor_b32 v240,3,v238
v_lshl_add_u32 v15,v240,4,v239
v_lshrrev_b32 v237,2,v233
v_lshl_add_u32 v237,v235,2,v237
v_and_b32 v238,1,v234
v_lshlrev_b32 v238,5,v238
v_and_b32 v239,3,v233
v_lshl_add_u32 v238,v239,3,v238
v_add_u32 v238,124416,v238
v_lshl_add_u32 v16,v237,6,v238
v_mul_u32_u24 v237,192,v237
v_add_u32 v17,v237,v238
v_add_u32 v17,18432,v17
v_lshlrev_b32 v237,4,v235
v_add_u32 v18,161280,v237
v_add_u32 v237,s71,v248
v_add_u32 v19,3648,v237
v_and_b32 v237,1,v234
v_add_u32 v237,1,v237
v_mul_u32_u24 v237,18,v237
v_add_u32 v238,1,v233
v_add_u32 v237,v237,v238
v_mul_u32_u24 v237,192,v237
v_add_u32 v237,s71,v237
v_lshl_add_u32 v237,v235,3,v237
v_bfe_u32 v238,v238,2,2
v_xor_b32 v240,0,v238
v_lshl_add_u32 v20,v240,4,v237
v_xor_b32 v240,1,v238
v_lshl_add_u32 v21,v240,4,v237
v_xor_b32 v240,2,v238
v_lshl_add_u32 v22,v240,4,v237
v_xor_b32 v240,3,v238
v_lshl_add_u32 v23,v240,4,v237
v_add_u32 v237,0,v232
v_mul_u32_u24 v238,0xaaab,v237
v_lshrrev_b32 v238,19,v238
v_mul_u32_u24 v239,12,v238
v_sub_u32 v239,v237,v239
v_add_u32 v237,1,v238
v_bfe_u32 v237,v237,2,2
v_xor_b32 v239,v239,v237
v_lshlrev_b32 v239,4,v239
v_mul_u32_u24 v237,192,v238
v_add_u32 v24,v237,v239
v_add_u32 v237,64,v232
v_mul_u32_u24 v238,0xaaab,v237
v_lshrrev_b32 v238,19,v238
v_mul_u32_u24 v239,12,v238
v_sub_u32 v239,v237,v239
v_add_u32 v237,1,v238
v_bfe_u32 v237,v237,2,2
v_xor_b32 v239,v239,v237
v_lshlrev_b32 v239,4,v239
v_mul_u32_u24 v237,192,v238
v_add_u32 v25,v237,v239
v_add_u32 v237,128,v232
v_mul_u32_u24 v238,0xaaab,v237
v_lshrrev_b32 v238,19,v238
v_mul_u32_u24 v239,12,v238
v_sub_u32 v239,v237,v239
v_add_u32 v237,1,v238
v_bfe_u32 v237,v237,2,2
v_xor_b32 v239,v239,v237
v_lshlrev_b32 v239,4,v239
v_mul_u32_u24 v237,192,v238
v_add_u32 v26,v237,v239
s_cmp_ge_u32 s52,4
s_cbranch_scc1 L_wB_%=
s_waitcnt vmcnt(17)
s_branch L_wA_%=
L_wB_%=:
s_waitcnt vmcnt(11)
L_wA_%=:
ds_read_u16 v36,v250 offset:0
ds_read_u16 v37,v250 offset:16
ds_read_u16 v38,v250 offset:32
ds_read_u16 v39,v250 offset:48
ds_read_u16 v40,v250 offset:64
ds_read_u16 v41,v250 offset:80
ds_read_u16 v42,v250 offset:96
ds_read_u16 v43,v250 offset:112
ds_read_u16 v44,v250 offset:128
ds_read_u16 v45,v250 offset:144
ds_read_u16 v46,v250 offset:160
ds_read_u16 v47,v250 offset:176
ds_read_u16 v48,v250 offset:192
ds_read_u16 v49,v250 offset:208
ds_read_u16 v50,v250 offset:224
ds_read_u16 v51,v250 offset:240
ds_read_u16 v52,v250 offset:256
ds_read_u16 v53,v250 offset:272
ds_read_u16 v54,v250 offset:288
ds_read_u16 v55,v250 offset:304
ds_read_u16 v56,v250 offset:320
ds_read_u16 v57,v250 offset:336
ds_read_u16 v58,v250 offset:352
ds_read_u16 v59,v250 offset:368
ds_read_u16 v60,v250 offset:384
ds_read_u16 v61,v250 offset:400
ds_read_u16 v62,v250 offset:416
ds_read_u16 v63,v250 offset:432
ds_read_u16 v64,v250 offset:448
ds_read_u16 v65,v250 offset:464
ds_read_u16 v66,v250 offset:480
ds_read_u16 v67,v250 offset:496
ds_read_u16 v68,v250 offset:512
ds_read_u16 v69,v250 offset:528
ds_read_u16 v70,v250 offset:544
ds_read_u16 v71,v250 offset:560
v_lshlrev_b32 v244,16,v244
v_lshlrev_b32 v245,16,v245
v_lshlrev_b32 v246,16,v246
v_mul_f32 v244,0xbeeff558,v244
v_mul_f32 v245,0xbeeff558,v245
v_mul_f32 v246,0xc0088e99,v246
v_min_u32 v237,383,%1
v_lshlrev_b32 v237,2,v237
v_add_u32 v237,161280,v237
ds_write_b32 v237,v244 offset:0
v_min_u32 v237,95,%1
v_lshlrev_b32 v237,2,v237
v_add_u32 v237,161280,v237
ds_write_b32 v237,v245 offset:1920
ds_write_b32 v237,v246 offset:1536
s_waitcnt lgkmcnt(0)
v_lshlrev_b32 v36,v243,v36
v_lshlrev_b32 v37,v243,v37
v_lshlrev_b32 v38,v243,v38
v_lshlrev_b32 v39,v243,v39
v_lshlrev_b32 v40,v243,v40
v_lshlrev_b32 v41,v243,v41
v_lshlrev_b32 v42,v243,v42
v_lshlrev_b32 v43,v243,v43
v_lshlrev_b32 v44,v243,v44
v_lshlrev_b32 v45,v243,v45
v_lshlrev_b32 v46,v243,v46
v_lshlrev_b32 v47,v243,v47
v_lshlrev_b32 v48,v243,v48
v_lshlrev_b32 v49,v243,v49
v_lshlrev_b32 v50,v243,v50
v_lshlrev_b32 v51,v243,v51
v_lshlrev_b32 v52,v243,v52
v_lshlrev_b32 v53,v243,v53
v_lshlrev_b32 v54,v243,v54
v_lshlrev_b32 v55,v243,v55
v_lshlrev_b32 v56,v243,v56
v_lshlrev_b32 v57,v243,v57
v_lshlrev_b32 v58,v243,v58
v_lshlrev_b32 v59,v243,v59
v_lshlrev_b32 v60,v243,v60
v_lshlrev_b32 v61,v243,v61
v_lshlrev_b32 v62,v243,v62
v_lshlrev_b32 v63,v243,v63
v_lshlrev_b32 v64,v243,v64
v_lshlrev_b32 v65,v243,v65
v_lshlrev_b32 v66,v243,v66
v_lshlrev_b32 v67,v243,v67
v_lshlrev_b32 v68,v243,v68
v_lshlrev_b32 v69,v243,v69
v_lshlrev_b32 v70,v243,v70
v_lshlrev_b32 v71,v243,v71
s_cmp_ge_u32 s52,4
s_cbranch_scc1 L_fB_%=
s_waitcnt vmcnt(6)
s_branch L_fA_%=
L_fB_%=:
s_waitcnt vmcnt(2)
L_fA_%=:
s_cmp_ge_u32 s52,4
s_cbranch_scc1 L_pass1_%=
L_pass0_%=:
s_barrier
ds_read_b128 v[200:203],v4 offset:0
ds_read_b128 v[204:207],v8 offset:0
ds_read_b128 v[208:211],v12 offset:0
ds_read_b128 v[212:215],v5 offset:0
ds_read_b128 v[216:219],v9 offset:0
ds_read_b128 v[220:223],v13 offset:0
ds_read_b128 v[96:99],v18 offset:1920
ds_read_b128 v[100:103],v18 offset:1952
ds_read_b128 v[104:107],v18 offset:1984
ds_read_b128 v[108:111],v18 offset:2016
ds_read_b128 v[112:115],v18 offset:2048
ds_read_b128 v[116:119],v18 offset:2080
ds_read_b128 v[120:123],v18 offset:2112
ds_read_b128 v[124:127],v18 offset:2144
ds_read_b128 v[128:131],v18 offset:2176
ds_read_b128 v[132:135],v18 offset:2208
ds_read_b128 v[136:139],v18 offset:2240
ds_read_b128 v[140:143],v18 offset:2272
v_and_b32 v72,v32,v36
v_and_b32 v73,v33,v36
v_and_b32 v74,v34,v36
v_and_b32 v75,v35,v36
v_and_b32 v76,v32,v48
v_and_b32 v77,v33,v48
v_and_b32 v78,v34,v48
v_and_b32 v79,v35,v48
v_and_b32 v80,v32,v60
v_and_b32 v81,v33,v60
v_and_b32 v82,v34,v60
v_and_b32 v83,v35,v60
v_and_b32 v84,v32,v37
v_and_b32 v85,v33,v37
v_and_b32 v86,v34,v37
v_and_b32 v87,v35,v37
v_and_b32 v88,v32,v49
v_and_b32 v89,v33,v49
v_and_b32 v90,v34,v49
v_and_b32 v91,v35,v49
v_and_b32 v92,v32,v61
v_and_b32 v93,v33,v61
v_and_b32 v94,v34,v61
v_and_b32 v95,v35,v61
s_setprio 1
s_waitcnt lgkmcnt(15)
v_mfma_f32_16x16x32_bf16 v[144:147],v[72:75],v[200:203],0
v_mfma_f32_16x16x32_bf16 v[144:147],v[76:79],v[204:207],v[144:147]
v_mfma_f32_16x16x32_bf16 v[144:147],v[80:83],v[208:211],v[144:147]
ds_read_b128 v[200:203],v6 offset:0
ds_read_b128 v[204:207],v10 offset:0
ds_read_b128 v[208:211],v14 offset:0
s_setprio 0
v_and_b32 v72,v32,v38
v_and_b32 v73,v33,v38
v_and_b32 v74,v34,v38
v_and_b32 v75,v35,v38
v_and_b32 v76,v32,v50
v_and_b32 v77,v33,v50
v_and_b32 v78,v34,v50
v_and_b32 v79,v35,v50
v_and_b32 v80,v32,v62
v_and_b32 v81,v33,v62
v_and_b32 v82,v34,v62
v_and_b32 v83,v35,v62
v_mul_f32 v237,v144,v144
v_mul_f32 v236,v145,v145
v_mul_f32 v239,v146,v146
v_mul_f32 v238,v147,v147
s_setprio 1
s_waitcnt lgkmcnt(15)
v_mfma_f32_16x16x32_bf16 v[148:151],v[84:87],v[212:215],0
v_mfma_f32_16x16x32_bf16 v[148:151],v[88:91],v[216:219],v[148:151]
v_mfma_f32_16x16x32_bf16 v[148:151],v[92:95],v[220:223],v[148:151]
ds_read_b128 v[212:215],v7 offset:0
ds_read_b128 v[216:219],v11 offset:0
ds_read_b128 v[220:223],v15 offset:0
s_setprio 0
v_and_b32 v84,v32,v39
v_and_b32 v85,v33,v39
v_and_b32 v86,v34,v39
v_and_b32 v87,v35,v39
v_and_b32 v88,v32,v51
v_and_b32 v89,v33,v51
v_and_b32 v90,v34,v51
v_and_b32 v91,v35,v51
v_and_b32 v92,v32,v63
v_and_b32 v93,v33,v63
v_and_b32 v94,v34,v63
v_and_b32 v95,v35,v63
v_add_f32 v232,v144,v148
v_fmac_f32 v237,v148,v148
v_add_f32 v233,v145,v149
v_fmac_f32 v236,v149,v149
v_add_f32 v234,v146,v150
v_fmac_f32 v239,v150,v150
v_add_f32 v235,v147,v151
v_fmac_f32 v238,v151,v151
s_setprio 1
s_waitcnt lgkmcnt(3)
v_mfma_f32_16x16x32_bf16 v[152:155],v[72:75],v[200:203],0
v_mfma_f32_16x16x32_bf16 v[152:155],v[76:79],v[204:207],v[152:155]
v_mfma_f32_16x16x32_bf16 v[152:155],v[80:83],v[208:211],v[152:155]
ds_read_b128 v[200:203],v4 offset:64
ds_read_b128 v[204:207],v8 offset:64
ds_read_b128 v[208:211],v12 offset:64
s_setprio 0
v_and_b32 v72,v32,v40
v_and_b32 v73,v33,v40
v_and_b32 v74,v34,v40
v_and_b32 v75,v35,v40
v_and_b32 v76,v32,v52
v_and_b32 v77,v33,v52
v_and_b32 v78,v34,v52
v_and_b32 v79,v35,v52
v_and_b32 v80,v32,v64
v_and_b32 v81,v33,v64
v_and_b32 v82,v34,v64
v_and_b32 v83,v35,v64
v_add_f32 v232,v232,v152
v_fmac_f32 v237,v152,v152
v_add_f32 v233,v233,v153
v_fmac_f32 v236,v153,v153
v_add_f32 v234,v234,v154
v_fmac_f32 v239,v154,v154
v_add_f32 v235,v235,v155
v_fmac_f32 v238,v155,v155
s_setprio 1
s_waitcnt lgkmcnt(3)
v_mfma_f32_16x16x32_bf16 v[156:159],v[84:87],v[212:215],0
v_mfma_f32_16x16x32_bf16 v[156:159],v[88:91],v[216:219],v[156:159]
v_mfma_f32_16x16x32_bf16 v[156:159],v[92:95],v[220:223],v[156:159]
ds_read_b128 v[212:215],v5 offset:64
ds_read_b128 v[216:219],v9 offset:64
ds_read_b128 v[220:223],v13 offset:64
s_setprio 0
v_and_b32 v84,v32,v41
v_and_b32 v85,v33,v41
v_and_b32 v86,v34,v41
v_and_b32 v87,v35,v41
v_and_b32 v88,v32,v53
v_and_b32 v89,v33,v53
v_and_b32 v90,v34,v53
v_and_b32 v91,v35,v53
v_and_b32 v92,v32,v65
v_and_b32 v93,v33,v65
v_and_b32 v94,v34,v65
v_and_b32 v95,v35,v65
v_add_f32 v232,v232,v156
v_fmac_f32 v237,v156,v156
v_add_f32 v233,v233,v157
v_fmac_f32 v236,v157,v157
v_add_f32 v234,v234,v158
v_fmac_f32 v239,v158,v158
v_add_f32 v235,v235,v159
v_fmac_f32 v238,v159,v159
s_setprio 1
s_waitcnt lgkmcnt(3)
v_mfma_f32_16x16x32_bf16 v[160:163],v[72:75],v[200:203],0
v_mfma_f32_16x16x32_bf16 v[160:163],v[76:79],v[204:207],v[160:163]
v_mfma_f32_16x16x32_bf16 v[160:163],v[80:83],v[208:211],v[160:163]
ds_read_b128 v[200:203],v6 offset:64
ds_read_b128 v[204:207],v10 offset:64
ds_read_b128 v[208:211],v14 offset:64
s_setprio 0
v_and_b32 v72,v32,v42
v_and_b32 v73,v33,v42
v_and_b32 v74,v34,v42
v_and_b32 v75,v35,v42
v_and_b32 v76,v32,v54
v_and_b32 v77,v33,v54
v_and_b32 v78,v34,v54
v_and_b32 v79,v35,v54
v_and_b32 v80,v32,v66
v_and_b32 v81,v33,v66
v_and_b32 v82,v34,v66
v_and_b32 v83,v35,v66
v_add_f32 v232,v232,v160
v_fmac_f32 v237,v160,v160
v_add_f32 v233,v233,v161
v_fmac_f32 v236,v161,v161
v_add_f32 v234,v234,v162
v_fmac_f32 v239,v162,v162
v_add_f32 v235,v235,v163
v_fmac_f32 v238,v163,v163
s_setprio 1
s_waitcnt lgkmcnt(3)
v_mfma_f32_16x16x32_bf16 v[164:167],v[84:87],v[212:215],0
v_mfma_f32_16x16x32_bf16 v[164:167],v[88:91],v[216:219],v[164:167]
v_mfma_f32_16x16x32_bf16 v[164:167],v[92:95],v[220:223],v[164:167]
ds_read_b128 v[212:215],v7 offset:64
ds_read_b128 v[216:219],v11 offset:64
ds_read_b128 v[220:223],v15 offset:64
s_setprio 0
v_and_b32 v84,v32,v43
v_and_b32 v85,v33,v43
v_and_b32 v86,v34,v43
v_and_b32 v87,v35,v43
v_and_b32 v88,v32,v55
v_and_b32 v89,v33,v55
v_and_b32 v90,v34,v55
v_and_b32 v91,v35,v55
v_and_b32 v92,v32,v67
v_and_b32 v93,v33,v67
v_and_b32 v94,v34,v67
v_and_b32 v95,v35,v67
v_add_f32 v232,v232,v164
v_fmac_f32 v237,v164,v164
v_add_f32 v233,v233,v165
v_fmac_f32 v236,v165,v165
v_add_f32 v234,v234,v166
v_fmac_f32 v239,v166,v166
v_add_f32 v235,v235,v167
v_fmac_f32 v238,v167,v167
s_setprio 1
s_waitcnt lgkmcnt(3)
v_mfma_f32_16x16x32_bf16 v[168:171],v[72:75],v[200:203],0
v_mfma_f32_16x16x32_bf16 v[168:171],v[76:79],v[204:207],v[168:171]
v_mfma_f32_16x16x32_bf16 v[168:171],v[80:83],v[208:211],v[168:171]
ds_read_b128 v[200:203],v4 offset:128
ds_read_b128 v[204:207],v8 offset:128
ds_read_b128 v[208:211],v12 offset:128
s_setprio 0
v_and_b32 v72,v32,v44
v_and_b32 v73,v33,v44
v_and_b32 v74,v34,v44
v_and_b32 v75,v35,v44
v_and_b32 v76,v32,v56
v_and_b32 v77,v33,v56
v_and_b32 v78,v34,v56
v_and_b32 v79,v35,v56
v_and_b32 v80,v32,v68
v_and_b32 v81,v33,v68
v_and_b32 v82,v34,v68
v_and_b32 v83,v35,v68
v_add_f32 v232,v232,v168
v_fmac_f32 v237,v168,v168
v_add_f32 v233,v233,v169
v_fmac_f32 v236,v169,v169
v_add_f32 v234,v234,v170
v_fmac_f32 v239,v170,v170
v_add_f32 v235,v235,v171
v_fmac_f32 v238,v171,v171
s_setprio 1
s_waitcnt lgkmcnt(3)
v_mfma_f32_16x16x32_bf16 v[172:175],v[84:87],v[212:215],0
v_mfma_f32_16x16x32_bf16 v[172:175],v[88:91],v[216:219],v[172:175]
v_mfma_f32_16x16x32_bf16 v[172:175],v[92:95],v[220:223],v[172:175]
ds_read_b128 v[212:215],v5 offset:128
ds_read_b128 v[216:219],v9 offset:128
ds_read_b128 v[220:223],v13 offset:128
s_setprio 0
v_and_b32 v84,v32,v45
v_and_b32 v85,v33,v45
v_and_b32 v86,v34,v45
v_and_b32 v87,v35,v45
v_and_b32 v88,v32,v57
v_and_b32 v89,v33,v57
v_and_b32 v90,v34,v57
v_and_b32 v91,v35,v57
v_and_b32 v92,v32,v69
v_and_b32 v93,v33,v69
v_and_b32 v94,v34,v69
v_and_b32 v95,v35,v69
v_add_f32 v232,v232,v172
v_fmac_f32 v237,v172,v172
v_add_f32 v233,v233,v173
v_fmac_f32 v236,v173,v173
v_add_f32 v234,v234,v174
v_fmac_f32 v239,v174,v174
v_add_f32 v235,v235,v175
v_fmac_f32 v238,v175,v175
s_setprio 1
s_waitcnt lgkmcnt(3)
v_mfma_f32_16x16x32_bf16 v[176:179],v[72:75],v[200:203],0
v_mfma_f32_16x16x32_bf16 v[176:179],v[76:79],v[204:207],v[176:179]
v_mfma_f32_16x16x32_bf16 v[176:179],v[80:83],v[208:211],v[176:179]
ds_read_b128 v[200:203],v6 offset:128
ds_read_b128 v[204:207],v10 offset:128
ds_read_b128 v[208:211],v14 offset:128
s_setprio 0
v_and_b32 v72,v32,v46
v_and_b32 v73,v33,v46
v_and_b32 v74,v34,v46
v_and_b32 v75,v35,v46
v_and_b32 v76,v32,v58
v_and_b32 v77,v33,v58
v_and_b32 v78,v34,v58
v_and_b32 v79,v35,v58
v_and_b32 v80,v32,v70
v_and_b32 v81,v33,v70
v_and_b32 v82,v34,v70
v_and_b32 v83,v35,v70
v_add_f32 v232,v232,v176
v_fmac_f32 v237,v176,v176
v_add_f32 v233,v233,v177
v_fmac_f32 v236,v177,v177
v_add_f32 v234,v234,v178
v_fmac_f32 v239,v178,v178
v_add_f32 v235,v235,v179
v_fmac_f32 v238,v179,v179
s_setprio 1
s_waitcnt lgkmcnt(3)
v_mfma_f32_16x16x32_bf16 v[180:183],v[84:87],v[212:215],0
v_mfma_f32_16x16x32_bf16 v[180:183],v[88:91],v[216:219],v[180:183]
v_mfma_f32_16x16x32_bf16 v[180:183],v[92:95],v[220:223],v[180:183]
ds_read_b128 v[212:215],v7 offset:128
ds_read_b128 v[216:219],v11 offset:128
ds_read_b128 v[220:223],v15 offset:128
s_setprio 0
v_and_b32 v84,v32,v47
v_and_b32 v85,v33,v47
v_and_b32 v86,v34,v47
v_and_b32 v87,v35,v47
v_and_b32 v88,v32,v59
v_and_b32 v89,v33,v59
v_and_b32 v90,v34,v59
v_and_b32 v91,v35,v59
v_and_b32 v92,v32,v71
v_and_b32 v93,v33,v71
v_and_b32 v94,v34,v71
v_and_b32 v95,v35,v71
v_add_f32 v232,v232,v180
v_fmac_f32 v237,v180,v180
v_add_f32 v233,v233,v181
v_fmac_f32 v236,v181,v181
v_add_f32 v234,v234,v182
v_fmac_f32 v239,v182,v182
v_add_f32 v235,v235,v183
v_fmac_f32 v238,v183,v183
s_setprio 1
s_waitcnt lgkmcnt(3)
v_mfma_f32_16x16x32_bf16 v[184:187],v[72:75],v[200:203],0
v_mfma_f32_16x16x32_bf16 v[184:187],v[76:79],v[204:207],v[184:187]
v_mfma_f32_16x16x32_bf16 v[184:187],v[80:83],v[208:211],v[184:187]
s_setprio 0
s_nop 7
v_add_f32 v232,v232,v184
v_fmac_f32 v237,v184,v184
v_add_f32 v233,v233,v185
v_fmac_f32 v236,v185,v185
v_add_f32 v234,v234,v186
v_fmac_f32 v239,v186,v186
v_add_f32 v235,v235,v187
v_fmac_f32 v238,v187,v187
s_setprio 1
s_waitcnt lgkmcnt(0)
v_mfma_f32_16x16x32_bf16 v[188:191],v[84:87],v[212:215],0
v_mfma_f32_16x16x32_bf16 v[188:191],v[88:91],v[216:219],v[188:191]
v_mfma_f32_16x16x32_bf16 v[188:191],v[92:95],v[220:223],v[188:191]
s_setprio 0
ds_read_b64_tr_b16 v[200:201],v16 offset:0
ds_read_b64_tr_b16 v[202:203],v16 offset:512
ds_read_b64_tr_b16 v[204:205],v16 offset:1024
ds_read_b64_tr_b16 v[206:207],v16 offset:1536
ds_read_b64_tr_b16 v[208:209],v16 offset:2048
ds_read_b64_tr_b16 v[210:211],v16 offset:2560
ds_read_b64_tr_b16 v[212:213],v16 offset:3072
ds_read_b64_tr_b16 v[214:215],v16 offset:3584
ds_read_b64_tr_b16 v[216:217],v16 offset:4096
ds_read_b64_tr_b16 v[218:219],v16 offset:4608
ds_read_b64_tr_b16 v[220:221],v16 offset:5120
ds_read_b64_tr_b16 v[222:223],v16 offset:5632
v_add_f32 v232,v232,v188
v_fmac_f32 v237,v188,v188
v_add_f32 v233,v233,v189
v_fmac_f32 v236,v189,v189
v_add_f32 v234,v234,v190
v_fmac_f32 v239,v190,v190
v_add_f32 v235,v235,v191
v_fmac_f32 v238,v191,v191
v_add_f32 v232,v232,v233
v_add_f32 v234,v234,v235
v_add_f32 v237,v237,v236
v_add_f32 v239,v239,v238
v_add_f32 v240,v232,v234
v_add_f32 v241,v237,v239
s_nop 1
v_permlane32_swap_b32 v240,v241
v_add_f32 v240,v240,v241
v_mov_b32 v241,v240
s_nop 1
v_permlane32_swap_b32 v240,v241
v_mul_f32 v240,0x3c2aaaab,v240
v_mul_f32 v241,0x3c2aaaab,v241
v_fma_f32 v241,-v240,v240,v241
v_max_f32 v241,0,v241
v_add_f32 v241,0x3727c5ac,v241
v_rsq_f32 v241,v241
s_nop 0
v_mul_f32 v242,0xbeeff558,v241
v_mul_f32 v243,0x3eeff558,v241
v_mul_f32 v243,v240,v243
v_fma_f32 v144,v144,v242,v243
v_fma_f32 v145,v145,v242,v243
v_fma_f32 v146,v146,v242,v243
v_fma_f32 v147,v147,v242,v243
v_fma_f32 v148,v148,v242,v243
v_fma_f32 v149,v149,v242,v243
v_fma_f32 v150,v150,v242,v243
v_fma_f32 v151,v151,v242,v243
v_fma_f32 v152,v152,v242,v243
v_fma_f32 v153,v153,v242,v243
v_fma_f32 v154,v154,v242,v243
v_fma_f32 v155,v155,v242,v243
v_fma_f32 v156,v156,v242,v243
v_fma_f32 v157,v157,v242,v243
v_fma_f32 v158,v158,v242,v243
v_fma_f32 v159,v159,v242,v243
v_fma_f32 v160,v160,v242,v243
v_fma_f32 v161,v161,v242,v243
v_fma_f32 v162,v162,v242,v243
v_fma_f32 v163,v163,v242,v243
v_fma_f32 v164,v164,v242,v243
v_fma_f32 v165,v165,v242,v243
v_fma_f32 v166,v166,v242,v243
v_fma_f32 v167,v167,v242,v243
v_fma_f32 v168,v168,v242,v243
v_fma_f32 v169,v169,v242,v243
v_fma_f32 v170,v170,v242,v243
v_fma_f32 v171,v171,v242,v243
v_fma_f32 v172,v172,v242,v243
v_fma_f32 v173,v173,v242,v243
v_fma_f32 v174,v174,v242,v243
v_fma_f32 v175,v175,v242,v243
v_fma_f32 v176,v176,v242,v243
v_fma_f32 v177,v177,v242,v243
v_fma_f32 v178,v178,v242,v243
v_fma_f32 v179,v179,v242,v243
v_fma_f32 v180,v180,v242,v243
v_fma_f32 v181,v181,v242,v243
v_fma_f32 v182,v182,v242,v243
v_fma_f32 v183,v183,v242,v243
v_fma_f32 v184,v184,v242,v243
v_fma_f32 v185,v185,v242,v243
v_fma_f32 v186,v186,v242,v243
v_fma_f32 v187,v187,v242,v243
v_fma_f32 v188,v188,v242,v243
v_fma_f32 v189,v189,v242,v243
v_fma_f32 v190,v190,v242,v243
v_fma_f32 v191,v191,v242,v243
s_setprio 1
v_cvt_pk_bf16_f32 v72,v144,v145
v_cvt_pk_bf16_f32 v73,v146,v147
v_cvt_pk_bf16_f32 v74,v148,v149
v_cvt_pk_bf16_f32 v75,v150,v151
v_cvt_pk_bf16_f32 v76,v152,v153
v_cvt_pk_bf16_f32 v77,v154,v155
v_cvt_pk_bf16_f32 v78,v156,v157
v_cvt_pk_bf16_f32 v79,v158,v159
ds_read_b128 v[144:147],v18 offset:0
ds_read_b128 v[148:151],v18 offset:32
ds_read_b128 v[152:155],v18 offset:64
ds_read_b128 v[156:159],v18 offset:96
v_cvt_pk_bf16_f32 v80,v160,v161
v_cvt_pk_bf16_f32 v81,v162,v163
v_cvt_pk_bf16_f32 v82,v164,v165
v_cvt_pk_bf16_f32 v83,v166,v167
v_cvt_pk_bf16_f32 v84,v168,v169
v_cvt_pk_bf16_f32 v85,v170,v171
v_cvt_pk_bf16_f32 v86,v172,v173
v_cvt_pk_bf16_f32 v87,v174,v175
s_waitcnt lgkmcnt(0)
v_mfma_f32_32x32x16_bf16 v[144:159],v[200:203],v[72:75],v[144:159]
v_cvt_pk_bf16_f32 v88,v176,v177
v_cvt_pk_bf16_f32 v89,v178,v179
v_cvt_pk_bf16_f32 v90,v180,v181
v_cvt_pk_bf16_f32 v91,v182,v183
v_mfma_f32_32x32x16_bf16 v[144:159],v[204:207],v[76:79],v[144:159]
v_cvt_pk_bf16_f32 v92,v184,v185
v_cvt_pk_bf16_f32 v93,v186,v187
v_cvt_pk_bf16_f32 v94,v188,v189
v_cvt_pk_bf16_f32 v95,v190,v191
v_mfma_f32_32x32x16_bf16 v[144:159],v[208:211],v[80:83],v[144:159]
v_mfma_f32_32x32x16_bf16 v[144:159],v[212:215],v[84:87],v[144:159]
v_mfma_f32_32x32x16_bf16 v[144:159],v[216:219],v[88:91],v[144:159]
v_mfma_f32_32x32x16_bf16 v[144:159],v[220:223],v[92:95],v[144:159]
s_setprio 0
s_nop 7
s_nop 1
s_waitcnt vmcnt(2)
s_barrier
v_fmaak_f32 v176,v144,v144,0x409d30fc
v_fmaak_f32 v177,v145,v145,0x409d30fc
v_fmaak_f32 v178,v146,v146,0x409d30fc
v_fmaak_f32 v179,v147,v147,0x409d30fc
v_fmaak_f32 v180,v148,v148,0x409d30fc
v_fmaak_f32 v181,v149,v149,0x409d30fc
v_fmaak_f32 v182,v150,v150,0x409d30fc
v_fmaak_f32 v183,v151,v151,0x409d30fc
v_fmaak_f32 v184,v152,v152,0x409d30fc
v_fmaak_f32 v185,v153,v153,0x409d30fc
v_fmaak_f32 v186,v154,v154,0x409d30fc
v_fmaak_f32 v187,v155,v155,0x409d30fc
v_fmaak_f32 v188,v156,v156,0x409d30fc
v_fmaak_f32 v189,v157,v157,0x409d30fc
v_fmaak_f32 v190,v158,v158,0x409d30fc
v_fmaak_f32 v191,v159,v159,0x409d30fc
v_mul_f32 v176,v144,v176
v_mul_f32 v177,v145,v177
v_mul_f32 v178,v146,v178
v_mul_f32 v179,v147,v179
v_mul_f32 v180,v148,v180
v_mul_f32 v181,v149,v181
v_mul_f32 v182,v150,v182
v_mul_f32 v183,v151,v183
v_mul_f32 v184,v152,v184
v_mul_f32 v185,v153,v185
v_mul_f32 v186,v154,v186
v_mul_f32 v187,v155,v187
v_mul_f32 v188,v156,v188
v_mul_f32 v189,v157,v189
v_mul_f32 v190,v158,v190
v_mul_f32 v191,v159,v191
s_setprio 1
v_exp_f32 v176,v176
v_exp_f32 v177,v177
v_exp_f32 v178,v178
v_exp_f32 v179,v179
v_exp_f32 v180,v180
v_exp_f32 v181,v181
v_exp_f32 v182,v182
v_exp_f32 v183,v183
v_exp_f32 v184,v184
v_exp_f32 v185,v185
v_exp_f32 v186,v186
v_exp_f32 v187,v187
v_exp_f32 v188,v188
v_exp_f32 v189,v189
v_exp_f32 v190,v190
v_exp_f32 v191,v191
ds_read_b64_tr_b16 v[200:201],v16 offset:6144
ds_read_b64_tr_b16 v[202:203],v16 offset:6656
ds_read_b64_tr_b16 v[204:205],v16 offset:7168
ds_read_b64_tr_b16 v[206:207],v16 offset:7680
ds_read_b64_tr_b16 v[208:209],v16 offset:8192
ds_read_b64_tr_b16 v[210:211],v16 offset:8704
ds_read_b64_tr_b16 v[212:213],v16 offset:9216
ds_read_b64_tr_b16 v[214:215],v16 offset:9728
ds_read_b64_tr_b16 v[216:217],v16 offset:10240
ds_read_b64_tr_b16 v[218:219],v16 offset:10752
ds_read_b64_tr_b16 v[220:221],v16 offset:11264
ds_read_b64_tr_b16 v[222:223],v16 offset:11776
ds_read_b128 v[160:163],v18 offset:128
ds_read_b128 v[164:167],v18 offset:160
ds_read_b128 v[168:171],v18 offset:192
ds_read_b128 v[172:175],v18 offset:224
s_mov_b64 exec,s[60:61]
s_add_u32 m0,s58,148992
s_mov_b32 s64,6144
buffer_load_dwordx4 v28,s[48:51],s64 offen lds
s_add_u32 m0,s58,149760
s_mov_b32 s65,6912
buffer_load_dwordx4 v28,s[48:51],s65 offen lds
s_mov_b64 exec,-1
s_setprio 0
v_add_f32 v176,1.0,v176
v_add_f32 v177,1.0,v177
v_add_f32 v178,1.0,v178
v_add_f32 v179,1.0,v179
v_add_f32 v180,1.0,v180
v_add_f32 v181,1.0,v181
v_add_f32 v182,1.0,v182
v_add_f32 v183,1.0,v183
v_add_f32 v184,1.0,v184
v_add_f32 v185,1.0,v185
v_add_f32 v186,1.0,v186
v_add_f32 v187,1.0,v187
v_add_f32 v188,1.0,v188
v_add_f32 v189,1.0,v189
v_add_f32 v190,1.0,v190
v_add_f32 v191,1.0,v191
s_setprio 1
s_waitcnt lgkmcnt(0)
v_mfma_f32_32x32x16_bf16 v[160:175],v[200:203],v[72:75],v[160:175]
v_rcp_f32 v176,v176
v_rcp_f32 v177,v177
v_rcp_f32 v178,v178
v_mfma_f32_32x32x16_bf16 v[160:175],v[204:207],v[76:79],v[160:175]
v_rcp_f32 v179,v179
v_rcp_f32 v180,v180
v_rcp_f32 v181,v181
v_mfma_f32_32x32x16_bf16 v[160:175],v[208:211],v[80:83],v[160:175]
v_rcp_f32 v182,v182
v_rcp_f32 v183,v183
v_rcp_f32 v184,v184
v_mfma_f32_32x32x16_bf16 v[160:175],v[212:215],v[84:87],v[160:175]
v_rcp_f32 v185,v185
v_rcp_f32 v186,v186
v_rcp_f32 v187,v187
v_mfma_f32_32x32x16_bf16 v[160:175],v[216:219],v[88:91],v[160:175]
v_rcp_f32 v188,v188
v_rcp_f32 v189,v189
v_mfma_f32_32x32x16_bf16 v[160:175],v[220:223],v[92:95],v[160:175]
v_rcp_f32 v190,v190
v_rcp_f32 v191,v191
ds_read_b64_tr_b16 v[200:201],v17 offset:0
ds_read_b64_tr_b16 v[202:203],v17 offset:1536
ds_read_b64_tr_b16 v[204:205],v17 offset:3072
ds_read_b64_tr_b16 v[206:207],v17 offset:4608
ds_read_b64_tr_b16 v[208:209],v17 offset:64
ds_read_b64_tr_b16 v[210:211],v17 offset:1600
ds_read_b64_tr_b16 v[212:213],v17 offset:3136
ds_read_b64_tr_b16 v[214:215],v17 offset:4672
ds_read_b64_tr_b16 v[216:217],v17 offset:128
ds_read_b64_tr_b16 v[218:219],v17 offset:1664
ds_read_b64_tr_b16 v[220:221],v17 offset:3200
ds_read_b64_tr_b16 v[222:223],v17 offset:4736
s_mov_b64 exec,s[60:61]
s_add_u32 m0,s58,124416
s_movk_i32 s64,192
buffer_load_dwordx4 v27,s[44:47],s64 offen lds
s_add_u32 m0,s58,125184
s_movk_i32 s65,9408
buffer_load_dwordx4 v27,s[44:47],s65 offen lds
s_mov_b64 exec,-1
s_setprio 0
v_mul_f32 v176,v144,v176
v_mul_f32 v177,v145,v177
v_mul_f32 v178,v146,v178
v_mul_f32 v179,v147,v179
v_mul_f32 v180,v148,v180
v_mul_f32 v181,v149,v181
v_mul_f32 v182,v150,v182
v_mul_f32 v183,v151,v183
v_mul_f32 v184,v152,v184
v_mul_f32 v185,v153,v185
v_mul_f32 v186,v154,v186
v_mul_f32 v187,v155,v187
v_mul_f32 v188,v156,v188
v_mul_f32 v189,v157,v189
v_mul_f32 v190,v158,v190
v_mul_f32 v191,v159,v191
v_cvt_pk_bf16_f32 v192,v176,v177
v_cvt_pk_bf16_f32 v193,v178,v179
v_cvt_pk_bf16_f32 v194,v180,v181
v_cvt_pk_bf16_f32 v195,v182,v183
v_cvt_pk_bf16_f32 v196,v184,v185
v_cvt_pk_bf16_f32 v197,v186,v187
v_cvt_pk_bf16_f32 v198,v188,v189
v_cvt_pk_bf16_f32 v199,v190,v191
s_waitcnt lgkmcnt(0)
s_waitcnt vmcnt(2)
s_barrier
v_fmaak_f32 v176,v160,v160,0x409d30fc
v_fmaak_f32 v177,v161,v161,0x409d30fc
v_fmaak_f32 v178,v162,v162,0x409d30fc
v_fmaak_f32 v179,v163,v163,0x409d30fc
v_fmaak_f32 v180,v164,v164,0x409d30fc
v_fmaak_f32 v181,v165,v165,0x409d30fc
v_fmaak_f32 v182,v166,v166,0x409d30fc
v_fmaak_f32 v183,v167,v167,0x409d30fc
v_fmaak_f32 v184,v168,v168,0x409d30fc
v_fmaak_f32 v185,v169,v169,0x409d30fc
v_fmaak_f32 v186,v170,v170,0x409d30fc
v_fmaak_f32 v187,v171,v171,0x409d30fc
v_fmaak_f32 v188,v172,v172,0x409d30fc
v_fmaak_f32 v189,v173,v173,0x409d30fc
v_fmaak_f32 v190,v174,v174,0x409d30fc
v_fmaak_f32 v191,v175,v175,0x409d30fc
v_mul_f32 v176,v160,v176
v_mul_f32 v177,v161,v177
v_mul_f32 v178,v162,v178
v_mul_f32 v179,v163,v179
v_mul_f32 v180,v164,v180
v_mul_f32 v181,v165,v181
v_mul_f32 v182,v166,v182
v_mul_f32 v183,v167,v183
v_mul_f32 v184,v168,v184
v_mul_f32 v185,v169,v185
v_mul_f32 v186,v170,v186
v_mul_f32 v187,v171,v187
v_mul_f32 v188,v172,v188
v_mul_f32 v189,v173,v189
v_mul_f32 v190,v174,v190
v_mul_f32 v191,v175,v191
s_setprio 1
v_mfma_f32_32x32x16_bf16 v[96:111],v[200:203],v[192:195],v[96:111]
v_exp_f32 v176,v176
v_exp_f32 v177,v177
v_exp_f32 v178,v178
v_mfma_f32_32x32x16_bf16 v[96:111],v[204:207],v[196:199],v[96:111]
v_exp_f32 v179,v179
v_exp_f32 v180,v180
v_exp_f32 v181,v181
v_mfma_f32_32x32x16_bf16 v[112:127],v[208:211],v[192:195],v[112:127]
v_exp_f32 v182,v182
v_exp_f32 v183,v183
v_exp_f32 v184,v184
v_mfma_f32_32x32x16_bf16 v[112:127],v[212:215],v[196:199],v[112:127]
v_exp_f32 v185,v185
v_exp_f32 v186,v186
v_exp_f32 v187,v187
v_mfma_f32_32x32x16_bf16 v[128:143],v[216:219],v[192:195],v[128:143]
v_exp_f32 v188,v188
v_exp_f32 v189,v189
v_mfma_f32_32x32x16_bf16 v[128:143],v[220:223],v[196:199],v[128:143]
v_exp_f32 v190,v190
v_exp_f32 v191,v191
ds_read_b64_tr_b16 v[200:201],v16 offset:12288
ds_read_b64_tr_b16 v[202:203],v16 offset:12800
ds_read_b64_tr_b16 v[204:205],v16 offset:13312
ds_read_b64_tr_b16 v[206:207],v16 offset:13824
ds_read_b64_tr_b16 v[208:209],v16 offset:14336
ds_read_b64_tr_b16 v[210:211],v16 offset:14848
ds_read_b64_tr_b16 v[212:213],v16 offset:15360
ds_read_b64_tr_b16 v[214:215],v16 offset:15872
ds_read_b64_tr_b16 v[216:217],v16 offset:16384
ds_read_b64_tr_b16 v[218:219],v16 offset:16896
ds_read_b64_tr_b16 v[220:221],v16 offset:17408
ds_read_b64_tr_b16 v[222:223],v16 offset:17920
ds_read_b128 v[144:147],v18 offset:256
ds_read_b128 v[148:151],v18 offset:288
ds_read_b128 v[152:155],v18 offset:320
ds_read_b128 v[156:159],v18 offset:352
s_mov_b64 exec,s[60:61]
s_add_u32 m0,s58,155136
s_mov_b32 s64,12288
buffer_load_dwordx4 v28,s[48:51],s64 offen lds
s_add_u32 m0,s58,155904
s_mov_b32 s65,13056
buffer_load_dwordx4 v28,s[48:51],s65 offen lds
s_mov_b64 exec,-1
s_setprio 0
v_add_f32 v176,1.0,v176
v_add_f32 v177,1.0,v177
v_add_f32 v178,1.0,v178
v_add_f32 v179,1.0,v179
v_add_f32 v180,1.0,v180
v_add_f32 v181,1.0,v181
v_add_f32 v182,1.0,v182
v_add_f32 v183,1.0,v183
v_add_f32 v184,1.0,v184
v_add_f32 v185,1.0,v185
v_add_f32 v186,1.0,v186
v_add_f32 v187,1.0,v187
v_add_f32 v188,1.0,v188
v_add_f32 v189,1.0,v189
v_add_f32 v190,1.0,v190
v_add_f32 v191,1.0,v191
s_setprio 1
s_waitcnt lgkmcnt(0)
v_mfma_f32_32x32x16_bf16 v[144:159],v[200:203],v[72:75],v[144:159]
v_rcp_f32 v176,v176
v_rcp_f32 v177,v177
v_rcp_f32 v178,v178
v_mfma_f32_32x32x16_bf16 v[144:159],v[204:207],v[76:79],v[144:159]
v_rcp_f32 v179,v179
v_rcp_f32 v180,v180
v_rcp_f32 v181,v181
v_mfma_f32_32x32x16_bf16 v[144:159],v[208:211],v[80:83],v[144:159]
v_rcp_f32 v182,v182
v_rcp_f32 v183,v183
v_rcp_f32 v184,v184
v_mfma_f32_32x32x16_bf16 v[144:159],v[212:215],v[84:87],v[144:159]
v_rcp_f32 v185,v185
v_rcp_f32 v186,v186
v_rcp_f32 v187,v187
v_mfma_f32_32x32x16_bf16 v[144:159],v[216:219],v[88:91],v[144:159]
v_rcp_f32 v188,v188
v_rcp_f32 v189,v189
v_mfma_f32_32x32x16_bf16 v[144:159],v[220:223],v[92:95],v[144:159]
v_rcp_f32 v190,v190
v_rcp_f32 v191,v191
ds_read_b64_tr_b16 v[200:201],v17 offset:6144
ds_read_b64_tr_b16 v[202:203],v17 offset:7680
ds_read_b64_tr_b16 v[204:205],v17 offset:9216
ds_read_b64_tr_b16 v[206:207],v17 offset:10752
ds_read_b64_tr_b16 v[208:209],v17 offset:6208
ds_read_b64_tr_b16 v[210:211],v17 offset:7744
ds_read_b64_tr_b16 v[212:213],v17 offset:9280
ds_read_b64_tr_b16 v[214:215],v17 offset:10816
ds_read_b64_tr_b16 v[216:217],v17 offset:6272
ds_read_b64_tr_b16 v[218:219],v17 offset:7808
ds_read_b64_tr_b16 v[220:221],v17 offset:9344
ds_read_b64_tr_b16 v[222:223],v17 offset:10880
s_mov_b64 exec,s[60:61]
s_add_u32 m0,s58,130560
s_movk_i32 s64,256
buffer_load_dwordx4 v27,s[44:47],s64 offen lds
s_add_u32 m0,s58,131328
s_movk_i32 s65,9472
buffer_load_dwordx4 v27,s[44:47],s65 offen lds
s_mov_b64 exec,-1
s_setprio 0
v_mul_f32 v176,v160,v176
v_mul_f32 v177,v161,v177
v_mul_f32 v178,v162,v178
v_mul_f32 v179,v163,v179
v_mul_f32 v180,v164,v180
v_mul_f32 v181,v165,v181
v_mul_f32 v182,v166,v182
v_mul_f32 v183,v167,v183
v_mul_f32 v184,v168,v184
v_mul_f32 v185,v169,v185
v_mul_f32 v186,v170,v186
v_mul_f32 v187,v171,v187
v_mul_f32 v188,v172,v188
v_mul_f32 v189,v173,v189
v_mul_f32 v190,v174,v190
v_mul_f32 v191,v175,v191
v_cvt_pk_bf16_f32 v192,v176,v177
v_cvt_pk_bf16_f32 v193,v178,v179
v_cvt_pk_bf16_f32 v194,v180,v181
v_cvt_pk_bf16_f32 v195,v182,v183
v_cvt_pk_bf16_f32 v196,v184,v185
v_cvt_pk_bf16_f32 v197,v186,v187
v_cvt_pk_bf16_f32 v198,v188,v189
v_cvt_pk_bf16_f32 v199,v190,v191
s_waitcnt lgkmcnt(0)
s_waitcnt vmcnt(2)
s_barrier
v_fmaak_f32 v176,v144,v144,0x409d30fc
v_fmaak_f32 v177,v145,v145,0x409d30fc
v_fmaak_f32 v178,v146,v146,0x409d30fc
v_fmaak_f32 v179,v147,v147,0x409d30fc
v_fmaak_f32 v180,v148,v148,0x409d30fc
v_fmaak_f32 v181,v149,v149,0x409d30fc
v_fmaak_f32 v182,v150,v150,0x409d30fc
v_fmaak_f32 v183,v151,v151,0x409d30fc
v_fmaak_f32 v184,v152,v152,0x409d30fc
v_fmaak_f32 v185,v153,v153,0x409d30fc
v_fmaak_f32 v186,v154,v154,0x409d30fc
v_fmaak_f32 v187,v155,v155,0x409d30fc
v_fmaak_f32 v188,v156,v156,0x409d30fc
v_fmaak_f32 v189,v157,v157,0x409d30fc
v_fmaak_f32 v190,v158,v158,0x409d30fc
v_fmaak_f32 v191,v159,v159,0x409d30fc
v_mul_f32 v176,v144,v176
v_mul_f32 v177,v145,v177
v_mul_f32 v178,v146,v178
v_mul_f32 v179,v147,v179
v_mul_f32 v180,v148,v180
v_mul_f32 v181,v149,v181
v_mul_f32 v182,v150,v182
v_mul_f32 v183,v151,v183
v_mul_f32 v184,v152,v184
v_mul_f32 v185,v153,v185
v_mul_f32 v186,v154,v186
v_mul_f32 v187,v155,v187
v_mul_f32 v188,v156,v188
v_mul_f32 v189,v157,v189
v_mul_f32 v190,v158,v190
v_mul_f32 v191,v159,v191
s_setprio 1
v_mfma_f32_32x32x16_bf16 v[96:111],v[200:203],v[192:195],v[96:111]
v_exp_f32 v176,v176
v_exp_f32 v177,v177
v_exp_f32 v178,v178
v_mfma_f32_32x32x16_bf16 v[96:111],v[204:207],v[196:199],v[96:111]
v_exp_f32 v179,v179
v_exp_f32 v180,v180
v_exp_f32 v181,v181
v_mfma_f32_32x32x16_bf16 v[112:127],v[208:211],v[192:195],v[112:127]
v_exp_f32 v182,v182
v_exp_f32 v183,v183
v_exp_f32 v184,v184
v_mfma_f32_32x32x16_bf16 v[112:127],v[212:215],v[196:199],v[112:127]
v_exp_f32 v185,v185
v_exp_f32 v186,v186
v_exp_f32 v187,v187
v_mfma_f32_32x32x16_bf16 v[128:143],v[216:219],v[192:195],v[128:143]
v_exp_f32 v188,v188
v_exp_f32 v189,v189
v_mfma_f32_32x32x16_bf16 v[128:143],v[220:223],v[196:199],v[128:143]
v_exp_f32 v190,v190
v_exp_f32 v191,v191
ds_read_b64_tr_b16 v[200:201],v16 offset:0
ds_read_b64_tr_b16 v[202:203],v16 offset:512
ds_read_b64_tr_b16 v[204:205],v16 offset:1024
ds_read_b64_tr_b16 v[206:207],v16 offset:1536
ds_read_b64_tr_b16 v[208:209],v16 offset:2048
ds_read_b64_tr_b16 v[210:211],v16 offset:2560
ds_read_b64_tr_b16 v[212:213],v16 offset:3072
ds_read_b64_tr_b16 v[214:215],v16 offset:3584
ds_read_b64_tr_b16 v[216:217],v16 offset:4096
ds_read_b64_tr_b16 v[218:219],v16 offset:4608
ds_read_b64_tr_b16 v[220:221],v16 offset:5120
ds_read_b64_tr_b16 v[222:223],v16 offset:5632
ds_read_b128 v[160:163],v18 offset:384
ds_read_b128 v[164:167],v18 offset:416
ds_read_b128 v[168:171],v18 offset:448
ds_read_b128 v[172:175],v18 offset:480
s_mov_b64 exec,s[60:61]
s_add_u32 m0,s58,142848
s_mov_b32 s64,18432
buffer_load_dwordx4 v28,s[48:51],s64 offen lds
s_add_u32 m0,s58,143616
s_mov_b32 s65,19200
buffer_load_dwordx4 v28,s[48:51],s65 offen lds
s_mov_b64 exec,-1
s_setprio 0
v_add_f32 v176,1.0,v176
v_add_f32 v177,1.0,v177
v_add_f32 v178,1.0,v178
v_add_f32 v179,1.0,v179
v_add_f32 v180,1.0,v180
v_add_f32 v181,1.0,v181
v_add_f32 v182,1.0,v182
v_add_f32 v183,1.0,v183
v_add_f32 v184,1.0,v184
v_add_f32 v185,1.0,v185
v_add_f32 v186,1.0,v186
v_add_f32 v187,1.0,v187
v_add_f32 v188,1.0,v188
v_add_f32 v189,1.0,v189
v_add_f32 v190,1.0,v190
v_add_f32 v191,1.0,v191
s_setprio 1
s_waitcnt lgkmcnt(0)
v_mfma_f32_32x32x16_bf16 v[160:175],v[200:203],v[72:75],v[160:175]
v_rcp_f32 v176,v176
v_rcp_f32 v177,v177
v_rcp_f32 v178,v178
v_mfma_f32_32x32x16_bf16 v[160:175],v[204:207],v[76:79],v[160:175]
v_rcp_f32 v179,v179
v_rcp_f32 v180,v180
v_rcp_f32 v181,v181
v_mfma_f32_32x32x16_bf16 v[160:175],v[208:211],v[80:83],v[160:175]
v_rcp_f32 v182,v182
v_rcp_f32 v183,v183
v_rcp_f32 v184,v184
v_mfma_f32_32x32x16_bf16 v[160:175],v[212:215],v[84:87],v[160:175]
v_rcp_f32 v185,v185
v_rcp_f32 v186,v186
v_rcp_f32 v187,v187
v_mfma_f32_32x32x16_bf16 v[160:175],v[216:219],v[88:91],v[160:175]
v_rcp_f32 v188,v188
v_rcp_f32 v189,v189
v_mfma_f32_32x32x16_bf16 v[160:175],v[220:223],v[92:95],v[160:175]
v_rcp_f32 v190,v190
v_rcp_f32 v191,v191
ds_read_b64_tr_b16 v[200:201],v17 offset:12288
ds_read_b64_tr_b16 v[202:203],v17 offset:13824
ds_read_b64_tr_b16 v[204:205],v17 offset:15360
ds_read_b64_tr_b16 v[206:207],v17 offset:16896
ds_read_b64_tr_b16 v[208:209],v17 offset:12352
ds_read_b64_tr_b16 v[210:211],v17 offset:13888
ds_read_b64_tr_b16 v[212:213],v17 offset:15424
ds_read_b64_tr_b16 v[214:215],v17 offset:16960
ds_read_b64_tr_b16 v[216:217],v17 offset:12416
ds_read_b64_tr_b16 v[218:219],v17 offset:13952
ds_read_b64_tr_b16 v[220:221],v17 offset:15488
ds_read_b64_tr_b16 v[222:223],v17 offset:17024
s_mov_b64 exec,s[60:61]
s_add_u32 m0,s58,136704
s_movk_i32 s64,320
buffer_load_dwordx4 v27,s[44:47],s64 offen lds
s_add_u32 m0,s58,137472
s_movk_i32 s65,9536
buffer_load_dwordx4 v27,s[44:47],s65 offen lds
s_mov_b64 exec,-1
s_setprio 0
v_mul_f32 v176,v144,v176
v_mul_f32 v177,v145,v177
v_mul_f32 v178,v146,v178
v_mul_f32 v179,v147,v179
v_mul_f32 v180,v148,v180
v_mul_f32 v181,v149,v181
v_mul_f32 v182,v150,v182
v_mul_f32 v183,v151,v183
v_mul_f32 v184,v152,v184
v_mul_f32 v185,v153,v185
v_mul_f32 v186,v154,v186
v_mul_f32 v187,v155,v187
v_mul_f32 v188,v156,v188
v_mul_f32 v189,v157,v189
v_mul_f32 v190,v158,v190
v_mul_f32 v191,v159,v191
v_cvt_pk_bf16_f32 v192,v176,v177
v_cvt_pk_bf16_f32 v193,v178,v179
v_cvt_pk_bf16_f32 v194,v180,v181
v_cvt_pk_bf16_f32 v195,v182,v183
v_cvt_pk_bf16_f32 v196,v184,v185
v_cvt_pk_bf16_f32 v197,v186,v187
v_cvt_pk_bf16_f32 v198,v188,v189
v_cvt_pk_bf16_f32 v199,v190,v191
s_waitcnt lgkmcnt(0)
s_waitcnt vmcnt(2)
s_barrier
v_fmaak_f32 v176,v160,v160,0x409d30fc
v_fmaak_f32 v177,v161,v161,0x409d30fc
v_fmaak_f32 v178,v162,v162,0x409d30fc
v_fmaak_f32 v179,v163,v163,0x409d30fc
v_fmaak_f32 v180,v164,v164,0x409d30fc
v_fmaak_f32 v181,v165,v165,0x409d30fc
v_fmaak_f32 v182,v166,v166,0x409d30fc
v_fmaak_f32 v183,v167,v167,0x409d30fc
v_fmaak_f32 v184,v168,v168,0x409d30fc
v_fmaak_f32 v185,v169,v169,0x409d30fc
v_fmaak_f32 v186,v170,v170,0x409d30fc
v_fmaak_f32 v187,v171,v171,0x409d30fc
v_fmaak_f32 v188,v172,v172,0x409d30fc
v_fmaak_f32 v189,v173,v173,0x409d30fc
v_fmaak_f32 v190,v174,v174,0x409d30fc
v_fmaak_f32 v191,v175,v175,0x409d30fc
v_mul_f32 v176,v160,v176
v_mul_f32 v177,v161,v177
v_mul_f32 v178,v162,v178
v_mul_f32 v179,v163,v179
v_mul_f32 v180,v164,v180
v_mul_f32 v181,v165,v181
v_mul_f32 v182,v166,v182
v_mul_f32 v183,v167,v183
v_mul_f32 v184,v168,v184
v_mul_f32 v185,v169,v185
v_mul_f32 v186,v170,v186
v_mul_f32 v187,v171,v187
v_mul_f32 v188,v172,v188
v_mul_f32 v189,v173,v189
v_mul_f32 v190,v174,v190
v_mul_f32 v191,v175,v191
s_setprio 1
v_mfma_f32_32x32x16_bf16 v[96:111],v[200:203],v[192:195],v[96:111]
v_exp_f32 v176,v176
v_exp_f32 v177,v177
v_exp_f32 v178,v178
v_mfma_f32_32x32x16_bf16 v[96:111],v[204:207],v[196:199],v[96:111]
v_exp_f32 v179,v179
v_exp_f32 v180,v180
v_exp_f32 v181,v181
v_mfma_f32_32x32x16_bf16 v[112:127],v[208:211],v[192:195],v[112:127]
v_exp_f32 v182,v182
v_exp_f32 v183,v183
v_exp_f32 v184,v184
v_mfma_f32_32x32x16_bf16 v[112:127],v[212:215],v[196:199],v[112:127]
v_exp_f32 v185,v185
v_exp_f32 v186,v186
v_exp_f32 v187,v187
v_mfma_f32_32x32x16_bf16 v[128:143],v[216:219],v[192:195],v[128:143]
v_exp_f32 v188,v188
v_exp_f32 v189,v189
v_mfma_f32_32x32x16_bf16 v[128:143],v[220:223],v[196:199],v[128:143]
v_exp_f32 v190,v190
v_exp_f32 v191,v191
ds_read_b64_tr_b16 v[200:201],v16 offset:6144
ds_read_b64_tr_b16 v[202:203],v16 offset:6656
ds_read_b64_tr_b16 v[204:205],v16 offset:7168
ds_read_b64_tr_b16 v[206:207],v16 offset:7680
ds_read_b64_tr_b16 v[208:209],v16 offset:8192
ds_read_b64_tr_b16 v[210:211],v16 offset:8704
ds_read_b64_tr_b16 v[212:213],v16 offset:9216
ds_read_b64_tr_b16 v[214:215],v16 offset:9728
ds_read_b64_tr_b16 v[216:217],v16 offset:10240
ds_read_b64_tr_b16 v[218:219],v16 offset:10752
ds_read_b64_tr_b16 v[220:221],v16 offset:11264
ds_read_b64_tr_b16 v[222:223],v16 offset:11776
ds_read_b128 v[144:147],v18 offset:512
ds_read_b128 v[148:151],v18 offset:544
ds_read_b128 v[152:155],v18 offset:576
ds_read_b128 v[156:159],v18 offset:608
s_mov_b64 exec,s[60:61]
s_add_u32 m0,s58,148992
s_mov_b32 s64,24576
buffer_load_dwordx4 v28,s[48:51],s64 offen lds
s_add_u32 m0,s58,149760
s_mov_b32 s65,25344
buffer_load_dwordx4 v28,s[48:51],s65 offen lds
s_mov_b64 exec,-1
s_setprio 0
v_add_f32 v176,1.0,v176
v_add_f32 v177,1.0,v177
v_add_f32 v178,1.0,v178
v_add_f32 v179,1.0,v179
v_add_f32 v180,1.0,v180
v_add_f32 v181,1.0,v181
v_add_f32 v182,1.0,v182
v_add_f32 v183,1.0,v183
v_add_f32 v184,1.0,v184
v_add_f32 v185,1.0,v185
v_add_f32 v186,1.0,v186
v_add_f32 v187,1.0,v187
v_add_f32 v188,1.0,v188
v_add_f32 v189,1.0,v189
v_add_f32 v190,1.0,v190
v_add_f32 v191,1.0,v191
s_setprio 1
s_waitcnt lgkmcnt(0)
v_mfma_f32_32x32x16_bf16 v[144:159],v[200:203],v[72:75],v[144:159]
v_rcp_f32 v176,v176
v_rcp_f32 v177,v177
v_rcp_f32 v178,v178
v_mfma_f32_32x32x16_bf16 v[144:159],v[204:207],v[76:79],v[144:159]
v_rcp_f32 v179,v179
v_rcp_f32 v180,v180
v_rcp_f32 v181,v181
v_mfma_f32_32x32x16_bf16 v[144:159],v[208:211],v[80:83],v[144:159]
v_rcp_f32 v182,v182
v_rcp_f32 v183,v183
v_rcp_f32 v184,v184
v_mfma_f32_32x32x16_bf16 v[144:159],v[212:215],v[84:87],v[144:159]
v_rcp_f32 v185,v185
v_rcp_f32 v186,v186
v_rcp_f32 v187,v187
v_mfma_f32_32x32x16_bf16 v[144:159],v[216:219],v[88:91],v[144:159]
v_rcp_f32 v188,v188
v_rcp_f32 v189,v189
v_mfma_f32_32x32x16_bf16 v[144:159],v[220:223],v[92:95],v[144:159]
v_rcp_f32 v190,v190
v_rcp_f32 v191,v191
ds_read_b64_tr_b16 v[200:201],v17 offset:0
ds_read_b64_tr_b16 v[202:203],v17 offset:1536
ds_read_b64_tr_b16 v[204:205],v17 offset:3072
ds_read_b64_tr_b16 v[206:207],v17 offset:4608
ds_read_b64_tr_b16 v[208:209],v17 offset:64
ds_read_b64_tr_b16 v[210:211],v17 offset:1600
ds_read_b64_tr_b16 v[212:213],v17 offset:3136
ds_read_b64_tr_b16 v[214:215],v17 offset:4672
ds_read_b64_tr_b16 v[216:217],v17 offset:128
ds_read_b64_tr_b16 v[218:219],v17 offset:1664
ds_read_b64_tr_b16 v[220:221],v17 offset:3200
ds_read_b64_tr_b16 v[222:223],v17 offset:4736
s_mov_b64 exec,s[60:61]
s_add_u32 m0,s58,124416
s_movk_i32 s64,384
buffer_load_dwordx4 v27,s[44:47],s64 offen lds
s_add_u32 m0,s58,125184
s_movk_i32 s65,9600
buffer_load_dwordx4 v27,s[44:47],s65 offen lds
s_mov_b64 exec,-1
s_setprio 0
v_mul_f32 v176,v160,v176
v_mul_f32 v177,v161,v177
v_mul_f32 v178,v162,v178
v_mul_f32 v179,v163,v179
v_mul_f32 v180,v164,v180
v_mul_f32 v181,v165,v181
v_mul_f32 v182,v166,v182
v_mul_f32 v183,v167,v183
v_mul_f32 v184,v168,v184
v_mul_f32 v185,v169,v185
v_mul_f32 v186,v170,v186
v_mul_f32 v187,v171,v187
v_mul_f32 v188,v172,v188
v_mul_f32 v189,v173,v189
v_mul_f32 v190,v174,v190
v_mul_f32 v191,v175,v191
v_cvt_pk_bf16_f32 v192,v176,v177
v_cvt_pk_bf16_f32 v193,v178,v179
v_cvt_pk_bf16_f32 v194,v180,v181
v_cvt_pk_bf16_f32 v195,v182,v183
v_cvt_pk_bf16_f32 v196,v184,v185
v_cvt_pk_bf16_f32 v197,v186,v187
v_cvt_pk_bf16_f32 v198,v188,v189
v_cvt_pk_bf16_f32 v199,v190,v191
s_waitcnt lgkmcnt(0)
s_waitcnt vmcnt(2)
s_barrier
v_fmaak_f32 v176,v144,v144,0x409d30fc
v_fmaak_f32 v177,v145,v145,0x409d30fc
v_fmaak_f32 v178,v146,v146,0x409d30fc
v_fmaak_f32 v179,v147,v147,0x409d30fc
v_fmaak_f32 v180,v148,v148,0x409d30fc
v_fmaak_f32 v181,v149,v149,0x409d30fc
v_fmaak_f32 v182,v150,v150,0x409d30fc
v_fmaak_f32 v183,v151,v151,0x409d30fc
v_fmaak_f32 v184,v152,v152,0x409d30fc
v_fmaak_f32 v185,v153,v153,0x409d30fc
v_fmaak_f32 v186,v154,v154,0x409d30fc
v_fmaak_f32 v187,v155,v155,0x409d30fc
v_fmaak_f32 v188,v156,v156,0x409d30fc
v_fmaak_f32 v189,v157,v157,0x409d30fc
v_fmaak_f32 v190,v158,v158,0x409d30fc
v_fmaak_f32 v191,v159,v159,0x409d30fc
v_mul_f32 v176,v144,v176
v_mul_f32 v177,v145,v177
v_mul_f32 v178,v146,v178
v_mul_f32 v179,v147,v179
v_mul_f32 v180,v148,v180
v_mul_f32 v181,v149,v181
v_mul_f32 v182,v150,v182
v_mul_f32 v183,v151,v183
v_mul_f32 v184,v152,v184
v_mul_f32 v185,v153,v185
v_mul_f32 v186,v154,v186
v_mul_f32 v187,v155,v187
v_mul_f32 v188,v156,v188
v_mul_f32 v189,v157,v189
v_mul_f32 v190,v158,v190
v_mul_f32 v191,v159,v191
s_setprio 1
v_mfma_f32_32x32x16_bf16 v[96:111],v[200:203],v[192:195],v[96:111]
v_exp_f32 v176,v176
v_exp_f32 v177,v177
v_exp_f32 v178,v178
v_mfma_f32_32x32x16_bf16 v[96:111],v[204:207],v[196:199],v[96:111]
v_exp_f32 v179,v179
v_exp_f32 v180,v180
v_exp_f32 v181,v181
v_mfma_f32_32x32x16_bf16 v[112:127],v[208:211],v[192:195],v[112:127]
v_exp_f32 v182,v182
v_exp_f32 v183,v183
v_exp_f32 v184,v184
v_mfma_f32_32x32x16_bf16 v[112:127],v[212:215],v[196:199],v[112:127]
v_exp_f32 v185,v185
v_exp_f32 v186,v186
v_exp_f32 v187,v187
v_mfma_f32_32x32x16_bf16 v[128:143],v[216:219],v[192:195],v[128:143]
v_exp_f32 v188,v188
v_exp_f32 v189,v189
v_mfma_f32_32x32x16_bf16 v[128:143],v[220:223],v[196:199],v[128:143]
v_exp_f32 v190,v190
v_exp_f32 v191,v191
ds_read_b64_tr_b16 v[200:201],v16 offset:12288
ds_read_b64_tr_b16 v[202:203],v16 offset:12800
ds_read_b64_tr_b16 v[204:205],v16 offset:13312
ds_read_b64_tr_b16 v[206:207],v16 offset:13824
ds_read_b64_tr_b16 v[208:209],v16 offset:14336
ds_read_b64_tr_b16 v[210:211],v16 offset:14848
ds_read_b64_tr_b16 v[212:213],v16 offset:15360
ds_read_b64_tr_b16 v[214:215],v16 offset:15872
ds_read_b64_tr_b16 v[216:217],v16 offset:16384
ds_read_b64_tr_b16 v[218:219],v16 offset:16896
ds_read_b64_tr_b16 v[220:221],v16 offset:17408
ds_read_b64_tr_b16 v[222:223],v16 offset:17920
ds_read_b128 v[160:163],v18 offset:640
ds_read_b128 v[164:167],v18 offset:672
ds_read_b128 v[168:171],v18 offset:704
ds_read_b128 v[172:175],v18 offset:736
s_mov_b64 exec,s[60:61]
s_add_u32 m0,s58,155136
s_mov_b32 s64,30720
buffer_load_dwordx4 v28,s[48:51],s64 offen lds
s_add_u32 m0,s58,155904
s_mov_b32 s65,31488
buffer_load_dwordx4 v28,s[48:51],s65 offen lds
s_mov_b64 exec,-1
s_setprio 0
v_add_f32 v176,1.0,v176
v_add_f32 v177,1.0,v177
v_add_f32 v178,1.0,v178
v_add_f32 v179,1.0,v179
v_add_f32 v180,1.0,v180
v_add_f32 v181,1.0,v181
v_add_f32 v182,1.0,v182
v_add_f32 v183,1.0,v183
v_add_f32 v184,1.0,v184
v_add_f32 v185,1.0,v185
v_add_f32 v186,1.0,v186
v_add_f32 v187,1.0,v187
v_add_f32 v188,1.0,v188
v_add_f32 v189,1.0,v189
v_add_f32 v190,1.0,v190
v_add_f32 v191,1.0,v191
s_setprio 1
s_waitcnt lgkmcnt(0)
v_mfma_f32_32x32x16_bf16 v[160:175],v[200:203],v[72:75],v[160:175]
v_rcp_f32 v176,v176
v_rcp_f32 v177,v177
v_rcp_f32 v178,v178
v_mfma_f32_32x32x16_bf16 v[160:175],v[204:207],v[76:79],v[160:175]
v_rcp_f32 v179,v179
v_rcp_f32 v180,v180
v_rcp_f32 v181,v181
v_mfma_f32_32x32x16_bf16 v[160:175],v[208:211],v[80:83],v[160:175]
v_rcp_f32 v182,v182
v_rcp_f32 v183,v183
v_rcp_f32 v184,v184
v_mfma_f32_32x32x16_bf16 v[160:175],v[212:215],v[84:87],v[160:175]
v_rcp_f32 v185,v185
v_rcp_f32 v186,v186
v_rcp_f32 v187,v187
v_mfma_f32_32x32x16_bf16 v[160:175],v[216:219],v[88:91],v[160:175]
v_rcp_f32 v188,v188
v_rcp_f32 v189,v189
v_mfma_f32_32x32x16_bf16 v[160:175],v[220:223],v[92:95],v[160:175]
v_rcp_f32 v190,v190
v_rcp_f32 v191,v191
ds_read_b64_tr_b16 v[200:201],v17 offset:6144
ds_read_b64_tr_b16 v[202:203],v17 offset:7680
ds_read_b64_tr_b16 v[204:205],v17 offset:9216
ds_read_b64_tr_b16 v[206:207],v17 offset:10752
ds_read_b64_tr_b16 v[208:209],v17 offset:6208
ds_read_b64_tr_b16 v[210:211],v17 offset:7744
ds_read_b64_tr_b16 v[212:213],v17 offset:9280
ds_read_b64_tr_b16 v[214:215],v17 offset:10816
ds_read_b64_tr_b16 v[216:217],v17 offset:6272
ds_read_b64_tr_b16 v[218:219],v17 offset:7808
ds_read_b64_tr_b16 v[220:221],v17 offset:9344
ds_read_b64_tr_b16 v[222:223],v17 offset:10880
s_mov_b64 exec,s[60:61]
s_add_u32 m0,s58,130560
s_movk_i32 s64,448
buffer_load_dwordx4 v27,s[44:47],s64 offen lds
s_add_u32 m0,s58,131328
s_movk_i32 s65,9664
buffer_load_dwordx4 v27,s[44:47],s65 offen lds
s_mov_b64 exec,-1
s_setprio 0
v_mul_f32 v176,v144,v176
v_mul_f32 v177,v145,v177
v_mul_f32 v178,v146,v178
v_mul_f32 v179,v147,v179
v_mul_f32 v180,v148,v180
v_mul_f32 v181,v149,v181
v_mul_f32 v182,v150,v182
v_mul_f32 v183,v151,v183
v_mul_f32 v184,v152,v184
v_mul_f32 v185,v153,v185
v_mul_f32 v186,v154,v186
v_mul_f32 v187,v155,v187
v_mul_f32 v188,v156,v188
v_mul_f32 v189,v157,v189
v_mul_f32 v190,v158,v190
v_mul_f32 v191,v159,v191
v_cvt_pk_bf16_f32 v192,v176,v177
v_cvt_pk_bf16_f32 v193,v178,v179
v_cvt_pk_bf16_f32 v194,v180,v181
v_cvt_pk_bf16_f32 v195,v182,v183
v_cvt_pk_bf16_f32 v196,v184,v185
v_cvt_pk_bf16_f32 v197,v186,v187
v_cvt_pk_bf16_f32 v198,v188,v189
v_cvt_pk_bf16_f32 v199,v190,v191
s_waitcnt lgkmcnt(0)
s_waitcnt vmcnt(2)
s_barrier
v_fmaak_f32 v176,v160,v160,0x409d30fc
v_fmaak_f32 v177,v161,v161,0x409d30fc
v_fmaak_f32 v178,v162,v162,0x409d30fc
v_fmaak_f32 v179,v163,v163,0x409d30fc
v_fmaak_f32 v180,v164,v164,0x409d30fc
v_fmaak_f32 v181,v165,v165,0x409d30fc
v_fmaak_f32 v182,v166,v166,0x409d30fc
v_fmaak_f32 v183,v167,v167,0x409d30fc
v_fmaak_f32 v184,v168,v168,0x409d30fc
v_fmaak_f32 v185,v169,v169,0x409d30fc
v_fmaak_f32 v186,v170,v170,0x409d30fc
v_fmaak_f32 v187,v171,v171,0x409d30fc
v_fmaak_f32 v188,v172,v172,0x409d30fc
v_fmaak_f32 v189,v173,v173,0x409d30fc
v_fmaak_f32 v190,v174,v174,0x409d30fc
v_fmaak_f32 v191,v175,v175,0x409d30fc
v_mul_f32 v176,v160,v176
v_mul_f32 v177,v161,v177
v_mul_f32 v178,v162,v178
v_mul_f32 v179,v163,v179
v_mul_f32 v180,v164,v180
v_mul_f32 v181,v165,v181
v_mul_f32 v182,v166,v182
v_mul_f32 v183,v167,v183
v_mul_f32 v184,v168,v184
v_mul_f32 v185,v169,v185
v_mul_f32 v186,v170,v186
v_mul_f32 v187,v171,v187
v_mul_f32 v188,v172,v188
v_mul_f32 v189,v173,v189
v_mul_f32 v190,v174,v190
v_mul_f32 v191,v175,v191
s_setprio 1
v_mfma_f32_32x32x16_bf16 v[96:111],v[200:203],v[192:195],v[96:111]
v_exp_f32 v176,v176
v_exp_f32 v177,v177
v_exp_f32 v178,v178
v_mfma_f32_32x32x16_bf16 v[96:111],v[204:207],v[196:199],v[96:111]
v_exp_f32 v179,v179
v_exp_f32 v180,v180
v_exp_f32 v181,v181
v_mfma_f32_32x32x16_bf16 v[112:127],v[208:211],v[192:195],v[112:127]
v_exp_f32 v182,v182
v_exp_f32 v183,v183
v_exp_f32 v184,v184
v_mfma_f32_32x32x16_bf16 v[112:127],v[212:215],v[196:199],v[112:127]
v_exp_f32 v185,v185
v_exp_f32 v186,v186
v_exp_f32 v187,v187
v_mfma_f32_32x32x16_bf16 v[128:143],v[216:219],v[192:195],v[128:143]
v_exp_f32 v188,v188
v_exp_f32 v189,v189
v_mfma_f32_32x32x16_bf16 v[128:143],v[220:223],v[196:199],v[128:143]
v_exp_f32 v190,v190
v_exp_f32 v191,v191
ds_read_b64_tr_b16 v[200:201],v16 offset:0
ds_read_b64_tr_b16 v[202:203],v16 offset:512
ds_read_b64_tr_b16 v[204:205],v16 offset:1024
ds_read_b64_tr_b16 v[206:207],v16 offset:1536
ds_read_b64_tr_b16 v[208:209],v16 offset:2048
ds_read_b64_tr_b16 v[210:211],v16 offset:2560
ds_read_b64_tr_b16 v[212:213],v16 offset:3072
ds_read_b64_tr_b16 v[214:215],v16 offset:3584
ds_read_b64_tr_b16 v[216:217],v16 offset:4096
ds_read_b64_tr_b16 v[218:219],v16 offset:4608
ds_read_b64_tr_b16 v[220:221],v16 offset:5120
ds_read_b64_tr_b16 v[222:223],v16 offset:5632
ds_read_b128 v[144:147],v18 offset:768
ds_read_b128 v[148:151],v18 offset:800
ds_read_b128 v[152:155],v18 offset:832
ds_read_b128 v[156:159],v18 offset:864
s_mov_b64 exec,s[60:61]
s_add_u32 m0,s58,142848
s_mov_b32 s64,36864
buffer_load_dwordx4 v28,s[48:51],s64 offen lds
s_add_u32 m0,s58,143616
s_mov_b32 s65,37632
buffer_load_dwordx4 v28,s[48:51],s65 offen lds
s_mov_b64 exec,-1
s_setprio 0
v_add_f32 v176,1.0,v176
v_add_f32 v177,1.0,v177
v_add_f32 v178,1.0,v178
v_add_f32 v179,1.0,v179
v_add_f32 v180,1.0,v180
v_add_f32 v181,1.0,v181
v_add_f32 v182,1.0,v182
v_add_f32 v183,1.0,v183
v_add_f32 v184,1.0,v184
v_add_f32 v185,1.0,v185
v_add_f32 v186,1.0,v186
v_add_f32 v187,1.0,v187
v_add_f32 v188,1.0,v188
v_add_f32 v189,1.0,v189
v_add_f32 v190,1.0,v190
v_add_f32 v191,1.0,v191
s_setprio 1
s_waitcnt lgkmcnt(0)
v_mfma_f32_32x32x16_bf16 v[144:159],v[200:203],v[72:75],v[144:159]
v_rcp_f32 v176,v176
v_rcp_f32 v177,v177
v_rcp_f32 v178,v178
v_mfma_f32_32x32x16_bf16 v[144:159],v[204:207],v[76:79],v[144:159]
v_rcp_f32 v179,v179
v_rcp_f32 v180,v180
v_rcp_f32 v181,v181
v_mfma_f32_32x32x16_bf16 v[144:159],v[208:211],v[80:83],v[144:159]
v_rcp_f32 v182,v182
v_rcp_f32 v183,v183
v_rcp_f32 v184,v184
v_mfma_f32_32x32x16_bf16 v[144:159],v[212:215],v[84:87],v[144:159]
v_rcp_f32 v185,v185
v_rcp_f32 v186,v186
v_rcp_f32 v187,v187
v_mfma_f32_32x32x16_bf16 v[144:159],v[216:219],v[88:91],v[144:159]
v_rcp_f32 v188,v188
v_rcp_f32 v189,v189
v_mfma_f32_32x32x16_bf16 v[144:159],v[220:223],v[92:95],v[144:159]
v_rcp_f32 v190,v190
v_rcp_f32 v191,v191
ds_read_b64_tr_b16 v[200:201],v17 offset:12288
ds_read_b64_tr_b16 v[202:203],v17 offset:13824
ds_read_b64_tr_b16 v[204:205],v17 offset:15360
ds_read_b64_tr_b16 v[206:207],v17 offset:16896
ds_read_b64_tr_b16 v[208:209],v17 offset:12352
ds_read_b64_tr_b16 v[210:211],v17 offset:13888
ds_read_b64_tr_b16 v[212:213],v17 offset:15424
ds_read_b64_tr_b16 v[214:215],v17 offset:16960
ds_read_b64_tr_b16 v[216:217],v17 offset:12416
ds_read_b64_tr_b16 v[218:219],v17 offset:13952
ds_read_b64_tr_b16 v[220:221],v17 offset:15488
ds_read_b64_tr_b16 v[222:223],v17 offset:17024
s_mov_b64 exec,s[60:61]
s_add_u32 m0,s58,136704
s_movk_i32 s64,512
buffer_load_dwordx4 v27,s[44:47],s64 offen lds
s_add_u32 m0,s58,137472
s_movk_i32 s65,9728
buffer_load_dwordx4 v27,s[44:47],s65 offen lds
s_mov_b64 exec,-1
s_setprio 0
v_mul_f32 v176,v160,v176
v_mul_f32 v177,v161,v177
v_mul_f32 v178,v162,v178
v_mul_f32 v179,v163,v179
v_mul_f32 v180,v164,v180
v_mul_f32 v181,v165,v181
v_mul_f32 v182,v166,v182
v_mul_f32 v183,v167,v183
v_mul_f32 v184,v168,v184
v_mul_f32 v185,v169,v185
v_mul_f32 v186,v170,v186
v_mul_f32 v187,v171,v187
v_mul_f32 v188,v172,v188
v_mul_f32 v189,v173,v189
v_mul_f32 v190,v174,v190
v_mul_f32 v191,v175,v191
v_cvt_pk_bf16_f32 v192,v176,v177
v_cvt_pk_bf16_f32 v193,v178,v179
v_cvt_pk_bf16_f32 v194,v180,v181
v_cvt_pk_bf16_f32 v195,v182,v183
v_cvt_pk_bf16_f32 v196,v184,v185
v_cvt_pk_bf16_f32 v197,v186,v187
v_cvt_pk_bf16_f32 v198,v188,v189
v_cvt_pk_bf16_f32 v199,v190,v191
s_waitcnt lgkmcnt(0)
s_waitcnt vmcnt(2)
s_barrier
v_fmaak_f32 v176,v144,v144,0x409d30fc
v_fmaak_f32 v177,v145,v145,0x409d30fc
v_fmaak_f32 v178,v146,v146,0x409d30fc
v_fmaak_f32 v179,v147,v147,0x409d30fc
v_fmaak_f32 v180,v148,v148,0x409d30fc
v_fmaak_f32 v181,v149,v149,0x409d30fc
v_fmaak_f32 v182,v150,v150,0x409d30fc
v_fmaak_f32 v183,v151,v151,0x409d30fc
v_fmaak_f32 v184,v152,v152,0x409d30fc
v_fmaak_f32 v185,v153,v153,0x409d30fc
v_fmaak_f32 v186,v154,v154,0x409d30fc
v_fmaak_f32 v187,v155,v155,0x409d30fc
v_fmaak_f32 v188,v156,v156,0x409d30fc
v_fmaak_f32 v189,v157,v157,0x409d30fc
v_fmaak_f32 v190,v158,v158,0x409d30fc
v_fmaak_f32 v191,v159,v159,0x409d30fc
v_mul_f32 v176,v144,v176
v_mul_f32 v177,v145,v177
v_mul_f32 v178,v146,v178
v_mul_f32 v179,v147,v179
v_mul_f32 v180,v148,v180
v_mul_f32 v181,v149,v181
v_mul_f32 v182,v150,v182
v_mul_f32 v183,v151,v183
v_mul_f32 v184,v152,v184
v_mul_f32 v185,v153,v185
v_mul_f32 v186,v154,v186
v_mul_f32 v187,v155,v187
v_mul_f32 v188,v156,v188
v_mul_f32 v189,v157,v189
v_mul_f32 v190,v158,v190
v_mul_f32 v191,v159,v191
s_setprio 1
v_mfma_f32_32x32x16_bf16 v[96:111],v[200:203],v[192:195],v[96:111]
v_exp_f32 v176,v176
v_exp_f32 v177,v177
v_exp_f32 v178,v178
v_mfma_f32_32x32x16_bf16 v[96:111],v[204:207],v[196:199],v[96:111]
v_exp_f32 v179,v179
v_exp_f32 v180,v180
v_exp_f32 v181,v181
v_mfma_f32_32x32x16_bf16 v[112:127],v[208:211],v[192:195],v[112:127]
v_exp_f32 v182,v182
v_exp_f32 v183,v183
v_exp_f32 v184,v184
v_mfma_f32_32x32x16_bf16 v[112:127],v[212:215],v[196:199],v[112:127]
v_exp_f32 v185,v185
v_exp_f32 v186,v186
v_exp_f32 v187,v187
v_mfma_f32_32x32x16_bf16 v[128:143],v[216:219],v[192:195],v[128:143]
v_exp_f32 v188,v188
v_exp_f32 v189,v189
v_mfma_f32_32x32x16_bf16 v[128:143],v[220:223],v[196:199],v[128:143]
v_exp_f32 v190,v190
v_exp_f32 v191,v191
ds_read_b64_tr_b16 v[200:201],v16 offset:6144
ds_read_b64_tr_b16 v[202:203],v16 offset:6656
ds_read_b64_tr_b16 v[204:205],v16 offset:7168
ds_read_b64_tr_b16 v[206:207],v16 offset:7680
ds_read_b64_tr_b16 v[208:209],v16 offset:8192
ds_read_b64_tr_b16 v[210:211],v16 offset:8704
ds_read_b64_tr_b16 v[212:213],v16 offset:9216
ds_read_b64_tr_b16 v[214:215],v16 offset:9728
ds_read_b64_tr_b16 v[216:217],v16 offset:10240
ds_read_b64_tr_b16 v[218:219],v16 offset:10752
ds_read_b64_tr_b16 v[220:221],v16 offset:11264
ds_read_b64_tr_b16 v[222:223],v16 offset:11776
ds_read_b128 v[160:163],v18 offset:896
ds_read_b128 v[164:167],v18 offset:928
ds_read_b128 v[168:171],v18 offset:960
ds_read_b128 v[172:175],v18 offset:992
s_mov_b64 exec,s[60:61]
s_add_u32 m0,s58,148992
s_mov_b32 s64,43008
buffer_load_dwordx4 v28,s[48:51],s64 offen lds
s_add_u32 m0,s58,149760
s_mov_b32 s65,43776
buffer_load_dwordx4 v28,s[48:51],s65 offen lds
s_mov_b64 exec,-1
s_setprio 0
v_add_f32 v176,1.0,v176
v_add_f32 v177,1.0,v177
v_add_f32 v178,1.0,v178
v_add_f32 v179,1.0,v179
v_add_f32 v180,1.0,v180
v_add_f32 v181,1.0,v181
v_add_f32 v182,1.0,v182
v_add_f32 v183,1.0,v183
v_add_f32 v184,1.0,v184
v_add_f32 v185,1.0,v185
v_add_f32 v186,1.0,v186
v_add_f32 v187,1.0,v187
v_add_f32 v188,1.0,v188
v_add_f32 v189,1.0,v189
v_add_f32 v190,1.0,v190
v_add_f32 v191,1.0,v191
s_setprio 1
s_waitcnt lgkmcnt(0)
v_mfma_f32_32x32x16_bf16 v[160:175],v[200:203],v[72:75],v[160:175]
v_rcp_f32 v176,v176
v_rcp_f32 v177,v177
v_rcp_f32 v178,v178
v_mfma_f32_32x32x16_bf16 v[160:175],v[204:207],v[76:79],v[160:175]
v_rcp_f32 v179,v179
v_rcp_f32 v180,v180
v_rcp_f32 v181,v181
v_mfma_f32_32x32x16_bf16 v[160:175],v[208:211],v[80:83],v[160:175]
v_rcp_f32 v182,v182
v_rcp_f32 v183,v183
v_rcp_f32 v184,v184
v_mfma_f32_32x32x16_bf16 v[160:175],v[212:215],v[84:87],v[160:175]
v_rcp_f32 v185,v185
v_rcp_f32 v186,v186
v_rcp_f32 v187,v187
v_mfma_f32_32x32x16_bf16 v[160:175],v[216:219],v[88:91],v[160:175]
v_rcp_f32 v188,v188
v_rcp_f32 v189,v189
v_mfma_f32_32x32x16_bf16 v[160:175],v[220:223],v[92:95],v[160:175]
v_rcp_f32 v190,v190
v_rcp_f32 v191,v191
ds_read_b64_tr_b16 v[200:201],v17 offset:0
ds_read_b64_tr_b16 v[202:203],v17 offset:1536
ds_read_b64_tr_b16 v[204:205],v17 offset:3072
ds_read_b64_tr_b16 v[206:207],v17 offset:4608
ds_read_b64_tr_b16 v[208:209],v17 offset:64
ds_read_b64_tr_b16 v[210:211],v17 offset:1600
ds_read_b64_tr_b16 v[212:213],v17 offset:3136
ds_read_b64_tr_b16 v[214:215],v17 offset:4672
ds_read_b64_tr_b16 v[216:217],v17 offset:128
ds_read_b64_tr_b16 v[218:219],v17 offset:1664
ds_read_b64_tr_b16 v[220:221],v17 offset:3200
ds_read_b64_tr_b16 v[222:223],v17 offset:4736
s_mov_b64 exec,s[60:61]
s_add_u32 m0,s58,124416
s_movk_i32 s64,576
buffer_load_dwordx4 v27,s[44:47],s64 offen lds
s_add_u32 m0,s58,125184
s_movk_i32 s65,9792
buffer_load_dwordx4 v27,s[44:47],s65 offen lds
s_mov_b64 exec,-1
s_setprio 0
v_mul_f32 v176,v144,v176
v_mul_f32 v177,v145,v177
v_mul_f32 v178,v146,v178
v_mul_f32 v179,v147,v179
v_mul_f32 v180,v148,v180
v_mul_f32 v181,v149,v181
v_mul_f32 v182,v150,v182
v_mul_f32 v183,v151,v183
v_mul_f32 v184,v152,v184
v_mul_f32 v185,v153,v185
v_mul_f32 v186,v154,v186
v_mul_f32 v187,v155,v187
v_mul_f32 v188,v156,v188
v_mul_f32 v189,v157,v189
v_mul_f32 v190,v158,v190
v_mul_f32 v191,v159,v191
v_cvt_pk_bf16_f32 v192,v176,v177
v_cvt_pk_bf16_f32 v193,v178,v179
v_cvt_pk_bf16_f32 v194,v180,v181
v_cvt_pk_bf16_f32 v195,v182,v183
v_cvt_pk_bf16_f32 v196,v184,v185
v_cvt_pk_bf16_f32 v197,v186,v187
v_cvt_pk_bf16_f32 v198,v188,v189
v_cvt_pk_bf16_f32 v199,v190,v191
s_waitcnt lgkmcnt(0)
s_waitcnt vmcnt(2)
s_barrier
v_fmaak_f32 v176,v160,v160,0x409d30fc
v_fmaak_f32 v177,v161,v161,0x409d30fc
v_fmaak_f32 v178,v162,v162,0x409d30fc
v_fmaak_f32 v179,v163,v163,0x409d30fc
v_fmaak_f32 v180,v164,v164,0x409d30fc
v_fmaak_f32 v181,v165,v165,0x409d30fc
v_fmaak_f32 v182,v166,v166,0x409d30fc
v_fmaak_f32 v183,v167,v167,0x409d30fc
v_fmaak_f32 v184,v168,v168,0x409d30fc
v_fmaak_f32 v185,v169,v169,0x409d30fc
v_fmaak_f32 v186,v170,v170,0x409d30fc
v_fmaak_f32 v187,v171,v171,0x409d30fc
v_fmaak_f32 v188,v172,v172,0x409d30fc
v_fmaak_f32 v189,v173,v173,0x409d30fc
v_fmaak_f32 v190,v174,v174,0x409d30fc
v_fmaak_f32 v191,v175,v175,0x409d30fc
v_mul_f32 v176,v160,v176
v_mul_f32 v177,v161,v177
v_mul_f32 v178,v162,v178
v_mul_f32 v179,v163,v179
v_mul_f32 v180,v164,v180
v_mul_f32 v181,v165,v181
v_mul_f32 v182,v166,v182
v_mul_f32 v183,v167,v183
v_mul_f32 v184,v168,v184
v_mul_f32 v185,v169,v185
v_mul_f32 v186,v170,v186
v_mul_f32 v187,v171,v187
v_mul_f32 v188,v172,v188
v_mul_f32 v189,v173,v189
v_mul_f32 v190,v174,v190
v_mul_f32 v191,v175,v191
s_setprio 1
v_mfma_f32_32x32x16_bf16 v[96:111],v[200:203],v[192:195],v[96:111]
v_exp_f32 v176,v176
v_exp_f32 v177,v177
v_exp_f32 v178,v178
v_mfma_f32_32x32x16_bf16 v[96:111],v[204:207],v[196:199],v[96:111]
v_exp_f32 v179,v179
v_exp_f32 v180,v180
v_exp_f32 v181,v181
v_mfma_f32_32x32x16_bf16 v[112:127],v[208:211],v[192:195],v[112:127]
v_exp_f32 v182,v182
v_exp_f32 v183,v183
v_exp_f32 v184,v184
v_mfma_f32_32x32x16_bf16 v[112:127],v[212:215],v[196:199],v[112:127]
v_exp_f32 v185,v185
v_exp_f32 v186,v186
v_exp_f32 v187,v187
v_mfma_f32_32x32x16_bf16 v[128:143],v[216:219],v[192:195],v[128:143]
v_exp_f32 v188,v188
v_exp_f32 v189,v189
v_mfma_f32_32x32x16_bf16 v[128:143],v[220:223],v[196:199],v[128:143]
v_exp_f32 v190,v190
v_exp_f32 v191,v191
ds_read_b64_tr_b16 v[200:201],v16 offset:12288
ds_read_b64_tr_b16 v[202:203],v16 offset:12800
ds_read_b64_tr_b16 v[204:205],v16 offset:13312
ds_read_b64_tr_b16 v[206:207],v16 offset:13824
ds_read_b64_tr_b16 v[208:209],v16 offset:14336
ds_read_b64_tr_b16 v[210:211],v16 offset:14848
ds_read_b64_tr_b16 v[212:213],v16 offset:15360
ds_read_b64_tr_b16 v[214:215],v16 offset:15872
ds_read_b64_tr_b16 v[216:217],v16 offset:16384
ds_read_b64_tr_b16 v[218:219],v16 offset:16896
ds_read_b64_tr_b16 v[220:221],v16 offset:17408
ds_read_b64_tr_b16 v[222:223],v16 offset:17920
ds_read_b128 v[144:147],v18 offset:1024
ds_read_b128 v[148:151],v18 offset:1056
ds_read_b128 v[152:155],v18 offset:1088
ds_read_b128 v[156:159],v18 offset:1120
s_mov_b64 exec,s[60:61]
s_add_u32 m0,s58,155136
s_mov_b32 s64,49152
buffer_load_dwordx4 v28,s[48:51],s64 offen lds
s_add_u32 m0,s58,155904
s_mov_b32 s65,49920
buffer_load_dwordx4 v28,s[48:51],s65 offen lds
s_mov_b64 exec,-1
s_setprio 0
v_add_f32 v176,1.0,v176
v_add_f32 v177,1.0,v177
v_add_f32 v178,1.0,v178
v_add_f32 v179,1.0,v179
v_add_f32 v180,1.0,v180
v_add_f32 v181,1.0,v181
v_add_f32 v182,1.0,v182
v_add_f32 v183,1.0,v183
v_add_f32 v184,1.0,v184
v_add_f32 v185,1.0,v185
v_add_f32 v186,1.0,v186
v_add_f32 v187,1.0,v187
v_add_f32 v188,1.0,v188
v_add_f32 v189,1.0,v189
v_add_f32 v190,1.0,v190
v_add_f32 v191,1.0,v191
s_setprio 1
s_waitcnt lgkmcnt(0)
v_mfma_f32_32x32x16_bf16 v[144:159],v[200:203],v[72:75],v[144:159]
v_rcp_f32 v176,v176
v_rcp_f32 v177,v177
v_rcp_f32 v178,v178
v_mfma_f32_32x32x16_bf16 v[144:159],v[204:207],v[76:79],v[144:159]
v_rcp_f32 v179,v179
v_rcp_f32 v180,v180
v_rcp_f32 v181,v181
v_mfma_f32_32x32x16_bf16 v[144:159],v[208:211],v[80:83],v[144:159]
v_rcp_f32 v182,v182
v_rcp_f32 v183,v183
v_rcp_f32 v184,v184
v_mfma_f32_32x32x16_bf16 v[144:159],v[212:215],v[84:87],v[144:159]
v_rcp_f32 v185,v185
v_rcp_f32 v186,v186
v_rcp_f32 v187,v187
v_mfma_f32_32x32x16_bf16 v[144:159],v[216:219],v[88:91],v[144:159]
v_rcp_f32 v188,v188
v_rcp_f32 v189,v189
v_mfma_f32_32x32x16_bf16 v[144:159],v[220:223],v[92:95],v[144:159]
v_rcp_f32 v190,v190
v_rcp_f32 v191,v191
ds_read_b64_tr_b16 v[200:201],v17 offset:6144
ds_read_b64_tr_b16 v[202:203],v17 offset:7680
ds_read_b64_tr_b16 v[204:205],v17 offset:9216
ds_read_b64_tr_b16 v[206:207],v17 offset:10752
ds_read_b64_tr_b16 v[208:209],v17 offset:6208
ds_read_b64_tr_b16 v[210:211],v17 offset:7744
ds_read_b64_tr_b16 v[212:213],v17 offset:9280
ds_read_b64_tr_b16 v[214:215],v17 offset:10816
ds_read_b64_tr_b16 v[216:217],v17 offset:6272
ds_read_b64_tr_b16 v[218:219],v17 offset:7808
ds_read_b64_tr_b16 v[220:221],v17 offset:9344
ds_read_b64_tr_b16 v[222:223],v17 offset:10880
s_mov_b64 exec,s[60:61]
s_add_u32 m0,s58,130560
s_movk_i32 s64,640
buffer_load_dwordx4 v27,s[44:47],s64 offen lds
s_add_u32 m0,s58,131328
s_movk_i32 s65,9856
buffer_load_dwordx4 v27,s[44:47],s65 offen lds
s_mov_b64 exec,-1
s_setprio 0
v_mul_f32 v176,v160,v176
v_mul_f32 v177,v161,v177
v_mul_f32 v178,v162,v178
v_mul_f32 v179,v163,v179
v_mul_f32 v180,v164,v180
v_mul_f32 v181,v165,v181
v_mul_f32 v182,v166,v182
v_mul_f32 v183,v167,v183
v_mul_f32 v184,v168,v184
v_mul_f32 v185,v169,v185
v_mul_f32 v186,v170,v186
v_mul_f32 v187,v171,v187
v_mul_f32 v188,v172,v188
v_mul_f32 v189,v173,v189
v_mul_f32 v190,v174,v190
v_mul_f32 v191,v175,v191
v_cvt_pk_bf16_f32 v192,v176,v177
v_cvt_pk_bf16_f32 v193,v178,v179
v_cvt_pk_bf16_f32 v194,v180,v181
v_cvt_pk_bf16_f32 v195,v182,v183
v_cvt_pk_bf16_f32 v196,v184,v185
v_cvt_pk_bf16_f32 v197,v186,v187
v_cvt_pk_bf16_f32 v198,v188,v189
v_cvt_pk_bf16_f32 v199,v190,v191
s_waitcnt lgkmcnt(0)
s_waitcnt vmcnt(2)
s_barrier
v_fmaak_f32 v176,v144,v144,0x409d30fc
v_fmaak_f32 v177,v145,v145,0x409d30fc
v_fmaak_f32 v178,v146,v146,0x409d30fc
v_fmaak_f32 v179,v147,v147,0x409d30fc
v_fmaak_f32 v180,v148,v148,0x409d30fc
v_fmaak_f32 v181,v149,v149,0x409d30fc
v_fmaak_f32 v182,v150,v150,0x409d30fc
v_fmaak_f32 v183,v151,v151,0x409d30fc
v_fmaak_f32 v184,v152,v152,0x409d30fc
v_fmaak_f32 v185,v153,v153,0x409d30fc
v_fmaak_f32 v186,v154,v154,0x409d30fc
v_fmaak_f32 v187,v155,v155,0x409d30fc
v_fmaak_f32 v188,v156,v156,0x409d30fc
v_fmaak_f32 v189,v157,v157,0x409d30fc
v_fmaak_f32 v190,v158,v158,0x409d30fc
v_fmaak_f32 v191,v159,v159,0x409d30fc
v_mul_f32 v176,v144,v176
v_mul_f32 v177,v145,v177
v_mul_f32 v178,v146,v178
v_mul_f32 v179,v147,v179
v_mul_f32 v180,v148,v180
v_mul_f32 v181,v149,v181
v_mul_f32 v182,v150,v182
v_mul_f32 v183,v151,v183
v_mul_f32 v184,v152,v184
v_mul_f32 v185,v153,v185
v_mul_f32 v186,v154,v186
v_mul_f32 v187,v155,v187
v_mul_f32 v188,v156,v188
v_mul_f32 v189,v157,v189
v_mul_f32 v190,v158,v190
v_mul_f32 v191,v159,v191
s_setprio 1
v_mfma_f32_32x32x16_bf16 v[96:111],v[200:203],v[192:195],v[96:111]
v_exp_f32 v176,v176
v_exp_f32 v177,v177
v_exp_f32 v178,v178
v_mfma_f32_32x32x16_bf16 v[96:111],v[204:207],v[196:199],v[96:111]
v_exp_f32 v179,v179
v_exp_f32 v180,v180
v_exp_f32 v181,v181
v_mfma_f32_32x32x16_bf16 v[112:127],v[208:211],v[192:195],v[112:127]
v_exp_f32 v182,v182
v_exp_f32 v183,v183
v_exp_f32 v184,v184
v_mfma_f32_32x32x16_bf16 v[112:127],v[212:215],v[196:199],v[112:127]
v_exp_f32 v185,v185
v_exp_f32 v186,v186
v_exp_f32 v187,v187
v_mfma_f32_32x32x16_bf16 v[128:143],v[216:219],v[192:195],v[128:143]
v_exp_f32 v188,v188
v_exp_f32 v189,v189
v_mfma_f32_32x32x16_bf16 v[128:143],v[220:223],v[196:199],v[128:143]
v_exp_f32 v190,v190
v_exp_f32 v191,v191
ds_read_b64_tr_b16 v[200:201],v16 offset:0
ds_read_b64_tr_b16 v[202:203],v16 offset:512
ds_read_b64_tr_b16 v[204:205],v16 offset:1024
ds_read_b64_tr_b16 v[206:207],v16 offset:1536
ds_read_b64_tr_b16 v[208:209],v16 offset:2048
ds_read_b64_tr_b16 v[210:211],v16 offset:2560
ds_read_b64_tr_b16 v[212:213],v16 offset:3072
ds_read_b64_tr_b16 v[214:215],v16 offset:3584
ds_read_b64_tr_b16 v[216:217],v16 offset:4096
ds_read_b64_tr_b16 v[218:219],v16 offset:4608
ds_read_b64_tr_b16 v[220:221],v16 offset:5120
ds_read_b64_tr_b16 v[222:223],v16 offset:5632
ds_read_b128 v[160:163],v18 offset:1152
ds_read_b128 v[164:167],v18 offset:1184
ds_read_b128 v[168:171],v18 offset:1216
ds_read_b128 v[172:175],v18 offset:1248
s_mov_b64 exec,s[60:61]
s_add_u32 m0,s58,142848
s_mov_b32 s64,55296
buffer_load_dwordx4 v28,s[48:51],s64 offen lds
s_add_u32 m0,s58,143616
s_mov_b32 s65,56064
buffer_load_dwordx4 v28,s[48:51],s65 offen lds
s_mov_b64 exec,-1
s_setprio 0
v_add_f32 v176,1.0,v176
v_add_f32 v177,1.0,v177
v_add_f32 v178,1.0,v178
v_add_f32 v179,1.0,v179
v_add_f32 v180,1.0,v180
v_add_f32 v181,1.0,v181
v_add_f32 v182,1.0,v182
v_add_f32 v183,1.0,v183
v_add_f32 v184,1.0,v184
v_add_f32 v185,1.0,v185
v_add_f32 v186,1.0,v186
v_add_f32 v187,1.0,v187
v_add_f32 v188,1.0,v188
v_add_f32 v189,1.0,v189
v_add_f32 v190,1.0,v190
v_add_f32 v191,1.0,v191
s_setprio 1
s_waitcnt lgkmcnt(0)
v_mfma_f32_32x32x16_bf16 v[160:175],v[200:203],v[72:75],v[160:175]
v_rcp_f32 v176,v176
v_rcp_f32 v177,v177
v_rcp_f32 v178,v178
v_mfma_f32_32x32x16_bf16 v[160:175],v[204:207],v[76:79],v[160:175]
v_rcp_f32 v179,v179
v_rcp_f32 v180,v180
v_rcp_f32 v181,v181
v_mfma_f32_32x32x16_bf16 v[160:175],v[208:211],v[80:83],v[160:175]
v_rcp_f32 v182,v182
v_rcp_f32 v183,v183
v_rcp_f32 v184,v184
v_mfma_f32_32x32x16_bf16 v[160:175],v[212:215],v[84:87],v[160:175]
v_rcp_f32 v185,v185
v_rcp_f32 v186,v186
v_rcp_f32 v187,v187
v_mfma_f32_32x32x16_bf16 v[160:175],v[216:219],v[88:91],v[160:175]
v_rcp_f32 v188,v188
v_rcp_f32 v189,v189
v_mfma_f32_32x32x16_bf16 v[160:175],v[220:223],v[92:95],v[160:175]
v_rcp_f32 v190,v190
v_rcp_f32 v191,v191
ds_read_b64_tr_b16 v[200:201],v17 offset:12288
ds_read_b64_tr_b16 v[202:203],v17 offset:13824
ds_read_b64_tr_b16 v[204:205],v17 offset:15360
ds_read_b64_tr_b16 v[206:207],v17 offset:16896
ds_read_b64_tr_b16 v[208:209],v17 offset:12352
ds_read_b64_tr_b16 v[210:211],v17 offset:13888
ds_read_b64_tr_b16 v[212:213],v17 offset:15424
ds_read_b64_tr_b16 v[214:215],v17 offset:16960
ds_read_b64_tr_b16 v[216:217],v17 offset:12416
ds_read_b64_tr_b16 v[218:219],v17 offset:13952
ds_read_b64_tr_b16 v[220:221],v17 offset:15488
ds_read_b64_tr_b16 v[222:223],v17 offset:17024
s_mov_b64 exec,s[60:61]
s_add_u32 m0,s58,136704
s_movk_i32 s64,704
buffer_load_dwordx4 v27,s[44:47],s64 offen lds
s_add_u32 m0,s58,137472
s_movk_i32 s65,9920
buffer_load_dwordx4 v27,s[44:47],s65 offen lds
s_mov_b64 exec,-1
s_setprio 0
v_mul_f32 v176,v144,v176
v_mul_f32 v177,v145,v177
v_mul_f32 v178,v146,v178
v_mul_f32 v179,v147,v179
v_mul_f32 v180,v148,v180
v_mul_f32 v181,v149,v181
v_mul_f32 v182,v150,v182
v_mul_f32 v183,v151,v183
v_mul_f32 v184,v152,v184
v_mul_f32 v185,v153,v185
v_mul_f32 v186,v154,v186
v_mul_f32 v187,v155,v187
v_mul_f32 v188,v156,v188
v_mul_f32 v189,v157,v189
v_mul_f32 v190,v158,v190
v_mul_f32 v191,v159,v191
v_cvt_pk_bf16_f32 v192,v176,v177
v_cvt_pk_bf16_f32 v193,v178,v179
v_cvt_pk_bf16_f32 v194,v180,v181
v_cvt_pk_bf16_f32 v195,v182,v183
v_cvt_pk_bf16_f32 v196,v184,v185
v_cvt_pk_bf16_f32 v197,v186,v187
v_cvt_pk_bf16_f32 v198,v188,v189
v_cvt_pk_bf16_f32 v199,v190,v191
s_waitcnt lgkmcnt(0)
s_waitcnt vmcnt(2)
s_barrier
v_fmaak_f32 v176,v160,v160,0x409d30fc
v_fmaak_f32 v177,v161,v161,0x409d30fc
v_fmaak_f32 v178,v162,v162,0x409d30fc
v_fmaak_f32 v179,v163,v163,0x409d30fc
v_fmaak_f32 v180,v164,v164,0x409d30fc
v_fmaak_f32 v181,v165,v165,0x409d30fc
v_fmaak_f32 v182,v166,v166,0x409d30fc
v_fmaak_f32 v183,v167,v167,0x409d30fc
v_fmaak_f32 v184,v168,v168,0x409d30fc
v_fmaak_f32 v185,v169,v169,0x409d30fc
v_fmaak_f32 v186,v170,v170,0x409d30fc
v_fmaak_f32 v187,v171,v171,0x409d30fc
v_fmaak_f32 v188,v172,v172,0x409d30fc
v_fmaak_f32 v189,v173,v173,0x409d30fc
v_fmaak_f32 v190,v174,v174,0x409d30fc
v_fmaak_f32 v191,v175,v175,0x409d30fc
v_mul_f32 v176,v160,v176
v_mul_f32 v177,v161,v177
v_mul_f32 v178,v162,v178
v_mul_f32 v179,v163,v179
v_mul_f32 v180,v164,v180
v_mul_f32 v181,v165,v181
v_mul_f32 v182,v166,v182
v_mul_f32 v183,v167,v183
v_mul_f32 v184,v168,v184
v_mul_f32 v185,v169,v185
v_mul_f32 v186,v170,v186
v_mul_f32 v187,v171,v187
v_mul_f32 v188,v172,v188
v_mul_f32 v189,v173,v189
v_mul_f32 v190,v174,v190
v_mul_f32 v191,v175,v191
s_setprio 1
v_mfma_f32_32x32x16_bf16 v[96:111],v[200:203],v[192:195],v[96:111]
v_exp_f32 v176,v176
v_exp_f32 v177,v177
v_exp_f32 v178,v178
v_mfma_f32_32x32x16_bf16 v[96:111],v[204:207],v[196:199],v[96:111]
v_exp_f32 v179,v179
v_exp_f32 v180,v180
v_exp_f32 v181,v181
v_mfma_f32_32x32x16_bf16 v[112:127],v[208:211],v[192:195],v[112:127]
v_exp_f32 v182,v182
v_exp_f32 v183,v183
v_exp_f32 v184,v184
v_mfma_f32_32x32x16_bf16 v[112:127],v[212:215],v[196:199],v[112:127]
v_exp_f32 v185,v185
v_exp_f32 v186,v186
v_exp_f32 v187,v187
v_mfma_f32_32x32x16_bf16 v[128:143],v[216:219],v[192:195],v[128:143]
v_exp_f32 v188,v188
v_exp_f32 v189,v189
v_mfma_f32_32x32x16_bf16 v[128:143],v[220:223],v[196:199],v[128:143]
v_exp_f32 v190,v190
v_exp_f32 v191,v191
ds_read_b64_tr_b16 v[200:201],v16 offset:6144
ds_read_b64_tr_b16 v[202:203],v16 offset:6656
ds_read_b64_tr_b16 v[204:205],v16 offset:7168
ds_read_b64_tr_b16 v[206:207],v16 offset:7680
ds_read_b64_tr_b16 v[208:209],v16 offset:8192
ds_read_b64_tr_b16 v[210:211],v16 offset:8704
ds_read_b64_tr_b16 v[212:213],v16 offset:9216
ds_read_b64_tr_b16 v[214:215],v16 offset:9728
ds_read_b64_tr_b16 v[216:217],v16 offset:10240
ds_read_b64_tr_b16 v[218:219],v16 offset:10752
ds_read_b64_tr_b16 v[220:221],v16 offset:11264
ds_read_b64_tr_b16 v[222:223],v16 offset:11776
ds_read_b128 v[144:147],v18 offset:1280
ds_read_b128 v[148:151],v18 offset:1312
ds_read_b128 v[152:155],v18 offset:1344
ds_read_b128 v[156:159],v18 offset:1376
s_mov_b64 exec,s[60:61]
s_add_u32 m0,s58,148992
s_mov_b32 s64,61440
buffer_load_dwordx4 v28,s[48:51],s64 offen lds
s_add_u32 m0,s58,149760
s_mov_b32 s65,62208
buffer_load_dwordx4 v28,s[48:51],s65 offen lds
s_mov_b64 exec,-1
s_setprio 0
v_add_f32 v176,1.0,v176
v_add_f32 v177,1.0,v177
v_add_f32 v178,1.0,v178
v_add_f32 v179,1.0,v179
v_add_f32 v180,1.0,v180
v_add_f32 v181,1.0,v181
v_add_f32 v182,1.0,v182
v_add_f32 v183,1.0,v183
v_add_f32 v184,1.0,v184
v_add_f32 v185,1.0,v185
v_add_f32 v186,1.0,v186
v_add_f32 v187,1.0,v187
v_add_f32 v188,1.0,v188
v_add_f32 v189,1.0,v189
v_add_f32 v190,1.0,v190
v_add_f32 v191,1.0,v191
s_setprio 1
s_waitcnt lgkmcnt(0)
v_mfma_f32_32x32x16_bf16 v[144:159],v[200:203],v[72:75],v[144:159]
v_rcp_f32 v176,v176
v_rcp_f32 v177,v177
v_rcp_f32 v178,v178
v_mfma_f32_32x32x16_bf16 v[144:159],v[204:207],v[76:79],v[144:159]
v_rcp_f32 v179,v179
v_rcp_f32 v180,v180
v_rcp_f32 v181,v181
v_mfma_f32_32x32x16_bf16 v[144:159],v[208:211],v[80:83],v[144:159]
v_rcp_f32 v182,v182
v_rcp_f32 v183,v183
v_rcp_f32 v184,v184
v_mfma_f32_32x32x16_bf16 v[144:159],v[212:215],v[84:87],v[144:159]
v_rcp_f32 v185,v185
v_rcp_f32 v186,v186
v_rcp_f32 v187,v187
v_mfma_f32_32x32x16_bf16 v[144:159],v[216:219],v[88:91],v[144:159]
v_rcp_f32 v188,v188
v_rcp_f32 v189,v189
v_mfma_f32_32x32x16_bf16 v[144:159],v[220:223],v[92:95],v[144:159]
v_rcp_f32 v190,v190
v_rcp_f32 v191,v191
ds_read_b64_tr_b16 v[200:201],v17 offset:0
ds_read_b64_tr_b16 v[202:203],v17 offset:1536
ds_read_b64_tr_b16 v[204:205],v17 offset:3072
ds_read_b64_tr_b16 v[206:207],v17 offset:4608
ds_read_b64_tr_b16 v[208:209],v17 offset:64
ds_read_b64_tr_b16 v[210:211],v17 offset:1600
ds_read_b64_tr_b16 v[212:213],v17 offset:3136
ds_read_b64_tr_b16 v[214:215],v17 offset:4672
ds_read_b64_tr_b16 v[216:217],v17 offset:128
ds_read_b64_tr_b16 v[218:219],v17 offset:1664
ds_read_b64_tr_b16 v[220:221],v17 offset:3200
ds_read_b64_tr_b16 v[222:223],v17 offset:4736
s_mov_b64 exec,s[60:61]
s_add_u32 m0,s58,124416
s_movk_i32 s64,0
buffer_load_dwordx4 v27,s[44:47],s64 offen lds
s_add_u32 m0,s58,125184
s_movk_i32 s65,9216
buffer_load_dwordx4 v27,s[44:47],s65 offen lds
s_mov_b64 exec,-1
s_setprio 0
v_mul_f32 v176,v160,v176
v_mul_f32 v177,v161,v177
v_mul_f32 v178,v162,v178
v_mul_f32 v179,v163,v179
v_mul_f32 v180,v164,v180
v_mul_f32 v181,v165,v181
v_mul_f32 v182,v166,v182
v_mul_f32 v183,v167,v183
v_mul_f32 v184,v168,v184
v_mul_f32 v185,v169,v185
v_mul_f32 v186,v170,v186
v_mul_f32 v187,v171,v187
v_mul_f32 v188,v172,v188
v_mul_f32 v189,v173,v189
v_mul_f32 v190,v174,v190
v_mul_f32 v191,v175,v191
v_cvt_pk_bf16_f32 v192,v176,v177
v_cvt_pk_bf16_f32 v193,v178,v179
v_cvt_pk_bf16_f32 v194,v180,v181
v_cvt_pk_bf16_f32 v195,v182,v183
v_cvt_pk_bf16_f32 v196,v184,v185
v_cvt_pk_bf16_f32 v197,v186,v187
v_cvt_pk_bf16_f32 v198,v188,v189
v_cvt_pk_bf16_f32 v199,v190,v191
s_waitcnt lgkmcnt(0)
s_waitcnt vmcnt(2)
s_barrier
v_fmaak_f32 v176,v144,v144,0x409d30fc
v_fmaak_f32 v177,v145,v145,0x409d30fc
v_fmaak_f32 v178,v146,v146,0x409d30fc
v_fmaak_f32 v179,v147,v147,0x409d30fc
v_fmaak_f32 v180,v148,v148,0x409d30fc
v_fmaak_f32 v181,v149,v149,0x409d30fc
v_fmaak_f32 v182,v150,v150,0x409d30fc
v_fmaak_f32 v183,v151,v151,0x409d30fc
v_fmaak_f32 v184,v152,v152,0x409d30fc
v_fmaak_f32 v185,v153,v153,0x409d30fc
v_fmaak_f32 v186,v154,v154,0x409d30fc
v_fmaak_f32 v187,v155,v155,0x409d30fc
v_fmaak_f32 v188,v156,v156,0x409d30fc
v_fmaak_f32 v189,v157,v157,0x409d30fc
v_fmaak_f32 v190,v158,v158,0x409d30fc
v_fmaak_f32 v191,v159,v159,0x409d30fc
v_mul_f32 v176,v144,v176
v_mul_f32 v177,v145,v177
v_mul_f32 v178,v146,v178
v_mul_f32 v179,v147,v179
v_mul_f32 v180,v148,v180
v_mul_f32 v181,v149,v181
v_mul_f32 v182,v150,v182
v_mul_f32 v183,v151,v183
v_mul_f32 v184,v152,v184
v_mul_f32 v185,v153,v185
v_mul_f32 v186,v154,v186
v_mul_f32 v187,v155,v187
v_mul_f32 v188,v156,v188
v_mul_f32 v189,v157,v189
v_mul_f32 v190,v158,v190
v_mul_f32 v191,v159,v191
s_setprio 1
v_mfma_f32_32x32x16_bf16 v[96:111],v[200:203],v[192:195],v[96:111]
v_exp_f32 v176,v176
v_exp_f32 v177,v177
v_exp_f32 v178,v178
v_mfma_f32_32x32x16_bf16 v[96:111],v[204:207],v[196:199],v[96:111]
v_exp_f32 v179,v179
v_exp_f32 v180,v180
v_exp_f32 v181,v181
v_mfma_f32_32x32x16_bf16 v[112:127],v[208:211],v[192:195],v[112:127]
v_exp_f32 v182,v182
v_exp_f32 v183,v183
v_exp_f32 v184,v184
v_mfma_f32_32x32x16_bf16 v[112:127],v[212:215],v[196:199],v[112:127]
v_exp_f32 v185,v185
v_exp_f32 v186,v186
v_exp_f32 v187,v187
v_mfma_f32_32x32x16_bf16 v[128:143],v[216:219],v[192:195],v[128:143]
v_exp_f32 v188,v188
v_exp_f32 v189,v189
v_mfma_f32_32x32x16_bf16 v[128:143],v[220:223],v[196:199],v[128:143]
v_exp_f32 v190,v190
v_exp_f32 v191,v191
ds_read_b64_tr_b16 v[200:201],v16 offset:12288
ds_read_b64_tr_b16 v[202:203],v16 offset:12800
ds_read_b64_tr_b16 v[204:205],v16 offset:13312
ds_read_b64_tr_b16 v[206:207],v16 offset:13824
ds_read_b64_tr_b16 v[208:209],v16 offset:14336
ds_read_b64_tr_b16 v[210:211],v16 offset:14848
ds_read_b64_tr_b16 v[212:213],v16 offset:15360
ds_read_b64_tr_b16 v[214:215],v16 offset:15872
ds_read_b64_tr_b16 v[216:217],v16 offset:16384
ds_read_b64_tr_b16 v[218:219],v16 offset:16896
ds_read_b64_tr_b16 v[220:221],v16 offset:17408
ds_read_b64_tr_b16 v[222:223],v16 offset:17920
ds_read_b128 v[160:163],v18 offset:1408
ds_read_b128 v[164:167],v18 offset:1440
ds_read_b128 v[168:171],v18 offset:1472
ds_read_b128 v[172:175],v18 offset:1504
s_mov_b64 exec,s[60:61]
s_add_u32 m0,s58,155136
s_mov_b32 s64,67584
buffer_load_dwordx4 v28,s[48:51],s64 offen lds
s_add_u32 m0,s58,155904
s_mov_b32 s65,68352
buffer_load_dwordx4 v28,s[48:51],s65 offen lds
s_mov_b64 exec,-1
s_setprio 0
v_add_f32 v176,1.0,v176
v_add_f32 v177,1.0,v177
v_add_f32 v178,1.0,v178
v_add_f32 v179,1.0,v179
v_add_f32 v180,1.0,v180
v_add_f32 v181,1.0,v181
v_add_f32 v182,1.0,v182
v_add_f32 v183,1.0,v183
v_add_f32 v184,1.0,v184
v_add_f32 v185,1.0,v185
v_add_f32 v186,1.0,v186
v_add_f32 v187,1.0,v187
v_add_f32 v188,1.0,v188
v_add_f32 v189,1.0,v189
v_add_f32 v190,1.0,v190
v_add_f32 v191,1.0,v191
s_setprio 1
s_waitcnt lgkmcnt(0)
v_mfma_f32_32x32x16_bf16 v[160:175],v[200:203],v[72:75],v[160:175]
v_rcp_f32 v176,v176
v_rcp_f32 v177,v177
v_rcp_f32 v178,v178
v_mfma_f32_32x32x16_bf16 v[160:175],v[204:207],v[76:79],v[160:175]
v_rcp_f32 v179,v179
v_rcp_f32 v180,v180
v_rcp_f32 v181,v181
v_mfma_f32_32x32x16_bf16 v[160:175],v[208:211],v[80:83],v[160:175]
v_rcp_f32 v182,v182
v_rcp_f32 v183,v183
v_rcp_f32 v184,v184
v_mfma_f32_32x32x16_bf16 v[160:175],v[212:215],v[84:87],v[160:175]
v_rcp_f32 v185,v185
v_rcp_f32 v186,v186
v_rcp_f32 v187,v187
v_mfma_f32_32x32x16_bf16 v[160:175],v[216:219],v[88:91],v[160:175]
v_rcp_f32 v188,v188
v_rcp_f32 v189,v189
v_mfma_f32_32x32x16_bf16 v[160:175],v[220:223],v[92:95],v[160:175]
v_rcp_f32 v190,v190
v_rcp_f32 v191,v191
ds_read_b64_tr_b16 v[200:201],v17 offset:6144
ds_read_b64_tr_b16 v[202:203],v17 offset:7680
ds_read_b64_tr_b16 v[204:205],v17 offset:9216
ds_read_b64_tr_b16 v[206:207],v17 offset:10752
ds_read_b64_tr_b16 v[208:209],v17 offset:6208
ds_read_b64_tr_b16 v[210:211],v17 offset:7744
ds_read_b64_tr_b16 v[212:213],v17 offset:9280
ds_read_b64_tr_b16 v[214:215],v17 offset:10816
ds_read_b64_tr_b16 v[216:217],v17 offset:6272
ds_read_b64_tr_b16 v[218:219],v17 offset:7808
ds_read_b64_tr_b16 v[220:221],v17 offset:9344
ds_read_b64_tr_b16 v[222:223],v17 offset:10880
s_mov_b64 exec,s[60:61]
s_add_u32 m0,s58,130560
s_movk_i32 s64,64
buffer_load_dwordx4 v27,s[44:47],s64 offen lds
s_add_u32 m0,s58,131328
s_movk_i32 s65,9280
buffer_load_dwordx4 v27,s[44:47],s65 offen lds
s_mov_b64 exec,-1
s_setprio 0
v_mul_f32 v176,v144,v176
v_mul_f32 v177,v145,v177
v_mul_f32 v178,v146,v178
v_mul_f32 v179,v147,v179
v_mul_f32 v180,v148,v180
v_mul_f32 v181,v149,v181
v_mul_f32 v182,v150,v182
v_mul_f32 v183,v151,v183
v_mul_f32 v184,v152,v184
v_mul_f32 v185,v153,v185
v_mul_f32 v186,v154,v186
v_mul_f32 v187,v155,v187
v_mul_f32 v188,v156,v188
v_mul_f32 v189,v157,v189
v_mul_f32 v190,v158,v190
v_mul_f32 v191,v159,v191
v_cvt_pk_bf16_f32 v192,v176,v177
v_cvt_pk_bf16_f32 v193,v178,v179
v_cvt_pk_bf16_f32 v194,v180,v181
v_cvt_pk_bf16_f32 v195,v182,v183
v_cvt_pk_bf16_f32 v196,v184,v185
v_cvt_pk_bf16_f32 v197,v186,v187
v_cvt_pk_bf16_f32 v198,v188,v189
v_cvt_pk_bf16_f32 v199,v190,v191
s_waitcnt lgkmcnt(0)
s_waitcnt vmcnt(2)
s_barrier
v_fmaak_f32 v176,v160,v160,0x409d30fc
v_fmaak_f32 v177,v161,v161,0x409d30fc
v_fmaak_f32 v178,v162,v162,0x409d30fc
v_fmaak_f32 v179,v163,v163,0x409d30fc
v_fmaak_f32 v180,v164,v164,0x409d30fc
v_fmaak_f32 v181,v165,v165,0x409d30fc
v_fmaak_f32 v182,v166,v166,0x409d30fc
v_fmaak_f32 v183,v167,v167,0x409d30fc
v_fmaak_f32 v184,v168,v168,0x409d30fc
v_fmaak_f32 v185,v169,v169,0x409d30fc
v_fmaak_f32 v186,v170,v170,0x409d30fc
v_fmaak_f32 v187,v171,v171,0x409d30fc
v_fmaak_f32 v188,v172,v172,0x409d30fc
v_fmaak_f32 v189,v173,v173,0x409d30fc
v_fmaak_f32 v190,v174,v174,0x409d30fc
v_fmaak_f32 v191,v175,v175,0x409d30fc
v_mul_f32 v176,v160,v176
v_mul_f32 v177,v161,v177
v_mul_f32 v178,v162,v178
v_mul_f32 v179,v163,v179
v_mul_f32 v180,v164,v180
v_mul_f32 v181,v165,v181
v_mul_f32 v182,v166,v182
v_mul_f32 v183,v167,v183
v_mul_f32 v184,v168,v184
v_mul_f32 v185,v169,v185
v_mul_f32 v186,v170,v186
v_mul_f32 v187,v171,v187
v_mul_f32 v188,v172,v188
v_mul_f32 v189,v173,v189
v_mul_f32 v190,v174,v190
v_mul_f32 v191,v175,v191
s_setprio 1
v_mfma_f32_32x32x16_bf16 v[96:111],v[200:203],v[192:195],v[96:111]
v_exp_f32 v176,v176
v_exp_f32 v177,v177
v_exp_f32 v178,v178
v_mfma_f32_32x32x16_bf16 v[96:111],v[204:207],v[196:199],v[96:111]
v_exp_f32 v179,v179
v_exp_f32 v180,v180
v_exp_f32 v181,v181
v_mfma_f32_32x32x16_bf16 v[112:127],v[208:211],v[192:195],v[112:127]
v_exp_f32 v182,v182
v_exp_f32 v183,v183
v_exp_f32 v184,v184
v_mfma_f32_32x32x16_bf16 v[112:127],v[212:215],v[196:199],v[112:127]
v_exp_f32 v185,v185
v_exp_f32 v186,v186
v_exp_f32 v187,v187
v_mfma_f32_32x32x16_bf16 v[128:143],v[216:219],v[192:195],v[128:143]
v_exp_f32 v188,v188
v_exp_f32 v189,v189
v_mfma_f32_32x32x16_bf16 v[128:143],v[220:223],v[196:199],v[128:143]
v_exp_f32 v190,v190
v_exp_f32 v191,v191
ds_read_b64 v[72:73],v20 offset:0
ds_read_b64 v[74:75],v21 offset:0
ds_read_b64 v[76:77],v22 offset:0
ds_read_b64 v[78:79],v23 offset:0
ds_read_b64 v[80:81],v20 offset:64
ds_read_b64 v[82:83],v21 offset:64
ds_read_b64 v[84:85],v22 offset:64
ds_read_b64 v[86:87],v23 offset:64
ds_read_b64 v[88:89],v20 offset:128
ds_read_b64 v[90:91],v21 offset:128
ds_read_b64 v[92:93],v22 offset:128
ds_read_b64 v[94:95],v23 offset:128
s_mov_b64 exec,s[60:61]
s_add_u32 m0,s58,142848
s_mov_b32 s64,0
buffer_load_dwordx4 v28,s[48:51],s64 offen lds
s_add_u32 m0,s58,143616
s_mov_b32 s65,768
buffer_load_dwordx4 v28,s[48:51],s65 offen lds
s_mov_b64 exec,-1
s_setprio 0
v_add_f32 v176,1.0,v176
v_add_f32 v177,1.0,v177
v_add_f32 v178,1.0,v178
v_add_f32 v179,1.0,v179
v_add_f32 v180,1.0,v180
v_add_f32 v181,1.0,v181
v_add_f32 v182,1.0,v182
v_add_f32 v183,1.0,v183
v_add_f32 v184,1.0,v184
v_add_f32 v185,1.0,v185
v_add_f32 v186,1.0,v186
v_add_f32 v187,1.0,v187
v_add_f32 v188,1.0,v188
v_add_f32 v189,1.0,v189
v_add_f32 v190,1.0,v190
v_add_f32 v191,1.0,v191
s_setprio 1
s_waitcnt lgkmcnt(0)
v_rcp_f32 v176,v176
v_rcp_f32 v177,v177
v_rcp_f32 v178,v178
v_rcp_f32 v179,v179
v_rcp_f32 v180,v180
v_rcp_f32 v181,v181
v_rcp_f32 v182,v182
v_rcp_f32 v183,v183
v_rcp_f32 v184,v184
v_rcp_f32 v185,v185
v_rcp_f32 v186,v186
v_rcp_f32 v187,v187
v_rcp_f32 v188,v188
v_rcp_f32 v189,v189
v_rcp_f32 v190,v190
v_rcp_f32 v191,v191
ds_read_b64_tr_b16 v[200:201],v17 offset:12288
ds_read_b64_tr_b16 v[202:203],v17 offset:13824
ds_read_b64_tr_b16 v[204:205],v17 offset:15360
ds_read_b64_tr_b16 v[206:207],v17 offset:16896
ds_read_b64_tr_b16 v[208:209],v17 offset:12352
ds_read_b64_tr_b16 v[210:211],v17 offset:13888
ds_read_b64_tr_b16 v[212:213],v17 offset:15424
ds_read_b64_tr_b16 v[214:215],v17 offset:16960
ds_read_b64_tr_b16 v[216:217],v17 offset:12416
ds_read_b64_tr_b16 v[218:219],v17 offset:13952
ds_read_b64_tr_b16 v[220:221],v17 offset:15488
ds_read_b64_tr_b16 v[222:223],v17 offset:17024
s_mov_b64 exec,s[60:61]
s_add_u32 m0,s58,136704
s_movk_i32 s64,128
buffer_load_dwordx4 v27,s[44:47],s64 offen lds
s_add_u32 m0,s58,137472
s_movk_i32 s65,9344
buffer_load_dwordx4 v27,s[44:47],s65 offen lds
s_mov_b64 exec,-1
s_setprio 0
v_mul_f32 v176,v160,v176
v_mul_f32 v177,v161,v177
v_mul_f32 v178,v162,v178
v_mul_f32 v179,v163,v179
v_mul_f32 v180,v164,v180
v_mul_f32 v181,v165,v181
v_mul_f32 v182,v166,v182
v_mul_f32 v183,v167,v183
v_mul_f32 v184,v168,v184
v_mul_f32 v185,v169,v185
v_mul_f32 v186,v170,v186
v_mul_f32 v187,v171,v187
v_mul_f32 v188,v172,v188
v_mul_f32 v189,v173,v189
v_mul_f32 v190,v174,v190
v_mul_f32 v191,v175,v191
v_cvt_pk_bf16_f32 v192,v176,v177
v_cvt_pk_bf16_f32 v193,v178,v179
v_cvt_pk_bf16_f32 v194,v180,v181
v_cvt_pk_bf16_f32 v195,v182,v183
v_cvt_pk_bf16_f32 v196,v184,v185
v_cvt_pk_bf16_f32 v197,v186,v187
v_cvt_pk_bf16_f32 v198,v188,v189
v_cvt_pk_bf16_f32 v199,v190,v191
ds_read_b128 v[144:147],v18 offset:1536
ds_read_b128 v[148:151],v18 offset:1568
ds_read_b128 v[152:155],v18 offset:1600
ds_read_b128 v[156:159],v18 offset:1632
ds_read_b128 v[160:163],v18 offset:1664
ds_read_b128 v[164:167],v18 offset:1696
ds_read_b128 v[168:171],v18 offset:1728
ds_read_b128 v[172:175],v18 offset:1760
ds_read_b128 v[176:179],v18 offset:1792
ds_read_b128 v[180:183],v18 offset:1824
ds_read_b128 v[184:187],v18 offset:1856
ds_read_b128 v[188:191],v18 offset:1888
s_waitcnt lgkmcnt(12)
s_setprio 1
v_mfma_f32_32x32x16_bf16 v[96:111],v[200:203],v[192:195],v[96:111]
v_lshlrev_b32 v232,16,v72
v_lshlrev_b32 v233,16,v73
v_lshlrev_b32 v234,16,v74
v_lshlrev_b32 v235,16,v75
v_mfma_f32_32x32x16_bf16 v[96:111],v[204:207],v[196:199],v[96:111]
v_lshlrev_b32 v236,16,v76
v_lshlrev_b32 v237,16,v77
v_lshlrev_b32 v238,16,v78
v_lshlrev_b32 v239,16,v79
v_mfma_f32_32x32x16_bf16 v[112:127],v[208:211],v[192:195],v[112:127]
v_lshlrev_b32 v240,16,v80
v_lshlrev_b32 v241,16,v81
v_lshlrev_b32 v242,16,v82
v_lshlrev_b32 v243,16,v83
v_mfma_f32_32x32x16_bf16 v[112:127],v[212:215],v[196:199],v[112:127]
v_lshlrev_b32 v244,16,v84
v_lshlrev_b32 v245,16,v85
v_lshlrev_b32 v246,16,v86
v_lshlrev_b32 v247,16,v87
v_mfma_f32_32x32x16_bf16 v[128:143],v[216:219],v[192:195],v[128:143]
v_lshlrev_b32 v248,16,v88
v_lshlrev_b32 v249,16,v89
v_lshlrev_b32 v250,16,v90
v_lshlrev_b32 v251,16,v91
v_mfma_f32_32x32x16_bf16 v[128:143],v[220:223],v[196:199],v[128:143]
v_lshlrev_b32 v252,16,v92
v_lshlrev_b32 v253,16,v93
v_lshlrev_b32 v254,16,v94
v_lshlrev_b32 v255,16,v95
s_setprio 0
s_waitcnt lgkmcnt(0)
v_and_b32 v72,0xffff0000,v72
v_and_b32 v73,0xffff0000,v73
v_and_b32 v74,0xffff0000,v74
v_and_b32 v75,0xffff0000,v75
v_and_b32 v76,0xffff0000,v76
v_and_b32 v77,0xffff0000,v77
v_and_b32 v78,0xffff0000,v78
v_and_b32 v79,0xffff0000,v79
v_and_b32 v80,0xffff0000,v80
v_and_b32 v81,0xffff0000,v81
v_and_b32 v82,0xffff0000,v82
v_and_b32 v83,0xffff0000,v83
v_and_b32 v84,0xffff0000,v84
v_and_b32 v85,0xffff0000,v85
v_and_b32 v86,0xffff0000,v86
v_and_b32 v87,0xffff0000,v87
v_and_b32 v88,0xffff0000,v88
v_and_b32 v89,0xffff0000,v89
v_and_b32 v90,0xffff0000,v90
v_and_b32 v91,0xffff0000,v91
v_and_b32 v92,0xffff0000,v92
v_and_b32 v93,0xffff0000,v93
v_and_b32 v94,0xffff0000,v94
v_and_b32 v95,0xffff0000,v95
v_fma_f32 v232,v96,v144,v232
v_fma_f32 v72,v97,v145,v72
v_fma_f32 v233,v98,v146,v233
v_fma_f32 v73,v99,v147,v73
v_fma_f32 v234,v100,v148,v234
v_fma_f32 v74,v101,v149,v74
v_fma_f32 v235,v102,v150,v235
v_fma_f32 v75,v103,v151,v75
v_fma_f32 v236,v104,v152,v236
v_fma_f32 v76,v105,v153,v76
v_fma_f32 v237,v106,v154,v237
v_fma_f32 v77,v107,v155,v77
v_fma_f32 v238,v108,v156,v238
v_fma_f32 v78,v109,v157,v78
v_fma_f32 v239,v110,v158,v239
v_fma_f32 v79,v111,v159,v79
v_fma_f32 v240,v112,v160,v240
v_fma_f32 v80,v113,v161,v80
v_fma_f32 v241,v114,v162,v241
v_fma_f32 v81,v115,v163,v81
v_fma_f32 v242,v116,v164,v242
v_fma_f32 v82,v117,v165,v82
v_fma_f32 v243,v118,v166,v243
v_fma_f32 v83,v119,v167,v83
v_fma_f32 v244,v120,v168,v244
v_fma_f32 v84,v121,v169,v84
v_fma_f32 v245,v122,v170,v245
v_fma_f32 v85,v123,v171,v85
v_fma_f32 v246,v124,v172,v246
v_fma_f32 v86,v125,v173,v86
v_fma_f32 v247,v126,v174,v247
v_fma_f32 v87,v127,v175,v87
v_fma_f32 v248,v128,v176,v248
v_fma_f32 v88,v129,v177,v88
v_fma_f32 v249,v130,v178,v249
v_fma_f32 v89,v131,v179,v89
v_fma_f32 v250,v132,v180,v250
v_fma_f32 v90,v133,v181,v90
v_fma_f32 v251,v134,v182,v251
v_fma_f32 v91,v135,v183,v91
v_fma_f32 v252,v136,v184,v252
v_fma_f32 v92,v137,v185,v92
v_fma_f32 v253,v138,v186,v253
v_fma_f32 v93,v139,v187,v93
v_fma_f32 v254,v140,v188,v254
v_fma_f32 v94,v141,v189,v94
v_fma_f32 v255,v142,v190,v255
v_fma_f32 v95,v143,v191,v95
v_add_u32 v4,s54,v4
v_add_u32 v5,s54,v5
v_add_u32 v6,s54,v6
v_add_u32 v7,s54,v7
v_add_u32 v8,s54,v8
v_add_u32 v9,s54,v9
v_add_u32 v10,s54,v10
v_add_u32 v11,s54,v11
v_add_u32 v12,s54,v12
v_add_u32 v13,s54,v13
v_add_u32 v14,s54,v14
v_add_u32 v15,s54,v15
v_cvt_pk_bf16_f32 v200,v232,v72
v_cvt_pk_bf16_f32 v201,v233,v73
v_cvt_pk_bf16_f32 v202,v234,v74
v_cvt_pk_bf16_f32 v203,v235,v75
v_cvt_pk_bf16_f32 v204,v236,v76
v_cvt_pk_bf16_f32 v205,v237,v77
v_cvt_pk_bf16_f32 v206,v238,v78
v_cvt_pk_bf16_f32 v207,v239,v79
v_cvt_pk_bf16_f32 v208,v240,v80
v_cvt_pk_bf16_f32 v209,v241,v81
v_cvt_pk_bf16_f32 v210,v242,v82
v_cvt_pk_bf16_f32 v211,v243,v83
v_cvt_pk_bf16_f32 v212,v244,v84
v_cvt_pk_bf16_f32 v213,v245,v85
v_cvt_pk_bf16_f32 v214,v246,v86
v_cvt_pk_bf16_f32 v215,v247,v87
v_cvt_pk_bf16_f32 v216,v248,v88
v_cvt_pk_bf16_f32 v217,v249,v89
v_cvt_pk_bf16_f32 v218,v250,v90
v_cvt_pk_bf16_f32 v219,v251,v91
v_cvt_pk_bf16_f32 v220,v252,v92
v_cvt_pk_bf16_f32 v221,v253,v93
v_cvt_pk_bf16_f32 v222,v254,v94
v_cvt_pk_bf16_f32 v223,v255,v95
ds_write_b64 v20,v[200:201] offset:0
ds_write_b64 v21,v[202:203] offset:0
ds_write_b64 v22,v[204:205] offset:0
ds_write_b64 v23,v[206:207] offset:0
ds_write_b64 v20,v[208:209] offset:64
ds_write_b64 v21,v[210:211] offset:64
ds_write_b64 v22,v[212:213] offset:64
ds_write_b64 v23,v[214:215] offset:64
ds_write_b64 v20,v[216:217] offset:128
ds_write_b64 v21,v[218:219] offset:128
ds_write_b64 v22,v[220:221] offset:128
ds_write_b64 v23,v[222:223] offset:128
ds_read_b128 v[72:75],v19 offset:0
ds_read_b128 v[76:79],v19 offset:1024
ds_read_b128 v[80:83],v19 offset:2048
ds_read_b128 v[84:87],v19 offset:3456
ds_read_b128 v[88:91],v19 offset:4480
ds_read_b128 v[92:95],v19 offset:5504
v_add_u32 v20,s54,v20
v_add_u32 v21,s54,v21
v_add_u32 v22,s54,v22
v_add_u32 v23,s54,v23
s_sub_u32 s68,s68,s54
s_add_u32 s59,s59,1572864
s_add_u32 s53,s53,1
s_cmp_eq_u32 s53,3
s_cselect_b32 s59,0x70000000,s59
s_add_u32 s67,s69,98304
s_waitcnt lgkmcnt(5)
buffer_store_dwordx4 v[72:75],v24,s[40:43],s69 offen nt
s_waitcnt lgkmcnt(4)
buffer_store_dwordx4 v[76:79],v25,s[40:43],s69 offen nt
s_waitcnt lgkmcnt(3)
buffer_store_dwordx4 v[80:83],v26,s[40:43],s69 offen nt
s_waitcnt lgkmcnt(2)
buffer_store_dwordx4 v[84:87],v24,s[40:43],s67 offen nt
s_waitcnt lgkmcnt(1)
buffer_store_dwordx4 v[88:91],v25,s[40:43],s67 offen nt
s_waitcnt lgkmcnt(0)
buffer_store_dwordx4 v[92:95],v26,s[40:43],s67 offen nt
v_add_u32 v19,s54,v19
s_add_u32 s69,s69,1572864
s_sub_u32 s54,0,s54
s_cmp_lt_u32 s53,4
s_cbranch_scc1 L_pass0_%=
s_branch L_end_%=
L_pass1_%=:
s_waitcnt vmcnt(6)
s_barrier
ds_read_b128 v[200:203],v4 offset:0
ds_read_b128 v[204:207],v8 offset:0
ds_read_b128 v[208:211],v12 offset:0
ds_read_b128 v[212:215],v5 offset:0
ds_read_b128 v[216:219],v9 offset:0
ds_read_b128 v[220:223],v13 offset:0
ds_read_b128 v[96:99],v18 offset:1920
ds_read_b128 v[100:103],v18 offset:1952
ds_read_b128 v[104:107],v18 offset:1984
ds_read_b128 v[108:111],v18 offset:2016
ds_read_b128 v[112:115],v18 offset:2048
ds_read_b128 v[116:119],v18 offset:2080
ds_read_b128 v[120:123],v18 offset:2112
ds_read_b128 v[124:127],v18 offset:2144
ds_read_b128 v[128:131],v18 offset:2176
ds_read_b128 v[132:135],v18 offset:2208
ds_read_b128 v[136:139],v18 offset:2240
ds_read_b128 v[140:143],v18 offset:2272
v_and_b32 v72,v32,v36
v_and_b32 v73,v33,v36
v_and_b32 v74,v34,v36
v_and_b32 v75,v35,v36
v_and_b32 v76,v32,v48
v_and_b32 v77,v33,v48
v_and_b32 v78,v34,v48
v_and_b32 v79,v35,v48
v_and_b32 v80,v32,v60
v_and_b32 v81,v33,v60
v_and_b32 v82,v34,v60
v_and_b32 v83,v35,v60
v_and_b32 v84,v32,v37
v_and_b32 v85,v33,v37
v_and_b32 v86,v34,v37
v_and_b32 v87,v35,v37
v_and_b32 v88,v32,v49
v_and_b32 v89,v33,v49
v_and_b32 v90,v34,v49
v_and_b32 v91,v35,v49
v_and_b32 v92,v32,v61
v_and_b32 v93,v33,v61
v_and_b32 v94,v34,v61
v_and_b32 v95,v35,v61
s_setprio 1
s_waitcnt lgkmcnt(15)
v_mfma_f32_16x16x32_bf16 v[144:147],v[72:75],v[200:203],0
v_mfma_f32_16x16x32_bf16 v[144:147],v[76:79],v[204:207],v[144:147]
v_mfma_f32_16x16x32_bf16 v[144:147],v[80:83],v[208:211],v[144:147]
ds_read_b128 v[200:203],v6 offset:0
ds_read_b128 v[204:207],v10 offset:0
ds_read_b128 v[208:211],v14 offset:0
s_setprio 0
v_and_b32 v72,v32,v38
v_and_b32 v73,v33,v38
v_and_b32 v74,v34,v38
v_and_b32 v75,v35,v38
v_and_b32 v76,v32,v50
v_and_b32 v77,v33,v50
v_and_b32 v78,v34,v50
v_and_b32 v79,v35,v50
v_and_b32 v80,v32,v62
v_and_b32 v81,v33,v62
v_and_b32 v82,v34,v62
v_and_b32 v83,v35,v62
v_mul_f32 v237,v144,v144
v_mul_f32 v236,v145,v145
v_mul_f32 v239,v146,v146
v_mul_f32 v238,v147,v147
s_setprio 1
s_waitcnt lgkmcnt(15)
v_mfma_f32_16x16x32_bf16 v[148:151],v[84:87],v[212:215],0
v_mfma_f32_16x16x32_bf16 v[148:151],v[88:91],v[216:219],v[148:151]
v_mfma_f32_16x16x32_bf16 v[148:151],v[92:95],v[220:223],v[148:151]
ds_read_b128 v[212:215],v7 offset:0
ds_read_b128 v[216:219],v11 offset:0
ds_read_b128 v[220:223],v15 offset:0
s_setprio 0
v_and_b32 v84,v32,v39
v_and_b32 v85,v33,v39
v_and_b32 v86,v34,v39
v_and_b32 v87,v35,v39
v_and_b32 v88,v32,v51
v_and_b32 v89,v33,v51
v_and_b32 v90,v34,v51
v_and_b32 v91,v35,v51
v_and_b32 v92,v32,v63
v_and_b32 v93,v33,v63
v_and_b32 v94,v34,v63
v_and_b32 v95,v35,v63
v_add_f32 v232,v144,v148
v_fmac_f32 v237,v148,v148
v_add_f32 v233,v145,v149
v_fmac_f32 v236,v149,v149
v_add_f32 v234,v146,v150
v_fmac_f32 v239,v150,v150
v_add_f32 v235,v147,v151
v_fmac_f32 v238,v151,v151
s_setprio 1
s_waitcnt lgkmcnt(3)
v_mfma_f32_16x16x32_bf16 v[152:155],v[72:75],v[200:203],0
v_mfma_f32_16x16x32_bf16 v[152:155],v[76:79],v[204:207],v[152:155]
v_mfma_f32_16x16x32_bf16 v[152:155],v[80:83],v[208:211],v[152:155]
ds_read_b128 v[200:203],v4 offset:64
ds_read_b128 v[204:207],v8 offset:64
ds_read_b128 v[208:211],v12 offset:64
s_setprio 0
v_and_b32 v72,v32,v40
v_and_b32 v73,v33,v40
v_and_b32 v74,v34,v40
v_and_b32 v75,v35,v40
v_and_b32 v76,v32,v52
v_and_b32 v77,v33,v52
v_and_b32 v78,v34,v52
v_and_b32 v79,v35,v52
v_and_b32 v80,v32,v64
v_and_b32 v81,v33,v64
v_and_b32 v82,v34,v64
v_and_b32 v83,v35,v64
v_add_f32 v232,v232,v152
v_fmac_f32 v237,v152,v152
v_add_f32 v233,v233,v153
v_fmac_f32 v236,v153,v153
v_add_f32 v234,v234,v154
v_fmac_f32 v239,v154,v154
v_add_f32 v235,v235,v155
v_fmac_f32 v238,v155,v155
s_setprio 1
s_waitcnt lgkmcnt(3)
v_mfma_f32_16x16x32_bf16 v[156:159],v[84:87],v[212:215],0
v_mfma_f32_16x16x32_bf16 v[156:159],v[88:91],v[216:219],v[156:159]
v_mfma_f32_16x16x32_bf16 v[156:159],v[92:95],v[220:223],v[156:159]
ds_read_b128 v[212:215],v5 offset:64
ds_read_b128 v[216:219],v9 offset:64
ds_read_b128 v[220:223],v13 offset:64
s_setprio 0
v_and_b32 v84,v32,v41
v_and_b32 v85,v33,v41
v_and_b32 v86,v34,v41
v_and_b32 v87,v35,v41
v_and_b32 v88,v32,v53
v_and_b32 v89,v33,v53
v_and_b32 v90,v34,v53
v_and_b32 v91,v35,v53
v_and_b32 v92,v32,v65
v_and_b32 v93,v33,v65
v_and_b32 v94,v34,v65
v_and_b32 v95,v35,v65
v_add_f32 v232,v232,v156
v_fmac_f32 v237,v156,v156
v_add_f32 v233,v233,v157
v_fmac_f32 v236,v157,v157
v_add_f32 v234,v234,v158
v_fmac_f32 v239,v158,v158
v_add_f32 v235,v235,v159
v_fmac_f32 v238,v159,v159
s_setprio 1
s_waitcnt lgkmcnt(3)
v_mfma_f32_16x16x32_bf16 v[160:163],v[72:75],v[200:203],0
v_mfma_f32_16x16x32_bf16 v[160:163],v[76:79],v[204:207],v[160:163]
v_mfma_f32_16x16x32_bf16 v[160:163],v[80:83],v[208:211],v[160:163]
ds_read_b128 v[200:203],v6 offset:64
ds_read_b128 v[204:207],v10 offset:64
ds_read_b128 v[208:211],v14 offset:64
s_setprio 0
v_and_b32 v72,v32,v42
v_and_b32 v73,v33,v42
v_and_b32 v74,v34,v42
v_and_b32 v75,v35,v42
v_and_b32 v76,v32,v54
v_and_b32 v77,v33,v54
v_and_b32 v78,v34,v54
v_and_b32 v79,v35,v54
v_and_b32 v80,v32,v66
v_and_b32 v81,v33,v66
v_and_b32 v82,v34,v66
v_and_b32 v83,v35,v66
v_add_f32 v232,v232,v160
v_fmac_f32 v237,v160,v160
v_add_f32 v233,v233,v161
v_fmac_f32 v236,v161,v161
v_add_f32 v234,v234,v162
v_fmac_f32 v239,v162,v162
v_add_f32 v235,v235,v163
v_fmac_f32 v238,v163,v163
s_setprio 1
s_waitcnt lgkmcnt(3)
v_mfma_f32_16x16x32_bf16 v[164:167],v[84:87],v[212:215],0
v_mfma_f32_16x16x32_bf16 v[164:167],v[88:91],v[216:219],v[164:167]
v_mfma_f32_16x16x32_bf16 v[164:167],v[92:95],v[220:223],v[164:167]
ds_read_b128 v[212:215],v7 offset:64
ds_read_b128 v[216:219],v11 offset:64
ds_read_b128 v[220:223],v15 offset:64
s_setprio 0
v_and_b32 v84,v32,v43
v_and_b32 v85,v33,v43
v_and_b32 v86,v34,v43
v_and_b32 v87,v35,v43
v_and_b32 v88,v32,v55
v_and_b32 v89,v33,v55
v_and_b32 v90,v34,v55
v_and_b32 v91,v35,v55
v_and_b32 v92,v32,v67
v_and_b32 v93,v33,v67
v_and_b32 v94,v34,v67
v_and_b32 v95,v35,v67
v_add_f32 v232,v232,v164
v_fmac_f32 v237,v164,v164
v_add_f32 v233,v233,v165
v_fmac_f32 v236,v165,v165
v_add_f32 v234,v234,v166
v_fmac_f32 v239,v166,v166
v_add_f32 v235,v235,v167
v_fmac_f32 v238,v167,v167
s_setprio 1
s_waitcnt lgkmcnt(3)
v_mfma_f32_16x16x32_bf16 v[168:171],v[72:75],v[200:203],0
v_mfma_f32_16x16x32_bf16 v[168:171],v[76:79],v[204:207],v[168:171]
v_mfma_f32_16x16x32_bf16 v[168:171],v[80:83],v[208:211],v[168:171]
ds_read_b128 v[200:203],v4 offset:128
ds_read_b128 v[204:207],v8 offset:128
ds_read_b128 v[208:211],v12 offset:128
s_setprio 0
v_and_b32 v72,v32,v44
v_and_b32 v73,v33,v44
v_and_b32 v74,v34,v44
v_and_b32 v75,v35,v44
v_and_b32 v76,v32,v56
v_and_b32 v77,v33,v56
v_and_b32 v78,v34,v56
v_and_b32 v79,v35,v56
v_and_b32 v80,v32,v68
v_and_b32 v81,v33,v68
v_and_b32 v82,v34,v68
v_and_b32 v83,v35,v68
v_add_f32 v232,v232,v168
v_fmac_f32 v237,v168,v168
v_add_f32 v233,v233,v169
v_fmac_f32 v236,v169,v169
v_add_f32 v234,v234,v170
v_fmac_f32 v239,v170,v170
v_add_f32 v235,v235,v171
v_fmac_f32 v238,v171,v171
s_setprio 1
s_waitcnt lgkmcnt(3)
v_mfma_f32_16x16x32_bf16 v[172:175],v[84:87],v[212:215],0
v_mfma_f32_16x16x32_bf16 v[172:175],v[88:91],v[216:219],v[172:175]
v_mfma_f32_16x16x32_bf16 v[172:175],v[92:95],v[220:223],v[172:175]
ds_read_b128 v[212:215],v5 offset:128
ds_read_b128 v[216:219],v9 offset:128
ds_read_b128 v[220:223],v13 offset:128
s_setprio 0
v_and_b32 v84,v32,v45
v_and_b32 v85,v33,v45
v_and_b32 v86,v34,v45
v_and_b32 v87,v35,v45
v_and_b32 v88,v32,v57
v_and_b32 v89,v33,v57
v_and_b32 v90,v34,v57
v_and_b32 v91,v35,v57
v_and_b32 v92,v32,v69
v_and_b32 v93,v33,v69
v_and_b32 v94,v34,v69
v_and_b32 v95,v35,v69
v_add_f32 v232,v232,v172
v_fmac_f32 v237,v172,v172
v_add_f32 v233,v233,v173
v_fmac_f32 v236,v173,v173
v_add_f32 v234,v234,v174
v_fmac_f32 v239,v174,v174
v_add_f32 v235,v235,v175
v_fmac_f32 v238,v175,v175
s_setprio 1
s_waitcnt lgkmcnt(3)
v_mfma_f32_16x16x32_bf16 v[176:179],v[72:75],v[200:203],0
v_mfma_f32_16x16x32_bf16 v[176:179],v[76:79],v[204:207],v[176:179]
v_mfma_f32_16x16x32_bf16 v[176:179],v[80:83],v[208:211],v[176:179]
ds_read_b128 v[200:203],v6 offset:128
ds_read_b128 v[204:207],v10 offset:128
ds_read_b128 v[208:211],v14 offset:128
s_setprio 0
v_and_b32 v72,v32,v46
v_and_b32 v73,v33,v46
v_and_b32 v74,v34,v46
v_and_b32 v75,v35,v46
v_and_b32 v76,v32,v58
v_and_b32 v77,v33,v58
v_and_b32 v78,v34,v58
v_and_b32 v79,v35,v58
v_and_b32 v80,v32,v70
v_and_b32 v81,v33,v70
v_and_b32 v82,v34,v70
v_and_b32 v83,v35,v70
v_add_f32 v232,v232,v176
v_fmac_f32 v237,v176,v176
v_add_f32 v233,v233,v177
v_fmac_f32 v236,v177,v177
v_add_f32 v234,v234,v178
v_fmac_f32 v239,v178,v178
v_add_f32 v235,v235,v179
v_fmac_f32 v238,v179,v179
s_setprio 1
s_waitcnt lgkmcnt(3)
v_mfma_f32_16x16x32_bf16 v[180:183],v[84:87],v[212:215],0
v_mfma_f32_16x16x32_bf16 v[180:183],v[88:91],v[216:219],v[180:183]
v_mfma_f32_16x16x32_bf16 v[180:183],v[92:95],v[220:223],v[180:183]
ds_read_b128 v[212:215],v7 offset:128
ds_read_b128 v[216:219],v11 offset:128
ds_read_b128 v[220:223],v15 offset:128
s_setprio 0
v_and_b32 v84,v32,v47
v_and_b32 v85,v33,v47
v_and_b32 v86,v34,v47
v_and_b32 v87,v35,v47
v_and_b32 v88,v32,v59
v_and_b32 v89,v33,v59
v_and_b32 v90,v34,v59
v_and_b32 v91,v35,v59
v_and_b32 v92,v32,v71
v_and_b32 v93,v33,v71
v_and_b32 v94,v34,v71
v_and_b32 v95,v35,v71
v_add_f32 v232,v232,v180
v_fmac_f32 v237,v180,v180
v_add_f32 v233,v233,v181
v_fmac_f32 v236,v181,v181
v_add_f32 v234,v234,v182
v_fmac_f32 v239,v182,v182
v_add_f32 v235,v235,v183
v_fmac_f32 v238,v183,v183
s_setprio 1
s_waitcnt lgkmcnt(3)
v_mfma_f32_16x16x32_bf16 v[184:187],v[72:75],v[200:203],0
v_mfma_f32_16x16x32_bf16 v[184:187],v[76:79],v[204:207],v[184:187]
v_mfma_f32_16x16x32_bf16 v[184:187],v[80:83],v[208:211],v[184:187]
s_setprio 0
s_nop 7
v_add_f32 v232,v232,v184
v_fmac_f32 v237,v184,v184
v_add_f32 v233,v233,v185
v_fmac_f32 v236,v185,v185
v_add_f32 v234,v234,v186
v_fmac_f32 v239,v186,v186
v_add_f32 v235,v235,v187
v_fmac_f32 v238,v187,v187
s_setprio 1
s_waitcnt lgkmcnt(0)
v_mfma_f32_16x16x32_bf16 v[188:191],v[84:87],v[212:215],0
v_mfma_f32_16x16x32_bf16 v[188:191],v[88:91],v[216:219],v[188:191]
v_mfma_f32_16x16x32_bf16 v[188:191],v[92:95],v[220:223],v[188:191]
s_setprio 0
ds_read_b64_tr_b16 v[200:201],v16 offset:0
ds_read_b64_tr_b16 v[202:203],v16 offset:512
ds_read_b64_tr_b16 v[204:205],v16 offset:1024
ds_read_b64_tr_b16 v[206:207],v16 offset:1536
ds_read_b64_tr_b16 v[208:209],v16 offset:2048
ds_read_b64_tr_b16 v[210:211],v16 offset:2560
ds_read_b64_tr_b16 v[212:213],v16 offset:3072
ds_read_b64_tr_b16 v[214:215],v16 offset:3584
ds_read_b64_tr_b16 v[216:217],v16 offset:4096
ds_read_b64_tr_b16 v[218:219],v16 offset:4608
ds_read_b64_tr_b16 v[220:221],v16 offset:5120
ds_read_b64_tr_b16 v[222:223],v16 offset:5632
v_add_f32 v232,v232,v188
v_fmac_f32 v237,v188,v188
v_add_f32 v233,v233,v189
v_fmac_f32 v236,v189,v189
v_add_f32 v234,v234,v190
v_fmac_f32 v239,v190,v190
v_add_f32 v235,v235,v191
v_fmac_f32 v238,v191,v191
v_add_f32 v232,v232,v233
v_add_f32 v234,v234,v235
v_add_f32 v237,v237,v236
v_add_f32 v239,v239,v238
v_add_f32 v240,v232,v234
v_add_f32 v241,v237,v239
s_nop 1
v_permlane32_swap_b32 v240,v241
v_add_f32 v240,v240,v241
v_mov_b32 v241,v240
s_nop 1
v_permlane32_swap_b32 v240,v241
v_mul_f32 v240,0x3c2aaaab,v240
v_mul_f32 v241,0x3c2aaaab,v241
v_fma_f32 v241,-v240,v240,v241
v_max_f32 v241,0,v241
v_add_f32 v241,0x3727c5ac,v241
v_rsq_f32 v241,v241
s_nop 0
v_mul_f32 v242,0xbeeff558,v241
v_mul_f32 v243,0x3eeff558,v241
v_mul_f32 v243,v240,v243
v_fma_f32 v144,v144,v242,v243
v_fma_f32 v145,v145,v242,v243
v_fma_f32 v146,v146,v242,v243
v_fma_f32 v147,v147,v242,v243
v_fma_f32 v148,v148,v242,v243
v_fma_f32 v149,v149,v242,v243
v_fma_f32 v150,v150,v242,v243
v_fma_f32 v151,v151,v242,v243
v_fma_f32 v152,v152,v242,v243
v_fma_f32 v153,v153,v242,v243
v_fma_f32 v154,v154,v242,v243
v_fma_f32 v155,v155,v242,v243
v_fma_f32 v156,v156,v242,v243
v_fma_f32 v157,v157,v242,v243
v_fma_f32 v158,v158,v242,v243
v_fma_f32 v159,v159,v242,v243
v_fma_f32 v160,v160,v242,v243
v_fma_f32 v161,v161,v242,v243
v_fma_f32 v162,v162,v242,v243
v_fma_f32 v163,v163,v242,v243
v_fma_f32 v164,v164,v242,v243
v_fma_f32 v165,v165,v242,v243
v_fma_f32 v166,v166,v242,v243
v_fma_f32 v167,v167,v242,v243
v_fma_f32 v168,v168,v242,v243
v_fma_f32 v169,v169,v242,v243
v_fma_f32 v170,v170,v242,v243
v_fma_f32 v171,v171,v242,v243
v_fma_f32 v172,v172,v242,v243
v_fma_f32 v173,v173,v242,v243
v_fma_f32 v174,v174,v242,v243
v_fma_f32 v175,v175,v242,v243
v_fma_f32 v176,v176,v242,v243
v_fma_f32 v177,v177,v242,v243
v_fma_f32 v178,v178,v242,v243
v_fma_f32 v179,v179,v242,v243
v_fma_f32 v180,v180,v242,v243
v_fma_f32 v181,v181,v242,v243
v_fma_f32 v182,v182,v242,v243
v_fma_f32 v183,v183,v242,v243
v_fma_f32 v184,v184,v242,v243
v_fma_f32 v185,v185,v242,v243
v_fma_f32 v186,v186,v242,v243
v_fma_f32 v187,v187,v242,v243
v_fma_f32 v188,v188,v242,v243
v_fma_f32 v189,v189,v242,v243
v_fma_f32 v190,v190,v242,v243
v_fma_f32 v191,v191,v242,v243
s_setprio 1
v_cvt_pk_bf16_f32 v72,v144,v145
v_cvt_pk_bf16_f32 v73,v146,v147
v_cvt_pk_bf16_f32 v74,v148,v149
v_cvt_pk_bf16_f32 v75,v150,v151
v_cvt_pk_bf16_f32 v76,v152,v153
v_cvt_pk_bf16_f32 v77,v154,v155
v_cvt_pk_bf16_f32 v78,v156,v157
v_cvt_pk_bf16_f32 v79,v158,v159
ds_read_b128 v[144:147],v18 offset:0
ds_read_b128 v[148:151],v18 offset:32
ds_read_b128 v[152:155],v18 offset:64
ds_read_b128 v[156:159],v18 offset:96
v_cvt_pk_bf16_f32 v80,v160,v161
v_cvt_pk_bf16_f32 v81,v162,v163
v_cvt_pk_bf16_f32 v82,v164,v165
v_cvt_pk_bf16_f32 v83,v166,v167
v_cvt_pk_bf16_f32 v84,v168,v169
v_cvt_pk_bf16_f32 v85,v170,v171
v_cvt_pk_bf16_f32 v86,v172,v173
v_cvt_pk_bf16_f32 v87,v174,v175
s_waitcnt lgkmcnt(0)
v_mfma_f32_32x32x16_bf16 v[144:159],v[200:203],v[72:75],v[144:159]
v_cvt_pk_bf16_f32 v88,v176,v177
v_cvt_pk_bf16_f32 v89,v178,v179
v_cvt_pk_bf16_f32 v90,v180,v181
v_cvt_pk_bf16_f32 v91,v182,v183
v_mfma_f32_32x32x16_bf16 v[144:159],v[204:207],v[76:79],v[144:159]
v_cvt_pk_bf16_f32 v92,v184,v185
v_cvt_pk_bf16_f32 v93,v186,v187
v_cvt_pk_bf16_f32 v94,v188,v189
v_cvt_pk_bf16_f32 v95,v190,v191
v_mfma_f32_32x32x16_bf16 v[144:159],v[208:211],v[80:83],v[144:159]
v_mfma_f32_32x32x16_bf16 v[144:159],v[212:215],v[84:87],v[144:159]
v_mfma_f32_32x32x16_bf16 v[144:159],v[216:219],v[88:91],v[144:159]
v_mfma_f32_32x32x16_bf16 v[144:159],v[220:223],v[92:95],v[144:159]
s_setprio 0
s_nop 7
s_nop 1
s_waitcnt vmcnt(0)
s_barrier
v_fmaak_f32 v176,v144,v144,0x409d30fc
v_fmaak_f32 v177,v145,v145,0x409d30fc
v_fmaak_f32 v178,v146,v146,0x409d30fc
v_fmaak_f32 v179,v147,v147,0x409d30fc
v_fmaak_f32 v180,v148,v148,0x409d30fc
v_fmaak_f32 v181,v149,v149,0x409d30fc
v_fmaak_f32 v182,v150,v150,0x409d30fc
v_fmaak_f32 v183,v151,v151,0x409d30fc
v_fmaak_f32 v184,v152,v152,0x409d30fc
v_fmaak_f32 v185,v153,v153,0x409d30fc
v_fmaak_f32 v186,v154,v154,0x409d30fc
v_fmaak_f32 v187,v155,v155,0x409d30fc
v_fmaak_f32 v188,v156,v156,0x409d30fc
v_fmaak_f32 v189,v157,v157,0x409d30fc
v_fmaak_f32 v190,v158,v158,0x409d30fc
v_fmaak_f32 v191,v159,v159,0x409d30fc
v_mul_f32 v176,v144,v176
v_mul_f32 v177,v145,v177
v_mul_f32 v178,v146,v178
v_mul_f32 v179,v147,v179
v_mul_f32 v180,v148,v180
v_mul_f32 v181,v149,v181
v_mul_f32 v182,v150,v182
v_mul_f32 v183,v151,v183
v_mul_f32 v184,v152,v184
v_mul_f32 v185,v153,v185
v_mul_f32 v186,v154,v186
v_mul_f32 v187,v155,v187
v_mul_f32 v188,v156,v188
v_mul_f32 v189,v157,v189
v_mul_f32 v190,v158,v190
v_mul_f32 v191,v159,v191
s_setprio 1
v_exp_f32 v176,v176
v_exp_f32 v177,v177
v_exp_f32 v178,v178
v_exp_f32 v179,v179
v_exp_f32 v180,v180
v_exp_f32 v181,v181
v_exp_f32 v182,v182
v_exp_f32 v183,v183
v_exp_f32 v184,v184
v_exp_f32 v185,v185
v_exp_f32 v186,v186
v_exp_f32 v187,v187
v_exp_f32 v188,v188
v_exp_f32 v189,v189
v_exp_f32 v190,v190
v_exp_f32 v191,v191
ds_read_b64_tr_b16 v[200:201],v16 offset:6144
ds_read_b64_tr_b16 v[202:203],v16 offset:6656
ds_read_b64_tr_b16 v[204:205],v16 offset:7168
ds_read_b64_tr_b16 v[206:207],v16 offset:7680
ds_read_b64_tr_b16 v[208:209],v16 offset:8192
ds_read_b64_tr_b16 v[210:211],v16 offset:8704
ds_read_b64_tr_b16 v[212:213],v16 offset:9216
ds_read_b64_tr_b16 v[214:215],v16 offset:9728
ds_read_b64_tr_b16 v[216:217],v16 offset:10240
ds_read_b64_tr_b16 v[218:219],v16 offset:10752
ds_read_b64_tr_b16 v[220:221],v16 offset:11264
ds_read_b64_tr_b16 v[222:223],v16 offset:11776
ds_read_b128 v[160:163],v18 offset:128
ds_read_b128 v[164:167],v18 offset:160
ds_read_b128 v[168:171],v18 offset:192
ds_read_b128 v[172:175],v18 offset:224
s_mov_b64 exec,s[62:63]
s_add_u32 m0,s68,0
s_add_u32 s64,s59,0
buffer_load_dwordx4 v27,s[36:39],s64 offen lds
s_mov_b64 exec,-1
s_setprio 0
v_add_f32 v176,1.0,v176
v_add_f32 v177,1.0,v177
v_add_f32 v178,1.0,v178
v_add_f32 v179,1.0,v179
v_add_f32 v180,1.0,v180
v_add_f32 v181,1.0,v181
v_add_f32 v182,1.0,v182
v_add_f32 v183,1.0,v183
v_add_f32 v184,1.0,v184
v_add_f32 v185,1.0,v185
v_add_f32 v186,1.0,v186
v_add_f32 v187,1.0,v187
v_add_f32 v188,1.0,v188
v_add_f32 v189,1.0,v189
v_add_f32 v190,1.0,v190
v_add_f32 v191,1.0,v191
s_setprio 1
s_waitcnt lgkmcnt(0)
v_mfma_f32_32x32x16_bf16 v[160:175],v[200:203],v[72:75],v[160:175]
v_rcp_f32 v176,v176
v_rcp_f32 v177,v177
v_rcp_f32 v178,v178
v_mfma_f32_32x32x16_bf16 v[160:175],v[204:207],v[76:79],v[160:175]
v_rcp_f32 v179,v179
v_rcp_f32 v180,v180
v_rcp_f32 v181,v181
v_mfma_f32_32x32x16_bf16 v[160:175],v[208:211],v[80:83],v[160:175]
v_rcp_f32 v182,v182
v_rcp_f32 v183,v183
v_rcp_f32 v184,v184
v_mfma_f32_32x32x16_bf16 v[160:175],v[212:215],v[84:87],v[160:175]
v_rcp_f32 v185,v185
v_rcp_f32 v186,v186
v_rcp_f32 v187,v187
v_mfma_f32_32x32x16_bf16 v[160:175],v[216:219],v[88:91],v[160:175]
v_rcp_f32 v188,v188
v_rcp_f32 v189,v189
v_mfma_f32_32x32x16_bf16 v[160:175],v[220:223],v[92:95],v[160:175]
v_rcp_f32 v190,v190
v_rcp_f32 v191,v191
ds_read_b64_tr_b16 v[200:201],v17 offset:0
ds_read_b64_tr_b16 v[202:203],v17 offset:1536
ds_read_b64_tr_b16 v[204:205],v17 offset:3072
ds_read_b64_tr_b16 v[206:207],v17 offset:4608
ds_read_b64_tr_b16 v[208:209],v17 offset:64
ds_read_b64_tr_b16 v[210:211],v17 offset:1600
ds_read_b64_tr_b16 v[212:213],v17 offset:3136
ds_read_b64_tr_b16 v[214:215],v17 offset:4672
ds_read_b64_tr_b16 v[216:217],v17 offset:128
ds_read_b64_tr_b16 v[218:219],v17 offset:1664
ds_read_b64_tr_b16 v[220:221],v17 offset:3200
ds_read_b64_tr_b16 v[222:223],v17 offset:4736
s_mov_b64 exec,s[62:63]
s_add_u32 m0,s68,3456
s_add_u32 s64,s59,98304
buffer_load_dwordx4 v27,s[36:39],s64 offen lds
s_mov_b64 exec,-1
s_setprio 0
v_mul_f32 v176,v144,v176
v_mul_f32 v177,v145,v177
v_mul_f32 v178,v146,v178
v_mul_f32 v179,v147,v179
v_mul_f32 v180,v148,v180
v_mul_f32 v181,v149,v181
v_mul_f32 v182,v150,v182
v_mul_f32 v183,v151,v183
v_mul_f32 v184,v152,v184
v_mul_f32 v185,v153,v185
v_mul_f32 v186,v154,v186
v_mul_f32 v187,v155,v187
v_mul_f32 v188,v156,v188
v_mul_f32 v189,v157,v189
v_mul_f32 v190,v158,v190
v_mul_f32 v191,v159,v191
v_cvt_pk_bf16_f32 v192,v176,v177
v_cvt_pk_bf16_f32 v193,v178,v179
v_cvt_pk_bf16_f32 v194,v180,v181
v_cvt_pk_bf16_f32 v195,v182,v183
v_cvt_pk_bf16_f32 v196,v184,v185
v_cvt_pk_bf16_f32 v197,v186,v187
v_cvt_pk_bf16_f32 v198,v188,v189
v_cvt_pk_bf16_f32 v199,v190,v191
s_waitcnt lgkmcnt(0)
s_barrier
v_fmaak_f32 v176,v160,v160,0x409d30fc
v_fmaak_f32 v177,v161,v161,0x409d30fc
v_fmaak_f32 v178,v162,v162,0x409d30fc
v_fmaak_f32 v179,v163,v163,0x409d30fc
v_fmaak_f32 v180,v164,v164,0x409d30fc
v_fmaak_f32 v181,v165,v165,0x409d30fc
v_fmaak_f32 v182,v166,v166,0x409d30fc
v_fmaak_f32 v183,v167,v167,0x409d30fc
v_fmaak_f32 v184,v168,v168,0x409d30fc
v_fmaak_f32 v185,v169,v169,0x409d30fc
v_fmaak_f32 v186,v170,v170,0x409d30fc
v_fmaak_f32 v187,v171,v171,0x409d30fc
v_fmaak_f32 v188,v172,v172,0x409d30fc
v_fmaak_f32 v189,v173,v173,0x409d30fc
v_fmaak_f32 v190,v174,v174,0x409d30fc
v_fmaak_f32 v191,v175,v175,0x409d30fc
v_mul_f32 v176,v160,v176
v_mul_f32 v177,v161,v177
v_mul_f32 v178,v162,v178
v_mul_f32 v179,v163,v179
v_mul_f32 v180,v164,v180
v_mul_f32 v181,v165,v181
v_mul_f32 v182,v166,v182
v_mul_f32 v183,v167,v183
v_mul_f32 v184,v168,v184
v_mul_f32 v185,v169,v185
v_mul_f32 v186,v170,v186
v_mul_f32 v187,v171,v187
v_mul_f32 v188,v172,v188
v_mul_f32 v189,v173,v189
v_mul_f32 v190,v174,v190
v_mul_f32 v191,v175,v191
s_setprio 1
v_mfma_f32_32x32x16_bf16 v[96:111],v[200:203],v[192:195],v[96:111]
v_exp_f32 v176,v176
v_exp_f32 v177,v177
v_exp_f32 v178,v178
v_mfma_f32_32x32x16_bf16 v[96:111],v[204:207],v[196:199],v[96:111]
v_exp_f32 v179,v179
v_exp_f32 v180,v180
v_exp_f32 v181,v181
v_mfma_f32_32x32x16_bf16 v[112:127],v[208:211],v[192:195],v[112:127]
v_exp_f32 v182,v182
v_exp_f32 v183,v183
v_exp_f32 v184,v184
v_mfma_f32_32x32x16_bf16 v[112:127],v[212:215],v[196:199],v[112:127]
v_exp_f32 v185,v185
v_exp_f32 v186,v186
v_exp_f32 v187,v187
v_mfma_f32_32x32x16_bf16 v[128:143],v[216:219],v[192:195],v[128:143]
v_exp_f32 v188,v188
v_exp_f32 v189,v189
v_mfma_f32_32x32x16_bf16 v[128:143],v[220:223],v[196:199],v[128:143]
v_exp_f32 v190,v190
v_exp_f32 v191,v191
ds_read_b64_tr_b16 v[200:201],v16 offset:12288
ds_read_b64_tr_b16 v[202:203],v16 offset:12800
ds_read_b64_tr_b16 v[204:205],v16 offset:13312
ds_read_b64_tr_b16 v[206:207],v16 offset:13824
ds_read_b64_tr_b16 v[208:209],v16 offset:14336
ds_read_b64_tr_b16 v[210:211],v16 offset:14848
ds_read_b64_tr_b16 v[212:213],v16 offset:15360
ds_read_b64_tr_b16 v[214:215],v16 offset:15872
ds_read_b64_tr_b16 v[216:217],v16 offset:16384
ds_read_b64_tr_b16 v[218:219],v16 offset:16896
ds_read_b64_tr_b16 v[220:221],v16 offset:17408
ds_read_b64_tr_b16 v[222:223],v16 offset:17920
ds_read_b128 v[144:147],v18 offset:256
ds_read_b128 v[148:151],v18 offset:288
ds_read_b128 v[152:155],v18 offset:320
ds_read_b128 v[156:159],v18 offset:352
s_mov_b64 exec,s[62:63]
s_add_u32 m0,s68,6912
s_add_u32 s64,s59,196608
buffer_load_dwordx4 v27,s[36:39],s64 offen lds
s_mov_b64 exec,-1
s_setprio 0
v_add_f32 v176,1.0,v176
v_add_f32 v177,1.0,v177
v_add_f32 v178,1.0,v178
v_add_f32 v179,1.0,v179
v_add_f32 v180,1.0,v180
v_add_f32 v181,1.0,v181
v_add_f32 v182,1.0,v182
v_add_f32 v183,1.0,v183
v_add_f32 v184,1.0,v184
v_add_f32 v185,1.0,v185
v_add_f32 v186,1.0,v186
v_add_f32 v187,1.0,v187
v_add_f32 v188,1.0,v188
v_add_f32 v189,1.0,v189
v_add_f32 v190,1.0,v190
v_add_f32 v191,1.0,v191
s_setprio 1
s_waitcnt lgkmcnt(0)
v_mfma_f32_32x32x16_bf16 v[144:159],v[200:203],v[72:75],v[144:159]
v_rcp_f32 v176,v176
v_rcp_f32 v177,v177
v_rcp_f32 v178,v178
v_mfma_f32_32x32x16_bf16 v[144:159],v[204:207],v[76:79],v[144:159]
v_rcp_f32 v179,v179
v_rcp_f32 v180,v180
v_rcp_f32 v181,v181
v_mfma_f32_32x32x16_bf16 v[144:159],v[208:211],v[80:83],v[144:159]
v_rcp_f32 v182,v182
v_rcp_f32 v183,v183
v_rcp_f32 v184,v184
v_mfma_f32_32x32x16_bf16 v[144:159],v[212:215],v[84:87],v[144:159]
v_rcp_f32 v185,v185
v_rcp_f32 v186,v186
v_rcp_f32 v187,v187
v_mfma_f32_32x32x16_bf16 v[144:159],v[216:219],v[88:91],v[144:159]
v_rcp_f32 v188,v188
v_rcp_f32 v189,v189
v_mfma_f32_32x32x16_bf16 v[144:159],v[220:223],v[92:95],v[144:159]
v_rcp_f32 v190,v190
v_rcp_f32 v191,v191
ds_read_b64_tr_b16 v[200:201],v17 offset:6144
ds_read_b64_tr_b16 v[202:203],v17 offset:7680
ds_read_b64_tr_b16 v[204:205],v17 offset:9216
ds_read_b64_tr_b16 v[206:207],v17 offset:10752
ds_read_b64_tr_b16 v[208:209],v17 offset:6208
ds_read_b64_tr_b16 v[210:211],v17 offset:7744
ds_read_b64_tr_b16 v[212:213],v17 offset:9280
ds_read_b64_tr_b16 v[214:215],v17 offset:10816
ds_read_b64_tr_b16 v[216:217],v17 offset:6272
ds_read_b64_tr_b16 v[218:219],v17 offset:7808
ds_read_b64_tr_b16 v[220:221],v17 offset:9344
ds_read_b64_tr_b16 v[222:223],v17 offset:10880
s_mov_b64 exec,s[62:63]
s_add_u32 m0,s68,10368
s_add_u32 s64,s59,294912
buffer_load_dwordx4 v27,s[36:39],s64 offen lds
s_mov_b64 exec,-1
s_setprio 0
v_mul_f32 v176,v160,v176
v_mul_f32 v177,v161,v177
v_mul_f32 v178,v162,v178
v_mul_f32 v179,v163,v179
v_mul_f32 v180,v164,v180
v_mul_f32 v181,v165,v181
v_mul_f32 v182,v166,v182
v_mul_f32 v183,v167,v183
v_mul_f32 v184,v168,v184
v_mul_f32 v185,v169,v185
v_mul_f32 v186,v170,v186
v_mul_f32 v187,v171,v187
v_mul_f32 v188,v172,v188
v_mul_f32 v189,v173,v189
v_mul_f32 v190,v174,v190
v_mul_f32 v191,v175,v191
v_cvt_pk_bf16_f32 v192,v176,v177
v_cvt_pk_bf16_f32 v193,v178,v179
v_cvt_pk_bf16_f32 v194,v180,v181
v_cvt_pk_bf16_f32 v195,v182,v183
v_cvt_pk_bf16_f32 v196,v184,v185
v_cvt_pk_bf16_f32 v197,v186,v187
v_cvt_pk_bf16_f32 v198,v188,v189
v_cvt_pk_bf16_f32 v199,v190,v191
s_waitcnt lgkmcnt(0)
s_barrier
v_fmaak_f32 v176,v144,v144,0x409d30fc
v_fmaak_f32 v177,v145,v145,0x409d30fc
v_fmaak_f32 v178,v146,v146,0x409d30fc
v_fmaak_f32 v179,v147,v147,0x409d30fc
v_fmaak_f32 v180,v148,v148,0x409d30fc
v_fmaak_f32 v181,v149,v149,0x409d30fc
v_fmaak_f32 v182,v150,v150,0x409d30fc
v_fmaak_f32 v183,v151,v151,0x409d30fc
v_fmaak_f32 v184,v152,v152,0x409d30fc
v_fmaak_f32 v185,v153,v153,0x409d30fc
v_fmaak_f32 v186,v154,v154,0x409d30fc
v_fmaak_f32 v187,v155,v155,0x409d30fc
v_fmaak_f32 v188,v156,v156,0x409d30fc
v_fmaak_f32 v189,v157,v157,0x409d30fc
v_fmaak_f32 v190,v158,v158,0x409d30fc
v_fmaak_f32 v191,v159,v159,0x409d30fc
v_mul_f32 v176,v144,v176
v_mul_f32 v177,v145,v177
v_mul_f32 v178,v146,v178
v_mul_f32 v179,v147,v179
v_mul_f32 v180,v148,v180
v_mul_f32 v181,v149,v181
v_mul_f32 v182,v150,v182
v_mul_f32 v183,v151,v183
v_mul_f32 v184,v152,v184
v_mul_f32 v185,v153,v185
v_mul_f32 v186,v154,v186
v_mul_f32 v187,v155,v187
v_mul_f32 v188,v156,v188
v_mul_f32 v189,v157,v189
v_mul_f32 v190,v158,v190
v_mul_f32 v191,v159,v191
s_setprio 1
v_mfma_f32_32x32x16_bf16 v[96:111],v[200:203],v[192:195],v[96:111]
v_exp_f32 v176,v176
v_exp_f32 v177,v177
v_exp_f32 v178,v178
v_mfma_f32_32x32x16_bf16 v[96:111],v[204:207],v[196:199],v[96:111]
v_exp_f32 v179,v179
v_exp_f32 v180,v180
v_exp_f32 v181,v181
v_mfma_f32_32x32x16_bf16 v[112:127],v[208:211],v[192:195],v[112:127]
v_exp_f32 v182,v182
v_exp_f32 v183,v183
v_exp_f32 v184,v184
v_mfma_f32_32x32x16_bf16 v[112:127],v[212:215],v[196:199],v[112:127]
v_exp_f32 v185,v185
v_exp_f32 v186,v186
v_exp_f32 v187,v187
v_mfma_f32_32x32x16_bf16 v[128:143],v[216:219],v[192:195],v[128:143]
v_exp_f32 v188,v188
v_exp_f32 v189,v189
v_mfma_f32_32x32x16_bf16 v[128:143],v[220:223],v[196:199],v[128:143]
v_exp_f32 v190,v190
v_exp_f32 v191,v191
ds_read_b64_tr_b16 v[200:201],v16 offset:0
ds_read_b64_tr_b16 v[202:203],v16 offset:512
ds_read_b64_tr_b16 v[204:205],v16 offset:1024
ds_read_b64_tr_b16 v[206:207],v16 offset:1536
ds_read_b64_tr_b16 v[208:209],v16 offset:2048
ds_read_b64_tr_b16 v[210:211],v16 offset:2560
ds_read_b64_tr_b16 v[212:213],v16 offset:3072
ds_read_b64_tr_b16 v[214:215],v16 offset:3584
ds_read_b64_tr_b16 v[216:217],v16 offset:4096
ds_read_b64_tr_b16 v[218:219],v16 offset:4608
ds_read_b64_tr_b16 v[220:221],v16 offset:5120
ds_read_b64_tr_b16 v[222:223],v16 offset:5632
ds_read_b128 v[160:163],v18 offset:384
ds_read_b128 v[164:167],v18 offset:416
ds_read_b128 v[168:171],v18 offset:448
ds_read_b128 v[172:175],v18 offset:480
s_mov_b64 exec,s[62:63]
s_add_u32 m0,s68,13824
s_add_u32 s64,s59,393216
buffer_load_dwordx4 v27,s[36:39],s64 offen lds
s_mov_b64 exec,-1
s_setprio 0
v_add_f32 v176,1.0,v176
v_add_f32 v177,1.0,v177
v_add_f32 v178,1.0,v178
v_add_f32 v179,1.0,v179
v_add_f32 v180,1.0,v180
v_add_f32 v181,1.0,v181
v_add_f32 v182,1.0,v182
v_add_f32 v183,1.0,v183
v_add_f32 v184,1.0,v184
v_add_f32 v185,1.0,v185
v_add_f32 v186,1.0,v186
v_add_f32 v187,1.0,v187
v_add_f32 v188,1.0,v188
v_add_f32 v189,1.0,v189
v_add_f32 v190,1.0,v190
v_add_f32 v191,1.0,v191
s_setprio 1
s_waitcnt lgkmcnt(0)
v_mfma_f32_32x32x16_bf16 v[160:175],v[200:203],v[72:75],v[160:175]
v_rcp_f32 v176,v176
v_rcp_f32 v177,v177
v_rcp_f32 v178,v178
v_mfma_f32_32x32x16_bf16 v[160:175],v[204:207],v[76:79],v[160:175]
v_rcp_f32 v179,v179
v_rcp_f32 v180,v180
v_rcp_f32 v181,v181
v_mfma_f32_32x32x16_bf16 v[160:175],v[208:211],v[80:83],v[160:175]
v_rcp_f32 v182,v182
v_rcp_f32 v183,v183
v_rcp_f32 v184,v184
v_mfma_f32_32x32x16_bf16 v[160:175],v[212:215],v[84:87],v[160:175]
v_rcp_f32 v185,v185
v_rcp_f32 v186,v186
v_rcp_f32 v187,v187
v_mfma_f32_32x32x16_bf16 v[160:175],v[216:219],v[88:91],v[160:175]
v_rcp_f32 v188,v188
v_rcp_f32 v189,v189
v_mfma_f32_32x32x16_bf16 v[160:175],v[220:223],v[92:95],v[160:175]
v_rcp_f32 v190,v190
v_rcp_f32 v191,v191
ds_read_b64_tr_b16 v[200:201],v17 offset:12288
ds_read_b64_tr_b16 v[202:203],v17 offset:13824
ds_read_b64_tr_b16 v[204:205],v17 offset:15360
ds_read_b64_tr_b16 v[206:207],v17 offset:16896
ds_read_b64_tr_b16 v[208:209],v17 offset:12352
ds_read_b64_tr_b16 v[210:211],v17 offset:13888
ds_read_b64_tr_b16 v[212:213],v17 offset:15424
ds_read_b64_tr_b16 v[214:215],v17 offset:16960
ds_read_b64_tr_b16 v[216:217],v17 offset:12416
ds_read_b64_tr_b16 v[218:219],v17 offset:13952
ds_read_b64_tr_b16 v[220:221],v17 offset:15488
ds_read_b64_tr_b16 v[222:223],v17 offset:17024
s_mov_b64 exec,s[62:63]
s_add_u32 m0,s68,17280
s_add_u32 s64,s59,491520
buffer_load_dwordx4 v27,s[36:39],s64 offen lds
s_mov_b64 exec,-1
s_setprio 0
v_mul_f32 v176,v144,v176
v_mul_f32 v177,v145,v177
v_mul_f32 v178,v146,v178
v_mul_f32 v179,v147,v179
v_mul_f32 v180,v148,v180
v_mul_f32 v181,v149,v181
v_mul_f32 v182,v150,v182
v_mul_f32 v183,v151,v183
v_mul_f32 v184,v152,v184
v_mul_f32 v185,v153,v185
v_mul_f32 v186,v154,v186
v_mul_f32 v187,v155,v187
v_mul_f32 v188,v156,v188
v_mul_f32 v189,v157,v189
v_mul_f32 v190,v158,v190
v_mul_f32 v191,v159,v191
v_cvt_pk_bf16_f32 v192,v176,v177
v_cvt_pk_bf16_f32 v193,v178,v179
v_cvt_pk_bf16_f32 v194,v180,v181
v_cvt_pk_bf16_f32 v195,v182,v183
v_cvt_pk_bf16_f32 v196,v184,v185
v_cvt_pk_bf16_f32 v197,v186,v187
v_cvt_pk_bf16_f32 v198,v188,v189
v_cvt_pk_bf16_f32 v199,v190,v191
s_waitcnt lgkmcnt(0)
s_barrier
v_fmaak_f32 v176,v160,v160,0x409d30fc
v_fmaak_f32 v177,v161,v161,0x409d30fc
v_fmaak_f32 v178,v162,v162,0x409d30fc
v_fmaak_f32 v179,v163,v163,0x409d30fc
v_fmaak_f32 v180,v164,v164,0x409d30fc
v_fmaak_f32 v181,v165,v165,0x409d30fc
v_fmaak_f32 v182,v166,v166,0x409d30fc
v_fmaak_f32 v183,v167,v167,0x409d30fc
v_fmaak_f32 v184,v168,v168,0x409d30fc
v_fmaak_f32 v185,v169,v169,0x409d30fc
v_fmaak_f32 v186,v170,v170,0x409d30fc
v_fmaak_f32 v187,v171,v171,0x409d30fc
v_fmaak_f32 v188,v172,v172,0x409d30fc
v_fmaak_f32 v189,v173,v173,0x409d30fc
v_fmaak_f32 v190,v174,v174,0x409d30fc
v_fmaak_f32 v191,v175,v175,0x409d30fc
v_mul_f32 v176,v160,v176
v_mul_f32 v177,v161,v177
v_mul_f32 v178,v162,v178
v_mul_f32 v179,v163,v179
v_mul_f32 v180,v164,v180
v_mul_f32 v181,v165,v181
v_mul_f32 v182,v166,v182
v_mul_f32 v183,v167,v183
v_mul_f32 v184,v168,v184
v_mul_f32 v185,v169,v185
v_mul_f32 v186,v170,v186
v_mul_f32 v187,v171,v187
v_mul_f32 v188,v172,v188
v_mul_f32 v189,v173,v189
v_mul_f32 v190,v174,v190
v_mul_f32 v191,v175,v191
s_setprio 1
v_mfma_f32_32x32x16_bf16 v[96:111],v[200:203],v[192:195],v[96:111]
v_exp_f32 v176,v176
v_exp_f32 v177,v177
v_exp_f32 v178,v178
v_mfma_f32_32x32x16_bf16 v[96:111],v[204:207],v[196:199],v[96:111]
v_exp_f32 v179,v179
v_exp_f32 v180,v180
v_exp_f32 v181,v181
v_mfma_f32_32x32x16_bf16 v[112:127],v[208:211],v[192:195],v[112:127]
v_exp_f32 v182,v182
v_exp_f32 v183,v183
v_exp_f32 v184,v184
v_mfma_f32_32x32x16_bf16 v[112:127],v[212:215],v[196:199],v[112:127]
v_exp_f32 v185,v185
v_exp_f32 v186,v186
v_exp_f32 v187,v187
v_mfma_f32_32x32x16_bf16 v[128:143],v[216:219],v[192:195],v[128:143]
v_exp_f32 v188,v188
v_exp_f32 v189,v189
v_mfma_f32_32x32x16_bf16 v[128:143],v[220:223],v[196:199],v[128:143]
v_exp_f32 v190,v190
v_exp_f32 v191,v191
ds_read_b64_tr_b16 v[200:201],v16 offset:6144
ds_read_b64_tr_b16 v[202:203],v16 offset:6656
ds_read_b64_tr_b16 v[204:205],v16 offset:7168
ds_read_b64_tr_b16 v[206:207],v16 offset:7680
ds_read_b64_tr_b16 v[208:209],v16 offset:8192
ds_read_b64_tr_b16 v[210:211],v16 offset:8704
ds_read_b64_tr_b16 v[212:213],v16 offset:9216
ds_read_b64_tr_b16 v[214:215],v16 offset:9728
ds_read_b64_tr_b16 v[216:217],v16 offset:10240
ds_read_b64_tr_b16 v[218:219],v16 offset:10752
ds_read_b64_tr_b16 v[220:221],v16 offset:11264
ds_read_b64_tr_b16 v[222:223],v16 offset:11776
ds_read_b128 v[144:147],v18 offset:512
ds_read_b128 v[148:151],v18 offset:544
ds_read_b128 v[152:155],v18 offset:576
ds_read_b128 v[156:159],v18 offset:608
s_mov_b64 exec,s[62:63]
s_add_u32 m0,s68,20736
s_add_u32 s64,s59,589824
buffer_load_dwordx4 v27,s[36:39],s64 offen lds
s_mov_b64 exec,-1
s_setprio 0
v_add_f32 v176,1.0,v176
v_add_f32 v177,1.0,v177
v_add_f32 v178,1.0,v178
v_add_f32 v179,1.0,v179
v_add_f32 v180,1.0,v180
v_add_f32 v181,1.0,v181
v_add_f32 v182,1.0,v182
v_add_f32 v183,1.0,v183
v_add_f32 v184,1.0,v184
v_add_f32 v185,1.0,v185
v_add_f32 v186,1.0,v186
v_add_f32 v187,1.0,v187
v_add_f32 v188,1.0,v188
v_add_f32 v189,1.0,v189
v_add_f32 v190,1.0,v190
v_add_f32 v191,1.0,v191
s_setprio 1
s_waitcnt lgkmcnt(0)
v_mfma_f32_32x32x16_bf16 v[144:159],v[200:203],v[72:75],v[144:159]
v_rcp_f32 v176,v176
v_rcp_f32 v177,v177
v_rcp_f32 v178,v178
v_mfma_f32_32x32x16_bf16 v[144:159],v[204:207],v[76:79],v[144:159]
v_rcp_f32 v179,v179
v_rcp_f32 v180,v180
v_rcp_f32 v181,v181
v_mfma_f32_32x32x16_bf16 v[144:159],v[208:211],v[80:83],v[144:159]
v_rcp_f32 v182,v182
v_rcp_f32 v183,v183
v_rcp_f32 v184,v184
v_mfma_f32_32x32x16_bf16 v[144:159],v[212:215],v[84:87],v[144:159]
v_rcp_f32 v185,v185
v_rcp_f32 v186,v186
v_rcp_f32 v187,v187
v_mfma_f32_32x32x16_bf16 v[144:159],v[216:219],v[88:91],v[144:159]
v_rcp_f32 v188,v188
v_rcp_f32 v189,v189
v_mfma_f32_32x32x16_bf16 v[144:159],v[220:223],v[92:95],v[144:159]
v_rcp_f32 v190,v190
v_rcp_f32 v191,v191
ds_read_b64_tr_b16 v[200:201],v17 offset:0
ds_read_b64_tr_b16 v[202:203],v17 offset:1536
ds_read_b64_tr_b16 v[204:205],v17 offset:3072
ds_read_b64_tr_b16 v[206:207],v17 offset:4608
ds_read_b64_tr_b16 v[208:209],v17 offset:64
ds_read_b64_tr_b16 v[210:211],v17 offset:1600
ds_read_b64_tr_b16 v[212:213],v17 offset:3136
ds_read_b64_tr_b16 v[214:215],v17 offset:4672
ds_read_b64_tr_b16 v[216:217],v17 offset:128
ds_read_b64_tr_b16 v[218:219],v17 offset:1664
ds_read_b64_tr_b16 v[220:221],v17 offset:3200
ds_read_b64_tr_b16 v[222:223],v17 offset:4736
s_mov_b64 exec,s[62:63]
s_add_u32 m0,s68,24192
s_add_u32 s64,s59,688128
buffer_load_dwordx4 v27,s[36:39],s64 offen lds
s_mov_b64 exec,-1
s_setprio 0
v_mul_f32 v176,v160,v176
v_mul_f32 v177,v161,v177
v_mul_f32 v178,v162,v178
v_mul_f32 v179,v163,v179
v_mul_f32 v180,v164,v180
v_mul_f32 v181,v165,v181
v_mul_f32 v182,v166,v182
v_mul_f32 v183,v167,v183
v_mul_f32 v184,v168,v184
v_mul_f32 v185,v169,v185
v_mul_f32 v186,v170,v186
v_mul_f32 v187,v171,v187
v_mul_f32 v188,v172,v188
v_mul_f32 v189,v173,v189
v_mul_f32 v190,v174,v190
v_mul_f32 v191,v175,v191
v_cvt_pk_bf16_f32 v192,v176,v177
v_cvt_pk_bf16_f32 v193,v178,v179
v_cvt_pk_bf16_f32 v194,v180,v181
v_cvt_pk_bf16_f32 v195,v182,v183
v_cvt_pk_bf16_f32 v196,v184,v185
v_cvt_pk_bf16_f32 v197,v186,v187
v_cvt_pk_bf16_f32 v198,v188,v189
v_cvt_pk_bf16_f32 v199,v190,v191
s_waitcnt lgkmcnt(0)
s_barrier
v_fmaak_f32 v176,v144,v144,0x409d30fc
v_fmaak_f32 v177,v145,v145,0x409d30fc
v_fmaak_f32 v178,v146,v146,0x409d30fc
v_fmaak_f32 v179,v147,v147,0x409d30fc
v_fmaak_f32 v180,v148,v148,0x409d30fc
v_fmaak_f32 v181,v149,v149,0x409d30fc
v_fmaak_f32 v182,v150,v150,0x409d30fc
v_fmaak_f32 v183,v151,v151,0x409d30fc
v_fmaak_f32 v184,v152,v152,0x409d30fc
v_fmaak_f32 v185,v153,v153,0x409d30fc
v_fmaak_f32 v186,v154,v154,0x409d30fc
v_fmaak_f32 v187,v155,v155,0x409d30fc
v_fmaak_f32 v188,v156,v156,0x409d30fc
v_fmaak_f32 v189,v157,v157,0x409d30fc
v_fmaak_f32 v190,v158,v158,0x409d30fc
v_fmaak_f32 v191,v159,v159,0x409d30fc
v_mul_f32 v176,v144,v176
v_mul_f32 v177,v145,v177
v_mul_f32 v178,v146,v178
v_mul_f32 v179,v147,v179
v_mul_f32 v180,v148,v180
v_mul_f32 v181,v149,v181
v_mul_f32 v182,v150,v182
v_mul_f32 v183,v151,v183
v_mul_f32 v184,v152,v184
v_mul_f32 v185,v153,v185
v_mul_f32 v186,v154,v186
v_mul_f32 v187,v155,v187
v_mul_f32 v188,v156,v188
v_mul_f32 v189,v157,v189
v_mul_f32 v190,v158,v190
v_mul_f32 v191,v159,v191
s_setprio 1
v_mfma_f32_32x32x16_bf16 v[96:111],v[200:203],v[192:195],v[96:111]
v_exp_f32 v176,v176
v_exp_f32 v177,v177
v_exp_f32 v178,v178
v_mfma_f32_32x32x16_bf16 v[96:111],v[204:207],v[196:199],v[96:111]
v_exp_f32 v179,v179
v_exp_f32 v180,v180
v_exp_f32 v181,v181
v_mfma_f32_32x32x16_bf16 v[112:127],v[208:211],v[192:195],v[112:127]
v_exp_f32 v182,v182
v_exp_f32 v183,v183
v_exp_f32 v184,v184
v_mfma_f32_32x32x16_bf16 v[112:127],v[212:215],v[196:199],v[112:127]
v_exp_f32 v185,v185
v_exp_f32 v186,v186
v_exp_f32 v187,v187
v_mfma_f32_32x32x16_bf16 v[128:143],v[216:219],v[192:195],v[128:143]
v_exp_f32 v188,v188
v_exp_f32 v189,v189
v_mfma_f32_32x32x16_bf16 v[128:143],v[220:223],v[196:199],v[128:143]
v_exp_f32 v190,v190
v_exp_f32 v191,v191
ds_read_b64_tr_b16 v[200:201],v16 offset:12288
ds_read_b64_tr_b16 v[202:203],v16 offset:12800
ds_read_b64_tr_b16 v[204:205],v16 offset:13312
ds_read_b64_tr_b16 v[206:207],v16 offset:13824
ds_read_b64_tr_b16 v[208:209],v16 offset:14336
ds_read_b64_tr_b16 v[210:211],v16 offset:14848
ds_read_b64_tr_b16 v[212:213],v16 offset:15360
ds_read_b64_tr_b16 v[214:215],v16 offset:15872
ds_read_b64_tr_b16 v[216:217],v16 offset:16384
ds_read_b64_tr_b16 v[218:219],v16 offset:16896
ds_read_b64_tr_b16 v[220:221],v16 offset:17408
ds_read_b64_tr_b16 v[222:223],v16 offset:17920
ds_read_b128 v[160:163],v18 offset:640
ds_read_b128 v[164:167],v18 offset:672
ds_read_b128 v[168:171],v18 offset:704
ds_read_b128 v[172:175],v18 offset:736
s_mov_b64 exec,s[62:63]
s_add_u32 m0,s68,27648
s_add_u32 s64,s59,786432
buffer_load_dwordx4 v27,s[36:39],s64 offen lds
s_mov_b64 exec,-1
s_setprio 0
v_add_f32 v176,1.0,v176
v_add_f32 v177,1.0,v177
v_add_f32 v178,1.0,v178
v_add_f32 v179,1.0,v179
v_add_f32 v180,1.0,v180
v_add_f32 v181,1.0,v181
v_add_f32 v182,1.0,v182
v_add_f32 v183,1.0,v183
v_add_f32 v184,1.0,v184
v_add_f32 v185,1.0,v185
v_add_f32 v186,1.0,v186
v_add_f32 v187,1.0,v187
v_add_f32 v188,1.0,v188
v_add_f32 v189,1.0,v189
v_add_f32 v190,1.0,v190
v_add_f32 v191,1.0,v191
s_setprio 1
s_waitcnt lgkmcnt(0)
v_mfma_f32_32x32x16_bf16 v[160:175],v[200:203],v[72:75],v[160:175]
v_rcp_f32 v176,v176
v_rcp_f32 v177,v177
v_rcp_f32 v178,v178
v_mfma_f32_32x32x16_bf16 v[160:175],v[204:207],v[76:79],v[160:175]
v_rcp_f32 v179,v179
v_rcp_f32 v180,v180
v_rcp_f32 v181,v181
v_mfma_f32_32x32x16_bf16 v[160:175],v[208:211],v[80:83],v[160:175]
v_rcp_f32 v182,v182
v_rcp_f32 v183,v183
v_rcp_f32 v184,v184
v_mfma_f32_32x32x16_bf16 v[160:175],v[212:215],v[84:87],v[160:175]
v_rcp_f32 v185,v185
v_rcp_f32 v186,v186
v_rcp_f32 v187,v187
v_mfma_f32_32x32x16_bf16 v[160:175],v[216:219],v[88:91],v[160:175]
v_rcp_f32 v188,v188
v_rcp_f32 v189,v189
v_mfma_f32_32x32x16_bf16 v[160:175],v[220:223],v[92:95],v[160:175]
v_rcp_f32 v190,v190
v_rcp_f32 v191,v191
ds_read_b64_tr_b16 v[200:201],v17 offset:6144
ds_read_b64_tr_b16 v[202:203],v17 offset:7680
ds_read_b64_tr_b16 v[204:205],v17 offset:9216
ds_read_b64_tr_b16 v[206:207],v17 offset:10752
ds_read_b64_tr_b16 v[208:209],v17 offset:6208
ds_read_b64_tr_b16 v[210:211],v17 offset:7744
ds_read_b64_tr_b16 v[212:213],v17 offset:9280
ds_read_b64_tr_b16 v[214:215],v17 offset:10816
ds_read_b64_tr_b16 v[216:217],v17 offset:6272
ds_read_b64_tr_b16 v[218:219],v17 offset:7808
ds_read_b64_tr_b16 v[220:221],v17 offset:9344
ds_read_b64_tr_b16 v[222:223],v17 offset:10880
s_mov_b64 exec,s[62:63]
s_add_u32 m0,s68,31104
s_add_u32 s64,s59,884736
buffer_load_dwordx4 v27,s[36:39],s64 offen lds
s_mov_b64 exec,-1
s_setprio 0
v_mul_f32 v176,v144,v176
v_mul_f32 v177,v145,v177
v_mul_f32 v178,v146,v178
v_mul_f32 v179,v147,v179
v_mul_f32 v180,v148,v180
v_mul_f32 v181,v149,v181
v_mul_f32 v182,v150,v182
v_mul_f32 v183,v151,v183
v_mul_f32 v184,v152,v184
v_mul_f32 v185,v153,v185
v_mul_f32 v186,v154,v186
v_mul_f32 v187,v155,v187
v_mul_f32 v188,v156,v188
v_mul_f32 v189,v157,v189
v_mul_f32 v190,v158,v190
v_mul_f32 v191,v159,v191
v_cvt_pk_bf16_f32 v192,v176,v177
v_cvt_pk_bf16_f32 v193,v178,v179
v_cvt_pk_bf16_f32 v194,v180,v181
v_cvt_pk_bf16_f32 v195,v182,v183
v_cvt_pk_bf16_f32 v196,v184,v185
v_cvt_pk_bf16_f32 v197,v186,v187
v_cvt_pk_bf16_f32 v198,v188,v189
v_cvt_pk_bf16_f32 v199,v190,v191
s_waitcnt lgkmcnt(0)
s_barrier
v_fmaak_f32 v176,v160,v160,0x409d30fc
v_fmaak_f32 v177,v161,v161,0x409d30fc
v_fmaak_f32 v178,v162,v162,0x409d30fc
v_fmaak_f32 v179,v163,v163,0x409d30fc
v_fmaak_f32 v180,v164,v164,0x409d30fc
v_fmaak_f32 v181,v165,v165,0x409d30fc
v_fmaak_f32 v182,v166,v166,0x409d30fc
v_fmaak_f32 v183,v167,v167,0x409d30fc
v_fmaak_f32 v184,v168,v168,0x409d30fc
v_fmaak_f32 v185,v169,v169,0x409d30fc
v_fmaak_f32 v186,v170,v170,0x409d30fc
v_fmaak_f32 v187,v171,v171,0x409d30fc
v_fmaak_f32 v188,v172,v172,0x409d30fc
v_fmaak_f32 v189,v173,v173,0x409d30fc
v_fmaak_f32 v190,v174,v174,0x409d30fc
v_fmaak_f32 v191,v175,v175,0x409d30fc
v_mul_f32 v176,v160,v176
v_mul_f32 v177,v161,v177
v_mul_f32 v178,v162,v178
v_mul_f32 v179,v163,v179
v_mul_f32 v180,v164,v180
v_mul_f32 v181,v165,v181
v_mul_f32 v182,v166,v182
v_mul_f32 v183,v167,v183
v_mul_f32 v184,v168,v184
v_mul_f32 v185,v169,v185
v_mul_f32 v186,v170,v186
v_mul_f32 v187,v171,v187
v_mul_f32 v188,v172,v188
v_mul_f32 v189,v173,v189
v_mul_f32 v190,v174,v190
v_mul_f32 v191,v175,v191
s_setprio 1
v_mfma_f32_32x32x16_bf16 v[96:111],v[200:203],v[192:195],v[96:111]
v_exp_f32 v176,v176
v_exp_f32 v177,v177
v_exp_f32 v178,v178
v_mfma_f32_32x32x16_bf16 v[96:111],v[204:207],v[196:199],v[96:111]
v_exp_f32 v179,v179
v_exp_f32 v180,v180
v_exp_f32 v181,v181
v_mfma_f32_32x32x16_bf16 v[112:127],v[208:211],v[192:195],v[112:127]
v_exp_f32 v182,v182
v_exp_f32 v183,v183
v_exp_f32 v184,v184
v_mfma_f32_32x32x16_bf16 v[112:127],v[212:215],v[196:199],v[112:127]
v_exp_f32 v185,v185
v_exp_f32 v186,v186
v_exp_f32 v187,v187
v_mfma_f32_32x32x16_bf16 v[128:143],v[216:219],v[192:195],v[128:143]
v_exp_f32 v188,v188
v_exp_f32 v189,v189
v_mfma_f32_32x32x16_bf16 v[128:143],v[220:223],v[196:199],v[128:143]
v_exp_f32 v190,v190
v_exp_f32 v191,v191
ds_read_b64_tr_b16 v[200:201],v16 offset:0
ds_read_b64_tr_b16 v[202:203],v16 offset:512
ds_read_b64_tr_b16 v[204:205],v16 offset:1024
ds_read_b64_tr_b16 v[206:207],v16 offset:1536
ds_read_b64_tr_b16 v[208:209],v16 offset:2048
ds_read_b64_tr_b16 v[210:211],v16 offset:2560
ds_read_b64_tr_b16 v[212:213],v16 offset:3072
ds_read_b64_tr_b16 v[214:215],v16 offset:3584
ds_read_b64_tr_b16 v[216:217],v16 offset:4096
ds_read_b64_tr_b16 v[218:219],v16 offset:4608
ds_read_b64_tr_b16 v[220:221],v16 offset:5120
ds_read_b64_tr_b16 v[222:223],v16 offset:5632
ds_read_b128 v[144:147],v18 offset:768
ds_read_b128 v[148:151],v18 offset:800
ds_read_b128 v[152:155],v18 offset:832
ds_read_b128 v[156:159],v18 offset:864
s_mov_b64 exec,s[62:63]
s_add_u32 m0,s68,34560
s_add_u32 s64,s59,983040
buffer_load_dwordx4 v27,s[36:39],s64 offen lds
s_mov_b64 exec,-1
s_setprio 0
v_add_f32 v176,1.0,v176
v_add_f32 v177,1.0,v177
v_add_f32 v178,1.0,v178
v_add_f32 v179,1.0,v179
v_add_f32 v180,1.0,v180
v_add_f32 v181,1.0,v181
v_add_f32 v182,1.0,v182
v_add_f32 v183,1.0,v183
v_add_f32 v184,1.0,v184
v_add_f32 v185,1.0,v185
v_add_f32 v186,1.0,v186
v_add_f32 v187,1.0,v187
v_add_f32 v188,1.0,v188
v_add_f32 v189,1.0,v189
v_add_f32 v190,1.0,v190
v_add_f32 v191,1.0,v191
s_setprio 1
s_waitcnt lgkmcnt(0)
v_mfma_f32_32x32x16_bf16 v[144:159],v[200:203],v[72:75],v[144:159]
v_rcp_f32 v176,v176
v_rcp_f32 v177,v177
v_rcp_f32 v178,v178
v_mfma_f32_32x32x16_bf16 v[144:159],v[204:207],v[76:79],v[144:159]
v_rcp_f32 v179,v179
v_rcp_f32 v180,v180
v_rcp_f32 v181,v181
v_mfma_f32_32x32x16_bf16 v[144:159],v[208:211],v[80:83],v[144:159]
v_rcp_f32 v182,v182
v_rcp_f32 v183,v183
v_rcp_f32 v184,v184
v_mfma_f32_32x32x16_bf16 v[144:159],v[212:215],v[84:87],v[144:159]
v_rcp_f32 v185,v185
v_rcp_f32 v186,v186
v_rcp_f32 v187,v187
v_mfma_f32_32x32x16_bf16 v[144:159],v[216:219],v[88:91],v[144:159]
v_rcp_f32 v188,v188
v_rcp_f32 v189,v189
v_mfma_f32_32x32x16_bf16 v[144:159],v[220:223],v[92:95],v[144:159]
v_rcp_f32 v190,v190
v_rcp_f32 v191,v191
ds_read_b64_tr_b16 v[200:201],v17 offset:12288
ds_read_b64_tr_b16 v[202:203],v17 offset:13824
ds_read_b64_tr_b16 v[204:205],v17 offset:15360
ds_read_b64_tr_b16 v[206:207],v17 offset:16896
ds_read_b64_tr_b16 v[208:209],v17 offset:12352
ds_read_b64_tr_b16 v[210:211],v17 offset:13888
ds_read_b64_tr_b16 v[212:213],v17 offset:15424
ds_read_b64_tr_b16 v[214:215],v17 offset:16960
ds_read_b64_tr_b16 v[216:217],v17 offset:12416
ds_read_b64_tr_b16 v[218:219],v17 offset:13952
ds_read_b64_tr_b16 v[220:221],v17 offset:15488
ds_read_b64_tr_b16 v[222:223],v17 offset:17024
s_mov_b64 exec,s[62:63]
s_add_u32 m0,s68,38016
s_add_u32 s64,s59,1081344
buffer_load_dwordx4 v27,s[36:39],s64 offen lds
s_mov_b64 exec,-1
s_setprio 0
v_mul_f32 v176,v160,v176
v_mul_f32 v177,v161,v177
v_mul_f32 v178,v162,v178
v_mul_f32 v179,v163,v179
v_mul_f32 v180,v164,v180
v_mul_f32 v181,v165,v181
v_mul_f32 v182,v166,v182
v_mul_f32 v183,v167,v183
v_mul_f32 v184,v168,v184
v_mul_f32 v185,v169,v185
v_mul_f32 v186,v170,v186
v_mul_f32 v187,v171,v187
v_mul_f32 v188,v172,v188
v_mul_f32 v189,v173,v189
v_mul_f32 v190,v174,v190
v_mul_f32 v191,v175,v191
v_cvt_pk_bf16_f32 v192,v176,v177
v_cvt_pk_bf16_f32 v193,v178,v179
v_cvt_pk_bf16_f32 v194,v180,v181
v_cvt_pk_bf16_f32 v195,v182,v183
v_cvt_pk_bf16_f32 v196,v184,v185
v_cvt_pk_bf16_f32 v197,v186,v187
v_cvt_pk_bf16_f32 v198,v188,v189
v_cvt_pk_bf16_f32 v199,v190,v191
s_waitcnt lgkmcnt(0)
s_barrier
v_fmaak_f32 v176,v144,v144,0x409d30fc
v_fmaak_f32 v177,v145,v145,0x409d30fc
v_fmaak_f32 v178,v146,v146,0x409d30fc
v_fmaak_f32 v179,v147,v147,0x409d30fc
v_fmaak_f32 v180,v148,v148,0x409d30fc
v_fmaak_f32 v181,v149,v149,0x409d30fc
v_fmaak_f32 v182,v150,v150,0x409d30fc
v_fmaak_f32 v183,v151,v151,0x409d30fc
v_fmaak_f32 v184,v152,v152,0x409d30fc
v_fmaak_f32 v185,v153,v153,0x409d30fc
v_fmaak_f32 v186,v154,v154,0x409d30fc
v_fmaak_f32 v187,v155,v155,0x409d30fc
v_fmaak_f32 v188,v156,v156,0x409d30fc
v_fmaak_f32 v189,v157,v157,0x409d30fc
v_fmaak_f32 v190,v158,v158,0x409d30fc
v_fmaak_f32 v191,v159,v159,0x409d30fc
v_mul_f32 v176,v144,v176
v_mul_f32 v177,v145,v177
v_mul_f32 v178,v146,v178
v_mul_f32 v179,v147,v179
v_mul_f32 v180,v148,v180
v_mul_f32 v181,v149,v181
v_mul_f32 v182,v150,v182
v_mul_f32 v183,v151,v183
v_mul_f32 v184,v152,v184
v_mul_f32 v185,v153,v185
v_mul_f32 v186,v154,v186
v_mul_f32 v187,v155,v187
v_mul_f32 v188,v156,v188
v_mul_f32 v189,v157,v189
v_mul_f32 v190,v158,v190
v_mul_f32 v191,v159,v191
s_setprio 1
v_mfma_f32_32x32x16_bf16 v[96:111],v[200:203],v[192:195],v[96:111]
v_exp_f32 v176,v176
v_exp_f32 v177,v177
v_exp_f32 v178,v178
v_mfma_f32_32x32x16_bf16 v[96:111],v[204:207],v[196:199],v[96:111]
v_exp_f32 v179,v179
v_exp_f32 v180,v180
v_exp_f32 v181,v181
v_mfma_f32_32x32x16_bf16 v[112:127],v[208:211],v[192:195],v[112:127]
v_exp_f32 v182,v182
v_exp_f32 v183,v183
v_exp_f32 v184,v184
v_mfma_f32_32x32x16_bf16 v[112:127],v[212:215],v[196:199],v[112:127]
v_exp_f32 v185,v185
v_exp_f32 v186,v186
v_exp_f32 v187,v187
v_mfma_f32_32x32x16_bf16 v[128:143],v[216:219],v[192:195],v[128:143]
v_exp_f32 v188,v188
v_exp_f32 v189,v189
v_mfma_f32_32x32x16_bf16 v[128:143],v[220:223],v[196:199],v[128:143]
v_exp_f32 v190,v190
v_exp_f32 v191,v191
ds_read_b64_tr_b16 v[200:201],v16 offset:6144
ds_read_b64_tr_b16 v[202:203],v16 offset:6656
ds_read_b64_tr_b16 v[204:205],v16 offset:7168
ds_read_b64_tr_b16 v[206:207],v16 offset:7680
ds_read_b64_tr_b16 v[208:209],v16 offset:8192
ds_read_b64_tr_b16 v[210:211],v16 offset:8704
ds_read_b64_tr_b16 v[212:213],v16 offset:9216
ds_read_b64_tr_b16 v[214:215],v16 offset:9728
ds_read_b64_tr_b16 v[216:217],v16 offset:10240
ds_read_b64_tr_b16 v[218:219],v16 offset:10752
ds_read_b64_tr_b16 v[220:221],v16 offset:11264
ds_read_b64_tr_b16 v[222:223],v16 offset:11776
ds_read_b128 v[160:163],v18 offset:896
ds_read_b128 v[164:167],v18 offset:928
ds_read_b128 v[168:171],v18 offset:960
ds_read_b128 v[172:175],v18 offset:992
s_mov_b64 exec,s[62:63]
s_add_u32 m0,s68,41472
s_add_u32 s64,s59,1179648
buffer_load_dwordx4 v27,s[36:39],s64 offen lds
s_mov_b64 exec,-1
s_setprio 0
v_add_f32 v176,1.0,v176
v_add_f32 v177,1.0,v177
v_add_f32 v178,1.0,v178
v_add_f32 v179,1.0,v179
v_add_f32 v180,1.0,v180
v_add_f32 v181,1.0,v181
v_add_f32 v182,1.0,v182
v_add_f32 v183,1.0,v183
v_add_f32 v184,1.0,v184
v_add_f32 v185,1.0,v185
v_add_f32 v186,1.0,v186
v_add_f32 v187,1.0,v187
v_add_f32 v188,1.0,v188
v_add_f32 v189,1.0,v189
v_add_f32 v190,1.0,v190
v_add_f32 v191,1.0,v191
s_setprio 1
s_waitcnt lgkmcnt(0)
v_mfma_f32_32x32x16_bf16 v[160:175],v[200:203],v[72:75],v[160:175]
v_rcp_f32 v176,v176
v_rcp_f32 v177,v177
v_rcp_f32 v178,v178
v_mfma_f32_32x32x16_bf16 v[160:175],v[204:207],v[76:79],v[160:175]
v_rcp_f32 v179,v179
v_rcp_f32 v180,v180
v_rcp_f32 v181,v181
v_mfma_f32_32x32x16_bf16 v[160:175],v[208:211],v[80:83],v[160:175]
v_rcp_f32 v182,v182
v_rcp_f32 v183,v183
v_rcp_f32 v184,v184
v_mfma_f32_32x32x16_bf16 v[160:175],v[212:215],v[84:87],v[160:175]
v_rcp_f32 v185,v185
v_rcp_f32 v186,v186
v_rcp_f32 v187,v187
v_mfma_f32_32x32x16_bf16 v[160:175],v[216:219],v[88:91],v[160:175]
v_rcp_f32 v188,v188
v_rcp_f32 v189,v189
v_mfma_f32_32x32x16_bf16 v[160:175],v[220:223],v[92:95],v[160:175]
v_rcp_f32 v190,v190
v_rcp_f32 v191,v191
ds_read_b64_tr_b16 v[200:201],v17 offset:0
ds_read_b64_tr_b16 v[202:203],v17 offset:1536
ds_read_b64_tr_b16 v[204:205],v17 offset:3072
ds_read_b64_tr_b16 v[206:207],v17 offset:4608
ds_read_b64_tr_b16 v[208:209],v17 offset:64
ds_read_b64_tr_b16 v[210:211],v17 offset:1600
ds_read_b64_tr_b16 v[212:213],v17 offset:3136
ds_read_b64_tr_b16 v[214:215],v17 offset:4672
ds_read_b64_tr_b16 v[216:217],v17 offset:128
ds_read_b64_tr_b16 v[218:219],v17 offset:1664
ds_read_b64_tr_b16 v[220:221],v17 offset:3200
ds_read_b64_tr_b16 v[222:223],v17 offset:4736
s_mov_b64 exec,s[62:63]
s_add_u32 m0,s68,44928
s_add_u32 s64,s59,1277952
buffer_load_dwordx4 v27,s[36:39],s64 offen lds
s_mov_b64 exec,-1
s_setprio 0
v_mul_f32 v176,v144,v176
v_mul_f32 v177,v145,v177
v_mul_f32 v178,v146,v178
v_mul_f32 v179,v147,v179
v_mul_f32 v180,v148,v180
v_mul_f32 v181,v149,v181
v_mul_f32 v182,v150,v182
v_mul_f32 v183,v151,v183
v_mul_f32 v184,v152,v184
v_mul_f32 v185,v153,v185
v_mul_f32 v186,v154,v186
v_mul_f32 v187,v155,v187
v_mul_f32 v188,v156,v188
v_mul_f32 v189,v157,v189
v_mul_f32 v190,v158,v190
v_mul_f32 v191,v159,v191
v_cvt_pk_bf16_f32 v192,v176,v177
v_cvt_pk_bf16_f32 v193,v178,v179
v_cvt_pk_bf16_f32 v194,v180,v181
v_cvt_pk_bf16_f32 v195,v182,v183
v_cvt_pk_bf16_f32 v196,v184,v185
v_cvt_pk_bf16_f32 v197,v186,v187
v_cvt_pk_bf16_f32 v198,v188,v189
v_cvt_pk_bf16_f32 v199,v190,v191
s_waitcnt lgkmcnt(0)
s_barrier
v_fmaak_f32 v176,v160,v160,0x409d30fc
v_fmaak_f32 v177,v161,v161,0x409d30fc
v_fmaak_f32 v178,v162,v162,0x409d30fc
v_fmaak_f32 v179,v163,v163,0x409d30fc
v_fmaak_f32 v180,v164,v164,0x409d30fc
v_fmaak_f32 v181,v165,v165,0x409d30fc
v_fmaak_f32 v182,v166,v166,0x409d30fc
v_fmaak_f32 v183,v167,v167,0x409d30fc
v_fmaak_f32 v184,v168,v168,0x409d30fc
v_fmaak_f32 v185,v169,v169,0x409d30fc
v_fmaak_f32 v186,v170,v170,0x409d30fc
v_fmaak_f32 v187,v171,v171,0x409d30fc
v_fmaak_f32 v188,v172,v172,0x409d30fc
v_fmaak_f32 v189,v173,v173,0x409d30fc
v_fmaak_f32 v190,v174,v174,0x409d30fc
v_fmaak_f32 v191,v175,v175,0x409d30fc
v_mul_f32 v176,v160,v176
v_mul_f32 v177,v161,v177
v_mul_f32 v178,v162,v178
v_mul_f32 v179,v163,v179
v_mul_f32 v180,v164,v180
v_mul_f32 v181,v165,v181
v_mul_f32 v182,v166,v182
v_mul_f32 v183,v167,v183
v_mul_f32 v184,v168,v184
v_mul_f32 v185,v169,v185
v_mul_f32 v186,v170,v186
v_mul_f32 v187,v171,v187
v_mul_f32 v188,v172,v188
v_mul_f32 v189,v173,v189
v_mul_f32 v190,v174,v190
v_mul_f32 v191,v175,v191
s_setprio 1
v_mfma_f32_32x32x16_bf16 v[96:111],v[200:203],v[192:195],v[96:111]
v_exp_f32 v176,v176
v_exp_f32 v177,v177
v_exp_f32 v178,v178
v_mfma_f32_32x32x16_bf16 v[96:111],v[204:207],v[196:199],v[96:111]
v_exp_f32 v179,v179
v_exp_f32 v180,v180
v_exp_f32 v181,v181
v_mfma_f32_32x32x16_bf16 v[112:127],v[208:211],v[192:195],v[112:127]
v_exp_f32 v182,v182
v_exp_f32 v183,v183
v_exp_f32 v184,v184
v_mfma_f32_32x32x16_bf16 v[112:127],v[212:215],v[196:199],v[112:127]
v_exp_f32 v185,v185
v_exp_f32 v186,v186
v_exp_f32 v187,v187
v_mfma_f32_32x32x16_bf16 v[128:143],v[216:219],v[192:195],v[128:143]
v_exp_f32 v188,v188
v_exp_f32 v189,v189
v_mfma_f32_32x32x16_bf16 v[128:143],v[220:223],v[196:199],v[128:143]
v_exp_f32 v190,v190
v_exp_f32 v191,v191
ds_read_b64_tr_b16 v[200:201],v16 offset:12288
ds_read_b64_tr_b16 v[202:203],v16 offset:12800
ds_read_b64_tr_b16 v[204:205],v16 offset:13312
ds_read_b64_tr_b16 v[206:207],v16 offset:13824
ds_read_b64_tr_b16 v[208:209],v16 offset:14336
ds_read_b64_tr_b16 v[210:211],v16 offset:14848
ds_read_b64_tr_b16 v[212:213],v16 offset:15360
ds_read_b64_tr_b16 v[214:215],v16 offset:15872
ds_read_b64_tr_b16 v[216:217],v16 offset:16384
ds_read_b64_tr_b16 v[218:219],v16 offset:16896
ds_read_b64_tr_b16 v[220:221],v16 offset:17408
ds_read_b64_tr_b16 v[222:223],v16 offset:17920
ds_read_b128 v[144:147],v18 offset:1024
ds_read_b128 v[148:151],v18 offset:1056
ds_read_b128 v[152:155],v18 offset:1088
ds_read_b128 v[156:159],v18 offset:1120
s_mov_b64 exec,s[62:63]
s_add_u32 m0,s68,48384
s_add_u32 s64,s59,1376256
buffer_load_dwordx4 v27,s[36:39],s64 offen lds
s_mov_b64 exec,-1
s_setprio 0
v_add_f32 v176,1.0,v176
v_add_f32 v177,1.0,v177
v_add_f32 v178,1.0,v178
v_add_f32 v179,1.0,v179
v_add_f32 v180,1.0,v180
v_add_f32 v181,1.0,v181
v_add_f32 v182,1.0,v182
v_add_f32 v183,1.0,v183
v_add_f32 v184,1.0,v184
v_add_f32 v185,1.0,v185
v_add_f32 v186,1.0,v186
v_add_f32 v187,1.0,v187
v_add_f32 v188,1.0,v188
v_add_f32 v189,1.0,v189
v_add_f32 v190,1.0,v190
v_add_f32 v191,1.0,v191
s_setprio 1
s_waitcnt lgkmcnt(0)
v_mfma_f32_32x32x16_bf16 v[144:159],v[200:203],v[72:75],v[144:159]
v_rcp_f32 v176,v176
v_rcp_f32 v177,v177
v_rcp_f32 v178,v178
v_mfma_f32_32x32x16_bf16 v[144:159],v[204:207],v[76:79],v[144:159]
v_rcp_f32 v179,v179
v_rcp_f32 v180,v180
v_rcp_f32 v181,v181
v_mfma_f32_32x32x16_bf16 v[144:159],v[208:211],v[80:83],v[144:159]
v_rcp_f32 v182,v182
v_rcp_f32 v183,v183
v_rcp_f32 v184,v184
v_mfma_f32_32x32x16_bf16 v[144:159],v[212:215],v[84:87],v[144:159]
v_rcp_f32 v185,v185
v_rcp_f32 v186,v186
v_rcp_f32 v187,v187
v_mfma_f32_32x32x16_bf16 v[144:159],v[216:219],v[88:91],v[144:159]
v_rcp_f32 v188,v188
v_rcp_f32 v189,v189
v_mfma_f32_32x32x16_bf16 v[144:159],v[220:223],v[92:95],v[144:159]
v_rcp_f32 v190,v190
v_rcp_f32 v191,v191
ds_read_b64_tr_b16 v[200:201],v17 offset:6144
ds_read_b64_tr_b16 v[202:203],v17 offset:7680
ds_read_b64_tr_b16 v[204:205],v17 offset:9216
ds_read_b64_tr_b16 v[206:207],v17 offset:10752
ds_read_b64_tr_b16 v[208:209],v17 offset:6208
ds_read_b64_tr_b16 v[210:211],v17 offset:7744
ds_read_b64_tr_b16 v[212:213],v17 offset:9280
ds_read_b64_tr_b16 v[214:215],v17 offset:10816
ds_read_b64_tr_b16 v[216:217],v17 offset:6272
ds_read_b64_tr_b16 v[218:219],v17 offset:7808
ds_read_b64_tr_b16 v[220:221],v17 offset:9344
ds_read_b64_tr_b16 v[222:223],v17 offset:10880
s_mov_b64 exec,s[62:63]
s_add_u32 m0,s68,51840
s_add_u32 s64,s59,1474560
buffer_load_dwordx4 v27,s[36:39],s64 offen lds
s_mov_b64 exec,-1
s_setprio 0
v_mul_f32 v176,v160,v176
v_mul_f32 v177,v161,v177
v_mul_f32 v178,v162,v178
v_mul_f32 v179,v163,v179
v_mul_f32 v180,v164,v180
v_mul_f32 v181,v165,v181
v_mul_f32 v182,v166,v182
v_mul_f32 v183,v167,v183
v_mul_f32 v184,v168,v184
v_mul_f32 v185,v169,v185
v_mul_f32 v186,v170,v186
v_mul_f32 v187,v171,v187
v_mul_f32 v188,v172,v188
v_mul_f32 v189,v173,v189
v_mul_f32 v190,v174,v190
v_mul_f32 v191,v175,v191
v_cvt_pk_bf16_f32 v192,v176,v177
v_cvt_pk_bf16_f32 v193,v178,v179
v_cvt_pk_bf16_f32 v194,v180,v181
v_cvt_pk_bf16_f32 v195,v182,v183
v_cvt_pk_bf16_f32 v196,v184,v185
v_cvt_pk_bf16_f32 v197,v186,v187
v_cvt_pk_bf16_f32 v198,v188,v189
v_cvt_pk_bf16_f32 v199,v190,v191
s_waitcnt lgkmcnt(0)
s_barrier
v_fmaak_f32 v176,v144,v144,0x409d30fc
v_fmaak_f32 v177,v145,v145,0x409d30fc
v_fmaak_f32 v178,v146,v146,0x409d30fc
v_fmaak_f32 v179,v147,v147,0x409d30fc
v_fmaak_f32 v180,v148,v148,0x409d30fc
v_fmaak_f32 v181,v149,v149,0x409d30fc
v_fmaak_f32 v182,v150,v150,0x409d30fc
v_fmaak_f32 v183,v151,v151,0x409d30fc
v_fmaak_f32 v184,v152,v152,0x409d30fc
v_fmaak_f32 v185,v153,v153,0x409d30fc
v_fmaak_f32 v186,v154,v154,0x409d30fc
v_fmaak_f32 v187,v155,v155,0x409d30fc
v_fmaak_f32 v188,v156,v156,0x409d30fc
v_fmaak_f32 v189,v157,v157,0x409d30fc
v_fmaak_f32 v190,v158,v158,0x409d30fc
v_fmaak_f32 v191,v159,v159,0x409d30fc
v_mul_f32 v176,v144,v176
v_mul_f32 v177,v145,v177
v_mul_f32 v178,v146,v178
v_mul_f32 v179,v147,v179
v_mul_f32 v180,v148,v180
v_mul_f32 v181,v149,v181
v_mul_f32 v182,v150,v182
v_mul_f32 v183,v151,v183
v_mul_f32 v184,v152,v184
v_mul_f32 v185,v153,v185
v_mul_f32 v186,v154,v186
v_mul_f32 v187,v155,v187
v_mul_f32 v188,v156,v188
v_mul_f32 v189,v157,v189
v_mul_f32 v190,v158,v190
v_mul_f32 v191,v159,v191
s_setprio 1
v_mfma_f32_32x32x16_bf16 v[96:111],v[200:203],v[192:195],v[96:111]
v_exp_f32 v176,v176
v_exp_f32 v177,v177
v_exp_f32 v178,v178
v_mfma_f32_32x32x16_bf16 v[96:111],v[204:207],v[196:199],v[96:111]
v_exp_f32 v179,v179
v_exp_f32 v180,v180
v_exp_f32 v181,v181
v_mfma_f32_32x32x16_bf16 v[112:127],v[208:211],v[192:195],v[112:127]
v_exp_f32 v182,v182
v_exp_f32 v183,v183
v_exp_f32 v184,v184
v_mfma_f32_32x32x16_bf16 v[112:127],v[212:215],v[196:199],v[112:127]
v_exp_f32 v185,v185
v_exp_f32 v186,v186
v_exp_f32 v187,v187
v_mfma_f32_32x32x16_bf16 v[128:143],v[216:219],v[192:195],v[128:143]
v_exp_f32 v188,v188
v_exp_f32 v189,v189
v_mfma_f32_32x32x16_bf16 v[128:143],v[220:223],v[196:199],v[128:143]
v_exp_f32 v190,v190
v_exp_f32 v191,v191
ds_read_b64_tr_b16 v[200:201],v16 offset:0
ds_read_b64_tr_b16 v[202:203],v16 offset:512
ds_read_b64_tr_b16 v[204:205],v16 offset:1024
ds_read_b64_tr_b16 v[206:207],v16 offset:1536
ds_read_b64_tr_b16 v[208:209],v16 offset:2048
ds_read_b64_tr_b16 v[210:211],v16 offset:2560
ds_read_b64_tr_b16 v[212:213],v16 offset:3072
ds_read_b64_tr_b16 v[214:215],v16 offset:3584
ds_read_b64_tr_b16 v[216:217],v16 offset:4096
ds_read_b64_tr_b16 v[218:219],v16 offset:4608
ds_read_b64_tr_b16 v[220:221],v16 offset:5120
ds_read_b64_tr_b16 v[222:223],v16 offset:5632
ds_read_b128 v[160:163],v18 offset:1152
ds_read_b128 v[164:167],v18 offset:1184
ds_read_b128 v[168:171],v18 offset:1216
ds_read_b128 v[172:175],v18 offset:1248
s_mov_b64 exec,s[62:63]
s_add_u32 m0,s68,55296
s_add_u32 s64,s59,1572864
buffer_load_dwordx4 v27,s[36:39],s64 offen lds
s_mov_b64 exec,-1
s_setprio 0
v_add_f32 v176,1.0,v176
v_add_f32 v177,1.0,v177
v_add_f32 v178,1.0,v178
v_add_f32 v179,1.0,v179
v_add_f32 v180,1.0,v180
v_add_f32 v181,1.0,v181
v_add_f32 v182,1.0,v182
v_add_f32 v183,1.0,v183
v_add_f32 v184,1.0,v184
v_add_f32 v185,1.0,v185
v_add_f32 v186,1.0,v186
v_add_f32 v187,1.0,v187
v_add_f32 v188,1.0,v188
v_add_f32 v189,1.0,v189
v_add_f32 v190,1.0,v190
v_add_f32 v191,1.0,v191
s_setprio 1
s_waitcnt lgkmcnt(0)
v_mfma_f32_32x32x16_bf16 v[160:175],v[200:203],v[72:75],v[160:175]
v_rcp_f32 v176,v176
v_rcp_f32 v177,v177
v_rcp_f32 v178,v178
v_mfma_f32_32x32x16_bf16 v[160:175],v[204:207],v[76:79],v[160:175]
v_rcp_f32 v179,v179
v_rcp_f32 v180,v180
v_rcp_f32 v181,v181
v_mfma_f32_32x32x16_bf16 v[160:175],v[208:211],v[80:83],v[160:175]
v_rcp_f32 v182,v182
v_rcp_f32 v183,v183
v_rcp_f32 v184,v184
v_mfma_f32_32x32x16_bf16 v[160:175],v[212:215],v[84:87],v[160:175]
v_rcp_f32 v185,v185
v_rcp_f32 v186,v186
v_rcp_f32 v187,v187
v_mfma_f32_32x32x16_bf16 v[160:175],v[216:219],v[88:91],v[160:175]
v_rcp_f32 v188,v188
v_rcp_f32 v189,v189
v_mfma_f32_32x32x16_bf16 v[160:175],v[220:223],v[92:95],v[160:175]
v_rcp_f32 v190,v190
v_rcp_f32 v191,v191
ds_read_b64_tr_b16 v[200:201],v17 offset:12288
ds_read_b64_tr_b16 v[202:203],v17 offset:13824
ds_read_b64_tr_b16 v[204:205],v17 offset:15360
ds_read_b64_tr_b16 v[206:207],v17 offset:16896
ds_read_b64_tr_b16 v[208:209],v17 offset:12352
ds_read_b64_tr_b16 v[210:211],v17 offset:13888
ds_read_b64_tr_b16 v[212:213],v17 offset:15424
ds_read_b64_tr_b16 v[214:215],v17 offset:16960
ds_read_b64_tr_b16 v[216:217],v17 offset:12416
ds_read_b64_tr_b16 v[218:219],v17 offset:13952
ds_read_b64_tr_b16 v[220:221],v17 offset:15488
ds_read_b64_tr_b16 v[222:223],v17 offset:17024
s_mov_b64 exec,s[62:63]
s_add_u32 m0,s68,58752
s_add_u32 s64,s59,1671168
buffer_load_dwordx4 v27,s[36:39],s64 offen lds
s_mov_b64 exec,-1
s_setprio 0
v_mul_f32 v176,v144,v176
v_mul_f32 v177,v145,v177
v_mul_f32 v178,v146,v178
v_mul_f32 v179,v147,v179
v_mul_f32 v180,v148,v180
v_mul_f32 v181,v149,v181
v_mul_f32 v182,v150,v182
v_mul_f32 v183,v151,v183
v_mul_f32 v184,v152,v184
v_mul_f32 v185,v153,v185
v_mul_f32 v186,v154,v186
v_mul_f32 v187,v155,v187
v_mul_f32 v188,v156,v188
v_mul_f32 v189,v157,v189
v_mul_f32 v190,v158,v190
v_mul_f32 v191,v159,v191
v_cvt_pk_bf16_f32 v192,v176,v177
v_cvt_pk_bf16_f32 v193,v178,v179
v_cvt_pk_bf16_f32 v194,v180,v181
v_cvt_pk_bf16_f32 v195,v182,v183
v_cvt_pk_bf16_f32 v196,v184,v185
v_cvt_pk_bf16_f32 v197,v186,v187
v_cvt_pk_bf16_f32 v198,v188,v189
v_cvt_pk_bf16_f32 v199,v190,v191
s_waitcnt lgkmcnt(0)
s_barrier
v_fmaak_f32 v176,v160,v160,0x409d30fc
v_fmaak_f32 v177,v161,v161,0x409d30fc
v_fmaak_f32 v178,v162,v162,0x409d30fc
v_fmaak_f32 v179,v163,v163,0x409d30fc
v_fmaak_f32 v180,v164,v164,0x409d30fc
v_fmaak_f32 v181,v165,v165,0x409d30fc
v_fmaak_f32 v182,v166,v166,0x409d30fc
v_fmaak_f32 v183,v167,v167,0x409d30fc
v_fmaak_f32 v184,v168,v168,0x409d30fc
v_fmaak_f32 v185,v169,v169,0x409d30fc
v_fmaak_f32 v186,v170,v170,0x409d30fc
v_fmaak_f32 v187,v171,v171,0x409d30fc
v_fmaak_f32 v188,v172,v172,0x409d30fc
v_fmaak_f32 v189,v173,v173,0x409d30fc
v_fmaak_f32 v190,v174,v174,0x409d30fc
v_fmaak_f32 v191,v175,v175,0x409d30fc
v_mul_f32 v176,v160,v176
v_mul_f32 v177,v161,v177
v_mul_f32 v178,v162,v178
v_mul_f32 v179,v163,v179
v_mul_f32 v180,v164,v180
v_mul_f32 v181,v165,v181
v_mul_f32 v182,v166,v182
v_mul_f32 v183,v167,v183
v_mul_f32 v184,v168,v184
v_mul_f32 v185,v169,v185
v_mul_f32 v186,v170,v186
v_mul_f32 v187,v171,v187
v_mul_f32 v188,v172,v188
v_mul_f32 v189,v173,v189
v_mul_f32 v190,v174,v190
v_mul_f32 v191,v175,v191
s_setprio 1
v_mfma_f32_32x32x16_bf16 v[96:111],v[200:203],v[192:195],v[96:111]
v_exp_f32 v176,v176
v_exp_f32 v177,v177
v_exp_f32 v178,v178
v_mfma_f32_32x32x16_bf16 v[96:111],v[204:207],v[196:199],v[96:111]
v_exp_f32 v179,v179
v_exp_f32 v180,v180
v_exp_f32 v181,v181
v_mfma_f32_32x32x16_bf16 v[112:127],v[208:211],v[192:195],v[112:127]
v_exp_f32 v182,v182
v_exp_f32 v183,v183
v_exp_f32 v184,v184
v_mfma_f32_32x32x16_bf16 v[112:127],v[212:215],v[196:199],v[112:127]
v_exp_f32 v185,v185
v_exp_f32 v186,v186
v_exp_f32 v187,v187
v_mfma_f32_32x32x16_bf16 v[128:143],v[216:219],v[192:195],v[128:143]
v_exp_f32 v188,v188
v_exp_f32 v189,v189
v_mfma_f32_32x32x16_bf16 v[128:143],v[220:223],v[196:199],v[128:143]
v_exp_f32 v190,v190
v_exp_f32 v191,v191
ds_read_b64_tr_b16 v[200:201],v16 offset:6144
ds_read_b64_tr_b16 v[202:203],v16 offset:6656
ds_read_b64_tr_b16 v[204:205],v16 offset:7168
ds_read_b64_tr_b16 v[206:207],v16 offset:7680
ds_read_b64_tr_b16 v[208:209],v16 offset:8192
ds_read_b64_tr_b16 v[210:211],v16 offset:8704
ds_read_b64_tr_b16 v[212:213],v16 offset:9216
ds_read_b64_tr_b16 v[214:215],v16 offset:9728
ds_read_b64_tr_b16 v[216:217],v16 offset:10240
ds_read_b64_tr_b16 v[218:219],v16 offset:10752
ds_read_b64_tr_b16 v[220:221],v16 offset:11264
ds_read_b64_tr_b16 v[222:223],v16 offset:11776
ds_read_b128 v[144:147],v18 offset:1280
ds_read_b128 v[148:151],v18 offset:1312
ds_read_b128 v[152:155],v18 offset:1344
ds_read_b128 v[156:159],v18 offset:1376
s_setprio 0
v_add_f32 v176,1.0,v176
v_add_f32 v177,1.0,v177
v_add_f32 v178,1.0,v178
v_add_f32 v179,1.0,v179
v_add_f32 v180,1.0,v180
v_add_f32 v181,1.0,v181
v_add_f32 v182,1.0,v182
v_add_f32 v183,1.0,v183
v_add_f32 v184,1.0,v184
v_add_f32 v185,1.0,v185
v_add_f32 v186,1.0,v186
v_add_f32 v187,1.0,v187
v_add_f32 v188,1.0,v188
v_add_f32 v189,1.0,v189
v_add_f32 v190,1.0,v190
v_add_f32 v191,1.0,v191
s_setprio 1
s_waitcnt lgkmcnt(0)
v_mfma_f32_32x32x16_bf16 v[144:159],v[200:203],v[72:75],v[144:159]
v_rcp_f32 v176,v176
v_rcp_f32 v177,v177
v_rcp_f32 v178,v178
v_mfma_f32_32x32x16_bf16 v[144:159],v[204:207],v[76:79],v[144:159]
v_rcp_f32 v179,v179
v_rcp_f32 v180,v180
v_rcp_f32 v181,v181
v_mfma_f32_32x32x16_bf16 v[144:159],v[208:211],v[80:83],v[144:159]
v_rcp_f32 v182,v182
v_rcp_f32 v183,v183
v_rcp_f32 v184,v184
v_mfma_f32_32x32x16_bf16 v[144:159],v[212:215],v[84:87],v[144:159]
v_rcp_f32 v185,v185
v_rcp_f32 v186,v186
v_rcp_f32 v187,v187
v_mfma_f32_32x32x16_bf16 v[144:159],v[216:219],v[88:91],v[144:159]
v_rcp_f32 v188,v188
v_rcp_f32 v189,v189
v_mfma_f32_32x32x16_bf16 v[144:159],v[220:223],v[92:95],v[144:159]
v_rcp_f32 v190,v190
v_rcp_f32 v191,v191
ds_read_b64_tr_b16 v[200:201],v17 offset:0
ds_read_b64_tr_b16 v[202:203],v17 offset:1536
ds_read_b64_tr_b16 v[204:205],v17 offset:3072
ds_read_b64_tr_b16 v[206:207],v17 offset:4608
ds_read_b64_tr_b16 v[208:209],v17 offset:64
ds_read_b64_tr_b16 v[210:211],v17 offset:1600
ds_read_b64_tr_b16 v[212:213],v17 offset:3136
ds_read_b64_tr_b16 v[214:215],v17 offset:4672
ds_read_b64_tr_b16 v[216:217],v17 offset:128
ds_read_b64_tr_b16 v[218:219],v17 offset:1664
ds_read_b64_tr_b16 v[220:221],v17 offset:3200
ds_read_b64_tr_b16 v[222:223],v17 offset:4736
s_setprio 0
v_mul_f32 v176,v160,v176
v_mul_f32 v177,v161,v177
v_mul_f32 v178,v162,v178
v_mul_f32 v179,v163,v179
v_mul_f32 v180,v164,v180
v_mul_f32 v181,v165,v181
v_mul_f32 v182,v166,v182
v_mul_f32 v183,v167,v183
v_mul_f32 v184,v168,v184
v_mul_f32 v185,v169,v185
v_mul_f32 v186,v170,v186
v_mul_f32 v187,v171,v187
v_mul_f32 v188,v172,v188
v_mul_f32 v189,v173,v189
v_mul_f32 v190,v174,v190
v_mul_f32 v191,v175,v191
v_cvt_pk_bf16_f32 v192,v176,v177
v_cvt_pk_bf16_f32 v193,v178,v179
v_cvt_pk_bf16_f32 v194,v180,v181
v_cvt_pk_bf16_f32 v195,v182,v183
v_cvt_pk_bf16_f32 v196,v184,v185
v_cvt_pk_bf16_f32 v197,v186,v187
v_cvt_pk_bf16_f32 v198,v188,v189
v_cvt_pk_bf16_f32 v199,v190,v191
s_waitcnt lgkmcnt(0)
s_barrier
v_fmaak_f32 v176,v144,v144,0x409d30fc
v_fmaak_f32 v177,v145,v145,0x409d30fc
v_fmaak_f32 v178,v146,v146,0x409d30fc
v_fmaak_f32 v179,v147,v147,0x409d30fc
v_fmaak_f32 v180,v148,v148,0x409d30fc
v_fmaak_f32 v181,v149,v149,0x409d30fc
v_fmaak_f32 v182,v150,v150,0x409d30fc
v_fmaak_f32 v183,v151,v151,0x409d30fc
v_fmaak_f32 v184,v152,v152,0x409d30fc
v_fmaak_f32 v185,v153,v153,0x409d30fc
v_fmaak_f32 v186,v154,v154,0x409d30fc
v_fmaak_f32 v187,v155,v155,0x409d30fc
v_fmaak_f32 v188,v156,v156,0x409d30fc
v_fmaak_f32 v189,v157,v157,0x409d30fc
v_fmaak_f32 v190,v158,v158,0x409d30fc
v_fmaak_f32 v191,v159,v159,0x409d30fc
v_mul_f32 v176,v144,v176
v_mul_f32 v177,v145,v177
v_mul_f32 v178,v146,v178
v_mul_f32 v179,v147,v179
v_mul_f32 v180,v148,v180
v_mul_f32 v181,v149,v181
v_mul_f32 v182,v150,v182
v_mul_f32 v183,v151,v183
v_mul_f32 v184,v152,v184
v_mul_f32 v185,v153,v185
v_mul_f32 v186,v154,v186
v_mul_f32 v187,v155,v187
v_mul_f32 v188,v156,v188
v_mul_f32 v189,v157,v189
v_mul_f32 v190,v158,v190
v_mul_f32 v191,v159,v191
s_setprio 1
v_mfma_f32_32x32x16_bf16 v[96:111],v[200:203],v[192:195],v[96:111]
v_exp_f32 v176,v176
v_exp_f32 v177,v177
v_exp_f32 v178,v178
v_mfma_f32_32x32x16_bf16 v[96:111],v[204:207],v[196:199],v[96:111]
v_exp_f32 v179,v179
v_exp_f32 v180,v180
v_exp_f32 v181,v181
v_mfma_f32_32x32x16_bf16 v[112:127],v[208:211],v[192:195],v[112:127]
v_exp_f32 v182,v182
v_exp_f32 v183,v183
v_exp_f32 v184,v184
v_mfma_f32_32x32x16_bf16 v[112:127],v[212:215],v[196:199],v[112:127]
v_exp_f32 v185,v185
v_exp_f32 v186,v186
v_exp_f32 v187,v187
v_mfma_f32_32x32x16_bf16 v[128:143],v[216:219],v[192:195],v[128:143]
v_exp_f32 v188,v188
v_exp_f32 v189,v189
v_mfma_f32_32x32x16_bf16 v[128:143],v[220:223],v[196:199],v[128:143]
v_exp_f32 v190,v190
v_exp_f32 v191,v191
ds_read_b64_tr_b16 v[200:201],v16 offset:12288
ds_read_b64_tr_b16 v[202:203],v16 offset:12800
ds_read_b64_tr_b16 v[204:205],v16 offset:13312
ds_read_b64_tr_b16 v[206:207],v16 offset:13824
ds_read_b64_tr_b16 v[208:209],v16 offset:14336
ds_read_b64_tr_b16 v[210:211],v16 offset:14848
ds_read_b64_tr_b16 v[212:213],v16 offset:15360
ds_read_b64_tr_b16 v[214:215],v16 offset:15872
ds_read_b64_tr_b16 v[216:217],v16 offset:16384
ds_read_b64_tr_b16 v[218:219],v16 offset:16896
ds_read_b64_tr_b16 v[220:221],v16 offset:17408
ds_read_b64_tr_b16 v[222:223],v16 offset:17920
ds_read_b128 v[160:163],v18 offset:1408
ds_read_b128 v[164:167],v18 offset:1440
ds_read_b128 v[168:171],v18 offset:1472
ds_read_b128 v[172:175],v18 offset:1504
s_setprio 0
v_add_f32 v176,1.0,v176
v_add_f32 v177,1.0,v177
v_add_f32 v178,1.0,v178
v_add_f32 v179,1.0,v179
v_add_f32 v180,1.0,v180
v_add_f32 v181,1.0,v181
v_add_f32 v182,1.0,v182
v_add_f32 v183,1.0,v183
v_add_f32 v184,1.0,v184
v_add_f32 v185,1.0,v185
v_add_f32 v186,1.0,v186
v_add_f32 v187,1.0,v187
v_add_f32 v188,1.0,v188
v_add_f32 v189,1.0,v189
v_add_f32 v190,1.0,v190
v_add_f32 v191,1.0,v191
s_setprio 1
s_waitcnt lgkmcnt(0)
v_mfma_f32_32x32x16_bf16 v[160:175],v[200:203],v[72:75],v[160:175]
v_rcp_f32 v176,v176
v_rcp_f32 v177,v177
v_rcp_f32 v178,v178
v_mfma_f32_32x32x16_bf16 v[160:175],v[204:207],v[76:79],v[160:175]
v_rcp_f32 v179,v179
v_rcp_f32 v180,v180
v_rcp_f32 v181,v181
v_mfma_f32_32x32x16_bf16 v[160:175],v[208:211],v[80:83],v[160:175]
v_rcp_f32 v182,v182
v_rcp_f32 v183,v183
v_rcp_f32 v184,v184
v_mfma_f32_32x32x16_bf16 v[160:175],v[212:215],v[84:87],v[160:175]
v_rcp_f32 v185,v185
v_rcp_f32 v186,v186
v_rcp_f32 v187,v187
v_mfma_f32_32x32x16_bf16 v[160:175],v[216:219],v[88:91],v[160:175]
v_rcp_f32 v188,v188
v_rcp_f32 v189,v189
v_mfma_f32_32x32x16_bf16 v[160:175],v[220:223],v[92:95],v[160:175]
v_rcp_f32 v190,v190
v_rcp_f32 v191,v191
ds_read_b64_tr_b16 v[200:201],v17 offset:6144
ds_read_b64_tr_b16 v[202:203],v17 offset:7680
ds_read_b64_tr_b16 v[204:205],v17 offset:9216
ds_read_b64_tr_b16 v[206:207],v17 offset:10752
ds_read_b64_tr_b16 v[208:209],v17 offset:6208
ds_read_b64_tr_b16 v[210:211],v17 offset:7744
ds_read_b64_tr_b16 v[212:213],v17 offset:9280
ds_read_b64_tr_b16 v[214:215],v17 offset:10816
ds_read_b64_tr_b16 v[216:217],v17 offset:6272
ds_read_b64_tr_b16 v[218:219],v17 offset:7808
ds_read_b64_tr_b16 v[220:221],v17 offset:9344
ds_read_b64_tr_b16 v[222:223],v17 offset:10880
s_setprio 0
v_mul_f32 v176,v144,v176
v_mul_f32 v177,v145,v177
v_mul_f32 v178,v146,v178
v_mul_f32 v179,v147,v179
v_mul_f32 v180,v148,v180
v_mul_f32 v181,v149,v181
v_mul_f32 v182,v150,v182
v_mul_f32 v183,v151,v183
v_mul_f32 v184,v152,v184
v_mul_f32 v185,v153,v185
v_mul_f32 v186,v154,v186
v_mul_f32 v187,v155,v187
v_mul_f32 v188,v156,v188
v_mul_f32 v189,v157,v189
v_mul_f32 v190,v158,v190
v_mul_f32 v191,v159,v191
v_cvt_pk_bf16_f32 v192,v176,v177
v_cvt_pk_bf16_f32 v193,v178,v179
v_cvt_pk_bf16_f32 v194,v180,v181
v_cvt_pk_bf16_f32 v195,v182,v183
v_cvt_pk_bf16_f32 v196,v184,v185
v_cvt_pk_bf16_f32 v197,v186,v187
v_cvt_pk_bf16_f32 v198,v188,v189
v_cvt_pk_bf16_f32 v199,v190,v191
s_waitcnt lgkmcnt(0)
s_barrier
v_fmaak_f32 v176,v160,v160,0x409d30fc
v_fmaak_f32 v177,v161,v161,0x409d30fc
v_fmaak_f32 v178,v162,v162,0x409d30fc
v_fmaak_f32 v179,v163,v163,0x409d30fc
v_fmaak_f32 v180,v164,v164,0x409d30fc
v_fmaak_f32 v181,v165,v165,0x409d30fc
v_fmaak_f32 v182,v166,v166,0x409d30fc
v_fmaak_f32 v183,v167,v167,0x409d30fc
v_fmaak_f32 v184,v168,v168,0x409d30fc
v_fmaak_f32 v185,v169,v169,0x409d30fc
v_fmaak_f32 v186,v170,v170,0x409d30fc
v_fmaak_f32 v187,v171,v171,0x409d30fc
v_fmaak_f32 v188,v172,v172,0x409d30fc
v_fmaak_f32 v189,v173,v173,0x409d30fc
v_fmaak_f32 v190,v174,v174,0x409d30fc
v_fmaak_f32 v191,v175,v175,0x409d30fc
v_mul_f32 v176,v160,v176
v_mul_f32 v177,v161,v177
v_mul_f32 v178,v162,v178
v_mul_f32 v179,v163,v179
v_mul_f32 v180,v164,v180
v_mul_f32 v181,v165,v181
v_mul_f32 v182,v166,v182
v_mul_f32 v183,v167,v183
v_mul_f32 v184,v168,v184
v_mul_f32 v185,v169,v185
v_mul_f32 v186,v170,v186
v_mul_f32 v187,v171,v187
v_mul_f32 v188,v172,v188
v_mul_f32 v189,v173,v189
v_mul_f32 v190,v174,v190
v_mul_f32 v191,v175,v191
s_setprio 1
v_mfma_f32_32x32x16_bf16 v[96:111],v[200:203],v[192:195],v[96:111]
v_exp_f32 v176,v176
v_exp_f32 v177,v177
v_exp_f32 v178,v178
v_mfma_f32_32x32x16_bf16 v[96:111],v[204:207],v[196:199],v[96:111]
v_exp_f32 v179,v179
v_exp_f32 v180,v180
v_exp_f32 v181,v181
v_mfma_f32_32x32x16_bf16 v[112:127],v[208:211],v[192:195],v[112:127]
v_exp_f32 v182,v182
v_exp_f32 v183,v183
v_exp_f32 v184,v184
v_mfma_f32_32x32x16_bf16 v[112:127],v[212:215],v[196:199],v[112:127]
v_exp_f32 v185,v185
v_exp_f32 v186,v186
v_exp_f32 v187,v187
v_mfma_f32_32x32x16_bf16 v[128:143],v[216:219],v[192:195],v[128:143]
v_exp_f32 v188,v188
v_exp_f32 v189,v189
v_mfma_f32_32x32x16_bf16 v[128:143],v[220:223],v[196:199],v[128:143]
v_exp_f32 v190,v190
v_exp_f32 v191,v191
ds_read_b64 v[72:73],v20 offset:0
ds_read_b64 v[74:75],v21 offset:0
ds_read_b64 v[76:77],v22 offset:0
ds_read_b64 v[78:79],v23 offset:0
ds_read_b64 v[80:81],v20 offset:64
ds_read_b64 v[82:83],v21 offset:64
ds_read_b64 v[84:85],v22 offset:64
ds_read_b64 v[86:87],v23 offset:64
ds_read_b64 v[88:89],v20 offset:128
ds_read_b64 v[90:91],v21 offset:128
ds_read_b64 v[92:93],v22 offset:128
ds_read_b64 v[94:95],v23 offset:128
s_setprio 0
v_add_f32 v176,1.0,v176
v_add_f32 v177,1.0,v177
v_add_f32 v178,1.0,v178
v_add_f32 v179,1.0,v179
v_add_f32 v180,1.0,v180
v_add_f32 v181,1.0,v181
v_add_f32 v182,1.0,v182
v_add_f32 v183,1.0,v183
v_add_f32 v184,1.0,v184
v_add_f32 v185,1.0,v185
v_add_f32 v186,1.0,v186
v_add_f32 v187,1.0,v187
v_add_f32 v188,1.0,v188
v_add_f32 v189,1.0,v189
v_add_f32 v190,1.0,v190
v_add_f32 v191,1.0,v191
s_setprio 1
s_waitcnt lgkmcnt(0)
v_rcp_f32 v176,v176
v_rcp_f32 v177,v177
v_rcp_f32 v178,v178
v_rcp_f32 v179,v179
v_rcp_f32 v180,v180
v_rcp_f32 v181,v181
v_rcp_f32 v182,v182
v_rcp_f32 v183,v183
v_rcp_f32 v184,v184
v_rcp_f32 v185,v185
v_rcp_f32 v186,v186
v_rcp_f32 v187,v187
v_rcp_f32 v188,v188
v_rcp_f32 v189,v189
v_rcp_f32 v190,v190
v_rcp_f32 v191,v191
ds_read_b64_tr_b16 v[200:201],v17 offset:12288
ds_read_b64_tr_b16 v[202:203],v17 offset:13824
ds_read_b64_tr_b16 v[204:205],v17 offset:15360
ds_read_b64_tr_b16 v[206:207],v17 offset:16896
ds_read_b64_tr_b16 v[208:209],v17 offset:12352
ds_read_b64_tr_b16 v[210:211],v17 offset:13888
ds_read_b64_tr_b16 v[212:213],v17 offset:15424
ds_read_b64_tr_b16 v[214:215],v17 offset:16960
ds_read_b64_tr_b16 v[216:217],v17 offset:12416
ds_read_b64_tr_b16 v[218:219],v17 offset:13952
ds_read_b64_tr_b16 v[220:221],v17 offset:15488
ds_read_b64_tr_b16 v[222:223],v17 offset:17024
s_setprio 0
v_mul_f32 v176,v160,v176
v_mul_f32 v177,v161,v177
v_mul_f32 v178,v162,v178
v_mul_f32 v179,v163,v179
v_mul_f32 v180,v164,v180
v_mul_f32 v181,v165,v181
v_mul_f32 v182,v166,v182
v_mul_f32 v183,v167,v183
v_mul_f32 v184,v168,v184
v_mul_f32 v185,v169,v185
v_mul_f32 v186,v170,v186
v_mul_f32 v187,v171,v187
v_mul_f32 v188,v172,v188
v_mul_f32 v189,v173,v189
v_mul_f32 v190,v174,v190
v_mul_f32 v191,v175,v191
v_cvt_pk_bf16_f32 v192,v176,v177
v_cvt_pk_bf16_f32 v193,v178,v179
v_cvt_pk_bf16_f32 v194,v180,v181
v_cvt_pk_bf16_f32 v195,v182,v183
v_cvt_pk_bf16_f32 v196,v184,v185
v_cvt_pk_bf16_f32 v197,v186,v187
v_cvt_pk_bf16_f32 v198,v188,v189
v_cvt_pk_bf16_f32 v199,v190,v191
ds_read_b128 v[144:147],v18 offset:1536
ds_read_b128 v[148:151],v18 offset:1568
ds_read_b128 v[152:155],v18 offset:1600
ds_read_b128 v[156:159],v18 offset:1632
ds_read_b128 v[160:163],v18 offset:1664
ds_read_b128 v[164:167],v18 offset:1696
ds_read_b128 v[168:171],v18 offset:1728
ds_read_b128 v[172:175],v18 offset:1760
ds_read_b128 v[176:179],v18 offset:1792
ds_read_b128 v[180:183],v18 offset:1824
ds_read_b128 v[184:187],v18 offset:1856
ds_read_b128 v[188:191],v18 offset:1888
s_waitcnt lgkmcnt(12)
s_setprio 1
v_mfma_f32_32x32x16_bf16 v[96:111],v[200:203],v[192:195],v[96:111]
v_lshlrev_b32 v232,16,v72
v_lshlrev_b32 v233,16,v73
v_lshlrev_b32 v234,16,v74
v_lshlrev_b32 v235,16,v75
v_mfma_f32_32x32x16_bf16 v[96:111],v[204:207],v[196:199],v[96:111]
v_lshlrev_b32 v236,16,v76
v_lshlrev_b32 v237,16,v77
v_lshlrev_b32 v238,16,v78
v_lshlrev_b32 v239,16,v79
v_mfma_f32_32x32x16_bf16 v[112:127],v[208:211],v[192:195],v[112:127]
v_lshlrev_b32 v240,16,v80
v_lshlrev_b32 v241,16,v81
v_lshlrev_b32 v242,16,v82
v_lshlrev_b32 v243,16,v83
v_mfma_f32_32x32x16_bf16 v[112:127],v[212:215],v[196:199],v[112:127]
v_lshlrev_b32 v244,16,v84
v_lshlrev_b32 v245,16,v85
v_lshlrev_b32 v246,16,v86
v_lshlrev_b32 v247,16,v87
v_mfma_f32_32x32x16_bf16 v[128:143],v[216:219],v[192:195],v[128:143]
v_lshlrev_b32 v248,16,v88
v_lshlrev_b32 v249,16,v89
v_lshlrev_b32 v250,16,v90
v_lshlrev_b32 v251,16,v91
v_mfma_f32_32x32x16_bf16 v[128:143],v[220:223],v[196:199],v[128:143]
v_lshlrev_b32 v252,16,v92
v_lshlrev_b32 v253,16,v93
v_lshlrev_b32 v254,16,v94
v_lshlrev_b32 v255,16,v95
s_setprio 0
s_waitcnt lgkmcnt(0)
v_and_b32 v72,0xffff0000,v72
v_and_b32 v73,0xffff0000,v73
v_and_b32 v74,0xffff0000,v74
v_and_b32 v75,0xffff0000,v75
v_and_b32 v76,0xffff0000,v76
v_and_b32 v77,0xffff0000,v77
v_and_b32 v78,0xffff0000,v78
v_and_b32 v79,0xffff0000,v79
v_and_b32 v80,0xffff0000,v80
v_and_b32 v81,0xffff0000,v81
v_and_b32 v82,0xffff0000,v82
v_and_b32 v83,0xffff0000,v83
v_and_b32 v84,0xffff0000,v84
v_and_b32 v85,0xffff0000,v85
v_and_b32 v86,0xffff0000,v86
v_and_b32 v87,0xffff0000,v87
v_and_b32 v88,0xffff0000,v88
v_and_b32 v89,0xffff0000,v89
v_and_b32 v90,0xffff0000,v90
v_and_b32 v91,0xffff0000,v91
v_and_b32 v92,0xffff0000,v92
v_and_b32 v93,0xffff0000,v93
v_and_b32 v94,0xffff0000,v94
v_and_b32 v95,0xffff0000,v95
v_fma_f32 v232,v96,v144,v232
v_fma_f32 v72,v97,v145,v72
v_fma_f32 v233,v98,v146,v233
v_fma_f32 v73,v99,v147,v73
v_fma_f32 v234,v100,v148,v234
v_fma_f32 v74,v101,v149,v74
v_fma_f32 v235,v102,v150,v235
v_fma_f32 v75,v103,v151,v75
v_fma_f32 v236,v104,v152,v236
v_fma_f32 v76,v105,v153,v76
v_fma_f32 v237,v106,v154,v237
v_fma_f32 v77,v107,v155,v77
v_fma_f32 v238,v108,v156,v238
v_fma_f32 v78,v109,v157,v78
v_fma_f32 v239,v110,v158,v239
v_fma_f32 v79,v111,v159,v79
v_fma_f32 v240,v112,v160,v240
v_fma_f32 v80,v113,v161,v80
v_fma_f32 v241,v114,v162,v241
v_fma_f32 v81,v115,v163,v81
v_fma_f32 v242,v116,v164,v242
v_fma_f32 v82,v117,v165,v82
v_fma_f32 v243,v118,v166,v243
v_fma_f32 v83,v119,v167,v83
v_fma_f32 v244,v120,v168,v244
v_fma_f32 v84,v121,v169,v84
v_fma_f32 v245,v122,v170,v245
v_fma_f32 v85,v123,v171,v85
v_fma_f32 v246,v124,v172,v246
v_fma_f32 v86,v125,v173,v86
v_fma_f32 v247,v126,v174,v247
v_fma_f32 v87,v127,v175,v87
v_fma_f32 v248,v128,v176,v248
v_fma_f32 v88,v129,v177,v88
v_fma_f32 v249,v130,v178,v249
v_fma_f32 v89,v131,v179,v89
v_fma_f32 v250,v132,v180,v250
v_fma_f32 v90,v133,v181,v90
v_fma_f32 v251,v134,v182,v251
v_fma_f32 v91,v135,v183,v91
v_fma_f32 v252,v136,v184,v252
v_fma_f32 v92,v137,v185,v92
v_fma_f32 v253,v138,v186,v253
v_fma_f32 v93,v139,v187,v93
v_fma_f32 v254,v140,v188,v254
v_fma_f32 v94,v141,v189,v94
v_fma_f32 v255,v142,v190,v255
v_fma_f32 v95,v143,v191,v95
v_add_u32 v4,s54,v4
v_add_u32 v5,s54,v5
v_add_u32 v6,s54,v6
v_add_u32 v7,s54,v7
v_add_u32 v8,s54,v8
v_add_u32 v9,s54,v9
v_add_u32 v10,s54,v10
v_add_u32 v11,s54,v11
v_add_u32 v12,s54,v12
v_add_u32 v13,s54,v13
v_add_u32 v14,s54,v14
v_add_u32 v15,s54,v15
v_cvt_pk_bf16_f32 v200,v232,v72
v_cvt_pk_bf16_f32 v201,v233,v73
v_cvt_pk_bf16_f32 v202,v234,v74
v_cvt_pk_bf16_f32 v203,v235,v75
v_cvt_pk_bf16_f32 v204,v236,v76
v_cvt_pk_bf16_f32 v205,v237,v77
v_cvt_pk_bf16_f32 v206,v238,v78
v_cvt_pk_bf16_f32 v207,v239,v79
v_cvt_pk_bf16_f32 v208,v240,v80
v_cvt_pk_bf16_f32 v209,v241,v81
v_cvt_pk_bf16_f32 v210,v242,v82
v_cvt_pk_bf16_f32 v211,v243,v83
v_cvt_pk_bf16_f32 v212,v244,v84
v_cvt_pk_bf16_f32 v213,v245,v85
v_cvt_pk_bf16_f32 v214,v246,v86
v_cvt_pk_bf16_f32 v215,v247,v87
v_cvt_pk_bf16_f32 v216,v248,v88
v_cvt_pk_bf16_f32 v217,v249,v89
v_cvt_pk_bf16_f32 v218,v250,v90
v_cvt_pk_bf16_f32 v219,v251,v91
v_cvt_pk_bf16_f32 v220,v252,v92
v_cvt_pk_bf16_f32 v221,v253,v93
v_cvt_pk_bf16_f32 v222,v254,v94
v_cvt_pk_bf16_f32 v223,v255,v95
ds_write_b64 v20,v[200:201] offset:0
ds_write_b64 v21,v[202:203] offset:0
ds_write_b64 v22,v[204:205] offset:0
ds_write_b64 v23,v[206:207] offset:0
ds_write_b64 v20,v[208:209] offset:64
ds_write_b64 v21,v[210:211] offset:64
ds_write_b64 v22,v[212:213] offset:64
ds_write_b64 v23,v[214:215] offset:64
ds_write_b64 v20,v[216:217] offset:128
ds_write_b64 v21,v[218:219] offset:128
ds_write_b64 v22,v[220:221] offset:128
ds_write_b64 v23,v[222:223] offset:128
ds_read_b128 v[72:75],v19 offset:0
ds_read_b128 v[76:79],v19 offset:1024
ds_read_b128 v[80:83],v19 offset:2048
ds_read_b128 v[84:87],v19 offset:3456
ds_read_b128 v[88:91],v19 offset:4480
ds_read_b128 v[92:95],v19 offset:5504
v_add_u32 v20,s54,v20
v_add_u32 v21,s54,v21
v_add_u32 v22,s54,v22
v_add_u32 v23,s54,v23
s_sub_u32 s68,s68,s54
s_add_u32 s59,s59,1572864
s_add_u32 s53,s53,1
s_cmp_eq_u32 s53,3
s_cselect_b32 s59,0x70000000,s59
s_add_u32 s67,s69,98304
s_waitcnt lgkmcnt(5)
buffer_store_dwordx4 v[72:75],v24,s[40:43],s69 offen nt
s_waitcnt lgkmcnt(4)
buffer_store_dwordx4 v[76:79],v25,s[40:43],s69 offen nt
s_waitcnt lgkmcnt(3)
buffer_store_dwordx4 v[80:83],v26,s[40:43],s69 offen nt
s_waitcnt lgkmcnt(2)
buffer_store_dwordx4 v[84:87],v24,s[40:43],s67 offen nt
s_waitcnt lgkmcnt(1)
buffer_store_dwordx4 v[88:91],v25,s[40:43],s67 offen nt
s_waitcnt lgkmcnt(0)
buffer_store_dwordx4 v[92:95],v26,s[40:43],s67 offen nt
v_add_u32 v19,s54,v19
s_add_u32 s69,s69,1572864
s_sub_u32 s54,0,s54
s_cmp_lt_u32 s53,4
s_cbranch_scc1 L_pass1_%=
L_end_%=:
s_waitcnt vmcnt(0) lgkmcnt(0)
s_endpgm
.p2align 12
)ASM"
    :: "s"(blockIdx.x), "v"(tid), "s"((unsigned long long)__builtin_amdgcn_kernarg_segment_ptr())
    : "v1", "v2", "v3", "v4", "v5", "v6", "v7", "v8", "v9", "v10", "v11", "v12", "v13", "v14", "v15", "v16", "v17", "v18", "v19", "v20", "v21", "v22", "v23", "v24", "v25", "v26", "v27", "v28", "v29", "v30", "v31", "v32", "v33", "v34", "v35", "v36", "v37", "v38", "v39", "v40", "v41", "v42", "v43", "v44", "v45", "v46", "v47", "v48", "v49", "v50", "v51", "v52", "v53", "v54", "v55", "v56", "v57", "v58", "v59", "v60", "v61", "v62", "v63", "v64", "v65", "v66", "v67", "v68", "v69", "v70", "v71", "v72", "v73", "v74", "v75", "v76", "v77", "v78", "v79", "v80", "v81", "v82", "v83", "v84", "v85", "v86", "v87", "v88", "v89", "v90", "v91", "v92", "v93", "v94", "v95", "v96", "v97", "v98", "v99", "v100", "v101", "v102", "v103", "v104", "v105", "v106", "v107", "v108", "v109", "v110", "v111", "v112", "v113", "v114", "v115", "v116", "v117", "v118", "v119", "v120", "v121", "v122", "v123", "v124", "v125", "v126", "v127", "v128", "v129", "v130", "v131", "v132", "v133", "v134", "v135", "v136", "v137", "v138", "v139", "v140", "v141", "v142", "v143", "v144", "v145", "v146", "v147", "v148", "v149", "v150", "v151", "v152", "v153", "v154", "v155", "v156", "v157", "v158", "v159", "v160", "v161", "v162", "v163", "v164", "v165", "v166", "v167", "v168", "v169", "v170", "v171", "v172", "v173", "v174", "v175", "v176", "v177", "v178", "v179", "v180", "v181", "v182", "v183", "v184", "v185", "v186", "v187", "v188", "v189", "v190", "v191", "v192", "v193", "v194", "v195", "v196", "v197", "v198", "v199", "v200", "v201", "v202", "v203", "v204", "v205", "v206", "v207", "v208", "v209", "v210", "v211", "v212", "v213", "v214", "v215", "v216", "v217", "v218", "v219", "v220", "v221", "v222", "v223", "v224", "v225", "v226", "v227", "v228", "v229", "v230", "v231", "v232", "v233", "v234", "v235", "v236", "v237", "v238", "v239", "v240", "v241", "v242", "v243", "v244", "v245", "v246", "v247", "v248", "v249", "v250", "v251", "v252", "v253", "v254", "v255", "s4", "s5", "s6", "s7", "s8", "s9", "s10", "s11", "s12", "s13", "s14", "s15", "s16", "s17", "s18", "s19", "s20", "s21", "s22", "s23", "s24", "s25", "s26", "s27", "s28", "s29", "s30", "s31", "s32", "s33", "s34", "s35", "s36", "s37", "s38", "s39", "s40", "s41", "s42", "s43", "s44", "s45", "s46", "s47", "s48", "s49", "s50", "s51", "s52", "s53", "s54", "s55", "s56", "s57", "s58", "s59", "s60", "s61", "s62", "s63", "s64", "s65", "s66", "s67", "s68", "s69", "s70", "s71", "s72", "s73", "s74", "s75", "s76", "s77", "s78", "s79", "s80", "s81", "s82", "s83", "s84", "s85", "s86", "s87", "s88", "s89", "s90", "s91", "s92", "s93", "s94", "s95", "s96", "s97", "s98", "s99", "s100", "s101", "vcc", "scc", "memory");
}

extern "C" void kernel_launch(void* const* d_in, const int* in_sizes, int n_in,
                              void* d_out, int out_size, void* d_ws, size_t ws_size,
                              hipStream_t stream) {
    (void)hipFuncSetAttribute((const void*)convnext_kernel, hipFuncAttributeMaxDynamicSharedMemorySize, LDS_BYTES);
    convnext_kernel<<<256, 512, LDS_BYTES, stream>>>(
        (const unsigned short*)d_in[0], (const unsigned short*)d_in[1], (const unsigned short*)d_in[2], (const unsigned short*)d_in[3],
        (const unsigned short*)d_in[4], (const unsigned short*)d_in[5], (const unsigned short*)d_in[6], (unsigned short*)d_out);
}
```

```cpp
#include <hip/hip_runtime.h>
#include <stdint.h>

constexpr int LDS_BYTES = 163584;

__global__ __launch_bounds__(512, 2) void convnext_kernel(
    const unsigned short* __restrict__ x, const unsigned short* __restrict__ dw, const unsigned short* __restrict__ w1,
    const unsigned short* __restrict__ b1, const unsigned short* __restrict__ w2, const unsigned short* __restrict__ b2,
    const unsigned short* __restrict__ gamma, unsigned short* __restrict__ out) {
    const unsigned tid = threadIdx.x;
    asm volatile(
R"ASM(
N_main_%=:
s_load_dwordx16 s[20:35],%2,0
v_lshrrev_b32 v236,6,%1
v_and_b32 v232,63,%1
v_readfirstlane_b32 s52,v236
s_getpc_b64 s[14:15]
N_ep_%=:
v_lshlrev_b32 v252,7,v232
v_lshrrev_b32 v253,8,%1
v_mul_u32_u24 v253,N_B_%=-N_ep_%=,v253
v_add_u32 v252,v252,v253
global_load_dword v254,v252,s[14:15]
v_mov_b32 v228,0
v_mov_b32 v229,0
v_mov_b32 v230,0
v_mov_b32 v231,0
s_and_b32 s64,%0,7
s_lshr_b32 s70,%0,3
s_lshl_b32 s71,s64,6
s_mul_i32 s68,s70,3072
s_and_b32 s64,s52,3
s_mul_i32 s67,s64,54
s_cmp_eq_u32 s70,0
s_cselect_b32 s66,0,99
s_cmp_eq_u32 s70,31
s_cselect_b32 s66,17,s66
v_add_u32 v236,s67,v232
s_mul_i32 s57,s64,864
s_add_u32 s57,s57,73728
v_mul_u32_u24 v237,0xaaab,v236
s_sub_u32 s65,s71,1
v_lshrrev_b32 v237,19,v237
s_mul_i32 s56,s65,98304
v_mul_u32_u24 v238,12,v237
s_add_u32 s56,s56,s68
v_sub_u32 v238,v236,v238
s_cmp_lt_i32 s65,0
s_cselect_b32 s101,0x70000000,s56
v_add_u32 v236,1,v237
s_mov_b32 s62,-1
s_mov_b32 s63,0x3fffff
v_bfe_u32 v236,v236,2,2
s_mov_b32 s38,50331840
s_mov_b32 s39,0x00020000
v_xor_b32 v238,v238,v236
s_mov_b32 s96,0x8befe000
s_mov_b32 s99,0x72800000
v_mul_u32_u24 v236,192,v237
s_mov_b32 s98,0xffff0000
s_bitcmp1_b32 s52,1
s_cselect_b64 vcc,-1,0
v_lshl_add_u32 v241,v238,4,v236
v_cmp_eq_u32_e64 s[92:93],s66,v237
v_mov_b32 v236,0x7fff0000
s_nop 0
v_cndmask_b32_e64 v241,v241,v236,s[92:93]
s_cmp_ge_u32 s52,4
s_cbranch_scc1 N_B_%=
s_setprio 3
s_and_b32 s66,s52,3
s_cmp_eq_u32 s66,3
s_cselect_b32 s14,1,s66
v_min_u32 v236,47,v232
s_mul_i32 s64,s14,192
s_lshl_b32 s65,s14,7
v_lshl_add_u32 v200,v236,2,s64
s_add_u32 s65,s65,144256
v_and_b32 v199,255,%1
v_min_u32 v237,191,v199
v_lshlrev_b32 v201,2,v237
v_min_u32 v249,95,v199
v_lshlrev_b32 v202,1,v249
s_lshl_b32 s64,s66,3
s_lshr_b32 s67,0x30011100,s64
s_and_b32 s67,s67,0xff
s_lshl_b32 s67,s67,3
s_add_u32 s67,s67,143488
v_lshrrev_b32 v238,2,v236
v_and_b32 v239,3,v236
v_mul_u32_u24 v238,1152,v238
v_lshl_add_u32 v238,v239,5,v238
s_and_b32 s66,s66,1
s_lshl_b32 s64,s66,3
s_cmp_eq_u32 s66,0
s_movk_i32 s66,128
s_cselect_b32 s66,640,s66
v_add_u32 v203,s65,v238
v_add_u32 v209,s67,v238
v_lshl_add_u32 v204,v239,2,v203
v_add_u32 v215,s66,v209
v_xor_b32 v237,s64,v209
v_lshrrev_b32 v239,1,v239
v_xor_b32 v238,s64,v215
ds_write_b128 v203,v[228:231] offset:0
ds_write_b128 v203,v[228:231] offset:16
ds_write2_b64 v209,v[228:229],v[228:229] offset0:0 offset1:2
ds_write2_b64 v215,v[228:229],v[228:229] offset0:0 offset1:2
ds_write2_b64 v237,v[228:229],v[228:229] offset0:0 offset1:2
ds_write2_b64 v238,v[228:229],v[228:229] offset0:0 offset1:2
v_lshl_add_u32 v209,v239,2,v209
v_lshl_add_u32 v215,v239,2,v215
ds_write_b32 v228,v228 offset:11648
v_lshrrev_b32 v236,5,v249
v_bfe_u32 v237,v249,2,1
v_and_b32 v238,3,v249
v_lshlrev_b32 v206,7,v236
v_lshl_add_u32 v206,v237,5,v206
v_lshl_add_u32 v206,v238,2,v206
v_bfe_u32 v239,v249,3,1
v_lshl_add_u32 v206,v239,6,v206
v_bfe_u32 v239,v249,4,1
v_lshl_add_u32 v206,v239,4,v206
v_add_u32 v206,9728,v206
v_lshl_add_u32 v207,v236,1,v237
v_lshlrev_b32 v207,4,v207
v_bfe_u32 v239,v249,3,2
v_lshl_add_u32 v207,v239,2,v207
v_add_u32 v207,v207,v238
v_lshlrev_b32 v207,4,v207
v_add_u32 v207,10112,v207
v_lshlrev_b32 v208,4,v238
v_cmp_eq_u32_e64 s[48:49],1,v237
v_lshlrev_b32 v205,1,v201
v_add_u32 v205,8192,v205
v_mov_b32 v221,0
v_mov_b32 v222,0x3f800000
s_waitcnt lgkmcnt(0)
s_sub_u32 s36,s20,192
s_subb_u32 s37,s21,0
s_and_b32 s37,s37,0xffff
s_bitcmp0_b32 s52,0
s_cselect_b64 s[44:45],s[24:25],s[28:29]
s_and_b32 s45,s45,0xffff
s_mov_b32 s40,s34
s_and_b32 s41,s35,0xffff
global_load_dword v242,v200,s[22:23] offset:0
global_load_dword v243,v200,s[22:23] offset:576
global_load_dword v244,v200,s[22:23] offset:1152
global_load_ushort v247,v202,s[32:33]
global_load_dword v245,v201,s[26:27]
global_load_ushort v246,v202,s[30:31]
s_mov_b64 exec,s[62:63]
s_add_u32 m0,s57,0
s_nop 0
buffer_load_dwordx4 v241,s[36:39],s101 offen lds
s_add_u32 m0,s57,3488
s_add_u32 s101,s56,98304
buffer_load_dwordx4 v241,s[36:39],s101 offen lds
s_add_u32 m0,s57,6976
s_add_u32 s101,s56,196608
buffer_load_dwordx4 v241,s[36:39],s101 offen lds
s_add_u32 m0,s57,10464
s_add_u32 s101,s56,294912
buffer_load_dwordx4 v241,s[36:39],s101 offen lds
s_add_u32 m0,s57,13952
s_add_u32 s101,s56,393216
buffer_load_dwordx4 v241,s[36:39],s101 offen lds
s_add_u32 m0,s57,17440
s_add_u32 s101,s56,491520
buffer_load_dwordx4 v241,s[36:39],s101 offen lds
s_add_u32 m0,s57,20928
s_add_u32 s101,s56,589824
buffer_load_dwordx4 v241,s[36:39],s101 offen lds
s_add_u32 m0,s57,24416
s_add_u32 s101,s56,688128
buffer_load_dwordx4 v241,s[36:39],s101 offen lds
s_add_u32 m0,s57,27904
s_add_u32 s101,s56,786432
buffer_load_dwordx4 v241,s[36:39],s101 offen lds
s_add_u32 m0,s57,31392
s_add_u32 s101,s56,884736
buffer_load_dwordx4 v241,s[36:39],s101 offen lds
s_mov_b64 exec,-1
s_and_b32 s64,s52,3
s_lshl_b32 s10,s64,2
s_add_u32 s64,s10,s71
s_mul_i32 s61,s64,98304
s_add_u32 s61,s61,s68
s_mov_b32 s54,0x44ea44ea
s_mov_b32 s55,0x877f877f
s_mov_b32 s8,0x0ff00ff0
s_mov_b32 s9,0x0ff00ff0
s_and_b32 s64,s52,3
s_mul_i32 s67,s64,54
s_cmp_eq_u32 s70,31
s_cselect_b32 s66,17,99
s_mov_b32 s12,0xaaaaaaaa
s_mov_b32 s13,0xaaaaaaaa
s_mov_b32 s42,50331648
s_mov_b32 s43,0x00020000
s_bitcmp0_b32 s52,0
s_mov_b32 s58,40896
s_cselect_b32 s58,16256,s58
s_cselect_b32 s59,64,6144
s_movk_i32 s53,1024
s_cselect_b32 s53,6144,s53
s_cselect_b32 s60,0,67584
s_cselect_b32 s97,64,0
s_cselect_b32 s69,0,0x70000000
s_mov_b32 s100,73728
s_cselect_b32 s100,110688,s100
s_cselect_b32 s64,16,0
s_movk_i32 s50,1032
s_cselect_b32 s50,1024,s50
s_cselect_b32 s51,s53,-72704
s_mov_b32 s46,73728
s_mov_b32 s47,0x00020000
v_mov_b32 v1,0
v_lshlrev_b32 v233,4,v232
v_lshlrev_b32 v234,3,%1
v_add_u32 v233,157312,v233
v_add_u32 v234,157312,v234
s_waitcnt vmcnt(10)
v_and_b32 v212,0xffff,v242
v_and_b32 v242,s98,v242
ds_write2_b32 v209,v212,v242 offset0:0 offset1:4
v_and_b32 v213,0xffff,v243
v_and_b32 v243,s98,v243
ds_write2_b32 v215,v213,v243 offset0:0 offset1:4
v_and_b32 v214,0xffff,v244
v_and_b32 v244,s98,v244
ds_write2_b32 v204,v214,v244 offset0:0 offset1:4
v_lshlrev_b32 v247,16,v247
v_lshlrev_b32 v246,16,v246
v_mul_f32 v247,0x6c800000,v247
v_mul_f32 v246,0x12800000,v246
v_rcp_f32 v224,v247
v_lshlrev_b32 v210,16,v245
v_and_b32 v211,s98,v245
v_med3_f32 v224,v224,v222,-v222
v_pk_mul_f32 v[210:211],v[210:211],s[96:97] op_sel_hi:[1,0]
v_cvt_pk_bf16_f32 v220,v224,v228
ds_write_b64 v205,v[210:211]
ds_write_b32 v206,v246 offset:2048
v_lshlrev_b32 v222,16,v220
v_rcp_f32 v222,v222
v_lshlrev_b64 v[220:221],v208,v[220:221]
v_cndmask_b32_e64 v224,v220,0,s[48:49]
v_cndmask_b32_e64 v226,0,v220,s[48:49]
v_cndmask_b32_e64 v225,v221,0,s[48:49]
v_cndmask_b32_e64 v227,0,v221,s[48:49]
ds_write_b128 v207,v[224:227]
ds_write_b32 v206,v222
s_waitcnt lgkmcnt(0)
s_barrier
s_lshl_b32 s4,s52,1
s_and_b32 s5,s52,1
s_sub_u32 s4,s4,s5
s_sub_u32 s6,s4,s10
s_mul_i32 s4,s4,3488
s_mul_i32 s6,s6,98304
s_add_u32 s49,s61,s6
v_mov_b32 v124,s4
s_mov_b64 exec,s[8:9]
v_add_u32 v124,6976,v124
s_mov_b64 exec,-1
ds_read_b128 v[12:15],v233 offset:2048
ds_read_b128 v[8:11],v233 offset:1024
ds_read_b128 v[4:7],v233 offset:0
ds_read_b64 v[2:3],v234 offset:3072
s_waitcnt lgkmcnt(2)
v_xad_u32 v125,v15,16,v124
v_xad_u32 v126,v15,32,v124
v_xad_u32 v127,v15,48,v124
v_add_u32 v124,v15,v124
ds_read_b128 v[248:251],v11 offset:768
ds_read_b128 v[252:255],v12 offset:0
ds_read_b128 v[112:115],v11 offset:1920
ds_read_b128 v[116:119],v12 offset:1152
ds_read_b128 v[64:67],v11 offset:5376
ds_read_b128 v[68:71],v12 offset:4608
ds_read_b128 v[128:131],v11 offset:6528
ds_read_b128 v[132:135],v12 offset:5760
ds_read_b128 v[136:139],v11 offset:9984
ds_read_b128 v[140:143],v12 offset:9216
ds_read_b128 v[72:75],v11 offset:11136
ds_read_b128 v[76:79],v12 offset:10368
ds_read_b128 v[80:83],v11 offset:3072
ds_read_b128 v[84:87],v12 offset:2304
ds_read_b128 v[144:147],v11 offset:4224
ds_read_b128 v[148:151],v12 offset:3456
ds_read_b128 v[152:155],v11 offset:7680
ds_read_b128 v[156:159],v12 offset:6912
ds_read_b128 v[88:91],v11 offset:8832
ds_read_b128 v[92:95],v12 offset:8064
ds_read_b128 v[96:99],v11 offset:12288
ds_read_b128 v[100:103],v12 offset:11520
ds_read_b128 v[104:107],v11 offset:13440
ds_read_b128 v[108:111],v12 offset:12672
s_branch N_og0_%=
s_nop 0
s_nop 0
s_nop 0
s_nop 0
s_nop 0
s_nop 0
s_nop 0
s_nop 0
s_nop 0
s_nop 0
s_nop 0
s_nop 0
s_nop 0
s_nop 0
s_nop 0
s_nop 0
s_nop 0
s_nop 0
s_nop 0
s_nop 0
s_nop 0
s_nop 0
s_nop 0
s_nop 0
s_nop 0
s_nop 0
s_nop 0
s_nop 0
s_nop 0
s_nop 0
s_nop 0
s_nop 0
s_nop 0
s_nop 0
s_nop 0
s_nop 0
s_nop 0
s_nop 0
s_nop 0
s_nop 0
s_nop 0
s_nop 0
s_nop 0
s_nop 0
s_nop 0
s_nop 0
s_nop 0
s_nop 0
s_nop 0
s_nop 0
s_nop 0
s_nop 0
s_nop 0
s_nop 0
s_nop 0
s_nop 0
s_nop 0
s_nop 0
s_nop 0
s_nop 0
s_nop 0
s_nop 0
s_nop 0
s_nop 0
s_nop 0
s_nop 0
s_nop 0
s_nop 0
s_nop 0
s_nop 0
s_nop 0
s_nop 0
s_nop 0
s_nop 0
s_nop 0
s_nop 0
s_nop 0
s_nop 0
s_nop 0
s_nop 0
s_nop 0
s_nop 0
s_nop 0
s_nop 0
s_nop 0
s_nop 0
s_nop 0
s_nop 0
s_nop 0
s_nop 0
s_nop 0
s_nop 0
s_nop 0
s_nop 0
s_nop 0
s_nop 0
s_nop 0
s_nop 0
s_nop 0
s_nop 0
s_nop 0
s_nop 0
s_nop 0
N_og0_%=:
s_waitcnt vmcnt(0)
s_barrier
s_setprio 3
ds_read_b128 v[24:27],v124 offset:6976
ds_read_b128 v[16:19],v124 offset:0
ds_read_b128 v[20:23],v124 offset:3488
ds_read_b128 v[36:39],v125 offset:6976
ds_read_b128 v[28:31],v125 offset:0
ds_read_b128 v[32:35],v125 offset:3488
s_waitcnt lgkmcnt(5)
v_mfma_f32_16x16x32_bf16 v[160:163],v[248:251],v[24:27],0
s_waitcnt lgkmcnt(3)
ds_read_b128 v[24:27],v124 offset:7040
v_smfmac_f32_16x16x64_bf16 v[160:163],v[252:255],v[16:23],v13
s_waitcnt lgkmcnt(3)
ds_read_b128 v[16:19],v124 offset:64
ds_read_b128 v[20:23],v124 offset:3552
v_mfma_f32_16x16x32_bf16 v[164:167],v[112:115],v[36:39],0
s_waitcnt lgkmcnt(3)
ds_read_b128 v[36:39],v125 offset:7040
v_smfmac_f32_16x16x64_bf16 v[164:167],v[116:119],v[28:35],v13
s_waitcnt lgkmcnt(3)
ds_read_b128 v[28:31],v125 offset:64
ds_read_b128 v[32:35],v125 offset:3552
v_mfma_f32_16x16x32_bf16 v[176:179],v[64:67],v[24:27],0
s_waitcnt lgkmcnt(3)
ds_read_b128 v[24:27],v124 offset:7104
v_add_f32 v120,v160,v161
v_smfmac_f32_16x16x64_bf16 v[176:179],v[68:71],v[16:23],v13
s_waitcnt lgkmcnt(3)
ds_read_b128 v[16:19],v124 offset:128
ds_read_b128 v[20:23],v124 offset:3616
v_mul_f32 v122,v160,v160
v_fmac_f32 v122,v161,v161
v_add_f32 v121,v162,v163
v_mul_f32 v123,v162,v162
v_fmac_f32 v123,v163,v163
v_mfma_f32_16x16x32_bf16 v[180:183],v[128:131],v[36:39],0
s_waitcnt lgkmcnt(3)
ds_read_b128 v[36:39],v125 offset:7104
v_add_f32 v120,v120,v164
v_smfmac_f32_16x16x64_bf16 v[180:183],v[132:135],v[28:35],v13
s_waitcnt lgkmcnt(3)
ds_read_b128 v[28:31],v125 offset:128
ds_read_b128 v[32:35],v125 offset:3616
v_fmac_f32 v122,v164,v164
v_add_f32 v120,v120,v165
v_fmac_f32 v122,v165,v165
v_add_f32 v121,v121,v166
v_fmac_f32 v123,v166,v166
v_add_f32 v121,v121,v167
v_fmac_f32 v123,v167,v167
v_mfma_f32_16x16x32_bf16 v[192:195],v[136:139],v[24:27],0
s_waitcnt lgkmcnt(3)
ds_read_b128 v[24:27],v126 offset:6976
v_add_f32 v120,v120,v176
v_smfmac_f32_16x16x64_bf16 v[192:195],v[140:143],v[16:23],v13
s_waitcnt lgkmcnt(3)
ds_read_b128 v[16:19],v126 offset:0
ds_read_b128 v[20:23],v126 offset:3488
v_fmac_f32 v122,v176,v176
v_add_f32 v120,v120,v177
v_fmac_f32 v122,v177,v177
v_add_f32 v121,v121,v178
v_fmac_f32 v123,v178,v178
v_add_f32 v121,v121,v179
v_fmac_f32 v123,v179,v179
v_mfma_f32_16x16x32_bf16 v[196:199],v[72:75],v[36:39],0
s_waitcnt lgkmcnt(3)
ds_read_b128 v[36:39],v127 offset:6976
v_add_f32 v120,v120,v180
v_fmac_f32 v122,v180,v180
v_smfmac_f32_16x16x64_bf16 v[196:199],v[76:79],v[28:35],v13
s_waitcnt lgkmcnt(3)
ds_read_b128 v[28:31],v127 offset:0
ds_read_b128 v[32:35],v127 offset:3488
v_add_f32 v120,v120,v181
v_fmac_f32 v122,v181,v181
v_add_f32 v121,v121,v182
v_fmac_f32 v123,v182,v182
v_add_f32 v121,v121,v183
v_fmac_f32 v123,v183,v183
v_add_u32 v124,v124,v8
v_mfma_f32_16x16x32_bf16 v[168:171],v[80:83],v[24:27],0
s_waitcnt lgkmcnt(3)
ds_read_b128 v[24:27],v126 offset:7040
v_add_f32 v120,v120,v192
v_smfmac_f32_16x16x64_bf16 v[168:171],v[84:87],v[16:23],v13
s_waitcnt lgkmcnt(3)
ds_read_b128 v[16:19],v126 offset:64
ds_read_b128 v[20:23],v126 offset:3552
v_fmac_f32 v122,v192,v192
v_add_f32 v120,v120,v193
v_fmac_f32 v122,v193,v193
v_add_f32 v121,v121,v194
v_fmac_f32 v123,v194,v194
v_add_f32 v121,v121,v195
v_fmac_f32 v123,v195,v195
v_mfma_f32_16x16x32_bf16 v[172:175],v[144:147],v[36:39],0
s_waitcnt lgkmcnt(3)
ds_read_b128 v[36:39],v127 offset:7040
v_add_f32 v120,v120,v196
v_smfmac_f32_16x16x64_bf16 v[172:175],v[148:151],v[28:35],v13
s_waitcnt lgkmcnt(3)
ds_read_b128 v[28:31],v127 offset:64
ds_read_b128 v[32:35],v127 offset:3552
v_fmac_f32 v122,v196,v196
v_add_f32 v120,v120,v197
v_fmac_f32 v122,v197,v197
v_add_f32 v121,v121,v198
v_fmac_f32 v123,v198,v198
v_add_f32 v121,v121,v199
v_fmac_f32 v123,v199,v199
v_mfma_f32_16x16x32_bf16 v[184:187],v[152:155],v[24:27],0
s_waitcnt lgkmcnt(3)
ds_read_b128 v[24:27],v126 offset:7104
v_add_f32 v120,v120,v168
v_smfmac_f32_16x16x64_bf16 v[184:187],v[156:159],v[16:23],v13
s_waitcnt lgkmcnt(3)
ds_read_b128 v[16:19],v126 offset:128
ds_read_b128 v[20:23],v126 offset:3616
v_fmac_f32 v122,v168,v168
v_add_f32 v120,v120,v169
v_fmac_f32 v122,v169,v169
v_add_f32 v121,v121,v170
v_fmac_f32 v123,v170,v170
v_add_f32 v121,v121,v171
v_fmac_f32 v123,v171,v171
v_mfma_f32_16x16x32_bf16 v[188:191],v[88:91],v[36:39],0
s_waitcnt lgkmcnt(3)
ds_read_b128 v[36:39],v127 offset:7104
v_add_f32 v120,v120,v172
v_smfmac_f32_16x16x64_bf16 v[188:191],v[92:95],v[28:35],v13
s_waitcnt lgkmcnt(3)
ds_read_b128 v[28:31],v127 offset:128
ds_read_b128 v[32:35],v127 offset:3616
ds_read_b128 v[40:43],v124 offset:3488
v_fmac_f32 v122,v172,v172
v_add_f32 v120,v120,v173
v_fmac_f32 v122,v173,v173
v_add_f32 v121,v121,v174
v_fmac_f32 v123,v174,v174
v_add_f32 v121,v121,v175
v_fmac_f32 v123,v175,v175
v_mfma_f32_16x16x32_bf16 v[200:203],v[96:99],v[24:27],0
s_waitcnt lgkmcnt(4)
ds_read_b128 v[44:47],v124 offset:3680
ds_read_b128 v[48:51],v124 offset:3552
ds_read_b128 v[52:55],v124 offset:3744
v_add_f32 v120,v120,v184
v_smfmac_f32_16x16x64_bf16 v[200:203],v[100:103],v[16:23],v13
s_waitcnt lgkmcnt(6)
ds_read_b128 v[56:59],v124 offset:3616
ds_read_b128 v[60:63],v124 offset:3808
ds_read_b128 v[208:211],v14 offset:0
v_fmac_f32 v122,v184,v184
v_add_f32 v120,v120,v185
v_fmac_f32 v122,v185,v185
v_add_f32 v121,v121,v186
v_fmac_f32 v123,v186,v186
v_add_f32 v121,v121,v187
v_fmac_f32 v123,v187,v187
v_mfma_f32_16x16x32_bf16 v[204:207],v[104:107],v[36:39],0
s_waitcnt lgkmcnt(7)
ds_read_b128 v[212:215],v7 offset:3584
ds_read_b128 v[216:219],v14 offset:256
ds_read_b128 v[220:223],v7 offset:3616
v_add_f32 v120,v120,v188
v_smfmac_f32_16x16x64_bf16 v[204:207],v[108:111],v[28:35],v13
ds_read_b128 v[224:227],v14 offset:512
ds_read_b128 v[228:231],v7 offset:3712
ds_read_b128 v[232:235],v14 offset:768
v_fmac_f32 v122,v188,v188
v_add_f32 v120,v120,v189
v_fmac_f32 v122,v189,v189
v_add_f32 v121,v121,v190
v_fmac_f32 v123,v190,v190
v_add_f32 v121,v121,v191
v_fmac_f32 v123,v191,v191
ds_read_b128 v[236:239],v7 offset:3744
ds_read_b128 v[240:243],v14 offset:1024
ds_read_b128 v[244:247],v7 offset:3840
v_add_f32 v120,v120,v200
v_fmac_f32 v122,v200,v200
v_add_f32 v120,v120,v201
v_fmac_f32 v122,v201,v201
v_add_f32 v121,v121,v202
v_fmac_f32 v123,v202,v202
v_add_f32 v121,v121,v203
v_fmac_f32 v123,v203,v203
v_add_f32 v120,v120,v204
v_fmac_f32 v122,v204,v204
v_add_f32 v120,v120,v205
v_fmac_f32 v122,v205,v205
v_add_f32 v121,v121,v206
v_fmac_f32 v123,v206,v206
v_add_f32 v121,v121,v207
v_fmac_f32 v123,v207,v207
v_sub_u32 v124,v124,v8
v_permlane32_swap_b32 v120,v121
v_permlane32_swap_b32 v122,v123
v_add_f32 v120,v120,v121
v_add_f32 v122,v122,v123
s_nop 1
v_permlane16_swap_b32 v120,v122
v_add_f32 v37,v120,v122
v_add_f32 v38,v120,v122
s_nop 1
v_permlane16_swap_b32 v37,v38
v_mul_f32 v37,0x3c2aaaab,v37
v_mul_f32 v38,0x3c2aaaab,v38
v_mul_f32 v36,0x5e000000,v38
v_fma_f32 v38,-v37,v37,v38
v_pk_maximum3_f16 v1,v1,v36,v36 op_sel:[0,1,1] op_sel_hi:[1,1,1]
v_max_f32 v38,0,v38
v_add_f32 v38,0x3727c5ac,v38
v_rsq_f32 v38,v38
s_nop 0
v_mul_f32 v120,0x8befe000,v38
v_mul_f32 v121,0x8befe000,v38
v_mul_f32 v122,-v37,v120
v_mul_f32 v123,-v37,v120
v_permlane32_swap_b32 v120,v121
s_nop 0
v_permlane32_swap_b32 v122,v123
v_pk_fma_f32 v[160:161],v[160:161],v[120:121],v[122:123] op_sel:[0,0,0] op_sel_hi:[1,0,0]
v_pk_fma_f32 v[162:163],v[162:163],v[120:121],v[122:123] op_sel:[0,1,1] op_sel_hi:[1,1,1]
v_pk_fma_f32 v[164:165],v[164:165],v[120:121],v[122:123] op_sel:[0,0,0] op_sel_hi:[1,0,0]
v_pk_fma_f32 v[166:167],v[166:167],v[120:121],v[122:123] op_sel:[0,1,1] op_sel_hi:[1,1,1]
v_pk_fma_f32 v[168:169],v[168:169],v[120:121],v[122:123] op_sel:[0,0,0] op_sel_hi:[1,0,0]
v_pk_fma_f32 v[170:171],v[170:171],v[120:121],v[122:123] op_sel:[0,1,1] op_sel_hi:[1,1,1]
v_pk_fma_f32 v[172:173],v[172:173],v[120:121],v[122:123] op_sel:[0,0,0] op_sel_hi:[1,0,0]
v_pk_fma_f32 v[174:175],v[174:175],v[120:121],v[122:123] op_sel:[0,1,1] op_sel_hi:[1,1,1]
v_pk_fma_f32 v[176:177],v[176:177],v[120:121],v[122:123] op_sel:[0,0,0] op_sel_hi:[1,0,0]
v_pk_fma_f32 v[178:179],v[178:179],v[120:121],v[122:123] op_sel:[0,1,1] op_sel_hi:[1,1,1]
v_pk_fma_f32 v[180:181],v[180:181],v[120:121],v[122:123] op_sel:[0,0,0] op_sel_hi:[1,0,0]
v_pk_fma_f32 v[182:183],v[182:183],v[120:121],v[122:123] op_sel:[0,1,1] op_sel_hi:[1,1,1]
v_pk_fma_f32 v[184:185],v[184:185],v[120:121],v[122:123] op_sel:[0,0,0] op_sel_hi:[1,0,0]
v_pk_fma_f32 v[186:187],v[186:187],v[120:121],v[122:123] op_sel:[0,1,1] op_sel_hi:[1,1,1]
v_pk_fma_f32 v[188:189],v[188:189],v[120:121],v[122:123] op_sel:[0,0,0] op_sel_hi:[1,0,0]
v_pk_fma_f32 v[190:191],v[190:191],v[120:121],v[122:123] op_sel:[0,1,1] op_sel_hi:[1,1,1]
v_pk_fma_f32 v[192:193],v[192:193],v[120:121],v[122:123] op_sel:[0,0,0] op_sel_hi:[1,0,0]
v_pk_fma_f32 v[194:195],v[194:195],v[120:121],v[122:123] op_sel:[0,1,1] op_sel_hi:[1,1,1]
v_pk_fma_f32 v[196:197],v[196:197],v[120:121],v[122:123] op_sel:[0,0,0] op_sel_hi:[1,0,0]
v_pk_fma_f32 v[198:199],v[198:199],v[120:121],v[122:123] op_sel:[0,1,1] op_sel_hi:[1,1,1]
v_pk_fma_f32 v[200:201],v[200:201],v[120:121],v[122:123] op_sel:[0,0,0] op_sel_hi:[1,0,0]
v_pk_fma_f32 v[202:203],v[202:203],v[120:121],v[122:123] op_sel:[0,1,1] op_sel_hi:[1,1,1]
v_pk_fma_f32 v[204:205],v[204:205],v[120:121],v[122:123] op_sel:[0,0,0] op_sel_hi:[1,0,0]
v_pk_fma_f32 v[206:207],v[206:207],v[120:121],v[122:123] op_sel:[0,1,1] op_sel_hi:[1,1,1]
v_cvt_pk_bf16_f32 v16,v160,v161
v_cvt_pk_bf16_f32 v20,v162,v163
v_cvt_pk_bf16_f32 v17,v164,v165
v_cvt_pk_bf16_f32 v21,v166,v167
v_cvt_pk_bf16_f32 v18,v168,v169
v_cvt_pk_bf16_f32 v22,v170,v171
v_cvt_pk_bf16_f32 v19,v172,v173
v_cvt_pk_bf16_f32 v23,v174,v175
v_cvt_pk_bf16_f32 v24,v176,v177
s_waitcnt lgkmcnt(8)
v_mfma_f32_16x16x32_bf16 v[160:163],v[208:211],v[40:43],v[212:215]
v_cvt_pk_bf16_f32 v28,v178,v179
v_cvt_pk_bf16_f32 v25,v180,v181
v_mfma_f32_16x16x32_bf16 v[164:167],v[208:211],v[44:47],v[212:215]
s_waitcnt lgkmcnt(6)
ds_read_b128 v[208:211],v14 offset:1280
ds_read_b128 v[212:215],v7 offset:3872
v_cvt_pk_bf16_f32 v29,v182,v183
v_cvt_pk_bf16_f32 v26,v184,v185
v_mfma_f32_16x16x32_bf16 v[168:171],v[216:219],v[40:43],v[220:223]
v_cvt_pk_bf16_f32 v30,v186,v187
v_cvt_pk_bf16_f32 v27,v188,v189
v_mfma_f32_16x16x32_bf16 v[172:175],v[216:219],v[44:47],v[220:223]
v_cvt_pk_bf16_f32 v31,v190,v191
v_cvt_pk_bf16_f32 v32,v192,v193
s_waitcnt lgkmcnt(6)
v_mfma_f32_16x16x32_bf16 v[176:179],v[224:227],v[48:51],v[228:231]
v_cvt_pk_bf16_f32 v36,v194,v195
v_cvt_pk_bf16_f32 v33,v196,v197
v_mfma_f32_16x16x32_bf16 v[180:183],v[224:227],v[52:55],v[228:231]
s_waitcnt lgkmcnt(4)
v_cvt_pk_bf16_f32 v37,v198,v199
v_cvt_pk_bf16_f32 v34,v200,v201
v_mfma_f32_16x16x32_bf16 v[184:187],v[232:235],v[48:51],v[236:239]
v_cvt_pk_bf16_f32 v38,v202,v203
v_cvt_pk_bf16_f32 v35,v204,v205
v_mfma_f32_16x16x32_bf16 v[188:191],v[232:235],v[52:55],v[236:239]
v_cvt_pk_bf16_f32 v39,v206,v207
s_waitcnt lgkmcnt(2)
v_mfma_f32_16x16x32_bf16 v[192:195],v[240:243],v[56:59],v[244:247]
v_mfma_f32_16x16x32_bf16 v[196:199],v[240:243],v[60:63],v[244:247]
s_waitcnt lgkmcnt(0)
v_mfma_f32_16x16x32_bf16 v[200:203],v[208:211],v[56:59],v[212:215]
v_mfma_f32_16x16x32_bf16 v[204:207],v[208:211],v[60:63],v[212:215]
s_waitcnt lgkmcnt(0)
s_mov_b32 s86,0x1234567
s_barrier
ds_read_b128 v[48:51],v124 offset:34880
ds_read_b128 v[40:43],v124 offset:27904
ds_read_b128 v[44:47],v124 offset:31392
ds_read_b128 v[60:63],v125 offset:34880
ds_read_b128 v[52:55],v125 offset:27904
ds_read_b128 v[56:59],v125 offset:31392
ds_read_b128 v[232:235],v124 offset:34944
s_nop 0
s_mov_b32 s86,0xbe801d5a
ds_read_b128 v[216:219],v124 offset:27968
ds_read_b128 v[220:223],v124 offset:31456
s_waitcnt lgkmcnt(8)
v_mfma_f32_16x16x32_bf16 v[208:211],v[248:251],v[48:51],0
s_waitcnt lgkmcnt(6)
ds_read_b128 v[48:51],v125 offset:34944
v_smfmac_f32_16x16x64_bf16 v[208:211],v[252:255],v[40:47],v13
s_waitcnt lgkmcnt(6)
ds_read_b128 v[40:43],v125 offset:27968
ds_read_b128 v[44:47],v125 offset:31456
v_mfma_f32_16x16x32_bf16 v[212:215],v[112:115],v[60:63],0
s_waitcnt lgkmcnt(6)
ds_read_b128 v[60:63],v124 offset:35008
v_smfmac_f32_16x16x64_bf16 v[212:215],v[116:119],v[52:59],v13
s_waitcnt lgkmcnt(6)
ds_read_b128 v[112:115],v7 offset:128
ds_read_b128 v[52:55],v124 offset:28032
ds_read_b128 v[56:59],v124 offset:31520
v_mfma_f32_16x16x32_bf16 v[224:227],v[64:67],v[232:235],0
s_waitcnt lgkmcnt(7)
ds_read_b128 v[116:119],v7 offset:160
v_add_f32 v120,v208,v209
v_smfmac_f32_16x16x64_bf16 v[224:227],v[68:71],v[216:223],v13
s_waitcnt lgkmcnt(7)
ds_read_b128 v[236:239],v125 offset:35008
ds_read_b64_tr_b16 v[64:65],v4 offset:12832
ds_read_b64_tr_b16 v[66:67],v4 offset:14880
v_mul_f32 v122,v208,v208
v_fmac_f32 v122,v209,v209
v_add_f32 v121,v210,v211
v_mul_f32 v123,v210,v210
v_fmac_f32 v123,v211,v211
v_mfma_f32_16x16x32_bf16 v[228:231],v[128:131],v[48:51],0
s_waitcnt lgkmcnt(8)
ds_read_b128 v[248:251],v125 offset:28032
v_add_f32 v120,v120,v212
v_smfmac_f32_16x16x64_bf16 v[228:231],v[132:135],v[40:47],v13
s_waitcnt lgkmcnt(8)
ds_read_b128 v[252:255],v125 offset:31520
ds_read_b64_tr_b16 v[68:69],v5 offset:12832
ds_read_b64_tr_b16 v[70:71],v5 offset:14880
v_fmac_f32 v122,v212,v212
v_add_f32 v120,v120,v213
v_fmac_f32 v122,v213,v213
v_add_f32 v121,v121,v214
v_fmac_f32 v123,v214,v214
v_add_f32 v121,v121,v215
v_fmac_f32 v123,v215,v215
v_mfma_f32_16x16x32_bf16 v[240:243],v[136:139],v[60:63],0
s_waitcnt lgkmcnt(8)
ds_read_b128 v[48:51],v126 offset:34880
v_add_f32 v120,v120,v224
v_smfmac_f32_16x16x64_bf16 v[240:243],v[140:143],v[52:59],v13
s_waitcnt lgkmcnt(7)
ds_read_b128 v[40:43],v126 offset:27904
ds_read_b128 v[44:47],v126 offset:31392
ds_read_b128 v[60:63],v127 offset:34880
v_fmac_f32 v122,v224,v224
v_add_f32 v120,v120,v225
v_fmac_f32 v122,v225,v225
v_add_f32 v121,v121,v226
v_fmac_f32 v123,v226,v226
v_add_f32 v121,v121,v227
v_fmac_f32 v123,v227,v227
v_mfma_f32_16x16x32_bf16 v[244:247],v[72:75],v[236:239],0
s_waitcnt lgkmcnt(6)
ds_read_b128 v[52:55],v127 offset:27904
v_add_f32 v120,v120,v228
v_fmac_f32 v122,v228,v228
v_smfmac_f32_16x16x64_bf16 v[244:247],v[76:79],v[248:255],v13
ds_read_b128 v[56:59],v127 offset:31392
ds_read_b128 v[236:239],v126 offset:34944
ds_read_b64_tr_b16 v[72:73],v4 offset:16928
v_add_f32 v120,v120,v229
v_mfma_f32_16x16x32_bf16 v[128:131],v[64:67],v[16:19],v[112:115]
ds_read_b64_tr_b16 v[74:75],v4 offset:18976
ds_read_b128 v[248:251],v126 offset:27968
v_fmac_f32 v122,v229,v229
v_add_f32 v121,v121,v230
v_mfma_f32_16x16x32_bf16 v[136:139],v[64:67],v[20:23],v[112:115]
s_waitcnt lgkmcnt(9)
v_fmac_f32 v123,v230,v230
v_add_f32 v121,v121,v231
v_fmac_f32 v123,v231,v231
v_add_u32 v124,v124,v8
v_mfma_f32_16x16x32_bf16 v[216:219],v[80:83],v[48:51],0
s_waitcnt lgkmcnt(7)
ds_read_b128 v[252:255],v126 offset:31456
v_add_f32 v120,v120,v240
v_fmac_f32 v122,v240,v240
v_smfmac_f32_16x16x64_bf16 v[216:219],v[84:87],v[40:47],v13
ds_read_b64_tr_b16 v[76:77],v5 offset:16928
ds_read_b64_tr_b16 v[78:79],v5 offset:18976
ds_read_b128 v[48:51],v127 offset:34944
v_add_f32 v120,v120,v241
v_mfma_f32_16x16x32_bf16 v[140:143],v[68:71],v[20:23],v[116:119]
ds_read_b64_tr_b16 v[80:81],v4 offset:21024
ds_read_b64_tr_b16 v[82:83],v4 offset:23072
v_fmac_f32 v122,v241,v241
v_mfma_f32_16x16x32_bf16 v[132:135],v[68:71],v[16:19],v[116:119]
s_waitcnt lgkmcnt(12)
v_add_f32 v121,v121,v242
v_fmac_f32 v123,v242,v242
v_add_f32 v121,v121,v243
v_fmac_f32 v123,v243,v243
v_mfma_f32_16x16x32_bf16 v[220:223],v[144:147],v[60:63],0
s_waitcnt lgkmcnt(10)
ds_read_b128 v[40:43],v127 offset:27968
v_add_f32 v120,v120,v244
v_fmac_f32 v122,v244,v244
v_smfmac_f32_16x16x64_bf16 v[220:223],v[148:151],v[52:59],v13
s_waitcnt lgkmcnt(8)
ds_read_b128 v[44:47],v127 offset:31456
ds_read_b64_tr_b16 v[84:85],v5 offset:21024
ds_read_b64_tr_b16 v[86:87],v5 offset:23072
v_add_f32 v120,v120,v245
v_mfma_f32_16x16x32_bf16 v[128:131],v[72:75],v[24:27],v[128:131]
ds_read_b128 v[60:63],v126 offset:35008
ds_read_b128 v[144:147],v124 offset:31392
v_fmac_f32 v122,v245,v245
v_mfma_f32_16x16x32_bf16 v[136:139],v[72:75],v[28:31],v[136:139]
v_add_f32 v121,v121,v246
v_fmac_f32 v123,v246,v246
v_add_f32 v121,v121,v247
v_fmac_f32 v123,v247,v247
v_mfma_f32_16x16x32_bf16 v[232:235],v[152:155],v[236:239],0
s_waitcnt lgkmcnt(11)
ds_read_b128 v[52:55],v126 offset:28032
v_add_f32 v120,v120,v216
v_fmac_f32 v122,v216,v216
v_smfmac_f32_16x16x64_bf16 v[232:235],v[156:159],v[248:255],v13
s_waitcnt lgkmcnt(10)
ds_read_b128 v[56:59],v126 offset:31520
ds_read_b128 v[148:151],v124 offset:31584
ds_read_b128 v[152:155],v124 offset:31456
v_add_f32 v120,v120,v217
v_mfma_f32_16x16x32_bf16 v[140:143],v[76:79],v[28:31],v[140:143]
ds_read_b128 v[156:159],v124 offset:31648
v_fmac_f32 v122,v217,v217
v_mfma_f32_16x16x32_bf16 v[132:135],v[76:79],v[24:27],v[132:135]
s_waitcnt lgkmcnt(13)
v_add_f32 v121,v121,v218
v_fmac_f32 v123,v218,v218
v_add_f32 v121,v121,v219
v_fmac_f32 v123,v219,v219
v_mfma_f32_16x16x32_bf16 v[236:239],v[88:91],v[48:51],0
s_waitcnt lgkmcnt(9)
ds_read_b128 v[48:51],v127 offset:35008
v_add_f32 v120,v120,v220
v_fmac_f32 v122,v220,v220
v_smfmac_f32_16x16x64_bf16 v[236:239],v[92:95],v[40:47],v13
ds_read_b128 v[88:91],v124 offset:31520
ds_read_b128 v[40:43],v127 offset:28032
ds_read_b128 v[44:47],v127 offset:31520
v_add_f32 v120,v120,v221
v_mfma_f32_16x16x32_bf16 v[128:131],v[80:83],v[32:35],v[128:131]
ds_read_b128 v[92:95],v124 offset:31712
v_fmac_f32 v122,v221,v221
v_mfma_f32_16x16x32_bf16 v[136:139],v[80:83],v[36:39],v[136:139]
s_waitcnt lgkmcnt(11)
v_add_f32 v121,v121,v222
v_fmac_f32 v123,v222,v222
v_add_f32 v121,v121,v223
v_fmac_f32 v123,v223,v223
v_mfma_f32_16x16x32_bf16 v[248:251],v[96:99],v[60:63],0
s_waitcnt lgkmcnt(8)
v_add_f32 v120,v120,v232
v_fmac_f32 v122,v232,v232
v_smfmac_f32_16x16x64_bf16 v[248:251],v[100:103],v[52:59],v13
ds_read_b128 v[96:99],v14 offset:0
ds_read_b128 v[100:103],v7 offset:3584
v_add_f32 v120,v120,v233
v_fmac_f32 v122,v233,v233
v_mfma_f32_16x16x32_bf16 v[140:143],v[84:87],v[36:39],v[140:143]
s_waitcnt lgkmcnt(6)
v_add_f32 v121,v121,v234
v_fmac_f32 v123,v234,v234
v_add_f32 v121,v121,v235
v_fmac_f32 v123,v235,v235
v_mfma_f32_16x16x32_bf16 v[252:255],v[104:107],v[48:51],0
s_waitcnt lgkmcnt(3)
v_add_f32 v120,v120,v236
v_fmac_f32 v122,v236,v236
v_smfmac_f32_16x16x64_bf16 v[252:255],v[108:111],v[40:47],v13
ds_read_b128 v[104:107],v14 offset:256
ds_read_b128 v[108:111],v7 offset:3616
v_add_f32 v120,v120,v237
v_fmac_f32 v122,v237,v237
v_mfma_f32_16x16x32_bf16 v[132:135],v[84:87],v[32:35],v[132:135]
v_add_f32 v121,v121,v238
v_fmac_f32 v123,v238,v238
v_add_f32 v121,v121,v239
v_fmac_f32 v123,v239,v239
v_add_f32 v120,v120,v248
v_fmac_f32 v122,v248,v248
v_add_f32 v120,v120,v249
v_fmac_f32 v122,v249,v249
v_add_f32 v121,v121,v250
v_fmac_f32 v123,v250,v250
v_add_f32 v121,v121,v251
v_fmac_f32 v123,v251,v251
s_waitcnt lgkmcnt(4)
s_barrier
v_add_f32 v120,v120,v252
v_fmac_f32 v122,v252,v252
v_add_f32 v120,v120,v253
v_fmac_f32 v122,v253,v253
v_add_f32 v121,v121,v254
v_fmac_f32 v123,v254,v254
v_add_f32 v121,v121,v255
v_fmac_f32 v123,v255,v255
s_nop 0
v_permlane32_swap_b32 v120,v121
v_permlane32_swap_b32 v122,v123
v_add_f32 v120,v120,v121
v_add_f32 v122,v122,v123
s_nop 1
v_permlane16_swap_b32 v120,v122
v_add_f32 v61,v120,v122
v_add_f32 v62,v120,v122
s_nop 1
v_permlane16_swap_b32 v61,v62
v_mul_f32 v61,0x3c2aaaab,v61
v_mul_f32 v62,0x3c2aaaab,v62
v_mul_f32 v60,0x5e000000,v62
v_fma_f32 v62,-v61,v61,v62
v_pk_maximum3_f16 v1,v1,v60,v60 op_sel:[0,1,1] op_sel_hi:[1,1,1]
v_max_f32 v62,0,v62
v_add_f32 v62,0x3727c5ac,v62
v_rsq_f32 v62,v62
s_nop 0
v_mul_f32 v120,0x8befe000,v62
v_mul_f32 v121,0x8befe000,v62
v_mul_f32 v122,-v61,v120
v_mul_f32 v123,-v61,v120
v_permlane32_swap_b32 v120,v121
s_nop 0
v_permlane32_swap_b32 v122,v123
v_pk_fma_f32 v[208:209],v[208:209],v[120:121],v[122:123] op_sel:[0,0,0] op_sel_hi:[1,0,0]
v_pk_fma_f32 v[210:211],v[210:211],v[120:121],v[122:123] op_sel:[0,1,1] op_sel_hi:[1,1,1]
v_pk_fma_f32 v[212:213],v[212:213],v[120:121],v[122:123] op_sel:[0,0,0] op_sel_hi:[1,0,0]
v_pk_fma_f32 v[214:215],v[214:215],v[120:121],v[122:123] op_sel:[0,1,1] op_sel_hi:[1,1,1]
v_pk_fma_f32 v[216:217],v[216:217],v[120:121],v[122:123] op_sel:[0,0,0] op_sel_hi:[1,0,0]
v_pk_fma_f32 v[218:219],v[218:219],v[120:121],v[122:123] op_sel:[0,1,1] op_sel_hi:[1,1,1]
v_pk_fma_f32 v[220:221],v[220:221],v[120:121],v[122:123] op_sel:[0,0,0] op_sel_hi:[1,0,0]
v_pk_fma_f32 v[222:223],v[222:223],v[120:121],v[122:123] op_sel:[0,1,1] op_sel_hi:[1,1,1]
v_pk_fma_f32 v[224:225],v[224:225],v[120:121],v[122:123] op_sel:[0,0,0] op_sel_hi:[1,0,0]
v_pk_fma_f32 v[226:227],v[226:227],v[120:121],v[122:123] op_sel:[0,1,1] op_sel_hi:[1,1,1]
v_pk_fma_f32 v[228:229],v[228:229],v[120:121],v[122:123] op_sel:[0,0,0] op_sel_hi:[1,0,0]
v_pk_fma_f32 v[230:231],v[230:231],v[120:121],v[122:123] op_sel:[0,1,1] op_sel_hi:[1,1,1]
v_pk_fma_f32 v[232:233],v[232:233],v[120:121],v[122:123] op_sel:[0,0,0] op_sel_hi:[1,0,0]
v_pk_fma_f32 v[234:235],v[234:235],v[120:121],v[122:123] op_sel:[0,1,1] op_sel_hi:[1,1,1]
v_pk_fma_f32 v[236:237],v[236:237],v[120:121],v[122:123] op_sel:[0,0,0] op_sel_hi:[1,0,0]
v_pk_fma_f32 v[238:239],v[238:239],v[120:121],v[122:123] op_sel:[0,1,1] op_sel_hi:[1,1,1]
v_pk_fma_f32 v[240:241],v[240:241],v[120:121],v[122:123] op_sel:[0,0,0] op_sel_hi:[1,0,0]
v_pk_fma_f32 v[242:243],v[242:243],v[120:121],v[122:123] op_sel:[0,1,1] op_sel_hi:[1,1,1]
v_pk_fma_f32 v[244:245],v[244:245],v[120:121],v[122:123] op_sel:[0,0,0] op_sel_hi:[1,0,0]
v_pk_fma_f32 v[246:247],v[246:247],v[120:121],v[122:123] op_sel:[0,1,1] op_sel_hi:[1,1,1]
v_pk_fma_f32 v[248:249],v[248:249],v[120:121],v[122:123] op_sel:[0,0,0] op_sel_hi:[1,0,0]
v_pk_fma_f32 v[250:251],v[250:251],v[120:121],v[122:123] op_sel:[0,1,1] op_sel_hi:[1,1,1]
v_pk_fma_f32 v[252:253],v[252:253],v[120:121],v[122:123] op_sel:[0,0,0] op_sel_hi:[1,0,0]
v_pk_fma_f32 v[254:255],v[254:255],v[120:121],v[122:123] op_sel:[0,1,1] op_sel_hi:[1,1,1]
v_cvt_pk_bf16_f32 v40,v208,v209
v_cvt_pk_bf16_f32 v44,v210,v211
v_cvt_pk_bf16_f32 v41,v212,v213
v_cvt_pk_bf16_f32 v45,v214,v215
v_cvt_pk_bf16_f32 v42,v216,v217
v_cvt_pk_bf16_f32 v46,v218,v219
v_cvt_pk_bf16_f32 v43,v220,v221
v_cvt_pk_bf16_f32 v47,v222,v223
v_cvt_pk_bf16_f32 v48,v224,v225
s_waitcnt lgkmcnt(2)
v_mfma_f32_16x16x32_bf16 v[208:211],v[96:99],v[144:147],v[100:103]
v_cvt_pk_bf16_f32 v52,v226,v227
v_cvt_pk_bf16_f32 v49,v228,v229
v_mfma_f32_16x16x32_bf16 v[212:215],v[96:99],v[148:151],v[100:103]
s_waitcnt lgkmcnt(0)
ds_read_b128 v[96:99],v14 offset:512
ds_read_b128 v[100:103],v7 offset:3712
v_cvt_pk_bf16_f32 v53,v230,v231
v_cvt_pk_bf16_f32 v50,v232,v233
v_mfma_f32_16x16x32_bf16 v[216:219],v[104:107],v[144:147],v[108:111]
v_cvt_pk_bf16_f32 v54,v234,v235
v_cvt_pk_bf16_f32 v51,v236,v237
v_mfma_f32_16x16x32_bf16 v[220:223],v[104:107],v[148:151],v[108:111]
ds_read_b128 v[104:107],v14 offset:768
ds_read_b128 v[108:111],v7 offset:3744
v_cvt_pk_bf16_f32 v55,v238,v239
v_cvt_pk_bf16_f32 v56,v240,v241
s_waitcnt lgkmcnt(2)
v_mfma_f32_16x16x32_bf16 v[224:227],v[96:99],v[152:155],v[100:103]
s_nop 0
v_cvt_pk_bf16_f32 v60,v242,v243
v_cvt_pk_bf16_f32 v57,v244,v245
v_mfma_f32_16x16x32_bf16 v[228:231],v[96:99],v[156:159],v[100:103]
s_waitcnt lgkmcnt(0)
ds_read_b128 v[96:99],v14 offset:1024
ds_read_b128 v[100:103],v7 offset:3840
v_cvt_pk_bf16_f32 v61,v246,v247
v_cvt_pk_bf16_f32 v58,v248,v249
v_mfma_f32_16x16x32_bf16 v[232:235],v[104:107],v[152:155],v[108:111]
v_cvt_pk_bf16_f32 v62,v250,v251
v_cvt_pk_bf16_f32 v59,v252,v253
v_mfma_f32_16x16x32_bf16 v[236:239],v[104:107],v[156:159],v[108:111]
s_nop 0
ds_read_b128 v[104:107],v14 offset:1280
ds_read_b128 v[108:111],v7 offset:3872
v_cvt_pk_bf16_f32 v63,v254,v255
s_waitcnt lgkmcnt(2)
v_mfma_f32_16x16x32_bf16 v[240:243],v[96:99],v[88:91],v[100:103]
s_barrier
s_setprio 2
v_lshlrev_b32_sdwa v128,1,v128 dst_sel:DWORD dst_unused:UNUSED_PAD src0_sel:DWORD src1_sel:WORD_1
v_lshlrev_b32_sdwa v130,1,v130 dst_sel:DWORD dst_unused:UNUSED_PAD src0_sel:DWORD src1_sel:WORD_1
v_mfma_f32_16x16x32_bf16 v[244:247],v[96:99],v[92:95],v[100:103]
s_waitcnt lgkmcnt(0)
ds_read_u16 v128,v128
ds_read_b64_tr_b16 v[96:97],v6 offset:64
v_lshlrev_b32_sdwa v129,1,v129 dst_sel:DWORD dst_unused:UNUSED_PAD src0_sel:DWORD src1_sel:WORD_1
v_lshlrev_b32_sdwa v131,1,v131 dst_sel:DWORD dst_unused:UNUSED_PAD src0_sel:DWORD src1_sel:WORD_1
v_mfma_f32_16x16x32_bf16 v[248:251],v[104:107],v[88:91],v[108:111]
ds_read_u16_d16_hi v130,v130
ds_read_b64_tr_b16 v[98:99],v6 offset:1600
v_fma_mixlo_f16 v120,v141,s99,0
v_mfma_f32_16x16x32_bf16 v[252:255],v[104:107],v[92:95],v[108:111]
ds_read_u16 v129,v129
ds_read_b64_tr_b16 v[100:101],v6 offset:72
v_lshlrev_b32_sdwa v132,1,v132 dst_sel:DWORD dst_unused:UNUSED_PAD src0_sel:DWORD src1_sel:WORD_1
v_fma_mixhi_f16 v120,v143,s99,0
v_lshlrev_b32_sdwa v134,1,v134 dst_sel:DWORD dst_unused:UNUSED_PAD src0_sel:DWORD src1_sel:WORD_1
v_pk_fma_f16 v121,v120,v120,s54
v_lshlrev_b32_sdwa v133,1,v133 dst_sel:DWORD dst_unused:UNUSED_PAD src0_sel:DWORD src1_sel:WORD_1
v_mfma_f32_16x16x32_bf16 v[144:147],v[64:67],v[40:43],v[112:115]
ds_read_u16_d16_hi v131,v131
ds_read_b64_tr_b16 v[102:103],v6 offset:1608
v_pk_mul_f16 v121,v120,v121
v_lshlrev_b32_sdwa v135,1,v135 dst_sel:DWORD dst_unused:UNUSED_PAD src0_sel:DWORD src1_sel:WORD_1
v_mfma_f32_16x16x32_bf16 v[152:155],v[64:67],v[44:47],v[112:115]
ds_read_u16 v132,v132
ds_read_b64_tr_b16 v[88:89],v6 offset:0
v_exp_f16 v120,v121
v_mfma_f32_16x16x32_bf16 v[156:159],v[68:71],v[44:47],v[116:119]
ds_read_u16_d16_hi v134,v134
ds_read_b64_tr_b16 v[90:91],v6 offset:1536
v_exp_f16_sdwa v120,v121 dst_sel:WORD_1 dst_unused:UNUSED_PRESERVE src0_sel:WORD_1
v_mfma_f32_16x16x32_bf16 v[148:151],v[68:71],v[40:43],v[116:119]
ds_read_u16 v133,v133
ds_read_b64_tr_b16 v[92:93],v6 offset:8
v_lshlrev_b32_sdwa v136,1,v136 dst_sel:DWORD dst_unused:UNUSED_PAD src0_sel:DWORD src1_sel:WORD_1
v_lshlrev_b32_sdwa v138,1,v138 dst_sel:DWORD dst_unused:UNUSED_PAD src0_sel:DWORD src1_sel:WORD_1
v_mfma_f32_16x16x32_bf16 v[144:147],v[72:75],v[48:51],v[144:147]
ds_read_u16_d16_hi v135,v135
ds_read_b64_tr_b16 v[94:95],v6 offset:1544
v_pk_fma_f16 v120,v120,s55,s55
v_mfma_f32_16x16x32_bf16 v[152:155],v[72:75],v[52:55],v[152:155]
ds_read_u16 v136,v136
ds_read_b64_tr_b16 v[104:105],v6 offset:128
v_lshlrev_b32_sdwa v137,1,v137 dst_sel:DWORD dst_unused:UNUSED_PAD src0_sel:DWORD src1_sel:WORD_1
v_rcp_f16 v121,v120
v_mfma_f32_16x16x32_bf16 v[156:159],v[76:79],v[52:55],v[156:159]
ds_read_u16_d16_hi v138,v138
ds_read_b64_tr_b16 v[106:107],v6 offset:1664
v_lshlrev_b32_sdwa v139,1,v139 dst_sel:DWORD dst_unused:UNUSED_PAD src0_sel:DWORD src1_sel:WORD_1
v_mfma_f32_16x16x32_bf16 v[148:151],v[76:79],v[48:51],v[148:151]
ds_read_u16 v137,v137
ds_read_b64_tr_b16 v[108:109],v6 offset:136
v_rcp_f16_sdwa v121,v120 dst_sel:WORD_1 dst_unused:UNUSED_PRESERVE src0_sel:WORD_1
v_mfma_f32_16x16x32_bf16 v[144:147],v[80:83],v[56:59],v[144:147]
ds_read_u16_d16_hi v139,v139
ds_read_b64_tr_b16 v[110:111],v6 offset:1672
v_lshlrev_b32_sdwa v140,1,v140 dst_sel:DWORD dst_unused:UNUSED_PAD src0_sel:DWORD src1_sel:WORD_1
v_fma_mix_f32 v141,v141,v121,0 op_sel_hi:[0,1,0]
v_mfma_f32_16x16x32_bf16 v[152:155],v[80:83],v[60:63],v[152:155]
ds_read_u16 v140,v140
ds_read_b64_tr_b16 v[64:65],v4 offset:0
v_lshlrev_b32_sdwa v142,1,v142 dst_sel:DWORD dst_unused:UNUSED_PAD src0_sel:DWORD src1_sel:WORD_1
v_mfma_f32_16x16x32_bf16 v[156:159],v[84:87],v[60:63],v[156:159]
ds_read_u16_d16_hi v142,v142
ds_read_b64_tr_b16 v[66:67],v4 offset:2048
v_fma_mix_f32 v143,v143,v121,0 op_sel:[0,1,0] op_sel_hi:[0,1,0]
s_waitcnt lgkmcnt(15)
v_lshl_add_u64 v[128:129],v[128:129],0,v[130:131]
v_mfma_f32_16x16x32_bf16 v[148:151],v[84:87],v[56:59],v[148:151]
ds_read_b128 v[112:115],v7 offset:256
s_waitcnt lgkmcnt(14)
v_lshl_add_u64 v[130:131],v[132:133],0,v[134:135]
v_lshlrev_b32_sdwa v144,1,v144 dst_sel:DWORD dst_unused:UNUSED_PAD src0_sel:DWORD src1_sel:WORD_1
v_lshlrev_b32_sdwa v146,1,v146 dst_sel:DWORD dst_unused:UNUSED_PAD src0_sel:DWORD src1_sel:WORD_1
v_mfma_f32_16x16x32_bf16 v[160:163],v[88:91],v[128:131],v[160:163]
s_waitcnt lgkmcnt(13)
v_lshlrev_b32_sdwa v145,1,v145 dst_sel:DWORD dst_unused:UNUSED_PAD src0_sel:DWORD src1_sel:WORD_1
v_lshlrev_b32_sdwa v147,1,v147 dst_sel:DWORD dst_unused:UNUSED_PAD src0_sel:DWORD src1_sel:WORD_1
v_mfma_f32_16x16x32_bf16 v[168:171],v[92:95],v[128:131],v[168:171]
ds_read_b64_tr_b16 v[68:69],v5 offset:0
ds_read_b64_tr_b16 v[70:71],v5 offset:2048
v_fma_mixlo_f16 v120,v157,s99,0
v_mfma_f32_16x16x32_bf16 v[176:179],v[96:99],v[128:131],v[176:179]
s_waitcnt lgkmcnt(8)
ds_read_u16 v144,v144
v_lshlrev_b32_sdwa v148,1,v148 dst_sel:DWORD dst_unused:UNUSED_PAD src0_sel:DWORD src1_sel:WORD_1
v_lshl_add_u64 v[136:137],v[136:137],0,v[138:139]
v_fma_mixhi_f16 v120,v159,s99,0
v_mfma_f32_16x16x32_bf16 v[184:187],v[100:103],v[128:131],v[184:187]
ds_read_u16_d16_hi v146,v146
v_mfma_f32_16x16x32_bf16 v[192:195],v[104:107],v[128:131],v[192:195]
s_waitcnt lgkmcnt(6)
ds_read_b128 v[116:119],v7 offset:288
ds_read_b64_tr_b16 v[72:73],v4 offset:4096
v_lshlrev_b32_sdwa v150,1,v150 dst_sel:DWORD dst_unused:UNUSED_PAD src0_sel:DWORD src1_sel:WORD_1
v_or_b32 v138,v142,v140
v_cvt_pk_bf16_f32 v139,v141,v143
v_mfma_f32_16x16x32_bf16 v[200:203],v[108:111],v[128:131],v[200:203]
ds_read_b64_tr_b16 v[74:75],v4 offset:6144
ds_read_b64_tr_b16 v[76:77],v5 offset:4096
v_pk_fma_f16 v121,v120,v120,s54
v_lshlrev_b32_sdwa v149,1,v149 dst_sel:DWORD dst_unused:UNUSED_PAD src0_sel:DWORD src1_sel:WORD_1
v_mfma_f32_16x16x32_bf16 v[164:167],v[88:91],v[136:139],v[164:167]
ds_read_u16 v145,v145
v_pk_mul_f16 v121,v120,v121
v_lshlrev_b32_sdwa v151,1,v151 dst_sel:DWORD dst_unused:UNUSED_PAD src0_sel:DWORD src1_sel:WORD_1
v_mfma_f32_16x16x32_bf16 v[172:175],v[92:95],v[136:139],v[172:175]
ds_read_u16_d16_hi v147,v147
v_lshlrev_b32_sdwa v152,1,v152 dst_sel:DWORD dst_unused:UNUSED_PAD src0_sel:DWORD src1_sel:WORD_1
v_exp_f16 v120,v121
v_mfma_f32_16x16x32_bf16 v[180:183],v[96:99],v[136:139],v[180:183]
ds_read_b64_tr_b16 v[78:79],v5 offset:6144
ds_read_b64_tr_b16 v[80:81],v4 offset:8192
v_mfma_f32_16x16x32_bf16 v[188:191],v[100:103],v[136:139],v[188:191]
v_exp_f16_sdwa v120,v121 dst_sel:WORD_1 dst_unused:UNUSED_PRESERVE src0_sel:WORD_1
v_mfma_f32_16x16x32_bf16 v[196:199],v[104:107],v[136:139],v[196:199]
ds_read_u16 v148,v148
ds_read_u16_d16_hi v150,v150
v_lshlrev_b32_sdwa v154,1,v154 dst_sel:DWORD dst_unused:UNUSED_PAD src0_sel:DWORD src1_sel:WORD_1
v_mfma_f32_16x16x32_bf16 v[204:207],v[108:111],v[136:139],v[204:207]
s_waitcnt lgkmcnt(14)
v_pk_fma_f16 v120,v120,s55,s55
v_lshlrev_b32_sdwa v153,1,v153 dst_sel:DWORD dst_unused:UNUSED_PAD src0_sel:DWORD src1_sel:WORD_1
v_mfma_f32_16x16x32_bf16 v[128:131],v[64:67],v[16:19],v[112:115]
ds_read_u16 v149,v149
ds_read_b64_tr_b16 v[82:83],v4 offset:10240
v_rcp_f16 v121,v120
v_mfma_f32_16x16x32_bf16 v[136:139],v[64:67],v[20:23],v[112:115]
s_waitcnt lgkmcnt(11)
ds_read_b64_tr_b16 v[84:85],v5 offset:8192
v_lshlrev_b32_sdwa v155,1,v155 dst_sel:DWORD dst_unused:UNUSED_PAD src0_sel:DWORD src1_sel:WORD_1
v_mfma_f32_16x16x32_bf16 v[140:143],v[68:71],v[20:23],v[116:119]
ds_read_u16_d16_hi v151,v151
v_rcp_f16_sdwa v121,v120 dst_sel:WORD_1 dst_unused:UNUSED_PRESERVE src0_sel:WORD_1
v_mfma_f32_16x16x32_bf16 v[132:135],v[68:71],v[16:19],v[116:119]
s_waitcnt lgkmcnt(11)
ds_read_u16 v152,v152
v_lshlrev_b32_sdwa v156,1,v156 dst_sel:DWORD dst_unused:UNUSED_PAD src0_sel:DWORD src1_sel:WORD_1
v_mfma_f32_16x16x32_bf16 v[128:131],v[72:75],v[24:27],v[128:131]
ds_read_b64_tr_b16 v[86:87],v5 offset:10240
v_fma_mix_f32 v157,v157,v121,0 op_sel_hi:[0,1,0]
v_mfma_f32_16x16x32_bf16 v[136:139],v[72:75],v[28:31],v[136:139]
s_waitcnt lgkmcnt(9)
ds_read_u16_d16_hi v154,v154
v_fma_mix_f32 v159,v159,v121,0 op_sel:[0,1,0] op_sel_hi:[0,1,0]
v_mfma_f32_16x16x32_bf16 v[140:143],v[76:79],v[28:31],v[140:143]
v_mfma_f32_16x16x32_bf16 v[132:135],v[76:79],v[24:27],v[132:135]
s_waitcnt lgkmcnt(5)
ds_read_u16 v153,v153
v_lshlrev_b32_sdwa v158,1,v158 dst_sel:DWORD dst_unused:UNUSED_PAD src0_sel:DWORD src1_sel:WORD_1
v_mfma_f32_16x16x32_bf16 v[128:131],v[80:83],v[32:35],v[128:131]
ds_read_u16_d16_hi v155,v155
v_mfma_f32_16x16x32_bf16 v[136:139],v[80:83],v[36:39],v[136:139]
s_waitcnt lgkmcnt(3)
v_lshl_add_u64 v[144:145],v[144:145],0,v[146:147]
v_mfma_f32_16x16x32_bf16 v[140:143],v[84:87],v[36:39],v[140:143]
ds_read_u16 v156,v156
v_lshl_add_u64 v[146:147],v[148:149],0,v[150:151]
v_mfma_f32_16x16x32_bf16 v[132:135],v[84:87],v[32:35],v[132:135]
v_lshlrev_b32_sdwa v128,1,v128 dst_sel:DWORD dst_unused:UNUSED_PAD src0_sel:DWORD src1_sel:WORD_1
v_lshlrev_b32_sdwa v130,1,v130 dst_sel:DWORD dst_unused:UNUSED_PAD src0_sel:DWORD src1_sel:WORD_1
v_mfma_f32_16x16x32_bf16 v[208:211],v[88:91],v[144:147],v[208:211]
ds_read_u16_d16_hi v158,v158
v_lshlrev_b32_sdwa v129,1,v129 dst_sel:DWORD dst_unused:UNUSED_PAD src0_sel:DWORD src1_sel:WORD_1
v_lshlrev_b32_sdwa v131,1,v131 dst_sel:DWORD dst_unused:UNUSED_PAD src0_sel:DWORD src1_sel:WORD_1
v_mfma_f32_16x16x32_bf16 v[216:219],v[92:95],v[144:147],v[216:219]
v_fma_mixlo_f16 v120,v141,s99,0
v_lshlrev_b32_sdwa v132,1,v132 dst_sel:DWORD dst_unused:UNUSED_PAD src0_sel:DWORD src1_sel:WORD_1
v_mfma_f32_16x16x32_bf16 v[224:227],v[96:99],v[144:147],v[224:227]
ds_read_u16 v128,v128
v_fma_mixhi_f16 v120,v143,s99,0
v_lshlrev_b32_sdwa v134,1,v134 dst_sel:DWORD dst_unused:UNUSED_PAD src0_sel:DWORD src1_sel:WORD_1
v_mfma_f32_16x16x32_bf16 v[232:235],v[100:103],v[144:147],v[232:235]
s_waitcnt lgkmcnt(3)
ds_read_u16_d16_hi v130,v130
v_lshl_add_u64 v[152:153],v[152:153],0,v[154:155]
v_mfma_f32_16x16x32_bf16 v[240:243],v[104:107],v[144:147],v[240:243]
s_waitcnt lgkmcnt(2)
ds_read_u16 v129,v129
v_or_b32 v154,v158,v156
v_cvt_pk_bf16_f32 v155,v157,v159
v_mfma_f32_16x16x32_bf16 v[248:251],v[108:111],v[144:147],v[248:251]
v_pk_fma_f16 v121,v120,v120,s54
v_lshlrev_b32_sdwa v133,1,v133 dst_sel:DWORD dst_unused:UNUSED_PAD src0_sel:DWORD src1_sel:WORD_1
v_mfma_f32_16x16x32_bf16 v[212:215],v[88:91],v[152:155],v[212:215]
ds_read_u16_d16_hi v131,v131
ds_read_b64_tr_b16 v[88:89],v6 offset:6160
v_pk_mul_f16 v121,v120,v121
v_lshlrev_b32_sdwa v135,1,v135 dst_sel:DWORD dst_unused:UNUSED_PAD src0_sel:DWORD src1_sel:WORD_1
v_mfma_f32_16x16x32_bf16 v[220:223],v[92:95],v[152:155],v[220:223]
ds_read_u16 v132,v132
v_exp_f16 v120,v121
v_mfma_f32_16x16x32_bf16 v[228:231],v[96:99],v[152:155],v[228:231]
ds_read_b64_tr_b16 v[90:91],v6 offset:7696
ds_read_b64_tr_b16 v[92:93],v6 offset:6168
v_exp_f16_sdwa v120,v121 dst_sel:WORD_1 dst_unused:UNUSED_PRESERVE src0_sel:WORD_1
v_lshlrev_b32_sdwa v136,1,v136 dst_sel:DWORD dst_unused:UNUSED_PAD src0_sel:DWORD src1_sel:WORD_1
v_mfma_f32_16x16x32_bf16 v[236:239],v[100:103],v[152:155],v[236:239]
ds_read_u16_d16_hi v134,v134
v_mfma_f32_16x16x32_bf16 v[244:247],v[104:107],v[152:155],v[244:247]
v_lshlrev_b32_sdwa v138,1,v138 dst_sel:DWORD dst_unused:UNUSED_PAD src0_sel:DWORD src1_sel:WORD_1
v_mfma_f32_16x16x32_bf16 v[252:255],v[108:111],v[152:155],v[252:255]
ds_read_b64_tr_b16 v[94:95],v6 offset:7704
ds_read_b64_tr_b16 v[96:97],v6 offset:6224
v_pk_fma_f16 v120,v120,s55,s55
v_lshlrev_b32_sdwa v137,1,v137 dst_sel:DWORD dst_unused:UNUSED_PAD src0_sel:DWORD src1_sel:WORD_1
v_mfma_f32_16x16x32_bf16 v[144:147],v[64:67],v[40:43],v[112:115]
ds_read_u16 v133,v133
ds_read_u16_d16_hi v135,v135
v_rcp_f16 v121,v120
v_mfma_f32_16x16x32_bf16 v[152:155],v[64:67],v[44:47],v[112:115]
v_lshlrev_b32_sdwa v139,1,v139 dst_sel:DWORD dst_unused:UNUSED_PAD src0_sel:DWORD src1_sel:WORD_1
v_mfma_f32_16x16x32_bf16 v[156:159],v[68:71],v[44:47],v[116:119]
ds_read_b64_tr_b16 v[98:99],v6 offset:7760
ds_read_b64_tr_b16 v[100:101],v6 offset:6232
v_rcp_f16_sdwa v121,v120 dst_sel:WORD_1 dst_unused:UNUSED_PRESERVE src0_sel:WORD_1
v_mfma_f32_16x16x32_bf16 v[148:151],v[68:71],v[40:43],v[116:119]
ds_read_u16 v136,v136
v_lshlrev_b32_sdwa v140,1,v140 dst_sel:DWORD dst_unused:UNUSED_PAD src0_sel:DWORD src1_sel:WORD_1
v_mfma_f32_16x16x32_bf16 v[144:147],v[72:75],v[48:51],v[144:147]
ds_read_b64_tr_b16 v[102:103],v6 offset:7768
ds_read_b64_tr_b16 v[104:105],v6 offset:6288
v_fma_mix_f32 v141,v141,v121,0 op_sel_hi:[0,1,0]
v_lshlrev_b32_sdwa v142,1,v142 dst_sel:DWORD dst_unused:UNUSED_PAD src0_sel:DWORD src1_sel:WORD_1
v_mfma_f32_16x16x32_bf16 v[152:155],v[72:75],v[52:55],v[152:155]
ds_read_u16_d16_hi v138,v138
v_fma_mix_f32 v143,v143,v121,0 op_sel:[0,1,0] op_sel_hi:[0,1,0]
v_mfma_f32_16x16x32_bf16 v[156:159],v[76:79],v[52:55],v[156:159]
ds_read_b64_tr_b16 v[106:107],v6 offset:7824
ds_read_b64_tr_b16 v[108:109],v6 offset:6296
v_mfma_f32_16x16x32_bf16 v[148:151],v[76:79],v[48:51],v[148:151]
ds_read_u16 v137,v137
v_mfma_f32_16x16x32_bf16 v[144:147],v[80:83],v[56:59],v[144:147]
ds_read_b64_tr_b16 v[110:111],v6 offset:7832
ds_read_b64_tr_b16 v[64:65],v4 offset:512
v_mfma_f32_16x16x32_bf16 v[152:155],v[80:83],v[60:63],v[152:155]
s_waitcnt lgkmcnt(11)
ds_read_u16_d16_hi v139,v139
ds_read_u16 v140,v140
v_lshl_add_u64 v[128:129],v[128:129],0,v[130:131]
v_lshl_add_u64 v[130:131],v[132:133],0,v[134:135]
v_mfma_f32_16x16x32_bf16 v[156:159],v[84:87],v[60:63],v[156:159]
ds_read_u16_d16_hi v142,v142
v_mfma_f32_16x16x32_bf16 v[148:151],v[84:87],v[56:59],v[148:151]
ds_read_b64_tr_b16 v[66:67],v4 offset:2560
ds_read_b128 v[112:115],v7 offset:384
v_lshlrev_b32_sdwa v144,1,v144 dst_sel:DWORD dst_unused:UNUSED_PAD src0_sel:DWORD src1_sel:WORD_1
v_lshlrev_b32_sdwa v146,1,v146 dst_sel:DWORD dst_unused:UNUSED_PAD src0_sel:DWORD src1_sel:WORD_1
v_mfma_f32_16x16x32_bf16 v[160:163],v[88:91],v[128:131],v[160:163]
v_lshlrev_b32_sdwa v145,1,v145 dst_sel:DWORD dst_unused:UNUSED_PAD src0_sel:DWORD src1_sel:WORD_1
v_lshlrev_b32_sdwa v147,1,v147 dst_sel:DWORD dst_unused:UNUSED_PAD src0_sel:DWORD src1_sel:WORD_1
v_mfma_f32_16x16x32_bf16 v[168:171],v[92:95],v[128:131],v[168:171]
s_waitcnt lgkmcnt(15)
ds_read_b64_tr_b16 v[68:69],v5 offset:512
ds_read_b64_tr_b16 v[70:71],v5 offset:2560
v_fma_mixlo_f16 v120,v157,s99,0
v_mfma_f32_16x16x32_bf16 v[176:179],v[96:99],v[128:131],v[176:179]
s_waitcnt lgkmcnt(6)
ds_read_u16 v144,v144
v_lshlrev_b32_sdwa v148,1,v148 dst_sel:DWORD dst_unused:UNUSED_PAD src0_sel:DWORD src1_sel:WORD_1
v_lshl_add_u64 v[136:137],v[136:137],0,v[138:139]
v_fma_mixhi_f16 v120,v159,s99,0
v_mfma_f32_16x16x32_bf16 v[184:187],v[100:103],v[128:131],v[184:187]
ds_read_u16_d16_hi v146,v146
v_mfma_f32_16x16x32_bf16 v[192:195],v[104:107],v[128:131],v[192:195]
s_waitcnt lgkmcnt(6)
ds_read_b128 v[116:119],v7 offset:416
ds_read_b64_tr_b16 v[72:73],v4 offset:4608
v_lshlrev_b32_sdwa v150,1,v150 dst_sel:DWORD dst_unused:UNUSED_PAD src0_sel:DWORD src1_sel:WORD_1
v_or_b32 v138,v142,v140
v_cvt_pk_bf16_f32 v139,v141,v143
v_mfma_f32_16x16x32_bf16 v[200:203],v[108:111],v[128:131],v[200:203]
ds_read_b64_tr_b16 v[74:75],v4 offset:6656
ds_read_b64_tr_b16 v[76:77],v5 offset:4608
v_pk_fma_f16 v121,v120,v120,s54
v_lshlrev_b32_sdwa v149,1,v149 dst_sel:DWORD dst_unused:UNUSED_PAD src0_sel:DWORD src1_sel:WORD_1
v_mfma_f32_16x16x32_bf16 v[164:167],v[88:91],v[136:139],v[164:167]
ds_read_u16 v145,v145
v_pk_mul_f16 v121,v120,v121
v_lshlrev_b32_sdwa v151,1,v151 dst_sel:DWORD dst_unused:UNUSED_PAD src0_sel:DWORD src1_sel:WORD_1
v_mfma_f32_16x16x32_bf16 v[172:175],v[92:95],v[136:139],v[172:175]
ds_read_u16_d16_hi v147,v147
v_lshlrev_b32_sdwa v152,1,v152 dst_sel:DWORD dst_unused:UNUSED_PAD src0_sel:DWORD src1_sel:WORD_1
v_exp_f16 v120,v121
v_mfma_f32_16x16x32_bf16 v[180:183],v[96:99],v[136:139],v[180:183]
s_mov_b32 s86,0xbe801d5a
ds_read_b64_tr_b16 v[78:79],v5 offset:6656
ds_read_b64_tr_b16 v[80:81],v4 offset:8704
v_mfma_f32_16x16x32_bf16 v[188:191],v[100:103],v[136:139],v[188:191]
v_exp_f16_sdwa v120,v121 dst_sel:WORD_1 dst_unused:UNUSED_PRESERVE src0_sel:WORD_1
v_mfma_f32_16x16x32_bf16 v[196:199],v[104:107],v[136:139],v[196:199]
ds_read_u16 v148,v148
ds_read_u16_d16_hi v150,v150
v_lshlrev_b32_sdwa v154,1,v154 dst_sel:DWORD dst_unused:UNUSED_PAD src0_sel:DWORD src1_sel:WORD_1
v_mfma_f32_16x16x32_bf16 v[204:207],v[108:111],v[136:139],v[204:207]
s_waitcnt lgkmcnt(14)
v_pk_fma_f16 v120,v120,s55,s55
v_lshlrev_b32_sdwa v153,1,v153 dst_sel:DWORD dst_unused:UNUSED_PAD src0_sel:DWORD src1_sel:WORD_1
v_mfma_f32_16x16x32_bf16 v[128:131],v[64:67],v[16:19],v[112:115]
s_nop 0
ds_read_u16 v149,v149
ds_read_b64_tr_b16 v[82:83],v4 offset:10752
v_rcp_f16 v121,v120
v_mfma_f32_16x16x32_bf16 v[136:139],v[64:67],v[20:23],v[112:115]
s_waitcnt lgkmcnt(11)
ds_read_b64_tr_b16 v[84:85],v5 offset:8704
v_lshlrev_b32_sdwa v155,1,v155 dst_sel:DWORD dst_unused:UNUSED_PAD src0_sel:DWORD src1_sel:WORD_1
v_mfma_f32_16x16x32_bf16 v[140:143],v[68:71],v[20:23],v[116:119]
ds_read_u16_d16_hi v151,v151
v_rcp_f16_sdwa v121,v120 dst_sel:WORD_1 dst_unused:UNUSED_PRESERVE src0_sel:WORD_1
v_mfma_f32_16x16x32_bf16 v[132:135],v[68:71],v[16:19],v[116:119]
s_waitcnt lgkmcnt(11)
ds_read_u16 v152,v152
v_lshlrev_b32_sdwa v156,1,v156 dst_sel:DWORD dst_unused:UNUSED_PAD src0_sel:DWORD src1_sel:WORD_1
v_mfma_f32_16x16x32_bf16 v[128:131],v[72:75],v[24:27],v[128:131]
s_nop 0
ds_read_b64_tr_b16 v[86:87],v5 offset:10752
v_fma_mix_f32 v157,v157,v121,0 op_sel_hi:[0,1,0]
v_mfma_f32_16x16x32_bf16 v[136:139],v[72:75],v[28:31],v[136:139]
s_waitcnt lgkmcnt(9)
ds_read_u16_d16_hi v154,v154
v_fma_mix_f32 v159,v159,v121,0 op_sel:[0,1,0] op_sel_hi:[0,1,0]
v_mfma_f32_16x16x32_bf16 v[140:143],v[76:79],v[28:31],v[140:143]
v_mfma_f32_16x16x32_bf16 v[132:135],v[76:79],v[24:27],v[132:135]
s_waitcnt lgkmcnt(5)
ds_read_u16 v153,v153
v_lshlrev_b32_sdwa v158,1,v158 dst_sel:DWORD dst_unused:UNUSED_PAD src0_sel:DWORD src1_sel:WORD_1
v_mfma_f32_16x16x32_bf16 v[128:131],v[80:83],v[32:35],v[128:131]
s_nop 0
ds_read_u16_d16_hi v155,v155
v_mfma_f32_16x16x32_bf16 v[136:139],v[80:83],v[36:39],v[136:139]
s_waitcnt lgkmcnt(3)
v_lshl_add_u64 v[144:145],v[144:145],0,v[146:147]
v_mfma_f32_16x16x32_bf16 v[140:143],v[84:87],v[36:39],v[140:143]
ds_read_u16 v156,v156
v_lshl_add_u64 v[146:147],v[148:149],0,v[150:151]
v_mfma_f32_16x16x32_bf16 v[132:135],v[84:87],v[32:35],v[132:135]
s_nop 0
v_lshlrev_b32_sdwa v128,1,v128 dst_sel:DWORD dst_unused:UNUSED_PAD src0_sel:DWORD src1_sel:WORD_1
v_lshlrev_b32_sdwa v130,1,v130 dst_sel:DWORD dst_unused:UNUSED_PAD src0_sel:DWORD src1_sel:WORD_1
v_mfma_f32_16x16x32_bf16 v[208:211],v[88:91],v[144:147],v[208:211]
s_barrier
ds_read_u16_d16_hi v158,v158
v_lshlrev_b32_sdwa v129,1,v129 dst_sel:DWORD dst_unused:UNUSED_PAD src0_sel:DWORD src1_sel:WORD_1
v_lshlrev_b32_sdwa v131,1,v131 dst_sel:DWORD dst_unused:UNUSED_PAD src0_sel:DWORD src1_sel:WORD_1
v_mfma_f32_16x16x32_bf16 v[216:219],v[92:95],v[144:147],v[216:219]
v_fma_mixlo_f16 v120,v141,s99,0
v_lshlrev_b32_sdwa v132,1,v132 dst_sel:DWORD dst_unused:UNUSED_PAD src0_sel:DWORD src1_sel:WORD_1
v_mfma_f32_16x16x32_bf16 v[224:227],v[96:99],v[144:147],v[224:227]
ds_read_u16 v128,v128
v_fma_mixhi_f16 v120,v143,s99,0
v_lshlrev_b32_sdwa v134,1,v134 dst_sel:DWORD dst_unused:UNUSED_PAD src0_sel:DWORD src1_sel:WORD_1
v_mfma_f32_16x16x32_bf16 v[232:235],v[100:103],v[144:147],v[232:235]
s_waitcnt lgkmcnt(3)
ds_read_u16_d16_hi v130,v130
v_lshl_add_u64 v[152:153],v[152:153],0,v[154:155]
v_mfma_f32_16x16x32_bf16 v[240:243],v[104:107],v[144:147],v[240:243]
s_waitcnt lgkmcnt(2)
ds_read_u16 v129,v129
v_or_b32 v154,v158,v156
v_cvt_pk_bf16_f32 v155,v157,v159
v_mfma_f32_16x16x32_bf16 v[248:251],v[108:111],v[144:147],v[248:251]
v_pk_fma_f16 v121,v120,v120,s54
v_lshlrev_b32_sdwa v133,1,v133 dst_sel:DWORD dst_unused:UNUSED_PAD src0_sel:DWORD src1_sel:WORD_1
v_mfma_f32_16x16x32_bf16 v[212:215],v[88:91],v[152:155],v[212:215]
ds_read_u16_d16_hi v131,v131
ds_read_b64_tr_b16 v[88:89],v6 offset:12320
v_pk_mul_f16 v121,v120,v121
v_lshlrev_b32_sdwa v135,1,v135 dst_sel:DWORD dst_unused:UNUSED_PAD src0_sel:DWORD src1_sel:WORD_1
v_mfma_f32_16x16x32_bf16 v[220:223],v[92:95],v[152:155],v[220:223]
ds_read_u16 v132,v132
v_exp_f16 v120,v121
v_mfma_f32_16x16x32_bf16 v[228:231],v[96:99],v[152:155],v[228:231]
ds_read_b64_tr_b16 v[90:91],v6 offset:13856
ds_read_b64_tr_b16 v[92:93],v6 offset:12328
v_lshlrev_b32_sdwa v136,1,v136 dst_sel:DWORD dst_unused:UNUSED_PAD src0_sel:DWORD src1_sel:WORD_1
v_mfma_f32_16x16x32_bf16 v[236:239],v[100:103],v[152:155],v[236:239]
ds_read_u16_d16_hi v134,v134
v_exp_f16_sdwa v120,v121 dst_sel:WORD_1 dst_unused:UNUSED_PRESERVE src0_sel:WORD_1
v_mfma_f32_16x16x32_bf16 v[244:247],v[104:107],v[152:155],v[244:247]
v_lshlrev_b32_sdwa v138,1,v138 dst_sel:DWORD dst_unused:UNUSED_PAD src0_sel:DWORD src1_sel:WORD_1
v_mfma_f32_16x16x32_bf16 v[252:255],v[108:111],v[152:155],v[252:255]
ds_read_b64_tr_b16 v[94:95],v6 offset:13864
ds_read_b64_tr_b16 v[96:97],v6 offset:12384
v_pk_fma_f16 v120,v120,s55,s55
v_lshlrev_b32_sdwa v137,1,v137 dst_sel:DWORD dst_unused:UNUSED_PAD src0_sel:DWORD src1_sel:WORD_1
v_mfma_f32_16x16x32_bf16 v[144:147],v[64:67],v[40:43],v[112:115]
ds_read_u16 v133,v133
v_rcp_f16 v121,v120
v_mfma_f32_16x16x32_bf16 v[152:155],v[64:67],v[44:47],v[112:115]
ds_read_b64_tr_b16 v[98:99],v6 offset:13920
ds_read_b64_tr_b16 v[100:101],v6 offset:12392
v_lshlrev_b32_sdwa v139,1,v139 dst_sel:DWORD dst_unused:UNUSED_PAD src0_sel:DWORD src1_sel:WORD_1
v_mfma_f32_16x16x32_bf16 v[156:159],v[68:71],v[44:47],v[116:119]
ds_read_u16_d16_hi v135,v135
v_rcp_f16_sdwa v121,v120 dst_sel:WORD_1 dst_unused:UNUSED_PRESERVE src0_sel:WORD_1
v_mfma_f32_16x16x32_bf16 v[148:151],v[68:71],v[40:43],v[116:119]
ds_read_u16 v136,v136
v_lshlrev_b32_sdwa v140,1,v140 dst_sel:DWORD dst_unused:UNUSED_PAD src0_sel:DWORD src1_sel:WORD_1
v_mfma_f32_16x16x32_bf16 v[144:147],v[72:75],v[48:51],v[144:147]
ds_read_b64_tr_b16 v[102:103],v6 offset:13928
ds_read_b64_tr_b16 v[104:105],v6 offset:12448
v_fma_mix_f32 v141,v141,v121,0 op_sel_hi:[0,1,0]
v_lshlrev_b32_sdwa v142,1,v142 dst_sel:DWORD dst_unused:UNUSED_PAD src0_sel:DWORD src1_sel:WORD_1
v_mfma_f32_16x16x32_bf16 v[152:155],v[72:75],v[52:55],v[152:155]
ds_read_u16_d16_hi v138,v138
v_fma_mix_f32 v143,v143,v121,0 op_sel:[0,1,0] op_sel_hi:[0,1,0]
v_mfma_f32_16x16x32_bf16 v[156:159],v[76:79],v[52:55],v[156:159]
ds_read_b64_tr_b16 v[106:107],v6 offset:13984
ds_read_b64_tr_b16 v[108:109],v6 offset:12456
v_mfma_f32_16x16x32_bf16 v[148:151],v[76:79],v[48:51],v[148:151]
ds_read_u16 v137,v137
v_mfma_f32_16x16x32_bf16 v[144:147],v[80:83],v[56:59],v[144:147]
ds_read_b64_tr_b16 v[110:111],v6 offset:13992
ds_read_b64_tr_b16 v[64:65],v4 offset:12320
v_mfma_f32_16x16x32_bf16 v[152:155],v[80:83],v[60:63],v[152:155]
s_waitcnt lgkmcnt(9)
ds_read_u16_d16_hi v139,v139
v_lshl_add_u64 v[128:129],v[128:129],0,v[130:131]
v_lshl_add_u64 v[130:131],v[132:133],0,v[134:135]
v_mfma_f32_16x16x32_bf16 v[156:159],v[84:87],v[60:63],v[156:159]
ds_read_u16 v140,v140
v_mfma_f32_16x16x32_bf16 v[148:151],v[84:87],v[56:59],v[148:151]
ds_read_b64_tr_b16 v[66:67],v4 offset:14368
ds_read_b128 v[112:115],v7 offset:512
v_lshlrev_b32_sdwa v144,1,v144 dst_sel:DWORD dst_unused:UNUSED_PAD src0_sel:DWORD src1_sel:WORD_1
v_lshlrev_b32_sdwa v146,1,v146 dst_sel:DWORD dst_unused:UNUSED_PAD src0_sel:DWORD src1_sel:WORD_1
v_mfma_f32_16x16x32_bf16 v[160:163],v[88:91],v[128:131],v[160:163]
ds_read_u16_d16_hi v142,v142
v_lshlrev_b32_sdwa v145,1,v145 dst_sel:DWORD dst_unused:UNUSED_PAD src0_sel:DWORD src1_sel:WORD_1
v_lshlrev_b32_sdwa v147,1,v147 dst_sel:DWORD dst_unused:UNUSED_PAD src0_sel:DWORD src1_sel:WORD_1
v_mfma_f32_16x16x32_bf16 v[168:171],v[92:95],v[128:131],v[168:171]
s_waitcnt lgkmcnt(12)
ds_read_b64_tr_b16 v[68:69],v5 offset:12320
ds_read_b64_tr_b16 v[70:71],v5 offset:14368
v_fma_mixlo_f16 v120,v157,s99,0
v_mfma_f32_16x16x32_bf16 v[176:179],v[96:99],v[128:131],v[176:179]
s_waitcnt lgkmcnt(6)
ds_read_u16 v144,v144
v_lshl_add_u64 v[136:137],v[136:137],0,v[138:139]
v_lshlrev_b32_sdwa v148,1,v148 dst_sel:DWORD dst_unused:UNUSED_PAD src0_sel:DWORD src1_sel:WORD_1
v_fma_mixhi_f16 v120,v159,s99,0
v_lshlrev_b32_sdwa v150,1,v150 dst_sel:DWORD dst_unused:UNUSED_PAD src0_sel:DWORD src1_sel:WORD_1
v_mfma_f32_16x16x32_bf16 v[184:187],v[100:103],v[128:131],v[184:187]
ds_read_u16_d16_hi v146,v146
v_mfma_f32_16x16x32_bf16 v[192:195],v[104:107],v[128:131],v[192:195]
s_waitcnt lgkmcnt(4)
ds_read_b128 v[116:119],v7 offset:544
ds_read_b64_tr_b16 v[72:73],v4 offset:16416
v_or_b32 v138,v142,v140
v_cvt_pk_bf16_f32 v139,v141,v143
v_mfma_f32_16x16x32_bf16 v[200:203],v[108:111],v[128:131],v[200:203]
ds_read_b64_tr_b16 v[74:75],v4 offset:18464
ds_read_b64_tr_b16 v[76:77],v5 offset:16416
v_pk_fma_f16 v121,v120,v120,s54
v_lshlrev_b32_sdwa v149,1,v149 dst_sel:DWORD dst_unused:UNUSED_PAD src0_sel:DWORD src1_sel:WORD_1
v_mfma_f32_16x16x32_bf16 v[164:167],v[88:91],v[136:139],v[164:167]
ds_read_u16 v145,v145
v_pk_mul_f16 v121,v120,v121
v_lshlrev_b32_sdwa v151,1,v151 dst_sel:DWORD dst_unused:UNUSED_PAD src0_sel:DWORD src1_sel:WORD_1
v_mfma_f32_16x16x32_bf16 v[172:175],v[92:95],v[136:139],v[172:175]
ds_read_u16_d16_hi v147,v147
v_lshlrev_b32_sdwa v152,1,v152 dst_sel:DWORD dst_unused:UNUSED_PAD src0_sel:DWORD src1_sel:WORD_1
v_exp_f16 v120,v121
v_mfma_f32_16x16x32_bf16 v[180:183],v[96:99],v[136:139],v[180:183]
ds_read_b64_tr_b16 v[78:79],v5 offset:18464
ds_read_b64_tr_b16 v[80:81],v4 offset:20512
v_mfma_f32_16x16x32_bf16 v[188:191],v[100:103],v[136:139],v[188:191]
v_exp_f16_sdwa v120,v121 dst_sel:WORD_1 dst_unused:UNUSED_PRESERVE src0_sel:WORD_1
v_mfma_f32_16x16x32_bf16 v[196:199],v[104:107],v[136:139],v[196:199]
ds_read_u16 v148,v148
ds_read_u16_d16_hi v150,v150
v_lshlrev_b32_sdwa v154,1,v154 dst_sel:DWORD dst_unused:UNUSED_PAD src0_sel:DWORD src1_sel:WORD_1
v_mfma_f32_16x16x32_bf16 v[204:207],v[108:111],v[136:139],v[204:207]
v_pk_fma_f16 v120,v120,s55,s55
v_lshlrev_b32_sdwa v153,1,v153 dst_sel:DWORD dst_unused:UNUSED_PAD src0_sel:DWORD src1_sel:WORD_1
v_mfma_f32_16x16x32_bf16 v[128:131],v[64:67],v[16:19],v[112:115]
ds_read_u16 v149,v149
ds_read_b64_tr_b16 v[82:83],v4 offset:22560
v_rcp_f16 v121,v120
v_mfma_f32_16x16x32_bf16 v[136:139],v[64:67],v[20:23],v[112:115]
s_waitcnt lgkmcnt(11)
ds_read_b64_tr_b16 v[84:85],v5 offset:20512
v_lshlrev_b32_sdwa v155,1,v155 dst_sel:DWORD dst_unused:UNUSED_PAD src0_sel:DWORD src1_sel:WORD_1
v_mfma_f32_16x16x32_bf16 v[140:143],v[68:71],v[20:23],v[116:119]
ds_read_u16_d16_hi v151,v151
v_rcp_f16_sdwa v121,v120 dst_sel:WORD_1 dst_unused:UNUSED_PRESERVE src0_sel:WORD_1
v_mfma_f32_16x16x32_bf16 v[132:135],v[68:71],v[16:19],v[116:119]
s_waitcnt lgkmcnt(11)
ds_read_u16 v152,v152
v_lshlrev_b32_sdwa v156,1,v156 dst_sel:DWORD dst_unused:UNUSED_PAD src0_sel:DWORD src1_sel:WORD_1
v_mfma_f32_16x16x32_bf16 v[128:131],v[72:75],v[24:27],v[128:131]
ds_read_b64_tr_b16 v[86:87],v5 offset:22560
v_fma_mix_f32 v157,v157,v121,0 op_sel_hi:[0,1,0]
v_mfma_f32_16x16x32_bf16 v[136:139],v[72:75],v[28:31],v[136:139]
s_waitcnt lgkmcnt(9)
ds_read_u16_d16_hi v154,v154
v_fma_mix_f32 v159,v159,v121,0 op_sel:[0,1,0] op_sel_hi:[0,1,0]
v_mfma_f32_16x16x32_bf16 v[140:143],v[76:79],v[28:31],v[140:143]
v_mfma_f32_16x16x32_bf16 v[132:135],v[76:79],v[24:27],v[132:135]
s_waitcnt lgkmcnt(5)
ds_read_u16 v153,v153
v_lshlrev_b32_sdwa v158,1,v158 dst_sel:DWORD dst_unused:UNUSED_PAD src0_sel:DWORD src1_sel:WORD_1
v_mfma_f32_16x16x32_bf16 v[128:131],v[80:83],v[32:35],v[128:131]
ds_read_u16_d16_hi v155,v155
v_mfma_f32_16x16x32_bf16 v[136:139],v[80:83],v[36:39],v[136:139]
s_waitcnt lgkmcnt(3)
ds_read_u16 v156,v156
v_lshl_add_u64 v[144:145],v[144:145],0,v[146:147]
v_mfma_f32_16x16x32_bf16 v[140:143],v[84:87],v[36:39],v[140:143]
v_lshl_add_u64 v[146:147],v[148:149],0,v[150:151]
v_mfma_f32_16x16x32_bf16 v[132:135],v[84:87],v[32:35],v[132:135]
v_lshlrev_b32_sdwa v128,1,v128 dst_sel:DWORD dst_unused:UNUSED_PAD src0_sel:DWORD src1_sel:WORD_1
v_lshlrev_b32_sdwa v130,1,v130 dst_sel:DWORD dst_unused:UNUSED_PAD src0_sel:DWORD src1_sel:WORD_1
v_mfma_f32_16x16x32_bf16 v[208:211],v[88:91],v[144:147],v[208:211]
ds_read_u16_d16_hi v158,v158
v_lshlrev_b32_sdwa v129,1,v129 dst_sel:DWORD dst_unused:UNUSED_PAD src0_sel:DWORD src1_sel:WORD_1
v_lshlrev_b32_sdwa v131,1,v131 dst_sel:DWORD dst_unused:UNUSED_PAD src0_sel:DWORD src1_sel:WORD_1
v_mfma_f32_16x16x32_bf16 v[216:219],v[92:95],v[144:147],v[216:219]
v_fma_mixlo_f16 v120,v141,s99,0
v_lshlrev_b32_sdwa v132,1,v132 dst_sel:DWORD dst_unused:UNUSED_PAD src0_sel:DWORD src1_sel:WORD_1
v_mfma_f32_16x16x32_bf16 v[224:227],v[96:99],v[144:147],v[224:227]
ds_read_u16 v128,v128
v_fma_mixhi_f16 v120,v143,s99,0
v_lshlrev_b32_sdwa v134,1,v134 dst_sel:DWORD dst_unused:UNUSED_PAD src0_sel:DWORD src1_sel:WORD_1
v_mfma_f32_16x16x32_bf16 v[232:235],v[100:103],v[144:147],v[232:235]
s_waitcnt lgkmcnt(3)
ds_read_u16_d16_hi v130,v130
v_lshl_add_u64 v[152:153],v[152:153],0,v[154:155]
v_mfma_f32_16x16x32_bf16 v[240:243],v[104:107],v[144:147],v[240:243]
s_waitcnt lgkmcnt(2)
ds_read_u16 v129,v129
v_or_b32 v154,v158,v156
v_cvt_pk_bf16_f32 v155,v157,v159
v_mfma_f32_16x16x32_bf16 v[248:251],v[108:111],v[144:147],v[248:251]
v_pk_fma_f16 v121,v120,v120,s54
v_lshlrev_b32_sdwa v133,1,v133 dst_sel:DWORD dst_unused:UNUSED_PAD src0_sel:DWORD src1_sel:WORD_1
v_mfma_f32_16x16x32_bf16 v[212:215],v[88:91],v[152:155],v[212:215]
ds_read_u16_d16_hi v131,v131
ds_read_b64_tr_b16 v[88:89],v6 offset:18480
v_pk_mul_f16 v121,v120,v121
v_lshlrev_b32_sdwa v135,1,v135 dst_sel:DWORD dst_unused:UNUSED_PAD src0_sel:DWORD src1_sel:WORD_1
v_mfma_f32_16x16x32_bf16 v[220:223],v[92:95],v[152:155],v[220:223]
ds_read_u16 v132,v132
v_exp_f16 v120,v121
v_mfma_f32_16x16x32_bf16 v[228:231],v[96:99],v[152:155],v[228:231]
ds_read_b64_tr_b16 v[90:91],v6 offset:20016
ds_read_b64_tr_b16 v[92:93],v6 offset:18488
v_lshlrev_b32_sdwa v136,1,v136 dst_sel:DWORD dst_unused:UNUSED_PAD src0_sel:DWORD src1_sel:WORD_1
v_mfma_f32_16x16x32_bf16 v[236:239],v[100:103],v[152:155],v[236:239]
ds_read_u16_d16_hi v134,v134
v_exp_f16_sdwa v120,v121 dst_sel:WORD_1 dst_unused:UNUSED_PRESERVE src0_sel:WORD_1
v_mfma_f32_16x16x32_bf16 v[244:247],v[104:107],v[152:155],v[244:247]
v_lshlrev_b32_sdwa v138,1,v138 dst_sel:DWORD dst_unused:UNUSED_PAD src0_sel:DWORD src1_sel:WORD_1
v_mfma_f32_16x16x32_bf16 v[252:255],v[108:111],v[152:155],v[252:255]
ds_read_b64_tr_b16 v[94:95],v6 offset:20024
ds_read_b64_tr_b16 v[96:97],v6 offset:18544
v_pk_fma_f16 v120,v120,s55,s55
v_lshlrev_b32_sdwa v137,1,v137 dst_sel:DWORD dst_unused:UNUSED_PAD src0_sel:DWORD src1_sel:WORD_1
v_mfma_f32_16x16x32_bf16 v[144:147],v[64:67],v[40:43],v[112:115]
ds_read_u16 v133,v133
v_rcp_f16 v121,v120
v_mfma_f32_16x16x32_bf16 v[152:155],v[64:67],v[44:47],v[112:115]
ds_read_b64_tr_b16 v[98:99],v6 offset:20080
ds_read_b64_tr_b16 v[100:101],v6 offset:18552
v_lshlrev_b32_sdwa v139,1,v139 dst_sel:DWORD dst_unused:UNUSED_PAD src0_sel:DWORD src1_sel:WORD_1
v_mfma_f32_16x16x32_bf16 v[156:159],v[68:71],v[44:47],v[116:119]
ds_read_u16_d16_hi v135,v135
v_rcp_f16_sdwa v121,v120 dst_sel:WORD_1 dst_unused:UNUSED_PRESERVE src0_sel:WORD_1
v_mfma_f32_16x16x32_bf16 v[148:151],v[68:71],v[40:43],v[116:119]
ds_read_u16 v136,v136
v_lshlrev_b32_sdwa v140,1,v140 dst_sel:DWORD dst_unused:UNUSED_PAD src0_sel:DWORD src1_sel:WORD_1
v_mfma_f32_16x16x32_bf16 v[144:147],v[72:75],v[48:51],v[144:147]
ds_read_b64_tr_b16 v[102:103],v6 offset:20088
ds_read_b64_tr_b16 v[104:105],v6 offset:18608
v_fma_mix_f32 v141,v141,v121,0 op_sel_hi:[0,1,0]
v_lshlrev_b32_sdwa v142,1,v142 dst_sel:DWORD dst_unused:UNUSED_PAD src0_sel:DWORD src1_sel:WORD_1
v_mfma_f32_16x16x32_bf16 v[152:155],v[72:75],v[52:55],v[152:155]
ds_read_u16_d16_hi v138,v138
v_fma_mix_f32 v143,v143,v121,0 op_sel:[0,1,0] op_sel_hi:[0,1,0]
v_mfma_f32_16x16x32_bf16 v[156:159],v[76:79],v[52:55],v[156:159]
ds_read_b64_tr_b16 v[106:107],v6 offset:20144
ds_read_b64_tr_b16 v[108:109],v6 offset:18616
v_mfma_f32_16x16x32_bf16 v[148:151],v[76:79],v[48:51],v[148:151]
ds_read_u16 v137,v137
v_mfma_f32_16x16x32_bf16 v[144:147],v[80:83],v[56:59],v[144:147]
ds_read_b64_tr_b16 v[110:111],v6 offset:20152
ds_read_b64_tr_b16 v[64:65],v4 offset:12832
v_mfma_f32_16x16x32_bf16 v[152:155],v[80:83],v[60:63],v[152:155]
s_waitcnt lgkmcnt(9)
ds_read_u16_d16_hi v139,v139
v_lshl_add_u64 v[128:129],v[128:129],0,v[130:131]
v_lshl_add_u64 v[130:131],v[132:133],0,v[134:135]
v_mfma_f32_16x16x32_bf16 v[156:159],v[84:87],v[60:63],v[156:159]
ds_read_u16 v140,v140
v_mfma_f32_16x16x32_bf16 v[148:151],v[84:87],v[56:59],v[148:151]
ds_read_b64_tr_b16 v[66:67],v4 offset:14880
ds_read_b128 v[112:115],v7 offset:640
v_lshlrev_b32_sdwa v144,1,v144 dst_sel:DWORD dst_unused:UNUSED_PAD src0_sel:DWORD src1_sel:WORD_1
v_lshlrev_b32_sdwa v146,1,v146 dst_sel:DWORD dst_unused:UNUSED_PAD src0_sel:DWORD src1_sel:WORD_1
v_mfma_f32_16x16x32_bf16 v[160:163],v[88:91],v[128:131],v[160:163]
ds_read_u16_d16_hi v142,v142
v_lshlrev_b32_sdwa v145,1,v145 dst_sel:DWORD dst_unused:UNUSED_PAD src0_sel:DWORD src1_sel:WORD_1
v_lshlrev_b32_sdwa v147,1,v147 dst_sel:DWORD dst_unused:UNUSED_PAD src0_sel:DWORD src1_sel:WORD_1
v_mfma_f32_16x16x32_bf16 v[168:171],v[92:95],v[128:131],v[168:171]
s_waitcnt lgkmcnt(12)
ds_read_b64_tr_b16 v[68:69],v5 offset:12832
ds_read_b64_tr_b16 v[70:71],v5 offset:14880
v_fma_mixlo_f16 v120,v157,s99,0
v_mfma_f32_16x16x32_bf16 v[176:179],v[96:99],v[128:131],v[176:179]
s_waitcnt lgkmcnt(6)
ds_read_u16 v144,v144
v_lshl_add_u64 v[136:137],v[136:137],0,v[138:139]
v_lshlrev_b32_sdwa v148,1,v148 dst_sel:DWORD dst_unused:UNUSED_PAD src0_sel:DWORD src1_sel:WORD_1
v_fma_mixhi_f16 v120,v159,s99,0
v_lshlrev_b32_sdwa v150,1,v150 dst_sel:DWORD dst_unused:UNUSED_PAD src0_sel:DWORD src1_sel:WORD_1
v_mfma_f32_16x16x32_bf16 v[184:187],v[100:103],v[128:131],v[184:187]
ds_read_u16_d16_hi v146,v146
v_mfma_f32_16x16x32_bf16 v[192:195],v[104:107],v[128:131],v[192:195]
s_waitcnt lgkmcnt(4)
ds_read_b128 v[116:119],v7 offset:672
ds_read_b64_tr_b16 v[72:73],v4 offset:16928
v_or_b32 v138,v142,v140
v_cvt_pk_bf16_f32 v139,v141,v143
v_mfma_f32_16x16x32_bf16 v[200:203],v[108:111],v[128:131],v[200:203]
ds_read_b64_tr_b16 v[74:75],v4 offset:18976
ds_read_b64_tr_b16 v[76:77],v5 offset:16928
v_pk_fma_f16 v121,v120,v120,s54
v_lshlrev_b32_sdwa v149,1,v149 dst_sel:DWORD dst_unused:UNUSED_PAD src0_sel:DWORD src1_sel:WORD_1
v_mfma_f32_16x16x32_bf16 v[164:167],v[88:91],v[136:139],v[164:167]
ds_read_u16 v145,v145
v_pk_mul_f16 v121,v120,v121
v_lshlrev_b32_sdwa v151,1,v151 dst_sel:DWORD dst_unused:UNUSED_PAD src0_sel:DWORD src1_sel:WORD_1
v_mfma_f32_16x16x32_bf16 v[172:175],v[92:95],v[136:139],v[172:175]
ds_read_u16_d16_hi v147,v147
v_lshlrev_b32_sdwa v152,1,v152 dst_sel:DWORD dst_unused:UNUSED_PAD src0_sel:DWORD src1_sel:WORD_1
v_exp_f16 v120,v121
v_mfma_f32_16x16x32_bf16 v[180:183],v[96:99],v[136:139],v[180:183]
ds_read_b64_tr_b16 v[78:79],v5 offset:18976
ds_read_b64_tr_b16 v[80:81],v4 offset:21024
v_mfma_f32_16x16x32_bf16 v[188:191],v[100:103],v[136:139],v[188:191]
v_exp_f16_sdwa v120,v121 dst_sel:WORD_1 dst_unused:UNUSED_PRESERVE src0_sel:WORD_1
v_mfma_f32_16x16x32_bf16 v[196:199],v[104:107],v[136:139],v[196:199]
ds_read_u16 v148,v148
ds_read_u16_d16_hi v150,v150
v_lshlrev_b32_sdwa v154,1,v154 dst_sel:DWORD dst_unused:UNUSED_PAD src0_sel:DWORD src1_sel:WORD_1
v_mfma_f32_16x16x32_bf16 v[204:207],v[108:111],v[136:139],v[204:207]
v_pk_fma_f16 v120,v120,s55,s55
v_lshlrev_b32_sdwa v153,1,v153 dst_sel:DWORD dst_unused:UNUSED_PAD src0_sel:DWORD src1_sel:WORD_1
v_mfma_f32_16x16x32_bf16 v[128:131],v[64:67],v[16:19],v[112:115]
s_nop 0
ds_read_u16 v149,v149
ds_read_b64_tr_b16 v[82:83],v4 offset:23072
v_rcp_f16 v121,v120
v_mfma_f32_16x16x32_bf16 v[136:139],v[64:67],v[20:23],v[112:115]
s_waitcnt lgkmcnt(11)
ds_read_b64_tr_b16 v[84:85],v5 offset:21024
v_lshlrev_b32_sdwa v155,1,v155 dst_sel:DWORD dst_unused:UNUSED_PAD src0_sel:DWORD src1_sel:WORD_1
v_mfma_f32_16x16x32_bf16 v[140:143],v[68:71],v[20:23],v[116:119]
ds_read_u16_d16_hi v151,v151
v_rcp_f16_sdwa v121,v120 dst_sel:WORD_1 dst_unused:UNUSED_PRESERVE src0_sel:WORD_1
v_mfma_f32_16x16x32_bf16 v[132:135],v[68:71],v[16:19],v[116:119]
s_waitcnt lgkmcnt(11)
ds_read_u16 v152,v152
v_lshlrev_b32_sdwa v156,1,v156 dst_sel:DWORD dst_unused:UNUSED_PAD src0_sel:DWORD src1_sel:WORD_1
v_mfma_f32_16x16x32_bf16 v[128:131],v[72:75],v[24:27],v[128:131]
s_nop 0
ds_read_b64_tr_b16 v[86:87],v5 offset:23072
v_fma_mix_f32 v157,v157,v121,0 op_sel_hi:[0,1,0]
v_mfma_f32_16x16x32_bf16 v[136:139],v[72:75],v[28:31],v[136:139]
s_waitcnt lgkmcnt(9)
ds_read_u16_d16_hi v154,v154
v_fma_mix_f32 v159,v159,v121,0 op_sel:[0,1,0] op_sel_hi:[0,1,0]
v_mfma_f32_16x16x32_bf16 v[140:143],v[76:79],v[28:31],v[140:143]
v_mfma_f32_16x16x32_bf16 v[132:135],v[76:79],v[24:27],v[132:135]
s_waitcnt lgkmcnt(5)
ds_read_u16 v153,v153
v_lshlrev_b32_sdwa v158,1,v158 dst_sel:DWORD dst_unused:UNUSED_PAD src0_sel:DWORD src1_sel:WORD_1
v_mfma_f32_16x16x32_bf16 v[128:131],v[80:83],v[32:35],v[128:131]
s_nop 0
ds_read_u16_d16_hi v155,v155
v_mfma_f32_16x16x32_bf16 v[136:139],v[80:83],v[36:39],v[136:139]
s_waitcnt lgkmcnt(3)
ds_read_u16 v156,v156
v_lshl_add_u64 v[144:145],v[144:145],0,v[146:147]
v_mfma_f32_16x16x32_bf16 v[140:143],v[84:87],v[36:39],v[140:143]
v_lshl_add_u64 v[146:147],v[148:149],0,v[150:151]
v_mfma_f32_16x16x32_bf16 v[132:135],v[84:87],v[32:35],v[132:135]
s_nop 0
v_lshlrev_b32_sdwa v128,1,v128 dst_sel:DWORD dst_unused:UNUSED_PAD src0_sel:DWORD src1_sel:WORD_1
v_lshlrev_b32_sdwa v130,1,v130 dst_sel:DWORD dst_unused:UNUSED_PAD src0_sel:DWORD src1_sel:WORD_1
v_mfma_f32_16x16x32_bf16 v[208:211],v[88:91],v[144:147],v[208:211]
s_barrier
s_setprio 3
v_lshlrev_b32_sdwa v129,1,v129 dst_sel:DWORD dst_unused:UNUSED_PAD src0_sel:DWORD src1_sel:WORD_1
v_lshlrev_b32_sdwa v131,1,v131 dst_sel:DWORD dst_unused:UNUSED_PAD src0_sel:DWORD src1_sel:WORD_1
v_mfma_f32_16x16x32_bf16 v[216:219],v[92:95],v[144:147],v[216:219]
s_cbranch_vccnz RA1_%=
s_mov_b64 exec,s[62:63]
s_add_u32 m0,s57,48832
s_add_u32 s64,s56,3342336
buffer_load_dwordx4 v2,s[36:39],s64 offen lds
s_add_u32 m0,s57,50560
s_nop 0
buffer_load_dwordx4 v3,s[36:39],s64 offen lds
s_mov_b64 exec,-1
RA1_%=:
v_fma_mixlo_f16 v120,v141,s99,0
v_lshlrev_b32_sdwa v132,1,v132 dst_sel:DWORD dst_unused:UNUSED_PAD src0_sel:DWORD src1_sel:WORD_1
v_mfma_f32_16x16x32_bf16 v[224:227],v[96:99],v[144:147],v[224:227]
ds_read_u16_d16_hi v158,v158
v_fma_mixhi_f16 v120,v143,s99,0
v_lshlrev_b32_sdwa v134,1,v134 dst_sel:DWORD dst_unused:UNUSED_PAD src0_sel:DWORD src1_sel:WORD_1
v_mfma_f32_16x16x32_bf16 v[232:235],v[100:103],v[144:147],v[232:235]
s_waitcnt lgkmcnt(2)
ds_read_u16 v128,v128
v_lshl_add_u64 v[152:153],v[152:153],0,v[154:155]
v_mfma_f32_16x16x32_bf16 v[240:243],v[104:107],v[144:147],v[240:243]
s_waitcnt lgkmcnt(1)
v_or_b32 v154,v158,v156
v_cvt_pk_bf16_f32 v155,v157,v159
v_mfma_f32_16x16x32_bf16 v[248:251],v[108:111],v[144:147],v[248:251]
ds_read_u16_d16_hi v130,v130
v_pk_fma_f16 v121,v120,v120,s54
v_lshlrev_b32_sdwa v133,1,v133 dst_sel:DWORD dst_unused:UNUSED_PAD src0_sel:DWORD src1_sel:WORD_1
v_mfma_f32_16x16x32_bf16 v[212:215],v[88:91],v[152:155],v[212:215]
ds_read_u16 v129,v129
v_pk_mul_f16 v121,v120,v121
v_exp_f16 v120,v121
v_mfma_f32_16x16x32_bf16 v[220:223],v[92:95],v[152:155],v[220:223]
s_cbranch_vccnz RA2_%=
s_mov_b64 exec,s[62:63]
s_add_u32 m0,s57,52320
s_add_u32 s64,s56,3440640
buffer_load_dwordx4 v2,s[36:39],s64 offen lds
s_add_u32 m0,s57,54048
s_nop 0
buffer_load_dwordx4 v3,s[36:39],s64 offen lds
s_mov_b64 exec,-1
RA2_%=:
ds_read_b64_tr_b16 v[88:89],v6 offset:0
v_lshlrev_b32_sdwa v135,1,v135 dst_sel:DWORD dst_unused:UNUSED_PAD src0_sel:DWORD src1_sel:WORD_1
v_mfma_f32_16x16x32_bf16 v[228:231],v[96:99],v[152:155],v[228:231]
ds_read_b64_tr_b16 v[90:91],v6 offset:1536
ds_read_b64_tr_b16 v[92:93],v6 offset:8
v_lshlrev_b32_sdwa v136,1,v136 dst_sel:DWORD dst_unused:UNUSED_PAD src0_sel:DWORD src1_sel:WORD_1
v_mfma_f32_16x16x32_bf16 v[236:239],v[100:103],v[152:155],v[236:239]
ds_read_u16_d16_hi v131,v131
ds_read_u16 v132,v132
v_exp_f16_sdwa v120,v121 dst_sel:WORD_1 dst_unused:UNUSED_PRESERVE src0_sel:WORD_1
v_mfma_f32_16x16x32_bf16 v[244:247],v[104:107],v[152:155],v[244:247]
v_lshlrev_b32_sdwa v138,1,v138 dst_sel:DWORD dst_unused:UNUSED_PAD src0_sel:DWORD src1_sel:WORD_1
v_mfma_f32_16x16x32_bf16 v[252:255],v[108:111],v[152:155],v[252:255]
ds_read_b64_tr_b16 v[94:95],v6 offset:1544
ds_read_b64_tr_b16 v[96:97],v6 offset:64
v_pk_fma_f16 v120,v120,s55,s55
v_lshlrev_b32_sdwa v137,1,v137 dst_sel:DWORD dst_unused:UNUSED_PAD src0_sel:DWORD src1_sel:WORD_1
v_mfma_f32_16x16x32_bf16 v[144:147],v[64:67],v[40:43],v[112:115]
ds_read_u16_d16_hi v134,v134
v_mfma_f32_16x16x32_bf16 v[152:155],v[64:67],v[44:47],v[112:115]
ds_read_b64_tr_b16 v[98:99],v6 offset:1600
ds_read_b64_tr_b16 v[100:101],v6 offset:72
v_rcp_f16 v121,v120
v_lshlrev_b32_sdwa v139,1,v139 dst_sel:DWORD dst_unused:UNUSED_PAD src0_sel:DWORD src1_sel:WORD_1
v_mfma_f32_16x16x32_bf16 v[156:159],v[68:71],v[44:47],v[116:119]
ds_read_u16 v133,v133
v_mfma_f32_16x16x32_bf16 v[148:151],v[68:71],v[40:43],v[116:119]
ds_read_u16_d16_hi v135,v135
v_lshlrev_b32_sdwa v140,1,v140 dst_sel:DWORD dst_unused:UNUSED_PAD src0_sel:DWORD src1_sel:WORD_1
v_rcp_f16_sdwa v121,v120 dst_sel:WORD_1 dst_unused:UNUSED_PRESERVE src0_sel:WORD_1
v_mfma_f32_16x16x32_bf16 v[144:147],v[72:75],v[48:51],v[144:147]
ds_read_b64_tr_b16 v[102:103],v6 offset:1608
ds_read_b64_tr_b16 v[104:105],v6 offset:128
v_fma_mix_f32 v141,v141,v121,0 op_sel_hi:[0,1,0]
v_lshlrev_b32_sdwa v142,1,v142 dst_sel:DWORD dst_unused:UNUSED_PAD src0_sel:DWORD src1_sel:WORD_1
v_mfma_f32_16x16x32_bf16 v[152:155],v[72:75],v[52:55],v[152:155]
ds_read_b64_tr_b16 v[106:107],v6 offset:1664
ds_read_b64_tr_b16 v[108:109],v6 offset:136
v_fma_mix_f32 v143,v143,v121,0 op_sel:[0,1,0] op_sel_hi:[0,1,0]
v_mfma_f32_16x16x32_bf16 v[156:159],v[76:79],v[52:55],v[156:159]
ds_read_u16 v136,v136
v_mfma_f32_16x16x32_bf16 v[148:151],v[76:79],v[48:51],v[148:151]
ds_read_u16_d16_hi v138,v138
v_mfma_f32_16x16x32_bf16 v[144:147],v[80:83],v[56:59],v[144:147]
ds_read_b64_tr_b16 v[110:111],v6 offset:1672
ds_read_b64_tr_b16 v[64:65],v4 offset:0
v_mfma_f32_16x16x32_bf16 v[152:155],v[80:83],v[60:63],v[152:155]
s_waitcnt lgkmcnt(8)
ds_read_u16 v137,v137
v_lshl_add_u64 v[128:129],v[128:129],0,v[130:131]
v_lshl_add_u64 v[130:131],v[132:133],0,v[134:135]
v_mfma_f32_16x16x32_bf16 v[156:159],v[84:87],v[60:63],v[156:159]
ds_read_b64_tr_b16 v[66:67],v4 offset:2048
v_mfma_f32_16x16x32_bf16 v[148:151],v[84:87],v[56:59],v[148:151]
ds_read_u16_d16_hi v139,v139
v_lshlrev_b32_sdwa v144,1,v144 dst_sel:DWORD dst_unused:UNUSED_PAD src0_sel:DWORD src1_sel:WORD_1
v_lshlrev_b32_sdwa v146,1,v146 dst_sel:DWORD dst_unused:UNUSED_PAD src0_sel:DWORD src1_sel:WORD_1
v_mfma_f32_16x16x32_bf16 v[160:163],v[88:91],v[128:131],v[160:163]
ds_read_u16 v140,v140
v_lshlrev_b32_sdwa v145,1,v145 dst_sel:DWORD dst_unused:UNUSED_PAD src0_sel:DWORD src1_sel:WORD_1
v_lshlrev_b32_sdwa v147,1,v147 dst_sel:DWORD dst_unused:UNUSED_PAD src0_sel:DWORD src1_sel:WORD_1
v_mfma_f32_16x16x32_bf16 v[168:171],v[92:95],v[128:131],v[168:171]
ds_read_b128 v[112:115],v7 offset:768
ds_read_b64_tr_b16 v[68:69],v5 offset:0
v_fma_mixlo_f16 v120,v157,s99,0
v_mfma_f32_16x16x32_bf16 v[176:179],v[96:99],v[128:131],v[176:179]
s_waitcnt lgkmcnt(3)
ds_read_u16_d16_hi v142,v142
ds_read_b64_tr_b16 v[70:71],v5 offset:2048
v_lshlrev_b32_sdwa v148,1,v148 dst_sel:DWORD dst_unused:UNUSED_PAD src0_sel:DWORD src1_sel:WORD_1
v_lshl_add_u64 v[136:137],v[136:137],0,v[138:139]
v_fma_mixhi_f16 v120,v159,s99,0
v_lshlrev_b32_sdwa v150,1,v150 dst_sel:DWORD dst_unused:UNUSED_PAD src0_sel:DWORD src1_sel:WORD_1
v_mfma_f32_16x16x32_bf16 v[184:187],v[100:103],v[128:131],v[184:187]
ds_read_b128 v[116:119],v7 offset:800
ds_read_u16 v144,v144
v_mfma_f32_16x16x32_bf16 v[192:195],v[104:107],v[128:131],v[192:195]
s_waitcnt lgkmcnt(3)
ds_read_b64_tr_b16 v[72:73],v4 offset:4096
v_or_b32 v138,v142,v140
v_cvt_pk_bf16_f32 v139,v141,v143
v_mfma_f32_16x16x32_bf16 v[200:203],v[108:111],v[128:131],v[200:203]
ds_read_b64_tr_b16 v[74:75],v4 offset:6144
ds_read_b64_tr_b16 v[76:77],v5 offset:4096
v_pk_fma_f16 v121,v120,v120,s54
v_lshlrev_b32_sdwa v149,1,v149 dst_sel:DWORD dst_unused:UNUSED_PAD src0_sel:DWORD src1_sel:WORD_1
v_mfma_f32_16x16x32_bf16 v[164:167],v[88:91],v[136:139],v[164:167]
ds_read_u16_d16_hi v146,v146
v_pk_mul_f16 v121,v120,v121
v_lshlrev_b32_sdwa v151,1,v151 dst_sel:DWORD dst_unused:UNUSED_PAD src0_sel:DWORD src1_sel:WORD_1
v_mfma_f32_16x16x32_bf16 v[172:175],v[92:95],v[136:139],v[172:175]
ds_read_u16 v145,v145
v_exp_f16 v120,v121
v_mfma_f32_16x16x32_bf16 v[180:183],v[96:99],v[136:139],v[180:183]
s_mov_b32 s86,0xbe801d5a
ds_read_b64_tr_b16 v[78:79],v5 offset:6144
ds_read_b64_tr_b16 v[80:81],v4 offset:8192
v_mfma_f32_16x16x32_bf16 v[188:191],v[100:103],v[136:139],v[188:191]
v_exp_f16_sdwa v120,v121 dst_sel:WORD_1 dst_unused:UNUSED_PRESERVE src0_sel:WORD_1
v_lshlrev_b32_sdwa v152,1,v152 dst_sel:DWORD dst_unused:UNUSED_PAD src0_sel:DWORD src1_sel:WORD_1
v_mfma_f32_16x16x32_bf16 v[196:199],v[104:107],v[136:139],v[196:199]
ds_read_u16_d16_hi v147,v147
ds_read_u16 v148,v148
v_lshlrev_b32_sdwa v154,1,v154 dst_sel:DWORD dst_unused:UNUSED_PAD src0_sel:DWORD src1_sel:WORD_1
v_mfma_f32_16x16x32_bf16 v[204:207],v[108:111],v[136:139],v[204:207]
v_pk_fma_f16 v120,v120,s55,s55
v_lshlrev_b32_sdwa v153,1,v153 dst_sel:DWORD dst_unused:UNUSED_PAD src0_sel:DWORD src1_sel:WORD_1
v_mfma_f32_16x16x32_bf16 v[128:131],v[64:67],v[16:19],v[112:115]
ds_read_u16_d16_hi v150,v150
ds_read_b64_tr_b16 v[82:83],v4 offset:10240
v_rcp_f16 v121,v120
v_mfma_f32_16x16x32_bf16 v[136:139],v[64:67],v[20:23],v[112:115]
s_waitcnt lgkmcnt(12)
ds_read_b64_tr_b16 v[84:85],v5 offset:8192
v_lshlrev_b32_sdwa v155,1,v155 dst_sel:DWORD dst_unused:UNUSED_PAD src0_sel:DWORD src1_sel:WORD_1
v_mfma_f32_16x16x32_bf16 v[140:143],v[68:71],v[20:23],v[116:119]
ds_read_u16 v149,v149
v_rcp_f16_sdwa v121,v120 dst_sel:WORD_1 dst_unused:UNUSED_PRESERVE src0_sel:WORD_1
v_mfma_f32_16x16x32_bf16 v[132:135],v[68:71],v[16:19],v[116:119]
s_waitcnt lgkmcnt(11)
ds_read_u16_d16_hi v151,v151
v_lshlrev_b32_sdwa v156,1,v156 dst_sel:DWORD dst_unused:UNUSED_PAD src0_sel:DWORD src1_sel:WORD_1
v_fma_mix_f32 v157,v157,v121,0 op_sel_hi:[0,1,0]
v_mfma_f32_16x16x32_bf16 v[128:131],v[72:75],v[24:27],v[128:131]
v_mfma_f32_16x16x32_bf16 v[136:139],v[72:75],v[28:31],v[136:139]
s_waitcnt lgkmcnt(8)
ds_read_b64_tr_b16 v[86:87],v5 offset:10240
ds_read_u16 v152,v152
v_fma_mix_f32 v159,v159,v121,0 op_sel:[0,1,0] op_sel_hi:[0,1,0]
v_mfma_f32_16x16x32_bf16 v[140:143],v[76:79],v[28:31],v[140:143]
v_mfma_f32_16x16x32_bf16 v[132:135],v[76:79],v[24:27],v[132:135]
s_waitcnt lgkmcnt(5)
ds_read_u16_d16_hi v154,v154
v_lshlrev_b32_sdwa v158,1,v158 dst_sel:DWORD dst_unused:UNUSED_PAD src0_sel:DWORD src1_sel:WORD_1
v_mfma_f32_16x16x32_bf16 v[128:131],v[80:83],v[32:35],v[128:131]
v_mfma_f32_16x16x32_bf16 v[136:139],v[80:83],v[36:39],v[136:139]
s_waitcnt lgkmcnt(2)
v_lshl_add_u64 v[144:145],v[144:145],0,v[146:147]
v_mfma_f32_16x16x32_bf16 v[140:143],v[84:87],v[36:39],v[140:143]
ds_read_u16 v153,v153
ds_read_u16_d16_hi v155,v155
v_lshl_add_u64 v[146:147],v[148:149],0,v[150:151]
v_mfma_f32_16x16x32_bf16 v[132:135],v[84:87],v[32:35],v[132:135]
ds_read_u16 v156,v156
v_lshlrev_b32_sdwa v128,1,v128 dst_sel:DWORD dst_unused:UNUSED_PAD src0_sel:DWORD src1_sel:WORD_1
v_lshlrev_b32_sdwa v130,1,v130 dst_sel:DWORD dst_unused:UNUSED_PAD src0_sel:DWORD src1_sel:WORD_1
v_mfma_f32_16x16x32_bf16 v[208:211],v[88:91],v[144:147],v[208:211]
v_lshlrev_b32_sdwa v129,1,v129 dst_sel:DWORD dst_unused:UNUSED_PAD src0_sel:DWORD src1_sel:WORD_1
v_lshlrev_b32_sdwa v131,1,v131 dst_sel:DWORD dst_unused:UNUSED_PAD src0_sel:DWORD src1_sel:WORD_1
v_mfma_f32_16x16x32_bf16 v[216:219],v[92:95],v[144:147],v[216:219]
v_fma_mixlo_f16 v120,v141,s99,0
v_lshlrev_b32_sdwa v132,1,v132 dst_sel:DWORD dst_unused:UNUSED_PAD src0_sel:DWORD src1_sel:WORD_1
v_mfma_f32_16x16x32_bf16 v[224:227],v[96:99],v[144:147],v[224:227]
ds_read_u16_d16_hi v158,v158
v_fma_mixhi_f16 v120,v143,s99,0
v_lshlrev_b32_sdwa v134,1,v134 dst_sel:DWORD dst_unused:UNUSED_PAD src0_sel:DWORD src1_sel:WORD_1
v_mfma_f32_16x16x32_bf16 v[232:235],v[100:103],v[144:147],v[232:235]
s_waitcnt lgkmcnt(2)
ds_read_u16 v128,v128
v_lshl_add_u64 v[152:153],v[152:153],0,v[154:155]
v_mfma_f32_16x16x32_bf16 v[240:243],v[104:107],v[144:147],v[240:243]
s_waitcnt lgkmcnt(1)
v_or_b32 v154,v158,v156
v_cvt_pk_bf16_f32 v155,v157,v159
v_mfma_f32_16x16x32_bf16 v[248:251],v[108:111],v[144:147],v[248:251]
ds_read_u16_d16_hi v130,v130
v_pk_fma_f16 v121,v120,v120,s54
v_lshlrev_b32_sdwa v133,1,v133 dst_sel:DWORD dst_unused:UNUSED_PAD src0_sel:DWORD src1_sel:WORD_1
v_mfma_f32_16x16x32_bf16 v[212:215],v[88:91],v[152:155],v[212:215]
ds_read_u16 v129,v129
v_pk_mul_f16 v121,v120,v121
v_exp_f16 v120,v121
v_mfma_f32_16x16x32_bf16 v[220:223],v[92:95],v[152:155],v[220:223]
ds_read_b64_tr_b16 v[88:89],v6 offset:6160
v_lshlrev_b32_sdwa v135,1,v135 dst_sel:DWORD dst_unused:UNUSED_PAD src0_sel:DWORD src1_sel:WORD_1
v_mfma_f32_16x16x32_bf16 v[228:231],v[96:99],v[152:155],v[228:231]
ds_read_b64_tr_b16 v[90:91],v6 offset:7696
ds_read_b64_tr_b16 v[92:93],v6 offset:6168
v_lshlrev_b32_sdwa v136,1,v136 dst_sel:DWORD dst_unused:UNUSED_PAD src0_sel:DWORD src1_sel:WORD_1
v_mfma_f32_16x16x32_bf16 v[236:239],v[100:103],v[152:155],v[236:239]
ds_read_u16_d16_hi v131,v131
ds_read_u16 v132,v132
v_exp_f16_sdwa v120,v121 dst_sel:WORD_1 dst_unused:UNUSED_PRESERVE src0_sel:WORD_1
v_mfma_f32_16x16x32_bf16 v[244:247],v[104:107],v[152:155],v[244:247]
v_lshlrev_b32_sdwa v138,1,v138 dst_sel:DWORD dst_unused:UNUSED_PAD src0_sel:DWORD src1_sel:WORD_1
v_mfma_f32_16x16x32_bf16 v[252:255],v[108:111],v[152:155],v[252:255]
ds_read_b64_tr_b16 v[94:95],v6 offset:7704
ds_read_b64_tr_b16 v[96:97],v6 offset:6224
v_pk_fma_f16 v120,v120,s55,s55
v_lshlrev_b32_sdwa v137,1,v137 dst_sel:DWORD dst_unused:UNUSED_PAD src0_sel:DWORD src1_sel:WORD_1
v_mfma_f32_16x16x32_bf16 v[144:147],v[64:67],v[40:43],v[112:115]
ds_read_u16_d16_hi v134,v134
v_mfma_f32_16x16x32_bf16 v[152:155],v[64:67],v[44:47],v[112:115]
ds_read_b64_tr_b16 v[98:99],v6 offset:7760
ds_read_b64_tr_b16 v[100:101],v6 offset:6232
v_rcp_f16 v121,v120
v_lshlrev_b32_sdwa v139,1,v139 dst_sel:DWORD dst_unused:UNUSED_PAD src0_sel:DWORD src1_sel:WORD_1
v_mfma_f32_16x16x32_bf16 v[156:159],v[68:71],v[44:47],v[116:119]
ds_read_u16 v133,v133
v_mfma_f32_16x16x32_bf16 v[148:151],v[68:71],v[40:43],v[116:119]
ds_read_u16_d16_hi v135,v135
v_lshlrev_b32_sdwa v140,1,v140 dst_sel:DWORD dst_unused:UNUSED_PAD src0_sel:DWORD src1_sel:WORD_1
v_rcp_f16_sdwa v121,v120 dst_sel:WORD_1 dst_unused:UNUSED_PRESERVE src0_sel:WORD_1
v_mfma_f32_16x16x32_bf16 v[144:147],v[72:75],v[48:51],v[144:147]
s_setprio 2
ds_read_b64_tr_b16 v[102:103],v6 offset:7768
ds_read_b64_tr_b16 v[104:105],v6 offset:6288
v_fma_mix_f32 v141,v141,v121,0 op_sel_hi:[0,1,0]
v_lshlrev_b32_sdwa v142,1,v142 dst_sel:DWORD dst_unused:UNUSED_PAD src0_sel:DWORD src1_sel:WORD_1
v_mfma_f32_16x16x32_bf16 v[152:155],v[72:75],v[52:55],v[152:155]
ds_read_b64_tr_b16 v[106:107],v6 offset:7824
ds_read_b64_tr_b16 v[108:109],v6 offset:6296
v_fma_mix_f32 v143,v143,v121,0 op_sel:[0,1,0] op_sel_hi:[0,1,0]
v_mfma_f32_16x16x32_bf16 v[156:159],v[76:79],v[52:55],v[156:159]
ds_read_u16 v136,v136
v_mfma_f32_16x16x32_bf16 v[148:151],v[76:79],v[48:51],v[148:151]
ds_read_u16_d16_hi v138,v138
v_mfma_f32_16x16x32_bf16 v[144:147],v[80:83],v[56:59],v[144:147]
ds_read_b64_tr_b16 v[110:111],v6 offset:7832
ds_read_b64_tr_b16 v[64:65],v4 offset:512
v_mfma_f32_16x16x32_bf16 v[152:155],v[80:83],v[60:63],v[152:155]
s_waitcnt lgkmcnt(8)
ds_read_u16 v137,v137
v_lshl_add_u64 v[128:129],v[128:129],0,v[130:131]
v_lshl_add_u64 v[130:131],v[132:133],0,v[134:135]
v_mfma_f32_16x16x32_bf16 v[156:159],v[84:87],v[60:63],v[156:159]
ds_read_b64_tr_b16 v[66:67],v4 offset:2560
v_mfma_f32_16x16x32_bf16 v[148:151],v[84:87],v[56:59],v[148:151]
ds_read_u16_d16_hi v139,v139
v_lshlrev_b32_sdwa v144,1,v144 dst_sel:DWORD dst_unused:UNUSED_PAD src0_sel:DWORD src1_sel:WORD_1
v_lshlrev_b32_sdwa v146,1,v146 dst_sel:DWORD dst_unused:UNUSED_PAD src0_sel:DWORD src1_sel:WORD_1
v_mfma_f32_16x16x32_bf16 v[160:163],v[88:91],v[128:131],v[160:163]
ds_read_u16 v140,v140
v_lshlrev_b32_sdwa v145,1,v145 dst_sel:DWORD dst_unused:UNUSED_PAD src0_sel:DWORD src1_sel:WORD_1
v_lshlrev_b32_sdwa v147,1,v147 dst_sel:DWORD dst_unused:UNUSED_PAD src0_sel:DWORD src1_sel:WORD_1
v_mfma_f32_16x16x32_bf16 v[168:171],v[92:95],v[128:131],v[168:171]
ds_read_b128 v[112:115],v7 offset:896
ds_read_b64_tr_b16 v[68:69],v5 offset:512
v_fma_mixlo_f16 v120,v157,s99,0
v_mfma_f32_16x16x32_bf16 v[176:179],v[96:99],v[128:131],v[176:179]
s_waitcnt lgkmcnt(3)
ds_read_u16_d16_hi v142,v142
ds_read_b64_tr_b16 v[70:71],v5 offset:2560
v_lshlrev_b32_sdwa v148,1,v148 dst_sel:DWORD dst_unused:UNUSED_PAD src0_sel:DWORD src1_sel:WORD_1
v_lshl_add_u64 v[136:137],v[136:137],0,v[138:139]
v_fma_mixhi_f16 v120,v159,s99,0
v_lshlrev_b32_sdwa v150,1,v150 dst_sel:DWORD dst_unused:UNUSED_PAD src0_sel:DWORD src1_sel:WORD_1
v_mfma_f32_16x16x32_bf16 v[184:187],v[100:103],v[128:131],v[184:187]
s_setprio 1
ds_read_b128 v[116:119],v7 offset:928
ds_read_u16 v144,v144
v_mfma_f32_16x16x32_bf16 v[192:195],v[104:107],v[128:131],v[192:195]
s_waitcnt lgkmcnt(3)
ds_read_b64_tr_b16 v[72:73],v4 offset:4608
v_or_b32 v138,v142,v140
v_cvt_pk_bf16_f32 v139,v141,v143
v_mfma_f32_16x16x32_bf16 v[200:203],v[108:111],v[128:131],v[200:203]
ds_read_b64_tr_b16 v[74:75],v4 offset:6656
ds_read_b64_tr_b16 v[76:77],v5 offset:4608
v_pk_fma_f16 v121,v120,v120,s54
v_lshlrev_b32_sdwa v149,1,v149 dst_sel:DWORD dst_unused:UNUSED_PAD src0_sel:DWORD src1_sel:WORD_1
v_mfma_f32_16x16x32_bf16 v[164:167],v[88:91],v[136:139],v[164:167]
ds_read_u16_d16_hi v146,v146
v_pk_mul_f16 v121,v120,v121
v_lshlrev_b32_sdwa v151,1,v151 dst_sel:DWORD dst_unused:UNUSED_PAD src0_sel:DWORD src1_sel:WORD_1
v_mfma_f32_16x16x32_bf16 v[172:175],v[92:95],v[136:139],v[172:175]
s_nop 0
ds_read_u16 v145,v145
v_exp_f16 v120,v121
v_mfma_f32_16x16x32_bf16 v[180:183],v[96:99],v[136:139],v[180:183]
s_setprio 0
ds_read_b64_tr_b16 v[78:79],v5 offset:6656
ds_read_b64_tr_b16 v[80:81],v4 offset:8704
v_mfma_f32_16x16x32_bf16 v[188:191],v[100:103],v[136:139],v[188:191]
v_exp_f16_sdwa v120,v121 dst_sel:WORD_1 dst_unused:UNUSED_PRESERVE src0_sel:WORD_1
v_lshlrev_b32_sdwa v152,1,v152 dst_sel:DWORD dst_unused:UNUSED_PAD src0_sel:DWORD src1_sel:WORD_1
v_mfma_f32_16x16x32_bf16 v[196:199],v[104:107],v[136:139],v[196:199]
s_nop 0
ds_read_u16_d16_hi v147,v147
ds_read_u16 v148,v148
v_lshlrev_b32_sdwa v154,1,v154 dst_sel:DWORD dst_unused:UNUSED_PAD src0_sel:DWORD src1_sel:WORD_1
v_mfma_f32_16x16x32_bf16 v[204:207],v[108:111],v[136:139],v[204:207]
v_pk_fma_f16 v120,v120,s55,s55
v_lshlrev_b32_sdwa v153,1,v153 dst_sel:DWORD dst_unused:UNUSED_PAD src0_sel:DWORD src1_sel:WORD_1
v_mfma_f32_16x16x32_bf16 v[128:131],v[64:67],v[16:19],v[112:115]
s_nop 0
ds_read_u16_d16_hi v150,v150
ds_read_b64_tr_b16 v[82:83],v4 offset:10752
v_rcp_f16 v121,v120
v_mfma_f32_16x16x32_bf16 v[136:139],v[64:67],v[20:23],v[112:115]
s_waitcnt lgkmcnt(12)
ds_read_b64_tr_b16 v[84:85],v5 offset:8704
v_lshlrev_b32_sdwa v155,1,v155 dst_sel:DWORD dst_unused:UNUSED_PAD src0_sel:DWORD src1_sel:WORD_1
v_mfma_f32_16x16x32_bf16 v[140:143],v[68:71],v[20:23],v[116:119]
ds_read_u16 v149,v149
v_rcp_f16_sdwa v121,v120 dst_sel:WORD_1 dst_unused:UNUSED_PRESERVE src0_sel:WORD_1
v_mfma_f32_16x16x32_bf16 v[132:135],v[68:71],v[16:19],v[116:119]
s_waitcnt lgkmcnt(11)
ds_read_u16_d16_hi v151,v151
v_lshlrev_b32_sdwa v156,1,v156 dst_sel:DWORD dst_unused:UNUSED_PAD src0_sel:DWORD src1_sel:WORD_1
v_fma_mix_f32 v157,v157,v121,0 op_sel_hi:[0,1,0]
v_mfma_f32_16x16x32_bf16 v[128:131],v[72:75],v[24:27],v[128:131]
s_nop 0
v_mfma_f32_16x16x32_bf16 v[136:139],v[72:75],v[28:31],v[136:139]
s_waitcnt lgkmcnt(8)
ds_read_b64_tr_b16 v[86:87],v5 offset:10752
ds_read_u16 v152,v152
v_fma_mix_f32 v159,v159,v121,0 op_sel:[0,1,0] op_sel_hi:[0,1,0]
v_mfma_f32_16x16x32_bf16 v[140:143],v[76:79],v[28:31],v[140:143]
v_mfma_f32_16x16x32_bf16 v[132:135],v[76:79],v[24:27],v[132:135]
s_waitcnt lgkmcnt(5)
ds_read_u16_d16_hi v154,v154
v_lshlrev_b32_sdwa v158,1,v158 dst_sel:DWORD dst_unused:UNUSED_PAD src0_sel:DWORD src1_sel:WORD_1
v_mfma_f32_16x16x32_bf16 v[128:131],v[80:83],v[32:35],v[128:131]
s_nop 0
v_mfma_f32_16x16x32_bf16 v[136:139],v[80:83],v[36:39],v[136:139]
s_waitcnt lgkmcnt(2)
v_lshl_add_u64 v[144:145],v[144:145],0,v[146:147]
v_mfma_f32_16x16x32_bf16 v[140:143],v[84:87],v[36:39],v[140:143]
ds_read_u16 v153,v153
ds_read_u16_d16_hi v155,v155
v_lshl_add_u64 v[146:147],v[148:149],0,v[150:151]
v_mfma_f32_16x16x32_bf16 v[132:135],v[84:87],v[32:35],v[132:135]
s_nop 0
ds_read_u16 v156,v156
v_lshlrev_b32_sdwa v128,1,v128 dst_sel:DWORD dst_unused:UNUSED_PAD src0_sel:DWORD src1_sel:WORD_1
v_lshlrev_b32_sdwa v130,1,v130 dst_sel:DWORD dst_unused:UNUSED_PAD src0_sel:DWORD src1_sel:WORD_1
v_mfma_f32_16x16x32_bf16 v[208:211],v[88:91],v[144:147],v[208:211]
s_barrier
s_setprio 2
ds_read_u16_d16_hi v158,v158
v_lshlrev_b32_sdwa v129,1,v129 dst_sel:DWORD dst_unused:UNUSED_PAD src0_sel:DWORD src1_sel:WORD_1
v_lshlrev_b32_sdwa v131,1,v131 dst_sel:DWORD dst_unused:UNUSED_PAD src0_sel:DWORD src1_sel:WORD_1
v_mfma_f32_16x16x32_bf16 v[216:219],v[92:95],v[144:147],v[216:219]
s_cbranch_vccnz RA3_%=
s_mov_b64 exec,s[62:63]
s_add_u32 m0,s57,55808
s_add_u32 s64,s56,3538944
buffer_load_dwordx4 v2,s[36:39],s64 offen lds
s_add_u32 m0,s57,57536
s_nop 0
buffer_load_dwordx4 v3,s[36:39],s64 offen lds
s_mov_b64 exec,-1
RA3_%=:
v_mfma_f32_16x16x32_bf16 v[224:227],v[96:99],v[144:147],v[224:227]
ds_read_u16 v128,v128
v_lshlrev_b32_sdwa v132,1,v132 dst_sel:DWORD dst_unused:UNUSED_PAD src0_sel:DWORD src1_sel:WORD_1
v_mfma_f32_16x16x32_bf16 v[232:235],v[100:103],v[144:147],v[232:235]
s_waitcnt lgkmcnt(3)
v_lshl_add_u64 v[152:153],v[152:153],0,v[154:155]
v_lshlrev_b32_sdwa v134,1,v134 dst_sel:DWORD dst_unused:UNUSED_PAD src0_sel:DWORD src1_sel:WORD_1
v_cvt_pk_bf16_f32 v155,v157,v159
v_mfma_f32_16x16x32_bf16 v[240:243],v[104:107],v[144:147],v[240:243]
s_waitcnt lgkmcnt(1)
ds_read_u16_d16_hi v130,v130
v_or_b32 v154,v158,v156
v_mfma_f32_16x16x32_bf16 v[248:251],v[108:111],v[144:147],v[248:251]
ds_read_u16 v129,v129
v_lshlrev_b32_sdwa v133,1,v133 dst_sel:DWORD dst_unused:UNUSED_PAD src0_sel:DWORD src1_sel:WORD_1
v_mfma_f32_16x16x32_bf16 v[212:215],v[88:91],v[152:155],v[212:215]
v_lshlrev_b32_sdwa v135,1,v135 dst_sel:DWORD dst_unused:UNUSED_PAD src0_sel:DWORD src1_sel:WORD_1
v_mfma_f32_16x16x32_bf16 v[220:223],v[92:95],v[152:155],v[220:223]
ds_read_u16_d16_hi v131,v131
v_lshlrev_b32_sdwa v136,1,v136 dst_sel:DWORD dst_unused:UNUSED_PAD src0_sel:DWORD src1_sel:WORD_1
v_mfma_f32_16x16x32_bf16 v[228:231],v[96:99],v[152:155],v[228:231]
ds_read_b64_tr_b16 v[88:89],v6 offset:12320
ds_read_b64_tr_b16 v[90:91],v6 offset:13856
v_lshlrev_b32_sdwa v138,1,v138 dst_sel:DWORD dst_unused:UNUSED_PAD src0_sel:DWORD src1_sel:WORD_1
v_mfma_f32_16x16x32_bf16 v[236:239],v[100:103],v[152:155],v[236:239]
ds_read_u16 v132,v132
v_mfma_f32_16x16x32_bf16 v[244:247],v[104:107],v[152:155],v[244:247]
ds_read_u16_d16_hi v134,v134
v_lshlrev_b32_sdwa v137,1,v137 dst_sel:DWORD dst_unused:UNUSED_PAD src0_sel:DWORD src1_sel:WORD_1
v_lshlrev_b32_sdwa v139,1,v139 dst_sel:DWORD dst_unused:UNUSED_PAD src0_sel:DWORD src1_sel:WORD_1
v_mfma_f32_16x16x32_bf16 v[252:255],v[108:111],v[152:155],v[252:255]
ds_read_b64_tr_b16 v[92:93],v6 offset:12328
ds_read_b64_tr_b16 v[94:95],v6 offset:13864
v_lshlrev_b32_sdwa v140,1,v140 dst_sel:DWORD dst_unused:UNUSED_PAD src0_sel:DWORD src1_sel:WORD_1
v_mfma_f32_16x16x32_bf16 v[144:147],v[64:67],v[40:43],v[112:115]
ds_read_u16 v133,v133
v_lshlrev_b32_sdwa v142,1,v142 dst_sel:DWORD dst_unused:UNUSED_PAD src0_sel:DWORD src1_sel:WORD_1
v_mfma_f32_16x16x32_bf16 v[152:155],v[64:67],v[44:47],v[112:115]
ds_read_b64_tr_b16 v[96:97],v6 offset:12384
v_lshlrev_b32_sdwa v141,1,v141 dst_sel:DWORD dst_unused:UNUSED_PAD src0_sel:DWORD src1_sel:WORD_1
v_lshlrev_b32_sdwa v143,1,v143 dst_sel:DWORD dst_unused:UNUSED_PAD src0_sel:DWORD src1_sel:WORD_1
v_mfma_f32_16x16x32_bf16 v[156:159],v[68:71],v[44:47],v[116:119]
ds_read_u16_d16_hi v135,v135
v_mfma_f32_16x16x32_bf16 v[148:151],v[68:71],v[40:43],v[116:119]
ds_read_u16 v136,v136
ds_read_b64_tr_b16 v[98:99],v6 offset:13920
v_mfma_f32_16x16x32_bf16 v[144:147],v[72:75],v[48:51],v[144:147]
ds_read_u16_d16_hi v138,v138
v_mfma_f32_16x16x32_bf16 v[152:155],v[72:75],v[52:55],v[152:155]
ds_read_b64_tr_b16 v[100:101],v6 offset:12392
ds_read_b64_tr_b16 v[102:103],v6 offset:13928
v_mfma_f32_16x16x32_bf16 v[156:159],v[76:79],v[52:55],v[156:159]
ds_read_u16 v137,v137
v_mfma_f32_16x16x32_bf16 v[148:151],v[76:79],v[48:51],v[148:151]
s_waitcnt lgkmcnt(6)
ds_read_u16_d16_hi v139,v139
v_lshl_add_u64 v[128:129],v[128:129],0,v[130:131]
v_lshl_add_u64 v[130:131],v[132:133],0,v[134:135]
v_mfma_f32_16x16x32_bf16 v[144:147],v[80:83],v[56:59],v[144:147]
ds_read_b64_tr_b16 v[104:105],v6 offset:12448
ds_read_b64_tr_b16 v[106:107],v6 offset:13984
v_mfma_f32_16x16x32_bf16 v[152:155],v[80:83],v[60:63],v[152:155]
s_cbranch_vccnz RA4_%=
s_mov_b64 exec,s[62:63]
s_add_u32 m0,s57,59296
s_add_u32 s64,s56,3637248
buffer_load_dwordx4 v2,s[36:39],s64 offen lds
s_add_u32 m0,s57,61024
s_nop 0
buffer_load_dwordx4 v3,s[36:39],s64 offen lds
s_mov_b64 exec,-1
RA4_%=:
ds_read_u16 v140,v140
v_mfma_f32_16x16x32_bf16 v[156:159],v[84:87],v[60:63],v[156:159]
ds_read_u16_d16_hi v142,v142
s_nop 0
v_lshlrev_b32_sdwa v144,1,v144 dst_sel:DWORD dst_unused:UNUSED_PAD src0_sel:DWORD src1_sel:WORD_1
v_mfma_f32_16x16x32_bf16 v[148:151],v[84:87],v[56:59],v[148:151]
ds_read_b64_tr_b16 v[108:109],v6 offset:12456
ds_read_b64_tr_b16 v[110:111],v6 offset:13992
v_lshlrev_b32_sdwa v146,1,v146 dst_sel:DWORD dst_unused:UNUSED_PAD src0_sel:DWORD src1_sel:WORD_1
v_mfma_f32_16x16x32_bf16 v[160:163],v[88:91],v[128:131],v[160:163]
ds_read_b64_tr_b16 v[64:65],v4 offset:12320
ds_read_u16 v141,v141
v_lshlrev_b32_sdwa v145,1,v145 dst_sel:DWORD dst_unused:UNUSED_PAD src0_sel:DWORD src1_sel:WORD_1
v_mfma_f32_16x16x32_bf16 v[168:171],v[92:95],v[128:131],v[168:171]
s_waitcnt lgkmcnt(13)
ds_read_u16_d16_hi v143,v143
v_lshlrev_b32_sdwa v147,1,v147 dst_sel:DWORD dst_unused:UNUSED_PAD src0_sel:DWORD src1_sel:WORD_1
v_mfma_f32_16x16x32_bf16 v[176:179],v[96:99],v[128:131],v[176:179]
s_waitcnt lgkmcnt(11)
ds_read_b64_tr_b16 v[66:67],v4 offset:14368
v_lshlrev_b32_sdwa v148,1,v148 dst_sel:DWORD dst_unused:UNUSED_PAD src0_sel:DWORD src1_sel:WORD_1
v_mfma_f32_16x16x32_bf16 v[184:187],v[100:103],v[128:131],v[184:187]
s_waitcnt lgkmcnt(8)
ds_read_u16 v144,v144
v_lshl_add_u64 v[136:137],v[136:137],0,v[138:139]
v_lshlrev_b32_sdwa v150,1,v150 dst_sel:DWORD dst_unused:UNUSED_PAD src0_sel:DWORD src1_sel:WORD_1
v_mfma_f32_16x16x32_bf16 v[192:195],v[104:107],v[128:131],v[192:195]
s_waitcnt lgkmcnt(2)
ds_read_u16_d16_hi v146,v146
v_lshlrev_b32_sdwa v149,1,v149 dst_sel:DWORD dst_unused:UNUSED_PAD src0_sel:DWORD src1_sel:WORD_1
v_lshl_add_u64 v[138:139],v[140:141],0,v[142:143]
v_mfma_f32_16x16x32_bf16 v[200:203],v[108:111],v[128:131],v[200:203]
ds_read_b128 v[112:115],v7 offset:1024
v_mfma_f32_16x16x32_bf16 v[164:167],v[88:91],v[136:139],v[164:167]
ds_read_u16 v145,v145
v_lshlrev_b32_sdwa v151,1,v151 dst_sel:DWORD dst_unused:UNUSED_PAD src0_sel:DWORD src1_sel:WORD_1
v_mfma_f32_16x16x32_bf16 v[172:175],v[92:95],v[136:139],v[172:175]
ds_read_u16_d16_hi v147,v147
ds_read_b64_tr_b16 v[68:69],v5 offset:12320
v_lshlrev_b32_sdwa v152,1,v152 dst_sel:DWORD dst_unused:UNUSED_PAD src0_sel:DWORD src1_sel:WORD_1
v_mfma_f32_16x16x32_bf16 v[180:183],v[96:99],v[136:139],v[180:183]
ds_read_b64_tr_b16 v[70:71],v5 offset:14368
ds_read_b128 v[116:119],v7 offset:1056
v_lshlrev_b32_sdwa v154,1,v154 dst_sel:DWORD dst_unused:UNUSED_PAD src0_sel:DWORD src1_sel:WORD_1
v_mfma_f32_16x16x32_bf16 v[188:191],v[100:103],v[136:139],v[188:191]
ds_read_u16 v148,v148
ds_read_b64_tr_b16 v[72:73],v4 offset:16416
v_mfma_f32_16x16x32_bf16 v[196:199],v[104:107],v[136:139],v[196:199]
ds_read_u16_d16_hi v150,v150
v_lshlrev_b32_sdwa v153,1,v153 dst_sel:DWORD dst_unused:UNUSED_PAD src0_sel:DWORD src1_sel:WORD_1
v_lshlrev_b32_sdwa v155,1,v155 dst_sel:DWORD dst_unused:UNUSED_PAD src0_sel:DWORD src1_sel:WORD_1
v_mfma_f32_16x16x32_bf16 v[204:207],v[108:111],v[136:139],v[204:207]
s_waitcnt lgkmcnt(8)
ds_read_b64_tr_b16 v[74:75],v4 offset:18464
ds_read_b64_tr_b16 v[76:77],v5 offset:16416
v_mfma_f32_16x16x32_bf16 v[128:131],v[64:67],v[16:19],v[112:115]
ds_read_u16 v149,v149
v_lshlrev_b32_sdwa v156,1,v156 dst_sel:DWORD dst_unused:UNUSED_PAD src0_sel:DWORD src1_sel:WORD_1
v_lshlrev_b32_sdwa v158,1,v158 dst_sel:DWORD dst_unused:UNUSED_PAD src0_sel:DWORD src1_sel:WORD_1
v_mfma_f32_16x16x32_bf16 v[136:139],v[64:67],v[20:23],v[112:115]
s_waitcnt lgkmcnt(6)
ds_read_b64_tr_b16 v[78:79],v5 offset:18464
ds_read_b64_tr_b16 v[80:81],v4 offset:20512
v_lshlrev_b32_sdwa v157,1,v157 dst_sel:DWORD dst_unused:UNUSED_PAD src0_sel:DWORD src1_sel:WORD_1
v_mfma_f32_16x16x32_bf16 v[140:143],v[68:71],v[20:23],v[116:119]
ds_read_u16_d16_hi v151,v151
v_lshlrev_b32_sdwa v159,1,v159 dst_sel:DWORD dst_unused:UNUSED_PAD src0_sel:DWORD src1_sel:WORD_1
v_mfma_f32_16x16x32_bf16 v[132:135],v[68:71],v[16:19],v[116:119]
s_waitcnt lgkmcnt(5)
ds_read_b64_tr_b16 v[82:83],v4 offset:22560
v_mfma_f32_16x16x32_bf16 v[128:131],v[72:75],v[24:27],v[128:131]
s_cbranch_vccnz RA5_%=
s_mov_b64 exec,s[62:63]
s_add_u32 m0,s57,62784
s_add_u32 s64,s56,3735552
buffer_load_dwordx4 v2,s[36:39],s64 offen lds
s_add_u32 m0,s57,64512
s_nop 0
buffer_load_dwordx4 v3,s[36:39],s64 offen lds
s_mov_b64 exec,-1
RA5_%=:
ds_read_b64_tr_b16 v[84:85],v5 offset:20512
ds_read_b64_tr_b16 v[86:87],v5 offset:22560
v_mfma_f32_16x16x32_bf16 v[136:139],v[72:75],v[28:31],v[136:139]
s_waitcnt lgkmcnt(5)
ds_read_u16 v152,v152
ds_read_u16_d16_hi v154,v154
v_mfma_f32_16x16x32_bf16 v[140:143],v[76:79],v[28:31],v[140:143]
ds_read_u16 v153,v153
v_mfma_f32_16x16x32_bf16 v[132:135],v[76:79],v[24:27],v[132:135]
s_waitcnt lgkmcnt(5)
ds_read_u16_d16_hi v155,v155
v_lshl_add_u64 v[144:145],v[144:145],0,v[146:147]
v_mfma_f32_16x16x32_bf16 v[128:131],v[80:83],v[32:35],v[128:131]
v_lshl_add_u64 v[146:147],v[148:149],0,v[150:151]
v_mfma_f32_16x16x32_bf16 v[136:139],v[80:83],v[36:39],v[136:139]
s_waitcnt lgkmcnt(4)
ds_read_u16 v156,v156
v_mfma_f32_16x16x32_bf16 v[140:143],v[84:87],v[36:39],v[140:143]
ds_read_u16_d16_hi v158,v158
v_mfma_f32_16x16x32_bf16 v[132:135],v[84:87],v[32:35],v[132:135]
ds_read_u16 v157,v157
v_lshlrev_b32_sdwa v128,1,v128 dst_sel:DWORD dst_unused:UNUSED_PAD src0_sel:DWORD src1_sel:WORD_1
v_lshlrev_b32_sdwa v130,1,v130 dst_sel:DWORD dst_unused:UNUSED_PAD src0_sel:DWORD src1_sel:WORD_1
v_mfma_f32_16x16x32_bf16 v[208:211],v[88:91],v[144:147],v[208:211]
ds_read_u16_d16_hi v159,v159
v_lshlrev_b32_sdwa v129,1,v129 dst_sel:DWORD dst_unused:UNUSED_PAD src0_sel:DWORD src1_sel:WORD_1
v_lshlrev_b32_sdwa v131,1,v131 dst_sel:DWORD dst_unused:UNUSED_PAD src0_sel:DWORD src1_sel:WORD_1
v_mfma_f32_16x16x32_bf16 v[216:219],v[92:95],v[144:147],v[216:219]
v_mfma_f32_16x16x32_bf16 v[224:227],v[96:99],v[144:147],v[224:227]
ds_read_u16 v128,v128
v_lshlrev_b32_sdwa v132,1,v132 dst_sel:DWORD dst_unused:UNUSED_PAD src0_sel:DWORD src1_sel:WORD_1
v_mfma_f32_16x16x32_bf16 v[232:235],v[100:103],v[144:147],v[232:235]
s_waitcnt lgkmcnt(5)
v_lshl_add_u64 v[152:153],v[152:153],0,v[154:155]
v_lshlrev_b32_sdwa v134,1,v134 dst_sel:DWORD dst_unused:UNUSED_PAD src0_sel:DWORD src1_sel:WORD_1
v_mfma_f32_16x16x32_bf16 v[240:243],v[104:107],v[144:147],v[240:243]
s_waitcnt lgkmcnt(1)
ds_read_u16_d16_hi v130,v130
v_lshl_add_u64 v[154:155],v[156:157],0,v[158:159]
v_mfma_f32_16x16x32_bf16 v[248:251],v[108:111],v[144:147],v[248:251]
ds_read_u16 v129,v129
v_lshlrev_b32_sdwa v133,1,v133 dst_sel:DWORD dst_unused:UNUSED_PAD src0_sel:DWORD src1_sel:WORD_1
v_mfma_f32_16x16x32_bf16 v[212:215],v[88:91],v[152:155],v[212:215]
v_lshlrev_b32_sdwa v135,1,v135 dst_sel:DWORD dst_unused:UNUSED_PAD src0_sel:DWORD src1_sel:WORD_1
v_mfma_f32_16x16x32_bf16 v[220:223],v[92:95],v[152:155],v[220:223]
ds_read_u16_d16_hi v131,v131
v_lshlrev_b32_sdwa v136,1,v136 dst_sel:DWORD dst_unused:UNUSED_PAD src0_sel:DWORD src1_sel:WORD_1
v_mfma_f32_16x16x32_bf16 v[228:231],v[96:99],v[152:155],v[228:231]
ds_read_b64_tr_b16 v[88:89],v6 offset:18480
ds_read_b64_tr_b16 v[90:91],v6 offset:20016
v_lshlrev_b32_sdwa v138,1,v138 dst_sel:DWORD dst_unused:UNUSED_PAD src0_sel:DWORD src1_sel:WORD_1
v_mfma_f32_16x16x32_bf16 v[236:239],v[100:103],v[152:155],v[236:239]
ds_read_u16 v132,v132
v_mfma_f32_16x16x32_bf16 v[244:247],v[104:107],v[152:155],v[244:247]
ds_read_u16_d16_hi v134,v134
v_lshlrev_b32_sdwa v137,1,v137 dst_sel:DWORD dst_unused:UNUSED_PAD src0_sel:DWORD src1_sel:WORD_1
v_lshlrev_b32_sdwa v139,1,v139 dst_sel:DWORD dst_unused:UNUSED_PAD src0_sel:DWORD src1_sel:WORD_1
v_mfma_f32_16x16x32_bf16 v[252:255],v[108:111],v[152:155],v[252:255]
ds_read_b64_tr_b16 v[92:93],v6 offset:18488
ds_read_b64_tr_b16 v[94:95],v6 offset:20024
v_lshlrev_b32_sdwa v140,1,v140 dst_sel:DWORD dst_unused:UNUSED_PAD src0_sel:DWORD src1_sel:WORD_1
v_mfma_f32_16x16x32_bf16 v[144:147],v[64:67],v[40:43],v[112:115]
s_cbranch_vccnz RA6_%=
s_mov_b64 exec,s[62:63]
s_add_u32 m0,s57,66272
s_add_u32 s64,s56,3833856
buffer_load_dwordx4 v2,s[36:39],s64 offen lds
s_add_u32 m0,s57,68000
s_nop 0
buffer_load_dwordx4 v3,s[36:39],s64 offen lds
s_mov_b64 exec,-1
RA6_%=:
ds_read_u16 v133,v133
v_lshlrev_b32_sdwa v142,1,v142 dst_sel:DWORD dst_unused:UNUSED_PAD src0_sel:DWORD src1_sel:WORD_1
v_mfma_f32_16x16x32_bf16 v[152:155],v[64:67],v[44:47],v[112:115]
ds_read_b64_tr_b16 v[96:97],v6 offset:18544
v_lshlrev_b32_sdwa v141,1,v141 dst_sel:DWORD dst_unused:UNUSED_PAD src0_sel:DWORD src1_sel:WORD_1
v_lshlrev_b32_sdwa v143,1,v143 dst_sel:DWORD dst_unused:UNUSED_PAD src0_sel:DWORD src1_sel:WORD_1
v_mfma_f32_16x16x32_bf16 v[156:159],v[68:71],v[44:47],v[116:119]
ds_read_u16_d16_hi v135,v135
v_mfma_f32_16x16x32_bf16 v[148:151],v[68:71],v[40:43],v[116:119]
ds_read_u16 v136,v136
ds_read_b64_tr_b16 v[98:99],v6 offset:20080
v_mfma_f32_16x16x32_bf16 v[144:147],v[72:75],v[48:51],v[144:147]
ds_read_u16_d16_hi v138,v138
v_mfma_f32_16x16x32_bf16 v[152:155],v[72:75],v[52:55],v[152:155]
ds_read_b64_tr_b16 v[100:101],v6 offset:18552
ds_read_b64_tr_b16 v[102:103],v6 offset:20088
v_mfma_f32_16x16x32_bf16 v[156:159],v[76:79],v[52:55],v[156:159]
ds_read_u16 v137,v137
v_mfma_f32_16x16x32_bf16 v[148:151],v[76:79],v[48:51],v[148:151]
s_waitcnt lgkmcnt(6)
ds_read_u16_d16_hi v139,v139
v_lshl_add_u64 v[128:129],v[128:129],0,v[130:131]
v_lshl_add_u64 v[130:131],v[132:133],0,v[134:135]
v_mfma_f32_16x16x32_bf16 v[144:147],v[80:83],v[56:59],v[144:147]
ds_read_b64_tr_b16 v[104:105],v6 offset:18608
ds_read_b64_tr_b16 v[106:107],v6 offset:20144
v_mfma_f32_16x16x32_bf16 v[152:155],v[80:83],v[60:63],v[152:155]
ds_read_u16 v140,v140
v_mfma_f32_16x16x32_bf16 v[156:159],v[84:87],v[60:63],v[156:159]
ds_read_u16_d16_hi v142,v142
s_nop 1
v_lshlrev_b32_sdwa v144,1,v144 dst_sel:DWORD dst_unused:UNUSED_PAD src0_sel:DWORD src1_sel:WORD_1
v_mfma_f32_16x16x32_bf16 v[148:151],v[84:87],v[56:59],v[148:151]
ds_read_b64_tr_b16 v[108:109],v6 offset:18616
ds_read_b64_tr_b16 v[110:111],v6 offset:20152
v_lshlrev_b32_sdwa v146,1,v146 dst_sel:DWORD dst_unused:UNUSED_PAD src0_sel:DWORD src1_sel:WORD_1
v_mfma_f32_16x16x32_bf16 v[160:163],v[88:91],v[128:131],v[160:163]
ds_read_b64_tr_b16 v[64:65],v4 offset:12832
ds_read_u16 v141,v141
v_lshlrev_b32_sdwa v145,1,v145 dst_sel:DWORD dst_unused:UNUSED_PAD src0_sel:DWORD src1_sel:WORD_1
v_mfma_f32_16x16x32_bf16 v[168:171],v[92:95],v[128:131],v[168:171]
s_waitcnt lgkmcnt(13)
ds_read_u16_d16_hi v143,v143
v_lshlrev_b32_sdwa v147,1,v147 dst_sel:DWORD dst_unused:UNUSED_PAD src0_sel:DWORD src1_sel:WORD_1
v_mfma_f32_16x16x32_bf16 v[176:179],v[96:99],v[128:131],v[176:179]
s_waitcnt lgkmcnt(11)
ds_read_b64_tr_b16 v[66:67],v4 offset:14880
v_lshlrev_b32_sdwa v148,1,v148 dst_sel:DWORD dst_unused:UNUSED_PAD src0_sel:DWORD src1_sel:WORD_1
v_mfma_f32_16x16x32_bf16 v[184:187],v[100:103],v[128:131],v[184:187]
s_waitcnt lgkmcnt(8)
ds_read_u16 v144,v144
v_lshl_add_u64 v[136:137],v[136:137],0,v[138:139]
v_lshlrev_b32_sdwa v150,1,v150 dst_sel:DWORD dst_unused:UNUSED_PAD src0_sel:DWORD src1_sel:WORD_1
v_mfma_f32_16x16x32_bf16 v[192:195],v[104:107],v[128:131],v[192:195]
s_waitcnt lgkmcnt(2)
ds_read_u16_d16_hi v146,v146
v_lshlrev_b32_sdwa v149,1,v149 dst_sel:DWORD dst_unused:UNUSED_PAD src0_sel:DWORD src1_sel:WORD_1
v_lshl_add_u64 v[138:139],v[140:141],0,v[142:143]
v_mfma_f32_16x16x32_bf16 v[200:203],v[108:111],v[128:131],v[200:203]
ds_read_b128 v[112:115],v7 offset:1152
v_mfma_f32_16x16x32_bf16 v[164:167],v[88:91],v[136:139],v[164:167]
ds_read_u16 v145,v145
v_lshlrev_b32_sdwa v151,1,v151 dst_sel:DWORD dst_unused:UNUSED_PAD src0_sel:DWORD src1_sel:WORD_1
v_mfma_f32_16x16x32_bf16 v[172:175],v[92:95],v[136:139],v[172:175]
ds_read_u16_d16_hi v147,v147
ds_read_b64_tr_b16 v[68:69],v5 offset:12832
v_lshlrev_b32_sdwa v152,1,v152 dst_sel:DWORD dst_unused:UNUSED_PAD src0_sel:DWORD src1_sel:WORD_1
v_mfma_f32_16x16x32_bf16 v[180:183],v[96:99],v[136:139],v[180:183]
ds_read_b64_tr_b16 v[70:71],v5 offset:14880
ds_read_b128 v[116:119],v7 offset:1184
v_lshlrev_b32_sdwa v154,1,v154 dst_sel:DWORD dst_unused:UNUSED_PAD src0_sel:DWORD src1_sel:WORD_1
v_mfma_f32_16x16x32_bf16 v[188:191],v[100:103],v[136:139],v[188:191]
s_cbranch_vccnz RA7_%=
s_mov_b64 exec,s[62:63]
s_add_u32 m0,s57,0
s_add_u32 s64,s56,3932160
buffer_load_dwordx4 v2,s[36:39],s64 offen lds
s_add_u32 m0,s57,1728
s_nop 0
s_mov_b32 s86,0xbe801d5a
buffer_load_dwordx4 v3,s[36:39],s64 offen lds
s_mov_b64 exec,-1
RA7_%=:
ds_read_u16 v148,v148
ds_read_b64_tr_b16 v[72:73],v4 offset:16928
v_mfma_f32_16x16x32_bf16 v[196:199],v[104:107],v[136:139],v[196:199]
ds_read_u16_d16_hi v150,v150
v_lshlrev_b32_sdwa v153,1,v153 dst_sel:DWORD dst_unused:UNUSED_PAD src0_sel:DWORD src1_sel:WORD_1
v_lshlrev_b32_sdwa v155,1,v155 dst_sel:DWORD dst_unused:UNUSED_PAD src0_sel:DWORD src1_sel:WORD_1
v_mfma_f32_16x16x32_bf16 v[204:207],v[108:111],v[136:139],v[204:207]
s_waitcnt lgkmcnt(8)
ds_read_b64_tr_b16 v[74:75],v4 offset:18976
ds_read_b64_tr_b16 v[76:77],v5 offset:16928
v_mfma_f32_16x16x32_bf16 v[128:131],v[64:67],v[16:19],v[112:115]
ds_read_u16 v149,v149
v_lshlrev_b32_sdwa v156,1,v156 dst_sel:DWORD dst_unused:UNUSED_PAD src0_sel:DWORD src1_sel:WORD_1
v_lshlrev_b32_sdwa v158,1,v158 dst_sel:DWORD dst_unused:UNUSED_PAD src0_sel:DWORD src1_sel:WORD_1
v_mfma_f32_16x16x32_bf16 v[136:139],v[64:67],v[20:23],v[112:115]
s_waitcnt lgkmcnt(6)
ds_read_b64_tr_b16 v[78:79],v5 offset:18976
ds_read_b64_tr_b16 v[80:81],v4 offset:21024
v_lshlrev_b32_sdwa v157,1,v157 dst_sel:DWORD dst_unused:UNUSED_PAD src0_sel:DWORD src1_sel:WORD_1
v_mfma_f32_16x16x32_bf16 v[140:143],v[68:71],v[20:23],v[116:119]
ds_read_u16_d16_hi v151,v151
v_lshlrev_b32_sdwa v159,1,v159 dst_sel:DWORD dst_unused:UNUSED_PAD src0_sel:DWORD src1_sel:WORD_1
v_mfma_f32_16x16x32_bf16 v[132:135],v[68:71],v[16:19],v[116:119]
s_waitcnt lgkmcnt(5)
ds_read_b64_tr_b16 v[82:83],v4 offset:23072
v_mfma_f32_16x16x32_bf16 v[128:131],v[72:75],v[24:27],v[128:131]
ds_read_b64_tr_b16 v[84:85],v5 offset:21024
ds_read_b64_tr_b16 v[86:87],v5 offset:23072
v_mfma_f32_16x16x32_bf16 v[136:139],v[72:75],v[28:31],v[136:139]
s_waitcnt lgkmcnt(5)
ds_read_u16 v152,v152
ds_read_u16_d16_hi v154,v154
v_mfma_f32_16x16x32_bf16 v[140:143],v[76:79],v[28:31],v[140:143]
ds_read_u16 v153,v153
v_mfma_f32_16x16x32_bf16 v[132:135],v[76:79],v[24:27],v[132:135]
s_waitcnt lgkmcnt(5)
ds_read_u16_d16_hi v155,v155
v_lshl_add_u64 v[144:145],v[144:145],0,v[146:147]
v_mfma_f32_16x16x32_bf16 v[128:131],v[80:83],v[32:35],v[128:131]
s_nop 0
v_lshl_add_u64 v[146:147],v[148:149],0,v[150:151]
v_mfma_f32_16x16x32_bf16 v[136:139],v[80:83],v[36:39],v[136:139]
s_waitcnt lgkmcnt(4)
ds_read_u16 v156,v156
v_mfma_f32_16x16x32_bf16 v[140:143],v[84:87],v[36:39],v[140:143]
ds_read_u16_d16_hi v158,v158
v_mfma_f32_16x16x32_bf16 v[132:135],v[84:87],v[32:35],v[132:135]
s_nop 0
ds_read_u16 v157,v157
v_lshlrev_b32_sdwa v128,1,v128 dst_sel:DWORD dst_unused:UNUSED_PAD src0_sel:DWORD src1_sel:WORD_1
v_lshlrev_b32_sdwa v130,1,v130 dst_sel:DWORD dst_unused:UNUSED_PAD src0_sel:DWORD src1_sel:WORD_1
v_mfma_f32_16x16x32_bf16 v[208:211],v[88:91],v[144:147],v[208:211]
s_barrier
s_setprio 3
ds_read_u16_d16_hi v159,v159
v_lshlrev_b32_sdwa v129,1,v129 dst_sel:DWORD dst_unused:UNUSED_PAD src0_sel:DWORD src1_sel:WORD_1
v_lshlrev_b32_sdwa v131,1,v131 dst_sel:DWORD dst_unused:UNUSED_PAD src0_sel:DWORD src1_sel:WORD_1
v_mfma_f32_16x16x32_bf16 v[216:219],v[92:95],v[144:147],v[216:219]
ds_read_u16 v128,v128
ds_read_u16_d16_hi v130,v130
v_fma_mixlo_f16 v120,v140,s99,0
v_fma_mixlo_f16 v122,v141,s99,0
v_mfma_f32_16x16x32_bf16 v[224:227],v[96:99],v[144:147],v[224:227]
s_cbranch_vccz RA8_%=
s_add_u32 m0,s58,12320
s_mov_b32 s69,s60
buffer_load_dwordx4 v2,s[44:47],s69 offen lds
s_add_u32 m0,m0,1024
s_add_u32 s69,s69,s53
buffer_load_dwordx4 v3,s[44:47],s69 offen lds
s_add_u32 m0,m0,1024
s_add_u32 s69,s69,s53
buffer_load_dwordx4 v2,s[44:47],s69 offen lds
s_add_u32 m0,m0,s50
s_add_u32 s69,s69,s53
buffer_load_dwordx4 v3,s[44:47],s69 offen lds
RA8_%=:
v_lshlrev_b32_sdwa v132,1,v132 dst_sel:DWORD dst_unused:UNUSED_PAD src0_sel:DWORD src1_sel:WORD_1
v_lshlrev_b32_sdwa v134,1,v134 dst_sel:DWORD dst_unused:UNUSED_PAD src0_sel:DWORD src1_sel:WORD_1
v_mfma_f32_16x16x32_bf16 v[232:235],v[100:103],v[144:147],v[232:235]
s_waitcnt lgkmcnt(6)
v_lshl_add_u64 v[152:153],v[152:153],0,v[154:155]
v_mfma_f32_16x16x32_bf16 v[240:243],v[104:107],v[144:147],v[240:243]
s_waitcnt lgkmcnt(2)
s_cbranch_vccnz RA9_%=
s_mov_b64 exec,s[62:63]
s_add_u32 m0,s57,3488
s_add_u32 s64,s56,4030464
buffer_load_dwordx4 v2,s[36:39],s64 offen lds
s_add_u32 m0,s57,5216
s_nop 0
buffer_load_dwordx4 v3,s[36:39],s64 offen lds
s_mov_b64 exec,-1
RA9_%=:
v_lshl_add_u64 v[154:155],v[156:157],0,v[158:159]
v_fma_mixhi_f16 v120,v142,s99,0
v_mfma_f32_16x16x32_bf16 v[248:251],v[108:111],v[144:147],v[248:251]
ds_read_u16 v129,v129
v_lshlrev_b32_sdwa v133,1,v133 dst_sel:DWORD dst_unused:UNUSED_PAD src0_sel:DWORD src1_sel:WORD_1
v_fma_mixhi_f16 v122,v143,s99,0
v_mfma_f32_16x16x32_bf16 v[212:215],v[88:91],v[152:155],v[212:215]
s_cbranch_vccz RA10_%=
s_add_u32 m0,m0,1024
s_add_u32 s69,s69,s53
buffer_load_dwordx4 v2,s[44:47],s69 offen lds
s_add_u32 m0,m0,1024
s_add_u32 s69,s69,s53
buffer_load_dwordx4 v3,s[44:47],s69 offen lds
s_add_u32 m0,m0,s50
s_add_u32 s69,s69,s51
buffer_load_dwordx4 v2,s[44:47],s69 offen lds
s_add_u32 m0,m0,1024
s_add_u32 s69,s69,s53
buffer_load_dwordx4 v3,s[44:47],s69 offen lds
RA10_%=:
v_pk_fma_f16 v121,v120,v120,s54
v_lshlrev_b32_sdwa v135,1,v135 dst_sel:DWORD dst_unused:UNUSED_PAD src0_sel:DWORD src1_sel:WORD_1
v_mfma_f32_16x16x32_bf16 v[220:223],v[92:95],v[152:155],v[220:223]
ds_read_u16_d16_hi v131,v131
ds_read_u16 v132,v132
v_pk_fma_f16 v123,v122,v122,s54
v_mfma_f32_16x16x32_bf16 v[228:231],v[96:99],v[152:155],v[228:231]
ds_read_b64_tr_b16 v[88:89],v6 offset:0
v_pk_mul_f16 v121,v120,v121
v_lshlrev_b32_sdwa v136,1,v136 dst_sel:DWORD dst_unused:UNUSED_PAD src0_sel:DWORD src1_sel:WORD_1
v_pk_mul_f16 v123,v122,v123
v_mfma_f32_16x16x32_bf16 v[236:239],v[100:103],v[152:155],v[236:239]
s_cbranch_vccz RA11_%=
s_add_u32 m0,m0,1024
s_add_u32 s69,s69,s53
buffer_load_dwordx4 v2,s[44:47],s69 offen lds
s_add_u32 m0,m0,s50
s_add_u32 s69,s69,s53
buffer_load_dwordx4 v3,s[44:47],s69 offen lds
s_add_u32 m0,m0,1024
s_add_u32 s69,s69,s53
buffer_load_dwordx4 v2,s[44:47],s69 offen lds
s_add_u32 m0,m0,1024
s_add_u32 s69,s69,s53
buffer_load_dwordx4 v3,s[44:47],s69 offen lds
RA11_%=:
ds_read_b64_tr_b16 v[90:91],v6 offset:1536
v_lshlrev_b32_sdwa v138,1,v138 dst_sel:DWORD dst_unused:UNUSED_PAD src0_sel:DWORD src1_sel:WORD_1
v_mfma_f32_16x16x32_bf16 v[244:247],v[104:107],v[152:155],v[244:247]
ds_read_u16_d16_hi v134,v134
v_exp_f16 v120,v121
v_mfma_f32_16x16x32_bf16 v[252:255],v[108:111],v[152:155],v[252:255]
ds_read_b64_tr_b16 v[92:93],v6 offset:8
v_lshlrev_b32_sdwa v137,1,v137 dst_sel:DWORD dst_unused:UNUSED_PAD src0_sel:DWORD src1_sel:WORD_1
v_mfma_f32_16x16x32_bf16 v[144:147],v[64:67],v[40:43],v[112:115]
ds_read_b64_tr_b16 v[94:95],v6 offset:1544
v_lshlrev_b32_sdwa v139,1,v139 dst_sel:DWORD dst_unused:UNUSED_PAD src0_sel:DWORD src1_sel:WORD_1
v_exp_f16 v122,v123
v_mfma_f32_16x16x32_bf16 v[152:155],v[64:67],v[44:47],v[112:115]
ds_read_u16 v133,v133
ds_read_b64_tr_b16 v[96:97],v6 offset:64
v_exp_f16_sdwa v120,v121 dst_sel:WORD_1 dst_unused:UNUSED_PRESERVE src0_sel:WORD_1
v_mfma_f32_16x16x32_bf16 v[156:159],v[68:71],v[44:47],v[116:119]
ds_read_u16_d16_hi v135,v135
v_exp_f16_sdwa v122,v123 dst_sel:WORD_1 dst_unused:UNUSED_PRESERVE src0_sel:WORD_1
v_mfma_f32_16x16x32_bf16 v[148:151],v[68:71],v[40:43],v[116:119]
ds_read_b64_tr_b16 v[98:99],v6 offset:1600
v_pk_fma_f16 v120,v120,s55,s55
v_pk_fma_f16 v122,v122,s55,s55
v_mfma_f32_16x16x32_bf16 v[144:147],v[72:75],v[48:51],v[144:147]
v_rcp_f16 v121,v120
v_mfma_f32_16x16x32_bf16 v[152:155],v[72:75],v[52:55],v[152:155]
ds_read_b64_tr_b16 v[100:101],v6 offset:72
v_rcp_f16 v123,v122
v_mfma_f32_16x16x32_bf16 v[156:159],v[76:79],v[52:55],v[156:159]
ds_read_u16 v136,v136
v_rcp_f16_sdwa v121,v120 dst_sel:WORD_1 dst_unused:UNUSED_PRESERVE src0_sel:WORD_1
v_mfma_f32_16x16x32_bf16 v[148:151],v[76:79],v[48:51],v[148:151]
s_cbranch_vccnz RA12_%=
s_mov_b64 exec,s[62:63]
s_add_u32 m0,s57,6976
s_add_u32 s64,s56,4128768
buffer_load_dwordx4 v2,s[36:39],s64 offen lds
s_add_u32 m0,s57,8704
s_nop 0
buffer_load_dwordx4 v3,s[36:39],s64 offen lds
s_mov_b64 exec,-1
RA12_%=:
ds_read_b64_tr_b16 v[102:103],v6 offset:1608
ds_read_b64_tr_b16 v[104:105],v6 offset:128
v_rcp_f16_sdwa v123,v122 dst_sel:WORD_1 dst_unused:UNUSED_PRESERVE src0_sel:WORD_1
v_mfma_f32_16x16x32_bf16 v[144:147],v[80:83],v[56:59],v[144:147]
ds_read_u16_d16_hi v138,v138
v_fma_mix_f32 v142,v142,v121,0 op_sel:[0,1,0] op_sel_hi:[0,1,0]
v_mfma_f32_16x16x32_bf16 v[152:155],v[80:83],v[60:63],v[152:155]
s_waitcnt lgkmcnt(6)
ds_read_b64_tr_b16 v[106:107],v6 offset:1664
ds_read_b64_tr_b16 v[108:109],v6 offset:136
v_fma_mix_f32 v140,v140,v121,0 op_sel_hi:[0,1,0]
v_lshl_add_u64 v[128:129],v[128:129],0,v[130:131]
v_lshl_add_u64 v[130:131],v[132:133],0,v[134:135]
v_mfma_f32_16x16x32_bf16 v[156:159],v[84:87],v[60:63],v[156:159]
ds_read_u16 v137,v137
v_fma_mix_f32 v141,v141,v123,0 op_sel_hi:[0,1,0]
v_fma_mix_f32 v143,v143,v123,0 op_sel:[0,1,0] op_sel_hi:[0,1,0]
v_mfma_f32_16x16x32_bf16 v[148:151],v[84:87],v[56:59],v[148:151]
ds_read_b64_tr_b16 v[110:111],v6 offset:1672
ds_read_b64_tr_b16 v[64:65],v4 offset:0
v_lshlrev_b32_sdwa v144,1,v144 dst_sel:DWORD dst_unused:UNUSED_PAD src0_sel:DWORD src1_sel:WORD_1
v_lshlrev_b32_sdwa v146,1,v146 dst_sel:DWORD dst_unused:UNUSED_PAD src0_sel:DWORD src1_sel:WORD_1
v_mfma_f32_16x16x32_bf16 v[160:163],v[88:91],v[128:131],v[160:163]
ds_read_u16_d16_hi v139,v139
v_lshlrev_b32_sdwa v145,1,v145 dst_sel:DWORD dst_unused:UNUSED_PAD src0_sel:DWORD src1_sel:WORD_1
v_lshlrev_b32_sdwa v147,1,v147 dst_sel:DWORD dst_unused:UNUSED_PAD src0_sel:DWORD src1_sel:WORD_1
v_fma_mixlo_f16 v122,v157,s99,0
v_mfma_f32_16x16x32_bf16 v[168:171],v[92:95],v[128:131],v[168:171]
s_waitcnt lgkmcnt(11)
ds_read_b64_tr_b16 v[66:67],v4 offset:2048
ds_read_b128 v[112:115],v7 offset:1280
v_fma_mixlo_f16 v120,v156,s99,0
v_mfma_f32_16x16x32_bf16 v[176:179],v[96:99],v[128:131],v[176:179]
s_waitcnt lgkmcnt(10)
ds_read_u16 v144,v144
v_lshlrev_b32_sdwa v148,1,v148 dst_sel:DWORD dst_unused:UNUSED_PAD src0_sel:DWORD src1_sel:WORD_1
v_lshlrev_b32_sdwa v150,1,v150 dst_sel:DWORD dst_unused:UNUSED_PAD src0_sel:DWORD src1_sel:WORD_1
v_mfma_f32_16x16x32_bf16 v[184:187],v[100:103],v[128:131],v[184:187]
s_waitcnt lgkmcnt(3)
ds_read_b64_tr_b16 v[68:69],v5 offset:0
ds_read_b64_tr_b16 v[70:71],v5 offset:2048
v_lshl_add_u64 v[136:137],v[136:137],0,v[138:139]
v_fma_mixhi_f16 v120,v158,s99,0
v_mfma_f32_16x16x32_bf16 v[192:195],v[104:107],v[128:131],v[192:195]
ds_read_u16_d16_hi v146,v146
v_cvt_pk_bf16_f32 v138,v140,v142
v_cvt_pk_bf16_f32 v139,v141,v143
v_mfma_f32_16x16x32_bf16 v[200:203],v[108:111],v[128:131],v[200:203]
v_fma_mixhi_f16 v122,v159,s99,0
v_mfma_f32_16x16x32_bf16 v[164:167],v[88:91],v[136:139],v[164:167]
ds_read_b128 v[116:119],v7 offset:1312
ds_read_b64_tr_b16 v[72:73],v4 offset:4096
v_lshlrev_b32_sdwa v149,1,v149 dst_sel:DWORD dst_unused:UNUSED_PAD src0_sel:DWORD src1_sel:WORD_1
v_lshlrev_b32_sdwa v151,1,v151 dst_sel:DWORD dst_unused:UNUSED_PAD src0_sel:DWORD src1_sel:WORD_1
v_mfma_f32_16x16x32_bf16 v[172:175],v[92:95],v[136:139],v[172:175]
ds_read_b64_tr_b16 v[74:75],v4 offset:6144
ds_read_b64_tr_b16 v[76:77],v5 offset:4096
v_pk_fma_f16 v121,v120,v120,s54
v_pk_fma_f16 v123,v122,v122,s54
v_mfma_f32_16x16x32_bf16 v[180:183],v[96:99],v[136:139],v[180:183]
ds_read_u16 v145,v145
v_pk_mul_f16 v121,v120,v121
v_pk_mul_f16 v123,v122,v123
v_mfma_f32_16x16x32_bf16 v[188:191],v[100:103],v[136:139],v[188:191]
s_cbranch_vccnz RA13_%=
s_mov_b64 exec,s[62:63]
s_add_u32 m0,s57,10464
s_add_u32 s64,s56,4227072
buffer_load_dwordx4 v2,s[36:39],s64 offen lds
s_add_u32 m0,s57,12192
s_nop 0
buffer_load_dwordx4 v3,s[36:39],s64 offen lds
s_mov_b64 exec,-1
RA13_%=:
ds_read_u16_d16_hi v147,v147
v_lshlrev_b32_sdwa v152,1,v152 dst_sel:DWORD dst_unused:UNUSED_PAD src0_sel:DWORD src1_sel:WORD_1
v_lshlrev_b32_sdwa v154,1,v154 dst_sel:DWORD dst_unused:UNUSED_PAD src0_sel:DWORD src1_sel:WORD_1
v_mfma_f32_16x16x32_bf16 v[196:199],v[104:107],v[136:139],v[196:199]
v_exp_f16 v120,v121
v_mfma_f32_16x16x32_bf16 v[204:207],v[108:111],v[136:139],v[204:207]
s_waitcnt lgkmcnt(10)
ds_read_b64_tr_b16 v[78:79],v5 offset:6144
ds_read_b64_tr_b16 v[80:81],v4 offset:8192
v_exp_f16 v122,v123
v_mfma_f32_16x16x32_bf16 v[128:131],v[64:67],v[16:19],v[112:115]
v_lshlrev_b32_sdwa v153,1,v153 dst_sel:DWORD dst_unused:UNUSED_PAD src0_sel:DWORD src1_sel:WORD_1
v_lshlrev_b32_sdwa v155,1,v155 dst_sel:DWORD dst_unused:UNUSED_PAD src0_sel:DWORD src1_sel:WORD_1
v_mfma_f32_16x16x32_bf16 v[136:139],v[64:67],v[20:23],v[112:115]
s_waitcnt lgkmcnt(7)
ds_read_u16 v148,v148
ds_read_u16_d16_hi v150,v150
v_exp_f16_sdwa v120,v121 dst_sel:WORD_1 dst_unused:UNUSED_PRESERVE src0_sel:WORD_1
v_mfma_f32_16x16x32_bf16 v[140:143],v[68:71],v[20:23],v[116:119]
v_exp_f16_sdwa v122,v123 dst_sel:WORD_1 dst_unused:UNUSED_PRESERVE src0_sel:WORD_1
v_mfma_f32_16x16x32_bf16 v[132:135],v[68:71],v[16:19],v[116:119]
s_waitcnt lgkmcnt(7)
ds_read_b64_tr_b16 v[82:83],v4 offset:10240
v_pk_fma_f16 v120,v120,s55,s55
v_mfma_f32_16x16x32_bf16 v[128:131],v[72:75],v[24:27],v[128:131]
ds_read_b64_tr_b16 v[84:85],v5 offset:8192
v_pk_fma_f16 v122,v122,s55,s55
v_rcp_f16 v121,v120
v_mfma_f32_16x16x32_bf16 v[136:139],v[72:75],v[28:31],v[136:139]
s_waitcnt lgkmcnt(5)
ds_read_b64_tr_b16 v[86:87],v5 offset:10240
ds_read_u16 v149,v149
v_rcp_f16 v123,v122
v_mfma_f32_16x16x32_bf16 v[140:143],v[76:79],v[28:31],v[140:143]
ds_read_u16_d16_hi v151,v151
ds_read_u16 v152,v152
v_rcp_f16_sdwa v121,v120 dst_sel:WORD_1 dst_unused:UNUSED_PRESERVE src0_sel:WORD_1
v_mfma_f32_16x16x32_bf16 v[132:135],v[76:79],v[24:27],v[132:135]
s_waitcnt lgkmcnt(5)
v_rcp_f16_sdwa v123,v122 dst_sel:WORD_1 dst_unused:UNUSED_PRESERVE src0_sel:WORD_1
v_mfma_f32_16x16x32_bf16 v[128:131],v[80:83],v[32:35],v[128:131]
ds_read_u16_d16_hi v154,v154
v_fma_mix_f32 v156,v156,v121,0 op_sel_hi:[0,1,0]
v_fma_mix_f32 v158,v158,v121,0 op_sel:[0,1,0] op_sel_hi:[0,1,0]
v_mfma_f32_16x16x32_bf16 v[136:139],v[80:83],v[36:39],v[136:139]
s_waitcnt lgkmcnt(2)
v_lshl_add_u64 v[144:145],v[144:145],0,v[146:147]
v_lshl_add_u64 v[146:147],v[148:149],0,v[150:151]
v_mfma_f32_16x16x32_bf16 v[140:143],v[84:87],v[36:39],v[140:143]
ds_read_u16 v153,v153
v_fma_mix_f32 v157,v157,v123,0 op_sel_hi:[0,1,0]
v_fma_mix_f32 v159,v159,v123,0 op_sel:[0,1,0] op_sel_hi:[0,1,0]
v_mfma_f32_16x16x32_bf16 v[132:135],v[84:87],v[32:35],v[132:135]
v_lshlrev_b32_sdwa v128,1,v128 dst_sel:DWORD dst_unused:UNUSED_PAD src0_sel:DWORD src1_sel:WORD_1
v_lshlrev_b32_sdwa v130,1,v130 dst_sel:DWORD dst_unused:UNUSED_PAD src0_sel:DWORD src1_sel:WORD_1
v_mfma_f32_16x16x32_bf16 v[208:211],v[88:91],v[144:147],v[208:211]
s_cbranch_vccnz RA14_%=
s_mov_b64 exec,s[62:63]
s_add_u32 m0,s57,13952
s_add_u32 s64,s56,4325376
buffer_load_dwordx4 v2,s[36:39],s64 offen lds
s_add_u32 m0,s57,15680
s_nop 0
buffer_load_dwordx4 v3,s[36:39],s64 offen lds
s_mov_b64 exec,-1
RA14_%=:
ds_read_u16_d16_hi v155,v155
v_lshlrev_b32_sdwa v129,1,v129 dst_sel:DWORD dst_unused:UNUSED_PAD src0_sel:DWORD src1_sel:WORD_1
v_lshlrev_b32_sdwa v131,1,v131 dst_sel:DWORD dst_unused:UNUSED_PAD src0_sel:DWORD src1_sel:WORD_1
v_mfma_f32_16x16x32_bf16 v[216:219],v[92:95],v[144:147],v[216:219]
ds_read_u16 v128,v128
ds_read_u16_d16_hi v130,v130
v_fma_mixlo_f16 v120,v140,s99,0
v_fma_mixlo_f16 v122,v141,s99,0
v_mfma_f32_16x16x32_bf16 v[224:227],v[96:99],v[144:147],v[224:227]
v_lshlrev_b32_sdwa v132,1,v132 dst_sel:DWORD dst_unused:UNUSED_PAD src0_sel:DWORD src1_sel:WORD_1
v_lshlrev_b32_sdwa v134,1,v134 dst_sel:DWORD dst_unused:UNUSED_PAD src0_sel:DWORD src1_sel:WORD_1
v_mfma_f32_16x16x32_bf16 v[232:235],v[100:103],v[144:147],v[232:235]
s_waitcnt lgkmcnt(2)
v_lshl_add_u64 v[152:153],v[152:153],0,v[154:155]
v_cvt_pk_bf16_f32 v154,v156,v158
v_mfma_f32_16x16x32_bf16 v[240:243],v[104:107],v[144:147],v[240:243]
v_fma_mixhi_f16 v120,v142,s99,0
v_cvt_pk_bf16_f32 v155,v157,v159
v_mfma_f32_16x16x32_bf16 v[248:251],v[108:111],v[144:147],v[248:251]
s_setprio 2
ds_read_u16 v129,v129
v_lshlrev_b32_sdwa v133,1,v133 dst_sel:DWORD dst_unused:UNUSED_PAD src0_sel:DWORD src1_sel:WORD_1
v_fma_mixhi_f16 v122,v143,s99,0
v_mfma_f32_16x16x32_bf16 v[212:215],v[88:91],v[152:155],v[212:215]
v_pk_fma_f16 v121,v120,v120,s54
v_lshlrev_b32_sdwa v135,1,v135 dst_sel:DWORD dst_unused:UNUSED_PAD src0_sel:DWORD src1_sel:WORD_1
v_mfma_f32_16x16x32_bf16 v[220:223],v[92:95],v[152:155],v[220:223]
ds_read_u16_d16_hi v131,v131
ds_read_u16 v132,v132
v_pk_fma_f16 v123,v122,v122,s54
v_mfma_f32_16x16x32_bf16 v[228:231],v[96:99],v[152:155],v[228:231]
ds_read_b64_tr_b16 v[88:89],v6 offset:6160
v_pk_mul_f16 v121,v120,v121
v_lshlrev_b32_sdwa v136,1,v136 dst_sel:DWORD dst_unused:UNUSED_PAD src0_sel:DWORD src1_sel:WORD_1
v_pk_mul_f16 v123,v122,v123
v_mfma_f32_16x16x32_bf16 v[236:239],v[100:103],v[152:155],v[236:239]
ds_read_b64_tr_b16 v[90:91],v6 offset:7696
v_lshlrev_b32_sdwa v138,1,v138 dst_sel:DWORD dst_unused:UNUSED_PAD src0_sel:DWORD src1_sel:WORD_1
v_mfma_f32_16x16x32_bf16 v[244:247],v[104:107],v[152:155],v[244:247]
ds_read_u16_d16_hi v134,v134
v_exp_f16 v120,v121
v_mfma_f32_16x16x32_bf16 v[252:255],v[108:111],v[152:155],v[252:255]
ds_read_b64_tr_b16 v[92:93],v6 offset:6168
v_lshlrev_b32_sdwa v137,1,v137 dst_sel:DWORD dst_unused:UNUSED_PAD src0_sel:DWORD src1_sel:WORD_1
v_mfma_f32_16x16x32_bf16 v[144:147],v[64:67],v[40:43],v[112:115]
ds_read_b64_tr_b16 v[94:95],v6 offset:7704
v_lshlrev_b32_sdwa v139,1,v139 dst_sel:DWORD dst_unused:UNUSED_PAD src0_sel:DWORD src1_sel:WORD_1
v_exp_f16 v122,v123
v_mfma_f32_16x16x32_bf16 v[152:155],v[64:67],v[44:47],v[112:115]
ds_read_u16 v133,v133
ds_read_b64_tr_b16 v[96:97],v6 offset:6224
v_exp_f16_sdwa v120,v121 dst_sel:WORD_1 dst_unused:UNUSED_PRESERVE src0_sel:WORD_1
v_mfma_f32_16x16x32_bf16 v[156:159],v[68:71],v[44:47],v[116:119]
s_cbranch_vccnz RA15_%=
s_mov_b64 exec,s[62:63]
s_add_u32 m0,s57,17440
s_add_u32 s64,s56,4423680
buffer_load_dwordx4 v2,s[36:39],s64 offen lds
s_add_u32 m0,s57,19168
s_nop 0
buffer_load_dwordx4 v3,s[36:39],s64 offen lds
s_mov_b64 exec,-1
RA15_%=:
ds_read_u16_d16_hi v135,v135
v_exp_f16_sdwa v122,v123 dst_sel:WORD_1 dst_unused:UNUSED_PRESERVE src0_sel:WORD_1
v_mfma_f32_16x16x32_bf16 v[148:151],v[68:71],v[40:43],v[116:119]
ds_read_b64_tr_b16 v[98:99],v6 offset:7760
v_pk_fma_f16 v120,v120,s55,s55
v_pk_fma_f16 v122,v122,s55,s55
v_mfma_f32_16x16x32_bf16 v[144:147],v[72:75],v[48:51],v[144:147]
v_rcp_f16 v121,v120
v_mfma_f32_16x16x32_bf16 v[152:155],v[72:75],v[52:55],v[152:155]
ds_read_b64_tr_b16 v[100:101],v6 offset:6232
v_rcp_f16 v123,v122
v_mfma_f32_16x16x32_bf16 v[156:159],v[76:79],v[52:55],v[156:159]
ds_read_u16 v136,v136
v_rcp_f16_sdwa v121,v120 dst_sel:WORD_1 dst_unused:UNUSED_PRESERVE src0_sel:WORD_1
v_mfma_f32_16x16x32_bf16 v[148:151],v[76:79],v[48:51],v[148:151]
ds_read_b64_tr_b16 v[102:103],v6 offset:7768
ds_read_b64_tr_b16 v[104:105],v6 offset:6288
v_rcp_f16_sdwa v123,v122 dst_sel:WORD_1 dst_unused:UNUSED_PRESERVE src0_sel:WORD_1
v_mfma_f32_16x16x32_bf16 v[144:147],v[80:83],v[56:59],v[144:147]
ds_read_u16_d16_hi v138,v138
v_fma_mix_f32 v142,v142,v121,0 op_sel:[0,1,0] op_sel_hi:[0,1,0]
v_mfma_f32_16x16x32_bf16 v[152:155],v[80:83],v[60:63],v[152:155]
s_waitcnt lgkmcnt(6)
ds_read_b64_tr_b16 v[106:107],v6 offset:7824
ds_read_b64_tr_b16 v[108:109],v6 offset:6296
v_fma_mix_f32 v140,v140,v121,0 op_sel_hi:[0,1,0]
v_lshl_add_u64 v[128:129],v[128:129],0,v[130:131]
v_lshl_add_u64 v[130:131],v[132:133],0,v[134:135]
v_mfma_f32_16x16x32_bf16 v[156:159],v[84:87],v[60:63],v[156:159]
ds_read_u16 v137,v137
v_fma_mix_f32 v141,v141,v123,0 op_sel_hi:[0,1,0]
v_fma_mix_f32 v143,v143,v123,0 op_sel:[0,1,0] op_sel_hi:[0,1,0]
v_mfma_f32_16x16x32_bf16 v[148:151],v[84:87],v[56:59],v[148:151]
ds_read_b64_tr_b16 v[110:111],v6 offset:7832
ds_read_b64_tr_b16 v[64:65],v4 offset:512
v_lshlrev_b32_sdwa v144,1,v144 dst_sel:DWORD dst_unused:UNUSED_PAD src0_sel:DWORD src1_sel:WORD_1
v_lshlrev_b32_sdwa v146,1,v146 dst_sel:DWORD dst_unused:UNUSED_PAD src0_sel:DWORD src1_sel:WORD_1
v_mfma_f32_16x16x32_bf16 v[160:163],v[88:91],v[128:131],v[160:163]
ds_read_u16_d16_hi v139,v139
v_lshlrev_b32_sdwa v145,1,v145 dst_sel:DWORD dst_unused:UNUSED_PAD src0_sel:DWORD src1_sel:WORD_1
v_lshlrev_b32_sdwa v147,1,v147 dst_sel:DWORD dst_unused:UNUSED_PAD src0_sel:DWORD src1_sel:WORD_1
v_fma_mixlo_f16 v122,v157,s99,0
v_mfma_f32_16x16x32_bf16 v[168:171],v[92:95],v[128:131],v[168:171]
s_waitcnt lgkmcnt(11)
ds_read_b64_tr_b16 v[66:67],v4 offset:2560
ds_read_b128 v[112:115],v7 offset:1408
v_fma_mixlo_f16 v120,v156,s99,0
v_mfma_f32_16x16x32_bf16 v[176:179],v[96:99],v[128:131],v[176:179]
s_waitcnt lgkmcnt(10)
ds_read_u16 v144,v144
v_lshlrev_b32_sdwa v148,1,v148 dst_sel:DWORD dst_unused:UNUSED_PAD src0_sel:DWORD src1_sel:WORD_1
v_lshlrev_b32_sdwa v150,1,v150 dst_sel:DWORD dst_unused:UNUSED_PAD src0_sel:DWORD src1_sel:WORD_1
v_mfma_f32_16x16x32_bf16 v[184:187],v[100:103],v[128:131],v[184:187]
s_waitcnt lgkmcnt(3)
ds_read_b64_tr_b16 v[68:69],v5 offset:512
ds_read_b64_tr_b16 v[70:71],v5 offset:2560
v_lshl_add_u64 v[136:137],v[136:137],0,v[138:139]
v_fma_mixhi_f16 v120,v158,s99,0
v_mfma_f32_16x16x32_bf16 v[192:195],v[104:107],v[128:131],v[192:195]
s_cbranch_vccnz RA16_%=
s_mov_b64 exec,s[62:63]
s_add_u32 m0,s57,20928
s_add_u32 s64,s56,4521984
buffer_load_dwordx4 v2,s[36:39],s64 offen lds
s_add_u32 m0,s57,22656
s_nop 0
buffer_load_dwordx4 v3,s[36:39],s64 offen lds
s_mov_b64 exec,-1
RA16_%=:
ds_read_u16_d16_hi v146,v146
v_cvt_pk_bf16_f32 v138,v140,v142
v_cvt_pk_bf16_f32 v139,v141,v143
v_mfma_f32_16x16x32_bf16 v[200:203],v[108:111],v[128:131],v[200:203]
v_fma_mixhi_f16 v122,v159,s99,0
v_mfma_f32_16x16x32_bf16 v[164:167],v[88:91],v[136:139],v[164:167]
ds_read_b128 v[116:119],v7 offset:1440
ds_read_b64_tr_b16 v[72:73],v4 offset:4608
v_lshlrev_b32_sdwa v149,1,v149 dst_sel:DWORD dst_unused:UNUSED_PAD src0_sel:DWORD src1_sel:WORD_1
v_lshlrev_b32_sdwa v151,1,v151 dst_sel:DWORD dst_unused:UNUSED_PAD src0_sel:DWORD src1_sel:WORD_1
v_mfma_f32_16x16x32_bf16 v[172:175],v[92:95],v[136:139],v[172:175]
s_setprio 1
ds_read_b64_tr_b16 v[74:75],v4 offset:6656
ds_read_b64_tr_b16 v[76:77],v5 offset:4608
v_pk_fma_f16 v121,v120,v120,s54
v_pk_fma_f16 v123,v122,v122,s54
v_mfma_f32_16x16x32_bf16 v[180:183],v[96:99],v[136:139],v[180:183]
ds_read_u16 v145,v145
v_pk_mul_f16 v121,v120,v121
v_pk_mul_f16 v123,v122,v123
v_mfma_f32_16x16x32_bf16 v[188:191],v[100:103],v[136:139],v[188:191]
ds_read_u16_d16_hi v147,v147
v_lshlrev_b32_sdwa v152,1,v152 dst_sel:DWORD dst_unused:UNUSED_PAD src0_sel:DWORD src1_sel:WORD_1
v_lshlrev_b32_sdwa v154,1,v154 dst_sel:DWORD dst_unused:UNUSED_PAD src0_sel:DWORD src1_sel:WORD_1
v_mfma_f32_16x16x32_bf16 v[196:199],v[104:107],v[136:139],v[196:199]
s_nop 0
v_exp_f16 v120,v121
v_mfma_f32_16x16x32_bf16 v[204:207],v[108:111],v[136:139],v[204:207]
s_waitcnt lgkmcnt(10)
ds_read_b64_tr_b16 v[78:79],v5 offset:6656
ds_read_b64_tr_b16 v[80:81],v4 offset:8704
v_exp_f16 v122,v123
v_mfma_f32_16x16x32_bf16 v[128:131],v[64:67],v[16:19],v[112:115]
v_lshlrev_b32_sdwa v153,1,v153 dst_sel:DWORD dst_unused:UNUSED_PAD src0_sel:DWORD src1_sel:WORD_1
v_lshlrev_b32_sdwa v155,1,v155 dst_sel:DWORD dst_unused:UNUSED_PAD src0_sel:DWORD src1_sel:WORD_1
v_mfma_f32_16x16x32_bf16 v[136:139],v[64:67],v[20:23],v[112:115]
s_waitcnt lgkmcnt(7)
ds_read_u16 v148,v148
ds_read_u16_d16_hi v150,v150
v_exp_f16_sdwa v120,v121 dst_sel:WORD_1 dst_unused:UNUSED_PRESERVE src0_sel:WORD_1
v_mfma_f32_16x16x32_bf16 v[140:143],v[68:71],v[20:23],v[116:119]
s_nop 0
v_exp_f16_sdwa v122,v123 dst_sel:WORD_1 dst_unused:UNUSED_PRESERVE src0_sel:WORD_1
v_mfma_f32_16x16x32_bf16 v[132:135],v[68:71],v[16:19],v[116:119]
s_waitcnt lgkmcnt(7)
ds_read_b64_tr_b16 v[82:83],v4 offset:10752
v_pk_fma_f16 v120,v120,s55,s55
v_mfma_f32_16x16x32_bf16 v[128:131],v[72:75],v[24:27],v[128:131]
ds_read_b64_tr_b16 v[84:85],v5 offset:8704
v_pk_fma_f16 v122,v122,s55,s55
v_rcp_f16 v121,v120
v_mfma_f32_16x16x32_bf16 v[136:139],v[72:75],v[28:31],v[136:139]
s_waitcnt lgkmcnt(5)
ds_read_b64_tr_b16 v[86:87],v5 offset:10752
ds_read_u16 v149,v149
v_rcp_f16 v123,v122
v_mfma_f32_16x16x32_bf16 v[140:143],v[76:79],v[28:31],v[140:143]
s_setprio 0
ds_read_u16_d16_hi v151,v151
ds_read_u16 v152,v152
v_rcp_f16_sdwa v121,v120 dst_sel:WORD_1 dst_unused:UNUSED_PRESERVE src0_sel:WORD_1
v_mfma_f32_16x16x32_bf16 v[132:135],v[76:79],v[24:27],v[132:135]
s_waitcnt lgkmcnt(5)
s_cbranch_vccnz RA17_%=
s_mov_b64 exec,s[62:63]
s_add_u32 m0,s57,24416
s_add_u32 s64,s56,4620288
buffer_load_dwordx4 v2,s[36:39],s64 offen lds
s_add_u32 m0,s57,26144
s_nop 0
buffer_load_dwordx4 v3,s[36:39],s64 offen lds
s_mov_b64 exec,-1
RA17_%=:
v_rcp_f16_sdwa v123,v122 dst_sel:WORD_1 dst_unused:UNUSED_PRESERVE src0_sel:WORD_1
v_mfma_f32_16x16x32_bf16 v[128:131],v[80:83],v[32:35],v[128:131]
s_nop 0
ds_read_u16_d16_hi v154,v154
v_fma_mix_f32 v156,v156,v121,0 op_sel_hi:[0,1,0]
v_fma_mix_f32 v158,v158,v121,0 op_sel:[0,1,0] op_sel_hi:[0,1,0]
v_mfma_f32_16x16x32_bf16 v[136:139],v[80:83],v[36:39],v[136:139]
s_waitcnt lgkmcnt(2)
v_lshl_add_u64 v[144:145],v[144:145],0,v[146:147]
v_lshl_add_u64 v[146:147],v[148:149],0,v[150:151]
v_mfma_f32_16x16x32_bf16 v[140:143],v[84:87],v[36:39],v[140:143]
ds_read_u16 v153,v153
v_fma_mix_f32 v157,v157,v123,0 op_sel_hi:[0,1,0]
v_fma_mix_f32 v159,v159,v123,0 op_sel:[0,1,0] op_sel_hi:[0,1,0]
v_mfma_f32_16x16x32_bf16 v[132:135],v[84:87],v[32:35],v[132:135]
s_nop 0
v_lshlrev_b32_sdwa v128,1,v128 dst_sel:DWORD dst_unused:UNUSED_PAD src0_sel:DWORD src1_sel:WORD_1
v_lshlrev_b32_sdwa v130,1,v130 dst_sel:DWORD dst_unused:UNUSED_PAD src0_sel:DWORD src1_sel:WORD_1
v_mfma_f32_16x16x32_bf16 v[208:211],v[88:91],v[144:147],v[208:211]
s_cbranch_vccz RA18_%=
s_waitcnt vmcnt(0)
RA18_%=:
s_barrier
s_setprio 3
v_lshlrev_b32_sdwa v129,1,v129 dst_sel:DWORD dst_unused:UNUSED_PAD src0_sel:DWORD src1_sel:WORD_1
v_lshlrev_b32_sdwa v131,1,v131 dst_sel:DWORD dst_unused:UNUSED_PAD src0_sel:DWORD src1_sel:WORD_1
v_mfma_f32_16x16x32_bf16 v[216:219],v[92:95],v[144:147],v[216:219]
ds_read_u16_d16_hi v155,v155
ds_read_u16 v128,v128
v_fma_mixlo_f16 v120,v140,s99,0
v_fma_mixlo_f16 v122,v141,s99,0
v_mfma_f32_16x16x32_bf16 v[224:227],v[96:99],v[144:147],v[224:227]
s_cbranch_vccz RA19_%=
s_add_u32 m0,s58,0
s_mul_i32 s69,s59,1
s_add_u32 s69,s69,s97
buffer_load_dwordx4 v2,s[44:47],s69 offen lds
s_add_u32 m0,m0,1024
s_add_u32 s69,s69,s53
buffer_load_dwordx4 v3,s[44:47],s69 offen lds
s_add_u32 m0,m0,1024
s_add_u32 s69,s69,s53
buffer_load_dwordx4 v2,s[44:47],s69 offen lds
s_add_u32 m0,m0,s50
s_add_u32 s69,s69,s53
buffer_load_dwordx4 v3,s[44:47],s69 offen lds
RA19_%=:
v_lshlrev_b32_sdwa v132,1,v132 dst_sel:DWORD dst_unused:UNUSED_PAD src0_sel:DWORD src1_sel:WORD_1
v_lshlrev_b32_sdwa v134,1,v134 dst_sel:DWORD dst_unused:UNUSED_PAD src0_sel:DWORD src1_sel:WORD_1
v_mfma_f32_16x16x32_bf16 v[232:235],v[100:103],v[144:147],v[232:235]
s_waitcnt lgkmcnt(1)
s_mov_b32 s86,0xbe801d5a
ds_read_u16_d16_hi v130,v130
v_lshl_add_u64 v[152:153],v[152:153],0,v[154:155]
v_cvt_pk_bf16_f32 v154,v156,v158
v_mfma_f32_16x16x32_bf16 v[240:243],v[104:107],v[144:147],v[240:243]
v_fma_mixhi_f16 v120,v142,s99,0
v_cvt_pk_bf16_f32 v155,v157,v159
v_mfma_f32_16x16x32_bf16 v[248:251],v[108:111],v[144:147],v[248:251]
s_cbranch_vccz RA20_%=
s_add_u32 m0,m0,1024
s_add_u32 s69,s69,s53
buffer_load_dwordx4 v2,s[44:47],s69 offen lds
s_add_u32 m0,m0,1024
s_add_u32 s69,s69,s53
buffer_load_dwordx4 v3,s[44:47],s69 offen lds
s_add_u32 m0,m0,s50
s_add_u32 s69,s69,s53
buffer_load_dwordx4 v2,s[44:47],s69 offen lds
s_add_u32 m0,m0,1024
s_add_u32 s69,s69,s53
buffer_load_dwordx4 v3,s[44:47],s69 offen lds
RA20_%=:
ds_read_u16 v129,v129
v_fma_mixhi_f16 v122,v143,s99,0
v_mfma_f32_16x16x32_bf16 v[212:215],v[88:91],v[152:155],v[212:215]
v_pk_fma_f16 v121,v120,v120,s54
v_lshlrev_b32_sdwa v133,1,v133 dst_sel:DWORD dst_unused:UNUSED_PAD src0_sel:DWORD src1_sel:WORD_1
v_lshlrev_b32_sdwa v135,1,v135 dst_sel:DWORD dst_unused:UNUSED_PAD src0_sel:DWORD src1_sel:WORD_1
v_mfma_f32_16x16x32_bf16 v[220:223],v[92:95],v[152:155],v[220:223]
ds_read_u16_d16_hi v131,v131
ds_read_u16 v132,v132
v_pk_fma_f16 v123,v122,v122,s54
v_mfma_f32_16x16x32_bf16 v[228:231],v[96:99],v[152:155],v[228:231]
ds_read_b64_tr_b16 v[88:89],v6 offset:12320
v_pk_mul_f16 v121,v120,v121
v_lshlrev_b32_sdwa v136,1,v136 dst_sel:DWORD dst_unused:UNUSED_PAD src0_sel:DWORD src1_sel:WORD_1
v_pk_mul_f16 v123,v122,v123
v_mfma_f32_16x16x32_bf16 v[236:239],v[100:103],v[152:155],v[236:239]
s_cbranch_vccnz RA21_%=
s_mov_b64 exec,s[62:63]
s_add_u32 m0,s57,27904
s_add_u32 s64,s56,4718592
buffer_load_dwordx4 v2,s[36:39],s64 offen lds
s_add_u32 m0,s57,29632
s_nop 0
buffer_load_dwordx4 v3,s[36:39],s64 offen lds
s_mov_b64 exec,-1
RA21_%=:
ds_read_b64_tr_b16 v[90:91],v6 offset:13856
v_lshlrev_b32_sdwa v138,1,v138 dst_sel:DWORD dst_unused:UNUSED_PAD src0_sel:DWORD src1_sel:WORD_1
v_mfma_f32_16x16x32_bf16 v[244:247],v[104:107],v[152:155],v[244:247]
s_cbranch_vccz RA22_%=
s_add_u32 m0,m0,1024
s_add_u32 s69,s69,s53
buffer_load_dwordx4 v2,s[44:47],s69 offen lds
s_add_u32 m0,m0,s50
s_add_u32 s69,s69,s53
buffer_load_dwordx4 v3,s[44:47],s69 offen lds
s_add_u32 m0,m0,1024
s_add_u32 s69,s69,s53
buffer_load_dwordx4 v2,s[44:47],s69 offen lds
s_add_u32 m0,m0,1024
s_add_u32 s69,s69,s53
buffer_load_dwordx4 v3,s[44:47],s69 offen lds
RA22_%=:
ds_read_u16_d16_hi v134,v134
v_exp_f16 v120,v121
v_mfma_f32_16x16x32_bf16 v[252:255],v[108:111],v[152:155],v[252:255]
ds_read_b64_tr_b16 v[92:93],v6 offset:12328
v_lshlrev_b32_sdwa v137,1,v137 dst_sel:DWORD dst_unused:UNUSED_PAD src0_sel:DWORD src1_sel:WORD_1
v_mfma_f32_16x16x32_bf16 v[144:147],v[64:67],v[40:43],v[112:115]
ds_read_b64_tr_b16 v[94:95],v6 offset:13864
v_lshlrev_b32_sdwa v139,1,v139 dst_sel:DWORD dst_unused:UNUSED_PAD src0_sel:DWORD src1_sel:WORD_1
v_exp_f16 v122,v123
v_mfma_f32_16x16x32_bf16 v[152:155],v[64:67],v[44:47],v[112:115]
ds_read_u16 v133,v133
ds_read_b64_tr_b16 v[96:97],v6 offset:12384
v_mfma_f32_16x16x32_bf16 v[156:159],v[68:71],v[44:47],v[116:119]
ds_read_u16_d16_hi v135,v135
v_exp_f16_sdwa v122,v123 dst_sel:WORD_1 dst_unused:UNUSED_PRESERVE src0_sel:WORD_1
v_exp_f16_sdwa v120,v121 dst_sel:WORD_1 dst_unused:UNUSED_PRESERVE src0_sel:WORD_1
v_mfma_f32_16x16x32_bf16 v[148:151],v[68:71],v[40:43],v[116:119]
ds_read_b64_tr_b16 v[98:99],v6 offset:13920
v_pk_fma_f16 v120,v120,s55,s55
v_pk_fma_f16 v122,v122,s55,s55
v_mfma_f32_16x16x32_bf16 v[144:147],v[72:75],v[48:51],v[144:147]
v_rcp_f16 v121,v120
v_mfma_f32_16x16x32_bf16 v[152:155],v[72:75],v[52:55],v[152:155]
ds_read_b64_tr_b16 v[100:101],v6 offset:12392
v_rcp_f16 v123,v122
v_mfma_f32_16x16x32_bf16 v[156:159],v[76:79],v[52:55],v[156:159]
ds_read_u16 v136,v136
v_rcp_f16_sdwa v121,v120 dst_sel:WORD_1 dst_unused:UNUSED_PRESERVE src0_sel:WORD_1
v_mfma_f32_16x16x32_bf16 v[148:151],v[76:79],v[48:51],v[148:151]
ds_read_b64_tr_b16 v[102:103],v6 offset:13928
ds_read_b64_tr_b16 v[104:105],v6 offset:12448
v_fma_mix_f32 v141,v141,v123,0 op_sel_hi:[0,1,0]
v_rcp_f16_sdwa v123,v122 dst_sel:WORD_1 dst_unused:UNUSED_PRESERVE src0_sel:WORD_1
v_mfma_f32_16x16x32_bf16 v[144:147],v[80:83],v[56:59],v[144:147]
ds_read_u16_d16_hi v138,v138
v_fma_mix_f32 v142,v142,v121,0 op_sel:[0,1,0] op_sel_hi:[0,1,0]
v_mfma_f32_16x16x32_bf16 v[152:155],v[80:83],v[60:63],v[152:155]
s_waitcnt lgkmcnt(6)
ds_read_b64_tr_b16 v[106:107],v6 offset:13984
ds_read_b64_tr_b16 v[108:109],v6 offset:12456
v_fma_mix_f32 v140,v140,v121,0 op_sel_hi:[0,1,0]
v_lshl_add_u64 v[128:129],v[128:129],0,v[130:131]
v_lshl_add_u64 v[130:131],v[132:133],0,v[134:135]
v_mfma_f32_16x16x32_bf16 v[156:159],v[84:87],v[60:63],v[156:159]
ds_read_u16 v137,v137
v_fma_mix_f32 v143,v143,v123,0 op_sel:[0,1,0] op_sel_hi:[0,1,0]
v_mfma_f32_16x16x32_bf16 v[148:151],v[84:87],v[56:59],v[148:151]
ds_read_b64_tr_b16 v[110:111],v6 offset:13992
ds_read_b64_tr_b16 v[64:65],v4 offset:12320
v_lshlrev_b32_sdwa v144,1,v144 dst_sel:DWORD dst_unused:UNUSED_PAD src0_sel:DWORD src1_sel:WORD_1
v_lshlrev_b32_sdwa v146,1,v146 dst_sel:DWORD dst_unused:UNUSED_PAD src0_sel:DWORD src1_sel:WORD_1
v_mfma_f32_16x16x32_bf16 v[160:163],v[88:91],v[128:131],v[160:163]
s_cbranch_vccnz RA23_%=
s_mov_b64 exec,s[62:63]
s_add_u32 m0,s57,31392
s_add_u32 s64,s56,4816896
buffer_load_dwordx4 v2,s[36:39],s64 offen lds
s_add_u32 m0,s57,33120
s_nop 0
buffer_load_dwordx4 v3,s[36:39],s64 offen lds
s_mov_b64 exec,-1
RA23_%=:
ds_read_u16_d16_hi v139,v139
v_lshlrev_b32_sdwa v145,1,v145 dst_sel:DWORD dst_unused:UNUSED_PAD src0_sel:DWORD src1_sel:WORD_1
v_lshlrev_b32_sdwa v147,1,v147 dst_sel:DWORD dst_unused:UNUSED_PAD src0_sel:DWORD src1_sel:WORD_1
v_fma_mixlo_f16 v122,v157,s99,0
v_mfma_f32_16x16x32_bf16 v[168:171],v[92:95],v[128:131],v[168:171]
s_waitcnt lgkmcnt(11)
ds_read_b64_tr_b16 v[66:67],v4 offset:14368
ds_read_b128 v[112:115],v7 offset:0
v_fma_mixlo_f16 v120,v156,s99,0
v_mfma_f32_16x16x32_bf16 v[176:179],v[96:99],v[128:131],v[176:179]
s_waitcnt lgkmcnt(10)
ds_read_u16 v144,v144
v_lshlrev_b32_sdwa v148,1,v148 dst_sel:DWORD dst_unused:UNUSED_PAD src0_sel:DWORD src1_sel:WORD_1
v_lshlrev_b32_sdwa v150,1,v150 dst_sel:DWORD dst_unused:UNUSED_PAD src0_sel:DWORD src1_sel:WORD_1
v_mfma_f32_16x16x32_bf16 v[184:187],v[100:103],v[128:131],v[184:187]
s_waitcnt lgkmcnt(3)
ds_read_b64_tr_b16 v[68:69],v5 offset:12320
ds_read_b64_tr_b16 v[70:71],v5 offset:14368
v_lshl_add_u64 v[136:137],v[136:137],0,v[138:139]
v_fma_mixhi_f16 v120,v158,s99,0
v_mfma_f32_16x16x32_bf16 v[192:195],v[104:107],v[128:131],v[192:195]
ds_read_u16_d16_hi v146,v146
v_cvt_pk_bf16_f32 v138,v140,v142
v_cvt_pk_bf16_f32 v139,v141,v143
v_mfma_f32_16x16x32_bf16 v[200:203],v[108:111],v[128:131],v[200:203]
v_fma_mixhi_f16 v122,v159,s99,0
v_mfma_f32_16x16x32_bf16 v[164:167],v[88:91],v[136:139],v[164:167]
ds_read_b128 v[116:119],v7 offset:32
ds_read_b64_tr_b16 v[72:73],v4 offset:16416
v_lshlrev_b32_sdwa v149,1,v149 dst_sel:DWORD dst_unused:UNUSED_PAD src0_sel:DWORD src1_sel:WORD_1
v_lshlrev_b32_sdwa v151,1,v151 dst_sel:DWORD dst_unused:UNUSED_PAD src0_sel:DWORD src1_sel:WORD_1
v_mfma_f32_16x16x32_bf16 v[172:175],v[92:95],v[136:139],v[172:175]
ds_read_b64_tr_b16 v[74:75],v4 offset:18464
ds_read_b64_tr_b16 v[76:77],v5 offset:16416
v_pk_fma_f16 v123,v122,v122,s54
v_mfma_f32_16x16x32_bf16 v[180:183],v[96:99],v[136:139],v[180:183]
ds_read_u16 v145,v145
v_pk_fma_f16 v121,v120,v120,s54
v_pk_mul_f16 v121,v120,v121
v_pk_mul_f16 v123,v122,v123
v_mfma_f32_16x16x32_bf16 v[188:191],v[100:103],v[136:139],v[188:191]
ds_read_u16_d16_hi v147,v147
v_lshlrev_b32_sdwa v152,1,v152 dst_sel:DWORD dst_unused:UNUSED_PAD src0_sel:DWORD src1_sel:WORD_1
v_lshlrev_b32_sdwa v154,1,v154 dst_sel:DWORD dst_unused:UNUSED_PAD src0_sel:DWORD src1_sel:WORD_1
v_mfma_f32_16x16x32_bf16 v[196:199],v[104:107],v[136:139],v[196:199]
v_exp_f16 v120,v121
v_mfma_f32_16x16x32_bf16 v[204:207],v[108:111],v[136:139],v[204:207]
s_waitcnt lgkmcnt(10)
ds_read_b64_tr_b16 v[78:79],v5 offset:18464
ds_read_b64_tr_b16 v[80:81],v4 offset:20512
v_exp_f16 v122,v123
v_mfma_f32_16x16x32_bf16 v[128:131],v[64:67],v[16:19],v[112:115]
v_lshlrev_b32_sdwa v153,1,v153 dst_sel:DWORD dst_unused:UNUSED_PAD src0_sel:DWORD src1_sel:WORD_1
v_lshlrev_b32_sdwa v155,1,v155 dst_sel:DWORD dst_unused:UNUSED_PAD src0_sel:DWORD src1_sel:WORD_1
v_mfma_f32_16x16x32_bf16 v[136:139],v[64:67],v[20:23],v[112:115]
s_waitcnt lgkmcnt(7)
ds_read_u16 v148,v148
ds_read_u16_d16_hi v150,v150
v_exp_f16_sdwa v120,v121 dst_sel:WORD_1 dst_unused:UNUSED_PRESERVE src0_sel:WORD_1
v_mfma_f32_16x16x32_bf16 v[140:143],v[68:71],v[20:23],v[116:119]
v_exp_f16_sdwa v122,v123 dst_sel:WORD_1 dst_unused:UNUSED_PRESERVE src0_sel:WORD_1
v_mfma_f32_16x16x32_bf16 v[132:135],v[68:71],v[16:19],v[116:119]
s_waitcnt lgkmcnt(7)
ds_read_b64_tr_b16 v[82:83],v4 offset:22560
v_pk_fma_f16 v120,v120,s55,s55
v_mfma_f32_16x16x32_bf16 v[128:131],v[72:75],v[24:27],v[128:131]
ds_read_b64_tr_b16 v[84:85],v5 offset:20512
v_pk_fma_f16 v122,v122,s55,s55
v_rcp_f16 v121,v120
v_mfma_f32_16x16x32_bf16 v[136:139],v[72:75],v[28:31],v[136:139]
s_waitcnt lgkmcnt(5)
ds_read_b64_tr_b16 v[86:87],v5 offset:22560
ds_read_u16 v149,v149
v_rcp_f16 v123,v122
v_mfma_f32_16x16x32_bf16 v[140:143],v[76:79],v[28:31],v[140:143]
ds_read_u16_d16_hi v151,v151
ds_read_u16 v152,v152
v_rcp_f16_sdwa v121,v120 dst_sel:WORD_1 dst_unused:UNUSED_PRESERVE src0_sel:WORD_1
v_mfma_f32_16x16x32_bf16 v[132:135],v[76:79],v[24:27],v[132:135]
s_waitcnt lgkmcnt(5)
v_rcp_f16_sdwa v123,v122 dst_sel:WORD_1 dst_unused:UNUSED_PRESERVE src0_sel:WORD_1
v_mfma_f32_16x16x32_bf16 v[128:131],v[80:83],v[32:35],v[128:131]
ds_read_u16_d16_hi v154,v154
v_fma_mix_f32 v156,v156,v121,0 op_sel_hi:[0,1,0]
v_fma_mix_f32 v158,v158,v121,0 op_sel:[0,1,0] op_sel_hi:[0,1,0]
v_mfma_f32_16x16x32_bf16 v[136:139],v[80:83],v[36:39],v[136:139]
s_waitcnt lgkmcnt(2)
v_lshl_add_u64 v[144:145],v[144:145],0,v[146:147]
v_lshl_add_u64 v[146:147],v[148:149],0,v[150:151]
v_mfma_f32_16x16x32_bf16 v[140:143],v[84:87],v[36:39],v[140:143]
ds_read_u16 v153,v153
v_fma_mix_f32 v157,v157,v123,0 op_sel_hi:[0,1,0]
v_fma_mix_f32 v159,v159,v123,0 op_sel:[0,1,0] op_sel_hi:[0,1,0]
v_mfma_f32_16x16x32_bf16 v[132:135],v[84:87],v[32:35],v[132:135]
s_setprio 2
v_lshlrev_b32_sdwa v128,1,v128 dst_sel:DWORD dst_unused:UNUSED_PAD src0_sel:DWORD src1_sel:WORD_1
v_lshlrev_b32_sdwa v130,1,v130 dst_sel:DWORD dst_unused:UNUSED_PAD src0_sel:DWORD src1_sel:WORD_1
v_mfma_f32_16x16x32_bf16 v[208:211],v[88:91],v[144:147],v[208:211]
v_lshlrev_b32_sdwa v129,1,v129 dst_sel:DWORD dst_unused:UNUSED_PAD src0_sel:DWORD src1_sel:WORD_1
v_lshlrev_b32_sdwa v131,1,v131 dst_sel:DWORD dst_unused:UNUSED_PAD src0_sel:DWORD src1_sel:WORD_1
v_mfma_f32_16x16x32_bf16 v[216:219],v[92:95],v[144:147],v[216:219]
ds_read_u16_d16_hi v155,v155
ds_read_u16 v128,v128
v_fma_mixlo_f16 v120,v140,s99,0
v_fma_mixlo_f16 v122,v141,s99,0
v_mfma_f32_16x16x32_bf16 v[224:227],v[96:99],v[144:147],v[224:227]
v_lshlrev_b32_sdwa v132,1,v132 dst_sel:DWORD dst_unused:UNUSED_PAD src0_sel:DWORD src1_sel:WORD_1
v_lshlrev_b32_sdwa v134,1,v134 dst_sel:DWORD dst_unused:UNUSED_PAD src0_sel:DWORD src1_sel:WORD_1
v_mfma_f32_16x16x32_bf16 v[232:235],v[100:103],v[144:147],v[232:235]
s_waitcnt lgkmcnt(1)
ds_read_u16_d16_hi v130,v130
v_lshl_add_u64 v[152:153],v[152:153],0,v[154:155]
v_cvt_pk_bf16_f32 v154,v156,v158
v_mfma_f32_16x16x32_bf16 v[240:243],v[104:107],v[144:147],v[240:243]
v_fma_mixhi_f16 v120,v142,s99,0
v_cvt_pk_bf16_f32 v155,v157,v159
v_mfma_f32_16x16x32_bf16 v[248:251],v[108:111],v[144:147],v[248:251]
ds_read_u16 v129,v129
v_fma_mixhi_f16 v122,v143,s99,0
v_mfma_f32_16x16x32_bf16 v[212:215],v[88:91],v[152:155],v[212:215]
v_pk_fma_f16 v121,v120,v120,s54
v_lshlrev_b32_sdwa v133,1,v133 dst_sel:DWORD dst_unused:UNUSED_PAD src0_sel:DWORD src1_sel:WORD_1
v_lshlrev_b32_sdwa v135,1,v135 dst_sel:DWORD dst_unused:UNUSED_PAD src0_sel:DWORD src1_sel:WORD_1
v_mfma_f32_16x16x32_bf16 v[220:223],v[92:95],v[152:155],v[220:223]
ds_read_u16_d16_hi v131,v131
ds_read_u16 v132,v132
v_pk_fma_f16 v123,v122,v122,s54
v_mfma_f32_16x16x32_bf16 v[228:231],v[96:99],v[152:155],v[228:231]
ds_read_b64_tr_b16 v[88:89],v6 offset:18480
v_pk_mul_f16 v121,v120,v121
v_lshlrev_b32_sdwa v136,1,v136 dst_sel:DWORD dst_unused:UNUSED_PAD src0_sel:DWORD src1_sel:WORD_1
v_pk_mul_f16 v123,v122,v123
v_mfma_f32_16x16x32_bf16 v[236:239],v[100:103],v[152:155],v[236:239]
ds_read_b64_tr_b16 v[90:91],v6 offset:20016
v_lshlrev_b32_sdwa v138,1,v138 dst_sel:DWORD dst_unused:UNUSED_PAD src0_sel:DWORD src1_sel:WORD_1
v_mfma_f32_16x16x32_bf16 v[244:247],v[104:107],v[152:155],v[244:247]
ds_read_u16_d16_hi v134,v134
v_exp_f16 v120,v121
v_mfma_f32_16x16x32_bf16 v[252:255],v[108:111],v[152:155],v[252:255]
ds_read_b64_tr_b16 v[92:93],v6 offset:18488
v_lshlrev_b32_sdwa v137,1,v137 dst_sel:DWORD dst_unused:UNUSED_PAD src0_sel:DWORD src1_sel:WORD_1
v_mfma_f32_16x16x32_bf16 v[144:147],v[64:67],v[40:43],v[112:115]
ds_read_b64_tr_b16 v[94:95],v6 offset:20024
v_lshlrev_b32_sdwa v139,1,v139 dst_sel:DWORD dst_unused:UNUSED_PAD src0_sel:DWORD src1_sel:WORD_1
v_exp_f16 v122,v123
v_mfma_f32_16x16x32_bf16 v[152:155],v[64:67],v[44:47],v[112:115]
ds_read_u16 v133,v133
ds_read_b64_tr_b16 v[96:97],v6 offset:18544
v_mfma_f32_16x16x32_bf16 v[156:159],v[68:71],v[44:47],v[116:119]
ds_read_u16_d16_hi v135,v135
v_exp_f16_sdwa v122,v123 dst_sel:WORD_1 dst_unused:UNUSED_PRESERVE src0_sel:WORD_1
v_exp_f16_sdwa v120,v121 dst_sel:WORD_1 dst_unused:UNUSED_PRESERVE src0_sel:WORD_1
v_mfma_f32_16x16x32_bf16 v[148:151],v[68:71],v[40:43],v[116:119]
ds_read_b64_tr_b16 v[98:99],v6 offset:20080
v_pk_fma_f16 v120,v120,s55,s55
v_pk_fma_f16 v122,v122,s55,s55
v_mfma_f32_16x16x32_bf16 v[144:147],v[72:75],v[48:51],v[144:147]
v_rcp_f16 v121,v120
v_mfma_f32_16x16x32_bf16 v[152:155],v[72:75],v[52:55],v[152:155]
ds_read_b64_tr_b16 v[100:101],v6 offset:18552
v_rcp_f16 v123,v122
v_mfma_f32_16x16x32_bf16 v[156:159],v[76:79],v[52:55],v[156:159]
ds_read_u16 v136,v136
v_rcp_f16_sdwa v121,v120 dst_sel:WORD_1 dst_unused:UNUSED_PRESERVE src0_sel:WORD_1
v_mfma_f32_16x16x32_bf16 v[148:151],v[76:79],v[48:51],v[148:151]
ds_read_b64_tr_b16 v[102:103],v6 offset:20088
ds_read_b64_tr_b16 v[104:105],v6 offset:18608
v_fma_mix_f32 v141,v141,v123,0 op_sel_hi:[0,1,0]
v_rcp_f16_sdwa v123,v122 dst_sel:WORD_1 dst_unused:UNUSED_PRESERVE src0_sel:WORD_1
v_mfma_f32_16x16x32_bf16 v[144:147],v[80:83],v[56:59],v[144:147]
ds_read_u16_d16_hi v138,v138
v_fma_mix_f32 v142,v142,v121,0 op_sel:[0,1,0] op_sel_hi:[0,1,0]
v_mfma_f32_16x16x32_bf16 v[152:155],v[80:83],v[60:63],v[152:155]
s_waitcnt lgkmcnt(6)
ds_read_b64_tr_b16 v[106:107],v6 offset:20144
ds_read_b64_tr_b16 v[108:109],v6 offset:18616
v_fma_mix_f32 v140,v140,v121,0 op_sel_hi:[0,1,0]
v_lshl_add_u64 v[128:129],v[128:129],0,v[130:131]
v_lshl_add_u64 v[130:131],v[132:133],0,v[134:135]
v_mfma_f32_16x16x32_bf16 v[156:159],v[84:87],v[60:63],v[156:159]
ds_read_u16 v137,v137
v_fma_mix_f32 v143,v143,v123,0 op_sel:[0,1,0] op_sel_hi:[0,1,0]
v_mfma_f32_16x16x32_bf16 v[148:151],v[84:87],v[56:59],v[148:151]
ds_read_b64_tr_b16 v[110:111],v6 offset:20152
ds_read_b128 v[64:67],v7 offset:1536
v_lshlrev_b32_sdwa v144,1,v144 dst_sel:DWORD dst_unused:UNUSED_PAD src0_sel:DWORD src1_sel:WORD_1
v_lshlrev_b32_sdwa v146,1,v146 dst_sel:DWORD dst_unused:UNUSED_PAD src0_sel:DWORD src1_sel:WORD_1
v_mfma_f32_16x16x32_bf16 v[160:163],v[88:91],v[128:131],v[160:163]
ds_read_u16_d16_hi v139,v139
ds_read_b128 v[68:71],v7 offset:1568
v_lshlrev_b32_sdwa v145,1,v145 dst_sel:DWORD dst_unused:UNUSED_PAD src0_sel:DWORD src1_sel:WORD_1
v_lshlrev_b32_sdwa v147,1,v147 dst_sel:DWORD dst_unused:UNUSED_PAD src0_sel:DWORD src1_sel:WORD_1
v_fma_mixlo_f16 v122,v157,s99,0
v_fma_mixlo_f16 v120,v156,s99,0
v_mfma_f32_16x16x32_bf16 v[168:171],v[92:95],v[128:131],v[168:171]
s_waitcnt lgkmcnt(12)
ds_read_u16 v144,v144
ds_read_b128 v[72:75],v7 offset:1664
v_lshlrev_b32_sdwa v148,1,v148 dst_sel:DWORD dst_unused:UNUSED_PAD src0_sel:DWORD src1_sel:WORD_1
v_lshlrev_b32_sdwa v150,1,v150 dst_sel:DWORD dst_unused:UNUSED_PAD src0_sel:DWORD src1_sel:WORD_1
s_waitcnt lgkmcnt(3)
v_lshl_add_u64 v[136:137],v[136:137],0,v[138:139]
v_fma_mixhi_f16 v120,v158,s99,0
v_mfma_f32_16x16x32_bf16 v[176:179],v[96:99],v[128:131],v[176:179]
s_setprio 1
ds_read_u16_d16_hi v146,v146
ds_read_b128 v[76:79],v7 offset:1696
v_cvt_pk_bf16_f32 v138,v140,v142
v_cvt_pk_bf16_f32 v139,v141,v143
v_fma_mixhi_f16 v122,v159,s99,0
v_lshlrev_b32_sdwa v149,1,v149 dst_sel:DWORD dst_unused:UNUSED_PAD src0_sel:DWORD src1_sel:WORD_1
v_mfma_f32_16x16x32_bf16 v[184:187],v[100:103],v[128:131],v[184:187]
ds_read_u16 v145,v145
ds_read_b128 v[80:83],v7 offset:1792
v_lshlrev_b32_sdwa v151,1,v151 dst_sel:DWORD dst_unused:UNUSED_PAD src0_sel:DWORD src1_sel:WORD_1
v_pk_fma_f16 v123,v122,v122,s54
v_pk_fma_f16 v121,v120,v120,s54
v_pk_mul_f16 v121,v120,v121
v_mfma_f32_16x16x32_bf16 v[192:195],v[104:107],v[128:131],v[192:195]
ds_read_u16_d16_hi v147,v147
ds_read_b128 v[84:87],v7 offset:1824
v_pk_mul_f16 v123,v122,v123
v_lshlrev_b32_sdwa v152,1,v152 dst_sel:DWORD dst_unused:UNUSED_PAD src0_sel:DWORD src1_sel:WORD_1
v_lshlrev_b32_sdwa v154,1,v154 dst_sel:DWORD dst_unused:UNUSED_PAD src0_sel:DWORD src1_sel:WORD_1
v_exp_f16 v120,v121
v_mfma_f32_16x16x32_bf16 v[200:203],v[108:111],v[128:131],v[200:203]
ds_read_u16 v148,v148
v_exp_f16 v122,v123
v_lshlrev_b32_sdwa v153,1,v153 dst_sel:DWORD dst_unused:UNUSED_PAD src0_sel:DWORD src1_sel:WORD_1
v_mfma_f32_16x16x32_bf16 v[164:167],v[88:91],v[136:139],v[164:167]
s_nop 0
ds_read_u16_d16_hi v150,v150
v_lshlrev_b32_sdwa v155,1,v155 dst_sel:DWORD dst_unused:UNUSED_PAD src0_sel:DWORD src1_sel:WORD_1
v_exp_f16_sdwa v120,v121 dst_sel:WORD_1 dst_unused:UNUSED_PRESERVE src0_sel:WORD_1
v_exp_f16_sdwa v122,v123 dst_sel:WORD_1 dst_unused:UNUSED_PRESERVE src0_sel:WORD_1
v_mfma_f32_16x16x32_bf16 v[172:175],v[92:95],v[136:139],v[172:175]
ds_read_u16 v149,v149
v_pk_fma_f16 v120,v120,s55,s55
v_pk_fma_f16 v122,v122,s55,s55
v_rcp_f16 v121,v120
v_mfma_f32_16x16x32_bf16 v[180:183],v[96:99],v[136:139],v[180:183]
s_nop 0
ds_read_u16_d16_hi v151,v151
v_rcp_f16 v123,v122
v_rcp_f16_sdwa v121,v120 dst_sel:WORD_1 dst_unused:UNUSED_PRESERVE src0_sel:WORD_1
v_mfma_f32_16x16x32_bf16 v[188:191],v[100:103],v[136:139],v[188:191]
ds_read_u16 v152,v152
v_rcp_f16_sdwa v123,v122 dst_sel:WORD_1 dst_unused:UNUSED_PRESERVE src0_sel:WORD_1
v_fma_mix_f32 v156,v156,v121,0 op_sel_hi:[0,1,0]
v_mfma_f32_16x16x32_bf16 v[196:199],v[104:107],v[136:139],v[196:199]
s_setprio 0
ds_read_u16_d16_hi v154,v154
v_fma_mix_f32 v158,v158,v121,0 op_sel:[0,1,0] op_sel_hi:[0,1,0]
s_waitcnt lgkmcnt(7)
v_lshl_add_u64 v[144:145],v[144:145],0,v[146:147]
s_waitcnt lgkmcnt(2)
v_lshl_add_u64 v[146:147],v[148:149],0,v[150:151]
v_fma_mix_f32 v157,v157,v123,0 op_sel_hi:[0,1,0]
v_mfma_f32_16x16x32_bf16 v[204:207],v[108:111],v[136:139],v[204:207]
s_nop 0
ds_read_u16 v153,v153
v_fma_mix_f32 v159,v159,v123,0 op_sel:[0,1,0] op_sel_hi:[0,1,0]
ds_read_u16_d16_hi v155,v155
s_waitcnt lgkmcnt(0)
v_lshl_add_u64 v[152:153],v[152:153],0,v[154:155]
v_cvt_pk_bf16_f32 v154,v156,v158
v_cvt_pk_bf16_f32 v155,v157,v159
v_mfma_f32_16x16x32_bf16 v[208:211],v[88:91],v[144:147],v[208:211]
s_cbranch_vccz RA24_%=
s_waitcnt vmcnt(0)
RA24_%=:
s_barrier
s_setprio 0
v_xor_b32 v9,64,v10
v_mul_f32 v160,v160,v64
v_mul_f32 v161,v161,v65
v_mul_f32 v162,v162,v66
v_mfma_f32_16x16x32_bf16 v[216:219],v[92:95],v[144:147],v[216:219]
v_mul_f32 v163,v163,v67
v_mul_f32 v168,v168,v68
v_mul_f32 v169,v169,v69
v_mfma_f32_16x16x32_bf16 v[224:227],v[96:99],v[144:147],v[224:227]
v_mul_f32 v170,v170,v70
v_mul_f32 v171,v171,v71
v_cvt_pk_bf16_f32 v16,v160,v161
v_mfma_f32_16x16x32_bf16 v[232:235],v[100:103],v[144:147],v[232:235]
v_cvt_pk_bf16_f32 v17,v162,v163
v_cvt_pk_bf16_f32 v18,v168,v169
v_cvt_pk_bf16_f32 v19,v170,v171
v_mfma_f32_16x16x32_bf16 v[240:243],v[104:107],v[144:147],v[240:243]
v_mul_f32_dpp v176,v176,v72 quad_perm:[1,0,3,2] row_mask:0xf bank_mask:0xf
v_mul_f32_dpp v177,v177,v73 quad_perm:[1,0,3,2] row_mask:0xf bank_mask:0xf
v_mul_f32_dpp v178,v178,v74 quad_perm:[1,0,3,2] row_mask:0xf bank_mask:0xf
v_mfma_f32_16x16x32_bf16 v[248:251],v[108:111],v[144:147],v[248:251]
v_mul_f32_dpp v179,v179,v75 quad_perm:[1,0,3,2] row_mask:0xf bank_mask:0xf
v_mul_f32_dpp v184,v184,v76 quad_perm:[1,0,3,2] row_mask:0xf bank_mask:0xf
v_mul_f32_dpp v185,v185,v77 quad_perm:[1,0,3,2] row_mask:0xf bank_mask:0xf
v_mul_f32_dpp v186,v186,v78 quad_perm:[1,0,3,2] row_mask:0xf bank_mask:0xf
v_mfma_f32_16x16x32_bf16 v[212:215],v[88:91],v[152:155],v[212:215]
v_mul_f32_dpp v187,v187,v79 quad_perm:[1,0,3,2] row_mask:0xf bank_mask:0xf
v_cvt_pk_bf16_f32 v24,v176,v177
v_cvt_pk_bf16_f32 v25,v178,v179
v_mfma_f32_16x16x32_bf16 v[220:223],v[92:95],v[152:155],v[220:223]
v_cvt_pk_bf16_f32 v26,v184,v185
v_cvt_pk_bf16_f32 v27,v186,v187
s_mov_b64 exec,s[12:13]
v_swap_b32 v16,v24
v_swap_b32 v17,v25
v_swap_b32 v18,v26
v_swap_b32 v19,v27
s_mov_b64 exec,-1
v_mfma_f32_16x16x32_bf16 v[228:231],v[96:99],v[152:155],v[228:231]
s_add_u32 s65,s49,0
buffer_store_dwordx4 v[16:19],v10,s[40:43],s65 offen offset:0 sc1
v_mfma_f32_16x16x32_bf16 v[236:239],v[100:103],v[152:155],v[236:239]
v_mfma_f32_16x16x32_bf16 v[244:247],v[104:107],v[152:155],v[244:247]
v_mul_f32 v192,v192,v80
v_mul_f32 v193,v193,v81
v_mfma_f32_16x16x32_bf16 v[252:255],v[108:111],v[152:155],v[252:255]
s_nop 0
v_mul_f32 v194,v194,v82
v_mul_f32 v195,v195,v83
v_mul_f32 v200,v200,v84
v_mul_f32 v201,v201,v85
v_mul_f32 v202,v202,v86
v_mul_f32 v203,v203,v87
v_cvt_pk_bf16_f32 v32,v192,v193
v_cvt_pk_bf16_f32 v33,v194,v195
v_cvt_pk_bf16_f32 v34,v200,v201
v_cvt_pk_bf16_f32 v35,v202,v203
v_mul_f32_dpp v164,v164,v64 quad_perm:[1,0,3,2] row_mask:0xf bank_mask:0xf
v_mul_f32_dpp v165,v165,v65 quad_perm:[1,0,3,2] row_mask:0xf bank_mask:0xf
v_mul_f32_dpp v166,v166,v66 quad_perm:[1,0,3,2] row_mask:0xf bank_mask:0xf
v_mul_f32_dpp v167,v167,v67 quad_perm:[1,0,3,2] row_mask:0xf bank_mask:0xf
v_mul_f32_dpp v172,v172,v68 quad_perm:[1,0,3,2] row_mask:0xf bank_mask:0xf
v_mul_f32_dpp v173,v173,v69 quad_perm:[1,0,3,2] row_mask:0xf bank_mask:0xf
v_mul_f32_dpp v174,v174,v70 quad_perm:[1,0,3,2] row_mask:0xf bank_mask:0xf
v_mul_f32_dpp v175,v175,v71 quad_perm:[1,0,3,2] row_mask:0xf bank_mask:0xf
v_cvt_pk_bf16_f32 v20,v164,v165
v_cvt_pk_bf16_f32 v21,v166,v167
s_add_u32 s65,s49,0
buffer_store_dwordx4 v[24:27],v9,s[40:43],s65 offen offset:384 sc1
v_cvt_pk_bf16_f32 v22,v172,v173
v_cvt_pk_bf16_f32 v23,v174,v175
s_mov_b64 exec,s[12:13]
v_swap_b32 v32,v20
v_swap_b32 v33,v21
v_swap_b32 v34,v22
v_swap_b32 v35,v23
s_mov_b64 exec,-1
v_mul_f32 v180,v180,v72
v_mul_f32 v181,v181,v73
v_mul_f32 v182,v182,v74
v_mul_f32 v183,v183,v75
v_mul_f32 v188,v188,v76
v_mul_f32 v189,v189,v77
v_mul_f32 v190,v190,v78
v_mul_f32 v191,v191,v79
v_cvt_pk_bf16_f32 v28,v180,v181
v_cvt_pk_bf16_f32 v29,v182,v183
v_cvt_pk_bf16_f32 v30,v188,v189
v_cvt_pk_bf16_f32 v31,v190,v191
v_mul_f32_dpp v196,v196,v80 quad_perm:[1,0,3,2] row_mask:0xf bank_mask:0xf
v_mul_f32_dpp v197,v197,v81 quad_perm:[1,0,3,2] row_mask:0xf bank_mask:0xf
v_mul_f32_dpp v198,v198,v82 quad_perm:[1,0,3,2] row_mask:0xf bank_mask:0xf
v_mul_f32_dpp v199,v199,v83 quad_perm:[1,0,3,2] row_mask:0xf bank_mask:0xf
v_mul_f32_dpp v204,v204,v84 quad_perm:[1,0,3,2] row_mask:0xf bank_mask:0xf
v_mul_f32_dpp v205,v205,v85 quad_perm:[1,0,3,2] row_mask:0xf bank_mask:0xf
v_mul_f32_dpp v206,v206,v86 quad_perm:[1,0,3,2] row_mask:0xf bank_mask:0xf
v_mul_f32_dpp v207,v207,v87 quad_perm:[1,0,3,2] row_mask:0xf bank_mask:0xf
s_add_u32 s65,s49,0
buffer_store_dwordx4 v[32:35],v10,s[40:43],s65 offen offset:128 sc1
v_cvt_pk_bf16_f32 v36,v196,v197
v_cvt_pk_bf16_f32 v37,v198,v199
v_cvt_pk_bf16_f32 v38,v204,v205
v_cvt_pk_bf16_f32 v39,v206,v207
s_mov_b64 exec,s[12:13]
v_swap_b32 v28,v36
v_swap_b32 v29,v37
v_swap_b32 v30,v38
v_swap_b32 v31,v39
s_mov_b64 exec,-1
v_mul_f32 v208,v208,v64
v_mul_f32 v209,v209,v65
v_mul_f32 v210,v210,v66
v_mul_f32 v211,v211,v67
v_mul_f32 v216,v216,v68
v_mul_f32 v217,v217,v69
v_mul_f32 v218,v218,v70
v_mul_f32 v219,v219,v71
v_cvt_pk_bf16_f32 v40,v208,v209
v_cvt_pk_bf16_f32 v41,v210,v211
v_cvt_pk_bf16_f32 v42,v216,v217
v_cvt_pk_bf16_f32 v43,v218,v219
v_mul_f32_dpp v224,v224,v72 quad_perm:[1,0,3,2] row_mask:0xf bank_mask:0xf
v_mul_f32_dpp v225,v225,v73 quad_perm:[1,0,3,2] row_mask:0xf bank_mask:0xf
v_mul_f32_dpp v226,v226,v74 quad_perm:[1,0,3,2] row_mask:0xf bank_mask:0xf
v_mul_f32_dpp v227,v227,v75 quad_perm:[1,0,3,2] row_mask:0xf bank_mask:0xf
v_mul_f32_dpp v232,v232,v76 quad_perm:[1,0,3,2] row_mask:0xf bank_mask:0xf
v_mul_f32_dpp v233,v233,v77 quad_perm:[1,0,3,2] row_mask:0xf bank_mask:0xf
s_add_u32 s65,s49,0
buffer_store_dwordx4 v[20:23],v9,s[40:43],s65 offen offset:512 sc1
v_mul_f32_dpp v234,v234,v78 quad_perm:[1,0,3,2] row_mask:0xf bank_mask:0xf
v_mul_f32_dpp v235,v235,v79 quad_perm:[1,0,3,2] row_mask:0xf bank_mask:0xf
v_cvt_pk_bf16_f32 v48,v224,v225
v_cvt_pk_bf16_f32 v49,v226,v227
v_cvt_pk_bf16_f32 v50,v232,v233
v_cvt_pk_bf16_f32 v51,v234,v235
s_mov_b64 exec,s[12:13]
v_swap_b32 v40,v48
v_swap_b32 v41,v49
v_swap_b32 v42,v50
v_swap_b32 v43,v51
s_mov_b64 exec,-1
v_mul_f32 v240,v240,v80
v_mul_f32 v241,v241,v81
v_mul_f32 v242,v242,v82
v_mul_f32 v243,v243,v83
v_mul_f32 v248,v248,v84
v_mul_f32 v249,v249,v85
v_mul_f32 v250,v250,v86
v_mul_f32 v251,v251,v87
v_cvt_pk_bf16_f32 v56,v240,v241
v_cvt_pk_bf16_f32 v57,v242,v243
v_cvt_pk_bf16_f32 v58,v248,v249
v_cvt_pk_bf16_f32 v59,v250,v251
v_mul_f32_dpp v212,v212,v64 quad_perm:[1,0,3,2] row_mask:0xf bank_mask:0xf
v_mul_f32_dpp v213,v213,v65 quad_perm:[1,0,3,2] row_mask:0xf bank_mask:0xf
v_mul_f32_dpp v214,v214,v66 quad_perm:[1,0,3,2] row_mask:0xf bank_mask:0xf
v_mul_f32_dpp v215,v215,v67 quad_perm:[1,0,3,2] row_mask:0xf bank_mask:0xf
s_add_u32 s65,s49,0
buffer_store_dwordx4 v[28:31],v10,s[40:43],s65 offen offset:256 sc1
v_mul_f32_dpp v220,v220,v68 quad_perm:[1,0,3,2] row_mask:0xf bank_mask:0xf
v_mul_f32_dpp v221,v221,v69 quad_perm:[1,0,3,2] row_mask:0xf bank_mask:0xf
v_mul_f32_dpp v222,v222,v70 quad_perm:[1,0,3,2] row_mask:0xf bank_mask:0xf
v_mul_f32_dpp v223,v223,v71 quad_perm:[1,0,3,2] row_mask:0xf bank_mask:0xf
v_cvt_pk_bf16_f32 v44,v212,v213
v_cvt_pk_bf16_f32 v45,v214,v215
v_cvt_pk_bf16_f32 v46,v220,v221
v_cvt_pk_bf16_f32 v47,v222,v223
s_mov_b64 exec,s[12:13]
v_swap_b32 v56,v44
v_swap_b32 v57,v45
v_swap_b32 v58,v46
v_swap_b32 v59,v47
s_mov_b64 exec,-1
v_mul_f32 v228,v228,v72
v_mul_f32 v229,v229,v73
v_mul_f32 v230,v230,v74
v_mul_f32 v231,v231,v75
v_mul_f32 v236,v236,v76
v_mul_f32 v237,v237,v77
v_mul_f32 v238,v238,v78
v_mul_f32 v239,v239,v79
v_cvt_pk_bf16_f32 v52,v228,v229
v_cvt_pk_bf16_f32 v53,v230,v231
v_cvt_pk_bf16_f32 v54,v236,v237
v_cvt_pk_bf16_f32 v55,v238,v239
v_mul_f32_dpp v244,v244,v80 quad_perm:[1,0,3,2] row_mask:0xf bank_mask:0xf
v_mul_f32_dpp v245,v245,v81 quad_perm:[1,0,3,2] row_mask:0xf bank_mask:0xf
s_add_u32 s65,s49,0
buffer_store_dwordx4 v[36:39],v9,s[40:43],s65 offen offset:640 sc1
v_mul_f32_dpp v246,v246,v82 quad_perm:[1,0,3,2] row_mask:0xf bank_mask:0xf
v_mul_f32_dpp v247,v247,v83 quad_perm:[1,0,3,2] row_mask:0xf bank_mask:0xf
s_nop 0
s_mov_b32 s86,0xbe801d5a
v_mul_f32_dpp v252,v252,v84 quad_perm:[1,0,3,2] row_mask:0xf bank_mask:0xf
v_mul_f32_dpp v253,v253,v85 quad_perm:[1,0,3,2] row_mask:0xf bank_mask:0xf
v_mul_f32_dpp v254,v254,v86 quad_perm:[1,0,3,2] row_mask:0xf bank_mask:0xf
v_mul_f32_dpp v255,v255,v87 quad_perm:[1,0,3,2] row_mask:0xf bank_mask:0xf
v_cvt_pk_bf16_f32 v60,v244,v245
v_cvt_pk_bf16_f32 v61,v246,v247
v_cvt_pk_bf16_f32 v62,v252,v253
v_cvt_pk_bf16_f32 v63,v254,v255
s_mov_b64 exec,s[12:13]
v_swap_b32 v52,v60
v_swap_b32 v53,v61
v_swap_b32 v54,v62
v_swap_b32 v55,v63
s_mov_b64 exec,-1
s_cbranch_vccnz RA26_%=
v_and_b32 v112,63,%1
s_bfe_u32 s64,%0,0x20003
s_lshl_b32 s64,s64,13
s_bitcmp1_b32 s52,0
s_mov_b32 s66,28672
s_cselect_b32 s66,69632,s66
s_add_u32 s64,s64,s66
v_lshl_add_u32 v112,v112,7,s64
s_getpc_b64 s[14:15]
N_pgA25_%=:
s_sub_u32 s14,s14,N_pgA25_%=-N_main_%=
s_subb_u32 s15,s15,0
s_mov_b32 m0,157312
v_min_u32 v112,N_mainend_%=-N_main_%=,v112
global_load_lds_dword v112,s[14:15]
RA26_%=:
s_add_u32 s4,s10,12
s_cmp_ge_u32 s4,20
s_cselect_b32 s7,20,0
s_sub_u32 s4,s4,s7
s_mul_i32 s4,s4,3488
s_add_u32 s5,s10,14
s_cmp_ge_u32 s5,20
s_cselect_b32 s7,20,0
s_sub_u32 s5,s5,s7
s_mul_i32 s5,s5,3488
s_add_u32 s6,s10,16
s_cmp_ge_u32 s6,20
s_cselect_b32 s7,20,0
s_sub_u32 s6,s6,s7
s_mul_i32 s6,s6,3488
v_add_u32 v112,s4,v15
v_add_u32 v116,s5,v15
s_mov_b64 exec,s[8:9]
v_add_u32 v112,s5,v15
v_add_u32 v116,s6,v15
s_mov_b64 exec,-1
v_xor_b32 v113,16,v112
v_xor_b32 v117,16,v116
v_xor_b32 v114,32,v112
v_xor_b32 v118,32,v116
v_xor_b32 v115,48,v112
v_xor_b32 v119,48,v116
s_waitcnt vmcnt(7)
s_barrier
s_setprio 3
ds_read_b128 v[136:139],v116 offset:0
ds_read_b128 v[88:91],v11 offset:768
ds_read_b128 v[140:143],v116 offset:3488
ds_read_b128 v[128:131],v112 offset:0
ds_read_b128 v[132:135],v112 offset:3488
ds_read_b128 v[92:95],v12 offset:0
ds_read_b128 v[152:155],v117 offset:0
ds_read_b128 v[96:99],v11 offset:1920
ds_read_b128 v[156:159],v117 offset:3488
ds_read_b128 v[144:147],v113 offset:0
ds_read_b128 v[148:151],v113 offset:3488
ds_read_b128 v[100:103],v12 offset:1152
s_waitcnt lgkmcnt(10)
v_mfma_f32_16x16x32_bf16 v[160:163],v[88:91],v[136:139],0
s_waitcnt lgkmcnt(9)
s_add_u32 s65,s49,786432
buffer_store_dwordx4 v[40:43],v10,s[40:43],s65 offen offset:0 sc1
v_mfma_f32_16x16x32_bf16 v[184:187],v[88:91],v[140:143],0
s_waitcnt lgkmcnt(6)
ds_read_b128 v[140:143],v116 offset:3552
v_smfmac_f32_16x16x64_bf16 v[160:163],v[92:95],v[128:135],v13
ds_read_b128 v[88:91],v11 offset:5376
ds_read_b128 v[128:131],v112 offset:64
v_smfmac_f32_16x16x64_bf16 v[184:187],v[92:95],v[132:139],v13
s_waitcnt lgkmcnt(7)
ds_read_b128 v[136:139],v116 offset:64
ds_read_b128 v[132:135],v112 offset:3552
v_mfma_f32_16x16x32_bf16 v[164:167],v[96:99],v[152:155],0
s_waitcnt lgkmcnt(8)
ds_read_b128 v[92:95],v12 offset:4608
v_add_f32 v120,v160,v161
v_mul_f32 v124,v160,v160
v_fmac_f32 v124,v161,v161
v_mfma_f32_16x16x32_bf16 v[188:191],v[96:99],v[156:159],0
s_waitcnt lgkmcnt(6)
ds_read_b128 v[156:159],v117 offset:3552
v_add_f32 v121,v162,v163
v_mul_f32 v125,v162,v162
v_fmac_f32 v125,v163,v163
v_smfmac_f32_16x16x64_bf16 v[164:167],v[100:103],v[144:151],v13
ds_read_b128 v[96:99],v11 offset:6528
ds_read_b128 v[144:147],v113 offset:64
v_add_f32 v122,v184,v185
v_mul_f32 v126,v184,v184
v_fmac_f32 v126,v185,v185
v_smfmac_f32_16x16x64_bf16 v[188:191],v[100:103],v[148:155],v13
s_waitcnt lgkmcnt(5)
s_add_u32 s65,s49,786432
buffer_store_dwordx4 v[48:51],v9,s[40:43],s65 offen offset:384 sc1
ds_read_b128 v[152:155],v117 offset:64
ds_read_b128 v[148:151],v113 offset:3552
v_add_f32 v123,v186,v187
v_mul_f32 v127,v186,v186
v_fmac_f32 v127,v187,v187
v_mfma_f32_16x16x32_bf16 v[168:171],v[88:91],v[136:139],0
ds_read_b128 v[100:103],v12 offset:5760
v_add_f32 v120,v120,v164
v_fmac_f32 v124,v164,v164
v_add_f32 v120,v120,v165
v_fmac_f32 v124,v165,v165
v_mfma_f32_16x16x32_bf16 v[192:195],v[88:91],v[140:143],0
s_waitcnt lgkmcnt(6)
ds_read_b128 v[140:143],v116 offset:3616
v_add_f32 v121,v121,v166
v_fmac_f32 v125,v166,v166
v_add_f32 v121,v121,v167
v_fmac_f32 v125,v167,v167
v_smfmac_f32_16x16x64_bf16 v[168:171],v[92:95],v[128:135],v13
ds_read_b128 v[88:91],v11 offset:9984
ds_read_b128 v[128:131],v112 offset:128
v_add_f32 v122,v122,v188
v_fmac_f32 v126,v188,v188
v_add_f32 v122,v122,v189
v_fmac_f32 v126,v189,v189
v_smfmac_f32_16x16x64_bf16 v[192:195],v[92:95],v[132:139],v13
s_waitcnt lgkmcnt(5)
ds_read_b128 v[136:139],v116 offset:128
ds_read_b128 v[132:135],v112 offset:3616
v_add_f32 v123,v123,v190
v_fmac_f32 v127,v190,v190
v_add_f32 v123,v123,v191
v_fmac_f32 v127,v191,v191
v_mfma_f32_16x16x32_bf16 v[172:175],v[96:99],v[152:155],0
s_add_u32 s65,s49,786432
buffer_store_dwordx4 v[56:59],v10,s[40:43],s65 offen offset:128 sc1
ds_read_b128 v[92:95],v12 offset:9216
v_add_f32 v120,v120,v168
v_fmac_f32 v124,v168,v168
v_add_f32 v120,v120,v169
v_fmac_f32 v124,v169,v169
v_mfma_f32_16x16x32_bf16 v[196:199],v[96:99],v[156:159],0
s_waitcnt lgkmcnt(6)
ds_read_b128 v[156:159],v117 offset:3616
v_add_f32 v121,v121,v170
v_fmac_f32 v125,v170,v170
v_add_f32 v121,v121,v171
v_fmac_f32 v125,v171,v171
v_smfmac_f32_16x16x64_bf16 v[172:175],v[100:103],v[144:151],v13
ds_read_b128 v[96:99],v11 offset:11136
ds_read_b128 v[144:147],v113 offset:128
v_add_f32 v122,v122,v192
v_fmac_f32 v126,v192,v192
v_add_f32 v122,v122,v193
v_fmac_f32 v126,v193,v193
v_smfmac_f32_16x16x64_bf16 v[196:199],v[100:103],v[148:155],v13
s_waitcnt lgkmcnt(5)
ds_read_b128 v[152:155],v117 offset:128
ds_read_b128 v[148:151],v113 offset:3616
v_add_f32 v123,v123,v194
v_fmac_f32 v127,v194,v194
v_add_f32 v123,v123,v195
v_fmac_f32 v127,v195,v195
v_mfma_f32_16x16x32_bf16 v[176:179],v[88:91],v[136:139],0
ds_read_b128 v[100:103],v12 offset:10368
v_add_f32 v120,v120,v172
v_fmac_f32 v124,v172,v172
v_add_f32 v120,v120,v173
v_fmac_f32 v124,v173,v173
v_mfma_f32_16x16x32_bf16 v[200:203],v[88:91],v[140:143],0
s_waitcnt lgkmcnt(6)
s_add_u32 s65,s49,786432
buffer_store_dwordx4 v[44:47],v9,s[40:43],s65 offen offset:512 sc1
ds_read_b128 v[140:143],v118 offset:3488
v_add_f32 v121,v121,v174
v_fmac_f32 v125,v174,v174
v_add_f32 v121,v121,v175
v_fmac_f32 v125,v175,v175
v_smfmac_f32_16x16x64_bf16 v[176:179],v[92:95],v[128:135],v13
ds_read_b128 v[88:91],v11 offset:3072
ds_read_b128 v[128:131],v114 offset:0
v_add_f32 v122,v122,v196
v_fmac_f32 v126,v196,v196
v_add_f32 v122,v122,v197
v_fmac_f32 v126,v197,v197
v_smfmac_f32_16x16x64_bf16 v[200:203],v[92:95],v[132:139],v13
s_waitcnt lgkmcnt(5)
ds_read_b128 v[136:139],v118 offset:0
ds_read_b128 v[132:135],v114 offset:3488
v_add_f32 v123,v123,v198
v_fmac_f32 v127,v198,v198
v_add_f32 v123,v123,v199
v_fmac_f32 v127,v199,v199
v_mfma_f32_16x16x32_bf16 v[180:183],v[96:99],v[152:155],0
ds_read_b128 v[92:95],v12 offset:2304
v_add_f32 v120,v120,v176
v_fmac_f32 v124,v176,v176
v_add_f32 v120,v120,v177
v_fmac_f32 v124,v177,v177
v_mfma_f32_16x16x32_bf16 v[204:207],v[96:99],v[156:159],0
s_waitcnt lgkmcnt(6)
ds_read_b128 v[156:159],v119 offset:3488
v_add_f32 v121,v121,v178
v_fmac_f32 v125,v178,v178
v_add_f32 v121,v121,v179
v_fmac_f32 v125,v179,v179
v_smfmac_f32_16x16x64_bf16 v[180:183],v[100:103],v[144:151],v13
s_add_u32 s65,s49,786432
buffer_store_dwordx4 v[52:55],v10,s[40:43],s65 offen offset:256 sc1
ds_read_b128 v[96:99],v11 offset:4224
ds_read_b128 v[144:147],v115 offset:0
v_add_f32 v122,v122,v200
v_fmac_f32 v126,v200,v200
v_add_f32 v122,v122,v201
v_fmac_f32 v126,v201,v201
v_smfmac_f32_16x16x64_bf16 v[204:207],v[100:103],v[148:155],v13
s_waitcnt lgkmcnt(5)
ds_read_b128 v[152:155],v119 offset:0
ds_read_b128 v[148:151],v115 offset:3488
v_add_f32 v123,v123,v202
v_fmac_f32 v127,v202,v202
v_add_f32 v123,v123,v203
v_fmac_f32 v127,v203,v203
v_mfma_f32_16x16x32_bf16 v[208:211],v[88:91],v[136:139],0
ds_read_b128 v[100:103],v12 offset:3456
v_add_f32 v120,v120,v180
v_fmac_f32 v124,v180,v180
v_add_f32 v120,v120,v181
v_fmac_f32 v124,v181,v181
v_mfma_f32_16x16x32_bf16 v[232:235],v[88:91],v[140:143],0
s_waitcnt lgkmcnt(6)
ds_read_b128 v[140:143],v118 offset:3552
v_add_f32 v121,v121,v182
v_fmac_f32 v125,v182,v182
v_add_f32 v121,v121,v183
v_fmac_f32 v125,v183,v183
v_smfmac_f32_16x16x64_bf16 v[208:211],v[92:95],v[128:135],v13
s_add_u32 s65,s49,786432
buffer_store_dwordx4 v[60:63],v9,s[40:43],s65 offen offset:640 sc1
ds_read_b128 v[88:91],v11 offset:7680
ds_read_b128 v[128:131],v114 offset:64
v_add_f32 v122,v122,v204
v_fmac_f32 v126,v204,v204
v_add_f32 v122,v122,v205
v_fmac_f32 v126,v205,v205
v_smfmac_f32_16x16x64_bf16 v[232:235],v[92:95],v[132:139],v13
s_waitcnt lgkmcnt(5)
ds_read_b128 v[136:139],v118 offset:64
ds_read_b128 v[132:135],v114 offset:3552
v_add_f32 v123,v123,v206
v_fmac_f32 v127,v206,v206
v_add_f32 v123,v123,v207
v_fmac_f32 v127,v207,v207
v_mfma_f32_16x16x32_bf16 v[212:215],v[96:99],v[152:155],0
ds_read_b128 v[92:95],v12 offset:6912
v_add_f32 v120,v120,v208
v_fmac_f32 v124,v208,v208
v_add_f32 v120,v120,v209
v_fmac_f32 v124,v209,v209
v_mfma_f32_16x16x32_bf16 v[236:239],v[96:99],v[156:159],0
s_waitcnt lgkmcnt(6)
s_setprio 2
ds_read_b128 v[156:159],v119 offset:3552
v_add_f32 v121,v121,v210
v_fmac_f32 v125,v210,v210
v_add_f32 v121,v121,v211
v_fmac_f32 v125,v211,v211
v_smfmac_f32_16x16x64_bf16 v[212:215],v[100:103],v[144:151],v13
ds_read_b128 v[96:99],v11 offset:8832
ds_read_b128 v[144:147],v115 offset:64
v_add_f32 v122,v122,v232
v_fmac_f32 v126,v232,v232
v_add_f32 v122,v122,v233
v_fmac_f32 v126,v233,v233
v_smfmac_f32_16x16x64_bf16 v[236:239],v[100:103],v[148:155],v13
s_waitcnt lgkmcnt(5)
ds_read_b128 v[152:155],v119 offset:64
ds_read_b128 v[148:151],v115 offset:3552
v_add_f32 v123,v123,v234
v_fmac_f32 v127,v234,v234
v_add_f32 v123,v123,v235
v_fmac_f32 v127,v235,v235
v_mfma_f32_16x16x32_bf16 v[216:219],v[88:91],v[136:139],0
ds_read_b128 v[100:103],v12 offset:8064
v_add_f32 v120,v120,v212
v_fmac_f32 v124,v212,v212
v_add_f32 v120,v120,v213
v_fmac_f32 v124,v213,v213
v_add_f32 v121,v121,v214
v_mfma_f32_16x16x32_bf16 v[240:243],v[88:91],v[140:143],0
s_waitcnt lgkmcnt(6)
ds_read_b128 v[140:143],v118 offset:3616
v_fmac_f32 v125,v214,v214
v_add_f32 v121,v121,v215
v_fmac_f32 v125,v215,v215
v_add_f32 v122,v122,v236
v_smfmac_f32_16x16x64_bf16 v[216:219],v[92:95],v[128:135],v13
ds_read_b128 v[88:91],v11 offset:12288
ds_read_b128 v[128:131],v114 offset:128
v_fmac_f32 v126,v236,v236
v_add_f32 v122,v122,v237
v_fmac_f32 v126,v237,v237
v_add_f32 v123,v123,v238
v_fmac_f32 v127,v238,v238
v_smfmac_f32_16x16x64_bf16 v[240:243],v[92:95],v[132:139],v13
s_waitcnt lgkmcnt(5)
ds_read_b128 v[136:139],v118 offset:128
ds_read_b128 v[132:135],v114 offset:3616
v_add_f32 v123,v123,v239
v_fmac_f32 v127,v239,v239
v_add_u32 v112,v112,v8
v_add_u32 v116,v116,v8
v_mfma_f32_16x16x32_bf16 v[220:223],v[96:99],v[152:155],0
ds_read_b128 v[92:95],v12 offset:11520
ds_read_b128 v[104:107],v14 offset:0
ds_read_b128 v[108:111],v7 offset:3584
v_add_f32 v120,v120,v216
v_fmac_f32 v124,v216,v216
v_add_f32 v120,v120,v217
v_fmac_f32 v124,v217,v217
v_mfma_f32_16x16x32_bf16 v[244:247],v[96:99],v[156:159],0
s_waitcnt lgkmcnt(8)
ds_read_b128 v[156:159],v119 offset:3616
v_add_f32 v121,v121,v218
v_fmac_f32 v125,v218,v218
v_add_f32 v121,v121,v219
v_fmac_f32 v125,v219,v219
v_smfmac_f32_16x16x64_bf16 v[220:223],v[100:103],v[144:151],v13
ds_read_b128 v[96:99],v11 offset:13440
ds_read_b128 v[144:147],v115 offset:128
v_add_f32 v122,v122,v240
v_fmac_f32 v126,v240,v240
v_add_f32 v122,v122,v241
v_fmac_f32 v126,v241,v241
v_smfmac_f32_16x16x64_bf16 v[244:247],v[100:103],v[148:155],v13
s_waitcnt lgkmcnt(7)
ds_read_b128 v[152:155],v119 offset:128
ds_read_b128 v[148:151],v115 offset:3616
v_add_f32 v123,v123,v242
v_fmac_f32 v127,v242,v242
v_add_f32 v123,v123,v243
v_fmac_f32 v127,v243,v243
v_mfma_f32_16x16x32_bf16 v[224:227],v[88:91],v[136:139],0
ds_read_b128 v[100:103],v12 offset:12672
v_add_f32 v120,v120,v220
v_fmac_f32 v124,v220,v220
v_add_f32 v120,v120,v221
v_fmac_f32 v124,v221,v221
v_mfma_f32_16x16x32_bf16 v[248:251],v[88:91],v[140:143],0
s_waitcnt lgkmcnt(8)
s_setprio 1
ds_read_b128 v[140:143],v116 offset:192
v_add_f32 v121,v121,v222
v_fmac_f32 v125,v222,v222
v_add_f32 v121,v121,v223
v_fmac_f32 v125,v223,v223
v_smfmac_f32_16x16x64_bf16 v[224:227],v[92:95],v[128:135],v13
s_nop 0
ds_read_b128 v[88:91],v112 offset:3616
ds_read_b128 v[128:131],v112 offset:3488
v_add_f32 v122,v122,v244
v_fmac_f32 v126,v244,v244
v_add_f32 v122,v122,v245
v_fmac_f32 v126,v245,v245
v_smfmac_f32_16x16x64_bf16 v[248:251],v[92:95],v[132:139],v13
s_waitcnt lgkmcnt(5)
ds_read_b128 v[136:139],v116 offset:0
ds_read_b128 v[132:135],v112 offset:3680
v_add_f32 v123,v123,v246
v_fmac_f32 v127,v246,v246
v_add_f32 v123,v123,v247
v_fmac_f32 v127,v247,v247
v_mfma_f32_16x16x32_bf16 v[228:231],v[96:99],v[152:155],0
ds_read_b128 v[92:95],v112 offset:3808
v_add_f32 v120,v120,v224
v_fmac_f32 v124,v224,v224
v_add_f32 v120,v120,v225
v_fmac_f32 v124,v225,v225
v_mfma_f32_16x16x32_bf16 v[252:255],v[96:99],v[156:159],0
s_waitcnt lgkmcnt(6)
s_cbranch_vccnz RA27_%=
s_mov_b64 exec,s[62:63]
s_add_u32 m0,s57,34880
s_add_u32 s64,s56,4915200
buffer_load_dwordx4 v2,s[36:39],s64 offen lds
s_add_u32 m0,s57,36608
s_nop 0
buffer_load_dwordx4 v3,s[36:39],s64 offen lds
s_mov_b64 exec,-1
RA27_%=:
ds_read_b128 v[156:159],v116 offset:256
v_add_f32 v121,v121,v226
v_fmac_f32 v125,v226,v226
v_add_f32 v121,v121,v227
v_fmac_f32 v125,v227,v227
v_smfmac_f32_16x16x64_bf16 v[228:231],v[100:103],v[144:151],v13
s_setprio 0
ds_read_b128 v[96:99],v116 offset:128
ds_read_b128 v[144:147],v112 offset:3552
v_add_f32 v122,v122,v248
v_fmac_f32 v126,v248,v248
v_add_f32 v122,v122,v249
v_fmac_f32 v126,v249,v249
v_smfmac_f32_16x16x64_bf16 v[252:255],v[100:103],v[148:155],v13
s_nop 0
ds_read_b128 v[152:155],v116 offset:64
ds_read_b128 v[148:151],v112 offset:3744
v_add_f32 v123,v123,v250
v_fmac_f32 v127,v250,v250
v_add_f32 v123,v123,v251
v_fmac_f32 v127,v251,v251
ds_read_b128 v[100:103],v116 offset:320
v_add_f32 v120,v120,v228
v_fmac_f32 v124,v228,v228
v_add_f32 v120,v120,v229
v_fmac_f32 v124,v229,v229
v_add_f32 v121,v121,v230
v_fmac_f32 v125,v230,v230
v_add_f32 v121,v121,v231
v_fmac_f32 v125,v231,v231
s_waitcnt lgkmcnt(0)
s_barrier
s_setprio 0
v_add_f32 v122,v122,v252
s_cbranch_vccnz RA28_%=
s_mov_b64 exec,s[62:63]
s_add_u32 m0,s57,38368
s_add_u32 s64,s56,5013504
buffer_load_dwordx4 v2,s[36:39],s64 offen lds
s_add_u32 m0,s57,40096
s_nop 0
buffer_load_dwordx4 v3,s[36:39],s64 offen lds
s_mov_b64 exec,-1
RA28_%=:
v_fmac_f32 v126,v252,v252
v_add_f32 v122,v122,v253
s_cbranch_vccnz RA29_%=
s_mov_b64 exec,s[62:63]
s_add_u32 m0,s57,41856
s_add_u32 s64,s56,5111808
buffer_load_dwordx4 v2,s[36:39],s64 offen lds
s_add_u32 m0,s57,43584
s_nop 0
buffer_load_dwordx4 v3,s[36:39],s64 offen lds
s_mov_b64 exec,-1
RA29_%=:
v_fmac_f32 v126,v253,v253
v_add_f32 v123,v123,v254
v_fmac_f32 v127,v254,v254
v_add_f32 v123,v123,v255
v_fmac_f32 v127,v255,v255
s_setprio 3
ds_read_b128 v[56:59],v14 offset:256
ds_read_b128 v[60:63],v7 offset:3616
s_setprio 0
v_permlane32_swap_b32 v120,v122
v_permlane32_swap_b32 v121,v123
v_permlane32_swap_b32 v124,v126
v_permlane32_swap_b32 v125,v127
v_add_f32 v120,v120,v122
v_add_f32 v121,v121,v123
v_add_f32 v124,v124,v126
v_add_f32 v125,v125,v127
v_permlane16_swap_b32 v120,v121
s_nop 0
v_permlane16_swap_b32 v124,v125
v_add_f32 v113,v120,v121
v_add_f32 v114,v124,v125
v_mul_f32 v113,0x3c2aaaab,v113
v_mul_f32 v114,0x3c2aaaab,v114
v_mul_f32 v115,0x5e000000,v114
v_fma_f32 v114,-v113,v113,v114
v_pk_maximum3_f16 v1,v1,v115,v115 op_sel:[0,1,1] op_sel_hi:[1,1,1]
v_max_f32 v114,0,v114
v_add_f32 v114,0x3727c5ac,v114
v_rsq_f32 v114,v114
s_nop 0
v_mul_f32 v120,0x8befe000,v114
v_mul_f32 v121,0x8befe000,v114
v_mul_f32 v124,-v113,v120
v_mul_f32 v125,-v113,v120
v_permlane16_swap_b32 v120,v121
s_nop 0
v_permlane16_swap_b32 v124,v125
v_mov_b32 v122,v120
v_mov_b32 v123,v121
s_cbranch_vccnz RA30_%=
s_mov_b64 exec,s[62:63]
s_add_u32 m0,s57,45344
s_add_u32 s64,s56,5210112
buffer_load_dwordx4 v2,s[36:39],s64 offen lds
s_add_u32 m0,s57,47072
s_nop 0
buffer_load_dwordx4 v3,s[36:39],s64 offen lds
s_mov_b64 exec,-1
RA30_%=:
v_mov_b32 v126,v124
v_mov_b32 v127,v125
v_permlane32_swap_b32 v120,v122
v_permlane32_swap_b32 v124,v126
v_permlane32_swap_b32 v121,v123
v_permlane32_swap_b32 v125,v127
v_add_u32 v8,94432,v4
v_add_u32 v9,94432,v5
v_fma_f32 v160,v160,v120,v124
s_setprio 3
ds_read_b64_tr_b16 v[64:65],v4 offset:512
s_setprio 0
v_fma_f32 v161,v161,v120,v124
s_setprio 3
ds_read_b64_tr_b16 v[66:67],v4 offset:2560
s_setprio 0
v_fma_f32 v162,v162,v121,v125
s_setprio 3
ds_read_b128 v[112:115],v7 offset:896
s_setprio 0
v_fma_f32 v163,v163,v121,v125
s_setprio 3
ds_read_b64_tr_b16 v[68:69],v5 offset:512
s_setprio 0
v_fma_f32 v164,v164,v120,v124
s_setprio 3
ds_read_b64_tr_b16 v[70:71],v5 offset:2560
s_setprio 0
v_fma_f32 v165,v165,v120,v124
s_setprio 3
ds_read_b128 v[116:119],v7 offset:928
s_setprio 0
v_fma_f32 v166,v166,v121,v125
s_setprio 3
ds_read_b64_tr_b16 v[72:73],v4 offset:4608
s_setprio 0
v_fma_f32 v167,v167,v121,v125
s_setprio 3
ds_read_b64_tr_b16 v[74:75],v4 offset:6656
s_setprio 0
v_fma_f32 v208,v208,v120,v124
s_setprio 3
ds_read_b64_tr_b16 v[76:77],v5 offset:4608
s_setprio 0
v_fma_f32 v209,v209,v120,v124
s_setprio 3
ds_read_b64_tr_b16 v[78:79],v5 offset:6656
s_setprio 0
v_fma_f32 v210,v210,v121,v125
s_setprio 3
ds_read_b64_tr_b16 v[80:81],v4 offset:8704
s_setprio 0
v_fma_f32 v211,v211,v121,v125
s_setprio 3
ds_read_b64_tr_b16 v[82:83],v4 offset:10752
s_setprio 0
v_fma_f32 v212,v212,v120,v124
s_setprio 3
ds_read_b64_tr_b16 v[84:85],v5 offset:8704
s_setprio 0
v_fma_f32 v213,v213,v120,v124
s_setprio 3
ds_read_b64_tr_b16 v[86:87],v5 offset:10752
s_setprio 0
v_fma_f32 v214,v214,v121,v125
v_fma_f32 v215,v215,v121,v125
v_cvt_pk_bf16_f32 v16,v160,v161
v_cvt_pk_bf16_f32 v20,v162,v163
v_cvt_pk_bf16_f32 v17,v164,v165
v_cvt_pk_bf16_f32 v21,v166,v167
v_cvt_pk_bf16_f32 v18,v208,v209
v_cvt_pk_bf16_f32 v22,v210,v211
v_cvt_pk_bf16_f32 v19,v212,v213
v_cvt_pk_bf16_f32 v23,v214,v215
v_fma_f32 v168,v168,v120,v124
v_fma_f32 v169,v169,v120,v124
v_fma_f32 v170,v170,v121,v125
v_fma_f32 v171,v171,v121,v125
v_mfma_f32_16x16x32_bf16 v[160:163],v[104:107],v[128:131],v[108:111]
s_cbranch_vccnz RA31_%=
s_mov_b64 exec,s[62:63]
s_add_u32 m0,s57,48832
s_add_u32 s64,s56,5308416
buffer_load_dwordx4 v2,s[36:39],s64 offen lds
s_add_u32 m0,s57,50560
s_nop 0
buffer_load_dwordx4 v3,s[36:39],s64 offen lds
s_mov_b64 exec,-1
RA31_%=:
v_fma_f32 v172,v172,v120,v124
v_fma_f32 v173,v173,v120,v124
v_fma_f32 v174,v174,v121,v125
v_fma_f32 v175,v175,v121,v125
v_fma_f32 v216,v216,v120,v124
v_mfma_f32_16x16x32_bf16 v[164:167],v[104:107],v[132:135],v[108:111]
v_fma_f32 v217,v217,v120,v124
v_fma_f32 v218,v218,v121,v125
v_fma_f32 v219,v219,v121,v125
v_fma_f32 v220,v220,v120,v124
v_fma_f32 v221,v221,v120,v124
v_mfma_f32_16x16x32_bf16 v[208:211],v[104:107],v[136:139],v[108:111]
v_fma_f32 v222,v222,v121,v125
v_fma_f32 v223,v223,v121,v125
v_cvt_pk_bf16_f32 v24,v168,v169
v_cvt_pk_bf16_f32 v28,v170,v171
v_mfma_f32_16x16x32_bf16 v[212:215],v[104:107],v[140:143],v[108:111]
v_cvt_pk_bf16_f32 v25,v172,v173
v_cvt_pk_bf16_f32 v29,v174,v175
v_cvt_pk_bf16_f32 v26,v216,v217
v_cvt_pk_bf16_f32 v30,v218,v219
v_cvt_pk_bf16_f32 v27,v220,v221
v_cvt_pk_bf16_f32 v31,v222,v223
v_fma_f32 v176,v176,v120,v124
v_fma_f32 v177,v177,v120,v124
v_fma_f32 v178,v178,v121,v125
v_fma_f32 v179,v179,v121,v125
s_waitcnt lgkmcnt(14)
s_setprio 3
v_mfma_f32_16x16x32_bf16 v[168:171],v[56:59],v[128:131],v[60:63]
ds_read_b128 v[104:107],v14 offset:512
ds_read_b128 v[108:111],v7 offset:3712
s_setprio 0
v_fma_f32 v180,v180,v120,v124
v_fma_f32 v181,v181,v120,v124
v_fma_f32 v182,v182,v121,v125
v_fma_f32 v183,v183,v121,v125
v_fma_f32 v224,v224,v120,v124
v_mfma_f32_16x16x32_bf16 v[172:175],v[56:59],v[132:135],v[60:63]
v_fma_f32 v225,v225,v120,v124
v_fma_f32 v226,v226,v121,v125
v_fma_f32 v227,v227,v121,v125
v_fma_f32 v228,v228,v120,v124
v_fma_f32 v229,v229,v120,v124
v_mfma_f32_16x16x32_bf16 v[216:219],v[56:59],v[136:139],v[60:63]
s_cbranch_vccnz RA32_%=
s_mov_b64 exec,s[62:63]
s_add_u32 m0,s57,52320
s_add_u32 s64,s56,5406720
buffer_load_dwordx4 v2,s[36:39],s64 offen lds
s_add_u32 m0,s57,54048
s_nop 0
buffer_load_dwordx4 v3,s[36:39],s64 offen lds
s_mov_b64 exec,-1
RA32_%=:
v_fma_f32 v230,v230,v121,v125
v_fma_f32 v231,v231,v121,v125
v_cvt_pk_bf16_f32 v32,v176,v177
v_cvt_pk_bf16_f32 v36,v178,v179
s_setprio 3
v_mfma_f32_16x16x32_bf16 v[220:223],v[56:59],v[140:143],v[60:63]
ds_read_b128 v[56:59],v14 offset:768
ds_read_b128 v[60:63],v7 offset:3744
s_setprio 0
v_cvt_pk_bf16_f32 v33,v180,v181
v_cvt_pk_bf16_f32 v37,v182,v183
v_cvt_pk_bf16_f32 v34,v224,v225
v_cvt_pk_bf16_f32 v38,v226,v227
v_cvt_pk_bf16_f32 v35,v228,v229
v_cvt_pk_bf16_f32 v39,v230,v231
v_fma_f32 v184,v184,v122,v126
v_fma_f32 v185,v185,v122,v126
s_waitcnt lgkmcnt(2)
v_mfma_f32_16x16x32_bf16 v[176:179],v[104:107],v[144:147],v[108:111]
v_fma_f32 v186,v186,v123,v127
v_fma_f32 v187,v187,v123,v127
v_fma_f32 v188,v188,v122,v126
v_mfma_f32_16x16x32_bf16 v[128:131],v[64:67],v[16:19],v[112:115]
v_fma_f32 v189,v189,v122,v126
v_fma_f32 v190,v190,v123,v127
v_fma_f32 v191,v191,v123,v127
v_mfma_f32_16x16x32_bf16 v[180:183],v[104:107],v[148:151],v[108:111]
v_fma_f32 v232,v232,v122,v126
v_fma_f32 v233,v233,v122,v126
v_mfma_f32_16x16x32_bf16 v[136:139],v[64:67],v[20:23],v[112:115]
v_fma_f32 v234,v234,v123,v127
v_fma_f32 v235,v235,v123,v127
v_fma_f32 v236,v236,v122,v126
v_mfma_f32_16x16x32_bf16 v[224:227],v[104:107],v[152:155],v[108:111]
s_nop 0
v_fma_f32 v237,v237,v122,v126
v_fma_f32 v238,v238,v123,v127
v_mfma_f32_16x16x32_bf16 v[140:143],v[68:71],v[20:23],v[116:119]
s_cbranch_vccnz RA33_%=
s_mov_b64 exec,s[62:63]
s_add_u32 m0,s57,55808
s_add_u32 s64,s56,5505024
buffer_load_dwordx4 v2,s[36:39],s64 offen lds
s_add_u32 m0,s57,57536
s_nop 0
buffer_load_dwordx4 v3,s[36:39],s64 offen lds
s_mov_b64 exec,-1
RA33_%=:
s_mov_b32 s86,0xbe801d5a
v_fma_f32 v239,v239,v123,v127
v_cvt_pk_bf16_f32 v40,v184,v185
v_cvt_pk_bf16_f32 v44,v186,v187
s_setprio 3
v_mfma_f32_16x16x32_bf16 v[228:231],v[104:107],v[156:159],v[108:111]
ds_read_b128 v[104:107],v14 offset:1024
s_setprio 0
v_cvt_pk_bf16_f32 v41,v188,v189
v_cvt_pk_bf16_f32 v45,v190,v191
s_setprio 3
v_mfma_f32_16x16x32_bf16 v[132:135],v[68:71],v[16:19],v[116:119]
s_nop 0
ds_read_b128 v[108:111],v7 offset:3840
s_setprio 0
v_cvt_pk_bf16_f32 v42,v232,v233
v_cvt_pk_bf16_f32 v46,v234,v235
v_cvt_pk_bf16_f32 v43,v236,v237
v_cvt_pk_bf16_f32 v47,v238,v239
s_setprio 3
v_fma_f32 v192,v192,v122,v126
v_fma_f32 v193,v193,v122,v126
s_waitcnt lgkmcnt(2)
v_mfma_f32_16x16x32_bf16 v[184:187],v[56:59],v[144:147],v[60:63]
v_fma_f32 v194,v194,v123,v127
v_fma_f32 v195,v195,v123,v127
v_mfma_f32_16x16x32_bf16 v[128:131],v[72:75],v[24:27],v[128:131]
s_nop 0
v_fma_f32 v196,v196,v122,v126
v_fma_f32 v197,v197,v122,v126
v_mfma_f32_16x16x32_bf16 v[188:191],v[56:59],v[148:151],v[60:63]
v_fma_f32 v198,v198,v123,v127
v_fma_f32 v199,v199,v123,v127
v_mfma_f32_16x16x32_bf16 v[136:139],v[72:75],v[28:31],v[136:139]
s_nop 0
v_fma_f32 v240,v240,v122,v126
v_fma_f32 v241,v241,v122,v126
v_mfma_f32_16x16x32_bf16 v[232:235],v[56:59],v[152:155],v[60:63]
v_fma_f32 v242,v242,v123,v127
v_fma_f32 v243,v243,v123,v127
v_mfma_f32_16x16x32_bf16 v[140:143],v[76:79],v[28:31],v[140:143]
s_nop 0
v_fma_f32 v244,v244,v122,v126
v_fma_f32 v245,v245,v122,v126
v_mfma_f32_16x16x32_bf16 v[236:239],v[56:59],v[156:159],v[60:63]
s_cbranch_vccnz RA34_%=
s_mov_b64 exec,s[62:63]
s_add_u32 m0,s57,59296
s_add_u32 s64,s56,5603328
buffer_load_dwordx4 v2,s[36:39],s64 offen lds
s_add_u32 m0,s57,61024
s_nop 0
buffer_load_dwordx4 v3,s[36:39],s64 offen lds
s_mov_b64 exec,-1
RA34_%=:
ds_read_b128 v[144:147],v14 offset:1280
ds_read_b128 v[148:151],v7 offset:3872
v_fma_f32 v246,v246,v123,v127
v_mfma_f32_16x16x32_bf16 v[132:135],v[76:79],v[24:27],v[132:135]
v_fma_f32 v247,v247,v123,v127
v_cvt_pk_bf16_f32 v48,v192,v193
v_cvt_pk_bf16_f32 v52,v194,v195
v_cvt_pk_bf16_f32 v49,v196,v197
v_cvt_pk_bf16_f32 v53,v198,v199
v_cvt_pk_bf16_f32 v50,v240,v241
v_cvt_pk_bf16_f32 v54,v242,v243
v_cvt_pk_bf16_f32 v51,v244,v245
v_cvt_pk_bf16_f32 v55,v246,v247
v_fma_f32 v200,v200,v122,v126
v_fma_f32 v201,v201,v122,v126
s_waitcnt lgkmcnt(2)
v_mfma_f32_16x16x32_bf16 v[192:195],v[104:107],v[88:91],v[108:111]
s_nop 0
v_fma_f32 v202,v202,v123,v127
v_fma_f32 v203,v203,v123,v127
v_fma_f32 v204,v204,v122,v126
v_mfma_f32_16x16x32_bf16 v[128:131],v[80:83],v[32:35],v[128:131]
v_fma_f32 v205,v205,v122,v126
v_fma_f32 v206,v206,v123,v127
v_fma_f32 v207,v207,v123,v127
v_mfma_f32_16x16x32_bf16 v[196:199],v[104:107],v[92:95],v[108:111]
s_nop 0
v_fma_f32 v248,v248,v122,v126
v_fma_f32 v249,v249,v122,v126
v_mfma_f32_16x16x32_bf16 v[136:139],v[80:83],v[36:39],v[136:139]
v_fma_f32 v250,v250,v123,v127
v_fma_f32 v251,v251,v123,v127
v_fma_f32 v252,v252,v122,v126
v_mfma_f32_16x16x32_bf16 v[240:243],v[104:107],v[96:99],v[108:111]
s_nop 0
v_fma_f32 v253,v253,v122,v126
v_fma_f32 v254,v254,v123,v127
v_mfma_f32_16x16x32_bf16 v[140:143],v[84:87],v[36:39],v[140:143]
s_cbranch_vccnz RA35_%=
s_mov_b64 exec,s[62:63]
s_add_u32 m0,s57,62784
s_add_u32 s64,s56,5701632
buffer_load_dwordx4 v2,s[36:39],s64 offen lds
s_add_u32 m0,s57,64512
s_nop 0
buffer_load_dwordx4 v3,s[36:39],s64 offen lds
s_mov_b64 exec,-1
RA35_%=:
v_fma_f32 v255,v255,v123,v127
v_cvt_pk_bf16_f32 v56,v200,v201
v_cvt_pk_bf16_f32 v60,v202,v203
v_mfma_f32_16x16x32_bf16 v[244:247],v[104:107],v[100:103],v[108:111]
v_cvt_pk_bf16_f32 v57,v204,v205
v_cvt_pk_bf16_f32 v61,v206,v207
v_mfma_f32_16x16x32_bf16 v[132:135],v[84:87],v[32:35],v[132:135]
s_nop 0
v_cvt_pk_bf16_f32 v58,v248,v249
v_cvt_pk_bf16_f32 v62,v250,v251
v_cvt_pk_bf16_f32 v59,v252,v253
v_cvt_pk_bf16_f32 v63,v254,v255
s_waitcnt lgkmcnt(0)
s_setprio 0
v_mfma_f32_16x16x32_bf16 v[200:203],v[144:147],v[88:91],v[148:151]
s_barrier
s_setprio 3
ds_read_b64_tr_b16 v[104:105],v6 offset:12448
v_lshlrev_b32_sdwa v128,1,v128 dst_sel:DWORD dst_unused:UNUSED_PAD src0_sel:DWORD src1_sel:WORD_1
v_lshlrev_b32_sdwa v130,1,v130 dst_sel:DWORD dst_unused:UNUSED_PAD src0_sel:DWORD src1_sel:WORD_1
v_lshlrev_b32_sdwa v129,1,v129 dst_sel:DWORD dst_unused:UNUSED_PAD src0_sel:DWORD src1_sel:WORD_1
v_mfma_f32_16x16x32_bf16 v[204:207],v[144:147],v[92:95],v[148:151]
s_cbranch_vccz RA36_%=
s_add_u32 m0,s58,0
s_mul_i32 s69,s59,9
s_add_u32 s69,s69,s97
buffer_load_dwordx4 v2,s[44:47],s69 offen lds
s_add_u32 m0,m0,1024
s_add_u32 s69,s69,s53
buffer_load_dwordx4 v3,s[44:47],s69 offen lds
s_add_u32 m0,m0,1024
s_add_u32 s69,s69,s53
buffer_load_dwordx4 v2,s[44:47],s69 offen lds
s_add_u32 m0,m0,s50
s_add_u32 s69,s69,s53
buffer_load_dwordx4 v3,s[44:47],s69 offen lds
RA36_%=:
ds_read_u16 v128,v128
ds_read_b64_tr_b16 v[106:107],v6 offset:13984
v_lshlrev_b32_sdwa v131,1,v131 dst_sel:DWORD dst_unused:UNUSED_PAD src0_sel:DWORD src1_sel:WORD_1
v_fma_mixlo_f16 v120,v140,s99,0
v_fma_mixlo_f16 v122,v141,s99,0
v_mfma_f32_16x16x32_bf16 v[248:251],v[144:147],v[96:99],v[148:151]
ds_read_u16_d16_hi v130,v130
ds_read_b64_tr_b16 v[108:109],v6 offset:12456
v_lshlrev_b32_sdwa v132,1,v132 dst_sel:DWORD dst_unused:UNUSED_PAD src0_sel:DWORD src1_sel:WORD_1
v_lshlrev_b32_sdwa v134,1,v134 dst_sel:DWORD dst_unused:UNUSED_PAD src0_sel:DWORD src1_sel:WORD_1
v_mfma_f32_16x16x32_bf16 v[252:255],v[144:147],v[100:103],v[148:151]
ds_read_u16 v129,v129
ds_read_b64_tr_b16 v[110:111],v6 offset:13992
v_fma_mixhi_f16 v120,v142,s99,0
v_fma_mixhi_f16 v122,v143,s99,0
v_mfma_f32_16x16x32_bf16 v[144:147],v[64:67],v[40:43],v[112:115]
ds_read_u16_d16_hi v131,v131
ds_read_b64_tr_b16 v[88:89],v6 offset:12320
v_lshlrev_b32_sdwa v133,1,v133 dst_sel:DWORD dst_unused:UNUSED_PAD src0_sel:DWORD src1_sel:WORD_1
v_pk_fma_f16 v121,v120,v120,s54
v_lshlrev_b32_sdwa v135,1,v135 dst_sel:DWORD dst_unused:UNUSED_PAD src0_sel:DWORD src1_sel:WORD_1
v_mfma_f32_16x16x32_bf16 v[152:155],v[64:67],v[44:47],v[112:115]
s_cbranch_vccz RA37_%=
s_add_u32 m0,m0,1024
s_add_u32 s69,s69,s53
buffer_load_dwordx4 v2,s[44:47],s69 offen lds
s_add_u32 m0,m0,1024
s_add_u32 s69,s69,s53
buffer_load_dwordx4 v3,s[44:47],s69 offen lds
s_add_u32 m0,m0,s50
s_add_u32 s69,s69,s53
buffer_load_dwordx4 v2,s[44:47],s69 offen lds
s_add_u32 m0,m0,1024
s_add_u32 s69,s69,s53
buffer_load_dwordx4 v3,s[44:47],s69 offen lds
RA37_%=:
ds_read_u16 v132,v132
ds_read_b64_tr_b16 v[90:91],v6 offset:13856
v_pk_fma_f16 v123,v122,v122,s54
v_pk_mul_f16 v123,v122,v123
v_pk_mul_f16 v121,v120,v121
v_mfma_f32_16x16x32_bf16 v[156:159],v[68:71],v[44:47],v[116:119]
s_cbranch_vccnz RA38_%=
s_mov_b64 exec,s[62:63]
s_add_u32 m0,s57,66272
s_add_u32 s64,s56,5799936
buffer_load_dwordx4 v2,s[36:39],s64 offen lds
s_add_u32 m0,s57,68000
s_nop 0
buffer_load_dwordx4 v3,s[36:39],s64 offen lds
s_mov_b64 exec,-1
RA38_%=:
ds_read_u16_d16_hi v134,v134
ds_read_b64_tr_b16 v[92:93],v6 offset:12328
v_lshlrev_b32_sdwa v136,1,v136 dst_sel:DWORD dst_unused:UNUSED_PAD src0_sel:DWORD src1_sel:WORD_1
v_lshlrev_b32_sdwa v138,1,v138 dst_sel:DWORD dst_unused:UNUSED_PAD src0_sel:DWORD src1_sel:WORD_1
v_mfma_f32_16x16x32_bf16 v[148:151],v[68:71],v[40:43],v[116:119]
ds_read_u16 v133,v133
ds_read_b64_tr_b16 v[94:95],v6 offset:13864
v_exp_f16 v120,v121
v_lshlrev_b32_sdwa v137,1,v137 dst_sel:DWORD dst_unused:UNUSED_PAD src0_sel:DWORD src1_sel:WORD_1
v_mfma_f32_16x16x32_bf16 v[144:147],v[72:75],v[48:51],v[144:147]
s_cbranch_vccz RA39_%=
s_add_u32 m0,m0,1024
s_add_u32 s69,s69,s53
buffer_load_dwordx4 v2,s[44:47],s69 offen lds
s_add_u32 m0,m0,s50
s_add_u32 s69,s69,s53
buffer_load_dwordx4 v3,s[44:47],s69 offen lds
s_add_u32 m0,m0,1024
s_add_u32 s69,s69,s53
buffer_load_dwordx4 v2,s[44:47],s69 offen lds
s_add_u32 m0,m0,1024
s_add_u32 s69,s69,s53
buffer_load_dwordx4 v3,s[44:47],s69 offen lds
RA39_%=:
ds_read_u16_d16_hi v135,v135
ds_read_b64_tr_b16 v[96:97],v6 offset:12384
v_lshlrev_b32_sdwa v139,1,v139 dst_sel:DWORD dst_unused:UNUSED_PAD src0_sel:DWORD src1_sel:WORD_1
v_exp_f16 v122,v123
v_mfma_f32_16x16x32_bf16 v[152:155],v[72:75],v[52:55],v[152:155]
ds_read_u16 v136,v136
ds_read_b64_tr_b16 v[98:99],v6 offset:13920
v_exp_f16_sdwa v120,v121 dst_sel:WORD_1 dst_unused:UNUSED_PRESERVE src0_sel:WORD_1
v_exp_f16_sdwa v122,v123 dst_sel:WORD_1 dst_unused:UNUSED_PRESERVE src0_sel:WORD_1
v_mfma_f32_16x16x32_bf16 v[156:159],v[76:79],v[52:55],v[156:159]
ds_read_u16_d16_hi v138,v138
ds_read_b64_tr_b16 v[100:101],v6 offset:12392
v_pk_fma_f16 v120,v120,s55,s55
v_pk_fma_f16 v122,v122,s55,s55
v_mfma_f32_16x16x32_bf16 v[148:151],v[76:79],v[48:51],v[148:151]
ds_read_u16 v137,v137
ds_read_b64_tr_b16 v[102:103],v6 offset:13928
v_rcp_f16 v121,v120
v_rcp_f16 v123,v122
v_mfma_f32_16x16x32_bf16 v[144:147],v[80:83],v[56:59],v[144:147]
ds_read_u16_d16_hi v139,v139
ds_read_b64_tr_b16 v[64:65],v4 offset:12320
v_rcp_f16_sdwa v121,v120 dst_sel:WORD_1 dst_unused:UNUSED_PRESERVE src0_sel:WORD_1
v_mfma_f32_16x16x32_bf16 v[152:155],v[80:83],v[60:63],v[152:155]
ds_read_b64_tr_b16 v[66:67],v4 offset:14368
v_rcp_f16_sdwa v123,v122 dst_sel:WORD_1 dst_unused:UNUSED_PRESERVE src0_sel:WORD_1
v_fma_mix_f32 v142,v142,v121,0 op_sel:[0,1,0] op_sel_hi:[0,1,0]
v_mfma_f32_16x16x32_bf16 v[156:159],v[84:87],v[60:63],v[156:159]
ds_read_b128 v[112:115],v7 offset:1024
v_fma_mix_f32 v140,v140,v121,0 op_sel_hi:[0,1,0]
s_waitcnt lgkmcnt(15)
v_lshl_add_u64 v[128:129],v[128:129],0,v[130:131]
s_waitcnt lgkmcnt(11)
v_lshl_add_u64 v[130:131],v[132:133],0,v[134:135]
v_mfma_f32_16x16x32_bf16 v[148:151],v[84:87],v[56:59],v[148:151]
s_cbranch_vccnz RA40_%=
s_mov_b64 exec,s[62:63]
s_add_u32 m0,s57,0
s_add_u32 s64,s56,5898240
buffer_load_dwordx4 v2,s[36:39],s64 offen lds
s_add_u32 m0,s57,1728
s_nop 0
buffer_load_dwordx4 v3,s[36:39],s64 offen lds
s_mov_b64 exec,-1
RA40_%=:
ds_read_b64_tr_b16 v[68:69],v5 offset:12320
v_fma_mix_f32 v141,v141,v123,0 op_sel_hi:[0,1,0]
v_fma_mix_f32 v143,v143,v123,0 op_sel:[0,1,0] op_sel_hi:[0,1,0]
v_lshlrev_b32_sdwa v144,1,v144 dst_sel:DWORD dst_unused:UNUSED_PAD src0_sel:DWORD src1_sel:WORD_1
v_lshlrev_b32_sdwa v146,1,v146 dst_sel:DWORD dst_unused:UNUSED_PAD src0_sel:DWORD src1_sel:WORD_1
v_mfma_f32_16x16x32_bf16 v[160:163],v[88:91],v[128:131],v[160:163]
ds_read_u16 v144,v144
v_lshlrev_b32_sdwa v145,1,v145 dst_sel:DWORD dst_unused:UNUSED_PAD src0_sel:DWORD src1_sel:WORD_1
v_lshlrev_b32_sdwa v147,1,v147 dst_sel:DWORD dst_unused:UNUSED_PAD src0_sel:DWORD src1_sel:WORD_1
v_fma_mixlo_f16 v122,v157,s99,0
v_mfma_f32_16x16x32_bf16 v[168:171],v[92:95],v[128:131],v[168:171]
s_waitcnt lgkmcnt(10)
ds_read_b64_tr_b16 v[70:71],v5 offset:14368
ds_read_b128 v[116:119],v7 offset:1056
v_fma_mixlo_f16 v120,v156,s99,0
v_mfma_f32_16x16x32_bf16 v[176:179],v[96:99],v[128:131],v[176:179]
s_waitcnt lgkmcnt(8)
ds_read_u16_d16_hi v146,v146
v_lshlrev_b32_sdwa v148,1,v148 dst_sel:DWORD dst_unused:UNUSED_PAD src0_sel:DWORD src1_sel:WORD_1
v_lshlrev_b32_sdwa v150,1,v150 dst_sel:DWORD dst_unused:UNUSED_PAD src0_sel:DWORD src1_sel:WORD_1
v_mfma_f32_16x16x32_bf16 v[184:187],v[100:103],v[128:131],v[184:187]
s_waitcnt lgkmcnt(8)
ds_read_b64_tr_b16 v[72:73],v4 offset:16416
ds_read_b64_tr_b16 v[74:75],v4 offset:18464
v_lshl_add_u64 v[136:137],v[136:137],0,v[138:139]
v_fma_mixhi_f16 v120,v158,s99,0
v_mfma_f32_16x16x32_bf16 v[192:195],v[104:107],v[128:131],v[192:195]
ds_read_u16 v145,v145
v_cvt_pk_bf16_f32 v138,v140,v142
v_cvt_pk_bf16_f32 v139,v141,v143
v_mfma_f32_16x16x32_bf16 v[200:203],v[108:111],v[128:131],v[200:203]
v_fma_mixhi_f16 v122,v159,s99,0
v_mfma_f32_16x16x32_bf16 v[164:167],v[88:91],v[136:139],v[164:167]
ds_read_b64_tr_b16 v[76:77],v5 offset:16416
ds_read_b64_tr_b16 v[78:79],v5 offset:18464
v_lshlrev_b32_sdwa v149,1,v149 dst_sel:DWORD dst_unused:UNUSED_PAD src0_sel:DWORD src1_sel:WORD_1
v_lshlrev_b32_sdwa v151,1,v151 dst_sel:DWORD dst_unused:UNUSED_PAD src0_sel:DWORD src1_sel:WORD_1
v_mfma_f32_16x16x32_bf16 v[172:175],v[92:95],v[136:139],v[172:175]
ds_read_b64_tr_b16 v[80:81],v4 offset:20512
ds_read_b64_tr_b16 v[82:83],v4 offset:22560
v_pk_fma_f16 v121,v120,v120,s54
v_pk_fma_f16 v123,v122,v122,s54
v_mfma_f32_16x16x32_bf16 v[180:183],v[96:99],v[136:139],v[180:183]
ds_read_u16_d16_hi v147,v147
v_pk_mul_f16 v121,v120,v121
v_pk_mul_f16 v123,v122,v123
v_mfma_f32_16x16x32_bf16 v[188:191],v[100:103],v[136:139],v[188:191]
s_cbranch_vccnz RA41_%=
s_mov_b64 exec,s[62:63]
s_add_u32 m0,s57,3488
s_add_u32 s64,s56,5996544
buffer_load_dwordx4 v2,s[36:39],s64 offen lds
s_add_u32 m0,s57,5216
s_nop 0
buffer_load_dwordx4 v3,s[36:39],s64 offen lds
s_mov_b64 exec,-1
RA41_%=:
ds_read_u16 v148,v148
v_lshlrev_b32_sdwa v152,1,v152 dst_sel:DWORD dst_unused:UNUSED_PAD src0_sel:DWORD src1_sel:WORD_1
v_lshlrev_b32_sdwa v154,1,v154 dst_sel:DWORD dst_unused:UNUSED_PAD src0_sel:DWORD src1_sel:WORD_1
v_mfma_f32_16x16x32_bf16 v[196:199],v[104:107],v[136:139],v[196:199]
v_exp_f16 v120,v121
v_mfma_f32_16x16x32_bf16 v[204:207],v[108:111],v[136:139],v[204:207]
s_waitcnt lgkmcnt(14)
ds_read_b64_tr_b16 v[84:85],v5 offset:20512
ds_read_b64_tr_b16 v[86:87],v5 offset:22560
v_exp_f16 v122,v123
v_mfma_f32_16x16x32_bf16 v[128:131],v[64:67],v[16:19],v[112:115]
v_lshlrev_b32_sdwa v153,1,v153 dst_sel:DWORD dst_unused:UNUSED_PAD src0_sel:DWORD src1_sel:WORD_1
v_lshlrev_b32_sdwa v155,1,v155 dst_sel:DWORD dst_unused:UNUSED_PAD src0_sel:DWORD src1_sel:WORD_1
v_mfma_f32_16x16x32_bf16 v[136:139],v[64:67],v[20:23],v[112:115]
s_waitcnt lgkmcnt(12)
ds_read_u16_d16_hi v150,v150
ds_read_u16 v149,v149
v_exp_f16_sdwa v120,v121 dst_sel:WORD_1 dst_unused:UNUSED_PRESERVE src0_sel:WORD_1
v_mfma_f32_16x16x32_bf16 v[140:143],v[68:71],v[20:23],v[116:119]
v_exp_f16_sdwa v122,v123 dst_sel:WORD_1 dst_unused:UNUSED_PRESERVE src0_sel:WORD_1
v_mfma_f32_16x16x32_bf16 v[132:135],v[68:71],v[16:19],v[116:119]
s_waitcnt lgkmcnt(11)
v_mfma_f32_16x16x32_bf16 v[128:131],v[72:75],v[24:27],v[128:131]
v_pk_fma_f16 v122,v122,s55,s55
v_pk_fma_f16 v120,v120,s55,s55
v_rcp_f16 v121,v120
v_mfma_f32_16x16x32_bf16 v[136:139],v[72:75],v[28:31],v[136:139]
s_waitcnt lgkmcnt(8)
ds_read_u16_d16_hi v151,v151
v_rcp_f16 v123,v122
v_mfma_f32_16x16x32_bf16 v[140:143],v[76:79],v[28:31],v[140:143]
ds_read_u16 v152,v152
ds_read_u16_d16_hi v154,v154
v_rcp_f16_sdwa v121,v120 dst_sel:WORD_1 dst_unused:UNUSED_PRESERVE src0_sel:WORD_1
v_mfma_f32_16x16x32_bf16 v[132:135],v[76:79],v[24:27],v[132:135]
s_waitcnt lgkmcnt(9)
s_cbranch_vccnz RA42_%=
s_mov_b64 exec,s[62:63]
s_add_u32 m0,s57,6976
s_add_u32 s64,s56,6094848
buffer_load_dwordx4 v2,s[36:39],s64 offen lds
s_add_u32 m0,s57,8704
s_nop 0
buffer_load_dwordx4 v3,s[36:39],s64 offen lds
s_mov_b64 exec,-1
RA42_%=:
v_rcp_f16_sdwa v123,v122 dst_sel:WORD_1 dst_unused:UNUSED_PRESERVE src0_sel:WORD_1
v_mfma_f32_16x16x32_bf16 v[128:131],v[80:83],v[32:35],v[128:131]
ds_read_u16 v153,v153
v_fma_mix_f32 v156,v156,v121,0 op_sel_hi:[0,1,0]
v_fma_mix_f32 v158,v158,v121,0 op_sel:[0,1,0] op_sel_hi:[0,1,0]
v_mfma_f32_16x16x32_bf16 v[136:139],v[80:83],v[36:39],v[136:139]
s_waitcnt lgkmcnt(3)
v_lshl_add_u64 v[144:145],v[144:145],0,v[146:147]
v_lshl_add_u64 v[146:147],v[148:149],0,v[150:151]
v_mfma_f32_16x16x32_bf16 v[140:143],v[84:87],v[36:39],v[140:143]
v_fma_mix_f32 v157,v157,v123,0 op_sel_hi:[0,1,0]
v_fma_mix_f32 v159,v159,v123,0 op_sel:[0,1,0] op_sel_hi:[0,1,0]
v_mfma_f32_16x16x32_bf16 v[132:135],v[84:87],v[32:35],v[132:135]
v_lshlrev_b32_sdwa v128,1,v128 dst_sel:DWORD dst_unused:UNUSED_PAD src0_sel:DWORD src1_sel:WORD_1
v_lshlrev_b32_sdwa v130,1,v130 dst_sel:DWORD dst_unused:UNUSED_PAD src0_sel:DWORD src1_sel:WORD_1
v_lshlrev_b32_sdwa v129,1,v129 dst_sel:DWORD dst_unused:UNUSED_PAD src0_sel:DWORD src1_sel:WORD_1
v_mfma_f32_16x16x32_bf16 v[208:211],v[88:91],v[144:147],v[208:211]
ds_read_u16_d16_hi v155,v155
v_lshlrev_b32_sdwa v131,1,v131 dst_sel:DWORD dst_unused:UNUSED_PAD src0_sel:DWORD src1_sel:WORD_1
v_mfma_f32_16x16x32_bf16 v[216:219],v[92:95],v[144:147],v[216:219]
ds_read_u16 v128,v128
ds_read_u16_d16_hi v130,v130
v_fma_mixlo_f16 v120,v140,s99,0
v_fma_mixlo_f16 v122,v141,s99,0
v_mfma_f32_16x16x32_bf16 v[224:227],v[96:99],v[144:147],v[224:227]
ds_read_u16 v129,v129
v_lshlrev_b32_sdwa v132,1,v132 dst_sel:DWORD dst_unused:UNUSED_PAD src0_sel:DWORD src1_sel:WORD_1
v_lshlrev_b32_sdwa v134,1,v134 dst_sel:DWORD dst_unused:UNUSED_PAD src0_sel:DWORD src1_sel:WORD_1
v_mfma_f32_16x16x32_bf16 v[232:235],v[100:103],v[144:147],v[232:235]
s_waitcnt lgkmcnt(3)
v_lshl_add_u64 v[152:153],v[152:153],0,v[154:155]
v_cvt_pk_bf16_f32 v154,v156,v158
v_mfma_f32_16x16x32_bf16 v[240:243],v[104:107],v[144:147],v[240:243]
v_fma_mixhi_f16 v120,v142,s99,0
v_cvt_pk_bf16_f32 v155,v157,v159
v_mfma_f32_16x16x32_bf16 v[248:251],v[108:111],v[144:147],v[248:251]
s_cbranch_vccnz RA43_%=
s_mov_b64 exec,s[62:63]
s_add_u32 m0,s57,10464
s_add_u32 s64,s56,6193152
buffer_load_dwordx4 v2,s[36:39],s64 offen lds
s_add_u32 m0,s57,12192
s_nop 0
buffer_load_dwordx4 v3,s[36:39],s64 offen lds
s_mov_b64 exec,-1
RA43_%=:
ds_read_u16_d16_hi v131,v131
v_fma_mixhi_f16 v122,v143,s99,0
v_mfma_f32_16x16x32_bf16 v[212:215],v[88:91],v[152:155],v[212:215]
v_lshlrev_b32_sdwa v133,1,v133 dst_sel:DWORD dst_unused:UNUSED_PAD src0_sel:DWORD src1_sel:WORD_1
v_pk_fma_f16 v121,v120,v120,s54
v_lshlrev_b32_sdwa v135,1,v135 dst_sel:DWORD dst_unused:UNUSED_PAD src0_sel:DWORD src1_sel:WORD_1
v_mfma_f32_16x16x32_bf16 v[220:223],v[92:95],v[152:155],v[220:223]
s_setprio 2
ds_read_u16 v132,v132
ds_read_u16_d16_hi v134,v134
v_pk_fma_f16 v123,v122,v122,s54
v_pk_mul_f16 v123,v122,v123
v_mfma_f32_16x16x32_bf16 v[228:231],v[96:99],v[152:155],v[228:231]
ds_read_b64_tr_b16 v[88:89],v6 offset:18480
v_pk_mul_f16 v121,v120,v121
v_lshlrev_b32_sdwa v136,1,v136 dst_sel:DWORD dst_unused:UNUSED_PAD src0_sel:DWORD src1_sel:WORD_1
v_mfma_f32_16x16x32_bf16 v[236:239],v[100:103],v[152:155],v[236:239]
ds_read_b64_tr_b16 v[90:91],v6 offset:20016
v_lshlrev_b32_sdwa v138,1,v138 dst_sel:DWORD dst_unused:UNUSED_PAD src0_sel:DWORD src1_sel:WORD_1
v_mfma_f32_16x16x32_bf16 v[244:247],v[104:107],v[152:155],v[244:247]
ds_read_u16 v133,v133
v_exp_f16 v120,v121
v_mfma_f32_16x16x32_bf16 v[252:255],v[108:111],v[152:155],v[252:255]
ds_read_b64_tr_b16 v[92:93],v6 offset:18488
ds_read_b64_tr_b16 v[94:95],v6 offset:20024
v_lshlrev_b32_sdwa v137,1,v137 dst_sel:DWORD dst_unused:UNUSED_PAD src0_sel:DWORD src1_sel:WORD_1
v_mfma_f32_16x16x32_bf16 v[144:147],v[64:67],v[40:43],v[112:115]
ds_read_b64_tr_b16 v[96:97],v6 offset:18544
v_lshlrev_b32_sdwa v139,1,v139 dst_sel:DWORD dst_unused:UNUSED_PAD src0_sel:DWORD src1_sel:WORD_1
v_exp_f16 v122,v123
v_mfma_f32_16x16x32_bf16 v[152:155],v[64:67],v[44:47],v[112:115]
ds_read_u16_d16_hi v135,v135
ds_read_b64_tr_b16 v[98:99],v6 offset:20080
v_mfma_f32_16x16x32_bf16 v[156:159],v[68:71],v[44:47],v[116:119]
ds_read_u16 v136,v136
v_exp_f16_sdwa v120,v121 dst_sel:WORD_1 dst_unused:UNUSED_PRESERVE src0_sel:WORD_1
v_exp_f16_sdwa v122,v123 dst_sel:WORD_1 dst_unused:UNUSED_PRESERVE src0_sel:WORD_1
v_mfma_f32_16x16x32_bf16 v[148:151],v[68:71],v[40:43],v[116:119]
ds_read_b64_tr_b16 v[100:101],v6 offset:18552
v_pk_fma_f16 v120,v120,s55,s55
v_pk_fma_f16 v122,v122,s55,s55
v_mfma_f32_16x16x32_bf16 v[144:147],v[72:75],v[48:51],v[144:147]
s_cbranch_vccnz RA44_%=
s_mov_b64 exec,s[62:63]
s_add_u32 m0,s57,13952
s_add_u32 s64,s56,6291456
buffer_load_dwordx4 v2,s[36:39],s64 offen lds
s_add_u32 m0,s57,15680
s_nop 0
buffer_load_dwordx4 v3,s[36:39],s64 offen lds
s_mov_b64 exec,-1
RA44_%=:
v_rcp_f16 v121,v120
v_mfma_f32_16x16x32_bf16 v[152:155],v[72:75],v[52:55],v[152:155]
ds_read_b64_tr_b16 v[102:103],v6 offset:20088
ds_read_b64_tr_b16 v[104:105],v6 offset:18608
v_rcp_f16 v123,v122
v_mfma_f32_16x16x32_bf16 v[156:159],v[76:79],v[52:55],v[156:159]
ds_read_u16_d16_hi v138,v138
v_rcp_f16_sdwa v121,v120 dst_sel:WORD_1 dst_unused:UNUSED_PRESERVE src0_sel:WORD_1
v_mfma_f32_16x16x32_bf16 v[148:151],v[76:79],v[48:51],v[148:151]
ds_read_b64_tr_b16 v[106:107],v6 offset:20144
ds_read_b64_tr_b16 v[108:109],v6 offset:18616
v_rcp_f16_sdwa v123,v122 dst_sel:WORD_1 dst_unused:UNUSED_PRESERVE src0_sel:WORD_1
v_mfma_f32_16x16x32_bf16 v[144:147],v[80:83],v[56:59],v[144:147]
ds_read_u16 v137,v137
v_fma_mix_f32 v142,v142,v121,0 op_sel:[0,1,0] op_sel_hi:[0,1,0]
v_mfma_f32_16x16x32_bf16 v[152:155],v[80:83],v[60:63],v[152:155]
s_waitcnt lgkmcnt(9)
ds_read_b64_tr_b16 v[110:111],v6 offset:20152
ds_read_b64_tr_b16 v[64:65],v4 offset:12832
v_fma_mix_f32 v140,v140,v121,0 op_sel_hi:[0,1,0]
v_lshl_add_u64 v[128:129],v[128:129],0,v[130:131]
v_lshl_add_u64 v[130:131],v[132:133],0,v[134:135]
v_mfma_f32_16x16x32_bf16 v[156:159],v[84:87],v[60:63],v[156:159]
ds_read_u16_d16_hi v139,v139
v_fma_mix_f32 v141,v141,v123,0 op_sel_hi:[0,1,0]
v_fma_mix_f32 v143,v143,v123,0 op_sel:[0,1,0] op_sel_hi:[0,1,0]
v_mfma_f32_16x16x32_bf16 v[148:151],v[84:87],v[56:59],v[148:151]
ds_read_b64_tr_b16 v[66:67],v4 offset:14880
ds_read_b128 v[112:115],v7 offset:1152
v_lshlrev_b32_sdwa v144,1,v144 dst_sel:DWORD dst_unused:UNUSED_PAD src0_sel:DWORD src1_sel:WORD_1
v_lshlrev_b32_sdwa v146,1,v146 dst_sel:DWORD dst_unused:UNUSED_PAD src0_sel:DWORD src1_sel:WORD_1
v_mfma_f32_16x16x32_bf16 v[160:163],v[88:91],v[128:131],v[160:163]
ds_read_u16 v144,v144
v_lshlrev_b32_sdwa v145,1,v145 dst_sel:DWORD dst_unused:UNUSED_PAD src0_sel:DWORD src1_sel:WORD_1
v_lshlrev_b32_sdwa v147,1,v147 dst_sel:DWORD dst_unused:UNUSED_PAD src0_sel:DWORD src1_sel:WORD_1
v_fma_mixlo_f16 v122,v157,s99,0
v_mfma_f32_16x16x32_bf16 v[168:171],v[92:95],v[128:131],v[168:171]
s_waitcnt lgkmcnt(14)
ds_read_b64_tr_b16 v[68:69],v5 offset:12832
ds_read_b64_tr_b16 v[70:71],v5 offset:14880
v_fma_mixlo_f16 v120,v156,s99,0
v_mfma_f32_16x16x32_bf16 v[176:179],v[96:99],v[128:131],v[176:179]
s_waitcnt lgkmcnt(13)
s_cbranch_vccnz RA45_%=
s_mov_b64 exec,s[62:63]
s_add_u32 m0,s57,17440
s_add_u32 s64,s56,6389760
buffer_load_dwordx4 v2,s[36:39],s64 offen lds
s_add_u32 m0,s57,19168
s_nop 0
buffer_load_dwordx4 v3,s[36:39],s64 offen lds
s_mov_b64 exec,-1
RA45_%=:
ds_read_u16_d16_hi v146,v146
v_lshlrev_b32_sdwa v148,1,v148 dst_sel:DWORD dst_unused:UNUSED_PAD src0_sel:DWORD src1_sel:WORD_1
v_lshlrev_b32_sdwa v150,1,v150 dst_sel:DWORD dst_unused:UNUSED_PAD src0_sel:DWORD src1_sel:WORD_1
v_mfma_f32_16x16x32_bf16 v[184:187],v[100:103],v[128:131],v[184:187]
s_waitcnt lgkmcnt(6)
ds_read_b128 v[116:119],v7 offset:1184
ds_read_b64_tr_b16 v[72:73],v4 offset:16928
v_lshl_add_u64 v[136:137],v[136:137],0,v[138:139]
v_fma_mixhi_f16 v120,v158,s99,0
v_mfma_f32_16x16x32_bf16 v[192:195],v[104:107],v[128:131],v[192:195]
ds_read_u16 v145,v145
v_cvt_pk_bf16_f32 v138,v140,v142
v_cvt_pk_bf16_f32 v139,v141,v143
v_mfma_f32_16x16x32_bf16 v[200:203],v[108:111],v[128:131],v[200:203]
v_fma_mixhi_f16 v122,v159,s99,0
v_mfma_f32_16x16x32_bf16 v[164:167],v[88:91],v[136:139],v[164:167]
ds_read_b64_tr_b16 v[74:75],v4 offset:18976
ds_read_b64_tr_b16 v[76:77],v5 offset:16928
v_lshlrev_b32_sdwa v149,1,v149 dst_sel:DWORD dst_unused:UNUSED_PAD src0_sel:DWORD src1_sel:WORD_1
v_lshlrev_b32_sdwa v151,1,v151 dst_sel:DWORD dst_unused:UNUSED_PAD src0_sel:DWORD src1_sel:WORD_1
v_mfma_f32_16x16x32_bf16 v[172:175],v[92:95],v[136:139],v[172:175]
ds_read_b64_tr_b16 v[78:79],v5 offset:18976
ds_read_b64_tr_b16 v[80:81],v4 offset:21024
v_pk_fma_f16 v121,v120,v120,s54
v_pk_fma_f16 v123,v122,v122,s54
v_mfma_f32_16x16x32_bf16 v[180:183],v[96:99],v[136:139],v[180:183]
s_setprio 1
ds_read_u16_d16_hi v147,v147
v_pk_mul_f16 v121,v120,v121
v_pk_mul_f16 v123,v122,v123
v_mfma_f32_16x16x32_bf16 v[188:191],v[100:103],v[136:139],v[188:191]
ds_read_u16 v148,v148
v_lshlrev_b32_sdwa v152,1,v152 dst_sel:DWORD dst_unused:UNUSED_PAD src0_sel:DWORD src1_sel:WORD_1
v_lshlrev_b32_sdwa v154,1,v154 dst_sel:DWORD dst_unused:UNUSED_PAD src0_sel:DWORD src1_sel:WORD_1
v_mfma_f32_16x16x32_bf16 v[196:199],v[104:107],v[136:139],v[196:199]
v_exp_f16 v120,v121
v_mfma_f32_16x16x32_bf16 v[204:207],v[108:111],v[136:139],v[204:207]
s_waitcnt lgkmcnt(13)
ds_read_b64_tr_b16 v[82:83],v4 offset:23072
ds_read_b64_tr_b16 v[84:85],v5 offset:21024
v_exp_f16 v122,v123
v_mfma_f32_16x16x32_bf16 v[128:131],v[64:67],v[16:19],v[112:115]
v_lshlrev_b32_sdwa v153,1,v153 dst_sel:DWORD dst_unused:UNUSED_PAD src0_sel:DWORD src1_sel:WORD_1
v_lshlrev_b32_sdwa v155,1,v155 dst_sel:DWORD dst_unused:UNUSED_PAD src0_sel:DWORD src1_sel:WORD_1
v_mfma_f32_16x16x32_bf16 v[136:139],v[64:67],v[20:23],v[112:115]
s_waitcnt lgkmcnt(10)
ds_read_u16_d16_hi v150,v150
ds_read_u16 v149,v149
v_exp_f16_sdwa v120,v121 dst_sel:WORD_1 dst_unused:UNUSED_PRESERVE src0_sel:WORD_1
v_mfma_f32_16x16x32_bf16 v[140:143],v[68:71],v[20:23],v[116:119]
v_exp_f16_sdwa v122,v123 dst_sel:WORD_1 dst_unused:UNUSED_PRESERVE src0_sel:WORD_1
v_mfma_f32_16x16x32_bf16 v[132:135],v[68:71],v[16:19],v[116:119]
s_waitcnt lgkmcnt(9)
ds_read_b64_tr_b16 v[86:87],v5 offset:23072
v_mfma_f32_16x16x32_bf16 v[128:131],v[72:75],v[24:27],v[128:131]
s_nop 0
v_pk_fma_f16 v122,v122,s55,s55
v_pk_fma_f16 v120,v120,s55,s55
v_rcp_f16 v121,v120
v_mfma_f32_16x16x32_bf16 v[136:139],v[72:75],v[28:31],v[136:139]
s_waitcnt lgkmcnt(8)
ds_read_u16_d16_hi v151,v151
v_rcp_f16 v123,v122
v_mfma_f32_16x16x32_bf16 v[140:143],v[76:79],v[28:31],v[140:143]
ds_read_u16 v152,v152
ds_read_u16_d16_hi v154,v154
s_nop 0
s_mov_b32 s86,0xbe801d5a
v_rcp_f16_sdwa v121,v120 dst_sel:WORD_1 dst_unused:UNUSED_PRESERVE src0_sel:WORD_1
v_mfma_f32_16x16x32_bf16 v[132:135],v[76:79],v[24:27],v[132:135]
s_waitcnt lgkmcnt(7)
v_rcp_f16_sdwa v123,v122 dst_sel:WORD_1 dst_unused:UNUSED_PRESERVE src0_sel:WORD_1
v_mfma_f32_16x16x32_bf16 v[128:131],v[80:83],v[32:35],v[128:131]
s_nop 0
ds_read_u16 v153,v153
v_fma_mix_f32 v156,v156,v121,0 op_sel_hi:[0,1,0]
v_fma_mix_f32 v158,v158,v121,0 op_sel:[0,1,0] op_sel_hi:[0,1,0]
v_mfma_f32_16x16x32_bf16 v[136:139],v[80:83],v[36:39],v[136:139]
s_waitcnt lgkmcnt(3)
s_setprio 0
v_lshl_add_u64 v[144:145],v[144:145],0,v[146:147]
v_lshl_add_u64 v[146:147],v[148:149],0,v[150:151]
v_mfma_f32_16x16x32_bf16 v[140:143],v[84:87],v[36:39],v[140:143]
v_fma_mix_f32 v157,v157,v123,0 op_sel_hi:[0,1,0]
v_fma_mix_f32 v159,v159,v123,0 op_sel:[0,1,0] op_sel_hi:[0,1,0]
v_mfma_f32_16x16x32_bf16 v[132:135],v[84:87],v[32:35],v[132:135]
s_nop 0
v_lshlrev_b32_sdwa v128,1,v128 dst_sel:DWORD dst_unused:UNUSED_PAD src0_sel:DWORD src1_sel:WORD_1
v_lshlrev_b32_sdwa v130,1,v130 dst_sel:DWORD dst_unused:UNUSED_PAD src0_sel:DWORD src1_sel:WORD_1
v_lshlrev_b32_sdwa v129,1,v129 dst_sel:DWORD dst_unused:UNUSED_PAD src0_sel:DWORD src1_sel:WORD_1
v_mfma_f32_16x16x32_bf16 v[208:211],v[88:91],v[144:147],v[208:211]
s_cbranch_vccz RA46_%=
s_waitcnt vmcnt(0)
RA46_%=:
s_barrier
s_setprio 2
ds_read_u16_d16_hi v155,v155
v_mfma_f32_16x16x32_bf16 v[216:219],v[92:95],v[144:147],v[216:219]
ds_read_u16 v128,v128
v_lshlrev_b32_sdwa v131,1,v131 dst_sel:DWORD dst_unused:UNUSED_PAD src0_sel:DWORD src1_sel:WORD_1
v_mfma_f32_16x16x32_bf16 v[224:227],v[96:99],v[144:147],v[224:227]
s_cbranch_vccz RA47_%=
s_add_u32 m0,s58,12320
s_mov_b32 s69,s60
buffer_load_dwordx4 v2,s[44:47],s69 offen lds
s_add_u32 m0,m0,1024
s_add_u32 s69,s69,s53
buffer_load_dwordx4 v3,s[44:47],s69 offen lds
s_add_u32 m0,m0,1024
s_add_u32 s69,s69,s53
buffer_load_dwordx4 v2,s[44:47],s69 offen lds
s_add_u32 m0,m0,s50
s_add_u32 s69,s69,s53
buffer_load_dwordx4 v3,s[44:47],s69 offen lds
RA47_%=:
ds_read_u16_d16_hi v130,v130
v_lshlrev_b32_sdwa v132,1,v132 dst_sel:DWORD dst_unused:UNUSED_PAD src0_sel:DWORD src1_sel:WORD_1
v_mfma_f32_16x16x32_bf16 v[232:235],v[100:103],v[144:147],v[232:235]
s_waitcnt lgkmcnt(2)
v_lshlrev_b32_sdwa v134,1,v134 dst_sel:DWORD dst_unused:UNUSED_PAD src0_sel:DWORD src1_sel:WORD_1
v_lshl_add_u64 v[152:153],v[152:153],0,v[154:155]
v_mfma_f32_16x16x32_bf16 v[240:243],v[104:107],v[144:147],v[240:243]
v_cvt_pk_bf16_f32 v155,v157,v159
v_cvt_pk_bf16_f32 v154,v156,v158
v_mfma_f32_16x16x32_bf16 v[248:251],v[108:111],v[144:147],v[248:251]
s_cbranch_vccz RA48_%=
s_add_u32 m0,m0,1024
s_add_u32 s69,s69,s53
buffer_load_dwordx4 v2,s[44:47],s69 offen lds
s_add_u32 m0,m0,1024
s_add_u32 s69,s69,s53
buffer_load_dwordx4 v3,s[44:47],s69 offen lds
s_add_u32 m0,m0,s50
s_add_u32 s69,s69,s51
buffer_load_dwordx4 v2,s[44:47],s69 offen lds
s_add_u32 m0,m0,1024
s_add_u32 s69,s69,s53
buffer_load_dwordx4 v3,s[44:47],s69 offen lds
RA48_%=:
ds_read_u16 v129,v129
v_lshlrev_b32_sdwa v133,1,v133 dst_sel:DWORD dst_unused:UNUSED_PAD src0_sel:DWORD src1_sel:WORD_1
v_mfma_f32_16x16x32_bf16 v[212:215],v[88:91],v[152:155],v[212:215]
ds_read_u16_d16_hi v131,v131
v_lshlrev_b32_sdwa v135,1,v135 dst_sel:DWORD dst_unused:UNUSED_PAD src0_sel:DWORD src1_sel:WORD_1
v_mfma_f32_16x16x32_bf16 v[220:223],v[92:95],v[152:155],v[220:223]
ds_read_b64_tr_b16 v[88:89],v6 offset:0
ds_read_b64_tr_b16 v[90:91],v6 offset:1536
v_lshlrev_b32_sdwa v136,1,v136 dst_sel:DWORD dst_unused:UNUSED_PAD src0_sel:DWORD src1_sel:WORD_1
v_mfma_f32_16x16x32_bf16 v[228:231],v[96:99],v[152:155],v[228:231]
s_cbranch_vccz RA49_%=
s_add_u32 m0,m0,1024
s_add_u32 s69,s69,s53
buffer_load_dwordx4 v2,s[44:47],s69 offen lds
s_add_u32 m0,m0,s50
s_add_u32 s69,s69,s53
buffer_load_dwordx4 v3,s[44:47],s69 offen lds
s_add_u32 m0,m0,1024
s_add_u32 s69,s69,s53
buffer_load_dwordx4 v2,s[44:47],s69 offen lds
s_add_u32 m0,m0,1024
s_add_u32 s69,s69,s53
buffer_load_dwordx4 v3,s[44:47],s69 offen lds
RA49_%=:
ds_read_u16 v132,v132
v_lshlrev_b32_sdwa v138,1,v138 dst_sel:DWORD dst_unused:UNUSED_PAD src0_sel:DWORD src1_sel:WORD_1
v_mfma_f32_16x16x32_bf16 v[236:239],v[100:103],v[152:155],v[236:239]
ds_read_u16_d16_hi v134,v134
v_lshlrev_b32_sdwa v137,1,v137 dst_sel:DWORD dst_unused:UNUSED_PAD src0_sel:DWORD src1_sel:WORD_1
v_mfma_f32_16x16x32_bf16 v[244:247],v[104:107],v[152:155],v[244:247]
ds_read_u16 v133,v133
v_lshlrev_b32_sdwa v139,1,v139 dst_sel:DWORD dst_unused:UNUSED_PAD src0_sel:DWORD src1_sel:WORD_1
v_mfma_f32_16x16x32_bf16 v[252:255],v[108:111],v[152:155],v[252:255]
ds_read_b64_tr_b16 v[92:93],v6 offset:8
ds_read_b64_tr_b16 v[94:95],v6 offset:1544
v_lshlrev_b32_sdwa v140,1,v140 dst_sel:DWORD dst_unused:UNUSED_PAD src0_sel:DWORD src1_sel:WORD_1
v_mfma_f32_16x16x32_bf16 v[144:147],v[64:67],v[40:43],v[112:115]
ds_read_u16_d16_hi v135,v135
v_lshlrev_b32_sdwa v142,1,v142 dst_sel:DWORD dst_unused:UNUSED_PAD src0_sel:DWORD src1_sel:WORD_1
v_mfma_f32_16x16x32_bf16 v[152:155],v[64:67],v[44:47],v[112:115]
ds_read_u16 v136,v136
ds_read_b64_tr_b16 v[96:97],v6 offset:64
v_lshlrev_b32_sdwa v141,1,v141 dst_sel:DWORD dst_unused:UNUSED_PAD src0_sel:DWORD src1_sel:WORD_1
v_mfma_f32_16x16x32_bf16 v[156:159],v[68:71],v[44:47],v[116:119]
ds_read_b64_tr_b16 v[98:99],v6 offset:1600
ds_read_b64_tr_b16 v[100:101],v6 offset:72
v_mfma_f32_16x16x32_bf16 v[148:151],v[68:71],v[40:43],v[116:119]
ds_read_u16_d16_hi v138,v138
v_lshlrev_b32_sdwa v143,1,v143 dst_sel:DWORD dst_unused:UNUSED_PAD src0_sel:DWORD src1_sel:WORD_1
v_mfma_f32_16x16x32_bf16 v[144:147],v[72:75],v[48:51],v[144:147]
ds_read_u16 v137,v137
v_mfma_f32_16x16x32_bf16 v[152:155],v[72:75],v[52:55],v[152:155]
ds_read_b64_tr_b16 v[102:103],v6 offset:1608
ds_read_b64_tr_b16 v[104:105],v6 offset:128
v_mfma_f32_16x16x32_bf16 v[156:159],v[76:79],v[52:55],v[156:159]
s_waitcnt lgkmcnt(15)
ds_read_u16_d16_hi v139,v139
v_lshl_add_u64 v[128:129],v[128:129],0,v[130:131]
v_mfma_f32_16x16x32_bf16 v[148:151],v[76:79],v[48:51],v[148:151]
ds_read_u16 v140,v140
v_mfma_f32_16x16x32_bf16 v[144:147],v[80:83],v[56:59],v[144:147]
ds_read_b64_tr_b16 v[106:107],v6 offset:1664
ds_read_b64_tr_b16 v[108:109],v6 offset:136
v_mfma_f32_16x16x32_bf16 v[152:155],v[80:83],v[60:63],v[152:155]
s_waitcnt lgkmcnt(12)
ds_read_u16_d16_hi v142,v142
v_lshl_add_u64 v[130:131],v[132:133],0,v[134:135]
v_mfma_f32_16x16x32_bf16 v[156:159],v[84:87],v[60:63],v[156:159]
ds_read_u16 v141,v141
ds_read_u16_d16_hi v143,v143
v_mfma_f32_16x16x32_bf16 v[148:151],v[84:87],v[56:59],v[148:151]
ds_read_b64_tr_b16 v[110:111],v6 offset:1672
ds_read_b64_tr_b16 v[64:65],v4 offset:0
v_lshlrev_b32_sdwa v144,1,v144 dst_sel:DWORD dst_unused:UNUSED_PAD src0_sel:DWORD src1_sel:WORD_1
v_lshlrev_b32_sdwa v146,1,v146 dst_sel:DWORD dst_unused:UNUSED_PAD src0_sel:DWORD src1_sel:WORD_1
v_mfma_f32_16x16x32_bf16 v[160:163],v[88:91],v[128:131],v[160:163]
ds_read_b64_tr_b16 v[66:67],v4 offset:2048
v_lshlrev_b32_sdwa v145,1,v145 dst_sel:DWORD dst_unused:UNUSED_PAD src0_sel:DWORD src1_sel:WORD_1
v_mfma_f32_16x16x32_bf16 v[168:171],v[92:95],v[128:131],v[168:171]
s_waitcnt lgkmcnt(15)
ds_read_u16 v144,v144
v_lshlrev_b32_sdwa v147,1,v147 dst_sel:DWORD dst_unused:UNUSED_PAD src0_sel:DWORD src1_sel:WORD_1
v_mfma_f32_16x16x32_bf16 v[176:179],v[96:99],v[128:131],v[176:179]
s_waitcnt lgkmcnt(12)
ds_read_b128 v[112:115],v7 offset:1280
ds_read_b64_tr_b16 v[68:69],v5 offset:0
v_mfma_f32_16x16x32_bf16 v[184:187],v[100:103],v[128:131],v[184:187]
s_waitcnt lgkmcnt(10)
ds_read_u16_d16_hi v146,v146
v_lshlrev_b32_sdwa v148,1,v148 dst_sel:DWORD dst_unused:UNUSED_PAD src0_sel:DWORD src1_sel:WORD_1
v_lshlrev_b32_sdwa v150,1,v150 dst_sel:DWORD dst_unused:UNUSED_PAD src0_sel:DWORD src1_sel:WORD_1
v_lshl_add_u64 v[136:137],v[136:137],0,v[138:139]
v_mfma_f32_16x16x32_bf16 v[192:195],v[104:107],v[128:131],v[192:195]
s_waitcnt lgkmcnt(6)
ds_read_u16 v145,v145
ds_read_u16_d16_hi v147,v147
v_lshl_add_u64 v[138:139],v[140:141],0,v[142:143]
v_mfma_f32_16x16x32_bf16 v[200:203],v[108:111],v[128:131],v[200:203]
ds_read_b64_tr_b16 v[70:71],v5 offset:2048
ds_read_b128 v[116:119],v7 offset:1312
v_mfma_f32_16x16x32_bf16 v[164:167],v[88:91],v[136:139],v[164:167]
ds_read_u16 v148,v148
v_lshlrev_b32_sdwa v149,1,v149 dst_sel:DWORD dst_unused:UNUSED_PAD src0_sel:DWORD src1_sel:WORD_1
v_lshlrev_b32_sdwa v151,1,v151 dst_sel:DWORD dst_unused:UNUSED_PAD src0_sel:DWORD src1_sel:WORD_1
v_mfma_f32_16x16x32_bf16 v[172:175],v[92:95],v[136:139],v[172:175]
ds_read_b64_tr_b16 v[72:73],v4 offset:4096
v_lshlrev_b32_sdwa v152,1,v152 dst_sel:DWORD dst_unused:UNUSED_PAD src0_sel:DWORD src1_sel:WORD_1
v_mfma_f32_16x16x32_bf16 v[180:183],v[96:99],v[136:139],v[180:183]
ds_read_b64_tr_b16 v[74:75],v4 offset:6144
ds_read_b64_tr_b16 v[76:77],v5 offset:4096
v_lshlrev_b32_sdwa v154,1,v154 dst_sel:DWORD dst_unused:UNUSED_PAD src0_sel:DWORD src1_sel:WORD_1
v_mfma_f32_16x16x32_bf16 v[188:191],v[100:103],v[136:139],v[188:191]
ds_read_u16_d16_hi v150,v150
v_lshlrev_b32_sdwa v153,1,v153 dst_sel:DWORD dst_unused:UNUSED_PAD src0_sel:DWORD src1_sel:WORD_1
v_mfma_f32_16x16x32_bf16 v[196:199],v[104:107],v[136:139],v[196:199]
ds_read_u16 v149,v149
v_lshlrev_b32_sdwa v155,1,v155 dst_sel:DWORD dst_unused:UNUSED_PAD src0_sel:DWORD src1_sel:WORD_1
v_mfma_f32_16x16x32_bf16 v[204:207],v[108:111],v[136:139],v[204:207]
s_waitcnt lgkmcnt(12)
ds_read_u16_d16_hi v151,v151
v_lshlrev_b32_sdwa v156,1,v156 dst_sel:DWORD dst_unused:UNUSED_PAD src0_sel:DWORD src1_sel:WORD_1
v_mfma_f32_16x16x32_bf16 v[128:131],v[64:67],v[16:19],v[112:115]
ds_read_b64_tr_b16 v[78:79],v5 offset:6144
ds_read_b64_tr_b16 v[80:81],v4 offset:8192
v_lshlrev_b32_sdwa v158,1,v158 dst_sel:DWORD dst_unused:UNUSED_PAD src0_sel:DWORD src1_sel:WORD_1
v_mfma_f32_16x16x32_bf16 v[136:139],v[64:67],v[20:23],v[112:115]
s_waitcnt lgkmcnt(9)
ds_read_b64_tr_b16 v[82:83],v4 offset:10240
v_lshlrev_b32_sdwa v157,1,v157 dst_sel:DWORD dst_unused:UNUSED_PAD src0_sel:DWORD src1_sel:WORD_1
v_mfma_f32_16x16x32_bf16 v[140:143],v[68:71],v[20:23],v[116:119]
ds_read_b64_tr_b16 v[84:85],v5 offset:8192
ds_read_b64_tr_b16 v[86:87],v5 offset:10240
v_lshlrev_b32_sdwa v159,1,v159 dst_sel:DWORD dst_unused:UNUSED_PAD src0_sel:DWORD src1_sel:WORD_1
v_mfma_f32_16x16x32_bf16 v[132:135],v[68:71],v[16:19],v[116:119]
s_waitcnt lgkmcnt(9)
ds_read_u16 v152,v152
v_mfma_f32_16x16x32_bf16 v[128:131],v[72:75],v[24:27],v[128:131]
ds_read_u16_d16_hi v154,v154
ds_read_u16 v153,v153
v_mfma_f32_16x16x32_bf16 v[136:139],v[72:75],v[28:31],v[136:139]
s_waitcnt lgkmcnt(7)
v_mfma_f32_16x16x32_bf16 v[140:143],v[76:79],v[28:31],v[140:143]
v_lshl_add_u64 v[144:145],v[144:145],0,v[146:147]
v_mfma_f32_16x16x32_bf16 v[132:135],v[76:79],v[24:27],v[132:135]
s_waitcnt lgkmcnt(5)
ds_read_u16_d16_hi v155,v155
v_mfma_f32_16x16x32_bf16 v[128:131],v[80:83],v[32:35],v[128:131]
v_lshl_add_u64 v[146:147],v[148:149],0,v[150:151]
v_mfma_f32_16x16x32_bf16 v[136:139],v[80:83],v[36:39],v[136:139]
s_waitcnt lgkmcnt(4)
ds_read_u16 v156,v156
ds_read_u16_d16_hi v158,v158
v_mfma_f32_16x16x32_bf16 v[140:143],v[84:87],v[36:39],v[140:143]
ds_read_u16 v157,v157
v_mfma_f32_16x16x32_bf16 v[132:135],v[84:87],v[32:35],v[132:135]
v_lshlrev_b32_sdwa v128,1,v128 dst_sel:DWORD dst_unused:UNUSED_PAD src0_sel:DWORD src1_sel:WORD_1
v_lshlrev_b32_sdwa v130,1,v130 dst_sel:DWORD dst_unused:UNUSED_PAD src0_sel:DWORD src1_sel:WORD_1
v_mfma_f32_16x16x32_bf16 v[208:211],v[88:91],v[144:147],v[208:211]
ds_read_u16_d16_hi v159,v159
v_lshlrev_b32_sdwa v129,1,v129 dst_sel:DWORD dst_unused:UNUSED_PAD src0_sel:DWORD src1_sel:WORD_1
v_mfma_f32_16x16x32_bf16 v[216:219],v[92:95],v[144:147],v[216:219]
ds_read_u16 v128,v128
v_lshlrev_b32_sdwa v131,1,v131 dst_sel:DWORD dst_unused:UNUSED_PAD src0_sel:DWORD src1_sel:WORD_1
v_mfma_f32_16x16x32_bf16 v[224:227],v[96:99],v[144:147],v[224:227]
ds_read_u16_d16_hi v130,v130
v_lshlrev_b32_sdwa v132,1,v132 dst_sel:DWORD dst_unused:UNUSED_PAD src0_sel:DWORD src1_sel:WORD_1
v_mfma_f32_16x16x32_bf16 v[232:235],v[100:103],v[144:147],v[232:235]
s_waitcnt lgkmcnt(6)
v_lshlrev_b32_sdwa v134,1,v134 dst_sel:DWORD dst_unused:UNUSED_PAD src0_sel:DWORD src1_sel:WORD_1
v_lshl_add_u64 v[152:153],v[152:153],0,v[154:155]
v_mfma_f32_16x16x32_bf16 v[240:243],v[104:107],v[144:147],v[240:243]
s_waitcnt lgkmcnt(2)
v_lshl_add_u64 v[154:155],v[156:157],0,v[158:159]
v_mfma_f32_16x16x32_bf16 v[248:251],v[108:111],v[144:147],v[248:251]
ds_read_u16 v129,v129
v_lshlrev_b32_sdwa v133,1,v133 dst_sel:DWORD dst_unused:UNUSED_PAD src0_sel:DWORD src1_sel:WORD_1
v_mfma_f32_16x16x32_bf16 v[212:215],v[88:91],v[152:155],v[212:215]
ds_read_u16_d16_hi v131,v131
v_lshlrev_b32_sdwa v135,1,v135 dst_sel:DWORD dst_unused:UNUSED_PAD src0_sel:DWORD src1_sel:WORD_1
v_mfma_f32_16x16x32_bf16 v[220:223],v[92:95],v[152:155],v[220:223]
ds_read_b64_tr_b16 v[88:89],v6 offset:6160
ds_read_b64_tr_b16 v[90:91],v6 offset:7696
v_lshlrev_b32_sdwa v136,1,v136 dst_sel:DWORD dst_unused:UNUSED_PAD src0_sel:DWORD src1_sel:WORD_1
v_mfma_f32_16x16x32_bf16 v[228:231],v[96:99],v[152:155],v[228:231]
ds_read_u16 v132,v132
v_lshlrev_b32_sdwa v138,1,v138 dst_sel:DWORD dst_unused:UNUSED_PAD src0_sel:DWORD src1_sel:WORD_1
v_mfma_f32_16x16x32_bf16 v[236:239],v[100:103],v[152:155],v[236:239]
ds_read_u16_d16_hi v134,v134
v_lshlrev_b32_sdwa v137,1,v137 dst_sel:DWORD dst_unused:UNUSED_PAD src0_sel:DWORD src1_sel:WORD_1
v_mfma_f32_16x16x32_bf16 v[244:247],v[104:107],v[152:155],v[244:247]
ds_read_u16 v133,v133
v_lshlrev_b32_sdwa v139,1,v139 dst_sel:DWORD dst_unused:UNUSED_PAD src0_sel:DWORD src1_sel:WORD_1
v_mfma_f32_16x16x32_bf16 v[252:255],v[108:111],v[152:155],v[252:255]
ds_read_b64_tr_b16 v[92:93],v6 offset:6168
ds_read_b64_tr_b16 v[94:95],v6 offset:7704
v_lshlrev_b32_sdwa v140,1,v140 dst_sel:DWORD dst_unused:UNUSED_PAD src0_sel:DWORD src1_sel:WORD_1
v_mfma_f32_16x16x32_bf16 v[144:147],v[64:67],v[40:43],v[112:115]
ds_read_u16_d16_hi v135,v135
v_lshlrev_b32_sdwa v142,1,v142 dst_sel:DWORD dst_unused:UNUSED_PAD src0_sel:DWORD src1_sel:WORD_1
v_mfma_f32_16x16x32_bf16 v[152:155],v[64:67],v[44:47],v[112:115]
ds_read_u16 v136,v136
ds_read_b64_tr_b16 v[96:97],v6 offset:6224
v_lshlrev_b32_sdwa v141,1,v141 dst_sel:DWORD dst_unused:UNUSED_PAD src0_sel:DWORD src1_sel:WORD_1
v_mfma_f32_16x16x32_bf16 v[156:159],v[68:71],v[44:47],v[116:119]
ds_read_b64_tr_b16 v[98:99],v6 offset:7760
ds_read_b64_tr_b16 v[100:101],v6 offset:6232
v_mfma_f32_16x16x32_bf16 v[148:151],v[68:71],v[40:43],v[116:119]
ds_read_u16_d16_hi v138,v138
v_lshlrev_b32_sdwa v143,1,v143 dst_sel:DWORD dst_unused:UNUSED_PAD src0_sel:DWORD src1_sel:WORD_1
v_mfma_f32_16x16x32_bf16 v[144:147],v[72:75],v[48:51],v[144:147]
ds_read_u16 v137,v137
v_mfma_f32_16x16x32_bf16 v[152:155],v[72:75],v[52:55],v[152:155]
ds_read_b64_tr_b16 v[102:103],v6 offset:7768
ds_read_b64_tr_b16 v[104:105],v6 offset:6288
v_mfma_f32_16x16x32_bf16 v[156:159],v[76:79],v[52:55],v[156:159]
s_waitcnt lgkmcnt(15)
ds_read_u16_d16_hi v139,v139
v_lshl_add_u64 v[128:129],v[128:129],0,v[130:131]
v_mfma_f32_16x16x32_bf16 v[148:151],v[76:79],v[48:51],v[148:151]
ds_read_u16 v140,v140
v_mfma_f32_16x16x32_bf16 v[144:147],v[80:83],v[56:59],v[144:147]
ds_read_b64_tr_b16 v[106:107],v6 offset:7824
ds_read_b64_tr_b16 v[108:109],v6 offset:6296
v_mfma_f32_16x16x32_bf16 v[152:155],v[80:83],v[60:63],v[152:155]
s_waitcnt lgkmcnt(12)
ds_read_u16_d16_hi v142,v142
v_lshl_add_u64 v[130:131],v[132:133],0,v[134:135]
v_mfma_f32_16x16x32_bf16 v[156:159],v[84:87],v[60:63],v[156:159]
ds_read_u16 v141,v141
ds_read_u16_d16_hi v143,v143
v_mfma_f32_16x16x32_bf16 v[148:151],v[84:87],v[56:59],v[148:151]
ds_read_b64_tr_b16 v[110:111],v6 offset:7832
ds_read_b64_tr_b16 v[64:65],v4 offset:512
v_lshlrev_b32_sdwa v144,1,v144 dst_sel:DWORD dst_unused:UNUSED_PAD src0_sel:DWORD src1_sel:WORD_1
v_lshlrev_b32_sdwa v146,1,v146 dst_sel:DWORD dst_unused:UNUSED_PAD src0_sel:DWORD src1_sel:WORD_1
v_mfma_f32_16x16x32_bf16 v[160:163],v[88:91],v[128:131],v[160:163]
ds_read_b64_tr_b16 v[66:67],v4 offset:2560
v_lshlrev_b32_sdwa v145,1,v145 dst_sel:DWORD dst_unused:UNUSED_PAD src0_sel:DWORD src1_sel:WORD_1
v_mfma_f32_16x16x32_bf16 v[168:171],v[92:95],v[128:131],v[168:171]
s_waitcnt lgkmcnt(15)
ds_read_u16 v144,v144
v_lshlrev_b32_sdwa v147,1,v147 dst_sel:DWORD dst_unused:UNUSED_PAD src0_sel:DWORD src1_sel:WORD_1
v_mfma_f32_16x16x32_bf16 v[176:179],v[96:99],v[128:131],v[176:179]
s_waitcnt lgkmcnt(12)
ds_read_b128 v[112:115],v7 offset:1408
ds_read_b64_tr_b16 v[68:69],v5 offset:512
v_mfma_f32_16x16x32_bf16 v[184:187],v[100:103],v[128:131],v[184:187]
s_waitcnt lgkmcnt(10)
ds_read_u16_d16_hi v146,v146
v_lshlrev_b32_sdwa v148,1,v148 dst_sel:DWORD dst_unused:UNUSED_PAD src0_sel:DWORD src1_sel:WORD_1
v_lshlrev_b32_sdwa v150,1,v150 dst_sel:DWORD dst_unused:UNUSED_PAD src0_sel:DWORD src1_sel:WORD_1
v_lshl_add_u64 v[136:137],v[136:137],0,v[138:139]
v_mfma_f32_16x16x32_bf16 v[192:195],v[104:107],v[128:131],v[192:195]
s_waitcnt lgkmcnt(6)
ds_read_u16 v145,v145
ds_read_u16_d16_hi v147,v147
v_lshl_add_u64 v[138:139],v[140:141],0,v[142:143]
v_mfma_f32_16x16x32_bf16 v[200:203],v[108:111],v[128:131],v[200:203]
ds_read_b64_tr_b16 v[70:71],v5 offset:2560
ds_read_b128 v[116:119],v7 offset:1440
v_mfma_f32_16x16x32_bf16 v[164:167],v[88:91],v[136:139],v[164:167]
ds_read_u16 v148,v148
v_lshlrev_b32_sdwa v149,1,v149 dst_sel:DWORD dst_unused:UNUSED_PAD src0_sel:DWORD src1_sel:WORD_1
v_lshlrev_b32_sdwa v151,1,v151 dst_sel:DWORD dst_unused:UNUSED_PAD src0_sel:DWORD src1_sel:WORD_1
v_mfma_f32_16x16x32_bf16 v[172:175],v[92:95],v[136:139],v[172:175]
ds_read_b64_tr_b16 v[72:73],v4 offset:4608
v_lshlrev_b32_sdwa v152,1,v152 dst_sel:DWORD dst_unused:UNUSED_PAD src0_sel:DWORD src1_sel:WORD_1
v_mfma_f32_16x16x32_bf16 v[180:183],v[96:99],v[136:139],v[180:183]
ds_read_b64_tr_b16 v[74:75],v4 offset:6656
ds_read_b64_tr_b16 v[76:77],v5 offset:4608
v_lshlrev_b32_sdwa v154,1,v154 dst_sel:DWORD dst_unused:UNUSED_PAD src0_sel:DWORD src1_sel:WORD_1
v_mfma_f32_16x16x32_bf16 v[188:191],v[100:103],v[136:139],v[188:191]
s_nop 0
ds_read_u16_d16_hi v150,v150
v_lshlrev_b32_sdwa v153,1,v153 dst_sel:DWORD dst_unused:UNUSED_PAD src0_sel:DWORD src1_sel:WORD_1
v_mfma_f32_16x16x32_bf16 v[196:199],v[104:107],v[136:139],v[196:199]
ds_read_u16 v149,v149
v_lshlrev_b32_sdwa v155,1,v155 dst_sel:DWORD dst_unused:UNUSED_PAD src0_sel:DWORD src1_sel:WORD_1
v_mfma_f32_16x16x32_bf16 v[204:207],v[108:111],v[136:139],v[204:207]
s_waitcnt lgkmcnt(12)
ds_read_u16_d16_hi v151,v151
v_lshlrev_b32_sdwa v156,1,v156 dst_sel:DWORD dst_unused:UNUSED_PAD src0_sel:DWORD src1_sel:WORD_1
v_mfma_f32_16x16x32_bf16 v[128:131],v[64:67],v[16:19],v[112:115]
s_nop 0
ds_read_b64_tr_b16 v[78:79],v5 offset:6656
ds_read_b64_tr_b16 v[80:81],v4 offset:8704
v_lshlrev_b32_sdwa v158,1,v158 dst_sel:DWORD dst_unused:UNUSED_PAD src0_sel:DWORD src1_sel:WORD_1
v_mfma_f32_16x16x32_bf16 v[136:139],v[64:67],v[20:23],v[112:115]
s_waitcnt lgkmcnt(9)
ds_read_b64_tr_b16 v[82:83],v4 offset:10752
v_lshlrev_b32_sdwa v157,1,v157 dst_sel:DWORD dst_unused:UNUSED_PAD src0_sel:DWORD src1_sel:WORD_1
v_mfma_f32_16x16x32_bf16 v[140:143],v[68:71],v[20:23],v[116:119]
ds_read_b64_tr_b16 v[84:85],v5 offset:8704
ds_read_b64_tr_b16 v[86:87],v5 offset:10752
v_lshlrev_b32_sdwa v159,1,v159 dst_sel:DWORD dst_unused:UNUSED_PAD src0_sel:DWORD src1_sel:WORD_1
v_mfma_f32_16x16x32_bf16 v[132:135],v[68:71],v[16:19],v[116:119]
s_waitcnt lgkmcnt(9)
ds_read_u16 v152,v152
v_mfma_f32_16x16x32_bf16 v[128:131],v[72:75],v[24:27],v[128:131]
s_nop 0
ds_read_u16_d16_hi v154,v154
ds_read_u16 v153,v153
v_mfma_f32_16x16x32_bf16 v[136:139],v[72:75],v[28:31],v[136:139]
s_waitcnt lgkmcnt(7)
v_mfma_f32_16x16x32_bf16 v[140:143],v[76:79],v[28:31],v[140:143]
v_lshl_add_u64 v[144:145],v[144:145],0,v[146:147]
v_mfma_f32_16x16x32_bf16 v[132:135],v[76:79],v[24:27],v[132:135]
s_waitcnt lgkmcnt(5)
ds_read_u16_d16_hi v155,v155
v_mfma_f32_16x16x32_bf16 v[128:131],v[80:83],v[32:35],v[128:131]
s_nop 0
v_lshl_add_u64 v[146:147],v[148:149],0,v[150:151]
v_mfma_f32_16x16x32_bf16 v[136:139],v[80:83],v[36:39],v[136:139]
s_waitcnt lgkmcnt(4)
ds_read_u16 v156,v156
ds_read_u16_d16_hi v158,v158
v_mfma_f32_16x16x32_bf16 v[140:143],v[84:87],v[36:39],v[140:143]
ds_read_u16 v157,v157
v_mfma_f32_16x16x32_bf16 v[132:135],v[84:87],v[32:35],v[132:135]
s_nop 0
v_lshlrev_b32_sdwa v128,1,v128 dst_sel:DWORD dst_unused:UNUSED_PAD src0_sel:DWORD src1_sel:WORD_1
v_lshlrev_b32_sdwa v130,1,v130 dst_sel:DWORD dst_unused:UNUSED_PAD src0_sel:DWORD src1_sel:WORD_1
v_mfma_f32_16x16x32_bf16 v[208:211],v[88:91],v[144:147],v[208:211]
s_waitcnt vmcnt(0)
s_barrier
s_setprio 3
ds_read_u16_d16_hi v159,v159
v_fma_mixlo_f16 v120,v140,s99,0
v_lshlrev_b32_sdwa v129,1,v129 dst_sel:DWORD dst_unused:UNUSED_PAD src0_sel:DWORD src1_sel:WORD_1
v_lshlrev_b32_sdwa v131,1,v131 dst_sel:DWORD dst_unused:UNUSED_PAD src0_sel:DWORD src1_sel:WORD_1
v_mfma_f32_16x16x32_bf16 v[216:219],v[92:95],v[144:147],v[216:219]
s_cbranch_vccz RA50_%=
s_add_u32 m0,s58,0
s_mul_i32 s69,s59,1
s_add_u32 s69,s69,s97
buffer_load_dwordx4 v2,s[44:47],s69 offen lds
s_add_u32 m0,m0,1024
s_add_u32 s69,s69,s53
buffer_load_dwordx4 v3,s[44:47],s69 offen lds
s_add_u32 m0,m0,1024
s_add_u32 s69,s69,s53
buffer_load_dwordx4 v2,s[44:47],s69 offen lds
s_add_u32 m0,m0,s50
s_add_u32 s69,s69,s53
buffer_load_dwordx4 v3,s[44:47],s69 offen lds
RA50_%=:
v_fma_mixlo_f16 v122,v141,s99,0
v_lshlrev_b32_sdwa v132,1,v132 dst_sel:DWORD dst_unused:UNUSED_PAD src0_sel:DWORD src1_sel:WORD_1
v_mfma_f32_16x16x32_bf16 v[224:227],v[96:99],v[144:147],v[224:227]
v_lshlrev_b32_sdwa v134,1,v134 dst_sel:DWORD dst_unused:UNUSED_PAD src0_sel:DWORD src1_sel:WORD_1
v_mfma_f32_16x16x32_bf16 v[232:235],v[100:103],v[144:147],v[232:235]
s_waitcnt lgkmcnt(4)
ds_read_u16 v128,v128
v_lshl_add_u64 v[152:153],v[152:153],0,v[154:155]
v_mfma_f32_16x16x32_bf16 v[240:243],v[104:107],v[144:147],v[240:243]
s_waitcnt lgkmcnt(1)
ds_read_u16_d16_hi v130,v130
v_lshl_add_u64 v[154:155],v[156:157],0,v[158:159]
v_mfma_f32_16x16x32_bf16 v[248:251],v[108:111],v[144:147],v[248:251]
v_fma_mixhi_f16 v120,v142,s99,0
v_fma_mixhi_f16 v122,v143,s99,0
v_mfma_f32_16x16x32_bf16 v[212:215],v[88:91],v[152:155],v[212:215]
s_cbranch_vccz RA51_%=
s_add_u32 m0,m0,1024
s_add_u32 s69,s69,s53
buffer_load_dwordx4 v2,s[44:47],s69 offen lds
s_add_u32 m0,m0,1024
s_add_u32 s69,s69,s53
buffer_load_dwordx4 v3,s[44:47],s69 offen lds
s_add_u32 m0,m0,s50
s_add_u32 s69,s69,s53
buffer_load_dwordx4 v2,s[44:47],s69 offen lds
s_add_u32 m0,m0,1024
s_add_u32 s69,s69,s53
buffer_load_dwordx4 v3,s[44:47],s69 offen lds
RA51_%=:
ds_read_u16 v129,v129
v_lshlrev_b32_sdwa v133,1,v133 dst_sel:DWORD dst_unused:UNUSED_PAD src0_sel:DWORD src1_sel:WORD_1
v_lshlrev_b32_sdwa v135,1,v135 dst_sel:DWORD dst_unused:UNUSED_PAD src0_sel:DWORD src1_sel:WORD_1
v_mfma_f32_16x16x32_bf16 v[220:223],v[92:95],v[152:155],v[220:223]
v_pk_fma_f16 v123,v122,v122,s54
v_lshlrev_b32_sdwa v136,1,v136 dst_sel:DWORD dst_unused:UNUSED_PAD src0_sel:DWORD src1_sel:WORD_1
v_mfma_f32_16x16x32_bf16 v[228:231],v[96:99],v[152:155],v[228:231]
ds_read_b64_tr_b16 v[88:89],v6 offset:12320
ds_read_b64_tr_b16 v[90:91],v6 offset:13856
v_pk_fma_f16 v121,v120,v120,s54
v_pk_mul_f16 v121,v120,v121
v_pk_mul_f16 v123,v122,v123
v_mfma_f32_16x16x32_bf16 v[236:239],v[100:103],v[152:155],v[236:239]
s_cbranch_vccz RA52_%=
s_add_u32 m0,m0,1024
s_add_u32 s69,s69,s53
buffer_load_dwordx4 v2,s[44:47],s69 offen lds
s_add_u32 m0,m0,s50
s_add_u32 s69,s69,s53
buffer_load_dwordx4 v3,s[44:47],s69 offen lds
s_add_u32 m0,m0,1024
s_add_u32 s69,s69,s53
buffer_load_dwordx4 v2,s[44:47],s69 offen lds
s_add_u32 m0,m0,1024
s_add_u32 s69,s69,s53
buffer_load_dwordx4 v3,s[44:47],s69 offen lds
RA52_%=:
ds_read_u16_d16_hi v131,v131
v_mfma_f32_16x16x32_bf16 v[244:247],v[104:107],v[152:155],v[244:247]
ds_read_u16 v132,v132
v_exp_f16 v120,v121
v_mfma_f32_16x16x32_bf16 v[252:255],v[108:111],v[152:155],v[252:255]
ds_read_b64_tr_b16 v[92:93],v6 offset:12328
v_lshlrev_b32_sdwa v138,1,v138 dst_sel:DWORD dst_unused:UNUSED_PAD src0_sel:DWORD src1_sel:WORD_1
v_exp_f16 v122,v123
v_mfma_f32_16x16x32_bf16 v[144:147],v[64:67],v[40:43],v[112:115]
ds_read_u16_d16_hi v134,v134
v_lshlrev_b32_sdwa v137,1,v137 dst_sel:DWORD dst_unused:UNUSED_PAD src0_sel:DWORD src1_sel:WORD_1
v_lshlrev_b32_sdwa v139,1,v139 dst_sel:DWORD dst_unused:UNUSED_PAD src0_sel:DWORD src1_sel:WORD_1
v_exp_f16_sdwa v120,v121 dst_sel:WORD_1 dst_unused:UNUSED_PRESERVE src0_sel:WORD_1
v_mfma_f32_16x16x32_bf16 v[152:155],v[64:67],v[44:47],v[112:115]
ds_read_b64_tr_b16 v[94:95],v6 offset:13864
v_mfma_f32_16x16x32_bf16 v[156:159],v[68:71],v[44:47],v[116:119]
ds_read_u16 v133,v133
v_exp_f16_sdwa v122,v123 dst_sel:WORD_1 dst_unused:UNUSED_PRESERVE src0_sel:WORD_1
v_mfma_f32_16x16x32_bf16 v[148:151],v[68:71],v[40:43],v[116:119]
ds_read_b64_tr_b16 v[96:97],v6 offset:12384
ds_read_u16_d16_hi v135,v135
v_pk_fma_f16 v120,v120,s55,s55
v_mfma_f32_16x16x32_bf16 v[144:147],v[72:75],v[48:51],v[144:147]
ds_read_b64_tr_b16 v[98:99],v6 offset:13920
v_rcp_f16 v121,v120
v_pk_fma_f16 v122,v122,s55,s55
v_mfma_f32_16x16x32_bf16 v[152:155],v[72:75],v[52:55],v[152:155]
v_rcp_f16 v123,v122
v_mfma_f32_16x16x32_bf16 v[156:159],v[76:79],v[52:55],v[156:159]
ds_read_u16 v136,v136
v_rcp_f16_sdwa v121,v120 dst_sel:WORD_1 dst_unused:UNUSED_PRESERVE src0_sel:WORD_1
v_mfma_f32_16x16x32_bf16 v[148:151],v[76:79],v[48:51],v[148:151]
ds_read_b64_tr_b16 v[100:101],v6 offset:12392
ds_read_b64_tr_b16 v[102:103],v6 offset:13928
v_rcp_f16_sdwa v123,v122 dst_sel:WORD_1 dst_unused:UNUSED_PRESERVE src0_sel:WORD_1
v_mfma_f32_16x16x32_bf16 v[144:147],v[80:83],v[56:59],v[144:147]
s_waitcnt lgkmcnt(11)
ds_read_u16_d16_hi v138,v138
v_lshl_add_u64 v[128:129],v[128:129],0,v[130:131]
v_fma_mix_f32 v140,v140,v121,0 op_sel_hi:[0,1,0]
v_fma_mix_f32 v142,v142,v121,0 op_sel:[0,1,0] op_sel_hi:[0,1,0]
v_mfma_f32_16x16x32_bf16 v[152:155],v[80:83],v[60:63],v[152:155]
ds_read_b64_tr_b16 v[104:105],v6 offset:12448
ds_read_b64_tr_b16 v[106:107],v6 offset:13984
v_fma_mix_f32 v141,v141,v123,0 op_sel_hi:[0,1,0]
v_mfma_f32_16x16x32_bf16 v[156:159],v[84:87],v[60:63],v[156:159]
s_waitcnt lgkmcnt(7)
ds_read_u16 v137,v137
v_lshl_add_u64 v[130:131],v[132:133],0,v[134:135]
v_mfma_f32_16x16x32_bf16 v[148:151],v[84:87],v[56:59],v[148:151]
ds_read_u16_d16_hi v139,v139
v_fma_mix_f32 v143,v143,v123,0 op_sel:[0,1,0] op_sel_hi:[0,1,0]
v_lshlrev_b32_sdwa v144,1,v144 dst_sel:DWORD dst_unused:UNUSED_PAD src0_sel:DWORD src1_sel:WORD_1
v_lshlrev_b32_sdwa v146,1,v146 dst_sel:DWORD dst_unused:UNUSED_PAD src0_sel:DWORD src1_sel:WORD_1
v_mfma_f32_16x16x32_bf16 v[160:163],v[88:91],v[128:131],v[160:163]
ds_read_b64_tr_b16 v[108:109],v6 offset:12456
ds_read_b64_tr_b16 v[110:111],v6 offset:13992
v_lshlrev_b32_sdwa v145,1,v145 dst_sel:DWORD dst_unused:UNUSED_PAD src0_sel:DWORD src1_sel:WORD_1
v_fma_mixlo_f16 v122,v157,s99,0
v_lshlrev_b32_sdwa v147,1,v147 dst_sel:DWORD dst_unused:UNUSED_PAD src0_sel:DWORD src1_sel:WORD_1
v_lshlrev_b32_sdwa v148,1,v148 dst_sel:DWORD dst_unused:UNUSED_PAD src0_sel:DWORD src1_sel:WORD_1
v_mfma_f32_16x16x32_bf16 v[168:171],v[92:95],v[128:131],v[168:171]
s_waitcnt lgkmcnt(10)
ds_read_b64_tr_b16 v[64:65],v4 offset:12320
ds_read_b64_tr_b16 v[66:67],v4 offset:14368
v_fma_mixlo_f16 v120,v156,s99,0
v_mfma_f32_16x16x32_bf16 v[176:179],v[96:99],v[128:131],v[176:179]
s_waitcnt lgkmcnt(9)
ds_read_b128 v[112:115],v7 offset:0
ds_read_u16 v144,v144
v_lshlrev_b32_sdwa v150,1,v150 dst_sel:DWORD dst_unused:UNUSED_PAD src0_sel:DWORD src1_sel:WORD_1
v_mfma_f32_16x16x32_bf16 v[184:187],v[100:103],v[128:131],v[184:187]
s_waitcnt lgkmcnt(6)
ds_read_u16_d16_hi v146,v146
v_lshl_add_u64 v[136:137],v[136:137],0,v[138:139]
v_mfma_f32_16x16x32_bf16 v[192:195],v[104:107],v[128:131],v[192:195]
s_waitcnt lgkmcnt(5)
ds_read_b64_tr_b16 v[68:69],v5 offset:12320
v_fma_mixhi_f16 v120,v158,s99,0
v_cvt_pk_bf16_f32 v138,v140,v142
v_cvt_pk_bf16_f32 v139,v141,v143
v_mfma_f32_16x16x32_bf16 v[200:203],v[108:111],v[128:131],v[200:203]
ds_read_b64_tr_b16 v[70:71],v5 offset:14368
ds_read_b128 v[116:119],v7 offset:32
v_fma_mixhi_f16 v122,v159,s99,0
v_mfma_f32_16x16x32_bf16 v[164:167],v[88:91],v[136:139],v[164:167]
v_lshlrev_b32_sdwa v149,1,v149 dst_sel:DWORD dst_unused:UNUSED_PAD src0_sel:DWORD src1_sel:WORD_1
v_lshlrev_b32_sdwa v151,1,v151 dst_sel:DWORD dst_unused:UNUSED_PAD src0_sel:DWORD src1_sel:WORD_1
v_mfma_f32_16x16x32_bf16 v[172:175],v[92:95],v[136:139],v[172:175]
ds_read_b64_tr_b16 v[72:73],v4 offset:16416
ds_read_b64_tr_b16 v[74:75],v4 offset:18464
s_mov_b32 s86,0xbe801d5a
v_pk_fma_f16 v123,v122,v122,s54
v_pk_fma_f16 v121,v120,v120,s54
v_mfma_f32_16x16x32_bf16 v[180:183],v[96:99],v[136:139],v[180:183]
ds_read_u16 v145,v145
v_pk_mul_f16 v121,v120,v121
v_pk_mul_f16 v123,v122,v123
v_mfma_f32_16x16x32_bf16 v[188:191],v[100:103],v[136:139],v[188:191]
ds_read_b64_tr_b16 v[76:77],v5 offset:16416
v_lshlrev_b32_sdwa v152,1,v152 dst_sel:DWORD dst_unused:UNUSED_PAD src0_sel:DWORD src1_sel:WORD_1
v_mfma_f32_16x16x32_bf16 v[196:199],v[104:107],v[136:139],v[196:199]
ds_read_u16_d16_hi v147,v147
v_lshlrev_b32_sdwa v154,1,v154 dst_sel:DWORD dst_unused:UNUSED_PAD src0_sel:DWORD src1_sel:WORD_1
v_exp_f16 v120,v121
v_mfma_f32_16x16x32_bf16 v[204:207],v[108:111],v[136:139],v[204:207]
s_waitcnt lgkmcnt(10)
ds_read_b64_tr_b16 v[78:79],v5 offset:18464
v_exp_f16 v122,v123
v_mfma_f32_16x16x32_bf16 v[128:131],v[64:67],v[16:19],v[112:115]
ds_read_u16 v148,v148
ds_read_u16_d16_hi v150,v150
v_lshlrev_b32_sdwa v153,1,v153 dst_sel:DWORD dst_unused:UNUSED_PAD src0_sel:DWORD src1_sel:WORD_1
v_exp_f16_sdwa v122,v123 dst_sel:WORD_1 dst_unused:UNUSED_PRESERVE src0_sel:WORD_1
v_mfma_f32_16x16x32_bf16 v[136:139],v[64:67],v[20:23],v[112:115]
s_waitcnt lgkmcnt(8)
v_lshlrev_b32_sdwa v155,1,v155 dst_sel:DWORD dst_unused:UNUSED_PAD src0_sel:DWORD src1_sel:WORD_1
v_exp_f16_sdwa v120,v121 dst_sel:WORD_1 dst_unused:UNUSED_PRESERVE src0_sel:WORD_1
v_mfma_f32_16x16x32_bf16 v[140:143],v[68:71],v[20:23],v[116:119]
ds_read_b64_tr_b16 v[80:81],v4 offset:20512
ds_read_b64_tr_b16 v[82:83],v4 offset:22560
v_pk_fma_f16 v120,v120,s55,s55
v_mfma_f32_16x16x32_bf16 v[132:135],v[68:71],v[16:19],v[116:119]
s_waitcnt lgkmcnt(8)
ds_read_u16 v149,v149
v_pk_fma_f16 v122,v122,s55,s55
v_mfma_f32_16x16x32_bf16 v[128:131],v[72:75],v[24:27],v[128:131]
ds_read_b64_tr_b16 v[84:85],v5 offset:20512
ds_read_u16_d16_hi v151,v151
v_rcp_f16 v121,v120
v_mfma_f32_16x16x32_bf16 v[136:139],v[72:75],v[28:31],v[136:139]
s_waitcnt lgkmcnt(7)
ds_read_b64_tr_b16 v[86:87],v5 offset:22560
v_rcp_f16 v123,v122
v_rcp_f16_sdwa v121,v120 dst_sel:WORD_1 dst_unused:UNUSED_PRESERVE src0_sel:WORD_1
v_mfma_f32_16x16x32_bf16 v[140:143],v[76:79],v[28:31],v[140:143]
ds_read_u16 v152,v152
v_mfma_f32_16x16x32_bf16 v[132:135],v[76:79],v[24:27],v[132:135]
s_waitcnt lgkmcnt(5)
v_rcp_f16_sdwa v123,v122 dst_sel:WORD_1 dst_unused:UNUSED_PRESERVE src0_sel:WORD_1
v_mfma_f32_16x16x32_bf16 v[128:131],v[80:83],v[32:35],v[128:131]
ds_read_u16_d16_hi v154,v154
v_fma_mix_f32 v159,v159,v123,0 op_sel:[0,1,0] op_sel_hi:[0,1,0]
v_lshl_add_u64 v[144:145],v[144:145],0,v[146:147]
v_fma_mix_f32 v156,v156,v121,0 op_sel_hi:[0,1,0]
v_mfma_f32_16x16x32_bf16 v[136:139],v[80:83],v[36:39],v[136:139]
s_waitcnt lgkmcnt(2)
v_fma_mix_f32 v158,v158,v121,0 op_sel:[0,1,0] op_sel_hi:[0,1,0]
v_lshl_add_u64 v[146:147],v[148:149],0,v[150:151]
v_mfma_f32_16x16x32_bf16 v[140:143],v[84:87],v[36:39],v[140:143]
ds_read_u16 v153,v153
v_fma_mix_f32 v157,v157,v123,0 op_sel_hi:[0,1,0]
v_mfma_f32_16x16x32_bf16 v[132:135],v[84:87],v[32:35],v[132:135]
v_lshlrev_b32_sdwa v128,1,v128 dst_sel:DWORD dst_unused:UNUSED_PAD src0_sel:DWORD src1_sel:WORD_1
v_lshlrev_b32_sdwa v130,1,v130 dst_sel:DWORD dst_unused:UNUSED_PAD src0_sel:DWORD src1_sel:WORD_1
v_mfma_f32_16x16x32_bf16 v[208:211],v[88:91],v[144:147],v[208:211]
ds_read_u16_d16_hi v155,v155
s_nop 0
v_fma_mixlo_f16 v120,v140,s99,0
v_lshlrev_b32_sdwa v129,1,v129 dst_sel:DWORD dst_unused:UNUSED_PAD src0_sel:DWORD src1_sel:WORD_1
v_lshlrev_b32_sdwa v131,1,v131 dst_sel:DWORD dst_unused:UNUSED_PAD src0_sel:DWORD src1_sel:WORD_1
v_mfma_f32_16x16x32_bf16 v[216:219],v[92:95],v[144:147],v[216:219]
v_fma_mixlo_f16 v122,v141,s99,0
v_lshlrev_b32_sdwa v132,1,v132 dst_sel:DWORD dst_unused:UNUSED_PAD src0_sel:DWORD src1_sel:WORD_1
v_mfma_f32_16x16x32_bf16 v[224:227],v[96:99],v[144:147],v[224:227]
v_lshlrev_b32_sdwa v134,1,v134 dst_sel:DWORD dst_unused:UNUSED_PAD src0_sel:DWORD src1_sel:WORD_1
v_mfma_f32_16x16x32_bf16 v[232:235],v[100:103],v[144:147],v[232:235]
s_waitcnt lgkmcnt(0)
ds_read_u16 v128,v128
v_lshl_add_u64 v[152:153],v[152:153],0,v[154:155]
v_mfma_f32_16x16x32_bf16 v[240:243],v[104:107],v[144:147],v[240:243]
ds_read_u16_d16_hi v130,v130
v_cvt_pk_bf16_f32 v155,v157,v159
v_cvt_pk_bf16_f32 v154,v156,v158
v_mfma_f32_16x16x32_bf16 v[248:251],v[108:111],v[144:147],v[248:251]
v_fma_mixhi_f16 v120,v142,s99,0
v_fma_mixhi_f16 v122,v143,s99,0
v_mfma_f32_16x16x32_bf16 v[212:215],v[88:91],v[152:155],v[212:215]
ds_read_u16 v129,v129
v_lshlrev_b32_sdwa v133,1,v133 dst_sel:DWORD dst_unused:UNUSED_PAD src0_sel:DWORD src1_sel:WORD_1
v_lshlrev_b32_sdwa v135,1,v135 dst_sel:DWORD dst_unused:UNUSED_PAD src0_sel:DWORD src1_sel:WORD_1
v_mfma_f32_16x16x32_bf16 v[220:223],v[92:95],v[152:155],v[220:223]
v_pk_fma_f16 v123,v122,v122,s54
v_lshlrev_b32_sdwa v136,1,v136 dst_sel:DWORD dst_unused:UNUSED_PAD src0_sel:DWORD src1_sel:WORD_1
v_mfma_f32_16x16x32_bf16 v[228:231],v[96:99],v[152:155],v[228:231]
ds_read_b64_tr_b16 v[88:89],v6 offset:18480
ds_read_b64_tr_b16 v[90:91],v6 offset:20016
v_pk_fma_f16 v121,v120,v120,s54
v_pk_mul_f16 v121,v120,v121
v_pk_mul_f16 v123,v122,v123
v_mfma_f32_16x16x32_bf16 v[236:239],v[100:103],v[152:155],v[236:239]
s_setprio 2
ds_read_u16_d16_hi v131,v131
v_mfma_f32_16x16x32_bf16 v[244:247],v[104:107],v[152:155],v[244:247]
ds_read_u16 v132,v132
v_exp_f16 v120,v121
v_mfma_f32_16x16x32_bf16 v[252:255],v[108:111],v[152:155],v[252:255]
ds_read_b64_tr_b16 v[92:93],v6 offset:18488
v_lshlrev_b32_sdwa v138,1,v138 dst_sel:DWORD dst_unused:UNUSED_PAD src0_sel:DWORD src1_sel:WORD_1
v_exp_f16 v122,v123
v_mfma_f32_16x16x32_bf16 v[144:147],v[64:67],v[40:43],v[112:115]
ds_read_u16_d16_hi v134,v134
v_lshlrev_b32_sdwa v137,1,v137 dst_sel:DWORD dst_unused:UNUSED_PAD src0_sel:DWORD src1_sel:WORD_1
v_lshlrev_b32_sdwa v139,1,v139 dst_sel:DWORD dst_unused:UNUSED_PAD src0_sel:DWORD src1_sel:WORD_1
v_exp_f16_sdwa v120,v121 dst_sel:WORD_1 dst_unused:UNUSED_PRESERVE src0_sel:WORD_1
v_mfma_f32_16x16x32_bf16 v[152:155],v[64:67],v[44:47],v[112:115]
ds_read_b64_tr_b16 v[94:95],v6 offset:20024
v_mfma_f32_16x16x32_bf16 v[156:159],v[68:71],v[44:47],v[116:119]
ds_read_u16 v133,v133
v_exp_f16_sdwa v122,v123 dst_sel:WORD_1 dst_unused:UNUSED_PRESERVE src0_sel:WORD_1
v_mfma_f32_16x16x32_bf16 v[148:151],v[68:71],v[40:43],v[116:119]
ds_read_b64_tr_b16 v[96:97],v6 offset:18544
ds_read_u16_d16_hi v135,v135
v_pk_fma_f16 v120,v120,s55,s55
v_mfma_f32_16x16x32_bf16 v[144:147],v[72:75],v[48:51],v[144:147]
ds_read_b64_tr_b16 v[98:99],v6 offset:20080
v_rcp_f16 v121,v120
v_pk_fma_f16 v122,v122,s55,s55
v_mfma_f32_16x16x32_bf16 v[152:155],v[72:75],v[52:55],v[152:155]
v_rcp_f16 v123,v122
v_mfma_f32_16x16x32_bf16 v[156:159],v[76:79],v[52:55],v[156:159]
ds_read_u16 v136,v136
v_rcp_f16_sdwa v121,v120 dst_sel:WORD_1 dst_unused:UNUSED_PRESERVE src0_sel:WORD_1
v_mfma_f32_16x16x32_bf16 v[148:151],v[76:79],v[48:51],v[148:151]
ds_read_b64_tr_b16 v[100:101],v6 offset:18552
ds_read_b64_tr_b16 v[102:103],v6 offset:20088
v_rcp_f16_sdwa v123,v122 dst_sel:WORD_1 dst_unused:UNUSED_PRESERVE src0_sel:WORD_1
v_mfma_f32_16x16x32_bf16 v[144:147],v[80:83],v[56:59],v[144:147]
s_waitcnt lgkmcnt(11)
ds_read_u16_d16_hi v138,v138
v_lshl_add_u64 v[128:129],v[128:129],0,v[130:131]
v_fma_mix_f32 v140,v140,v121,0 op_sel_hi:[0,1,0]
v_fma_mix_f32 v142,v142,v121,0 op_sel:[0,1,0] op_sel_hi:[0,1,0]
v_mfma_f32_16x16x32_bf16 v[152:155],v[80:83],v[60:63],v[152:155]
ds_read_b64_tr_b16 v[104:105],v6 offset:18608
ds_read_b64_tr_b16 v[106:107],v6 offset:20144
v_fma_mix_f32 v141,v141,v123,0 op_sel_hi:[0,1,0]
v_mfma_f32_16x16x32_bf16 v[156:159],v[84:87],v[60:63],v[156:159]
s_waitcnt lgkmcnt(7)
ds_read_u16 v137,v137
v_lshl_add_u64 v[130:131],v[132:133],0,v[134:135]
v_mfma_f32_16x16x32_bf16 v[148:151],v[84:87],v[56:59],v[148:151]
ds_read_u16_d16_hi v139,v139
v_fma_mix_f32 v143,v143,v123,0 op_sel:[0,1,0] op_sel_hi:[0,1,0]
v_lshlrev_b32_sdwa v144,1,v144 dst_sel:DWORD dst_unused:UNUSED_PAD src0_sel:DWORD src1_sel:WORD_1
v_lshlrev_b32_sdwa v146,1,v146 dst_sel:DWORD dst_unused:UNUSED_PAD src0_sel:DWORD src1_sel:WORD_1
v_mfma_f32_16x16x32_bf16 v[160:163],v[88:91],v[128:131],v[160:163]
ds_read_b64_tr_b16 v[108:109],v6 offset:18616
ds_read_b64_tr_b16 v[110:111],v6 offset:20152
v_lshlrev_b32_sdwa v145,1,v145 dst_sel:DWORD dst_unused:UNUSED_PAD src0_sel:DWORD src1_sel:WORD_1
v_fma_mixlo_f16 v122,v157,s99,0
v_lshlrev_b32_sdwa v147,1,v147 dst_sel:DWORD dst_unused:UNUSED_PAD src0_sel:DWORD src1_sel:WORD_1
v_lshlrev_b32_sdwa v148,1,v148 dst_sel:DWORD dst_unused:UNUSED_PAD src0_sel:DWORD src1_sel:WORD_1
v_mfma_f32_16x16x32_bf16 v[168:171],v[92:95],v[128:131],v[168:171]
s_waitcnt lgkmcnt(10)
ds_read_b64_tr_b16 v[64:65],v4 offset:12832
ds_read_b64_tr_b16 v[66:67],v4 offset:14880
v_fma_mixlo_f16 v120,v156,s99,0
v_mfma_f32_16x16x32_bf16 v[176:179],v[96:99],v[128:131],v[176:179]
s_waitcnt lgkmcnt(9)
ds_read_b128 v[112:115],v7 offset:128
ds_read_u16 v144,v144
v_lshlrev_b32_sdwa v150,1,v150 dst_sel:DWORD dst_unused:UNUSED_PAD src0_sel:DWORD src1_sel:WORD_1
v_mfma_f32_16x16x32_bf16 v[184:187],v[100:103],v[128:131],v[184:187]
s_waitcnt lgkmcnt(6)
s_setprio 1
ds_read_u16_d16_hi v146,v146
v_lshl_add_u64 v[136:137],v[136:137],0,v[138:139]
v_mfma_f32_16x16x32_bf16 v[192:195],v[104:107],v[128:131],v[192:195]
s_waitcnt lgkmcnt(5)
ds_read_b64_tr_b16 v[68:69],v5 offset:12832
v_fma_mixhi_f16 v120,v158,s99,0
v_cvt_pk_bf16_f32 v138,v140,v142
v_cvt_pk_bf16_f32 v139,v141,v143
v_mfma_f32_16x16x32_bf16 v[200:203],v[108:111],v[128:131],v[200:203]
ds_read_b64_tr_b16 v[70:71],v5 offset:14880
ds_read_b128 v[116:119],v7 offset:160
v_fma_mixhi_f16 v122,v159,s99,0
v_mfma_f32_16x16x32_bf16 v[164:167],v[88:91],v[136:139],v[164:167]
v_lshlrev_b32_sdwa v149,1,v149 dst_sel:DWORD dst_unused:UNUSED_PAD src0_sel:DWORD src1_sel:WORD_1
v_lshlrev_b32_sdwa v151,1,v151 dst_sel:DWORD dst_unused:UNUSED_PAD src0_sel:DWORD src1_sel:WORD_1
v_mfma_f32_16x16x32_bf16 v[172:175],v[92:95],v[136:139],v[172:175]
ds_read_b64_tr_b16 v[72:73],v4 offset:16928
ds_read_b64_tr_b16 v[74:75],v4 offset:18976
v_pk_fma_f16 v123,v122,v122,s54
v_pk_fma_f16 v121,v120,v120,s54
v_mfma_f32_16x16x32_bf16 v[180:183],v[96:99],v[136:139],v[180:183]
ds_read_u16 v145,v145
v_pk_mul_f16 v121,v120,v121
v_pk_mul_f16 v123,v122,v123
v_mfma_f32_16x16x32_bf16 v[188:191],v[100:103],v[136:139],v[188:191]
ds_read_b64_tr_b16 v[76:77],v5 offset:16928
v_lshlrev_b32_sdwa v152,1,v152 dst_sel:DWORD dst_unused:UNUSED_PAD src0_sel:DWORD src1_sel:WORD_1
v_mfma_f32_16x16x32_bf16 v[196:199],v[104:107],v[136:139],v[196:199]
ds_read_u16_d16_hi v147,v147
v_lshlrev_b32_sdwa v154,1,v154 dst_sel:DWORD dst_unused:UNUSED_PAD src0_sel:DWORD src1_sel:WORD_1
v_exp_f16 v120,v121
v_mfma_f32_16x16x32_bf16 v[204:207],v[108:111],v[136:139],v[204:207]
s_waitcnt lgkmcnt(10)
ds_read_b64_tr_b16 v[78:79],v5 offset:18976
v_exp_f16 v122,v123
v_mfma_f32_16x16x32_bf16 v[128:131],v[64:67],v[16:19],v[112:115]
ds_read_u16 v148,v148
ds_read_u16_d16_hi v150,v150
v_lshlrev_b32_sdwa v153,1,v153 dst_sel:DWORD dst_unused:UNUSED_PAD src0_sel:DWORD src1_sel:WORD_1
v_exp_f16_sdwa v122,v123 dst_sel:WORD_1 dst_unused:UNUSED_PRESERVE src0_sel:WORD_1
v_mfma_f32_16x16x32_bf16 v[136:139],v[64:67],v[20:23],v[112:115]
s_waitcnt lgkmcnt(8)
s_setprio 0
v_lshlrev_b32_sdwa v155,1,v155 dst_sel:DWORD dst_unused:UNUSED_PAD src0_sel:DWORD src1_sel:WORD_1
v_exp_f16_sdwa v120,v121 dst_sel:WORD_1 dst_unused:UNUSED_PRESERVE src0_sel:WORD_1
v_mfma_f32_16x16x32_bf16 v[140:143],v[68:71],v[20:23],v[116:119]
ds_read_b64_tr_b16 v[80:81],v4 offset:21024
ds_read_b64_tr_b16 v[82:83],v4 offset:23072
v_pk_fma_f16 v120,v120,s55,s55
v_mfma_f32_16x16x32_bf16 v[132:135],v[68:71],v[16:19],v[116:119]
s_waitcnt lgkmcnt(8)
ds_read_u16 v149,v149
v_pk_fma_f16 v122,v122,s55,s55
v_mfma_f32_16x16x32_bf16 v[128:131],v[72:75],v[24:27],v[128:131]
ds_read_b64_tr_b16 v[84:85],v5 offset:21024
ds_read_u16_d16_hi v151,v151
v_rcp_f16 v121,v120
v_mfma_f32_16x16x32_bf16 v[136:139],v[72:75],v[28:31],v[136:139]
s_waitcnt lgkmcnt(7)
ds_read_b64_tr_b16 v[86:87],v5 offset:23072
v_rcp_f16 v123,v122
v_rcp_f16_sdwa v121,v120 dst_sel:WORD_1 dst_unused:UNUSED_PRESERVE src0_sel:WORD_1
v_mfma_f32_16x16x32_bf16 v[140:143],v[76:79],v[28:31],v[140:143]
ds_read_u16 v152,v152
v_mfma_f32_16x16x32_bf16 v[132:135],v[76:79],v[24:27],v[132:135]
s_waitcnt lgkmcnt(5)
v_rcp_f16_sdwa v123,v122 dst_sel:WORD_1 dst_unused:UNUSED_PRESERVE src0_sel:WORD_1
v_mfma_f32_16x16x32_bf16 v[128:131],v[80:83],v[32:35],v[128:131]
ds_read_u16_d16_hi v154,v154
v_fma_mix_f32 v159,v159,v123,0 op_sel:[0,1,0] op_sel_hi:[0,1,0]
v_lshl_add_u64 v[144:145],v[144:145],0,v[146:147]
v_fma_mix_f32 v156,v156,v121,0 op_sel_hi:[0,1,0]
v_mfma_f32_16x16x32_bf16 v[136:139],v[80:83],v[36:39],v[136:139]
s_waitcnt lgkmcnt(2)
v_fma_mix_f32 v158,v158,v121,0 op_sel:[0,1,0] op_sel_hi:[0,1,0]
v_lshl_add_u64 v[146:147],v[148:149],0,v[150:151]
v_mfma_f32_16x16x32_bf16 v[140:143],v[84:87],v[36:39],v[140:143]
ds_read_u16 v153,v153
v_fma_mix_f32 v157,v157,v123,0 op_sel_hi:[0,1,0]
v_mfma_f32_16x16x32_bf16 v[132:135],v[84:87],v[32:35],v[132:135]
s_nop 0
v_lshlrev_b32_sdwa v128,1,v128 dst_sel:DWORD dst_unused:UNUSED_PAD src0_sel:DWORD src1_sel:WORD_1
v_lshlrev_b32_sdwa v130,1,v130 dst_sel:DWORD dst_unused:UNUSED_PAD src0_sel:DWORD src1_sel:WORD_1
v_mfma_f32_16x16x32_bf16 v[208:211],v[88:91],v[144:147],v[208:211]
s_waitcnt vmcnt(0)
s_barrier
s_setprio 2
v_lshlrev_b32_sdwa v129,1,v129 dst_sel:DWORD dst_unused:UNUSED_PAD src0_sel:DWORD src1_sel:WORD_1
v_lshlrev_b32_sdwa v131,1,v131 dst_sel:DWORD dst_unused:UNUSED_PAD src0_sel:DWORD src1_sel:WORD_1
v_mfma_f32_16x16x32_bf16 v[216:219],v[92:95],v[144:147],v[216:219]
ds_read_u16_d16_hi v155,v155
v_mfma_f32_16x16x32_bf16 v[224:227],v[96:99],v[144:147],v[224:227]
s_cbranch_vccz RA53_%=
s_add_u32 m0,s58,12320
s_mul_i32 s69,s59,3
s_add_u32 s69,s69,s97
buffer_load_dwordx4 v2,s[44:47],s69 offen lds
s_add_u32 m0,m0,1024
s_add_u32 s69,s69,s53
buffer_load_dwordx4 v3,s[44:47],s69 offen lds
s_add_u32 m0,m0,1024
s_add_u32 s69,s69,s53
buffer_load_dwordx4 v2,s[44:47],s69 offen lds
s_add_u32 m0,m0,s50
s_add_u32 s69,s69,s53
buffer_load_dwordx4 v3,s[44:47],s69 offen lds
RA53_%=:
v_mfma_f32_16x16x32_bf16 v[232:235],v[100:103],v[144:147],v[232:235]
s_waitcnt lgkmcnt(0)
ds_read_u16 v128,v128
v_lshl_add_u64 v[152:153],v[152:153],0,v[154:155]
v_cvt_pk_bf16_f32 v155,v157,v159
v_lshlrev_b32_sdwa v132,1,v132 dst_sel:DWORD dst_unused:UNUSED_PAD src0_sel:DWORD src1_sel:WORD_1
v_lshlrev_b32_sdwa v134,1,v134 dst_sel:DWORD dst_unused:UNUSED_PAD src0_sel:DWORD src1_sel:WORD_1
v_cvt_pk_bf16_f32 v154,v156,v158
v_mfma_f32_16x16x32_bf16 v[240:243],v[104:107],v[144:147],v[240:243]
ds_read_u16_d16_hi v130,v130
v_mfma_f32_16x16x32_bf16 v[248:251],v[108:111],v[144:147],v[248:251]
ds_read_u16 v129,v129
v_lshlrev_b32_sdwa v133,1,v133 dst_sel:DWORD dst_unused:UNUSED_PAD src0_sel:DWORD src1_sel:WORD_1
v_mfma_f32_16x16x32_bf16 v[212:215],v[88:91],v[152:155],v[212:215]
s_cbranch_vccz RA54_%=
s_add_u32 m0,m0,1024
s_add_u32 s69,s69,s53
buffer_load_dwordx4 v2,s[44:47],s69 offen lds
s_add_u32 m0,m0,1024
s_add_u32 s69,s69,s53
buffer_load_dwordx4 v3,s[44:47],s69 offen lds
s_add_u32 m0,m0,s50
s_add_u32 s69,s69,s53
buffer_load_dwordx4 v2,s[44:47],s69 offen lds
s_add_u32 m0,m0,1024
s_add_u32 s69,s69,s53
buffer_load_dwordx4 v3,s[44:47],s69 offen lds
RA54_%=:
v_lshlrev_b32_sdwa v135,1,v135 dst_sel:DWORD dst_unused:UNUSED_PAD src0_sel:DWORD src1_sel:WORD_1
v_mfma_f32_16x16x32_bf16 v[220:223],v[92:95],v[152:155],v[220:223]
ds_read_u16_d16_hi v131,v131
v_lshlrev_b32_sdwa v136,1,v136 dst_sel:DWORD dst_unused:UNUSED_PAD src0_sel:DWORD src1_sel:WORD_1
v_mfma_f32_16x16x32_bf16 v[228:231],v[96:99],v[152:155],v[228:231]
ds_read_b64_tr_b16 v[88:89],v6 offset:0
ds_read_b64_tr_b16 v[90:91],v6 offset:1536
v_lshlrev_b32_sdwa v138,1,v138 dst_sel:DWORD dst_unused:UNUSED_PAD src0_sel:DWORD src1_sel:WORD_1
v_mfma_f32_16x16x32_bf16 v[236:239],v[100:103],v[152:155],v[236:239]
ds_read_u16 v132,v132
v_mfma_f32_16x16x32_bf16 v[244:247],v[104:107],v[152:155],v[244:247]
ds_read_u16_d16_hi v134,v134
v_lshlrev_b32_sdwa v137,1,v137 dst_sel:DWORD dst_unused:UNUSED_PAD src0_sel:DWORD src1_sel:WORD_1
v_lshlrev_b32_sdwa v139,1,v139 dst_sel:DWORD dst_unused:UNUSED_PAD src0_sel:DWORD src1_sel:WORD_1
v_mfma_f32_16x16x32_bf16 v[252:255],v[108:111],v[152:155],v[252:255]
s_cbranch_vccz RA55_%=
s_add_u32 m0,m0,1024
s_add_u32 s69,s69,s53
buffer_load_dwordx4 v2,s[44:47],s69 offen lds
s_add_u32 m0,m0,s50
s_add_u32 s69,s69,s53
buffer_load_dwordx4 v3,s[44:47],s69 offen lds
s_add_u32 m0,m0,1024
s_add_u32 s69,s69,s53
buffer_load_dwordx4 v2,s[44:47],s69 offen lds
s_add_u32 m0,m0,1024
s_add_u32 s69,s69,s53
buffer_load_dwordx4 v3,s[44:47],s69 offen lds
RA55_%=:
ds_read_b64_tr_b16 v[92:93],v6 offset:8
ds_read_b64_tr_b16 v[94:95],v6 offset:1544
v_mfma_f32_16x16x32_bf16 v[144:147],v[64:67],v[40:43],v[112:115]
ds_read_u16 v133,v133
v_lshlrev_b32_sdwa v140,1,v140 dst_sel:DWORD dst_unused:UNUSED_PAD src0_sel:DWORD src1_sel:WORD_1
v_lshlrev_b32_sdwa v142,1,v142 dst_sel:DWORD dst_unused:UNUSED_PAD src0_sel:DWORD src1_sel:WORD_1
v_mfma_f32_16x16x32_bf16 v[152:155],v[64:67],v[44:47],v[112:115]
ds_read_b64_tr_b16 v[96:97],v6 offset:64
v_lshlrev_b32_sdwa v141,1,v141 dst_sel:DWORD dst_unused:UNUSED_PAD src0_sel:DWORD src1_sel:WORD_1
v_lshlrev_b32_sdwa v143,1,v143 dst_sel:DWORD dst_unused:UNUSED_PAD src0_sel:DWORD src1_sel:WORD_1
v_mfma_f32_16x16x32_bf16 v[156:159],v[68:71],v[44:47],v[116:119]
ds_read_u16_d16_hi v135,v135
v_mfma_f32_16x16x32_bf16 v[148:151],v[68:71],v[40:43],v[116:119]
ds_read_u16 v136,v136
ds_read_b64_tr_b16 v[98:99],v6 offset:1600
v_mfma_f32_16x16x32_bf16 v[144:147],v[72:75],v[48:51],v[144:147]
ds_read_u16_d16_hi v138,v138
v_mfma_f32_16x16x32_bf16 v[152:155],v[72:75],v[52:55],v[152:155]
s_cbranch_vccz RA56_%=
s_add_u32 m0,s100,0
s_mul_i32 s69,s59,9
s_add_u32 s69,s69,s97
buffer_load_dwordx4 v2,s[44:47],s69 offen lds
s_add_u32 m0,m0,1024
s_add_u32 s69,s69,s53
buffer_load_dwordx4 v3,s[44:47],s69 offen lds
s_add_u32 m0,m0,1024
s_add_u32 s69,s69,s53
buffer_load_dwordx4 v2,s[44:47],s69 offen lds
s_add_u32 m0,m0,s50
s_add_u32 s69,s69,s53
buffer_load_dwordx4 v3,s[44:47],s69 offen lds
RA56_%=:
ds_read_u16 v137,v137
v_mfma_f32_16x16x32_bf16 v[156:159],v[76:79],v[52:55],v[156:159]
ds_read_b64_tr_b16 v[100:101],v6 offset:72
ds_read_b64_tr_b16 v[102:103],v6 offset:1608
v_mfma_f32_16x16x32_bf16 v[148:151],v[76:79],v[48:51],v[148:151]
s_waitcnt lgkmcnt(6)
ds_read_u16_d16_hi v139,v139
v_lshl_add_u64 v[128:129],v[128:129],0,v[130:131]
v_lshl_add_u64 v[130:131],v[132:133],0,v[134:135]
v_mfma_f32_16x16x32_bf16 v[144:147],v[80:83],v[56:59],v[144:147]
ds_read_b64_tr_b16 v[104:105],v6 offset:128
ds_read_b64_tr_b16 v[106:107],v6 offset:1664
v_mfma_f32_16x16x32_bf16 v[152:155],v[80:83],v[60:63],v[152:155]
ds_read_u16 v140,v140
v_mfma_f32_16x16x32_bf16 v[156:159],v[84:87],v[60:63],v[156:159]
ds_read_u16_d16_hi v142,v142
s_nop 1
v_lshlrev_b32_sdwa v144,1,v144 dst_sel:DWORD dst_unused:UNUSED_PAD src0_sel:DWORD src1_sel:WORD_1
v_mfma_f32_16x16x32_bf16 v[148:151],v[84:87],v[56:59],v[148:151]
s_cbranch_vccz RA57_%=
s_add_u32 m0,m0,1024
s_add_u32 s69,s69,s53
buffer_load_dwordx4 v2,s[44:47],s69 offen lds
s_add_u32 m0,m0,1024
s_add_u32 s69,s69,s53
buffer_load_dwordx4 v3,s[44:47],s69 offen lds
s_add_u32 m0,m0,s50
s_add_u32 s69,s69,s53
buffer_load_dwordx4 v2,s[44:47],s69 offen lds
s_add_u32 m0,m0,1024
s_add_u32 s69,s69,s53
buffer_load_dwordx4 v3,s[44:47],s69 offen lds
RA57_%=:
ds_read_b64_tr_b16 v[108:109],v6 offset:136
ds_read_b64_tr_b16 v[110:111],v6 offset:1672
v_lshlrev_b32_sdwa v146,1,v146 dst_sel:DWORD dst_unused:UNUSED_PAD src0_sel:DWORD src1_sel:WORD_1
v_mfma_f32_16x16x32_bf16 v[160:163],v[88:91],v[128:131],v[160:163]
ds_read_u16 v141,v141
ds_read_b64_tr_b16 v[64:65],v4 offset:0
v_lshlrev_b32_sdwa v145,1,v145 dst_sel:DWORD dst_unused:UNUSED_PAD src0_sel:DWORD src1_sel:WORD_1
v_mfma_f32_16x16x32_bf16 v[168:171],v[92:95],v[128:131],v[168:171]
s_waitcnt lgkmcnt(13)
ds_read_u16_d16_hi v143,v143
v_lshlrev_b32_sdwa v147,1,v147 dst_sel:DWORD dst_unused:UNUSED_PAD src0_sel:DWORD src1_sel:WORD_1
v_mfma_f32_16x16x32_bf16 v[176:179],v[96:99],v[128:131],v[176:179]
s_waitcnt lgkmcnt(10)
ds_read_b64_tr_b16 v[66:67],v4 offset:2048
v_lshlrev_b32_sdwa v148,1,v148 dst_sel:DWORD dst_unused:UNUSED_PAD src0_sel:DWORD src1_sel:WORD_1
v_mfma_f32_16x16x32_bf16 v[184:187],v[100:103],v[128:131],v[184:187]
s_waitcnt lgkmcnt(8)
ds_read_u16 v144,v144
v_lshl_add_u64 v[136:137],v[136:137],0,v[138:139]
v_lshlrev_b32_sdwa v150,1,v150 dst_sel:DWORD dst_unused:UNUSED_PAD src0_sel:DWORD src1_sel:WORD_1
v_lshlrev_b32_sdwa v149,1,v149 dst_sel:DWORD dst_unused:UNUSED_PAD src0_sel:DWORD src1_sel:WORD_1
v_mfma_f32_16x16x32_bf16 v[192:195],v[104:107],v[128:131],v[192:195]
s_waitcnt lgkmcnt(2)
s_cbranch_vccz RA58_%=
s_add_u32 m0,m0,1024
s_add_u32 s69,s69,s53
buffer_load_dwordx4 v2,s[44:47],s69 offen lds
s_add_u32 m0,m0,s50
s_add_u32 s69,s69,s53
buffer_load_dwordx4 v3,s[44:47],s69 offen lds
s_add_u32 m0,m0,1024
s_add_u32 s69,s69,s53
buffer_load_dwordx4 v2,s[44:47],s69 offen lds
s_add_u32 m0,m0,1024
s_add_u32 s69,s69,s53
buffer_load_dwordx4 v3,s[44:47],s69 offen lds
RA58_%=:
ds_read_u16_d16_hi v146,v146
v_lshl_add_u64 v[138:139],v[140:141],0,v[142:143]
v_mfma_f32_16x16x32_bf16 v[200:203],v[108:111],v[128:131],v[200:203]
ds_read_b128 v[112:115],v7 offset:256
ds_read_b64_tr_b16 v[68:69],v5 offset:0
v_mfma_f32_16x16x32_bf16 v[164:167],v[88:91],v[136:139],v[164:167]
ds_read_u16 v145,v145
v_lshlrev_b32_sdwa v151,1,v151 dst_sel:DWORD dst_unused:UNUSED_PAD src0_sel:DWORD src1_sel:WORD_1
v_lshlrev_b32_sdwa v152,1,v152 dst_sel:DWORD dst_unused:UNUSED_PAD src0_sel:DWORD src1_sel:WORD_1
v_mfma_f32_16x16x32_bf16 v[172:175],v[92:95],v[136:139],v[172:175]
ds_read_u16_d16_hi v147,v147
ds_read_b64_tr_b16 v[70:71],v5 offset:2048
v_mfma_f32_16x16x32_bf16 v[180:183],v[96:99],v[136:139],v[180:183]
ds_read_b128 v[116:119],v7 offset:288
v_lshlrev_b32_sdwa v154,1,v154 dst_sel:DWORD dst_unused:UNUSED_PAD src0_sel:DWORD src1_sel:WORD_1
v_mfma_f32_16x16x32_bf16 v[188:191],v[100:103],v[136:139],v[188:191]
ds_read_u16 v148,v148
ds_read_b64_tr_b16 v[72:73],v4 offset:4096
v_lshlrev_b32_sdwa v153,1,v153 dst_sel:DWORD dst_unused:UNUSED_PAD src0_sel:DWORD src1_sel:WORD_1
v_lshlrev_b32_sdwa v155,1,v155 dst_sel:DWORD dst_unused:UNUSED_PAD src0_sel:DWORD src1_sel:WORD_1
v_mfma_f32_16x16x32_bf16 v[196:199],v[104:107],v[136:139],v[196:199]
ds_read_u16_d16_hi v150,v150
v_mfma_f32_16x16x32_bf16 v[204:207],v[108:111],v[136:139],v[204:207]
s_waitcnt lgkmcnt(8)
ds_read_b64_tr_b16 v[74:75],v4 offset:6144
ds_read_b64_tr_b16 v[76:77],v5 offset:4096
v_lshlrev_b32_sdwa v156,1,v156 dst_sel:DWORD dst_unused:UNUSED_PAD src0_sel:DWORD src1_sel:WORD_1
v_mfma_f32_16x16x32_bf16 v[128:131],v[64:67],v[16:19],v[112:115]
ds_read_u16 v149,v149
v_lshlrev_b32_sdwa v158,1,v158 dst_sel:DWORD dst_unused:UNUSED_PAD src0_sel:DWORD src1_sel:WORD_1
v_mfma_f32_16x16x32_bf16 v[136:139],v[64:67],v[20:23],v[112:115]
s_waitcnt lgkmcnt(6)
ds_read_b64_tr_b16 v[78:79],v5 offset:6144
ds_read_b64_tr_b16 v[80:81],v4 offset:8192
v_lshlrev_b32_sdwa v157,1,v157 dst_sel:DWORD dst_unused:UNUSED_PAD src0_sel:DWORD src1_sel:WORD_1
v_mfma_f32_16x16x32_bf16 v[140:143],v[68:71],v[20:23],v[116:119]
ds_read_u16_d16_hi v151,v151
v_lshlrev_b32_sdwa v159,1,v159 dst_sel:DWORD dst_unused:UNUSED_PAD src0_sel:DWORD src1_sel:WORD_1
v_mfma_f32_16x16x32_bf16 v[132:135],v[68:71],v[16:19],v[116:119]
s_waitcnt lgkmcnt(5)
ds_read_u16 v152,v152
ds_read_b64_tr_b16 v[82:83],v4 offset:10240
v_mfma_f32_16x16x32_bf16 v[128:131],v[72:75],v[24:27],v[128:131]
ds_read_b64_tr_b16 v[84:85],v5 offset:8192
ds_read_b64_tr_b16 v[86:87],v5 offset:10240
v_lshl_add_u64 v[144:145],v[144:145],0,v[146:147]
v_mfma_f32_16x16x32_bf16 v[136:139],v[72:75],v[28:31],v[136:139]
s_waitcnt lgkmcnt(6)
ds_read_u16_d16_hi v154,v154
v_mfma_f32_16x16x32_bf16 v[140:143],v[76:79],v[28:31],v[140:143]
ds_read_u16 v153,v153
v_mfma_f32_16x16x32_bf16 v[132:135],v[76:79],v[24:27],v[132:135]
s_waitcnt lgkmcnt(4)
ds_read_u16_d16_hi v155,v155
v_mfma_f32_16x16x32_bf16 v[128:131],v[80:83],v[32:35],v[128:131]
v_lshl_add_u64 v[146:147],v[148:149],0,v[150:151]
v_mfma_f32_16x16x32_bf16 v[136:139],v[80:83],v[36:39],v[136:139]
s_waitcnt lgkmcnt(3)
ds_read_u16 v156,v156
v_mfma_f32_16x16x32_bf16 v[140:143],v[84:87],v[36:39],v[140:143]
ds_read_u16_d16_hi v158,v158
v_mfma_f32_16x16x32_bf16 v[132:135],v[84:87],v[32:35],v[132:135]
ds_read_u16 v157,v157
v_lshlrev_b32_sdwa v128,1,v128 dst_sel:DWORD dst_unused:UNUSED_PAD src0_sel:DWORD src1_sel:WORD_1
v_lshlrev_b32_sdwa v130,1,v130 dst_sel:DWORD dst_unused:UNUSED_PAD src0_sel:DWORD src1_sel:WORD_1
v_mfma_f32_16x16x32_bf16 v[208:211],v[88:91],v[144:147],v[208:211]
v_lshlrev_b32_sdwa v129,1,v129 dst_sel:DWORD dst_unused:UNUSED_PAD src0_sel:DWORD src1_sel:WORD_1
v_lshlrev_b32_sdwa v131,1,v131 dst_sel:DWORD dst_unused:UNUSED_PAD src0_sel:DWORD src1_sel:WORD_1
v_mfma_f32_16x16x32_bf16 v[216:219],v[92:95],v[144:147],v[216:219]
ds_read_u16_d16_hi v159,v159
v_mfma_f32_16x16x32_bf16 v[224:227],v[96:99],v[144:147],v[224:227]
v_mfma_f32_16x16x32_bf16 v[232:235],v[100:103],v[144:147],v[232:235]
s_waitcnt lgkmcnt(4)
ds_read_u16 v128,v128
v_lshl_add_u64 v[152:153],v[152:153],0,v[154:155]
v_lshlrev_b32_sdwa v132,1,v132 dst_sel:DWORD dst_unused:UNUSED_PAD src0_sel:DWORD src1_sel:WORD_1
v_lshlrev_b32_sdwa v134,1,v134 dst_sel:DWORD dst_unused:UNUSED_PAD src0_sel:DWORD src1_sel:WORD_1
v_mfma_f32_16x16x32_bf16 v[240:243],v[104:107],v[144:147],v[240:243]
s_waitcnt lgkmcnt(1)
ds_read_u16_d16_hi v130,v130
v_lshl_add_u64 v[154:155],v[156:157],0,v[158:159]
v_mfma_f32_16x16x32_bf16 v[248:251],v[108:111],v[144:147],v[248:251]
ds_read_u16 v129,v129
v_lshlrev_b32_sdwa v133,1,v133 dst_sel:DWORD dst_unused:UNUSED_PAD src0_sel:DWORD src1_sel:WORD_1
v_mfma_f32_16x16x32_bf16 v[212:215],v[88:91],v[152:155],v[212:215]
v_lshlrev_b32_sdwa v135,1,v135 dst_sel:DWORD dst_unused:UNUSED_PAD src0_sel:DWORD src1_sel:WORD_1
v_mfma_f32_16x16x32_bf16 v[220:223],v[92:95],v[152:155],v[220:223]
ds_read_u16_d16_hi v131,v131
v_lshlrev_b32_sdwa v136,1,v136 dst_sel:DWORD dst_unused:UNUSED_PAD src0_sel:DWORD src1_sel:WORD_1
v_mfma_f32_16x16x32_bf16 v[228:231],v[96:99],v[152:155],v[228:231]
ds_read_b64_tr_b16 v[88:89],v6 offset:6160
ds_read_b64_tr_b16 v[90:91],v6 offset:7696
v_lshlrev_b32_sdwa v138,1,v138 dst_sel:DWORD dst_unused:UNUSED_PAD src0_sel:DWORD src1_sel:WORD_1
v_mfma_f32_16x16x32_bf16 v[236:239],v[100:103],v[152:155],v[236:239]
ds_read_u16 v132,v132
v_mfma_f32_16x16x32_bf16 v[244:247],v[104:107],v[152:155],v[244:247]
ds_read_u16_d16_hi v134,v134
v_lshlrev_b32_sdwa v137,1,v137 dst_sel:DWORD dst_unused:UNUSED_PAD src0_sel:DWORD src1_sel:WORD_1
v_lshlrev_b32_sdwa v139,1,v139 dst_sel:DWORD dst_unused:UNUSED_PAD src0_sel:DWORD src1_sel:WORD_1
v_mfma_f32_16x16x32_bf16 v[252:255],v[108:111],v[152:155],v[252:255]
ds_read_b64_tr_b16 v[92:93],v6 offset:6168
ds_read_b64_tr_b16 v[94:95],v6 offset:7704
v_mfma_f32_16x16x32_bf16 v[144:147],v[64:67],v[40:43],v[112:115]
ds_read_u16 v133,v133
v_lshlrev_b32_sdwa v140,1,v140 dst_sel:DWORD dst_unused:UNUSED_PAD src0_sel:DWORD src1_sel:WORD_1
v_lshlrev_b32_sdwa v142,1,v142 dst_sel:DWORD dst_unused:UNUSED_PAD src0_sel:DWORD src1_sel:WORD_1
v_mfma_f32_16x16x32_bf16 v[152:155],v[64:67],v[44:47],v[112:115]
ds_read_b64_tr_b16 v[96:97],v6 offset:6224
v_lshlrev_b32_sdwa v141,1,v141 dst_sel:DWORD dst_unused:UNUSED_PAD src0_sel:DWORD src1_sel:WORD_1
v_lshlrev_b32_sdwa v143,1,v143 dst_sel:DWORD dst_unused:UNUSED_PAD src0_sel:DWORD src1_sel:WORD_1
v_mfma_f32_16x16x32_bf16 v[156:159],v[68:71],v[44:47],v[116:119]
ds_read_u16_d16_hi v135,v135
v_mfma_f32_16x16x32_bf16 v[148:151],v[68:71],v[40:43],v[116:119]
ds_read_u16 v136,v136
ds_read_b64_tr_b16 v[98:99],v6 offset:7760
v_mfma_f32_16x16x32_bf16 v[144:147],v[72:75],v[48:51],v[144:147]
ds_read_u16_d16_hi v138,v138
v_mfma_f32_16x16x32_bf16 v[152:155],v[72:75],v[52:55],v[152:155]
ds_read_u16 v137,v137
v_mfma_f32_16x16x32_bf16 v[156:159],v[76:79],v[52:55],v[156:159]
ds_read_b64_tr_b16 v[100:101],v6 offset:6232
ds_read_b64_tr_b16 v[102:103],v6 offset:7768
s_nop 0
s_mov_b32 s86,0xbe801d5a
v_mfma_f32_16x16x32_bf16 v[148:151],v[76:79],v[48:51],v[148:151]
s_waitcnt lgkmcnt(6)
ds_read_u16_d16_hi v139,v139
v_lshl_add_u64 v[128:129],v[128:129],0,v[130:131]
v_lshl_add_u64 v[130:131],v[132:133],0,v[134:135]
v_mfma_f32_16x16x32_bf16 v[144:147],v[80:83],v[56:59],v[144:147]
ds_read_b64_tr_b16 v[104:105],v6 offset:6288
ds_read_b64_tr_b16 v[106:107],v6 offset:7824
v_mfma_f32_16x16x32_bf16 v[152:155],v[80:83],v[60:63],v[152:155]
ds_read_u16 v140,v140
v_mfma_f32_16x16x32_bf16 v[156:159],v[84:87],v[60:63],v[156:159]
ds_read_u16_d16_hi v142,v142
s_nop 1
v_lshlrev_b32_sdwa v144,1,v144 dst_sel:DWORD dst_unused:UNUSED_PAD src0_sel:DWORD src1_sel:WORD_1
v_mfma_f32_16x16x32_bf16 v[148:151],v[84:87],v[56:59],v[148:151]
ds_read_b64_tr_b16 v[108:109],v6 offset:6296
ds_read_b64_tr_b16 v[110:111],v6 offset:7832
v_lshlrev_b32_sdwa v146,1,v146 dst_sel:DWORD dst_unused:UNUSED_PAD src0_sel:DWORD src1_sel:WORD_1
v_mfma_f32_16x16x32_bf16 v[160:163],v[88:91],v[128:131],v[160:163]
ds_read_u16 v141,v141
ds_read_b64_tr_b16 v[64:65],v4 offset:512
v_lshlrev_b32_sdwa v145,1,v145 dst_sel:DWORD dst_unused:UNUSED_PAD src0_sel:DWORD src1_sel:WORD_1
v_mfma_f32_16x16x32_bf16 v[168:171],v[92:95],v[128:131],v[168:171]
s_waitcnt lgkmcnt(13)
ds_read_u16_d16_hi v143,v143
v_lshlrev_b32_sdwa v147,1,v147 dst_sel:DWORD dst_unused:UNUSED_PAD src0_sel:DWORD src1_sel:WORD_1
v_mfma_f32_16x16x32_bf16 v[176:179],v[96:99],v[128:131],v[176:179]
s_waitcnt lgkmcnt(10)
ds_read_b64_tr_b16 v[66:67],v4 offset:2560
v_lshlrev_b32_sdwa v148,1,v148 dst_sel:DWORD dst_unused:UNUSED_PAD src0_sel:DWORD src1_sel:WORD_1
v_mfma_f32_16x16x32_bf16 v[184:187],v[100:103],v[128:131],v[184:187]
s_waitcnt lgkmcnt(8)
ds_read_u16 v144,v144
v_lshl_add_u64 v[136:137],v[136:137],0,v[138:139]
v_lshlrev_b32_sdwa v150,1,v150 dst_sel:DWORD dst_unused:UNUSED_PAD src0_sel:DWORD src1_sel:WORD_1
v_lshlrev_b32_sdwa v149,1,v149 dst_sel:DWORD dst_unused:UNUSED_PAD src0_sel:DWORD src1_sel:WORD_1
v_mfma_f32_16x16x32_bf16 v[192:195],v[104:107],v[128:131],v[192:195]
s_waitcnt lgkmcnt(2)
ds_read_u16_d16_hi v146,v146
v_lshl_add_u64 v[138:139],v[140:141],0,v[142:143]
v_mfma_f32_16x16x32_bf16 v[200:203],v[108:111],v[128:131],v[200:203]
ds_read_b128 v[112:115],v7 offset:384
ds_read_b64_tr_b16 v[68:69],v5 offset:512
v_mfma_f32_16x16x32_bf16 v[164:167],v[88:91],v[136:139],v[164:167]
ds_read_u16 v145,v145
v_lshlrev_b32_sdwa v151,1,v151 dst_sel:DWORD dst_unused:UNUSED_PAD src0_sel:DWORD src1_sel:WORD_1
v_lshlrev_b32_sdwa v152,1,v152 dst_sel:DWORD dst_unused:UNUSED_PAD src0_sel:DWORD src1_sel:WORD_1
v_mfma_f32_16x16x32_bf16 v[172:175],v[92:95],v[136:139],v[172:175]
ds_read_u16_d16_hi v147,v147
ds_read_b64_tr_b16 v[70:71],v5 offset:2560
v_mfma_f32_16x16x32_bf16 v[180:183],v[96:99],v[136:139],v[180:183]
ds_read_b128 v[116:119],v7 offset:416
v_lshlrev_b32_sdwa v154,1,v154 dst_sel:DWORD dst_unused:UNUSED_PAD src0_sel:DWORD src1_sel:WORD_1
v_mfma_f32_16x16x32_bf16 v[188:191],v[100:103],v[136:139],v[188:191]
ds_read_u16 v148,v148
ds_read_b64_tr_b16 v[72:73],v4 offset:4608
v_lshlrev_b32_sdwa v153,1,v153 dst_sel:DWORD dst_unused:UNUSED_PAD src0_sel:DWORD src1_sel:WORD_1
v_lshlrev_b32_sdwa v155,1,v155 dst_sel:DWORD dst_unused:UNUSED_PAD src0_sel:DWORD src1_sel:WORD_1
v_mfma_f32_16x16x32_bf16 v[196:199],v[104:107],v[136:139],v[196:199]
ds_read_u16_d16_hi v150,v150
v_mfma_f32_16x16x32_bf16 v[204:207],v[108:111],v[136:139],v[204:207]
s_waitcnt lgkmcnt(8)
ds_read_b64_tr_b16 v[74:75],v4 offset:6656
ds_read_b64_tr_b16 v[76:77],v5 offset:4608
v_lshlrev_b32_sdwa v156,1,v156 dst_sel:DWORD dst_unused:UNUSED_PAD src0_sel:DWORD src1_sel:WORD_1
v_mfma_f32_16x16x32_bf16 v[128:131],v[64:67],v[16:19],v[112:115]
ds_read_u16 v149,v149
v_lshlrev_b32_sdwa v158,1,v158 dst_sel:DWORD dst_unused:UNUSED_PAD src0_sel:DWORD src1_sel:WORD_1
v_mfma_f32_16x16x32_bf16 v[136:139],v[64:67],v[20:23],v[112:115]
s_waitcnt lgkmcnt(6)
ds_read_b64_tr_b16 v[78:79],v5 offset:6656
ds_read_b64_tr_b16 v[80:81],v4 offset:8704
v_lshlrev_b32_sdwa v157,1,v157 dst_sel:DWORD dst_unused:UNUSED_PAD src0_sel:DWORD src1_sel:WORD_1
v_mfma_f32_16x16x32_bf16 v[140:143],v[68:71],v[20:23],v[116:119]
ds_read_u16_d16_hi v151,v151
v_lshlrev_b32_sdwa v159,1,v159 dst_sel:DWORD dst_unused:UNUSED_PAD src0_sel:DWORD src1_sel:WORD_1
v_mfma_f32_16x16x32_bf16 v[132:135],v[68:71],v[16:19],v[116:119]
s_waitcnt lgkmcnt(5)
ds_read_u16 v152,v152
ds_read_b64_tr_b16 v[82:83],v4 offset:10752
v_mfma_f32_16x16x32_bf16 v[128:131],v[72:75],v[24:27],v[128:131]
ds_read_b64_tr_b16 v[84:85],v5 offset:8704
ds_read_b64_tr_b16 v[86:87],v5 offset:10752
v_lshl_add_u64 v[144:145],v[144:145],0,v[146:147]
v_mfma_f32_16x16x32_bf16 v[136:139],v[72:75],v[28:31],v[136:139]
s_waitcnt lgkmcnt(6)
ds_read_u16_d16_hi v154,v154
v_mfma_f32_16x16x32_bf16 v[140:143],v[76:79],v[28:31],v[140:143]
ds_read_u16 v153,v153
v_mfma_f32_16x16x32_bf16 v[132:135],v[76:79],v[24:27],v[132:135]
s_waitcnt lgkmcnt(4)
ds_read_u16_d16_hi v155,v155
v_mfma_f32_16x16x32_bf16 v[128:131],v[80:83],v[32:35],v[128:131]
s_nop 0
v_lshl_add_u64 v[146:147],v[148:149],0,v[150:151]
v_mfma_f32_16x16x32_bf16 v[136:139],v[80:83],v[36:39],v[136:139]
s_waitcnt lgkmcnt(3)
ds_read_u16 v156,v156
v_mfma_f32_16x16x32_bf16 v[140:143],v[84:87],v[36:39],v[140:143]
ds_read_u16_d16_hi v158,v158
v_mfma_f32_16x16x32_bf16 v[132:135],v[84:87],v[32:35],v[132:135]
s_nop 0
ds_read_u16 v157,v157
v_lshlrev_b32_sdwa v128,1,v128 dst_sel:DWORD dst_unused:UNUSED_PAD src0_sel:DWORD src1_sel:WORD_1
v_lshlrev_b32_sdwa v130,1,v130 dst_sel:DWORD dst_unused:UNUSED_PAD src0_sel:DWORD src1_sel:WORD_1
v_mfma_f32_16x16x32_bf16 v[208:211],v[88:91],v[144:147],v[208:211]
s_waitcnt vmcnt(12)
s_barrier
s_setprio 3
v_lshlrev_b32_sdwa v129,1,v129 dst_sel:DWORD dst_unused:UNUSED_PAD src0_sel:DWORD src1_sel:WORD_1
v_lshlrev_b32_sdwa v131,1,v131 dst_sel:DWORD dst_unused:UNUSED_PAD src0_sel:DWORD src1_sel:WORD_1
v_mfma_f32_16x16x32_bf16 v[216:219],v[92:95],v[144:147],v[216:219]
ds_read_u16_d16_hi v159,v159
ds_read_u16 v128,v128
v_fma_mixlo_f16 v120,v140,s99,0
v_fma_mixlo_f16 v122,v141,s99,0
v_mfma_f32_16x16x32_bf16 v[224:227],v[96:99],v[144:147],v[224:227]
s_cbranch_vccz RA59_%=
s_add_u32 m0,s58,0
s_mul_i32 s69,s59,5
s_add_u32 s69,s69,s97
buffer_load_dwordx4 v2,s[44:47],s69 offen lds
s_add_u32 m0,m0,1024
s_add_u32 s69,s69,s53
buffer_load_dwordx4 v3,s[44:47],s69 offen lds
s_add_u32 m0,m0,1024
s_add_u32 s69,s69,s53
buffer_load_dwordx4 v2,s[44:47],s69 offen lds
s_add_u32 m0,m0,s50
s_add_u32 s69,s69,s53
buffer_load_dwordx4 v3,s[44:47],s69 offen lds
RA59_%=:
ds_read_u16_d16_hi v130,v130
v_lshlrev_b32_sdwa v132,1,v132 dst_sel:DWORD dst_unused:UNUSED_PAD src0_sel:DWORD src1_sel:WORD_1
v_lshlrev_b32_sdwa v134,1,v134 dst_sel:DWORD dst_unused:UNUSED_PAD src0_sel:DWORD src1_sel:WORD_1
v_mfma_f32_16x16x32_bf16 v[232:235],v[100:103],v[144:147],v[232:235]
s_waitcnt lgkmcnt(6)
v_lshl_add_u64 v[152:153],v[152:153],0,v[154:155]
v_mfma_f32_16x16x32_bf16 v[240:243],v[104:107],v[144:147],v[240:243]
s_waitcnt lgkmcnt(2)
v_lshl_add_u64 v[154:155],v[156:157],0,v[158:159]
v_fma_mixhi_f16 v120,v142,s99,0
v_mfma_f32_16x16x32_bf16 v[248:251],v[108:111],v[144:147],v[248:251]
ds_read_u16 v129,v129
v_fma_mixhi_f16 v122,v143,s99,0
v_mfma_f32_16x16x32_bf16 v[212:215],v[88:91],v[152:155],v[212:215]
s_cbranch_vccz RA60_%=
s_add_u32 m0,m0,1024
s_add_u32 s69,s69,s53
buffer_load_dwordx4 v2,s[44:47],s69 offen lds
s_add_u32 m0,m0,1024
s_add_u32 s69,s69,s53
buffer_load_dwordx4 v3,s[44:47],s69 offen lds
s_add_u32 m0,m0,s50
s_add_u32 s69,s69,s53
buffer_load_dwordx4 v2,s[44:47],s69 offen lds
s_add_u32 m0,m0,1024
s_add_u32 s69,s69,s53
buffer_load_dwordx4 v3,s[44:47],s69 offen lds
RA60_%=:
v_lshlrev_b32_sdwa v133,1,v133 dst_sel:DWORD dst_unused:UNUSED_PAD src0_sel:DWORD src1_sel:WORD_1
v_pk_fma_f16 v121,v120,v120,s54
v_lshlrev_b32_sdwa v135,1,v135 dst_sel:DWORD dst_unused:UNUSED_PAD src0_sel:DWORD src1_sel:WORD_1
v_mfma_f32_16x16x32_bf16 v[220:223],v[92:95],v[152:155],v[220:223]
ds_read_u16_d16_hi v131,v131
ds_read_u16 v132,v132
v_pk_fma_f16 v123,v122,v122,s54
v_mfma_f32_16x16x32_bf16 v[228:231],v[96:99],v[152:155],v[228:231]
ds_read_b64_tr_b16 v[88:89],v6 offset:12320
v_pk_mul_f16 v121,v120,v121
v_lshlrev_b32_sdwa v136,1,v136 dst_sel:DWORD dst_unused:UNUSED_PAD src0_sel:DWORD src1_sel:WORD_1
v_pk_mul_f16 v123,v122,v123
v_mfma_f32_16x16x32_bf16 v[236:239],v[100:103],v[152:155],v[236:239]
s_cbranch_vccz RA61_%=
s_add_u32 m0,m0,1024
s_add_u32 s69,s69,s53
buffer_load_dwordx4 v2,s[44:47],s69 offen lds
s_add_u32 m0,m0,s50
s_add_u32 s69,s69,s53
buffer_load_dwordx4 v3,s[44:47],s69 offen lds
s_add_u32 m0,m0,1024
s_add_u32 s69,s69,s53
buffer_load_dwordx4 v2,s[44:47],s69 offen lds
s_add_u32 m0,m0,1024
s_add_u32 s69,s69,s53
buffer_load_dwordx4 v3,s[44:47],s69 offen lds
RA61_%=:
ds_read_b64_tr_b16 v[90:91],v6 offset:13856
v_lshlrev_b32_sdwa v138,1,v138 dst_sel:DWORD dst_unused:UNUSED_PAD src0_sel:DWORD src1_sel:WORD_1
v_mfma_f32_16x16x32_bf16 v[244:247],v[104:107],v[152:155],v[244:247]
ds_read_u16_d16_hi v134,v134
v_exp_f16 v120,v121
v_mfma_f32_16x16x32_bf16 v[252:255],v[108:111],v[152:155],v[252:255]
ds_read_b64_tr_b16 v[92:93],v6 offset:12328
v_lshlrev_b32_sdwa v137,1,v137 dst_sel:DWORD dst_unused:UNUSED_PAD src0_sel:DWORD src1_sel:WORD_1
v_mfma_f32_16x16x32_bf16 v[144:147],v[64:67],v[40:43],v[112:115]
ds_read_b64_tr_b16 v[94:95],v6 offset:13864
v_lshlrev_b32_sdwa v139,1,v139 dst_sel:DWORD dst_unused:UNUSED_PAD src0_sel:DWORD src1_sel:WORD_1
v_exp_f16 v122,v123
v_mfma_f32_16x16x32_bf16 v[152:155],v[64:67],v[44:47],v[112:115]
s_cbranch_vccz RA62_%=
s_add_u32 m0,s100,12320
s_mov_b32 s69,s60
buffer_load_dwordx4 v2,s[44:47],s69 offen lds
s_add_u32 m0,m0,1024
s_add_u32 s69,s69,s53
buffer_load_dwordx4 v3,s[44:47],s69 offen lds
s_add_u32 m0,m0,1024
s_add_u32 s69,s69,s53
buffer_load_dwordx4 v2,s[44:47],s69 offen lds
s_add_u32 m0,m0,s50
s_add_u32 s69,s69,s53
buffer_load_dwordx4 v3,s[44:47],s69 offen lds
RA62_%=:
ds_read_u16 v133,v133
ds_read_b64_tr_b16 v[96:97],v6 offset:12384
v_exp_f16_sdwa v122,v123 dst_sel:WORD_1 dst_unused:UNUSED_PRESERVE src0_sel:WORD_1
v_mfma_f32_16x16x32_bf16 v[156:159],v[68:71],v[44:47],v[116:119]
ds_read_u16_d16_hi v135,v135
v_exp_f16_sdwa v120,v121 dst_sel:WORD_1 dst_unused:UNUSED_PRESERVE src0_sel:WORD_1
v_mfma_f32_16x16x32_bf16 v[148:151],v[68:71],v[40:43],v[116:119]
ds_read_b64_tr_b16 v[98:99],v6 offset:13920
v_pk_fma_f16 v120,v120,s55,s55
v_pk_fma_f16 v122,v122,s55,s55
v_mfma_f32_16x16x32_bf16 v[144:147],v[72:75],v[48:51],v[144:147]
ds_read_b64_tr_b16 v[100:101],v6 offset:12392
v_rcp_f16 v121,v120
v_mfma_f32_16x16x32_bf16 v[152:155],v[72:75],v[52:55],v[152:155]
s_cbranch_vccz RA63_%=
s_add_u32 m0,m0,1024
s_add_u32 s69,s69,s53
buffer_load_dwordx4 v2,s[44:47],s69 offen lds
s_add_u32 m0,m0,1024
s_add_u32 s69,s69,s53
buffer_load_dwordx4 v3,s[44:47],s69 offen lds
s_add_u32 m0,m0,s50
s_add_u32 s69,s69,s51
buffer_load_dwordx4 v2,s[44:47],s69 offen lds
s_add_u32 m0,m0,1024
s_add_u32 s69,s69,s53
buffer_load_dwordx4 v3,s[44:47],s69 offen lds
RA63_%=:
ds_read_b64_tr_b16 v[102:103],v6 offset:13928
ds_read_u16 v136,v136
v_rcp_f16 v123,v122
v_mfma_f32_16x16x32_bf16 v[156:159],v[76:79],v[52:55],v[156:159]
v_rcp_f16_sdwa v121,v120 dst_sel:WORD_1 dst_unused:UNUSED_PRESERVE src0_sel:WORD_1
v_mfma_f32_16x16x32_bf16 v[148:151],v[76:79],v[48:51],v[148:151]
ds_read_b64_tr_b16 v[104:105],v6 offset:12448
ds_read_b64_tr_b16 v[106:107],v6 offset:13984
v_rcp_f16_sdwa v123,v122 dst_sel:WORD_1 dst_unused:UNUSED_PRESERVE src0_sel:WORD_1
v_mfma_f32_16x16x32_bf16 v[144:147],v[80:83],v[56:59],v[144:147]
ds_read_u16_d16_hi v138,v138
v_fma_mix_f32 v142,v142,v121,0 op_sel:[0,1,0] op_sel_hi:[0,1,0]
v_mfma_f32_16x16x32_bf16 v[152:155],v[80:83],v[60:63],v[152:155]
s_waitcnt lgkmcnt(7)
s_cbranch_vccz RA64_%=
s_add_u32 m0,m0,1024
s_add_u32 s69,s69,s53
buffer_load_dwordx4 v2,s[44:47],s69 offen lds
s_add_u32 m0,m0,s50
s_add_u32 s69,s69,s53
buffer_load_dwordx4 v3,s[44:47],s69 offen lds
s_add_u32 m0,m0,1024
s_add_u32 s69,s69,s53
buffer_load_dwordx4 v2,s[44:47],s69 offen lds
s_add_u32 m0,m0,1024
s_add_u32 s69,s69,s53
buffer_load_dwordx4 v3,s[44:47],s69 offen lds
RA64_%=:
ds_read_b64_tr_b16 v[108:109],v6 offset:12456
v_fma_mix_f32 v140,v140,v121,0 op_sel_hi:[0,1,0]
v_lshl_add_u64 v[128:129],v[128:129],0,v[130:131]
v_lshl_add_u64 v[130:131],v[132:133],0,v[134:135]
v_mfma_f32_16x16x32_bf16 v[156:159],v[84:87],v[60:63],v[156:159]
ds_read_u16 v137,v137
v_fma_mix_f32 v141,v141,v123,0 op_sel_hi:[0,1,0]
v_fma_mix_f32 v143,v143,v123,0 op_sel:[0,1,0] op_sel_hi:[0,1,0]
v_mfma_f32_16x16x32_bf16 v[148:151],v[84:87],v[56:59],v[148:151]
ds_read_b64_tr_b16 v[110:111],v6 offset:13992
ds_read_b64_tr_b16 v[64:65],v4 offset:12320
v_lshlrev_b32_sdwa v144,1,v144 dst_sel:DWORD dst_unused:UNUSED_PAD src0_sel:DWORD src1_sel:WORD_1
v_lshlrev_b32_sdwa v146,1,v146 dst_sel:DWORD dst_unused:UNUSED_PAD src0_sel:DWORD src1_sel:WORD_1
v_mfma_f32_16x16x32_bf16 v[160:163],v[88:91],v[128:131],v[160:163]
ds_read_u16_d16_hi v139,v139
v_lshlrev_b32_sdwa v145,1,v145 dst_sel:DWORD dst_unused:UNUSED_PAD src0_sel:DWORD src1_sel:WORD_1
v_lshlrev_b32_sdwa v147,1,v147 dst_sel:DWORD dst_unused:UNUSED_PAD src0_sel:DWORD src1_sel:WORD_1
v_fma_mixlo_f16 v122,v157,s99,0
v_mfma_f32_16x16x32_bf16 v[168:171],v[92:95],v[128:131],v[168:171]
s_waitcnt lgkmcnt(11)
ds_read_b64_tr_b16 v[66:67],v4 offset:14368
ds_read_b128 v[112:115],v7 offset:512
v_fma_mixlo_f16 v120,v156,s99,0
v_mfma_f32_16x16x32_bf16 v[176:179],v[96:99],v[128:131],v[176:179]
s_waitcnt lgkmcnt(11)
ds_read_u16 v144,v144
v_lshlrev_b32_sdwa v148,1,v148 dst_sel:DWORD dst_unused:UNUSED_PAD src0_sel:DWORD src1_sel:WORD_1
v_lshlrev_b32_sdwa v150,1,v150 dst_sel:DWORD dst_unused:UNUSED_PAD src0_sel:DWORD src1_sel:WORD_1
v_mfma_f32_16x16x32_bf16 v[184:187],v[100:103],v[128:131],v[184:187]
s_waitcnt lgkmcnt(3)
ds_read_b64_tr_b16 v[68:69],v5 offset:12320
ds_read_b64_tr_b16 v[70:71],v5 offset:14368
v_lshl_add_u64 v[136:137],v[136:137],0,v[138:139]
v_fma_mixhi_f16 v120,v158,s99,0
v_mfma_f32_16x16x32_bf16 v[192:195],v[104:107],v[128:131],v[192:195]
ds_read_u16_d16_hi v146,v146
v_cvt_pk_bf16_f32 v138,v140,v142
v_cvt_pk_bf16_f32 v139,v141,v143
v_mfma_f32_16x16x32_bf16 v[200:203],v[108:111],v[128:131],v[200:203]
v_fma_mixhi_f16 v122,v159,s99,0
v_mfma_f32_16x16x32_bf16 v[164:167],v[88:91],v[136:139],v[164:167]
ds_read_b128 v[116:119],v7 offset:544
ds_read_b64_tr_b16 v[72:73],v4 offset:16416
v_lshlrev_b32_sdwa v149,1,v149 dst_sel:DWORD dst_unused:UNUSED_PAD src0_sel:DWORD src1_sel:WORD_1
v_lshlrev_b32_sdwa v151,1,v151 dst_sel:DWORD dst_unused:UNUSED_PAD src0_sel:DWORD src1_sel:WORD_1
v_mfma_f32_16x16x32_bf16 v[172:175],v[92:95],v[136:139],v[172:175]
ds_read_b64_tr_b16 v[74:75],v4 offset:18464
ds_read_b64_tr_b16 v[76:77],v5 offset:16416
v_pk_fma_f16 v121,v120,v120,s54
v_pk_fma_f16 v123,v122,v122,s54
v_mfma_f32_16x16x32_bf16 v[180:183],v[96:99],v[136:139],v[180:183]
ds_read_u16 v145,v145
v_pk_mul_f16 v121,v120,v121
v_pk_mul_f16 v123,v122,v123
v_mfma_f32_16x16x32_bf16 v[188:191],v[100:103],v[136:139],v[188:191]
ds_read_u16_d16_hi v147,v147
v_lshlrev_b32_sdwa v152,1,v152 dst_sel:DWORD dst_unused:UNUSED_PAD src0_sel:DWORD src1_sel:WORD_1
v_lshlrev_b32_sdwa v154,1,v154 dst_sel:DWORD dst_unused:UNUSED_PAD src0_sel:DWORD src1_sel:WORD_1
v_mfma_f32_16x16x32_bf16 v[196:199],v[104:107],v[136:139],v[196:199]
v_exp_f16 v120,v121
v_mfma_f32_16x16x32_bf16 v[204:207],v[108:111],v[136:139],v[204:207]
s_waitcnt lgkmcnt(10)
ds_read_b64_tr_b16 v[78:79],v5 offset:18464
ds_read_b64_tr_b16 v[80:81],v4 offset:20512
v_exp_f16 v122,v123
v_mfma_f32_16x16x32_bf16 v[128:131],v[64:67],v[16:19],v[112:115]
v_lshlrev_b32_sdwa v153,1,v153 dst_sel:DWORD dst_unused:UNUSED_PAD src0_sel:DWORD src1_sel:WORD_1
v_lshlrev_b32_sdwa v155,1,v155 dst_sel:DWORD dst_unused:UNUSED_PAD src0_sel:DWORD src1_sel:WORD_1
v_mfma_f32_16x16x32_bf16 v[136:139],v[64:67],v[20:23],v[112:115]
s_waitcnt lgkmcnt(7)
ds_read_u16 v148,v148
ds_read_u16_d16_hi v150,v150
v_exp_f16_sdwa v120,v121 dst_sel:WORD_1 dst_unused:UNUSED_PRESERVE src0_sel:WORD_1
v_mfma_f32_16x16x32_bf16 v[140:143],v[68:71],v[20:23],v[116:119]
v_exp_f16_sdwa v122,v123 dst_sel:WORD_1 dst_unused:UNUSED_PRESERVE src0_sel:WORD_1
v_mfma_f32_16x16x32_bf16 v[132:135],v[68:71],v[16:19],v[116:119]
s_waitcnt lgkmcnt(7)
ds_read_b64_tr_b16 v[82:83],v4 offset:22560
v_pk_fma_f16 v120,v120,s55,s55
v_mfma_f32_16x16x32_bf16 v[128:131],v[72:75],v[24:27],v[128:131]
ds_read_b64_tr_b16 v[84:85],v5 offset:20512
v_pk_fma_f16 v122,v122,s55,s55
v_rcp_f16 v121,v120
v_mfma_f32_16x16x32_bf16 v[136:139],v[72:75],v[28:31],v[136:139]
s_waitcnt lgkmcnt(5)
ds_read_b64_tr_b16 v[86:87],v5 offset:22560
ds_read_u16 v149,v149
v_rcp_f16 v123,v122
v_mfma_f32_16x16x32_bf16 v[140:143],v[76:79],v[28:31],v[140:143]
ds_read_u16_d16_hi v151,v151
ds_read_u16 v152,v152
v_rcp_f16_sdwa v121,v120 dst_sel:WORD_1 dst_unused:UNUSED_PRESERVE src0_sel:WORD_1
v_mfma_f32_16x16x32_bf16 v[132:135],v[76:79],v[24:27],v[132:135]
s_waitcnt lgkmcnt(5)
v_rcp_f16_sdwa v123,v122 dst_sel:WORD_1 dst_unused:UNUSED_PRESERVE src0_sel:WORD_1
v_mfma_f32_16x16x32_bf16 v[128:131],v[80:83],v[32:35],v[128:131]
ds_read_u16_d16_hi v154,v154
v_fma_mix_f32 v156,v156,v121,0 op_sel_hi:[0,1,0]
v_fma_mix_f32 v158,v158,v121,0 op_sel:[0,1,0] op_sel_hi:[0,1,0]
v_mfma_f32_16x16x32_bf16 v[136:139],v[80:83],v[36:39],v[136:139]
s_waitcnt lgkmcnt(2)
v_lshl_add_u64 v[144:145],v[144:145],0,v[146:147]
v_lshl_add_u64 v[146:147],v[148:149],0,v[150:151]
v_mfma_f32_16x16x32_bf16 v[140:143],v[84:87],v[36:39],v[140:143]
ds_read_u16 v153,v153
v_fma_mix_f32 v159,v159,v123,0 op_sel:[0,1,0] op_sel_hi:[0,1,0]
v_fma_mix_f32 v157,v157,v123,0 op_sel_hi:[0,1,0]
v_mfma_f32_16x16x32_bf16 v[132:135],v[84:87],v[32:35],v[132:135]
v_lshlrev_b32_sdwa v128,1,v128 dst_sel:DWORD dst_unused:UNUSED_PAD src0_sel:DWORD src1_sel:WORD_1
v_lshlrev_b32_sdwa v130,1,v130 dst_sel:DWORD dst_unused:UNUSED_PAD src0_sel:DWORD src1_sel:WORD_1
v_mfma_f32_16x16x32_bf16 v[208:211],v[88:91],v[144:147],v[208:211]
v_lshlrev_b32_sdwa v129,1,v129 dst_sel:DWORD dst_unused:UNUSED_PAD src0_sel:DWORD src1_sel:WORD_1
v_lshlrev_b32_sdwa v131,1,v131 dst_sel:DWORD dst_unused:UNUSED_PAD src0_sel:DWORD src1_sel:WORD_1
v_mfma_f32_16x16x32_bf16 v[216:219],v[92:95],v[144:147],v[216:219]
ds_read_u16_d16_hi v155,v155
ds_read_u16 v128,v128
v_fma_mixlo_f16 v120,v140,s99,0
v_fma_mixlo_f16 v122,v141,s99,0
v_mfma_f32_16x16x32_bf16 v[224:227],v[96:99],v[144:147],v[224:227]
ds_read_u16_d16_hi v130,v130
v_lshlrev_b32_sdwa v132,1,v132 dst_sel:DWORD dst_unused:UNUSED_PAD src0_sel:DWORD src1_sel:WORD_1
v_lshlrev_b32_sdwa v134,1,v134 dst_sel:DWORD dst_unused:UNUSED_PAD src0_sel:DWORD src1_sel:WORD_1
v_mfma_f32_16x16x32_bf16 v[232:235],v[100:103],v[144:147],v[232:235]
s_waitcnt lgkmcnt(2)
v_lshl_add_u64 v[152:153],v[152:153],0,v[154:155]
v_cvt_pk_bf16_f32 v154,v156,v158
v_mfma_f32_16x16x32_bf16 v[240:243],v[104:107],v[144:147],v[240:243]
v_fma_mixhi_f16 v120,v142,s99,0
v_cvt_pk_bf16_f32 v155,v157,v159
v_mfma_f32_16x16x32_bf16 v[248:251],v[108:111],v[144:147],v[248:251]
s_setprio 2
ds_read_u16 v129,v129
v_fma_mixhi_f16 v122,v143,s99,0
v_mfma_f32_16x16x32_bf16 v[212:215],v[88:91],v[152:155],v[212:215]
v_lshlrev_b32_sdwa v133,1,v133 dst_sel:DWORD dst_unused:UNUSED_PAD src0_sel:DWORD src1_sel:WORD_1
v_pk_fma_f16 v121,v120,v120,s54
v_lshlrev_b32_sdwa v135,1,v135 dst_sel:DWORD dst_unused:UNUSED_PAD src0_sel:DWORD src1_sel:WORD_1
v_mfma_f32_16x16x32_bf16 v[220:223],v[92:95],v[152:155],v[220:223]
ds_read_u16_d16_hi v131,v131
ds_read_u16 v132,v132
v_pk_fma_f16 v123,v122,v122,s54
v_mfma_f32_16x16x32_bf16 v[228:231],v[96:99],v[152:155],v[228:231]
ds_read_b64_tr_b16 v[88:89],v6 offset:18480
v_pk_mul_f16 v121,v120,v121
v_lshlrev_b32_sdwa v136,1,v136 dst_sel:DWORD dst_unused:UNUSED_PAD src0_sel:DWORD src1_sel:WORD_1
v_pk_mul_f16 v123,v122,v123
v_mfma_f32_16x16x32_bf16 v[236:239],v[100:103],v[152:155],v[236:239]
ds_read_b64_tr_b16 v[90:91],v6 offset:20016
v_lshlrev_b32_sdwa v138,1,v138 dst_sel:DWORD dst_unused:UNUSED_PAD src0_sel:DWORD src1_sel:WORD_1
v_mfma_f32_16x16x32_bf16 v[244:247],v[104:107],v[152:155],v[244:247]
ds_read_u16_d16_hi v134,v134
v_exp_f16 v120,v121
v_mfma_f32_16x16x32_bf16 v[252:255],v[108:111],v[152:155],v[252:255]
ds_read_b64_tr_b16 v[92:93],v6 offset:18488
v_lshlrev_b32_sdwa v137,1,v137 dst_sel:DWORD dst_unused:UNUSED_PAD src0_sel:DWORD src1_sel:WORD_1
v_mfma_f32_16x16x32_bf16 v[144:147],v[64:67],v[40:43],v[112:115]
ds_read_b64_tr_b16 v[94:95],v6 offset:20024
v_lshlrev_b32_sdwa v139,1,v139 dst_sel:DWORD dst_unused:UNUSED_PAD src0_sel:DWORD src1_sel:WORD_1
v_exp_f16 v122,v123
v_mfma_f32_16x16x32_bf16 v[152:155],v[64:67],v[44:47],v[112:115]
ds_read_u16 v133,v133
ds_read_b64_tr_b16 v[96:97],v6 offset:18544
v_exp_f16_sdwa v122,v123 dst_sel:WORD_1 dst_unused:UNUSED_PRESERVE src0_sel:WORD_1
v_mfma_f32_16x16x32_bf16 v[156:159],v[68:71],v[44:47],v[116:119]
ds_read_u16_d16_hi v135,v135
v_exp_f16_sdwa v120,v121 dst_sel:WORD_1 dst_unused:UNUSED_PRESERVE src0_sel:WORD_1
v_mfma_f32_16x16x32_bf16 v[148:151],v[68:71],v[40:43],v[116:119]
ds_read_b64_tr_b16 v[98:99],v6 offset:20080
v_pk_fma_f16 v120,v120,s55,s55
v_pk_fma_f16 v122,v122,s55,s55
v_mfma_f32_16x16x32_bf16 v[144:147],v[72:75],v[48:51],v[144:147]
ds_read_b64_tr_b16 v[100:101],v6 offset:18552
v_rcp_f16 v121,v120
v_mfma_f32_16x16x32_bf16 v[152:155],v[72:75],v[52:55],v[152:155]
ds_read_b64_tr_b16 v[102:103],v6 offset:20088
ds_read_u16 v136,v136
v_rcp_f16 v123,v122
v_mfma_f32_16x16x32_bf16 v[156:159],v[76:79],v[52:55],v[156:159]
v_rcp_f16_sdwa v121,v120 dst_sel:WORD_1 dst_unused:UNUSED_PRESERVE src0_sel:WORD_1
v_mfma_f32_16x16x32_bf16 v[148:151],v[76:79],v[48:51],v[148:151]
ds_read_b64_tr_b16 v[104:105],v6 offset:18608
ds_read_b64_tr_b16 v[106:107],v6 offset:20144
v_rcp_f16_sdwa v123,v122 dst_sel:WORD_1 dst_unused:UNUSED_PRESERVE src0_sel:WORD_1
v_mfma_f32_16x16x32_bf16 v[144:147],v[80:83],v[56:59],v[144:147]
ds_read_u16_d16_hi v138,v138
v_fma_mix_f32 v142,v142,v121,0 op_sel:[0,1,0] op_sel_hi:[0,1,0]
v_mfma_f32_16x16x32_bf16 v[152:155],v[80:83],v[60:63],v[152:155]
s_waitcnt lgkmcnt(7)
ds_read_b64_tr_b16 v[108:109],v6 offset:18616
v_fma_mix_f32 v140,v140,v121,0 op_sel_hi:[0,1,0]
v_lshl_add_u64 v[128:129],v[128:129],0,v[130:131]
v_lshl_add_u64 v[130:131],v[132:133],0,v[134:135]
v_mfma_f32_16x16x32_bf16 v[156:159],v[84:87],v[60:63],v[156:159]
ds_read_u16 v137,v137
v_fma_mix_f32 v141,v141,v123,0 op_sel_hi:[0,1,0]
v_fma_mix_f32 v143,v143,v123,0 op_sel:[0,1,0] op_sel_hi:[0,1,0]
v_mfma_f32_16x16x32_bf16 v[148:151],v[84:87],v[56:59],v[148:151]
ds_read_b64_tr_b16 v[110:111],v6 offset:20152
ds_read_b64_tr_b16 v[64:65],v4 offset:12832
v_lshlrev_b32_sdwa v144,1,v144 dst_sel:DWORD dst_unused:UNUSED_PAD src0_sel:DWORD src1_sel:WORD_1
v_lshlrev_b32_sdwa v146,1,v146 dst_sel:DWORD dst_unused:UNUSED_PAD src0_sel:DWORD src1_sel:WORD_1
v_mfma_f32_16x16x32_bf16 v[160:163],v[88:91],v[128:131],v[160:163]
ds_read_u16_d16_hi v139,v139
v_lshlrev_b32_sdwa v145,1,v145 dst_sel:DWORD dst_unused:UNUSED_PAD src0_sel:DWORD src1_sel:WORD_1
v_lshlrev_b32_sdwa v147,1,v147 dst_sel:DWORD dst_unused:UNUSED_PAD src0_sel:DWORD src1_sel:WORD_1
v_fma_mixlo_f16 v122,v157,s99,0
v_mfma_f32_16x16x32_bf16 v[168:171],v[92:95],v[128:131],v[168:171]
s_waitcnt lgkmcnt(11)
ds_read_b64_tr_b16 v[66:67],v4 offset:14880
ds_read_b128 v[112:115],v7 offset:640
v_fma_mixlo_f16 v120,v156,s99,0
v_mfma_f32_16x16x32_bf16 v[176:179],v[96:99],v[128:131],v[176:179]
s_waitcnt lgkmcnt(11)
ds_read_u16 v144,v144
v_lshlrev_b32_sdwa v148,1,v148 dst_sel:DWORD dst_unused:UNUSED_PAD src0_sel:DWORD src1_sel:WORD_1
v_lshlrev_b32_sdwa v150,1,v150 dst_sel:DWORD dst_unused:UNUSED_PAD src0_sel:DWORD src1_sel:WORD_1
v_mfma_f32_16x16x32_bf16 v[184:187],v[100:103],v[128:131],v[184:187]
s_waitcnt lgkmcnt(3)
ds_read_b64_tr_b16 v[68:69],v5 offset:12832
ds_read_b64_tr_b16 v[70:71],v5 offset:14880
v_lshl_add_u64 v[136:137],v[136:137],0,v[138:139]
v_fma_mixhi_f16 v120,v158,s99,0
v_mfma_f32_16x16x32_bf16 v[192:195],v[104:107],v[128:131],v[192:195]
ds_read_u16_d16_hi v146,v146
v_cvt_pk_bf16_f32 v138,v140,v142
v_cvt_pk_bf16_f32 v139,v141,v143
v_mfma_f32_16x16x32_bf16 v[200:203],v[108:111],v[128:131],v[200:203]
v_fma_mixhi_f16 v122,v159,s99,0
v_mfma_f32_16x16x32_bf16 v[164:167],v[88:91],v[136:139],v[164:167]
ds_read_b128 v[116:119],v7 offset:672
ds_read_b64_tr_b16 v[72:73],v4 offset:16928
v_lshlrev_b32_sdwa v149,1,v149 dst_sel:DWORD dst_unused:UNUSED_PAD src0_sel:DWORD src1_sel:WORD_1
v_lshlrev_b32_sdwa v151,1,v151 dst_sel:DWORD dst_unused:UNUSED_PAD src0_sel:DWORD src1_sel:WORD_1
v_mfma_f32_16x16x32_bf16 v[172:175],v[92:95],v[136:139],v[172:175]
s_setprio 1
ds_read_b64_tr_b16 v[74:75],v4 offset:18976
ds_read_b64_tr_b16 v[76:77],v5 offset:16928
v_pk_fma_f16 v121,v120,v120,s54
v_pk_fma_f16 v123,v122,v122,s54
v_mfma_f32_16x16x32_bf16 v[180:183],v[96:99],v[136:139],v[180:183]
ds_read_u16 v145,v145
v_pk_mul_f16 v121,v120,v121
v_pk_mul_f16 v123,v122,v123
v_mfma_f32_16x16x32_bf16 v[188:191],v[100:103],v[136:139],v[188:191]
ds_read_u16_d16_hi v147,v147
v_lshlrev_b32_sdwa v152,1,v152 dst_sel:DWORD dst_unused:UNUSED_PAD src0_sel:DWORD src1_sel:WORD_1
v_lshlrev_b32_sdwa v154,1,v154 dst_sel:DWORD dst_unused:UNUSED_PAD src0_sel:DWORD src1_sel:WORD_1
v_mfma_f32_16x16x32_bf16 v[196:199],v[104:107],v[136:139],v[196:199]
v_exp_f16 v120,v121
v_mfma_f32_16x16x32_bf16 v[204:207],v[108:111],v[136:139],v[204:207]
s_waitcnt lgkmcnt(10)
ds_read_b64_tr_b16 v[78:79],v5 offset:18976
ds_read_b64_tr_b16 v[80:81],v4 offset:21024
v_exp_f16 v122,v123
v_mfma_f32_16x16x32_bf16 v[128:131],v[64:67],v[16:19],v[112:115]
v_lshlrev_b32_sdwa v153,1,v153 dst_sel:DWORD dst_unused:UNUSED_PAD src0_sel:DWORD src1_sel:WORD_1
v_lshlrev_b32_sdwa v155,1,v155 dst_sel:DWORD dst_unused:UNUSED_PAD src0_sel:DWORD src1_sel:WORD_1
v_mfma_f32_16x16x32_bf16 v[136:139],v[64:67],v[20:23],v[112:115]
s_waitcnt lgkmcnt(7)
ds_read_u16 v148,v148
ds_read_u16_d16_hi v150,v150
v_exp_f16_sdwa v120,v121 dst_sel:WORD_1 dst_unused:UNUSED_PRESERVE src0_sel:WORD_1
v_mfma_f32_16x16x32_bf16 v[140:143],v[68:71],v[20:23],v[116:119]
v_exp_f16_sdwa v122,v123 dst_sel:WORD_1 dst_unused:UNUSED_PRESERVE src0_sel:WORD_1
v_mfma_f32_16x16x32_bf16 v[132:135],v[68:71],v[16:19],v[116:119]
s_waitcnt lgkmcnt(7)
ds_read_b64_tr_b16 v[82:83],v4 offset:23072
v_pk_fma_f16 v120,v120,s55,s55
v_mfma_f32_16x16x32_bf16 v[128:131],v[72:75],v[24:27],v[128:131]
ds_read_b64_tr_b16 v[84:85],v5 offset:21024
v_pk_fma_f16 v122,v122,s55,s55
v_rcp_f16 v121,v120
v_mfma_f32_16x16x32_bf16 v[136:139],v[72:75],v[28:31],v[136:139]
s_waitcnt lgkmcnt(5)
ds_read_b64_tr_b16 v[86:87],v5 offset:23072
ds_read_u16 v149,v149
v_rcp_f16 v123,v122
v_mfma_f32_16x16x32_bf16 v[140:143],v[76:79],v[28:31],v[140:143]
s_setprio 0
ds_read_u16_d16_hi v151,v151
ds_read_u16 v152,v152
v_rcp_f16_sdwa v121,v120 dst_sel:WORD_1 dst_unused:UNUSED_PRESERVE src0_sel:WORD_1
v_mfma_f32_16x16x32_bf16 v[132:135],v[76:79],v[24:27],v[132:135]
s_waitcnt lgkmcnt(5)
v_rcp_f16_sdwa v123,v122 dst_sel:WORD_1 dst_unused:UNUSED_PRESERVE src0_sel:WORD_1
v_mfma_f32_16x16x32_bf16 v[128:131],v[80:83],v[32:35],v[128:131]
ds_read_u16_d16_hi v154,v154
v_fma_mix_f32 v156,v156,v121,0 op_sel_hi:[0,1,0]
v_fma_mix_f32 v158,v158,v121,0 op_sel:[0,1,0] op_sel_hi:[0,1,0]
v_mfma_f32_16x16x32_bf16 v[136:139],v[80:83],v[36:39],v[136:139]
s_waitcnt lgkmcnt(2)
v_lshl_add_u64 v[144:145],v[144:145],0,v[146:147]
v_lshl_add_u64 v[146:147],v[148:149],0,v[150:151]
v_mfma_f32_16x16x32_bf16 v[140:143],v[84:87],v[36:39],v[140:143]
ds_read_u16 v153,v153
s_nop 0
s_mov_b32 s86,0xbe801d5a
v_fma_mix_f32 v159,v159,v123,0 op_sel:[0,1,0] op_sel_hi:[0,1,0]
v_fma_mix_f32 v157,v157,v123,0 op_sel_hi:[0,1,0]
v_mfma_f32_16x16x32_bf16 v[132:135],v[84:87],v[32:35],v[132:135]
s_nop 0
v_lshlrev_b32_sdwa v128,1,v128 dst_sel:DWORD dst_unused:UNUSED_PAD src0_sel:DWORD src1_sel:WORD_1
v_lshlrev_b32_sdwa v130,1,v130 dst_sel:DWORD dst_unused:UNUSED_PAD src0_sel:DWORD src1_sel:WORD_1
v_mfma_f32_16x16x32_bf16 v[208:211],v[88:91],v[144:147],v[208:211]
s_waitcnt vmcnt(12)
s_barrier
s_setprio 3
v_lshlrev_b32_sdwa v129,1,v129 dst_sel:DWORD dst_unused:UNUSED_PAD src0_sel:DWORD src1_sel:WORD_1
v_lshlrev_b32_sdwa v131,1,v131 dst_sel:DWORD dst_unused:UNUSED_PAD src0_sel:DWORD src1_sel:WORD_1
v_mfma_f32_16x16x32_bf16 v[216:219],v[92:95],v[144:147],v[216:219]
ds_read_u16_d16_hi v155,v155
ds_read_u16 v128,v128
v_fma_mixlo_f16 v120,v140,s99,0
v_fma_mixlo_f16 v122,v141,s99,0
v_mfma_f32_16x16x32_bf16 v[224:227],v[96:99],v[144:147],v[224:227]
ds_read_u16_d16_hi v130,v130
v_lshlrev_b32_sdwa v132,1,v132 dst_sel:DWORD dst_unused:UNUSED_PAD src0_sel:DWORD src1_sel:WORD_1
v_lshlrev_b32_sdwa v134,1,v134 dst_sel:DWORD dst_unused:UNUSED_PAD src0_sel:DWORD src1_sel:WORD_1
v_mfma_f32_16x16x32_bf16 v[232:235],v[100:103],v[144:147],v[232:235]
s_waitcnt lgkmcnt(2)
v_lshl_add_u64 v[152:153],v[152:153],0,v[154:155]
v_cvt_pk_bf16_f32 v154,v156,v158
v_mfma_f32_16x16x32_bf16 v[240:243],v[104:107],v[144:147],v[240:243]
v_fma_mixhi_f16 v120,v142,s99,0
v_cvt_pk_bf16_f32 v155,v157,v159
v_mfma_f32_16x16x32_bf16 v[248:251],v[108:111],v[144:147],v[248:251]
ds_read_u16 v129,v129
v_fma_mixhi_f16 v122,v143,s99,0
v_mfma_f32_16x16x32_bf16 v[212:215],v[88:91],v[152:155],v[212:215]
v_lshlrev_b32_sdwa v133,1,v133 dst_sel:DWORD dst_unused:UNUSED_PAD src0_sel:DWORD src1_sel:WORD_1
v_pk_fma_f16 v121,v120,v120,s54
v_lshlrev_b32_sdwa v135,1,v135 dst_sel:DWORD dst_unused:UNUSED_PAD src0_sel:DWORD src1_sel:WORD_1
v_mfma_f32_16x16x32_bf16 v[220:223],v[92:95],v[152:155],v[220:223]
ds_read_u16_d16_hi v131,v131
ds_read_u16 v132,v132
v_pk_fma_f16 v123,v122,v122,s54
v_mfma_f32_16x16x32_bf16 v[228:231],v[96:99],v[152:155],v[228:231]
ds_read_b64_tr_b16 v[88:89],v6 offset:0
v_pk_mul_f16 v121,v120,v121
v_lshlrev_b32_sdwa v136,1,v136 dst_sel:DWORD dst_unused:UNUSED_PAD src0_sel:DWORD src1_sel:WORD_1
v_pk_mul_f16 v123,v122,v123
v_mfma_f32_16x16x32_bf16 v[236:239],v[100:103],v[152:155],v[236:239]
s_cbranch_vccz RA65_%=
s_add_u32 m0,s58,12320
s_mul_i32 s69,s59,7
s_add_u32 s69,s69,s97
buffer_load_dwordx4 v2,s[44:47],s69 offen lds
s_add_u32 m0,m0,1024
s_add_u32 s69,s69,s53
buffer_load_dwordx4 v3,s[44:47],s69 offen lds
s_add_u32 m0,m0,1024
s_add_u32 s69,s69,s53
buffer_load_dwordx4 v2,s[44:47],s69 offen lds
s_add_u32 m0,m0,s50
s_add_u32 s69,s69,s53
buffer_load_dwordx4 v3,s[44:47],s69 offen lds
RA65_%=:
ds_read_b64_tr_b16 v[90:91],v6 offset:1536
v_lshlrev_b32_sdwa v138,1,v138 dst_sel:DWORD dst_unused:UNUSED_PAD src0_sel:DWORD src1_sel:WORD_1
v_mfma_f32_16x16x32_bf16 v[244:247],v[104:107],v[152:155],v[244:247]
ds_read_u16_d16_hi v134,v134
v_mfma_f32_16x16x32_bf16 v[252:255],v[108:111],v[152:155],v[252:255]
ds_read_b64_tr_b16 v[92:93],v6 offset:8
v_lshlrev_b32_sdwa v137,1,v137 dst_sel:DWORD dst_unused:UNUSED_PAD src0_sel:DWORD src1_sel:WORD_1
v_exp_f16 v120,v121
v_mfma_f32_16x16x32_bf16 v[144:147],v[64:67],v[40:43],v[112:115]
ds_read_b64_tr_b16 v[94:95],v6 offset:1544
v_lshlrev_b32_sdwa v139,1,v139 dst_sel:DWORD dst_unused:UNUSED_PAD src0_sel:DWORD src1_sel:WORD_1
v_mfma_f32_16x16x32_bf16 v[152:155],v[64:67],v[44:47],v[112:115]
ds_read_u16 v133,v133
ds_read_b64_tr_b16 v[96:97],v6 offset:64
v_exp_f16 v122,v123
v_mfma_f32_16x16x32_bf16 v[156:159],v[68:71],v[44:47],v[116:119]
ds_read_u16_d16_hi v135,v135
v_exp_f16_sdwa v122,v123 dst_sel:WORD_1 dst_unused:UNUSED_PRESERVE src0_sel:WORD_1
v_exp_f16_sdwa v120,v121 dst_sel:WORD_1 dst_unused:UNUSED_PRESERVE src0_sel:WORD_1
v_mfma_f32_16x16x32_bf16 v[148:151],v[68:71],v[40:43],v[116:119]
ds_read_b64_tr_b16 v[98:99],v6 offset:1600
v_pk_fma_f16 v120,v120,s55,s55
v_pk_fma_f16 v122,v122,s55,s55
v_mfma_f32_16x16x32_bf16 v[144:147],v[72:75],v[48:51],v[144:147]
s_cbranch_vccz RA66_%=
s_add_u32 m0,m0,1024
s_add_u32 s69,s69,s53
buffer_load_dwordx4 v2,s[44:47],s69 offen lds
s_add_u32 m0,m0,1024
s_add_u32 s69,s69,s53
buffer_load_dwordx4 v3,s[44:47],s69 offen lds
s_add_u32 m0,m0,s50
s_add_u32 s69,s69,s53
buffer_load_dwordx4 v2,s[44:47],s69 offen lds
s_add_u32 m0,m0,1024
s_add_u32 s69,s69,s53
buffer_load_dwordx4 v3,s[44:47],s69 offen lds
RA66_%=:
v_rcp_f16 v121,v120
v_mfma_f32_16x16x32_bf16 v[152:155],v[72:75],v[52:55],v[152:155]
ds_read_b64_tr_b16 v[100:101],v6 offset:72
v_rcp_f16 v123,v122
v_mfma_f32_16x16x32_bf16 v[156:159],v[76:79],v[52:55],v[156:159]
ds_read_u16 v136,v136
v_rcp_f16_sdwa v121,v120 dst_sel:WORD_1 dst_unused:UNUSED_PRESERVE src0_sel:WORD_1
v_mfma_f32_16x16x32_bf16 v[148:151],v[76:79],v[48:51],v[148:151]
ds_read_b64_tr_b16 v[102:103],v6 offset:1608
ds_read_b64_tr_b16 v[104:105],v6 offset:128
v_rcp_f16_sdwa v123,v122 dst_sel:WORD_1 dst_unused:UNUSED_PRESERVE src0_sel:WORD_1
v_mfma_f32_16x16x32_bf16 v[144:147],v[80:83],v[56:59],v[144:147]
ds_read_u16_d16_hi v138,v138
v_fma_mix_f32 v142,v142,v121,0 op_sel:[0,1,0] op_sel_hi:[0,1,0]
v_mfma_f32_16x16x32_bf16 v[152:155],v[80:83],v[60:63],v[152:155]
s_waitcnt lgkmcnt(6)
ds_read_b64_tr_b16 v[106:107],v6 offset:1664
ds_read_b64_tr_b16 v[108:109],v6 offset:136
v_fma_mix_f32 v140,v140,v121,0 op_sel_hi:[0,1,0]
v_lshl_add_u64 v[128:129],v[128:129],0,v[130:131]
v_lshl_add_u64 v[130:131],v[132:133],0,v[134:135]
v_mfma_f32_16x16x32_bf16 v[156:159],v[84:87],v[60:63],v[156:159]
s_cbranch_vccz RA67_%=
s_add_u32 m0,m0,1024
s_add_u32 s69,s69,s53
buffer_load_dwordx4 v2,s[44:47],s69 offen lds
s_add_u32 m0,m0,s50
s_add_u32 s69,s69,s53
buffer_load_dwordx4 v3,s[44:47],s69 offen lds
s_add_u32 m0,m0,1024
s_add_u32 s69,s69,s53
buffer_load_dwordx4 v2,s[44:47],s69 offen lds
s_add_u32 m0,m0,1024
s_add_u32 s69,s69,s53
buffer_load_dwordx4 v3,s[44:47],s69 offen lds
RA67_%=:
ds_read_u16 v137,v137
v_fma_mix_f32 v141,v141,v123,0 op_sel_hi:[0,1,0]
v_fma_mix_f32 v143,v143,v123,0 op_sel:[0,1,0] op_sel_hi:[0,1,0]
v_mfma_f32_16x16x32_bf16 v[148:151],v[84:87],v[56:59],v[148:151]
ds_read_b64_tr_b16 v[110:111],v6 offset:1672
ds_read_b64_tr_b16 v[64:65],v4 offset:0
v_lshlrev_b32_sdwa v144,1,v144 dst_sel:DWORD dst_unused:UNUSED_PAD src0_sel:DWORD src1_sel:WORD_1
v_lshlrev_b32_sdwa v146,1,v146 dst_sel:DWORD dst_unused:UNUSED_PAD src0_sel:DWORD src1_sel:WORD_1
v_mfma_f32_16x16x32_bf16 v[160:163],v[88:91],v[128:131],v[160:163]
ds_read_u16_d16_hi v139,v139
v_lshlrev_b32_sdwa v145,1,v145 dst_sel:DWORD dst_unused:UNUSED_PAD src0_sel:DWORD src1_sel:WORD_1
v_lshlrev_b32_sdwa v147,1,v147 dst_sel:DWORD dst_unused:UNUSED_PAD src0_sel:DWORD src1_sel:WORD_1
v_fma_mixlo_f16 v122,v157,s99,0
v_mfma_f32_16x16x32_bf16 v[168:171],v[92:95],v[128:131],v[168:171]
s_waitcnt lgkmcnt(11)
s_cbranch_vccz RA68_%=
s_add_u32 m0,s100,24640
s_mul_i32 s69,s59,1
s_add_u32 s69,s69,s97
buffer_load_dwordx4 v2,s[44:47],s69 offen lds
s_add_u32 m0,m0,1024
s_add_u32 s69,s69,s53
buffer_load_dwordx4 v3,s[44:47],s69 offen lds
s_add_u32 m0,m0,1024
s_add_u32 s69,s69,s53
buffer_load_dwordx4 v2,s[44:47],s69 offen lds
s_add_u32 m0,m0,s50
s_add_u32 s69,s69,s53
buffer_load_dwordx4 v3,s[44:47],s69 offen lds
RA68_%=:
ds_read_b64_tr_b16 v[66:67],v4 offset:2048
ds_read_b128 v[112:115],v7 offset:768
v_fma_mixlo_f16 v120,v156,s99,0
v_mfma_f32_16x16x32_bf16 v[176:179],v[96:99],v[128:131],v[176:179]
s_waitcnt lgkmcnt(10)
ds_read_u16 v144,v144
ds_read_b64_tr_b16 v[68:69],v5 offset:0
v_lshlrev_b32_sdwa v148,1,v148 dst_sel:DWORD dst_unused:UNUSED_PAD src0_sel:DWORD src1_sel:WORD_1
v_lshlrev_b32_sdwa v150,1,v150 dst_sel:DWORD dst_unused:UNUSED_PAD src0_sel:DWORD src1_sel:WORD_1
v_mfma_f32_16x16x32_bf16 v[184:187],v[100:103],v[128:131],v[184:187]
s_waitcnt lgkmcnt(4)
ds_read_b64_tr_b16 v[70:71],v5 offset:2048
ds_read_b128 v[116:119],v7 offset:800
v_lshl_add_u64 v[136:137],v[136:137],0,v[138:139]
v_fma_mixhi_f16 v120,v158,s99,0
v_mfma_f32_16x16x32_bf16 v[192:195],v[104:107],v[128:131],v[192:195]
ds_read_u16_d16_hi v146,v146
v_cvt_pk_bf16_f32 v138,v140,v142
v_cvt_pk_bf16_f32 v139,v141,v143
v_mfma_f32_16x16x32_bf16 v[200:203],v[108:111],v[128:131],v[200:203]
s_cbranch_vccz RA69_%=
s_add_u32 m0,m0,1024
s_add_u32 s69,s69,s53
buffer_load_dwordx4 v2,s[44:47],s69 offen lds
s_add_u32 m0,m0,1024
s_add_u32 s69,s69,s53
buffer_load_dwordx4 v3,s[44:47],s69 offen lds
s_add_u32 m0,m0,s50
s_add_u32 s69,s69,s53
buffer_load_dwordx4 v2,s[44:47],s69 offen lds
s_add_u32 m0,m0,1024
s_add_u32 s69,s69,s53
buffer_load_dwordx4 v3,s[44:47],s69 offen lds
RA69_%=:
v_fma_mixhi_f16 v122,v159,s99,0
v_mfma_f32_16x16x32_bf16 v[164:167],v[88:91],v[136:139],v[164:167]
ds_read_b64_tr_b16 v[72:73],v4 offset:4096
v_lshlrev_b32_sdwa v149,1,v149 dst_sel:DWORD dst_unused:UNUSED_PAD src0_sel:DWORD src1_sel:WORD_1
v_lshlrev_b32_sdwa v151,1,v151 dst_sel:DWORD dst_unused:UNUSED_PAD src0_sel:DWORD src1_sel:WORD_1
v_mfma_f32_16x16x32_bf16 v[172:175],v[92:95],v[136:139],v[172:175]
ds_read_b64_tr_b16 v[74:75],v4 offset:6144
ds_read_b64_tr_b16 v[76:77],v5 offset:4096
v_pk_fma_f16 v121,v120,v120,s54
v_pk_fma_f16 v123,v122,v122,s54
v_mfma_f32_16x16x32_bf16 v[180:183],v[96:99],v[136:139],v[180:183]
ds_read_u16 v145,v145
v_pk_mul_f16 v121,v120,v121
v_pk_mul_f16 v123,v122,v123
v_mfma_f32_16x16x32_bf16 v[188:191],v[100:103],v[136:139],v[188:191]
s_cbranch_vccz RA70_%=
s_add_u32 m0,m0,1024
s_add_u32 s69,s69,s53
buffer_load_dwordx4 v2,s[44:47],s69 offen lds
s_add_u32 m0,m0,s50
s_add_u32 s69,s69,s53
buffer_load_dwordx4 v3,s[44:47],s69 offen lds
s_add_u32 m0,m0,1024
s_add_u32 s69,s69,s53
buffer_load_dwordx4 v2,s[44:47],s69 offen lds
s_add_u32 m0,m0,1024
s_add_u32 s69,s69,s53
buffer_load_dwordx4 v3,s[44:47],s69 offen lds
RA70_%=:
v_lshlrev_b32_sdwa v152,1,v152 dst_sel:DWORD dst_unused:UNUSED_PAD src0_sel:DWORD src1_sel:WORD_1
v_lshlrev_b32_sdwa v154,1,v154 dst_sel:DWORD dst_unused:UNUSED_PAD src0_sel:DWORD src1_sel:WORD_1
v_mfma_f32_16x16x32_bf16 v[196:199],v[104:107],v[136:139],v[196:199]
ds_read_u16_d16_hi v147,v147
v_exp_f16 v120,v121
v_mfma_f32_16x16x32_bf16 v[204:207],v[108:111],v[136:139],v[204:207]
s_waitcnt lgkmcnt(10)
ds_read_b64_tr_b16 v[78:79],v5 offset:6144
ds_read_b64_tr_b16 v[80:81],v4 offset:8192
v_exp_f16 v122,v123
v_mfma_f32_16x16x32_bf16 v[128:131],v[64:67],v[16:19],v[112:115]
v_lshlrev_b32_sdwa v153,1,v153 dst_sel:DWORD dst_unused:UNUSED_PAD src0_sel:DWORD src1_sel:WORD_1
v_lshlrev_b32_sdwa v155,1,v155 dst_sel:DWORD dst_unused:UNUSED_PAD src0_sel:DWORD src1_sel:WORD_1
v_mfma_f32_16x16x32_bf16 v[136:139],v[64:67],v[20:23],v[112:115]
s_waitcnt lgkmcnt(8)
ds_read_u16 v148,v148
ds_read_u16_d16_hi v150,v150
v_exp_f16_sdwa v120,v121 dst_sel:WORD_1 dst_unused:UNUSED_PRESERVE src0_sel:WORD_1
v_mfma_f32_16x16x32_bf16 v[140:143],v[68:71],v[20:23],v[116:119]
v_exp_f16_sdwa v122,v123 dst_sel:WORD_1 dst_unused:UNUSED_PRESERVE src0_sel:WORD_1
v_mfma_f32_16x16x32_bf16 v[132:135],v[68:71],v[16:19],v[116:119]
s_waitcnt lgkmcnt(7)
ds_read_b64_tr_b16 v[82:83],v4 offset:10240
v_pk_fma_f16 v120,v120,s55,s55
v_mfma_f32_16x16x32_bf16 v[128:131],v[72:75],v[24:27],v[128:131]
ds_read_b64_tr_b16 v[84:85],v5 offset:8192
v_pk_fma_f16 v122,v122,s55,s55
v_rcp_f16 v121,v120
v_mfma_f32_16x16x32_bf16 v[136:139],v[72:75],v[28:31],v[136:139]
s_waitcnt lgkmcnt(5)
ds_read_b64_tr_b16 v[86:87],v5 offset:10240
v_rcp_f16 v123,v122
v_mfma_f32_16x16x32_bf16 v[140:143],v[76:79],v[28:31],v[140:143]
ds_read_u16 v149,v149
ds_read_u16_d16_hi v151,v151
v_rcp_f16_sdwa v121,v120 dst_sel:WORD_1 dst_unused:UNUSED_PRESERVE src0_sel:WORD_1
v_mfma_f32_16x16x32_bf16 v[132:135],v[76:79],v[24:27],v[132:135]
s_waitcnt lgkmcnt(4)
s_setprio 2
ds_read_u16 v152,v152
v_rcp_f16_sdwa v123,v122 dst_sel:WORD_1 dst_unused:UNUSED_PRESERVE src0_sel:WORD_1
v_mfma_f32_16x16x32_bf16 v[128:131],v[80:83],v[32:35],v[128:131]
ds_read_u16_d16_hi v154,v154
v_fma_mix_f32 v156,v156,v121,0 op_sel_hi:[0,1,0]
v_fma_mix_f32 v158,v158,v121,0 op_sel:[0,1,0] op_sel_hi:[0,1,0]
v_mfma_f32_16x16x32_bf16 v[136:139],v[80:83],v[36:39],v[136:139]
s_waitcnt lgkmcnt(2)
v_lshl_add_u64 v[144:145],v[144:145],0,v[146:147]
v_lshl_add_u64 v[146:147],v[148:149],0,v[150:151]
v_mfma_f32_16x16x32_bf16 v[140:143],v[84:87],v[36:39],v[140:143]
ds_read_u16 v153,v153
v_fma_mix_f32 v157,v157,v123,0 op_sel_hi:[0,1,0]
v_fma_mix_f32 v159,v159,v123,0 op_sel:[0,1,0] op_sel_hi:[0,1,0]
v_mfma_f32_16x16x32_bf16 v[132:135],v[84:87],v[32:35],v[132:135]
v_lshlrev_b32_sdwa v128,1,v128 dst_sel:DWORD dst_unused:UNUSED_PAD src0_sel:DWORD src1_sel:WORD_1
v_lshlrev_b32_sdwa v130,1,v130 dst_sel:DWORD dst_unused:UNUSED_PAD src0_sel:DWORD src1_sel:WORD_1
v_mfma_f32_16x16x32_bf16 v[208:211],v[88:91],v[144:147],v[208:211]
v_lshlrev_b32_sdwa v129,1,v129 dst_sel:DWORD dst_unused:UNUSED_PAD src0_sel:DWORD src1_sel:WORD_1
v_lshlrev_b32_sdwa v131,1,v131 dst_sel:DWORD dst_unused:UNUSED_PAD src0_sel:DWORD src1_sel:WORD_1
v_mfma_f32_16x16x32_bf16 v[216:219],v[92:95],v[144:147],v[216:219]
ds_read_u16_d16_hi v155,v155
ds_read_u16 v128,v128
v_fma_mixlo_f16 v120,v140,s99,0
v_fma_mixlo_f16 v122,v141,s99,0
v_mfma_f32_16x16x32_bf16 v[224:227],v[96:99],v[144:147],v[224:227]
ds_read_u16_d16_hi v130,v130
v_lshlrev_b32_sdwa v132,1,v132 dst_sel:DWORD dst_unused:UNUSED_PAD src0_sel:DWORD src1_sel:WORD_1
v_lshlrev_b32_sdwa v134,1,v134 dst_sel:DWORD dst_unused:UNUSED_PAD src0_sel:DWORD src1_sel:WORD_1
v_mfma_f32_16x16x32_bf16 v[232:235],v[100:103],v[144:147],v[232:235]
s_waitcnt lgkmcnt(2)
v_lshl_add_u64 v[152:153],v[152:153],0,v[154:155]
v_cvt_pk_bf16_f32 v154,v156,v158
v_mfma_f32_16x16x32_bf16 v[240:243],v[104:107],v[144:147],v[240:243]
v_fma_mixhi_f16 v120,v142,s99,0
v_cvt_pk_bf16_f32 v155,v157,v159
v_mfma_f32_16x16x32_bf16 v[248:251],v[108:111],v[144:147],v[248:251]
ds_read_u16 v129,v129
v_fma_mixhi_f16 v122,v143,s99,0
v_mfma_f32_16x16x32_bf16 v[212:215],v[88:91],v[152:155],v[212:215]
v_lshlrev_b32_sdwa v133,1,v133 dst_sel:DWORD dst_unused:UNUSED_PAD src0_sel:DWORD src1_sel:WORD_1
v_pk_fma_f16 v121,v120,v120,s54
v_lshlrev_b32_sdwa v135,1,v135 dst_sel:DWORD dst_unused:UNUSED_PAD src0_sel:DWORD src1_sel:WORD_1
v_mfma_f32_16x16x32_bf16 v[220:223],v[92:95],v[152:155],v[220:223]
ds_read_u16_d16_hi v131,v131
ds_read_u16 v132,v132
v_pk_fma_f16 v123,v122,v122,s54
v_mfma_f32_16x16x32_bf16 v[228:231],v[96:99],v[152:155],v[228:231]
ds_read_b64_tr_b16 v[88:89],v6 offset:6160
v_pk_mul_f16 v121,v120,v121
v_lshlrev_b32_sdwa v136,1,v136 dst_sel:DWORD dst_unused:UNUSED_PAD src0_sel:DWORD src1_sel:WORD_1
v_pk_mul_f16 v123,v122,v123
v_mfma_f32_16x16x32_bf16 v[236:239],v[100:103],v[152:155],v[236:239]
ds_read_b64_tr_b16 v[90:91],v6 offset:7696
v_lshlrev_b32_sdwa v138,1,v138 dst_sel:DWORD dst_unused:UNUSED_PAD src0_sel:DWORD src1_sel:WORD_1
v_mfma_f32_16x16x32_bf16 v[244:247],v[104:107],v[152:155],v[244:247]
ds_read_u16_d16_hi v134,v134
v_mfma_f32_16x16x32_bf16 v[252:255],v[108:111],v[152:155],v[252:255]
ds_read_b64_tr_b16 v[92:93],v6 offset:6168
v_lshlrev_b32_sdwa v137,1,v137 dst_sel:DWORD dst_unused:UNUSED_PAD src0_sel:DWORD src1_sel:WORD_1
v_exp_f16 v120,v121
v_mfma_f32_16x16x32_bf16 v[144:147],v[64:67],v[40:43],v[112:115]
ds_read_b64_tr_b16 v[94:95],v6 offset:7704
v_lshlrev_b32_sdwa v139,1,v139 dst_sel:DWORD dst_unused:UNUSED_PAD src0_sel:DWORD src1_sel:WORD_1
v_mfma_f32_16x16x32_bf16 v[152:155],v[64:67],v[44:47],v[112:115]
ds_read_u16 v133,v133
ds_read_b64_tr_b16 v[96:97],v6 offset:6224
v_exp_f16 v122,v123
v_mfma_f32_16x16x32_bf16 v[156:159],v[68:71],v[44:47],v[116:119]
ds_read_u16_d16_hi v135,v135
v_exp_f16_sdwa v122,v123 dst_sel:WORD_1 dst_unused:UNUSED_PRESERVE src0_sel:WORD_1
v_exp_f16_sdwa v120,v121 dst_sel:WORD_1 dst_unused:UNUSED_PRESERVE src0_sel:WORD_1
v_mfma_f32_16x16x32_bf16 v[148:151],v[68:71],v[40:43],v[116:119]
ds_read_b64_tr_b16 v[98:99],v6 offset:7760
v_pk_fma_f16 v120,v120,s55,s55
v_pk_fma_f16 v122,v122,s55,s55
v_mfma_f32_16x16x32_bf16 v[144:147],v[72:75],v[48:51],v[144:147]
v_rcp_f16 v121,v120
v_mfma_f32_16x16x32_bf16 v[152:155],v[72:75],v[52:55],v[152:155]
ds_read_b64_tr_b16 v[100:101],v6 offset:6232
v_rcp_f16 v123,v122
v_mfma_f32_16x16x32_bf16 v[156:159],v[76:79],v[52:55],v[156:159]
ds_read_u16 v136,v136
v_rcp_f16_sdwa v121,v120 dst_sel:WORD_1 dst_unused:UNUSED_PRESERVE src0_sel:WORD_1
v_mfma_f32_16x16x32_bf16 v[148:151],v[76:79],v[48:51],v[148:151]
ds_read_b64_tr_b16 v[102:103],v6 offset:7768
ds_read_b64_tr_b16 v[104:105],v6 offset:6288
v_rcp_f16_sdwa v123,v122 dst_sel:WORD_1 dst_unused:UNUSED_PRESERVE src0_sel:WORD_1
v_mfma_f32_16x16x32_bf16 v[144:147],v[80:83],v[56:59],v[144:147]
ds_read_u16_d16_hi v138,v138
v_fma_mix_f32 v142,v142,v121,0 op_sel:[0,1,0] op_sel_hi:[0,1,0]
v_mfma_f32_16x16x32_bf16 v[152:155],v[80:83],v[60:63],v[152:155]
s_waitcnt lgkmcnt(6)
ds_read_b64_tr_b16 v[106:107],v6 offset:7824
ds_read_b64_tr_b16 v[108:109],v6 offset:6296
v_fma_mix_f32 v140,v140,v121,0 op_sel_hi:[0,1,0]
v_lshl_add_u64 v[128:129],v[128:129],0,v[130:131]
v_lshl_add_u64 v[130:131],v[132:133],0,v[134:135]
v_mfma_f32_16x16x32_bf16 v[156:159],v[84:87],v[60:63],v[156:159]
ds_read_u16 v137,v137
v_fma_mix_f32 v141,v141,v123,0 op_sel_hi:[0,1,0]
v_fma_mix_f32 v143,v143,v123,0 op_sel:[0,1,0] op_sel_hi:[0,1,0]
v_mfma_f32_16x16x32_bf16 v[148:151],v[84:87],v[56:59],v[148:151]
s_setprio 1
ds_read_b64_tr_b16 v[110:111],v6 offset:7832
ds_read_b128 v[64:67],v7 offset:1536
v_lshlrev_b32_sdwa v144,1,v144 dst_sel:DWORD dst_unused:UNUSED_PAD src0_sel:DWORD src1_sel:WORD_1
v_lshlrev_b32_sdwa v146,1,v146 dst_sel:DWORD dst_unused:UNUSED_PAD src0_sel:DWORD src1_sel:WORD_1
v_mfma_f32_16x16x32_bf16 v[160:163],v[88:91],v[128:131],v[160:163]
ds_read_u16_d16_hi v139,v139
ds_read_b128 v[68:71],v7 offset:1568
v_lshlrev_b32_sdwa v145,1,v145 dst_sel:DWORD dst_unused:UNUSED_PAD src0_sel:DWORD src1_sel:WORD_1
v_lshlrev_b32_sdwa v147,1,v147 dst_sel:DWORD dst_unused:UNUSED_PAD src0_sel:DWORD src1_sel:WORD_1
v_fma_mixlo_f16 v122,v157,s99,0
v_fma_mixlo_f16 v120,v156,s99,0
v_mfma_f32_16x16x32_bf16 v[168:171],v[92:95],v[128:131],v[168:171]
s_waitcnt lgkmcnt(12)
ds_read_u16 v144,v144
ds_read_b128 v[72:75],v7 offset:1664
v_lshlrev_b32_sdwa v148,1,v148 dst_sel:DWORD dst_unused:UNUSED_PAD src0_sel:DWORD src1_sel:WORD_1
v_lshlrev_b32_sdwa v150,1,v150 dst_sel:DWORD dst_unused:UNUSED_PAD src0_sel:DWORD src1_sel:WORD_1
s_waitcnt lgkmcnt(3)
v_lshl_add_u64 v[136:137],v[136:137],0,v[138:139]
v_fma_mixhi_f16 v120,v158,s99,0
v_mfma_f32_16x16x32_bf16 v[176:179],v[96:99],v[128:131],v[176:179]
ds_read_u16_d16_hi v146,v146
ds_read_b128 v[76:79],v7 offset:1696
v_cvt_pk_bf16_f32 v138,v140,v142
v_cvt_pk_bf16_f32 v139,v141,v143
v_fma_mixhi_f16 v122,v159,s99,0
v_lshlrev_b32_sdwa v149,1,v149 dst_sel:DWORD dst_unused:UNUSED_PAD src0_sel:DWORD src1_sel:WORD_1
v_mfma_f32_16x16x32_bf16 v[184:187],v[100:103],v[128:131],v[184:187]
ds_read_u16 v145,v145
ds_read_b128 v[80:83],v7 offset:1792
v_lshlrev_b32_sdwa v151,1,v151 dst_sel:DWORD dst_unused:UNUSED_PAD src0_sel:DWORD src1_sel:WORD_1
v_pk_fma_f16 v121,v120,v120,s54
v_pk_fma_f16 v123,v122,v122,s54
v_pk_mul_f16 v121,v120,v121
v_mfma_f32_16x16x32_bf16 v[192:195],v[104:107],v[128:131],v[192:195]
ds_read_u16_d16_hi v147,v147
ds_read_b128 v[84:87],v7 offset:1824
v_pk_mul_f16 v123,v122,v123
v_lshlrev_b32_sdwa v152,1,v152 dst_sel:DWORD dst_unused:UNUSED_PAD src0_sel:DWORD src1_sel:WORD_1
v_lshlrev_b32_sdwa v154,1,v154 dst_sel:DWORD dst_unused:UNUSED_PAD src0_sel:DWORD src1_sel:WORD_1
v_exp_f16 v120,v121
v_mfma_f32_16x16x32_bf16 v[200:203],v[108:111],v[128:131],v[200:203]
ds_read_u16 v148,v148
v_exp_f16 v122,v123
v_lshlrev_b32_sdwa v153,1,v153 dst_sel:DWORD dst_unused:UNUSED_PAD src0_sel:DWORD src1_sel:WORD_1
v_mfma_f32_16x16x32_bf16 v[164:167],v[88:91],v[136:139],v[164:167]
ds_read_u16_d16_hi v150,v150
v_lshlrev_b32_sdwa v155,1,v155 dst_sel:DWORD dst_unused:UNUSED_PAD src0_sel:DWORD src1_sel:WORD_1
v_exp_f16_sdwa v120,v121 dst_sel:WORD_1 dst_unused:UNUSED_PRESERVE src0_sel:WORD_1
v_exp_f16_sdwa v122,v123 dst_sel:WORD_1 dst_unused:UNUSED_PRESERVE src0_sel:WORD_1
v_mfma_f32_16x16x32_bf16 v[172:175],v[92:95],v[136:139],v[172:175]
ds_read_u16 v149,v149
v_pk_fma_f16 v120,v120,s55,s55
v_pk_fma_f16 v122,v122,s55,s55
v_rcp_f16 v121,v120
v_mfma_f32_16x16x32_bf16 v[180:183],v[96:99],v[136:139],v[180:183]
s_nop 0
ds_read_u16_d16_hi v151,v151
v_rcp_f16 v123,v122
v_rcp_f16_sdwa v121,v120 dst_sel:WORD_1 dst_unused:UNUSED_PRESERVE src0_sel:WORD_1
v_mfma_f32_16x16x32_bf16 v[188:191],v[100:103],v[136:139],v[188:191]
ds_read_u16 v152,v152
v_rcp_f16_sdwa v123,v122 dst_sel:WORD_1 dst_unused:UNUSED_PRESERVE src0_sel:WORD_1
v_fma_mix_f32 v156,v156,v121,0 op_sel_hi:[0,1,0]
v_mfma_f32_16x16x32_bf16 v[196:199],v[104:107],v[136:139],v[196:199]
s_setprio 0
ds_read_u16_d16_hi v154,v154
v_fma_mix_f32 v158,v158,v121,0 op_sel:[0,1,0] op_sel_hi:[0,1,0]
s_waitcnt lgkmcnt(7)
v_lshl_add_u64 v[144:145],v[144:145],0,v[146:147]
s_waitcnt lgkmcnt(2)
v_lshl_add_u64 v[146:147],v[148:149],0,v[150:151]
v_fma_mix_f32 v157,v157,v123,0 op_sel_hi:[0,1,0]
v_mfma_f32_16x16x32_bf16 v[204:207],v[108:111],v[136:139],v[204:207]
s_nop 0
ds_read_u16 v153,v153
v_fma_mix_f32 v159,v159,v123,0 op_sel:[0,1,0] op_sel_hi:[0,1,0]
ds_read_u16_d16_hi v155,v155
s_waitcnt lgkmcnt(0)
v_lshl_add_u64 v[152:153],v[152:153],0,v[154:155]
v_cvt_pk_bf16_f32 v154,v156,v158
v_cvt_pk_bf16_f32 v155,v157,v159
v_mfma_f32_16x16x32_bf16 v[208:211],v[88:91],v[144:147],v[208:211]
s_waitcnt vmcnt(12)
s_barrier
s_setprio 0
v_xor_b32 v15,64,v10
v_mul_f32 v160,v160,v64
v_mul_f32 v161,v161,v65
v_mul_f32 v162,v162,v66
v_mul_f32 v163,v163,v67
v_mfma_f32_16x16x32_bf16 v[216:219],v[92:95],v[144:147],v[216:219]
v_mul_f32 v168,v168,v68
v_mul_f32 v169,v169,v69
v_mul_f32 v170,v170,v70
v_mul_f32 v171,v171,v71
v_cvt_pk_bf16_f32 v16,v160,v161
v_mfma_f32_16x16x32_bf16 v[224:227],v[96:99],v[144:147],v[224:227]
v_cvt_pk_bf16_f32 v17,v162,v163
v_cvt_pk_bf16_f32 v18,v168,v169
v_cvt_pk_bf16_f32 v19,v170,v171
v_mul_f32_dpp v176,v176,v72 quad_perm:[1,0,3,2] row_mask:0xf bank_mask:0xf
v_mfma_f32_16x16x32_bf16 v[232:235],v[100:103],v[144:147],v[232:235]
v_mul_f32_dpp v177,v177,v73 quad_perm:[1,0,3,2] row_mask:0xf bank_mask:0xf
v_mul_f32_dpp v178,v178,v74 quad_perm:[1,0,3,2] row_mask:0xf bank_mask:0xf
v_mul_f32_dpp v179,v179,v75 quad_perm:[1,0,3,2] row_mask:0xf bank_mask:0xf
v_mul_f32_dpp v184,v184,v76 quad_perm:[1,0,3,2] row_mask:0xf bank_mask:0xf
v_mul_f32_dpp v185,v185,v77 quad_perm:[1,0,3,2] row_mask:0xf bank_mask:0xf
v_mfma_f32_16x16x32_bf16 v[240:243],v[104:107],v[144:147],v[240:243]
v_mul_f32_dpp v186,v186,v78 quad_perm:[1,0,3,2] row_mask:0xf bank_mask:0xf
v_mul_f32_dpp v187,v187,v79 quad_perm:[1,0,3,2] row_mask:0xf bank_mask:0xf
v_cvt_pk_bf16_f32 v24,v176,v177
v_cvt_pk_bf16_f32 v25,v178,v179
v_mfma_f32_16x16x32_bf16 v[248:251],v[108:111],v[144:147],v[248:251]
v_cvt_pk_bf16_f32 v26,v184,v185
v_cvt_pk_bf16_f32 v27,v186,v187
s_mov_b64 exec,s[12:13]
v_swap_b32 v16,v24
v_swap_b32 v17,v25
v_swap_b32 v18,v26
v_swap_b32 v19,v27
s_mov_b64 exec,-1
v_mfma_f32_16x16x32_bf16 v[212:215],v[88:91],v[152:155],v[212:215]
s_add_u32 s65,s61,3145728
buffer_store_dwordx4 v[16:19],v10,s[40:43],s65 offen offset:0 sc1
v_mfma_f32_16x16x32_bf16 v[220:223],v[92:95],v[152:155],v[220:223]
v_mul_f32 v192,v192,v80
v_mul_f32 v193,v193,v81
v_mul_f32 v194,v194,v82
v_mul_f32 v195,v195,v83
v_mfma_f32_16x16x32_bf16 v[228:231],v[96:99],v[152:155],v[228:231]
v_mul_f32 v200,v200,v84
v_mul_f32 v201,v201,v85
v_mul_f32 v202,v202,v86
v_mul_f32 v203,v203,v87
v_mfma_f32_16x16x32_bf16 v[236:239],v[100:103],v[152:155],v[236:239]
v_cvt_pk_bf16_f32 v32,v192,v193
v_cvt_pk_bf16_f32 v33,v194,v195
v_cvt_pk_bf16_f32 v34,v200,v201
v_cvt_pk_bf16_f32 v35,v202,v203
v_mul_f32_dpp v164,v164,v64 quad_perm:[1,0,3,2] row_mask:0xf bank_mask:0xf
v_mfma_f32_16x16x32_bf16 v[244:247],v[104:107],v[152:155],v[244:247]
v_mul_f32_dpp v165,v165,v65 quad_perm:[1,0,3,2] row_mask:0xf bank_mask:0xf
v_mul_f32_dpp v166,v166,v66 quad_perm:[1,0,3,2] row_mask:0xf bank_mask:0xf
v_mul_f32_dpp v167,v167,v67 quad_perm:[1,0,3,2] row_mask:0xf bank_mask:0xf
v_mul_f32_dpp v172,v172,v68 quad_perm:[1,0,3,2] row_mask:0xf bank_mask:0xf
v_mfma_f32_16x16x32_bf16 v[252:255],v[108:111],v[152:155],v[252:255]
v_mul_f32_dpp v173,v173,v69 quad_perm:[1,0,3,2] row_mask:0xf bank_mask:0xf
v_mul_f32_dpp v174,v174,v70 quad_perm:[1,0,3,2] row_mask:0xf bank_mask:0xf
v_mul_f32_dpp v175,v175,v71 quad_perm:[1,0,3,2] row_mask:0xf bank_mask:0xf
v_cvt_pk_bf16_f32 v20,v164,v165
v_cvt_pk_bf16_f32 v21,v166,v167
v_cvt_pk_bf16_f32 v22,v172,v173
v_cvt_pk_bf16_f32 v23,v174,v175
s_mov_b64 exec,s[12:13]
v_swap_b32 v32,v20
v_swap_b32 v33,v21
v_swap_b32 v34,v22
v_swap_b32 v35,v23
s_mov_b64 exec,-1
v_mul_f32 v180,v180,v72
v_mul_f32 v181,v181,v73
v_mul_f32 v182,v182,v74
v_mul_f32 v183,v183,v75
v_mul_f32 v188,v188,v76
v_mul_f32 v189,v189,v77
v_mul_f32 v190,v190,v78
v_mul_f32 v191,v191,v79
v_cvt_pk_bf16_f32 v28,v180,v181
s_add_u32 s65,s61,3145728
buffer_store_dwordx4 v[24:27],v15,s[40:43],s65 offen offset:384 sc1
v_cvt_pk_bf16_f32 v29,v182,v183
v_cvt_pk_bf16_f32 v30,v188,v189
v_cvt_pk_bf16_f32 v31,v190,v191
v_mul_f32_dpp v196,v196,v80 quad_perm:[1,0,3,2] row_mask:0xf bank_mask:0xf
v_mul_f32_dpp v197,v197,v81 quad_perm:[1,0,3,2] row_mask:0xf bank_mask:0xf
v_mul_f32_dpp v198,v198,v82 quad_perm:[1,0,3,2] row_mask:0xf bank_mask:0xf
v_mul_f32_dpp v199,v199,v83 quad_perm:[1,0,3,2] row_mask:0xf bank_mask:0xf
v_mul_f32_dpp v204,v204,v84 quad_perm:[1,0,3,2] row_mask:0xf bank_mask:0xf
v_mul_f32_dpp v205,v205,v85 quad_perm:[1,0,3,2] row_mask:0xf bank_mask:0xf
v_mul_f32_dpp v206,v206,v86 quad_perm:[1,0,3,2] row_mask:0xf bank_mask:0xf
v_mul_f32_dpp v207,v207,v87 quad_perm:[1,0,3,2] row_mask:0xf bank_mask:0xf
v_cvt_pk_bf16_f32 v36,v196,v197
v_cvt_pk_bf16_f32 v37,v198,v199
v_cvt_pk_bf16_f32 v38,v204,v205
v_cvt_pk_bf16_f32 v39,v206,v207
s_mov_b64 exec,s[12:13]
v_swap_b32 v28,v36
v_swap_b32 v29,v37
v_swap_b32 v30,v38
v_swap_b32 v31,v39
s_mov_b64 exec,-1
v_mul_f32 v208,v208,v64
v_mul_f32 v209,v209,v65
v_mul_f32 v210,v210,v66
v_mul_f32 v211,v211,v67
v_mul_f32 v216,v216,v68
v_mul_f32 v217,v217,v69
v_mul_f32 v218,v218,v70
v_mul_f32 v219,v219,v71
v_cvt_pk_bf16_f32 v40,v208,v209
v_cvt_pk_bf16_f32 v41,v210,v211
v_cvt_pk_bf16_f32 v42,v216,v217
v_cvt_pk_bf16_f32 v43,v218,v219
v_mul_f32_dpp v224,v224,v72 quad_perm:[1,0,3,2] row_mask:0xf bank_mask:0xf
v_mul_f32_dpp v225,v225,v73 quad_perm:[1,0,3,2] row_mask:0xf bank_mask:0xf
v_mul_f32_dpp v226,v226,v74 quad_perm:[1,0,3,2] row_mask:0xf bank_mask:0xf
v_mul_f32_dpp v227,v227,v75 quad_perm:[1,0,3,2] row_mask:0xf bank_mask:0xf
v_mul_f32_dpp v232,v232,v76 quad_perm:[1,0,3,2] row_mask:0xf bank_mask:0xf
v_mul_f32_dpp v233,v233,v77 quad_perm:[1,0,3,2] row_mask:0xf bank_mask:0xf
v_mul_f32_dpp v234,v234,v78 quad_perm:[1,0,3,2] row_mask:0xf bank_mask:0xf
v_mul_f32_dpp v235,v235,v79 quad_perm:[1,0,3,2] row_mask:0xf bank_mask:0xf
v_cvt_pk_bf16_f32 v48,v224,v225
v_cvt_pk_bf16_f32 v49,v226,v227
s_nop 0
s_mov_b32 s86,0xbe801d5a
v_cvt_pk_bf16_f32 v50,v232,v233
v_cvt_pk_bf16_f32 v51,v234,v235
s_mov_b64 exec,s[12:13]
v_swap_b32 v40,v48
v_swap_b32 v41,v49
v_swap_b32 v42,v50
v_swap_b32 v43,v51
s_mov_b64 exec,-1
s_add_u32 s65,s61,3145728
buffer_store_dwordx4 v[32:35],v10,s[40:43],s65 offen offset:128 sc1
v_mul_f32 v240,v240,v80
v_mul_f32 v241,v241,v81
v_mul_f32 v242,v242,v82
v_mul_f32 v243,v243,v83
v_mul_f32 v248,v248,v84
v_mul_f32 v249,v249,v85
v_mul_f32 v250,v250,v86
v_mul_f32 v251,v251,v87
v_cvt_pk_bf16_f32 v56,v240,v241
v_cvt_pk_bf16_f32 v57,v242,v243
v_cvt_pk_bf16_f32 v58,v248,v249
v_cvt_pk_bf16_f32 v59,v250,v251
v_mul_f32_dpp v212,v212,v64 quad_perm:[1,0,3,2] row_mask:0xf bank_mask:0xf
v_mul_f32_dpp v213,v213,v65 quad_perm:[1,0,3,2] row_mask:0xf bank_mask:0xf
v_mul_f32_dpp v214,v214,v66 quad_perm:[1,0,3,2] row_mask:0xf bank_mask:0xf
v_mul_f32_dpp v215,v215,v67 quad_perm:[1,0,3,2] row_mask:0xf bank_mask:0xf
v_mul_f32_dpp v220,v220,v68 quad_perm:[1,0,3,2] row_mask:0xf bank_mask:0xf
v_mul_f32_dpp v221,v221,v69 quad_perm:[1,0,3,2] row_mask:0xf bank_mask:0xf
v_mul_f32_dpp v222,v222,v70 quad_perm:[1,0,3,2] row_mask:0xf bank_mask:0xf
v_mul_f32_dpp v223,v223,v71 quad_perm:[1,0,3,2] row_mask:0xf bank_mask:0xf
v_cvt_pk_bf16_f32 v44,v212,v213
v_cvt_pk_bf16_f32 v45,v214,v215
v_cvt_pk_bf16_f32 v46,v220,v221
v_cvt_pk_bf16_f32 v47,v222,v223
s_mov_b64 exec,s[12:13]
v_swap_b32 v56,v44
v_swap_b32 v57,v45
v_swap_b32 v58,v46
v_swap_b32 v59,v47
s_mov_b64 exec,-1
v_mul_f32 v228,v228,v72
v_mul_f32 v229,v229,v73
v_mul_f32 v230,v230,v74
v_mul_f32 v231,v231,v75
v_mul_f32 v236,v236,v76
v_mul_f32 v237,v237,v77
v_mul_f32 v238,v238,v78
v_mul_f32 v239,v239,v79
v_cvt_pk_bf16_f32 v52,v228,v229
v_cvt_pk_bf16_f32 v53,v230,v231
v_cvt_pk_bf16_f32 v54,v236,v237
v_cvt_pk_bf16_f32 v55,v238,v239
v_mul_f32_dpp v244,v244,v80 quad_perm:[1,0,3,2] row_mask:0xf bank_mask:0xf
v_mul_f32_dpp v245,v245,v81 quad_perm:[1,0,3,2] row_mask:0xf bank_mask:0xf
v_mul_f32_dpp v246,v246,v82 quad_perm:[1,0,3,2] row_mask:0xf bank_mask:0xf
v_mul_f32_dpp v247,v247,v83 quad_perm:[1,0,3,2] row_mask:0xf bank_mask:0xf
v_mul_f32_dpp v252,v252,v84 quad_perm:[1,0,3,2] row_mask:0xf bank_mask:0xf
v_mul_f32_dpp v253,v253,v85 quad_perm:[1,0,3,2] row_mask:0xf bank_mask:0xf
s_add_u32 s65,s61,3145728
buffer_store_dwordx4 v[20:23],v15,s[40:43],s65 offen offset:512 sc1
v_mul_f32_dpp v254,v254,v86 quad_perm:[1,0,3,2] row_mask:0xf bank_mask:0xf
v_mul_f32_dpp v255,v255,v87 quad_perm:[1,0,3,2] row_mask:0xf bank_mask:0xf
v_cvt_pk_bf16_f32 v60,v244,v245
v_cvt_pk_bf16_f32 v61,v246,v247
v_cvt_pk_bf16_f32 v62,v252,v253
v_cvt_pk_bf16_f32 v63,v254,v255
s_mov_b64 exec,s[12:13]
v_swap_b32 v52,v60
v_swap_b32 v53,v61
v_swap_b32 v54,v62
v_swap_b32 v55,v63
s_mov_b64 exec,-1
s_waitcnt vmcnt(4)
s_barrier
s_sleep 2
s_add_u32 s65,s61,3145728
buffer_store_dwordx4 v[28:31],v10,s[40:43],s65 offen offset:256 sc1
s_sleep 3
s_add_u32 s65,s61,3145728
buffer_store_dwordx4 v[36:39],v15,s[40:43],s65 offen offset:640 sc1
s_sleep 3
s_add_u32 s65,s61,3244032
buffer_store_dwordx4 v[40:43],v10,s[40:43],s65 offen offset:0 sc1
s_sleep 3
s_add_u32 s65,s61,3244032
buffer_store_dwordx4 v[48:51],v15,s[40:43],s65 offen offset:384 sc1
s_sleep 3
s_add_u32 s65,s61,3244032
buffer_store_dwordx4 v[56:59],v10,s[40:43],s65 offen offset:128 sc1
s_sleep 3
s_add_u32 s65,s61,3244032
buffer_store_dwordx4 v[44:47],v15,s[40:43],s65 offen offset:512 sc1
s_sleep 3
s_add_u32 s65,s61,3244032
buffer_store_dwordx4 v[52:55],v10,s[40:43],s65 offen offset:256 sc1
s_sleep 3
s_add_u32 s65,s61,3244032
buffer_store_dwordx4 v[60:63],v15,s[40:43],s65 offen offset:640 sc1
v_and_b32 v232,63,%1
s_getpc_b64 s[88:89]
N_itA71_%=:
s_lshl_b32 s87,s10,10
s_add_u32 s87,s87,((N_last_%=-N_main_%=-16)&0xff000)-12280
s_sub_u32 s88,s88,N_itA71_%=-N_main_%=
s_subb_u32 s89,s89,0
s_add_u32 s88,s88,s87
s_addc_u32 s89,s89,0
s_swappc_b64 s[90:91],s[88:89]
s_cmp_lg_u32 s52,1
s_cbranch_scc1 N_s0_%=
v_min_u32 v236,47,v232
v_lshlrev_b32 v152,4,v236
v_lshlrev_b32 v153,2,v236
v_min_u32 v236,59,v232
v_mul_u32_u24 v237,0xaaab,v236
v_lshrrev_b32 v237,19,v237
v_mul_u32_u24 v237,14592,v237
v_lshl_add_u32 v148,v236,4,v237
s_add_u32 s64,s24,0
s_addc_u32 s65,s25,0
global_load_dwordx4 v[112:115],v152,s[64:65]
s_add_u32 s64,s24,9216
s_addc_u32 s65,s25,0
global_load_dwordx4 v[116:119],v152,s[64:65]
s_add_u32 s64,s24,18432
s_addc_u32 s65,s25,0
global_load_dwordx4 v[120:123],v152,s[64:65]
s_add_u32 s64,s24,27648
s_addc_u32 s65,s25,0
global_load_dwordx4 v[124:127],v152,s[64:65]
s_add_u32 s64,s24,36864
s_addc_u32 s65,s25,0
global_load_dwordx4 v[128:131],v152,s[64:65]
s_add_u32 s64,s24,46080
s_addc_u32 s65,s25,0
global_load_dwordx4 v[132:135],v152,s[64:65]
s_add_u32 s64,s24,55296
s_addc_u32 s65,s25,0
global_load_dwordx4 v[136:139],v152,s[64:65]
s_add_u32 s64,s24,64512
s_addc_u32 s65,s25,0
global_load_dwordx4 v[140:143],v152,s[64:65]
global_load_dwordx4 v[144:147],v148,s[28:29]
global_load_dwordx4 v[148:151],v148,s[28:29] offset:3648
global_load_dwordx4 v[156:159],v152,s[26:27]
global_load_dword v152,v153,s[32:33]
global_load_dword v153,v153,s[30:31]
N_s0_%=:
s_and_b32 s64,s52,3
s_mul_i32 s64,s64,18432
s_add_u32 s64,s64,4096
v_lshl_add_u32 v255,v232,4,s64
global_load_dwordx4 v[160:163],v255,s[24:25] offset:-4096
global_load_dwordx4 v[164:167],v255,s[24:25] offset:-3072
global_load_dwordx4 v[168:171],v255,s[24:25] offset:-2048
global_load_dwordx4 v[172:175],v255,s[24:25] offset:-1024
global_load_dwordx4 v[176:179],v255,s[24:25] offset:0
global_load_dwordx4 v[180:183],v255,s[24:25] offset:1024
global_load_dwordx4 v[184:187],v255,s[24:25] offset:2048
global_load_dwordx4 v[188:191],v255,s[24:25] offset:3072
v_add_u32 v255,8192,v255
global_load_dwordx4 v[192:195],v255,s[24:25] offset:-4096
global_load_dwordx4 v[196:199],v255,s[24:25] offset:-3072
global_load_dwordx4 v[200:203],v255,s[24:25] offset:-2048
global_load_dwordx4 v[204:207],v255,s[24:25] offset:-1024
global_load_dwordx4 v[208:211],v255,s[24:25] offset:0
global_load_dwordx4 v[212:215],v255,s[24:25] offset:1024
global_load_dwordx4 v[216:219],v255,s[24:25] offset:2048
global_load_dwordx4 v[220:223],v255,s[24:25] offset:3072
v_add_u32 v255,8192,v255
global_load_dwordx4 v[224:227],v255,s[24:25] offset:-4096
global_load_dwordx4 v[228:231],v255,s[24:25] offset:-3072
s_waitcnt vmcnt(0)
v_or3_b32 v160,v160,v164,v165
v_or3_b32 v162,v162,v166,v167
v_or3_b32 v160,v160,v168,v169
v_or3_b32 v162,v162,v170,v171
v_or3_b32 v160,v160,v172,v173
v_or3_b32 v162,v162,v174,v175
v_or3_b32 v160,v160,v176,v177
v_or3_b32 v162,v162,v178,v179
v_or3_b32 v160,v160,v180,v181
v_or3_b32 v162,v162,v182,v183
v_or3_b32 v160,v160,v184,v185
v_or3_b32 v162,v162,v186,v187
v_or3_b32 v160,v160,v188,v189
v_or3_b32 v162,v162,v190,v191
v_or3_b32 v160,v160,v192,v193
v_or3_b32 v162,v162,v194,v195
v_or3_b32 v160,v160,v196,v197
v_or3_b32 v162,v162,v198,v199
v_or3_b32 v160,v160,v200,v201
v_or3_b32 v162,v162,v202,v203
v_or3_b32 v160,v160,v204,v205
v_or3_b32 v162,v162,v206,v207
v_or3_b32 v160,v160,v208,v209
v_or3_b32 v162,v162,v210,v211
v_or3_b32 v160,v160,v212,v213
v_or3_b32 v162,v162,v214,v215
v_or3_b32 v160,v160,v216,v217
v_or3_b32 v162,v162,v218,v219
v_or3_b32 v160,v160,v220,v221
v_or3_b32 v162,v162,v222,v223
v_or3_b32 v160,v160,v224,v225
v_or3_b32 v162,v162,v226,v227
v_or3_b32 v160,v160,v228,v229
v_or3_b32 v162,v162,v230,v231
v_or3_b32 v160,v160,v161,v162
v_or_b32 v160,v160,v163
s_mov_b32 s64,0x7bff7bff
v_and_b32 v160,0x40004000,v160
v_pk_mul_f16 v160,v160,s64
v_or_b32 v1,v1,v160
s_cmp_lg_u32 s52,1
s_cbranch_scc1 N_s1_%=
s_mov_b32 s65,0x7fff7fff
v_and_b32 v112,s65,v112
v_and_b32 v113,s65,v113
v_and_b32 v114,s65,v114
v_and_b32 v115,s65,v115
v_and_b32 v116,s65,v116
v_and_b32 v117,s65,v117
v_and_b32 v118,s65,v118
v_and_b32 v119,s65,v119
v_and_b32 v120,s65,v120
v_and_b32 v121,s65,v121
v_and_b32 v122,s65,v122
v_and_b32 v123,s65,v123
v_and_b32 v124,s65,v124
v_and_b32 v125,s65,v125
v_and_b32 v126,s65,v126
v_and_b32 v127,s65,v127
v_and_b32 v128,s65,v128
v_and_b32 v129,s65,v129
v_and_b32 v130,s65,v130
v_and_b32 v131,s65,v131
v_and_b32 v132,s65,v132
v_and_b32 v133,s65,v133
v_and_b32 v134,s65,v134
v_and_b32 v135,s65,v135
v_and_b32 v136,s65,v136
v_and_b32 v137,s65,v137
v_and_b32 v138,s65,v138
v_and_b32 v139,s65,v139
v_and_b32 v140,s65,v140
v_and_b32 v141,s65,v141
v_and_b32 v142,s65,v142
v_and_b32 v143,s65,v143
v_pk_max_u16 v100,v112,v116
v_pk_max_u16 v101,v113,v117
v_pk_max_u16 v102,v114,v118
v_pk_max_u16 v103,v115,v119
v_pk_max_u16 v100,v100,v120
v_pk_max_u16 v101,v101,v121
v_pk_max_u16 v102,v102,v122
v_pk_max_u16 v103,v103,v123
v_pk_max_u16 v100,v100,v124
v_pk_max_u16 v101,v101,v125
v_pk_max_u16 v102,v102,v126
v_pk_max_u16 v103,v103,v127
v_pk_max_u16 v100,v100,v128
v_pk_max_u16 v101,v101,v129
v_pk_max_u16 v102,v102,v130
v_pk_max_u16 v103,v103,v131
v_pk_max_u16 v100,v100,v132
v_pk_max_u16 v101,v101,v133
v_pk_max_u16 v102,v102,v134
v_pk_max_u16 v103,v103,v135
v_pk_max_u16 v100,v100,v136
v_pk_max_u16 v101,v101,v137
v_pk_max_u16 v102,v102,v138
v_pk_max_u16 v103,v103,v139
v_pk_max_u16 v100,v100,v140
v_pk_max_u16 v101,v101,v141
v_pk_max_u16 v102,v102,v142
v_pk_max_u16 v103,v103,v143
v_pk_min_u16 v100,v100,v101
v_pk_min_u16 v102,v102,v103
v_pk_min_u16 v100,v100,v102
v_lshrrev_b32 v101,16,v100
v_and_b32 v100,0xffff,v100
v_min_u32 v100,v100,v101
v_mov_b32 v101,0x3a00
v_cmp_lt_u32_e64 s[92:93],v100,v101
s_nop 0
s_or_b32 s64,s92,s93
v_and_b32 v152,s65,v152
v_lshrrev_b32 v101,16,v152
v_and_b32 v152,0xffff,v152
v_max_u32 v152,v152,v101
v_mov_b32 v101,0x4580
v_cmp_le_u32_e64 s[92:93],v101,v152
s_nop 0
s_or_b32 s64,s64,s92
s_or_b32 s64,s64,s93
v_and_b32 v153,s65,v153
v_and_b32 v144,s65,v144
v_pk_max_u16 v153,v153,v144
v_and_b32 v145,s65,v145
v_pk_max_u16 v153,v153,v145
v_and_b32 v146,s65,v146
v_pk_max_u16 v153,v153,v146
v_and_b32 v147,s65,v147
v_pk_max_u16 v153,v153,v147
v_and_b32 v148,s65,v148
v_pk_max_u16 v153,v153,v148
v_and_b32 v149,s65,v149
v_pk_max_u16 v153,v153,v149
v_and_b32 v150,s65,v150
v_pk_max_u16 v153,v153,v150
v_and_b32 v151,s65,v151
v_pk_max_u16 v153,v153,v151
v_lshrrev_b32 v101,16,v153
v_and_b32 v153,0xffff,v153
v_max_u32 v153,v153,v101
v_mov_b32 v101,0x5380
v_cmp_le_u32_e64 s[92:93],v101,v153
s_nop 0
s_or_b32 s64,s64,s92
s_or_b32 s64,s64,s93
v_and_b32 v156,s65,v156
v_and_b32 v157,s65,v157
v_and_b32 v158,s65,v158
v_and_b32 v159,s65,v159
v_pk_max_u16 v156,v156,v157
v_pk_max_u16 v156,v156,v158
v_pk_max_u16 v156,v156,v159
v_lshrrev_b32 v101,16,v156
v_and_b32 v156,0xffff,v156
v_max_u32 v156,v156,v101
v_mov_b32 v101,0x4280
v_cmp_le_u32_e64 s[92:93],v101,v156
s_nop 0
s_or_b32 s64,s64,s92
s_or_b32 s64,s64,s93
s_cmp_eq_u32 s64,0
s_cbranch_scc1 N_s1_%=
v_mov_b32 v1,0x7c007c00
N_s1_%=:
v_and_b32 v1,0x7c007c00,v1
v_lshrrev_b32 v2,16,v1
v_and_b32 v1,0xffff,v1
v_max_u32 v1,v1,v2
v_cmp_eq_u32 vcc,0x7c00,v1
s_or_b32 s64,vcc_lo,vcc_hi
v_mov_b32 v1,s64
v_mov_b32 v2,8192
s_mov_b64 exec,1
ds_or_b32 v2,v1 offset:3456
s_mov_b64 exec,-1
s_waitcnt lgkmcnt(0)
s_barrier
s_setprio 3
ds_read_b32 v1,v2 offset:3456
s_waitcnt lgkmcnt(0)
s_setprio 0
v_readfirstlane_b32 s64,v1
s_cmp_lg_u32 s64,0
s_cbranch_scc1 N_fallback_%=
s_endpgm
s_nop 4
s_branch N_og18_%=
s_nop 0
s_nop 0
s_nop 0
s_nop 0
s_nop 0
s_nop 0
s_nop 0
s_nop 0
s_nop 0
s_nop 0
s_nop 0
s_nop 0
s_nop 0
N_og18_%=:
N_B_%=:
v_mov_b32 v192,0x409d3ef8
v_and_b32 v248,255,%1
v_lshlrev_b32 v198,4,v248
v_lshlrev_b32 v248,19,v248
v_cmp_eq_u32_e64 s[94:95],0,v248
v_add_u32 v199,0x8000,v198
s_mov_b32 s4,0x3f000000
s_mov_b32 s5,0x3fc00000
s_mov_b32 s6,0x40200000
s_mov_b32 s7,0x40600000
s_mov_b32 s8,0x40900000
s_mov_b32 s9,0x40b00000
s_mov_b32 s10,0x40d00000
s_mov_b32 s11,0x40f00000
s_mov_b32 s12,0x3f80222e
s_mov_b32 s13,0x13089abf
s_mov_b32 s96,0xebefe000
s_mov_b32 s15,0
N_tl_%=:
v_add_u32 v237,0x10000,v248
v_mul_f32 v194,s99,v248
v_fma_f32 v196,v237,s99,-v194
v_pk_fma_f32 v[160:161],v[196:197],s[4:5],v[194:195] op_sel_hi:[0,1,0]
v_pk_fma_f32 v[162:163],v[196:197],s[6:7],v[194:195] op_sel_hi:[0,1,0]
v_pk_fma_f32 v[164:165],v[196:197],s[8:9],v[194:195] op_sel_hi:[0,1,0]
v_pk_fma_f32 v[166:167],v[196:197],s[10:11],v[194:195] op_sel_hi:[0,1,0]
v_cndmask_b32_e64 v160,v160,0,s[94:95]
v_pk_mul_f32 v[168:169],v[160:161],v[160:161]
v_pk_mul_f32 v[170:171],v[162:163],v[162:163]
v_pk_mul_f32 v[172:173],v[164:165],v[164:165]
v_pk_mul_f32 v[174:175],v[166:167],v[166:167]
v_pk_fma_f32 v[168:169],v[168:169],s[12:13],v[192:193] op_sel_hi:[1,0,0]
v_pk_fma_f32 v[170:171],v[170:171],s[12:13],v[192:193] op_sel_hi:[1,0,0]
v_pk_fma_f32 v[172:173],v[172:173],s[12:13],v[192:193] op_sel_hi:[1,0,0]
v_pk_fma_f32 v[174:175],v[174:175],s[12:13],v[192:193] op_sel_hi:[1,0,0]
v_pk_mul_f32 v[168:169],v[168:169],v[160:161]
v_pk_mul_f32 v[170:171],v[170:171],v[162:163]
v_pk_mul_f32 v[172:173],v[172:173],v[164:165]
v_pk_mul_f32 v[174:175],v[174:175],v[166:167]
v_exp_f32 v168,v168
v_exp_f32 v169,v169
v_exp_f32 v170,v170
v_exp_f32 v171,v171
v_exp_f32 v172,v172
v_exp_f32 v173,v173
v_exp_f32 v174,v174
v_exp_f32 v175,v175
v_pk_fma_f32 v[168:169],v[168:169],s[96:97],s[96:97] op_sel_hi:[1,0,0]
v_pk_fma_f32 v[170:171],v[170:171],s[96:97],s[96:97] op_sel_hi:[1,0,0]
v_pk_fma_f32 v[172:173],v[172:173],s[96:97],s[96:97] op_sel_hi:[1,0,0]
v_pk_fma_f32 v[174:175],v[174:175],s[96:97],s[96:97] op_sel_hi:[1,0,0]
v_rcp_f32 v168,v168
v_rcp_f32 v169,v169
v_rcp_f32 v170,v170
v_rcp_f32 v171,v171
v_rcp_f32 v172,v172
v_rcp_f32 v173,v173
v_rcp_f32 v174,v174
v_rcp_f32 v175,v175
v_pk_mul_f32 v[168:169],v[168:169],v[160:161]
v_pk_mul_f32 v[170:171],v[170:171],v[162:163]
v_pk_mul_f32 v[172:173],v[172:173],v[164:165]
v_pk_mul_f32 v[174:175],v[174:175],v[166:167]
v_pk_fma_f32 v[160:161],v[160:161],s[12:13],v[168:169] op_sel:[0,1,0] op_sel_hi:[1,1,1]
v_pk_fma_f32 v[162:163],v[162:163],s[12:13],v[170:171] op_sel:[0,1,0] op_sel_hi:[1,1,1]
v_pk_fma_f32 v[164:165],v[164:165],s[12:13],v[172:173] op_sel:[0,1,0] op_sel_hi:[1,1,1]
v_pk_fma_f32 v[166:167],v[166:167],s[12:13],v[174:175] op_sel:[0,1,0] op_sel_hi:[1,1,1]
v_cvt_pk_bf16_f32 v176,v168,v169
v_cvt_pk_bf16_f32 v177,v170,v171
v_cvt_pk_bf16_f32 v178,v172,v173
v_cvt_pk_bf16_f32 v179,v174,v175
v_cvt_pk_bf16_f32 v180,v160,v161
v_cvt_pk_bf16_f32 v181,v162,v163
v_cvt_pk_bf16_f32 v182,v164,v165
v_cvt_pk_bf16_f32 v183,v166,v167
ds_write_b128 v198,v[176:179] offset:0
ds_write_b128 v199,v[180:183] offset:32768
s_mov_b64 s[94:95],0
v_add_u32 v248,0x8000000,v248
v_add_u32 v198,4096,v198
v_add_u32 v199,4096,v199
s_add_u32 s15,s15,1
s_cmp_lt_u32 s15,2
s_cbranch_scc1 N_tl_%=
s_and_b32 s64,s52,3
s_lshl_b32 s10,s64,2
s_mov_b32 s8,0x0ff00ff0
s_mov_b32 s9,0x0ff00ff0
s_and_b32 s64,s52,3
s_mov_b32 s12,0xaaaaaaaa
s_mov_b32 s13,0xaaaaaaaa
s_and_b32 s64,s52,3
s_lshl_b32 s10,s64,2
s_add_u32 s64,s10,s71
v_and_b32 v233,15,v232
v_lshrrev_b32 v234,4,v232
s_mul_i32 s61,s64,98304
s_add_u32 s61,s61,s68
v_and_b32 v236,1,v234
v_lshlrev_b32 v236,1,v236
v_lshrrev_b32 v237,5,v232
v_or_b32 v235,v236,v237
s_mov_b32 s54,0x44ea44ea
s_mov_b32 s55,0x877f877f
s_mov_b32 s8,0x0ff00ff0
s_mov_b32 s9,0x0ff00ff0
v_mov_b32 v1,0
v_mov_b32 v239,0x70000
v_bfe_u32 v236,v233,1,1
v_sub_u32 v236,v235,v236
v_cmp_gt_u32_e64 s[92:93],3,v236
v_lshrrev_b32 v237,2,v233
v_lshlrev_b32 v237,1,v237
v_and_b32 v238,1,v233
v_or_b32 v237,v237,v238
v_lshl_add_u32 v236,v236,3,v237
v_lshlrev_b32 v236,4,v236
v_add_u32 v236,143488,v236
v_cndmask_b32_e64 v11,v239,v236,s[92:93]
v_and_b32 v13,3,v237
v_lshlrev_b32 v236,1,v233
v_and_b32 v236,4,v236
v_lshl_add_u32 v236,v234,3,v236
v_mov_b32 v238,0x54250310
v_mul_u32_u24 v13,0x5555,v13
v_bfe_u32 v236,v238,v236,3
v_lshl_add_u32 v236,v236,3,v237
v_lshlrev_b32 v236,4,v236
v_add_u32 v12,143488,v236
v_lshrrev_b32 v237,2,v233
v_cmp_eq_u32_e64 s[92:93],v234,v237
v_lshlrev_b32 v236,4,v233
v_add_u32 v236,10112,v236
v_cndmask_b32_e64 v14,v239,v236,s[92:93]
v_and_b32 v239,7,v233
v_lshl_add_u32 v236,v239,1,v235
v_add_u32 v237,1,v236
v_bfe_u32 v237,v237,2,2
v_mul_u32_u24 v236,192,v236
v_lshl_add_u32 v236,v237,4,v236
v_add_u32 v237,1,v239
v_bfe_u32 v237,v237,1,2
v_xor_b32 v237,v237,v234
v_mul_u32_u24 v239,384,v239
v_lshl_add_u32 v237,v237,4,v239
v_sub_u32 v237,v237,v236
v_add_u32 v8,192,v237
v_add_u32 v15,73728,v236
v_and_b32 v238,1,v234
v_lshrrev_b32 v237,1,v234
v_lshlrev_b32 v238,6,v238
v_lshl_add_u32 v238,v237,4,v238
v_add_u32 v7,8192,v238
s_and_b32 s64,s52,3
s_mul_i32 s67,s64,54
s_cmp_eq_u32 s70,31
s_cselect_b32 s66,17,99
v_add_u32 v236,s67,v232
v_add_u32 v236,108,v236
v_mul_u32_u24 v237,0xaaab,v236
v_lshrrev_b32 v237,19,v237
v_mul_u32_u24 v238,12,v237
v_sub_u32 v238,v236,v238
v_add_u32 v236,1,v237
v_bfe_u32 v236,v236,2,2
v_xor_b32 v238,v238,v236
v_mul_u32_u24 v236,192,v237
v_lshl_add_u32 v239,v238,4,v236
v_cmp_eq_u32_e64 s[92:93],s66,v237
v_mov_b32 v236,0x7fff0000
s_nop 0
v_cndmask_b32_e64 v239,v239,v236,s[92:93]
v_cndmask_b32_e64 v3,v239,v3,vcc
v_and_b32 v239,7,v233
v_mul_u32_u24 v239,384,v239
v_and_b32 v238,1,v233
v_mul_u32_u24 v238,320,v238
v_sub_u32 v239,v239,v238
s_mov_b32 s12,0xaaaaaaaa
s_mov_b32 s13,0xaaaaaaaa
v_lshl_add_u32 v10,v234,4,v239
s_mov_b64 exec,s[8:9]
v_add_u32 v10,196608,v10
s_mov_b64 exec,-1
v_lshrrev_b32 v236,3,v233
v_bfe_u32 v237,v233,2,1
v_lshl_add_u32 v237,v236,4,v237
v_lshl_add_u32 v237,v234,1,v237
v_lshlrev_b32 v237,6,v237
v_lshl_add_u32 v237,v236,4,v237
v_and_b32 v238,1,v233
v_lshl_add_u32 v237,v238,5,v237
v_bfe_u32 v238,v233,1,1
v_lshl_add_u32 v237,v238,3,v237
v_xor_b32 v238,16,v237
v_add_u32 v4,16256,v237
v_add_u32 v5,16256,v238
v_lshrrev_b32 v236,3,v233
v_bfe_u32 v237,v233,2,1
v_lshl_add_u32 v236,v237,1,v236
v_lshrrev_b32 v237,1,v234
v_lshl_add_u32 v236,v237,2,v236
v_mul_u32_u24 v236,192,v236
v_and_b32 v238,1,v234
v_mul_u32_u24 v239,3080,v238
v_add_u32 v236,v236,v239
v_and_b32 v239,3,v233
v_lshl_add_u32 v236,v239,4,v236
v_add_u32 v6,40896,v236
s_mov_b32 s42,50331648
s_mov_b32 s43,0x00020000
v_bfe_u32 v236,v232,2,3
v_mul_u32_u24 v236,768,v236
v_and_b32 v237,3,v232
v_lshrrev_b32 v238,5,v232
v_lshl_add_u32 v237,v238,2,v237
v_lshl_add_u32 v236,v237,4,v236
v_lshlrev_b32 v237,4,v232
s_bitcmp0_b32 s52,0
s_mov_b32 s58,40896
s_cselect_b32 s58,16256,s58
s_cselect_b64 s[92:93],-1,0
s_cselect_b32 s59,64,6144
s_movk_i32 s53,1024
s_cselect_b32 s53,6144,s53
s_cselect_b32 s60,0,67584
s_cselect_b32 s97,64,0
s_cselect_b32 s69,0,0x70000000
s_mov_b32 s100,73728
s_cselect_b32 s100,110688,s100
s_cselect_b32 s64,16,0
s_movk_i32 s50,1032
s_cselect_b32 s50,1024,s50
s_cselect_b32 s51,s53,-72704
v_cndmask_b32_e64 v236,v237,v236,s[92:93]
v_cndmask_b32_e64 v2,v241,v236,vcc
v_xor_b32 v236,s64,v236
v_cndmask_b32_e64 v3,v3,v236,vcc
s_mov_b32 s46,73728
s_mov_b32 s47,0x00020000
v_lshlrev_b32 v236,4,v232
v_and_b32 v237,255,%1
v_add_u32 v236,157312,v236
v_lshlrev_b32 v237,3,v237
v_add_u32 v237,157312,v237
ds_write_b128 v236,v[4:7] offset:0
ds_write_b128 v236,v[8:11] offset:1024
ds_write_b128 v236,v[12:15] offset:2048
ds_write_b64 v237,v[2:3] offset:3072
s_waitcnt lgkmcnt(0)
s_sub_u32 s36,s20,192
s_subb_u32 s37,s21,0
s_and_b32 s37,s37,0xffff
s_bitcmp0_b32 s52,0
s_cselect_b64 s[44:45],s[24:25],s[28:29]
s_and_b32 s45,s45,0xffff
s_mov_b32 s40,s34
s_and_b32 s41,s35,0xffff
s_cbranch_vccz RB1_%=
s_add_u32 m0,s58,12320
s_nop 0
buffer_load_dwordx4 v2,s[44:47],s69 offen lds
s_add_u32 m0,m0,1024
s_add_u32 s69,s69,s53
buffer_load_dwordx4 v3,s[44:47],s69 offen lds
s_add_u32 m0,m0,1024
s_add_u32 s69,s69,s53
buffer_load_dwordx4 v2,s[44:47],s69 offen lds
s_add_u32 m0,m0,s50
s_add_u32 s69,s69,s53
buffer_load_dwordx4 v3,s[44:47],s69 offen lds
s_add_u32 m0,m0,1024
s_add_u32 s69,s69,s53
buffer_load_dwordx4 v2,s[44:47],s69 offen lds
s_add_u32 m0,m0,1024
s_add_u32 s69,s69,s53
buffer_load_dwordx4 v3,s[44:47],s69 offen lds
s_add_u32 m0,m0,s50
s_add_u32 s69,s69,s51
buffer_load_dwordx4 v2,s[44:47],s69 offen lds
s_mov_b32 s86,0xbe801d5a
s_add_u32 m0,m0,1024
s_add_u32 s69,s69,s53
buffer_load_dwordx4 v3,s[44:47],s69 offen lds
s_add_u32 m0,m0,1024
s_add_u32 s69,s69,s53
buffer_load_dwordx4 v2,s[44:47],s69 offen lds
s_add_u32 m0,m0,s50
s_add_u32 s69,s69,s53
buffer_load_dwordx4 v3,s[44:47],s69 offen lds
s_add_u32 m0,m0,1024
s_add_u32 s69,s69,s53
buffer_load_dwordx4 v2,s[44:47],s69 offen lds
s_add_u32 m0,m0,1024
s_add_u32 s69,s69,s53
buffer_load_dwordx4 v3,s[44:47],s69 offen lds
RB1_%=:
s_waitcnt lgkmcnt(0)
s_barrier
s_mov_b64 exec,s[62:63]
s_add_u32 m0,s57,34880
s_add_u32 s101,s56,983040
buffer_load_dwordx4 v241,s[36:39],s101 offen lds
s_add_u32 m0,s57,38368
s_add_u32 s101,s56,1081344
buffer_load_dwordx4 v241,s[36:39],s101 offen lds
s_add_u32 m0,s57,41856
s_add_u32 s101,s56,1179648
buffer_load_dwordx4 v241,s[36:39],s101 offen lds
s_add_u32 m0,s57,45344
s_add_u32 s101,s56,1277952
buffer_load_dwordx4 v241,s[36:39],s101 offen lds
s_add_u32 m0,s57,48832
s_add_u32 s101,s56,1376256
buffer_load_dwordx4 v241,s[36:39],s101 offen lds
s_add_u32 m0,s57,52320
s_add_u32 s101,s56,1474560
buffer_load_dwordx4 v241,s[36:39],s101 offen lds
s_add_u32 m0,s57,55808
s_add_u32 s101,s56,1572864
buffer_load_dwordx4 v241,s[36:39],s101 offen lds
s_add_u32 m0,s57,59296
s_add_u32 s101,s56,1671168
buffer_load_dwordx4 v241,s[36:39],s101 offen lds
s_mov_b64 exec,-1
ds_read_b128 v[104:107],v11 offset:768
ds_read_b128 v[108:111],v12 offset:0
ds_read_b128 v[88:91],v11 offset:1920
ds_read_b128 v[92:95],v12 offset:1152
ds_read_b128 v[96:99],v11 offset:5376
ds_read_b128 v[100:103],v12 offset:4608
ds_read_b128 v[64:67],v11 offset:6528
ds_read_b128 v[68:71],v12 offset:5760
ds_read_b128 v[72:75],v11 offset:9984
ds_read_b128 v[76:79],v12 offset:9216
ds_read_b128 v[80:83],v11 offset:11136
ds_read_b128 v[84:87],v12 offset:10368
ds_read_b128 v[16:19],v11 offset:3072
ds_read_b128 v[20:23],v12 offset:2304
ds_read_b128 v[24:27],v11 offset:4224
ds_read_b128 v[28:31],v12 offset:3456
ds_read_b128 v[32:35],v11 offset:7680
ds_read_b128 v[36:39],v12 offset:6912
ds_read_b128 v[40:43],v11 offset:8832
ds_read_b128 v[44:47],v12 offset:8064
ds_read_b128 v[48:51],v11 offset:12288
ds_read_b128 v[52:55],v12 offset:11520
ds_read_b128 v[56:59],v11 offset:13440
ds_read_b128 v[60:63],v12 offset:12672
s_barrier
s_setprio 0
s_cbranch_vccz RB2_%=
s_add_u32 m0,s58,0
s_mul_i32 s69,s59,1
s_add_u32 s69,s69,s97
buffer_load_dwordx4 v2,s[44:47],s69 offen lds
s_add_u32 m0,m0,1024
s_add_u32 s69,s69,s53
buffer_load_dwordx4 v3,s[44:47],s69 offen lds
s_add_u32 m0,m0,1024
s_add_u32 s69,s69,s53
buffer_load_dwordx4 v2,s[44:47],s69 offen lds
s_add_u32 m0,m0,s50
s_add_u32 s69,s69,s53
buffer_load_dwordx4 v3,s[44:47],s69 offen lds
s_add_u32 m0,m0,1024
s_add_u32 s69,s69,s53
buffer_load_dwordx4 v2,s[44:47],s69 offen lds
s_add_u32 m0,m0,1024
s_add_u32 s69,s69,s53
buffer_load_dwordx4 v3,s[44:47],s69 offen lds
s_add_u32 m0,m0,s50
s_add_u32 s69,s69,s53
buffer_load_dwordx4 v2,s[44:47],s69 offen lds
s_add_u32 m0,m0,1024
s_add_u32 s69,s69,s53
buffer_load_dwordx4 v3,s[44:47],s69 offen lds
s_add_u32 m0,m0,1024
s_add_u32 s69,s69,s53
buffer_load_dwordx4 v2,s[44:47],s69 offen lds
s_add_u32 m0,m0,s50
s_add_u32 s69,s69,s53
buffer_load_dwordx4 v3,s[44:47],s69 offen lds
s_add_u32 m0,m0,1024
s_add_u32 s69,s69,s53
buffer_load_dwordx4 v2,s[44:47],s69 offen lds
s_add_u32 m0,m0,1024
s_add_u32 s69,s69,s53
buffer_load_dwordx4 v3,s[44:47],s69 offen lds
RB2_%=:
s_getpc_b64 s[88:89]
N_itB3_%=:
s_lshl_b32 s87,s10,10
s_add_u32 s87,s87,4104
s_sub_u32 s88,s88,N_itB3_%=-N_main_%=
s_subb_u32 s89,s89,0
s_add_u32 s88,s88,s87
s_addc_u32 s89,s89,0
s_swappc_b64 s[90:91],s[88:89]
s_mov_b32 s86,0x1234567
s_cbranch_vccnz RB4_%=
s_waitcnt vmcnt(0)
RB4_%=:
s_cbranch_vccz RB5_%=
s_waitcnt vmcnt(12)
RB5_%=:
s_barrier
v_lshlrev_b32 v253,7,v232
s_bfe_u32 s64,%0,0x20003
s_and_b32 s65,s52,3
s_lshl_b32 s65,s65,2
s_add_u32 s64,s64,s65
s_lshl_b32 s65,s64,13
v_add_u32 v252,s65,v253
s_getpc_b64 s[14:15]
N_pf_%=:
s_sub_u32 s14,s14,N_pf_%=-N_main_%=
s_subb_u32 s15,s15,0
v_min_u32 v252,N_mainend_%=-N_main_%=,v252
global_load_dword v254,v252,s[14:15]
s_cbranch_vccnz RB6_%=
s_mov_b64 exec,s[62:63]
s_add_u32 m0,s57,62784
s_add_u32 s64,s56,1769472
buffer_load_dwordx4 v2,s[36:39],s64 offen lds
s_add_u32 m0,s57,64512
s_nop 0
buffer_load_dwordx4 v3,s[36:39],s64 offen lds
s_mov_b64 exec,-1
RB6_%=:
s_cbranch_vccnz RB7_%=
s_mov_b64 exec,s[62:63]
s_add_u32 m0,s57,0
s_add_u32 s64,s56,1966080
buffer_load_dwordx4 v2,s[36:39],s64 offen lds
s_add_u32 m0,s57,1728
s_nop 0
buffer_load_dwordx4 v3,s[36:39],s64 offen lds
s_mov_b64 exec,-1
RB7_%=:
s_cbranch_vccnz RB8_%=
s_mov_b64 exec,s[62:63]
s_add_u32 m0,s57,3488
s_add_u32 s64,s56,2064384
buffer_load_dwordx4 v2,s[36:39],s64 offen lds
s_add_u32 m0,s57,5216
s_nop 0
buffer_load_dwordx4 v3,s[36:39],s64 offen lds
s_mov_b64 exec,-1
RB8_%=:
s_cbranch_vccnz RB9_%=
s_mov_b64 exec,s[62:63]
s_add_u32 m0,s57,6976
s_add_u32 s64,s56,2162688
buffer_load_dwordx4 v2,s[36:39],s64 offen lds
s_add_u32 m0,s57,8704
s_nop 0
buffer_load_dwordx4 v3,s[36:39],s64 offen lds
s_mov_b64 exec,-1
RB9_%=:
s_cbranch_vccnz RB10_%=
s_mov_b64 exec,s[62:63]
s_add_u32 m0,s57,10464
s_add_u32 s64,s56,2260992
buffer_load_dwordx4 v2,s[36:39],s64 offen lds
s_add_u32 m0,s57,12192
s_nop 0
buffer_load_dwordx4 v3,s[36:39],s64 offen lds
s_mov_b64 exec,-1
RB10_%=:
s_cbranch_vccnz RB11_%=
s_mov_b64 exec,s[62:63]
s_add_u32 m0,s57,66272
s_add_u32 s64,s56,1867776
buffer_load_dwordx4 v2,s[36:39],s64 offen lds
s_add_u32 m0,s57,68000
s_nop 0
buffer_load_dwordx4 v3,s[36:39],s64 offen lds
s_mov_b64 exec,-1
RB11_%=:
s_barrier
s_cbranch_vccz RB12_%=
s_add_u32 m0,s58,12320
s_mul_i32 s69,s59,3
s_add_u32 s69,s69,s97
buffer_load_dwordx4 v2,s[44:47],s69 offen lds
s_add_u32 m0,m0,1024
s_add_u32 s69,s69,s53
buffer_load_dwordx4 v3,s[44:47],s69 offen lds
s_add_u32 m0,m0,1024
s_add_u32 s69,s69,s53
buffer_load_dwordx4 v2,s[44:47],s69 offen lds
s_add_u32 m0,m0,s50
s_add_u32 s69,s69,s53
buffer_load_dwordx4 v3,s[44:47],s69 offen lds
RB12_%=:
s_cbranch_vccz RB13_%=
s_add_u32 m0,m0,1024
s_add_u32 s69,s69,s53
buffer_load_dwordx4 v2,s[44:47],s69 offen lds
s_add_u32 m0,m0,1024
s_add_u32 s69,s69,s53
buffer_load_dwordx4 v3,s[44:47],s69 offen lds
s_add_u32 m0,m0,s50
s_add_u32 s69,s69,s53
buffer_load_dwordx4 v2,s[44:47],s69 offen lds
s_add_u32 m0,m0,1024
s_add_u32 s69,s69,s53
buffer_load_dwordx4 v3,s[44:47],s69 offen lds
RB13_%=:
s_cbranch_vccz RB14_%=
s_add_u32 m0,m0,1024
s_add_u32 s69,s69,s53
buffer_load_dwordx4 v2,s[44:47],s69 offen lds
s_add_u32 m0,m0,s50
s_add_u32 s69,s69,s53
buffer_load_dwordx4 v3,s[44:47],s69 offen lds
s_add_u32 m0,m0,1024
s_add_u32 s69,s69,s53
buffer_load_dwordx4 v2,s[44:47],s69 offen lds
s_add_u32 m0,m0,1024
s_add_u32 s69,s69,s53
buffer_load_dwordx4 v3,s[44:47],s69 offen lds
RB14_%=:
s_waitcnt vmcnt(13)
s_barrier
s_cbranch_vccnz RB15_%=
s_mov_b64 exec,s[62:63]
s_add_u32 m0,s57,13952
s_add_u32 s64,s56,2359296
buffer_load_dwordx4 v2,s[36:39],s64 offen lds
s_add_u32 m0,s57,15680
s_nop 0
buffer_load_dwordx4 v3,s[36:39],s64 offen lds
s_mov_b64 exec,-1
RB15_%=:
s_cbranch_vccnz RB16_%=
s_mov_b64 exec,s[62:63]
s_add_u32 m0,s57,17440
s_add_u32 s64,s56,2457600
buffer_load_dwordx4 v2,s[36:39],s64 offen lds
s_add_u32 m0,s57,19168
s_nop 0
buffer_load_dwordx4 v3,s[36:39],s64 offen lds
s_mov_b64 exec,-1
RB16_%=:
s_cbranch_vccnz RB17_%=
s_mov_b64 exec,s[62:63]
s_add_u32 m0,s57,20928
s_add_u32 s64,s56,2555904
buffer_load_dwordx4 v2,s[36:39],s64 offen lds
s_add_u32 m0,s57,22656
s_nop 0
buffer_load_dwordx4 v3,s[36:39],s64 offen lds
s_mov_b64 exec,-1
RB17_%=:
s_cbranch_vccnz RB18_%=
s_mov_b64 exec,s[62:63]
s_add_u32 m0,s57,24416
s_add_u32 s64,s56,2654208
buffer_load_dwordx4 v2,s[36:39],s64 offen lds
s_add_u32 m0,s57,26144
s_nop 0
buffer_load_dwordx4 v3,s[36:39],s64 offen lds
s_mov_b64 exec,-1
RB18_%=:
s_cbranch_vccnz RB19_%=
s_mov_b64 exec,s[62:63]
s_add_u32 m0,s57,27904
s_add_u32 s64,s56,2752512
buffer_load_dwordx4 v2,s[36:39],s64 offen lds
s_add_u32 m0,s57,29632
s_nop 0
buffer_load_dwordx4 v3,s[36:39],s64 offen lds
s_mov_b64 exec,-1
RB19_%=:
s_cbranch_vccnz RB20_%=
s_mov_b64 exec,s[62:63]
s_add_u32 m0,s57,31392
s_add_u32 s64,s56,2850816
buffer_load_dwordx4 v2,s[36:39],s64 offen lds
s_add_u32 m0,s57,33120
s_nop 0
buffer_load_dwordx4 v3,s[36:39],s64 offen lds
s_mov_b64 exec,-1
RB20_%=:
s_cbranch_vccnz RB21_%=
s_mov_b64 exec,s[62:63]
s_add_u32 m0,s57,34880
s_add_u32 s64,s56,2949120
buffer_load_dwordx4 v2,s[36:39],s64 offen lds
s_add_u32 m0,s57,36608
s_nop 0
buffer_load_dwordx4 v3,s[36:39],s64 offen lds
s_mov_b64 exec,-1
RB21_%=:
s_cbranch_vccnz RB22_%=
s_mov_b64 exec,s[62:63]
s_add_u32 m0,s57,38368
s_add_u32 s64,s56,3047424
buffer_load_dwordx4 v2,s[36:39],s64 offen lds
s_add_u32 m0,s57,40096
s_nop 0
buffer_load_dwordx4 v3,s[36:39],s64 offen lds
s_mov_b64 exec,-1
RB22_%=:
s_cbranch_vccnz RB23_%=
s_mov_b64 exec,s[62:63]
s_add_u32 m0,s57,41856
s_add_u32 s64,s56,3145728
buffer_load_dwordx4 v2,s[36:39],s64 offen lds
s_add_u32 m0,s57,43584
s_nop 0
buffer_load_dwordx4 v3,s[36:39],s64 offen lds
s_mov_b64 exec,-1
RB23_%=:
s_cbranch_vccnz RB24_%=
s_mov_b64 exec,s[62:63]
s_add_u32 m0,s57,45344
s_add_u32 s64,s56,3244032
buffer_load_dwordx4 v2,s[36:39],s64 offen lds
s_add_u32 m0,s57,47072
s_nop 0
buffer_load_dwordx4 v3,s[36:39],s64 offen lds
s_mov_b64 exec,-1
RB24_%=:
s_cbranch_vccz RB25_%=
s_waitcnt vmcnt(0)
RB25_%=:
s_barrier
s_cbranch_vccz RB26_%=
s_add_u32 m0,s58,0
s_mul_i32 s69,s59,5
s_add_u32 s69,s69,s97
buffer_load_dwordx4 v2,s[44:47],s69 offen lds
s_add_u32 m0,m0,1024
s_add_u32 s69,s69,s53
buffer_load_dwordx4 v3,s[44:47],s69 offen lds
s_add_u32 m0,m0,1024
s_add_u32 s69,s69,s53
buffer_load_dwordx4 v2,s[44:47],s69 offen lds
s_add_u32 m0,m0,s50
s_add_u32 s69,s69,s53
buffer_load_dwordx4 v3,s[44:47],s69 offen lds
RB26_%=:
s_cbranch_vccz RB27_%=
s_add_u32 m0,m0,1024
s_add_u32 s69,s69,s53
buffer_load_dwordx4 v2,s[44:47],s69 offen lds
s_add_u32 m0,m0,1024
s_add_u32 s69,s69,s53
buffer_load_dwordx4 v3,s[44:47],s69 offen lds
s_add_u32 m0,m0,s50
s_add_u32 s69,s69,s53
buffer_load_dwordx4 v2,s[44:47],s69 offen lds
s_add_u32 m0,m0,1024
s_add_u32 s69,s69,s53
buffer_load_dwordx4 v3,s[44:47],s69 offen lds
RB27_%=:
s_cbranch_vccz RB28_%=
s_add_u32 m0,m0,1024
s_add_u32 s69,s69,s53
buffer_load_dwordx4 v2,s[44:47],s69 offen lds
s_add_u32 m0,m0,s50
s_add_u32 s69,s69,s53
buffer_load_dwordx4 v3,s[44:47],s69 offen lds
s_add_u32 m0,m0,1024
s_add_u32 s69,s69,s53
buffer_load_dwordx4 v2,s[44:47],s69 offen lds
s_add_u32 m0,m0,1024
s_add_u32 s69,s69,s53
buffer_load_dwordx4 v3,s[44:47],s69 offen lds
RB28_%=:
s_add_u32 s4,s10,16
s_cmp_ge_u32 s4,20
s_cselect_b32 s7,20,0
s_sub_u32 s4,s4,s7
s_mul_i32 s4,s4,3488
s_add_u32 s5,s10,18
s_cmp_ge_u32 s5,20
s_cselect_b32 s7,20,0
s_sub_u32 s5,s5,s7
s_mul_i32 s5,s5,3488
s_add_u32 s6,s10,0
s_cmp_ge_u32 s6,20
s_cselect_b32 s7,20,0
s_sub_u32 s6,s6,s7
s_mul_i32 s6,s6,3488
v_add_u32 v112,s4,v15
v_add_u32 v116,s5,v15
s_mov_b64 exec,s[8:9]
v_add_u32 v112,s5,v15
v_add_u32 v116,s6,v15
s_mov_b64 exec,-1
v_xor_b32 v113,16,v112
v_xor_b32 v117,16,v116
v_xor_b32 v114,32,v112
v_xor_b32 v118,32,v116
v_xor_b32 v115,48,v112
v_xor_b32 v119,48,v116
s_branch N_og19_%=
s_nop 0
s_nop 0
s_nop 0
s_nop 0
s_nop 0
s_nop 0
s_nop 0
s_nop 0
s_nop 0
s_nop 0
s_nop 0
s_nop 0
s_nop 0
s_nop 0
s_nop 0
s_nop 0
s_nop 0
s_nop 0
s_nop 0
s_nop 0
s_nop 0
s_nop 0
s_nop 0
s_nop 0
s_nop 0
s_nop 0
s_nop 0
s_nop 0
s_nop 0
s_nop 0
s_nop 0
s_nop 0
s_nop 0
s_nop 0
s_nop 0
s_nop 0
s_nop 0
s_nop 0
s_nop 0
s_nop 0
s_nop 0
s_nop 0
s_nop 0
s_nop 0
s_nop 0
s_nop 0
s_nop 0
s_nop 0
s_nop 0
s_nop 0
s_nop 0
s_nop 0
s_nop 0
s_nop 0
s_nop 0
s_nop 0
s_nop 0
s_nop 0
s_nop 0
s_nop 0
s_nop 0
s_nop 0
s_nop 0
s_nop 0
s_nop 0
s_nop 0
s_nop 0
s_nop 0
s_nop 0
s_nop 0
s_nop 0
s_nop 0
s_nop 0
s_nop 0
s_nop 0
s_nop 0
s_nop 0
s_nop 0
s_nop 0
s_nop 0
s_nop 0
s_nop 0
s_nop 0
s_nop 0
s_nop 0
s_nop 0
s_nop 0
s_nop 0
s_nop 0
s_nop 0
s_nop 0
s_nop 0
s_nop 0
s_nop 0
s_nop 0
s_nop 0
s_nop 0
s_nop 0
s_nop 0
s_nop 0
s_nop 0
s_nop 0
s_nop 0
s_nop 0
s_nop 0
s_nop 0
s_nop 0
s_nop 0
s_nop 0
s_nop 0
s_nop 0
s_nop 0
s_nop 0
s_nop 0
s_nop 0
s_nop 0
s_nop 0
s_nop 0
s_nop 0
s_nop 0
s_nop 0
s_nop 0
s_nop 0
s_nop 0
s_nop 0
s_nop 0
s_nop 0
s_nop 0
s_nop 0
s_nop 0
s_nop 0
s_nop 0
s_nop 0
s_nop 0
s_nop 0
s_nop 0
s_nop 0
s_nop 0
s_nop 0
s_nop 0
s_nop 0
s_nop 0
N_og19_%=:
s_waitcnt vmcnt(0)
s_barrier
s_setprio 3
ds_read_b128 v[136:139],v116 offset:0
ds_read_b128 v[140:143],v116 offset:3488
ds_read_b128 v[128:131],v112 offset:0
ds_read_b128 v[132:135],v112 offset:3488
ds_read_b128 v[152:155],v117 offset:0
ds_read_b128 v[156:159],v117 offset:3488
ds_read_b128 v[144:147],v113 offset:0
ds_read_b128 v[148:151],v113 offset:3488
s_waitcnt lgkmcnt(7)
v_mfma_f32_16x16x32_bf16 v[160:163],v[104:107],v[136:139],0
s_waitcnt lgkmcnt(6)
v_mfma_f32_16x16x32_bf16 v[184:187],v[104:107],v[140:143],0
s_waitcnt lgkmcnt(4)
ds_read_b128 v[140:143],v116 offset:3552
v_smfmac_f32_16x16x64_bf16 v[160:163],v[108:111],v[128:135],v13
ds_read_b128 v[128:131],v112 offset:64
v_smfmac_f32_16x16x64_bf16 v[184:187],v[108:111],v[132:139],v13
s_waitcnt lgkmcnt(5)
ds_read_b128 v[136:139],v116 offset:64
v_mfma_f32_16x16x32_bf16 v[164:167],v[88:91],v[152:155],0
s_waitcnt lgkmcnt(5)
ds_read_b128 v[132:135],v112 offset:3552
s_nop 0
v_add_f32 v120,v160,v161
v_mul_f32 v124,v160,v160
v_fmac_f32 v124,v161,v161
v_mfma_f32_16x16x32_bf16 v[188:191],v[88:91],v[156:159],0
s_waitcnt lgkmcnt(4)
ds_read_b128 v[156:159],v117 offset:3552
v_add_f32 v121,v162,v163
v_mul_f32 v125,v162,v162
v_fmac_f32 v125,v163,v163
v_add_f32 v122,v184,v185
v_smfmac_f32_16x16x64_bf16 v[164:167],v[92:95],v[144:151],v13
s_cbranch_vccz RB29_%=
s_add_u32 m0,s58,12320
s_mul_i32 s69,s59,7
s_add_u32 s69,s69,s97
buffer_load_dwordx4 v2,s[44:47],s69 offen lds
s_add_u32 m0,m0,1024
s_add_u32 s69,s69,s53
buffer_load_dwordx4 v3,s[44:47],s69 offen lds
s_add_u32 m0,m0,1024
s_add_u32 s69,s69,s53
buffer_load_dwordx4 v2,s[44:47],s69 offen lds
s_add_u32 m0,m0,s50
s_add_u32 s69,s69,s53
buffer_load_dwordx4 v3,s[44:47],s69 offen lds
RB29_%=:
ds_read_b128 v[144:147],v113 offset:64
v_mul_f32 v126,v184,v184
v_fmac_f32 v126,v185,v185
v_smfmac_f32_16x16x64_bf16 v[188:191],v[92:95],v[148:155],v13
s_waitcnt lgkmcnt(3)
ds_read_b128 v[152:155],v117 offset:64
v_add_f32 v123,v186,v187
v_mul_f32 v127,v186,v186
v_fmac_f32 v127,v187,v187
v_mfma_f32_16x16x32_bf16 v[168:171],v[96:99],v[136:139],0
s_cbranch_vccz RB30_%=
s_add_u32 m0,m0,1024
s_add_u32 s69,s69,s53
buffer_load_dwordx4 v2,s[44:47],s69 offen lds
s_add_u32 m0,m0,1024
s_add_u32 s69,s69,s53
buffer_load_dwordx4 v3,s[44:47],s69 offen lds
s_add_u32 m0,m0,s50
s_add_u32 s69,s69,s53
buffer_load_dwordx4 v2,s[44:47],s69 offen lds
s_add_u32 m0,m0,1024
s_add_u32 s69,s69,s53
buffer_load_dwordx4 v3,s[44:47],s69 offen lds
RB30_%=:
ds_read_b128 v[148:151],v113 offset:3552
v_add_f32 v120,v120,v164
v_fmac_f32 v124,v164,v164
v_add_f32 v120,v120,v165
v_fmac_f32 v124,v165,v165
v_mfma_f32_16x16x32_bf16 v[192:195],v[96:99],v[140:143],0
s_waitcnt lgkmcnt(4)
s_cbranch_vccz RB31_%=
s_add_u32 m0,m0,1024
s_add_u32 s69,s69,s53
buffer_load_dwordx4 v2,s[44:47],s69 offen lds
s_add_u32 m0,m0,s50
s_add_u32 s69,s69,s53
buffer_load_dwordx4 v3,s[44:47],s69 offen lds
s_add_u32 m0,m0,1024
s_add_u32 s69,s69,s53
buffer_load_dwordx4 v2,s[44:47],s69 offen lds
s_add_u32 m0,m0,1024
s_add_u32 s69,s69,s53
buffer_load_dwordx4 v3,s[44:47],s69 offen lds
RB31_%=:
ds_read_b128 v[140:143],v116 offset:3616
v_add_f32 v121,v121,v166
v_fmac_f32 v125,v166,v166
v_add_f32 v121,v121,v167
v_fmac_f32 v125,v167,v167
v_add_f32 v122,v122,v188
v_smfmac_f32_16x16x64_bf16 v[168:171],v[100:103],v[128:135],v13
ds_read_b128 v[128:131],v112 offset:128
v_fmac_f32 v126,v188,v188
v_add_f32 v122,v122,v189
v_fmac_f32 v126,v189,v189
v_smfmac_f32_16x16x64_bf16 v[192:195],v[100:103],v[132:139],v13
s_waitcnt lgkmcnt(3)
ds_read_b128 v[136:139],v116 offset:128
v_add_f32 v123,v123,v190
v_fmac_f32 v127,v190,v190
v_add_f32 v123,v123,v191
v_fmac_f32 v127,v191,v191
v_mfma_f32_16x16x32_bf16 v[172:175],v[64:67],v[152:155],0
ds_read_b128 v[132:135],v112 offset:3616
v_add_f32 v120,v120,v168
v_fmac_f32 v124,v168,v168
v_add_f32 v120,v120,v169
v_fmac_f32 v124,v169,v169
v_mfma_f32_16x16x32_bf16 v[196:199],v[64:67],v[156:159],0
s_waitcnt lgkmcnt(4)
ds_read_b128 v[156:159],v117 offset:3616
v_add_f32 v121,v121,v170
v_fmac_f32 v125,v170,v170
v_add_f32 v121,v121,v171
v_fmac_f32 v125,v171,v171
v_add_f32 v122,v122,v192
v_smfmac_f32_16x16x64_bf16 v[172:175],v[68:71],v[144:151],v13
ds_read_b128 v[144:147],v113 offset:128
v_fmac_f32 v126,v192,v192
v_add_f32 v122,v122,v193
v_fmac_f32 v126,v193,v193
v_smfmac_f32_16x16x64_bf16 v[196:199],v[68:71],v[148:155],v13
s_waitcnt lgkmcnt(3)
ds_read_b128 v[152:155],v117 offset:128
v_add_f32 v123,v123,v194
v_fmac_f32 v127,v194,v194
v_add_f32 v123,v123,v195
v_fmac_f32 v127,v195,v195
v_mfma_f32_16x16x32_bf16 v[176:179],v[72:75],v[136:139],0
ds_read_b128 v[148:151],v113 offset:3616
v_add_f32 v120,v120,v172
v_fmac_f32 v124,v172,v172
v_add_f32 v120,v120,v173
v_fmac_f32 v124,v173,v173
v_mfma_f32_16x16x32_bf16 v[200:203],v[72:75],v[140:143],0
s_waitcnt lgkmcnt(4)
ds_read_b128 v[140:143],v118 offset:3488
v_add_f32 v121,v121,v174
v_fmac_f32 v125,v174,v174
v_add_f32 v121,v121,v175
v_fmac_f32 v125,v175,v175
v_add_f32 v122,v122,v196
v_smfmac_f32_16x16x64_bf16 v[176:179],v[76:79],v[128:135],v13
ds_read_b128 v[128:131],v114 offset:0
v_fmac_f32 v126,v196,v196
v_add_f32 v122,v122,v197
v_fmac_f32 v126,v197,v197
v_smfmac_f32_16x16x64_bf16 v[200:203],v[76:79],v[132:139],v13
s_waitcnt lgkmcnt(3)
ds_read_b128 v[136:139],v118 offset:0
v_add_f32 v123,v123,v198
v_fmac_f32 v127,v198,v198
v_add_f32 v123,v123,v199
v_fmac_f32 v127,v199,v199
v_mfma_f32_16x16x32_bf16 v[180:183],v[80:83],v[152:155],0
ds_read_b128 v[132:135],v114 offset:3488
v_add_f32 v120,v120,v176
v_fmac_f32 v124,v176,v176
v_add_f32 v120,v120,v177
v_fmac_f32 v124,v177,v177
v_mfma_f32_16x16x32_bf16 v[204:207],v[80:83],v[156:159],0
s_waitcnt lgkmcnt(4)
ds_read_b128 v[156:159],v119 offset:3488
v_add_f32 v121,v121,v178
v_fmac_f32 v125,v178,v178
v_add_f32 v121,v121,v179
v_fmac_f32 v125,v179,v179
v_add_f32 v122,v122,v200
v_smfmac_f32_16x16x64_bf16 v[180:183],v[84:87],v[144:151],v13
ds_read_b128 v[144:147],v115 offset:0
v_fmac_f32 v126,v200,v200
v_add_f32 v122,v122,v201
v_fmac_f32 v126,v201,v201
v_smfmac_f32_16x16x64_bf16 v[204:207],v[84:87],v[148:155],v13
s_waitcnt lgkmcnt(3)
ds_read_b128 v[152:155],v119 offset:0
v_add_f32 v123,v123,v202
v_fmac_f32 v127,v202,v202
v_add_f32 v123,v123,v203
v_fmac_f32 v127,v203,v203
v_mfma_f32_16x16x32_bf16 v[208:211],v[16:19],v[136:139],0
ds_read_b128 v[148:151],v115 offset:3488
v_add_f32 v120,v120,v180
v_fmac_f32 v124,v180,v180
v_add_f32 v120,v120,v181
v_fmac_f32 v124,v181,v181
v_mfma_f32_16x16x32_bf16 v[232:235],v[16:19],v[140:143],0
s_waitcnt lgkmcnt(4)
ds_read_b128 v[140:143],v118 offset:3552
v_add_f32 v121,v121,v182
v_fmac_f32 v125,v182,v182
v_add_f32 v121,v121,v183
s_mov_b32 s86,0xbe801d5a
v_fmac_f32 v125,v183,v183
v_add_f32 v122,v122,v204
v_smfmac_f32_16x16x64_bf16 v[208:211],v[20:23],v[128:135],v13
ds_read_b128 v[128:131],v114 offset:64
v_fmac_f32 v126,v204,v204
v_add_f32 v122,v122,v205
v_fmac_f32 v126,v205,v205
v_smfmac_f32_16x16x64_bf16 v[232:235],v[20:23],v[132:139],v13
s_waitcnt lgkmcnt(3)
ds_read_b128 v[136:139],v118 offset:64
v_add_f32 v123,v123,v206
v_fmac_f32 v127,v206,v206
v_add_f32 v123,v123,v207
v_fmac_f32 v127,v207,v207
v_mfma_f32_16x16x32_bf16 v[212:215],v[24:27],v[152:155],0
ds_read_b128 v[132:135],v114 offset:3552
v_add_f32 v120,v120,v208
v_fmac_f32 v124,v208,v208
v_add_f32 v120,v120,v209
v_fmac_f32 v124,v209,v209
v_mfma_f32_16x16x32_bf16 v[236:239],v[24:27],v[156:159],0
s_waitcnt lgkmcnt(4)
s_setprio 2
ds_read_b128 v[156:159],v119 offset:3552
v_add_f32 v121,v121,v210
v_fmac_f32 v125,v210,v210
v_add_f32 v121,v121,v211
v_fmac_f32 v125,v211,v211
v_add_f32 v122,v122,v232
v_smfmac_f32_16x16x64_bf16 v[212:215],v[28:31],v[144:151],v13
ds_read_b128 v[144:147],v115 offset:64
v_fmac_f32 v126,v232,v232
v_add_f32 v122,v122,v233
v_fmac_f32 v126,v233,v233
v_smfmac_f32_16x16x64_bf16 v[236:239],v[28:31],v[148:155],v13
s_waitcnt lgkmcnt(3)
ds_read_b128 v[152:155],v119 offset:64
v_add_f32 v123,v123,v234
v_fmac_f32 v127,v234,v234
v_add_f32 v123,v123,v235
v_fmac_f32 v127,v235,v235
v_mfma_f32_16x16x32_bf16 v[216:219],v[32:35],v[136:139],0
ds_read_b128 v[148:151],v115 offset:3552
v_add_f32 v120,v120,v212
v_fmac_f32 v124,v212,v212
v_add_f32 v120,v120,v213
v_fmac_f32 v124,v213,v213
v_add_f32 v121,v121,v214
v_mfma_f32_16x16x32_bf16 v[240:243],v[32:35],v[140:143],0
s_waitcnt lgkmcnt(4)
ds_read_b128 v[140:143],v118 offset:3616
v_fmac_f32 v125,v214,v214
v_add_f32 v121,v121,v215
v_fmac_f32 v125,v215,v215
v_add_f32 v122,v122,v236
v_fmac_f32 v126,v236,v236
v_add_f32 v122,v122,v237
v_smfmac_f32_16x16x64_bf16 v[216:219],v[36:39],v[128:135],v13
ds_read_b128 v[128:131],v114 offset:128
v_fmac_f32 v126,v237,v237
v_add_f32 v123,v123,v238
v_fmac_f32 v127,v238,v238
v_smfmac_f32_16x16x64_bf16 v[240:243],v[36:39],v[132:139],v13
s_waitcnt lgkmcnt(3)
ds_read_b128 v[136:139],v118 offset:128
v_add_f32 v123,v123,v239
v_fmac_f32 v127,v239,v239
v_add_u32 v112,v112,v8
v_add_u32 v116,v116,v8
v_mfma_f32_16x16x32_bf16 v[220:223],v[40:43],v[152:155],0
ds_read_b128 v[132:135],v114 offset:3616
ds_read_b128 v[104:107],v14 offset:0
v_add_f32 v120,v120,v216
v_fmac_f32 v124,v216,v216
v_add_f32 v120,v120,v217
v_fmac_f32 v124,v217,v217
v_mfma_f32_16x16x32_bf16 v[244:247],v[40:43],v[156:159],0
s_waitcnt lgkmcnt(5)
ds_read_b128 v[108:111],v7 offset:3584
ds_read_b128 v[156:159],v119 offset:3616
v_add_f32 v121,v121,v218
v_fmac_f32 v125,v218,v218
v_add_f32 v121,v121,v219
v_fmac_f32 v125,v219,v219
v_add_f32 v122,v122,v240
v_smfmac_f32_16x16x64_bf16 v[220:223],v[44:47],v[144:151],v13
ds_read_b128 v[144:147],v115 offset:128
v_fmac_f32 v126,v240,v240
v_add_f32 v122,v122,v241
v_fmac_f32 v126,v241,v241
v_smfmac_f32_16x16x64_bf16 v[244:247],v[44:47],v[148:155],v13
s_waitcnt lgkmcnt(5)
s_setprio 1
ds_read_b128 v[152:155],v119 offset:128
ds_read_b128 v[148:151],v115 offset:3616
v_add_f32 v123,v123,v242
v_fmac_f32 v127,v242,v242
v_add_f32 v123,v123,v243
v_fmac_f32 v127,v243,v243
v_mfma_f32_16x16x32_bf16 v[224:227],v[48:51],v[136:139],0
v_add_f32 v120,v120,v220
v_fmac_f32 v124,v220,v220
v_add_f32 v120,v120,v221
v_fmac_f32 v124,v221,v221
v_mfma_f32_16x16x32_bf16 v[248:251],v[48:51],v[140:143],0
s_waitcnt lgkmcnt(6)
ds_read_b128 v[140:143],v116 offset:192
ds_read_b128 v[88:91],v112 offset:3616
v_add_f32 v121,v121,v222
v_fmac_f32 v125,v222,v222
v_add_f32 v121,v121,v223
v_fmac_f32 v125,v223,v223
v_add_f32 v122,v122,v244
v_smfmac_f32_16x16x64_bf16 v[224:227],v[52:55],v[128:135],v13
ds_read_b128 v[128:131],v112 offset:3488
v_fmac_f32 v126,v244,v244
v_add_f32 v122,v122,v245
v_fmac_f32 v126,v245,v245
v_smfmac_f32_16x16x64_bf16 v[248:251],v[52:55],v[132:139],v13
s_waitcnt lgkmcnt(4)
ds_read_b128 v[136:139],v116 offset:0
ds_read_b128 v[132:135],v112 offset:3680
v_add_f32 v123,v123,v246
v_fmac_f32 v127,v246,v246
v_add_f32 v123,v123,v247
v_fmac_f32 v127,v247,v247
v_mfma_f32_16x16x32_bf16 v[228:231],v[56:59],v[152:155],0
ds_read_b128 v[92:95],v112 offset:3808
v_add_f32 v120,v120,v224
v_fmac_f32 v124,v224,v224
v_add_f32 v120,v120,v225
v_fmac_f32 v124,v225,v225
v_mfma_f32_16x16x32_bf16 v[252:255],v[56:59],v[156:159],0
s_waitcnt lgkmcnt(6)
ds_read_b128 v[156:159],v116 offset:256
ds_read_b128 v[96:99],v116 offset:128
v_add_f32 v121,v121,v226
v_fmac_f32 v125,v226,v226
v_add_f32 v121,v121,v227
v_fmac_f32 v125,v227,v227
v_add_f32 v122,v122,v248
v_smfmac_f32_16x16x64_bf16 v[228:231],v[60:63],v[144:151],v13
s_nop 0
ds_read_b128 v[144:147],v112 offset:3552
v_fmac_f32 v126,v248,v248
v_add_f32 v122,v122,v249
v_fmac_f32 v126,v249,v249
v_smfmac_f32_16x16x64_bf16 v[252:255],v[60:63],v[148:155],v13
s_setprio 0
ds_read_b128 v[152:155],v116 offset:64
ds_read_b128 v[148:151],v112 offset:3744
v_add_f32 v123,v123,v250
v_fmac_f32 v127,v250,v250
v_add_f32 v123,v123,v251
v_fmac_f32 v127,v251,v251
ds_read_b128 v[100:103],v116 offset:320
v_add_f32 v120,v120,v228
v_fmac_f32 v124,v228,v228
v_add_f32 v120,v120,v229
v_fmac_f32 v124,v229,v229
v_add_f32 v121,v121,v230
v_fmac_f32 v125,v230,v230
v_add_f32 v121,v121,v231
v_fmac_f32 v125,v231,v231
s_waitcnt lgkmcnt(0)
s_mov_b32 s86,0x1234567
s_waitcnt vmcnt(0)
s_barrier
s_setprio 0
v_add_f32 v122,v122,v252
v_fmac_f32 v126,v252,v252
v_add_f32 v122,v122,v253
v_fmac_f32 v126,v253,v253
v_add_f32 v123,v123,v254
v_fmac_f32 v127,v254,v254
v_add_f32 v123,v123,v255
v_fmac_f32 v127,v255,v255
s_setprio 3
ds_read_b128 v[56:59],v14 offset:256
ds_read_b128 v[60:63],v7 offset:3616
s_setprio 0
v_permlane32_swap_b32 v120,v122
v_permlane32_swap_b32 v121,v123
v_permlane32_swap_b32 v124,v126
v_permlane32_swap_b32 v125,v127
v_add_f32 v120,v120,v122
v_add_f32 v121,v121,v123
v_add_f32 v124,v124,v126
v_add_f32 v125,v125,v127
s_cbranch_vccz RB32_%=
s_add_u32 m0,s58,0
s_mul_i32 s69,s59,9
s_add_u32 s69,s69,s97
buffer_load_dwordx4 v2,s[44:47],s69 offen lds
s_add_u32 m0,m0,1024
s_add_u32 s69,s69,s53
buffer_load_dwordx4 v3,s[44:47],s69 offen lds
s_add_u32 m0,m0,1024
s_add_u32 s69,s69,s53
buffer_load_dwordx4 v2,s[44:47],s69 offen lds
s_add_u32 m0,m0,s50
s_add_u32 s69,s69,s53
buffer_load_dwordx4 v3,s[44:47],s69 offen lds
RB32_%=:
v_permlane16_swap_b32 v120,v121
v_permlane16_swap_b32 v124,v125
v_add_f32 v113,v120,v121
v_add_f32 v114,v124,v125
v_mul_f32 v113,0x3c2aaaab,v113
v_mul_f32 v114,0x3c2aaaab,v114
v_mul_f32 v115,0x5e000000,v114
v_fma_f32 v114,-v113,v113,v114
v_pk_maximum3_f16 v1,v1,v115,v115 op_sel:[0,1,1] op_sel_hi:[1,1,1]
v_max_f32 v114,0,v114
v_add_f32 v114,0x3727c5ac,v114
v_rsq_f32 v114,v114
s_cbranch_vccz RB33_%=
s_add_u32 m0,m0,1024
s_add_u32 s69,s69,s53
buffer_load_dwordx4 v2,s[44:47],s69 offen lds
s_add_u32 m0,m0,1024
s_add_u32 s69,s69,s53
buffer_load_dwordx4 v3,s[44:47],s69 offen lds
s_add_u32 m0,m0,s50
s_add_u32 s69,s69,s53
buffer_load_dwordx4 v2,s[44:47],s69 offen lds
s_add_u32 m0,m0,1024
s_add_u32 s69,s69,s53
buffer_load_dwordx4 v3,s[44:47],s69 offen lds
RB33_%=:
v_mul_f32 v120,0x8befe000,v114
v_mul_f32 v121,0x8befe000,v114
v_mul_f32 v124,-v113,v120
v_mul_f32 v125,-v113,v120
v_permlane16_swap_b32 v120,v121
s_nop 0
v_permlane16_swap_b32 v124,v125
v_mov_b32 v122,v120
v_mov_b32 v123,v121
v_mov_b32 v126,v124
v_mov_b32 v127,v125
v_permlane32_swap_b32 v120,v122
v_permlane32_swap_b32 v124,v126
s_cbranch_vccz RB34_%=
s_add_u32 m0,m0,1024
s_add_u32 s69,s69,s53
buffer_load_dwordx4 v2,s[44:47],s69 offen lds
s_add_u32 m0,m0,s50
s_add_u32 s69,s69,s53
buffer_load_dwordx4 v3,s[44:47],s69 offen lds
s_add_u32 m0,m0,1024
s_add_u32 s69,s69,s53
buffer_load_dwordx4 v2,s[44:47],s69 offen lds
s_add_u32 m0,m0,1024
s_add_u32 s69,s69,s53
buffer_load_dwordx4 v3,s[44:47],s69 offen lds
RB34_%=:
v_permlane32_swap_b32 v121,v123
v_permlane32_swap_b32 v125,v127
v_fma_f32 v160,v160,v120,v124
s_setprio 3
ds_read_b64_tr_b16 v[64:65],v4 offset:12832
s_setprio 0
v_fma_f32 v161,v161,v120,v124
s_setprio 3
ds_read_b64_tr_b16 v[66:67],v4 offset:14880
s_setprio 0
v_fma_f32 v162,v162,v121,v125
s_setprio 3
ds_read_b128 v[112:115],v7 offset:1152
s_setprio 0
v_fma_f32 v163,v163,v121,v125
s_setprio 3
ds_read_b64_tr_b16 v[68:69],v5 offset:12832
s_setprio 0
v_fma_f32 v164,v164,v120,v124
s_setprio 3
ds_read_b64_tr_b16 v[70:71],v5 offset:14880
s_setprio 0
v_fma_f32 v165,v165,v120,v124
s_setprio 3
ds_read_b128 v[116:119],v7 offset:1184
s_setprio 0
v_fma_f32 v166,v166,v121,v125
s_setprio 3
ds_read_b64_tr_b16 v[72:73],v4 offset:16928
s_setprio 0
v_fma_f32 v167,v167,v121,v125
s_setprio 3
ds_read_b64_tr_b16 v[74:75],v4 offset:18976
s_setprio 0
v_fma_f32 v208,v208,v120,v124
s_setprio 3
ds_read_b64_tr_b16 v[76:77],v5 offset:16928
s_setprio 0
v_fma_f32 v209,v209,v120,v124
s_setprio 3
ds_read_b64_tr_b16 v[78:79],v5 offset:18976
s_setprio 0
v_fma_f32 v210,v210,v121,v125
s_setprio 3
ds_read_b64_tr_b16 v[80:81],v4 offset:21024
s_setprio 0
v_fma_f32 v211,v211,v121,v125
s_setprio 3
ds_read_b64_tr_b16 v[82:83],v4 offset:23072
s_setprio 0
v_fma_f32 v212,v212,v120,v124
s_setprio 3
ds_read_b64_tr_b16 v[84:85],v5 offset:21024
s_setprio 0
v_fma_f32 v213,v213,v120,v124
s_setprio 3
ds_read_b64_tr_b16 v[86:87],v5 offset:23072
s_setprio 0
v_fma_f32 v214,v214,v121,v125
v_fma_f32 v215,v215,v121,v125
v_cvt_pk_bf16_f32 v16,v160,v161
v_cvt_pk_bf16_f32 v20,v162,v163
v_cvt_pk_bf16_f32 v17,v164,v165
v_cvt_pk_bf16_f32 v21,v166,v167
v_cvt_pk_bf16_f32 v18,v208,v209
v_cvt_pk_bf16_f32 v22,v210,v211
v_cvt_pk_bf16_f32 v19,v212,v213
v_cvt_pk_bf16_f32 v23,v214,v215
v_fma_f32 v168,v168,v120,v124
v_fma_f32 v169,v169,v120,v124
v_fma_f32 v170,v170,v121,v125
v_mfma_f32_16x16x32_bf16 v[160:163],v[104:107],v[128:131],v[108:111]
v_fma_f32 v171,v171,v121,v125
v_fma_f32 v172,v172,v120,v124
v_fma_f32 v173,v173,v120,v124
v_fma_f32 v174,v174,v121,v125
v_fma_f32 v175,v175,v121,v125
v_fma_f32 v216,v216,v120,v124
v_mfma_f32_16x16x32_bf16 v[164:167],v[104:107],v[132:135],v[108:111]
v_fma_f32 v217,v217,v120,v124
v_fma_f32 v218,v218,v121,v125
v_fma_f32 v219,v219,v121,v125
v_fma_f32 v220,v220,v120,v124
v_fma_f32 v221,v221,v120,v124
v_fma_f32 v222,v222,v121,v125
v_mfma_f32_16x16x32_bf16 v[208:211],v[104:107],v[136:139],v[108:111]
v_fma_f32 v223,v223,v121,v125
v_cvt_pk_bf16_f32 v24,v168,v169
v_cvt_pk_bf16_f32 v28,v170,v171
v_cvt_pk_bf16_f32 v25,v172,v173
v_cvt_pk_bf16_f32 v29,v174,v175
s_setprio 3
v_mfma_f32_16x16x32_bf16 v[212:215],v[104:107],v[140:143],v[108:111]
ds_read_b128 v[104:107],v14 offset:512
s_setprio 0
v_cvt_pk_bf16_f32 v26,v216,v217
v_cvt_pk_bf16_f32 v30,v218,v219
v_cvt_pk_bf16_f32 v27,v220,v221
v_cvt_pk_bf16_f32 v31,v222,v223
v_fma_f32 v176,v176,v120,v124
v_fma_f32 v177,v177,v120,v124
v_fma_f32 v178,v178,v121,v125
s_waitcnt lgkmcnt(15)
s_setprio 3
v_mfma_f32_16x16x32_bf16 v[168:171],v[56:59],v[128:131],v[60:63]
ds_read_b128 v[108:111],v7 offset:3712
s_setprio 0
v_fma_f32 v179,v179,v121,v125
v_fma_f32 v180,v180,v120,v124
v_fma_f32 v181,v181,v120,v124
v_fma_f32 v182,v182,v121,v125
v_fma_f32 v183,v183,v121,v125
v_fma_f32 v224,v224,v120,v124
v_mfma_f32_16x16x32_bf16 v[172:175],v[56:59],v[132:135],v[60:63]
v_fma_f32 v225,v225,v120,v124
v_fma_f32 v226,v226,v121,v125
v_fma_f32 v227,v227,v121,v125
v_fma_f32 v228,v228,v120,v124
v_fma_f32 v229,v229,v120,v124
v_fma_f32 v230,v230,v121,v125
v_mfma_f32_16x16x32_bf16 v[216:219],v[56:59],v[136:139],v[60:63]
v_fma_f32 v231,v231,v121,v125
v_cvt_pk_bf16_f32 v32,v176,v177
v_cvt_pk_bf16_f32 v36,v178,v179
v_cvt_pk_bf16_f32 v33,v180,v181
v_cvt_pk_bf16_f32 v37,v182,v183
s_setprio 3
v_mfma_f32_16x16x32_bf16 v[220:223],v[56:59],v[140:143],v[60:63]
ds_read_b128 v[56:59],v14 offset:768
ds_read_b128 v[60:63],v7 offset:3744
s_setprio 0
v_cvt_pk_bf16_f32 v34,v224,v225
v_cvt_pk_bf16_f32 v38,v226,v227
v_cvt_pk_bf16_f32 v35,v228,v229
v_cvt_pk_bf16_f32 v39,v230,v231
v_fma_f32 v184,v184,v122,v126
s_waitcnt lgkmcnt(2)
v_mfma_f32_16x16x32_bf16 v[176:179],v[104:107],v[144:147],v[108:111]
v_fma_f32 v185,v185,v122,v126
v_fma_f32 v186,v186,v123,v127
v_fma_f32 v187,v187,v123,v127
v_mfma_f32_16x16x32_bf16 v[128:131],v[64:67],v[16:19],v[112:115]
v_fma_f32 v188,v188,v122,v126
v_fma_f32 v189,v189,v122,v126
v_fma_f32 v190,v190,v123,v127
v_mfma_f32_16x16x32_bf16 v[180:183],v[104:107],v[148:151],v[108:111]
v_fma_f32 v191,v191,v123,v127
v_fma_f32 v232,v232,v122,v126
v_fma_f32 v233,v233,v122,v126
v_mfma_f32_16x16x32_bf16 v[136:139],v[64:67],v[20:23],v[112:115]
v_fma_f32 v234,v234,v123,v127
v_fma_f32 v235,v235,v123,v127
v_fma_f32 v236,v236,v122,v126
v_mfma_f32_16x16x32_bf16 v[224:227],v[104:107],v[152:155],v[108:111]
v_fma_f32 v237,v237,v122,v126
v_fma_f32 v238,v238,v123,v127
v_fma_f32 v239,v239,v123,v127
v_mfma_f32_16x16x32_bf16 v[140:143],v[68:71],v[20:23],v[116:119]
v_cvt_pk_bf16_f32 v40,v184,v185
v_cvt_pk_bf16_f32 v44,v186,v187
v_cvt_pk_bf16_f32 v41,v188,v189
s_setprio 3
v_mfma_f32_16x16x32_bf16 v[228:231],v[104:107],v[156:159],v[108:111]
ds_read_b128 v[104:107],v14 offset:1024
ds_read_b128 v[108:111],v7 offset:3840
s_setprio 0
v_cvt_pk_bf16_f32 v45,v190,v191
v_cvt_pk_bf16_f32 v42,v232,v233
v_cvt_pk_bf16_f32 v46,v234,v235
v_mfma_f32_16x16x32_bf16 v[132:135],v[68:71],v[16:19],v[116:119]
v_cvt_pk_bf16_f32 v43,v236,v237
v_cvt_pk_bf16_f32 v47,v238,v239
v_fma_f32 v192,v192,v122,v126
s_waitcnt lgkmcnt(2)
v_mfma_f32_16x16x32_bf16 v[184:187],v[56:59],v[144:147],v[60:63]
v_fma_f32 v193,v193,v122,v126
v_fma_f32 v194,v194,v123,v127
v_mfma_f32_16x16x32_bf16 v[128:131],v[72:75],v[24:27],v[128:131]
s_nop 0
v_fma_f32 v195,v195,v123,v127
v_fma_f32 v196,v196,v122,v126
v_mfma_f32_16x16x32_bf16 v[188:191],v[56:59],v[148:151],v[60:63]
v_fma_f32 v197,v197,v122,v126
v_fma_f32 v198,v198,v123,v127
v_mfma_f32_16x16x32_bf16 v[136:139],v[72:75],v[28:31],v[136:139]
s_nop 0
v_fma_f32 v199,v199,v123,v127
v_fma_f32 v240,v240,v122,v126
v_mfma_f32_16x16x32_bf16 v[232:235],v[56:59],v[152:155],v[60:63]
v_fma_f32 v241,v241,v122,v126
v_fma_f32 v242,v242,v123,v127
v_mfma_f32_16x16x32_bf16 v[140:143],v[76:79],v[28:31],v[140:143]
s_nop 0
v_fma_f32 v243,v243,v123,v127
v_fma_f32 v244,v244,v122,v126
s_setprio 3
v_mfma_f32_16x16x32_bf16 v[236:239],v[56:59],v[156:159],v[60:63]
ds_read_b128 v[144:147],v14 offset:1280
ds_read_b128 v[148:151],v7 offset:3872
s_setprio 0
v_fma_f32 v245,v245,v122,v126
v_fma_f32 v246,v246,v123,v127
v_mfma_f32_16x16x32_bf16 v[132:135],v[76:79],v[24:27],v[132:135]
s_nop 0
v_fma_f32 v247,v247,v123,v127
v_cvt_pk_bf16_f32 v48,v192,v193
v_cvt_pk_bf16_f32 v52,v194,v195
v_cvt_pk_bf16_f32 v49,v196,v197
v_cvt_pk_bf16_f32 v53,v198,v199
v_cvt_pk_bf16_f32 v50,v240,v241
v_cvt_pk_bf16_f32 v54,v242,v243
v_cvt_pk_bf16_f32 v51,v244,v245
v_cvt_pk_bf16_f32 v55,v246,v247
v_fma_f32 v200,v200,v122,v126
s_waitcnt lgkmcnt(2)
v_mfma_f32_16x16x32_bf16 v[192:195],v[104:107],v[88:91],v[108:111]
v_fma_f32 v201,v201,v122,v126
v_fma_f32 v202,v202,v123,v127
v_fma_f32 v203,v203,v123,v127
v_mfma_f32_16x16x32_bf16 v[128:131],v[80:83],v[32:35],v[128:131]
s_nop 0
v_fma_f32 v204,v204,v122,v126
v_fma_f32 v205,v205,v122,v126
v_fma_f32 v206,v206,v123,v127
v_mfma_f32_16x16x32_bf16 v[196:199],v[104:107],v[92:95],v[108:111]
v_fma_f32 v207,v207,v123,v127
v_fma_f32 v248,v248,v122,v126
v_fma_f32 v249,v249,v122,v126
v_mfma_f32_16x16x32_bf16 v[136:139],v[80:83],v[36:39],v[136:139]
s_nop 0
v_fma_f32 v250,v250,v123,v127
v_fma_f32 v251,v251,v123,v127
v_fma_f32 v252,v252,v122,v126
v_mfma_f32_16x16x32_bf16 v[240:243],v[104:107],v[96:99],v[108:111]
v_fma_f32 v253,v253,v122,v126
v_fma_f32 v254,v254,v123,v127
v_fma_f32 v255,v255,v123,v127
v_mfma_f32_16x16x32_bf16 v[140:143],v[84:87],v[36:39],v[140:143]
s_nop 0
v_cvt_pk_bf16_f32 v56,v200,v201
v_cvt_pk_bf16_f32 v60,v202,v203
v_cvt_pk_bf16_f32 v57,v204,v205
v_mfma_f32_16x16x32_bf16 v[244:247],v[104:107],v[100:103],v[108:111]
v_cvt_pk_bf16_f32 v61,v206,v207
v_cvt_pk_bf16_f32 v58,v248,v249
v_cvt_pk_bf16_f32 v62,v250,v251
v_mfma_f32_16x16x32_bf16 v[132:135],v[84:87],v[32:35],v[132:135]
s_nop 0
v_cvt_pk_bf16_f32 v59,v252,v253
v_cvt_pk_bf16_f32 v63,v254,v255
s_waitcnt lgkmcnt(0)
v_mfma_f32_16x16x32_bf16 v[200:203],v[144:147],v[88:91],v[148:151]
s_mov_b32 s86,0x1234567
s_waitcnt vmcnt(0)
s_barrier
s_setprio 3
ds_read_b64_tr_b16 v[104:105],v6 offset:128
v_lshlrev_b32_sdwa v128,1,v128 dst_sel:DWORD dst_unused:UNUSED_PAD src0_sel:DWORD src1_sel:WORD_1
v_lshlrev_b32_sdwa v130,1,v130 dst_sel:DWORD dst_unused:UNUSED_PAD src0_sel:DWORD src1_sel:WORD_1
v_lshlrev_b32_sdwa v129,1,v129 dst_sel:DWORD dst_unused:UNUSED_PAD src0_sel:DWORD src1_sel:WORD_1
v_mfma_f32_16x16x32_bf16 v[204:207],v[144:147],v[92:95],v[148:151]
ds_read_u16 v128,v128
ds_read_b64_tr_b16 v[106:107],v6 offset:1664
v_lshlrev_b32_sdwa v131,1,v131 dst_sel:DWORD dst_unused:UNUSED_PAD src0_sel:DWORD src1_sel:WORD_1
v_fma_mixlo_f16 v120,v140,s99,0
v_fma_mixlo_f16 v122,v141,s99,0
v_mfma_f32_16x16x32_bf16 v[248:251],v[144:147],v[96:99],v[148:151]
ds_read_u16_d16_hi v130,v130
ds_read_b64_tr_b16 v[108:109],v6 offset:136
v_lshlrev_b32_sdwa v132,1,v132 dst_sel:DWORD dst_unused:UNUSED_PAD src0_sel:DWORD src1_sel:WORD_1
v_lshlrev_b32_sdwa v134,1,v134 dst_sel:DWORD dst_unused:UNUSED_PAD src0_sel:DWORD src1_sel:WORD_1
v_mfma_f32_16x16x32_bf16 v[252:255],v[144:147],v[100:103],v[148:151]
ds_read_u16 v129,v129
ds_read_b64_tr_b16 v[110:111],v6 offset:1672
v_fma_mixhi_f16 v120,v142,s99,0
v_fma_mixhi_f16 v122,v143,s99,0
v_lshlrev_b32_sdwa v133,1,v133 dst_sel:DWORD dst_unused:UNUSED_PAD src0_sel:DWORD src1_sel:WORD_1
v_pk_fma_f16 v121,v120,v120,s54
v_mfma_f32_16x16x32_bf16 v[144:147],v[64:67],v[40:43],v[112:115]
ds_read_u16_d16_hi v131,v131
ds_read_b64_tr_b16 v[88:89],v6 offset:0
v_lshlrev_b32_sdwa v135,1,v135 dst_sel:DWORD dst_unused:UNUSED_PAD src0_sel:DWORD src1_sel:WORD_1
v_pk_fma_f16 v123,v122,v122,s54
v_pk_mul_f16 v121,v120,v121
v_mfma_f32_16x16x32_bf16 v[152:155],v[64:67],v[44:47],v[112:115]
ds_read_u16 v132,v132
ds_read_b64_tr_b16 v[90:91],v6 offset:1536
v_lshlrev_b32_sdwa v136,1,v136 dst_sel:DWORD dst_unused:UNUSED_PAD src0_sel:DWORD src1_sel:WORD_1
v_pk_mul_f16 v123,v122,v123
v_mfma_f32_16x16x32_bf16 v[156:159],v[68:71],v[44:47],v[116:119]
ds_read_u16_d16_hi v134,v134
ds_read_b64_tr_b16 v[92:93],v6 offset:8
v_lshlrev_b32_sdwa v138,1,v138 dst_sel:DWORD dst_unused:UNUSED_PAD src0_sel:DWORD src1_sel:WORD_1
v_exp_f16 v120,v121
v_mfma_f32_16x16x32_bf16 v[148:151],v[68:71],v[40:43],v[116:119]
ds_read_u16 v133,v133
ds_read_b64_tr_b16 v[94:95],v6 offset:1544
v_lshlrev_b32_sdwa v137,1,v137 dst_sel:DWORD dst_unused:UNUSED_PAD src0_sel:DWORD src1_sel:WORD_1
v_lshlrev_b32_sdwa v139,1,v139 dst_sel:DWORD dst_unused:UNUSED_PAD src0_sel:DWORD src1_sel:WORD_1
v_exp_f16 v122,v123
v_mfma_f32_16x16x32_bf16 v[144:147],v[72:75],v[48:51],v[144:147]
ds_read_u16_d16_hi v135,v135
ds_read_b64_tr_b16 v[96:97],v6 offset:64
v_exp_f16_sdwa v122,v123 dst_sel:WORD_1 dst_unused:UNUSED_PRESERVE src0_sel:WORD_1
v_mfma_f32_16x16x32_bf16 v[152:155],v[72:75],v[52:55],v[152:155]
ds_read_u16 v136,v136
ds_read_b64_tr_b16 v[98:99],v6 offset:1600
v_exp_f16_sdwa v120,v121 dst_sel:WORD_1 dst_unused:UNUSED_PRESERVE src0_sel:WORD_1
v_mfma_f32_16x16x32_bf16 v[156:159],v[76:79],v[52:55],v[156:159]
ds_read_u16_d16_hi v138,v138
ds_read_b64_tr_b16 v[100:101],v6 offset:72
v_pk_fma_f16 v120,v120,s55,s55
v_pk_fma_f16 v122,v122,s55,s55
v_rcp_f16 v121,v120
v_mfma_f32_16x16x32_bf16 v[148:151],v[76:79],v[48:51],v[148:151]
ds_read_u16 v137,v137
ds_read_b64_tr_b16 v[102:103],v6 offset:1608
v_rcp_f16 v123,v122
v_mfma_f32_16x16x32_bf16 v[144:147],v[80:83],v[56:59],v[144:147]
ds_read_u16_d16_hi v139,v139
v_rcp_f16_sdwa v121,v120 dst_sel:WORD_1 dst_unused:UNUSED_PRESERVE src0_sel:WORD_1
v_mfma_f32_16x16x32_bf16 v[152:155],v[80:83],v[60:63],v[152:155]
ds_read_b64_tr_b16 v[64:65],v4 offset:0
v_rcp_f16_sdwa v123,v122 dst_sel:WORD_1 dst_unused:UNUSED_PRESERVE src0_sel:WORD_1
v_fma_mix_f32 v142,v142,v121,0 op_sel:[0,1,0] op_sel_hi:[0,1,0]
v_mfma_f32_16x16x32_bf16 v[156:159],v[84:87],v[60:63],v[156:159]
ds_read_b64_tr_b16 v[66:67],v4 offset:2048
v_fma_mix_f32 v140,v140,v121,0 op_sel_hi:[0,1,0]
s_waitcnt lgkmcnt(15)
v_lshl_add_u64 v[128:129],v[128:129],0,v[130:131]
s_waitcnt lgkmcnt(10)
v_lshl_add_u64 v[130:131],v[132:133],0,v[134:135]
v_mfma_f32_16x16x32_bf16 v[148:151],v[84:87],v[56:59],v[148:151]
ds_read_b128 v[112:115],v7 offset:1280
v_fma_mix_f32 v141,v141,v123,0 op_sel_hi:[0,1,0]
v_fma_mix_f32 v143,v143,v123,0 op_sel:[0,1,0] op_sel_hi:[0,1,0]
v_lshlrev_b32_sdwa v144,1,v144 dst_sel:DWORD dst_unused:UNUSED_PAD src0_sel:DWORD src1_sel:WORD_1
v_lshlrev_b32_sdwa v146,1,v146 dst_sel:DWORD dst_unused:UNUSED_PAD src0_sel:DWORD src1_sel:WORD_1
v_mfma_f32_16x16x32_bf16 v[160:163],v[88:91],v[128:131],v[160:163]
ds_read_u16 v144,v144
v_lshlrev_b32_sdwa v145,1,v145 dst_sel:DWORD dst_unused:UNUSED_PAD src0_sel:DWORD src1_sel:WORD_1
v_lshlrev_b32_sdwa v147,1,v147 dst_sel:DWORD dst_unused:UNUSED_PAD src0_sel:DWORD src1_sel:WORD_1
v_fma_mixlo_f16 v122,v157,s99,0
v_mfma_f32_16x16x32_bf16 v[168:171],v[92:95],v[128:131],v[168:171]
s_waitcnt lgkmcnt(9)
ds_read_b64_tr_b16 v[68:69],v5 offset:0
ds_read_b64_tr_b16 v[70:71],v5 offset:2048
v_fma_mixlo_f16 v120,v156,s99,0
v_mfma_f32_16x16x32_bf16 v[176:179],v[96:99],v[128:131],v[176:179]
s_waitcnt lgkmcnt(7)
ds_read_u16_d16_hi v146,v146
v_lshlrev_b32_sdwa v148,1,v148 dst_sel:DWORD dst_unused:UNUSED_PAD src0_sel:DWORD src1_sel:WORD_1
v_lshlrev_b32_sdwa v150,1,v150 dst_sel:DWORD dst_unused:UNUSED_PAD src0_sel:DWORD src1_sel:WORD_1
v_mfma_f32_16x16x32_bf16 v[184:187],v[100:103],v[128:131],v[184:187]
s_waitcnt lgkmcnt(7)
ds_read_b128 v[116:119],v7 offset:1312
ds_read_b64_tr_b16 v[72:73],v4 offset:4096
v_lshl_add_u64 v[136:137],v[136:137],0,v[138:139]
v_fma_mixhi_f16 v120,v158,s99,0
v_mfma_f32_16x16x32_bf16 v[192:195],v[104:107],v[128:131],v[192:195]
ds_read_u16 v145,v145
v_cvt_pk_bf16_f32 v138,v140,v142
v_cvt_pk_bf16_f32 v139,v141,v143
v_mfma_f32_16x16x32_bf16 v[200:203],v[108:111],v[128:131],v[200:203]
v_fma_mixhi_f16 v122,v159,s99,0
v_mfma_f32_16x16x32_bf16 v[164:167],v[88:91],v[136:139],v[164:167]
ds_read_b64_tr_b16 v[74:75],v4 offset:6144
ds_read_b64_tr_b16 v[76:77],v5 offset:4096
v_lshlrev_b32_sdwa v149,1,v149 dst_sel:DWORD dst_unused:UNUSED_PAD src0_sel:DWORD src1_sel:WORD_1
v_lshlrev_b32_sdwa v151,1,v151 dst_sel:DWORD dst_unused:UNUSED_PAD src0_sel:DWORD src1_sel:WORD_1
v_mfma_f32_16x16x32_bf16 v[172:175],v[92:95],v[136:139],v[172:175]
ds_read_b64_tr_b16 v[78:79],v5 offset:6144
ds_read_b64_tr_b16 v[80:81],v4 offset:8192
v_pk_fma_f16 v121,v120,v120,s54
s_nop 0
s_mov_b32 s86,0xbe801d5a
v_pk_fma_f16 v123,v122,v122,s54
v_mfma_f32_16x16x32_bf16 v[180:183],v[96:99],v[136:139],v[180:183]
ds_read_u16_d16_hi v147,v147
v_pk_mul_f16 v121,v120,v121
v_pk_mul_f16 v123,v122,v123
v_mfma_f32_16x16x32_bf16 v[188:191],v[100:103],v[136:139],v[188:191]
ds_read_u16 v148,v148
v_lshlrev_b32_sdwa v152,1,v152 dst_sel:DWORD dst_unused:UNUSED_PAD src0_sel:DWORD src1_sel:WORD_1
v_lshlrev_b32_sdwa v154,1,v154 dst_sel:DWORD dst_unused:UNUSED_PAD src0_sel:DWORD src1_sel:WORD_1
v_mfma_f32_16x16x32_bf16 v[196:199],v[104:107],v[136:139],v[196:199]
v_exp_f16 v120,v121
v_mfma_f32_16x16x32_bf16 v[204:207],v[108:111],v[136:139],v[204:207]
s_waitcnt lgkmcnt(13)
ds_read_b64_tr_b16 v[82:83],v4 offset:10240
ds_read_b64_tr_b16 v[84:85],v5 offset:8192
v_exp_f16 v122,v123
v_mfma_f32_16x16x32_bf16 v[128:131],v[64:67],v[16:19],v[112:115]
v_lshlrev_b32_sdwa v153,1,v153 dst_sel:DWORD dst_unused:UNUSED_PAD src0_sel:DWORD src1_sel:WORD_1
v_lshlrev_b32_sdwa v155,1,v155 dst_sel:DWORD dst_unused:UNUSED_PAD src0_sel:DWORD src1_sel:WORD_1
v_mfma_f32_16x16x32_bf16 v[136:139],v[64:67],v[20:23],v[112:115]
s_waitcnt lgkmcnt(10)
ds_read_u16_d16_hi v150,v150
ds_read_u16 v149,v149
v_exp_f16_sdwa v120,v121 dst_sel:WORD_1 dst_unused:UNUSED_PRESERVE src0_sel:WORD_1
v_mfma_f32_16x16x32_bf16 v[140:143],v[68:71],v[20:23],v[116:119]
v_exp_f16_sdwa v122,v123 dst_sel:WORD_1 dst_unused:UNUSED_PRESERVE src0_sel:WORD_1
v_mfma_f32_16x16x32_bf16 v[132:135],v[68:71],v[16:19],v[116:119]
s_waitcnt lgkmcnt(9)
ds_read_b64_tr_b16 v[86:87],v5 offset:10240
v_pk_fma_f16 v120,v120,s55,s55
v_mfma_f32_16x16x32_bf16 v[128:131],v[72:75],v[24:27],v[128:131]
v_pk_fma_f16 v122,v122,s55,s55
v_rcp_f16 v121,v120
v_mfma_f32_16x16x32_bf16 v[136:139],v[72:75],v[28:31],v[136:139]
s_waitcnt lgkmcnt(8)
ds_read_u16_d16_hi v151,v151
v_rcp_f16 v123,v122
v_mfma_f32_16x16x32_bf16 v[140:143],v[76:79],v[28:31],v[140:143]
v_rcp_f16_sdwa v121,v120 dst_sel:WORD_1 dst_unused:UNUSED_PRESERVE src0_sel:WORD_1
v_mfma_f32_16x16x32_bf16 v[132:135],v[76:79],v[24:27],v[132:135]
s_waitcnt lgkmcnt(5)
ds_read_u16 v152,v152
ds_read_u16_d16_hi v154,v154
v_rcp_f16_sdwa v123,v122 dst_sel:WORD_1 dst_unused:UNUSED_PRESERVE src0_sel:WORD_1
v_mfma_f32_16x16x32_bf16 v[128:131],v[80:83],v[32:35],v[128:131]
ds_read_u16 v153,v153
v_fma_mix_f32 v156,v156,v121,0 op_sel_hi:[0,1,0]
v_fma_mix_f32 v158,v158,v121,0 op_sel:[0,1,0] op_sel_hi:[0,1,0]
v_mfma_f32_16x16x32_bf16 v[136:139],v[80:83],v[36:39],v[136:139]
s_waitcnt lgkmcnt(3)
v_lshl_add_u64 v[144:145],v[144:145],0,v[146:147]
v_lshl_add_u64 v[146:147],v[148:149],0,v[150:151]
v_mfma_f32_16x16x32_bf16 v[140:143],v[84:87],v[36:39],v[140:143]
ds_read_u16_d16_hi v155,v155
v_fma_mix_f32 v157,v157,v123,0 op_sel_hi:[0,1,0]
v_fma_mix_f32 v159,v159,v123,0 op_sel:[0,1,0] op_sel_hi:[0,1,0]
v_mfma_f32_16x16x32_bf16 v[132:135],v[84:87],v[32:35],v[132:135]
v_lshlrev_b32_sdwa v128,1,v128 dst_sel:DWORD dst_unused:UNUSED_PAD src0_sel:DWORD src1_sel:WORD_1
v_lshlrev_b32_sdwa v130,1,v130 dst_sel:DWORD dst_unused:UNUSED_PAD src0_sel:DWORD src1_sel:WORD_1
v_mfma_f32_16x16x32_bf16 v[208:211],v[88:91],v[144:147],v[208:211]
v_lshlrev_b32_sdwa v129,1,v129 dst_sel:DWORD dst_unused:UNUSED_PAD src0_sel:DWORD src1_sel:WORD_1
v_lshlrev_b32_sdwa v131,1,v131 dst_sel:DWORD dst_unused:UNUSED_PAD src0_sel:DWORD src1_sel:WORD_1
v_mfma_f32_16x16x32_bf16 v[216:219],v[92:95],v[144:147],v[216:219]
ds_read_u16 v128,v128
ds_read_u16_d16_hi v130,v130
v_fma_mixlo_f16 v120,v140,s99,0
v_fma_mixlo_f16 v122,v141,s99,0
v_mfma_f32_16x16x32_bf16 v[224:227],v[96:99],v[144:147],v[224:227]
ds_read_u16 v129,v129
v_lshlrev_b32_sdwa v132,1,v132 dst_sel:DWORD dst_unused:UNUSED_PAD src0_sel:DWORD src1_sel:WORD_1
v_lshlrev_b32_sdwa v134,1,v134 dst_sel:DWORD dst_unused:UNUSED_PAD src0_sel:DWORD src1_sel:WORD_1
v_mfma_f32_16x16x32_bf16 v[232:235],v[100:103],v[144:147],v[232:235]
s_waitcnt lgkmcnt(3)
v_lshl_add_u64 v[152:153],v[152:153],0,v[154:155]
v_cvt_pk_bf16_f32 v154,v156,v158
v_mfma_f32_16x16x32_bf16 v[240:243],v[104:107],v[144:147],v[240:243]
v_fma_mixhi_f16 v120,v142,s99,0
v_cvt_pk_bf16_f32 v155,v157,v159
v_mfma_f32_16x16x32_bf16 v[248:251],v[108:111],v[144:147],v[248:251]
ds_read_u16_d16_hi v131,v131
v_fma_mixhi_f16 v122,v143,s99,0
v_mfma_f32_16x16x32_bf16 v[212:215],v[88:91],v[152:155],v[212:215]
v_lshlrev_b32_sdwa v133,1,v133 dst_sel:DWORD dst_unused:UNUSED_PAD src0_sel:DWORD src1_sel:WORD_1
v_pk_fma_f16 v121,v120,v120,s54
v_lshlrev_b32_sdwa v135,1,v135 dst_sel:DWORD dst_unused:UNUSED_PAD src0_sel:DWORD src1_sel:WORD_1
v_mfma_f32_16x16x32_bf16 v[220:223],v[92:95],v[152:155],v[220:223]
s_setprio 2
ds_read_u16 v132,v132
ds_read_u16_d16_hi v134,v134
v_pk_fma_f16 v123,v122,v122,s54
v_mfma_f32_16x16x32_bf16 v[228:231],v[96:99],v[152:155],v[228:231]
ds_read_b64_tr_b16 v[88:89],v6 offset:6160
v_pk_mul_f16 v121,v120,v121
v_lshlrev_b32_sdwa v136,1,v136 dst_sel:DWORD dst_unused:UNUSED_PAD src0_sel:DWORD src1_sel:WORD_1
v_pk_mul_f16 v123,v122,v123
v_mfma_f32_16x16x32_bf16 v[236:239],v[100:103],v[152:155],v[236:239]
ds_read_b64_tr_b16 v[90:91],v6 offset:7696
v_lshlrev_b32_sdwa v138,1,v138 dst_sel:DWORD dst_unused:UNUSED_PAD src0_sel:DWORD src1_sel:WORD_1
v_mfma_f32_16x16x32_bf16 v[244:247],v[104:107],v[152:155],v[244:247]
ds_read_u16 v133,v133
v_exp_f16 v120,v121
v_mfma_f32_16x16x32_bf16 v[252:255],v[108:111],v[152:155],v[252:255]
ds_read_b64_tr_b16 v[92:93],v6 offset:6168
v_lshlrev_b32_sdwa v137,1,v137 dst_sel:DWORD dst_unused:UNUSED_PAD src0_sel:DWORD src1_sel:WORD_1
v_lshlrev_b32_sdwa v139,1,v139 dst_sel:DWORD dst_unused:UNUSED_PAD src0_sel:DWORD src1_sel:WORD_1
v_mfma_f32_16x16x32_bf16 v[144:147],v[64:67],v[40:43],v[112:115]
ds_read_b64_tr_b16 v[94:95],v6 offset:7704
v_exp_f16 v122,v123
s_nop 0
v_exp_f16_sdwa v122,v123 dst_sel:WORD_1 dst_unused:UNUSED_PRESERVE src0_sel:WORD_1
v_mfma_f32_16x16x32_bf16 v[152:155],v[64:67],v[44:47],v[112:115]
ds_read_u16_d16_hi v135,v135
ds_read_b64_tr_b16 v[96:97],v6 offset:6224
v_mfma_f32_16x16x32_bf16 v[156:159],v[68:71],v[44:47],v[116:119]
ds_read_u16 v136,v136
v_exp_f16_sdwa v120,v121 dst_sel:WORD_1 dst_unused:UNUSED_PRESERVE src0_sel:WORD_1
v_mfma_f32_16x16x32_bf16 v[148:151],v[68:71],v[40:43],v[116:119]
ds_read_b64_tr_b16 v[98:99],v6 offset:7760
v_pk_fma_f16 v120,v120,s55,s55
v_pk_fma_f16 v122,v122,s55,s55
v_mfma_f32_16x16x32_bf16 v[144:147],v[72:75],v[48:51],v[144:147]
v_rcp_f16 v121,v120
v_mfma_f32_16x16x32_bf16 v[152:155],v[72:75],v[52:55],v[152:155]
ds_read_u16_d16_hi v138,v138
v_rcp_f16 v123,v122
v_mfma_f32_16x16x32_bf16 v[156:159],v[76:79],v[52:55],v[156:159]
ds_read_b64_tr_b16 v[100:101],v6 offset:6232
v_rcp_f16_sdwa v121,v120 dst_sel:WORD_1 dst_unused:UNUSED_PRESERVE src0_sel:WORD_1
v_mfma_f32_16x16x32_bf16 v[148:151],v[76:79],v[48:51],v[148:151]
ds_read_b64_tr_b16 v[102:103],v6 offset:7768
ds_read_b64_tr_b16 v[104:105],v6 offset:6288
v_rcp_f16_sdwa v123,v122 dst_sel:WORD_1 dst_unused:UNUSED_PRESERVE src0_sel:WORD_1
v_mfma_f32_16x16x32_bf16 v[144:147],v[80:83],v[56:59],v[144:147]
ds_read_b64_tr_b16 v[106:107],v6 offset:7824
ds_read_b64_tr_b16 v[108:109],v6 offset:6296
v_fma_mix_f32 v142,v142,v121,0 op_sel:[0,1,0] op_sel_hi:[0,1,0]
v_mfma_f32_16x16x32_bf16 v[152:155],v[80:83],v[60:63],v[152:155]
s_waitcnt lgkmcnt(9)
ds_read_u16 v137,v137
v_fma_mix_f32 v140,v140,v121,0 op_sel_hi:[0,1,0]
v_lshl_add_u64 v[128:129],v[128:129],0,v[130:131]
v_lshl_add_u64 v[130:131],v[132:133],0,v[134:135]
v_mfma_f32_16x16x32_bf16 v[156:159],v[84:87],v[60:63],v[156:159]
ds_read_u16_d16_hi v139,v139
v_fma_mix_f32 v141,v141,v123,0 op_sel_hi:[0,1,0]
v_fma_mix_f32 v143,v143,v123,0 op_sel:[0,1,0] op_sel_hi:[0,1,0]
v_mfma_f32_16x16x32_bf16 v[148:151],v[84:87],v[56:59],v[148:151]
ds_read_b64_tr_b16 v[110:111],v6 offset:7832
ds_read_b64_tr_b16 v[64:65],v4 offset:512
v_lshlrev_b32_sdwa v144,1,v144 dst_sel:DWORD dst_unused:UNUSED_PAD src0_sel:DWORD src1_sel:WORD_1
v_lshlrev_b32_sdwa v146,1,v146 dst_sel:DWORD dst_unused:UNUSED_PAD src0_sel:DWORD src1_sel:WORD_1
v_mfma_f32_16x16x32_bf16 v[160:163],v[88:91],v[128:131],v[160:163]
ds_read_u16 v144,v144
v_lshlrev_b32_sdwa v145,1,v145 dst_sel:DWORD dst_unused:UNUSED_PAD src0_sel:DWORD src1_sel:WORD_1
v_lshlrev_b32_sdwa v147,1,v147 dst_sel:DWORD dst_unused:UNUSED_PAD src0_sel:DWORD src1_sel:WORD_1
v_fma_mixlo_f16 v122,v157,s99,0
v_mfma_f32_16x16x32_bf16 v[168:171],v[92:95],v[128:131],v[168:171]
s_waitcnt lgkmcnt(11)
ds_read_b64_tr_b16 v[66:67],v4 offset:2560
ds_read_b128 v[112:115],v7 offset:1408
v_fma_mixlo_f16 v120,v156,s99,0
v_mfma_f32_16x16x32_bf16 v[176:179],v[96:99],v[128:131],v[176:179]
s_waitcnt lgkmcnt(10)
ds_read_u16_d16_hi v146,v146
v_lshlrev_b32_sdwa v148,1,v148 dst_sel:DWORD dst_unused:UNUSED_PAD src0_sel:DWORD src1_sel:WORD_1
v_lshlrev_b32_sdwa v150,1,v150 dst_sel:DWORD dst_unused:UNUSED_PAD src0_sel:DWORD src1_sel:WORD_1
v_mfma_f32_16x16x32_bf16 v[184:187],v[100:103],v[128:131],v[184:187]
s_waitcnt lgkmcnt(6)
ds_read_b64_tr_b16 v[68:69],v5 offset:512
ds_read_b64_tr_b16 v[70:71],v5 offset:2560
v_lshl_add_u64 v[136:137],v[136:137],0,v[138:139]
v_fma_mixhi_f16 v120,v158,s99,0
v_mfma_f32_16x16x32_bf16 v[192:195],v[104:107],v[128:131],v[192:195]
s_waitcnt lgkmcnt(7)
ds_read_u16 v145,v145
v_cvt_pk_bf16_f32 v138,v140,v142
v_cvt_pk_bf16_f32 v139,v141,v143
v_mfma_f32_16x16x32_bf16 v[200:203],v[108:111],v[128:131],v[200:203]
v_fma_mixhi_f16 v122,v159,s99,0
v_mfma_f32_16x16x32_bf16 v[164:167],v[88:91],v[136:139],v[164:167]
ds_read_b128 v[116:119],v7 offset:1440
ds_read_b64_tr_b16 v[72:73],v4 offset:4608
v_lshlrev_b32_sdwa v149,1,v149 dst_sel:DWORD dst_unused:UNUSED_PAD src0_sel:DWORD src1_sel:WORD_1
v_lshlrev_b32_sdwa v151,1,v151 dst_sel:DWORD dst_unused:UNUSED_PAD src0_sel:DWORD src1_sel:WORD_1
v_mfma_f32_16x16x32_bf16 v[172:175],v[92:95],v[136:139],v[172:175]
ds_read_b64_tr_b16 v[74:75],v4 offset:6656
ds_read_b64_tr_b16 v[76:77],v5 offset:4608
v_pk_fma_f16 v121,v120,v120,s54
v_pk_fma_f16 v123,v122,v122,s54
v_mfma_f32_16x16x32_bf16 v[180:183],v[96:99],v[136:139],v[180:183]
s_setprio 1
ds_read_u16_d16_hi v147,v147
v_pk_mul_f16 v121,v120,v121
v_pk_mul_f16 v123,v122,v123
v_mfma_f32_16x16x32_bf16 v[188:191],v[100:103],v[136:139],v[188:191]
ds_read_u16 v148,v148
v_lshlrev_b32_sdwa v152,1,v152 dst_sel:DWORD dst_unused:UNUSED_PAD src0_sel:DWORD src1_sel:WORD_1
v_lshlrev_b32_sdwa v154,1,v154 dst_sel:DWORD dst_unused:UNUSED_PAD src0_sel:DWORD src1_sel:WORD_1
v_mfma_f32_16x16x32_bf16 v[196:199],v[104:107],v[136:139],v[196:199]
v_exp_f16 v120,v121
v_mfma_f32_16x16x32_bf16 v[204:207],v[108:111],v[136:139],v[204:207]
s_waitcnt lgkmcnt(10)
ds_read_b64_tr_b16 v[78:79],v5 offset:6656
ds_read_b64_tr_b16 v[80:81],v4 offset:8704
v_exp_f16 v122,v123
v_mfma_f32_16x16x32_bf16 v[128:131],v[64:67],v[16:19],v[112:115]
v_lshlrev_b32_sdwa v153,1,v153 dst_sel:DWORD dst_unused:UNUSED_PAD src0_sel:DWORD src1_sel:WORD_1
v_lshlrev_b32_sdwa v155,1,v155 dst_sel:DWORD dst_unused:UNUSED_PAD src0_sel:DWORD src1_sel:WORD_1
v_mfma_f32_16x16x32_bf16 v[136:139],v[64:67],v[20:23],v[112:115]
s_waitcnt lgkmcnt(7)
ds_read_u16_d16_hi v150,v150
ds_read_u16 v149,v149
v_exp_f16_sdwa v120,v121 dst_sel:WORD_1 dst_unused:UNUSED_PRESERVE src0_sel:WORD_1
v_mfma_f32_16x16x32_bf16 v[140:143],v[68:71],v[20:23],v[116:119]
v_exp_f16_sdwa v122,v123 dst_sel:WORD_1 dst_unused:UNUSED_PRESERVE src0_sel:WORD_1
v_mfma_f32_16x16x32_bf16 v[132:135],v[68:71],v[16:19],v[116:119]
s_waitcnt lgkmcnt(7)
ds_read_b64_tr_b16 v[82:83],v4 offset:10752
v_pk_fma_f16 v120,v120,s55,s55
v_mfma_f32_16x16x32_bf16 v[128:131],v[72:75],v[24:27],v[128:131]
ds_read_b64_tr_b16 v[84:85],v5 offset:8704
v_pk_fma_f16 v122,v122,s55,s55
v_rcp_f16 v121,v120
v_mfma_f32_16x16x32_bf16 v[136:139],v[72:75],v[28:31],v[136:139]
s_waitcnt lgkmcnt(5)
ds_read_b64_tr_b16 v[86:87],v5 offset:10752
ds_read_u16_d16_hi v151,v151
v_rcp_f16 v123,v122
v_mfma_f32_16x16x32_bf16 v[140:143],v[76:79],v[28:31],v[140:143]
v_rcp_f16_sdwa v121,v120 dst_sel:WORD_1 dst_unused:UNUSED_PRESERVE src0_sel:WORD_1
v_mfma_f32_16x16x32_bf16 v[132:135],v[76:79],v[24:27],v[132:135]
s_waitcnt lgkmcnt(3)
ds_read_u16 v152,v152
ds_read_u16_d16_hi v154,v154
v_rcp_f16_sdwa v123,v122 dst_sel:WORD_1 dst_unused:UNUSED_PRESERVE src0_sel:WORD_1
v_mfma_f32_16x16x32_bf16 v[128:131],v[80:83],v[32:35],v[128:131]
ds_read_u16 v153,v153
v_fma_mix_f32 v156,v156,v121,0 op_sel_hi:[0,1,0]
v_fma_mix_f32 v158,v158,v121,0 op_sel:[0,1,0] op_sel_hi:[0,1,0]
v_mfma_f32_16x16x32_bf16 v[136:139],v[80:83],v[36:39],v[136:139]
s_waitcnt lgkmcnt(3)
s_setprio 0
v_lshl_add_u64 v[144:145],v[144:145],0,v[146:147]
v_lshl_add_u64 v[146:147],v[148:149],0,v[150:151]
v_mfma_f32_16x16x32_bf16 v[140:143],v[84:87],v[36:39],v[140:143]
ds_read_u16_d16_hi v155,v155
v_fma_mix_f32 v157,v157,v123,0 op_sel_hi:[0,1,0]
v_fma_mix_f32 v159,v159,v123,0 op_sel:[0,1,0] op_sel_hi:[0,1,0]
v_mfma_f32_16x16x32_bf16 v[132:135],v[84:87],v[32:35],v[132:135]
s_nop 0
v_lshlrev_b32_sdwa v128,1,v128 dst_sel:DWORD dst_unused:UNUSED_PAD src0_sel:DWORD src1_sel:WORD_1
v_lshlrev_b32_sdwa v130,1,v130 dst_sel:DWORD dst_unused:UNUSED_PAD src0_sel:DWORD src1_sel:WORD_1
v_mfma_f32_16x16x32_bf16 v[208:211],v[88:91],v[144:147],v[208:211]
s_mov_b32 s86,0x1234567
s_barrier
s_setprio 3
v_lshlrev_b32_sdwa v129,1,v129 dst_sel:DWORD dst_unused:UNUSED_PAD src0_sel:DWORD src1_sel:WORD_1
v_lshlrev_b32_sdwa v131,1,v131 dst_sel:DWORD dst_unused:UNUSED_PAD src0_sel:DWORD src1_sel:WORD_1
v_mfma_f32_16x16x32_bf16 v[216:219],v[92:95],v[144:147],v[216:219]
ds_read_u16 v128,v128
ds_read_u16_d16_hi v130,v130
v_fma_mixlo_f16 v120,v140,s99,0
v_fma_mixlo_f16 v122,v141,s99,0
v_mfma_f32_16x16x32_bf16 v[224:227],v[96:99],v[144:147],v[224:227]
ds_read_u16 v129,v129
v_lshlrev_b32_sdwa v132,1,v132 dst_sel:DWORD dst_unused:UNUSED_PAD src0_sel:DWORD src1_sel:WORD_1
v_lshlrev_b32_sdwa v134,1,v134 dst_sel:DWORD dst_unused:UNUSED_PAD src0_sel:DWORD src1_sel:WORD_1
v_mfma_f32_16x16x32_bf16 v[232:235],v[100:103],v[144:147],v[232:235]
s_waitcnt lgkmcnt(3)
v_lshl_add_u64 v[152:153],v[152:153],0,v[154:155]
v_cvt_pk_bf16_f32 v154,v156,v158
v_mfma_f32_16x16x32_bf16 v[240:243],v[104:107],v[144:147],v[240:243]
v_fma_mixhi_f16 v120,v142,s99,0
v_cvt_pk_bf16_f32 v155,v157,v159
v_mfma_f32_16x16x32_bf16 v[248:251],v[108:111],v[144:147],v[248:251]
ds_read_u16_d16_hi v131,v131
v_fma_mixhi_f16 v122,v143,s99,0
v_mfma_f32_16x16x32_bf16 v[212:215],v[88:91],v[152:155],v[212:215]
v_lshlrev_b32_sdwa v133,1,v133 dst_sel:DWORD dst_unused:UNUSED_PAD src0_sel:DWORD src1_sel:WORD_1
v_pk_fma_f16 v121,v120,v120,s54
v_lshlrev_b32_sdwa v135,1,v135 dst_sel:DWORD dst_unused:UNUSED_PAD src0_sel:DWORD src1_sel:WORD_1
v_mfma_f32_16x16x32_bf16 v[220:223],v[92:95],v[152:155],v[220:223]
ds_read_u16 v132,v132
ds_read_u16_d16_hi v134,v134
v_pk_fma_f16 v123,v122,v122,s54
v_mfma_f32_16x16x32_bf16 v[228:231],v[96:99],v[152:155],v[228:231]
ds_read_b64_tr_b16 v[88:89],v6 offset:12320
v_pk_mul_f16 v121,v120,v121
v_lshlrev_b32_sdwa v136,1,v136 dst_sel:DWORD dst_unused:UNUSED_PAD src0_sel:DWORD src1_sel:WORD_1
v_pk_mul_f16 v123,v122,v123
v_mfma_f32_16x16x32_bf16 v[236:239],v[100:103],v[152:155],v[236:239]
ds_read_b64_tr_b16 v[90:91],v6 offset:13856
v_lshlrev_b32_sdwa v138,1,v138 dst_sel:DWORD dst_unused:UNUSED_PAD src0_sel:DWORD src1_sel:WORD_1
v_mfma_f32_16x16x32_bf16 v[244:247],v[104:107],v[152:155],v[244:247]
ds_read_u16 v133,v133
v_exp_f16 v120,v121
v_mfma_f32_16x16x32_bf16 v[252:255],v[108:111],v[152:155],v[252:255]
ds_read_b64_tr_b16 v[92:93],v6 offset:12328
ds_read_b64_tr_b16 v[94:95],v6 offset:13864
v_lshlrev_b32_sdwa v137,1,v137 dst_sel:DWORD dst_unused:UNUSED_PAD src0_sel:DWORD src1_sel:WORD_1
v_mfma_f32_16x16x32_bf16 v[144:147],v[64:67],v[40:43],v[112:115]
v_lshlrev_b32_sdwa v139,1,v139 dst_sel:DWORD dst_unused:UNUSED_PAD src0_sel:DWORD src1_sel:WORD_1
v_exp_f16 v122,v123
v_mfma_f32_16x16x32_bf16 v[152:155],v[64:67],v[44:47],v[112:115]
ds_read_u16_d16_hi v135,v135
ds_read_b64_tr_b16 v[96:97],v6 offset:12384
v_mfma_f32_16x16x32_bf16 v[156:159],v[68:71],v[44:47],v[116:119]
ds_read_u16 v136,v136
v_exp_f16_sdwa v122,v123 dst_sel:WORD_1 dst_unused:UNUSED_PRESERVE src0_sel:WORD_1
v_exp_f16_sdwa v120,v121 dst_sel:WORD_1 dst_unused:UNUSED_PRESERVE src0_sel:WORD_1
v_mfma_f32_16x16x32_bf16 v[148:151],v[68:71],v[40:43],v[116:119]
ds_read_b64_tr_b16 v[98:99],v6 offset:13920
v_pk_fma_f16 v120,v120,s55,s55
v_pk_fma_f16 v122,v122,s55,s55
v_mfma_f32_16x16x32_bf16 v[144:147],v[72:75],v[48:51],v[144:147]
v_rcp_f16 v121,v120
v_mfma_f32_16x16x32_bf16 v[152:155],v[72:75],v[52:55],v[152:155]
ds_read_b64_tr_b16 v[100:101],v6 offset:12392
v_rcp_f16 v123,v122
v_mfma_f32_16x16x32_bf16 v[156:159],v[76:79],v[52:55],v[156:159]
ds_read_b64_tr_b16 v[102:103],v6 offset:13928
ds_read_u16_d16_hi v138,v138
v_rcp_f16_sdwa v121,v120 dst_sel:WORD_1 dst_unused:UNUSED_PRESERVE src0_sel:WORD_1
v_mfma_f32_16x16x32_bf16 v[148:151],v[76:79],v[48:51],v[148:151]
ds_read_b64_tr_b16 v[104:105],v6 offset:12448
ds_read_b64_tr_b16 v[106:107],v6 offset:13984
v_rcp_f16_sdwa v123,v122 dst_sel:WORD_1 dst_unused:UNUSED_PRESERVE src0_sel:WORD_1
v_mfma_f32_16x16x32_bf16 v[144:147],v[80:83],v[56:59],v[144:147]
ds_read_u16 v137,v137
v_fma_mix_f32 v142,v142,v121,0 op_sel:[0,1,0] op_sel_hi:[0,1,0]
v_mfma_f32_16x16x32_bf16 v[152:155],v[80:83],v[60:63],v[152:155]
s_waitcnt lgkmcnt(9)
ds_read_b64_tr_b16 v[108:109],v6 offset:12456
v_fma_mix_f32 v140,v140,v121,0 op_sel_hi:[0,1,0]
v_lshl_add_u64 v[128:129],v[128:129],0,v[130:131]
v_lshl_add_u64 v[130:131],v[132:133],0,v[134:135]
v_mfma_f32_16x16x32_bf16 v[156:159],v[84:87],v[60:63],v[156:159]
ds_read_u16_d16_hi v139,v139
v_fma_mix_f32 v141,v141,v123,0 op_sel_hi:[0,1,0]
v_fma_mix_f32 v143,v143,v123,0 op_sel:[0,1,0] op_sel_hi:[0,1,0]
v_mfma_f32_16x16x32_bf16 v[148:151],v[84:87],v[56:59],v[148:151]
ds_read_b64_tr_b16 v[110:111],v6 offset:13992
ds_read_b64_tr_b16 v[64:65],v4 offset:12320
v_lshlrev_b32_sdwa v144,1,v144 dst_sel:DWORD dst_unused:UNUSED_PAD src0_sel:DWORD src1_sel:WORD_1
v_lshlrev_b32_sdwa v146,1,v146 dst_sel:DWORD dst_unused:UNUSED_PAD src0_sel:DWORD src1_sel:WORD_1
v_mfma_f32_16x16x32_bf16 v[160:163],v[88:91],v[128:131],v[160:163]
ds_read_u16 v144,v144
ds_read_b64_tr_b16 v[66:67],v4 offset:14368
v_lshlrev_b32_sdwa v145,1,v145 dst_sel:DWORD dst_unused:UNUSED_PAD src0_sel:DWORD src1_sel:WORD_1
v_lshlrev_b32_sdwa v147,1,v147 dst_sel:DWORD dst_unused:UNUSED_PAD src0_sel:DWORD src1_sel:WORD_1
v_fma_mixlo_f16 v122,v157,s99,0
v_mfma_f32_16x16x32_bf16 v[168:171],v[92:95],v[128:131],v[168:171]
s_waitcnt lgkmcnt(12)
ds_read_b128 v[112:115],v7 offset:0
ds_read_b64_tr_b16 v[68:69],v5 offset:12320
v_fma_mixlo_f16 v120,v156,s99,0
v_mfma_f32_16x16x32_bf16 v[176:179],v[96:99],v[128:131],v[176:179]
s_waitcnt lgkmcnt(12)
ds_read_u16_d16_hi v146,v146
v_lshlrev_b32_sdwa v148,1,v148 dst_sel:DWORD dst_unused:UNUSED_PAD src0_sel:DWORD src1_sel:WORD_1
v_lshlrev_b32_sdwa v150,1,v150 dst_sel:DWORD dst_unused:UNUSED_PAD src0_sel:DWORD src1_sel:WORD_1
v_mfma_f32_16x16x32_bf16 v[184:187],v[100:103],v[128:131],v[184:187]
s_waitcnt lgkmcnt(7)
ds_read_b64_tr_b16 v[70:71],v5 offset:14368
v_lshl_add_u64 v[136:137],v[136:137],0,v[138:139]
v_fma_mixhi_f16 v120,v158,s99,0
v_mfma_f32_16x16x32_bf16 v[192:195],v[104:107],v[128:131],v[192:195]
s_waitcnt lgkmcnt(7)
ds_read_u16 v145,v145
v_cvt_pk_bf16_f32 v138,v140,v142
v_cvt_pk_bf16_f32 v139,v141,v143
v_mfma_f32_16x16x32_bf16 v[200:203],v[108:111],v[128:131],v[200:203]
v_fma_mixhi_f16 v122,v159,s99,0
v_mfma_f32_16x16x32_bf16 v[164:167],v[88:91],v[136:139],v[164:167]
ds_read_b128 v[116:119],v7 offset:32
ds_read_b64_tr_b16 v[72:73],v4 offset:16416
v_lshlrev_b32_sdwa v149,1,v149 dst_sel:DWORD dst_unused:UNUSED_PAD src0_sel:DWORD src1_sel:WORD_1
v_lshlrev_b32_sdwa v151,1,v151 dst_sel:DWORD dst_unused:UNUSED_PAD src0_sel:DWORD src1_sel:WORD_1
v_mfma_f32_16x16x32_bf16 v[172:175],v[92:95],v[136:139],v[172:175]
ds_read_b64_tr_b16 v[74:75],v4 offset:18464
ds_read_b64_tr_b16 v[76:77],v5 offset:16416
v_pk_fma_f16 v121,v120,v120,s54
v_pk_fma_f16 v123,v122,v122,s54
v_mfma_f32_16x16x32_bf16 v[180:183],v[96:99],v[136:139],v[180:183]
ds_read_u16_d16_hi v147,v147
v_pk_mul_f16 v121,v120,v121
v_pk_mul_f16 v123,v122,v123
v_mfma_f32_16x16x32_bf16 v[188:191],v[100:103],v[136:139],v[188:191]
ds_read_u16 v148,v148
v_lshlrev_b32_sdwa v152,1,v152 dst_sel:DWORD dst_unused:UNUSED_PAD src0_sel:DWORD src1_sel:WORD_1
v_lshlrev_b32_sdwa v154,1,v154 dst_sel:DWORD dst_unused:UNUSED_PAD src0_sel:DWORD src1_sel:WORD_1
v_mfma_f32_16x16x32_bf16 v[196:199],v[104:107],v[136:139],v[196:199]
v_exp_f16 v120,v121
v_mfma_f32_16x16x32_bf16 v[204:207],v[108:111],v[136:139],v[204:207]
s_waitcnt lgkmcnt(10)
ds_read_b64_tr_b16 v[78:79],v5 offset:18464
ds_read_b64_tr_b16 v[80:81],v4 offset:20512
v_exp_f16 v122,v123
v_mfma_f32_16x16x32_bf16 v[128:131],v[64:67],v[16:19],v[112:115]
v_lshlrev_b32_sdwa v153,1,v153 dst_sel:DWORD dst_unused:UNUSED_PAD src0_sel:DWORD src1_sel:WORD_1
v_lshlrev_b32_sdwa v155,1,v155 dst_sel:DWORD dst_unused:UNUSED_PAD src0_sel:DWORD src1_sel:WORD_1
v_mfma_f32_16x16x32_bf16 v[136:139],v[64:67],v[20:23],v[112:115]
s_waitcnt lgkmcnt(7)
ds_read_u16_d16_hi v150,v150
ds_read_u16 v149,v149
v_exp_f16_sdwa v120,v121 dst_sel:WORD_1 dst_unused:UNUSED_PRESERVE src0_sel:WORD_1
v_mfma_f32_16x16x32_bf16 v[140:143],v[68:71],v[20:23],v[116:119]
v_exp_f16_sdwa v122,v123 dst_sel:WORD_1 dst_unused:UNUSED_PRESERVE src0_sel:WORD_1
v_mfma_f32_16x16x32_bf16 v[132:135],v[68:71],v[16:19],v[116:119]
s_waitcnt lgkmcnt(7)
ds_read_b64_tr_b16 v[82:83],v4 offset:22560
v_pk_fma_f16 v120,v120,s55,s55
v_mfma_f32_16x16x32_bf16 v[128:131],v[72:75],v[24:27],v[128:131]
ds_read_b64_tr_b16 v[84:85],v5 offset:20512
v_pk_fma_f16 v122,v122,s55,s55
v_rcp_f16 v121,v120
v_mfma_f32_16x16x32_bf16 v[136:139],v[72:75],v[28:31],v[136:139]
s_waitcnt lgkmcnt(5)
ds_read_b64_tr_b16 v[86:87],v5 offset:22560
ds_read_u16_d16_hi v151,v151
v_rcp_f16 v123,v122
v_mfma_f32_16x16x32_bf16 v[140:143],v[76:79],v[28:31],v[140:143]
v_rcp_f16_sdwa v121,v120 dst_sel:WORD_1 dst_unused:UNUSED_PRESERVE src0_sel:WORD_1
v_mfma_f32_16x16x32_bf16 v[132:135],v[76:79],v[24:27],v[132:135]
s_waitcnt lgkmcnt(3)
ds_read_u16 v152,v152
ds_read_u16_d16_hi v154,v154
v_rcp_f16_sdwa v123,v122 dst_sel:WORD_1 dst_unused:UNUSED_PRESERVE src0_sel:WORD_1
v_mfma_f32_16x16x32_bf16 v[128:131],v[80:83],v[32:35],v[128:131]
ds_read_u16 v153,v153
v_fma_mix_f32 v156,v156,v121,0 op_sel_hi:[0,1,0]
v_fma_mix_f32 v158,v158,v121,0 op_sel:[0,1,0] op_sel_hi:[0,1,0]
v_mfma_f32_16x16x32_bf16 v[136:139],v[80:83],v[36:39],v[136:139]
s_waitcnt lgkmcnt(3)
v_lshl_add_u64 v[144:145],v[144:145],0,v[146:147]
v_lshl_add_u64 v[146:147],v[148:149],0,v[150:151]
v_mfma_f32_16x16x32_bf16 v[140:143],v[84:87],v[36:39],v[140:143]
ds_read_u16_d16_hi v155,v155
v_fma_mix_f32 v157,v157,v123,0 op_sel_hi:[0,1,0]
v_fma_mix_f32 v159,v159,v123,0 op_sel:[0,1,0] op_sel_hi:[0,1,0]
v_mfma_f32_16x16x32_bf16 v[132:135],v[84:87],v[32:35],v[132:135]
v_lshlrev_b32_sdwa v128,1,v128 dst_sel:DWORD dst_unused:UNUSED_PAD src0_sel:DWORD src1_sel:WORD_1
v_lshlrev_b32_sdwa v130,1,v130 dst_sel:DWORD dst_unused:UNUSED_PAD src0_sel:DWORD src1_sel:WORD_1
v_mfma_f32_16x16x32_bf16 v[208:211],v[88:91],v[144:147],v[208:211]
v_lshlrev_b32_sdwa v129,1,v129 dst_sel:DWORD dst_unused:UNUSED_PAD src0_sel:DWORD src1_sel:WORD_1
v_lshlrev_b32_sdwa v131,1,v131 dst_sel:DWORD dst_unused:UNUSED_PAD src0_sel:DWORD src1_sel:WORD_1
v_mfma_f32_16x16x32_bf16 v[216:219],v[92:95],v[144:147],v[216:219]
ds_read_u16 v128,v128
ds_read_u16_d16_hi v130,v130
v_fma_mixlo_f16 v120,v140,s99,0
v_fma_mixlo_f16 v122,v141,s99,0
v_mfma_f32_16x16x32_bf16 v[224:227],v[96:99],v[144:147],v[224:227]
ds_read_u16 v129,v129
v_lshlrev_b32_sdwa v132,1,v132 dst_sel:DWORD dst_unused:UNUSED_PAD src0_sel:DWORD src1_sel:WORD_1
v_lshlrev_b32_sdwa v134,1,v134 dst_sel:DWORD dst_unused:UNUSED_PAD src0_sel:DWORD src1_sel:WORD_1
v_mfma_f32_16x16x32_bf16 v[232:235],v[100:103],v[144:147],v[232:235]
s_waitcnt lgkmcnt(3)
v_lshl_add_u64 v[152:153],v[152:153],0,v[154:155]
v_cvt_pk_bf16_f32 v154,v156,v158
v_mfma_f32_16x16x32_bf16 v[240:243],v[104:107],v[144:147],v[240:243]
v_fma_mixhi_f16 v120,v142,s99,0
v_cvt_pk_bf16_f32 v155,v157,v159
v_mfma_f32_16x16x32_bf16 v[248:251],v[108:111],v[144:147],v[248:251]
s_setprio 2
ds_read_u16_d16_hi v131,v131
v_fma_mixhi_f16 v122,v143,s99,0
v_mfma_f32_16x16x32_bf16 v[212:215],v[88:91],v[152:155],v[212:215]
v_lshlrev_b32_sdwa v133,1,v133 dst_sel:DWORD dst_unused:UNUSED_PAD src0_sel:DWORD src1_sel:WORD_1
v_pk_fma_f16 v121,v120,v120,s54
v_lshlrev_b32_sdwa v135,1,v135 dst_sel:DWORD dst_unused:UNUSED_PAD src0_sel:DWORD src1_sel:WORD_1
v_mfma_f32_16x16x32_bf16 v[220:223],v[92:95],v[152:155],v[220:223]
ds_read_u16 v132,v132
ds_read_u16_d16_hi v134,v134
v_pk_fma_f16 v123,v122,v122,s54
v_mfma_f32_16x16x32_bf16 v[228:231],v[96:99],v[152:155],v[228:231]
ds_read_b64_tr_b16 v[88:89],v6 offset:18480
v_pk_mul_f16 v121,v120,v121
v_lshlrev_b32_sdwa v136,1,v136 dst_sel:DWORD dst_unused:UNUSED_PAD src0_sel:DWORD src1_sel:WORD_1
v_pk_mul_f16 v123,v122,v123
v_mfma_f32_16x16x32_bf16 v[236:239],v[100:103],v[152:155],v[236:239]
ds_read_b64_tr_b16 v[90:91],v6 offset:20016
v_lshlrev_b32_sdwa v138,1,v138 dst_sel:DWORD dst_unused:UNUSED_PAD src0_sel:DWORD src1_sel:WORD_1
v_mfma_f32_16x16x32_bf16 v[244:247],v[104:107],v[152:155],v[244:247]
ds_read_u16 v133,v133
v_exp_f16 v120,v121
v_mfma_f32_16x16x32_bf16 v[252:255],v[108:111],v[152:155],v[252:255]
ds_read_b64_tr_b16 v[92:93],v6 offset:18488
ds_read_b64_tr_b16 v[94:95],v6 offset:20024
v_lshlrev_b32_sdwa v137,1,v137 dst_sel:DWORD dst_unused:UNUSED_PAD src0_sel:DWORD src1_sel:WORD_1
v_mfma_f32_16x16x32_bf16 v[144:147],v[64:67],v[40:43],v[112:115]
v_lshlrev_b32_sdwa v139,1,v139 dst_sel:DWORD dst_unused:UNUSED_PAD src0_sel:DWORD src1_sel:WORD_1
v_exp_f16 v122,v123
v_mfma_f32_16x16x32_bf16 v[152:155],v[64:67],v[44:47],v[112:115]
ds_read_u16_d16_hi v135,v135
ds_read_b64_tr_b16 v[96:97],v6 offset:18544
v_mfma_f32_16x16x32_bf16 v[156:159],v[68:71],v[44:47],v[116:119]
ds_read_u16 v136,v136
v_exp_f16_sdwa v122,v123 dst_sel:WORD_1 dst_unused:UNUSED_PRESERVE src0_sel:WORD_1
v_exp_f16_sdwa v120,v121 dst_sel:WORD_1 dst_unused:UNUSED_PRESERVE src0_sel:WORD_1
v_mfma_f32_16x16x32_bf16 v[148:151],v[68:71],v[40:43],v[116:119]
ds_read_b64_tr_b16 v[98:99],v6 offset:20080
v_pk_fma_f16 v120,v120,s55,s55
v_pk_fma_f16 v122,v122,s55,s55
v_mfma_f32_16x16x32_bf16 v[144:147],v[72:75],v[48:51],v[144:147]
v_rcp_f16 v121,v120
v_mfma_f32_16x16x32_bf16 v[152:155],v[72:75],v[52:55],v[152:155]
ds_read_b64_tr_b16 v[100:101],v6 offset:18552
v_rcp_f16 v123,v122
v_mfma_f32_16x16x32_bf16 v[156:159],v[76:79],v[52:55],v[156:159]
ds_read_b64_tr_b16 v[102:103],v6 offset:20088
ds_read_u16_d16_hi v138,v138
v_rcp_f16_sdwa v121,v120 dst_sel:WORD_1 dst_unused:UNUSED_PRESERVE src0_sel:WORD_1
v_mfma_f32_16x16x32_bf16 v[148:151],v[76:79],v[48:51],v[148:151]
ds_read_b64_tr_b16 v[104:105],v6 offset:18608
ds_read_b64_tr_b16 v[106:107],v6 offset:20144
v_rcp_f16_sdwa v123,v122 dst_sel:WORD_1 dst_unused:UNUSED_PRESERVE src0_sel:WORD_1
v_mfma_f32_16x16x32_bf16 v[144:147],v[80:83],v[56:59],v[144:147]
ds_read_u16 v137,v137
v_fma_mix_f32 v142,v142,v121,0 op_sel:[0,1,0] op_sel_hi:[0,1,0]
v_mfma_f32_16x16x32_bf16 v[152:155],v[80:83],v[60:63],v[152:155]
s_waitcnt lgkmcnt(9)
ds_read_b64_tr_b16 v[108:109],v6 offset:18616
v_fma_mix_f32 v140,v140,v121,0 op_sel_hi:[0,1,0]
v_lshl_add_u64 v[128:129],v[128:129],0,v[130:131]
v_lshl_add_u64 v[130:131],v[132:133],0,v[134:135]
v_mfma_f32_16x16x32_bf16 v[156:159],v[84:87],v[60:63],v[156:159]
ds_read_u16_d16_hi v139,v139
v_fma_mix_f32 v141,v141,v123,0 op_sel_hi:[0,1,0]
v_fma_mix_f32 v143,v143,v123,0 op_sel:[0,1,0] op_sel_hi:[0,1,0]
v_mfma_f32_16x16x32_bf16 v[148:151],v[84:87],v[56:59],v[148:151]
ds_read_b64_tr_b16 v[110:111],v6 offset:20152
ds_read_b64_tr_b16 v[64:65],v4 offset:12832
v_lshlrev_b32_sdwa v144,1,v144 dst_sel:DWORD dst_unused:UNUSED_PAD src0_sel:DWORD src1_sel:WORD_1
v_lshlrev_b32_sdwa v146,1,v146 dst_sel:DWORD dst_unused:UNUSED_PAD src0_sel:DWORD src1_sel:WORD_1
v_mfma_f32_16x16x32_bf16 v[160:163],v[88:91],v[128:131],v[160:163]
ds_read_u16 v144,v144
ds_read_b64_tr_b16 v[66:67],v4 offset:14880
v_lshlrev_b32_sdwa v145,1,v145 dst_sel:DWORD dst_unused:UNUSED_PAD src0_sel:DWORD src1_sel:WORD_1
v_lshlrev_b32_sdwa v147,1,v147 dst_sel:DWORD dst_unused:UNUSED_PAD src0_sel:DWORD src1_sel:WORD_1
v_fma_mixlo_f16 v122,v157,s99,0
v_mfma_f32_16x16x32_bf16 v[168:171],v[92:95],v[128:131],v[168:171]
s_waitcnt lgkmcnt(12)
ds_read_b128 v[112:115],v7 offset:128
s_nop 0
s_mov_b32 s86,0xbe801d5a
ds_read_b64_tr_b16 v[68:69],v5 offset:12832
v_fma_mixlo_f16 v120,v156,s99,0
v_mfma_f32_16x16x32_bf16 v[176:179],v[96:99],v[128:131],v[176:179]
s_waitcnt lgkmcnt(12)
ds_read_u16_d16_hi v146,v146
v_lshlrev_b32_sdwa v148,1,v148 dst_sel:DWORD dst_unused:UNUSED_PAD src0_sel:DWORD src1_sel:WORD_1
v_lshlrev_b32_sdwa v150,1,v150 dst_sel:DWORD dst_unused:UNUSED_PAD src0_sel:DWORD src1_sel:WORD_1
v_mfma_f32_16x16x32_bf16 v[184:187],v[100:103],v[128:131],v[184:187]
s_waitcnt lgkmcnt(7)
ds_read_b64_tr_b16 v[70:71],v5 offset:14880
v_lshl_add_u64 v[136:137],v[136:137],0,v[138:139]
v_fma_mixhi_f16 v120,v158,s99,0
v_mfma_f32_16x16x32_bf16 v[192:195],v[104:107],v[128:131],v[192:195]
s_waitcnt lgkmcnt(7)
ds_read_u16 v145,v145
v_cvt_pk_bf16_f32 v138,v140,v142
v_cvt_pk_bf16_f32 v139,v141,v143
v_mfma_f32_16x16x32_bf16 v[200:203],v[108:111],v[128:131],v[200:203]
v_fma_mixhi_f16 v122,v159,s99,0
v_mfma_f32_16x16x32_bf16 v[164:167],v[88:91],v[136:139],v[164:167]
ds_read_b128 v[116:119],v7 offset:160
ds_read_b64_tr_b16 v[72:73],v4 offset:16928
v_lshlrev_b32_sdwa v149,1,v149 dst_sel:DWORD dst_unused:UNUSED_PAD src0_sel:DWORD src1_sel:WORD_1
v_lshlrev_b32_sdwa v151,1,v151 dst_sel:DWORD dst_unused:UNUSED_PAD src0_sel:DWORD src1_sel:WORD_1
v_mfma_f32_16x16x32_bf16 v[172:175],v[92:95],v[136:139],v[172:175]
s_setprio 1
ds_read_b64_tr_b16 v[74:75],v4 offset:18976
ds_read_b64_tr_b16 v[76:77],v5 offset:16928
v_pk_fma_f16 v121,v120,v120,s54
v_pk_fma_f16 v123,v122,v122,s54
v_mfma_f32_16x16x32_bf16 v[180:183],v[96:99],v[136:139],v[180:183]
ds_read_u16_d16_hi v147,v147
v_pk_mul_f16 v121,v120,v121
v_pk_mul_f16 v123,v122,v123
v_mfma_f32_16x16x32_bf16 v[188:191],v[100:103],v[136:139],v[188:191]
ds_read_u16 v148,v148
v_lshlrev_b32_sdwa v152,1,v152 dst_sel:DWORD dst_unused:UNUSED_PAD src0_sel:DWORD src1_sel:WORD_1
v_lshlrev_b32_sdwa v154,1,v154 dst_sel:DWORD dst_unused:UNUSED_PAD src0_sel:DWORD src1_sel:WORD_1
v_mfma_f32_16x16x32_bf16 v[196:199],v[104:107],v[136:139],v[196:199]
v_exp_f16 v120,v121
v_mfma_f32_16x16x32_bf16 v[204:207],v[108:111],v[136:139],v[204:207]
s_waitcnt lgkmcnt(10)
ds_read_b64_tr_b16 v[78:79],v5 offset:18976
ds_read_b64_tr_b16 v[80:81],v4 offset:21024
v_exp_f16 v122,v123
v_mfma_f32_16x16x32_bf16 v[128:131],v[64:67],v[16:19],v[112:115]
v_lshlrev_b32_sdwa v153,1,v153 dst_sel:DWORD dst_unused:UNUSED_PAD src0_sel:DWORD src1_sel:WORD_1
v_lshlrev_b32_sdwa v155,1,v155 dst_sel:DWORD dst_unused:UNUSED_PAD src0_sel:DWORD src1_sel:WORD_1
v_mfma_f32_16x16x32_bf16 v[136:139],v[64:67],v[20:23],v[112:115]
s_waitcnt lgkmcnt(7)
ds_read_u16_d16_hi v150,v150
ds_read_u16 v149,v149
v_exp_f16_sdwa v120,v121 dst_sel:WORD_1 dst_unused:UNUSED_PRESERVE src0_sel:WORD_1
v_mfma_f32_16x16x32_bf16 v[140:143],v[68:71],v[20:23],v[116:119]
v_exp_f16_sdwa v122,v123 dst_sel:WORD_1 dst_unused:UNUSED_PRESERVE src0_sel:WORD_1
v_mfma_f32_16x16x32_bf16 v[132:135],v[68:71],v[16:19],v[116:119]
s_waitcnt lgkmcnt(7)
ds_read_b64_tr_b16 v[82:83],v4 offset:23072
v_pk_fma_f16 v120,v120,s55,s55
v_mfma_f32_16x16x32_bf16 v[128:131],v[72:75],v[24:27],v[128:131]
ds_read_b64_tr_b16 v[84:85],v5 offset:21024
v_pk_fma_f16 v122,v122,s55,s55
v_rcp_f16 v121,v120
v_mfma_f32_16x16x32_bf16 v[136:139],v[72:75],v[28:31],v[136:139]
s_waitcnt lgkmcnt(5)
ds_read_b64_tr_b16 v[86:87],v5 offset:23072
ds_read_u16_d16_hi v151,v151
v_rcp_f16 v123,v122
v_mfma_f32_16x16x32_bf16 v[140:143],v[76:79],v[28:31],v[140:143]
s_setprio 0
v_rcp_f16_sdwa v121,v120 dst_sel:WORD_1 dst_unused:UNUSED_PRESERVE src0_sel:WORD_1
v_mfma_f32_16x16x32_bf16 v[132:135],v[76:79],v[24:27],v[132:135]
s_waitcnt lgkmcnt(3)
ds_read_u16 v152,v152
ds_read_u16_d16_hi v154,v154
v_rcp_f16_sdwa v123,v122 dst_sel:WORD_1 dst_unused:UNUSED_PRESERVE src0_sel:WORD_1
v_mfma_f32_16x16x32_bf16 v[128:131],v[80:83],v[32:35],v[128:131]
s_nop 0
ds_read_u16 v153,v153
v_fma_mix_f32 v156,v156,v121,0 op_sel_hi:[0,1,0]
v_fma_mix_f32 v158,v158,v121,0 op_sel:[0,1,0] op_sel_hi:[0,1,0]
v_mfma_f32_16x16x32_bf16 v[136:139],v[80:83],v[36:39],v[136:139]
s_waitcnt lgkmcnt(3)
v_lshl_add_u64 v[144:145],v[144:145],0,v[146:147]
v_lshl_add_u64 v[146:147],v[148:149],0,v[150:151]
v_mfma_f32_16x16x32_bf16 v[140:143],v[84:87],v[36:39],v[140:143]
ds_read_u16_d16_hi v155,v155
v_fma_mix_f32 v157,v157,v123,0 op_sel_hi:[0,1,0]
v_fma_mix_f32 v159,v159,v123,0 op_sel:[0,1,0] op_sel_hi:[0,1,0]
v_mfma_f32_16x16x32_bf16 v[132:135],v[84:87],v[32:35],v[132:135]
s_nop 0
v_lshlrev_b32_sdwa v128,1,v128 dst_sel:DWORD dst_unused:UNUSED_PAD src0_sel:DWORD src1_sel:WORD_1
v_lshlrev_b32_sdwa v130,1,v130 dst_sel:DWORD dst_unused:UNUSED_PAD src0_sel:DWORD src1_sel:WORD_1
v_mfma_f32_16x16x32_bf16 v[208:211],v[88:91],v[144:147],v[208:211]
s_mov_b32 s86,0x1234567
s_barrier
s_setprio 2
v_lshlrev_b32_sdwa v129,1,v129 dst_sel:DWORD dst_unused:UNUSED_PAD src0_sel:DWORD src1_sel:WORD_1
v_mfma_f32_16x16x32_bf16 v[216:219],v[92:95],v[144:147],v[216:219]
ds_read_u16 v128,v128
v_lshlrev_b32_sdwa v131,1,v131 dst_sel:DWORD dst_unused:UNUSED_PAD src0_sel:DWORD src1_sel:WORD_1
v_mfma_f32_16x16x32_bf16 v[224:227],v[96:99],v[144:147],v[224:227]
s_cbranch_vccz RB35_%=
s_add_u32 m0,s58,12320
s_mul_i32 s69,s59,3
s_add_u32 s69,s69,s97
buffer_load_dwordx4 v2,s[44:47],s69 offen lds
s_add_u32 m0,m0,1024
s_add_u32 s69,s69,s53
buffer_load_dwordx4 v3,s[44:47],s69 offen lds
s_add_u32 m0,m0,1024
s_add_u32 s69,s69,s53
buffer_load_dwordx4 v2,s[44:47],s69 offen lds
s_add_u32 m0,m0,s50
s_add_u32 s69,s69,s53
buffer_load_dwordx4 v3,s[44:47],s69 offen lds
RB35_%=:
v_lshlrev_b32_sdwa v132,1,v132 dst_sel:DWORD dst_unused:UNUSED_PAD src0_sel:DWORD src1_sel:WORD_1
v_mfma_f32_16x16x32_bf16 v[232:235],v[100:103],v[144:147],v[232:235]
s_waitcnt lgkmcnt(1)
ds_read_u16_d16_hi v130,v130
ds_read_u16 v129,v129
v_lshl_add_u64 v[152:153],v[152:153],0,v[154:155]
v_lshlrev_b32_sdwa v134,1,v134 dst_sel:DWORD dst_unused:UNUSED_PAD src0_sel:DWORD src1_sel:WORD_1
v_mfma_f32_16x16x32_bf16 v[240:243],v[104:107],v[144:147],v[240:243]
s_cbranch_vccz RB36_%=
s_add_u32 m0,m0,1024
s_add_u32 s69,s69,s53
buffer_load_dwordx4 v2,s[44:47],s69 offen lds
s_add_u32 m0,m0,1024
s_add_u32 s69,s69,s53
buffer_load_dwordx4 v3,s[44:47],s69 offen lds
s_add_u32 m0,m0,s50
s_add_u32 s69,s69,s53
buffer_load_dwordx4 v2,s[44:47],s69 offen lds
s_add_u32 m0,m0,1024
s_add_u32 s69,s69,s53
buffer_load_dwordx4 v3,s[44:47],s69 offen lds
RB36_%=:
v_lshlrev_b32_sdwa v133,1,v133 dst_sel:DWORD dst_unused:UNUSED_PAD src0_sel:DWORD src1_sel:WORD_1
v_cvt_pk_bf16_f32 v155,v157,v159
v_cvt_pk_bf16_f32 v154,v156,v158
v_mfma_f32_16x16x32_bf16 v[248:251],v[108:111],v[144:147],v[248:251]
s_cbranch_vccz RB37_%=
s_add_u32 m0,m0,1024
s_add_u32 s69,s69,s53
buffer_load_dwordx4 v2,s[44:47],s69 offen lds
s_add_u32 m0,m0,s50
s_add_u32 s69,s69,s53
buffer_load_dwordx4 v3,s[44:47],s69 offen lds
s_add_u32 m0,m0,1024
s_add_u32 s69,s69,s53
buffer_load_dwordx4 v2,s[44:47],s69 offen lds
s_add_u32 m0,m0,1024
s_add_u32 s69,s69,s53
buffer_load_dwordx4 v3,s[44:47],s69 offen lds
RB37_%=:
ds_read_u16_d16_hi v131,v131
v_mfma_f32_16x16x32_bf16 v[212:215],v[88:91],v[152:155],v[212:215]
ds_read_u16 v132,v132
v_mfma_f32_16x16x32_bf16 v[220:223],v[92:95],v[152:155],v[220:223]
ds_read_b64_tr_b16 v[88:89],v6 offset:0
ds_read_b64_tr_b16 v[90:91],v6 offset:1536
v_lshlrev_b32_sdwa v135,1,v135 dst_sel:DWORD dst_unused:UNUSED_PAD src0_sel:DWORD src1_sel:WORD_1
v_lshlrev_b32_sdwa v136,1,v136 dst_sel:DWORD dst_unused:UNUSED_PAD src0_sel:DWORD src1_sel:WORD_1
v_mfma_f32_16x16x32_bf16 v[228:231],v[96:99],v[152:155],v[228:231]
ds_read_u16_d16_hi v134,v134
v_lshlrev_b32_sdwa v138,1,v138 dst_sel:DWORD dst_unused:UNUSED_PAD src0_sel:DWORD src1_sel:WORD_1
v_mfma_f32_16x16x32_bf16 v[236:239],v[100:103],v[152:155],v[236:239]
ds_read_u16 v133,v133
v_lshlrev_b32_sdwa v137,1,v137 dst_sel:DWORD dst_unused:UNUSED_PAD src0_sel:DWORD src1_sel:WORD_1
v_mfma_f32_16x16x32_bf16 v[244:247],v[104:107],v[152:155],v[244:247]
ds_read_u16_d16_hi v135,v135
v_lshlrev_b32_sdwa v139,1,v139 dst_sel:DWORD dst_unused:UNUSED_PAD src0_sel:DWORD src1_sel:WORD_1
v_mfma_f32_16x16x32_bf16 v[252:255],v[108:111],v[152:155],v[252:255]
ds_read_b64_tr_b16 v[92:93],v6 offset:8
ds_read_b64_tr_b16 v[94:95],v6 offset:1544
v_mfma_f32_16x16x32_bf16 v[144:147],v[64:67],v[40:43],v[112:115]
ds_read_u16 v136,v136
v_lshlrev_b32_sdwa v140,1,v140 dst_sel:DWORD dst_unused:UNUSED_PAD src0_sel:DWORD src1_sel:WORD_1
v_lshlrev_b32_sdwa v142,1,v142 dst_sel:DWORD dst_unused:UNUSED_PAD src0_sel:DWORD src1_sel:WORD_1
v_mfma_f32_16x16x32_bf16 v[152:155],v[64:67],v[44:47],v[112:115]
ds_read_u16_d16_hi v138,v138
ds_read_b64_tr_b16 v[96:97],v6 offset:64
v_lshlrev_b32_sdwa v141,1,v141 dst_sel:DWORD dst_unused:UNUSED_PAD src0_sel:DWORD src1_sel:WORD_1
v_mfma_f32_16x16x32_bf16 v[156:159],v[68:71],v[44:47],v[116:119]
ds_read_b64_tr_b16 v[98:99],v6 offset:1600
ds_read_b64_tr_b16 v[100:101],v6 offset:72
v_mfma_f32_16x16x32_bf16 v[148:151],v[68:71],v[40:43],v[116:119]
ds_read_u16 v137,v137
v_mfma_f32_16x16x32_bf16 v[144:147],v[72:75],v[48:51],v[144:147]
ds_read_u16_d16_hi v139,v139
v_mfma_f32_16x16x32_bf16 v[152:155],v[72:75],v[52:55],v[152:155]
ds_read_b64_tr_b16 v[102:103],v6 offset:1608
ds_read_b64_tr_b16 v[104:105],v6 offset:128
v_mfma_f32_16x16x32_bf16 v[156:159],v[76:79],v[52:55],v[156:159]
ds_read_u16 v140,v140
v_mfma_f32_16x16x32_bf16 v[148:151],v[76:79],v[48:51],v[148:151]
ds_read_u16_d16_hi v142,v142
v_lshlrev_b32_sdwa v143,1,v143 dst_sel:DWORD dst_unused:UNUSED_PAD src0_sel:DWORD src1_sel:WORD_1
v_mfma_f32_16x16x32_bf16 v[144:147],v[80:83],v[56:59],v[144:147]
s_waitcnt lgkmcnt(15)
ds_read_b64_tr_b16 v[106:107],v6 offset:1664
ds_read_b64_tr_b16 v[108:109],v6 offset:136
v_lshl_add_u64 v[128:129],v[128:129],0,v[130:131]
v_mfma_f32_16x16x32_bf16 v[152:155],v[80:83],v[60:63],v[152:155]
s_waitcnt lgkmcnt(15)
ds_read_u16 v141,v141
ds_read_u16_d16_hi v143,v143
v_lshl_add_u64 v[130:131],v[132:133],0,v[134:135]
v_mfma_f32_16x16x32_bf16 v[156:159],v[84:87],v[60:63],v[156:159]
ds_read_b64_tr_b16 v[110:111],v6 offset:1672
v_mfma_f32_16x16x32_bf16 v[148:151],v[84:87],v[56:59],v[148:151]
ds_read_b64_tr_b16 v[64:65],v4 offset:0
ds_read_b64_tr_b16 v[66:67],v4 offset:2048
v_lshlrev_b32_sdwa v144,1,v144 dst_sel:DWORD dst_unused:UNUSED_PAD src0_sel:DWORD src1_sel:WORD_1
v_lshlrev_b32_sdwa v146,1,v146 dst_sel:DWORD dst_unused:UNUSED_PAD src0_sel:DWORD src1_sel:WORD_1
v_mfma_f32_16x16x32_bf16 v[160:163],v[88:91],v[128:131],v[160:163]
s_waitcnt lgkmcnt(15)
ds_read_u16 v144,v144
v_lshlrev_b32_sdwa v145,1,v145 dst_sel:DWORD dst_unused:UNUSED_PAD src0_sel:DWORD src1_sel:WORD_1
v_mfma_f32_16x16x32_bf16 v[168:171],v[92:95],v[128:131],v[168:171]
s_waitcnt lgkmcnt(15)
v_lshlrev_b32_sdwa v147,1,v147 dst_sel:DWORD dst_unused:UNUSED_PAD src0_sel:DWORD src1_sel:WORD_1
v_mfma_f32_16x16x32_bf16 v[176:179],v[96:99],v[128:131],v[176:179]
s_waitcnt lgkmcnt(11)
ds_read_b128 v[112:115],v7 offset:256
ds_read_b64_tr_b16 v[68:69],v5 offset:0
v_mfma_f32_16x16x32_bf16 v[184:187],v[100:103],v[128:131],v[184:187]
s_waitcnt lgkmcnt(9)
ds_read_b64_tr_b16 v[70:71],v5 offset:2048
ds_read_u16_d16_hi v146,v146
v_lshlrev_b32_sdwa v148,1,v148 dst_sel:DWORD dst_unused:UNUSED_PAD src0_sel:DWORD src1_sel:WORD_1
v_lshl_add_u64 v[136:137],v[136:137],0,v[138:139]
v_lshlrev_b32_sdwa v150,1,v150 dst_sel:DWORD dst_unused:UNUSED_PAD src0_sel:DWORD src1_sel:WORD_1
v_mfma_f32_16x16x32_bf16 v[192:195],v[104:107],v[128:131],v[192:195]
s_waitcnt lgkmcnt(7)
ds_read_u16 v145,v145
ds_read_u16_d16_hi v147,v147
v_lshl_add_u64 v[138:139],v[140:141],0,v[142:143]
v_mfma_f32_16x16x32_bf16 v[200:203],v[108:111],v[128:131],v[200:203]
ds_read_b128 v[116:119],v7 offset:288
ds_read_b64_tr_b16 v[72:73],v4 offset:4096
v_mfma_f32_16x16x32_bf16 v[164:167],v[88:91],v[136:139],v[164:167]
ds_read_u16 v148,v148
v_lshlrev_b32_sdwa v149,1,v149 dst_sel:DWORD dst_unused:UNUSED_PAD src0_sel:DWORD src1_sel:WORD_1
v_lshlrev_b32_sdwa v151,1,v151 dst_sel:DWORD dst_unused:UNUSED_PAD src0_sel:DWORD src1_sel:WORD_1
v_mfma_f32_16x16x32_bf16 v[172:175],v[92:95],v[136:139],v[172:175]
ds_read_b64_tr_b16 v[74:75],v4 offset:6144
v_lshlrev_b32_sdwa v152,1,v152 dst_sel:DWORD dst_unused:UNUSED_PAD src0_sel:DWORD src1_sel:WORD_1
v_mfma_f32_16x16x32_bf16 v[180:183],v[96:99],v[136:139],v[180:183]
ds_read_u16_d16_hi v150,v150
v_lshlrev_b32_sdwa v154,1,v154 dst_sel:DWORD dst_unused:UNUSED_PAD src0_sel:DWORD src1_sel:WORD_1
v_mfma_f32_16x16x32_bf16 v[188:191],v[100:103],v[136:139],v[188:191]
ds_read_b64_tr_b16 v[76:77],v5 offset:4096
ds_read_b64_tr_b16 v[78:79],v5 offset:6144
v_lshlrev_b32_sdwa v153,1,v153 dst_sel:DWORD dst_unused:UNUSED_PAD src0_sel:DWORD src1_sel:WORD_1
v_mfma_f32_16x16x32_bf16 v[196:199],v[104:107],v[136:139],v[196:199]
ds_read_u16 v149,v149
v_lshlrev_b32_sdwa v155,1,v155 dst_sel:DWORD dst_unused:UNUSED_PAD src0_sel:DWORD src1_sel:WORD_1
v_mfma_f32_16x16x32_bf16 v[204:207],v[108:111],v[136:139],v[204:207]
s_waitcnt lgkmcnt(13)
ds_read_b64_tr_b16 v[80:81],v4 offset:8192
ds_read_b64_tr_b16 v[82:83],v4 offset:10240
v_lshlrev_b32_sdwa v156,1,v156 dst_sel:DWORD dst_unused:UNUSED_PAD src0_sel:DWORD src1_sel:WORD_1
v_mfma_f32_16x16x32_bf16 v[128:131],v[64:67],v[16:19],v[112:115]
ds_read_u16_d16_hi v151,v151
v_lshlrev_b32_sdwa v158,1,v158 dst_sel:DWORD dst_unused:UNUSED_PAD src0_sel:DWORD src1_sel:WORD_1
v_mfma_f32_16x16x32_bf16 v[136:139],v[64:67],v[20:23],v[112:115]
s_waitcnt lgkmcnt(10)
ds_read_u16 v152,v152
ds_read_b64_tr_b16 v[84:85],v5 offset:8192
v_lshlrev_b32_sdwa v157,1,v157 dst_sel:DWORD dst_unused:UNUSED_PAD src0_sel:DWORD src1_sel:WORD_1
v_mfma_f32_16x16x32_bf16 v[140:143],v[68:71],v[20:23],v[116:119]
ds_read_b64_tr_b16 v[86:87],v5 offset:10240
v_lshlrev_b32_sdwa v159,1,v159 dst_sel:DWORD dst_unused:UNUSED_PAD src0_sel:DWORD src1_sel:WORD_1
v_mfma_f32_16x16x32_bf16 v[132:135],v[68:71],v[16:19],v[116:119]
s_waitcnt lgkmcnt(10)
ds_read_u16_d16_hi v154,v154
v_mfma_f32_16x16x32_bf16 v[128:131],v[72:75],v[24:27],v[128:131]
ds_read_u16 v153,v153
v_mfma_f32_16x16x32_bf16 v[136:139],v[72:75],v[28:31],v[136:139]
s_waitcnt lgkmcnt(9)
v_mfma_f32_16x16x32_bf16 v[140:143],v[76:79],v[28:31],v[140:143]
ds_read_u16_d16_hi v155,v155
v_lshl_add_u64 v[144:145],v[144:145],0,v[146:147]
v_mfma_f32_16x16x32_bf16 v[132:135],v[76:79],v[24:27],v[132:135]
s_waitcnt lgkmcnt(7)
ds_read_u16 v156,v156
v_mfma_f32_16x16x32_bf16 v[128:131],v[80:83],v[32:35],v[128:131]
s_waitcnt lgkmcnt(7)
v_lshl_add_u64 v[146:147],v[148:149],0,v[150:151]
v_mfma_f32_16x16x32_bf16 v[136:139],v[80:83],v[36:39],v[136:139]
s_waitcnt lgkmcnt(4)
ds_read_u16_d16_hi v158,v158
v_mfma_f32_16x16x32_bf16 v[140:143],v[84:87],v[36:39],v[140:143]
ds_read_u16 v157,v157
v_mfma_f32_16x16x32_bf16 v[132:135],v[84:87],v[32:35],v[132:135]
v_lshlrev_b32_sdwa v128,1,v128 dst_sel:DWORD dst_unused:UNUSED_PAD src0_sel:DWORD src1_sel:WORD_1
v_lshlrev_b32_sdwa v130,1,v130 dst_sel:DWORD dst_unused:UNUSED_PAD src0_sel:DWORD src1_sel:WORD_1
v_mfma_f32_16x16x32_bf16 v[208:211],v[88:91],v[144:147],v[208:211]
v_lshlrev_b32_sdwa v129,1,v129 dst_sel:DWORD dst_unused:UNUSED_PAD src0_sel:DWORD src1_sel:WORD_1
v_mfma_f32_16x16x32_bf16 v[216:219],v[92:95],v[144:147],v[216:219]
ds_read_u16_d16_hi v159,v159
v_lshlrev_b32_sdwa v131,1,v131 dst_sel:DWORD dst_unused:UNUSED_PAD src0_sel:DWORD src1_sel:WORD_1
v_mfma_f32_16x16x32_bf16 v[224:227],v[96:99],v[144:147],v[224:227]
v_lshlrev_b32_sdwa v132,1,v132 dst_sel:DWORD dst_unused:UNUSED_PAD src0_sel:DWORD src1_sel:WORD_1
v_mfma_f32_16x16x32_bf16 v[232:235],v[100:103],v[144:147],v[232:235]
s_waitcnt lgkmcnt(0)
ds_read_u16 v128,v128
ds_read_u16_d16_hi v130,v130
v_lshl_add_u64 v[152:153],v[152:153],0,v[154:155]
v_lshl_add_u64 v[154:155],v[156:157],0,v[158:159]
v_lshlrev_b32_sdwa v134,1,v134 dst_sel:DWORD dst_unused:UNUSED_PAD src0_sel:DWORD src1_sel:WORD_1
v_mfma_f32_16x16x32_bf16 v[240:243],v[104:107],v[144:147],v[240:243]
v_lshlrev_b32_sdwa v133,1,v133 dst_sel:DWORD dst_unused:UNUSED_PAD src0_sel:DWORD src1_sel:WORD_1
v_mfma_f32_16x16x32_bf16 v[248:251],v[108:111],v[144:147],v[248:251]
ds_read_u16 v129,v129
v_mfma_f32_16x16x32_bf16 v[212:215],v[88:91],v[152:155],v[212:215]
ds_read_u16_d16_hi v131,v131
v_mfma_f32_16x16x32_bf16 v[220:223],v[92:95],v[152:155],v[220:223]
ds_read_b64_tr_b16 v[88:89],v6 offset:6160
ds_read_b64_tr_b16 v[90:91],v6 offset:7696
v_lshlrev_b32_sdwa v135,1,v135 dst_sel:DWORD dst_unused:UNUSED_PAD src0_sel:DWORD src1_sel:WORD_1
v_lshlrev_b32_sdwa v136,1,v136 dst_sel:DWORD dst_unused:UNUSED_PAD src0_sel:DWORD src1_sel:WORD_1
v_mfma_f32_16x16x32_bf16 v[228:231],v[96:99],v[152:155],v[228:231]
ds_read_u16 v132,v132
v_lshlrev_b32_sdwa v138,1,v138 dst_sel:DWORD dst_unused:UNUSED_PAD src0_sel:DWORD src1_sel:WORD_1
v_mfma_f32_16x16x32_bf16 v[236:239],v[100:103],v[152:155],v[236:239]
ds_read_u16_d16_hi v134,v134
v_lshlrev_b32_sdwa v137,1,v137 dst_sel:DWORD dst_unused:UNUSED_PAD src0_sel:DWORD src1_sel:WORD_1
v_mfma_f32_16x16x32_bf16 v[244:247],v[104:107],v[152:155],v[244:247]
ds_read_u16 v133,v133
v_lshlrev_b32_sdwa v139,1,v139 dst_sel:DWORD dst_unused:UNUSED_PAD src0_sel:DWORD src1_sel:WORD_1
v_mfma_f32_16x16x32_bf16 v[252:255],v[108:111],v[152:155],v[252:255]
ds_read_b64_tr_b16 v[92:93],v6 offset:6168
ds_read_b64_tr_b16 v[94:95],v6 offset:7704
v_mfma_f32_16x16x32_bf16 v[144:147],v[64:67],v[40:43],v[112:115]
ds_read_u16_d16_hi v135,v135
v_lshlrev_b32_sdwa v140,1,v140 dst_sel:DWORD dst_unused:UNUSED_PAD src0_sel:DWORD src1_sel:WORD_1
v_lshlrev_b32_sdwa v142,1,v142 dst_sel:DWORD dst_unused:UNUSED_PAD src0_sel:DWORD src1_sel:WORD_1
v_mfma_f32_16x16x32_bf16 v[152:155],v[64:67],v[44:47],v[112:115]
ds_read_u16 v136,v136
ds_read_b64_tr_b16 v[96:97],v6 offset:6224
v_lshlrev_b32_sdwa v141,1,v141 dst_sel:DWORD dst_unused:UNUSED_PAD src0_sel:DWORD src1_sel:WORD_1
v_mfma_f32_16x16x32_bf16 v[156:159],v[68:71],v[44:47],v[116:119]
ds_read_b64_tr_b16 v[98:99],v6 offset:7760
ds_read_b64_tr_b16 v[100:101],v6 offset:6232
v_mfma_f32_16x16x32_bf16 v[148:151],v[68:71],v[40:43],v[116:119]
ds_read_u16_d16_hi v138,v138
v_mfma_f32_16x16x32_bf16 v[144:147],v[72:75],v[48:51],v[144:147]
ds_read_u16 v137,v137
v_mfma_f32_16x16x32_bf16 v[152:155],v[72:75],v[52:55],v[152:155]
ds_read_b64_tr_b16 v[102:103],v6 offset:7768
ds_read_b64_tr_b16 v[104:105],v6 offset:6288
v_mfma_f32_16x16x32_bf16 v[156:159],v[76:79],v[52:55],v[156:159]
ds_read_u16_d16_hi v139,v139
v_mfma_f32_16x16x32_bf16 v[148:151],v[76:79],v[48:51],v[148:151]
ds_read_u16 v140,v140
v_lshlrev_b32_sdwa v143,1,v143 dst_sel:DWORD dst_unused:UNUSED_PAD src0_sel:DWORD src1_sel:WORD_1
v_mfma_f32_16x16x32_bf16 v[144:147],v[80:83],v[56:59],v[144:147]
s_waitcnt lgkmcnt(15)
ds_read_b64_tr_b16 v[106:107],v6 offset:7824
ds_read_b64_tr_b16 v[108:109],v6 offset:6296
v_lshl_add_u64 v[128:129],v[128:129],0,v[130:131]
v_mfma_f32_16x16x32_bf16 v[152:155],v[80:83],v[60:63],v[152:155]
s_waitcnt lgkmcnt(12)
ds_read_u16_d16_hi v142,v142
ds_read_u16 v141,v141
v_lshl_add_u64 v[130:131],v[132:133],0,v[134:135]
v_mfma_f32_16x16x32_bf16 v[156:159],v[84:87],v[60:63],v[156:159]
ds_read_u16_d16_hi v143,v143
v_mfma_f32_16x16x32_bf16 v[148:151],v[84:87],v[56:59],v[148:151]
ds_read_b64_tr_b16 v[110:111],v6 offset:7832
ds_read_b64_tr_b16 v[64:65],v4 offset:512
v_lshlrev_b32_sdwa v144,1,v144 dst_sel:DWORD dst_unused:UNUSED_PAD src0_sel:DWORD src1_sel:WORD_1
v_lshlrev_b32_sdwa v146,1,v146 dst_sel:DWORD dst_unused:UNUSED_PAD src0_sel:DWORD src1_sel:WORD_1
v_mfma_f32_16x16x32_bf16 v[160:163],v[88:91],v[128:131],v[160:163]
ds_read_u16 v144,v144
v_lshlrev_b32_sdwa v145,1,v145 dst_sel:DWORD dst_unused:UNUSED_PAD src0_sel:DWORD src1_sel:WORD_1
v_mfma_f32_16x16x32_bf16 v[168:171],v[92:95],v[128:131],v[168:171]
s_waitcnt lgkmcnt(15)
v_lshlrev_b32_sdwa v147,1,v147 dst_sel:DWORD dst_unused:UNUSED_PAD src0_sel:DWORD src1_sel:WORD_1
v_mfma_f32_16x16x32_bf16 v[176:179],v[96:99],v[128:131],v[176:179]
s_waitcnt lgkmcnt(11)
ds_read_b64_tr_b16 v[66:67],v4 offset:2560
ds_read_b128 v[112:115],v7 offset:384
v_mfma_f32_16x16x32_bf16 v[184:187],v[100:103],v[128:131],v[184:187]
s_waitcnt lgkmcnt(9)
ds_read_b64_tr_b16 v[68:69],v5 offset:512
ds_read_u16_d16_hi v146,v146
v_lshlrev_b32_sdwa v148,1,v148 dst_sel:DWORD dst_unused:UNUSED_PAD src0_sel:DWORD src1_sel:WORD_1
v_lshl_add_u64 v[136:137],v[136:137],0,v[138:139]
v_lshlrev_b32_sdwa v150,1,v150 dst_sel:DWORD dst_unused:UNUSED_PAD src0_sel:DWORD src1_sel:WORD_1
v_mfma_f32_16x16x32_bf16 v[192:195],v[104:107],v[128:131],v[192:195]
s_waitcnt lgkmcnt(6)
ds_read_u16 v145,v145
ds_read_u16_d16_hi v147,v147
v_lshl_add_u64 v[138:139],v[140:141],0,v[142:143]
v_mfma_f32_16x16x32_bf16 v[200:203],v[108:111],v[128:131],v[200:203]
ds_read_b64_tr_b16 v[70:71],v5 offset:2560
ds_read_b128 v[116:119],v7 offset:416
v_mfma_f32_16x16x32_bf16 v[164:167],v[88:91],v[136:139],v[164:167]
ds_read_u16 v148,v148
v_lshlrev_b32_sdwa v149,1,v149 dst_sel:DWORD dst_unused:UNUSED_PAD src0_sel:DWORD src1_sel:WORD_1
v_lshlrev_b32_sdwa v151,1,v151 dst_sel:DWORD dst_unused:UNUSED_PAD src0_sel:DWORD src1_sel:WORD_1
v_mfma_f32_16x16x32_bf16 v[172:175],v[92:95],v[136:139],v[172:175]
ds_read_b64_tr_b16 v[72:73],v4 offset:4608
v_lshlrev_b32_sdwa v152,1,v152 dst_sel:DWORD dst_unused:UNUSED_PAD src0_sel:DWORD src1_sel:WORD_1
v_mfma_f32_16x16x32_bf16 v[180:183],v[96:99],v[136:139],v[180:183]
ds_read_u16_d16_hi v150,v150
v_lshlrev_b32_sdwa v154,1,v154 dst_sel:DWORD dst_unused:UNUSED_PAD src0_sel:DWORD src1_sel:WORD_1
v_mfma_f32_16x16x32_bf16 v[188:191],v[100:103],v[136:139],v[188:191]
ds_read_b64_tr_b16 v[74:75],v4 offset:6656
ds_read_b64_tr_b16 v[76:77],v5 offset:4608
v_lshlrev_b32_sdwa v153,1,v153 dst_sel:DWORD dst_unused:UNUSED_PAD src0_sel:DWORD src1_sel:WORD_1
v_mfma_f32_16x16x32_bf16 v[196:199],v[104:107],v[136:139],v[196:199]
ds_read_u16 v149,v149
v_lshlrev_b32_sdwa v155,1,v155 dst_sel:DWORD dst_unused:UNUSED_PAD src0_sel:DWORD src1_sel:WORD_1
v_mfma_f32_16x16x32_bf16 v[204:207],v[108:111],v[136:139],v[204:207]
s_waitcnt lgkmcnt(12)
ds_read_b64_tr_b16 v[78:79],v5 offset:6656
ds_read_b64_tr_b16 v[80:81],v4 offset:8704
v_lshlrev_b32_sdwa v156,1,v156 dst_sel:DWORD dst_unused:UNUSED_PAD src0_sel:DWORD src1_sel:WORD_1
v_mfma_f32_16x16x32_bf16 v[128:131],v[64:67],v[16:19],v[112:115]
ds_read_u16_d16_hi v151,v151
v_lshlrev_b32_sdwa v158,1,v158 dst_sel:DWORD dst_unused:UNUSED_PAD src0_sel:DWORD src1_sel:WORD_1
v_mfma_f32_16x16x32_bf16 v[136:139],v[64:67],v[20:23],v[112:115]
s_waitcnt lgkmcnt(9)
ds_read_u16 v152,v152
ds_read_b64_tr_b16 v[82:83],v4 offset:10752
v_lshlrev_b32_sdwa v157,1,v157 dst_sel:DWORD dst_unused:UNUSED_PAD src0_sel:DWORD src1_sel:WORD_1
v_mfma_f32_16x16x32_bf16 v[140:143],v[68:71],v[20:23],v[116:119]
s_nop 0
ds_read_b64_tr_b16 v[84:85],v5 offset:8704
ds_read_b64_tr_b16 v[86:87],v5 offset:10752
v_lshlrev_b32_sdwa v159,1,v159 dst_sel:DWORD dst_unused:UNUSED_PAD src0_sel:DWORD src1_sel:WORD_1
v_mfma_f32_16x16x32_bf16 v[132:135],v[68:71],v[16:19],v[116:119]
s_waitcnt lgkmcnt(9)
ds_read_u16_d16_hi v154,v154
v_mfma_f32_16x16x32_bf16 v[128:131],v[72:75],v[24:27],v[128:131]
ds_read_u16 v153,v153
v_mfma_f32_16x16x32_bf16 v[136:139],v[72:75],v[28:31],v[136:139]
s_waitcnt lgkmcnt(8)
v_mfma_f32_16x16x32_bf16 v[140:143],v[76:79],v[28:31],v[140:143]
s_nop 0
ds_read_u16_d16_hi v155,v155
v_lshl_add_u64 v[144:145],v[144:145],0,v[146:147]
v_mfma_f32_16x16x32_bf16 v[132:135],v[76:79],v[24:27],v[132:135]
s_waitcnt lgkmcnt(5)
ds_read_u16 v156,v156
v_mfma_f32_16x16x32_bf16 v[128:131],v[80:83],v[32:35],v[128:131]
v_lshl_add_u64 v[146:147],v[148:149],0,v[150:151]
v_mfma_f32_16x16x32_bf16 v[136:139],v[80:83],v[36:39],v[136:139]
s_waitcnt lgkmcnt(4)
ds_read_u16_d16_hi v158,v158
v_mfma_f32_16x16x32_bf16 v[140:143],v[84:87],v[36:39],v[140:143]
s_nop 0
ds_read_u16 v157,v157
v_mfma_f32_16x16x32_bf16 v[132:135],v[84:87],v[32:35],v[132:135]
s_nop 0
v_lshlrev_b32_sdwa v128,1,v128 dst_sel:DWORD dst_unused:UNUSED_PAD src0_sel:DWORD src1_sel:WORD_1
v_lshlrev_b32_sdwa v130,1,v130 dst_sel:DWORD dst_unused:UNUSED_PAD src0_sel:DWORD src1_sel:WORD_1
v_mfma_f32_16x16x32_bf16 v[208:211],v[88:91],v[144:147],v[208:211]
s_mov_b32 s86,0x1234567
s_waitcnt vmcnt(0)
s_barrier
s_setprio 3
v_fma_mixlo_f16 v120,v140,s99,0
v_lshlrev_b32_sdwa v129,1,v129 dst_sel:DWORD dst_unused:UNUSED_PAD src0_sel:DWORD src1_sel:WORD_1
v_lshlrev_b32_sdwa v131,1,v131 dst_sel:DWORD dst_unused:UNUSED_PAD src0_sel:DWORD src1_sel:WORD_1
v_mfma_f32_16x16x32_bf16 v[216:219],v[92:95],v[144:147],v[216:219]
s_cbranch_vccz RB38_%=
s_add_u32 m0,s58,0
s_mul_i32 s69,s59,5
s_add_u32 s69,s69,s97
buffer_load_dwordx4 v2,s[44:47],s69 offen lds
s_add_u32 m0,m0,1024
s_add_u32 s69,s69,s53
buffer_load_dwordx4 v3,s[44:47],s69 offen lds
s_add_u32 m0,m0,1024
s_add_u32 s69,s69,s53
buffer_load_dwordx4 v2,s[44:47],s69 offen lds
s_add_u32 m0,m0,s50
s_add_u32 s69,s69,s53
buffer_load_dwordx4 v3,s[44:47],s69 offen lds
RB38_%=:
v_fma_mixlo_f16 v122,v141,s99,0
v_lshlrev_b32_sdwa v132,1,v132 dst_sel:DWORD dst_unused:UNUSED_PAD src0_sel:DWORD src1_sel:WORD_1
v_mfma_f32_16x16x32_bf16 v[224:227],v[96:99],v[144:147],v[224:227]
v_lshlrev_b32_sdwa v134,1,v134 dst_sel:DWORD dst_unused:UNUSED_PAD src0_sel:DWORD src1_sel:WORD_1
v_mfma_f32_16x16x32_bf16 v[232:235],v[100:103],v[144:147],v[232:235]
s_waitcnt lgkmcnt(3)
s_cbranch_vccz RB39_%=
s_add_u32 m0,m0,1024
s_add_u32 s69,s69,s53
buffer_load_dwordx4 v2,s[44:47],s69 offen lds
s_add_u32 m0,m0,1024
s_add_u32 s69,s69,s53
buffer_load_dwordx4 v3,s[44:47],s69 offen lds
s_add_u32 m0,m0,s50
s_add_u32 s69,s69,s53
buffer_load_dwordx4 v2,s[44:47],s69 offen lds
s_add_u32 m0,m0,1024
s_add_u32 s69,s69,s53
buffer_load_dwordx4 v3,s[44:47],s69 offen lds
RB39_%=:
ds_read_u16_d16_hi v159,v159
ds_read_u16 v128,v128
v_lshl_add_u64 v[152:153],v[152:153],0,v[154:155]
v_mfma_f32_16x16x32_bf16 v[240:243],v[104:107],v[144:147],v[240:243]
s_waitcnt lgkmcnt(1)
s_cbranch_vccz RB40_%=
s_add_u32 m0,m0,1024
s_add_u32 s69,s69,s53
buffer_load_dwordx4 v2,s[44:47],s69 offen lds
s_add_u32 m0,m0,s50
s_add_u32 s69,s69,s53
buffer_load_dwordx4 v3,s[44:47],s69 offen lds
s_add_u32 m0,m0,1024
s_add_u32 s69,s69,s53
buffer_load_dwordx4 v2,s[44:47],s69 offen lds
s_add_u32 m0,m0,1024
s_add_u32 s69,s69,s53
buffer_load_dwordx4 v3,s[44:47],s69 offen lds
RB40_%=:
v_lshl_add_u64 v[154:155],v[156:157],0,v[158:159]
v_fma_mixhi_f16 v120,v142,s99,0
v_mfma_f32_16x16x32_bf16 v[248:251],v[108:111],v[144:147],v[248:251]
ds_read_u16_d16_hi v130,v130
v_fma_mixhi_f16 v122,v143,s99,0
v_mfma_f32_16x16x32_bf16 v[212:215],v[88:91],v[152:155],v[212:215]
ds_read_u16 v129,v129
v_lshlrev_b32_sdwa v133,1,v133 dst_sel:DWORD dst_unused:UNUSED_PAD src0_sel:DWORD src1_sel:WORD_1
v_lshlrev_b32_sdwa v135,1,v135 dst_sel:DWORD dst_unused:UNUSED_PAD src0_sel:DWORD src1_sel:WORD_1
v_mfma_f32_16x16x32_bf16 v[220:223],v[92:95],v[152:155],v[220:223]
v_pk_fma_f16 v121,v120,v120,s54
v_pk_fma_f16 v123,v122,v122,s54
v_mfma_f32_16x16x32_bf16 v[228:231],v[96:99],v[152:155],v[228:231]
ds_read_u16_d16_hi v131,v131
v_pk_mul_f16 v121,v120,v121
v_pk_mul_f16 v123,v122,v123
v_lshlrev_b32_sdwa v136,1,v136 dst_sel:DWORD dst_unused:UNUSED_PAD src0_sel:DWORD src1_sel:WORD_1
v_mfma_f32_16x16x32_bf16 v[236:239],v[100:103],v[152:155],v[236:239]
ds_read_b64_tr_b16 v[88:89],v6 offset:12320
ds_read_b64_tr_b16 v[90:91],v6 offset:13856
v_mfma_f32_16x16x32_bf16 v[244:247],v[104:107],v[152:155],v[244:247]
ds_read_u16 v132,v132
v_exp_f16 v120,v121
v_mfma_f32_16x16x32_bf16 v[252:255],v[108:111],v[152:155],v[252:255]
ds_read_b64_tr_b16 v[92:93],v6 offset:12328
v_lshlrev_b32_sdwa v138,1,v138 dst_sel:DWORD dst_unused:UNUSED_PAD src0_sel:DWORD src1_sel:WORD_1
v_exp_f16 v122,v123
v_mfma_f32_16x16x32_bf16 v[144:147],v[64:67],v[40:43],v[112:115]
ds_read_u16_d16_hi v134,v134
v_lshlrev_b32_sdwa v137,1,v137 dst_sel:DWORD dst_unused:UNUSED_PAD src0_sel:DWORD src1_sel:WORD_1
v_lshlrev_b32_sdwa v139,1,v139 dst_sel:DWORD dst_unused:UNUSED_PAD src0_sel:DWORD src1_sel:WORD_1
v_mfma_f32_16x16x32_bf16 v[152:155],v[64:67],v[44:47],v[112:115]
ds_read_b64_tr_b16 v[94:95],v6 offset:13864
v_exp_f16_sdwa v120,v121 dst_sel:WORD_1 dst_unused:UNUSED_PRESERVE src0_sel:WORD_1
v_mfma_f32_16x16x32_bf16 v[156:159],v[68:71],v[44:47],v[116:119]
ds_read_u16 v133,v133
v_exp_f16_sdwa v122,v123 dst_sel:WORD_1 dst_unused:UNUSED_PRESERVE src0_sel:WORD_1
v_mfma_f32_16x16x32_bf16 v[148:151],v[68:71],v[40:43],v[116:119]
ds_read_u16_d16_hi v135,v135
s_nop 0
s_mov_b32 s86,0xbe801d5a
ds_read_b64_tr_b16 v[96:97],v6 offset:12384
v_pk_fma_f16 v120,v120,s55,s55
v_mfma_f32_16x16x32_bf16 v[144:147],v[72:75],v[48:51],v[144:147]
v_rcp_f16 v121,v120
v_pk_fma_f16 v122,v122,s55,s55
v_mfma_f32_16x16x32_bf16 v[152:155],v[72:75],v[52:55],v[152:155]
ds_read_b64_tr_b16 v[98:99],v6 offset:13920
ds_read_u16 v136,v136
v_rcp_f16 v123,v122
v_mfma_f32_16x16x32_bf16 v[156:159],v[76:79],v[52:55],v[156:159]
ds_read_u16_d16_hi v138,v138
v_rcp_f16_sdwa v121,v120 dst_sel:WORD_1 dst_unused:UNUSED_PRESERVE src0_sel:WORD_1
v_mfma_f32_16x16x32_bf16 v[148:151],v[76:79],v[48:51],v[148:151]
ds_read_b64_tr_b16 v[100:101],v6 offset:12392
ds_read_b64_tr_b16 v[102:103],v6 offset:13928
v_rcp_f16_sdwa v123,v122 dst_sel:WORD_1 dst_unused:UNUSED_PRESERVE src0_sel:WORD_1
v_mfma_f32_16x16x32_bf16 v[144:147],v[80:83],v[56:59],v[144:147]
s_waitcnt lgkmcnt(14)
v_fma_mix_f32 v140,v140,v121,0 op_sel_hi:[0,1,0]
v_lshl_add_u64 v[128:129],v[128:129],0,v[130:131]
v_fma_mix_f32 v142,v142,v121,0 op_sel:[0,1,0] op_sel_hi:[0,1,0]
v_mfma_f32_16x16x32_bf16 v[152:155],v[80:83],v[60:63],v[152:155]
ds_read_b64_tr_b16 v[104:105],v6 offset:12448
ds_read_b64_tr_b16 v[106:107],v6 offset:13984
v_fma_mix_f32 v141,v141,v123,0 op_sel_hi:[0,1,0]
v_mfma_f32_16x16x32_bf16 v[156:159],v[84:87],v[60:63],v[156:159]
ds_read_u16 v137,v137
ds_read_u16_d16_hi v139,v139
v_fma_mix_f32 v143,v143,v123,0 op_sel:[0,1,0] op_sel_hi:[0,1,0]
v_mfma_f32_16x16x32_bf16 v[148:151],v[84:87],v[56:59],v[148:151]
s_waitcnt lgkmcnt(10)
v_lshl_add_u64 v[130:131],v[132:133],0,v[134:135]
v_lshlrev_b32_sdwa v144,1,v144 dst_sel:DWORD dst_unused:UNUSED_PAD src0_sel:DWORD src1_sel:WORD_1
v_lshlrev_b32_sdwa v146,1,v146 dst_sel:DWORD dst_unused:UNUSED_PAD src0_sel:DWORD src1_sel:WORD_1
v_mfma_f32_16x16x32_bf16 v[160:163],v[88:91],v[128:131],v[160:163]
ds_read_b64_tr_b16 v[108:109],v6 offset:12456
ds_read_b64_tr_b16 v[110:111],v6 offset:13992
v_lshlrev_b32_sdwa v145,1,v145 dst_sel:DWORD dst_unused:UNUSED_PAD src0_sel:DWORD src1_sel:WORD_1
v_lshlrev_b32_sdwa v147,1,v147 dst_sel:DWORD dst_unused:UNUSED_PAD src0_sel:DWORD src1_sel:WORD_1
v_mfma_f32_16x16x32_bf16 v[168:171],v[92:95],v[128:131],v[168:171]
s_waitcnt lgkmcnt(10)
ds_read_b64_tr_b16 v[64:65],v4 offset:12320
ds_read_b64_tr_b16 v[66:67],v4 offset:14368
v_mfma_f32_16x16x32_bf16 v[176:179],v[96:99],v[128:131],v[176:179]
s_waitcnt lgkmcnt(8)
ds_read_b128 v[112:115],v7 offset:512
ds_read_u16 v144,v144
v_fma_mixlo_f16 v120,v156,s99,0
v_fma_mixlo_f16 v122,v157,s99,0
v_lshlrev_b32_sdwa v148,1,v148 dst_sel:DWORD dst_unused:UNUSED_PAD src0_sel:DWORD src1_sel:WORD_1
v_lshlrev_b32_sdwa v150,1,v150 dst_sel:DWORD dst_unused:UNUSED_PAD src0_sel:DWORD src1_sel:WORD_1
v_mfma_f32_16x16x32_bf16 v[184:187],v[100:103],v[128:131],v[184:187]
s_waitcnt lgkmcnt(6)
ds_read_u16_d16_hi v146,v146
v_lshl_add_u64 v[136:137],v[136:137],0,v[138:139]
v_mfma_f32_16x16x32_bf16 v[192:195],v[104:107],v[128:131],v[192:195]
s_waitcnt lgkmcnt(5)
ds_read_b64_tr_b16 v[68:69],v5 offset:12320
v_fma_mixhi_f16 v120,v158,s99,0
v_cvt_pk_bf16_f32 v138,v140,v142
v_cvt_pk_bf16_f32 v139,v141,v143
v_mfma_f32_16x16x32_bf16 v[200:203],v[108:111],v[128:131],v[200:203]
ds_read_b64_tr_b16 v[70:71],v5 offset:14368
v_fma_mixhi_f16 v122,v159,s99,0
v_mfma_f32_16x16x32_bf16 v[164:167],v[88:91],v[136:139],v[164:167]
v_lshlrev_b32_sdwa v149,1,v149 dst_sel:DWORD dst_unused:UNUSED_PAD src0_sel:DWORD src1_sel:WORD_1
v_lshlrev_b32_sdwa v151,1,v151 dst_sel:DWORD dst_unused:UNUSED_PAD src0_sel:DWORD src1_sel:WORD_1
v_mfma_f32_16x16x32_bf16 v[172:175],v[92:95],v[136:139],v[172:175]
ds_read_b128 v[116:119],v7 offset:544
ds_read_b64_tr_b16 v[72:73],v4 offset:16416
v_pk_fma_f16 v123,v122,v122,s54
v_pk_fma_f16 v121,v120,v120,s54
v_mfma_f32_16x16x32_bf16 v[180:183],v[96:99],v[136:139],v[180:183]
ds_read_b64_tr_b16 v[74:75],v4 offset:18464
v_pk_mul_f16 v121,v120,v121
v_pk_mul_f16 v123,v122,v123
v_mfma_f32_16x16x32_bf16 v[188:191],v[100:103],v[136:139],v[188:191]
ds_read_u16 v145,v145
ds_read_b64_tr_b16 v[76:77],v5 offset:16416
v_lshlrev_b32_sdwa v152,1,v152 dst_sel:DWORD dst_unused:UNUSED_PAD src0_sel:DWORD src1_sel:WORD_1
v_mfma_f32_16x16x32_bf16 v[196:199],v[104:107],v[136:139],v[196:199]
ds_read_u16_d16_hi v147,v147
v_lshlrev_b32_sdwa v154,1,v154 dst_sel:DWORD dst_unused:UNUSED_PAD src0_sel:DWORD src1_sel:WORD_1
v_exp_f16 v120,v121
v_mfma_f32_16x16x32_bf16 v[204:207],v[108:111],v[136:139],v[204:207]
s_waitcnt lgkmcnt(10)
ds_read_b64_tr_b16 v[78:79],v5 offset:18464
v_exp_f16 v122,v123
v_mfma_f32_16x16x32_bf16 v[128:131],v[64:67],v[16:19],v[112:115]
ds_read_u16 v148,v148
ds_read_u16_d16_hi v150,v150
v_lshlrev_b32_sdwa v153,1,v153 dst_sel:DWORD dst_unused:UNUSED_PAD src0_sel:DWORD src1_sel:WORD_1
v_exp_f16_sdwa v122,v123 dst_sel:WORD_1 dst_unused:UNUSED_PRESERVE src0_sel:WORD_1
v_lshlrev_b32_sdwa v155,1,v155 dst_sel:DWORD dst_unused:UNUSED_PAD src0_sel:DWORD src1_sel:WORD_1
v_mfma_f32_16x16x32_bf16 v[136:139],v[64:67],v[20:23],v[112:115]
s_waitcnt lgkmcnt(8)
v_exp_f16_sdwa v120,v121 dst_sel:WORD_1 dst_unused:UNUSED_PRESERVE src0_sel:WORD_1
v_mfma_f32_16x16x32_bf16 v[140:143],v[68:71],v[20:23],v[116:119]
ds_read_b64_tr_b16 v[80:81],v4 offset:20512
ds_read_b64_tr_b16 v[82:83],v4 offset:22560
v_pk_fma_f16 v120,v120,s55,s55
v_mfma_f32_16x16x32_bf16 v[132:135],v[68:71],v[16:19],v[116:119]
s_waitcnt lgkmcnt(8)
ds_read_u16 v149,v149
v_pk_fma_f16 v122,v122,s55,s55
v_mfma_f32_16x16x32_bf16 v[128:131],v[72:75],v[24:27],v[128:131]
ds_read_b64_tr_b16 v[84:85],v5 offset:20512
ds_read_u16_d16_hi v151,v151
v_rcp_f16 v121,v120
v_mfma_f32_16x16x32_bf16 v[136:139],v[72:75],v[28:31],v[136:139]
s_waitcnt lgkmcnt(7)
ds_read_b64_tr_b16 v[86:87],v5 offset:22560
v_rcp_f16 v123,v122
v_mfma_f32_16x16x32_bf16 v[140:143],v[76:79],v[28:31],v[140:143]
v_rcp_f16_sdwa v121,v120 dst_sel:WORD_1 dst_unused:UNUSED_PRESERVE src0_sel:WORD_1
v_mfma_f32_16x16x32_bf16 v[132:135],v[76:79],v[24:27],v[132:135]
s_waitcnt lgkmcnt(4)
ds_read_u16 v152,v152
v_rcp_f16_sdwa v123,v122 dst_sel:WORD_1 dst_unused:UNUSED_PRESERVE src0_sel:WORD_1
v_mfma_f32_16x16x32_bf16 v[128:131],v[80:83],v[32:35],v[128:131]
ds_read_u16_d16_hi v154,v154
v_fma_mix_f32 v156,v156,v121,0 op_sel_hi:[0,1,0]
v_mfma_f32_16x16x32_bf16 v[136:139],v[80:83],v[36:39],v[136:139]
s_waitcnt lgkmcnt(2)
v_lshl_add_u64 v[144:145],v[144:145],0,v[146:147]
v_fma_mix_f32 v158,v158,v121,0 op_sel:[0,1,0] op_sel_hi:[0,1,0]
v_lshl_add_u64 v[146:147],v[148:149],0,v[150:151]
v_mfma_f32_16x16x32_bf16 v[140:143],v[84:87],v[36:39],v[140:143]
ds_read_u16 v153,v153
v_fma_mix_f32 v157,v157,v123,0 op_sel_hi:[0,1,0]
v_fma_mix_f32 v159,v159,v123,0 op_sel:[0,1,0] op_sel_hi:[0,1,0]
v_mfma_f32_16x16x32_bf16 v[132:135],v[84:87],v[32:35],v[132:135]
v_lshlrev_b32_sdwa v128,1,v128 dst_sel:DWORD dst_unused:UNUSED_PAD src0_sel:DWORD src1_sel:WORD_1
v_lshlrev_b32_sdwa v130,1,v130 dst_sel:DWORD dst_unused:UNUSED_PAD src0_sel:DWORD src1_sel:WORD_1
v_mfma_f32_16x16x32_bf16 v[208:211],v[88:91],v[144:147],v[208:211]
s_nop 0
v_fma_mixlo_f16 v120,v140,s99,0
v_lshlrev_b32_sdwa v129,1,v129 dst_sel:DWORD dst_unused:UNUSED_PAD src0_sel:DWORD src1_sel:WORD_1
v_lshlrev_b32_sdwa v131,1,v131 dst_sel:DWORD dst_unused:UNUSED_PAD src0_sel:DWORD src1_sel:WORD_1
v_mfma_f32_16x16x32_bf16 v[216:219],v[92:95],v[144:147],v[216:219]
v_fma_mixlo_f16 v122,v141,s99,0
v_lshlrev_b32_sdwa v132,1,v132 dst_sel:DWORD dst_unused:UNUSED_PAD src0_sel:DWORD src1_sel:WORD_1
v_mfma_f32_16x16x32_bf16 v[224:227],v[96:99],v[144:147],v[224:227]
v_lshlrev_b32_sdwa v134,1,v134 dst_sel:DWORD dst_unused:UNUSED_PAD src0_sel:DWORD src1_sel:WORD_1
v_mfma_f32_16x16x32_bf16 v[232:235],v[100:103],v[144:147],v[232:235]
s_waitcnt lgkmcnt(0)
ds_read_u16_d16_hi v155,v155
ds_read_u16 v128,v128
s_waitcnt lgkmcnt(1)
v_lshl_add_u64 v[152:153],v[152:153],0,v[154:155]
v_mfma_f32_16x16x32_bf16 v[240:243],v[104:107],v[144:147],v[240:243]
v_cvt_pk_bf16_f32 v154,v156,v158
v_cvt_pk_bf16_f32 v155,v157,v159
v_fma_mixhi_f16 v120,v142,s99,0
v_mfma_f32_16x16x32_bf16 v[248:251],v[108:111],v[144:147],v[248:251]
ds_read_u16_d16_hi v130,v130
v_fma_mixhi_f16 v122,v143,s99,0
v_mfma_f32_16x16x32_bf16 v[212:215],v[88:91],v[152:155],v[212:215]
ds_read_u16 v129,v129
v_lshlrev_b32_sdwa v133,1,v133 dst_sel:DWORD dst_unused:UNUSED_PAD src0_sel:DWORD src1_sel:WORD_1
v_lshlrev_b32_sdwa v135,1,v135 dst_sel:DWORD dst_unused:UNUSED_PAD src0_sel:DWORD src1_sel:WORD_1
v_mfma_f32_16x16x32_bf16 v[220:223],v[92:95],v[152:155],v[220:223]
v_pk_fma_f16 v121,v120,v120,s54
v_pk_fma_f16 v123,v122,v122,s54
v_mfma_f32_16x16x32_bf16 v[228:231],v[96:99],v[152:155],v[228:231]
ds_read_u16_d16_hi v131,v131
v_pk_mul_f16 v121,v120,v121
v_pk_mul_f16 v123,v122,v123
v_lshlrev_b32_sdwa v136,1,v136 dst_sel:DWORD dst_unused:UNUSED_PAD src0_sel:DWORD src1_sel:WORD_1
v_mfma_f32_16x16x32_bf16 v[236:239],v[100:103],v[152:155],v[236:239]
s_setprio 2
ds_read_b64_tr_b16 v[88:89],v6 offset:18480
ds_read_b64_tr_b16 v[90:91],v6 offset:20016
v_mfma_f32_16x16x32_bf16 v[244:247],v[104:107],v[152:155],v[244:247]
ds_read_u16 v132,v132
v_exp_f16 v120,v121
v_mfma_f32_16x16x32_bf16 v[252:255],v[108:111],v[152:155],v[252:255]
ds_read_b64_tr_b16 v[92:93],v6 offset:18488
v_lshlrev_b32_sdwa v138,1,v138 dst_sel:DWORD dst_unused:UNUSED_PAD src0_sel:DWORD src1_sel:WORD_1
v_exp_f16 v122,v123
v_mfma_f32_16x16x32_bf16 v[144:147],v[64:67],v[40:43],v[112:115]
ds_read_u16_d16_hi v134,v134
v_lshlrev_b32_sdwa v137,1,v137 dst_sel:DWORD dst_unused:UNUSED_PAD src0_sel:DWORD src1_sel:WORD_1
v_lshlrev_b32_sdwa v139,1,v139 dst_sel:DWORD dst_unused:UNUSED_PAD src0_sel:DWORD src1_sel:WORD_1
v_mfma_f32_16x16x32_bf16 v[152:155],v[64:67],v[44:47],v[112:115]
ds_read_b64_tr_b16 v[94:95],v6 offset:20024
v_exp_f16_sdwa v120,v121 dst_sel:WORD_1 dst_unused:UNUSED_PRESERVE src0_sel:WORD_1
v_mfma_f32_16x16x32_bf16 v[156:159],v[68:71],v[44:47],v[116:119]
ds_read_u16 v133,v133
v_exp_f16_sdwa v122,v123 dst_sel:WORD_1 dst_unused:UNUSED_PRESERVE src0_sel:WORD_1
v_mfma_f32_16x16x32_bf16 v[148:151],v[68:71],v[40:43],v[116:119]
ds_read_u16_d16_hi v135,v135
ds_read_b64_tr_b16 v[96:97],v6 offset:18544
v_pk_fma_f16 v120,v120,s55,s55
v_mfma_f32_16x16x32_bf16 v[144:147],v[72:75],v[48:51],v[144:147]
v_rcp_f16 v121,v120
v_pk_fma_f16 v122,v122,s55,s55
v_mfma_f32_16x16x32_bf16 v[152:155],v[72:75],v[52:55],v[152:155]
ds_read_b64_tr_b16 v[98:99],v6 offset:20080
ds_read_u16 v136,v136
v_rcp_f16 v123,v122
v_mfma_f32_16x16x32_bf16 v[156:159],v[76:79],v[52:55],v[156:159]
ds_read_u16_d16_hi v138,v138
v_rcp_f16_sdwa v121,v120 dst_sel:WORD_1 dst_unused:UNUSED_PRESERVE src0_sel:WORD_1
v_mfma_f32_16x16x32_bf16 v[148:151],v[76:79],v[48:51],v[148:151]
ds_read_b64_tr_b16 v[100:101],v6 offset:18552
ds_read_b64_tr_b16 v[102:103],v6 offset:20088
v_rcp_f16_sdwa v123,v122 dst_sel:WORD_1 dst_unused:UNUSED_PRESERVE src0_sel:WORD_1
v_mfma_f32_16x16x32_bf16 v[144:147],v[80:83],v[56:59],v[144:147]
s_waitcnt lgkmcnt(14)
v_fma_mix_f32 v140,v140,v121,0 op_sel_hi:[0,1,0]
v_lshl_add_u64 v[128:129],v[128:129],0,v[130:131]
v_fma_mix_f32 v142,v142,v121,0 op_sel:[0,1,0] op_sel_hi:[0,1,0]
v_mfma_f32_16x16x32_bf16 v[152:155],v[80:83],v[60:63],v[152:155]
ds_read_b64_tr_b16 v[104:105],v6 offset:18608
ds_read_b64_tr_b16 v[106:107],v6 offset:20144
v_fma_mix_f32 v141,v141,v123,0 op_sel_hi:[0,1,0]
v_mfma_f32_16x16x32_bf16 v[156:159],v[84:87],v[60:63],v[156:159]
ds_read_u16 v137,v137
ds_read_u16_d16_hi v139,v139
v_fma_mix_f32 v143,v143,v123,0 op_sel:[0,1,0] op_sel_hi:[0,1,0]
v_mfma_f32_16x16x32_bf16 v[148:151],v[84:87],v[56:59],v[148:151]
s_waitcnt lgkmcnt(10)
v_lshl_add_u64 v[130:131],v[132:133],0,v[134:135]
v_lshlrev_b32_sdwa v144,1,v144 dst_sel:DWORD dst_unused:UNUSED_PAD src0_sel:DWORD src1_sel:WORD_1
v_lshlrev_b32_sdwa v146,1,v146 dst_sel:DWORD dst_unused:UNUSED_PAD src0_sel:DWORD src1_sel:WORD_1
v_mfma_f32_16x16x32_bf16 v[160:163],v[88:91],v[128:131],v[160:163]
ds_read_b64_tr_b16 v[108:109],v6 offset:18616
ds_read_b64_tr_b16 v[110:111],v6 offset:20152
v_lshlrev_b32_sdwa v145,1,v145 dst_sel:DWORD dst_unused:UNUSED_PAD src0_sel:DWORD src1_sel:WORD_1
v_lshlrev_b32_sdwa v147,1,v147 dst_sel:DWORD dst_unused:UNUSED_PAD src0_sel:DWORD src1_sel:WORD_1
v_mfma_f32_16x16x32_bf16 v[168:171],v[92:95],v[128:131],v[168:171]
s_waitcnt lgkmcnt(10)
ds_read_b64_tr_b16 v[64:65],v4 offset:12832
ds_read_b64_tr_b16 v[66:67],v4 offset:14880
v_mfma_f32_16x16x32_bf16 v[176:179],v[96:99],v[128:131],v[176:179]
s_waitcnt lgkmcnt(8)
ds_read_b128 v[112:115],v7 offset:640
ds_read_u16 v144,v144
v_fma_mixlo_f16 v120,v156,s99,0
v_fma_mixlo_f16 v122,v157,s99,0
v_lshlrev_b32_sdwa v148,1,v148 dst_sel:DWORD dst_unused:UNUSED_PAD src0_sel:DWORD src1_sel:WORD_1
v_lshlrev_b32_sdwa v150,1,v150 dst_sel:DWORD dst_unused:UNUSED_PAD src0_sel:DWORD src1_sel:WORD_1
v_mfma_f32_16x16x32_bf16 v[184:187],v[100:103],v[128:131],v[184:187]
s_waitcnt lgkmcnt(6)
ds_read_u16_d16_hi v146,v146
v_lshl_add_u64 v[136:137],v[136:137],0,v[138:139]
v_mfma_f32_16x16x32_bf16 v[192:195],v[104:107],v[128:131],v[192:195]
s_waitcnt lgkmcnt(5)
ds_read_b64_tr_b16 v[68:69],v5 offset:12832
v_fma_mixhi_f16 v120,v158,s99,0
v_cvt_pk_bf16_f32 v138,v140,v142
v_cvt_pk_bf16_f32 v139,v141,v143
v_mfma_f32_16x16x32_bf16 v[200:203],v[108:111],v[128:131],v[200:203]
ds_read_b64_tr_b16 v[70:71],v5 offset:14880
v_fma_mixhi_f16 v122,v159,s99,0
v_mfma_f32_16x16x32_bf16 v[164:167],v[88:91],v[136:139],v[164:167]
v_lshlrev_b32_sdwa v149,1,v149 dst_sel:DWORD dst_unused:UNUSED_PAD src0_sel:DWORD src1_sel:WORD_1
v_lshlrev_b32_sdwa v151,1,v151 dst_sel:DWORD dst_unused:UNUSED_PAD src0_sel:DWORD src1_sel:WORD_1
v_mfma_f32_16x16x32_bf16 v[172:175],v[92:95],v[136:139],v[172:175]
s_setprio 1
ds_read_b128 v[116:119],v7 offset:672
ds_read_b64_tr_b16 v[72:73],v4 offset:16928
v_pk_fma_f16 v123,v122,v122,s54
v_pk_fma_f16 v121,v120,v120,s54
v_mfma_f32_16x16x32_bf16 v[180:183],v[96:99],v[136:139],v[180:183]
ds_read_b64_tr_b16 v[74:75],v4 offset:18976
v_pk_mul_f16 v121,v120,v121
v_pk_mul_f16 v123,v122,v123
v_mfma_f32_16x16x32_bf16 v[188:191],v[100:103],v[136:139],v[188:191]
ds_read_u16 v145,v145
ds_read_b64_tr_b16 v[76:77],v5 offset:16928
v_lshlrev_b32_sdwa v152,1,v152 dst_sel:DWORD dst_unused:UNUSED_PAD src0_sel:DWORD src1_sel:WORD_1
v_mfma_f32_16x16x32_bf16 v[196:199],v[104:107],v[136:139],v[196:199]
ds_read_u16_d16_hi v147,v147
v_lshlrev_b32_sdwa v154,1,v154 dst_sel:DWORD dst_unused:UNUSED_PAD src0_sel:DWORD src1_sel:WORD_1
v_exp_f16 v120,v121
v_mfma_f32_16x16x32_bf16 v[204:207],v[108:111],v[136:139],v[204:207]
s_waitcnt lgkmcnt(10)
ds_read_b64_tr_b16 v[78:79],v5 offset:18976
v_exp_f16 v122,v123
v_mfma_f32_16x16x32_bf16 v[128:131],v[64:67],v[16:19],v[112:115]
ds_read_u16 v148,v148
ds_read_u16_d16_hi v150,v150
v_lshlrev_b32_sdwa v153,1,v153 dst_sel:DWORD dst_unused:UNUSED_PAD src0_sel:DWORD src1_sel:WORD_1
v_exp_f16_sdwa v122,v123 dst_sel:WORD_1 dst_unused:UNUSED_PRESERVE src0_sel:WORD_1
v_lshlrev_b32_sdwa v155,1,v155 dst_sel:DWORD dst_unused:UNUSED_PAD src0_sel:DWORD src1_sel:WORD_1
v_mfma_f32_16x16x32_bf16 v[136:139],v[64:67],v[20:23],v[112:115]
s_waitcnt lgkmcnt(8)
s_setprio 0
v_exp_f16_sdwa v120,v121 dst_sel:WORD_1 dst_unused:UNUSED_PRESERVE src0_sel:WORD_1
v_mfma_f32_16x16x32_bf16 v[140:143],v[68:71],v[20:23],v[116:119]
ds_read_b64_tr_b16 v[80:81],v4 offset:21024
ds_read_b64_tr_b16 v[82:83],v4 offset:23072
v_pk_fma_f16 v120,v120,s55,s55
v_mfma_f32_16x16x32_bf16 v[132:135],v[68:71],v[16:19],v[116:119]
s_waitcnt lgkmcnt(8)
ds_read_u16 v149,v149
v_pk_fma_f16 v122,v122,s55,s55
v_mfma_f32_16x16x32_bf16 v[128:131],v[72:75],v[24:27],v[128:131]
ds_read_b64_tr_b16 v[84:85],v5 offset:21024
ds_read_u16_d16_hi v151,v151
v_rcp_f16 v121,v120
v_mfma_f32_16x16x32_bf16 v[136:139],v[72:75],v[28:31],v[136:139]
s_waitcnt lgkmcnt(7)
ds_read_b64_tr_b16 v[86:87],v5 offset:23072
v_rcp_f16 v123,v122
v_mfma_f32_16x16x32_bf16 v[140:143],v[76:79],v[28:31],v[140:143]
v_rcp_f16_sdwa v121,v120 dst_sel:WORD_1 dst_unused:UNUSED_PRESERVE src0_sel:WORD_1
v_mfma_f32_16x16x32_bf16 v[132:135],v[76:79],v[24:27],v[132:135]
s_waitcnt lgkmcnt(4)
ds_read_u16 v152,v152
v_rcp_f16_sdwa v123,v122 dst_sel:WORD_1 dst_unused:UNUSED_PRESERVE src0_sel:WORD_1
v_mfma_f32_16x16x32_bf16 v[128:131],v[80:83],v[32:35],v[128:131]
ds_read_u16_d16_hi v154,v154
v_fma_mix_f32 v156,v156,v121,0 op_sel_hi:[0,1,0]
v_mfma_f32_16x16x32_bf16 v[136:139],v[80:83],v[36:39],v[136:139]
s_waitcnt lgkmcnt(2)
v_lshl_add_u64 v[144:145],v[144:145],0,v[146:147]
v_fma_mix_f32 v158,v158,v121,0 op_sel:[0,1,0] op_sel_hi:[0,1,0]
v_lshl_add_u64 v[146:147],v[148:149],0,v[150:151]
v_mfma_f32_16x16x32_bf16 v[140:143],v[84:87],v[36:39],v[140:143]
ds_read_u16 v153,v153
v_fma_mix_f32 v157,v157,v123,0 op_sel_hi:[0,1,0]
v_fma_mix_f32 v159,v159,v123,0 op_sel:[0,1,0] op_sel_hi:[0,1,0]
v_mfma_f32_16x16x32_bf16 v[132:135],v[84:87],v[32:35],v[132:135]
v_lshlrev_b32_sdwa v128,1,v128 dst_sel:DWORD dst_unused:UNUSED_PAD src0_sel:DWORD src1_sel:WORD_1
v_lshlrev_b32_sdwa v130,1,v130 dst_sel:DWORD dst_unused:UNUSED_PAD src0_sel:DWORD src1_sel:WORD_1
v_mfma_f32_16x16x32_bf16 v[208:211],v[88:91],v[144:147],v[208:211]
s_mov_b32 s86,0x1234567
s_waitcnt vmcnt(0)
s_barrier
s_setprio 2
ds_read_u16_d16_hi v155,v155
v_lshlrev_b32_sdwa v129,1,v129 dst_sel:DWORD dst_unused:UNUSED_PAD src0_sel:DWORD src1_sel:WORD_1
v_lshlrev_b32_sdwa v131,1,v131 dst_sel:DWORD dst_unused:UNUSED_PAD src0_sel:DWORD src1_sel:WORD_1
v_mfma_f32_16x16x32_bf16 v[216:219],v[92:95],v[144:147],v[216:219]
v_mfma_f32_16x16x32_bf16 v[224:227],v[96:99],v[144:147],v[224:227]
v_lshlrev_b32_sdwa v132,1,v132 dst_sel:DWORD dst_unused:UNUSED_PAD src0_sel:DWORD src1_sel:WORD_1
v_mfma_f32_16x16x32_bf16 v[232:235],v[100:103],v[144:147],v[232:235]
s_waitcnt lgkmcnt(0)
ds_read_u16 v128,v128
v_lshl_add_u64 v[152:153],v[152:153],0,v[154:155]
v_cvt_pk_bf16_f32 v155,v157,v159
v_lshlrev_b32_sdwa v134,1,v134 dst_sel:DWORD dst_unused:UNUSED_PAD src0_sel:DWORD src1_sel:WORD_1
v_cvt_pk_bf16_f32 v154,v156,v158
v_mfma_f32_16x16x32_bf16 v[240:243],v[104:107],v[144:147],v[240:243]
ds_read_u16_d16_hi v130,v130
v_mfma_f32_16x16x32_bf16 v[248:251],v[108:111],v[144:147],v[248:251]
ds_read_u16 v129,v129
v_lshlrev_b32_sdwa v133,1,v133 dst_sel:DWORD dst_unused:UNUSED_PAD src0_sel:DWORD src1_sel:WORD_1
v_mfma_f32_16x16x32_bf16 v[212:215],v[88:91],v[152:155],v[212:215]
v_lshlrev_b32_sdwa v135,1,v135 dst_sel:DWORD dst_unused:UNUSED_PAD src0_sel:DWORD src1_sel:WORD_1
v_mfma_f32_16x16x32_bf16 v[220:223],v[92:95],v[152:155],v[220:223]
ds_read_u16_d16_hi v131,v131
v_lshlrev_b32_sdwa v136,1,v136 dst_sel:DWORD dst_unused:UNUSED_PAD src0_sel:DWORD src1_sel:WORD_1
v_mfma_f32_16x16x32_bf16 v[228:231],v[96:99],v[152:155],v[228:231]
ds_read_b64_tr_b16 v[88:89],v6 offset:0
ds_read_b64_tr_b16 v[90:91],v6 offset:1536
v_lshlrev_b32_sdwa v138,1,v138 dst_sel:DWORD dst_unused:UNUSED_PAD src0_sel:DWORD src1_sel:WORD_1
v_mfma_f32_16x16x32_bf16 v[236:239],v[100:103],v[152:155],v[236:239]
ds_read_u16 v132,v132
v_mfma_f32_16x16x32_bf16 v[244:247],v[104:107],v[152:155],v[244:247]
ds_read_u16_d16_hi v134,v134
v_lshlrev_b32_sdwa v137,1,v137 dst_sel:DWORD dst_unused:UNUSED_PAD src0_sel:DWORD src1_sel:WORD_1
v_lshlrev_b32_sdwa v139,1,v139 dst_sel:DWORD dst_unused:UNUSED_PAD src0_sel:DWORD src1_sel:WORD_1
v_mfma_f32_16x16x32_bf16 v[252:255],v[108:111],v[152:155],v[252:255]
ds_read_b64_tr_b16 v[92:93],v6 offset:8
ds_read_b64_tr_b16 v[94:95],v6 offset:1544
v_lshlrev_b32_sdwa v140,1,v140 dst_sel:DWORD dst_unused:UNUSED_PAD src0_sel:DWORD src1_sel:WORD_1
v_mfma_f32_16x16x32_bf16 v[144:147],v[64:67],v[40:43],v[112:115]
ds_read_u16 v133,v133
v_mfma_f32_16x16x32_bf16 v[152:155],v[64:67],v[44:47],v[112:115]
ds_read_b64_tr_b16 v[96:97],v6 offset:64
v_lshlrev_b32_sdwa v142,1,v142 dst_sel:DWORD dst_unused:UNUSED_PAD src0_sel:DWORD src1_sel:WORD_1
v_lshlrev_b32_sdwa v141,1,v141 dst_sel:DWORD dst_unused:UNUSED_PAD src0_sel:DWORD src1_sel:WORD_1
v_lshlrev_b32_sdwa v143,1,v143 dst_sel:DWORD dst_unused:UNUSED_PAD src0_sel:DWORD src1_sel:WORD_1
v_mfma_f32_16x16x32_bf16 v[156:159],v[68:71],v[44:47],v[116:119]
ds_read_u16_d16_hi v135,v135
v_mfma_f32_16x16x32_bf16 v[148:151],v[68:71],v[40:43],v[116:119]
ds_read_u16 v136,v136
ds_read_b64_tr_b16 v[98:99],v6 offset:1600
v_mfma_f32_16x16x32_bf16 v[144:147],v[72:75],v[48:51],v[144:147]
s_waitcnt lgkmcnt(11)
ds_read_u16_d16_hi v138,v138
v_lshl_add_u64 v[128:129],v[128:129],0,v[130:131]
v_mfma_f32_16x16x32_bf16 v[152:155],v[72:75],v[52:55],v[152:155]
s_cbranch_vccz RB41_%=
s_add_u32 m0,s58,12320
s_mul_i32 s69,s59,7
s_add_u32 s69,s69,s97
buffer_load_dwordx4 v2,s[44:47],s69 offen lds
s_add_u32 m0,m0,1024
s_add_u32 s69,s69,s53
buffer_load_dwordx4 v3,s[44:47],s69 offen lds
s_add_u32 m0,m0,1024
s_add_u32 s69,s69,s53
buffer_load_dwordx4 v2,s[44:47],s69 offen lds
s_add_u32 m0,m0,s50
s_add_u32 s69,s69,s53
buffer_load_dwordx4 v3,s[44:47],s69 offen lds
RB41_%=:
ds_read_b64_tr_b16 v[100:101],v6 offset:72
ds_read_b64_tr_b16 v[102:103],v6 offset:1608
v_mfma_f32_16x16x32_bf16 v[156:159],v[76:79],v[52:55],v[156:159]
ds_read_u16 v137,v137
v_mfma_f32_16x16x32_bf16 v[148:151],v[76:79],v[48:51],v[148:151]
s_waitcnt lgkmcnt(6)
ds_read_u16_d16_hi v139,v139
v_lshl_add_u64 v[130:131],v[132:133],0,v[134:135]
v_mfma_f32_16x16x32_bf16 v[144:147],v[80:83],v[56:59],v[144:147]
ds_read_b64_tr_b16 v[104:105],v6 offset:128
ds_read_b64_tr_b16 v[106:107],v6 offset:1664
v_mfma_f32_16x16x32_bf16 v[152:155],v[80:83],v[60:63],v[152:155]
ds_read_u16 v140,v140
v_mfma_f32_16x16x32_bf16 v[156:159],v[84:87],v[60:63],v[156:159]
ds_read_u16_d16_hi v142,v142
s_nop 1
v_lshlrev_b32_sdwa v144,1,v144 dst_sel:DWORD dst_unused:UNUSED_PAD src0_sel:DWORD src1_sel:WORD_1
v_mfma_f32_16x16x32_bf16 v[148:151],v[84:87],v[56:59],v[148:151]
s_cbranch_vccz RB42_%=
s_add_u32 m0,m0,1024
s_add_u32 s69,s69,s53
buffer_load_dwordx4 v2,s[44:47],s69 offen lds
s_add_u32 m0,m0,1024
s_add_u32 s69,s69,s53
buffer_load_dwordx4 v3,s[44:47],s69 offen lds
s_add_u32 m0,m0,s50
s_add_u32 s69,s69,s53
buffer_load_dwordx4 v2,s[44:47],s69 offen lds
s_add_u32 m0,m0,1024
s_add_u32 s69,s69,s53
buffer_load_dwordx4 v3,s[44:47],s69 offen lds
RB42_%=:
ds_read_b64_tr_b16 v[108:109],v6 offset:136
ds_read_b64_tr_b16 v[110:111],v6 offset:1672
v_lshlrev_b32_sdwa v146,1,v146 dst_sel:DWORD dst_unused:UNUSED_PAD src0_sel:DWORD src1_sel:WORD_1
v_mfma_f32_16x16x32_bf16 v[160:163],v[88:91],v[128:131],v[160:163]
ds_read_b64_tr_b16 v[64:65],v4 offset:0
ds_read_u16 v141,v141
v_lshlrev_b32_sdwa v145,1,v145 dst_sel:DWORD dst_unused:UNUSED_PAD src0_sel:DWORD src1_sel:WORD_1
v_mfma_f32_16x16x32_bf16 v[168:171],v[92:95],v[128:131],v[168:171]
s_waitcnt lgkmcnt(13)
ds_read_u16_d16_hi v143,v143
v_lshlrev_b32_sdwa v147,1,v147 dst_sel:DWORD dst_unused:UNUSED_PAD src0_sel:DWORD src1_sel:WORD_1
v_mfma_f32_16x16x32_bf16 v[176:179],v[96:99],v[128:131],v[176:179]
s_waitcnt lgkmcnt(11)
ds_read_b64_tr_b16 v[66:67],v4 offset:2048
v_lshlrev_b32_sdwa v148,1,v148 dst_sel:DWORD dst_unused:UNUSED_PAD src0_sel:DWORD src1_sel:WORD_1
v_mfma_f32_16x16x32_bf16 v[184:187],v[100:103],v[128:131],v[184:187]
s_waitcnt lgkmcnt(8)
ds_read_u16 v144,v144
v_lshl_add_u64 v[136:137],v[136:137],0,v[138:139]
v_lshlrev_b32_sdwa v150,1,v150 dst_sel:DWORD dst_unused:UNUSED_PAD src0_sel:DWORD src1_sel:WORD_1
v_mfma_f32_16x16x32_bf16 v[192:195],v[104:107],v[128:131],v[192:195]
s_waitcnt lgkmcnt(2)
s_cbranch_vccz RB43_%=
s_add_u32 m0,m0,1024
s_add_u32 s69,s69,s53
buffer_load_dwordx4 v2,s[44:47],s69 offen lds
s_add_u32 m0,m0,s50
s_add_u32 s69,s69,s53
buffer_load_dwordx4 v3,s[44:47],s69 offen lds
s_add_u32 m0,m0,1024
s_add_u32 s69,s69,s53
buffer_load_dwordx4 v2,s[44:47],s69 offen lds
s_add_u32 m0,m0,1024
s_add_u32 s69,s69,s53
buffer_load_dwordx4 v3,s[44:47],s69 offen lds
RB43_%=:
ds_read_u16_d16_hi v146,v146
v_lshl_add_u64 v[138:139],v[140:141],0,v[142:143]
v_lshlrev_b32_sdwa v149,1,v149 dst_sel:DWORD dst_unused:UNUSED_PAD src0_sel:DWORD src1_sel:WORD_1
v_mfma_f32_16x16x32_bf16 v[200:203],v[108:111],v[128:131],v[200:203]
ds_read_b128 v[112:115],v7 offset:768
v_mfma_f32_16x16x32_bf16 v[164:167],v[88:91],v[136:139],v[164:167]
ds_read_u16 v145,v145
v_lshlrev_b32_sdwa v151,1,v151 dst_sel:DWORD dst_unused:UNUSED_PAD src0_sel:DWORD src1_sel:WORD_1
v_mfma_f32_16x16x32_bf16 v[172:175],v[92:95],v[136:139],v[172:175]
ds_read_b64_tr_b16 v[68:69],v5 offset:0
v_lshlrev_b32_sdwa v152,1,v152 dst_sel:DWORD dst_unused:UNUSED_PAD src0_sel:DWORD src1_sel:WORD_1
v_mfma_f32_16x16x32_bf16 v[180:183],v[96:99],v[136:139],v[180:183]
ds_read_b64_tr_b16 v[70:71],v5 offset:2048
ds_read_b128 v[116:119],v7 offset:800
v_lshlrev_b32_sdwa v154,1,v154 dst_sel:DWORD dst_unused:UNUSED_PAD src0_sel:DWORD src1_sel:WORD_1
v_mfma_f32_16x16x32_bf16 v[188:191],v[100:103],v[136:139],v[188:191]
ds_read_u16_d16_hi v147,v147
ds_read_b64_tr_b16 v[72:73],v4 offset:4096
v_lshlrev_b32_sdwa v153,1,v153 dst_sel:DWORD dst_unused:UNUSED_PAD src0_sel:DWORD src1_sel:WORD_1
v_mfma_f32_16x16x32_bf16 v[196:199],v[104:107],v[136:139],v[196:199]
ds_read_u16 v148,v148
ds_read_u16_d16_hi v150,v150
v_lshlrev_b32_sdwa v155,1,v155 dst_sel:DWORD dst_unused:UNUSED_PAD src0_sel:DWORD src1_sel:WORD_1
v_mfma_f32_16x16x32_bf16 v[204:207],v[108:111],v[136:139],v[204:207]
s_waitcnt lgkmcnt(8)
ds_read_b64_tr_b16 v[74:75],v4 offset:6144
ds_read_b64_tr_b16 v[76:77],v5 offset:4096
v_mfma_f32_16x16x32_bf16 v[128:131],v[64:67],v[16:19],v[112:115]
ds_read_u16 v149,v149
v_lshlrev_b32_sdwa v156,1,v156 dst_sel:DWORD dst_unused:UNUSED_PAD src0_sel:DWORD src1_sel:WORD_1
v_lshlrev_b32_sdwa v158,1,v158 dst_sel:DWORD dst_unused:UNUSED_PAD src0_sel:DWORD src1_sel:WORD_1
v_mfma_f32_16x16x32_bf16 v[136:139],v[64:67],v[20:23],v[112:115]
s_waitcnt lgkmcnt(7)
ds_read_b64_tr_b16 v[78:79],v5 offset:6144
ds_read_b64_tr_b16 v[80:81],v4 offset:8192
v_lshlrev_b32_sdwa v157,1,v157 dst_sel:DWORD dst_unused:UNUSED_PAD src0_sel:DWORD src1_sel:WORD_1
v_mfma_f32_16x16x32_bf16 v[140:143],v[68:71],v[20:23],v[116:119]
ds_read_u16_d16_hi v151,v151
v_mfma_f32_16x16x32_bf16 v[132:135],v[68:71],v[16:19],v[116:119]
s_waitcnt lgkmcnt(5)
ds_read_b64_tr_b16 v[82:83],v4 offset:10240
v_mfma_f32_16x16x32_bf16 v[128:131],v[72:75],v[24:27],v[128:131]
ds_read_b64_tr_b16 v[84:85],v5 offset:8192
ds_read_b64_tr_b16 v[86:87],v5 offset:10240
v_mfma_f32_16x16x32_bf16 v[136:139],v[72:75],v[28:31],v[136:139]
s_waitcnt lgkmcnt(5)
ds_read_u16 v152,v152
ds_read_u16_d16_hi v154,v154
v_lshlrev_b32_sdwa v159,1,v159 dst_sel:DWORD dst_unused:UNUSED_PAD src0_sel:DWORD src1_sel:WORD_1
v_mfma_f32_16x16x32_bf16 v[140:143],v[76:79],v[28:31],v[140:143]
ds_read_u16 v153,v153
v_mfma_f32_16x16x32_bf16 v[132:135],v[76:79],v[24:27],v[132:135]
s_waitcnt lgkmcnt(5)
ds_read_u16_d16_hi v155,v155
v_lshl_add_u64 v[144:145],v[144:145],0,v[146:147]
v_mfma_f32_16x16x32_bf16 v[128:131],v[80:83],v[32:35],v[128:131]
v_mfma_f32_16x16x32_bf16 v[136:139],v[80:83],v[36:39],v[136:139]
s_waitcnt lgkmcnt(4)
ds_read_u16 v156,v156
v_lshl_add_u64 v[146:147],v[148:149],0,v[150:151]
v_mfma_f32_16x16x32_bf16 v[140:143],v[84:87],v[36:39],v[140:143]
ds_read_u16_d16_hi v158,v158
v_mfma_f32_16x16x32_bf16 v[132:135],v[84:87],v[32:35],v[132:135]
ds_read_u16 v157,v157
v_lshlrev_b32_sdwa v128,1,v128 dst_sel:DWORD dst_unused:UNUSED_PAD src0_sel:DWORD src1_sel:WORD_1
v_lshlrev_b32_sdwa v130,1,v130 dst_sel:DWORD dst_unused:UNUSED_PAD src0_sel:DWORD src1_sel:WORD_1
v_mfma_f32_16x16x32_bf16 v[208:211],v[88:91],v[144:147],v[208:211]
ds_read_u16_d16_hi v159,v159
v_lshlrev_b32_sdwa v129,1,v129 dst_sel:DWORD dst_unused:UNUSED_PAD src0_sel:DWORD src1_sel:WORD_1
v_lshlrev_b32_sdwa v131,1,v131 dst_sel:DWORD dst_unused:UNUSED_PAD src0_sel:DWORD src1_sel:WORD_1
v_mfma_f32_16x16x32_bf16 v[216:219],v[92:95],v[144:147],v[216:219]
v_mfma_f32_16x16x32_bf16 v[224:227],v[96:99],v[144:147],v[224:227]
v_lshlrev_b32_sdwa v132,1,v132 dst_sel:DWORD dst_unused:UNUSED_PAD src0_sel:DWORD src1_sel:WORD_1
v_mfma_f32_16x16x32_bf16 v[232:235],v[100:103],v[144:147],v[232:235]
s_waitcnt lgkmcnt(4)
ds_read_u16 v128,v128
v_lshl_add_u64 v[152:153],v[152:153],0,v[154:155]
v_lshlrev_b32_sdwa v134,1,v134 dst_sel:DWORD dst_unused:UNUSED_PAD src0_sel:DWORD src1_sel:WORD_1
v_mfma_f32_16x16x32_bf16 v[240:243],v[104:107],v[144:147],v[240:243]
s_waitcnt lgkmcnt(1)
ds_read_u16_d16_hi v130,v130
v_lshl_add_u64 v[154:155],v[156:157],0,v[158:159]
v_mfma_f32_16x16x32_bf16 v[248:251],v[108:111],v[144:147],v[248:251]
ds_read_u16 v129,v129
v_lshlrev_b32_sdwa v133,1,v133 dst_sel:DWORD dst_unused:UNUSED_PAD src0_sel:DWORD src1_sel:WORD_1
s_mov_b32 s86,0xbe801d5a
v_mfma_f32_16x16x32_bf16 v[212:215],v[88:91],v[152:155],v[212:215]
v_lshlrev_b32_sdwa v135,1,v135 dst_sel:DWORD dst_unused:UNUSED_PAD src0_sel:DWORD src1_sel:WORD_1
v_mfma_f32_16x16x32_bf16 v[220:223],v[92:95],v[152:155],v[220:223]
ds_read_u16_d16_hi v131,v131
v_lshlrev_b32_sdwa v136,1,v136 dst_sel:DWORD dst_unused:UNUSED_PAD src0_sel:DWORD src1_sel:WORD_1
v_mfma_f32_16x16x32_bf16 v[228:231],v[96:99],v[152:155],v[228:231]
ds_read_b64_tr_b16 v[88:89],v6 offset:6160
ds_read_b64_tr_b16 v[90:91],v6 offset:7696
v_lshlrev_b32_sdwa v138,1,v138 dst_sel:DWORD dst_unused:UNUSED_PAD src0_sel:DWORD src1_sel:WORD_1
v_mfma_f32_16x16x32_bf16 v[236:239],v[100:103],v[152:155],v[236:239]
ds_read_u16 v132,v132
v_mfma_f32_16x16x32_bf16 v[244:247],v[104:107],v[152:155],v[244:247]
ds_read_u16_d16_hi v134,v134
v_lshlrev_b32_sdwa v137,1,v137 dst_sel:DWORD dst_unused:UNUSED_PAD src0_sel:DWORD src1_sel:WORD_1
v_lshlrev_b32_sdwa v139,1,v139 dst_sel:DWORD dst_unused:UNUSED_PAD src0_sel:DWORD src1_sel:WORD_1
v_mfma_f32_16x16x32_bf16 v[252:255],v[108:111],v[152:155],v[252:255]
ds_read_b64_tr_b16 v[92:93],v6 offset:6168
ds_read_b64_tr_b16 v[94:95],v6 offset:7704
v_lshlrev_b32_sdwa v140,1,v140 dst_sel:DWORD dst_unused:UNUSED_PAD src0_sel:DWORD src1_sel:WORD_1
v_mfma_f32_16x16x32_bf16 v[144:147],v[64:67],v[40:43],v[112:115]
ds_read_u16 v133,v133
v_mfma_f32_16x16x32_bf16 v[152:155],v[64:67],v[44:47],v[112:115]
ds_read_b64_tr_b16 v[96:97],v6 offset:6224
v_lshlrev_b32_sdwa v142,1,v142 dst_sel:DWORD dst_unused:UNUSED_PAD src0_sel:DWORD src1_sel:WORD_1
v_lshlrev_b32_sdwa v141,1,v141 dst_sel:DWORD dst_unused:UNUSED_PAD src0_sel:DWORD src1_sel:WORD_1
v_lshlrev_b32_sdwa v143,1,v143 dst_sel:DWORD dst_unused:UNUSED_PAD src0_sel:DWORD src1_sel:WORD_1
v_mfma_f32_16x16x32_bf16 v[156:159],v[68:71],v[44:47],v[116:119]
ds_read_u16_d16_hi v135,v135
v_mfma_f32_16x16x32_bf16 v[148:151],v[68:71],v[40:43],v[116:119]
ds_read_u16 v136,v136
ds_read_b64_tr_b16 v[98:99],v6 offset:7760
v_mfma_f32_16x16x32_bf16 v[144:147],v[72:75],v[48:51],v[144:147]
s_waitcnt lgkmcnt(11)
ds_read_u16_d16_hi v138,v138
v_lshl_add_u64 v[128:129],v[128:129],0,v[130:131]
v_mfma_f32_16x16x32_bf16 v[152:155],v[72:75],v[52:55],v[152:155]
ds_read_b64_tr_b16 v[100:101],v6 offset:6232
ds_read_b64_tr_b16 v[102:103],v6 offset:7768
v_mfma_f32_16x16x32_bf16 v[156:159],v[76:79],v[52:55],v[156:159]
ds_read_u16 v137,v137
v_mfma_f32_16x16x32_bf16 v[148:151],v[76:79],v[48:51],v[148:151]
s_waitcnt lgkmcnt(6)
ds_read_u16_d16_hi v139,v139
v_lshl_add_u64 v[130:131],v[132:133],0,v[134:135]
v_mfma_f32_16x16x32_bf16 v[144:147],v[80:83],v[56:59],v[144:147]
ds_read_b64_tr_b16 v[104:105],v6 offset:6288
ds_read_b64_tr_b16 v[106:107],v6 offset:7824
v_mfma_f32_16x16x32_bf16 v[152:155],v[80:83],v[60:63],v[152:155]
ds_read_u16 v140,v140
v_mfma_f32_16x16x32_bf16 v[156:159],v[84:87],v[60:63],v[156:159]
ds_read_u16_d16_hi v142,v142
s_nop 1
v_lshlrev_b32_sdwa v144,1,v144 dst_sel:DWORD dst_unused:UNUSED_PAD src0_sel:DWORD src1_sel:WORD_1
v_mfma_f32_16x16x32_bf16 v[148:151],v[84:87],v[56:59],v[148:151]
ds_read_b64_tr_b16 v[108:109],v6 offset:6296
ds_read_b64_tr_b16 v[110:111],v6 offset:7832
v_lshlrev_b32_sdwa v146,1,v146 dst_sel:DWORD dst_unused:UNUSED_PAD src0_sel:DWORD src1_sel:WORD_1
v_mfma_f32_16x16x32_bf16 v[160:163],v[88:91],v[128:131],v[160:163]
ds_read_b64_tr_b16 v[64:65],v4 offset:512
ds_read_u16 v141,v141
v_lshlrev_b32_sdwa v145,1,v145 dst_sel:DWORD dst_unused:UNUSED_PAD src0_sel:DWORD src1_sel:WORD_1
v_mfma_f32_16x16x32_bf16 v[168:171],v[92:95],v[128:131],v[168:171]
s_waitcnt lgkmcnt(13)
ds_read_u16_d16_hi v143,v143
v_lshlrev_b32_sdwa v147,1,v147 dst_sel:DWORD dst_unused:UNUSED_PAD src0_sel:DWORD src1_sel:WORD_1
v_mfma_f32_16x16x32_bf16 v[176:179],v[96:99],v[128:131],v[176:179]
s_waitcnt lgkmcnt(11)
ds_read_b64_tr_b16 v[66:67],v4 offset:2560
v_lshlrev_b32_sdwa v148,1,v148 dst_sel:DWORD dst_unused:UNUSED_PAD src0_sel:DWORD src1_sel:WORD_1
v_mfma_f32_16x16x32_bf16 v[184:187],v[100:103],v[128:131],v[184:187]
s_waitcnt lgkmcnt(8)
ds_read_u16 v144,v144
v_lshl_add_u64 v[136:137],v[136:137],0,v[138:139]
v_lshlrev_b32_sdwa v150,1,v150 dst_sel:DWORD dst_unused:UNUSED_PAD src0_sel:DWORD src1_sel:WORD_1
v_mfma_f32_16x16x32_bf16 v[192:195],v[104:107],v[128:131],v[192:195]
s_waitcnt lgkmcnt(2)
ds_read_u16_d16_hi v146,v146
v_lshl_add_u64 v[138:139],v[140:141],0,v[142:143]
v_lshlrev_b32_sdwa v149,1,v149 dst_sel:DWORD dst_unused:UNUSED_PAD src0_sel:DWORD src1_sel:WORD_1
v_mfma_f32_16x16x32_bf16 v[200:203],v[108:111],v[128:131],v[200:203]
ds_read_b128 v[112:115],v7 offset:896
v_mfma_f32_16x16x32_bf16 v[164:167],v[88:91],v[136:139],v[164:167]
ds_read_u16 v145,v145
v_lshlrev_b32_sdwa v151,1,v151 dst_sel:DWORD dst_unused:UNUSED_PAD src0_sel:DWORD src1_sel:WORD_1
v_mfma_f32_16x16x32_bf16 v[172:175],v[92:95],v[136:139],v[172:175]
ds_read_b64_tr_b16 v[68:69],v5 offset:512
v_lshlrev_b32_sdwa v152,1,v152 dst_sel:DWORD dst_unused:UNUSED_PAD src0_sel:DWORD src1_sel:WORD_1
v_mfma_f32_16x16x32_bf16 v[180:183],v[96:99],v[136:139],v[180:183]
ds_read_b64_tr_b16 v[70:71],v5 offset:2560
ds_read_b128 v[116:119],v7 offset:928
v_lshlrev_b32_sdwa v154,1,v154 dst_sel:DWORD dst_unused:UNUSED_PAD src0_sel:DWORD src1_sel:WORD_1
v_mfma_f32_16x16x32_bf16 v[188:191],v[100:103],v[136:139],v[188:191]
ds_read_u16_d16_hi v147,v147
ds_read_b64_tr_b16 v[72:73],v4 offset:4608
v_lshlrev_b32_sdwa v153,1,v153 dst_sel:DWORD dst_unused:UNUSED_PAD src0_sel:DWORD src1_sel:WORD_1
v_mfma_f32_16x16x32_bf16 v[196:199],v[104:107],v[136:139],v[196:199]
ds_read_u16 v148,v148
ds_read_u16_d16_hi v150,v150
v_lshlrev_b32_sdwa v155,1,v155 dst_sel:DWORD dst_unused:UNUSED_PAD src0_sel:DWORD src1_sel:WORD_1
v_mfma_f32_16x16x32_bf16 v[204:207],v[108:111],v[136:139],v[204:207]
s_waitcnt lgkmcnt(8)
ds_read_b64_tr_b16 v[74:75],v4 offset:6656
ds_read_b64_tr_b16 v[76:77],v5 offset:4608
v_mfma_f32_16x16x32_bf16 v[128:131],v[64:67],v[16:19],v[112:115]
ds_read_u16 v149,v149
v_lshlrev_b32_sdwa v156,1,v156 dst_sel:DWORD dst_unused:UNUSED_PAD src0_sel:DWORD src1_sel:WORD_1
v_lshlrev_b32_sdwa v158,1,v158 dst_sel:DWORD dst_unused:UNUSED_PAD src0_sel:DWORD src1_sel:WORD_1
v_mfma_f32_16x16x32_bf16 v[136:139],v[64:67],v[20:23],v[112:115]
s_waitcnt lgkmcnt(7)
ds_read_b64_tr_b16 v[78:79],v5 offset:6656
ds_read_b64_tr_b16 v[80:81],v4 offset:8704
v_lshlrev_b32_sdwa v157,1,v157 dst_sel:DWORD dst_unused:UNUSED_PAD src0_sel:DWORD src1_sel:WORD_1
v_mfma_f32_16x16x32_bf16 v[140:143],v[68:71],v[20:23],v[116:119]
ds_read_u16_d16_hi v151,v151
v_mfma_f32_16x16x32_bf16 v[132:135],v[68:71],v[16:19],v[116:119]
s_waitcnt lgkmcnt(5)
ds_read_b64_tr_b16 v[82:83],v4 offset:10752
v_mfma_f32_16x16x32_bf16 v[128:131],v[72:75],v[24:27],v[128:131]
ds_read_b64_tr_b16 v[84:85],v5 offset:8704
ds_read_b64_tr_b16 v[86:87],v5 offset:10752
v_mfma_f32_16x16x32_bf16 v[136:139],v[72:75],v[28:31],v[136:139]
s_waitcnt lgkmcnt(5)
ds_read_u16 v152,v152
ds_read_u16_d16_hi v154,v154
v_lshlrev_b32_sdwa v159,1,v159 dst_sel:DWORD dst_unused:UNUSED_PAD src0_sel:DWORD src1_sel:WORD_1
v_mfma_f32_16x16x32_bf16 v[140:143],v[76:79],v[28:31],v[140:143]
ds_read_u16 v153,v153
v_mfma_f32_16x16x32_bf16 v[132:135],v[76:79],v[24:27],v[132:135]
s_waitcnt lgkmcnt(5)
ds_read_u16_d16_hi v155,v155
v_lshl_add_u64 v[144:145],v[144:145],0,v[146:147]
v_mfma_f32_16x16x32_bf16 v[128:131],v[80:83],v[32:35],v[128:131]
v_mfma_f32_16x16x32_bf16 v[136:139],v[80:83],v[36:39],v[136:139]
s_waitcnt lgkmcnt(4)
ds_read_u16 v156,v156
v_lshl_add_u64 v[146:147],v[148:149],0,v[150:151]
v_mfma_f32_16x16x32_bf16 v[140:143],v[84:87],v[36:39],v[140:143]
ds_read_u16_d16_hi v158,v158
v_mfma_f32_16x16x32_bf16 v[132:135],v[84:87],v[32:35],v[132:135]
s_nop 0
ds_read_u16 v157,v157
v_lshlrev_b32_sdwa v128,1,v128 dst_sel:DWORD dst_unused:UNUSED_PAD src0_sel:DWORD src1_sel:WORD_1
v_lshlrev_b32_sdwa v130,1,v130 dst_sel:DWORD dst_unused:UNUSED_PAD src0_sel:DWORD src1_sel:WORD_1
v_mfma_f32_16x16x32_bf16 v[208:211],v[88:91],v[144:147],v[208:211]
s_mov_b32 s86,0x1234567
s_waitcnt vmcnt(0)
s_barrier
s_setprio 3
ds_read_u16_d16_hi v159,v159
ds_read_u16 v128,v128
v_lshlrev_b32_sdwa v129,1,v129 dst_sel:DWORD dst_unused:UNUSED_PAD src0_sel:DWORD src1_sel:WORD_1
v_lshlrev_b32_sdwa v131,1,v131 dst_sel:DWORD dst_unused:UNUSED_PAD src0_sel:DWORD src1_sel:WORD_1
v_mfma_f32_16x16x32_bf16 v[216:219],v[92:95],v[144:147],v[216:219]
v_fma_mixlo_f16 v120,v140,s99,0
v_fma_mixlo_f16 v122,v141,s99,0
v_mfma_f32_16x16x32_bf16 v[224:227],v[96:99],v[144:147],v[224:227]
ds_read_u16_d16_hi v130,v130
v_lshlrev_b32_sdwa v132,1,v132 dst_sel:DWORD dst_unused:UNUSED_PAD src0_sel:DWORD src1_sel:WORD_1
v_lshlrev_b32_sdwa v134,1,v134 dst_sel:DWORD dst_unused:UNUSED_PAD src0_sel:DWORD src1_sel:WORD_1
v_mfma_f32_16x16x32_bf16 v[232:235],v[100:103],v[144:147],v[232:235]
s_waitcnt lgkmcnt(6)
v_lshl_add_u64 v[152:153],v[152:153],0,v[154:155]
v_mfma_f32_16x16x32_bf16 v[240:243],v[104:107],v[144:147],v[240:243]
s_waitcnt lgkmcnt(2)
v_lshl_add_u64 v[154:155],v[156:157],0,v[158:159]
v_fma_mixhi_f16 v120,v142,s99,0
v_mfma_f32_16x16x32_bf16 v[248:251],v[108:111],v[144:147],v[248:251]
ds_read_u16 v129,v129
v_fma_mixhi_f16 v122,v143,s99,0
v_mfma_f32_16x16x32_bf16 v[212:215],v[88:91],v[152:155],v[212:215]
ds_read_u16_d16_hi v131,v131
ds_read_u16 v132,v132
v_lshlrev_b32_sdwa v133,1,v133 dst_sel:DWORD dst_unused:UNUSED_PAD src0_sel:DWORD src1_sel:WORD_1
v_pk_fma_f16 v121,v120,v120,s54
v_lshlrev_b32_sdwa v135,1,v135 dst_sel:DWORD dst_unused:UNUSED_PAD src0_sel:DWORD src1_sel:WORD_1
v_mfma_f32_16x16x32_bf16 v[220:223],v[92:95],v[152:155],v[220:223]
v_mfma_f32_16x16x32_bf16 v[228:231],v[96:99],v[152:155],v[228:231]
ds_read_b64_tr_b16 v[88:89],v6 offset:12320
v_pk_fma_f16 v123,v122,v122,s54
v_pk_mul_f16 v121,v120,v121
v_lshlrev_b32_sdwa v136,1,v136 dst_sel:DWORD dst_unused:UNUSED_PAD src0_sel:DWORD src1_sel:WORD_1
v_pk_mul_f16 v123,v122,v123
v_mfma_f32_16x16x32_bf16 v[236:239],v[100:103],v[152:155],v[236:239]
ds_read_b64_tr_b16 v[90:91],v6 offset:13856
v_lshlrev_b32_sdwa v138,1,v138 dst_sel:DWORD dst_unused:UNUSED_PAD src0_sel:DWORD src1_sel:WORD_1
v_mfma_f32_16x16x32_bf16 v[244:247],v[104:107],v[152:155],v[244:247]
ds_read_u16_d16_hi v134,v134
v_exp_f16 v120,v121
v_mfma_f32_16x16x32_bf16 v[252:255],v[108:111],v[152:155],v[252:255]
ds_read_b64_tr_b16 v[92:93],v6 offset:12328
v_lshlrev_b32_sdwa v137,1,v137 dst_sel:DWORD dst_unused:UNUSED_PAD src0_sel:DWORD src1_sel:WORD_1
v_mfma_f32_16x16x32_bf16 v[144:147],v[64:67],v[40:43],v[112:115]
ds_read_b64_tr_b16 v[94:95],v6 offset:13864
v_lshlrev_b32_sdwa v139,1,v139 dst_sel:DWORD dst_unused:UNUSED_PAD src0_sel:DWORD src1_sel:WORD_1
v_exp_f16 v122,v123
v_mfma_f32_16x16x32_bf16 v[152:155],v[64:67],v[44:47],v[112:115]
ds_read_u16 v133,v133
ds_read_b64_tr_b16 v[96:97],v6 offset:12384
v_mfma_f32_16x16x32_bf16 v[156:159],v[68:71],v[44:47],v[116:119]
ds_read_u16_d16_hi v135,v135
v_exp_f16_sdwa v122,v123 dst_sel:WORD_1 dst_unused:UNUSED_PRESERVE src0_sel:WORD_1
v_exp_f16_sdwa v120,v121 dst_sel:WORD_1 dst_unused:UNUSED_PRESERVE src0_sel:WORD_1
v_mfma_f32_16x16x32_bf16 v[148:151],v[68:71],v[40:43],v[116:119]
ds_read_b64_tr_b16 v[98:99],v6 offset:13920
v_pk_fma_f16 v120,v120,s55,s55
v_pk_fma_f16 v122,v122,s55,s55
v_mfma_f32_16x16x32_bf16 v[144:147],v[72:75],v[48:51],v[144:147]
v_rcp_f16 v121,v120
v_mfma_f32_16x16x32_bf16 v[152:155],v[72:75],v[52:55],v[152:155]
ds_read_b64_tr_b16 v[100:101],v6 offset:12392
v_rcp_f16 v123,v122
v_mfma_f32_16x16x32_bf16 v[156:159],v[76:79],v[52:55],v[156:159]
ds_read_u16 v136,v136
v_rcp_f16_sdwa v121,v120 dst_sel:WORD_1 dst_unused:UNUSED_PRESERVE src0_sel:WORD_1
v_mfma_f32_16x16x32_bf16 v[148:151],v[76:79],v[48:51],v[148:151]
ds_read_b64_tr_b16 v[102:103],v6 offset:13928
ds_read_b64_tr_b16 v[104:105],v6 offset:12448
v_rcp_f16_sdwa v123,v122 dst_sel:WORD_1 dst_unused:UNUSED_PRESERVE src0_sel:WORD_1
v_fma_mix_f32 v140,v140,v121,0 op_sel_hi:[0,1,0]
v_mfma_f32_16x16x32_bf16 v[144:147],v[80:83],v[56:59],v[144:147]
ds_read_u16_d16_hi v138,v138
v_mfma_f32_16x16x32_bf16 v[152:155],v[80:83],v[60:63],v[152:155]
s_waitcnt lgkmcnt(6)
ds_read_b64_tr_b16 v[106:107],v6 offset:13984
ds_read_b64_tr_b16 v[108:109],v6 offset:12456
v_lshl_add_u64 v[128:129],v[128:129],0,v[130:131]
v_lshl_add_u64 v[130:131],v[132:133],0,v[134:135]
v_fma_mix_f32 v142,v142,v121,0 op_sel:[0,1,0] op_sel_hi:[0,1,0]
v_mfma_f32_16x16x32_bf16 v[156:159],v[84:87],v[60:63],v[156:159]
ds_read_u16 v137,v137
v_fma_mix_f32 v143,v143,v123,0 op_sel:[0,1,0] op_sel_hi:[0,1,0]
v_mfma_f32_16x16x32_bf16 v[148:151],v[84:87],v[56:59],v[148:151]
ds_read_b64_tr_b16 v[110:111],v6 offset:13992
ds_read_b64_tr_b16 v[64:65],v4 offset:12320
v_fma_mix_f32 v141,v141,v123,0 op_sel_hi:[0,1,0]
v_lshlrev_b32_sdwa v144,1,v144 dst_sel:DWORD dst_unused:UNUSED_PAD src0_sel:DWORD src1_sel:WORD_1
v_lshlrev_b32_sdwa v146,1,v146 dst_sel:DWORD dst_unused:UNUSED_PAD src0_sel:DWORD src1_sel:WORD_1
v_mfma_f32_16x16x32_bf16 v[160:163],v[88:91],v[128:131],v[160:163]
ds_read_u16_d16_hi v139,v139
v_lshlrev_b32_sdwa v145,1,v145 dst_sel:DWORD dst_unused:UNUSED_PAD src0_sel:DWORD src1_sel:WORD_1
v_lshlrev_b32_sdwa v147,1,v147 dst_sel:DWORD dst_unused:UNUSED_PAD src0_sel:DWORD src1_sel:WORD_1
v_fma_mixlo_f16 v122,v157,s99,0
v_mfma_f32_16x16x32_bf16 v[168:171],v[92:95],v[128:131],v[168:171]
s_waitcnt lgkmcnt(11)
ds_read_b64_tr_b16 v[66:67],v4 offset:14368
ds_read_b128 v[112:115],v7 offset:1024
v_fma_mixlo_f16 v120,v156,s99,0
v_mfma_f32_16x16x32_bf16 v[176:179],v[96:99],v[128:131],v[176:179]
s_waitcnt lgkmcnt(10)
ds_read_u16 v144,v144
v_lshlrev_b32_sdwa v148,1,v148 dst_sel:DWORD dst_unused:UNUSED_PAD src0_sel:DWORD src1_sel:WORD_1
v_lshlrev_b32_sdwa v150,1,v150 dst_sel:DWORD dst_unused:UNUSED_PAD src0_sel:DWORD src1_sel:WORD_1
v_mfma_f32_16x16x32_bf16 v[184:187],v[100:103],v[128:131],v[184:187]
s_waitcnt lgkmcnt(3)
ds_read_b64_tr_b16 v[68:69],v5 offset:12320
ds_read_b64_tr_b16 v[70:71],v5 offset:14368
v_lshl_add_u64 v[136:137],v[136:137],0,v[138:139]
v_fma_mixhi_f16 v120,v158,s99,0
v_mfma_f32_16x16x32_bf16 v[192:195],v[104:107],v[128:131],v[192:195]
ds_read_u16_d16_hi v146,v146
v_cvt_pk_bf16_f32 v138,v140,v142
v_cvt_pk_bf16_f32 v139,v141,v143
v_mfma_f32_16x16x32_bf16 v[200:203],v[108:111],v[128:131],v[200:203]
v_fma_mixhi_f16 v122,v159,s99,0
v_mfma_f32_16x16x32_bf16 v[164:167],v[88:91],v[136:139],v[164:167]
ds_read_b128 v[116:119],v7 offset:1056
ds_read_b64_tr_b16 v[72:73],v4 offset:16416
v_lshlrev_b32_sdwa v149,1,v149 dst_sel:DWORD dst_unused:UNUSED_PAD src0_sel:DWORD src1_sel:WORD_1
v_lshlrev_b32_sdwa v151,1,v151 dst_sel:DWORD dst_unused:UNUSED_PAD src0_sel:DWORD src1_sel:WORD_1
v_mfma_f32_16x16x32_bf16 v[172:175],v[92:95],v[136:139],v[172:175]
ds_read_b64_tr_b16 v[74:75],v4 offset:18464
ds_read_b64_tr_b16 v[76:77],v5 offset:16416
v_pk_fma_f16 v121,v120,v120,s54
v_pk_fma_f16 v123,v122,v122,s54
v_mfma_f32_16x16x32_bf16 v[180:183],v[96:99],v[136:139],v[180:183]
ds_read_u16 v145,v145
v_pk_mul_f16 v121,v120,v121
v_pk_mul_f16 v123,v122,v123
v_mfma_f32_16x16x32_bf16 v[188:191],v[100:103],v[136:139],v[188:191]
ds_read_u16_d16_hi v147,v147
v_lshlrev_b32_sdwa v152,1,v152 dst_sel:DWORD dst_unused:UNUSED_PAD src0_sel:DWORD src1_sel:WORD_1
v_lshlrev_b32_sdwa v154,1,v154 dst_sel:DWORD dst_unused:UNUSED_PAD src0_sel:DWORD src1_sel:WORD_1
v_mfma_f32_16x16x32_bf16 v[196:199],v[104:107],v[136:139],v[196:199]
v_exp_f16 v120,v121
v_mfma_f32_16x16x32_bf16 v[204:207],v[108:111],v[136:139],v[204:207]
s_waitcnt lgkmcnt(10)
ds_read_b64_tr_b16 v[78:79],v5 offset:18464
ds_read_b64_tr_b16 v[80:81],v4 offset:20512
v_exp_f16 v122,v123
v_mfma_f32_16x16x32_bf16 v[128:131],v[64:67],v[16:19],v[112:115]
v_lshlrev_b32_sdwa v153,1,v153 dst_sel:DWORD dst_unused:UNUSED_PAD src0_sel:DWORD src1_sel:WORD_1
v_lshlrev_b32_sdwa v155,1,v155 dst_sel:DWORD dst_unused:UNUSED_PAD src0_sel:DWORD src1_sel:WORD_1
v_mfma_f32_16x16x32_bf16 v[136:139],v[64:67],v[20:23],v[112:115]
s_waitcnt lgkmcnt(7)
ds_read_u16 v148,v148
ds_read_u16_d16_hi v150,v150
v_exp_f16_sdwa v120,v121 dst_sel:WORD_1 dst_unused:UNUSED_PRESERVE src0_sel:WORD_1
v_mfma_f32_16x16x32_bf16 v[140:143],v[68:71],v[20:23],v[116:119]
v_exp_f16_sdwa v122,v123 dst_sel:WORD_1 dst_unused:UNUSED_PRESERVE src0_sel:WORD_1
v_mfma_f32_16x16x32_bf16 v[132:135],v[68:71],v[16:19],v[116:119]
s_waitcnt lgkmcnt(7)
ds_read_b64_tr_b16 v[82:83],v4 offset:22560
v_pk_fma_f16 v120,v120,s55,s55
v_mfma_f32_16x16x32_bf16 v[128:131],v[72:75],v[24:27],v[128:131]
ds_read_b64_tr_b16 v[84:85],v5 offset:20512
ds_read_u16 v149,v149
v_pk_fma_f16 v122,v122,s55,s55
v_rcp_f16 v121,v120
v_mfma_f32_16x16x32_bf16 v[136:139],v[72:75],v[28:31],v[136:139]
s_waitcnt lgkmcnt(6)
ds_read_b64_tr_b16 v[86:87],v5 offset:22560
v_rcp_f16 v123,v122
v_mfma_f32_16x16x32_bf16 v[140:143],v[76:79],v[28:31],v[140:143]
ds_read_u16_d16_hi v151,v151
ds_read_u16 v152,v152
v_rcp_f16_sdwa v121,v120 dst_sel:WORD_1 dst_unused:UNUSED_PRESERVE src0_sel:WORD_1
v_mfma_f32_16x16x32_bf16 v[132:135],v[76:79],v[24:27],v[132:135]
s_waitcnt lgkmcnt(5)
v_rcp_f16_sdwa v123,v122 dst_sel:WORD_1 dst_unused:UNUSED_PRESERVE src0_sel:WORD_1
v_mfma_f32_16x16x32_bf16 v[128:131],v[80:83],v[32:35],v[128:131]
ds_read_u16_d16_hi v154,v154
v_fma_mix_f32 v156,v156,v121,0 op_sel_hi:[0,1,0]
v_fma_mix_f32 v158,v158,v121,0 op_sel:[0,1,0] op_sel_hi:[0,1,0]
v_mfma_f32_16x16x32_bf16 v[136:139],v[80:83],v[36:39],v[136:139]
s_waitcnt lgkmcnt(2)
v_lshl_add_u64 v[144:145],v[144:145],0,v[146:147]
v_lshl_add_u64 v[146:147],v[148:149],0,v[150:151]
v_mfma_f32_16x16x32_bf16 v[140:143],v[84:87],v[36:39],v[140:143]
ds_read_u16 v153,v153
v_fma_mix_f32 v157,v157,v123,0 op_sel_hi:[0,1,0]
v_fma_mix_f32 v159,v159,v123,0 op_sel:[0,1,0] op_sel_hi:[0,1,0]
v_mfma_f32_16x16x32_bf16 v[132:135],v[84:87],v[32:35],v[132:135]
v_lshlrev_b32_sdwa v128,1,v128 dst_sel:DWORD dst_unused:UNUSED_PAD src0_sel:DWORD src1_sel:WORD_1
v_lshlrev_b32_sdwa v130,1,v130 dst_sel:DWORD dst_unused:UNUSED_PAD src0_sel:DWORD src1_sel:WORD_1
v_mfma_f32_16x16x32_bf16 v[208:211],v[88:91],v[144:147],v[208:211]
ds_read_u16_d16_hi v155,v155
ds_read_u16 v128,v128
v_lshlrev_b32_sdwa v129,1,v129 dst_sel:DWORD dst_unused:UNUSED_PAD src0_sel:DWORD src1_sel:WORD_1
v_lshlrev_b32_sdwa v131,1,v131 dst_sel:DWORD dst_unused:UNUSED_PAD src0_sel:DWORD src1_sel:WORD_1
v_mfma_f32_16x16x32_bf16 v[216:219],v[92:95],v[144:147],v[216:219]
v_fma_mixlo_f16 v120,v140,s99,0
v_fma_mixlo_f16 v122,v141,s99,0
v_mfma_f32_16x16x32_bf16 v[224:227],v[96:99],v[144:147],v[224:227]
s_setprio 2
ds_read_u16_d16_hi v130,v130
v_lshlrev_b32_sdwa v132,1,v132 dst_sel:DWORD dst_unused:UNUSED_PAD src0_sel:DWORD src1_sel:WORD_1
v_lshlrev_b32_sdwa v134,1,v134 dst_sel:DWORD dst_unused:UNUSED_PAD src0_sel:DWORD src1_sel:WORD_1
v_mfma_f32_16x16x32_bf16 v[232:235],v[100:103],v[144:147],v[232:235]
s_waitcnt lgkmcnt(2)
v_lshl_add_u64 v[152:153],v[152:153],0,v[154:155]
v_cvt_pk_bf16_f32 v154,v156,v158
v_mfma_f32_16x16x32_bf16 v[240:243],v[104:107],v[144:147],v[240:243]
v_fma_mixhi_f16 v120,v142,s99,0
v_cvt_pk_bf16_f32 v155,v157,v159
v_mfma_f32_16x16x32_bf16 v[248:251],v[108:111],v[144:147],v[248:251]
ds_read_u16 v129,v129
v_fma_mixhi_f16 v122,v143,s99,0
v_mfma_f32_16x16x32_bf16 v[212:215],v[88:91],v[152:155],v[212:215]
ds_read_u16_d16_hi v131,v131
ds_read_u16 v132,v132
v_lshlrev_b32_sdwa v133,1,v133 dst_sel:DWORD dst_unused:UNUSED_PAD src0_sel:DWORD src1_sel:WORD_1
v_pk_fma_f16 v121,v120,v120,s54
v_lshlrev_b32_sdwa v135,1,v135 dst_sel:DWORD dst_unused:UNUSED_PAD src0_sel:DWORD src1_sel:WORD_1
v_mfma_f32_16x16x32_bf16 v[220:223],v[92:95],v[152:155],v[220:223]
v_mfma_f32_16x16x32_bf16 v[228:231],v[96:99],v[152:155],v[228:231]
ds_read_b64_tr_b16 v[88:89],v6 offset:18480
v_pk_fma_f16 v123,v122,v122,s54
v_pk_mul_f16 v121,v120,v121
v_lshlrev_b32_sdwa v136,1,v136 dst_sel:DWORD dst_unused:UNUSED_PAD src0_sel:DWORD src1_sel:WORD_1
v_pk_mul_f16 v123,v122,v123
v_mfma_f32_16x16x32_bf16 v[236:239],v[100:103],v[152:155],v[236:239]
ds_read_b64_tr_b16 v[90:91],v6 offset:20016
v_lshlrev_b32_sdwa v138,1,v138 dst_sel:DWORD dst_unused:UNUSED_PAD src0_sel:DWORD src1_sel:WORD_1
v_mfma_f32_16x16x32_bf16 v[244:247],v[104:107],v[152:155],v[244:247]
ds_read_u16_d16_hi v134,v134
v_exp_f16 v120,v121
v_mfma_f32_16x16x32_bf16 v[252:255],v[108:111],v[152:155],v[252:255]
ds_read_b64_tr_b16 v[92:93],v6 offset:18488
v_lshlrev_b32_sdwa v137,1,v137 dst_sel:DWORD dst_unused:UNUSED_PAD src0_sel:DWORD src1_sel:WORD_1
v_mfma_f32_16x16x32_bf16 v[144:147],v[64:67],v[40:43],v[112:115]
ds_read_b64_tr_b16 v[94:95],v6 offset:20024
v_lshlrev_b32_sdwa v139,1,v139 dst_sel:DWORD dst_unused:UNUSED_PAD src0_sel:DWORD src1_sel:WORD_1
v_exp_f16 v122,v123
v_mfma_f32_16x16x32_bf16 v[152:155],v[64:67],v[44:47],v[112:115]
ds_read_u16 v133,v133
ds_read_b64_tr_b16 v[96:97],v6 offset:18544
v_mfma_f32_16x16x32_bf16 v[156:159],v[68:71],v[44:47],v[116:119]
ds_read_u16_d16_hi v135,v135
v_exp_f16_sdwa v122,v123 dst_sel:WORD_1 dst_unused:UNUSED_PRESERVE src0_sel:WORD_1
v_exp_f16_sdwa v120,v121 dst_sel:WORD_1 dst_unused:UNUSED_PRESERVE src0_sel:WORD_1
v_mfma_f32_16x16x32_bf16 v[148:151],v[68:71],v[40:43],v[116:119]
ds_read_b64_tr_b16 v[98:99],v6 offset:20080
v_pk_fma_f16 v120,v120,s55,s55
v_pk_fma_f16 v122,v122,s55,s55
v_mfma_f32_16x16x32_bf16 v[144:147],v[72:75],v[48:51],v[144:147]
v_rcp_f16 v121,v120
v_mfma_f32_16x16x32_bf16 v[152:155],v[72:75],v[52:55],v[152:155]
ds_read_b64_tr_b16 v[100:101],v6 offset:18552
v_rcp_f16 v123,v122
v_mfma_f32_16x16x32_bf16 v[156:159],v[76:79],v[52:55],v[156:159]
ds_read_u16 v136,v136
v_rcp_f16_sdwa v121,v120 dst_sel:WORD_1 dst_unused:UNUSED_PRESERVE src0_sel:WORD_1
v_mfma_f32_16x16x32_bf16 v[148:151],v[76:79],v[48:51],v[148:151]
ds_read_b64_tr_b16 v[102:103],v6 offset:20088
ds_read_b64_tr_b16 v[104:105],v6 offset:18608
v_rcp_f16_sdwa v123,v122 dst_sel:WORD_1 dst_unused:UNUSED_PRESERVE src0_sel:WORD_1
v_fma_mix_f32 v140,v140,v121,0 op_sel_hi:[0,1,0]
v_mfma_f32_16x16x32_bf16 v[144:147],v[80:83],v[56:59],v[144:147]
ds_read_u16_d16_hi v138,v138
v_mfma_f32_16x16x32_bf16 v[152:155],v[80:83],v[60:63],v[152:155]
s_waitcnt lgkmcnt(6)
ds_read_b64_tr_b16 v[106:107],v6 offset:20144
ds_read_b64_tr_b16 v[108:109],v6 offset:18616
v_lshl_add_u64 v[128:129],v[128:129],0,v[130:131]
v_lshl_add_u64 v[130:131],v[132:133],0,v[134:135]
v_fma_mix_f32 v142,v142,v121,0 op_sel:[0,1,0] op_sel_hi:[0,1,0]
v_mfma_f32_16x16x32_bf16 v[156:159],v[84:87],v[60:63],v[156:159]
ds_read_u16 v137,v137
v_fma_mix_f32 v143,v143,v123,0 op_sel:[0,1,0] op_sel_hi:[0,1,0]
v_mfma_f32_16x16x32_bf16 v[148:151],v[84:87],v[56:59],v[148:151]
s_setprio 1
ds_read_b64_tr_b16 v[110:111],v6 offset:20152
ds_read_b128 v[64:67],v7 offset:1536
v_fma_mix_f32 v141,v141,v123,0 op_sel_hi:[0,1,0]
v_lshlrev_b32_sdwa v144,1,v144 dst_sel:DWORD dst_unused:UNUSED_PAD src0_sel:DWORD src1_sel:WORD_1
v_lshlrev_b32_sdwa v146,1,v146 dst_sel:DWORD dst_unused:UNUSED_PAD src0_sel:DWORD src1_sel:WORD_1
v_mfma_f32_16x16x32_bf16 v[160:163],v[88:91],v[128:131],v[160:163]
ds_read_u16_d16_hi v139,v139
ds_read_b128 v[68:71],v7 offset:1568
v_lshlrev_b32_sdwa v145,1,v145 dst_sel:DWORD dst_unused:UNUSED_PAD src0_sel:DWORD src1_sel:WORD_1
v_lshlrev_b32_sdwa v147,1,v147 dst_sel:DWORD dst_unused:UNUSED_PAD src0_sel:DWORD src1_sel:WORD_1
v_fma_mixlo_f16 v122,v157,s99,0
v_fma_mixlo_f16 v120,v156,s99,0
v_mfma_f32_16x16x32_bf16 v[168:171],v[92:95],v[128:131],v[168:171]
s_waitcnt lgkmcnt(12)
ds_read_u16 v144,v144
ds_read_b128 v[72:75],v7 offset:1664
v_lshlrev_b32_sdwa v148,1,v148 dst_sel:DWORD dst_unused:UNUSED_PAD src0_sel:DWORD src1_sel:WORD_1
v_lshlrev_b32_sdwa v150,1,v150 dst_sel:DWORD dst_unused:UNUSED_PAD src0_sel:DWORD src1_sel:WORD_1
s_waitcnt lgkmcnt(3)
v_lshl_add_u64 v[136:137],v[136:137],0,v[138:139]
v_fma_mixhi_f16 v120,v158,s99,0
v_mfma_f32_16x16x32_bf16 v[176:179],v[96:99],v[128:131],v[176:179]
ds_read_u16_d16_hi v146,v146
ds_read_b128 v[76:79],v7 offset:1696
v_cvt_pk_bf16_f32 v138,v140,v142
v_cvt_pk_bf16_f32 v139,v141,v143
v_fma_mixhi_f16 v122,v159,s99,0
v_lshlrev_b32_sdwa v149,1,v149 dst_sel:DWORD dst_unused:UNUSED_PAD src0_sel:DWORD src1_sel:WORD_1
v_mfma_f32_16x16x32_bf16 v[184:187],v[100:103],v[128:131],v[184:187]
ds_read_u16 v145,v145
ds_read_b128 v[80:83],v7 offset:1792
v_lshlrev_b32_sdwa v151,1,v151 dst_sel:DWORD dst_unused:UNUSED_PAD src0_sel:DWORD src1_sel:WORD_1
v_pk_fma_f16 v121,v120,v120,s54
v_pk_fma_f16 v123,v122,v122,s54
v_pk_mul_f16 v121,v120,v121
v_mfma_f32_16x16x32_bf16 v[192:195],v[104:107],v[128:131],v[192:195]
ds_read_u16_d16_hi v147,v147
ds_read_b128 v[84:87],v7 offset:1824
v_pk_mul_f16 v123,v122,v123
v_lshlrev_b32_sdwa v152,1,v152 dst_sel:DWORD dst_unused:UNUSED_PAD src0_sel:DWORD src1_sel:WORD_1
v_lshlrev_b32_sdwa v154,1,v154 dst_sel:DWORD dst_unused:UNUSED_PAD src0_sel:DWORD src1_sel:WORD_1
v_exp_f16 v120,v121
v_mfma_f32_16x16x32_bf16 v[200:203],v[108:111],v[128:131],v[200:203]
ds_read_u16 v148,v148
v_exp_f16 v122,v123
v_lshlrev_b32_sdwa v153,1,v153 dst_sel:DWORD dst_unused:UNUSED_PAD src0_sel:DWORD src1_sel:WORD_1
v_mfma_f32_16x16x32_bf16 v[164:167],v[88:91],v[136:139],v[164:167]
ds_read_u16_d16_hi v150,v150
v_lshlrev_b32_sdwa v155,1,v155 dst_sel:DWORD dst_unused:UNUSED_PAD src0_sel:DWORD src1_sel:WORD_1
v_exp_f16_sdwa v120,v121 dst_sel:WORD_1 dst_unused:UNUSED_PRESERVE src0_sel:WORD_1
v_exp_f16_sdwa v122,v123 dst_sel:WORD_1 dst_unused:UNUSED_PRESERVE src0_sel:WORD_1
v_mfma_f32_16x16x32_bf16 v[172:175],v[92:95],v[136:139],v[172:175]
s_setprio 0
ds_read_u16 v149,v149
v_pk_fma_f16 v120,v120,s55,s55
v_pk_fma_f16 v122,v122,s55,s55
v_rcp_f16 v121,v120
v_mfma_f32_16x16x32_bf16 v[180:183],v[96:99],v[136:139],v[180:183]
ds_read_u16_d16_hi v151,v151
v_rcp_f16 v123,v122
v_rcp_f16_sdwa v121,v120 dst_sel:WORD_1 dst_unused:UNUSED_PRESERVE src0_sel:WORD_1
v_mfma_f32_16x16x32_bf16 v[188:191],v[100:103],v[136:139],v[188:191]
s_nop 0
ds_read_u16 v152,v152
v_rcp_f16_sdwa v123,v122 dst_sel:WORD_1 dst_unused:UNUSED_PRESERVE src0_sel:WORD_1
v_fma_mix_f32 v156,v156,v121,0 op_sel_hi:[0,1,0]
v_mfma_f32_16x16x32_bf16 v[196:199],v[104:107],v[136:139],v[196:199]
ds_read_u16_d16_hi v154,v154
v_fma_mix_f32 v158,v158,v121,0 op_sel:[0,1,0] op_sel_hi:[0,1,0]
s_waitcnt lgkmcnt(7)
v_lshl_add_u64 v[144:145],v[144:145],0,v[146:147]
s_waitcnt lgkmcnt(2)
v_lshl_add_u64 v[146:147],v[148:149],0,v[150:151]
v_fma_mix_f32 v157,v157,v123,0 op_sel_hi:[0,1,0]
v_mfma_f32_16x16x32_bf16 v[204:207],v[108:111],v[136:139],v[204:207]
s_nop 0
ds_read_u16 v153,v153
v_fma_mix_f32 v159,v159,v123,0 op_sel:[0,1,0] op_sel_hi:[0,1,0]
ds_read_u16_d16_hi v155,v155
s_waitcnt lgkmcnt(0)
v_lshl_add_u64 v[152:153],v[152:153],0,v[154:155]
v_cvt_pk_bf16_f32 v154,v156,v158
v_cvt_pk_bf16_f32 v155,v157,v159
v_mfma_f32_16x16x32_bf16 v[208:211],v[88:91],v[144:147],v[208:211]
s_mov_b32 s86,0x1234567
s_barrier
s_setprio 0
v_xor_b32 v9,64,v10
v_mul_f32 v160,v160,v64
v_mul_f32 v161,v161,v65
v_mfma_f32_16x16x32_bf16 v[216:219],v[92:95],v[144:147],v[216:219]
v_mul_f32 v162,v162,v66
v_mul_f32 v163,v163,v67
v_mfma_f32_16x16x32_bf16 v[224:227],v[96:99],v[144:147],v[224:227]
v_mul_f32 v168,v168,v68
v_mul_f32 v169,v169,v69
v_mul_f32 v170,v170,v70
v_mfma_f32_16x16x32_bf16 v[232:235],v[100:103],v[144:147],v[232:235]
v_mul_f32 v171,v171,v71
v_cvt_pk_bf16_f32 v16,v160,v161
v_mfma_f32_16x16x32_bf16 v[240:243],v[104:107],v[144:147],v[240:243]
v_cvt_pk_bf16_f32 v17,v162,v163
v_cvt_pk_bf16_f32 v18,v168,v169
v_cvt_pk_bf16_f32 v19,v170,v171
v_mfma_f32_16x16x32_bf16 v[248:251],v[108:111],v[144:147],v[248:251]
v_mul_f32_dpp v176,v176,v72 quad_perm:[1,0,3,2] row_mask:0xf bank_mask:0xf
v_mul_f32_dpp v177,v177,v73 quad_perm:[1,0,3,2] row_mask:0xf bank_mask:0xf
v_mfma_f32_16x16x32_bf16 v[212:215],v[88:91],v[152:155],v[212:215]
v_mul_f32_dpp v178,v178,v74 quad_perm:[1,0,3,2] row_mask:0xf bank_mask:0xf
v_mul_f32_dpp v179,v179,v75 quad_perm:[1,0,3,2] row_mask:0xf bank_mask:0xf
v_mul_f32_dpp v184,v184,v76 quad_perm:[1,0,3,2] row_mask:0xf bank_mask:0xf
v_mfma_f32_16x16x32_bf16 v[220:223],v[92:95],v[152:155],v[220:223]
s_nop 0
v_mul_f32_dpp v185,v185,v77 quad_perm:[1,0,3,2] row_mask:0xf bank_mask:0xf
v_mul_f32_dpp v186,v186,v78 quad_perm:[1,0,3,2] row_mask:0xf bank_mask:0xf
v_mfma_f32_16x16x32_bf16 v[228:231],v[96:99],v[152:155],v[228:231]
v_mul_f32_dpp v187,v187,v79 quad_perm:[1,0,3,2] row_mask:0xf bank_mask:0xf
v_cvt_pk_bf16_f32 v24,v176,v177
v_cvt_pk_bf16_f32 v25,v178,v179
v_mfma_f32_16x16x32_bf16 v[236:239],v[100:103],v[152:155],v[236:239]
s_nop 0
v_cvt_pk_bf16_f32 v26,v184,v185
v_cvt_pk_bf16_f32 v27,v186,v187
v_mfma_f32_16x16x32_bf16 v[244:247],v[104:107],v[152:155],v[244:247]
s_mov_b64 exec,s[12:13]
v_swap_b32 v16,v24
s_mov_b32 s86,0xbe801d5a
v_swap_b32 v17,v25
v_swap_b32 v18,v26
v_swap_b32 v19,v27
s_mov_b64 exec,-1
v_mfma_f32_16x16x32_bf16 v[252:255],v[108:111],v[152:155],v[252:255]
s_add_u32 s65,s61,1572864
buffer_store_dwordx4 v[16:19],v10,s[40:43],s65 offen offset:0 sc1
v_mul_f32 v192,v192,v80
v_mul_f32 v193,v193,v81
v_mul_f32 v194,v194,v82
v_mul_f32 v195,v195,v83
v_mul_f32 v200,v200,v84
v_mul_f32 v201,v201,v85
v_mul_f32 v202,v202,v86
v_mul_f32 v203,v203,v87
v_cvt_pk_bf16_f32 v32,v192,v193
v_cvt_pk_bf16_f32 v33,v194,v195
v_cvt_pk_bf16_f32 v34,v200,v201
v_cvt_pk_bf16_f32 v35,v202,v203
v_mul_f32_dpp v164,v164,v64 quad_perm:[1,0,3,2] row_mask:0xf bank_mask:0xf
v_mul_f32_dpp v165,v165,v65 quad_perm:[1,0,3,2] row_mask:0xf bank_mask:0xf
v_mul_f32_dpp v166,v166,v66 quad_perm:[1,0,3,2] row_mask:0xf bank_mask:0xf
v_mul_f32_dpp v167,v167,v67 quad_perm:[1,0,3,2] row_mask:0xf bank_mask:0xf
v_mul_f32_dpp v172,v172,v68 quad_perm:[1,0,3,2] row_mask:0xf bank_mask:0xf
v_mul_f32_dpp v173,v173,v69 quad_perm:[1,0,3,2] row_mask:0xf bank_mask:0xf
v_mul_f32_dpp v174,v174,v70 quad_perm:[1,0,3,2] row_mask:0xf bank_mask:0xf
v_mul_f32_dpp v175,v175,v71 quad_perm:[1,0,3,2] row_mask:0xf bank_mask:0xf
v_cvt_pk_bf16_f32 v20,v164,v165
v_cvt_pk_bf16_f32 v21,v166,v167
v_cvt_pk_bf16_f32 v22,v172,v173
v_cvt_pk_bf16_f32 v23,v174,v175
s_mov_b64 exec,s[12:13]
v_swap_b32 v32,v20
v_swap_b32 v33,v21
v_swap_b32 v34,v22
v_swap_b32 v35,v23
s_mov_b64 exec,-1
s_add_u32 s65,s61,1572864
buffer_store_dwordx4 v[24:27],v9,s[40:43],s65 offen offset:384 sc1
v_mul_f32 v180,v180,v72
v_mul_f32 v181,v181,v73
v_mul_f32 v182,v182,v74
v_mul_f32 v183,v183,v75
v_mul_f32 v188,v188,v76
v_mul_f32 v189,v189,v77
v_mul_f32 v190,v190,v78
v_mul_f32 v191,v191,v79
v_cvt_pk_bf16_f32 v28,v180,v181
v_cvt_pk_bf16_f32 v29,v182,v183
v_cvt_pk_bf16_f32 v30,v188,v189
v_cvt_pk_bf16_f32 v31,v190,v191
v_mul_f32_dpp v196,v196,v80 quad_perm:[1,0,3,2] row_mask:0xf bank_mask:0xf
v_mul_f32_dpp v197,v197,v81 quad_perm:[1,0,3,2] row_mask:0xf bank_mask:0xf
v_mul_f32_dpp v198,v198,v82 quad_perm:[1,0,3,2] row_mask:0xf bank_mask:0xf
v_mul_f32_dpp v199,v199,v83 quad_perm:[1,0,3,2] row_mask:0xf bank_mask:0xf
v_mul_f32_dpp v204,v204,v84 quad_perm:[1,0,3,2] row_mask:0xf bank_mask:0xf
v_mul_f32_dpp v205,v205,v85 quad_perm:[1,0,3,2] row_mask:0xf bank_mask:0xf
v_mul_f32_dpp v206,v206,v86 quad_perm:[1,0,3,2] row_mask:0xf bank_mask:0xf
v_mul_f32_dpp v207,v207,v87 quad_perm:[1,0,3,2] row_mask:0xf bank_mask:0xf
v_cvt_pk_bf16_f32 v36,v196,v197
v_cvt_pk_bf16_f32 v37,v198,v199
v_cvt_pk_bf16_f32 v38,v204,v205
v_cvt_pk_bf16_f32 v39,v206,v207
s_mov_b64 exec,s[12:13]
v_swap_b32 v28,v36
v_swap_b32 v29,v37
v_swap_b32 v30,v38
v_swap_b32 v31,v39
s_mov_b64 exec,-1
s_add_u32 s65,s61,1572864
buffer_store_dwordx4 v[32:35],v10,s[40:43],s65 offen offset:128 sc1
v_mul_f32 v208,v208,v64
v_mul_f32 v209,v209,v65
v_mul_f32 v210,v210,v66
v_mul_f32 v211,v211,v67
v_mul_f32 v216,v216,v68
v_mul_f32 v217,v217,v69
v_mul_f32 v218,v218,v70
v_mul_f32 v219,v219,v71
v_cvt_pk_bf16_f32 v40,v208,v209
v_cvt_pk_bf16_f32 v41,v210,v211
v_cvt_pk_bf16_f32 v42,v216,v217
v_cvt_pk_bf16_f32 v43,v218,v219
v_mul_f32_dpp v224,v224,v72 quad_perm:[1,0,3,2] row_mask:0xf bank_mask:0xf
v_mul_f32_dpp v225,v225,v73 quad_perm:[1,0,3,2] row_mask:0xf bank_mask:0xf
v_mul_f32_dpp v226,v226,v74 quad_perm:[1,0,3,2] row_mask:0xf bank_mask:0xf
v_mul_f32_dpp v227,v227,v75 quad_perm:[1,0,3,2] row_mask:0xf bank_mask:0xf
v_mul_f32_dpp v232,v232,v76 quad_perm:[1,0,3,2] row_mask:0xf bank_mask:0xf
v_mul_f32_dpp v233,v233,v77 quad_perm:[1,0,3,2] row_mask:0xf bank_mask:0xf
v_mul_f32_dpp v234,v234,v78 quad_perm:[1,0,3,2] row_mask:0xf bank_mask:0xf
v_mul_f32_dpp v235,v235,v79 quad_perm:[1,0,3,2] row_mask:0xf bank_mask:0xf
v_cvt_pk_bf16_f32 v48,v224,v225
v_cvt_pk_bf16_f32 v49,v226,v227
v_cvt_pk_bf16_f32 v50,v232,v233
v_cvt_pk_bf16_f32 v51,v234,v235
s_add_u32 s65,s61,1572864
buffer_store_dwordx4 v[20:23],v9,s[40:43],s65 offen offset:512 sc1
s_mov_b64 exec,s[12:13]
v_swap_b32 v40,v48
v_swap_b32 v41,v49
v_swap_b32 v42,v50
v_swap_b32 v43,v51
s_mov_b64 exec,-1
v_mul_f32 v240,v240,v80
v_mul_f32 v241,v241,v81
v_mul_f32 v242,v242,v82
v_mul_f32 v243,v243,v83
v_mul_f32 v248,v248,v84
v_mul_f32 v249,v249,v85
v_mul_f32 v250,v250,v86
v_mul_f32 v251,v251,v87
v_cvt_pk_bf16_f32 v56,v240,v241
v_cvt_pk_bf16_f32 v57,v242,v243
v_cvt_pk_bf16_f32 v58,v248,v249
v_cvt_pk_bf16_f32 v59,v250,v251
v_mul_f32_dpp v212,v212,v64 quad_perm:[1,0,3,2] row_mask:0xf bank_mask:0xf
v_mul_f32_dpp v213,v213,v65 quad_perm:[1,0,3,2] row_mask:0xf bank_mask:0xf
v_mul_f32_dpp v214,v214,v66 quad_perm:[1,0,3,2] row_mask:0xf bank_mask:0xf
v_mul_f32_dpp v215,v215,v67 quad_perm:[1,0,3,2] row_mask:0xf bank_mask:0xf
v_mul_f32_dpp v220,v220,v68 quad_perm:[1,0,3,2] row_mask:0xf bank_mask:0xf
v_mul_f32_dpp v221,v221,v69 quad_perm:[1,0,3,2] row_mask:0xf bank_mask:0xf
v_mul_f32_dpp v222,v222,v70 quad_perm:[1,0,3,2] row_mask:0xf bank_mask:0xf
v_mul_f32_dpp v223,v223,v71 quad_perm:[1,0,3,2] row_mask:0xf bank_mask:0xf
v_cvt_pk_bf16_f32 v44,v212,v213
v_cvt_pk_bf16_f32 v45,v214,v215
s_add_u32 s65,s61,1572864
buffer_store_dwordx4 v[28:31],v10,s[40:43],s65 offen offset:256 sc1
v_cvt_pk_bf16_f32 v46,v220,v221
v_cvt_pk_bf16_f32 v47,v222,v223
s_mov_b64 exec,s[12:13]
v_swap_b32 v56,v44
v_swap_b32 v57,v45
v_swap_b32 v58,v46
v_swap_b32 v59,v47
s_mov_b64 exec,-1
v_mul_f32 v228,v228,v72
v_mul_f32 v229,v229,v73
v_mul_f32 v230,v230,v74
v_mul_f32 v231,v231,v75
v_mul_f32 v236,v236,v76
v_mul_f32 v237,v237,v77
v_mul_f32 v238,v238,v78
v_mul_f32 v239,v239,v79
v_cvt_pk_bf16_f32 v52,v228,v229
v_cvt_pk_bf16_f32 v53,v230,v231
v_cvt_pk_bf16_f32 v54,v236,v237
v_cvt_pk_bf16_f32 v55,v238,v239
v_mul_f32_dpp v244,v244,v80 quad_perm:[1,0,3,2] row_mask:0xf bank_mask:0xf
v_mul_f32_dpp v245,v245,v81 quad_perm:[1,0,3,2] row_mask:0xf bank_mask:0xf
v_mul_f32_dpp v246,v246,v82 quad_perm:[1,0,3,2] row_mask:0xf bank_mask:0xf
v_mul_f32_dpp v247,v247,v83 quad_perm:[1,0,3,2] row_mask:0xf bank_mask:0xf
v_mul_f32_dpp v252,v252,v84 quad_perm:[1,0,3,2] row_mask:0xf bank_mask:0xf
v_mul_f32_dpp v253,v253,v85 quad_perm:[1,0,3,2] row_mask:0xf bank_mask:0xf
v_mul_f32_dpp v254,v254,v86 quad_perm:[1,0,3,2] row_mask:0xf bank_mask:0xf
v_mul_f32_dpp v255,v255,v87 quad_perm:[1,0,3,2] row_mask:0xf bank_mask:0xf
s_add_u32 s65,s61,1572864
buffer_store_dwordx4 v[36:39],v9,s[40:43],s65 offen offset:640 sc1
v_cvt_pk_bf16_f32 v60,v244,v245
v_cvt_pk_bf16_f32 v61,v246,v247
v_cvt_pk_bf16_f32 v62,v252,v253
v_cvt_pk_bf16_f32 v63,v254,v255
s_mov_b64 exec,s[12:13]
v_swap_b32 v52,v60
v_swap_b32 v53,v61
v_swap_b32 v54,v62
v_swap_b32 v55,v63
s_mov_b64 exec,-1
s_add_u32 s4,s10,8
s_cmp_ge_u32 s4,20
s_cselect_b32 s7,20,0
s_sub_u32 s4,s4,s7
s_mul_i32 s4,s4,3488
s_add_u32 s5,s10,10
s_cmp_ge_u32 s5,20
s_cselect_b32 s7,20,0
s_sub_u32 s5,s5,s7
s_mul_i32 s5,s5,3488
s_add_u32 s6,s10,12
s_cmp_ge_u32 s6,20
s_cselect_b32 s7,20,0
s_sub_u32 s6,s6,s7
s_mul_i32 s6,s6,3488
v_add_u32 v112,s4,v15
v_add_u32 v116,s5,v15
s_mov_b64 exec,s[8:9]
v_add_u32 v112,s5,v15
v_add_u32 v116,s6,v15
s_mov_b64 exec,-1
v_xor_b32 v113,16,v112
v_xor_b32 v117,16,v116
v_xor_b32 v114,32,v112
v_xor_b32 v118,32,v116
v_xor_b32 v115,48,v112
v_xor_b32 v119,48,v116
s_mov_b32 s86,0x1234567
s_barrier
s_setprio 3
ds_read_b128 v[136:139],v116 offset:0
ds_read_b128 v[88:91],v11 offset:768
ds_read_b128 v[140:143],v116 offset:3488
ds_read_b128 v[128:131],v112 offset:0
ds_read_b128 v[132:135],v112 offset:3488
ds_read_b128 v[92:95],v12 offset:0
ds_read_b128 v[152:155],v117 offset:0
ds_read_b128 v[96:99],v11 offset:1920
ds_read_b128 v[156:159],v117 offset:3488
ds_read_b128 v[144:147],v113 offset:0
ds_read_b128 v[148:151],v113 offset:3488
ds_read_b128 v[100:103],v12 offset:1152
s_waitcnt lgkmcnt(10)
v_mfma_f32_16x16x32_bf16 v[160:163],v[88:91],v[136:139],0
s_waitcnt lgkmcnt(9)
v_mfma_f32_16x16x32_bf16 v[184:187],v[88:91],v[140:143],0
s_waitcnt lgkmcnt(6)
ds_read_b128 v[140:143],v116 offset:3552
ds_read_b128 v[88:91],v11 offset:5376
v_smfmac_f32_16x16x64_bf16 v[160:163],v[92:95],v[128:135],v13
ds_read_b128 v[128:131],v112 offset:64
v_smfmac_f32_16x16x64_bf16 v[184:187],v[92:95],v[132:139],v13
s_waitcnt lgkmcnt(7)
s_add_u32 s65,s61,1671168
buffer_store_dwordx4 v[40:43],v10,s[40:43],s65 offen offset:0 sc1
ds_read_b128 v[136:139],v116 offset:64
ds_read_b128 v[132:135],v112 offset:3552
v_mfma_f32_16x16x32_bf16 v[164:167],v[96:99],v[152:155],0
s_waitcnt lgkmcnt(8)
ds_read_b128 v[92:95],v12 offset:4608
v_add_f32 v120,v160,v161
v_mul_f32 v124,v160,v160
v_fmac_f32 v124,v161,v161
v_mfma_f32_16x16x32_bf16 v[188:191],v[96:99],v[156:159],0
s_waitcnt lgkmcnt(6)
ds_read_b128 v[156:159],v117 offset:3552
ds_read_b128 v[96:99],v11 offset:6528
v_add_f32 v121,v162,v163
v_mul_f32 v125,v162,v162
v_fmac_f32 v125,v163,v163
v_smfmac_f32_16x16x64_bf16 v[164:167],v[100:103],v[144:151],v13
ds_read_b128 v[144:147],v113 offset:64
v_add_f32 v122,v184,v185
v_mul_f32 v126,v184,v184
v_fmac_f32 v126,v185,v185
v_smfmac_f32_16x16x64_bf16 v[188:191],v[100:103],v[148:155],v13
s_waitcnt lgkmcnt(5)
ds_read_b128 v[152:155],v117 offset:64
ds_read_b128 v[148:151],v113 offset:3552
v_add_f32 v123,v186,v187
v_mul_f32 v127,v186,v186
v_fmac_f32 v127,v187,v187
v_mfma_f32_16x16x32_bf16 v[168:171],v[88:91],v[136:139],0
ds_read_b128 v[100:103],v12 offset:5760
v_add_f32 v120,v120,v164
v_fmac_f32 v124,v164,v164
v_add_f32 v120,v120,v165
v_fmac_f32 v124,v165,v165
v_mfma_f32_16x16x32_bf16 v[192:195],v[88:91],v[140:143],0
s_waitcnt lgkmcnt(6)
s_add_u32 s65,s61,1671168
buffer_store_dwordx4 v[48:51],v9,s[40:43],s65 offen offset:384 sc1
ds_read_b128 v[140:143],v116 offset:3616
ds_read_b128 v[88:91],v11 offset:9984
v_add_f32 v121,v121,v166
v_fmac_f32 v125,v166,v166
v_add_f32 v121,v121,v167
v_fmac_f32 v125,v167,v167
v_smfmac_f32_16x16x64_bf16 v[168:171],v[92:95],v[128:135],v13
ds_read_b128 v[128:131],v112 offset:128
v_add_f32 v122,v122,v188
v_fmac_f32 v126,v188,v188
v_add_f32 v122,v122,v189
v_fmac_f32 v126,v189,v189
v_smfmac_f32_16x16x64_bf16 v[192:195],v[92:95],v[132:139],v13
s_waitcnt lgkmcnt(5)
ds_read_b128 v[136:139],v116 offset:128
ds_read_b128 v[132:135],v112 offset:3616
v_add_f32 v123,v123,v190
v_fmac_f32 v127,v190,v190
v_add_f32 v123,v123,v191
v_fmac_f32 v127,v191,v191
v_mfma_f32_16x16x32_bf16 v[172:175],v[96:99],v[152:155],0
ds_read_b128 v[92:95],v12 offset:9216
v_add_f32 v120,v120,v168
v_fmac_f32 v124,v168,v168
v_add_f32 v120,v120,v169
v_fmac_f32 v124,v169,v169
v_mfma_f32_16x16x32_bf16 v[196:199],v[96:99],v[156:159],0
s_waitcnt lgkmcnt(6)
ds_read_b128 v[156:159],v117 offset:3616
ds_read_b128 v[96:99],v11 offset:11136
v_add_f32 v121,v121,v170
v_fmac_f32 v125,v170,v170
v_add_f32 v121,v121,v171
v_fmac_f32 v125,v171,v171
v_smfmac_f32_16x16x64_bf16 v[172:175],v[100:103],v[144:151],v13
s_add_u32 s65,s61,1671168
buffer_store_dwordx4 v[56:59],v10,s[40:43],s65 offen offset:128 sc1
ds_read_b128 v[144:147],v113 offset:128
v_add_f32 v122,v122,v192
v_fmac_f32 v126,v192,v192
v_add_f32 v122,v122,v193
v_fmac_f32 v126,v193,v193
v_smfmac_f32_16x16x64_bf16 v[196:199],v[100:103],v[148:155],v13
s_waitcnt lgkmcnt(5)
ds_read_b128 v[152:155],v117 offset:128
ds_read_b128 v[148:151],v113 offset:3616
v_add_f32 v123,v123,v194
v_fmac_f32 v127,v194,v194
v_add_f32 v123,v123,v195
v_fmac_f32 v127,v195,v195
v_mfma_f32_16x16x32_bf16 v[176:179],v[88:91],v[136:139],0
ds_read_b128 v[100:103],v12 offset:10368
v_add_f32 v120,v120,v172
v_fmac_f32 v124,v172,v172
v_add_f32 v120,v120,v173
v_fmac_f32 v124,v173,v173
v_mfma_f32_16x16x32_bf16 v[200:203],v[88:91],v[140:143],0
s_waitcnt lgkmcnt(6)
ds_read_b128 v[140:143],v118 offset:3488
ds_read_b128 v[88:91],v11 offset:3072
v_add_f32 v121,v121,v174
v_fmac_f32 v125,v174,v174
v_add_f32 v121,v121,v175
v_fmac_f32 v125,v175,v175
v_smfmac_f32_16x16x64_bf16 v[176:179],v[92:95],v[128:135],v13
s_add_u32 s65,s61,1671168
buffer_store_dwordx4 v[44:47],v9,s[40:43],s65 offen offset:512 sc1
ds_read_b128 v[128:131],v114 offset:0
v_add_f32 v122,v122,v196
v_fmac_f32 v126,v196,v196
v_add_f32 v122,v122,v197
v_fmac_f32 v126,v197,v197
v_smfmac_f32_16x16x64_bf16 v[200:203],v[92:95],v[132:139],v13
s_waitcnt lgkmcnt(5)
ds_read_b128 v[136:139],v118 offset:0
ds_read_b128 v[132:135],v114 offset:3488
v_add_f32 v123,v123,v198
v_fmac_f32 v127,v198,v198
v_add_f32 v123,v123,v199
v_fmac_f32 v127,v199,v199
v_mfma_f32_16x16x32_bf16 v[180:183],v[96:99],v[152:155],0
ds_read_b128 v[92:95],v12 offset:2304
v_add_f32 v120,v120,v176
v_fmac_f32 v124,v176,v176
v_add_f32 v120,v120,v177
v_fmac_f32 v124,v177,v177
v_mfma_f32_16x16x32_bf16 v[204:207],v[96:99],v[156:159],0
s_waitcnt lgkmcnt(6)
ds_read_b128 v[156:159],v119 offset:3488
ds_read_b128 v[96:99],v11 offset:4224
v_add_f32 v121,v121,v178
v_fmac_f32 v125,v178,v178
v_add_f32 v121,v121,v179
v_fmac_f32 v125,v179,v179
v_smfmac_f32_16x16x64_bf16 v[180:183],v[100:103],v[144:151],v13
ds_read_b128 v[144:147],v115 offset:0
v_add_f32 v122,v122,v200
v_fmac_f32 v126,v200,v200
v_add_f32 v122,v122,v201
v_fmac_f32 v126,v201,v201
v_smfmac_f32_16x16x64_bf16 v[204:207],v[100:103],v[148:155],v13
s_waitcnt lgkmcnt(5)
s_add_u32 s65,s61,1671168
buffer_store_dwordx4 v[52:55],v10,s[40:43],s65 offen offset:256 sc1
ds_read_b128 v[152:155],v119 offset:0
ds_read_b128 v[148:151],v115 offset:3488
v_add_f32 v123,v123,v202
v_fmac_f32 v127,v202,v202
v_add_f32 v123,v123,v203
v_fmac_f32 v127,v203,v203
v_mfma_f32_16x16x32_bf16 v[208:211],v[88:91],v[136:139],0
ds_read_b128 v[100:103],v12 offset:3456
v_add_f32 v120,v120,v180
v_fmac_f32 v124,v180,v180
v_add_f32 v120,v120,v181
v_fmac_f32 v124,v181,v181
v_mfma_f32_16x16x32_bf16 v[232:235],v[88:91],v[140:143],0
s_waitcnt lgkmcnt(6)
ds_read_b128 v[140:143],v118 offset:3552
ds_read_b128 v[88:91],v11 offset:7680
v_add_f32 v121,v121,v182
v_fmac_f32 v125,v182,v182
v_add_f32 v121,v121,v183
v_fmac_f32 v125,v183,v183
v_smfmac_f32_16x16x64_bf16 v[208:211],v[92:95],v[128:135],v13
ds_read_b128 v[128:131],v114 offset:64
v_add_f32 v122,v122,v204
v_fmac_f32 v126,v204,v204
v_add_f32 v122,v122,v205
v_fmac_f32 v126,v205,v205
v_smfmac_f32_16x16x64_bf16 v[232:235],v[92:95],v[132:139],v13
s_waitcnt lgkmcnt(5)
s_add_u32 s65,s61,1671168
buffer_store_dwordx4 v[60:63],v9,s[40:43],s65 offen offset:640 sc1
ds_read_b128 v[136:139],v118 offset:64
ds_read_b128 v[132:135],v114 offset:3552
v_add_f32 v123,v123,v206
v_fmac_f32 v127,v206,v206
v_add_f32 v123,v123,v207
v_fmac_f32 v127,v207,v207
v_mfma_f32_16x16x32_bf16 v[212:215],v[96:99],v[152:155],0
ds_read_b128 v[92:95],v12 offset:6912
v_add_f32 v120,v120,v208
v_fmac_f32 v124,v208,v208
v_add_f32 v120,v120,v209
v_fmac_f32 v124,v209,v209
v_mfma_f32_16x16x32_bf16 v[236:239],v[96:99],v[156:159],0
s_waitcnt lgkmcnt(6)
s_setprio 2
ds_read_b128 v[156:159],v119 offset:3552
ds_read_b128 v[96:99],v11 offset:8832
v_add_f32 v121,v121,v210
v_fmac_f32 v125,v210,v210
v_add_f32 v121,v121,v211
v_fmac_f32 v125,v211,v211
v_smfmac_f32_16x16x64_bf16 v[212:215],v[100:103],v[144:151],v13
ds_read_b128 v[144:147],v115 offset:64
v_add_f32 v122,v122,v232
v_fmac_f32 v126,v232,v232
v_add_f32 v122,v122,v233
v_fmac_f32 v126,v233,v233
v_smfmac_f32_16x16x64_bf16 v[236:239],v[100:103],v[148:155],v13
s_waitcnt lgkmcnt(5)
ds_read_b128 v[152:155],v119 offset:64
ds_read_b128 v[148:151],v115 offset:3552
v_add_f32 v123,v123,v234
v_fmac_f32 v127,v234,v234
v_add_f32 v123,v123,v235
v_fmac_f32 v127,v235,v235
v_mfma_f32_16x16x32_bf16 v[216:219],v[88:91],v[136:139],0
ds_read_b128 v[100:103],v12 offset:8064
v_add_f32 v120,v120,v212
v_fmac_f32 v124,v212,v212
v_add_f32 v120,v120,v213
v_fmac_f32 v124,v213,v213
v_add_f32 v121,v121,v214
v_mfma_f32_16x16x32_bf16 v[240:243],v[88:91],v[140:143],0
s_waitcnt lgkmcnt(6)
ds_read_b128 v[140:143],v118 offset:3616
ds_read_b128 v[88:91],v11 offset:12288
v_fmac_f32 v125,v214,v214
v_add_f32 v121,v121,v215
v_fmac_f32 v125,v215,v215
v_add_f32 v122,v122,v236
v_smfmac_f32_16x16x64_bf16 v[216:219],v[92:95],v[128:135],v13
ds_read_b128 v[128:131],v114 offset:128
v_fmac_f32 v126,v236,v236
v_add_f32 v122,v122,v237
v_fmac_f32 v126,v237,v237
v_add_f32 v123,v123,v238
v_fmac_f32 v127,v238,v238
v_smfmac_f32_16x16x64_bf16 v[240:243],v[92:95],v[132:139],v13
s_waitcnt lgkmcnt(5)
ds_read_b128 v[136:139],v118 offset:128
ds_read_b128 v[132:135],v114 offset:3616
v_add_f32 v123,v123,v239
v_fmac_f32 v127,v239,v239
v_add_u32 v112,v112,v8
v_add_u32 v116,v116,v8
v_mfma_f32_16x16x32_bf16 v[220:223],v[96:99],v[152:155],0
ds_read_b128 v[92:95],v12 offset:11520
ds_read_b128 v[104:107],v14 offset:0
v_add_f32 v120,v120,v216
v_fmac_f32 v124,v216,v216
v_add_f32 v120,v120,v217
v_fmac_f32 v124,v217,v217
v_mfma_f32_16x16x32_bf16 v[244:247],v[96:99],v[156:159],0
s_waitcnt lgkmcnt(7)
ds_read_b128 v[108:111],v7 offset:3584
ds_read_b128 v[156:159],v119 offset:3616
v_add_f32 v121,v121,v218
v_fmac_f32 v125,v218,v218
v_add_f32 v121,v121,v219
v_fmac_f32 v125,v219,v219
v_smfmac_f32_16x16x64_bf16 v[220:223],v[100:103],v[144:151],v13
ds_read_b128 v[96:99],v11 offset:13440
ds_read_b128 v[144:147],v115 offset:128
v_add_f32 v122,v122,v240
v_fmac_f32 v126,v240,v240
v_add_f32 v122,v122,v241
v_fmac_f32 v126,v241,v241
v_smfmac_f32_16x16x64_bf16 v[244:247],v[100:103],v[148:155],v13
s_waitcnt lgkmcnt(7)
ds_read_b128 v[152:155],v119 offset:128
ds_read_b128 v[148:151],v115 offset:3616
v_add_f32 v123,v123,v242
v_fmac_f32 v127,v242,v242
v_add_f32 v123,v123,v243
v_fmac_f32 v127,v243,v243
v_mfma_f32_16x16x32_bf16 v[224:227],v[88:91],v[136:139],0
ds_read_b128 v[100:103],v12 offset:12672
v_add_f32 v120,v120,v220
v_fmac_f32 v124,v220,v220
v_add_f32 v120,v120,v221
v_fmac_f32 v124,v221,v221
v_mfma_f32_16x16x32_bf16 v[248:251],v[88:91],v[140:143],0
s_waitcnt lgkmcnt(8)
ds_read_b128 v[140:143],v116 offset:192
ds_read_b128 v[88:91],v112 offset:3616
v_add_f32 v121,v121,v222
v_fmac_f32 v125,v222,v222
v_add_f32 v121,v121,v223
v_fmac_f32 v125,v223,v223
v_smfmac_f32_16x16x64_bf16 v[224:227],v[92:95],v[128:135],v13
s_nop 0
ds_read_b128 v[128:131],v112 offset:3488
v_add_f32 v122,v122,v244
v_fmac_f32 v126,v244,v244
v_add_f32 v122,v122,v245
v_fmac_f32 v126,v245,v245
v_smfmac_f32_16x16x64_bf16 v[248:251],v[92:95],v[132:139],v13
s_waitcnt lgkmcnt(5)
ds_read_b128 v[136:139],v116 offset:0
ds_read_b128 v[132:135],v112 offset:3680
v_add_f32 v123,v123,v246
v_fmac_f32 v127,v246,v246
v_add_f32 v123,v123,v247
v_fmac_f32 v127,v247,v247
v_mfma_f32_16x16x32_bf16 v[228:231],v[96:99],v[152:155],0
ds_read_b128 v[92:95],v112 offset:3808
v_add_f32 v120,v120,v224
v_fmac_f32 v124,v224,v224
v_add_f32 v120,v120,v225
v_fmac_f32 v124,v225,v225
v_mfma_f32_16x16x32_bf16 v[252:255],v[96:99],v[156:159],0
s_waitcnt lgkmcnt(6)
s_setprio 1
ds_read_b128 v[156:159],v116 offset:256
ds_read_b128 v[96:99],v116 offset:128
v_add_f32 v121,v121,v226
v_fmac_f32 v125,v226,v226
v_add_f32 v121,v121,v227
v_fmac_f32 v125,v227,v227
v_smfmac_f32_16x16x64_bf16 v[228:231],v[100:103],v[144:151],v13
s_nop 0
ds_read_b128 v[144:147],v112 offset:3552
v_add_f32 v122,v122,v248
v_fmac_f32 v126,v248,v248
v_add_f32 v122,v122,v249
v_fmac_f32 v126,v249,v249
v_smfmac_f32_16x16x64_bf16 v[252:255],v[100:103],v[148:155],v13
s_setprio 0
ds_read_b128 v[152:155],v116 offset:64
ds_read_b128 v[148:151],v112 offset:3744
v_add_f32 v123,v123,v250
v_fmac_f32 v127,v250,v250
v_add_f32 v123,v123,v251
v_fmac_f32 v127,v251,v251
ds_read_b128 v[100:103],v116 offset:320
v_add_f32 v120,v120,v228
v_fmac_f32 v124,v228,v228
v_add_f32 v120,v120,v229
v_fmac_f32 v124,v229,v229
v_add_f32 v121,v121,v230
v_fmac_f32 v125,v230,v230
v_add_f32 v121,v121,v231
v_fmac_f32 v125,v231,v231
s_waitcnt lgkmcnt(0)
s_mov_b32 s86,0x1234567
s_barrier
s_setprio 0
v_add_f32 v122,v122,v252
v_fmac_f32 v126,v252,v252
v_add_f32 v122,v122,v253
v_fmac_f32 v126,v253,v253
v_add_f32 v123,v123,v254
v_fmac_f32 v127,v254,v254
v_add_f32 v123,v123,v255
v_fmac_f32 v127,v255,v255
s_setprio 3
ds_read_b128 v[56:59],v14 offset:256
ds_read_b128 v[60:63],v7 offset:3616
s_setprio 0
v_permlane32_swap_b32 v120,v122
v_permlane32_swap_b32 v121,v123
v_permlane32_swap_b32 v124,v126
v_permlane32_swap_b32 v125,v127
v_add_f32 v120,v120,v122
v_add_f32 v121,v121,v123
v_add_f32 v124,v124,v126
v_add_f32 v125,v125,v127
v_permlane16_swap_b32 v120,v121
s_nop 0
v_permlane16_swap_b32 v124,v125
v_add_f32 v113,v120,v121
v_add_f32 v114,v124,v125
v_mul_f32 v113,0x3c2aaaab,v113
v_mul_f32 v114,0x3c2aaaab,v114
v_mul_f32 v115,0x5e000000,v114
v_fma_f32 v114,-v113,v113,v114
v_pk_maximum3_f16 v1,v1,v115,v115 op_sel:[0,1,1] op_sel_hi:[1,1,1]
v_max_f32 v114,0,v114
v_add_f32 v114,0x3727c5ac,v114
v_rsq_f32 v114,v114
s_nop 0
v_mul_f32 v120,0x8befe000,v114
v_mul_f32 v121,0x8befe000,v114
v_mul_f32 v124,-v113,v120
v_mul_f32 v125,-v113,v120
v_permlane16_swap_b32 v120,v121
s_nop 0
v_permlane16_swap_b32 v124,v125
v_mov_b32 v122,v120
v_mov_b32 v123,v121
v_mov_b32 v126,v124
v_mov_b32 v127,v125
v_permlane32_swap_b32 v120,v122
v_permlane32_swap_b32 v124,v126
v_permlane32_swap_b32 v121,v123
v_permlane32_swap_b32 v125,v127
v_add_u32 v8,94432,v4
v_add_u32 v9,94432,v5
v_fma_f32 v160,v160,v120,v124
s_setprio 3
ds_read_b64_tr_b16 v[64:65],v4 offset:512
s_setprio 0
v_fma_f32 v161,v161,v120,v124
s_setprio 3
ds_read_b64_tr_b16 v[66:67],v4 offset:2560
s_setprio 0
v_fma_f32 v162,v162,v121,v125
s_setprio 3
ds_read_b128 v[112:115],v7 offset:384
s_setprio 0
v_fma_f32 v163,v163,v121,v125
s_setprio 3
ds_read_b64_tr_b16 v[68:69],v5 offset:512
s_setprio 0
v_fma_f32 v164,v164,v120,v124
s_setprio 3
ds_read_b64_tr_b16 v[70:71],v5 offset:2560
s_setprio 0
v_fma_f32 v165,v165,v120,v124
s_setprio 3
ds_read_b128 v[116:119],v7 offset:416
s_setprio 0
v_fma_f32 v166,v166,v121,v125
s_setprio 3
ds_read_b64_tr_b16 v[72:73],v4 offset:4608
s_setprio 0
v_fma_f32 v167,v167,v121,v125
s_setprio 3
ds_read_b64_tr_b16 v[74:75],v4 offset:6656
s_setprio 0
v_fma_f32 v208,v208,v120,v124
s_setprio 3
ds_read_b64_tr_b16 v[76:77],v5 offset:4608
s_setprio 0
v_fma_f32 v209,v209,v120,v124
s_setprio 3
ds_read_b64_tr_b16 v[78:79],v5 offset:6656
s_setprio 0
v_fma_f32 v210,v210,v121,v125
s_setprio 3
ds_read_b64_tr_b16 v[80:81],v4 offset:8704
s_setprio 0
v_fma_f32 v211,v211,v121,v125
s_setprio 3
ds_read_b64_tr_b16 v[82:83],v4 offset:10752
s_setprio 0
v_fma_f32 v212,v212,v120,v124
s_setprio 3
ds_read_b64_tr_b16 v[84:85],v5 offset:8704
s_setprio 0
v_fma_f32 v213,v213,v120,v124
s_setprio 3
ds_read_b64_tr_b16 v[86:87],v5 offset:10752
s_setprio 0
v_fma_f32 v214,v214,v121,v125
v_fma_f32 v215,v215,v121,v125
v_cvt_pk_bf16_f32 v16,v160,v161
v_cvt_pk_bf16_f32 v20,v162,v163
v_cvt_pk_bf16_f32 v17,v164,v165
v_cvt_pk_bf16_f32 v21,v166,v167
v_cvt_pk_bf16_f32 v18,v208,v209
v_cvt_pk_bf16_f32 v22,v210,v211
v_cvt_pk_bf16_f32 v19,v212,v213
v_cvt_pk_bf16_f32 v23,v214,v215
v_mfma_f32_16x16x32_bf16 v[160:163],v[104:107],v[128:131],v[108:111]
v_fma_f32 v168,v168,v120,v124
v_fma_f32 v169,v169,v120,v124
v_fma_f32 v170,v170,v121,v125
v_fma_f32 v171,v171,v121,v125
v_fma_f32 v172,v172,v120,v124
v_fma_f32 v173,v173,v120,v124
v_fma_f32 v174,v174,v121,v125
v_fma_f32 v175,v175,v121,v125
v_mfma_f32_16x16x32_bf16 v[164:167],v[104:107],v[132:135],v[108:111]
v_fma_f32 v216,v216,v120,v124
v_fma_f32 v217,v217,v120,v124
v_fma_f32 v218,v218,v121,v125
v_fma_f32 v219,v219,v121,v125
v_fma_f32 v220,v220,v120,v124
v_fma_f32 v221,v221,v120,v124
v_fma_f32 v222,v222,v121,v125
v_fma_f32 v223,v223,v121,v125
v_mfma_f32_16x16x32_bf16 v[208:211],v[104:107],v[136:139],v[108:111]
v_cvt_pk_bf16_f32 v24,v168,v169
v_cvt_pk_bf16_f32 v28,v170,v171
v_cvt_pk_bf16_f32 v25,v172,v173
v_cvt_pk_bf16_f32 v29,v174,v175
v_cvt_pk_bf16_f32 v26,v216,v217
s_nop 0
s_mov_b32 s86,0xbe801d5a
v_cvt_pk_bf16_f32 v30,v218,v219
v_cvt_pk_bf16_f32 v27,v220,v221
v_cvt_pk_bf16_f32 v31,v222,v223
s_setprio 3
v_mfma_f32_16x16x32_bf16 v[212:215],v[104:107],v[140:143],v[108:111]
ds_read_b128 v[104:107],v14 offset:512
ds_read_b128 v[108:111],v7 offset:3712
s_waitcnt lgkmcnt(15)
s_setprio 0
v_mfma_f32_16x16x32_bf16 v[168:171],v[56:59],v[128:131],v[60:63]
v_fma_f32 v176,v176,v120,v124
v_fma_f32 v177,v177,v120,v124
v_fma_f32 v178,v178,v121,v125
v_fma_f32 v179,v179,v121,v125
v_fma_f32 v180,v180,v120,v124
v_fma_f32 v181,v181,v120,v124
v_fma_f32 v182,v182,v121,v125
v_fma_f32 v183,v183,v121,v125
v_mfma_f32_16x16x32_bf16 v[172:175],v[56:59],v[132:135],v[60:63]
v_fma_f32 v224,v224,v120,v124
v_fma_f32 v225,v225,v120,v124
v_fma_f32 v226,v226,v121,v125
v_fma_f32 v227,v227,v121,v125
v_fma_f32 v228,v228,v120,v124
v_fma_f32 v229,v229,v120,v124
v_fma_f32 v230,v230,v121,v125
v_fma_f32 v231,v231,v121,v125
v_mfma_f32_16x16x32_bf16 v[216:219],v[56:59],v[136:139],v[60:63]
v_cvt_pk_bf16_f32 v32,v176,v177
v_cvt_pk_bf16_f32 v36,v178,v179
v_cvt_pk_bf16_f32 v33,v180,v181
v_cvt_pk_bf16_f32 v37,v182,v183
v_cvt_pk_bf16_f32 v34,v224,v225
v_cvt_pk_bf16_f32 v38,v226,v227
v_cvt_pk_bf16_f32 v35,v228,v229
v_cvt_pk_bf16_f32 v39,v230,v231
v_mfma_f32_16x16x32_bf16 v[220:223],v[56:59],v[140:143],v[60:63]
s_waitcnt lgkmcnt(0)
s_setprio 3
v_mfma_f32_16x16x32_bf16 v[176:179],v[104:107],v[144:147],v[108:111]
ds_read_b128 v[56:59],v14 offset:768
s_setprio 0
v_fma_f32 v184,v184,v122,v126
v_fma_f32 v185,v185,v122,v126
v_fma_f32 v186,v186,v123,v127
v_fma_f32 v187,v187,v123,v127
s_setprio 3
v_mfma_f32_16x16x32_bf16 v[128:131],v[64:67],v[16:19],v[112:115]
ds_read_b128 v[60:63],v7 offset:3744
s_setprio 0
v_fma_f32 v188,v188,v122,v126
v_fma_f32 v189,v189,v122,v126
v_fma_f32 v190,v190,v123,v127
v_mfma_f32_16x16x32_bf16 v[180:183],v[104:107],v[148:151],v[108:111]
v_fma_f32 v191,v191,v123,v127
v_fma_f32 v232,v232,v122,v126
v_fma_f32 v233,v233,v122,v126
v_mfma_f32_16x16x32_bf16 v[136:139],v[64:67],v[20:23],v[112:115]
v_fma_f32 v234,v234,v123,v127
v_fma_f32 v235,v235,v123,v127
v_fma_f32 v236,v236,v122,v126
v_mfma_f32_16x16x32_bf16 v[224:227],v[104:107],v[152:155],v[108:111]
v_fma_f32 v237,v237,v122,v126
v_fma_f32 v238,v238,v123,v127
v_fma_f32 v239,v239,v123,v127
v_mfma_f32_16x16x32_bf16 v[140:143],v[68:71],v[20:23],v[116:119]
v_cvt_pk_bf16_f32 v40,v184,v185
v_cvt_pk_bf16_f32 v44,v186,v187
v_cvt_pk_bf16_f32 v41,v188,v189
s_setprio 3
v_mfma_f32_16x16x32_bf16 v[228:231],v[104:107],v[156:159],v[108:111]
ds_read_b128 v[104:107],v14 offset:1024
s_setprio 0
v_cvt_pk_bf16_f32 v45,v190,v191
v_cvt_pk_bf16_f32 v42,v232,v233
v_cvt_pk_bf16_f32 v46,v234,v235
s_setprio 3
v_mfma_f32_16x16x32_bf16 v[132:135],v[68:71],v[16:19],v[116:119]
ds_read_b128 v[108:111],v7 offset:3840
s_setprio 0
v_cvt_pk_bf16_f32 v43,v236,v237
v_cvt_pk_bf16_f32 v47,v238,v239
s_waitcnt lgkmcnt(2)
v_mfma_f32_16x16x32_bf16 v[184:187],v[56:59],v[144:147],v[60:63]
v_fma_f32 v192,v192,v122,v126
v_fma_f32 v193,v193,v122,v126
v_fma_f32 v194,v194,v123,v127
v_mfma_f32_16x16x32_bf16 v[128:131],v[72:75],v[24:27],v[128:131]
v_fma_f32 v195,v195,v123,v127
v_fma_f32 v196,v196,v122,v126
v_mfma_f32_16x16x32_bf16 v[188:191],v[56:59],v[148:151],v[60:63]
v_fma_f32 v197,v197,v122,v126
v_fma_f32 v198,v198,v123,v127
v_mfma_f32_16x16x32_bf16 v[136:139],v[72:75],v[28:31],v[136:139]
v_fma_f32 v199,v199,v123,v127
v_fma_f32 v240,v240,v122,v126
v_mfma_f32_16x16x32_bf16 v[232:235],v[56:59],v[152:155],v[60:63]
v_fma_f32 v241,v241,v122,v126
v_fma_f32 v242,v242,v123,v127
v_mfma_f32_16x16x32_bf16 v[140:143],v[76:79],v[28:31],v[140:143]
v_fma_f32 v243,v243,v123,v127
v_fma_f32 v244,v244,v122,v126
s_setprio 3
v_mfma_f32_16x16x32_bf16 v[236:239],v[56:59],v[156:159],v[60:63]
ds_read_b128 v[144:147],v14 offset:1280
s_setprio 0
v_fma_f32 v245,v245,v122,v126
v_fma_f32 v246,v246,v123,v127
s_setprio 3
v_mfma_f32_16x16x32_bf16 v[132:135],v[76:79],v[24:27],v[132:135]
ds_read_b128 v[148:151],v7 offset:3872
s_setprio 0
v_fma_f32 v247,v247,v123,v127
v_cvt_pk_bf16_f32 v48,v192,v193
v_cvt_pk_bf16_f32 v52,v194,v195
v_cvt_pk_bf16_f32 v49,v196,v197
v_cvt_pk_bf16_f32 v53,v198,v199
v_cvt_pk_bf16_f32 v50,v240,v241
v_cvt_pk_bf16_f32 v54,v242,v243
v_cvt_pk_bf16_f32 v51,v244,v245
v_cvt_pk_bf16_f32 v55,v246,v247
s_waitcnt lgkmcnt(2)
v_mfma_f32_16x16x32_bf16 v[192:195],v[104:107],v[88:91],v[108:111]
v_fma_f32 v200,v200,v122,v126
v_fma_f32 v201,v201,v122,v126
v_fma_f32 v202,v202,v123,v127
v_fma_f32 v203,v203,v123,v127
v_mfma_f32_16x16x32_bf16 v[128:131],v[80:83],v[32:35],v[128:131]
v_fma_f32 v204,v204,v122,v126
v_fma_f32 v205,v205,v122,v126
v_fma_f32 v206,v206,v123,v127
v_mfma_f32_16x16x32_bf16 v[196:199],v[104:107],v[92:95],v[108:111]
v_fma_f32 v207,v207,v123,v127
v_fma_f32 v248,v248,v122,v126
v_fma_f32 v249,v249,v122,v126
v_mfma_f32_16x16x32_bf16 v[136:139],v[80:83],v[36:39],v[136:139]
s_nop 0
v_fma_f32 v250,v250,v123,v127
v_fma_f32 v251,v251,v123,v127
v_fma_f32 v252,v252,v122,v126
v_mfma_f32_16x16x32_bf16 v[240:243],v[104:107],v[96:99],v[108:111]
v_fma_f32 v253,v253,v122,v126
v_fma_f32 v254,v254,v123,v127
v_fma_f32 v255,v255,v123,v127
v_mfma_f32_16x16x32_bf16 v[140:143],v[84:87],v[36:39],v[140:143]
s_nop 0
v_cvt_pk_bf16_f32 v56,v200,v201
v_cvt_pk_bf16_f32 v60,v202,v203
v_cvt_pk_bf16_f32 v57,v204,v205
v_mfma_f32_16x16x32_bf16 v[244:247],v[104:107],v[100:103],v[108:111]
v_cvt_pk_bf16_f32 v61,v206,v207
v_cvt_pk_bf16_f32 v58,v248,v249
v_cvt_pk_bf16_f32 v62,v250,v251
v_mfma_f32_16x16x32_bf16 v[132:135],v[84:87],v[32:35],v[132:135]
s_nop 0
v_cvt_pk_bf16_f32 v59,v252,v253
v_cvt_pk_bf16_f32 v63,v254,v255
s_waitcnt lgkmcnt(0)
v_mfma_f32_16x16x32_bf16 v[200:203],v[144:147],v[88:91],v[148:151]
s_mov_b32 s86,0x1234567
s_barrier
s_setprio 3
ds_read_b64_tr_b16 v[104:105],v6 offset:12448
v_lshlrev_b32_sdwa v128,1,v128 dst_sel:DWORD dst_unused:UNUSED_PAD src0_sel:DWORD src1_sel:WORD_1
v_lshlrev_b32_sdwa v130,1,v130 dst_sel:DWORD dst_unused:UNUSED_PAD src0_sel:DWORD src1_sel:WORD_1
v_lshlrev_b32_sdwa v129,1,v129 dst_sel:DWORD dst_unused:UNUSED_PAD src0_sel:DWORD src1_sel:WORD_1
v_lshlrev_b32_sdwa v131,1,v131 dst_sel:DWORD dst_unused:UNUSED_PAD src0_sel:DWORD src1_sel:WORD_1
v_mfma_f32_16x16x32_bf16 v[204:207],v[144:147],v[92:95],v[148:151]
ds_read_u16 v128,v128
ds_read_b64_tr_b16 v[106:107],v6 offset:13984
v_fma_mixlo_f16 v120,v140,s99,0
v_fma_mixlo_f16 v122,v141,s99,0
v_lshlrev_b32_sdwa v132,1,v132 dst_sel:DWORD dst_unused:UNUSED_PAD src0_sel:DWORD src1_sel:WORD_1
v_lshlrev_b32_sdwa v134,1,v134 dst_sel:DWORD dst_unused:UNUSED_PAD src0_sel:DWORD src1_sel:WORD_1
v_mfma_f32_16x16x32_bf16 v[248:251],v[144:147],v[96:99],v[148:151]
ds_read_u16_d16_hi v130,v130
ds_read_b64_tr_b16 v[108:109],v6 offset:12456
v_fma_mixhi_f16 v120,v142,s99,0
v_fma_mixhi_f16 v122,v143,s99,0
v_lshlrev_b32_sdwa v133,1,v133 dst_sel:DWORD dst_unused:UNUSED_PAD src0_sel:DWORD src1_sel:WORD_1
v_mfma_f32_16x16x32_bf16 v[252:255],v[144:147],v[100:103],v[148:151]
ds_read_u16 v129,v129
ds_read_b64_tr_b16 v[110:111],v6 offset:13992
v_pk_fma_f16 v121,v120,v120,s54
v_lshlrev_b32_sdwa v135,1,v135 dst_sel:DWORD dst_unused:UNUSED_PAD src0_sel:DWORD src1_sel:WORD_1
v_pk_fma_f16 v123,v122,v122,s54
v_pk_mul_f16 v121,v120,v121
v_lshlrev_b32_sdwa v136,1,v136 dst_sel:DWORD dst_unused:UNUSED_PAD src0_sel:DWORD src1_sel:WORD_1
v_pk_mul_f16 v123,v122,v123
v_mfma_f32_16x16x32_bf16 v[144:147],v[64:67],v[40:43],v[112:115]
ds_read_u16_d16_hi v131,v131
ds_read_b64_tr_b16 v[88:89],v6 offset:12320
v_lshlrev_b32_sdwa v138,1,v138 dst_sel:DWORD dst_unused:UNUSED_PAD src0_sel:DWORD src1_sel:WORD_1
v_exp_f16 v120,v121
v_mfma_f32_16x16x32_bf16 v[152:155],v[64:67],v[44:47],v[112:115]
ds_read_u16 v132,v132
ds_read_b64_tr_b16 v[90:91],v6 offset:13856
v_lshlrev_b32_sdwa v137,1,v137 dst_sel:DWORD dst_unused:UNUSED_PAD src0_sel:DWORD src1_sel:WORD_1
v_mfma_f32_16x16x32_bf16 v[156:159],v[68:71],v[44:47],v[116:119]
ds_read_u16_d16_hi v134,v134
ds_read_b64_tr_b16 v[92:93],v6 offset:12328
v_lshlrev_b32_sdwa v139,1,v139 dst_sel:DWORD dst_unused:UNUSED_PAD src0_sel:DWORD src1_sel:WORD_1
v_exp_f16 v122,v123
v_mfma_f32_16x16x32_bf16 v[148:151],v[68:71],v[40:43],v[116:119]
ds_read_u16 v133,v133
ds_read_b64_tr_b16 v[94:95],v6 offset:13864
v_exp_f16_sdwa v122,v123 dst_sel:WORD_1 dst_unused:UNUSED_PRESERVE src0_sel:WORD_1
v_mfma_f32_16x16x32_bf16 v[144:147],v[72:75],v[48:51],v[144:147]
ds_read_u16_d16_hi v135,v135
ds_read_b64_tr_b16 v[96:97],v6 offset:12384
v_exp_f16_sdwa v120,v121 dst_sel:WORD_1 dst_unused:UNUSED_PRESERVE src0_sel:WORD_1
v_mfma_f32_16x16x32_bf16 v[152:155],v[72:75],v[52:55],v[152:155]
ds_read_u16 v136,v136
ds_read_b64_tr_b16 v[98:99],v6 offset:13920
v_pk_fma_f16 v122,v122,s55,s55
v_pk_fma_f16 v120,v120,s55,s55
v_mfma_f32_16x16x32_bf16 v[156:159],v[76:79],v[52:55],v[156:159]
ds_read_u16_d16_hi v138,v138
ds_read_b64_tr_b16 v[100:101],v6 offset:12392
v_rcp_f16 v121,v120
v_rcp_f16 v123,v122
v_mfma_f32_16x16x32_bf16 v[148:151],v[76:79],v[48:51],v[148:151]
ds_read_u16 v137,v137
ds_read_b64_tr_b16 v[102:103],v6 offset:13928
v_rcp_f16_sdwa v121,v120 dst_sel:WORD_1 dst_unused:UNUSED_PRESERVE src0_sel:WORD_1
v_mfma_f32_16x16x32_bf16 v[144:147],v[80:83],v[56:59],v[144:147]
ds_read_u16_d16_hi v139,v139
ds_read_b64_tr_b16 v[64:65],v4 offset:12320
v_rcp_f16_sdwa v123,v122 dst_sel:WORD_1 dst_unused:UNUSED_PRESERVE src0_sel:WORD_1
v_mfma_f32_16x16x32_bf16 v[152:155],v[80:83],v[60:63],v[152:155]
ds_read_b64_tr_b16 v[66:67],v4 offset:14368
v_fma_mix_f32 v142,v142,v121,0 op_sel:[0,1,0] op_sel_hi:[0,1,0]
v_fma_mix_f32 v140,v140,v121,0 op_sel_hi:[0,1,0]
v_mfma_f32_16x16x32_bf16 v[156:159],v[84:87],v[60:63],v[156:159]
ds_read_b128 v[112:115],v7 offset:512
s_waitcnt lgkmcnt(15)
v_lshl_add_u64 v[128:129],v[128:129],0,v[130:131]
s_waitcnt lgkmcnt(11)
v_lshl_add_u64 v[130:131],v[132:133],0,v[134:135]
v_mfma_f32_16x16x32_bf16 v[148:151],v[84:87],v[56:59],v[148:151]
ds_read_b64_tr_b16 v[68:69],v5 offset:12320
v_fma_mix_f32 v141,v141,v123,0 op_sel_hi:[0,1,0]
v_fma_mix_f32 v143,v143,v123,0 op_sel:[0,1,0] op_sel_hi:[0,1,0]
v_lshlrev_b32_sdwa v144,1,v144 dst_sel:DWORD dst_unused:UNUSED_PAD src0_sel:DWORD src1_sel:WORD_1
v_lshlrev_b32_sdwa v146,1,v146 dst_sel:DWORD dst_unused:UNUSED_PAD src0_sel:DWORD src1_sel:WORD_1
v_mfma_f32_16x16x32_bf16 v[160:163],v[88:91],v[128:131],v[160:163]
ds_read_u16 v144,v144
v_lshlrev_b32_sdwa v145,1,v145 dst_sel:DWORD dst_unused:UNUSED_PAD src0_sel:DWORD src1_sel:WORD_1
v_lshlrev_b32_sdwa v147,1,v147 dst_sel:DWORD dst_unused:UNUSED_PAD src0_sel:DWORD src1_sel:WORD_1
v_fma_mixlo_f16 v122,v157,s99,0
v_mfma_f32_16x16x32_bf16 v[168:171],v[92:95],v[128:131],v[168:171]
s_waitcnt lgkmcnt(10)
ds_read_b64_tr_b16 v[70:71],v5 offset:14368
ds_read_b128 v[116:119],v7 offset:544
v_fma_mixlo_f16 v120,v156,s99,0
v_mfma_f32_16x16x32_bf16 v[176:179],v[96:99],v[128:131],v[176:179]
s_waitcnt lgkmcnt(8)
ds_read_u16_d16_hi v146,v146
v_lshlrev_b32_sdwa v148,1,v148 dst_sel:DWORD dst_unused:UNUSED_PAD src0_sel:DWORD src1_sel:WORD_1
v_lshlrev_b32_sdwa v150,1,v150 dst_sel:DWORD dst_unused:UNUSED_PAD src0_sel:DWORD src1_sel:WORD_1
v_mfma_f32_16x16x32_bf16 v[184:187],v[100:103],v[128:131],v[184:187]
s_waitcnt lgkmcnt(8)
ds_read_b64_tr_b16 v[72:73],v4 offset:16416
ds_read_b64_tr_b16 v[74:75],v4 offset:18464
v_lshl_add_u64 v[136:137],v[136:137],0,v[138:139]
v_fma_mixhi_f16 v120,v158,s99,0
v_mfma_f32_16x16x32_bf16 v[192:195],v[104:107],v[128:131],v[192:195]
ds_read_u16 v145,v145
v_cvt_pk_bf16_f32 v138,v140,v142
v_cvt_pk_bf16_f32 v139,v141,v143
v_mfma_f32_16x16x32_bf16 v[200:203],v[108:111],v[128:131],v[200:203]
v_fma_mixhi_f16 v122,v159,s99,0
v_mfma_f32_16x16x32_bf16 v[164:167],v[88:91],v[136:139],v[164:167]
ds_read_b64_tr_b16 v[76:77],v5 offset:16416
ds_read_b64_tr_b16 v[78:79],v5 offset:18464
v_lshlrev_b32_sdwa v149,1,v149 dst_sel:DWORD dst_unused:UNUSED_PAD src0_sel:DWORD src1_sel:WORD_1
v_lshlrev_b32_sdwa v151,1,v151 dst_sel:DWORD dst_unused:UNUSED_PAD src0_sel:DWORD src1_sel:WORD_1
v_mfma_f32_16x16x32_bf16 v[172:175],v[92:95],v[136:139],v[172:175]
ds_read_b64_tr_b16 v[80:81],v4 offset:20512
ds_read_b64_tr_b16 v[82:83],v4 offset:22560
v_pk_fma_f16 v121,v120,v120,s54
v_pk_fma_f16 v123,v122,v122,s54
v_mfma_f32_16x16x32_bf16 v[180:183],v[96:99],v[136:139],v[180:183]
ds_read_u16_d16_hi v147,v147
v_pk_mul_f16 v121,v120,v121
v_pk_mul_f16 v123,v122,v123
v_mfma_f32_16x16x32_bf16 v[188:191],v[100:103],v[136:139],v[188:191]
ds_read_u16 v148,v148
v_lshlrev_b32_sdwa v152,1,v152 dst_sel:DWORD dst_unused:UNUSED_PAD src0_sel:DWORD src1_sel:WORD_1
v_lshlrev_b32_sdwa v154,1,v154 dst_sel:DWORD dst_unused:UNUSED_PAD src0_sel:DWORD src1_sel:WORD_1
v_mfma_f32_16x16x32_bf16 v[196:199],v[104:107],v[136:139],v[196:199]
v_exp_f16 v120,v121
v_mfma_f32_16x16x32_bf16 v[204:207],v[108:111],v[136:139],v[204:207]
s_waitcnt lgkmcnt(14)
ds_read_b64_tr_b16 v[84:85],v5 offset:20512
ds_read_b64_tr_b16 v[86:87],v5 offset:22560
v_exp_f16 v122,v123
v_mfma_f32_16x16x32_bf16 v[128:131],v[64:67],v[16:19],v[112:115]
v_lshlrev_b32_sdwa v153,1,v153 dst_sel:DWORD dst_unused:UNUSED_PAD src0_sel:DWORD src1_sel:WORD_1
v_lshlrev_b32_sdwa v155,1,v155 dst_sel:DWORD dst_unused:UNUSED_PAD src0_sel:DWORD src1_sel:WORD_1
v_mfma_f32_16x16x32_bf16 v[136:139],v[64:67],v[20:23],v[112:115]
s_waitcnt lgkmcnt(12)
v_exp_f16_sdwa v120,v121 dst_sel:WORD_1 dst_unused:UNUSED_PRESERVE src0_sel:WORD_1
v_mfma_f32_16x16x32_bf16 v[140:143],v[68:71],v[20:23],v[116:119]
ds_read_u16_d16_hi v150,v150
ds_read_u16 v149,v149
v_exp_f16_sdwa v122,v123 dst_sel:WORD_1 dst_unused:UNUSED_PRESERVE src0_sel:WORD_1
v_mfma_f32_16x16x32_bf16 v[132:135],v[68:71],v[16:19],v[116:119]
s_waitcnt lgkmcnt(11)
v_pk_fma_f16 v120,v120,s55,s55
v_mfma_f32_16x16x32_bf16 v[128:131],v[72:75],v[24:27],v[128:131]
v_pk_fma_f16 v122,v122,s55,s55
v_rcp_f16 v121,v120
v_mfma_f32_16x16x32_bf16 v[136:139],v[72:75],v[28:31],v[136:139]
s_waitcnt lgkmcnt(8)
ds_read_u16_d16_hi v151,v151
v_rcp_f16 v123,v122
v_mfma_f32_16x16x32_bf16 v[140:143],v[76:79],v[28:31],v[140:143]
ds_read_u16 v152,v152
ds_read_u16_d16_hi v154,v154
v_rcp_f16_sdwa v121,v120 dst_sel:WORD_1 dst_unused:UNUSED_PRESERVE src0_sel:WORD_1
v_mfma_f32_16x16x32_bf16 v[132:135],v[76:79],v[24:27],v[132:135]
s_waitcnt lgkmcnt(9)
v_rcp_f16_sdwa v123,v122 dst_sel:WORD_1 dst_unused:UNUSED_PRESERVE src0_sel:WORD_1
v_mfma_f32_16x16x32_bf16 v[128:131],v[80:83],v[32:35],v[128:131]
ds_read_u16 v153,v153
v_fma_mix_f32 v156,v156,v121,0 op_sel_hi:[0,1,0]
v_fma_mix_f32 v158,v158,v121,0 op_sel:[0,1,0] op_sel_hi:[0,1,0]
v_mfma_f32_16x16x32_bf16 v[136:139],v[80:83],v[36:39],v[136:139]
s_waitcnt lgkmcnt(3)
v_lshl_add_u64 v[144:145],v[144:145],0,v[146:147]
v_lshl_add_u64 v[146:147],v[148:149],0,v[150:151]
v_mfma_f32_16x16x32_bf16 v[140:143],v[84:87],v[36:39],v[140:143]
ds_read_u16_d16_hi v155,v155
v_fma_mix_f32 v157,v157,v123,0 op_sel_hi:[0,1,0]
v_fma_mix_f32 v159,v159,v123,0 op_sel:[0,1,0] op_sel_hi:[0,1,0]
v_mfma_f32_16x16x32_bf16 v[132:135],v[84:87],v[32:35],v[132:135]
v_lshlrev_b32_sdwa v128,1,v128 dst_sel:DWORD dst_unused:UNUSED_PAD src0_sel:DWORD src1_sel:WORD_1
v_lshlrev_b32_sdwa v130,1,v130 dst_sel:DWORD dst_unused:UNUSED_PAD src0_sel:DWORD src1_sel:WORD_1
v_mfma_f32_16x16x32_bf16 v[208:211],v[88:91],v[144:147],v[208:211]
s_setprio 2
v_lshlrev_b32_sdwa v129,1,v129 dst_sel:DWORD dst_unused:UNUSED_PAD src0_sel:DWORD src1_sel:WORD_1
v_lshlrev_b32_sdwa v131,1,v131 dst_sel:DWORD dst_unused:UNUSED_PAD src0_sel:DWORD src1_sel:WORD_1
v_mfma_f32_16x16x32_bf16 v[216:219],v[92:95],v[144:147],v[216:219]
ds_read_u16 v128,v128
ds_read_u16_d16_hi v130,v130
v_fma_mixlo_f16 v120,v140,s99,0
v_fma_mixlo_f16 v122,v141,s99,0
v_mfma_f32_16x16x32_bf16 v[224:227],v[96:99],v[144:147],v[224:227]
ds_read_u16 v129,v129
v_lshlrev_b32_sdwa v132,1,v132 dst_sel:DWORD dst_unused:UNUSED_PAD src0_sel:DWORD src1_sel:WORD_1
v_lshlrev_b32_sdwa v134,1,v134 dst_sel:DWORD dst_unused:UNUSED_PAD src0_sel:DWORD src1_sel:WORD_1
v_mfma_f32_16x16x32_bf16 v[232:235],v[100:103],v[144:147],v[232:235]
s_waitcnt lgkmcnt(3)
v_lshl_add_u64 v[152:153],v[152:153],0,v[154:155]
v_cvt_pk_bf16_f32 v154,v156,v158
v_mfma_f32_16x16x32_bf16 v[240:243],v[104:107],v[144:147],v[240:243]
v_fma_mixhi_f16 v120,v142,s99,0
v_cvt_pk_bf16_f32 v155,v157,v159
v_mfma_f32_16x16x32_bf16 v[248:251],v[108:111],v[144:147],v[248:251]
ds_read_u16_d16_hi v131,v131
v_fma_mixhi_f16 v122,v143,s99,0
v_mfma_f32_16x16x32_bf16 v[212:215],v[88:91],v[152:155],v[212:215]
v_lshlrev_b32_sdwa v133,1,v133 dst_sel:DWORD dst_unused:UNUSED_PAD src0_sel:DWORD src1_sel:WORD_1
v_pk_fma_f16 v121,v120,v120,s54
v_lshlrev_b32_sdwa v135,1,v135 dst_sel:DWORD dst_unused:UNUSED_PAD src0_sel:DWORD src1_sel:WORD_1
v_mfma_f32_16x16x32_bf16 v[220:223],v[92:95],v[152:155],v[220:223]
ds_read_u16 v132,v132
ds_read_u16_d16_hi v134,v134
v_pk_fma_f16 v123,v122,v122,s54
v_mfma_f32_16x16x32_bf16 v[228:231],v[96:99],v[152:155],v[228:231]
ds_read_b64_tr_b16 v[88:89],v6 offset:18480
v_pk_mul_f16 v121,v120,v121
v_lshlrev_b32_sdwa v136,1,v136 dst_sel:DWORD dst_unused:UNUSED_PAD src0_sel:DWORD src1_sel:WORD_1
v_pk_mul_f16 v123,v122,v123
v_mfma_f32_16x16x32_bf16 v[236:239],v[100:103],v[152:155],v[236:239]
ds_read_b64_tr_b16 v[90:91],v6 offset:20016
v_lshlrev_b32_sdwa v138,1,v138 dst_sel:DWORD dst_unused:UNUSED_PAD src0_sel:DWORD src1_sel:WORD_1
v_mfma_f32_16x16x32_bf16 v[244:247],v[104:107],v[152:155],v[244:247]
ds_read_u16 v133,v133
v_exp_f16 v120,v121
v_mfma_f32_16x16x32_bf16 v[252:255],v[108:111],v[152:155],v[252:255]
ds_read_b64_tr_b16 v[92:93],v6 offset:18488
v_lshlrev_b32_sdwa v137,1,v137 dst_sel:DWORD dst_unused:UNUSED_PAD src0_sel:DWORD src1_sel:WORD_1
v_mfma_f32_16x16x32_bf16 v[144:147],v[64:67],v[40:43],v[112:115]
ds_read_b64_tr_b16 v[94:95],v6 offset:20024
v_lshlrev_b32_sdwa v139,1,v139 dst_sel:DWORD dst_unused:UNUSED_PAD src0_sel:DWORD src1_sel:WORD_1
v_mfma_f32_16x16x32_bf16 v[152:155],v[64:67],v[44:47],v[112:115]
ds_read_u16_d16_hi v135,v135
ds_read_b64_tr_b16 v[96:97],v6 offset:18544
v_exp_f16 v122,v123
v_mfma_f32_16x16x32_bf16 v[156:159],v[68:71],v[44:47],v[116:119]
ds_read_u16 v136,v136
v_exp_f16_sdwa v122,v123 dst_sel:WORD_1 dst_unused:UNUSED_PRESERVE src0_sel:WORD_1
v_exp_f16_sdwa v120,v121 dst_sel:WORD_1 dst_unused:UNUSED_PRESERVE src0_sel:WORD_1
v_mfma_f32_16x16x32_bf16 v[148:151],v[68:71],v[40:43],v[116:119]
ds_read_b64_tr_b16 v[98:99],v6 offset:20080
v_pk_fma_f16 v122,v122,s55,s55
v_pk_fma_f16 v120,v120,s55,s55
v_mfma_f32_16x16x32_bf16 v[144:147],v[72:75],v[48:51],v[144:147]
v_rcp_f16 v121,v120
v_mfma_f32_16x16x32_bf16 v[152:155],v[72:75],v[52:55],v[152:155]
ds_read_b64_tr_b16 v[100:101],v6 offset:18552
v_rcp_f16 v123,v122
v_mfma_f32_16x16x32_bf16 v[156:159],v[76:79],v[52:55],v[156:159]
ds_read_u16_d16_hi v138,v138
v_rcp_f16_sdwa v121,v120 dst_sel:WORD_1 dst_unused:UNUSED_PRESERVE src0_sel:WORD_1
v_mfma_f32_16x16x32_bf16 v[148:151],v[76:79],v[48:51],v[148:151]
ds_read_b64_tr_b16 v[102:103],v6 offset:20088
ds_read_b64_tr_b16 v[104:105],v6 offset:18608
v_rcp_f16_sdwa v123,v122 dst_sel:WORD_1 dst_unused:UNUSED_PRESERVE src0_sel:WORD_1
v_mfma_f32_16x16x32_bf16 v[144:147],v[80:83],v[56:59],v[144:147]
ds_read_u16 v137,v137
v_fma_mix_f32 v142,v142,v121,0 op_sel:[0,1,0] op_sel_hi:[0,1,0]
v_mfma_f32_16x16x32_bf16 v[152:155],v[80:83],v[60:63],v[152:155]
s_waitcnt lgkmcnt(8)
ds_read_b64_tr_b16 v[106:107],v6 offset:20144
ds_read_b64_tr_b16 v[108:109],v6 offset:18616
v_fma_mix_f32 v140,v140,v121,0 op_sel_hi:[0,1,0]
v_lshl_add_u64 v[128:129],v[128:129],0,v[130:131]
v_lshl_add_u64 v[130:131],v[132:133],0,v[134:135]
v_mfma_f32_16x16x32_bf16 v[156:159],v[84:87],v[60:63],v[156:159]
ds_read_u16_d16_hi v139,v139
v_fma_mix_f32 v141,v141,v123,0 op_sel_hi:[0,1,0]
v_fma_mix_f32 v143,v143,v123,0 op_sel:[0,1,0] op_sel_hi:[0,1,0]
v_mfma_f32_16x16x32_bf16 v[148:151],v[84:87],v[56:59],v[148:151]
ds_read_b64_tr_b16 v[110:111],v6 offset:20152
ds_read_b64_tr_b16 v[64:65],v4 offset:12832
v_lshlrev_b32_sdwa v144,1,v144 dst_sel:DWORD dst_unused:UNUSED_PAD src0_sel:DWORD src1_sel:WORD_1
v_lshlrev_b32_sdwa v146,1,v146 dst_sel:DWORD dst_unused:UNUSED_PAD src0_sel:DWORD src1_sel:WORD_1
v_mfma_f32_16x16x32_bf16 v[160:163],v[88:91],v[128:131],v[160:163]
ds_read_u16 v144,v144
v_lshlrev_b32_sdwa v145,1,v145 dst_sel:DWORD dst_unused:UNUSED_PAD src0_sel:DWORD src1_sel:WORD_1
v_lshlrev_b32_sdwa v147,1,v147 dst_sel:DWORD dst_unused:UNUSED_PAD src0_sel:DWORD src1_sel:WORD_1
v_fma_mixlo_f16 v122,v157,s99,0
v_mfma_f32_16x16x32_bf16 v[168:171],v[92:95],v[128:131],v[168:171]
s_waitcnt lgkmcnt(11)
ds_read_b64_tr_b16 v[66:67],v4 offset:14880
ds_read_b128 v[112:115],v7 offset:640
v_fma_mixlo_f16 v120,v156,s99,0
v_mfma_f32_16x16x32_bf16 v[176:179],v[96:99],v[128:131],v[176:179]
s_waitcnt lgkmcnt(10)
ds_read_u16_d16_hi v146,v146
v_lshlrev_b32_sdwa v148,1,v148 dst_sel:DWORD dst_unused:UNUSED_PAD src0_sel:DWORD src1_sel:WORD_1
v_lshlrev_b32_sdwa v150,1,v150 dst_sel:DWORD dst_unused:UNUSED_PAD src0_sel:DWORD src1_sel:WORD_1
v_mfma_f32_16x16x32_bf16 v[184:187],v[100:103],v[128:131],v[184:187]
s_waitcnt lgkmcnt(6)
ds_read_b64_tr_b16 v[68:69],v5 offset:12832
ds_read_b64_tr_b16 v[70:71],v5 offset:14880
v_lshl_add_u64 v[136:137],v[136:137],0,v[138:139]
v_fma_mixhi_f16 v120,v158,s99,0
v_mfma_f32_16x16x32_bf16 v[192:195],v[104:107],v[128:131],v[192:195]
s_waitcnt lgkmcnt(7)
ds_read_u16 v145,v145
v_cvt_pk_bf16_f32 v138,v140,v142
v_cvt_pk_bf16_f32 v139,v141,v143
v_mfma_f32_16x16x32_bf16 v[200:203],v[108:111],v[128:131],v[200:203]
v_fma_mixhi_f16 v122,v159,s99,0
v_mfma_f32_16x16x32_bf16 v[164:167],v[88:91],v[136:139],v[164:167]
ds_read_b128 v[116:119],v7 offset:672
ds_read_b64_tr_b16 v[72:73],v4 offset:16928
v_lshlrev_b32_sdwa v149,1,v149 dst_sel:DWORD dst_unused:UNUSED_PAD src0_sel:DWORD src1_sel:WORD_1
v_lshlrev_b32_sdwa v151,1,v151 dst_sel:DWORD dst_unused:UNUSED_PAD src0_sel:DWORD src1_sel:WORD_1
v_mfma_f32_16x16x32_bf16 v[172:175],v[92:95],v[136:139],v[172:175]
ds_read_b64_tr_b16 v[74:75],v4 offset:18976
ds_read_b64_tr_b16 v[76:77],v5 offset:16928
v_pk_fma_f16 v121,v120,v120,s54
v_pk_fma_f16 v123,v122,v122,s54
v_mfma_f32_16x16x32_bf16 v[180:183],v[96:99],v[136:139],v[180:183]
ds_read_u16_d16_hi v147,v147
v_pk_mul_f16 v121,v120,v121
v_pk_mul_f16 v123,v122,v123
v_mfma_f32_16x16x32_bf16 v[188:191],v[100:103],v[136:139],v[188:191]
ds_read_u16 v148,v148
v_lshlrev_b32_sdwa v152,1,v152 dst_sel:DWORD dst_unused:UNUSED_PAD src0_sel:DWORD src1_sel:WORD_1
v_lshlrev_b32_sdwa v154,1,v154 dst_sel:DWORD dst_unused:UNUSED_PAD src0_sel:DWORD src1_sel:WORD_1
v_mfma_f32_16x16x32_bf16 v[196:199],v[104:107],v[136:139],v[196:199]
v_exp_f16 v120,v121
v_mfma_f32_16x16x32_bf16 v[204:207],v[108:111],v[136:139],v[204:207]
s_waitcnt lgkmcnt(10)
ds_read_b64_tr_b16 v[78:79],v5 offset:18976
ds_read_b64_tr_b16 v[80:81],v4 offset:21024
v_exp_f16 v122,v123
v_mfma_f32_16x16x32_bf16 v[128:131],v[64:67],v[16:19],v[112:115]
s_setprio 1
v_lshlrev_b32_sdwa v153,1,v153 dst_sel:DWORD dst_unused:UNUSED_PAD src0_sel:DWORD src1_sel:WORD_1
v_lshlrev_b32_sdwa v155,1,v155 dst_sel:DWORD dst_unused:UNUSED_PAD src0_sel:DWORD src1_sel:WORD_1
v_mfma_f32_16x16x32_bf16 v[136:139],v[64:67],v[20:23],v[112:115]
s_waitcnt lgkmcnt(7)
v_exp_f16_sdwa v120,v121 dst_sel:WORD_1 dst_unused:UNUSED_PRESERVE src0_sel:WORD_1
v_mfma_f32_16x16x32_bf16 v[140:143],v[68:71],v[20:23],v[116:119]
ds_read_u16_d16_hi v150,v150
ds_read_u16 v149,v149
v_exp_f16_sdwa v122,v123 dst_sel:WORD_1 dst_unused:UNUSED_PRESERVE src0_sel:WORD_1
v_mfma_f32_16x16x32_bf16 v[132:135],v[68:71],v[16:19],v[116:119]
s_waitcnt lgkmcnt(7)
ds_read_b64_tr_b16 v[82:83],v4 offset:23072
v_pk_fma_f16 v120,v120,s55,s55
v_mfma_f32_16x16x32_bf16 v[128:131],v[72:75],v[24:27],v[128:131]
ds_read_b64_tr_b16 v[84:85],v5 offset:21024
v_pk_fma_f16 v122,v122,s55,s55
v_rcp_f16 v121,v120
v_mfma_f32_16x16x32_bf16 v[136:139],v[72:75],v[28:31],v[136:139]
s_waitcnt lgkmcnt(5)
ds_read_b64_tr_b16 v[86:87],v5 offset:23072
ds_read_u16_d16_hi v151,v151
v_rcp_f16 v123,v122
v_mfma_f32_16x16x32_bf16 v[140:143],v[76:79],v[28:31],v[140:143]
s_setprio 0
ds_read_u16 v152,v152
ds_read_u16_d16_hi v154,v154
v_rcp_f16_sdwa v121,v120 dst_sel:WORD_1 dst_unused:UNUSED_PRESERVE src0_sel:WORD_1
v_mfma_f32_16x16x32_bf16 v[132:135],v[76:79],v[24:27],v[132:135]
s_waitcnt lgkmcnt(5)
v_rcp_f16_sdwa v123,v122 dst_sel:WORD_1 dst_unused:UNUSED_PRESERVE src0_sel:WORD_1
v_mfma_f32_16x16x32_bf16 v[128:131],v[80:83],v[32:35],v[128:131]
s_nop 0
ds_read_u16 v153,v153
v_fma_mix_f32 v156,v156,v121,0 op_sel_hi:[0,1,0]
v_fma_mix_f32 v158,v158,v121,0 op_sel:[0,1,0] op_sel_hi:[0,1,0]
v_mfma_f32_16x16x32_bf16 v[136:139],v[80:83],v[36:39],v[136:139]
s_waitcnt lgkmcnt(3)
v_lshl_add_u64 v[144:145],v[144:145],0,v[146:147]
v_lshl_add_u64 v[146:147],v[148:149],0,v[150:151]
v_mfma_f32_16x16x32_bf16 v[140:143],v[84:87],v[36:39],v[140:143]
ds_read_u16_d16_hi v155,v155
v_fma_mix_f32 v157,v157,v123,0 op_sel_hi:[0,1,0]
v_fma_mix_f32 v159,v159,v123,0 op_sel:[0,1,0] op_sel_hi:[0,1,0]
v_mfma_f32_16x16x32_bf16 v[132:135],v[84:87],v[32:35],v[132:135]
s_nop 0
v_lshlrev_b32_sdwa v128,1,v128 dst_sel:DWORD dst_unused:UNUSED_PAD src0_sel:DWORD src1_sel:WORD_1
v_lshlrev_b32_sdwa v130,1,v130 dst_sel:DWORD dst_unused:UNUSED_PAD src0_sel:DWORD src1_sel:WORD_1
v_mfma_f32_16x16x32_bf16 v[208:211],v[88:91],v[144:147],v[208:211]
s_mov_b32 s86,0x1234567
s_barrier
s_setprio 3
v_lshlrev_b32_sdwa v129,1,v129 dst_sel:DWORD dst_unused:UNUSED_PAD src0_sel:DWORD src1_sel:WORD_1
v_lshlrev_b32_sdwa v131,1,v131 dst_sel:DWORD dst_unused:UNUSED_PAD src0_sel:DWORD src1_sel:WORD_1
v_mfma_f32_16x16x32_bf16 v[216:219],v[92:95],v[144:147],v[216:219]
ds_read_u16 v128,v128
ds_read_u16_d16_hi v130,v130
v_fma_mixlo_f16 v120,v140,s99,0
v_fma_mixlo_f16 v122,v141,s99,0
v_mfma_f32_16x16x32_bf16 v[224:227],v[96:99],v[144:147],v[224:227]
ds_read_u16 v129,v129
v_lshlrev_b32_sdwa v132,1,v132 dst_sel:DWORD dst_unused:UNUSED_PAD src0_sel:DWORD src1_sel:WORD_1
s_mov_b32 s86,0xbe801d5a
v_lshlrev_b32_sdwa v134,1,v134 dst_sel:DWORD dst_unused:UNUSED_PAD src0_sel:DWORD src1_sel:WORD_1
v_mfma_f32_16x16x32_bf16 v[232:235],v[100:103],v[144:147],v[232:235]
s_waitcnt lgkmcnt(3)
v_lshl_add_u64 v[152:153],v[152:153],0,v[154:155]
v_cvt_pk_bf16_f32 v154,v156,v158
v_mfma_f32_16x16x32_bf16 v[240:243],v[104:107],v[144:147],v[240:243]
v_fma_mixhi_f16 v120,v142,s99,0
v_cvt_pk_bf16_f32 v155,v157,v159
v_mfma_f32_16x16x32_bf16 v[248:251],v[108:111],v[144:147],v[248:251]
ds_read_u16_d16_hi v131,v131
v_fma_mixhi_f16 v122,v143,s99,0
v_mfma_f32_16x16x32_bf16 v[212:215],v[88:91],v[152:155],v[212:215]
v_lshlrev_b32_sdwa v133,1,v133 dst_sel:DWORD dst_unused:UNUSED_PAD src0_sel:DWORD src1_sel:WORD_1
v_pk_fma_f16 v121,v120,v120,s54
v_lshlrev_b32_sdwa v135,1,v135 dst_sel:DWORD dst_unused:UNUSED_PAD src0_sel:DWORD src1_sel:WORD_1
v_mfma_f32_16x16x32_bf16 v[220:223],v[92:95],v[152:155],v[220:223]
ds_read_u16 v132,v132
ds_read_u16_d16_hi v134,v134
v_pk_fma_f16 v123,v122,v122,s54
v_mfma_f32_16x16x32_bf16 v[228:231],v[96:99],v[152:155],v[228:231]
ds_read_b64_tr_b16 v[88:89],v6 offset:0
v_pk_mul_f16 v121,v120,v121
v_lshlrev_b32_sdwa v136,1,v136 dst_sel:DWORD dst_unused:UNUSED_PAD src0_sel:DWORD src1_sel:WORD_1
v_pk_mul_f16 v123,v122,v123
v_mfma_f32_16x16x32_bf16 v[236:239],v[100:103],v[152:155],v[236:239]
ds_read_b64_tr_b16 v[90:91],v6 offset:1536
v_lshlrev_b32_sdwa v138,1,v138 dst_sel:DWORD dst_unused:UNUSED_PAD src0_sel:DWORD src1_sel:WORD_1
v_mfma_f32_16x16x32_bf16 v[244:247],v[104:107],v[152:155],v[244:247]
ds_read_u16 v133,v133
v_exp_f16 v120,v121
v_mfma_f32_16x16x32_bf16 v[252:255],v[108:111],v[152:155],v[252:255]
ds_read_b64_tr_b16 v[92:93],v6 offset:8
v_lshlrev_b32_sdwa v137,1,v137 dst_sel:DWORD dst_unused:UNUSED_PAD src0_sel:DWORD src1_sel:WORD_1
v_mfma_f32_16x16x32_bf16 v[144:147],v[64:67],v[40:43],v[112:115]
ds_read_b64_tr_b16 v[94:95],v6 offset:1544
v_lshlrev_b32_sdwa v139,1,v139 dst_sel:DWORD dst_unused:UNUSED_PAD src0_sel:DWORD src1_sel:WORD_1
v_exp_f16 v122,v123
v_mfma_f32_16x16x32_bf16 v[152:155],v[64:67],v[44:47],v[112:115]
ds_read_u16_d16_hi v135,v135
ds_read_b64_tr_b16 v[96:97],v6 offset:64
v_mfma_f32_16x16x32_bf16 v[156:159],v[68:71],v[44:47],v[116:119]
ds_read_u16 v136,v136
v_exp_f16_sdwa v122,v123 dst_sel:WORD_1 dst_unused:UNUSED_PRESERVE src0_sel:WORD_1
v_exp_f16_sdwa v120,v121 dst_sel:WORD_1 dst_unused:UNUSED_PRESERVE src0_sel:WORD_1
v_mfma_f32_16x16x32_bf16 v[148:151],v[68:71],v[40:43],v[116:119]
ds_read_b64_tr_b16 v[98:99],v6 offset:1600
v_pk_fma_f16 v120,v120,s55,s55
v_pk_fma_f16 v122,v122,s55,s55
v_mfma_f32_16x16x32_bf16 v[144:147],v[72:75],v[48:51],v[144:147]
v_rcp_f16 v121,v120
v_mfma_f32_16x16x32_bf16 v[152:155],v[72:75],v[52:55],v[152:155]
ds_read_b64_tr_b16 v[100:101],v6 offset:72
v_rcp_f16 v123,v122
v_mfma_f32_16x16x32_bf16 v[156:159],v[76:79],v[52:55],v[156:159]
ds_read_u16_d16_hi v138,v138
v_rcp_f16_sdwa v121,v120 dst_sel:WORD_1 dst_unused:UNUSED_PRESERVE src0_sel:WORD_1
v_mfma_f32_16x16x32_bf16 v[148:151],v[76:79],v[48:51],v[148:151]
ds_read_b64_tr_b16 v[102:103],v6 offset:1608
ds_read_b64_tr_b16 v[104:105],v6 offset:128
v_rcp_f16_sdwa v123,v122 dst_sel:WORD_1 dst_unused:UNUSED_PRESERVE src0_sel:WORD_1
v_mfma_f32_16x16x32_bf16 v[144:147],v[80:83],v[56:59],v[144:147]
ds_read_u16 v137,v137
v_fma_mix_f32 v142,v142,v121,0 op_sel:[0,1,0] op_sel_hi:[0,1,0]
v_mfma_f32_16x16x32_bf16 v[152:155],v[80:83],v[60:63],v[152:155]
s_waitcnt lgkmcnt(8)
ds_read_b64_tr_b16 v[106:107],v6 offset:1664
ds_read_b64_tr_b16 v[108:109],v6 offset:136
v_fma_mix_f32 v140,v140,v121,0 op_sel_hi:[0,1,0]
v_lshl_add_u64 v[128:129],v[128:129],0,v[130:131]
v_lshl_add_u64 v[130:131],v[132:133],0,v[134:135]
v_fma_mix_f32 v143,v143,v123,0 op_sel:[0,1,0] op_sel_hi:[0,1,0]
v_mfma_f32_16x16x32_bf16 v[156:159],v[84:87],v[60:63],v[156:159]
ds_read_u16_d16_hi v139,v139
v_fma_mix_f32 v141,v141,v123,0 op_sel_hi:[0,1,0]
v_mfma_f32_16x16x32_bf16 v[148:151],v[84:87],v[56:59],v[148:151]
ds_read_b64_tr_b16 v[110:111],v6 offset:1672
ds_read_b64_tr_b16 v[64:65],v4 offset:0
v_lshlrev_b32_sdwa v144,1,v144 dst_sel:DWORD dst_unused:UNUSED_PAD src0_sel:DWORD src1_sel:WORD_1
v_lshlrev_b32_sdwa v146,1,v146 dst_sel:DWORD dst_unused:UNUSED_PAD src0_sel:DWORD src1_sel:WORD_1
v_mfma_f32_16x16x32_bf16 v[160:163],v[88:91],v[128:131],v[160:163]
ds_read_u16 v144,v144
v_lshlrev_b32_sdwa v145,1,v145 dst_sel:DWORD dst_unused:UNUSED_PAD src0_sel:DWORD src1_sel:WORD_1
v_lshlrev_b32_sdwa v147,1,v147 dst_sel:DWORD dst_unused:UNUSED_PAD src0_sel:DWORD src1_sel:WORD_1
v_fma_mixlo_f16 v122,v157,s99,0
v_mfma_f32_16x16x32_bf16 v[168:171],v[92:95],v[128:131],v[168:171]
s_waitcnt lgkmcnt(11)
ds_read_b64_tr_b16 v[66:67],v4 offset:2048
ds_read_b128 v[112:115],v7 offset:768
v_mfma_f32_16x16x32_bf16 v[176:179],v[96:99],v[128:131],v[176:179]
s_waitcnt lgkmcnt(10)
ds_read_u16_d16_hi v146,v146
v_fma_mixlo_f16 v120,v156,s99,0
v_lshlrev_b32_sdwa v148,1,v148 dst_sel:DWORD dst_unused:UNUSED_PAD src0_sel:DWORD src1_sel:WORD_1
v_lshlrev_b32_sdwa v150,1,v150 dst_sel:DWORD dst_unused:UNUSED_PAD src0_sel:DWORD src1_sel:WORD_1
v_mfma_f32_16x16x32_bf16 v[184:187],v[100:103],v[128:131],v[184:187]
s_waitcnt lgkmcnt(6)
ds_read_b64_tr_b16 v[68:69],v5 offset:0
ds_read_b64_tr_b16 v[70:71],v5 offset:2048
v_lshl_add_u64 v[136:137],v[136:137],0,v[138:139]
v_fma_mixhi_f16 v120,v158,s99,0
v_mfma_f32_16x16x32_bf16 v[192:195],v[104:107],v[128:131],v[192:195]
s_waitcnt lgkmcnt(7)
ds_read_u16 v145,v145
v_cvt_pk_bf16_f32 v138,v140,v142
v_cvt_pk_bf16_f32 v139,v141,v143
v_mfma_f32_16x16x32_bf16 v[200:203],v[108:111],v[128:131],v[200:203]
v_fma_mixhi_f16 v122,v159,s99,0
v_mfma_f32_16x16x32_bf16 v[164:167],v[88:91],v[136:139],v[164:167]
ds_read_b128 v[116:119],v7 offset:800
ds_read_b64_tr_b16 v[72:73],v4 offset:4096
v_lshlrev_b32_sdwa v149,1,v149 dst_sel:DWORD dst_unused:UNUSED_PAD src0_sel:DWORD src1_sel:WORD_1
v_lshlrev_b32_sdwa v151,1,v151 dst_sel:DWORD dst_unused:UNUSED_PAD src0_sel:DWORD src1_sel:WORD_1
v_pk_fma_f16 v121,v120,v120,s54
v_mfma_f32_16x16x32_bf16 v[172:175],v[92:95],v[136:139],v[172:175]
ds_read_b64_tr_b16 v[74:75],v4 offset:6144
ds_read_b64_tr_b16 v[76:77],v5 offset:4096
v_pk_fma_f16 v123,v122,v122,s54
v_mfma_f32_16x16x32_bf16 v[180:183],v[96:99],v[136:139],v[180:183]
ds_read_u16_d16_hi v147,v147
v_pk_mul_f16 v121,v120,v121
v_pk_mul_f16 v123,v122,v123
v_mfma_f32_16x16x32_bf16 v[188:191],v[100:103],v[136:139],v[188:191]
ds_read_u16 v148,v148
v_lshlrev_b32_sdwa v152,1,v152 dst_sel:DWORD dst_unused:UNUSED_PAD src0_sel:DWORD src1_sel:WORD_1
v_lshlrev_b32_sdwa v154,1,v154 dst_sel:DWORD dst_unused:UNUSED_PAD src0_sel:DWORD src1_sel:WORD_1
v_mfma_f32_16x16x32_bf16 v[196:199],v[104:107],v[136:139],v[196:199]
v_exp_f16 v120,v121
v_mfma_f32_16x16x32_bf16 v[204:207],v[108:111],v[136:139],v[204:207]
s_waitcnt lgkmcnt(10)
ds_read_b64_tr_b16 v[78:79],v5 offset:6144
ds_read_b64_tr_b16 v[80:81],v4 offset:8192
v_exp_f16 v122,v123
v_mfma_f32_16x16x32_bf16 v[128:131],v[64:67],v[16:19],v[112:115]
v_lshlrev_b32_sdwa v153,1,v153 dst_sel:DWORD dst_unused:UNUSED_PAD src0_sel:DWORD src1_sel:WORD_1
v_lshlrev_b32_sdwa v155,1,v155 dst_sel:DWORD dst_unused:UNUSED_PAD src0_sel:DWORD src1_sel:WORD_1
v_mfma_f32_16x16x32_bf16 v[136:139],v[64:67],v[20:23],v[112:115]
s_waitcnt lgkmcnt(7)
ds_read_u16_d16_hi v150,v150
ds_read_u16 v149,v149
v_exp_f16_sdwa v120,v121 dst_sel:WORD_1 dst_unused:UNUSED_PRESERVE src0_sel:WORD_1
v_mfma_f32_16x16x32_bf16 v[140:143],v[68:71],v[20:23],v[116:119]
v_exp_f16_sdwa v122,v123 dst_sel:WORD_1 dst_unused:UNUSED_PRESERVE src0_sel:WORD_1
v_mfma_f32_16x16x32_bf16 v[132:135],v[68:71],v[16:19],v[116:119]
s_waitcnt lgkmcnt(7)
ds_read_b64_tr_b16 v[82:83],v4 offset:10240
v_pk_fma_f16 v120,v120,s55,s55
v_mfma_f32_16x16x32_bf16 v[128:131],v[72:75],v[24:27],v[128:131]
ds_read_b64_tr_b16 v[84:85],v5 offset:8192
v_pk_fma_f16 v122,v122,s55,s55
v_rcp_f16 v121,v120
v_mfma_f32_16x16x32_bf16 v[136:139],v[72:75],v[28:31],v[136:139]
s_waitcnt lgkmcnt(5)
ds_read_b64_tr_b16 v[86:87],v5 offset:10240
ds_read_u16_d16_hi v151,v151
v_rcp_f16 v123,v122
v_mfma_f32_16x16x32_bf16 v[140:143],v[76:79],v[28:31],v[140:143]
ds_read_u16 v152,v152
ds_read_u16_d16_hi v154,v154
v_rcp_f16_sdwa v121,v120 dst_sel:WORD_1 dst_unused:UNUSED_PRESERVE src0_sel:WORD_1
v_mfma_f32_16x16x32_bf16 v[132:135],v[76:79],v[24:27],v[132:135]
s_waitcnt lgkmcnt(5)
v_rcp_f16_sdwa v123,v122 dst_sel:WORD_1 dst_unused:UNUSED_PRESERVE src0_sel:WORD_1
v_mfma_f32_16x16x32_bf16 v[128:131],v[80:83],v[32:35],v[128:131]
ds_read_u16 v153,v153
v_fma_mix_f32 v156,v156,v121,0 op_sel_hi:[0,1,0]
v_fma_mix_f32 v158,v158,v121,0 op_sel:[0,1,0] op_sel_hi:[0,1,0]
v_mfma_f32_16x16x32_bf16 v[136:139],v[80:83],v[36:39],v[136:139]
s_waitcnt lgkmcnt(3)
ds_read_u16_d16_hi v155,v155
v_lshl_add_u64 v[144:145],v[144:145],0,v[146:147]
v_lshl_add_u64 v[146:147],v[148:149],0,v[150:151]
v_mfma_f32_16x16x32_bf16 v[140:143],v[84:87],v[36:39],v[140:143]
v_fma_mix_f32 v157,v157,v123,0 op_sel_hi:[0,1,0]
v_fma_mix_f32 v159,v159,v123,0 op_sel:[0,1,0] op_sel_hi:[0,1,0]
v_mfma_f32_16x16x32_bf16 v[132:135],v[84:87],v[32:35],v[132:135]
v_lshlrev_b32_sdwa v128,1,v128 dst_sel:DWORD dst_unused:UNUSED_PAD src0_sel:DWORD src1_sel:WORD_1
v_lshlrev_b32_sdwa v130,1,v130 dst_sel:DWORD dst_unused:UNUSED_PAD src0_sel:DWORD src1_sel:WORD_1
v_mfma_f32_16x16x32_bf16 v[208:211],v[88:91],v[144:147],v[208:211]
v_lshlrev_b32_sdwa v129,1,v129 dst_sel:DWORD dst_unused:UNUSED_PAD src0_sel:DWORD src1_sel:WORD_1
v_lshlrev_b32_sdwa v131,1,v131 dst_sel:DWORD dst_unused:UNUSED_PAD src0_sel:DWORD src1_sel:WORD_1
v_mfma_f32_16x16x32_bf16 v[216:219],v[92:95],v[144:147],v[216:219]
ds_read_u16 v128,v128
ds_read_u16_d16_hi v130,v130
v_fma_mixlo_f16 v120,v140,s99,0
v_fma_mixlo_f16 v122,v141,s99,0
v_mfma_f32_16x16x32_bf16 v[224:227],v[96:99],v[144:147],v[224:227]
ds_read_u16 v129,v129
v_lshlrev_b32_sdwa v132,1,v132 dst_sel:DWORD dst_unused:UNUSED_PAD src0_sel:DWORD src1_sel:WORD_1
v_lshlrev_b32_sdwa v134,1,v134 dst_sel:DWORD dst_unused:UNUSED_PAD src0_sel:DWORD src1_sel:WORD_1
v_mfma_f32_16x16x32_bf16 v[232:235],v[100:103],v[144:147],v[232:235]
s_waitcnt lgkmcnt(3)
v_lshl_add_u64 v[152:153],v[152:153],0,v[154:155]
v_cvt_pk_bf16_f32 v154,v156,v158
v_mfma_f32_16x16x32_bf16 v[240:243],v[104:107],v[144:147],v[240:243]
v_fma_mixhi_f16 v120,v142,s99,0
v_cvt_pk_bf16_f32 v155,v157,v159
v_mfma_f32_16x16x32_bf16 v[248:251],v[108:111],v[144:147],v[248:251]
ds_read_u16_d16_hi v131,v131
v_fma_mixhi_f16 v122,v143,s99,0
v_mfma_f32_16x16x32_bf16 v[212:215],v[88:91],v[152:155],v[212:215]
v_lshlrev_b32_sdwa v133,1,v133 dst_sel:DWORD dst_unused:UNUSED_PAD src0_sel:DWORD src1_sel:WORD_1
v_pk_fma_f16 v121,v120,v120,s54
v_lshlrev_b32_sdwa v135,1,v135 dst_sel:DWORD dst_unused:UNUSED_PAD src0_sel:DWORD src1_sel:WORD_1
v_mfma_f32_16x16x32_bf16 v[220:223],v[92:95],v[152:155],v[220:223]
ds_read_u16 v132,v132
ds_read_u16_d16_hi v134,v134
v_pk_fma_f16 v123,v122,v122,s54
v_mfma_f32_16x16x32_bf16 v[228:231],v[96:99],v[152:155],v[228:231]
ds_read_b64_tr_b16 v[88:89],v6 offset:6160
v_pk_mul_f16 v121,v120,v121
v_lshlrev_b32_sdwa v136,1,v136 dst_sel:DWORD dst_unused:UNUSED_PAD src0_sel:DWORD src1_sel:WORD_1
v_pk_mul_f16 v123,v122,v123
v_mfma_f32_16x16x32_bf16 v[236:239],v[100:103],v[152:155],v[236:239]
s_setprio 2
ds_read_b64_tr_b16 v[90:91],v6 offset:7696
v_lshlrev_b32_sdwa v138,1,v138 dst_sel:DWORD dst_unused:UNUSED_PAD src0_sel:DWORD src1_sel:WORD_1
v_mfma_f32_16x16x32_bf16 v[244:247],v[104:107],v[152:155],v[244:247]
ds_read_u16 v133,v133
v_exp_f16 v120,v121
v_mfma_f32_16x16x32_bf16 v[252:255],v[108:111],v[152:155],v[252:255]
ds_read_b64_tr_b16 v[92:93],v6 offset:6168
v_lshlrev_b32_sdwa v137,1,v137 dst_sel:DWORD dst_unused:UNUSED_PAD src0_sel:DWORD src1_sel:WORD_1
v_mfma_f32_16x16x32_bf16 v[144:147],v[64:67],v[40:43],v[112:115]
ds_read_b64_tr_b16 v[94:95],v6 offset:7704
v_lshlrev_b32_sdwa v139,1,v139 dst_sel:DWORD dst_unused:UNUSED_PAD src0_sel:DWORD src1_sel:WORD_1
v_exp_f16 v122,v123
v_mfma_f32_16x16x32_bf16 v[152:155],v[64:67],v[44:47],v[112:115]
ds_read_u16_d16_hi v135,v135
ds_read_b64_tr_b16 v[96:97],v6 offset:6224
v_mfma_f32_16x16x32_bf16 v[156:159],v[68:71],v[44:47],v[116:119]
ds_read_u16 v136,v136
v_exp_f16_sdwa v122,v123 dst_sel:WORD_1 dst_unused:UNUSED_PRESERVE src0_sel:WORD_1
v_exp_f16_sdwa v120,v121 dst_sel:WORD_1 dst_unused:UNUSED_PRESERVE src0_sel:WORD_1
v_mfma_f32_16x16x32_bf16 v[148:151],v[68:71],v[40:43],v[116:119]
ds_read_b64_tr_b16 v[98:99],v6 offset:7760
v_pk_fma_f16 v120,v120,s55,s55
v_pk_fma_f16 v122,v122,s55,s55
v_mfma_f32_16x16x32_bf16 v[144:147],v[72:75],v[48:51],v[144:147]
v_rcp_f16 v121,v120
v_mfma_f32_16x16x32_bf16 v[152:155],v[72:75],v[52:55],v[152:155]
ds_read_b64_tr_b16 v[100:101],v6 offset:6232
v_rcp_f16 v123,v122
v_mfma_f32_16x16x32_bf16 v[156:159],v[76:79],v[52:55],v[156:159]
ds_read_u16_d16_hi v138,v138
v_rcp_f16_sdwa v121,v120 dst_sel:WORD_1 dst_unused:UNUSED_PRESERVE src0_sel:WORD_1
v_mfma_f32_16x16x32_bf16 v[148:151],v[76:79],v[48:51],v[148:151]
ds_read_b64_tr_b16 v[102:103],v6 offset:7768
ds_read_b64_tr_b16 v[104:105],v6 offset:6288
v_rcp_f16_sdwa v123,v122 dst_sel:WORD_1 dst_unused:UNUSED_PRESERVE src0_sel:WORD_1
v_mfma_f32_16x16x32_bf16 v[144:147],v[80:83],v[56:59],v[144:147]
ds_read_u16 v137,v137
v_fma_mix_f32 v142,v142,v121,0 op_sel:[0,1,0] op_sel_hi:[0,1,0]
v_mfma_f32_16x16x32_bf16 v[152:155],v[80:83],v[60:63],v[152:155]
s_waitcnt lgkmcnt(8)
ds_read_b64_tr_b16 v[106:107],v6 offset:7824
ds_read_b64_tr_b16 v[108:109],v6 offset:6296
v_fma_mix_f32 v140,v140,v121,0 op_sel_hi:[0,1,0]
v_lshl_add_u64 v[128:129],v[128:129],0,v[130:131]
v_lshl_add_u64 v[130:131],v[132:133],0,v[134:135]
v_fma_mix_f32 v143,v143,v123,0 op_sel:[0,1,0] op_sel_hi:[0,1,0]
v_mfma_f32_16x16x32_bf16 v[156:159],v[84:87],v[60:63],v[156:159]
ds_read_u16_d16_hi v139,v139
v_fma_mix_f32 v141,v141,v123,0 op_sel_hi:[0,1,0]
v_mfma_f32_16x16x32_bf16 v[148:151],v[84:87],v[56:59],v[148:151]
ds_read_b64_tr_b16 v[110:111],v6 offset:7832
ds_read_b64_tr_b16 v[64:65],v4 offset:512
v_lshlrev_b32_sdwa v144,1,v144 dst_sel:DWORD dst_unused:UNUSED_PAD src0_sel:DWORD src1_sel:WORD_1
v_lshlrev_b32_sdwa v146,1,v146 dst_sel:DWORD dst_unused:UNUSED_PAD src0_sel:DWORD src1_sel:WORD_1
v_mfma_f32_16x16x32_bf16 v[160:163],v[88:91],v[128:131],v[160:163]
ds_read_u16 v144,v144
v_lshlrev_b32_sdwa v145,1,v145 dst_sel:DWORD dst_unused:UNUSED_PAD src0_sel:DWORD src1_sel:WORD_1
v_lshlrev_b32_sdwa v147,1,v147 dst_sel:DWORD dst_unused:UNUSED_PAD src0_sel:DWORD src1_sel:WORD_1
v_fma_mixlo_f16 v122,v157,s99,0
v_mfma_f32_16x16x32_bf16 v[168:171],v[92:95],v[128:131],v[168:171]
s_waitcnt lgkmcnt(11)
ds_read_b64_tr_b16 v[66:67],v4 offset:2560
ds_read_b128 v[112:115],v7 offset:896
v_mfma_f32_16x16x32_bf16 v[176:179],v[96:99],v[128:131],v[176:179]
s_waitcnt lgkmcnt(10)
ds_read_u16_d16_hi v146,v146
v_fma_mixlo_f16 v120,v156,s99,0
v_lshlrev_b32_sdwa v148,1,v148 dst_sel:DWORD dst_unused:UNUSED_PAD src0_sel:DWORD src1_sel:WORD_1
v_lshlrev_b32_sdwa v150,1,v150 dst_sel:DWORD dst_unused:UNUSED_PAD src0_sel:DWORD src1_sel:WORD_1
v_mfma_f32_16x16x32_bf16 v[184:187],v[100:103],v[128:131],v[184:187]
s_waitcnt lgkmcnt(6)
ds_read_b64_tr_b16 v[68:69],v5 offset:512
ds_read_b64_tr_b16 v[70:71],v5 offset:2560
v_lshl_add_u64 v[136:137],v[136:137],0,v[138:139]
v_fma_mixhi_f16 v120,v158,s99,0
v_mfma_f32_16x16x32_bf16 v[192:195],v[104:107],v[128:131],v[192:195]
s_waitcnt lgkmcnt(7)
ds_read_u16 v145,v145
v_cvt_pk_bf16_f32 v138,v140,v142
v_cvt_pk_bf16_f32 v139,v141,v143
v_mfma_f32_16x16x32_bf16 v[200:203],v[108:111],v[128:131],v[200:203]
v_fma_mixhi_f16 v122,v159,s99,0
v_mfma_f32_16x16x32_bf16 v[164:167],v[88:91],v[136:139],v[164:167]
ds_read_b128 v[116:119],v7 offset:928
ds_read_b64_tr_b16 v[72:73],v4 offset:4608
v_lshlrev_b32_sdwa v149,1,v149 dst_sel:DWORD dst_unused:UNUSED_PAD src0_sel:DWORD src1_sel:WORD_1
v_lshlrev_b32_sdwa v151,1,v151 dst_sel:DWORD dst_unused:UNUSED_PAD src0_sel:DWORD src1_sel:WORD_1
v_pk_fma_f16 v121,v120,v120,s54
v_mfma_f32_16x16x32_bf16 v[172:175],v[92:95],v[136:139],v[172:175]
s_setprio 1
ds_read_b64_tr_b16 v[74:75],v4 offset:6656
ds_read_b64_tr_b16 v[76:77],v5 offset:4608
v_pk_fma_f16 v123,v122,v122,s54
v_mfma_f32_16x16x32_bf16 v[180:183],v[96:99],v[136:139],v[180:183]
ds_read_u16_d16_hi v147,v147
v_pk_mul_f16 v121,v120,v121
v_pk_mul_f16 v123,v122,v123
v_mfma_f32_16x16x32_bf16 v[188:191],v[100:103],v[136:139],v[188:191]
ds_read_u16 v148,v148
v_lshlrev_b32_sdwa v152,1,v152 dst_sel:DWORD dst_unused:UNUSED_PAD src0_sel:DWORD src1_sel:WORD_1
v_lshlrev_b32_sdwa v154,1,v154 dst_sel:DWORD dst_unused:UNUSED_PAD src0_sel:DWORD src1_sel:WORD_1
v_mfma_f32_16x16x32_bf16 v[196:199],v[104:107],v[136:139],v[196:199]
v_exp_f16 v120,v121
v_mfma_f32_16x16x32_bf16 v[204:207],v[108:111],v[136:139],v[204:207]
s_waitcnt lgkmcnt(10)
ds_read_b64_tr_b16 v[78:79],v5 offset:6656
ds_read_b64_tr_b16 v[80:81],v4 offset:8704
v_exp_f16 v122,v123
v_mfma_f32_16x16x32_bf16 v[128:131],v[64:67],v[16:19],v[112:115]
v_lshlrev_b32_sdwa v153,1,v153 dst_sel:DWORD dst_unused:UNUSED_PAD src0_sel:DWORD src1_sel:WORD_1
v_lshlrev_b32_sdwa v155,1,v155 dst_sel:DWORD dst_unused:UNUSED_PAD src0_sel:DWORD src1_sel:WORD_1
v_mfma_f32_16x16x32_bf16 v[136:139],v[64:67],v[20:23],v[112:115]
s_waitcnt lgkmcnt(7)
ds_read_u16_d16_hi v150,v150
ds_read_u16 v149,v149
v_exp_f16_sdwa v120,v121 dst_sel:WORD_1 dst_unused:UNUSED_PRESERVE src0_sel:WORD_1
v_mfma_f32_16x16x32_bf16 v[140:143],v[68:71],v[20:23],v[116:119]
v_exp_f16_sdwa v122,v123 dst_sel:WORD_1 dst_unused:UNUSED_PRESERVE src0_sel:WORD_1
v_mfma_f32_16x16x32_bf16 v[132:135],v[68:71],v[16:19],v[116:119]
s_waitcnt lgkmcnt(7)
s_setprio 0
ds_read_b64_tr_b16 v[82:83],v4 offset:10752
v_pk_fma_f16 v120,v120,s55,s55
v_mfma_f32_16x16x32_bf16 v[128:131],v[72:75],v[24:27],v[128:131]
ds_read_b64_tr_b16 v[84:85],v5 offset:8704
v_pk_fma_f16 v122,v122,s55,s55
v_rcp_f16 v121,v120
v_mfma_f32_16x16x32_bf16 v[136:139],v[72:75],v[28:31],v[136:139]
s_waitcnt lgkmcnt(5)
ds_read_b64_tr_b16 v[86:87],v5 offset:10752
ds_read_u16_d16_hi v151,v151
v_rcp_f16 v123,v122
v_mfma_f32_16x16x32_bf16 v[140:143],v[76:79],v[28:31],v[140:143]
ds_read_u16 v152,v152
ds_read_u16_d16_hi v154,v154
v_rcp_f16_sdwa v121,v120 dst_sel:WORD_1 dst_unused:UNUSED_PRESERVE src0_sel:WORD_1
v_mfma_f32_16x16x32_bf16 v[132:135],v[76:79],v[24:27],v[132:135]
s_waitcnt lgkmcnt(5)
v_rcp_f16_sdwa v123,v122 dst_sel:WORD_1 dst_unused:UNUSED_PRESERVE src0_sel:WORD_1
v_mfma_f32_16x16x32_bf16 v[128:131],v[80:83],v[32:35],v[128:131]
s_nop 0
ds_read_u16 v153,v153
v_fma_mix_f32 v156,v156,v121,0 op_sel_hi:[0,1,0]
v_fma_mix_f32 v158,v158,v121,0 op_sel:[0,1,0] op_sel_hi:[0,1,0]
v_mfma_f32_16x16x32_bf16 v[136:139],v[80:83],v[36:39],v[136:139]
s_waitcnt lgkmcnt(3)
ds_read_u16_d16_hi v155,v155
v_lshl_add_u64 v[144:145],v[144:145],0,v[146:147]
v_lshl_add_u64 v[146:147],v[148:149],0,v[150:151]
v_mfma_f32_16x16x32_bf16 v[140:143],v[84:87],v[36:39],v[140:143]
v_fma_mix_f32 v157,v157,v123,0 op_sel_hi:[0,1,0]
v_fma_mix_f32 v159,v159,v123,0 op_sel:[0,1,0] op_sel_hi:[0,1,0]
v_mfma_f32_16x16x32_bf16 v[132:135],v[84:87],v[32:35],v[132:135]
s_nop 0
v_lshlrev_b32_sdwa v128,1,v128 dst_sel:DWORD dst_unused:UNUSED_PAD src0_sel:DWORD src1_sel:WORD_1
v_lshlrev_b32_sdwa v130,1,v130 dst_sel:DWORD dst_unused:UNUSED_PAD src0_sel:DWORD src1_sel:WORD_1
v_mfma_f32_16x16x32_bf16 v[208:211],v[88:91],v[144:147],v[208:211]
s_mov_b32 s86,0x1234567
s_barrier
s_setprio 2
ds_read_u16 v128,v128
v_lshlrev_b32_sdwa v129,1,v129 dst_sel:DWORD dst_unused:UNUSED_PAD src0_sel:DWORD src1_sel:WORD_1
v_mfma_f32_16x16x32_bf16 v[216:219],v[92:95],v[144:147],v[216:219]
ds_read_u16_d16_hi v130,v130
v_lshlrev_b32_sdwa v131,1,v131 dst_sel:DWORD dst_unused:UNUSED_PAD src0_sel:DWORD src1_sel:WORD_1
v_mfma_f32_16x16x32_bf16 v[224:227],v[96:99],v[144:147],v[224:227]
v_lshlrev_b32_sdwa v132,1,v132 dst_sel:DWORD dst_unused:UNUSED_PAD src0_sel:DWORD src1_sel:WORD_1
v_mfma_f32_16x16x32_bf16 v[232:235],v[100:103],v[144:147],v[232:235]
s_waitcnt lgkmcnt(2)
ds_read_u16 v129,v129
v_lshl_add_u64 v[152:153],v[152:153],0,v[154:155]
v_lshlrev_b32_sdwa v134,1,v134 dst_sel:DWORD dst_unused:UNUSED_PAD src0_sel:DWORD src1_sel:WORD_1
v_mfma_f32_16x16x32_bf16 v[240:243],v[104:107],v[144:147],v[240:243]
v_cvt_pk_bf16_f32 v155,v157,v159
v_cvt_pk_bf16_f32 v154,v156,v158
v_mfma_f32_16x16x32_bf16 v[248:251],v[108:111],v[144:147],v[248:251]
ds_read_u16_d16_hi v131,v131
v_lshlrev_b32_sdwa v133,1,v133 dst_sel:DWORD dst_unused:UNUSED_PAD src0_sel:DWORD src1_sel:WORD_1
v_mfma_f32_16x16x32_bf16 v[212:215],v[88:91],v[152:155],v[212:215]
ds_read_u16 v132,v132
v_lshlrev_b32_sdwa v135,1,v135 dst_sel:DWORD dst_unused:UNUSED_PAD src0_sel:DWORD src1_sel:WORD_1
v_mfma_f32_16x16x32_bf16 v[220:223],v[92:95],v[152:155],v[220:223]
ds_read_b64_tr_b16 v[88:89],v6 offset:12320
ds_read_b64_tr_b16 v[90:91],v6 offset:13856
v_lshlrev_b32_sdwa v136,1,v136 dst_sel:DWORD dst_unused:UNUSED_PAD src0_sel:DWORD src1_sel:WORD_1
v_mfma_f32_16x16x32_bf16 v[228:231],v[96:99],v[152:155],v[228:231]
ds_read_u16_d16_hi v134,v134
v_lshlrev_b32_sdwa v138,1,v138 dst_sel:DWORD dst_unused:UNUSED_PAD src0_sel:DWORD src1_sel:WORD_1
v_mfma_f32_16x16x32_bf16 v[236:239],v[100:103],v[152:155],v[236:239]
ds_read_u16 v133,v133
ds_read_b64_tr_b16 v[92:93],v6 offset:12328
v_mfma_f32_16x16x32_bf16 v[244:247],v[104:107],v[152:155],v[244:247]
ds_read_u16_d16_hi v135,v135
v_lshlrev_b32_sdwa v137,1,v137 dst_sel:DWORD dst_unused:UNUSED_PAD src0_sel:DWORD src1_sel:WORD_1
v_lshlrev_b32_sdwa v139,1,v139 dst_sel:DWORD dst_unused:UNUSED_PAD src0_sel:DWORD src1_sel:WORD_1
v_mfma_f32_16x16x32_bf16 v[252:255],v[108:111],v[152:155],v[252:255]
ds_read_b64_tr_b16 v[94:95],v6 offset:13864
ds_read_b64_tr_b16 v[96:97],v6 offset:12384
v_lshlrev_b32_sdwa v140,1,v140 dst_sel:DWORD dst_unused:UNUSED_PAD src0_sel:DWORD src1_sel:WORD_1
v_mfma_f32_16x16x32_bf16 v[144:147],v[64:67],v[40:43],v[112:115]
ds_read_u16 v136,v136
v_lshlrev_b32_sdwa v142,1,v142 dst_sel:DWORD dst_unused:UNUSED_PAD src0_sel:DWORD src1_sel:WORD_1
v_mfma_f32_16x16x32_bf16 v[152:155],v[64:67],v[44:47],v[112:115]
ds_read_u16_d16_hi v138,v138
v_lshlrev_b32_sdwa v141,1,v141 dst_sel:DWORD dst_unused:UNUSED_PAD src0_sel:DWORD src1_sel:WORD_1
v_mfma_f32_16x16x32_bf16 v[156:159],v[68:71],v[44:47],v[116:119]
ds_read_b64_tr_b16 v[98:99],v6 offset:13920
ds_read_b64_tr_b16 v[100:101],v6 offset:12392
v_mfma_f32_16x16x32_bf16 v[148:151],v[68:71],v[40:43],v[116:119]
ds_read_u16 v137,v137
v_lshlrev_b32_sdwa v143,1,v143 dst_sel:DWORD dst_unused:UNUSED_PAD src0_sel:DWORD src1_sel:WORD_1
v_mfma_f32_16x16x32_bf16 v[144:147],v[72:75],v[48:51],v[144:147]
ds_read_u16_d16_hi v139,v139
v_mfma_f32_16x16x32_bf16 v[152:155],v[72:75],v[52:55],v[152:155]
ds_read_b64_tr_b16 v[102:103],v6 offset:13928
ds_read_b64_tr_b16 v[104:105],v6 offset:12448
v_mfma_f32_16x16x32_bf16 v[156:159],v[76:79],v[52:55],v[156:159]
s_waitcnt lgkmcnt(15)
ds_read_u16 v140,v140
v_lshl_add_u64 v[128:129],v[128:129],0,v[130:131]
v_mfma_f32_16x16x32_bf16 v[148:151],v[76:79],v[48:51],v[148:151]
s_waitcnt lgkmcnt(11)
ds_read_u16_d16_hi v142,v142
v_lshl_add_u64 v[130:131],v[132:133],0,v[134:135]
v_mfma_f32_16x16x32_bf16 v[144:147],v[80:83],v[56:59],v[144:147]
ds_read_b64_tr_b16 v[106:107],v6 offset:13984
ds_read_b64_tr_b16 v[108:109],v6 offset:12456
v_mfma_f32_16x16x32_bf16 v[152:155],v[80:83],v[60:63],v[152:155]
ds_read_u16 v141,v141
ds_read_u16_d16_hi v143,v143
v_mfma_f32_16x16x32_bf16 v[156:159],v[84:87],v[60:63],v[156:159]
ds_read_b64_tr_b16 v[110:111],v6 offset:13992
ds_read_b64_tr_b16 v[64:65],v4 offset:12320
v_mfma_f32_16x16x32_bf16 v[148:151],v[84:87],v[56:59],v[148:151]
ds_read_b64_tr_b16 v[66:67],v4 offset:14368
v_lshlrev_b32_sdwa v144,1,v144 dst_sel:DWORD dst_unused:UNUSED_PAD src0_sel:DWORD src1_sel:WORD_1
v_lshlrev_b32_sdwa v146,1,v146 dst_sel:DWORD dst_unused:UNUSED_PAD src0_sel:DWORD src1_sel:WORD_1
v_mfma_f32_16x16x32_bf16 v[160:163],v[88:91],v[128:131],v[160:163]
s_waitcnt lgkmcnt(15)
ds_read_u16 v144,v144
v_lshlrev_b32_sdwa v145,1,v145 dst_sel:DWORD dst_unused:UNUSED_PAD src0_sel:DWORD src1_sel:WORD_1
v_lshlrev_b32_sdwa v147,1,v147 dst_sel:DWORD dst_unused:UNUSED_PAD src0_sel:DWORD src1_sel:WORD_1
v_mfma_f32_16x16x32_bf16 v[168:171],v[92:95],v[128:131],v[168:171]
s_waitcnt lgkmcnt(15)
v_mfma_f32_16x16x32_bf16 v[176:179],v[96:99],v[128:131],v[176:179]
s_waitcnt lgkmcnt(11)
ds_read_b128 v[112:115],v7 offset:1024
ds_read_b64_tr_b16 v[68:69],v5 offset:12320
v_mfma_f32_16x16x32_bf16 v[184:187],v[100:103],v[128:131],v[184:187]
s_waitcnt lgkmcnt(9)
ds_read_b64_tr_b16 v[70:71],v5 offset:14368
ds_read_u16_d16_hi v146,v146
v_lshlrev_b32_sdwa v148,1,v148 dst_sel:DWORD dst_unused:UNUSED_PAD src0_sel:DWORD src1_sel:WORD_1
v_lshl_add_u64 v[136:137],v[136:137],0,v[138:139]
v_mfma_f32_16x16x32_bf16 v[192:195],v[104:107],v[128:131],v[192:195]
s_waitcnt lgkmcnt(7)
ds_read_b128 v[116:119],v7 offset:1056
ds_read_b64_tr_b16 v[72:73],v4 offset:16416
v_lshl_add_u64 v[138:139],v[140:141],0,v[142:143]
v_lshlrev_b32_sdwa v150,1,v150 dst_sel:DWORD dst_unused:UNUSED_PAD src0_sel:DWORD src1_sel:WORD_1
v_mfma_f32_16x16x32_bf16 v[200:203],v[108:111],v[128:131],v[200:203]
ds_read_u16 v145,v145
ds_read_u16_d16_hi v147,v147
v_lshlrev_b32_sdwa v149,1,v149 dst_sel:DWORD dst_unused:UNUSED_PAD src0_sel:DWORD src1_sel:WORD_1
v_mfma_f32_16x16x32_bf16 v[164:167],v[88:91],v[136:139],v[164:167]
ds_read_u16 v148,v148
v_lshlrev_b32_sdwa v151,1,v151 dst_sel:DWORD dst_unused:UNUSED_PAD src0_sel:DWORD src1_sel:WORD_1
v_mfma_f32_16x16x32_bf16 v[172:175],v[92:95],v[136:139],v[172:175]
ds_read_b64_tr_b16 v[74:75],v4 offset:18464
v_lshlrev_b32_sdwa v152,1,v152 dst_sel:DWORD dst_unused:UNUSED_PAD src0_sel:DWORD src1_sel:WORD_1
v_mfma_f32_16x16x32_bf16 v[180:183],v[96:99],v[136:139],v[180:183]
ds_read_b64_tr_b16 v[76:77],v5 offset:16416
ds_read_b64_tr_b16 v[78:79],v5 offset:18464
v_lshlrev_b32_sdwa v154,1,v154 dst_sel:DWORD dst_unused:UNUSED_PAD src0_sel:DWORD src1_sel:WORD_1
v_mfma_f32_16x16x32_bf16 v[188:191],v[100:103],v[136:139],v[188:191]
ds_read_u16_d16_hi v150,v150
v_lshlrev_b32_sdwa v153,1,v153 dst_sel:DWORD dst_unused:UNUSED_PAD src0_sel:DWORD src1_sel:WORD_1
v_mfma_f32_16x16x32_bf16 v[196:199],v[104:107],v[136:139],v[196:199]
ds_read_b64_tr_b16 v[80:81],v4 offset:20512
ds_read_b64_tr_b16 v[82:83],v4 offset:22560
v_lshlrev_b32_sdwa v155,1,v155 dst_sel:DWORD dst_unused:UNUSED_PAD src0_sel:DWORD src1_sel:WORD_1
v_mfma_f32_16x16x32_bf16 v[204:207],v[108:111],v[136:139],v[204:207]
s_waitcnt lgkmcnt(14)
ds_read_u16 v149,v149
v_lshlrev_b32_sdwa v156,1,v156 dst_sel:DWORD dst_unused:UNUSED_PAD src0_sel:DWORD src1_sel:WORD_1
v_mfma_f32_16x16x32_bf16 v[128:131],v[64:67],v[16:19],v[112:115]
ds_read_u16_d16_hi v151,v151
v_lshlrev_b32_sdwa v158,1,v158 dst_sel:DWORD dst_unused:UNUSED_PAD src0_sel:DWORD src1_sel:WORD_1
v_mfma_f32_16x16x32_bf16 v[136:139],v[64:67],v[20:23],v[112:115]
s_waitcnt lgkmcnt(12)
ds_read_u16 v152,v152
ds_read_b64_tr_b16 v[84:85],v5 offset:20512
v_lshlrev_b32_sdwa v157,1,v157 dst_sel:DWORD dst_unused:UNUSED_PAD src0_sel:DWORD src1_sel:WORD_1
v_mfma_f32_16x16x32_bf16 v[140:143],v[68:71],v[20:23],v[116:119]
ds_read_b64_tr_b16 v[86:87],v5 offset:22560
v_lshlrev_b32_sdwa v159,1,v159 dst_sel:DWORD dst_unused:UNUSED_PAD src0_sel:DWORD src1_sel:WORD_1
v_mfma_f32_16x16x32_bf16 v[132:135],v[68:71],v[16:19],v[116:119]
s_waitcnt lgkmcnt(10)
ds_read_u16_d16_hi v154,v154
v_mfma_f32_16x16x32_bf16 v[128:131],v[72:75],v[24:27],v[128:131]
ds_read_u16 v153,v153
v_mfma_f32_16x16x32_bf16 v[136:139],v[72:75],v[28:31],v[136:139]
s_waitcnt lgkmcnt(10)
v_mfma_f32_16x16x32_bf16 v[140:143],v[76:79],v[28:31],v[140:143]
ds_read_u16_d16_hi v155,v155
v_lshl_add_u64 v[144:145],v[144:145],0,v[146:147]
v_mfma_f32_16x16x32_bf16 v[132:135],v[76:79],v[24:27],v[132:135]
s_waitcnt lgkmcnt(8)
ds_read_u16 v156,v156
v_mfma_f32_16x16x32_bf16 v[128:131],v[80:83],v[32:35],v[128:131]
v_mfma_f32_16x16x32_bf16 v[136:139],v[80:83],v[36:39],v[136:139]
s_waitcnt lgkmcnt(4)
ds_read_u16_d16_hi v158,v158
v_mfma_f32_16x16x32_bf16 v[140:143],v[84:87],v[36:39],v[140:143]
ds_read_u16 v157,v157
v_lshl_add_u64 v[146:147],v[148:149],0,v[150:151]
v_mfma_f32_16x16x32_bf16 v[132:135],v[84:87],v[32:35],v[132:135]
s_nop 0
v_lshlrev_b32_sdwa v128,1,v128 dst_sel:DWORD dst_unused:UNUSED_PAD src0_sel:DWORD src1_sel:WORD_1
v_lshlrev_b32_sdwa v130,1,v130 dst_sel:DWORD dst_unused:UNUSED_PAD src0_sel:DWORD src1_sel:WORD_1
v_mfma_f32_16x16x32_bf16 v[208:211],v[88:91],v[144:147],v[208:211]
ds_read_u16_d16_hi v159,v159
v_lshlrev_b32_sdwa v129,1,v129 dst_sel:DWORD dst_unused:UNUSED_PAD src0_sel:DWORD src1_sel:WORD_1
v_mfma_f32_16x16x32_bf16 v[216:219],v[92:95],v[144:147],v[216:219]
ds_read_u16 v128,v128
v_lshlrev_b32_sdwa v131,1,v131 dst_sel:DWORD dst_unused:UNUSED_PAD src0_sel:DWORD src1_sel:WORD_1
v_mfma_f32_16x16x32_bf16 v[224:227],v[96:99],v[144:147],v[224:227]
v_lshlrev_b32_sdwa v132,1,v132 dst_sel:DWORD dst_unused:UNUSED_PAD src0_sel:DWORD src1_sel:WORD_1
s_mov_b32 s86,0xbe801d5a
v_mfma_f32_16x16x32_bf16 v[232:235],v[100:103],v[144:147],v[232:235]
s_waitcnt lgkmcnt(5)
ds_read_u16_d16_hi v130,v130
v_lshl_add_u64 v[152:153],v[152:153],0,v[154:155]
v_lshlrev_b32_sdwa v134,1,v134 dst_sel:DWORD dst_unused:UNUSED_PAD src0_sel:DWORD src1_sel:WORD_1
v_mfma_f32_16x16x32_bf16 v[240:243],v[104:107],v[144:147],v[240:243]
s_waitcnt lgkmcnt(2)
v_lshl_add_u64 v[154:155],v[156:157],0,v[158:159]
v_mfma_f32_16x16x32_bf16 v[248:251],v[108:111],v[144:147],v[248:251]
ds_read_u16 v129,v129
v_lshlrev_b32_sdwa v133,1,v133 dst_sel:DWORD dst_unused:UNUSED_PAD src0_sel:DWORD src1_sel:WORD_1
v_mfma_f32_16x16x32_bf16 v[212:215],v[88:91],v[152:155],v[212:215]
ds_read_u16_d16_hi v131,v131
v_lshlrev_b32_sdwa v135,1,v135 dst_sel:DWORD dst_unused:UNUSED_PAD src0_sel:DWORD src1_sel:WORD_1
v_mfma_f32_16x16x32_bf16 v[220:223],v[92:95],v[152:155],v[220:223]
ds_read_b64_tr_b16 v[88:89],v6 offset:18480
ds_read_b64_tr_b16 v[90:91],v6 offset:20016
v_lshlrev_b32_sdwa v136,1,v136 dst_sel:DWORD dst_unused:UNUSED_PAD src0_sel:DWORD src1_sel:WORD_1
v_mfma_f32_16x16x32_bf16 v[228:231],v[96:99],v[152:155],v[228:231]
ds_read_u16 v132,v132
v_lshlrev_b32_sdwa v138,1,v138 dst_sel:DWORD dst_unused:UNUSED_PAD src0_sel:DWORD src1_sel:WORD_1
v_mfma_f32_16x16x32_bf16 v[236:239],v[100:103],v[152:155],v[236:239]
ds_read_u16_d16_hi v134,v134
ds_read_b64_tr_b16 v[92:93],v6 offset:18488
v_mfma_f32_16x16x32_bf16 v[244:247],v[104:107],v[152:155],v[244:247]
ds_read_u16 v133,v133
v_lshlrev_b32_sdwa v137,1,v137 dst_sel:DWORD dst_unused:UNUSED_PAD src0_sel:DWORD src1_sel:WORD_1
v_lshlrev_b32_sdwa v139,1,v139 dst_sel:DWORD dst_unused:UNUSED_PAD src0_sel:DWORD src1_sel:WORD_1
v_mfma_f32_16x16x32_bf16 v[252:255],v[108:111],v[152:155],v[252:255]
ds_read_b64_tr_b16 v[94:95],v6 offset:20024
ds_read_b64_tr_b16 v[96:97],v6 offset:18544
v_lshlrev_b32_sdwa v140,1,v140 dst_sel:DWORD dst_unused:UNUSED_PAD src0_sel:DWORD src1_sel:WORD_1
v_mfma_f32_16x16x32_bf16 v[144:147],v[64:67],v[40:43],v[112:115]
ds_read_u16_d16_hi v135,v135
v_lshlrev_b32_sdwa v142,1,v142 dst_sel:DWORD dst_unused:UNUSED_PAD src0_sel:DWORD src1_sel:WORD_1
v_mfma_f32_16x16x32_bf16 v[152:155],v[64:67],v[44:47],v[112:115]
ds_read_u16 v136,v136
v_lshlrev_b32_sdwa v141,1,v141 dst_sel:DWORD dst_unused:UNUSED_PAD src0_sel:DWORD src1_sel:WORD_1
v_mfma_f32_16x16x32_bf16 v[156:159],v[68:71],v[44:47],v[116:119]
ds_read_b64_tr_b16 v[98:99],v6 offset:20080
ds_read_b64_tr_b16 v[100:101],v6 offset:18552
v_mfma_f32_16x16x32_bf16 v[148:151],v[68:71],v[40:43],v[116:119]
ds_read_u16_d16_hi v138,v138
v_lshlrev_b32_sdwa v143,1,v143 dst_sel:DWORD dst_unused:UNUSED_PAD src0_sel:DWORD src1_sel:WORD_1
v_mfma_f32_16x16x32_bf16 v[144:147],v[72:75],v[48:51],v[144:147]
ds_read_u16 v137,v137
v_mfma_f32_16x16x32_bf16 v[152:155],v[72:75],v[52:55],v[152:155]
ds_read_b64_tr_b16 v[102:103],v6 offset:20088
ds_read_b64_tr_b16 v[104:105],v6 offset:18608
v_mfma_f32_16x16x32_bf16 v[156:159],v[76:79],v[52:55],v[156:159]
s_waitcnt lgkmcnt(15)
ds_read_u16_d16_hi v139,v139
v_lshl_add_u64 v[128:129],v[128:129],0,v[130:131]
v_mfma_f32_16x16x32_bf16 v[148:151],v[76:79],v[48:51],v[148:151]
s_waitcnt lgkmcnt(8)
ds_read_u16 v140,v140
v_lshl_add_u64 v[130:131],v[132:133],0,v[134:135]
v_mfma_f32_16x16x32_bf16 v[144:147],v[80:83],v[56:59],v[144:147]
ds_read_b64_tr_b16 v[106:107],v6 offset:20144
ds_read_b64_tr_b16 v[108:109],v6 offset:18616
v_mfma_f32_16x16x32_bf16 v[152:155],v[80:83],v[60:63],v[152:155]
ds_read_u16_d16_hi v142,v142
ds_read_u16 v141,v141
v_mfma_f32_16x16x32_bf16 v[156:159],v[84:87],v[60:63],v[156:159]
ds_read_b64_tr_b16 v[110:111],v6 offset:20152
ds_read_b64_tr_b16 v[64:65],v4 offset:12832
v_mfma_f32_16x16x32_bf16 v[148:151],v[84:87],v[56:59],v[148:151]
ds_read_u16_d16_hi v143,v143
v_lshlrev_b32_sdwa v144,1,v144 dst_sel:DWORD dst_unused:UNUSED_PAD src0_sel:DWORD src1_sel:WORD_1
v_lshlrev_b32_sdwa v146,1,v146 dst_sel:DWORD dst_unused:UNUSED_PAD src0_sel:DWORD src1_sel:WORD_1
v_mfma_f32_16x16x32_bf16 v[160:163],v[88:91],v[128:131],v[160:163]
ds_read_u16 v144,v144
v_lshlrev_b32_sdwa v145,1,v145 dst_sel:DWORD dst_unused:UNUSED_PAD src0_sel:DWORD src1_sel:WORD_1
v_lshlrev_b32_sdwa v147,1,v147 dst_sel:DWORD dst_unused:UNUSED_PAD src0_sel:DWORD src1_sel:WORD_1
v_mfma_f32_16x16x32_bf16 v[168:171],v[92:95],v[128:131],v[168:171]
s_waitcnt lgkmcnt(15)
v_mfma_f32_16x16x32_bf16 v[176:179],v[96:99],v[128:131],v[176:179]
s_waitcnt lgkmcnt(11)
ds_read_b64_tr_b16 v[66:67],v4 offset:14880
ds_read_b128 v[112:115],v7 offset:1152
v_mfma_f32_16x16x32_bf16 v[184:187],v[100:103],v[128:131],v[184:187]
s_waitcnt lgkmcnt(9)
ds_read_b64_tr_b16 v[68:69],v5 offset:12832
ds_read_u16_d16_hi v146,v146
v_lshlrev_b32_sdwa v148,1,v148 dst_sel:DWORD dst_unused:UNUSED_PAD src0_sel:DWORD src1_sel:WORD_1
v_lshl_add_u64 v[136:137],v[136:137],0,v[138:139]
v_mfma_f32_16x16x32_bf16 v[192:195],v[104:107],v[128:131],v[192:195]
s_waitcnt lgkmcnt(5)
ds_read_b64_tr_b16 v[70:71],v5 offset:14880
ds_read_b128 v[116:119],v7 offset:1184
v_lshl_add_u64 v[138:139],v[140:141],0,v[142:143]
v_lshlrev_b32_sdwa v150,1,v150 dst_sel:DWORD dst_unused:UNUSED_PAD src0_sel:DWORD src1_sel:WORD_1
v_mfma_f32_16x16x32_bf16 v[200:203],v[108:111],v[128:131],v[200:203]
ds_read_u16 v145,v145
ds_read_u16_d16_hi v147,v147
v_lshlrev_b32_sdwa v149,1,v149 dst_sel:DWORD dst_unused:UNUSED_PAD src0_sel:DWORD src1_sel:WORD_1
v_mfma_f32_16x16x32_bf16 v[164:167],v[88:91],v[136:139],v[164:167]
ds_read_u16 v148,v148
v_lshlrev_b32_sdwa v151,1,v151 dst_sel:DWORD dst_unused:UNUSED_PAD src0_sel:DWORD src1_sel:WORD_1
v_mfma_f32_16x16x32_bf16 v[172:175],v[92:95],v[136:139],v[172:175]
ds_read_b64_tr_b16 v[72:73],v4 offset:16928
v_lshlrev_b32_sdwa v152,1,v152 dst_sel:DWORD dst_unused:UNUSED_PAD src0_sel:DWORD src1_sel:WORD_1
v_mfma_f32_16x16x32_bf16 v[180:183],v[96:99],v[136:139],v[180:183]
ds_read_b64_tr_b16 v[74:75],v4 offset:18976
ds_read_b64_tr_b16 v[76:77],v5 offset:16928
v_lshlrev_b32_sdwa v154,1,v154 dst_sel:DWORD dst_unused:UNUSED_PAD src0_sel:DWORD src1_sel:WORD_1
v_mfma_f32_16x16x32_bf16 v[188:191],v[100:103],v[136:139],v[188:191]
ds_read_u16_d16_hi v150,v150
v_lshlrev_b32_sdwa v153,1,v153 dst_sel:DWORD dst_unused:UNUSED_PAD src0_sel:DWORD src1_sel:WORD_1
v_mfma_f32_16x16x32_bf16 v[196:199],v[104:107],v[136:139],v[196:199]
ds_read_b64_tr_b16 v[78:79],v5 offset:18976
ds_read_b64_tr_b16 v[80:81],v4 offset:21024
v_lshlrev_b32_sdwa v155,1,v155 dst_sel:DWORD dst_unused:UNUSED_PAD src0_sel:DWORD src1_sel:WORD_1
v_mfma_f32_16x16x32_bf16 v[204:207],v[108:111],v[136:139],v[204:207]
s_waitcnt lgkmcnt(13)
ds_read_u16 v149,v149
v_lshlrev_b32_sdwa v156,1,v156 dst_sel:DWORD dst_unused:UNUSED_PAD src0_sel:DWORD src1_sel:WORD_1
v_mfma_f32_16x16x32_bf16 v[128:131],v[64:67],v[16:19],v[112:115]
ds_read_u16_d16_hi v151,v151
v_lshlrev_b32_sdwa v158,1,v158 dst_sel:DWORD dst_unused:UNUSED_PAD src0_sel:DWORD src1_sel:WORD_1
v_mfma_f32_16x16x32_bf16 v[136:139],v[64:67],v[20:23],v[112:115]
s_waitcnt lgkmcnt(11)
ds_read_u16 v152,v152
ds_read_b64_tr_b16 v[82:83],v4 offset:23072
v_lshlrev_b32_sdwa v157,1,v157 dst_sel:DWORD dst_unused:UNUSED_PAD src0_sel:DWORD src1_sel:WORD_1
v_mfma_f32_16x16x32_bf16 v[140:143],v[68:71],v[20:23],v[116:119]
ds_read_b64_tr_b16 v[84:85],v5 offset:21024
ds_read_b64_tr_b16 v[86:87],v5 offset:23072
v_lshlrev_b32_sdwa v159,1,v159 dst_sel:DWORD dst_unused:UNUSED_PAD src0_sel:DWORD src1_sel:WORD_1
v_mfma_f32_16x16x32_bf16 v[132:135],v[68:71],v[16:19],v[116:119]
s_waitcnt lgkmcnt(10)
ds_read_u16_d16_hi v154,v154
v_mfma_f32_16x16x32_bf16 v[128:131],v[72:75],v[24:27],v[128:131]
ds_read_u16 v153,v153
v_mfma_f32_16x16x32_bf16 v[136:139],v[72:75],v[28:31],v[136:139]
s_waitcnt lgkmcnt(9)
v_mfma_f32_16x16x32_bf16 v[140:143],v[76:79],v[28:31],v[140:143]
s_nop 0
ds_read_u16_d16_hi v155,v155
v_lshl_add_u64 v[144:145],v[144:145],0,v[146:147]
v_mfma_f32_16x16x32_bf16 v[132:135],v[76:79],v[24:27],v[132:135]
s_waitcnt lgkmcnt(5)
ds_read_u16 v156,v156
v_mfma_f32_16x16x32_bf16 v[128:131],v[80:83],v[32:35],v[128:131]
v_mfma_f32_16x16x32_bf16 v[136:139],v[80:83],v[36:39],v[136:139]
s_waitcnt lgkmcnt(4)
ds_read_u16_d16_hi v158,v158
v_mfma_f32_16x16x32_bf16 v[140:143],v[84:87],v[36:39],v[140:143]
s_nop 0
ds_read_u16 v157,v157
v_lshl_add_u64 v[146:147],v[148:149],0,v[150:151]
v_mfma_f32_16x16x32_bf16 v[132:135],v[84:87],v[32:35],v[132:135]
s_nop 0
v_lshlrev_b32_sdwa v128,1,v128 dst_sel:DWORD dst_unused:UNUSED_PAD src0_sel:DWORD src1_sel:WORD_1
v_lshlrev_b32_sdwa v130,1,v130 dst_sel:DWORD dst_unused:UNUSED_PAD src0_sel:DWORD src1_sel:WORD_1
v_mfma_f32_16x16x32_bf16 v[208:211],v[88:91],v[144:147],v[208:211]
s_mov_b32 s86,0x1234567
s_barrier
ds_read_u16_d16_hi v159,v159
v_lshlrev_b32_sdwa v129,1,v129 dst_sel:DWORD dst_unused:UNUSED_PAD src0_sel:DWORD src1_sel:WORD_1
v_lshlrev_b32_sdwa v131,1,v131 dst_sel:DWORD dst_unused:UNUSED_PAD src0_sel:DWORD src1_sel:WORD_1
v_mfma_f32_16x16x32_bf16 v[216:219],v[92:95],v[144:147],v[216:219]
v_fma_mixlo_f16 v120,v141,s99,0
v_lshlrev_b32_sdwa v132,1,v132 dst_sel:DWORD dst_unused:UNUSED_PAD src0_sel:DWORD src1_sel:WORD_1
v_mfma_f32_16x16x32_bf16 v[224:227],v[96:99],v[144:147],v[224:227]
ds_read_u16 v128,v128
v_fma_mixhi_f16 v120,v143,s99,0
v_lshlrev_b32_sdwa v134,1,v134 dst_sel:DWORD dst_unused:UNUSED_PAD src0_sel:DWORD src1_sel:WORD_1
v_mfma_f32_16x16x32_bf16 v[232:235],v[100:103],v[144:147],v[232:235]
s_waitcnt lgkmcnt(5)
ds_read_u16_d16_hi v130,v130
v_lshl_add_u64 v[152:153],v[152:153],0,v[154:155]
v_mfma_f32_16x16x32_bf16 v[240:243],v[104:107],v[144:147],v[240:243]
s_waitcnt lgkmcnt(2)
ds_read_u16 v129,v129
v_lshl_add_u64 v[154:155],v[156:157],0,v[158:159]
v_mfma_f32_16x16x32_bf16 v[248:251],v[108:111],v[144:147],v[248:251]
v_pk_fma_f16 v121,v120,v120,s54
v_lshlrev_b32_sdwa v133,1,v133 dst_sel:DWORD dst_unused:UNUSED_PAD src0_sel:DWORD src1_sel:WORD_1
v_mfma_f32_16x16x32_bf16 v[212:215],v[88:91],v[152:155],v[212:215]
ds_read_u16_d16_hi v131,v131
ds_read_b64_tr_b16 v[88:89],v6 offset:32832
v_pk_mul_f16 v121,v120,v121
v_lshlrev_b32_sdwa v135,1,v135 dst_sel:DWORD dst_unused:UNUSED_PAD src0_sel:DWORD src1_sel:WORD_1
v_mfma_f32_16x16x32_bf16 v[220:223],v[92:95],v[152:155],v[220:223]
ds_read_u16 v132,v132
v_exp_f16 v120,v121
v_mfma_f32_16x16x32_bf16 v[228:231],v[96:99],v[152:155],v[228:231]
ds_read_b64_tr_b16 v[90:91],v6 offset:34368
ds_read_b64_tr_b16 v[92:93],v6 offset:32840
v_lshlrev_b32_sdwa v136,1,v136 dst_sel:DWORD dst_unused:UNUSED_PAD src0_sel:DWORD src1_sel:WORD_1
v_mfma_f32_16x16x32_bf16 v[236:239],v[100:103],v[152:155],v[236:239]
ds_read_u16_d16_hi v134,v134
v_exp_f16_sdwa v120,v121 dst_sel:WORD_1 dst_unused:UNUSED_PRESERVE src0_sel:WORD_1
v_mfma_f32_16x16x32_bf16 v[244:247],v[104:107],v[152:155],v[244:247]
v_lshlrev_b32_sdwa v138,1,v138 dst_sel:DWORD dst_unused:UNUSED_PAD src0_sel:DWORD src1_sel:WORD_1
v_mfma_f32_16x16x32_bf16 v[252:255],v[108:111],v[152:155],v[252:255]
ds_read_b64_tr_b16 v[94:95],v6 offset:34376
ds_read_b64_tr_b16 v[96:97],v6 offset:32896
v_pk_fma_f16 v120,v120,s55,s55
v_lshlrev_b32_sdwa v137,1,v137 dst_sel:DWORD dst_unused:UNUSED_PAD src0_sel:DWORD src1_sel:WORD_1
v_mfma_f32_16x16x32_bf16 v[144:147],v[64:67],v[40:43],v[112:115]
ds_read_u16 v133,v133
v_mfma_f32_16x16x32_bf16 v[152:155],v[64:67],v[44:47],v[112:115]
ds_read_b64_tr_b16 v[98:99],v6 offset:34432
ds_read_b64_tr_b16 v[100:101],v6 offset:32904
v_lshlrev_b32_sdwa v139,1,v139 dst_sel:DWORD dst_unused:UNUSED_PAD src0_sel:DWORD src1_sel:WORD_1
v_mfma_f32_16x16x32_bf16 v[156:159],v[68:71],v[44:47],v[116:119]
ds_read_u16_d16_hi v135,v135
v_rcp_f16 v121,v120
s_nop 0
v_rcp_f16_sdwa v121,v120 dst_sel:WORD_1 dst_unused:UNUSED_PRESERVE src0_sel:WORD_1
v_mfma_f32_16x16x32_bf16 v[148:151],v[68:71],v[40:43],v[116:119]
ds_read_u16 v136,v136
v_lshlrev_b32_sdwa v140,1,v140 dst_sel:DWORD dst_unused:UNUSED_PAD src0_sel:DWORD src1_sel:WORD_1
v_mfma_f32_16x16x32_bf16 v[144:147],v[72:75],v[48:51],v[144:147]
ds_read_b64_tr_b16 v[102:103],v6 offset:34440
ds_read_b64_tr_b16 v[104:105],v6 offset:32960
v_fma_mix_f32 v141,v141,v121,0 op_sel_hi:[0,1,0]
v_lshlrev_b32_sdwa v142,1,v142 dst_sel:DWORD dst_unused:UNUSED_PAD src0_sel:DWORD src1_sel:WORD_1
v_mfma_f32_16x16x32_bf16 v[152:155],v[72:75],v[52:55],v[152:155]
ds_read_u16_d16_hi v138,v138
v_fma_mix_f32 v143,v143,v121,0 op_sel:[0,1,0] op_sel_hi:[0,1,0]
v_mfma_f32_16x16x32_bf16 v[156:159],v[76:79],v[52:55],v[156:159]
ds_read_b64_tr_b16 v[106:107],v6 offset:34496
ds_read_b64_tr_b16 v[108:109],v6 offset:32968
v_mfma_f32_16x16x32_bf16 v[148:151],v[76:79],v[48:51],v[148:151]
ds_read_u16 v137,v137
v_mfma_f32_16x16x32_bf16 v[144:147],v[80:83],v[56:59],v[144:147]
ds_read_b64_tr_b16 v[110:111],v6 offset:34504
ds_read_b64_tr_b16 v[64:65],v8 offset:0
v_mfma_f32_16x16x32_bf16 v[152:155],v[80:83],v[60:63],v[152:155]
s_waitcnt lgkmcnt(9)
ds_read_u16_d16_hi v139,v139
v_lshl_add_u64 v[128:129],v[128:129],0,v[130:131]
v_lshl_add_u64 v[130:131],v[132:133],0,v[134:135]
v_mfma_f32_16x16x32_bf16 v[156:159],v[84:87],v[60:63],v[156:159]
ds_read_u16 v140,v140
v_mfma_f32_16x16x32_bf16 v[148:151],v[84:87],v[56:59],v[148:151]
ds_read_b64_tr_b16 v[66:67],v8 offset:2048
ds_read_b128 v[112:115],v7 offset:1280
v_lshlrev_b32_sdwa v144,1,v144 dst_sel:DWORD dst_unused:UNUSED_PAD src0_sel:DWORD src1_sel:WORD_1
v_lshlrev_b32_sdwa v146,1,v146 dst_sel:DWORD dst_unused:UNUSED_PAD src0_sel:DWORD src1_sel:WORD_1
v_mfma_f32_16x16x32_bf16 v[160:163],v[88:91],v[128:131],v[160:163]
ds_read_u16_d16_hi v142,v142
v_lshlrev_b32_sdwa v145,1,v145 dst_sel:DWORD dst_unused:UNUSED_PAD src0_sel:DWORD src1_sel:WORD_1
v_lshlrev_b32_sdwa v147,1,v147 dst_sel:DWORD dst_unused:UNUSED_PAD src0_sel:DWORD src1_sel:WORD_1
v_mfma_f32_16x16x32_bf16 v[168:171],v[92:95],v[128:131],v[168:171]
ds_read_b64_tr_b16 v[68:69],v9 offset:0
ds_read_b64_tr_b16 v[70:71],v9 offset:2048
v_fma_mixlo_f16 v120,v157,s99,0
v_mfma_f32_16x16x32_bf16 v[176:179],v[96:99],v[128:131],v[176:179]
s_waitcnt lgkmcnt(6)
ds_read_u16 v144,v144
v_lshl_add_u64 v[136:137],v[136:137],0,v[138:139]
v_lshlrev_b32_sdwa v148,1,v148 dst_sel:DWORD dst_unused:UNUSED_PAD src0_sel:DWORD src1_sel:WORD_1
v_fma_mixhi_f16 v120,v159,s99,0
v_lshlrev_b32_sdwa v150,1,v150 dst_sel:DWORD dst_unused:UNUSED_PAD src0_sel:DWORD src1_sel:WORD_1
v_mfma_f32_16x16x32_bf16 v[184:187],v[100:103],v[128:131],v[184:187]
ds_read_u16_d16_hi v146,v146
v_mfma_f32_16x16x32_bf16 v[192:195],v[104:107],v[128:131],v[192:195]
s_waitcnt lgkmcnt(4)
ds_read_b128 v[116:119],v7 offset:1312
ds_read_b64_tr_b16 v[72:73],v8 offset:4096
v_or_b32 v138,v142,v140
v_cvt_pk_bf16_f32 v139,v141,v143
v_mfma_f32_16x16x32_bf16 v[200:203],v[108:111],v[128:131],v[200:203]
ds_read_b64_tr_b16 v[74:75],v8 offset:6144
ds_read_b64_tr_b16 v[76:77],v9 offset:4096
v_pk_fma_f16 v121,v120,v120,s54
v_lshlrev_b32_sdwa v149,1,v149 dst_sel:DWORD dst_unused:UNUSED_PAD src0_sel:DWORD src1_sel:WORD_1
v_mfma_f32_16x16x32_bf16 v[164:167],v[88:91],v[136:139],v[164:167]
ds_read_u16 v145,v145
v_pk_mul_f16 v121,v120,v121
v_lshlrev_b32_sdwa v151,1,v151 dst_sel:DWORD dst_unused:UNUSED_PAD src0_sel:DWORD src1_sel:WORD_1
v_mfma_f32_16x16x32_bf16 v[172:175],v[92:95],v[136:139],v[172:175]
ds_read_u16_d16_hi v147,v147
v_lshlrev_b32_sdwa v152,1,v152 dst_sel:DWORD dst_unused:UNUSED_PAD src0_sel:DWORD src1_sel:WORD_1
v_exp_f16 v120,v121
v_mfma_f32_16x16x32_bf16 v[180:183],v[96:99],v[136:139],v[180:183]
ds_read_b64_tr_b16 v[78:79],v9 offset:6144
ds_read_b64_tr_b16 v[80:81],v8 offset:8192
v_mfma_f32_16x16x32_bf16 v[188:191],v[100:103],v[136:139],v[188:191]
v_exp_f16_sdwa v120,v121 dst_sel:WORD_1 dst_unused:UNUSED_PRESERVE src0_sel:WORD_1
v_mfma_f32_16x16x32_bf16 v[196:199],v[104:107],v[136:139],v[196:199]
ds_read_u16 v148,v148
ds_read_u16_d16_hi v150,v150
v_lshlrev_b32_sdwa v154,1,v154 dst_sel:DWORD dst_unused:UNUSED_PAD src0_sel:DWORD src1_sel:WORD_1
v_mfma_f32_16x16x32_bf16 v[204:207],v[108:111],v[136:139],v[204:207]
v_pk_fma_f16 v120,v120,s55,s55
v_lshlrev_b32_sdwa v153,1,v153 dst_sel:DWORD dst_unused:UNUSED_PAD src0_sel:DWORD src1_sel:WORD_1
v_mfma_f32_16x16x32_bf16 v[128:131],v[64:67],v[16:19],v[112:115]
ds_read_u16 v149,v149
ds_read_b64_tr_b16 v[82:83],v8 offset:10240
v_rcp_f16 v121,v120
v_mfma_f32_16x16x32_bf16 v[136:139],v[64:67],v[20:23],v[112:115]
s_waitcnt lgkmcnt(11)
ds_read_b64_tr_b16 v[84:85],v9 offset:8192
v_lshlrev_b32_sdwa v155,1,v155 dst_sel:DWORD dst_unused:UNUSED_PAD src0_sel:DWORD src1_sel:WORD_1
v_mfma_f32_16x16x32_bf16 v[140:143],v[68:71],v[20:23],v[116:119]
ds_read_u16_d16_hi v151,v151
v_rcp_f16_sdwa v121,v120 dst_sel:WORD_1 dst_unused:UNUSED_PRESERVE src0_sel:WORD_1
v_mfma_f32_16x16x32_bf16 v[132:135],v[68:71],v[16:19],v[116:119]
s_waitcnt lgkmcnt(11)
ds_read_u16 v152,v152
v_lshlrev_b32_sdwa v156,1,v156 dst_sel:DWORD dst_unused:UNUSED_PAD src0_sel:DWORD src1_sel:WORD_1
v_mfma_f32_16x16x32_bf16 v[128:131],v[72:75],v[24:27],v[128:131]
ds_read_b64_tr_b16 v[86:87],v9 offset:10240
v_fma_mix_f32 v157,v157,v121,0 op_sel_hi:[0,1,0]
v_mfma_f32_16x16x32_bf16 v[136:139],v[72:75],v[28:31],v[136:139]
s_waitcnt lgkmcnt(9)
ds_read_u16_d16_hi v154,v154
v_fma_mix_f32 v159,v159,v121,0 op_sel:[0,1,0] op_sel_hi:[0,1,0]
v_mfma_f32_16x16x32_bf16 v[140:143],v[76:79],v[28:31],v[140:143]
v_mfma_f32_16x16x32_bf16 v[132:135],v[76:79],v[24:27],v[132:135]
s_waitcnt lgkmcnt(5)
ds_read_u16 v153,v153
v_lshlrev_b32_sdwa v158,1,v158 dst_sel:DWORD dst_unused:UNUSED_PAD src0_sel:DWORD src1_sel:WORD_1
v_mfma_f32_16x16x32_bf16 v[128:131],v[80:83],v[32:35],v[128:131]
ds_read_u16_d16_hi v155,v155
v_mfma_f32_16x16x32_bf16 v[136:139],v[80:83],v[36:39],v[136:139]
s_waitcnt lgkmcnt(3)
ds_read_u16 v156,v156
v_lshl_add_u64 v[144:145],v[144:145],0,v[146:147]
v_mfma_f32_16x16x32_bf16 v[140:143],v[84:87],v[36:39],v[140:143]
v_lshl_add_u64 v[146:147],v[148:149],0,v[150:151]
v_mfma_f32_16x16x32_bf16 v[132:135],v[84:87],v[32:35],v[132:135]
v_lshlrev_b32_sdwa v128,1,v128 dst_sel:DWORD dst_unused:UNUSED_PAD src0_sel:DWORD src1_sel:WORD_1
v_lshlrev_b32_sdwa v130,1,v130 dst_sel:DWORD dst_unused:UNUSED_PAD src0_sel:DWORD src1_sel:WORD_1
v_mfma_f32_16x16x32_bf16 v[208:211],v[88:91],v[144:147],v[208:211]
ds_read_u16_d16_hi v158,v158
v_lshlrev_b32_sdwa v129,1,v129 dst_sel:DWORD dst_unused:UNUSED_PAD src0_sel:DWORD src1_sel:WORD_1
v_lshlrev_b32_sdwa v131,1,v131 dst_sel:DWORD dst_unused:UNUSED_PAD src0_sel:DWORD src1_sel:WORD_1
v_mfma_f32_16x16x32_bf16 v[216:219],v[92:95],v[144:147],v[216:219]
v_fma_mixlo_f16 v120,v141,s99,0
v_lshlrev_b32_sdwa v132,1,v132 dst_sel:DWORD dst_unused:UNUSED_PAD src0_sel:DWORD src1_sel:WORD_1
v_mfma_f32_16x16x32_bf16 v[224:227],v[96:99],v[144:147],v[224:227]
ds_read_u16 v128,v128
v_fma_mixhi_f16 v120,v143,s99,0
v_lshlrev_b32_sdwa v134,1,v134 dst_sel:DWORD dst_unused:UNUSED_PAD src0_sel:DWORD src1_sel:WORD_1
v_mfma_f32_16x16x32_bf16 v[232:235],v[100:103],v[144:147],v[232:235]
s_waitcnt lgkmcnt(3)
ds_read_u16_d16_hi v130,v130
v_lshl_add_u64 v[152:153],v[152:153],0,v[154:155]
v_mfma_f32_16x16x32_bf16 v[240:243],v[104:107],v[144:147],v[240:243]
s_waitcnt lgkmcnt(2)
ds_read_u16 v129,v129
v_or_b32 v154,v158,v156
v_cvt_pk_bf16_f32 v155,v157,v159
v_mfma_f32_16x16x32_bf16 v[248:251],v[108:111],v[144:147],v[248:251]
v_pk_fma_f16 v121,v120,v120,s54
v_lshlrev_b32_sdwa v133,1,v133 dst_sel:DWORD dst_unused:UNUSED_PAD src0_sel:DWORD src1_sel:WORD_1
v_mfma_f32_16x16x32_bf16 v[212:215],v[88:91],v[152:155],v[212:215]
ds_read_u16_d16_hi v131,v131
ds_read_b64_tr_b16 v[88:89],v6 offset:38992
v_pk_mul_f16 v121,v120,v121
v_lshlrev_b32_sdwa v135,1,v135 dst_sel:DWORD dst_unused:UNUSED_PAD src0_sel:DWORD src1_sel:WORD_1
v_mfma_f32_16x16x32_bf16 v[220:223],v[92:95],v[152:155],v[220:223]
ds_read_u16 v132,v132
v_lshlrev_b32_sdwa v136,1,v136 dst_sel:DWORD dst_unused:UNUSED_PAD src0_sel:DWORD src1_sel:WORD_1
v_exp_f16 v120,v121
v_mfma_f32_16x16x32_bf16 v[228:231],v[96:99],v[152:155],v[228:231]
ds_read_b64_tr_b16 v[90:91],v6 offset:40528
ds_read_b64_tr_b16 v[92:93],v6 offset:39000
v_mfma_f32_16x16x32_bf16 v[236:239],v[100:103],v[152:155],v[236:239]
v_exp_f16_sdwa v120,v121 dst_sel:WORD_1 dst_unused:UNUSED_PRESERVE src0_sel:WORD_1
v_mfma_f32_16x16x32_bf16 v[244:247],v[104:107],v[152:155],v[244:247]
ds_read_u16_d16_hi v134,v134
v_lshlrev_b32_sdwa v138,1,v138 dst_sel:DWORD dst_unused:UNUSED_PAD src0_sel:DWORD src1_sel:WORD_1
v_mfma_f32_16x16x32_bf16 v[252:255],v[108:111],v[152:155],v[252:255]
ds_read_b64_tr_b16 v[94:95],v6 offset:40536
ds_read_b64_tr_b16 v[96:97],v6 offset:39056
v_pk_fma_f16 v120,v120,s55,s55
v_lshlrev_b32_sdwa v137,1,v137 dst_sel:DWORD dst_unused:UNUSED_PAD src0_sel:DWORD src1_sel:WORD_1
v_mfma_f32_16x16x32_bf16 v[144:147],v[64:67],v[40:43],v[112:115]
ds_read_u16 v133,v133
v_rcp_f16 v121,v120
v_mfma_f32_16x16x32_bf16 v[152:155],v[64:67],v[44:47],v[112:115]
ds_read_b64_tr_b16 v[98:99],v6 offset:40592
ds_read_b64_tr_b16 v[100:101],v6 offset:39064
v_lshlrev_b32_sdwa v139,1,v139 dst_sel:DWORD dst_unused:UNUSED_PAD src0_sel:DWORD src1_sel:WORD_1
v_mfma_f32_16x16x32_bf16 v[156:159],v[68:71],v[44:47],v[116:119]
ds_read_u16_d16_hi v135,v135
v_rcp_f16_sdwa v121,v120 dst_sel:WORD_1 dst_unused:UNUSED_PRESERVE src0_sel:WORD_1
v_mfma_f32_16x16x32_bf16 v[148:151],v[68:71],v[40:43],v[116:119]
ds_read_u16 v136,v136
v_lshlrev_b32_sdwa v140,1,v140 dst_sel:DWORD dst_unused:UNUSED_PAD src0_sel:DWORD src1_sel:WORD_1
v_mfma_f32_16x16x32_bf16 v[144:147],v[72:75],v[48:51],v[144:147]
ds_read_b64_tr_b16 v[102:103],v6 offset:40600
ds_read_b64_tr_b16 v[104:105],v6 offset:39120
v_fma_mix_f32 v141,v141,v121,0 op_sel_hi:[0,1,0]
v_lshlrev_b32_sdwa v142,1,v142 dst_sel:DWORD dst_unused:UNUSED_PAD src0_sel:DWORD src1_sel:WORD_1
v_mfma_f32_16x16x32_bf16 v[152:155],v[72:75],v[52:55],v[152:155]
ds_read_u16_d16_hi v138,v138
v_fma_mix_f32 v143,v143,v121,0 op_sel:[0,1,0] op_sel_hi:[0,1,0]
v_mfma_f32_16x16x32_bf16 v[156:159],v[76:79],v[52:55],v[156:159]
ds_read_b64_tr_b16 v[106:107],v6 offset:40656
ds_read_b64_tr_b16 v[108:109],v6 offset:39128
v_mfma_f32_16x16x32_bf16 v[148:151],v[76:79],v[48:51],v[148:151]
ds_read_u16 v137,v137
v_mfma_f32_16x16x32_bf16 v[144:147],v[80:83],v[56:59],v[144:147]
ds_read_b64_tr_b16 v[110:111],v6 offset:40664
ds_read_b64_tr_b16 v[64:65],v8 offset:512
v_mfma_f32_16x16x32_bf16 v[152:155],v[80:83],v[60:63],v[152:155]
s_waitcnt lgkmcnt(9)
ds_read_u16_d16_hi v139,v139
v_lshl_add_u64 v[128:129],v[128:129],0,v[130:131]
v_lshl_add_u64 v[130:131],v[132:133],0,v[134:135]
v_mfma_f32_16x16x32_bf16 v[156:159],v[84:87],v[60:63],v[156:159]
ds_read_u16 v140,v140
v_mfma_f32_16x16x32_bf16 v[148:151],v[84:87],v[56:59],v[148:151]
ds_read_b64_tr_b16 v[66:67],v8 offset:2560
ds_read_b128 v[112:115],v7 offset:1408
v_lshlrev_b32_sdwa v144,1,v144 dst_sel:DWORD dst_unused:UNUSED_PAD src0_sel:DWORD src1_sel:WORD_1
v_lshlrev_b32_sdwa v146,1,v146 dst_sel:DWORD dst_unused:UNUSED_PAD src0_sel:DWORD src1_sel:WORD_1
v_mfma_f32_16x16x32_bf16 v[160:163],v[88:91],v[128:131],v[160:163]
ds_read_u16_d16_hi v142,v142
v_lshlrev_b32_sdwa v145,1,v145 dst_sel:DWORD dst_unused:UNUSED_PAD src0_sel:DWORD src1_sel:WORD_1
v_lshlrev_b32_sdwa v147,1,v147 dst_sel:DWORD dst_unused:UNUSED_PAD src0_sel:DWORD src1_sel:WORD_1
v_mfma_f32_16x16x32_bf16 v[168:171],v[92:95],v[128:131],v[168:171]
ds_read_b64_tr_b16 v[68:69],v9 offset:512
ds_read_b64_tr_b16 v[70:71],v9 offset:2560
v_fma_mixlo_f16 v120,v157,s99,0
v_mfma_f32_16x16x32_bf16 v[176:179],v[96:99],v[128:131],v[176:179]
s_waitcnt lgkmcnt(6)
ds_read_u16 v144,v144
v_lshl_add_u64 v[136:137],v[136:137],0,v[138:139]
v_lshlrev_b32_sdwa v148,1,v148 dst_sel:DWORD dst_unused:UNUSED_PAD src0_sel:DWORD src1_sel:WORD_1
v_fma_mixhi_f16 v120,v159,s99,0
v_lshlrev_b32_sdwa v150,1,v150 dst_sel:DWORD dst_unused:UNUSED_PAD src0_sel:DWORD src1_sel:WORD_1
v_mfma_f32_16x16x32_bf16 v[184:187],v[100:103],v[128:131],v[184:187]
ds_read_u16_d16_hi v146,v146
v_mfma_f32_16x16x32_bf16 v[192:195],v[104:107],v[128:131],v[192:195]
s_waitcnt lgkmcnt(4)
ds_read_b128 v[116:119],v7 offset:1440
ds_read_b64_tr_b16 v[72:73],v8 offset:4608
v_or_b32 v138,v142,v140
v_cvt_pk_bf16_f32 v139,v141,v143
v_mfma_f32_16x16x32_bf16 v[200:203],v[108:111],v[128:131],v[200:203]
ds_read_b64_tr_b16 v[74:75],v8 offset:6656
ds_read_b64_tr_b16 v[76:77],v9 offset:4608
v_pk_fma_f16 v121,v120,v120,s54
v_lshlrev_b32_sdwa v149,1,v149 dst_sel:DWORD dst_unused:UNUSED_PAD src0_sel:DWORD src1_sel:WORD_1
v_mfma_f32_16x16x32_bf16 v[164:167],v[88:91],v[136:139],v[164:167]
ds_read_u16 v145,v145
v_pk_mul_f16 v121,v120,v121
v_lshlrev_b32_sdwa v151,1,v151 dst_sel:DWORD dst_unused:UNUSED_PAD src0_sel:DWORD src1_sel:WORD_1
v_mfma_f32_16x16x32_bf16 v[172:175],v[92:95],v[136:139],v[172:175]
ds_read_u16_d16_hi v147,v147
v_lshlrev_b32_sdwa v152,1,v152 dst_sel:DWORD dst_unused:UNUSED_PAD src0_sel:DWORD src1_sel:WORD_1
v_exp_f16 v120,v121
v_mfma_f32_16x16x32_bf16 v[180:183],v[96:99],v[136:139],v[180:183]
ds_read_b64_tr_b16 v[78:79],v9 offset:6656
ds_read_b64_tr_b16 v[80:81],v8 offset:8704
v_mfma_f32_16x16x32_bf16 v[188:191],v[100:103],v[136:139],v[188:191]
v_exp_f16_sdwa v120,v121 dst_sel:WORD_1 dst_unused:UNUSED_PRESERVE src0_sel:WORD_1
v_mfma_f32_16x16x32_bf16 v[196:199],v[104:107],v[136:139],v[196:199]
ds_read_u16 v148,v148
ds_read_u16_d16_hi v150,v150
v_lshlrev_b32_sdwa v154,1,v154 dst_sel:DWORD dst_unused:UNUSED_PAD src0_sel:DWORD src1_sel:WORD_1
v_mfma_f32_16x16x32_bf16 v[204:207],v[108:111],v[136:139],v[204:207]
v_pk_fma_f16 v120,v120,s55,s55
v_lshlrev_b32_sdwa v153,1,v153 dst_sel:DWORD dst_unused:UNUSED_PAD src0_sel:DWORD src1_sel:WORD_1
v_mfma_f32_16x16x32_bf16 v[128:131],v[64:67],v[16:19],v[112:115]
ds_read_u16 v149,v149
ds_read_b64_tr_b16 v[82:83],v8 offset:10752
v_rcp_f16 v121,v120
v_mfma_f32_16x16x32_bf16 v[136:139],v[64:67],v[20:23],v[112:115]
s_waitcnt lgkmcnt(11)
ds_read_b64_tr_b16 v[84:85],v9 offset:8704
v_lshlrev_b32_sdwa v155,1,v155 dst_sel:DWORD dst_unused:UNUSED_PAD src0_sel:DWORD src1_sel:WORD_1
v_mfma_f32_16x16x32_bf16 v[140:143],v[68:71],v[20:23],v[116:119]
ds_read_u16_d16_hi v151,v151
v_lshlrev_b32_sdwa v156,1,v156 dst_sel:DWORD dst_unused:UNUSED_PAD src0_sel:DWORD src1_sel:WORD_1
v_rcp_f16_sdwa v121,v120 dst_sel:WORD_1 dst_unused:UNUSED_PRESERVE src0_sel:WORD_1
v_mfma_f32_16x16x32_bf16 v[132:135],v[68:71],v[16:19],v[116:119]
s_waitcnt lgkmcnt(11)
ds_read_u16 v152,v152
v_mfma_f32_16x16x32_bf16 v[128:131],v[72:75],v[24:27],v[128:131]
ds_read_b64_tr_b16 v[86:87],v9 offset:10752
v_fma_mix_f32 v157,v157,v121,0 op_sel_hi:[0,1,0]
v_mfma_f32_16x16x32_bf16 v[136:139],v[72:75],v[28:31],v[136:139]
s_waitcnt lgkmcnt(9)
ds_read_u16_d16_hi v154,v154
v_fma_mix_f32 v159,v159,v121,0 op_sel:[0,1,0] op_sel_hi:[0,1,0]
v_mfma_f32_16x16x32_bf16 v[140:143],v[76:79],v[28:31],v[140:143]
v_mfma_f32_16x16x32_bf16 v[132:135],v[76:79],v[24:27],v[132:135]
s_waitcnt lgkmcnt(5)
ds_read_u16 v153,v153
v_lshlrev_b32_sdwa v158,1,v158 dst_sel:DWORD dst_unused:UNUSED_PAD src0_sel:DWORD src1_sel:WORD_1
v_mfma_f32_16x16x32_bf16 v[128:131],v[80:83],v[32:35],v[128:131]
ds_read_u16_d16_hi v155,v155
v_mfma_f32_16x16x32_bf16 v[136:139],v[80:83],v[36:39],v[136:139]
s_waitcnt lgkmcnt(3)
ds_read_u16 v156,v156
v_lshl_add_u64 v[144:145],v[144:145],0,v[146:147]
v_mfma_f32_16x16x32_bf16 v[140:143],v[84:87],v[36:39],v[140:143]
v_lshl_add_u64 v[146:147],v[148:149],0,v[150:151]
v_mfma_f32_16x16x32_bf16 v[132:135],v[84:87],v[32:35],v[132:135]
v_lshlrev_b32_sdwa v128,1,v128 dst_sel:DWORD dst_unused:UNUSED_PAD src0_sel:DWORD src1_sel:WORD_1
v_lshlrev_b32_sdwa v130,1,v130 dst_sel:DWORD dst_unused:UNUSED_PAD src0_sel:DWORD src1_sel:WORD_1
v_mfma_f32_16x16x32_bf16 v[208:211],v[88:91],v[144:147],v[208:211]
ds_read_u16_d16_hi v158,v158
v_lshlrev_b32_sdwa v129,1,v129 dst_sel:DWORD dst_unused:UNUSED_PAD src0_sel:DWORD src1_sel:WORD_1
v_lshlrev_b32_sdwa v131,1,v131 dst_sel:DWORD dst_unused:UNUSED_PAD src0_sel:DWORD src1_sel:WORD_1
v_mfma_f32_16x16x32_bf16 v[216:219],v[92:95],v[144:147],v[216:219]
v_fma_mixlo_f16 v120,v141,s99,0
v_lshlrev_b32_sdwa v132,1,v132 dst_sel:DWORD dst_unused:UNUSED_PAD src0_sel:DWORD src1_sel:WORD_1
v_mfma_f32_16x16x32_bf16 v[224:227],v[96:99],v[144:147],v[224:227]
ds_read_u16 v128,v128
v_fma_mixhi_f16 v120,v143,s99,0
v_lshlrev_b32_sdwa v134,1,v134 dst_sel:DWORD dst_unused:UNUSED_PAD src0_sel:DWORD src1_sel:WORD_1
v_mfma_f32_16x16x32_bf16 v[232:235],v[100:103],v[144:147],v[232:235]
s_waitcnt lgkmcnt(3)
ds_read_u16_d16_hi v130,v130
v_lshl_add_u64 v[152:153],v[152:153],0,v[154:155]
v_mfma_f32_16x16x32_bf16 v[240:243],v[104:107],v[144:147],v[240:243]
s_waitcnt lgkmcnt(2)
ds_read_u16 v129,v129
v_or_b32 v154,v158,v156
v_cvt_pk_bf16_f32 v155,v157,v159
v_mfma_f32_16x16x32_bf16 v[248:251],v[108:111],v[144:147],v[248:251]
v_pk_fma_f16 v121,v120,v120,s54
v_lshlrev_b32_sdwa v133,1,v133 dst_sel:DWORD dst_unused:UNUSED_PAD src0_sel:DWORD src1_sel:WORD_1
v_mfma_f32_16x16x32_bf16 v[212:215],v[88:91],v[152:155],v[212:215]
ds_read_u16_d16_hi v131,v131
ds_read_b64_tr_b16 v[88:89],v6 offset:45152
v_pk_mul_f16 v121,v120,v121
v_lshlrev_b32_sdwa v135,1,v135 dst_sel:DWORD dst_unused:UNUSED_PAD src0_sel:DWORD src1_sel:WORD_1
v_mfma_f32_16x16x32_bf16 v[220:223],v[92:95],v[152:155],v[220:223]
ds_read_u16 v132,v132
v_exp_f16 v120,v121
v_mfma_f32_16x16x32_bf16 v[228:231],v[96:99],v[152:155],v[228:231]
ds_read_b64_tr_b16 v[90:91],v6 offset:46688
ds_read_b64_tr_b16 v[92:93],v6 offset:45160
v_lshlrev_b32_sdwa v136,1,v136 dst_sel:DWORD dst_unused:UNUSED_PAD src0_sel:DWORD src1_sel:WORD_1
v_mfma_f32_16x16x32_bf16 v[236:239],v[100:103],v[152:155],v[236:239]
ds_read_u16_d16_hi v134,v134
s_mov_b32 s86,0xbe801d5a
v_exp_f16_sdwa v120,v121 dst_sel:WORD_1 dst_unused:UNUSED_PRESERVE src0_sel:WORD_1
v_mfma_f32_16x16x32_bf16 v[244:247],v[104:107],v[152:155],v[244:247]
v_lshlrev_b32_sdwa v138,1,v138 dst_sel:DWORD dst_unused:UNUSED_PAD src0_sel:DWORD src1_sel:WORD_1
v_mfma_f32_16x16x32_bf16 v[252:255],v[108:111],v[152:155],v[252:255]
ds_read_b64_tr_b16 v[94:95],v6 offset:46696
ds_read_b64_tr_b16 v[96:97],v6 offset:45216
v_pk_fma_f16 v120,v120,s55,s55
v_lshlrev_b32_sdwa v137,1,v137 dst_sel:DWORD dst_unused:UNUSED_PAD src0_sel:DWORD src1_sel:WORD_1
v_mfma_f32_16x16x32_bf16 v[144:147],v[64:67],v[40:43],v[112:115]
ds_read_u16 v133,v133
v_rcp_f16 v121,v120
v_mfma_f32_16x16x32_bf16 v[152:155],v[64:67],v[44:47],v[112:115]
ds_read_b64_tr_b16 v[98:99],v6 offset:46752
ds_read_b64_tr_b16 v[100:101],v6 offset:45224
v_lshlrev_b32_sdwa v139,1,v139 dst_sel:DWORD dst_unused:UNUSED_PAD src0_sel:DWORD src1_sel:WORD_1
v_mfma_f32_16x16x32_bf16 v[156:159],v[68:71],v[44:47],v[116:119]
ds_read_u16_d16_hi v135,v135
v_rcp_f16_sdwa v121,v120 dst_sel:WORD_1 dst_unused:UNUSED_PRESERVE src0_sel:WORD_1
v_mfma_f32_16x16x32_bf16 v[148:151],v[68:71],v[40:43],v[116:119]
ds_read_u16 v136,v136
v_lshlrev_b32_sdwa v140,1,v140 dst_sel:DWORD dst_unused:UNUSED_PAD src0_sel:DWORD src1_sel:WORD_1
v_mfma_f32_16x16x32_bf16 v[144:147],v[72:75],v[48:51],v[144:147]
ds_read_b64_tr_b16 v[102:103],v6 offset:46760
ds_read_b64_tr_b16 v[104:105],v6 offset:45280
v_fma_mix_f32 v141,v141,v121,0 op_sel_hi:[0,1,0]
v_lshlrev_b32_sdwa v142,1,v142 dst_sel:DWORD dst_unused:UNUSED_PAD src0_sel:DWORD src1_sel:WORD_1
v_mfma_f32_16x16x32_bf16 v[152:155],v[72:75],v[52:55],v[152:155]
ds_read_u16_d16_hi v138,v138
v_fma_mix_f32 v143,v143,v121,0 op_sel:[0,1,0] op_sel_hi:[0,1,0]
v_mfma_f32_16x16x32_bf16 v[156:159],v[76:79],v[52:55],v[156:159]
ds_read_b64_tr_b16 v[106:107],v6 offset:46816
ds_read_b64_tr_b16 v[108:109],v6 offset:45288
v_mfma_f32_16x16x32_bf16 v[148:151],v[76:79],v[48:51],v[148:151]
ds_read_u16 v137,v137
v_mfma_f32_16x16x32_bf16 v[144:147],v[80:83],v[56:59],v[144:147]
ds_read_b64_tr_b16 v[110:111],v6 offset:46824
ds_read_b64_tr_b16 v[64:65],v8 offset:12320
v_mfma_f32_16x16x32_bf16 v[152:155],v[80:83],v[60:63],v[152:155]
s_waitcnt lgkmcnt(9)
ds_read_u16_d16_hi v139,v139
v_lshl_add_u64 v[128:129],v[128:129],0,v[130:131]
v_lshl_add_u64 v[130:131],v[132:133],0,v[134:135]
v_mfma_f32_16x16x32_bf16 v[156:159],v[84:87],v[60:63],v[156:159]
ds_read_u16 v140,v140
v_mfma_f32_16x16x32_bf16 v[148:151],v[84:87],v[56:59],v[148:151]
ds_read_b64_tr_b16 v[66:67],v8 offset:14368
ds_read_b128 v[112:115],v7 offset:0
v_lshlrev_b32_sdwa v144,1,v144 dst_sel:DWORD dst_unused:UNUSED_PAD src0_sel:DWORD src1_sel:WORD_1
v_lshlrev_b32_sdwa v146,1,v146 dst_sel:DWORD dst_unused:UNUSED_PAD src0_sel:DWORD src1_sel:WORD_1
v_mfma_f32_16x16x32_bf16 v[160:163],v[88:91],v[128:131],v[160:163]
ds_read_u16_d16_hi v142,v142
v_lshlrev_b32_sdwa v145,1,v145 dst_sel:DWORD dst_unused:UNUSED_PAD src0_sel:DWORD src1_sel:WORD_1
v_lshlrev_b32_sdwa v147,1,v147 dst_sel:DWORD dst_unused:UNUSED_PAD src0_sel:DWORD src1_sel:WORD_1
v_mfma_f32_16x16x32_bf16 v[168:171],v[92:95],v[128:131],v[168:171]
ds_read_b64_tr_b16 v[68:69],v9 offset:12320
ds_read_b64_tr_b16 v[70:71],v9 offset:14368
v_fma_mixlo_f16 v120,v157,s99,0
v_mfma_f32_16x16x32_bf16 v[176:179],v[96:99],v[128:131],v[176:179]
s_waitcnt lgkmcnt(6)
ds_read_u16 v144,v144
v_lshl_add_u64 v[136:137],v[136:137],0,v[138:139]
v_lshlrev_b32_sdwa v148,1,v148 dst_sel:DWORD dst_unused:UNUSED_PAD src0_sel:DWORD src1_sel:WORD_1
v_fma_mixhi_f16 v120,v159,s99,0
v_lshlrev_b32_sdwa v150,1,v150 dst_sel:DWORD dst_unused:UNUSED_PAD src0_sel:DWORD src1_sel:WORD_1
v_mfma_f32_16x16x32_bf16 v[184:187],v[100:103],v[128:131],v[184:187]
ds_read_u16_d16_hi v146,v146
v_mfma_f32_16x16x32_bf16 v[192:195],v[104:107],v[128:131],v[192:195]
s_waitcnt lgkmcnt(4)
ds_read_b128 v[116:119],v7 offset:32
ds_read_b64_tr_b16 v[72:73],v8 offset:16416
v_or_b32 v138,v142,v140
v_cvt_pk_bf16_f32 v139,v141,v143
v_mfma_f32_16x16x32_bf16 v[200:203],v[108:111],v[128:131],v[200:203]
ds_read_b64_tr_b16 v[74:75],v8 offset:18464
ds_read_b64_tr_b16 v[76:77],v9 offset:16416
v_pk_fma_f16 v121,v120,v120,s54
v_lshlrev_b32_sdwa v149,1,v149 dst_sel:DWORD dst_unused:UNUSED_PAD src0_sel:DWORD src1_sel:WORD_1
v_mfma_f32_16x16x32_bf16 v[164:167],v[88:91],v[136:139],v[164:167]
ds_read_u16 v145,v145
v_pk_mul_f16 v121,v120,v121
v_lshlrev_b32_sdwa v151,1,v151 dst_sel:DWORD dst_unused:UNUSED_PAD src0_sel:DWORD src1_sel:WORD_1
v_mfma_f32_16x16x32_bf16 v[172:175],v[92:95],v[136:139],v[172:175]
ds_read_u16_d16_hi v147,v147
v_lshlrev_b32_sdwa v152,1,v152 dst_sel:DWORD dst_unused:UNUSED_PAD src0_sel:DWORD src1_sel:WORD_1
v_exp_f16 v120,v121
v_mfma_f32_16x16x32_bf16 v[180:183],v[96:99],v[136:139],v[180:183]
ds_read_b64_tr_b16 v[78:79],v9 offset:18464
ds_read_b64_tr_b16 v[80:81],v8 offset:20512
v_mfma_f32_16x16x32_bf16 v[188:191],v[100:103],v[136:139],v[188:191]
v_exp_f16_sdwa v120,v121 dst_sel:WORD_1 dst_unused:UNUSED_PRESERVE src0_sel:WORD_1
v_mfma_f32_16x16x32_bf16 v[196:199],v[104:107],v[136:139],v[196:199]
ds_read_u16 v148,v148
ds_read_u16_d16_hi v150,v150
v_lshlrev_b32_sdwa v154,1,v154 dst_sel:DWORD dst_unused:UNUSED_PAD src0_sel:DWORD src1_sel:WORD_1
v_mfma_f32_16x16x32_bf16 v[204:207],v[108:111],v[136:139],v[204:207]
v_pk_fma_f16 v120,v120,s55,s55
v_lshlrev_b32_sdwa v153,1,v153 dst_sel:DWORD dst_unused:UNUSED_PAD src0_sel:DWORD src1_sel:WORD_1
v_mfma_f32_16x16x32_bf16 v[128:131],v[64:67],v[16:19],v[112:115]
ds_read_u16 v149,v149
ds_read_b64_tr_b16 v[82:83],v8 offset:22560
v_rcp_f16 v121,v120
v_mfma_f32_16x16x32_bf16 v[136:139],v[64:67],v[20:23],v[112:115]
s_waitcnt lgkmcnt(11)
ds_read_b64_tr_b16 v[84:85],v9 offset:20512
v_lshlrev_b32_sdwa v155,1,v155 dst_sel:DWORD dst_unused:UNUSED_PAD src0_sel:DWORD src1_sel:WORD_1
v_mfma_f32_16x16x32_bf16 v[140:143],v[68:71],v[20:23],v[116:119]
ds_read_u16_d16_hi v151,v151
v_rcp_f16_sdwa v121,v120 dst_sel:WORD_1 dst_unused:UNUSED_PRESERVE src0_sel:WORD_1
v_mfma_f32_16x16x32_bf16 v[132:135],v[68:71],v[16:19],v[116:119]
s_waitcnt lgkmcnt(11)
ds_read_u16 v152,v152
v_lshlrev_b32_sdwa v156,1,v156 dst_sel:DWORD dst_unused:UNUSED_PAD src0_sel:DWORD src1_sel:WORD_1
v_mfma_f32_16x16x32_bf16 v[128:131],v[72:75],v[24:27],v[128:131]
ds_read_b64_tr_b16 v[86:87],v9 offset:22560
v_fma_mix_f32 v157,v157,v121,0 op_sel_hi:[0,1,0]
v_mfma_f32_16x16x32_bf16 v[136:139],v[72:75],v[28:31],v[136:139]
s_waitcnt lgkmcnt(9)
ds_read_u16_d16_hi v154,v154
v_fma_mix_f32 v159,v159,v121,0 op_sel:[0,1,0] op_sel_hi:[0,1,0]
v_mfma_f32_16x16x32_bf16 v[140:143],v[76:79],v[28:31],v[140:143]
v_mfma_f32_16x16x32_bf16 v[132:135],v[76:79],v[24:27],v[132:135]
s_waitcnt lgkmcnt(5)
ds_read_u16 v153,v153
v_lshlrev_b32_sdwa v158,1,v158 dst_sel:DWORD dst_unused:UNUSED_PAD src0_sel:DWORD src1_sel:WORD_1
v_mfma_f32_16x16x32_bf16 v[128:131],v[80:83],v[32:35],v[128:131]
ds_read_u16_d16_hi v155,v155
v_mfma_f32_16x16x32_bf16 v[136:139],v[80:83],v[36:39],v[136:139]
s_waitcnt lgkmcnt(3)
ds_read_u16 v156,v156
v_lshl_add_u64 v[144:145],v[144:145],0,v[146:147]
v_mfma_f32_16x16x32_bf16 v[140:143],v[84:87],v[36:39],v[140:143]
v_lshl_add_u64 v[146:147],v[148:149],0,v[150:151]
v_mfma_f32_16x16x32_bf16 v[132:135],v[84:87],v[32:35],v[132:135]
v_lshlrev_b32_sdwa v128,1,v128 dst_sel:DWORD dst_unused:UNUSED_PAD src0_sel:DWORD src1_sel:WORD_1
v_lshlrev_b32_sdwa v130,1,v130 dst_sel:DWORD dst_unused:UNUSED_PAD src0_sel:DWORD src1_sel:WORD_1
v_mfma_f32_16x16x32_bf16 v[208:211],v[88:91],v[144:147],v[208:211]
ds_read_u16_d16_hi v158,v158
ds_read_b64_tr_b16 v[64:65],v8 offset:12832
v_lshlrev_b32_sdwa v129,1,v129 dst_sel:DWORD dst_unused:UNUSED_PAD src0_sel:DWORD src1_sel:WORD_1
v_lshlrev_b32_sdwa v131,1,v131 dst_sel:DWORD dst_unused:UNUSED_PAD src0_sel:DWORD src1_sel:WORD_1
v_mfma_f32_16x16x32_bf16 v[216:219],v[92:95],v[144:147],v[216:219]
ds_read_b64_tr_b16 v[66:67],v8 offset:14880
v_fma_mixlo_f16 v120,v140,s99,0
v_fma_mixlo_f16 v122,v141,s99,0
v_mfma_f32_16x16x32_bf16 v[224:227],v[96:99],v[144:147],v[224:227]
ds_read_u16 v128,v128
ds_read_b128 v[112:115],v7 offset:128
v_lshlrev_b32_sdwa v132,1,v132 dst_sel:DWORD dst_unused:UNUSED_PAD src0_sel:DWORD src1_sel:WORD_1
v_lshlrev_b32_sdwa v134,1,v134 dst_sel:DWORD dst_unused:UNUSED_PAD src0_sel:DWORD src1_sel:WORD_1
v_mfma_f32_16x16x32_bf16 v[232:235],v[100:103],v[144:147],v[232:235]
s_waitcnt lgkmcnt(6)
ds_read_b64_tr_b16 v[68:69],v9 offset:12832
v_lshl_add_u64 v[152:153],v[152:153],0,v[154:155]
v_mfma_f32_16x16x32_bf16 v[240:243],v[104:107],v[144:147],v[240:243]
s_waitcnt lgkmcnt(5)
ds_read_u16_d16_hi v130,v130
v_or_b32 v154,v158,v156
v_cvt_pk_bf16_f32 v155,v157,v159
v_mfma_f32_16x16x32_bf16 v[248:251],v[108:111],v[144:147],v[248:251]
ds_read_b64_tr_b16 v[70:71],v9 offset:14880
ds_read_b128 v[116:119],v7 offset:160
v_fma_mixhi_f16 v120,v142,s99,0
v_fma_mixhi_f16 v122,v143,s99,0
v_mfma_f32_16x16x32_bf16 v[212:215],v[88:91],v[152:155],v[212:215]
ds_read_u16 v129,v129
ds_read_b64_tr_b16 v[72:73],v8 offset:16928
v_lshlrev_b32_sdwa v133,1,v133 dst_sel:DWORD dst_unused:UNUSED_PAD src0_sel:DWORD src1_sel:WORD_1
v_lshlrev_b32_sdwa v135,1,v135 dst_sel:DWORD dst_unused:UNUSED_PAD src0_sel:DWORD src1_sel:WORD_1
v_mfma_f32_16x16x32_bf16 v[220:223],v[92:95],v[152:155],v[220:223]
ds_read_b64_tr_b16 v[74:75],v8 offset:18976
v_pk_fma_f16 v121,v120,v120,s54
v_pk_fma_f16 v123,v122,v122,s54
v_mfma_f32_16x16x32_bf16 v[228:231],v[96:99],v[152:155],v[228:231]
ds_read_u16_d16_hi v131,v131
v_pk_mul_f16 v121,v120,v121
v_pk_mul_f16 v123,v122,v123
v_mfma_f32_16x16x32_bf16 v[236:239],v[100:103],v[152:155],v[236:239]
ds_read_b64_tr_b16 v[76:77],v9 offset:16928
ds_read_b64_tr_b16 v[78:79],v9 offset:18976
v_lshlrev_b32_sdwa v136,1,v136 dst_sel:DWORD dst_unused:UNUSED_PAD src0_sel:DWORD src1_sel:WORD_1
v_exp_f16 v120,v121
v_lshlrev_b32_sdwa v138,1,v138 dst_sel:DWORD dst_unused:UNUSED_PAD src0_sel:DWORD src1_sel:WORD_1
v_mfma_f32_16x16x32_bf16 v[244:247],v[104:107],v[152:155],v[244:247]
ds_read_u16 v132,v132
ds_read_b64_tr_b16 v[80:81],v8 offset:21024
v_mfma_f32_16x16x32_bf16 v[252:255],v[108:111],v[152:155],v[252:255]
s_waitcnt lgkmcnt(12)
ds_read_b64_tr_b16 v[82:83],v8 offset:23072
ds_read_u16_d16_hi v134,v134
v_exp_f16 v122,v123
v_mfma_f32_16x16x32_bf16 v[144:147],v[64:67],v[16:19],v[112:115]
ds_read_u16 v133,v133
v_lshlrev_b32_sdwa v137,1,v137 dst_sel:DWORD dst_unused:UNUSED_PAD src0_sel:DWORD src1_sel:WORD_1
v_lshlrev_b32_sdwa v139,1,v139 dst_sel:DWORD dst_unused:UNUSED_PAD src0_sel:DWORD src1_sel:WORD_1
v_mfma_f32_16x16x32_bf16 v[152:155],v[64:67],v[20:23],v[112:115]
s_waitcnt lgkmcnt(11)
ds_read_b64_tr_b16 v[84:85],v9 offset:21024
ds_read_b64_tr_b16 v[86:87],v9 offset:23072
v_exp_f16_sdwa v120,v121 dst_sel:WORD_1 dst_unused:UNUSED_PRESERVE src0_sel:WORD_1
v_mfma_f32_16x16x32_bf16 v[156:159],v[68:71],v[20:23],v[116:119]
ds_read_u16_d16_hi v135,v135
ds_read_b64_tr_b16 v[88:89],v6 offset:51312
v_exp_f16_sdwa v122,v123 dst_sel:WORD_1 dst_unused:UNUSED_PRESERVE src0_sel:WORD_1
v_mfma_f32_16x16x32_bf16 v[148:151],v[68:71],v[16:19],v[116:119]
s_waitcnt lgkmcnt(12)
ds_read_b64_tr_b16 v[90:91],v6 offset:52848
v_pk_fma_f16 v120,v120,s55,s55
v_pk_fma_f16 v122,v122,s55,s55
v_mfma_f32_16x16x32_bf16 v[144:147],v[72:75],v[24:27],v[144:147]
ds_read_u16 v136,v136
v_rcp_f16 v121,v120
v_mfma_f32_16x16x32_bf16 v[152:155],v[72:75],v[28:31],v[152:155]
s_waitcnt lgkmcnt(11)
ds_read_b64_tr_b16 v[92:93],v6 offset:51320
ds_read_b64_tr_b16 v[94:95],v6 offset:52856
v_rcp_f16 v123,v122
v_mfma_f32_16x16x32_bf16 v[156:159],v[76:79],v[28:31],v[156:159]
ds_read_u16_d16_hi v138,v138
ds_read_b64_tr_b16 v[96:97],v6 offset:51376
v_rcp_f16_sdwa v121,v120 dst_sel:WORD_1 dst_unused:UNUSED_PRESERVE src0_sel:WORD_1
v_mfma_f32_16x16x32_bf16 v[148:151],v[76:79],v[24:27],v[148:151]
s_waitcnt lgkmcnt(12)
ds_read_b64_tr_b16 v[98:99],v6 offset:52912
ds_read_b64_tr_b16 v[100:101],v6 offset:51384
v_rcp_f16_sdwa v123,v122 dst_sel:WORD_1 dst_unused:UNUSED_PRESERVE src0_sel:WORD_1
v_mfma_f32_16x16x32_bf16 v[144:147],v[80:83],v[32:35],v[144:147]
ds_read_u16 v137,v137
v_fma_mix_f32 v140,v140,v121,0 op_sel_hi:[0,1,0]
v_fma_mix_f32 v142,v142,v121,0 op_sel:[0,1,0] op_sel_hi:[0,1,0]
v_mfma_f32_16x16x32_bf16 v[152:155],v[80:83],v[36:39],v[152:155]
s_waitcnt lgkmcnt(10)
ds_read_b64_tr_b16 v[102:103],v6 offset:52920
v_lshl_add_u64 v[128:129],v[128:129],0,v[130:131]
v_lshl_add_u64 v[130:131],v[132:133],0,v[134:135]
v_mfma_f32_16x16x32_bf16 v[156:159],v[84:87],v[36:39],v[156:159]
ds_read_u16_d16_hi v139,v139
ds_read_b64_tr_b16 v[104:105],v6 offset:51440
v_fma_mix_f32 v141,v141,v123,0 op_sel_hi:[0,1,0]
v_fma_mix_f32 v143,v143,v123,0 op_sel:[0,1,0] op_sel_hi:[0,1,0]
v_mfma_f32_16x16x32_bf16 v[148:151],v[84:87],v[32:35],v[148:151]
s_waitcnt lgkmcnt(11)
ds_read_b64_tr_b16 v[106:107],v6 offset:52976
ds_read_b64_tr_b16 v[108:109],v6 offset:51448
v_lshlrev_b32_sdwa v144,1,v144 dst_sel:DWORD dst_unused:UNUSED_PAD src0_sel:DWORD src1_sel:WORD_1
v_lshlrev_b32_sdwa v146,1,v146 dst_sel:DWORD dst_unused:UNUSED_PAD src0_sel:DWORD src1_sel:WORD_1
v_mfma_f32_16x16x32_bf16 v[160:163],v[88:91],v[128:131],v[160:163]
s_waitcnt lgkmcnt(10)
ds_read_u16 v144,v144
ds_read_b64_tr_b16 v[110:111],v6 offset:52984
v_lshlrev_b32_sdwa v145,1,v145 dst_sel:DWORD dst_unused:UNUSED_PAD src0_sel:DWORD src1_sel:WORD_1
v_lshlrev_b32_sdwa v147,1,v147 dst_sel:DWORD dst_unused:UNUSED_PAD src0_sel:DWORD src1_sel:WORD_1
v_mfma_f32_16x16x32_bf16 v[168:171],v[92:95],v[128:131],v[168:171]
s_waitcnt lgkmcnt(9)
ds_read_b64_tr_b16 v[64:65],v8 offset:24640
v_fma_mixlo_f16 v120,v156,s99,0
v_fma_mixlo_f16 v122,v157,s99,0
v_mfma_f32_16x16x32_bf16 v[176:179],v[96:99],v[128:131],v[176:179]
s_waitcnt lgkmcnt(7)
ds_read_u16_d16_hi v146,v146
ds_read_b64_tr_b16 v[66:67],v8 offset:26688
v_lshlrev_b32_sdwa v148,1,v148 dst_sel:DWORD dst_unused:UNUSED_PAD src0_sel:DWORD src1_sel:WORD_1
v_lshlrev_b32_sdwa v150,1,v150 dst_sel:DWORD dst_unused:UNUSED_PAD src0_sel:DWORD src1_sel:WORD_1
v_mfma_f32_16x16x32_bf16 v[184:187],v[100:103],v[128:131],v[184:187]
s_waitcnt lgkmcnt(6)
ds_read_b128 v[112:115],v7 offset:256
v_lshl_add_u64 v[136:137],v[136:137],0,v[138:139]
v_mfma_f32_16x16x32_bf16 v[192:195],v[104:107],v[128:131],v[192:195]
s_waitcnt lgkmcnt(4)
ds_read_u16 v145,v145
v_cvt_pk_bf16_f32 v138,v140,v142
v_cvt_pk_bf16_f32 v139,v141,v143
v_mfma_f32_16x16x32_bf16 v[200:203],v[108:111],v[128:131],v[200:203]
ds_read_b64_tr_b16 v[68:69],v9 offset:24640
ds_read_b64_tr_b16 v[70:71],v9 offset:26688
v_fma_mixhi_f16 v120,v158,s99,0
v_fma_mixhi_f16 v122,v159,s99,0
v_mfma_f32_16x16x32_bf16 v[164:167],v[88:91],v[136:139],v[164:167]
ds_read_u16_d16_hi v147,v147
ds_read_b128 v[116:119],v7 offset:288
v_lshlrev_b32_sdwa v149,1,v149 dst_sel:DWORD dst_unused:UNUSED_PAD src0_sel:DWORD src1_sel:WORD_1
v_lshlrev_b32_sdwa v151,1,v151 dst_sel:DWORD dst_unused:UNUSED_PAD src0_sel:DWORD src1_sel:WORD_1
v_mfma_f32_16x16x32_bf16 v[172:175],v[92:95],v[136:139],v[172:175]
ds_read_b64_tr_b16 v[72:73],v8 offset:28736
v_pk_fma_f16 v121,v120,v120,s54
v_pk_fma_f16 v123,v122,v122,s54
v_mfma_f32_16x16x32_bf16 v[180:183],v[96:99],v[136:139],v[180:183]
ds_read_u16 v148,v148
v_pk_mul_f16 v121,v120,v121
v_pk_mul_f16 v123,v122,v123
v_mfma_f32_16x16x32_bf16 v[188:191],v[100:103],v[136:139],v[188:191]
ds_read_b64_tr_b16 v[74:75],v8 offset:30784
ds_read_b64_tr_b16 v[76:77],v9 offset:28736
v_lshlrev_b32_sdwa v152,1,v152 dst_sel:DWORD dst_unused:UNUSED_PAD src0_sel:DWORD src1_sel:WORD_1
v_lshlrev_b32_sdwa v154,1,v154 dst_sel:DWORD dst_unused:UNUSED_PAD src0_sel:DWORD src1_sel:WORD_1
v_mfma_f32_16x16x32_bf16 v[196:199],v[104:107],v[136:139],v[196:199]
ds_read_u16_d16_hi v150,v150
ds_read_b64_tr_b16 v[78:79],v9 offset:30784
v_exp_f16 v120,v121
v_mfma_f32_16x16x32_bf16 v[204:207],v[108:111],v[136:139],v[204:207]
s_waitcnt lgkmcnt(11)
ds_read_b64_tr_b16 v[80:81],v8 offset:32832
v_exp_f16 v122,v123
v_mfma_f32_16x16x32_bf16 v[128:131],v[64:67],v[16:19],v[112:115]
ds_read_u16 v149,v149
v_lshlrev_b32_sdwa v153,1,v153 dst_sel:DWORD dst_unused:UNUSED_PAD src0_sel:DWORD src1_sel:WORD_1
v_lshlrev_b32_sdwa v155,1,v155 dst_sel:DWORD dst_unused:UNUSED_PAD src0_sel:DWORD src1_sel:WORD_1
v_exp_f16_sdwa v120,v121 dst_sel:WORD_1 dst_unused:UNUSED_PRESERVE src0_sel:WORD_1
v_mfma_f32_16x16x32_bf16 v[136:139],v[64:67],v[20:23],v[112:115]
s_waitcnt lgkmcnt(8)
ds_read_b64_tr_b16 v[82:83],v8 offset:34880
ds_read_b64_tr_b16 v[84:85],v9 offset:32832
v_mfma_f32_16x16x32_bf16 v[140:143],v[68:71],v[20:23],v[116:119]
ds_read_u16_d16_hi v151,v151
ds_read_b64_tr_b16 v[86:87],v9 offset:34880
v_exp_f16_sdwa v122,v123 dst_sel:WORD_1 dst_unused:UNUSED_PRESERVE src0_sel:WORD_1
v_mfma_f32_16x16x32_bf16 v[132:135],v[68:71],v[16:19],v[116:119]
s_waitcnt lgkmcnt(9)
ds_read_b64_tr_b16 v[88:89],v6 offset:57472
v_pk_fma_f16 v120,v120,s55,s55
v_pk_fma_f16 v122,v122,s55,s55
v_mfma_f32_16x16x32_bf16 v[128:131],v[72:75],v[24:27],v[128:131]
ds_read_u16 v152,v152
v_rcp_f16 v121,v120
v_mfma_f32_16x16x32_bf16 v[136:139],v[72:75],v[28:31],v[136:139]
s_waitcnt lgkmcnt(8)
ds_read_b64_tr_b16 v[90:91],v6 offset:59008
ds_read_b64_tr_b16 v[92:93],v6 offset:57480
v_rcp_f16 v123,v122
v_mfma_f32_16x16x32_bf16 v[140:143],v[76:79],v[28:31],v[140:143]
ds_read_u16_d16_hi v154,v154
ds_read_b64_tr_b16 v[94:95],v6 offset:59016
v_rcp_f16_sdwa v121,v120 dst_sel:WORD_1 dst_unused:UNUSED_PRESERVE src0_sel:WORD_1
v_mfma_f32_16x16x32_bf16 v[132:135],v[76:79],v[24:27],v[132:135]
s_waitcnt lgkmcnt(9)
ds_read_b64_tr_b16 v[96:97],v6 offset:57536
ds_read_b64_tr_b16 v[98:99],v6 offset:59072
v_rcp_f16_sdwa v123,v122 dst_sel:WORD_1 dst_unused:UNUSED_PRESERVE src0_sel:WORD_1
v_mfma_f32_16x16x32_bf16 v[128:131],v[80:83],v[32:35],v[128:131]
ds_read_u16 v153,v153
v_fma_mix_f32 v156,v156,v121,0 op_sel_hi:[0,1,0]
v_fma_mix_f32 v158,v158,v121,0 op_sel:[0,1,0] op_sel_hi:[0,1,0]
v_mfma_f32_16x16x32_bf16 v[136:139],v[80:83],v[36:39],v[136:139]
s_waitcnt lgkmcnt(9)
ds_read_b64_tr_b16 v[100:101],v6 offset:57544
ds_read_b64_tr_b16 v[102:103],v6 offset:59080
v_lshl_add_u64 v[144:145],v[144:145],0,v[146:147]
v_lshl_add_u64 v[146:147],v[148:149],0,v[150:151]
v_mfma_f32_16x16x32_bf16 v[140:143],v[84:87],v[36:39],v[140:143]
ds_read_u16_d16_hi v155,v155
ds_read_b64_tr_b16 v[104:105],v6 offset:57600
v_fma_mix_f32 v157,v157,v123,0 op_sel_hi:[0,1,0]
v_fma_mix_f32 v159,v159,v123,0 op_sel:[0,1,0] op_sel_hi:[0,1,0]
v_mfma_f32_16x16x32_bf16 v[132:135],v[84:87],v[32:35],v[132:135]
s_waitcnt lgkmcnt(10)
ds_read_b64_tr_b16 v[106:107],v6 offset:59136
ds_read_b64_tr_b16 v[108:109],v6 offset:57608
v_lshlrev_b32_sdwa v128,1,v128 dst_sel:DWORD dst_unused:UNUSED_PAD src0_sel:DWORD src1_sel:WORD_1
v_lshlrev_b32_sdwa v130,1,v130 dst_sel:DWORD dst_unused:UNUSED_PAD src0_sel:DWORD src1_sel:WORD_1
v_mfma_f32_16x16x32_bf16 v[160:163],v[88:91],v[144:147],v[160:163]
s_waitcnt lgkmcnt(9)
ds_read_u16 v128,v128
ds_read_b64_tr_b16 v[110:111],v6 offset:59144
v_lshlrev_b32_sdwa v129,1,v129 dst_sel:DWORD dst_unused:UNUSED_PAD src0_sel:DWORD src1_sel:WORD_1
v_lshlrev_b32_sdwa v131,1,v131 dst_sel:DWORD dst_unused:UNUSED_PAD src0_sel:DWORD src1_sel:WORD_1
v_mfma_f32_16x16x32_bf16 v[168:171],v[92:95],v[144:147],v[168:171]
s_waitcnt lgkmcnt(9)
v_fma_mixlo_f16 v120,v140,s99,0
v_fma_mixlo_f16 v122,v141,s99,0
v_mfma_f32_16x16x32_bf16 v[176:179],v[96:99],v[144:147],v[176:179]
s_waitcnt lgkmcnt(6)
ds_read_u16_d16_hi v130,v130
v_lshlrev_b32_sdwa v132,1,v132 dst_sel:DWORD dst_unused:UNUSED_PAD src0_sel:DWORD src1_sel:WORD_1
v_lshlrev_b32_sdwa v134,1,v134 dst_sel:DWORD dst_unused:UNUSED_PAD src0_sel:DWORD src1_sel:WORD_1
v_mfma_f32_16x16x32_bf16 v[184:187],v[100:103],v[144:147],v[184:187]
s_waitcnt lgkmcnt(4)
ds_read_u16 v129,v129
ds_read_u16_d16_hi v131,v131
v_lshl_add_u64 v[152:153],v[152:153],0,v[154:155]
v_mfma_f32_16x16x32_bf16 v[192:195],v[104:107],v[144:147],v[192:195]
s_waitcnt lgkmcnt(3)
ds_read_u16 v132,v132
v_cvt_pk_bf16_f32 v154,v156,v158
v_cvt_pk_bf16_f32 v155,v157,v159
v_mfma_f32_16x16x32_bf16 v[200:203],v[108:111],v[144:147],v[200:203]
v_fma_mixhi_f16 v122,v143,s99,0
v_mfma_f32_16x16x32_bf16 v[164:167],v[88:91],v[152:155],v[164:167]
ds_read_u16_d16_hi v134,v134
ds_read_b64_tr_b16 v[88:89],v6 offset:63632
v_fma_mixhi_f16 v120,v142,s99,0
v_lshlrev_b32_sdwa v133,1,v133 dst_sel:DWORD dst_unused:UNUSED_PAD src0_sel:DWORD src1_sel:WORD_1
v_lshlrev_b32_sdwa v135,1,v135 dst_sel:DWORD dst_unused:UNUSED_PAD src0_sel:DWORD src1_sel:WORD_1
v_mfma_f32_16x16x32_bf16 v[172:175],v[92:95],v[152:155],v[172:175]
ds_read_b64_tr_b16 v[90:91],v6 offset:65168
v_pk_fma_f16 v121,v120,v120,s54
v_pk_fma_f16 v123,v122,v122,s54
v_mfma_f32_16x16x32_bf16 v[180:183],v[96:99],v[152:155],v[180:183]
ds_read_u16 v133,v133
v_pk_mul_f16 v121,v120,v121
v_pk_mul_f16 v123,v122,v123
v_mfma_f32_16x16x32_bf16 v[188:191],v[100:103],v[152:155],v[188:191]
ds_read_b64_tr_b16 v[92:93],v6 offset:63640
ds_read_b64_tr_b16 v[94:95],v6 offset:65176
v_lshlrev_b32_sdwa v136,1,v136 dst_sel:DWORD dst_unused:UNUSED_PAD src0_sel:DWORD src1_sel:WORD_1
v_lshlrev_b32_sdwa v138,1,v138 dst_sel:DWORD dst_unused:UNUSED_PAD src0_sel:DWORD src1_sel:WORD_1
v_mfma_f32_16x16x32_bf16 v[196:199],v[104:107],v[152:155],v[196:199]
ds_read_u16_d16_hi v135,v135
ds_read_b64_tr_b16 v[96:97],v6 offset:63696
v_exp_f16 v120,v121
v_mfma_f32_16x16x32_bf16 v[204:207],v[108:111],v[152:155],v[204:207]
ds_read_b64_tr_b16 v[98:99],v6 offset:65232
v_exp_f16 v122,v123
v_mfma_f32_16x16x32_bf16 v[144:147],v[64:67],v[40:43],v[112:115]
ds_read_u16 v136,v136
v_lshlrev_b32_sdwa v137,1,v137 dst_sel:DWORD dst_unused:UNUSED_PAD src0_sel:DWORD src1_sel:WORD_1
v_lshlrev_b32_sdwa v139,1,v139 dst_sel:DWORD dst_unused:UNUSED_PAD src0_sel:DWORD src1_sel:WORD_1
v_mfma_f32_16x16x32_bf16 v[152:155],v[64:67],v[44:47],v[112:115]
ds_read_b64_tr_b16 v[100:101],v6 offset:63704
ds_read_b64_tr_b16 v[102:103],v6 offset:65240
v_exp_f16_sdwa v120,v121 dst_sel:WORD_1 dst_unused:UNUSED_PRESERVE src0_sel:WORD_1
v_mfma_f32_16x16x32_bf16 v[156:159],v[68:71],v[44:47],v[116:119]
ds_read_u16_d16_hi v138,v138
ds_read_b64_tr_b16 v[104:105],v6 offset:63760
v_exp_f16_sdwa v122,v123 dst_sel:WORD_1 dst_unused:UNUSED_PRESERVE src0_sel:WORD_1
v_mfma_f32_16x16x32_bf16 v[148:151],v[68:71],v[40:43],v[116:119]
ds_read_b64_tr_b16 v[106:107],v6 offset:65296
v_pk_fma_f16 v120,v120,s55,s55
v_pk_fma_f16 v122,v122,s55,s55
v_mfma_f32_16x16x32_bf16 v[144:147],v[72:75],v[48:51],v[144:147]
ds_read_u16 v137,v137
v_rcp_f16 v121,v120
v_mfma_f32_16x16x32_bf16 v[152:155],v[72:75],v[52:55],v[152:155]
ds_read_b64_tr_b16 v[108:109],v6 offset:63768
ds_read_b64_tr_b16 v[110:111],v6 offset:65304
v_rcp_f16 v123,v122
v_mfma_f32_16x16x32_bf16 v[156:159],v[76:79],v[52:55],v[156:159]
ds_read_u16_d16_hi v139,v139
ds_read_b64_tr_b16 v[64:65],v8 offset:12832
v_rcp_f16_sdwa v121,v120 dst_sel:WORD_1 dst_unused:UNUSED_PRESERVE src0_sel:WORD_1
v_mfma_f32_16x16x32_bf16 v[148:151],v[76:79],v[48:51],v[148:151]
ds_read_b64_tr_b16 v[66:67],v8 offset:14880
ds_read_b128 v[112:115],v7 offset:128
v_rcp_f16_sdwa v123,v122 dst_sel:WORD_1 dst_unused:UNUSED_PRESERVE src0_sel:WORD_1
v_mfma_f32_16x16x32_bf16 v[144:147],v[80:83],v[56:59],v[144:147]
ds_read_b64_tr_b16 v[68:69],v9 offset:12832
v_fma_mix_f32 v140,v140,v121,0 op_sel_hi:[0,1,0]
v_fma_mix_f32 v142,v142,v121,0 op_sel:[0,1,0] op_sel_hi:[0,1,0]
v_mfma_f32_16x16x32_bf16 v[152:155],v[80:83],v[60:63],v[152:155]
s_waitcnt lgkmcnt(15)
v_lshl_add_u64 v[128:129],v[128:129],0,v[130:131]
v_lshl_add_u64 v[130:131],v[132:133],0,v[134:135]
v_mfma_f32_16x16x32_bf16 v[156:159],v[84:87],v[60:63],v[156:159]
ds_read_b64_tr_b16 v[70:71],v9 offset:14880
v_fma_mix_f32 v141,v141,v123,0 op_sel_hi:[0,1,0]
v_fma_mix_f32 v143,v143,v123,0 op_sel:[0,1,0] op_sel_hi:[0,1,0]
v_mfma_f32_16x16x32_bf16 v[148:151],v[84:87],v[56:59],v[148:151]
ds_read_b128 v[116:119],v7 offset:160
ds_read_b64_tr_b16 v[72:73],v8 offset:16928
v_lshlrev_b32_sdwa v144,1,v144 dst_sel:DWORD dst_unused:UNUSED_PAD src0_sel:DWORD src1_sel:WORD_1
v_lshlrev_b32_sdwa v146,1,v146 dst_sel:DWORD dst_unused:UNUSED_PAD src0_sel:DWORD src1_sel:WORD_1
v_mfma_f32_16x16x32_bf16 v[160:163],v[88:91],v[128:131],v[160:163]
s_mov_b32 s86,0x1234567
ds_read_u16 v144,v144
ds_read_b128 v[20:23],v7 offset:1536
v_lshlrev_b32_sdwa v145,1,v145 dst_sel:DWORD dst_unused:UNUSED_PAD src0_sel:DWORD src1_sel:WORD_1
v_mfma_f32_16x16x32_bf16 v[168:171],v[92:95],v[128:131],v[168:171]
s_waitcnt lgkmcnt(15)
ds_read_u16_d16_hi v146,v146
ds_read_b128 v[124:127],v7 offset:1568
v_lshlrev_b32_sdwa v147,1,v147 dst_sel:DWORD dst_unused:UNUSED_PAD src0_sel:DWORD src1_sel:WORD_1
v_mfma_f32_16x16x32_bf16 v[176:179],v[96:99],v[128:131],v[176:179]
ds_read_b128 v[28:31],v7 offset:1664
ds_read_b128 v[36:39],v7 offset:1696
v_lshlrev_b32_sdwa v148,1,v148 dst_sel:DWORD dst_unused:UNUSED_PAD src0_sel:DWORD src1_sel:WORD_1
v_mfma_f32_16x16x32_bf16 v[184:187],v[100:103],v[128:131],v[184:187]
s_waitcnt lgkmcnt(13)
ds_read_u16 v145,v145
ds_read_b64_tr_b16 v[74:75],v8 offset:18976
v_lshl_add_u64 v[136:137],v[136:137],0,v[138:139]
v_lshlrev_b32_sdwa v150,1,v150 dst_sel:DWORD dst_unused:UNUSED_PAD src0_sel:DWORD src1_sel:WORD_1
v_mfma_f32_16x16x32_bf16 v[192:195],v[104:107],v[128:131],v[192:195]
ds_read_u16_d16_hi v147,v147
ds_read_b64_tr_b16 v[76:77],v9 offset:16928
v_cvt_pk_bf16_f32 v138,v140,v142
v_cvt_pk_bf16_f32 v139,v141,v143
v_mfma_f32_16x16x32_bf16 v[200:203],v[108:111],v[128:131],v[200:203]
s_waitcnt lgkmcnt(8)
ds_read_b64_tr_b16 v[78:79],v9 offset:18976
v_lshlrev_b32_sdwa v149,1,v149 dst_sel:DWORD dst_unused:UNUSED_PAD src0_sel:DWORD src1_sel:WORD_1
v_xor_b32 v15,64,v10
v_mul_f32 v160,v160,v20
v_mfma_f32_16x16x32_bf16 v[164:167],v[88:91],v[136:139],v[164:167]
ds_read_u16 v148,v148
ds_read_b64_tr_b16 v[80:81],v8 offset:21024
v_lshlrev_b32_sdwa v151,1,v151 dst_sel:DWORD dst_unused:UNUSED_PAD src0_sel:DWORD src1_sel:WORD_1
v_mul_f32 v161,v161,v21
v_mul_f32 v162,v162,v22
v_mfma_f32_16x16x32_bf16 v[172:175],v[92:95],v[136:139],v[172:175]
s_waitcnt lgkmcnt(9)
ds_read_u16_d16_hi v150,v150
ds_read_b64_tr_b16 v[82:83],v8 offset:23072
v_lshlrev_b32_sdwa v152,1,v152 dst_sel:DWORD dst_unused:UNUSED_PAD src0_sel:DWORD src1_sel:WORD_1
v_mul_f32 v163,v163,v23
v_mul_f32 v168,v168,v124
v_mfma_f32_16x16x32_bf16 v[180:183],v[96:99],v[136:139],v[180:183]
ds_read_b64_tr_b16 v[84:85],v9 offset:21024
ds_read_b64_tr_b16 v[86:87],v9 offset:23072
v_lshlrev_b32_sdwa v154,1,v154 dst_sel:DWORD dst_unused:UNUSED_PAD src0_sel:DWORD src1_sel:WORD_1
v_mul_f32 v169,v169,v125
v_mul_f32 v170,v170,v126
v_mfma_f32_16x16x32_bf16 v[188:191],v[100:103],v[136:139],v[188:191]
ds_read_u16 v149,v149
v_lshlrev_b32_sdwa v153,1,v153 dst_sel:DWORD dst_unused:UNUSED_PAD src0_sel:DWORD src1_sel:WORD_1
v_mul_f32 v171,v171,v127
v_cvt_pk_bf16_f32 v16,v160,v161
v_mfma_f32_16x16x32_bf16 v[196:199],v[104:107],v[136:139],v[196:199]
ds_read_u16_d16_hi v151,v151
v_lshlrev_b32_sdwa v155,1,v155 dst_sel:DWORD dst_unused:UNUSED_PAD src0_sel:DWORD src1_sel:WORD_1
v_cvt_pk_bf16_f32 v17,v162,v163
v_cvt_pk_bf16_f32 v18,v168,v169
v_mfma_f32_16x16x32_bf16 v[204:207],v[108:111],v[136:139],v[204:207]
s_waitcnt lgkmcnt(14)
v_lshlrev_b32_sdwa v156,1,v156 dst_sel:DWORD dst_unused:UNUSED_PAD src0_sel:DWORD src1_sel:WORD_1
v_cvt_pk_bf16_f32 v19,v170,v171
v_mul_f32_dpp v176,v176,v28 quad_perm:[1,0,3,2] row_mask:0xf bank_mask:0xf
v_mfma_f32_16x16x32_bf16 v[128:131],v[64:67],v[40:43],v[112:115]
ds_read_u16 v152,v152
ds_read_b128 v[160:163],v7 offset:1792
v_lshlrev_b32_sdwa v158,1,v158 dst_sel:DWORD dst_unused:UNUSED_PAD src0_sel:DWORD src1_sel:WORD_1
v_mul_f32_dpp v177,v177,v29 quad_perm:[1,0,3,2] row_mask:0xf bank_mask:0xf
v_mul_f32_dpp v178,v178,v30 quad_perm:[1,0,3,2] row_mask:0xf bank_mask:0xf
v_mfma_f32_16x16x32_bf16 v[136:139],v[64:67],v[44:47],v[112:115]
s_waitcnt lgkmcnt(15)
ds_read_u16_d16_hi v154,v154
ds_read_b128 v[168:171],v7 offset:1824
v_lshlrev_b32_sdwa v157,1,v157 dst_sel:DWORD dst_unused:UNUSED_PAD src0_sel:DWORD src1_sel:WORD_1
v_mul_f32_dpp v179,v179,v31 quad_perm:[1,0,3,2] row_mask:0xf bank_mask:0xf
v_mul_f32_dpp v184,v184,v36 quad_perm:[1,0,3,2] row_mask:0xf bank_mask:0xf
v_mfma_f32_16x16x32_bf16 v[140:143],v[68:71],v[44:47],v[116:119]
ds_read_b64_tr_b16 v[64:65],v8 offset:12320
ds_read_b64_tr_b16 v[66:67],v8 offset:14368
v_lshlrev_b32_sdwa v159,1,v159 dst_sel:DWORD dst_unused:UNUSED_PAD src0_sel:DWORD src1_sel:WORD_1
v_mul_f32_dpp v185,v185,v37 quad_perm:[1,0,3,2] row_mask:0xf bank_mask:0xf
v_mul_f32_dpp v186,v186,v38 quad_perm:[1,0,3,2] row_mask:0xf bank_mask:0xf
v_mfma_f32_16x16x32_bf16 v[132:135],v[68:71],v[40:43],v[116:119]
s_waitcnt lgkmcnt(15)
s_mov_b32 s86,0xbe801d5a
ds_read_u16 v153,v153
ds_read_u16_d16_hi v155,v155
v_mul_f32_dpp v187,v187,v39 quad_perm:[1,0,3,2] row_mask:0xf bank_mask:0xf
v_cvt_pk_bf16_f32 v24,v176,v177
v_cvt_pk_bf16_f32 v25,v178,v179
v_mfma_f32_16x16x32_bf16 v[128:131],v[72:75],v[48:51],v[128:131]
ds_read_u16 v156,v156
ds_read_b128 v[112:115],v7 offset:0
v_cvt_pk_bf16_f32 v26,v184,v185
v_cvt_pk_bf16_f32 v27,v186,v187
s_mov_b64 exec,s[12:13]
v_swap_b32 v16,v24
v_swap_b32 v17,v25
v_swap_b32 v18,v26
v_swap_b32 v19,v27
s_mov_b64 exec,-1
s_add_u32 s65,s61,4718592
buffer_store_dwordx4 v[16:19],v10,s[40:43],s65 offen offset:0 sc1
s_add_u32 s65,s61,4718592
buffer_store_dwordx4 v[24:27],v15,s[40:43],s65 offen offset:384 sc1
v_mfma_f32_16x16x32_bf16 v[136:139],v[72:75],v[52:55],v[136:139]
s_waitcnt lgkmcnt(8)
ds_read_b64_tr_b16 v[68:69],v9 offset:12320
v_mul_f32 v192,v192,v160
v_mul_f32 v193,v193,v161
v_mul_f32 v194,v194,v162
v_mfma_f32_16x16x32_bf16 v[140:143],v[76:79],v[52:55],v[140:143]
s_waitcnt lgkmcnt(7)
ds_read_u16_d16_hi v158,v158
ds_read_b64_tr_b16 v[70:71],v9 offset:14368
v_mul_f32 v195,v195,v163
v_mul_f32 v200,v200,v168
v_mul_f32 v201,v201,v169
v_mfma_f32_16x16x32_bf16 v[132:135],v[76:79],v[48:51],v[132:135]
ds_read_u16 v157,v157
ds_read_b128 v[116:119],v7 offset:32
v_lshl_add_u64 v[144:145],v[144:145],0,v[146:147]
v_mul_f32 v202,v202,v170
v_mul_f32 v203,v203,v171
v_mfma_f32_16x16x32_bf16 v[128:131],v[80:83],v[56:59],v[128:131]
ds_read_b64_tr_b16 v[72:73],v8 offset:16416
ds_read_b64_tr_b16 v[74:75],v8 offset:18464
v_lshl_add_u64 v[146:147],v[148:149],0,v[150:151]
v_cvt_pk_bf16_f32 v32,v192,v193
v_cvt_pk_bf16_f32 v33,v194,v195
v_mfma_f32_16x16x32_bf16 v[136:139],v[80:83],v[60:63],v[136:139]
ds_read_u16_d16_hi v159,v159
ds_read_b64_tr_b16 v[76:77],v9 offset:16416
v_cvt_pk_bf16_f32 v34,v200,v201
v_cvt_pk_bf16_f32 v35,v202,v203
v_mul_f32_dpp v164,v164,v20 quad_perm:[1,0,3,2] row_mask:0xf bank_mask:0xf
v_mfma_f32_16x16x32_bf16 v[140:143],v[84:87],v[60:63],v[140:143]
ds_read_b64_tr_b16 v[78:79],v9 offset:18464
v_mul_f32_dpp v165,v165,v21 quad_perm:[1,0,3,2] row_mask:0xf bank_mask:0xf
v_mul_f32_dpp v166,v166,v22 quad_perm:[1,0,3,2] row_mask:0xf bank_mask:0xf
v_mul_f32_dpp v167,v167,v23 quad_perm:[1,0,3,2] row_mask:0xf bank_mask:0xf
v_mfma_f32_16x16x32_bf16 v[132:135],v[84:87],v[56:59],v[132:135]
ds_read_b64_tr_b16 v[80:81],v8 offset:20512
v_lshlrev_b32_sdwa v128,1,v128 dst_sel:DWORD dst_unused:UNUSED_PAD src0_sel:DWORD src1_sel:WORD_1
v_lshlrev_b32_sdwa v130,1,v130 dst_sel:DWORD dst_unused:UNUSED_PAD src0_sel:DWORD src1_sel:WORD_1
v_mul_f32_dpp v172,v172,v124 quad_perm:[1,0,3,2] row_mask:0xf bank_mask:0xf
v_mfma_f32_16x16x32_bf16 v[208:211],v[88:91],v[144:147],v[208:211]
ds_read_u16 v128,v128
ds_read_b64_tr_b16 v[82:83],v8 offset:22560
v_lshlrev_b32_sdwa v129,1,v129 dst_sel:DWORD dst_unused:UNUSED_PAD src0_sel:DWORD src1_sel:WORD_1
v_mul_f32_dpp v173,v173,v125 quad_perm:[1,0,3,2] row_mask:0xf bank_mask:0xf
v_mfma_f32_16x16x32_bf16 v[216:219],v[92:95],v[144:147],v[216:219]
ds_read_u16_d16_hi v130,v130
ds_read_b64_tr_b16 v[84:85],v9 offset:20512
v_lshlrev_b32_sdwa v131,1,v131 dst_sel:DWORD dst_unused:UNUSED_PAD src0_sel:DWORD src1_sel:WORD_1
v_mul_f32_dpp v174,v174,v126 quad_perm:[1,0,3,2] row_mask:0xf bank_mask:0xf
v_mfma_f32_16x16x32_bf16 v[224:227],v[96:99],v[144:147],v[224:227]
ds_read_b64_tr_b16 v[86:87],v9 offset:22560
v_lshlrev_b32_sdwa v132,1,v132 dst_sel:DWORD dst_unused:UNUSED_PAD src0_sel:DWORD src1_sel:WORD_1
v_mul_f32_dpp v175,v175,v127 quad_perm:[1,0,3,2] row_mask:0xf bank_mask:0xf
v_mfma_f32_16x16x32_bf16 v[232:235],v[100:103],v[144:147],v[232:235]
s_waitcnt lgkmcnt(15)
ds_read_u16 v129,v129
v_lshl_add_u64 v[152:153],v[152:153],0,v[154:155]
v_lshlrev_b32_sdwa v134,1,v134 dst_sel:DWORD dst_unused:UNUSED_PAD src0_sel:DWORD src1_sel:WORD_1
v_mfma_f32_16x16x32_bf16 v[240:243],v[104:107],v[144:147],v[240:243]
s_waitcnt lgkmcnt(9)
ds_read_u16_d16_hi v131,v131
v_lshl_add_u64 v[154:155],v[156:157],0,v[158:159]
v_cvt_pk_bf16_f32 v20,v164,v165
v_mfma_f32_16x16x32_bf16 v[248:251],v[108:111],v[144:147],v[248:251]
v_lshlrev_b32_sdwa v133,1,v133 dst_sel:DWORD dst_unused:UNUSED_PAD src0_sel:DWORD src1_sel:WORD_1
v_cvt_pk_bf16_f32 v21,v166,v167
v_mfma_f32_16x16x32_bf16 v[212:215],v[88:91],v[152:155],v[212:215]
ds_read_u16 v132,v132
ds_read_b64_tr_b16 v[88:89],v6 offset:57472
v_lshlrev_b32_sdwa v135,1,v135 dst_sel:DWORD dst_unused:UNUSED_PAD src0_sel:DWORD src1_sel:WORD_1
v_cvt_pk_bf16_f32 v22,v172,v173
v_mfma_f32_16x16x32_bf16 v[220:223],v[92:95],v[152:155],v[220:223]
ds_read_u16_d16_hi v134,v134
ds_read_b64_tr_b16 v[90:91],v6 offset:59008
v_lshlrev_b32_sdwa v136,1,v136 dst_sel:DWORD dst_unused:UNUSED_PAD src0_sel:DWORD src1_sel:WORD_1
v_cvt_pk_bf16_f32 v23,v174,v175
v_mfma_f32_16x16x32_bf16 v[228:231],v[96:99],v[152:155],v[228:231]
ds_read_b64_tr_b16 v[92:93],v6 offset:57480
ds_read_b64_tr_b16 v[94:95],v6 offset:59016
v_lshlrev_b32_sdwa v138,1,v138 dst_sel:DWORD dst_unused:UNUSED_PAD src0_sel:DWORD src1_sel:WORD_1
s_mov_b64 exec,s[12:13]
v_swap_b32 v32,v20
v_swap_b32 v33,v21
v_swap_b32 v34,v22
v_swap_b32 v35,v23
s_mov_b64 exec,-1
s_add_u32 s65,s61,4718592
buffer_store_dwordx4 v[32:35],v10,s[40:43],s65 offen offset:128 sc1
s_add_u32 s65,s61,4718592
buffer_store_dwordx4 v[20:23],v15,s[40:43],s65 offen offset:512 sc1
v_mfma_f32_16x16x32_bf16 v[236:239],v[100:103],v[152:155],v[236:239]
ds_read_u16 v133,v133
ds_read_b64_tr_b16 v[96:97],v6 offset:57536
v_lshlrev_b32_sdwa v137,1,v137 dst_sel:DWORD dst_unused:UNUSED_PAD src0_sel:DWORD src1_sel:WORD_1
v_mul_f32 v180,v180,v28
v_mfma_f32_16x16x32_bf16 v[244:247],v[104:107],v[152:155],v[244:247]
ds_read_u16_d16_hi v135,v135
ds_read_b64_tr_b16 v[98:99],v6 offset:59072
v_lshlrev_b32_sdwa v139,1,v139 dst_sel:DWORD dst_unused:UNUSED_PAD src0_sel:DWORD src1_sel:WORD_1
v_mul_f32 v181,v181,v29
v_mfma_f32_16x16x32_bf16 v[252:255],v[108:111],v[152:155],v[252:255]
ds_read_b64_tr_b16 v[100:101],v6 offset:57544
v_lshlrev_b32_sdwa v140,1,v140 dst_sel:DWORD dst_unused:UNUSED_PAD src0_sel:DWORD src1_sel:WORD_1
v_mul_f32 v182,v182,v30
v_mfma_f32_16x16x32_bf16 v[144:147],v[64:67],v[40:43],v[112:115]
ds_read_u16 v136,v136
ds_read_b64_tr_b16 v[102:103],v6 offset:59080
v_lshlrev_b32_sdwa v142,1,v142 dst_sel:DWORD dst_unused:UNUSED_PAD src0_sel:DWORD src1_sel:WORD_1
v_mul_f32 v183,v183,v31
v_mfma_f32_16x16x32_bf16 v[152:155],v[64:67],v[44:47],v[112:115]
ds_read_u16_d16_hi v138,v138
ds_read_b64_tr_b16 v[104:105],v6 offset:57600
v_lshlrev_b32_sdwa v141,1,v141 dst_sel:DWORD dst_unused:UNUSED_PAD src0_sel:DWORD src1_sel:WORD_1
v_mul_f32 v188,v188,v36
v_mfma_f32_16x16x32_bf16 v[156:159],v[68:71],v[44:47],v[116:119]
ds_read_b64_tr_b16 v[106:107],v6 offset:59136
ds_read_b64_tr_b16 v[108:109],v6 offset:57608
v_lshlrev_b32_sdwa v143,1,v143 dst_sel:DWORD dst_unused:UNUSED_PAD src0_sel:DWORD src1_sel:WORD_1
v_mul_f32 v189,v189,v37
v_mfma_f32_16x16x32_bf16 v[148:151],v[68:71],v[40:43],v[116:119]
ds_read_u16 v137,v137
ds_read_b64_tr_b16 v[110:111],v6 offset:59144
v_mul_f32 v190,v190,v38
v_mul_f32 v191,v191,v39
v_mfma_f32_16x16x32_bf16 v[144:147],v[72:75],v[48:51],v[144:147]
ds_read_u16_d16_hi v139,v139
ds_read_b128 v[64:67],v7 offset:1536
v_cvt_pk_bf16_f32 v28,v180,v181
v_cvt_pk_bf16_f32 v29,v182,v183
v_mfma_f32_16x16x32_bf16 v[152:155],v[72:75],v[52:55],v[152:155]
s_waitcnt lgkmcnt(15)
ds_read_u16 v140,v140
ds_read_b128 v[68:71],v7 offset:1568
v_cvt_pk_bf16_f32 v30,v188,v189
v_cvt_pk_bf16_f32 v31,v190,v191
v_mfma_f32_16x16x32_bf16 v[156:159],v[76:79],v[52:55],v[156:159]
ds_read_b128 v[72:75],v7 offset:1664
v_mul_f32_dpp v196,v196,v160 quad_perm:[1,0,3,2] row_mask:0xf bank_mask:0xf
v_mul_f32_dpp v197,v197,v161 quad_perm:[1,0,3,2] row_mask:0xf bank_mask:0xf
v_mfma_f32_16x16x32_bf16 v[148:151],v[76:79],v[48:51],v[148:151]
ds_read_u16_d16_hi v142,v142
ds_read_b64_tr_b16 v[176:177],v6 offset:51312
v_lshl_add_u64 v[128:129],v[128:129],0,v[130:131]
v_mul_f32_dpp v198,v198,v162 quad_perm:[1,0,3,2] row_mask:0xf bank_mask:0xf
v_mfma_f32_16x16x32_bf16 v[144:147],v[80:83],v[56:59],v[144:147]
s_waitcnt lgkmcnt(15)
ds_read_b64_tr_b16 v[178:179],v6 offset:52848
ds_read_b64_tr_b16 v[184:185],v6 offset:51320
v_lshl_add_u64 v[130:131],v[132:133],0,v[134:135]
v_mul_f32_dpp v199,v199,v163 quad_perm:[1,0,3,2] row_mask:0xf bank_mask:0xf
v_mfma_f32_16x16x32_bf16 v[152:155],v[80:83],v[60:63],v[152:155]
ds_read_u16 v141,v141
ds_read_b64_tr_b16 v[186:187],v6 offset:52856
v_mul_f32_dpp v204,v204,v168 quad_perm:[1,0,3,2] row_mask:0xf bank_mask:0xf
v_mul_f32_dpp v205,v205,v169 quad_perm:[1,0,3,2] row_mask:0xf bank_mask:0xf
v_mfma_f32_16x16x32_bf16 v[156:159],v[84:87],v[60:63],v[156:159]
ds_read_u16_d16_hi v143,v143
ds_read_b64_tr_b16 v[192:193],v6 offset:51376
v_mul_f32_dpp v206,v206,v170 quad_perm:[1,0,3,2] row_mask:0xf bank_mask:0xf
v_mul_f32_dpp v207,v207,v171 quad_perm:[1,0,3,2] row_mask:0xf bank_mask:0xf
v_mfma_f32_16x16x32_bf16 v[148:151],v[84:87],v[56:59],v[148:151]
ds_read_b64_tr_b16 v[194:195],v6 offset:52912
v_lshlrev_b32_sdwa v144,1,v144 dst_sel:DWORD dst_unused:UNUSED_PAD src0_sel:DWORD src1_sel:WORD_1
v_lshlrev_b32_sdwa v146,1,v146 dst_sel:DWORD dst_unused:UNUSED_PAD src0_sel:DWORD src1_sel:WORD_1
v_mfma_f32_16x16x32_bf16 v[208:211],v[88:91],v[128:131],v[208:211]
ds_read_u16 v144,v144
ds_read_b64_tr_b16 v[200:201],v6 offset:51384
v_lshlrev_b32_sdwa v145,1,v145 dst_sel:DWORD dst_unused:UNUSED_PAD src0_sel:DWORD src1_sel:WORD_1
v_cvt_pk_bf16_f32 v36,v196,v197
v_cvt_pk_bf16_f32 v37,v198,v199
v_mfma_f32_16x16x32_bf16 v[216:219],v[92:95],v[128:131],v[216:219]
ds_read_u16_d16_hi v146,v146
ds_read_b64_tr_b16 v[202:203],v6 offset:52920
v_cvt_pk_bf16_f32 v38,v204,v205
v_lshlrev_b32_sdwa v147,1,v147 dst_sel:DWORD dst_unused:UNUSED_PAD src0_sel:DWORD src1_sel:WORD_1
v_mfma_f32_16x16x32_bf16 v[224:227],v[96:99],v[128:131],v[224:227]
s_waitcnt lgkmcnt(15)
ds_read_u16 v145,v145
ds_read_b64_tr_b16 v[164:165],v6 offset:51440
v_cvt_pk_bf16_f32 v39,v206,v207
s_mov_b64 exec,s[12:13]
v_swap_b32 v28,v36
v_swap_b32 v29,v37
v_swap_b32 v30,v38
v_swap_b32 v31,v39
s_mov_b64 exec,-1
s_add_u32 s65,s61,4718592
buffer_store_dwordx4 v[28:31],v10,s[40:43],s65 offen offset:256 sc1
s_add_u32 s65,s61,4718592
buffer_store_dwordx4 v[36:39],v15,s[40:43],s65 offen offset:640 sc1
v_mfma_f32_16x16x32_bf16 v[232:235],v[100:103],v[128:131],v[232:235]
ds_read_u16_d16_hi v147,v147
ds_read_b64_tr_b16 v[166:167],v6 offset:52976
v_mfma_f32_16x16x32_bf16 v[240:243],v[104:107],v[128:131],v[240:243]
ds_read_b64_tr_b16 v[172:173],v6 offset:51448
v_mfma_f32_16x16x32_bf16 v[248:251],v[108:111],v[128:131],v[248:251]
v_lshlrev_b32_sdwa v148,1,v148 dst_sel:DWORD dst_unused:UNUSED_PAD src0_sel:DWORD src1_sel:WORD_1
v_lshl_add_u64 v[136:137],v[136:137],0,v[138:139]
v_lshlrev_b32_sdwa v150,1,v150 dst_sel:DWORD dst_unused:UNUSED_PAD src0_sel:DWORD src1_sel:WORD_1
s_waitcnt lgkmcnt(11)
v_lshl_add_u64 v[138:139],v[140:141],0,v[142:143]
v_lshlrev_b32_sdwa v149,1,v149 dst_sel:DWORD dst_unused:UNUSED_PAD src0_sel:DWORD src1_sel:WORD_1
v_lshlrev_b32_sdwa v151,1,v151 dst_sel:DWORD dst_unused:UNUSED_PAD src0_sel:DWORD src1_sel:WORD_1
v_mfma_f32_16x16x32_bf16 v[212:215],v[88:91],v[136:139],v[212:215]
ds_read_u16 v148,v148
ds_read_b64_tr_b16 v[174:175],v6 offset:52984
v_mfma_f32_16x16x32_bf16 v[220:223],v[92:95],v[136:139],v[220:223]
ds_read_u16_d16_hi v150,v150
ds_read_b128 v[76:79],v7 offset:1696
v_mfma_f32_16x16x32_bf16 v[228:231],v[96:99],v[136:139],v[228:231]
ds_read_u16 v149,v149
ds_read_b128 v[80:83],v7 offset:1792
v_lshlrev_b32_sdwa v152,1,v152 dst_sel:DWORD dst_unused:UNUSED_PAD src0_sel:DWORD src1_sel:WORD_1
v_lshlrev_b32_sdwa v154,1,v154 dst_sel:DWORD dst_unused:UNUSED_PAD src0_sel:DWORD src1_sel:WORD_1
v_lshlrev_b32_sdwa v153,1,v153 dst_sel:DWORD dst_unused:UNUSED_PAD src0_sel:DWORD src1_sel:WORD_1
v_mfma_f32_16x16x32_bf16 v[236:239],v[100:103],v[136:139],v[236:239]
ds_read_u16_d16_hi v151,v151
ds_read_b128 v[84:87],v7 offset:1824
v_lshlrev_b32_sdwa v155,1,v155 dst_sel:DWORD dst_unused:UNUSED_PAD src0_sel:DWORD src1_sel:WORD_1
v_lshlrev_b32_sdwa v156,1,v156 dst_sel:DWORD dst_unused:UNUSED_PAD src0_sel:DWORD src1_sel:WORD_1
v_mfma_f32_16x16x32_bf16 v[244:247],v[104:107],v[136:139],v[244:247]
ds_read_u16 v152,v152
v_lshlrev_b32_sdwa v158,1,v158 dst_sel:DWORD dst_unused:UNUSED_PAD src0_sel:DWORD src1_sel:WORD_1
v_lshlrev_b32_sdwa v157,1,v157 dst_sel:DWORD dst_unused:UNUSED_PAD src0_sel:DWORD src1_sel:WORD_1
v_mfma_f32_16x16x32_bf16 v[252:255],v[108:111],v[136:139],v[252:255]
ds_read_u16_d16_hi v154,v154
v_lshlrev_b32_sdwa v159,1,v159 dst_sel:DWORD dst_unused:UNUSED_PAD src0_sel:DWORD src1_sel:WORD_1
s_waitcnt lgkmcnt(12)
v_lshl_add_u64 v[144:145],v[144:145],0,v[146:147]
s_waitcnt lgkmcnt(3)
v_lshl_add_u64 v[146:147],v[148:149],0,v[150:151]
s_nop 1
v_mfma_f32_16x16x32_bf16 v[208:211],v[176:179],v[144:147],v[208:211]
ds_read_u16 v153,v153
v_mfma_f32_16x16x32_bf16 v[216:219],v[184:187],v[144:147],v[216:219]
ds_read_u16_d16_hi v155,v155
v_mfma_f32_16x16x32_bf16 v[224:227],v[192:195],v[144:147],v[224:227]
ds_read_u16 v156,v156
v_mfma_f32_16x16x32_bf16 v[232:235],v[200:203],v[144:147],v[232:235]
ds_read_u16_d16_hi v158,v158
v_mfma_f32_16x16x32_bf16 v[240:243],v[164:167],v[144:147],v[240:243]
ds_read_u16 v157,v157
v_mfma_f32_16x16x32_bf16 v[248:251],v[172:175],v[144:147],v[248:251]
ds_read_u16_d16_hi v159,v159
v_mul_f32 v208,v208,v64
s_waitcnt lgkmcnt(4)
v_lshl_add_u64 v[152:153],v[152:153],0,v[154:155]
s_waitcnt lgkmcnt(0)
v_lshl_add_u64 v[154:155],v[156:157],0,v[158:159]
v_mul_f32 v209,v209,v65
v_mul_f32 v210,v210,v66
v_mfma_f32_16x16x32_bf16 v[212:215],v[176:179],v[152:155],v[212:215]
v_mul_f32 v211,v211,v67
v_mul_f32 v216,v216,v68
v_mul_f32 v217,v217,v69
v_mfma_f32_16x16x32_bf16 v[220:223],v[184:187],v[152:155],v[220:223]
v_mul_f32 v218,v218,v70
v_mul_f32 v219,v219,v71
v_cvt_pk_bf16_f32 v40,v208,v209
v_mfma_f32_16x16x32_bf16 v[228:231],v[192:195],v[152:155],v[228:231]
v_cvt_pk_bf16_f32 v41,v210,v211
v_cvt_pk_bf16_f32 v42,v216,v217
v_cvt_pk_bf16_f32 v43,v218,v219
v_mfma_f32_16x16x32_bf16 v[236:239],v[200:203],v[152:155],v[236:239]
v_mul_f32_dpp v224,v224,v72 quad_perm:[1,0,3,2] row_mask:0xf bank_mask:0xf
v_mul_f32_dpp v225,v225,v73 quad_perm:[1,0,3,2] row_mask:0xf bank_mask:0xf
v_mul_f32_dpp v226,v226,v74 quad_perm:[1,0,3,2] row_mask:0xf bank_mask:0xf
v_mfma_f32_16x16x32_bf16 v[244:247],v[164:167],v[152:155],v[244:247]
v_mul_f32_dpp v227,v227,v75 quad_perm:[1,0,3,2] row_mask:0xf bank_mask:0xf
v_mul_f32_dpp v232,v232,v76 quad_perm:[1,0,3,2] row_mask:0xf bank_mask:0xf
v_mul_f32_dpp v233,v233,v77 quad_perm:[1,0,3,2] row_mask:0xf bank_mask:0xf
v_mfma_f32_16x16x32_bf16 v[252:255],v[172:175],v[152:155],v[252:255]
v_mul_f32_dpp v234,v234,v78 quad_perm:[1,0,3,2] row_mask:0xf bank_mask:0xf
v_mul_f32_dpp v235,v235,v79 quad_perm:[1,0,3,2] row_mask:0xf bank_mask:0xf
v_cvt_pk_bf16_f32 v48,v224,v225
v_cvt_pk_bf16_f32 v49,v226,v227
v_cvt_pk_bf16_f32 v50,v232,v233
v_cvt_pk_bf16_f32 v51,v234,v235
s_mov_b64 exec,s[12:13]
v_swap_b32 v40,v48
v_swap_b32 v41,v49
v_swap_b32 v42,v50
v_swap_b32 v43,v51
s_mov_b64 exec,-1
s_add_u32 s65,s61,4816896
buffer_store_dwordx4 v[40:43],v10,s[40:43],s65 offen offset:0 sc1
s_add_u32 s65,s61,4816896
buffer_store_dwordx4 v[48:51],v15,s[40:43],s65 offen offset:384 sc1
v_mul_f32 v240,v240,v80
v_mul_f32 v241,v241,v81
v_mul_f32 v242,v242,v82
v_mul_f32 v243,v243,v83
v_mul_f32 v248,v248,v84
v_mul_f32 v249,v249,v85
v_mul_f32 v250,v250,v86
v_mul_f32 v251,v251,v87
v_cvt_pk_bf16_f32 v56,v240,v241
v_cvt_pk_bf16_f32 v57,v242,v243
v_cvt_pk_bf16_f32 v58,v248,v249
v_cvt_pk_bf16_f32 v59,v250,v251
v_mul_f32_dpp v212,v212,v64 quad_perm:[1,0,3,2] row_mask:0xf bank_mask:0xf
v_mul_f32_dpp v213,v213,v65 quad_perm:[1,0,3,2] row_mask:0xf bank_mask:0xf
v_mul_f32_dpp v214,v214,v66 quad_perm:[1,0,3,2] row_mask:0xf bank_mask:0xf
v_mul_f32_dpp v215,v215,v67 quad_perm:[1,0,3,2] row_mask:0xf bank_mask:0xf
v_mul_f32_dpp v220,v220,v68 quad_perm:[1,0,3,2] row_mask:0xf bank_mask:0xf
v_mul_f32_dpp v221,v221,v69 quad_perm:[1,0,3,2] row_mask:0xf bank_mask:0xf
v_mul_f32_dpp v222,v222,v70 quad_perm:[1,0,3,2] row_mask:0xf bank_mask:0xf
v_mul_f32_dpp v223,v223,v71 quad_perm:[1,0,3,2] row_mask:0xf bank_mask:0xf
v_cvt_pk_bf16_f32 v44,v212,v213
v_cvt_pk_bf16_f32 v45,v214,v215
v_cvt_pk_bf16_f32 v46,v220,v221
v_cvt_pk_bf16_f32 v47,v222,v223
s_mov_b64 exec,s[12:13]
v_swap_b32 v56,v44
v_swap_b32 v57,v45
v_swap_b32 v58,v46
v_swap_b32 v59,v47
s_mov_b64 exec,-1
s_add_u32 s65,s61,4816896
buffer_store_dwordx4 v[56:59],v10,s[40:43],s65 offen offset:128 sc1
s_add_u32 s65,s61,4816896
buffer_store_dwordx4 v[44:47],v15,s[40:43],s65 offen offset:512 sc1
v_mul_f32 v228,v228,v72
v_mul_f32 v229,v229,v73
v_mul_f32 v230,v230,v74
v_mul_f32 v231,v231,v75
v_mul_f32 v236,v236,v76
v_mul_f32 v237,v237,v77
v_mul_f32 v238,v238,v78
v_mul_f32 v239,v239,v79
v_cvt_pk_bf16_f32 v52,v228,v229
v_cvt_pk_bf16_f32 v53,v230,v231
v_cvt_pk_bf16_f32 v54,v236,v237
v_cvt_pk_bf16_f32 v55,v238,v239
v_mul_f32_dpp v244,v244,v80 quad_perm:[1,0,3,2] row_mask:0xf bank_mask:0xf
v_mul_f32_dpp v245,v245,v81 quad_perm:[1,0,3,2] row_mask:0xf bank_mask:0xf
v_mul_f32_dpp v246,v246,v82 quad_perm:[1,0,3,2] row_mask:0xf bank_mask:0xf
v_mul_f32_dpp v247,v247,v83 quad_perm:[1,0,3,2] row_mask:0xf bank_mask:0xf
v_mul_f32_dpp v252,v252,v84 quad_perm:[1,0,3,2] row_mask:0xf bank_mask:0xf
v_mul_f32_dpp v253,v253,v85 quad_perm:[1,0,3,2] row_mask:0xf bank_mask:0xf
v_mul_f32_dpp v254,v254,v86 quad_perm:[1,0,3,2] row_mask:0xf bank_mask:0xf
v_mul_f32_dpp v255,v255,v87 quad_perm:[1,0,3,2] row_mask:0xf bank_mask:0xf
v_cvt_pk_bf16_f32 v60,v244,v245
v_cvt_pk_bf16_f32 v61,v246,v247
v_cvt_pk_bf16_f32 v62,v252,v253
v_cvt_pk_bf16_f32 v63,v254,v255
s_mov_b64 exec,s[12:13]
v_swap_b32 v52,v60
v_swap_b32 v53,v61
v_swap_b32 v54,v62
v_swap_b32 v55,v63
s_mov_b64 exec,-1
s_add_u32 s65,s61,4816896
buffer_store_dwordx4 v[52:55],v10,s[40:43],s65 offen offset:256 sc1
s_add_u32 s65,s61,4816896
buffer_store_dwordx4 v[60:63],v15,s[40:43],s65 offen offset:640 sc1
N_last_%=:
v_and_b32 v1,0x7c007c00,v1
v_lshrrev_b32 v2,16,v1
v_and_b32 v1,0xffff,v1
v_max_u32 v1,v1,v2
v_cmp_eq_u32 vcc,0x7c00,v1
s_or_b32 s64,vcc_lo,vcc_hi
v_mov_b32 v1,s64
v_mov_b32 v2,8192
s_mov_b64 exec,1
ds_or_b32 v2,v1 offset:3456
s_mov_b64 exec,-1
s_waitcnt lgkmcnt(0)
s_barrier
ds_read_b32 v1,v2 offset:3456
s_waitcnt lgkmcnt(0)
v_readfirstlane_b32 s64,v1
s_cmp_lg_u32 s64,0
s_cbranch_scc1 N_fallback_%=
s_endpgm
N_mainend_%=:
N_fallback_%=:
s_setprio 0
s_waitcnt vmcnt(0) lgkmcnt(0)
s_barrier
v_lshrrev_b32 v237,6,%1
v_and_b32 v232,63,%1
v_readfirstlane_b32 s52,v237
v_lshlrev_b32 v248,4,v232
v_min_u32 v244,383,%1
v_lshlrev_b32 v238,1,v244
global_load_ushort v244,v238,s[26:27]
v_min_u32 v245,95,%1
v_lshlrev_b32 v239,1,v245
global_load_ushort v245,v239,s[30:31]
global_load_ushort v246,v239,s[32:33]
s_mov_b64 s[92:93],s[22:23]
s_and_b32 s93,s93,0xffff
s_mov_b32 s94,1728
s_mov_b32 s95,0x00020000
s_lshl_b32 s71,s52,11
s_add_u32 s71,s71,62208
s_mov_b32 m0,s71
s_movk_i32 s64,1024
buffer_load_dwordx4 v248,s[92:95],0 offen lds
s_add_u32 m0,s71,1024
s_nop 0
buffer_load_dwordx4 v248,s[92:95],s64 offen lds
v_mov_b32 v250,s71
s_mov_b64 s[36:37],s[20:21]
s_mov_b64 s[40:41],s[34:35]
s_mov_b64 s[44:45],s[24:25]
s_mov_b64 s[48:49],s[28:29]
s_sub_u32 s36,s36,192
s_subb_u32 s37,s37,0
s_and_b32 s37,s37,0xffff
s_mov_b32 s38,50331840
s_mov_b32 s39,0x00020000
s_and_b32 s41,s41,0xffff
s_mov_b32 s42,50331648
s_mov_b32 s43,0x00020000
s_and_b32 s45,s45,0xffff
s_mov_b32 s46,73728
s_mov_b32 s47,0x00020000
s_and_b32 s49,s49,0xffff
s_mov_b32 s50,73728
s_mov_b32 s51,0x00020000
s_mov_b32 s60,-1
s_mov_b32 s61,0xffff
s_mov_b32 s62,-1
s_mov_b32 s63,0x3fffff
s_and_b32 s64,%0,7
s_lshr_b32 s70,%0,3
s_lshl_b32 s56,s64,6
s_mul_i32 s57,s70,3072
s_and_b32 s66,s52,3
s_mul_i32 s68,s66,864
s_mul_i32 s67,s66,54
s_mul_i32 s58,s66,1536
v_add_u32 v237,s67,v232
v_mul_u32_u24 v238,0xaaab,v237
v_lshrrev_b32 v238,19,v238
v_mul_u32_u24 v239,12,v238
v_sub_u32 v239,v237,v239
v_bfe_u32 v237,v238,2,2
v_xor_b32 v239,v239,v237
v_lshlrev_b32 v239,4,v239
v_mul_u32_u24 v237,192,v238
v_add_u32 v247,v237,v239
s_cmp_eq_u32 s70,0
s_cselect_b32 s71,0,99
s_cmp_eq_u32 s70,31
s_cselect_b32 s71,17,s71
v_cmp_eq_u32 vcc,s71,v238
v_mov_b32 v237,0x7fff0000
s_sub_u32 s67,s56,1
s_mul_i32 s59,s67,98304
s_add_u32 s59,s59,s57
v_cndmask_b32 v247,v247,v237,vcc
s_lshr_b32 s66,s52,2
s_mul_i32 s64,s66,3456
s_add_u32 s64,s64,s68
s_mul_i32 s65,s66,98304
s_add_u32 s65,s65,s59
s_add_u32 s67,s67,s66
s_cmp_lt_i32 s67,0
s_cselect_b32 s71,0x70000000,s65
s_mov_b64 exec,s[62:63]
s_add_u32 m0,s64,0
s_nop 0
buffer_load_dwordx4 v247,s[36:39],s71 offen lds
s_add_u32 m0,s64,6912
s_add_u32 s71,s65,196608
buffer_load_dwordx4 v247,s[36:39],s71 offen lds
s_add_u32 m0,s64,13824
s_add_u32 s71,s65,393216
buffer_load_dwordx4 v247,s[36:39],s71 offen lds
s_add_u32 m0,s64,20736
s_add_u32 s71,s65,589824
buffer_load_dwordx4 v247,s[36:39],s71 offen lds
s_add_u32 m0,s64,27648
s_add_u32 s71,s65,786432
buffer_load_dwordx4 v247,s[36:39],s71 offen lds
s_add_u32 m0,s64,34560
s_add_u32 s71,s65,983040
buffer_load_dwordx4 v247,s[36:39],s71 offen lds
s_add_u32 m0,s64,41472
s_add_u32 s71,s65,1179648
buffer_load_dwordx4 v247,s[36:39],s71 offen lds
s_add_u32 m0,s64,48384
s_add_u32 s71,s65,1376256
buffer_load_dwordx4 v247,s[36:39],s71 offen lds
s_add_u32 m0,s64,55296
s_add_u32 s71,s65,1572864
buffer_load_dwordx4 v247,s[36:39],s71 offen lds
s_mov_b64 exec,-1
v_lshrrev_b32 v237,2,v232
v_mul_u32_u24 v237,768,v237
v_and_b32 v238,3,v232
v_lshl_add_u32 v237,v238,4,v237
s_and_b32 s66,s52,3
s_mul_i32 s71,s66,18432
v_add_u32 v27,s71,v237
v_add_u32 v28,s58,v248
s_mov_b32 s54,62208
s_mov_b32 s53,0
s_mov_b32 s96,0x70000000
s_add_u32 s68,s68,s54
s_add_u32 s59,s59,1572864
s_lshl_b32 s66,s52,1
s_add_u32 s66,s66,s56
s_mul_i32 s69,s66,98304
s_add_u32 s69,s69,s57
s_cmp_ge_u32 s52,4
s_cbranch_scc1 L_noW_%=
s_mov_b64 exec,s[60:61]
s_add_u32 m0,s58,124416
s_movk_i32 s64,0
buffer_load_dwordx4 v27,s[44:47],s64 offen lds
s_add_u32 m0,s58,125184
s_movk_i32 s65,9216
buffer_load_dwordx4 v27,s[44:47],s65 offen lds
s_mov_b64 exec,-1
s_mov_b64 exec,s[60:61]
s_add_u32 m0,s58,142848
s_mov_b32 s64,0
buffer_load_dwordx4 v28,s[48:51],s64 offen lds
s_add_u32 m0,s58,143616
s_mov_b32 s65,768
buffer_load_dwordx4 v28,s[48:51],s65 offen lds
s_mov_b64 exec,-1
s_mov_b64 exec,s[60:61]
s_add_u32 m0,s58,130560
s_movk_i32 s64,64
buffer_load_dwordx4 v27,s[44:47],s64 offen lds
s_add_u32 m0,s58,131328
s_movk_i32 s65,9280
buffer_load_dwordx4 v27,s[44:47],s65 offen lds
s_mov_b64 exec,-1
s_mov_b64 exec,s[60:61]
s_add_u32 m0,s58,136704
s_movk_i32 s64,128
buffer_load_dwordx4 v27,s[44:47],s64 offen lds
s_add_u32 m0,s58,137472
s_movk_i32 s65,9344
buffer_load_dwordx4 v27,s[44:47],s65 offen lds
s_mov_b64 exec,-1
s_branch L_W_%=
L_noW_%=:
v_mov_b32 v27,v247
s_mul_i32 s64,s70,2304
s_and_b32 s65,s52,3
s_mul_i32 s65,s65,576
s_add_u32 s64,s64,s65
v_min_u32 v237,560,v248
buffer_load_dwordx4 v[252:255],v237,s[44:47],s64 offen
buffer_load_dwordx4 v[252:255],v237,s[48:51],s64 offen
L_W_%=:
v_and_b32 v233,15,v232
v_lshrrev_b32 v234,4,v232
v_lshrrev_b32 v235,5,v232
v_and_b32 v237,1,v234
v_lshlrev_b32 v237,1,v237
v_or_b32 v236,v237,v235
v_lshrrev_b32 v237,3,v233
v_lshlrev_b32 v237,2,v237
v_and_b32 v238,3,v233
v_or_b32 v241,v237,v238
v_lshrrev_b32 v237,2,v233
v_and_b32 v242,1,v237
v_sub_u32 v237,v236,v242
v_cmp_gt_u32 vcc,3,v237
v_mov_b32 v238,0
v_mov_b32 v239,-1
v_cndmask_b32 v237,v238,v237,vcc
v_cndmask_b32 v249,v238,v239,vcc
v_mul_u32_u24 v237,576,v237
v_lshl_add_u32 v237,v241,1,v237
v_add_u32 v250,v250,v237
v_lshrrev_b32 v237,1,v241
v_cmp_eq_u32 vcc,0,v237
s_nop 1
v_cndmask_b32 v32,v238,v249,vcc
v_cmp_eq_u32 vcc,1,v237
s_nop 1
v_cndmask_b32 v33,v238,v249,vcc
v_cmp_eq_u32 vcc,2,v237
s_nop 1
v_cndmask_b32 v34,v238,v249,vcc
v_cmp_eq_u32 vcc,3,v237
s_nop 1
v_cndmask_b32 v35,v238,v249,vcc
v_and_b32 v243,1,v241
v_lshlrev_b32 v243,4,v243
s_mul_i32 s71,s52,6912
v_add_u32 v237,0,v233
v_bfe_u32 v238,v237,2,2
v_mul_u32_u24 v239,18,v236
v_add_u32 v239,v239,v237
v_mul_u32_u24 v239,192,v239
v_add_u32 v239,s71,v239
v_xor_b32 v240,0,v238
v_lshl_add_u32 v4,v240,4,v239
v_xor_b32 v240,1,v238
v_lshl_add_u32 v5,v240,4,v239
v_xor_b32 v240,2,v238
v_lshl_add_u32 v6,v240,4,v239
v_xor_b32 v240,3,v238
v_lshl_add_u32 v7,v240,4,v239
v_add_u32 v237,1,v233
v_bfe_u32 v238,v237,2,2
v_mul_u32_u24 v239,18,v236
v_add_u32 v239,v239,v237
v_mul_u32_u24 v239,192,v239
v_add_u32 v239,s71,v239
v_xor_b32 v240,0,v238
v_lshl_add_u32 v8,v240,4,v239
v_xor_b32 v240,1,v238
v_lshl_add_u32 v9,v240,4,v239
v_xor_b32 v240,2,v238
v_lshl_add_u32 v10,v240,4,v239
v_xor_b32 v240,3,v238
v_lshl_add_u32 v11,v240,4,v239
v_add_u32 v237,2,v233
v_bfe_u32 v238,v237,2,2
v_mul_u32_u24 v239,18,v236
v_add_u32 v239,v239,v237
v_mul_u32_u24 v239,192,v239
v_add_u32 v239,s71,v239
v_xor_b32 v240,0,v238
v_lshl_add_u32 v12,v240,4,v239
v_xor_b32 v240,1,v238
v_lshl_add_u32 v13,v240,4,v239
v_xor_b32 v240,2,v238
v_lshl_add_u32 v14,v240,4,v239
v_xor_b32 v240,3,v238
v_lshl_add_u32 v15,v240,4,v239
v_lshrrev_b32 v237,2,v233
v_lshl_add_u32 v237,v235,2,v237
v_and_b32 v238,1,v234
v_lshlrev_b32 v238,5,v238
v_and_b32 v239,3,v233
v_lshl_add_u32 v238,v239,3,v238
v_add_u32 v238,124416,v238
v_lshl_add_u32 v16,v237,6,v238
v_mul_u32_u24 v237,192,v237
v_add_u32 v17,v237,v238
v_add_u32 v17,18432,v17
v_lshlrev_b32 v237,4,v235
v_add_u32 v18,161280,v237
v_add_u32 v237,s71,v248
v_add_u32 v19,3648,v237
v_and_b32 v237,1,v234
v_add_u32 v237,1,v237
v_mul_u32_u24 v237,18,v237
v_add_u32 v238,1,v233
v_add_u32 v237,v237,v238
v_mul_u32_u24 v237,192,v237
v_add_u32 v237,s71,v237
v_lshl_add_u32 v237,v235,3,v237
v_bfe_u32 v238,v238,2,2
v_xor_b32 v240,0,v238
v_lshl_add_u32 v20,v240,4,v237
v_xor_b32 v240,1,v238
v_lshl_add_u32 v21,v240,4,v237
v_xor_b32 v240,2,v238
v_lshl_add_u32 v22,v240,4,v237
v_xor_b32 v240,3,v238
v_lshl_add_u32 v23,v240,4,v237
v_add_u32 v237,0,v232
v_mul_u32_u24 v238,0xaaab,v237
v_lshrrev_b32 v238,19,v238
v_mul_u32_u24 v239,12,v238
v_sub_u32 v239,v237,v239
v_add_u32 v237,1,v238
v_bfe_u32 v237,v237,2,2
v_xor_b32 v239,v239,v237
v_lshlrev_b32 v239,4,v239
v_mul_u32_u24 v237,192,v238
v_add_u32 v24,v237,v239
v_add_u32 v237,64,v232
v_mul_u32_u24 v238,0xaaab,v237
v_lshrrev_b32 v238,19,v238
v_mul_u32_u24 v239,12,v238
v_sub_u32 v239,v237,v239
v_add_u32 v237,1,v238
v_bfe_u32 v237,v237,2,2
v_xor_b32 v239,v239,v237
v_lshlrev_b32 v239,4,v239
v_mul_u32_u24 v237,192,v238
v_add_u32 v25,v237,v239
v_add_u32 v237,128,v232
v_mul_u32_u24 v238,0xaaab,v237
v_lshrrev_b32 v238,19,v238
v_mul_u32_u24 v239,12,v238
v_sub_u32 v239,v237,v239
v_add_u32 v237,1,v238
v_bfe_u32 v237,v237,2,2
v_xor_b32 v239,v239,v237
v_lshlrev_b32 v239,4,v239
v_mul_u32_u24 v237,192,v238
v_add_u32 v26,v237,v239
s_cmp_ge_u32 s52,4
s_cbranch_scc1 L_wB_%=
s_waitcnt vmcnt(17)
s_branch L_wA_%=
L_wB_%=:
s_waitcnt vmcnt(11)
L_wA_%=:
ds_read_u16 v36,v250 offset:0
ds_read_u16 v37,v250 offset:16
ds_read_u16 v38,v250 offset:32
ds_read_u16 v39,v250 offset:48
ds_read_u16 v40,v250 offset:64
ds_read_u16 v41,v250 offset:80
ds_read_u16 v42,v250 offset:96
ds_read_u16 v43,v250 offset:112
ds_read_u16 v44,v250 offset:128
ds_read_u16 v45,v250 offset:144
ds_read_u16 v46,v250 offset:160
ds_read_u16 v47,v250 offset:176
ds_read_u16 v48,v250 offset:192
ds_read_u16 v49,v250 offset:208
ds_read_u16 v50,v250 offset:224
ds_read_u16 v51,v250 offset:240
ds_read_u16 v52,v250 offset:256
ds_read_u16 v53,v250 offset:272
ds_read_u16 v54,v250 offset:288
ds_read_u16 v55,v250 offset:304
ds_read_u16 v56,v250 offset:320
ds_read_u16 v57,v250 offset:336
ds_read_u16 v58,v250 offset:352
ds_read_u16 v59,v250 offset:368
ds_read_u16 v60,v250 offset:384
ds_read_u16 v61,v250 offset:400
ds_read_u16 v62,v250 offset:416
ds_read_u16 v63,v250 offset:432
ds_read_u16 v64,v250 offset:448
ds_read_u16 v65,v250 offset:464
ds_read_u16 v66,v250 offset:480
ds_read_u16 v67,v250 offset:496
ds_read_u16 v68,v250 offset:512
ds_read_u16 v69,v250 offset:528
ds_read_u16 v70,v250 offset:544
ds_read_u16 v71,v250 offset:560
v_lshlrev_b32 v244,16,v244
v_lshlrev_b32 v245,16,v245
v_lshlrev_b32 v246,16,v246
v_mul_f32 v244,0xbeeff558,v244
v_mul_f32 v245,0xbeeff558,v245
v_mul_f32 v246,0xc0088e99,v246
v_min_u32 v237,383,%1
v_lshlrev_b32 v237,2,v237
v_add_u32 v237,161280,v237
ds_write_b32 v237,v244 offset:0
v_min_u32 v237,95,%1
v_lshlrev_b32 v237,2,v237
v_add_u32 v237,161280,v237
ds_write_b32 v237,v245 offset:1920
ds_write_b32 v237,v246 offset:1536
s_waitcnt lgkmcnt(0)
v_lshlrev_b32 v36,v243,v36
v_lshlrev_b32 v37,v243,v37
v_lshlrev_b32 v38,v243,v38
v_lshlrev_b32 v39,v243,v39
v_lshlrev_b32 v40,v243,v40
v_lshlrev_b32 v41,v243,v41
v_lshlrev_b32 v42,v243,v42
v_lshlrev_b32 v43,v243,v43
v_lshlrev_b32 v44,v243,v44
v_lshlrev_b32 v45,v243,v45
v_lshlrev_b32 v46,v243,v46
v_lshlrev_b32 v47,v243,v47
v_lshlrev_b32 v48,v243,v48
v_lshlrev_b32 v49,v243,v49
v_lshlrev_b32 v50,v243,v50
v_lshlrev_b32 v51,v243,v51
v_lshlrev_b32 v52,v243,v52
v_lshlrev_b32 v53,v243,v53
v_lshlrev_b32 v54,v243,v54
v_lshlrev_b32 v55,v243,v55
v_lshlrev_b32 v56,v243,v56
v_lshlrev_b32 v57,v243,v57
v_lshlrev_b32 v58,v243,v58
v_lshlrev_b32 v59,v243,v59
v_lshlrev_b32 v60,v243,v60
v_lshlrev_b32 v61,v243,v61
v_lshlrev_b32 v62,v243,v62
v_lshlrev_b32 v63,v243,v63
v_lshlrev_b32 v64,v243,v64
v_lshlrev_b32 v65,v243,v65
v_lshlrev_b32 v66,v243,v66
v_lshlrev_b32 v67,v243,v67
v_lshlrev_b32 v68,v243,v68
v_lshlrev_b32 v69,v243,v69
v_lshlrev_b32 v70,v243,v70
v_lshlrev_b32 v71,v243,v71
s_cmp_ge_u32 s52,4
s_cbranch_scc1 L_fB_%=
s_waitcnt vmcnt(6)
s_branch L_fA_%=
L_fB_%=:
s_waitcnt vmcnt(2)
L_fA_%=:
s_cmp_ge_u32 s52,4
s_cbranch_scc1 L_pass1_%=
L_pass0_%=:
s_barrier
ds_read_b128 v[200:203],v4 offset:0
ds_read_b128 v[204:207],v8 offset:0
ds_read_b128 v[208:211],v12 offset:0
ds_read_b128 v[212:215],v5 offset:0
ds_read_b128 v[216:219],v9 offset:0
ds_read_b128 v[220:223],v13 offset:0
ds_read_b128 v[96:99],v18 offset:1920
ds_read_b128 v[100:103],v18 offset:1952
ds_read_b128 v[104:107],v18 offset:1984
ds_read_b128 v[108:111],v18 offset:2016
ds_read_b128 v[112:115],v18 offset:2048
ds_read_b128 v[116:119],v18 offset:2080
ds_read_b128 v[120:123],v18 offset:2112
ds_read_b128 v[124:127],v18 offset:2144
ds_read_b128 v[128:131],v18 offset:2176
ds_read_b128 v[132:135],v18 offset:2208
ds_read_b128 v[136:139],v18 offset:2240
ds_read_b128 v[140:143],v18 offset:2272
v_and_b32 v72,v32,v36
v_and_b32 v73,v33,v36
v_and_b32 v74,v34,v36
v_and_b32 v75,v35,v36
v_and_b32 v76,v32,v48
v_and_b32 v77,v33,v48
v_and_b32 v78,v34,v48
v_and_b32 v79,v35,v48
v_and_b32 v80,v32,v60
v_and_b32 v81,v33,v60
v_and_b32 v82,v34,v60
v_and_b32 v83,v35,v60
v_and_b32 v84,v32,v37
v_and_b32 v85,v33,v37
v_and_b32 v86,v34,v37
v_and_b32 v87,v35,v37
v_and_b32 v88,v32,v49
v_and_b32 v89,v33,v49
v_and_b32 v90,v34,v49
v_and_b32 v91,v35,v49
v_and_b32 v92,v32,v61
v_and_b32 v93,v33,v61
v_and_b32 v94,v34,v61
v_and_b32 v95,v35,v61
s_setprio 1
s_waitcnt lgkmcnt(15)
v_mfma_f32_16x16x32_bf16 v[144:147],v[72:75],v[200:203],0
v_mfma_f32_16x16x32_bf16 v[144:147],v[76:79],v[204:207],v[144:147]
v_mfma_f32_16x16x32_bf16 v[144:147],v[80:83],v[208:211],v[144:147]
ds_read_b128 v[200:203],v6 offset:0
ds_read_b128 v[204:207],v10 offset:0
ds_read_b128 v[208:211],v14 offset:0
s_setprio 0
v_and_b32 v72,v32,v38
v_and_b32 v73,v33,v38
v_and_b32 v74,v34,v38
v_and_b32 v75,v35,v38
v_and_b32 v76,v32,v50
v_and_b32 v77,v33,v50
v_and_b32 v78,v34,v50
v_and_b32 v79,v35,v50
v_and_b32 v80,v32,v62
v_and_b32 v81,v33,v62
v_and_b32 v82,v34,v62
v_and_b32 v83,v35,v62
v_mul_f32 v237,v144,v144
v_mul_f32 v236,v145,v145
v_mul_f32 v239,v146,v146
v_mul_f32 v238,v147,v147
s_setprio 1
s_waitcnt lgkmcnt(15)
v_mfma_f32_16x16x32_bf16 v[148:151],v[84:87],v[212:215],0
v_mfma_f32_16x16x32_bf16 v[148:151],v[88:91],v[216:219],v[148:151]
v_mfma_f32_16x16x32_bf16 v[148:151],v[92:95],v[220:223],v[148:151]
ds_read_b128 v[212:215],v7 offset:0
ds_read_b128 v[216:219],v11 offset:0
ds_read_b128 v[220:223],v15 offset:0
s_setprio 0
v_and_b32 v84,v32,v39
v_and_b32 v85,v33,v39
v_and_b32 v86,v34,v39
v_and_b32 v87,v35,v39
v_and_b32 v88,v32,v51
v_and_b32 v89,v33,v51
v_and_b32 v90,v34,v51
v_and_b32 v91,v35,v51
v_and_b32 v92,v32,v63
v_and_b32 v93,v33,v63
v_and_b32 v94,v34,v63
v_and_b32 v95,v35,v63
v_add_f32 v232,v144,v148
v_fmac_f32 v237,v148,v148
v_add_f32 v233,v145,v149
v_fmac_f32 v236,v149,v149
v_add_f32 v234,v146,v150
v_fmac_f32 v239,v150,v150
v_add_f32 v235,v147,v151
v_fmac_f32 v238,v151,v151
s_setprio 1
s_waitcnt lgkmcnt(3)
v_mfma_f32_16x16x32_bf16 v[152:155],v[72:75],v[200:203],0
v_mfma_f32_16x16x32_bf16 v[152:155],v[76:79],v[204:207],v[152:155]
v_mfma_f32_16x16x32_bf16 v[152:155],v[80:83],v[208:211],v[152:155]
ds_read_b128 v[200:203],v4 offset:64
ds_read_b128 v[204:207],v8 offset:64
ds_read_b128 v[208:211],v12 offset:64
s_setprio 0
v_and_b32 v72,v32,v40
v_and_b32 v73,v33,v40
v_and_b32 v74,v34,v40
v_and_b32 v75,v35,v40
v_and_b32 v76,v32,v52
v_and_b32 v77,v33,v52
v_and_b32 v78,v34,v52
v_and_b32 v79,v35,v52
v_and_b32 v80,v32,v64
v_and_b32 v81,v33,v64
v_and_b32 v82,v34,v64
v_and_b32 v83,v35,v64
v_add_f32 v232,v232,v152
v_fmac_f32 v237,v152,v152
v_add_f32 v233,v233,v153
v_fmac_f32 v236,v153,v153
v_add_f32 v234,v234,v154
v_fmac_f32 v239,v154,v154
v_add_f32 v235,v235,v155
v_fmac_f32 v238,v155,v155
s_setprio 1
s_waitcnt lgkmcnt(3)
v_mfma_f32_16x16x32_bf16 v[156:159],v[84:87],v[212:215],0
v_mfma_f32_16x16x32_bf16 v[156:159],v[88:91],v[216:219],v[156:159]
v_mfma_f32_16x16x32_bf16 v[156:159],v[92:95],v[220:223],v[156:159]
ds_read_b128 v[212:215],v5 offset:64
ds_read_b128 v[216:219],v9 offset:64
ds_read_b128 v[220:223],v13 offset:64
s_setprio 0
v_and_b32 v84,v32,v41
v_and_b32 v85,v33,v41
v_and_b32 v86,v34,v41
v_and_b32 v87,v35,v41
v_and_b32 v88,v32,v53
v_and_b32 v89,v33,v53
v_and_b32 v90,v34,v53
v_and_b32 v91,v35,v53
v_and_b32 v92,v32,v65
v_and_b32 v93,v33,v65
v_and_b32 v94,v34,v65
v_and_b32 v95,v35,v65
v_add_f32 v232,v232,v156
v_fmac_f32 v237,v156,v156
v_add_f32 v233,v233,v157
v_fmac_f32 v236,v157,v157
v_add_f32 v234,v234,v158
v_fmac_f32 v239,v158,v158
v_add_f32 v235,v235,v159
v_fmac_f32 v238,v159,v159
s_setprio 1
s_waitcnt lgkmcnt(3)
v_mfma_f32_16x16x32_bf16 v[160:163],v[72:75],v[200:203],0
v_mfma_f32_16x16x32_bf16 v[160:163],v[76:79],v[204:207],v[160:163]
v_mfma_f32_16x16x32_bf16 v[160:163],v[80:83],v[208:211],v[160:163]
ds_read_b128 v[200:203],v6 offset:64
ds_read_b128 v[204:207],v10 offset:64
ds_read_b128 v[208:211],v14 offset:64
s_setprio 0
v_and_b32 v72,v32,v42
v_and_b32 v73,v33,v42
v_and_b32 v74,v34,v42
v_and_b32 v75,v35,v42
v_and_b32 v76,v32,v54
v_and_b32 v77,v33,v54
v_and_b32 v78,v34,v54
v_and_b32 v79,v35,v54
v_and_b32 v80,v32,v66
v_and_b32 v81,v33,v66
v_and_b32 v82,v34,v66
v_and_b32 v83,v35,v66
v_add_f32 v232,v232,v160
v_fmac_f32 v237,v160,v160
v_add_f32 v233,v233,v161
v_fmac_f32 v236,v161,v161
v_add_f32 v234,v234,v162
v_fmac_f32 v239,v162,v162
v_add_f32 v235,v235,v163
v_fmac_f32 v238,v163,v163
s_setprio 1
s_waitcnt lgkmcnt(3)
v_mfma_f32_16x16x32_bf16 v[164:167],v[84:87],v[212:215],0
v_mfma_f32_16x16x32_bf16 v[164:167],v[88:91],v[216:219],v[164:167]
v_mfma_f32_16x16x32_bf16 v[164:167],v[92:95],v[220:223],v[164:167]
ds_read_b128 v[212:215],v7 offset:64
ds_read_b128 v[216:219],v11 offset:64
ds_read_b128 v[220:223],v15 offset:64
s_setprio 0
v_and_b32 v84,v32,v43
v_and_b32 v85,v33,v43
v_and_b32 v86,v34,v43
v_and_b32 v87,v35,v43
v_and_b32 v88,v32,v55
v_and_b32 v89,v33,v55
v_and_b32 v90,v34,v55
v_and_b32 v91,v35,v55
v_and_b32 v92,v32,v67
v_and_b32 v93,v33,v67
v_and_b32 v94,v34,v67
v_and_b32 v95,v35,v67
v_add_f32 v232,v232,v164
v_fmac_f32 v237,v164,v164
v_add_f32 v233,v233,v165
v_fmac_f32 v236,v165,v165
v_add_f32 v234,v234,v166
v_fmac_f32 v239,v166,v166
v_add_f32 v235,v235,v167
v_fmac_f32 v238,v167,v167
s_setprio 1
s_waitcnt lgkmcnt(3)
v_mfma_f32_16x16x32_bf16 v[168:171],v[72:75],v[200:203],0
v_mfma_f32_16x16x32_bf16 v[168:171],v[76:79],v[204:207],v[168:171]
v_mfma_f32_16x16x32_bf16 v[168:171],v[80:83],v[208:211],v[168:171]
ds_read_b128 v[200:203],v4 offset:128
ds_read_b128 v[204:207],v8 offset:128
ds_read_b128 v[208:211],v12 offset:128
s_setprio 0
v_and_b32 v72,v32,v44
v_and_b32 v73,v33,v44
v_and_b32 v74,v34,v44
v_and_b32 v75,v35,v44
v_and_b32 v76,v32,v56
v_and_b32 v77,v33,v56
v_and_b32 v78,v34,v56
v_and_b32 v79,v35,v56
v_and_b32 v80,v32,v68
v_and_b32 v81,v33,v68
v_and_b32 v82,v34,v68
v_and_b32 v83,v35,v68
v_add_f32 v232,v232,v168
v_fmac_f32 v237,v168,v168
v_add_f32 v233,v233,v169
v_fmac_f32 v236,v169,v169
v_add_f32 v234,v234,v170
v_fmac_f32 v239,v170,v170
v_add_f32 v235,v235,v171
v_fmac_f32 v238,v171,v171
s_setprio 1
s_waitcnt lgkmcnt(3)
v_mfma_f32_16x16x32_bf16 v[172:175],v[84:87],v[212:215],0
v_mfma_f32_16x16x32_bf16 v[172:175],v[88:91],v[216:219],v[172:175]
v_mfma_f32_16x16x32_bf16 v[172:175],v[92:95],v[220:223],v[172:175]
ds_read_b128 v[212:215],v5 offset:128
ds_read_b128 v[216:219],v9 offset:128
ds_read_b128 v[220:223],v13 offset:128
s_setprio 0
v_and_b32 v84,v32,v45
v_and_b32 v85,v33,v45
v_and_b32 v86,v34,v45
v_and_b32 v87,v35,v45
v_and_b32 v88,v32,v57
v_and_b32 v89,v33,v57
v_and_b32 v90,v34,v57
v_and_b32 v91,v35,v57
v_and_b32 v92,v32,v69
v_and_b32 v93,v33,v69
v_and_b32 v94,v34,v69
v_and_b32 v95,v35,v69
v_add_f32 v232,v232,v172
v_fmac_f32 v237,v172,v172
v_add_f32 v233,v233,v173
v_fmac_f32 v236,v173,v173
v_add_f32 v234,v234,v174
v_fmac_f32 v239,v174,v174
v_add_f32 v235,v235,v175
v_fmac_f32 v238,v175,v175
s_setprio 1
s_waitcnt lgkmcnt(3)
v_mfma_f32_16x16x32_bf16 v[176:179],v[72:75],v[200:203],0
v_mfma_f32_16x16x32_bf16 v[176:179],v[76:79],v[204:207],v[176:179]
v_mfma_f32_16x16x32_bf16 v[176:179],v[80:83],v[208:211],v[176:179]
ds_read_b128 v[200:203],v6 offset:128
ds_read_b128 v[204:207],v10 offset:128
ds_read_b128 v[208:211],v14 offset:128
s_setprio 0
v_and_b32 v72,v32,v46
v_and_b32 v73,v33,v46
v_and_b32 v74,v34,v46
v_and_b32 v75,v35,v46
v_and_b32 v76,v32,v58
v_and_b32 v77,v33,v58
v_and_b32 v78,v34,v58
v_and_b32 v79,v35,v58
v_and_b32 v80,v32,v70
v_and_b32 v81,v33,v70
v_and_b32 v82,v34,v70
v_and_b32 v83,v35,v70
v_add_f32 v232,v232,v176
v_fmac_f32 v237,v176,v176
v_add_f32 v233,v233,v177
v_fmac_f32 v236,v177,v177
v_add_f32 v234,v234,v178
v_fmac_f32 v239,v178,v178
v_add_f32 v235,v235,v179
v_fmac_f32 v238,v179,v179
s_setprio 1
s_waitcnt lgkmcnt(3)
v_mfma_f32_16x16x32_bf16 v[180:183],v[84:87],v[212:215],0
v_mfma_f32_16x16x32_bf16 v[180:183],v[88:91],v[216:219],v[180:183]
v_mfma_f32_16x16x32_bf16 v[180:183],v[92:95],v[220:223],v[180:183]
ds_read_b128 v[212:215],v7 offset:128
ds_read_b128 v[216:219],v11 offset:128
ds_read_b128 v[220:223],v15 offset:128
s_setprio 0
v_and_b32 v84,v32,v47
v_and_b32 v85,v33,v47
v_and_b32 v86,v34,v47
v_and_b32 v87,v35,v47
v_and_b32 v88,v32,v59
v_and_b32 v89,v33,v59
v_and_b32 v90,v34,v59
v_and_b32 v91,v35,v59
v_and_b32 v92,v32,v71
v_and_b32 v93,v33,v71
v_and_b32 v94,v34,v71
v_and_b32 v95,v35,v71
v_add_f32 v232,v232,v180
v_fmac_f32 v237,v180,v180
v_add_f32 v233,v233,v181
v_fmac_f32 v236,v181,v181
v_add_f32 v234,v234,v182
v_fmac_f32 v239,v182,v182
v_add_f32 v235,v235,v183
v_fmac_f32 v238,v183,v183
s_setprio 1
s_waitcnt lgkmcnt(3)
v_mfma_f32_16x16x32_bf16 v[184:187],v[72:75],v[200:203],0
v_mfma_f32_16x16x32_bf16 v[184:187],v[76:79],v[204:207],v[184:187]
v_mfma_f32_16x16x32_bf16 v[184:187],v[80:83],v[208:211],v[184:187]
s_setprio 0
s_nop 7
v_add_f32 v232,v232,v184
v_fmac_f32 v237,v184,v184
v_add_f32 v233,v233,v185
v_fmac_f32 v236,v185,v185
v_add_f32 v234,v234,v186
v_fmac_f32 v239,v186,v186
v_add_f32 v235,v235,v187
v_fmac_f32 v238,v187,v187
s_setprio 1
s_waitcnt lgkmcnt(0)
v_mfma_f32_16x16x32_bf16 v[188:191],v[84:87],v[212:215],0
v_mfma_f32_16x16x32_bf16 v[188:191],v[88:91],v[216:219],v[188:191]
v_mfma_f32_16x16x32_bf16 v[188:191],v[92:95],v[220:223],v[188:191]
s_setprio 0
ds_read_b64_tr_b16 v[200:201],v16 offset:0
ds_read_b64_tr_b16 v[202:203],v16 offset:512
ds_read_b64_tr_b16 v[204:205],v16 offset:1024
ds_read_b64_tr_b16 v[206:207],v16 offset:1536
ds_read_b64_tr_b16 v[208:209],v16 offset:2048
ds_read_b64_tr_b16 v[210:211],v16 offset:2560
ds_read_b64_tr_b16 v[212:213],v16 offset:3072
ds_read_b64_tr_b16 v[214:215],v16 offset:3584
ds_read_b64_tr_b16 v[216:217],v16 offset:4096
ds_read_b64_tr_b16 v[218:219],v16 offset:4608
ds_read_b64_tr_b16 v[220:221],v16 offset:5120
ds_read_b64_tr_b16 v[222:223],v16 offset:5632
v_add_f32 v232,v232,v188
v_fmac_f32 v237,v188,v188
v_add_f32 v233,v233,v189
v_fmac_f32 v236,v189,v189
v_add_f32 v234,v234,v190
v_fmac_f32 v239,v190,v190
v_add_f32 v235,v235,v191
v_fmac_f32 v238,v191,v191
v_add_f32 v232,v232,v233
v_add_f32 v234,v234,v235
v_add_f32 v237,v237,v236
v_add_f32 v239,v239,v238
v_add_f32 v240,v232,v234
v_add_f32 v241,v237,v239
s_nop 1
v_permlane32_swap_b32 v240,v241
v_add_f32 v240,v240,v241
v_mov_b32 v241,v240
s_nop 1
v_permlane32_swap_b32 v240,v241
v_mul_f32 v240,0x3c2aaaab,v240
v_mul_f32 v241,0x3c2aaaab,v241
v_fma_f32 v241,-v240,v240,v241
v_max_f32 v241,0,v241
v_add_f32 v241,0x3727c5ac,v241
v_rsq_f32 v241,v241
s_nop 0
v_mul_f32 v242,0xbeeff558,v241
v_mul_f32 v243,0x3eeff558,v241
v_mul_f32 v243,v240,v243
v_fma_f32 v144,v144,v242,v243
v_fma_f32 v145,v145,v242,v243
v_fma_f32 v146,v146,v242,v243
v_fma_f32 v147,v147,v242,v243
v_fma_f32 v148,v148,v242,v243
v_fma_f32 v149,v149,v242,v243
v_fma_f32 v150,v150,v242,v243
v_fma_f32 v151,v151,v242,v243
v_fma_f32 v152,v152,v242,v243
v_fma_f32 v153,v153,v242,v243
v_fma_f32 v154,v154,v242,v243
v_fma_f32 v155,v155,v242,v243
v_fma_f32 v156,v156,v242,v243
v_fma_f32 v157,v157,v242,v243
v_fma_f32 v158,v158,v242,v243
v_fma_f32 v159,v159,v242,v243
v_fma_f32 v160,v160,v242,v243
v_fma_f32 v161,v161,v242,v243
v_fma_f32 v162,v162,v242,v243
v_fma_f32 v163,v163,v242,v243
v_fma_f32 v164,v164,v242,v243
v_fma_f32 v165,v165,v242,v243
v_fma_f32 v166,v166,v242,v243
v_fma_f32 v167,v167,v242,v243
v_fma_f32 v168,v168,v242,v243
v_fma_f32 v169,v169,v242,v243
v_fma_f32 v170,v170,v242,v243
v_fma_f32 v171,v171,v242,v243
v_fma_f32 v172,v172,v242,v243
v_fma_f32 v173,v173,v242,v243
v_fma_f32 v174,v174,v242,v243
v_fma_f32 v175,v175,v242,v243
v_fma_f32 v176,v176,v242,v243
v_fma_f32 v177,v177,v242,v243
v_fma_f32 v178,v178,v242,v243
v_fma_f32 v179,v179,v242,v243
v_fma_f32 v180,v180,v242,v243
v_fma_f32 v181,v181,v242,v243
v_fma_f32 v182,v182,v242,v243
v_fma_f32 v183,v183,v242,v243
v_fma_f32 v184,v184,v242,v243
v_fma_f32 v185,v185,v242,v243
v_fma_f32 v186,v186,v242,v243
v_fma_f32 v187,v187,v242,v243
v_fma_f32 v188,v188,v242,v243
v_fma_f32 v189,v189,v242,v243
v_fma_f32 v190,v190,v242,v243
v_fma_f32 v191,v191,v242,v243
s_setprio 1
v_cvt_pk_bf16_f32 v72,v144,v145
v_cvt_pk_bf16_f32 v73,v146,v147
v_cvt_pk_bf16_f32 v74,v148,v149
v_cvt_pk_bf16_f32 v75,v150,v151
v_cvt_pk_bf16_f32 v76,v152,v153
v_cvt_pk_bf16_f32 v77,v154,v155
v_cvt_pk_bf16_f32 v78,v156,v157
v_cvt_pk_bf16_f32 v79,v158,v159
ds_read_b128 v[144:147],v18 offset:0
ds_read_b128 v[148:151],v18 offset:32
ds_read_b128 v[152:155],v18 offset:64
ds_read_b128 v[156:159],v18 offset:96
v_cvt_pk_bf16_f32 v80,v160,v161
v_cvt_pk_bf16_f32 v81,v162,v163
v_cvt_pk_bf16_f32 v82,v164,v165
v_cvt_pk_bf16_f32 v83,v166,v167
v_cvt_pk_bf16_f32 v84,v168,v169
v_cvt_pk_bf16_f32 v85,v170,v171
v_cvt_pk_bf16_f32 v86,v172,v173
v_cvt_pk_bf16_f32 v87,v174,v175
s_waitcnt lgkmcnt(0)
v_mfma_f32_32x32x16_bf16 v[144:159],v[200:203],v[72:75],v[144:159]
v_cvt_pk_bf16_f32 v88,v176,v177
v_cvt_pk_bf16_f32 v89,v178,v179
v_cvt_pk_bf16_f32 v90,v180,v181
v_cvt_pk_bf16_f32 v91,v182,v183
v_mfma_f32_32x32x16_bf16 v[144:159],v[204:207],v[76:79],v[144:159]
v_cvt_pk_bf16_f32 v92,v184,v185
v_cvt_pk_bf16_f32 v93,v186,v187
v_cvt_pk_bf16_f32 v94,v188,v189
v_cvt_pk_bf16_f32 v95,v190,v191
v_mfma_f32_32x32x16_bf16 v[144:159],v[208:211],v[80:83],v[144:159]
v_mfma_f32_32x32x16_bf16 v[144:159],v[212:215],v[84:87],v[144:159]
v_mfma_f32_32x32x16_bf16 v[144:159],v[216:219],v[88:91],v[144:159]
v_mfma_f32_32x32x16_bf16 v[144:159],v[220:223],v[92:95],v[144:159]
s_setprio 0
s_nop 7
s_nop 1
s_waitcnt vmcnt(2)
s_barrier
v_fmaak_f32 v176,v144,v144,0x409d30fc
v_fmaak_f32 v177,v145,v145,0x409d30fc
v_fmaak_f32 v178,v146,v146,0x409d30fc
v_fmaak_f32 v179,v147,v147,0x409d30fc
v_fmaak_f32 v180,v148,v148,0x409d30fc
v_fmaak_f32 v181,v149,v149,0x409d30fc
v_fmaak_f32 v182,v150,v150,0x409d30fc
v_fmaak_f32 v183,v151,v151,0x409d30fc
v_fmaak_f32 v184,v152,v152,0x409d30fc
v_fmaak_f32 v185,v153,v153,0x409d30fc
v_fmaak_f32 v186,v154,v154,0x409d30fc
v_fmaak_f32 v187,v155,v155,0x409d30fc
v_fmaak_f32 v188,v156,v156,0x409d30fc
v_fmaak_f32 v189,v157,v157,0x409d30fc
v_fmaak_f32 v190,v158,v158,0x409d30fc
v_fmaak_f32 v191,v159,v159,0x409d30fc
v_mul_f32 v176,v144,v176
v_mul_f32 v177,v145,v177
v_mul_f32 v178,v146,v178
v_mul_f32 v179,v147,v179
v_mul_f32 v180,v148,v180
v_mul_f32 v181,v149,v181
v_mul_f32 v182,v150,v182
v_mul_f32 v183,v151,v183
v_mul_f32 v184,v152,v184
v_mul_f32 v185,v153,v185
v_mul_f32 v186,v154,v186
v_mul_f32 v187,v155,v187
v_mul_f32 v188,v156,v188
v_mul_f32 v189,v157,v189
v_mul_f32 v190,v158,v190
v_mul_f32 v191,v159,v191
s_setprio 1
v_exp_f32 v176,v176
v_exp_f32 v177,v177
v_exp_f32 v178,v178
v_exp_f32 v179,v179
v_exp_f32 v180,v180
v_exp_f32 v181,v181
v_exp_f32 v182,v182
v_exp_f32 v183,v183
v_exp_f32 v184,v184
v_exp_f32 v185,v185
v_exp_f32 v186,v186
v_exp_f32 v187,v187
v_exp_f32 v188,v188
v_exp_f32 v189,v189
v_exp_f32 v190,v190
v_exp_f32 v191,v191
ds_read_b64_tr_b16 v[200:201],v16 offset:6144
ds_read_b64_tr_b16 v[202:203],v16 offset:6656
ds_read_b64_tr_b16 v[204:205],v16 offset:7168
ds_read_b64_tr_b16 v[206:207],v16 offset:7680
ds_read_b64_tr_b16 v[208:209],v16 offset:8192
ds_read_b64_tr_b16 v[210:211],v16 offset:8704
ds_read_b64_tr_b16 v[212:213],v16 offset:9216
ds_read_b64_tr_b16 v[214:215],v16 offset:9728
ds_read_b64_tr_b16 v[216:217],v16 offset:10240
ds_read_b64_tr_b16 v[218:219],v16 offset:10752
ds_read_b64_tr_b16 v[220:221],v16 offset:11264
ds_read_b64_tr_b16 v[222:223],v16 offset:11776
ds_read_b128 v[160:163],v18 offset:128
ds_read_b128 v[164:167],v18 offset:160
ds_read_b128 v[168:171],v18 offset:192
ds_read_b128 v[172:175],v18 offset:224
s_mov_b64 exec,s[60:61]
s_add_u32 m0,s58,148992
s_mov_b32 s64,6144
buffer_load_dwordx4 v28,s[48:51],s64 offen lds
s_add_u32 m0,s58,149760
s_mov_b32 s65,6912
buffer_load_dwordx4 v28,s[48:51],s65 offen lds
s_mov_b64 exec,-1
s_setprio 0
v_add_f32 v176,1.0,v176
v_add_f32 v177,1.0,v177
v_add_f32 v178,1.0,v178
v_add_f32 v179,1.0,v179
v_add_f32 v180,1.0,v180
v_add_f32 v181,1.0,v181
v_add_f32 v182,1.0,v182
v_add_f32 v183,1.0,v183
v_add_f32 v184,1.0,v184
v_add_f32 v185,1.0,v185
v_add_f32 v186,1.0,v186
v_add_f32 v187,1.0,v187
v_add_f32 v188,1.0,v188
v_add_f32 v189,1.0,v189
v_add_f32 v190,1.0,v190
v_add_f32 v191,1.0,v191
s_setprio 1
s_waitcnt lgkmcnt(0)
v_mfma_f32_32x32x16_bf16 v[160:175],v[200:203],v[72:75],v[160:175]
v_rcp_f32 v176,v176
v_rcp_f32 v177,v177
v_rcp_f32 v178,v178
v_mfma_f32_32x32x16_bf16 v[160:175],v[204:207],v[76:79],v[160:175]
v_rcp_f32 v179,v179
v_rcp_f32 v180,v180
v_rcp_f32 v181,v181
v_mfma_f32_32x32x16_bf16 v[160:175],v[208:211],v[80:83],v[160:175]
v_rcp_f32 v182,v182
v_rcp_f32 v183,v183
v_rcp_f32 v184,v184
v_mfma_f32_32x32x16_bf16 v[160:175],v[212:215],v[84:87],v[160:175]
v_rcp_f32 v185,v185
v_rcp_f32 v186,v186
v_rcp_f32 v187,v187
v_mfma_f32_32x32x16_bf16 v[160:175],v[216:219],v[88:91],v[160:175]
v_rcp_f32 v188,v188
v_rcp_f32 v189,v189
v_mfma_f32_32x32x16_bf16 v[160:175],v[220:223],v[92:95],v[160:175]
v_rcp_f32 v190,v190
v_rcp_f32 v191,v191
ds_read_b64_tr_b16 v[200:201],v17 offset:0
ds_read_b64_tr_b16 v[202:203],v17 offset:1536
ds_read_b64_tr_b16 v[204:205],v17 offset:3072
ds_read_b64_tr_b16 v[206:207],v17 offset:4608
ds_read_b64_tr_b16 v[208:209],v17 offset:64
ds_read_b64_tr_b16 v[210:211],v17 offset:1600
ds_read_b64_tr_b16 v[212:213],v17 offset:3136
ds_read_b64_tr_b16 v[214:215],v17 offset:4672
ds_read_b64_tr_b16 v[216:217],v17 offset:128
ds_read_b64_tr_b16 v[218:219],v17 offset:1664
ds_read_b64_tr_b16 v[220:221],v17 offset:3200
ds_read_b64_tr_b16 v[222:223],v17 offset:4736
s_mov_b64 exec,s[60:61]
s_add_u32 m0,s58,124416
s_movk_i32 s64,192
buffer_load_dwordx4 v27,s[44:47],s64 offen lds
s_add_u32 m0,s58,125184
s_movk_i32 s65,9408
buffer_load_dwordx4 v27,s[44:47],s65 offen lds
s_mov_b64 exec,-1
s_setprio 0
v_mul_f32 v176,v144,v176
v_mul_f32 v177,v145,v177
v_mul_f32 v178,v146,v178
v_mul_f32 v179,v147,v179
v_mul_f32 v180,v148,v180
v_mul_f32 v181,v149,v181
v_mul_f32 v182,v150,v182
v_mul_f32 v183,v151,v183
v_mul_f32 v184,v152,v184
v_mul_f32 v185,v153,v185
v_mul_f32 v186,v154,v186
v_mul_f32 v187,v155,v187
v_mul_f32 v188,v156,v188
v_mul_f32 v189,v157,v189
v_mul_f32 v190,v158,v190
v_mul_f32 v191,v159,v191
v_cvt_pk_bf16_f32 v192,v176,v177
v_cvt_pk_bf16_f32 v193,v178,v179
v_cvt_pk_bf16_f32 v194,v180,v181
v_cvt_pk_bf16_f32 v195,v182,v183
v_cvt_pk_bf16_f32 v196,v184,v185
v_cvt_pk_bf16_f32 v197,v186,v187
v_cvt_pk_bf16_f32 v198,v188,v189
v_cvt_pk_bf16_f32 v199,v190,v191
s_waitcnt lgkmcnt(0)
s_waitcnt vmcnt(2)
s_barrier
v_fmaak_f32 v176,v160,v160,0x409d30fc
v_fmaak_f32 v177,v161,v161,0x409d30fc
v_fmaak_f32 v178,v162,v162,0x409d30fc
v_fmaak_f32 v179,v163,v163,0x409d30fc
v_fmaak_f32 v180,v164,v164,0x409d30fc
v_fmaak_f32 v181,v165,v165,0x409d30fc
v_fmaak_f32 v182,v166,v166,0x409d30fc
v_fmaak_f32 v183,v167,v167,0x409d30fc
v_fmaak_f32 v184,v168,v168,0x409d30fc
v_fmaak_f32 v185,v169,v169,0x409d30fc
v_fmaak_f32 v186,v170,v170,0x409d30fc
v_fmaak_f32 v187,v171,v171,0x409d30fc
v_fmaak_f32 v188,v172,v172,0x409d30fc
v_fmaak_f32 v189,v173,v173,0x409d30fc
v_fmaak_f32 v190,v174,v174,0x409d30fc
v_fmaak_f32 v191,v175,v175,0x409d30fc
v_mul_f32 v176,v160,v176
v_mul_f32 v177,v161,v177
v_mul_f32 v178,v162,v178
v_mul_f32 v179,v163,v179
v_mul_f32 v180,v164,v180
v_mul_f32 v181,v165,v181
v_mul_f32 v182,v166,v182
v_mul_f32 v183,v167,v183
v_mul_f32 v184,v168,v184
v_mul_f32 v185,v169,v185
v_mul_f32 v186,v170,v186
v_mul_f32 v187,v171,v187
v_mul_f32 v188,v172,v188
v_mul_f32 v189,v173,v189
v_mul_f32 v190,v174,v190
v_mul_f32 v191,v175,v191
s_setprio 1
v_mfma_f32_32x32x16_bf16 v[96:111],v[200:203],v[192:195],v[96:111]
v_exp_f32 v176,v176
v_exp_f32 v177,v177
v_exp_f32 v178,v178
v_mfma_f32_32x32x16_bf16 v[96:111],v[204:207],v[196:199],v[96:111]
v_exp_f32 v179,v179
v_exp_f32 v180,v180
v_exp_f32 v181,v181
v_mfma_f32_32x32x16_bf16 v[112:127],v[208:211],v[192:195],v[112:127]
v_exp_f32 v182,v182
v_exp_f32 v183,v183
v_exp_f32 v184,v184
v_mfma_f32_32x32x16_bf16 v[112:127],v[212:215],v[196:199],v[112:127]
v_exp_f32 v185,v185
v_exp_f32 v186,v186
v_exp_f32 v187,v187
v_mfma_f32_32x32x16_bf16 v[128:143],v[216:219],v[192:195],v[128:143]
v_exp_f32 v188,v188
v_exp_f32 v189,v189
v_mfma_f32_32x32x16_bf16 v[128:143],v[220:223],v[196:199],v[128:143]
v_exp_f32 v190,v190
v_exp_f32 v191,v191
ds_read_b64_tr_b16 v[200:201],v16 offset:12288
ds_read_b64_tr_b16 v[202:203],v16 offset:12800
ds_read_b64_tr_b16 v[204:205],v16 offset:13312
ds_read_b64_tr_b16 v[206:207],v16 offset:13824
ds_read_b64_tr_b16 v[208:209],v16 offset:14336
ds_read_b64_tr_b16 v[210:211],v16 offset:14848
ds_read_b64_tr_b16 v[212:213],v16 offset:15360
ds_read_b64_tr_b16 v[214:215],v16 offset:15872
ds_read_b64_tr_b16 v[216:217],v16 offset:16384
ds_read_b64_tr_b16 v[218:219],v16 offset:16896
ds_read_b64_tr_b16 v[220:221],v16 offset:17408
ds_read_b64_tr_b16 v[222:223],v16 offset:17920
ds_read_b128 v[144:147],v18 offset:256
ds_read_b128 v[148:151],v18 offset:288
ds_read_b128 v[152:155],v18 offset:320
ds_read_b128 v[156:159],v18 offset:352
s_mov_b64 exec,s[60:61]
s_add_u32 m0,s58,155136
s_mov_b32 s64,12288
buffer_load_dwordx4 v28,s[48:51],s64 offen lds
s_add_u32 m0,s58,155904
s_mov_b32 s65,13056
buffer_load_dwordx4 v28,s[48:51],s65 offen lds
s_mov_b64 exec,-1
s_setprio 0
v_add_f32 v176,1.0,v176
v_add_f32 v177,1.0,v177
v_add_f32 v178,1.0,v178
v_add_f32 v179,1.0,v179
v_add_f32 v180,1.0,v180
v_add_f32 v181,1.0,v181
v_add_f32 v182,1.0,v182
v_add_f32 v183,1.0,v183
v_add_f32 v184,1.0,v184
v_add_f32 v185,1.0,v185
v_add_f32 v186,1.0,v186
v_add_f32 v187,1.0,v187
v_add_f32 v188,1.0,v188
v_add_f32 v189,1.0,v189
v_add_f32 v190,1.0,v190
v_add_f32 v191,1.0,v191
s_setprio 1
s_waitcnt lgkmcnt(0)
v_mfma_f32_32x32x16_bf16 v[144:159],v[200:203],v[72:75],v[144:159]
v_rcp_f32 v176,v176
v_rcp_f32 v177,v177
v_rcp_f32 v178,v178
v_mfma_f32_32x32x16_bf16 v[144:159],v[204:207],v[76:79],v[144:159]
v_rcp_f32 v179,v179
v_rcp_f32 v180,v180
v_rcp_f32 v181,v181
v_mfma_f32_32x32x16_bf16 v[144:159],v[208:211],v[80:83],v[144:159]
v_rcp_f32 v182,v182
v_rcp_f32 v183,v183
v_rcp_f32 v184,v184
v_mfma_f32_32x32x16_bf16 v[144:159],v[212:215],v[84:87],v[144:159]
v_rcp_f32 v185,v185
v_rcp_f32 v186,v186
v_rcp_f32 v187,v187
v_mfma_f32_32x32x16_bf16 v[144:159],v[216:219],v[88:91],v[144:159]
v_rcp_f32 v188,v188
v_rcp_f32 v189,v189
v_mfma_f32_32x32x16_bf16 v[144:159],v[220:223],v[92:95],v[144:159]
v_rcp_f32 v190,v190
v_rcp_f32 v191,v191
ds_read_b64_tr_b16 v[200:201],v17 offset:6144
ds_read_b64_tr_b16 v[202:203],v17 offset:7680
ds_read_b64_tr_b16 v[204:205],v17 offset:9216
ds_read_b64_tr_b16 v[206:207],v17 offset:10752
ds_read_b64_tr_b16 v[208:209],v17 offset:6208
ds_read_b64_tr_b16 v[210:211],v17 offset:7744
ds_read_b64_tr_b16 v[212:213],v17 offset:9280
ds_read_b64_tr_b16 v[214:215],v17 offset:10816
ds_read_b64_tr_b16 v[216:217],v17 offset:6272
ds_read_b64_tr_b16 v[218:219],v17 offset:7808
ds_read_b64_tr_b16 v[220:221],v17 offset:9344
ds_read_b64_tr_b16 v[222:223],v17 offset:10880
s_mov_b64 exec,s[60:61]
s_add_u32 m0,s58,130560
s_movk_i32 s64,256
buffer_load_dwordx4 v27,s[44:47],s64 offen lds
s_add_u32 m0,s58,131328
s_movk_i32 s65,9472
buffer_load_dwordx4 v27,s[44:47],s65 offen lds
s_mov_b64 exec,-1
s_setprio 0
v_mul_f32 v176,v160,v176
v_mul_f32 v177,v161,v177
v_mul_f32 v178,v162,v178
v_mul_f32 v179,v163,v179
v_mul_f32 v180,v164,v180
v_mul_f32 v181,v165,v181
v_mul_f32 v182,v166,v182
v_mul_f32 v183,v167,v183
v_mul_f32 v184,v168,v184
v_mul_f32 v185,v169,v185
v_mul_f32 v186,v170,v186
v_mul_f32 v187,v171,v187
v_mul_f32 v188,v172,v188
v_mul_f32 v189,v173,v189
v_mul_f32 v190,v174,v190
v_mul_f32 v191,v175,v191
v_cvt_pk_bf16_f32 v192,v176,v177
v_cvt_pk_bf16_f32 v193,v178,v179
v_cvt_pk_bf16_f32 v194,v180,v181
v_cvt_pk_bf16_f32 v195,v182,v183
v_cvt_pk_bf16_f32 v196,v184,v185
v_cvt_pk_bf16_f32 v197,v186,v187
v_cvt_pk_bf16_f32 v198,v188,v189
v_cvt_pk_bf16_f32 v199,v190,v191
s_waitcnt lgkmcnt(0)
s_waitcnt vmcnt(2)
s_barrier
v_fmaak_f32 v176,v144,v144,0x409d30fc
v_fmaak_f32 v177,v145,v145,0x409d30fc
v_fmaak_f32 v178,v146,v146,0x409d30fc
v_fmaak_f32 v179,v147,v147,0x409d30fc
v_fmaak_f32 v180,v148,v148,0x409d30fc
v_fmaak_f32 v181,v149,v149,0x409d30fc
v_fmaak_f32 v182,v150,v150,0x409d30fc
v_fmaak_f32 v183,v151,v151,0x409d30fc
v_fmaak_f32 v184,v152,v152,0x409d30fc
v_fmaak_f32 v185,v153,v153,0x409d30fc
v_fmaak_f32 v186,v154,v154,0x409d30fc
v_fmaak_f32 v187,v155,v155,0x409d30fc
v_fmaak_f32 v188,v156,v156,0x409d30fc
v_fmaak_f32 v189,v157,v157,0x409d30fc
v_fmaak_f32 v190,v158,v158,0x409d30fc
v_fmaak_f32 v191,v159,v159,0x409d30fc
v_mul_f32 v176,v144,v176
v_mul_f32 v177,v145,v177
v_mul_f32 v178,v146,v178
v_mul_f32 v179,v147,v179
v_mul_f32 v180,v148,v180
v_mul_f32 v181,v149,v181
v_mul_f32 v182,v150,v182
v_mul_f32 v183,v151,v183
v_mul_f32 v184,v152,v184
v_mul_f32 v185,v153,v185
v_mul_f32 v186,v154,v186
v_mul_f32 v187,v155,v187
v_mul_f32 v188,v156,v188
v_mul_f32 v189,v157,v189
v_mul_f32 v190,v158,v190
v_mul_f32 v191,v159,v191
s_setprio 1
v_mfma_f32_32x32x16_bf16 v[96:111],v[200:203],v[192:195],v[96:111]
v_exp_f32 v176,v176
v_exp_f32 v177,v177
v_exp_f32 v178,v178
v_mfma_f32_32x32x16_bf16 v[96:111],v[204:207],v[196:199],v[96:111]
v_exp_f32 v179,v179
v_exp_f32 v180,v180
v_exp_f32 v181,v181
v_mfma_f32_32x32x16_bf16 v[112:127],v[208:211],v[192:195],v[112:127]
v_exp_f32 v182,v182
v_exp_f32 v183,v183
v_exp_f32 v184,v184
v_mfma_f32_32x32x16_bf16 v[112:127],v[212:215],v[196:199],v[112:127]
v_exp_f32 v185,v185
v_exp_f32 v186,v186
v_exp_f32 v187,v187
v_mfma_f32_32x32x16_bf16 v[128:143],v[216:219],v[192:195],v[128:143]
v_exp_f32 v188,v188
v_exp_f32 v189,v189
v_mfma_f32_32x32x16_bf16 v[128:143],v[220:223],v[196:199],v[128:143]
v_exp_f32 v190,v190
v_exp_f32 v191,v191
ds_read_b64_tr_b16 v[200:201],v16 offset:0
ds_read_b64_tr_b16 v[202:203],v16 offset:512
ds_read_b64_tr_b16 v[204:205],v16 offset:1024
ds_read_b64_tr_b16 v[206:207],v16 offset:1536
ds_read_b64_tr_b16 v[208:209],v16 offset:2048
ds_read_b64_tr_b16 v[210:211],v16 offset:2560
ds_read_b64_tr_b16 v[212:213],v16 offset:3072
ds_read_b64_tr_b16 v[214:215],v16 offset:3584
ds_read_b64_tr_b16 v[216:217],v16 offset:4096
ds_read_b64_tr_b16 v[218:219],v16 offset:4608
ds_read_b64_tr_b16 v[220:221],v16 offset:5120
ds_read_b64_tr_b16 v[222:223],v16 offset:5632
ds_read_b128 v[160:163],v18 offset:384
ds_read_b128 v[164:167],v18 offset:416
ds_read_b128 v[168:171],v18 offset:448
ds_read_b128 v[172:175],v18 offset:480
s_mov_b64 exec,s[60:61]
s_add_u32 m0,s58,142848
s_mov_b32 s64,18432
buffer_load_dwordx4 v28,s[48:51],s64 offen lds
s_add_u32 m0,s58,143616
s_mov_b32 s65,19200
buffer_load_dwordx4 v28,s[48:51],s65 offen lds
s_mov_b64 exec,-1
s_setprio 0
v_add_f32 v176,1.0,v176
v_add_f32 v177,1.0,v177
v_add_f32 v178,1.0,v178
v_add_f32 v179,1.0,v179
v_add_f32 v180,1.0,v180
v_add_f32 v181,1.0,v181
v_add_f32 v182,1.0,v182
v_add_f32 v183,1.0,v183
v_add_f32 v184,1.0,v184
v_add_f32 v185,1.0,v185
v_add_f32 v186,1.0,v186
v_add_f32 v187,1.0,v187
v_add_f32 v188,1.0,v188
v_add_f32 v189,1.0,v189
v_add_f32 v190,1.0,v190
v_add_f32 v191,1.0,v191
s_setprio 1
s_waitcnt lgkmcnt(0)
v_mfma_f32_32x32x16_bf16 v[160:175],v[200:203],v[72:75],v[160:175]
v_rcp_f32 v176,v176
v_rcp_f32 v177,v177
v_rcp_f32 v178,v178
v_mfma_f32_32x32x16_bf16 v[160:175],v[204:207],v[76:79],v[160:175]
v_rcp_f32 v179,v179
v_rcp_f32 v180,v180
v_rcp_f32 v181,v181
v_mfma_f32_32x32x16_bf16 v[160:175],v[208:211],v[80:83],v[160:175]
v_rcp_f32 v182,v182
v_rcp_f32 v183,v183
v_rcp_f32 v184,v184
v_mfma_f32_32x32x16_bf16 v[160:175],v[212:215],v[84:87],v[160:175]
v_rcp_f32 v185,v185
v_rcp_f32 v186,v186
v_rcp_f32 v187,v187
v_mfma_f32_32x32x16_bf16 v[160:175],v[216:219],v[88:91],v[160:175]
v_rcp_f32 v188,v188
v_rcp_f32 v189,v189
v_mfma_f32_32x32x16_bf16 v[160:175],v[220:223],v[92:95],v[160:175]
v_rcp_f32 v190,v190
v_rcp_f32 v191,v191
ds_read_b64_tr_b16 v[200:201],v17 offset:12288
ds_read_b64_tr_b16 v[202:203],v17 offset:13824
ds_read_b64_tr_b16 v[204:205],v17 offset:15360
ds_read_b64_tr_b16 v[206:207],v17 offset:16896
ds_read_b64_tr_b16 v[208:209],v17 offset:12352
ds_read_b64_tr_b16 v[210:211],v17 offset:13888
ds_read_b64_tr_b16 v[212:213],v17 offset:15424
ds_read_b64_tr_b16 v[214:215],v17 offset:16960
ds_read_b64_tr_b16 v[216:217],v17 offset:12416
ds_read_b64_tr_b16 v[218:219],v17 offset:13952
ds_read_b64_tr_b16 v[220:221],v17 offset:15488
ds_read_b64_tr_b16 v[222:223],v17 offset:17024
s_mov_b64 exec,s[60:61]
s_add_u32 m0,s58,136704
s_movk_i32 s64,320
buffer_load_dwordx4 v27,s[44:47],s64 offen lds
s_add_u32 m0,s58,137472
s_movk_i32 s65,9536
buffer_load_dwordx4 v27,s[44:47],s65 offen lds
s_mov_b64 exec,-1
s_setprio 0
v_mul_f32 v176,v144,v176
v_mul_f32 v177,v145,v177
v_mul_f32 v178,v146,v178
v_mul_f32 v179,v147,v179
v_mul_f32 v180,v148,v180
v_mul_f32 v181,v149,v181
v_mul_f32 v182,v150,v182
v_mul_f32 v183,v151,v183
v_mul_f32 v184,v152,v184
v_mul_f32 v185,v153,v185
v_mul_f32 v186,v154,v186
v_mul_f32 v187,v155,v187
v_mul_f32 v188,v156,v188
v_mul_f32 v189,v157,v189
v_mul_f32 v190,v158,v190
v_mul_f32 v191,v159,v191
v_cvt_pk_bf16_f32 v192,v176,v177
v_cvt_pk_bf16_f32 v193,v178,v179
v_cvt_pk_bf16_f32 v194,v180,v181
v_cvt_pk_bf16_f32 v195,v182,v183
v_cvt_pk_bf16_f32 v196,v184,v185
v_cvt_pk_bf16_f32 v197,v186,v187
v_cvt_pk_bf16_f32 v198,v188,v189
v_cvt_pk_bf16_f32 v199,v190,v191
s_waitcnt lgkmcnt(0)
s_waitcnt vmcnt(2)
s_barrier
v_fmaak_f32 v176,v160,v160,0x409d30fc
v_fmaak_f32 v177,v161,v161,0x409d30fc
v_fmaak_f32 v178,v162,v162,0x409d30fc
v_fmaak_f32 v179,v163,v163,0x409d30fc
v_fmaak_f32 v180,v164,v164,0x409d30fc
v_fmaak_f32 v181,v165,v165,0x409d30fc
v_fmaak_f32 v182,v166,v166,0x409d30fc
v_fmaak_f32 v183,v167,v167,0x409d30fc
v_fmaak_f32 v184,v168,v168,0x409d30fc
v_fmaak_f32 v185,v169,v169,0x409d30fc
v_fmaak_f32 v186,v170,v170,0x409d30fc
v_fmaak_f32 v187,v171,v171,0x409d30fc
v_fmaak_f32 v188,v172,v172,0x409d30fc
v_fmaak_f32 v189,v173,v173,0x409d30fc
v_fmaak_f32 v190,v174,v174,0x409d30fc
v_fmaak_f32 v191,v175,v175,0x409d30fc
v_mul_f32 v176,v160,v176
v_mul_f32 v177,v161,v177
v_mul_f32 v178,v162,v178
v_mul_f32 v179,v163,v179
v_mul_f32 v180,v164,v180
v_mul_f32 v181,v165,v181
v_mul_f32 v182,v166,v182
v_mul_f32 v183,v167,v183
v_mul_f32 v184,v168,v184
v_mul_f32 v185,v169,v185
v_mul_f32 v186,v170,v186
v_mul_f32 v187,v171,v187
v_mul_f32 v188,v172,v188
v_mul_f32 v189,v173,v189
v_mul_f32 v190,v174,v190
v_mul_f32 v191,v175,v191
s_setprio 1
v_mfma_f32_32x32x16_bf16 v[96:111],v[200:203],v[192:195],v[96:111]
v_exp_f32 v176,v176
v_exp_f32 v177,v177
v_exp_f32 v178,v178
v_mfma_f32_32x32x16_bf16 v[96:111],v[204:207],v[196:199],v[96:111]
v_exp_f32 v179,v179
v_exp_f32 v180,v180
v_exp_f32 v181,v181
v_mfma_f32_32x32x16_bf16 v[112:127],v[208:211],v[192:195],v[112:127]
v_exp_f32 v182,v182
v_exp_f32 v183,v183
v_exp_f32 v184,v184
v_mfma_f32_32x32x16_bf16 v[112:127],v[212:215],v[196:199],v[112:127]
v_exp_f32 v185,v185
v_exp_f32 v186,v186
v_exp_f32 v187,v187
v_mfma_f32_32x32x16_bf16 v[128:143],v[216:219],v[192:195],v[128:143]
v_exp_f32 v188,v188
v_exp_f32 v189,v189
v_mfma_f32_32x32x16_bf16 v[128:143],v[220:223],v[196:199],v[128:143]
v_exp_f32 v190,v190
v_exp_f32 v191,v191
ds_read_b64_tr_b16 v[200:201],v16 offset:6144
ds_read_b64_tr_b16 v[202:203],v16 offset:6656
ds_read_b64_tr_b16 v[204:205],v16 offset:7168
ds_read_b64_tr_b16 v[206:207],v16 offset:7680
ds_read_b64_tr_b16 v[208:209],v16 offset:8192
ds_read_b64_tr_b16 v[210:211],v16 offset:8704
ds_read_b64_tr_b16 v[212:213],v16 offset:9216
ds_read_b64_tr_b16 v[214:215],v16 offset:9728
ds_read_b64_tr_b16 v[216:217],v16 offset:10240
ds_read_b64_tr_b16 v[218:219],v16 offset:10752
ds_read_b64_tr_b16 v[220:221],v16 offset:11264
ds_read_b64_tr_b16 v[222:223],v16 offset:11776
ds_read_b128 v[144:147],v18 offset:512
ds_read_b128 v[148:151],v18 offset:544
ds_read_b128 v[152:155],v18 offset:576
ds_read_b128 v[156:159],v18 offset:608
s_mov_b64 exec,s[60:61]
s_add_u32 m0,s58,148992
s_mov_b32 s64,24576
buffer_load_dwordx4 v28,s[48:51],s64 offen lds
s_add_u32 m0,s58,149760
s_mov_b32 s65,25344
buffer_load_dwordx4 v28,s[48:51],s65 offen lds
s_mov_b64 exec,-1
s_setprio 0
v_add_f32 v176,1.0,v176
v_add_f32 v177,1.0,v177
v_add_f32 v178,1.0,v178
v_add_f32 v179,1.0,v179
v_add_f32 v180,1.0,v180
v_add_f32 v181,1.0,v181
v_add_f32 v182,1.0,v182
v_add_f32 v183,1.0,v183
v_add_f32 v184,1.0,v184
v_add_f32 v185,1.0,v185
v_add_f32 v186,1.0,v186
v_add_f32 v187,1.0,v187
v_add_f32 v188,1.0,v188
v_add_f32 v189,1.0,v189
v_add_f32 v190,1.0,v190
v_add_f32 v191,1.0,v191
s_setprio 1
s_waitcnt lgkmcnt(0)
v_mfma_f32_32x32x16_bf16 v[144:159],v[200:203],v[72:75],v[144:159]
v_rcp_f32 v176,v176
v_rcp_f32 v177,v177
v_rcp_f32 v178,v178
v_mfma_f32_32x32x16_bf16 v[144:159],v[204:207],v[76:79],v[144:159]
v_rcp_f32 v179,v179
v_rcp_f32 v180,v180
v_rcp_f32 v181,v181
v_mfma_f32_32x32x16_bf16 v[144:159],v[208:211],v[80:83],v[144:159]
v_rcp_f32 v182,v182
v_rcp_f32 v183,v183
v_rcp_f32 v184,v184
v_mfma_f32_32x32x16_bf16 v[144:159],v[212:215],v[84:87],v[144:159]
v_rcp_f32 v185,v185
v_rcp_f32 v186,v186
v_rcp_f32 v187,v187
v_mfma_f32_32x32x16_bf16 v[144:159],v[216:219],v[88:91],v[144:159]
v_rcp_f32 v188,v188
v_rcp_f32 v189,v189
v_mfma_f32_32x32x16_bf16 v[144:159],v[220:223],v[92:95],v[144:159]
v_rcp_f32 v190,v190
v_rcp_f32 v191,v191
ds_read_b64_tr_b16 v[200:201],v17 offset:0
ds_read_b64_tr_b16 v[202:203],v17 offset:1536
ds_read_b64_tr_b16 v[204:205],v17 offset:3072
ds_read_b64_tr_b16 v[206:207],v17 offset:4608
ds_read_b64_tr_b16 v[208:209],v17 offset:64
ds_read_b64_tr_b16 v[210:211],v17 offset:1600
ds_read_b64_tr_b16 v[212:213],v17 offset:3136
ds_read_b64_tr_b16 v[214:215],v17 offset:4672
ds_read_b64_tr_b16 v[216:217],v17 offset:128
ds_read_b64_tr_b16 v[218:219],v17 offset:1664
ds_read_b64_tr_b16 v[220:221],v17 offset:3200
ds_read_b64_tr_b16 v[222:223],v17 offset:4736
s_mov_b64 exec,s[60:61]
s_add_u32 m0,s58,124416
s_movk_i32 s64,384
buffer_load_dwordx4 v27,s[44:47],s64 offen lds
s_add_u32 m0,s58,125184
s_movk_i32 s65,9600
buffer_load_dwordx4 v27,s[44:47],s65 offen lds
s_mov_b64 exec,-1
s_setprio 0
v_mul_f32 v176,v160,v176
v_mul_f32 v177,v161,v177
v_mul_f32 v178,v162,v178
v_mul_f32 v179,v163,v179
v_mul_f32 v180,v164,v180
v_mul_f32 v181,v165,v181
v_mul_f32 v182,v166,v182
v_mul_f32 v183,v167,v183
v_mul_f32 v184,v168,v184
v_mul_f32 v185,v169,v185
v_mul_f32 v186,v170,v186
v_mul_f32 v187,v171,v187
v_mul_f32 v188,v172,v188
v_mul_f32 v189,v173,v189
v_mul_f32 v190,v174,v190
v_mul_f32 v191,v175,v191
v_cvt_pk_bf16_f32 v192,v176,v177
v_cvt_pk_bf16_f32 v193,v178,v179
v_cvt_pk_bf16_f32 v194,v180,v181
v_cvt_pk_bf16_f32 v195,v182,v183
v_cvt_pk_bf16_f32 v196,v184,v185
v_cvt_pk_bf16_f32 v197,v186,v187
v_cvt_pk_bf16_f32 v198,v188,v189
v_cvt_pk_bf16_f32 v199,v190,v191
s_waitcnt lgkmcnt(0)
s_waitcnt vmcnt(2)
s_barrier
v_fmaak_f32 v176,v144,v144,0x409d30fc
v_fmaak_f32 v177,v145,v145,0x409d30fc
v_fmaak_f32 v178,v146,v146,0x409d30fc
v_fmaak_f32 v179,v147,v147,0x409d30fc
v_fmaak_f32 v180,v148,v148,0x409d30fc
v_fmaak_f32 v181,v149,v149,0x409d30fc
v_fmaak_f32 v182,v150,v150,0x409d30fc
v_fmaak_f32 v183,v151,v151,0x409d30fc
v_fmaak_f32 v184,v152,v152,0x409d30fc
v_fmaak_f32 v185,v153,v153,0x409d30fc
v_fmaak_f32 v186,v154,v154,0x409d30fc
v_fmaak_f32 v187,v155,v155,0x409d30fc
v_fmaak_f32 v188,v156,v156,0x409d30fc
v_fmaak_f32 v189,v157,v157,0x409d30fc
v_fmaak_f32 v190,v158,v158,0x409d30fc
v_fmaak_f32 v191,v159,v159,0x409d30fc
v_mul_f32 v176,v144,v176
v_mul_f32 v177,v145,v177
v_mul_f32 v178,v146,v178
v_mul_f32 v179,v147,v179
v_mul_f32 v180,v148,v180
v_mul_f32 v181,v149,v181
v_mul_f32 v182,v150,v182
v_mul_f32 v183,v151,v183
v_mul_f32 v184,v152,v184
v_mul_f32 v185,v153,v185
v_mul_f32 v186,v154,v186
v_mul_f32 v187,v155,v187
v_mul_f32 v188,v156,v188
v_mul_f32 v189,v157,v189
v_mul_f32 v190,v158,v190
v_mul_f32 v191,v159,v191
s_setprio 1
v_mfma_f32_32x32x16_bf16 v[96:111],v[200:203],v[192:195],v[96:111]
v_exp_f32 v176,v176
v_exp_f32 v177,v177
v_exp_f32 v178,v178
v_mfma_f32_32x32x16_bf16 v[96:111],v[204:207],v[196:199],v[96:111]
v_exp_f32 v179,v179
v_exp_f32 v180,v180
v_exp_f32 v181,v181
v_mfma_f32_32x32x16_bf16 v[112:127],v[208:211],v[192:195],v[112:127]
v_exp_f32 v182,v182
v_exp_f32 v183,v183
v_exp_f32 v184,v184
v_mfma_f32_32x32x16_bf16 v[112:127],v[212:215],v[196:199],v[112:127]
v_exp_f32 v185,v185
v_exp_f32 v186,v186
v_exp_f32 v187,v187
v_mfma_f32_32x32x16_bf16 v[128:143],v[216:219],v[192:195],v[128:143]
v_exp_f32 v188,v188
v_exp_f32 v189,v189
v_mfma_f32_32x32x16_bf16 v[128:143],v[220:223],v[196:199],v[128:143]
v_exp_f32 v190,v190
v_exp_f32 v191,v191
ds_read_b64_tr_b16 v[200:201],v16 offset:12288
ds_read_b64_tr_b16 v[202:203],v16 offset:12800
ds_read_b64_tr_b16 v[204:205],v16 offset:13312
ds_read_b64_tr_b16 v[206:207],v16 offset:13824
ds_read_b64_tr_b16 v[208:209],v16 offset:14336
ds_read_b64_tr_b16 v[210:211],v16 offset:14848
ds_read_b64_tr_b16 v[212:213],v16 offset:15360
ds_read_b64_tr_b16 v[214:215],v16 offset:15872
ds_read_b64_tr_b16 v[216:217],v16 offset:16384
ds_read_b64_tr_b16 v[218:219],v16 offset:16896
ds_read_b64_tr_b16 v[220:221],v16 offset:17408
ds_read_b64_tr_b16 v[222:223],v16 offset:17920
ds_read_b128 v[160:163],v18 offset:640
ds_read_b128 v[164:167],v18 offset:672
ds_read_b128 v[168:171],v18 offset:704
ds_read_b128 v[172:175],v18 offset:736
s_mov_b64 exec,s[60:61]
s_add_u32 m0,s58,155136
s_mov_b32 s64,30720
buffer_load_dwordx4 v28,s[48:51],s64 offen lds
s_add_u32 m0,s58,155904
s_mov_b32 s65,31488
buffer_load_dwordx4 v28,s[48:51],s65 offen lds
s_mov_b64 exec,-1
s_setprio 0
v_add_f32 v176,1.0,v176
v_add_f32 v177,1.0,v177
v_add_f32 v178,1.0,v178
v_add_f32 v179,1.0,v179
v_add_f32 v180,1.0,v180
v_add_f32 v181,1.0,v181
v_add_f32 v182,1.0,v182
v_add_f32 v183,1.0,v183
v_add_f32 v184,1.0,v184
v_add_f32 v185,1.0,v185
v_add_f32 v186,1.0,v186
v_add_f32 v187,1.0,v187
v_add_f32 v188,1.0,v188
v_add_f32 v189,1.0,v189
v_add_f32 v190,1.0,v190
v_add_f32 v191,1.0,v191
s_setprio 1
s_waitcnt lgkmcnt(0)
v_mfma_f32_32x32x16_bf16 v[160:175],v[200:203],v[72:75],v[160:175]
v_rcp_f32 v176,v176
v_rcp_f32 v177,v177
v_rcp_f32 v178,v178
v_mfma_f32_32x32x16_bf16 v[160:175],v[204:207],v[76:79],v[160:175]
v_rcp_f32 v179,v179
v_rcp_f32 v180,v180
v_rcp_f32 v181,v181
v_mfma_f32_32x32x16_bf16 v[160:175],v[208:211],v[80:83],v[160:175]
v_rcp_f32 v182,v182
v_rcp_f32 v183,v183
v_rcp_f32 v184,v184
v_mfma_f32_32x32x16_bf16 v[160:175],v[212:215],v[84:87],v[160:175]
v_rcp_f32 v185,v185
v_rcp_f32 v186,v186
v_rcp_f32 v187,v187
v_mfma_f32_32x32x16_bf16 v[160:175],v[216:219],v[88:91],v[160:175]
v_rcp_f32 v188,v188
v_rcp_f32 v189,v189
v_mfma_f32_32x32x16_bf16 v[160:175],v[220:223],v[92:95],v[160:175]
v_rcp_f32 v190,v190
v_rcp_f32 v191,v191
ds_read_b64_tr_b16 v[200:201],v17 offset:6144
ds_read_b64_tr_b16 v[202:203],v17 offset:7680
ds_read_b64_tr_b16 v[204:205],v17 offset:9216
ds_read_b64_tr_b16 v[206:207],v17 offset:10752
ds_read_b64_tr_b16 v[208:209],v17 offset:6208
ds_read_b64_tr_b16 v[210:211],v17 offset:7744
ds_read_b64_tr_b16 v[212:213],v17 offset:9280
ds_read_b64_tr_b16 v[214:215],v17 offset:10816
ds_read_b64_tr_b16 v[216:217],v17 offset:6272
ds_read_b64_tr_b16 v[218:219],v17 offset:7808
ds_read_b64_tr_b16 v[220:221],v17 offset:9344
ds_read_b64_tr_b16 v[222:223],v17 offset:10880
s_mov_b64 exec,s[60:61]
s_add_u32 m0,s58,130560
s_movk_i32 s64,448
buffer_load_dwordx4 v27,s[44:47],s64 offen lds
s_add_u32 m0,s58,131328
s_movk_i32 s65,9664
buffer_load_dwordx4 v27,s[44:47],s65 offen lds
s_mov_b64 exec,-1
s_setprio 0
v_mul_f32 v176,v144,v176
v_mul_f32 v177,v145,v177
v_mul_f32 v178,v146,v178
v_mul_f32 v179,v147,v179
v_mul_f32 v180,v148,v180
v_mul_f32 v181,v149,v181
v_mul_f32 v182,v150,v182
v_mul_f32 v183,v151,v183
v_mul_f32 v184,v152,v184
v_mul_f32 v185,v153,v185
v_mul_f32 v186,v154,v186
v_mul_f32 v187,v155,v187
v_mul_f32 v188,v156,v188
v_mul_f32 v189,v157,v189
v_mul_f32 v190,v158,v190
v_mul_f32 v191,v159,v191
v_cvt_pk_bf16_f32 v192,v176,v177
v_cvt_pk_bf16_f32 v193,v178,v179
v_cvt_pk_bf16_f32 v194,v180,v181
v_cvt_pk_bf16_f32 v195,v182,v183
v_cvt_pk_bf16_f32 v196,v184,v185
v_cvt_pk_bf16_f32 v197,v186,v187
v_cvt_pk_bf16_f32 v198,v188,v189
v_cvt_pk_bf16_f32 v199,v190,v191
s_waitcnt lgkmcnt(0)
s_waitcnt vmcnt(2)
s_barrier
v_fmaak_f32 v176,v160,v160,0x409d30fc
v_fmaak_f32 v177,v161,v161,0x409d30fc
v_fmaak_f32 v178,v162,v162,0x409d30fc
v_fmaak_f32 v179,v163,v163,0x409d30fc
v_fmaak_f32 v180,v164,v164,0x409d30fc
v_fmaak_f32 v181,v165,v165,0x409d30fc
v_fmaak_f32 v182,v166,v166,0x409d30fc
v_fmaak_f32 v183,v167,v167,0x409d30fc
v_fmaak_f32 v184,v168,v168,0x409d30fc
v_fmaak_f32 v185,v169,v169,0x409d30fc
v_fmaak_f32 v186,v170,v170,0x409d30fc
v_fmaak_f32 v187,v171,v171,0x409d30fc
v_fmaak_f32 v188,v172,v172,0x409d30fc
v_fmaak_f32 v189,v173,v173,0x409d30fc
v_fmaak_f32 v190,v174,v174,0x409d30fc
v_fmaak_f32 v191,v175,v175,0x409d30fc
v_mul_f32 v176,v160,v176
v_mul_f32 v177,v161,v177
v_mul_f32 v178,v162,v178
v_mul_f32 v179,v163,v179
v_mul_f32 v180,v164,v180
v_mul_f32 v181,v165,v181
v_mul_f32 v182,v166,v182
v_mul_f32 v183,v167,v183
v_mul_f32 v184,v168,v184
v_mul_f32 v185,v169,v185
v_mul_f32 v186,v170,v186
v_mul_f32 v187,v171,v187
v_mul_f32 v188,v172,v188
v_mul_f32 v189,v173,v189
v_mul_f32 v190,v174,v190
v_mul_f32 v191,v175,v191
s_setprio 1
v_mfma_f32_32x32x16_bf16 v[96:111],v[200:203],v[192:195],v[96:111]
v_exp_f32 v176,v176
v_exp_f32 v177,v177
v_exp_f32 v178,v178
v_mfma_f32_32x32x16_bf16 v[96:111],v[204:207],v[196:199],v[96:111]
v_exp_f32 v179,v179
v_exp_f32 v180,v180
v_exp_f32 v181,v181
v_mfma_f32_32x32x16_bf16 v[112:127],v[208:211],v[192:195],v[112:127]
v_exp_f32 v182,v182
v_exp_f32 v183,v183
v_exp_f32 v184,v184
v_mfma_f32_32x32x16_bf16 v[112:127],v[212:215],v[196:199],v[112:127]
v_exp_f32 v185,v185
v_exp_f32 v186,v186
v_exp_f32 v187,v187
v_mfma_f32_32x32x16_bf16 v[128:143],v[216:219],v[192:195],v[128:143]
v_exp_f32 v188,v188
v_exp_f32 v189,v189
v_mfma_f32_32x32x16_bf16 v[128:143],v[220:223],v[196:199],v[128:143]
v_exp_f32 v190,v190
v_exp_f32 v191,v191
ds_read_b64_tr_b16 v[200:201],v16 offset:0
ds_read_b64_tr_b16 v[202:203],v16 offset:512
ds_read_b64_tr_b16 v[204:205],v16 offset:1024
ds_read_b64_tr_b16 v[206:207],v16 offset:1536
ds_read_b64_tr_b16 v[208:209],v16 offset:2048
ds_read_b64_tr_b16 v[210:211],v16 offset:2560
ds_read_b64_tr_b16 v[212:213],v16 offset:3072
ds_read_b64_tr_b16 v[214:215],v16 offset:3584
ds_read_b64_tr_b16 v[216:217],v16 offset:4096
ds_read_b64_tr_b16 v[218:219],v16 offset:4608
ds_read_b64_tr_b16 v[220:221],v16 offset:5120
ds_read_b64_tr_b16 v[222:223],v16 offset:5632
ds_read_b128 v[144:147],v18 offset:768
ds_read_b128 v[148:151],v18 offset:800
ds_read_b128 v[152:155],v18 offset:832
ds_read_b128 v[156:159],v18 offset:864
s_mov_b64 exec,s[60:61]
s_add_u32 m0,s58,142848
s_mov_b32 s64,36864
buffer_load_dwordx4 v28,s[48:51],s64 offen lds
s_add_u32 m0,s58,143616
s_mov_b32 s65,37632
buffer_load_dwordx4 v28,s[48:51],s65 offen lds
s_mov_b64 exec,-1
s_setprio 0
v_add_f32 v176,1.0,v176
v_add_f32 v177,1.0,v177
v_add_f32 v178,1.0,v178
v_add_f32 v179,1.0,v179
v_add_f32 v180,1.0,v180
v_add_f32 v181,1.0,v181
v_add_f32 v182,1.0,v182
v_add_f32 v183,1.0,v183
v_add_f32 v184,1.0,v184
v_add_f32 v185,1.0,v185
v_add_f32 v186,1.0,v186
v_add_f32 v187,1.0,v187
v_add_f32 v188,1.0,v188
v_add_f32 v189,1.0,v189
v_add_f32 v190,1.0,v190
v_add_f32 v191,1.0,v191
s_setprio 1
s_waitcnt lgkmcnt(0)
v_mfma_f32_32x32x16_bf16 v[144:159],v[200:203],v[72:75],v[144:159]
v_rcp_f32 v176,v176
v_rcp_f32 v177,v177
v_rcp_f32 v178,v178
v_mfma_f32_32x32x16_bf16 v[144:159],v[204:207],v[76:79],v[144:159]
v_rcp_f32 v179,v179
v_rcp_f32 v180,v180
v_rcp_f32 v181,v181
v_mfma_f32_32x32x16_bf16 v[144:159],v[208:211],v[80:83],v[144:159]
v_rcp_f32 v182,v182
v_rcp_f32 v183,v183
v_rcp_f32 v184,v184
v_mfma_f32_32x32x16_bf16 v[144:159],v[212:215],v[84:87],v[144:159]
v_rcp_f32 v185,v185
v_rcp_f32 v186,v186
v_rcp_f32 v187,v187
v_mfma_f32_32x32x16_bf16 v[144:159],v[216:219],v[88:91],v[144:159]
v_rcp_f32 v188,v188
v_rcp_f32 v189,v189
v_mfma_f32_32x32x16_bf16 v[144:159],v[220:223],v[92:95],v[144:159]
v_rcp_f32 v190,v190
v_rcp_f32 v191,v191
ds_read_b64_tr_b16 v[200:201],v17 offset:12288
ds_read_b64_tr_b16 v[202:203],v17 offset:13824
ds_read_b64_tr_b16 v[204:205],v17 offset:15360
ds_read_b64_tr_b16 v[206:207],v17 offset:16896
ds_read_b64_tr_b16 v[208:209],v17 offset:12352
ds_read_b64_tr_b16 v[210:211],v17 offset:13888
ds_read_b64_tr_b16 v[212:213],v17 offset:15424
ds_read_b64_tr_b16 v[214:215],v17 offset:16960
ds_read_b64_tr_b16 v[216:217],v17 offset:12416
ds_read_b64_tr_b16 v[218:219],v17 offset:13952
ds_read_b64_tr_b16 v[220:221],v17 offset:15488
ds_read_b64_tr_b16 v[222:223],v17 offset:17024
s_mov_b64 exec,s[60:61]
s_add_u32 m0,s58,136704
s_movk_i32 s64,512
buffer_load_dwordx4 v27,s[44:47],s64 offen lds
s_add_u32 m0,s58,137472
s_movk_i32 s65,9728
buffer_load_dwordx4 v27,s[44:47],s65 offen lds
s_mov_b64 exec,-1
s_setprio 0
v_mul_f32 v176,v160,v176
v_mul_f32 v177,v161,v177
v_mul_f32 v178,v162,v178
v_mul_f32 v179,v163,v179
v_mul_f32 v180,v164,v180
v_mul_f32 v181,v165,v181
v_mul_f32 v182,v166,v182
v_mul_f32 v183,v167,v183
v_mul_f32 v184,v168,v184
v_mul_f32 v185,v169,v185
v_mul_f32 v186,v170,v186
v_mul_f32 v187,v171,v187
v_mul_f32 v188,v172,v188
v_mul_f32 v189,v173,v189
v_mul_f32 v190,v174,v190
v_mul_f32 v191,v175,v191
v_cvt_pk_bf16_f32 v192,v176,v177
v_cvt_pk_bf16_f32 v193,v178,v179
v_cvt_pk_bf16_f32 v194,v180,v181
v_cvt_pk_bf16_f32 v195,v182,v183
v_cvt_pk_bf16_f32 v196,v184,v185
v_cvt_pk_bf16_f32 v197,v186,v187
v_cvt_pk_bf16_f32 v198,v188,v189
v_cvt_pk_bf16_f32 v199,v190,v191
s_waitcnt lgkmcnt(0)
s_waitcnt vmcnt(2)
s_barrier
v_fmaak_f32 v176,v144,v144,0x409d30fc
v_fmaak_f32 v177,v145,v145,0x409d30fc
v_fmaak_f32 v178,v146,v146,0x409d30fc
v_fmaak_f32 v179,v147,v147,0x409d30fc
v_fmaak_f32 v180,v148,v148,0x409d30fc
v_fmaak_f32 v181,v149,v149,0x409d30fc
v_fmaak_f32 v182,v150,v150,0x409d30fc
v_fmaak_f32 v183,v151,v151,0x409d30fc
v_fmaak_f32 v184,v152,v152,0x409d30fc
v_fmaak_f32 v185,v153,v153,0x409d30fc
v_fmaak_f32 v186,v154,v154,0x409d30fc
v_fmaak_f32 v187,v155,v155,0x409d30fc
v_fmaak_f32 v188,v156,v156,0x409d30fc
v_fmaak_f32 v189,v157,v157,0x409d30fc
v_fmaak_f32 v190,v158,v158,0x409d30fc
v_fmaak_f32 v191,v159,v159,0x409d30fc
v_mul_f32 v176,v144,v176
v_mul_f32 v177,v145,v177
v_mul_f32 v178,v146,v178
v_mul_f32 v179,v147,v179
v_mul_f32 v180,v148,v180
v_mul_f32 v181,v149,v181
v_mul_f32 v182,v150,v182
v_mul_f32 v183,v151,v183
v_mul_f32 v184,v152,v184
v_mul_f32 v185,v153,v185
v_mul_f32 v186,v154,v186
v_mul_f32 v187,v155,v187
v_mul_f32 v188,v156,v188
v_mul_f32 v189,v157,v189
v_mul_f32 v190,v158,v190
v_mul_f32 v191,v159,v191
s_setprio 1
v_mfma_f32_32x32x16_bf16 v[96:111],v[200:203],v[192:195],v[96:111]
v_exp_f32 v176,v176
v_exp_f32 v177,v177
v_exp_f32 v178,v178
v_mfma_f32_32x32x16_bf16 v[96:111],v[204:207],v[196:199],v[96:111]
v_exp_f32 v179,v179
v_exp_f32 v180,v180
v_exp_f32 v181,v181
v_mfma_f32_32x32x16_bf16 v[112:127],v[208:211],v[192:195],v[112:127]
v_exp_f32 v182,v182
v_exp_f32 v183,v183
v_exp_f32 v184,v184
v_mfma_f32_32x32x16_bf16 v[112:127],v[212:215],v[196:199],v[112:127]
v_exp_f32 v185,v185
v_exp_f32 v186,v186
v_exp_f32 v187,v187
v_mfma_f32_32x32x16_bf16 v[128:143],v[216:219],v[192:195],v[128:143]
v_exp_f32 v188,v188
v_exp_f32 v189,v189
v_mfma_f32_32x32x16_bf16 v[128:143],v[220:223],v[196:199],v[128:143]
v_exp_f32 v190,v190
v_exp_f32 v191,v191
ds_read_b64_tr_b16 v[200:201],v16 offset:6144
ds_read_b64_tr_b16 v[202:203],v16 offset:6656
ds_read_b64_tr_b16 v[204:205],v16 offset:7168
ds_read_b64_tr_b16 v[206:207],v16 offset:7680
ds_read_b64_tr_b16 v[208:209],v16 offset:8192
ds_read_b64_tr_b16 v[210:211],v16 offset:8704
ds_read_b64_tr_b16 v[212:213],v16 offset:9216
ds_read_b64_tr_b16 v[214:215],v16 offset:9728
ds_read_b64_tr_b16 v[216:217],v16 offset:10240
ds_read_b64_tr_b16 v[218:219],v16 offset:10752
ds_read_b64_tr_b16 v[220:221],v16 offset:11264
ds_read_b64_tr_b16 v[222:223],v16 offset:11776
ds_read_b128 v[160:163],v18 offset:896
ds_read_b128 v[164:167],v18 offset:928
ds_read_b128 v[168:171],v18 offset:960
ds_read_b128 v[172:175],v18 offset:992
s_mov_b64 exec,s[60:61]
s_add_u32 m0,s58,148992
s_mov_b32 s64,43008
buffer_load_dwordx4 v28,s[48:51],s64 offen lds
s_add_u32 m0,s58,149760
s_mov_b32 s65,43776
buffer_load_dwordx4 v28,s[48:51],s65 offen lds
s_mov_b64 exec,-1
s_setprio 0
v_add_f32 v176,1.0,v176
v_add_f32 v177,1.0,v177
v_add_f32 v178,1.0,v178
v_add_f32 v179,1.0,v179
v_add_f32 v180,1.0,v180
v_add_f32 v181,1.0,v181
v_add_f32 v182,1.0,v182
v_add_f32 v183,1.0,v183
v_add_f32 v184,1.0,v184
v_add_f32 v185,1.0,v185
v_add_f32 v186,1.0,v186
v_add_f32 v187,1.0,v187
v_add_f32 v188,1.0,v188
v_add_f32 v189,1.0,v189
v_add_f32 v190,1.0,v190
v_add_f32 v191,1.0,v191
s_setprio 1
s_waitcnt lgkmcnt(0)
v_mfma_f32_32x32x16_bf16 v[160:175],v[200:203],v[72:75],v[160:175]
v_rcp_f32 v176,v176
v_rcp_f32 v177,v177
v_rcp_f32 v178,v178
v_mfma_f32_32x32x16_bf16 v[160:175],v[204:207],v[76:79],v[160:175]
v_rcp_f32 v179,v179
v_rcp_f32 v180,v180
v_rcp_f32 v181,v181
v_mfma_f32_32x32x16_bf16 v[160:175],v[208:211],v[80:83],v[160:175]
v_rcp_f32 v182,v182
v_rcp_f32 v183,v183
v_rcp_f32 v184,v184
v_mfma_f32_32x32x16_bf16 v[160:175],v[212:215],v[84:87],v[160:175]
v_rcp_f32 v185,v185
v_rcp_f32 v186,v186
v_rcp_f32 v187,v187
v_mfma_f32_32x32x16_bf16 v[160:175],v[216:219],v[88:91],v[160:175]
v_rcp_f32 v188,v188
v_rcp_f32 v189,v189
v_mfma_f32_32x32x16_bf16 v[160:175],v[220:223],v[92:95],v[160:175]
v_rcp_f32 v190,v190
v_rcp_f32 v191,v191
ds_read_b64_tr_b16 v[200:201],v17 offset:0
ds_read_b64_tr_b16 v[202:203],v17 offset:1536
ds_read_b64_tr_b16 v[204:205],v17 offset:3072
ds_read_b64_tr_b16 v[206:207],v17 offset:4608
ds_read_b64_tr_b16 v[208:209],v17 offset:64
ds_read_b64_tr_b16 v[210:211],v17 offset:1600
ds_read_b64_tr_b16 v[212:213],v17 offset:3136
ds_read_b64_tr_b16 v[214:215],v17 offset:4672
ds_read_b64_tr_b16 v[216:217],v17 offset:128
ds_read_b64_tr_b16 v[218:219],v17 offset:1664
ds_read_b64_tr_b16 v[220:221],v17 offset:3200
ds_read_b64_tr_b16 v[222:223],v17 offset:4736
s_mov_b64 exec,s[60:61]
s_add_u32 m0,s58,124416
s_movk_i32 s64,576
buffer_load_dwordx4 v27,s[44:47],s64 offen lds
s_add_u32 m0,s58,125184
s_movk_i32 s65,9792
buffer_load_dwordx4 v27,s[44:47],s65 offen lds
s_mov_b64 exec,-1
s_setprio 0
v_mul_f32 v176,v144,v176
v_mul_f32 v177,v145,v177
v_mul_f32 v178,v146,v178
v_mul_f32 v179,v147,v179
v_mul_f32 v180,v148,v180
v_mul_f32 v181,v149,v181
v_mul_f32 v182,v150,v182
v_mul_f32 v183,v151,v183
v_mul_f32 v184,v152,v184
v_mul_f32 v185,v153,v185
v_mul_f32 v186,v154,v186
v_mul_f32 v187,v155,v187
v_mul_f32 v188,v156,v188
v_mul_f32 v189,v157,v189
v_mul_f32 v190,v158,v190
v_mul_f32 v191,v159,v191
v_cvt_pk_bf16_f32 v192,v176,v177
v_cvt_pk_bf16_f32 v193,v178,v179
v_cvt_pk_bf16_f32 v194,v180,v181
v_cvt_pk_bf16_f32 v195,v182,v183
v_cvt_pk_bf16_f32 v196,v184,v185
v_cvt_pk_bf16_f32 v197,v186,v187
v_cvt_pk_bf16_f32 v198,v188,v189
v_cvt_pk_bf16_f32 v199,v190,v191
s_waitcnt lgkmcnt(0)
s_waitcnt vmcnt(2)
s_barrier
v_fmaak_f32 v176,v160,v160,0x409d30fc
v_fmaak_f32 v177,v161,v161,0x409d30fc
v_fmaak_f32 v178,v162,v162,0x409d30fc
v_fmaak_f32 v179,v163,v163,0x409d30fc
v_fmaak_f32 v180,v164,v164,0x409d30fc
v_fmaak_f32 v181,v165,v165,0x409d30fc
v_fmaak_f32 v182,v166,v166,0x409d30fc
v_fmaak_f32 v183,v167,v167,0x409d30fc
v_fmaak_f32 v184,v168,v168,0x409d30fc
v_fmaak_f32 v185,v169,v169,0x409d30fc
v_fmaak_f32 v186,v170,v170,0x409d30fc
v_fmaak_f32 v187,v171,v171,0x409d30fc
v_fmaak_f32 v188,v172,v172,0x409d30fc
v_fmaak_f32 v189,v173,v173,0x409d30fc
v_fmaak_f32 v190,v174,v174,0x409d30fc
v_fmaak_f32 v191,v175,v175,0x409d30fc
v_mul_f32 v176,v160,v176
v_mul_f32 v177,v161,v177
v_mul_f32 v178,v162,v178
v_mul_f32 v179,v163,v179
v_mul_f32 v180,v164,v180
v_mul_f32 v181,v165,v181
v_mul_f32 v182,v166,v182
v_mul_f32 v183,v167,v183
v_mul_f32 v184,v168,v184
v_mul_f32 v185,v169,v185
v_mul_f32 v186,v170,v186
v_mul_f32 v187,v171,v187
v_mul_f32 v188,v172,v188
v_mul_f32 v189,v173,v189
v_mul_f32 v190,v174,v190
v_mul_f32 v191,v175,v191
s_setprio 1
v_mfma_f32_32x32x16_bf16 v[96:111],v[200:203],v[192:195],v[96:111]
v_exp_f32 v176,v176
v_exp_f32 v177,v177
v_exp_f32 v178,v178
v_mfma_f32_32x32x16_bf16 v[96:111],v[204:207],v[196:199],v[96:111]
v_exp_f32 v179,v179
v_exp_f32 v180,v180
v_exp_f32 v181,v181
v_mfma_f32_32x32x16_bf16 v[112:127],v[208:211],v[192:195],v[112:127]
v_exp_f32 v182,v182
v_exp_f32 v183,v183
v_exp_f32 v184,v184
v_mfma_f32_32x32x16_bf16 v[112:127],v[212:215],v[196:199],v[112:127]
v_exp_f32 v185,v185
v_exp_f32 v186,v186
v_exp_f32 v187,v187
v_mfma_f32_32x32x16_bf16 v[128:143],v[216:219],v[192:195],v[128:143]
v_exp_f32 v188,v188
v_exp_f32 v189,v189
v_mfma_f32_32x32x16_bf16 v[128:143],v[220:223],v[196:199],v[128:143]
v_exp_f32 v190,v190
v_exp_f32 v191,v191
ds_read_b64_tr_b16 v[200:201],v16 offset:12288
ds_read_b64_tr_b16 v[202:203],v16 offset:12800
ds_read_b64_tr_b16 v[204:205],v16 offset:13312
ds_read_b64_tr_b16 v[206:207],v16 offset:13824
ds_read_b64_tr_b16 v[208:209],v16 offset:14336
ds_read_b64_tr_b16 v[210:211],v16 offset:14848
ds_read_b64_tr_b16 v[212:213],v16 offset:15360
ds_read_b64_tr_b16 v[214:215],v16 offset:15872
ds_read_b64_tr_b16 v[216:217],v16 offset:16384
ds_read_b64_tr_b16 v[218:219],v16 offset:16896
ds_read_b64_tr_b16 v[220:221],v16 offset:17408
ds_read_b64_tr_b16 v[222:223],v16 offset:17920
ds_read_b128 v[144:147],v18 offset:1024
ds_read_b128 v[148:151],v18 offset:1056
ds_read_b128 v[152:155],v18 offset:1088
ds_read_b128 v[156:159],v18 offset:1120
s_mov_b64 exec,s[60:61]
s_add_u32 m0,s58,155136
s_mov_b32 s64,49152
buffer_load_dwordx4 v28,s[48:51],s64 offen lds
s_add_u32 m0,s58,155904
s_mov_b32 s65,49920
buffer_load_dwordx4 v28,s[48:51],s65 offen lds
s_mov_b64 exec,-1
s_setprio 0
v_add_f32 v176,1.0,v176
v_add_f32 v177,1.0,v177
v_add_f32 v178,1.0,v178
v_add_f32 v179,1.0,v179
v_add_f32 v180,1.0,v180
v_add_f32 v181,1.0,v181
v_add_f32 v182,1.0,v182
v_add_f32 v183,1.0,v183
v_add_f32 v184,1.0,v184
v_add_f32 v185,1.0,v185
v_add_f32 v186,1.0,v186
v_add_f32 v187,1.0,v187
v_add_f32 v188,1.0,v188
v_add_f32 v189,1.0,v189
v_add_f32 v190,1.0,v190
v_add_f32 v191,1.0,v191
s_setprio 1
s_waitcnt lgkmcnt(0)
v_mfma_f32_32x32x16_bf16 v[144:159],v[200:203],v[72:75],v[144:159]
v_rcp_f32 v176,v176
v_rcp_f32 v177,v177
v_rcp_f32 v178,v178
v_mfma_f32_32x32x16_bf16 v[144:159],v[204:207],v[76:79],v[144:159]
v_rcp_f32 v179,v179
v_rcp_f32 v180,v180
v_rcp_f32 v181,v181
v_mfma_f32_32x32x16_bf16 v[144:159],v[208:211],v[80:83],v[144:159]
v_rcp_f32 v182,v182
v_rcp_f32 v183,v183
v_rcp_f32 v184,v184
v_mfma_f32_32x32x16_bf16 v[144:159],v[212:215],v[84:87],v[144:159]
v_rcp_f32 v185,v185
v_rcp_f32 v186,v186
v_rcp_f32 v187,v187
v_mfma_f32_32x32x16_bf16 v[144:159],v[216:219],v[88:91],v[144:159]
v_rcp_f32 v188,v188
v_rcp_f32 v189,v189
v_mfma_f32_32x32x16_bf16 v[144:159],v[220:223],v[92:95],v[144:159]
v_rcp_f32 v190,v190
v_rcp_f32 v191,v191
ds_read_b64_tr_b16 v[200:201],v17 offset:6144
ds_read_b64_tr_b16 v[202:203],v17 offset:7680
ds_read_b64_tr_b16 v[204:205],v17 offset:9216
ds_read_b64_tr_b16 v[206:207],v17 offset:10752
ds_read_b64_tr_b16 v[208:209],v17 offset:6208
ds_read_b64_tr_b16 v[210:211],v17 offset:7744
ds_read_b64_tr_b16 v[212:213],v17 offset:9280
ds_read_b64_tr_b16 v[214:215],v17 offset:10816
ds_read_b64_tr_b16 v[216:217],v17 offset:6272
ds_read_b64_tr_b16 v[218:219],v17 offset:7808
ds_read_b64_tr_b16 v[220:221],v17 offset:9344
ds_read_b64_tr_b16 v[222:223],v17 offset:10880
s_mov_b64 exec,s[60:61]
s_add_u32 m0,s58,130560
s_movk_i32 s64,640
buffer_load_dwordx4 v27,s[44:47],s64 offen lds
s_add_u32 m0,s58,131328
s_movk_i32 s65,9856
buffer_load_dwordx4 v27,s[44:47],s65 offen lds
s_mov_b64 exec,-1
s_setprio 0
v_mul_f32 v176,v160,v176
v_mul_f32 v177,v161,v177
v_mul_f32 v178,v162,v178
v_mul_f32 v179,v163,v179
v_mul_f32 v180,v164,v180
v_mul_f32 v181,v165,v181
v_mul_f32 v182,v166,v182
v_mul_f32 v183,v167,v183
v_mul_f32 v184,v168,v184
v_mul_f32 v185,v169,v185
v_mul_f32 v186,v170,v186
v_mul_f32 v187,v171,v187
v_mul_f32 v188,v172,v188
v_mul_f32 v189,v173,v189
v_mul_f32 v190,v174,v190
v_mul_f32 v191,v175,v191
v_cvt_pk_bf16_f32 v192,v176,v177
v_cvt_pk_bf16_f32 v193,v178,v179
v_cvt_pk_bf16_f32 v194,v180,v181
v_cvt_pk_bf16_f32 v195,v182,v183
v_cvt_pk_bf16_f32 v196,v184,v185
v_cvt_pk_bf16_f32 v197,v186,v187
v_cvt_pk_bf16_f32 v198,v188,v189
v_cvt_pk_bf16_f32 v199,v190,v191
s_waitcnt lgkmcnt(0)
s_waitcnt vmcnt(2)
s_barrier
v_fmaak_f32 v176,v144,v144,0x409d30fc
v_fmaak_f32 v177,v145,v145,0x409d30fc
v_fmaak_f32 v178,v146,v146,0x409d30fc
v_fmaak_f32 v179,v147,v147,0x409d30fc
v_fmaak_f32 v180,v148,v148,0x409d30fc
v_fmaak_f32 v181,v149,v149,0x409d30fc
v_fmaak_f32 v182,v150,v150,0x409d30fc
v_fmaak_f32 v183,v151,v151,0x409d30fc
v_fmaak_f32 v184,v152,v152,0x409d30fc
v_fmaak_f32 v185,v153,v153,0x409d30fc
v_fmaak_f32 v186,v154,v154,0x409d30fc
v_fmaak_f32 v187,v155,v155,0x409d30fc
v_fmaak_f32 v188,v156,v156,0x409d30fc
v_fmaak_f32 v189,v157,v157,0x409d30fc
v_fmaak_f32 v190,v158,v158,0x409d30fc
v_fmaak_f32 v191,v159,v159,0x409d30fc
v_mul_f32 v176,v144,v176
v_mul_f32 v177,v145,v177
v_mul_f32 v178,v146,v178
v_mul_f32 v179,v147,v179
v_mul_f32 v180,v148,v180
v_mul_f32 v181,v149,v181
v_mul_f32 v182,v150,v182
v_mul_f32 v183,v151,v183
v_mul_f32 v184,v152,v184
v_mul_f32 v185,v153,v185
v_mul_f32 v186,v154,v186
v_mul_f32 v187,v155,v187
v_mul_f32 v188,v156,v188
v_mul_f32 v189,v157,v189
v_mul_f32 v190,v158,v190
v_mul_f32 v191,v159,v191
s_setprio 1
v_mfma_f32_32x32x16_bf16 v[96:111],v[200:203],v[192:195],v[96:111]
v_exp_f32 v176,v176
v_exp_f32 v177,v177
v_exp_f32 v178,v178
v_mfma_f32_32x32x16_bf16 v[96:111],v[204:207],v[196:199],v[96:111]
v_exp_f32 v179,v179
v_exp_f32 v180,v180
v_exp_f32 v181,v181
v_mfma_f32_32x32x16_bf16 v[112:127],v[208:211],v[192:195],v[112:127]
v_exp_f32 v182,v182
v_exp_f32 v183,v183
v_exp_f32 v184,v184
v_mfma_f32_32x32x16_bf16 v[112:127],v[212:215],v[196:199],v[112:127]
v_exp_f32 v185,v185
v_exp_f32 v186,v186
v_exp_f32 v187,v187
v_mfma_f32_32x32x16_bf16 v[128:143],v[216:219],v[192:195],v[128:143]
v_exp_f32 v188,v188
v_exp_f32 v189,v189
v_mfma_f32_32x32x16_bf16 v[128:143],v[220:223],v[196:199],v[128:143]
v_exp_f32 v190,v190
v_exp_f32 v191,v191
ds_read_b64_tr_b16 v[200:201],v16 offset:0
ds_read_b64_tr_b16 v[202:203],v16 offset:512
ds_read_b64_tr_b16 v[204:205],v16 offset:1024
ds_read_b64_tr_b16 v[206:207],v16 offset:1536
ds_read_b64_tr_b16 v[208:209],v16 offset:2048
ds_read_b64_tr_b16 v[210:211],v16 offset:2560
ds_read_b64_tr_b16 v[212:213],v16 offset:3072
ds_read_b64_tr_b16 v[214:215],v16 offset:3584
ds_read_b64_tr_b16 v[216:217],v16 offset:4096
ds_read_b64_tr_b16 v[218:219],v16 offset:4608
ds_read_b64_tr_b16 v[220:221],v16 offset:5120
ds_read_b64_tr_b16 v[222:223],v16 offset:5632
ds_read_b128 v[160:163],v18 offset:1152
ds_read_b128 v[164:167],v18 offset:1184
ds_read_b128 v[168:171],v18 offset:1216
ds_read_b128 v[172:175],v18 offset:1248
s_mov_b64 exec,s[60:61]
s_add_u32 m0,s58,142848
s_mov_b32 s64,55296
buffer_load_dwordx4 v28,s[48:51],s64 offen lds
s_add_u32 m0,s58,143616
s_mov_b32 s65,56064
buffer_load_dwordx4 v28,s[48:51],s65 offen lds
s_mov_b64 exec,-1
s_setprio 0
v_add_f32 v176,1.0,v176
v_add_f32 v177,1.0,v177
v_add_f32 v178,1.0,v178
v_add_f32 v179,1.0,v179
v_add_f32 v180,1.0,v180
v_add_f32 v181,1.0,v181
v_add_f32 v182,1.0,v182
v_add_f32 v183,1.0,v183
v_add_f32 v184,1.0,v184
v_add_f32 v185,1.0,v185
v_add_f32 v186,1.0,v186
v_add_f32 v187,1.0,v187
v_add_f32 v188,1.0,v188
v_add_f32 v189,1.0,v189
v_add_f32 v190,1.0,v190
v_add_f32 v191,1.0,v191
s_setprio 1
s_waitcnt lgkmcnt(0)
v_mfma_f32_32x32x16_bf16 v[160:175],v[200:203],v[72:75],v[160:175]
v_rcp_f32 v176,v176
v_rcp_f32 v177,v177
v_rcp_f32 v178,v178
v_mfma_f32_32x32x16_bf16 v[160:175],v[204:207],v[76:79],v[160:175]
v_rcp_f32 v179,v179
v_rcp_f32 v180,v180
v_rcp_f32 v181,v181
v_mfma_f32_32x32x16_bf16 v[160:175],v[208:211],v[80:83],v[160:175]
v_rcp_f32 v182,v182
v_rcp_f32 v183,v183
v_rcp_f32 v184,v184
v_mfma_f32_32x32x16_bf16 v[160:175],v[212:215],v[84:87],v[160:175]
v_rcp_f32 v185,v185
v_rcp_f32 v186,v186
v_rcp_f32 v187,v187
v_mfma_f32_32x32x16_bf16 v[160:175],v[216:219],v[88:91],v[160:175]
v_rcp_f32 v188,v188
v_rcp_f32 v189,v189
v_mfma_f32_32x32x16_bf16 v[160:175],v[220:223],v[92:95],v[160:175]
v_rcp_f32 v190,v190
v_rcp_f32 v191,v191
ds_read_b64_tr_b16 v[200:201],v17 offset:12288
ds_read_b64_tr_b16 v[202:203],v17 offset:13824
ds_read_b64_tr_b16 v[204:205],v17 offset:15360
ds_read_b64_tr_b16 v[206:207],v17 offset:16896
ds_read_b64_tr_b16 v[208:209],v17 offset:12352
ds_read_b64_tr_b16 v[210:211],v17 offset:13888
ds_read_b64_tr_b16 v[212:213],v17 offset:15424
ds_read_b64_tr_b16 v[214:215],v17 offset:16960
ds_read_b64_tr_b16 v[216:217],v17 offset:12416
ds_read_b64_tr_b16 v[218:219],v17 offset:13952
ds_read_b64_tr_b16 v[220:221],v17 offset:15488
ds_read_b64_tr_b16 v[222:223],v17 offset:17024
s_mov_b64 exec,s[60:61]
s_add_u32 m0,s58,136704
s_movk_i32 s64,704
buffer_load_dwordx4 v27,s[44:47],s64 offen lds
s_add_u32 m0,s58,137472
s_movk_i32 s65,9920
buffer_load_dwordx4 v27,s[44:47],s65 offen lds
s_mov_b64 exec,-1
s_setprio 0
v_mul_f32 v176,v144,v176
v_mul_f32 v177,v145,v177
v_mul_f32 v178,v146,v178
v_mul_f32 v179,v147,v179
v_mul_f32 v180,v148,v180
v_mul_f32 v181,v149,v181
v_mul_f32 v182,v150,v182
v_mul_f32 v183,v151,v183
v_mul_f32 v184,v152,v184
v_mul_f32 v185,v153,v185
v_mul_f32 v186,v154,v186
v_mul_f32 v187,v155,v187
v_mul_f32 v188,v156,v188
v_mul_f32 v189,v157,v189
v_mul_f32 v190,v158,v190
v_mul_f32 v191,v159,v191
v_cvt_pk_bf16_f32 v192,v176,v177
v_cvt_pk_bf16_f32 v193,v178,v179
v_cvt_pk_bf16_f32 v194,v180,v181
v_cvt_pk_bf16_f32 v195,v182,v183
v_cvt_pk_bf16_f32 v196,v184,v185
v_cvt_pk_bf16_f32 v197,v186,v187
v_cvt_pk_bf16_f32 v198,v188,v189
v_cvt_pk_bf16_f32 v199,v190,v191
s_waitcnt lgkmcnt(0)
s_waitcnt vmcnt(2)
s_barrier
v_fmaak_f32 v176,v160,v160,0x409d30fc
v_fmaak_f32 v177,v161,v161,0x409d30fc
v_fmaak_f32 v178,v162,v162,0x409d30fc
v_fmaak_f32 v179,v163,v163,0x409d30fc
v_fmaak_f32 v180,v164,v164,0x409d30fc
v_fmaak_f32 v181,v165,v165,0x409d30fc
v_fmaak_f32 v182,v166,v166,0x409d30fc
v_fmaak_f32 v183,v167,v167,0x409d30fc
v_fmaak_f32 v184,v168,v168,0x409d30fc
v_fmaak_f32 v185,v169,v169,0x409d30fc
v_fmaak_f32 v186,v170,v170,0x409d30fc
v_fmaak_f32 v187,v171,v171,0x409d30fc
v_fmaak_f32 v188,v172,v172,0x409d30fc
v_fmaak_f32 v189,v173,v173,0x409d30fc
v_fmaak_f32 v190,v174,v174,0x409d30fc
v_fmaak_f32 v191,v175,v175,0x409d30fc
v_mul_f32 v176,v160,v176
v_mul_f32 v177,v161,v177
v_mul_f32 v178,v162,v178
v_mul_f32 v179,v163,v179
v_mul_f32 v180,v164,v180
v_mul_f32 v181,v165,v181
v_mul_f32 v182,v166,v182
v_mul_f32 v183,v167,v183
v_mul_f32 v184,v168,v184
v_mul_f32 v185,v169,v185
v_mul_f32 v186,v170,v186
v_mul_f32 v187,v171,v187
v_mul_f32 v188,v172,v188
v_mul_f32 v189,v173,v189
v_mul_f32 v190,v174,v190
v_mul_f32 v191,v175,v191
s_setprio 1
v_mfma_f32_32x32x16_bf16 v[96:111],v[200:203],v[192:195],v[96:111]
v_exp_f32 v176,v176
v_exp_f32 v177,v177
v_exp_f32 v178,v178
v_mfma_f32_32x32x16_bf16 v[96:111],v[204:207],v[196:199],v[96:111]
v_exp_f32 v179,v179
v_exp_f32 v180,v180
v_exp_f32 v181,v181
v_mfma_f32_32x32x16_bf16 v[112:127],v[208:211],v[192:195],v[112:127]
v_exp_f32 v182,v182
v_exp_f32 v183,v183
v_exp_f32 v184,v184
v_mfma_f32_32x32x16_bf16 v[112:127],v[212:215],v[196:199],v[112:127]
v_exp_f32 v185,v185
v_exp_f32 v186,v186
v_exp_f32 v187,v187
v_mfma_f32_32x32x16_bf16 v[128:143],v[216:219],v[192:195],v[128:143]
v_exp_f32 v188,v188
v_exp_f32 v189,v189
v_mfma_f32_32x32x16_bf16 v[128:143],v[220:223],v[196:199],v[128:143]
v_exp_f32 v190,v190
v_exp_f32 v191,v191
ds_read_b64_tr_b16 v[200:201],v16 offset:6144
ds_read_b64_tr_b16 v[202:203],v16 offset:6656
ds_read_b64_tr_b16 v[204:205],v16 offset:7168
ds_read_b64_tr_b16 v[206:207],v16 offset:7680
ds_read_b64_tr_b16 v[208:209],v16 offset:8192
ds_read_b64_tr_b16 v[210:211],v16 offset:8704
ds_read_b64_tr_b16 v[212:213],v16 offset:9216
ds_read_b64_tr_b16 v[214:215],v16 offset:9728
ds_read_b64_tr_b16 v[216:217],v16 offset:10240
ds_read_b64_tr_b16 v[218:219],v16 offset:10752
ds_read_b64_tr_b16 v[220:221],v16 offset:11264
ds_read_b64_tr_b16 v[222:223],v16 offset:11776
ds_read_b128 v[144:147],v18 offset:1280
ds_read_b128 v[148:151],v18 offset:1312
ds_read_b128 v[152:155],v18 offset:1344
ds_read_b128 v[156:159],v18 offset:1376
s_mov_b64 exec,s[60:61]
s_add_u32 m0,s58,148992
s_mov_b32 s64,61440
buffer_load_dwordx4 v28,s[48:51],s64 offen lds
s_add_u32 m0,s58,149760
s_mov_b32 s65,62208
buffer_load_dwordx4 v28,s[48:51],s65 offen lds
s_mov_b64 exec,-1
s_setprio 0
v_add_f32 v176,1.0,v176
v_add_f32 v177,1.0,v177
v_add_f32 v178,1.0,v178
v_add_f32 v179,1.0,v179
v_add_f32 v180,1.0,v180
v_add_f32 v181,1.0,v181
v_add_f32 v182,1.0,v182
v_add_f32 v183,1.0,v183
v_add_f32 v184,1.0,v184
v_add_f32 v185,1.0,v185
v_add_f32 v186,1.0,v186
v_add_f32 v187,1.0,v187
v_add_f32 v188,1.0,v188
v_add_f32 v189,1.0,v189
v_add_f32 v190,1.0,v190
v_add_f32 v191,1.0,v191
s_setprio 1
s_waitcnt lgkmcnt(0)
v_mfma_f32_32x32x16_bf16 v[144:159],v[200:203],v[72:75],v[144:159]
v_rcp_f32 v176,v176
v_rcp_f32 v177,v177
v_rcp_f32 v178,v178
v_mfma_f32_32x32x16_bf16 v[144:159],v[204:207],v[76:79],v[144:159]
v_rcp_f32 v179,v179
v_rcp_f32 v180,v180
v_rcp_f32 v181,v181
v_mfma_f32_32x32x16_bf16 v[144:159],v[208:211],v[80:83],v[144:159]
v_rcp_f32 v182,v182
v_rcp_f32 v183,v183
v_rcp_f32 v184,v184
v_mfma_f32_32x32x16_bf16 v[144:159],v[212:215],v[84:87],v[144:159]
v_rcp_f32 v185,v185
v_rcp_f32 v186,v186
v_rcp_f32 v187,v187
v_mfma_f32_32x32x16_bf16 v[144:159],v[216:219],v[88:91],v[144:159]
v_rcp_f32 v188,v188
v_rcp_f32 v189,v189
v_mfma_f32_32x32x16_bf16 v[144:159],v[220:223],v[92:95],v[144:159]
v_rcp_f32 v190,v190
v_rcp_f32 v191,v191
ds_read_b64_tr_b16 v[200:201],v17 offset:0
ds_read_b64_tr_b16 v[202:203],v17 offset:1536
ds_read_b64_tr_b16 v[204:205],v17 offset:3072
ds_read_b64_tr_b16 v[206:207],v17 offset:4608
ds_read_b64_tr_b16 v[208:209],v17 offset:64
ds_read_b64_tr_b16 v[210:211],v17 offset:1600
ds_read_b64_tr_b16 v[212:213],v17 offset:3136
ds_read_b64_tr_b16 v[214:215],v17 offset:4672
ds_read_b64_tr_b16 v[216:217],v17 offset:128
ds_read_b64_tr_b16 v[218:219],v17 offset:1664
ds_read_b64_tr_b16 v[220:221],v17 offset:3200
ds_read_b64_tr_b16 v[222:223],v17 offset:4736
s_mov_b64 exec,s[60:61]
s_add_u32 m0,s58,124416
s_movk_i32 s64,0
buffer_load_dwordx4 v27,s[44:47],s64 offen lds
s_add_u32 m0,s58,125184
s_movk_i32 s65,9216
buffer_load_dwordx4 v27,s[44:47],s65 offen lds
s_mov_b64 exec,-1
s_setprio 0
v_mul_f32 v176,v160,v176
v_mul_f32 v177,v161,v177
v_mul_f32 v178,v162,v178
v_mul_f32 v179,v163,v179
v_mul_f32 v180,v164,v180
v_mul_f32 v181,v165,v181
v_mul_f32 v182,v166,v182
v_mul_f32 v183,v167,v183
v_mul_f32 v184,v168,v184
v_mul_f32 v185,v169,v185
v_mul_f32 v186,v170,v186
v_mul_f32 v187,v171,v187
v_mul_f32 v188,v172,v188
v_mul_f32 v189,v173,v189
v_mul_f32 v190,v174,v190
v_mul_f32 v191,v175,v191
v_cvt_pk_bf16_f32 v192,v176,v177
v_cvt_pk_bf16_f32 v193,v178,v179
v_cvt_pk_bf16_f32 v194,v180,v181
v_cvt_pk_bf16_f32 v195,v182,v183
v_cvt_pk_bf16_f32 v196,v184,v185
v_cvt_pk_bf16_f32 v197,v186,v187
v_cvt_pk_bf16_f32 v198,v188,v189
v_cvt_pk_bf16_f32 v199,v190,v191
s_waitcnt lgkmcnt(0)
s_waitcnt vmcnt(2)
s_barrier
v_fmaak_f32 v176,v144,v144,0x409d30fc
v_fmaak_f32 v177,v145,v145,0x409d30fc
v_fmaak_f32 v178,v146,v146,0x409d30fc
v_fmaak_f32 v179,v147,v147,0x409d30fc
v_fmaak_f32 v180,v148,v148,0x409d30fc
v_fmaak_f32 v181,v149,v149,0x409d30fc
v_fmaak_f32 v182,v150,v150,0x409d30fc
v_fmaak_f32 v183,v151,v151,0x409d30fc
v_fmaak_f32 v184,v152,v152,0x409d30fc
v_fmaak_f32 v185,v153,v153,0x409d30fc
v_fmaak_f32 v186,v154,v154,0x409d30fc
v_fmaak_f32 v187,v155,v155,0x409d30fc
v_fmaak_f32 v188,v156,v156,0x409d30fc
v_fmaak_f32 v189,v157,v157,0x409d30fc
v_fmaak_f32 v190,v158,v158,0x409d30fc
v_fmaak_f32 v191,v159,v159,0x409d30fc
v_mul_f32 v176,v144,v176
v_mul_f32 v177,v145,v177
v_mul_f32 v178,v146,v178
v_mul_f32 v179,v147,v179
v_mul_f32 v180,v148,v180
v_mul_f32 v181,v149,v181
v_mul_f32 v182,v150,v182
v_mul_f32 v183,v151,v183
v_mul_f32 v184,v152,v184
v_mul_f32 v185,v153,v185
v_mul_f32 v186,v154,v186
v_mul_f32 v187,v155,v187
v_mul_f32 v188,v156,v188
v_mul_f32 v189,v157,v189
v_mul_f32 v190,v158,v190
v_mul_f32 v191,v159,v191
s_setprio 1
v_mfma_f32_32x32x16_bf16 v[96:111],v[200:203],v[192:195],v[96:111]
v_exp_f32 v176,v176
v_exp_f32 v177,v177
v_exp_f32 v178,v178
v_mfma_f32_32x32x16_bf16 v[96:111],v[204:207],v[196:199],v[96:111]
v_exp_f32 v179,v179
v_exp_f32 v180,v180
v_exp_f32 v181,v181
v_mfma_f32_32x32x16_bf16 v[112:127],v[208:211],v[192:195],v[112:127]
v_exp_f32 v182,v182
v_exp_f32 v183,v183
v_exp_f32 v184,v184
v_mfma_f32_32x32x16_bf16 v[112:127],v[212:215],v[196:199],v[112:127]
v_exp_f32 v185,v185
v_exp_f32 v186,v186
v_exp_f32 v187,v187
v_mfma_f32_32x32x16_bf16 v[128:143],v[216:219],v[192:195],v[128:143]
v_exp_f32 v188,v188
v_exp_f32 v189,v189
v_mfma_f32_32x32x16_bf16 v[128:143],v[220:223],v[196:199],v[128:143]
v_exp_f32 v190,v190
v_exp_f32 v191,v191
ds_read_b64_tr_b16 v[200:201],v16 offset:12288
ds_read_b64_tr_b16 v[202:203],v16 offset:12800
ds_read_b64_tr_b16 v[204:205],v16 offset:13312
ds_read_b64_tr_b16 v[206:207],v16 offset:13824
ds_read_b64_tr_b16 v[208:209],v16 offset:14336
ds_read_b64_tr_b16 v[210:211],v16 offset:14848
ds_read_b64_tr_b16 v[212:213],v16 offset:15360
ds_read_b64_tr_b16 v[214:215],v16 offset:15872
ds_read_b64_tr_b16 v[216:217],v16 offset:16384
ds_read_b64_tr_b16 v[218:219],v16 offset:16896
ds_read_b64_tr_b16 v[220:221],v16 offset:17408
ds_read_b64_tr_b16 v[222:223],v16 offset:17920
ds_read_b128 v[160:163],v18 offset:1408
ds_read_b128 v[164:167],v18 offset:1440
ds_read_b128 v[168:171],v18 offset:1472
ds_read_b128 v[172:175],v18 offset:1504
s_mov_b64 exec,s[60:61]
s_add_u32 m0,s58,155136
s_mov_b32 s64,67584
buffer_load_dwordx4 v28,s[48:51],s64 offen lds
s_add_u32 m0,s58,155904
s_mov_b32 s65,68352
buffer_load_dwordx4 v28,s[48:51],s65 offen lds
s_mov_b64 exec,-1
s_setprio 0
v_add_f32 v176,1.0,v176
v_add_f32 v177,1.0,v177
v_add_f32 v178,1.0,v178
v_add_f32 v179,1.0,v179
v_add_f32 v180,1.0,v180
v_add_f32 v181,1.0,v181
v_add_f32 v182,1.0,v182
v_add_f32 v183,1.0,v183
v_add_f32 v184,1.0,v184
v_add_f32 v185,1.0,v185
v_add_f32 v186,1.0,v186
v_add_f32 v187,1.0,v187
v_add_f32 v188,1.0,v188
v_add_f32 v189,1.0,v189
v_add_f32 v190,1.0,v190
v_add_f32 v191,1.0,v191
s_setprio 1
s_waitcnt lgkmcnt(0)
v_mfma_f32_32x32x16_bf16 v[160:175],v[200:203],v[72:75],v[160:175]
v_rcp_f32 v176,v176
v_rcp_f32 v177,v177
v_rcp_f32 v178,v178
v_mfma_f32_32x32x16_bf16 v[160:175],v[204:207],v[76:79],v[160:175]
v_rcp_f32 v179,v179
v_rcp_f32 v180,v180
v_rcp_f32 v181,v181
v_mfma_f32_32x32x16_bf16 v[160:175],v[208:211],v[80:83],v[160:175]
v_rcp_f32 v182,v182
v_rcp_f32 v183,v183
v_rcp_f32 v184,v184
v_mfma_f32_32x32x16_bf16 v[160:175],v[212:215],v[84:87],v[160:175]
v_rcp_f32 v185,v185
v_rcp_f32 v186,v186
v_rcp_f32 v187,v187
v_mfma_f32_32x32x16_bf16 v[160:175],v[216:219],v[88:91],v[160:175]
v_rcp_f32 v188,v188
v_rcp_f32 v189,v189
v_mfma_f32_32x32x16_bf16 v[160:175],v[220:223],v[92:95],v[160:175]
v_rcp_f32 v190,v190
v_rcp_f32 v191,v191
ds_read_b64_tr_b16 v[200:201],v17 offset:6144
ds_read_b64_tr_b16 v[202:203],v17 offset:7680
ds_read_b64_tr_b16 v[204:205],v17 offset:9216
ds_read_b64_tr_b16 v[206:207],v17 offset:10752
ds_read_b64_tr_b16 v[208:209],v17 offset:6208
ds_read_b64_tr_b16 v[210:211],v17 offset:7744
ds_read_b64_tr_b16 v[212:213],v17 offset:9280
ds_read_b64_tr_b16 v[214:215],v17 offset:10816
ds_read_b64_tr_b16 v[216:217],v17 offset:6272
ds_read_b64_tr_b16 v[218:219],v17 offset:7808
ds_read_b64_tr_b16 v[220:221],v17 offset:9344
ds_read_b64_tr_b16 v[222:223],v17 offset:10880
s_mov_b64 exec,s[60:61]
s_add_u32 m0,s58,130560
s_movk_i32 s64,64
buffer_load_dwordx4 v27,s[44:47],s64 offen lds
s_add_u32 m0,s58,131328
s_movk_i32 s65,9280
buffer_load_dwordx4 v27,s[44:47],s65 offen lds
s_mov_b64 exec,-1
s_setprio 0
v_mul_f32 v176,v144,v176
v_mul_f32 v177,v145,v177
v_mul_f32 v178,v146,v178
v_mul_f32 v179,v147,v179
v_mul_f32 v180,v148,v180
v_mul_f32 v181,v149,v181
v_mul_f32 v182,v150,v182
v_mul_f32 v183,v151,v183
v_mul_f32 v184,v152,v184
v_mul_f32 v185,v153,v185
v_mul_f32 v186,v154,v186
v_mul_f32 v187,v155,v187
v_mul_f32 v188,v156,v188
v_mul_f32 v189,v157,v189
v_mul_f32 v190,v158,v190
v_mul_f32 v191,v159,v191
v_cvt_pk_bf16_f32 v192,v176,v177
v_cvt_pk_bf16_f32 v193,v178,v179
v_cvt_pk_bf16_f32 v194,v180,v181
v_cvt_pk_bf16_f32 v195,v182,v183
v_cvt_pk_bf16_f32 v196,v184,v185
v_cvt_pk_bf16_f32 v197,v186,v187
v_cvt_pk_bf16_f32 v198,v188,v189
v_cvt_pk_bf16_f32 v199,v190,v191
s_waitcnt lgkmcnt(0)
s_waitcnt vmcnt(2)
s_barrier
v_fmaak_f32 v176,v160,v160,0x409d30fc
v_fmaak_f32 v177,v161,v161,0x409d30fc
v_fmaak_f32 v178,v162,v162,0x409d30fc
v_fmaak_f32 v179,v163,v163,0x409d30fc
v_fmaak_f32 v180,v164,v164,0x409d30fc
v_fmaak_f32 v181,v165,v165,0x409d30fc
v_fmaak_f32 v182,v166,v166,0x409d30fc
v_fmaak_f32 v183,v167,v167,0x409d30fc
v_fmaak_f32 v184,v168,v168,0x409d30fc
v_fmaak_f32 v185,v169,v169,0x409d30fc
v_fmaak_f32 v186,v170,v170,0x409d30fc
v_fmaak_f32 v187,v171,v171,0x409d30fc
v_fmaak_f32 v188,v172,v172,0x409d30fc
v_fmaak_f32 v189,v173,v173,0x409d30fc
v_fmaak_f32 v190,v174,v174,0x409d30fc
v_fmaak_f32 v191,v175,v175,0x409d30fc
v_mul_f32 v176,v160,v176
v_mul_f32 v177,v161,v177
v_mul_f32 v178,v162,v178
v_mul_f32 v179,v163,v179
v_mul_f32 v180,v164,v180
v_mul_f32 v181,v165,v181
v_mul_f32 v182,v166,v182
v_mul_f32 v183,v167,v183
v_mul_f32 v184,v168,v184
v_mul_f32 v185,v169,v185
v_mul_f32 v186,v170,v186
v_mul_f32 v187,v171,v187
v_mul_f32 v188,v172,v188
v_mul_f32 v189,v173,v189
v_mul_f32 v190,v174,v190
v_mul_f32 v191,v175,v191
s_setprio 1
v_mfma_f32_32x32x16_bf16 v[96:111],v[200:203],v[192:195],v[96:111]
v_exp_f32 v176,v176
v_exp_f32 v177,v177
v_exp_f32 v178,v178
v_mfma_f32_32x32x16_bf16 v[96:111],v[204:207],v[196:199],v[96:111]
v_exp_f32 v179,v179
v_exp_f32 v180,v180
v_exp_f32 v181,v181
v_mfma_f32_32x32x16_bf16 v[112:127],v[208:211],v[192:195],v[112:127]
v_exp_f32 v182,v182
v_exp_f32 v183,v183
v_exp_f32 v184,v184
v_mfma_f32_32x32x16_bf16 v[112:127],v[212:215],v[196:199],v[112:127]
v_exp_f32 v185,v185
v_exp_f32 v186,v186
v_exp_f32 v187,v187
v_mfma_f32_32x32x16_bf16 v[128:143],v[216:219],v[192:195],v[128:143]
v_exp_f32 v188,v188
v_exp_f32 v189,v189
v_mfma_f32_32x32x16_bf16 v[128:143],v[220:223],v[196:199],v[128:143]
v_exp_f32 v190,v190
v_exp_f32 v191,v191
ds_read_b64 v[72:73],v20 offset:0
ds_read_b64 v[74:75],v21 offset:0
ds_read_b64 v[76:77],v22 offset:0
ds_read_b64 v[78:79],v23 offset:0
ds_read_b64 v[80:81],v20 offset:64
ds_read_b64 v[82:83],v21 offset:64
ds_read_b64 v[84:85],v22 offset:64
ds_read_b64 v[86:87],v23 offset:64
ds_read_b64 v[88:89],v20 offset:128
ds_read_b64 v[90:91],v21 offset:128
ds_read_b64 v[92:93],v22 offset:128
ds_read_b64 v[94:95],v23 offset:128
s_mov_b64 exec,s[60:61]
s_add_u32 m0,s58,142848
s_mov_b32 s64,0
buffer_load_dwordx4 v28,s[48:51],s64 offen lds
s_add_u32 m0,s58,143616
s_mov_b32 s65,768
buffer_load_dwordx4 v28,s[48:51],s65 offen lds
s_mov_b64 exec,-1
s_setprio 0
v_add_f32 v176,1.0,v176
v_add_f32 v177,1.0,v177
v_add_f32 v178,1.0,v178
v_add_f32 v179,1.0,v179
v_add_f32 v180,1.0,v180
v_add_f32 v181,1.0,v181
v_add_f32 v182,1.0,v182
v_add_f32 v183,1.0,v183
v_add_f32 v184,1.0,v184
v_add_f32 v185,1.0,v185
v_add_f32 v186,1.0,v186
v_add_f32 v187,1.0,v187
v_add_f32 v188,1.0,v188
v_add_f32 v189,1.0,v189
v_add_f32 v190,1.0,v190
v_add_f32 v191,1.0,v191
s_setprio 1
s_waitcnt lgkmcnt(0)
v_rcp_f32 v176,v176
v_rcp_f32 v177,v177
v_rcp_f32 v178,v178
v_rcp_f32 v179,v179
v_rcp_f32 v180,v180
v_rcp_f32 v181,v181
v_rcp_f32 v182,v182
v_rcp_f32 v183,v183
v_rcp_f32 v184,v184
v_rcp_f32 v185,v185
v_rcp_f32 v186,v186
v_rcp_f32 v187,v187
v_rcp_f32 v188,v188
v_rcp_f32 v189,v189
v_rcp_f32 v190,v190
v_rcp_f32 v191,v191
ds_read_b64_tr_b16 v[200:201],v17 offset:12288
ds_read_b64_tr_b16 v[202:203],v17 offset:13824
ds_read_b64_tr_b16 v[204:205],v17 offset:15360
ds_read_b64_tr_b16 v[206:207],v17 offset:16896
ds_read_b64_tr_b16 v[208:209],v17 offset:12352
ds_read_b64_tr_b16 v[210:211],v17 offset:13888
ds_read_b64_tr_b16 v[212:213],v17 offset:15424
ds_read_b64_tr_b16 v[214:215],v17 offset:16960
ds_read_b64_tr_b16 v[216:217],v17 offset:12416
ds_read_b64_tr_b16 v[218:219],v17 offset:13952
ds_read_b64_tr_b16 v[220:221],v17 offset:15488
ds_read_b64_tr_b16 v[222:223],v17 offset:17024
s_mov_b64 exec,s[60:61]
s_add_u32 m0,s58,136704
s_movk_i32 s64,128
buffer_load_dwordx4 v27,s[44:47],s64 offen lds
s_add_u32 m0,s58,137472
s_movk_i32 s65,9344
buffer_load_dwordx4 v27,s[44:47],s65 offen lds
s_mov_b64 exec,-1
s_setprio 0
v_mul_f32 v176,v160,v176
v_mul_f32 v177,v161,v177
v_mul_f32 v178,v162,v178
v_mul_f32 v179,v163,v179
v_mul_f32 v180,v164,v180
v_mul_f32 v181,v165,v181
v_mul_f32 v182,v166,v182
v_mul_f32 v183,v167,v183
v_mul_f32 v184,v168,v184
v_mul_f32 v185,v169,v185
v_mul_f32 v186,v170,v186
v_mul_f32 v187,v171,v187
v_mul_f32 v188,v172,v188
v_mul_f32 v189,v173,v189
v_mul_f32 v190,v174,v190
v_mul_f32 v191,v175,v191
v_cvt_pk_bf16_f32 v192,v176,v177
v_cvt_pk_bf16_f32 v193,v178,v179
v_cvt_pk_bf16_f32 v194,v180,v181
v_cvt_pk_bf16_f32 v195,v182,v183
v_cvt_pk_bf16_f32 v196,v184,v185
v_cvt_pk_bf16_f32 v197,v186,v187
v_cvt_pk_bf16_f32 v198,v188,v189
v_cvt_pk_bf16_f32 v199,v190,v191
ds_read_b128 v[144:147],v18 offset:1536
ds_read_b128 v[148:151],v18 offset:1568
ds_read_b128 v[152:155],v18 offset:1600
ds_read_b128 v[156:159],v18 offset:1632
ds_read_b128 v[160:163],v18 offset:1664
ds_read_b128 v[164:167],v18 offset:1696
ds_read_b128 v[168:171],v18 offset:1728
ds_read_b128 v[172:175],v18 offset:1760
ds_read_b128 v[176:179],v18 offset:1792
ds_read_b128 v[180:183],v18 offset:1824
ds_read_b128 v[184:187],v18 offset:1856
ds_read_b128 v[188:191],v18 offset:1888
s_waitcnt lgkmcnt(12)
s_setprio 1
v_mfma_f32_32x32x16_bf16 v[96:111],v[200:203],v[192:195],v[96:111]
v_lshlrev_b32 v232,16,v72
v_lshlrev_b32 v233,16,v73
v_lshlrev_b32 v234,16,v74
v_lshlrev_b32 v235,16,v75
v_mfma_f32_32x32x16_bf16 v[96:111],v[204:207],v[196:199],v[96:111]
v_lshlrev_b32 v236,16,v76
v_lshlrev_b32 v237,16,v77
v_lshlrev_b32 v238,16,v78
v_lshlrev_b32 v239,16,v79
v_mfma_f32_32x32x16_bf16 v[112:127],v[208:211],v[192:195],v[112:127]
v_lshlrev_b32 v240,16,v80
v_lshlrev_b32 v241,16,v81
v_lshlrev_b32 v242,16,v82
v_lshlrev_b32 v243,16,v83
v_mfma_f32_32x32x16_bf16 v[112:127],v[212:215],v[196:199],v[112:127]
v_lshlrev_b32 v244,16,v84
v_lshlrev_b32 v245,16,v85
v_lshlrev_b32 v246,16,v86
v_lshlrev_b32 v247,16,v87
v_mfma_f32_32x32x16_bf16 v[128:143],v[216:219],v[192:195],v[128:143]
v_lshlrev_b32 v248,16,v88
v_lshlrev_b32 v249,16,v89
v_lshlrev_b32 v250,16,v90
v_lshlrev_b32 v251,16,v91
v_mfma_f32_32x32x16_bf16 v[128:143],v[220:223],v[196:199],v[128:143]
v_lshlrev_b32 v252,16,v92
v_lshlrev_b32 v253,16,v93
v_lshlrev_b32 v254,16,v94
v_lshlrev_b32 v255,16,v95
s_setprio 0
s_waitcnt lgkmcnt(0)
v_and_b32 v72,0xffff0000,v72
v_and_b32 v73,0xffff0000,v73
v_and_b32 v74,0xffff0000,v74
v_and_b32 v75,0xffff0000,v75
v_and_b32 v76,0xffff0000,v76
v_and_b32 v77,0xffff0000,v77
v_and_b32 v78,0xffff0000,v78
v_and_b32 v79,0xffff0000,v79
v_and_b32 v80,0xffff0000,v80
v_and_b32 v81,0xffff0000,v81
v_and_b32 v82,0xffff0000,v82
v_and_b32 v83,0xffff0000,v83
v_and_b32 v84,0xffff0000,v84
v_and_b32 v85,0xffff0000,v85
v_and_b32 v86,0xffff0000,v86
v_and_b32 v87,0xffff0000,v87
v_and_b32 v88,0xffff0000,v88
v_and_b32 v89,0xffff0000,v89
v_and_b32 v90,0xffff0000,v90
v_and_b32 v91,0xffff0000,v91
v_and_b32 v92,0xffff0000,v92
v_and_b32 v93,0xffff0000,v93
v_and_b32 v94,0xffff0000,v94
v_and_b32 v95,0xffff0000,v95
v_fma_f32 v232,v96,v144,v232
v_fma_f32 v72,v97,v145,v72
v_fma_f32 v233,v98,v146,v233
v_fma_f32 v73,v99,v147,v73
v_fma_f32 v234,v100,v148,v234
v_fma_f32 v74,v101,v149,v74
v_fma_f32 v235,v102,v150,v235
v_fma_f32 v75,v103,v151,v75
v_fma_f32 v236,v104,v152,v236
v_fma_f32 v76,v105,v153,v76
v_fma_f32 v237,v106,v154,v237
v_fma_f32 v77,v107,v155,v77
v_fma_f32 v238,v108,v156,v238
v_fma_f32 v78,v109,v157,v78
v_fma_f32 v239,v110,v158,v239
v_fma_f32 v79,v111,v159,v79
v_fma_f32 v240,v112,v160,v240
v_fma_f32 v80,v113,v161,v80
v_fma_f32 v241,v114,v162,v241
v_fma_f32 v81,v115,v163,v81
v_fma_f32 v242,v116,v164,v242
v_fma_f32 v82,v117,v165,v82
v_fma_f32 v243,v118,v166,v243
v_fma_f32 v83,v119,v167,v83
v_fma_f32 v244,v120,v168,v244
v_fma_f32 v84,v121,v169,v84
v_fma_f32 v245,v122,v170,v245
v_fma_f32 v85,v123,v171,v85
v_fma_f32 v246,v124,v172,v246
v_fma_f32 v86,v125,v173,v86
v_fma_f32 v247,v126,v174,v247
v_fma_f32 v87,v127,v175,v87
v_fma_f32 v248,v128,v176,v248
v_fma_f32 v88,v129,v177,v88
v_fma_f32 v249,v130,v178,v249
v_fma_f32 v89,v131,v179,v89
v_fma_f32 v250,v132,v180,v250
v_fma_f32 v90,v133,v181,v90
v_fma_f32 v251,v134,v182,v251
v_fma_f32 v91,v135,v183,v91
v_fma_f32 v252,v136,v184,v252
v_fma_f32 v92,v137,v185,v92
v_fma_f32 v253,v138,v186,v253
v_fma_f32 v93,v139,v187,v93
v_fma_f32 v254,v140,v188,v254
v_fma_f32 v94,v141,v189,v94
v_fma_f32 v255,v142,v190,v255
v_fma_f32 v95,v143,v191,v95
v_add_u32 v4,s54,v4
v_add_u32 v5,s54,v5
v_add_u32 v6,s54,v6
v_add_u32 v7,s54,v7
v_add_u32 v8,s54,v8
v_add_u32 v9,s54,v9
v_add_u32 v10,s54,v10
v_add_u32 v11,s54,v11
v_add_u32 v12,s54,v12
v_add_u32 v13,s54,v13
v_add_u32 v14,s54,v14
v_add_u32 v15,s54,v15
v_cvt_pk_bf16_f32 v200,v232,v72
v_cvt_pk_bf16_f32 v201,v233,v73
v_cvt_pk_bf16_f32 v202,v234,v74
v_cvt_pk_bf16_f32 v203,v235,v75
v_cvt_pk_bf16_f32 v204,v236,v76
v_cvt_pk_bf16_f32 v205,v237,v77
v_cvt_pk_bf16_f32 v206,v238,v78
v_cvt_pk_bf16_f32 v207,v239,v79
v_cvt_pk_bf16_f32 v208,v240,v80
v_cvt_pk_bf16_f32 v209,v241,v81
v_cvt_pk_bf16_f32 v210,v242,v82
v_cvt_pk_bf16_f32 v211,v243,v83
v_cvt_pk_bf16_f32 v212,v244,v84
v_cvt_pk_bf16_f32 v213,v245,v85
v_cvt_pk_bf16_f32 v214,v246,v86
v_cvt_pk_bf16_f32 v215,v247,v87
v_cvt_pk_bf16_f32 v216,v248,v88
v_cvt_pk_bf16_f32 v217,v249,v89
v_cvt_pk_bf16_f32 v218,v250,v90
v_cvt_pk_bf16_f32 v219,v251,v91
v_cvt_pk_bf16_f32 v220,v252,v92
v_cvt_pk_bf16_f32 v221,v253,v93
v_cvt_pk_bf16_f32 v222,v254,v94
v_cvt_pk_bf16_f32 v223,v255,v95
ds_write_b64 v20,v[200:201] offset:0
ds_write_b64 v21,v[202:203] offset:0
ds_write_b64 v22,v[204:205] offset:0
ds_write_b64 v23,v[206:207] offset:0
ds_write_b64 v20,v[208:209] offset:64
ds_write_b64 v21,v[210:211] offset:64
ds_write_b64 v22,v[212:213] offset:64
ds_write_b64 v23,v[214:215] offset:64
ds_write_b64 v20,v[216:217] offset:128
ds_write_b64 v21,v[218:219] offset:128
ds_write_b64 v22,v[220:221] offset:128
ds_write_b64 v23,v[222:223] offset:128
ds_read_b128 v[72:75],v19 offset:0
ds_read_b128 v[76:79],v19 offset:1024
ds_read_b128 v[80:83],v19 offset:2048
ds_read_b128 v[84:87],v19 offset:3456
ds_read_b128 v[88:91],v19 offset:4480
ds_read_b128 v[92:95],v19 offset:5504
v_add_u32 v20,s54,v20
v_add_u32 v21,s54,v21
v_add_u32 v22,s54,v22
v_add_u32 v23,s54,v23
s_sub_u32 s68,s68,s54
s_add_u32 s59,s59,1572864
s_add_u32 s53,s53,1
s_cmp_eq_u32 s53,3
s_cselect_b32 s59,0x70000000,s59
s_add_u32 s67,s69,98304
s_waitcnt lgkmcnt(5)
buffer_store_dwordx4 v[72:75],v24,s[40:43],s69 offen nt
s_waitcnt lgkmcnt(4)
buffer_store_dwordx4 v[76:79],v25,s[40:43],s69 offen nt
s_waitcnt lgkmcnt(3)
buffer_store_dwordx4 v[80:83],v26,s[40:43],s69 offen nt
s_waitcnt lgkmcnt(2)
buffer_store_dwordx4 v[84:87],v24,s[40:43],s67 offen nt
s_waitcnt lgkmcnt(1)
buffer_store_dwordx4 v[88:91],v25,s[40:43],s67 offen nt
s_waitcnt lgkmcnt(0)
buffer_store_dwordx4 v[92:95],v26,s[40:43],s67 offen nt
v_add_u32 v19,s54,v19
s_add_u32 s69,s69,1572864
s_sub_u32 s54,0,s54
s_cmp_lt_u32 s53,4
s_cbranch_scc1 L_pass0_%=
s_branch L_end_%=
L_pass1_%=:
s_waitcnt vmcnt(6)
s_barrier
ds_read_b128 v[200:203],v4 offset:0
ds_read_b128 v[204:207],v8 offset:0
ds_read_b128 v[208:211],v12 offset:0
ds_read_b128 v[212:215],v5 offset:0
ds_read_b128 v[216:219],v9 offset:0
ds_read_b128 v[220:223],v13 offset:0
ds_read_b128 v[96:99],v18 offset:1920
ds_read_b128 v[100:103],v18 offset:1952
ds_read_b128 v[104:107],v18 offset:1984
ds_read_b128 v[108:111],v18 offset:2016
ds_read_b128 v[112:115],v18 offset:2048
ds_read_b128 v[116:119],v18 offset:2080
ds_read_b128 v[120:123],v18 offset:2112
ds_read_b128 v[124:127],v18 offset:2144
ds_read_b128 v[128:131],v18 offset:2176
ds_read_b128 v[132:135],v18 offset:2208
ds_read_b128 v[136:139],v18 offset:2240
ds_read_b128 v[140:143],v18 offset:2272
v_and_b32 v72,v32,v36
v_and_b32 v73,v33,v36
v_and_b32 v74,v34,v36
v_and_b32 v75,v35,v36
v_and_b32 v76,v32,v48
v_and_b32 v77,v33,v48
v_and_b32 v78,v34,v48
v_and_b32 v79,v35,v48
v_and_b32 v80,v32,v60
v_and_b32 v81,v33,v60
v_and_b32 v82,v34,v60
v_and_b32 v83,v35,v60
v_and_b32 v84,v32,v37
v_and_b32 v85,v33,v37
v_and_b32 v86,v34,v37
v_and_b32 v87,v35,v37
v_and_b32 v88,v32,v49
v_and_b32 v89,v33,v49
v_and_b32 v90,v34,v49
v_and_b32 v91,v35,v49
v_and_b32 v92,v32,v61
v_and_b32 v93,v33,v61
v_and_b32 v94,v34,v61
v_and_b32 v95,v35,v61
s_setprio 1
s_waitcnt lgkmcnt(15)
v_mfma_f32_16x16x32_bf16 v[144:147],v[72:75],v[200:203],0
v_mfma_f32_16x16x32_bf16 v[144:147],v[76:79],v[204:207],v[144:147]
v_mfma_f32_16x16x32_bf16 v[144:147],v[80:83],v[208:211],v[144:147]
ds_read_b128 v[200:203],v6 offset:0
ds_read_b128 v[204:207],v10 offset:0
ds_read_b128 v[208:211],v14 offset:0
s_setprio 0
v_and_b32 v72,v32,v38
v_and_b32 v73,v33,v38
v_and_b32 v74,v34,v38
v_and_b32 v75,v35,v38
v_and_b32 v76,v32,v50
v_and_b32 v77,v33,v50
v_and_b32 v78,v34,v50
v_and_b32 v79,v35,v50
v_and_b32 v80,v32,v62
v_and_b32 v81,v33,v62
v_and_b32 v82,v34,v62
v_and_b32 v83,v35,v62
v_mul_f32 v237,v144,v144
v_mul_f32 v236,v145,v145
v_mul_f32 v239,v146,v146
v_mul_f32 v238,v147,v147
s_setprio 1
s_waitcnt lgkmcnt(15)
v_mfma_f32_16x16x32_bf16 v[148:151],v[84:87],v[212:215],0
v_mfma_f32_16x16x32_bf16 v[148:151],v[88:91],v[216:219],v[148:151]
v_mfma_f32_16x16x32_bf16 v[148:151],v[92:95],v[220:223],v[148:151]
ds_read_b128 v[212:215],v7 offset:0
ds_read_b128 v[216:219],v11 offset:0
ds_read_b128 v[220:223],v15 offset:0
s_setprio 0
v_and_b32 v84,v32,v39
v_and_b32 v85,v33,v39
v_and_b32 v86,v34,v39
v_and_b32 v87,v35,v39
v_and_b32 v88,v32,v51
v_and_b32 v89,v33,v51
v_and_b32 v90,v34,v51
v_and_b32 v91,v35,v51
v_and_b32 v92,v32,v63
v_and_b32 v93,v33,v63
v_and_b32 v94,v34,v63
v_and_b32 v95,v35,v63
v_add_f32 v232,v144,v148
v_fmac_f32 v237,v148,v148
v_add_f32 v233,v145,v149
v_fmac_f32 v236,v149,v149
v_add_f32 v234,v146,v150
v_fmac_f32 v239,v150,v150
v_add_f32 v235,v147,v151
v_fmac_f32 v238,v151,v151
s_setprio 1
s_waitcnt lgkmcnt(3)
v_mfma_f32_16x16x32_bf16 v[152:155],v[72:75],v[200:203],0
v_mfma_f32_16x16x32_bf16 v[152:155],v[76:79],v[204:207],v[152:155]
v_mfma_f32_16x16x32_bf16 v[152:155],v[80:83],v[208:211],v[152:155]
ds_read_b128 v[200:203],v4 offset:64
ds_read_b128 v[204:207],v8 offset:64
ds_read_b128 v[208:211],v12 offset:64
s_setprio 0
v_and_b32 v72,v32,v40
v_and_b32 v73,v33,v40
v_and_b32 v74,v34,v40
v_and_b32 v75,v35,v40
v_and_b32 v76,v32,v52
v_and_b32 v77,v33,v52
v_and_b32 v78,v34,v52
v_and_b32 v79,v35,v52
v_and_b32 v80,v32,v64
v_and_b32 v81,v33,v64
v_and_b32 v82,v34,v64
v_and_b32 v83,v35,v64
v_add_f32 v232,v232,v152
v_fmac_f32 v237,v152,v152
v_add_f32 v233,v233,v153
v_fmac_f32 v236,v153,v153
v_add_f32 v234,v234,v154
v_fmac_f32 v239,v154,v154
v_add_f32 v235,v235,v155
v_fmac_f32 v238,v155,v155
s_setprio 1
s_waitcnt lgkmcnt(3)
v_mfma_f32_16x16x32_bf16 v[156:159],v[84:87],v[212:215],0
v_mfma_f32_16x16x32_bf16 v[156:159],v[88:91],v[216:219],v[156:159]
v_mfma_f32_16x16x32_bf16 v[156:159],v[92:95],v[220:223],v[156:159]
ds_read_b128 v[212:215],v5 offset:64
ds_read_b128 v[216:219],v9 offset:64
ds_read_b128 v[220:223],v13 offset:64
s_setprio 0
v_and_b32 v84,v32,v41
v_and_b32 v85,v33,v41
v_and_b32 v86,v34,v41
v_and_b32 v87,v35,v41
v_and_b32 v88,v32,v53
v_and_b32 v89,v33,v53
v_and_b32 v90,v34,v53
v_and_b32 v91,v35,v53
v_and_b32 v92,v32,v65
v_and_b32 v93,v33,v65
v_and_b32 v94,v34,v65
v_and_b32 v95,v35,v65
v_add_f32 v232,v232,v156
v_fmac_f32 v237,v156,v156
v_add_f32 v233,v233,v157
v_fmac_f32 v236,v157,v157
v_add_f32 v234,v234,v158
v_fmac_f32 v239,v158,v158
v_add_f32 v235,v235,v159
v_fmac_f32 v238,v159,v159
s_setprio 1
s_waitcnt lgkmcnt(3)
v_mfma_f32_16x16x32_bf16 v[160:163],v[72:75],v[200:203],0
v_mfma_f32_16x16x32_bf16 v[160:163],v[76:79],v[204:207],v[160:163]
v_mfma_f32_16x16x32_bf16 v[160:163],v[80:83],v[208:211],v[160:163]
ds_read_b128 v[200:203],v6 offset:64
ds_read_b128 v[204:207],v10 offset:64
ds_read_b128 v[208:211],v14 offset:64
s_setprio 0
v_and_b32 v72,v32,v42
v_and_b32 v73,v33,v42
v_and_b32 v74,v34,v42
v_and_b32 v75,v35,v42
v_and_b32 v76,v32,v54
v_and_b32 v77,v33,v54
v_and_b32 v78,v34,v54
v_and_b32 v79,v35,v54
v_and_b32 v80,v32,v66
v_and_b32 v81,v33,v66
v_and_b32 v82,v34,v66
v_and_b32 v83,v35,v66
v_add_f32 v232,v232,v160
v_fmac_f32 v237,v160,v160
v_add_f32 v233,v233,v161
v_fmac_f32 v236,v161,v161
v_add_f32 v234,v234,v162
v_fmac_f32 v239,v162,v162
v_add_f32 v235,v235,v163
v_fmac_f32 v238,v163,v163
s_setprio 1
s_waitcnt lgkmcnt(3)
v_mfma_f32_16x16x32_bf16 v[164:167],v[84:87],v[212:215],0
v_mfma_f32_16x16x32_bf16 v[164:167],v[88:91],v[216:219],v[164:167]
v_mfma_f32_16x16x32_bf16 v[164:167],v[92:95],v[220:223],v[164:167]
ds_read_b128 v[212:215],v7 offset:64
ds_read_b128 v[216:219],v11 offset:64
ds_read_b128 v[220:223],v15 offset:64
s_setprio 0
v_and_b32 v84,v32,v43
v_and_b32 v85,v33,v43
v_and_b32 v86,v34,v43
v_and_b32 v87,v35,v43
v_and_b32 v88,v32,v55
v_and_b32 v89,v33,v55
v_and_b32 v90,v34,v55
v_and_b32 v91,v35,v55
v_and_b32 v92,v32,v67
v_and_b32 v93,v33,v67
v_and_b32 v94,v34,v67
v_and_b32 v95,v35,v67
v_add_f32 v232,v232,v164
v_fmac_f32 v237,v164,v164
v_add_f32 v233,v233,v165
v_fmac_f32 v236,v165,v165
v_add_f32 v234,v234,v166
v_fmac_f32 v239,v166,v166
v_add_f32 v235,v235,v167
v_fmac_f32 v238,v167,v167
s_setprio 1
s_waitcnt lgkmcnt(3)
v_mfma_f32_16x16x32_bf16 v[168:171],v[72:75],v[200:203],0
v_mfma_f32_16x16x32_bf16 v[168:171],v[76:79],v[204:207],v[168:171]
v_mfma_f32_16x16x32_bf16 v[168:171],v[80:83],v[208:211],v[168:171]
ds_read_b128 v[200:203],v4 offset:128
ds_read_b128 v[204:207],v8 offset:128
ds_read_b128 v[208:211],v12 offset:128
s_setprio 0
v_and_b32 v72,v32,v44
v_and_b32 v73,v33,v44
v_and_b32 v74,v34,v44
v_and_b32 v75,v35,v44
v_and_b32 v76,v32,v56
v_and_b32 v77,v33,v56
v_and_b32 v78,v34,v56
v_and_b32 v79,v35,v56
v_and_b32 v80,v32,v68
v_and_b32 v81,v33,v68
v_and_b32 v82,v34,v68
v_and_b32 v83,v35,v68
v_add_f32 v232,v232,v168
v_fmac_f32 v237,v168,v168
v_add_f32 v233,v233,v169
v_fmac_f32 v236,v169,v169
v_add_f32 v234,v234,v170
v_fmac_f32 v239,v170,v170
v_add_f32 v235,v235,v171
v_fmac_f32 v238,v171,v171
s_setprio 1
s_waitcnt lgkmcnt(3)
v_mfma_f32_16x16x32_bf16 v[172:175],v[84:87],v[212:215],0
v_mfma_f32_16x16x32_bf16 v[172:175],v[88:91],v[216:219],v[172:175]
v_mfma_f32_16x16x32_bf16 v[172:175],v[92:95],v[220:223],v[172:175]
ds_read_b128 v[212:215],v5 offset:128
ds_read_b128 v[216:219],v9 offset:128
ds_read_b128 v[220:223],v13 offset:128
s_setprio 0
v_and_b32 v84,v32,v45
v_and_b32 v85,v33,v45
v_and_b32 v86,v34,v45
v_and_b32 v87,v35,v45
v_and_b32 v88,v32,v57
v_and_b32 v89,v33,v57
v_and_b32 v90,v34,v57
v_and_b32 v91,v35,v57
v_and_b32 v92,v32,v69
v_and_b32 v93,v33,v69
v_and_b32 v94,v34,v69
v_and_b32 v95,v35,v69
v_add_f32 v232,v232,v172
v_fmac_f32 v237,v172,v172
v_add_f32 v233,v233,v173
v_fmac_f32 v236,v173,v173
v_add_f32 v234,v234,v174
v_fmac_f32 v239,v174,v174
v_add_f32 v235,v235,v175
v_fmac_f32 v238,v175,v175
s_setprio 1
s_waitcnt lgkmcnt(3)
v_mfma_f32_16x16x32_bf16 v[176:179],v[72:75],v[200:203],0
v_mfma_f32_16x16x32_bf16 v[176:179],v[76:79],v[204:207],v[176:179]
v_mfma_f32_16x16x32_bf16 v[176:179],v[80:83],v[208:211],v[176:179]
ds_read_b128 v[200:203],v6 offset:128
ds_read_b128 v[204:207],v10 offset:128
ds_read_b128 v[208:211],v14 offset:128
s_setprio 0
v_and_b32 v72,v32,v46
v_and_b32 v73,v33,v46
v_and_b32 v74,v34,v46
v_and_b32 v75,v35,v46
v_and_b32 v76,v32,v58
v_and_b32 v77,v33,v58
v_and_b32 v78,v34,v58
v_and_b32 v79,v35,v58
v_and_b32 v80,v32,v70
v_and_b32 v81,v33,v70
v_and_b32 v82,v34,v70
v_and_b32 v83,v35,v70
v_add_f32 v232,v232,v176
v_fmac_f32 v237,v176,v176
v_add_f32 v233,v233,v177
v_fmac_f32 v236,v177,v177
v_add_f32 v234,v234,v178
v_fmac_f32 v239,v178,v178
v_add_f32 v235,v235,v179
v_fmac_f32 v238,v179,v179
s_setprio 1
s_waitcnt lgkmcnt(3)
v_mfma_f32_16x16x32_bf16 v[180:183],v[84:87],v[212:215],0
v_mfma_f32_16x16x32_bf16 v[180:183],v[88:91],v[216:219],v[180:183]
v_mfma_f32_16x16x32_bf16 v[180:183],v[92:95],v[220:223],v[180:183]
ds_read_b128 v[212:215],v7 offset:128
ds_read_b128 v[216:219],v11 offset:128
ds_read_b128 v[220:223],v15 offset:128
s_setprio 0
v_and_b32 v84,v32,v47
v_and_b32 v85,v33,v47
v_and_b32 v86,v34,v47
v_and_b32 v87,v35,v47
v_and_b32 v88,v32,v59
v_and_b32 v89,v33,v59
v_and_b32 v90,v34,v59
v_and_b32 v91,v35,v59
v_and_b32 v92,v32,v71
v_and_b32 v93,v33,v71
v_and_b32 v94,v34,v71
v_and_b32 v95,v35,v71
v_add_f32 v232,v232,v180
v_fmac_f32 v237,v180,v180
v_add_f32 v233,v233,v181
v_fmac_f32 v236,v181,v181
v_add_f32 v234,v234,v182
v_fmac_f32 v239,v182,v182
v_add_f32 v235,v235,v183
v_fmac_f32 v238,v183,v183
s_setprio 1
s_waitcnt lgkmcnt(3)
v_mfma_f32_16x16x32_bf16 v[184:187],v[72:75],v[200:203],0
v_mfma_f32_16x16x32_bf16 v[184:187],v[76:79],v[204:207],v[184:187]
v_mfma_f32_16x16x32_bf16 v[184:187],v[80:83],v[208:211],v[184:187]
s_setprio 0
s_nop 7
v_add_f32 v232,v232,v184
v_fmac_f32 v237,v184,v184
v_add_f32 v233,v233,v185
v_fmac_f32 v236,v185,v185
v_add_f32 v234,v234,v186
v_fmac_f32 v239,v186,v186
v_add_f32 v235,v235,v187
v_fmac_f32 v238,v187,v187
s_setprio 1
s_waitcnt lgkmcnt(0)
v_mfma_f32_16x16x32_bf16 v[188:191],v[84:87],v[212:215],0
v_mfma_f32_16x16x32_bf16 v[188:191],v[88:91],v[216:219],v[188:191]
v_mfma_f32_16x16x32_bf16 v[188:191],v[92:95],v[220:223],v[188:191]
s_setprio 0
ds_read_b64_tr_b16 v[200:201],v16 offset:0
ds_read_b64_tr_b16 v[202:203],v16 offset:512
ds_read_b64_tr_b16 v[204:205],v16 offset:1024
ds_read_b64_tr_b16 v[206:207],v16 offset:1536
ds_read_b64_tr_b16 v[208:209],v16 offset:2048
ds_read_b64_tr_b16 v[210:211],v16 offset:2560
ds_read_b64_tr_b16 v[212:213],v16 offset:3072
ds_read_b64_tr_b16 v[214:215],v16 offset:3584
ds_read_b64_tr_b16 v[216:217],v16 offset:4096
ds_read_b64_tr_b16 v[218:219],v16 offset:4608
ds_read_b64_tr_b16 v[220:221],v16 offset:5120
ds_read_b64_tr_b16 v[222:223],v16 offset:5632
v_add_f32 v232,v232,v188
v_fmac_f32 v237,v188,v188
v_add_f32 v233,v233,v189
v_fmac_f32 v236,v189,v189
v_add_f32 v234,v234,v190
v_fmac_f32 v239,v190,v190
v_add_f32 v235,v235,v191
v_fmac_f32 v238,v191,v191
v_add_f32 v232,v232,v233
v_add_f32 v234,v234,v235
v_add_f32 v237,v237,v236
v_add_f32 v239,v239,v238
v_add_f32 v240,v232,v234
v_add_f32 v241,v237,v239
s_nop 1
v_permlane32_swap_b32 v240,v241
v_add_f32 v240,v240,v241
v_mov_b32 v241,v240
s_nop 1
v_permlane32_swap_b32 v240,v241
v_mul_f32 v240,0x3c2aaaab,v240
v_mul_f32 v241,0x3c2aaaab,v241
v_fma_f32 v241,-v240,v240,v241
v_max_f32 v241,0,v241
v_add_f32 v241,0x3727c5ac,v241
v_rsq_f32 v241,v241
s_nop 0
v_mul_f32 v242,0xbeeff558,v241
v_mul_f32 v243,0x3eeff558,v241
v_mul_f32 v243,v240,v243
v_fma_f32 v144,v144,v242,v243
v_fma_f32 v145,v145,v242,v243
v_fma_f32 v146,v146,v242,v243
v_fma_f32 v147,v147,v242,v243
v_fma_f32 v148,v148,v242,v243
v_fma_f32 v149,v149,v242,v243
v_fma_f32 v150,v150,v242,v243
v_fma_f32 v151,v151,v242,v243
v_fma_f32 v152,v152,v242,v243
v_fma_f32 v153,v153,v242,v243
v_fma_f32 v154,v154,v242,v243
v_fma_f32 v155,v155,v242,v243
v_fma_f32 v156,v156,v242,v243
v_fma_f32 v157,v157,v242,v243
v_fma_f32 v158,v158,v242,v243
v_fma_f32 v159,v159,v242,v243
v_fma_f32 v160,v160,v242,v243
v_fma_f32 v161,v161,v242,v243
v_fma_f32 v162,v162,v242,v243
v_fma_f32 v163,v163,v242,v243
v_fma_f32 v164,v164,v242,v243
v_fma_f32 v165,v165,v242,v243
v_fma_f32 v166,v166,v242,v243
v_fma_f32 v167,v167,v242,v243
v_fma_f32 v168,v168,v242,v243
v_fma_f32 v169,v169,v242,v243
v_fma_f32 v170,v170,v242,v243
v_fma_f32 v171,v171,v242,v243
v_fma_f32 v172,v172,v242,v243
v_fma_f32 v173,v173,v242,v243
v_fma_f32 v174,v174,v242,v243
v_fma_f32 v175,v175,v242,v243
v_fma_f32 v176,v176,v242,v243
v_fma_f32 v177,v177,v242,v243
v_fma_f32 v178,v178,v242,v243
v_fma_f32 v179,v179,v242,v243
v_fma_f32 v180,v180,v242,v243
v_fma_f32 v181,v181,v242,v243
v_fma_f32 v182,v182,v242,v243
v_fma_f32 v183,v183,v242,v243
v_fma_f32 v184,v184,v242,v243
v_fma_f32 v185,v185,v242,v243
v_fma_f32 v186,v186,v242,v243
v_fma_f32 v187,v187,v242,v243
v_fma_f32 v188,v188,v242,v243
v_fma_f32 v189,v189,v242,v243
v_fma_f32 v190,v190,v242,v243
v_fma_f32 v191,v191,v242,v243
s_setprio 1
v_cvt_pk_bf16_f32 v72,v144,v145
v_cvt_pk_bf16_f32 v73,v146,v147
v_cvt_pk_bf16_f32 v74,v148,v149
v_cvt_pk_bf16_f32 v75,v150,v151
v_cvt_pk_bf16_f32 v76,v152,v153
v_cvt_pk_bf16_f32 v77,v154,v155
v_cvt_pk_bf16_f32 v78,v156,v157
v_cvt_pk_bf16_f32 v79,v158,v159
ds_read_b128 v[144:147],v18 offset:0
ds_read_b128 v[148:151],v18 offset:32
ds_read_b128 v[152:155],v18 offset:64
ds_read_b128 v[156:159],v18 offset:96
v_cvt_pk_bf16_f32 v80,v160,v161
v_cvt_pk_bf16_f32 v81,v162,v163
v_cvt_pk_bf16_f32 v82,v164,v165
v_cvt_pk_bf16_f32 v83,v166,v167
v_cvt_pk_bf16_f32 v84,v168,v169
v_cvt_pk_bf16_f32 v85,v170,v171
v_cvt_pk_bf16_f32 v86,v172,v173
v_cvt_pk_bf16_f32 v87,v174,v175
s_waitcnt lgkmcnt(0)
v_mfma_f32_32x32x16_bf16 v[144:159],v[200:203],v[72:75],v[144:159]
v_cvt_pk_bf16_f32 v88,v176,v177
v_cvt_pk_bf16_f32 v89,v178,v179
v_cvt_pk_bf16_f32 v90,v180,v181
v_cvt_pk_bf16_f32 v91,v182,v183
v_mfma_f32_32x32x16_bf16 v[144:159],v[204:207],v[76:79],v[144:159]
v_cvt_pk_bf16_f32 v92,v184,v185
v_cvt_pk_bf16_f32 v93,v186,v187
v_cvt_pk_bf16_f32 v94,v188,v189
v_cvt_pk_bf16_f32 v95,v190,v191
v_mfma_f32_32x32x16_bf16 v[144:159],v[208:211],v[80:83],v[144:159]
v_mfma_f32_32x32x16_bf16 v[144:159],v[212:215],v[84:87],v[144:159]
v_mfma_f32_32x32x16_bf16 v[144:159],v[216:219],v[88:91],v[144:159]
v_mfma_f32_32x32x16_bf16 v[144:159],v[220:223],v[92:95],v[144:159]
s_setprio 0
s_nop 7
s_nop 1
s_waitcnt vmcnt(0)
s_barrier
v_fmaak_f32 v176,v144,v144,0x409d30fc
v_fmaak_f32 v177,v145,v145,0x409d30fc
v_fmaak_f32 v178,v146,v146,0x409d30fc
v_fmaak_f32 v179,v147,v147,0x409d30fc
v_fmaak_f32 v180,v148,v148,0x409d30fc
v_fmaak_f32 v181,v149,v149,0x409d30fc
v_fmaak_f32 v182,v150,v150,0x409d30fc
v_fmaak_f32 v183,v151,v151,0x409d30fc
v_fmaak_f32 v184,v152,v152,0x409d30fc
v_fmaak_f32 v185,v153,v153,0x409d30fc
v_fmaak_f32 v186,v154,v154,0x409d30fc
v_fmaak_f32 v187,v155,v155,0x409d30fc
v_fmaak_f32 v188,v156,v156,0x409d30fc
v_fmaak_f32 v189,v157,v157,0x409d30fc
v_fmaak_f32 v190,v158,v158,0x409d30fc
v_fmaak_f32 v191,v159,v159,0x409d30fc
v_mul_f32 v176,v144,v176
v_mul_f32 v177,v145,v177
v_mul_f32 v178,v146,v178
v_mul_f32 v179,v147,v179
v_mul_f32 v180,v148,v180
v_mul_f32 v181,v149,v181
v_mul_f32 v182,v150,v182
v_mul_f32 v183,v151,v183
v_mul_f32 v184,v152,v184
v_mul_f32 v185,v153,v185
v_mul_f32 v186,v154,v186
v_mul_f32 v187,v155,v187
v_mul_f32 v188,v156,v188
v_mul_f32 v189,v157,v189
v_mul_f32 v190,v158,v190
v_mul_f32 v191,v159,v191
s_setprio 1
v_exp_f32 v176,v176
v_exp_f32 v177,v177
v_exp_f32 v178,v178
v_exp_f32 v179,v179
v_exp_f32 v180,v180
v_exp_f32 v181,v181
v_exp_f32 v182,v182
v_exp_f32 v183,v183
v_exp_f32 v184,v184
v_exp_f32 v185,v185
v_exp_f32 v186,v186
v_exp_f32 v187,v187
v_exp_f32 v188,v188
v_exp_f32 v189,v189
v_exp_f32 v190,v190
v_exp_f32 v191,v191
ds_read_b64_tr_b16 v[200:201],v16 offset:6144
ds_read_b64_tr_b16 v[202:203],v16 offset:6656
ds_read_b64_tr_b16 v[204:205],v16 offset:7168
ds_read_b64_tr_b16 v[206:207],v16 offset:7680
ds_read_b64_tr_b16 v[208:209],v16 offset:8192
ds_read_b64_tr_b16 v[210:211],v16 offset:8704
ds_read_b64_tr_b16 v[212:213],v16 offset:9216
ds_read_b64_tr_b16 v[214:215],v16 offset:9728
ds_read_b64_tr_b16 v[216:217],v16 offset:10240
ds_read_b64_tr_b16 v[218:219],v16 offset:10752
ds_read_b64_tr_b16 v[220:221],v16 offset:11264
ds_read_b64_tr_b16 v[222:223],v16 offset:11776
ds_read_b128 v[160:163],v18 offset:128
ds_read_b128 v[164:167],v18 offset:160
ds_read_b128 v[168:171],v18 offset:192
ds_read_b128 v[172:175],v18 offset:224
s_mov_b64 exec,s[62:63]
s_add_u32 m0,s68,0
s_add_u32 s64,s59,0
buffer_load_dwordx4 v27,s[36:39],s64 offen lds
s_mov_b64 exec,-1
s_setprio 0
v_add_f32 v176,1.0,v176
v_add_f32 v177,1.0,v177
v_add_f32 v178,1.0,v178
v_add_f32 v179,1.0,v179
v_add_f32 v180,1.0,v180
v_add_f32 v181,1.0,v181
v_add_f32 v182,1.0,v182
v_add_f32 v183,1.0,v183
v_add_f32 v184,1.0,v184
v_add_f32 v185,1.0,v185
v_add_f32 v186,1.0,v186
v_add_f32 v187,1.0,v187
v_add_f32 v188,1.0,v188
v_add_f32 v189,1.0,v189
v_add_f32 v190,1.0,v190
v_add_f32 v191,1.0,v191
s_setprio 1
s_waitcnt lgkmcnt(0)
v_mfma_f32_32x32x16_bf16 v[160:175],v[200:203],v[72:75],v[160:175]
v_rcp_f32 v176,v176
v_rcp_f32 v177,v177
v_rcp_f32 v178,v178
v_mfma_f32_32x32x16_bf16 v[160:175],v[204:207],v[76:79],v[160:175]
v_rcp_f32 v179,v179
v_rcp_f32 v180,v180
v_rcp_f32 v181,v181
v_mfma_f32_32x32x16_bf16 v[160:175],v[208:211],v[80:83],v[160:175]
v_rcp_f32 v182,v182
v_rcp_f32 v183,v183
v_rcp_f32 v184,v184
v_mfma_f32_32x32x16_bf16 v[160:175],v[212:215],v[84:87],v[160:175]
v_rcp_f32 v185,v185
v_rcp_f32 v186,v186
v_rcp_f32 v187,v187
v_mfma_f32_32x32x16_bf16 v[160:175],v[216:219],v[88:91],v[160:175]
v_rcp_f32 v188,v188
v_rcp_f32 v189,v189
v_mfma_f32_32x32x16_bf16 v[160:175],v[220:223],v[92:95],v[160:175]
v_rcp_f32 v190,v190
v_rcp_f32 v191,v191
ds_read_b64_tr_b16 v[200:201],v17 offset:0
ds_read_b64_tr_b16 v[202:203],v17 offset:1536
ds_read_b64_tr_b16 v[204:205],v17 offset:3072
ds_read_b64_tr_b16 v[206:207],v17 offset:4608
ds_read_b64_tr_b16 v[208:209],v17 offset:64
ds_read_b64_tr_b16 v[210:211],v17 offset:1600
ds_read_b64_tr_b16 v[212:213],v17 offset:3136
ds_read_b64_tr_b16 v[214:215],v17 offset:4672
ds_read_b64_tr_b16 v[216:217],v17 offset:128
ds_read_b64_tr_b16 v[218:219],v17 offset:1664
ds_read_b64_tr_b16 v[220:221],v17 offset:3200
ds_read_b64_tr_b16 v[222:223],v17 offset:4736
s_mov_b64 exec,s[62:63]
s_add_u32 m0,s68,3456
s_add_u32 s64,s59,98304
buffer_load_dwordx4 v27,s[36:39],s64 offen lds
s_mov_b64 exec,-1
s_setprio 0
v_mul_f32 v176,v144,v176
v_mul_f32 v177,v145,v177
v_mul_f32 v178,v146,v178
v_mul_f32 v179,v147,v179
v_mul_f32 v180,v148,v180
v_mul_f32 v181,v149,v181
v_mul_f32 v182,v150,v182
v_mul_f32 v183,v151,v183
v_mul_f32 v184,v152,v184
v_mul_f32 v185,v153,v185
v_mul_f32 v186,v154,v186
v_mul_f32 v187,v155,v187
v_mul_f32 v188,v156,v188
v_mul_f32 v189,v157,v189
v_mul_f32 v190,v158,v190
v_mul_f32 v191,v159,v191
v_cvt_pk_bf16_f32 v192,v176,v177
v_cvt_pk_bf16_f32 v193,v178,v179
v_cvt_pk_bf16_f32 v194,v180,v181
v_cvt_pk_bf16_f32 v195,v182,v183
v_cvt_pk_bf16_f32 v196,v184,v185
v_cvt_pk_bf16_f32 v197,v186,v187
v_cvt_pk_bf16_f32 v198,v188,v189
v_cvt_pk_bf16_f32 v199,v190,v191
s_waitcnt lgkmcnt(0)
s_barrier
v_fmaak_f32 v176,v160,v160,0x409d30fc
v_fmaak_f32 v177,v161,v161,0x409d30fc
v_fmaak_f32 v178,v162,v162,0x409d30fc
v_fmaak_f32 v179,v163,v163,0x409d30fc
v_fmaak_f32 v180,v164,v164,0x409d30fc
v_fmaak_f32 v181,v165,v165,0x409d30fc
v_fmaak_f32 v182,v166,v166,0x409d30fc
v_fmaak_f32 v183,v167,v167,0x409d30fc
v_fmaak_f32 v184,v168,v168,0x409d30fc
v_fmaak_f32 v185,v169,v169,0x409d30fc
v_fmaak_f32 v186,v170,v170,0x409d30fc
v_fmaak_f32 v187,v171,v171,0x409d30fc
v_fmaak_f32 v188,v172,v172,0x409d30fc
v_fmaak_f32 v189,v173,v173,0x409d30fc
v_fmaak_f32 v190,v174,v174,0x409d30fc
v_fmaak_f32 v191,v175,v175,0x409d30fc
v_mul_f32 v176,v160,v176
v_mul_f32 v177,v161,v177
v_mul_f32 v178,v162,v178
v_mul_f32 v179,v163,v179
v_mul_f32 v180,v164,v180
v_mul_f32 v181,v165,v181
v_mul_f32 v182,v166,v182
v_mul_f32 v183,v167,v183
v_mul_f32 v184,v168,v184
v_mul_f32 v185,v169,v185
v_mul_f32 v186,v170,v186
v_mul_f32 v187,v171,v187
v_mul_f32 v188,v172,v188
v_mul_f32 v189,v173,v189
v_mul_f32 v190,v174,v190
v_mul_f32 v191,v175,v191
s_setprio 1
v_mfma_f32_32x32x16_bf16 v[96:111],v[200:203],v[192:195],v[96:111]
v_exp_f32 v176,v176
v_exp_f32 v177,v177
v_exp_f32 v178,v178
v_mfma_f32_32x32x16_bf16 v[96:111],v[204:207],v[196:199],v[96:111]
v_exp_f32 v179,v179
v_exp_f32 v180,v180
v_exp_f32 v181,v181
v_mfma_f32_32x32x16_bf16 v[112:127],v[208:211],v[192:195],v[112:127]
v_exp_f32 v182,v182
v_exp_f32 v183,v183
v_exp_f32 v184,v184
v_mfma_f32_32x32x16_bf16 v[112:127],v[212:215],v[196:199],v[112:127]
v_exp_f32 v185,v185
v_exp_f32 v186,v186
v_exp_f32 v187,v187
v_mfma_f32_32x32x16_bf16 v[128:143],v[216:219],v[192:195],v[128:143]
v_exp_f32 v188,v188
v_exp_f32 v189,v189
v_mfma_f32_32x32x16_bf16 v[128:143],v[220:223],v[196:199],v[128:143]
v_exp_f32 v190,v190
v_exp_f32 v191,v191
ds_read_b64_tr_b16 v[200:201],v16 offset:12288
ds_read_b64_tr_b16 v[202:203],v16 offset:12800
ds_read_b64_tr_b16 v[204:205],v16 offset:13312
ds_read_b64_tr_b16 v[206:207],v16 offset:13824
ds_read_b64_tr_b16 v[208:209],v16 offset:14336
ds_read_b64_tr_b16 v[210:211],v16 offset:14848
ds_read_b64_tr_b16 v[212:213],v16 offset:15360
ds_read_b64_tr_b16 v[214:215],v16 offset:15872
ds_read_b64_tr_b16 v[216:217],v16 offset:16384
ds_read_b64_tr_b16 v[218:219],v16 offset:16896
ds_read_b64_tr_b16 v[220:221],v16 offset:17408
ds_read_b64_tr_b16 v[222:223],v16 offset:17920
ds_read_b128 v[144:147],v18 offset:256
ds_read_b128 v[148:151],v18 offset:288
ds_read_b128 v[152:155],v18 offset:320
ds_read_b128 v[156:159],v18 offset:352
s_mov_b64 exec,s[62:63]
s_add_u32 m0,s68,6912
s_add_u32 s64,s59,196608
buffer_load_dwordx4 v27,s[36:39],s64 offen lds
s_mov_b64 exec,-1
s_setprio 0
v_add_f32 v176,1.0,v176
v_add_f32 v177,1.0,v177
v_add_f32 v178,1.0,v178
v_add_f32 v179,1.0,v179
v_add_f32 v180,1.0,v180
v_add_f32 v181,1.0,v181
v_add_f32 v182,1.0,v182
v_add_f32 v183,1.0,v183
v_add_f32 v184,1.0,v184
v_add_f32 v185,1.0,v185
v_add_f32 v186,1.0,v186
v_add_f32 v187,1.0,v187
v_add_f32 v188,1.0,v188
v_add_f32 v189,1.0,v189
v_add_f32 v190,1.0,v190
v_add_f32 v191,1.0,v191
s_setprio 1
s_waitcnt lgkmcnt(0)
v_mfma_f32_32x32x16_bf16 v[144:159],v[200:203],v[72:75],v[144:159]
v_rcp_f32 v176,v176
v_rcp_f32 v177,v177
v_rcp_f32 v178,v178
v_mfma_f32_32x32x16_bf16 v[144:159],v[204:207],v[76:79],v[144:159]
v_rcp_f32 v179,v179
v_rcp_f32 v180,v180
v_rcp_f32 v181,v181
v_mfma_f32_32x32x16_bf16 v[144:159],v[208:211],v[80:83],v[144:159]
v_rcp_f32 v182,v182
v_rcp_f32 v183,v183
v_rcp_f32 v184,v184
v_mfma_f32_32x32x16_bf16 v[144:159],v[212:215],v[84:87],v[144:159]
v_rcp_f32 v185,v185
v_rcp_f32 v186,v186
v_rcp_f32 v187,v187
v_mfma_f32_32x32x16_bf16 v[144:159],v[216:219],v[88:91],v[144:159]
v_rcp_f32 v188,v188
v_rcp_f32 v189,v189
v_mfma_f32_32x32x16_bf16 v[144:159],v[220:223],v[92:95],v[144:159]
v_rcp_f32 v190,v190
v_rcp_f32 v191,v191
ds_read_b64_tr_b16 v[200:201],v17 offset:6144
ds_read_b64_tr_b16 v[202:203],v17 offset:7680
ds_read_b64_tr_b16 v[204:205],v17 offset:9216
ds_read_b64_tr_b16 v[206:207],v17 offset:10752
ds_read_b64_tr_b16 v[208:209],v17 offset:6208
ds_read_b64_tr_b16 v[210:211],v17 offset:7744
ds_read_b64_tr_b16 v[212:213],v17 offset:9280
ds_read_b64_tr_b16 v[214:215],v17 offset:10816
ds_read_b64_tr_b16 v[216:217],v17 offset:6272
ds_read_b64_tr_b16 v[218:219],v17 offset:7808
ds_read_b64_tr_b16 v[220:221],v17 offset:9344
ds_read_b64_tr_b16 v[222:223],v17 offset:10880
s_mov_b64 exec,s[62:63]
s_add_u32 m0,s68,10368
s_add_u32 s64,s59,294912
buffer_load_dwordx4 v27,s[36:39],s64 offen lds
s_mov_b64 exec,-1
s_setprio 0
v_mul_f32 v176,v160,v176
v_mul_f32 v177,v161,v177
v_mul_f32 v178,v162,v178
v_mul_f32 v179,v163,v179
v_mul_f32 v180,v164,v180
v_mul_f32 v181,v165,v181
v_mul_f32 v182,v166,v182
v_mul_f32 v183,v167,v183
v_mul_f32 v184,v168,v184
v_mul_f32 v185,v169,v185
v_mul_f32 v186,v170,v186
v_mul_f32 v187,v171,v187
v_mul_f32 v188,v172,v188
v_mul_f32 v189,v173,v189
v_mul_f32 v190,v174,v190
v_mul_f32 v191,v175,v191
v_cvt_pk_bf16_f32 v192,v176,v177
v_cvt_pk_bf16_f32 v193,v178,v179
v_cvt_pk_bf16_f32 v194,v180,v181
v_cvt_pk_bf16_f32 v195,v182,v183
v_cvt_pk_bf16_f32 v196,v184,v185
v_cvt_pk_bf16_f32 v197,v186,v187
v_cvt_pk_bf16_f32 v198,v188,v189
v_cvt_pk_bf16_f32 v199,v190,v191
s_waitcnt lgkmcnt(0)
s_barrier
v_fmaak_f32 v176,v144,v144,0x409d30fc
v_fmaak_f32 v177,v145,v145,0x409d30fc
v_fmaak_f32 v178,v146,v146,0x409d30fc
v_fmaak_f32 v179,v147,v147,0x409d30fc
v_fmaak_f32 v180,v148,v148,0x409d30fc
v_fmaak_f32 v181,v149,v149,0x409d30fc
v_fmaak_f32 v182,v150,v150,0x409d30fc
v_fmaak_f32 v183,v151,v151,0x409d30fc
v_fmaak_f32 v184,v152,v152,0x409d30fc
v_fmaak_f32 v185,v153,v153,0x409d30fc
v_fmaak_f32 v186,v154,v154,0x409d30fc
v_fmaak_f32 v187,v155,v155,0x409d30fc
v_fmaak_f32 v188,v156,v156,0x409d30fc
v_fmaak_f32 v189,v157,v157,0x409d30fc
v_fmaak_f32 v190,v158,v158,0x409d30fc
v_fmaak_f32 v191,v159,v159,0x409d30fc
v_mul_f32 v176,v144,v176
v_mul_f32 v177,v145,v177
v_mul_f32 v178,v146,v178
v_mul_f32 v179,v147,v179
v_mul_f32 v180,v148,v180
v_mul_f32 v181,v149,v181
v_mul_f32 v182,v150,v182
v_mul_f32 v183,v151,v183
v_mul_f32 v184,v152,v184
v_mul_f32 v185,v153,v185
v_mul_f32 v186,v154,v186
v_mul_f32 v187,v155,v187
v_mul_f32 v188,v156,v188
v_mul_f32 v189,v157,v189
v_mul_f32 v190,v158,v190
v_mul_f32 v191,v159,v191
s_setprio 1
v_mfma_f32_32x32x16_bf16 v[96:111],v[200:203],v[192:195],v[96:111]
v_exp_f32 v176,v176
v_exp_f32 v177,v177
v_exp_f32 v178,v178
v_mfma_f32_32x32x16_bf16 v[96:111],v[204:207],v[196:199],v[96:111]
v_exp_f32 v179,v179
v_exp_f32 v180,v180
v_exp_f32 v181,v181
v_mfma_f32_32x32x16_bf16 v[112:127],v[208:211],v[192:195],v[112:127]
v_exp_f32 v182,v182
v_exp_f32 v183,v183
v_exp_f32 v184,v184
v_mfma_f32_32x32x16_bf16 v[112:127],v[212:215],v[196:199],v[112:127]
v_exp_f32 v185,v185
v_exp_f32 v186,v186
v_exp_f32 v187,v187
v_mfma_f32_32x32x16_bf16 v[128:143],v[216:219],v[192:195],v[128:143]
v_exp_f32 v188,v188
v_exp_f32 v189,v189
v_mfma_f32_32x32x16_bf16 v[128:143],v[220:223],v[196:199],v[128:143]
v_exp_f32 v190,v190
v_exp_f32 v191,v191
ds_read_b64_tr_b16 v[200:201],v16 offset:0
ds_read_b64_tr_b16 v[202:203],v16 offset:512
ds_read_b64_tr_b16 v[204:205],v16 offset:1024
ds_read_b64_tr_b16 v[206:207],v16 offset:1536
ds_read_b64_tr_b16 v[208:209],v16 offset:2048
ds_read_b64_tr_b16 v[210:211],v16 offset:2560
ds_read_b64_tr_b16 v[212:213],v16 offset:3072
ds_read_b64_tr_b16 v[214:215],v16 offset:3584
ds_read_b64_tr_b16 v[216:217],v16 offset:4096
ds_read_b64_tr_b16 v[218:219],v16 offset:4608
ds_read_b64_tr_b16 v[220:221],v16 offset:5120
ds_read_b64_tr_b16 v[222:223],v16 offset:5632
ds_read_b128 v[160:163],v18 offset:384
ds_read_b128 v[164:167],v18 offset:416
ds_read_b128 v[168:171],v18 offset:448
ds_read_b128 v[172:175],v18 offset:480
s_mov_b64 exec,s[62:63]
s_add_u32 m0,s68,13824
s_add_u32 s64,s59,393216
buffer_load_dwordx4 v27,s[36:39],s64 offen lds
s_mov_b64 exec,-1
s_setprio 0
v_add_f32 v176,1.0,v176
v_add_f32 v177,1.0,v177
v_add_f32 v178,1.0,v178
v_add_f32 v179,1.0,v179
v_add_f32 v180,1.0,v180
v_add_f32 v181,1.0,v181
v_add_f32 v182,1.0,v182
v_add_f32 v183,1.0,v183
v_add_f32 v184,1.0,v184
v_add_f32 v185,1.0,v185
v_add_f32 v186,1.0,v186
v_add_f32 v187,1.0,v187
v_add_f32 v188,1.0,v188
v_add_f32 v189,1.0,v189
v_add_f32 v190,1.0,v190
v_add_f32 v191,1.0,v191
s_setprio 1
s_waitcnt lgkmcnt(0)
v_mfma_f32_32x32x16_bf16 v[160:175],v[200:203],v[72:75],v[160:175]
v_rcp_f32 v176,v176
v_rcp_f32 v177,v177
v_rcp_f32 v178,v178
v_mfma_f32_32x32x16_bf16 v[160:175],v[204:207],v[76:79],v[160:175]
v_rcp_f32 v179,v179
v_rcp_f32 v180,v180
v_rcp_f32 v181,v181
v_mfma_f32_32x32x16_bf16 v[160:175],v[208:211],v[80:83],v[160:175]
v_rcp_f32 v182,v182
v_rcp_f32 v183,v183
v_rcp_f32 v184,v184
v_mfma_f32_32x32x16_bf16 v[160:175],v[212:215],v[84:87],v[160:175]
v_rcp_f32 v185,v185
v_rcp_f32 v186,v186
v_rcp_f32 v187,v187
v_mfma_f32_32x32x16_bf16 v[160:175],v[216:219],v[88:91],v[160:175]
v_rcp_f32 v188,v188
v_rcp_f32 v189,v189
v_mfma_f32_32x32x16_bf16 v[160:175],v[220:223],v[92:95],v[160:175]
v_rcp_f32 v190,v190
v_rcp_f32 v191,v191
ds_read_b64_tr_b16 v[200:201],v17 offset:12288
ds_read_b64_tr_b16 v[202:203],v17 offset:13824
ds_read_b64_tr_b16 v[204:205],v17 offset:15360
ds_read_b64_tr_b16 v[206:207],v17 offset:16896
ds_read_b64_tr_b16 v[208:209],v17 offset:12352
ds_read_b64_tr_b16 v[210:211],v17 offset:13888
ds_read_b64_tr_b16 v[212:213],v17 offset:15424
ds_read_b64_tr_b16 v[214:215],v17 offset:16960
ds_read_b64_tr_b16 v[216:217],v17 offset:12416
ds_read_b64_tr_b16 v[218:219],v17 offset:13952
ds_read_b64_tr_b16 v[220:221],v17 offset:15488
ds_read_b64_tr_b16 v[222:223],v17 offset:17024
s_mov_b64 exec,s[62:63]
s_add_u32 m0,s68,17280
s_add_u32 s64,s59,491520
buffer_load_dwordx4 v27,s[36:39],s64 offen lds
s_mov_b64 exec,-1
s_setprio 0
v_mul_f32 v176,v144,v176
v_mul_f32 v177,v145,v177
v_mul_f32 v178,v146,v178
v_mul_f32 v179,v147,v179
v_mul_f32 v180,v148,v180
v_mul_f32 v181,v149,v181
v_mul_f32 v182,v150,v182
v_mul_f32 v183,v151,v183
v_mul_f32 v184,v152,v184
v_mul_f32 v185,v153,v185
v_mul_f32 v186,v154,v186
v_mul_f32 v187,v155,v187
v_mul_f32 v188,v156,v188
v_mul_f32 v189,v157,v189
v_mul_f32 v190,v158,v190
v_mul_f32 v191,v159,v191
v_cvt_pk_bf16_f32 v192,v176,v177
v_cvt_pk_bf16_f32 v193,v178,v179
v_cvt_pk_bf16_f32 v194,v180,v181
v_cvt_pk_bf16_f32 v195,v182,v183
v_cvt_pk_bf16_f32 v196,v184,v185
v_cvt_pk_bf16_f32 v197,v186,v187
v_cvt_pk_bf16_f32 v198,v188,v189
v_cvt_pk_bf16_f32 v199,v190,v191
s_waitcnt lgkmcnt(0)
s_barrier
v_fmaak_f32 v176,v160,v160,0x409d30fc
v_fmaak_f32 v177,v161,v161,0x409d30fc
v_fmaak_f32 v178,v162,v162,0x409d30fc
v_fmaak_f32 v179,v163,v163,0x409d30fc
v_fmaak_f32 v180,v164,v164,0x409d30fc
v_fmaak_f32 v181,v165,v165,0x409d30fc
v_fmaak_f32 v182,v166,v166,0x409d30fc
v_fmaak_f32 v183,v167,v167,0x409d30fc
v_fmaak_f32 v184,v168,v168,0x409d30fc
v_fmaak_f32 v185,v169,v169,0x409d30fc
v_fmaak_f32 v186,v170,v170,0x409d30fc
v_fmaak_f32 v187,v171,v171,0x409d30fc
v_fmaak_f32 v188,v172,v172,0x409d30fc
v_fmaak_f32 v189,v173,v173,0x409d30fc
v_fmaak_f32 v190,v174,v174,0x409d30fc
v_fmaak_f32 v191,v175,v175,0x409d30fc
v_mul_f32 v176,v160,v176
v_mul_f32 v177,v161,v177
v_mul_f32 v178,v162,v178
v_mul_f32 v179,v163,v179
v_mul_f32 v180,v164,v180
v_mul_f32 v181,v165,v181
v_mul_f32 v182,v166,v182
v_mul_f32 v183,v167,v183
v_mul_f32 v184,v168,v184
v_mul_f32 v185,v169,v185
v_mul_f32 v186,v170,v186
v_mul_f32 v187,v171,v187
v_mul_f32 v188,v172,v188
v_mul_f32 v189,v173,v189
v_mul_f32 v190,v174,v190
v_mul_f32 v191,v175,v191
s_setprio 1
v_mfma_f32_32x32x16_bf16 v[96:111],v[200:203],v[192:195],v[96:111]
v_exp_f32 v176,v176
v_exp_f32 v177,v177
v_exp_f32 v178,v178
v_mfma_f32_32x32x16_bf16 v[96:111],v[204:207],v[196:199],v[96:111]
v_exp_f32 v179,v179
v_exp_f32 v180,v180
v_exp_f32 v181,v181
v_mfma_f32_32x32x16_bf16 v[112:127],v[208:211],v[192:195],v[112:127]
v_exp_f32 v182,v182
v_exp_f32 v183,v183
v_exp_f32 v184,v184
v_mfma_f32_32x32x16_bf16 v[112:127],v[212:215],v[196:199],v[112:127]
v_exp_f32 v185,v185
v_exp_f32 v186,v186
v_exp_f32 v187,v187
v_mfma_f32_32x32x16_bf16 v[128:143],v[216:219],v[192:195],v[128:143]
v_exp_f32 v188,v188
v_exp_f32 v189,v189
v_mfma_f32_32x32x16_bf16 v[128:143],v[220:223],v[196:199],v[128:143]
v_exp_f32 v190,v190
v_exp_f32 v191,v191
ds_read_b64_tr_b16 v[200:201],v16 offset:6144
ds_read_b64_tr_b16 v[202:203],v16 offset:6656
ds_read_b64_tr_b16 v[204:205],v16 offset:7168
ds_read_b64_tr_b16 v[206:207],v16 offset:7680
ds_read_b64_tr_b16 v[208:209],v16 offset:8192
ds_read_b64_tr_b16 v[210:211],v16 offset:8704
ds_read_b64_tr_b16 v[212:213],v16 offset:9216
ds_read_b64_tr_b16 v[214:215],v16 offset:9728
ds_read_b64_tr_b16 v[216:217],v16 offset:10240
ds_read_b64_tr_b16 v[218:219],v16 offset:10752
ds_read_b64_tr_b16 v[220:221],v16 offset:11264
ds_read_b64_tr_b16 v[222:223],v16 offset:11776
ds_read_b128 v[144:147],v18 offset:512
ds_read_b128 v[148:151],v18 offset:544
ds_read_b128 v[152:155],v18 offset:576
ds_read_b128 v[156:159],v18 offset:608
s_mov_b64 exec,s[62:63]
s_add_u32 m0,s68,20736
s_add_u32 s64,s59,589824
buffer_load_dwordx4 v27,s[36:39],s64 offen lds
s_mov_b64 exec,-1
s_setprio 0
v_add_f32 v176,1.0,v176
v_add_f32 v177,1.0,v177
v_add_f32 v178,1.0,v178
v_add_f32 v179,1.0,v179
v_add_f32 v180,1.0,v180
v_add_f32 v181,1.0,v181
v_add_f32 v182,1.0,v182
v_add_f32 v183,1.0,v183
v_add_f32 v184,1.0,v184
v_add_f32 v185,1.0,v185
v_add_f32 v186,1.0,v186
v_add_f32 v187,1.0,v187
v_add_f32 v188,1.0,v188
v_add_f32 v189,1.0,v189
v_add_f32 v190,1.0,v190
v_add_f32 v191,1.0,v191
s_setprio 1
s_waitcnt lgkmcnt(0)
v_mfma_f32_32x32x16_bf16 v[144:159],v[200:203],v[72:75],v[144:159]
v_rcp_f32 v176,v176
v_rcp_f32 v177,v177
v_rcp_f32 v178,v178
v_mfma_f32_32x32x16_bf16 v[144:159],v[204:207],v[76:79],v[144:159]
v_rcp_f32 v179,v179
v_rcp_f32 v180,v180
v_rcp_f32 v181,v181
v_mfma_f32_32x32x16_bf16 v[144:159],v[208:211],v[80:83],v[144:159]
v_rcp_f32 v182,v182
v_rcp_f32 v183,v183
v_rcp_f32 v184,v184
v_mfma_f32_32x32x16_bf16 v[144:159],v[212:215],v[84:87],v[144:159]
v_rcp_f32 v185,v185
v_rcp_f32 v186,v186
v_rcp_f32 v187,v187
v_mfma_f32_32x32x16_bf16 v[144:159],v[216:219],v[88:91],v[144:159]
v_rcp_f32 v188,v188
v_rcp_f32 v189,v189
v_mfma_f32_32x32x16_bf16 v[144:159],v[220:223],v[92:95],v[144:159]
v_rcp_f32 v190,v190
v_rcp_f32 v191,v191
ds_read_b64_tr_b16 v[200:201],v17 offset:0
ds_read_b64_tr_b16 v[202:203],v17 offset:1536
ds_read_b64_tr_b16 v[204:205],v17 offset:3072
ds_read_b64_tr_b16 v[206:207],v17 offset:4608
ds_read_b64_tr_b16 v[208:209],v17 offset:64
ds_read_b64_tr_b16 v[210:211],v17 offset:1600
ds_read_b64_tr_b16 v[212:213],v17 offset:3136
ds_read_b64_tr_b16 v[214:215],v17 offset:4672
ds_read_b64_tr_b16 v[216:217],v17 offset:128
ds_read_b64_tr_b16 v[218:219],v17 offset:1664
ds_read_b64_tr_b16 v[220:221],v17 offset:3200
ds_read_b64_tr_b16 v[222:223],v17 offset:4736
s_mov_b64 exec,s[62:63]
s_add_u32 m0,s68,24192
s_add_u32 s64,s59,688128
buffer_load_dwordx4 v27,s[36:39],s64 offen lds
s_mov_b64 exec,-1
s_setprio 0
v_mul_f32 v176,v160,v176
v_mul_f32 v177,v161,v177
v_mul_f32 v178,v162,v178
v_mul_f32 v179,v163,v179
v_mul_f32 v180,v164,v180
v_mul_f32 v181,v165,v181
v_mul_f32 v182,v166,v182
v_mul_f32 v183,v167,v183
v_mul_f32 v184,v168,v184
v_mul_f32 v185,v169,v185
v_mul_f32 v186,v170,v186
v_mul_f32 v187,v171,v187
v_mul_f32 v188,v172,v188
v_mul_f32 v189,v173,v189
v_mul_f32 v190,v174,v190
v_mul_f32 v191,v175,v191
v_cvt_pk_bf16_f32 v192,v176,v177
v_cvt_pk_bf16_f32 v193,v178,v179
v_cvt_pk_bf16_f32 v194,v180,v181
v_cvt_pk_bf16_f32 v195,v182,v183
v_cvt_pk_bf16_f32 v196,v184,v185
v_cvt_pk_bf16_f32 v197,v186,v187
v_cvt_pk_bf16_f32 v198,v188,v189
v_cvt_pk_bf16_f32 v199,v190,v191
s_waitcnt lgkmcnt(0)
s_barrier
v_fmaak_f32 v176,v144,v144,0x409d30fc
v_fmaak_f32 v177,v145,v145,0x409d30fc
v_fmaak_f32 v178,v146,v146,0x409d30fc
v_fmaak_f32 v179,v147,v147,0x409d30fc
v_fmaak_f32 v180,v148,v148,0x409d30fc
v_fmaak_f32 v181,v149,v149,0x409d30fc
v_fmaak_f32 v182,v150,v150,0x409d30fc
v_fmaak_f32 v183,v151,v151,0x409d30fc
v_fmaak_f32 v184,v152,v152,0x409d30fc
v_fmaak_f32 v185,v153,v153,0x409d30fc
v_fmaak_f32 v186,v154,v154,0x409d30fc
v_fmaak_f32 v187,v155,v155,0x409d30fc
v_fmaak_f32 v188,v156,v156,0x409d30fc
v_fmaak_f32 v189,v157,v157,0x409d30fc
v_fmaak_f32 v190,v158,v158,0x409d30fc
v_fmaak_f32 v191,v159,v159,0x409d30fc
v_mul_f32 v176,v144,v176
v_mul_f32 v177,v145,v177
v_mul_f32 v178,v146,v178
v_mul_f32 v179,v147,v179
v_mul_f32 v180,v148,v180
v_mul_f32 v181,v149,v181
v_mul_f32 v182,v150,v182
v_mul_f32 v183,v151,v183
v_mul_f32 v184,v152,v184
v_mul_f32 v185,v153,v185
v_mul_f32 v186,v154,v186
v_mul_f32 v187,v155,v187
v_mul_f32 v188,v156,v188
v_mul_f32 v189,v157,v189
v_mul_f32 v190,v158,v190
v_mul_f32 v191,v159,v191
s_setprio 1
v_mfma_f32_32x32x16_bf16 v[96:111],v[200:203],v[192:195],v[96:111]
v_exp_f32 v176,v176
v_exp_f32 v177,v177
v_exp_f32 v178,v178
v_mfma_f32_32x32x16_bf16 v[96:111],v[204:207],v[196:199],v[96:111]
v_exp_f32 v179,v179
v_exp_f32 v180,v180
v_exp_f32 v181,v181
v_mfma_f32_32x32x16_bf16 v[112:127],v[208:211],v[192:195],v[112:127]
v_exp_f32 v182,v182
v_exp_f32 v183,v183
v_exp_f32 v184,v184
v_mfma_f32_32x32x16_bf16 v[112:127],v[212:215],v[196:199],v[112:127]
v_exp_f32 v185,v185
v_exp_f32 v186,v186
v_exp_f32 v187,v187
v_mfma_f32_32x32x16_bf16 v[128:143],v[216:219],v[192:195],v[128:143]
v_exp_f32 v188,v188
v_exp_f32 v189,v189
v_mfma_f32_32x32x16_bf16 v[128:143],v[220:223],v[196:199],v[128:143]
v_exp_f32 v190,v190
v_exp_f32 v191,v191
ds_read_b64_tr_b16 v[200:201],v16 offset:12288
ds_read_b64_tr_b16 v[202:203],v16 offset:12800
ds_read_b64_tr_b16 v[204:205],v16 offset:13312
ds_read_b64_tr_b16 v[206:207],v16 offset:13824
ds_read_b64_tr_b16 v[208:209],v16 offset:14336
ds_read_b64_tr_b16 v[210:211],v16 offset:14848
ds_read_b64_tr_b16 v[212:213],v16 offset:15360
ds_read_b64_tr_b16 v[214:215],v16 offset:15872
ds_read_b64_tr_b16 v[216:217],v16 offset:16384
ds_read_b64_tr_b16 v[218:219],v16 offset:16896
ds_read_b64_tr_b16 v[220:221],v16 offset:17408
ds_read_b64_tr_b16 v[222:223],v16 offset:17920
ds_read_b128 v[160:163],v18 offset:640
ds_read_b128 v[164:167],v18 offset:672
ds_read_b128 v[168:171],v18 offset:704
ds_read_b128 v[172:175],v18 offset:736
s_mov_b64 exec,s[62:63]
s_add_u32 m0,s68,27648
s_add_u32 s64,s59,786432
buffer_load_dwordx4 v27,s[36:39],s64 offen lds
s_mov_b64 exec,-1
s_setprio 0
v_add_f32 v176,1.0,v176
v_add_f32 v177,1.0,v177
v_add_f32 v178,1.0,v178
v_add_f32 v179,1.0,v179
v_add_f32 v180,1.0,v180
v_add_f32 v181,1.0,v181
v_add_f32 v182,1.0,v182
v_add_f32 v183,1.0,v183
v_add_f32 v184,1.0,v184
v_add_f32 v185,1.0,v185
v_add_f32 v186,1.0,v186
v_add_f32 v187,1.0,v187
v_add_f32 v188,1.0,v188
v_add_f32 v189,1.0,v189
v_add_f32 v190,1.0,v190
v_add_f32 v191,1.0,v191
s_setprio 1
s_waitcnt lgkmcnt(0)
v_mfma_f32_32x32x16_bf16 v[160:175],v[200:203],v[72:75],v[160:175]
v_rcp_f32 v176,v176
v_rcp_f32 v177,v177
v_rcp_f32 v178,v178
v_mfma_f32_32x32x16_bf16 v[160:175],v[204:207],v[76:79],v[160:175]
v_rcp_f32 v179,v179
v_rcp_f32 v180,v180
v_rcp_f32 v181,v181
v_mfma_f32_32x32x16_bf16 v[160:175],v[208:211],v[80:83],v[160:175]
v_rcp_f32 v182,v182
v_rcp_f32 v183,v183
v_rcp_f32 v184,v184
v_mfma_f32_32x32x16_bf16 v[160:175],v[212:215],v[84:87],v[160:175]
v_rcp_f32 v185,v185
v_rcp_f32 v186,v186
v_rcp_f32 v187,v187
v_mfma_f32_32x32x16_bf16 v[160:175],v[216:219],v[88:91],v[160:175]
v_rcp_f32 v188,v188
v_rcp_f32 v189,v189
v_mfma_f32_32x32x16_bf16 v[160:175],v[220:223],v[92:95],v[160:175]
v_rcp_f32 v190,v190
v_rcp_f32 v191,v191
ds_read_b64_tr_b16 v[200:201],v17 offset:6144
ds_read_b64_tr_b16 v[202:203],v17 offset:7680
ds_read_b64_tr_b16 v[204:205],v17 offset:9216
ds_read_b64_tr_b16 v[206:207],v17 offset:10752
ds_read_b64_tr_b16 v[208:209],v17 offset:6208
ds_read_b64_tr_b16 v[210:211],v17 offset:7744
ds_read_b64_tr_b16 v[212:213],v17 offset:9280
ds_read_b64_tr_b16 v[214:215],v17 offset:10816
ds_read_b64_tr_b16 v[216:217],v17 offset:6272
ds_read_b64_tr_b16 v[218:219],v17 offset:7808
ds_read_b64_tr_b16 v[220:221],v17 offset:9344
ds_read_b64_tr_b16 v[222:223],v17 offset:10880
s_mov_b64 exec,s[62:63]
s_add_u32 m0,s68,31104
s_add_u32 s64,s59,884736
buffer_load_dwordx4 v27,s[36:39],s64 offen lds
s_mov_b64 exec,-1
s_setprio 0
v_mul_f32 v176,v144,v176
v_mul_f32 v177,v145,v177
v_mul_f32 v178,v146,v178
v_mul_f32 v179,v147,v179
v_mul_f32 v180,v148,v180
v_mul_f32 v181,v149,v181
v_mul_f32 v182,v150,v182
v_mul_f32 v183,v151,v183
v_mul_f32 v184,v152,v184
v_mul_f32 v185,v153,v185
v_mul_f32 v186,v154,v186
v_mul_f32 v187,v155,v187
v_mul_f32 v188,v156,v188
v_mul_f32 v189,v157,v189
v_mul_f32 v190,v158,v190
v_mul_f32 v191,v159,v191
v_cvt_pk_bf16_f32 v192,v176,v177
v_cvt_pk_bf16_f32 v193,v178,v179
v_cvt_pk_bf16_f32 v194,v180,v181
v_cvt_pk_bf16_f32 v195,v182,v183
v_cvt_pk_bf16_f32 v196,v184,v185
v_cvt_pk_bf16_f32 v197,v186,v187
v_cvt_pk_bf16_f32 v198,v188,v189
v_cvt_pk_bf16_f32 v199,v190,v191
s_waitcnt lgkmcnt(0)
s_barrier
v_fmaak_f32 v176,v160,v160,0x409d30fc
v_fmaak_f32 v177,v161,v161,0x409d30fc
v_fmaak_f32 v178,v162,v162,0x409d30fc
v_fmaak_f32 v179,v163,v163,0x409d30fc
v_fmaak_f32 v180,v164,v164,0x409d30fc
v_fmaak_f32 v181,v165,v165,0x409d30fc
v_fmaak_f32 v182,v166,v166,0x409d30fc
v_fmaak_f32 v183,v167,v167,0x409d30fc
v_fmaak_f32 v184,v168,v168,0x409d30fc
v_fmaak_f32 v185,v169,v169,0x409d30fc
v_fmaak_f32 v186,v170,v170,0x409d30fc
v_fmaak_f32 v187,v171,v171,0x409d30fc
v_fmaak_f32 v188,v172,v172,0x409d30fc
v_fmaak_f32 v189,v173,v173,0x409d30fc
v_fmaak_f32 v190,v174,v174,0x409d30fc
v_fmaak_f32 v191,v175,v175,0x409d30fc
v_mul_f32 v176,v160,v176
v_mul_f32 v177,v161,v177
v_mul_f32 v178,v162,v178
v_mul_f32 v179,v163,v179
v_mul_f32 v180,v164,v180
v_mul_f32 v181,v165,v181
v_mul_f32 v182,v166,v182
v_mul_f32 v183,v167,v183
v_mul_f32 v184,v168,v184
v_mul_f32 v185,v169,v185
v_mul_f32 v186,v170,v186
v_mul_f32 v187,v171,v187
v_mul_f32 v188,v172,v188
v_mul_f32 v189,v173,v189
v_mul_f32 v190,v174,v190
v_mul_f32 v191,v175,v191
s_setprio 1
v_mfma_f32_32x32x16_bf16 v[96:111],v[200:203],v[192:195],v[96:111]
v_exp_f32 v176,v176
v_exp_f32 v177,v177
v_exp_f32 v178,v178
v_mfma_f32_32x32x16_bf16 v[96:111],v[204:207],v[196:199],v[96:111]
v_exp_f32 v179,v179
v_exp_f32 v180,v180
v_exp_f32 v181,v181
v_mfma_f32_32x32x16_bf16 v[112:127],v[208:211],v[192:195],v[112:127]
v_exp_f32 v182,v182
v_exp_f32 v183,v183
v_exp_f32 v184,v184
v_mfma_f32_32x32x16_bf16 v[112:127],v[212:215],v[196:199],v[112:127]
v_exp_f32 v185,v185
v_exp_f32 v186,v186
v_exp_f32 v187,v187
v_mfma_f32_32x32x16_bf16 v[128:143],v[216:219],v[192:195],v[128:143]
v_exp_f32 v188,v188
v_exp_f32 v189,v189
v_mfma_f32_32x32x16_bf16 v[128:143],v[220:223],v[196:199],v[128:143]
v_exp_f32 v190,v190
v_exp_f32 v191,v191
ds_read_b64_tr_b16 v[200:201],v16 offset:0
ds_read_b64_tr_b16 v[202:203],v16 offset:512
ds_read_b64_tr_b16 v[204:205],v16 offset:1024
ds_read_b64_tr_b16 v[206:207],v16 offset:1536
ds_read_b64_tr_b16 v[208:209],v16 offset:2048
ds_read_b64_tr_b16 v[210:211],v16 offset:2560
ds_read_b64_tr_b16 v[212:213],v16 offset:3072
ds_read_b64_tr_b16 v[214:215],v16 offset:3584
ds_read_b64_tr_b16 v[216:217],v16 offset:4096
ds_read_b64_tr_b16 v[218:219],v16 offset:4608
ds_read_b64_tr_b16 v[220:221],v16 offset:5120
ds_read_b64_tr_b16 v[222:223],v16 offset:5632
ds_read_b128 v[144:147],v18 offset:768
ds_read_b128 v[148:151],v18 offset:800
ds_read_b128 v[152:155],v18 offset:832
ds_read_b128 v[156:159],v18 offset:864
s_mov_b64 exec,s[62:63]
s_add_u32 m0,s68,34560
s_add_u32 s64,s59,983040
buffer_load_dwordx4 v27,s[36:39],s64 offen lds
s_mov_b64 exec,-1
s_setprio 0
v_add_f32 v176,1.0,v176
v_add_f32 v177,1.0,v177
v_add_f32 v178,1.0,v178
v_add_f32 v179,1.0,v179
v_add_f32 v180,1.0,v180
v_add_f32 v181,1.0,v181
v_add_f32 v182,1.0,v182
v_add_f32 v183,1.0,v183
v_add_f32 v184,1.0,v184
v_add_f32 v185,1.0,v185
v_add_f32 v186,1.0,v186
v_add_f32 v187,1.0,v187
v_add_f32 v188,1.0,v188
v_add_f32 v189,1.0,v189
v_add_f32 v190,1.0,v190
v_add_f32 v191,1.0,v191
s_setprio 1
s_waitcnt lgkmcnt(0)
v_mfma_f32_32x32x16_bf16 v[144:159],v[200:203],v[72:75],v[144:159]
v_rcp_f32 v176,v176
v_rcp_f32 v177,v177
v_rcp_f32 v178,v178
v_mfma_f32_32x32x16_bf16 v[144:159],v[204:207],v[76:79],v[144:159]
v_rcp_f32 v179,v179
v_rcp_f32 v180,v180
v_rcp_f32 v181,v181
v_mfma_f32_32x32x16_bf16 v[144:159],v[208:211],v[80:83],v[144:159]
v_rcp_f32 v182,v182
v_rcp_f32 v183,v183
v_rcp_f32 v184,v184
v_mfma_f32_32x32x16_bf16 v[144:159],v[212:215],v[84:87],v[144:159]
v_rcp_f32 v185,v185
v_rcp_f32 v186,v186
v_rcp_f32 v187,v187
v_mfma_f32_32x32x16_bf16 v[144:159],v[216:219],v[88:91],v[144:159]
v_rcp_f32 v188,v188
v_rcp_f32 v189,v189
v_mfma_f32_32x32x16_bf16 v[144:159],v[220:223],v[92:95],v[144:159]
v_rcp_f32 v190,v190
v_rcp_f32 v191,v191
ds_read_b64_tr_b16 v[200:201],v17 offset:12288
ds_read_b64_tr_b16 v[202:203],v17 offset:13824
ds_read_b64_tr_b16 v[204:205],v17 offset:15360
ds_read_b64_tr_b16 v[206:207],v17 offset:16896
ds_read_b64_tr_b16 v[208:209],v17 offset:12352
ds_read_b64_tr_b16 v[210:211],v17 offset:13888
ds_read_b64_tr_b16 v[212:213],v17 offset:15424
ds_read_b64_tr_b16 v[214:215],v17 offset:16960
ds_read_b64_tr_b16 v[216:217],v17 offset:12416
ds_read_b64_tr_b16 v[218:219],v17 offset:13952
ds_read_b64_tr_b16 v[220:221],v17 offset:15488
ds_read_b64_tr_b16 v[222:223],v17 offset:17024
s_mov_b64 exec,s[62:63]
s_add_u32 m0,s68,38016
s_add_u32 s64,s59,1081344
buffer_load_dwordx4 v27,s[36:39],s64 offen lds
s_mov_b64 exec,-1
s_setprio 0
v_mul_f32 v176,v160,v176
v_mul_f32 v177,v161,v177
v_mul_f32 v178,v162,v178
v_mul_f32 v179,v163,v179
v_mul_f32 v180,v164,v180
v_mul_f32 v181,v165,v181
v_mul_f32 v182,v166,v182
v_mul_f32 v183,v167,v183
v_mul_f32 v184,v168,v184
v_mul_f32 v185,v169,v185
v_mul_f32 v186,v170,v186
v_mul_f32 v187,v171,v187
v_mul_f32 v188,v172,v188
v_mul_f32 v189,v173,v189
v_mul_f32 v190,v174,v190
v_mul_f32 v191,v175,v191
v_cvt_pk_bf16_f32 v192,v176,v177
v_cvt_pk_bf16_f32 v193,v178,v179
v_cvt_pk_bf16_f32 v194,v180,v181
v_cvt_pk_bf16_f32 v195,v182,v183
v_cvt_pk_bf16_f32 v196,v184,v185
v_cvt_pk_bf16_f32 v197,v186,v187
v_cvt_pk_bf16_f32 v198,v188,v189
v_cvt_pk_bf16_f32 v199,v190,v191
s_waitcnt lgkmcnt(0)
s_barrier
v_fmaak_f32 v176,v144,v144,0x409d30fc
v_fmaak_f32 v177,v145,v145,0x409d30fc
v_fmaak_f32 v178,v146,v146,0x409d30fc
v_fmaak_f32 v179,v147,v147,0x409d30fc
v_fmaak_f32 v180,v148,v148,0x409d30fc
v_fmaak_f32 v181,v149,v149,0x409d30fc
v_fmaak_f32 v182,v150,v150,0x409d30fc
v_fmaak_f32 v183,v151,v151,0x409d30fc
v_fmaak_f32 v184,v152,v152,0x409d30fc
v_fmaak_f32 v185,v153,v153,0x409d30fc
v_fmaak_f32 v186,v154,v154,0x409d30fc
v_fmaak_f32 v187,v155,v155,0x409d30fc
v_fmaak_f32 v188,v156,v156,0x409d30fc
v_fmaak_f32 v189,v157,v157,0x409d30fc
v_fmaak_f32 v190,v158,v158,0x409d30fc
v_fmaak_f32 v191,v159,v159,0x409d30fc
v_mul_f32 v176,v144,v176
v_mul_f32 v177,v145,v177
v_mul_f32 v178,v146,v178
v_mul_f32 v179,v147,v179
v_mul_f32 v180,v148,v180
v_mul_f32 v181,v149,v181
v_mul_f32 v182,v150,v182
v_mul_f32 v183,v151,v183
v_mul_f32 v184,v152,v184
v_mul_f32 v185,v153,v185
v_mul_f32 v186,v154,v186
v_mul_f32 v187,v155,v187
v_mul_f32 v188,v156,v188
v_mul_f32 v189,v157,v189
v_mul_f32 v190,v158,v190
v_mul_f32 v191,v159,v191
s_setprio 1
v_mfma_f32_32x32x16_bf16 v[96:111],v[200:203],v[192:195],v[96:111]
v_exp_f32 v176,v176
v_exp_f32 v177,v177
v_exp_f32 v178,v178
v_mfma_f32_32x32x16_bf16 v[96:111],v[204:207],v[196:199],v[96:111]
v_exp_f32 v179,v179
v_exp_f32 v180,v180
v_exp_f32 v181,v181
v_mfma_f32_32x32x16_bf16 v[112:127],v[208:211],v[192:195],v[112:127]
v_exp_f32 v182,v182
v_exp_f32 v183,v183
v_exp_f32 v184,v184
v_mfma_f32_32x32x16_bf16 v[112:127],v[212:215],v[196:199],v[112:127]
v_exp_f32 v185,v185
v_exp_f32 v186,v186
v_exp_f32 v187,v187
v_mfma_f32_32x32x16_bf16 v[128:143],v[216:219],v[192:195],v[128:143]
v_exp_f32 v188,v188
v_exp_f32 v189,v189
v_mfma_f32_32x32x16_bf16 v[128:143],v[220:223],v[196:199],v[128:143]
v_exp_f32 v190,v190
v_exp_f32 v191,v191
ds_read_b64_tr_b16 v[200:201],v16 offset:6144
ds_read_b64_tr_b16 v[202:203],v16 offset:6656
ds_read_b64_tr_b16 v[204:205],v16 offset:7168
ds_read_b64_tr_b16 v[206:207],v16 offset:7680
ds_read_b64_tr_b16 v[208:209],v16 offset:8192
ds_read_b64_tr_b16 v[210:211],v16 offset:8704
ds_read_b64_tr_b16 v[212:213],v16 offset:9216
ds_read_b64_tr_b16 v[214:215],v16 offset:9728
ds_read_b64_tr_b16 v[216:217],v16 offset:10240
ds_read_b64_tr_b16 v[218:219],v16 offset:10752
ds_read_b64_tr_b16 v[220:221],v16 offset:11264
ds_read_b64_tr_b16 v[222:223],v16 offset:11776
ds_read_b128 v[160:163],v18 offset:896
ds_read_b128 v[164:167],v18 offset:928
ds_read_b128 v[168:171],v18 offset:960
ds_read_b128 v[172:175],v18 offset:992
s_mov_b64 exec,s[62:63]
s_add_u32 m0,s68,41472
s_add_u32 s64,s59,1179648
buffer_load_dwordx4 v27,s[36:39],s64 offen lds
s_mov_b64 exec,-1
s_setprio 0
v_add_f32 v176,1.0,v176
v_add_f32 v177,1.0,v177
v_add_f32 v178,1.0,v178
v_add_f32 v179,1.0,v179
v_add_f32 v180,1.0,v180
v_add_f32 v181,1.0,v181
v_add_f32 v182,1.0,v182
v_add_f32 v183,1.0,v183
v_add_f32 v184,1.0,v184
v_add_f32 v185,1.0,v185
v_add_f32 v186,1.0,v186
v_add_f32 v187,1.0,v187
v_add_f32 v188,1.0,v188
v_add_f32 v189,1.0,v189
v_add_f32 v190,1.0,v190
v_add_f32 v191,1.0,v191
s_setprio 1
s_waitcnt lgkmcnt(0)
v_mfma_f32_32x32x16_bf16 v[160:175],v[200:203],v[72:75],v[160:175]
v_rcp_f32 v176,v176
v_rcp_f32 v177,v177
v_rcp_f32 v178,v178
v_mfma_f32_32x32x16_bf16 v[160:175],v[204:207],v[76:79],v[160:175]
v_rcp_f32 v179,v179
v_rcp_f32 v180,v180
v_rcp_f32 v181,v181
v_mfma_f32_32x32x16_bf16 v[160:175],v[208:211],v[80:83],v[160:175]
v_rcp_f32 v182,v182
v_rcp_f32 v183,v183
v_rcp_f32 v184,v184
v_mfma_f32_32x32x16_bf16 v[160:175],v[212:215],v[84:87],v[160:175]
v_rcp_f32 v185,v185
v_rcp_f32 v186,v186
v_rcp_f32 v187,v187
v_mfma_f32_32x32x16_bf16 v[160:175],v[216:219],v[88:91],v[160:175]
v_rcp_f32 v188,v188
v_rcp_f32 v189,v189
v_mfma_f32_32x32x16_bf16 v[160:175],v[220:223],v[92:95],v[160:175]
v_rcp_f32 v190,v190
v_rcp_f32 v191,v191
ds_read_b64_tr_b16 v[200:201],v17 offset:0
ds_read_b64_tr_b16 v[202:203],v17 offset:1536
ds_read_b64_tr_b16 v[204:205],v17 offset:3072
ds_read_b64_tr_b16 v[206:207],v17 offset:4608
ds_read_b64_tr_b16 v[208:209],v17 offset:64
ds_read_b64_tr_b16 v[210:211],v17 offset:1600
ds_read_b64_tr_b16 v[212:213],v17 offset:3136
ds_read_b64_tr_b16 v[214:215],v17 offset:4672
ds_read_b64_tr_b16 v[216:217],v17 offset:128
ds_read_b64_tr_b16 v[218:219],v17 offset:1664
ds_read_b64_tr_b16 v[220:221],v17 offset:3200
ds_read_b64_tr_b16 v[222:223],v17 offset:4736
s_mov_b64 exec,s[62:63]
s_add_u32 m0,s68,44928
s_add_u32 s64,s59,1277952
buffer_load_dwordx4 v27,s[36:39],s64 offen lds
s_mov_b64 exec,-1
s_setprio 0
v_mul_f32 v176,v144,v176
v_mul_f32 v177,v145,v177
v_mul_f32 v178,v146,v178
v_mul_f32 v179,v147,v179
v_mul_f32 v180,v148,v180
v_mul_f32 v181,v149,v181
v_mul_f32 v182,v150,v182
v_mul_f32 v183,v151,v183
v_mul_f32 v184,v152,v184
v_mul_f32 v185,v153,v185
v_mul_f32 v186,v154,v186
v_mul_f32 v187,v155,v187
v_mul_f32 v188,v156,v188
v_mul_f32 v189,v157,v189
v_mul_f32 v190,v158,v190
v_mul_f32 v191,v159,v191
v_cvt_pk_bf16_f32 v192,v176,v177
v_cvt_pk_bf16_f32 v193,v178,v179
v_cvt_pk_bf16_f32 v194,v180,v181
v_cvt_pk_bf16_f32 v195,v182,v183
v_cvt_pk_bf16_f32 v196,v184,v185
v_cvt_pk_bf16_f32 v197,v186,v187
v_cvt_pk_bf16_f32 v198,v188,v189
v_cvt_pk_bf16_f32 v199,v190,v191
s_waitcnt lgkmcnt(0)
s_barrier
v_fmaak_f32 v176,v160,v160,0x409d30fc
v_fmaak_f32 v177,v161,v161,0x409d30fc
v_fmaak_f32 v178,v162,v162,0x409d30fc
v_fmaak_f32 v179,v163,v163,0x409d30fc
v_fmaak_f32 v180,v164,v164,0x409d30fc
v_fmaak_f32 v181,v165,v165,0x409d30fc
v_fmaak_f32 v182,v166,v166,0x409d30fc
v_fmaak_f32 v183,v167,v167,0x409d30fc
v_fmaak_f32 v184,v168,v168,0x409d30fc
v_fmaak_f32 v185,v169,v169,0x409d30fc
v_fmaak_f32 v186,v170,v170,0x409d30fc
v_fmaak_f32 v187,v171,v171,0x409d30fc
v_fmaak_f32 v188,v172,v172,0x409d30fc
v_fmaak_f32 v189,v173,v173,0x409d30fc
v_fmaak_f32 v190,v174,v174,0x409d30fc
v_fmaak_f32 v191,v175,v175,0x409d30fc
v_mul_f32 v176,v160,v176
v_mul_f32 v177,v161,v177
v_mul_f32 v178,v162,v178
v_mul_f32 v179,v163,v179
v_mul_f32 v180,v164,v180
v_mul_f32 v181,v165,v181
v_mul_f32 v182,v166,v182
v_mul_f32 v183,v167,v183
v_mul_f32 v184,v168,v184
v_mul_f32 v185,v169,v185
v_mul_f32 v186,v170,v186
v_mul_f32 v187,v171,v187
v_mul_f32 v188,v172,v188
v_mul_f32 v189,v173,v189
v_mul_f32 v190,v174,v190
v_mul_f32 v191,v175,v191
s_setprio 1
v_mfma_f32_32x32x16_bf16 v[96:111],v[200:203],v[192:195],v[96:111]
v_exp_f32 v176,v176
v_exp_f32 v177,v177
v_exp_f32 v178,v178
v_mfma_f32_32x32x16_bf16 v[96:111],v[204:207],v[196:199],v[96:111]
v_exp_f32 v179,v179
v_exp_f32 v180,v180
v_exp_f32 v181,v181
v_mfma_f32_32x32x16_bf16 v[112:127],v[208:211],v[192:195],v[112:127]
v_exp_f32 v182,v182
v_exp_f32 v183,v183
v_exp_f32 v184,v184
v_mfma_f32_32x32x16_bf16 v[112:127],v[212:215],v[196:199],v[112:127]
v_exp_f32 v185,v185
v_exp_f32 v186,v186
v_exp_f32 v187,v187
v_mfma_f32_32x32x16_bf16 v[128:143],v[216:219],v[192:195],v[128:143]
v_exp_f32 v188,v188
v_exp_f32 v189,v189
v_mfma_f32_32x32x16_bf16 v[128:143],v[220:223],v[196:199],v[128:143]
v_exp_f32 v190,v190
v_exp_f32 v191,v191
ds_read_b64_tr_b16 v[200:201],v16 offset:12288
ds_read_b64_tr_b16 v[202:203],v16 offset:12800
ds_read_b64_tr_b16 v[204:205],v16 offset:13312
ds_read_b64_tr_b16 v[206:207],v16 offset:13824
ds_read_b64_tr_b16 v[208:209],v16 offset:14336
ds_read_b64_tr_b16 v[210:211],v16 offset:14848
ds_read_b64_tr_b16 v[212:213],v16 offset:15360
ds_read_b64_tr_b16 v[214:215],v16 offset:15872
ds_read_b64_tr_b16 v[216:217],v16 offset:16384
ds_read_b64_tr_b16 v[218:219],v16 offset:16896
ds_read_b64_tr_b16 v[220:221],v16 offset:17408
ds_read_b64_tr_b16 v[222:223],v16 offset:17920
ds_read_b128 v[144:147],v18 offset:1024
ds_read_b128 v[148:151],v18 offset:1056
ds_read_b128 v[152:155],v18 offset:1088
ds_read_b128 v[156:159],v18 offset:1120
s_mov_b64 exec,s[62:63]
s_add_u32 m0,s68,48384
s_add_u32 s64,s59,1376256
buffer_load_dwordx4 v27,s[36:39],s64 offen lds
s_mov_b64 exec,-1
s_setprio 0
v_add_f32 v176,1.0,v176
v_add_f32 v177,1.0,v177
v_add_f32 v178,1.0,v178
v_add_f32 v179,1.0,v179
v_add_f32 v180,1.0,v180
v_add_f32 v181,1.0,v181
v_add_f32 v182,1.0,v182
v_add_f32 v183,1.0,v183
v_add_f32 v184,1.0,v184
v_add_f32 v185,1.0,v185
v_add_f32 v186,1.0,v186
v_add_f32 v187,1.0,v187
v_add_f32 v188,1.0,v188
v_add_f32 v189,1.0,v189
v_add_f32 v190,1.0,v190
v_add_f32 v191,1.0,v191
s_setprio 1
s_waitcnt lgkmcnt(0)
v_mfma_f32_32x32x16_bf16 v[144:159],v[200:203],v[72:75],v[144:159]
v_rcp_f32 v176,v176
v_rcp_f32 v177,v177
v_rcp_f32 v178,v178
v_mfma_f32_32x32x16_bf16 v[144:159],v[204:207],v[76:79],v[144:159]
v_rcp_f32 v179,v179
v_rcp_f32 v180,v180
v_rcp_f32 v181,v181
v_mfma_f32_32x32x16_bf16 v[144:159],v[208:211],v[80:83],v[144:159]
v_rcp_f32 v182,v182
v_rcp_f32 v183,v183
v_rcp_f32 v184,v184
v_mfma_f32_32x32x16_bf16 v[144:159],v[212:215],v[84:87],v[144:159]
v_rcp_f32 v185,v185
v_rcp_f32 v186,v186
v_rcp_f32 v187,v187
v_mfma_f32_32x32x16_bf16 v[144:159],v[216:219],v[88:91],v[144:159]
v_rcp_f32 v188,v188
v_rcp_f32 v189,v189
v_mfma_f32_32x32x16_bf16 v[144:159],v[220:223],v[92:95],v[144:159]
v_rcp_f32 v190,v190
v_rcp_f32 v191,v191
ds_read_b64_tr_b16 v[200:201],v17 offset:6144
ds_read_b64_tr_b16 v[202:203],v17 offset:7680
ds_read_b64_tr_b16 v[204:205],v17 offset:9216
ds_read_b64_tr_b16 v[206:207],v17 offset:10752
ds_read_b64_tr_b16 v[208:209],v17 offset:6208
ds_read_b64_tr_b16 v[210:211],v17 offset:7744
ds_read_b64_tr_b16 v[212:213],v17 offset:9280
ds_read_b64_tr_b16 v[214:215],v17 offset:10816
ds_read_b64_tr_b16 v[216:217],v17 offset:6272
ds_read_b64_tr_b16 v[218:219],v17 offset:7808
ds_read_b64_tr_b16 v[220:221],v17 offset:9344
ds_read_b64_tr_b16 v[222:223],v17 offset:10880
s_mov_b64 exec,s[62:63]
s_add_u32 m0,s68,51840
s_add_u32 s64,s59,1474560
buffer_load_dwordx4 v27,s[36:39],s64 offen lds
s_mov_b64 exec,-1
s_setprio 0
v_mul_f32 v176,v160,v176
v_mul_f32 v177,v161,v177
v_mul_f32 v178,v162,v178
v_mul_f32 v179,v163,v179
v_mul_f32 v180,v164,v180
v_mul_f32 v181,v165,v181
v_mul_f32 v182,v166,v182
v_mul_f32 v183,v167,v183
v_mul_f32 v184,v168,v184
v_mul_f32 v185,v169,v185
v_mul_f32 v186,v170,v186
v_mul_f32 v187,v171,v187
v_mul_f32 v188,v172,v188
v_mul_f32 v189,v173,v189
v_mul_f32 v190,v174,v190
v_mul_f32 v191,v175,v191
v_cvt_pk_bf16_f32 v192,v176,v177
v_cvt_pk_bf16_f32 v193,v178,v179
v_cvt_pk_bf16_f32 v194,v180,v181
v_cvt_pk_bf16_f32 v195,v182,v183
v_cvt_pk_bf16_f32 v196,v184,v185
v_cvt_pk_bf16_f32 v197,v186,v187
v_cvt_pk_bf16_f32 v198,v188,v189
v_cvt_pk_bf16_f32 v199,v190,v191
s_waitcnt lgkmcnt(0)
s_barrier
v_fmaak_f32 v176,v144,v144,0x409d30fc
v_fmaak_f32 v177,v145,v145,0x409d30fc
v_fmaak_f32 v178,v146,v146,0x409d30fc
v_fmaak_f32 v179,v147,v147,0x409d30fc
v_fmaak_f32 v180,v148,v148,0x409d30fc
v_fmaak_f32 v181,v149,v149,0x409d30fc
v_fmaak_f32 v182,v150,v150,0x409d30fc
v_fmaak_f32 v183,v151,v151,0x409d30fc
v_fmaak_f32 v184,v152,v152,0x409d30fc
v_fmaak_f32 v185,v153,v153,0x409d30fc
v_fmaak_f32 v186,v154,v154,0x409d30fc
v_fmaak_f32 v187,v155,v155,0x409d30fc
v_fmaak_f32 v188,v156,v156,0x409d30fc
v_fmaak_f32 v189,v157,v157,0x409d30fc
v_fmaak_f32 v190,v158,v158,0x409d30fc
v_fmaak_f32 v191,v159,v159,0x409d30fc
v_mul_f32 v176,v144,v176
v_mul_f32 v177,v145,v177
v_mul_f32 v178,v146,v178
v_mul_f32 v179,v147,v179
v_mul_f32 v180,v148,v180
v_mul_f32 v181,v149,v181
v_mul_f32 v182,v150,v182
v_mul_f32 v183,v151,v183
v_mul_f32 v184,v152,v184
v_mul_f32 v185,v153,v185
v_mul_f32 v186,v154,v186
v_mul_f32 v187,v155,v187
v_mul_f32 v188,v156,v188
v_mul_f32 v189,v157,v189
v_mul_f32 v190,v158,v190
v_mul_f32 v191,v159,v191
s_setprio 1
v_mfma_f32_32x32x16_bf16 v[96:111],v[200:203],v[192:195],v[96:111]
v_exp_f32 v176,v176
v_exp_f32 v177,v177
v_exp_f32 v178,v178
v_mfma_f32_32x32x16_bf16 v[96:111],v[204:207],v[196:199],v[96:111]
v_exp_f32 v179,v179
v_exp_f32 v180,v180
v_exp_f32 v181,v181
v_mfma_f32_32x32x16_bf16 v[112:127],v[208:211],v[192:195],v[112:127]
v_exp_f32 v182,v182
v_exp_f32 v183,v183
v_exp_f32 v184,v184
v_mfma_f32_32x32x16_bf16 v[112:127],v[212:215],v[196:199],v[112:127]
v_exp_f32 v185,v185
v_exp_f32 v186,v186
v_exp_f32 v187,v187
v_mfma_f32_32x32x16_bf16 v[128:143],v[216:219],v[192:195],v[128:143]
v_exp_f32 v188,v188
v_exp_f32 v189,v189
v_mfma_f32_32x32x16_bf16 v[128:143],v[220:223],v[196:199],v[128:143]
v_exp_f32 v190,v190
v_exp_f32 v191,v191
ds_read_b64_tr_b16 v[200:201],v16 offset:0
ds_read_b64_tr_b16 v[202:203],v16 offset:512
ds_read_b64_tr_b16 v[204:205],v16 offset:1024
ds_read_b64_tr_b16 v[206:207],v16 offset:1536
ds_read_b64_tr_b16 v[208:209],v16 offset:2048
ds_read_b64_tr_b16 v[210:211],v16 offset:2560
ds_read_b64_tr_b16 v[212:213],v16 offset:3072
ds_read_b64_tr_b16 v[214:215],v16 offset:3584
ds_read_b64_tr_b16 v[216:217],v16 offset:4096
ds_read_b64_tr_b16 v[218:219],v16 offset:4608
ds_read_b64_tr_b16 v[220:221],v16 offset:5120
ds_read_b64_tr_b16 v[222:223],v16 offset:5632
ds_read_b128 v[160:163],v18 offset:1152
ds_read_b128 v[164:167],v18 offset:1184
ds_read_b128 v[168:171],v18 offset:1216
ds_read_b128 v[172:175],v18 offset:1248
s_mov_b64 exec,s[62:63]
s_add_u32 m0,s68,55296
s_add_u32 s64,s59,1572864
buffer_load_dwordx4 v27,s[36:39],s64 offen lds
s_mov_b64 exec,-1
s_setprio 0
v_add_f32 v176,1.0,v176
v_add_f32 v177,1.0,v177
v_add_f32 v178,1.0,v178
v_add_f32 v179,1.0,v179
v_add_f32 v180,1.0,v180
v_add_f32 v181,1.0,v181
v_add_f32 v182,1.0,v182
v_add_f32 v183,1.0,v183
v_add_f32 v184,1.0,v184
v_add_f32 v185,1.0,v185
v_add_f32 v186,1.0,v186
v_add_f32 v187,1.0,v187
v_add_f32 v188,1.0,v188
v_add_f32 v189,1.0,v189
v_add_f32 v190,1.0,v190
v_add_f32 v191,1.0,v191
s_setprio 1
s_waitcnt lgkmcnt(0)
v_mfma_f32_32x32x16_bf16 v[160:175],v[200:203],v[72:75],v[160:175]
v_rcp_f32 v176,v176
v_rcp_f32 v177,v177
v_rcp_f32 v178,v178
v_mfma_f32_32x32x16_bf16 v[160:175],v[204:207],v[76:79],v[160:175]
v_rcp_f32 v179,v179
v_rcp_f32 v180,v180
v_rcp_f32 v181,v181
v_mfma_f32_32x32x16_bf16 v[160:175],v[208:211],v[80:83],v[160:175]
v_rcp_f32 v182,v182
v_rcp_f32 v183,v183
v_rcp_f32 v184,v184
v_mfma_f32_32x32x16_bf16 v[160:175],v[212:215],v[84:87],v[160:175]
v_rcp_f32 v185,v185
v_rcp_f32 v186,v186
v_rcp_f32 v187,v187
v_mfma_f32_32x32x16_bf16 v[160:175],v[216:219],v[88:91],v[160:175]
v_rcp_f32 v188,v188
v_rcp_f32 v189,v189
v_mfma_f32_32x32x16_bf16 v[160:175],v[220:223],v[92:95],v[160:175]
v_rcp_f32 v190,v190
v_rcp_f32 v191,v191
ds_read_b64_tr_b16 v[200:201],v17 offset:12288
ds_read_b64_tr_b16 v[202:203],v17 offset:13824
ds_read_b64_tr_b16 v[204:205],v17 offset:15360
ds_read_b64_tr_b16 v[206:207],v17 offset:16896
ds_read_b64_tr_b16 v[208:209],v17 offset:12352
ds_read_b64_tr_b16 v[210:211],v17 offset:13888
ds_read_b64_tr_b16 v[212:213],v17 offset:15424
ds_read_b64_tr_b16 v[214:215],v17 offset:16960
ds_read_b64_tr_b16 v[216:217],v17 offset:12416
ds_read_b64_tr_b16 v[218:219],v17 offset:13952
ds_read_b64_tr_b16 v[220:221],v17 offset:15488
ds_read_b64_tr_b16 v[222:223],v17 offset:17024
s_mov_b64 exec,s[62:63]
s_add_u32 m0,s68,58752
s_add_u32 s64,s59,1671168
buffer_load_dwordx4 v27,s[36:39],s64 offen lds
s_mov_b64 exec,-1
s_setprio 0
v_mul_f32 v176,v144,v176
v_mul_f32 v177,v145,v177
v_mul_f32 v178,v146,v178
v_mul_f32 v179,v147,v179
v_mul_f32 v180,v148,v180
v_mul_f32 v181,v149,v181
v_mul_f32 v182,v150,v182
v_mul_f32 v183,v151,v183
v_mul_f32 v184,v152,v184
v_mul_f32 v185,v153,v185
v_mul_f32 v186,v154,v186
v_mul_f32 v187,v155,v187
v_mul_f32 v188,v156,v188
v_mul_f32 v189,v157,v189
v_mul_f32 v190,v158,v190
v_mul_f32 v191,v159,v191
v_cvt_pk_bf16_f32 v192,v176,v177
v_cvt_pk_bf16_f32 v193,v178,v179
v_cvt_pk_bf16_f32 v194,v180,v181
v_cvt_pk_bf16_f32 v195,v182,v183
v_cvt_pk_bf16_f32 v196,v184,v185
v_cvt_pk_bf16_f32 v197,v186,v187
v_cvt_pk_bf16_f32 v198,v188,v189
v_cvt_pk_bf16_f32 v199,v190,v191
s_waitcnt lgkmcnt(0)
s_barrier
v_fmaak_f32 v176,v160,v160,0x409d30fc
v_fmaak_f32 v177,v161,v161,0x409d30fc
v_fmaak_f32 v178,v162,v162,0x409d30fc
v_fmaak_f32 v179,v163,v163,0x409d30fc
v_fmaak_f32 v180,v164,v164,0x409d30fc
v_fmaak_f32 v181,v165,v165,0x409d30fc
v_fmaak_f32 v182,v166,v166,0x409d30fc
v_fmaak_f32 v183,v167,v167,0x409d30fc
v_fmaak_f32 v184,v168,v168,0x409d30fc
v_fmaak_f32 v185,v169,v169,0x409d30fc
v_fmaak_f32 v186,v170,v170,0x409d30fc
v_fmaak_f32 v187,v171,v171,0x409d30fc
v_fmaak_f32 v188,v172,v172,0x409d30fc
v_fmaak_f32 v189,v173,v173,0x409d30fc
v_fmaak_f32 v190,v174,v174,0x409d30fc
v_fmaak_f32 v191,v175,v175,0x409d30fc
v_mul_f32 v176,v160,v176
v_mul_f32 v177,v161,v177
v_mul_f32 v178,v162,v178
v_mul_f32 v179,v163,v179
v_mul_f32 v180,v164,v180
v_mul_f32 v181,v165,v181
v_mul_f32 v182,v166,v182
v_mul_f32 v183,v167,v183
v_mul_f32 v184,v168,v184
v_mul_f32 v185,v169,v185
v_mul_f32 v186,v170,v186
v_mul_f32 v187,v171,v187
v_mul_f32 v188,v172,v188
v_mul_f32 v189,v173,v189
v_mul_f32 v190,v174,v190
v_mul_f32 v191,v175,v191
s_setprio 1
v_mfma_f32_32x32x16_bf16 v[96:111],v[200:203],v[192:195],v[96:111]
v_exp_f32 v176,v176
v_exp_f32 v177,v177
v_exp_f32 v178,v178
v_mfma_f32_32x32x16_bf16 v[96:111],v[204:207],v[196:199],v[96:111]
v_exp_f32 v179,v179
v_exp_f32 v180,v180
v_exp_f32 v181,v181
v_mfma_f32_32x32x16_bf16 v[112:127],v[208:211],v[192:195],v[112:127]
v_exp_f32 v182,v182
v_exp_f32 v183,v183
v_exp_f32 v184,v184
v_mfma_f32_32x32x16_bf16 v[112:127],v[212:215],v[196:199],v[112:127]
v_exp_f32 v185,v185
v_exp_f32 v186,v186
v_exp_f32 v187,v187
v_mfma_f32_32x32x16_bf16 v[128:143],v[216:219],v[192:195],v[128:143]
v_exp_f32 v188,v188
v_exp_f32 v189,v189
v_mfma_f32_32x32x16_bf16 v[128:143],v[220:223],v[196:199],v[128:143]
v_exp_f32 v190,v190
v_exp_f32 v191,v191
ds_read_b64_tr_b16 v[200:201],v16 offset:6144
ds_read_b64_tr_b16 v[202:203],v16 offset:6656
ds_read_b64_tr_b16 v[204:205],v16 offset:7168
ds_read_b64_tr_b16 v[206:207],v16 offset:7680
ds_read_b64_tr_b16 v[208:209],v16 offset:8192
ds_read_b64_tr_b16 v[210:211],v16 offset:8704
ds_read_b64_tr_b16 v[212:213],v16 offset:9216
ds_read_b64_tr_b16 v[214:215],v16 offset:9728
ds_read_b64_tr_b16 v[216:217],v16 offset:10240
ds_read_b64_tr_b16 v[218:219],v16 offset:10752
ds_read_b64_tr_b16 v[220:221],v16 offset:11264
ds_read_b64_tr_b16 v[222:223],v16 offset:11776
ds_read_b128 v[144:147],v18 offset:1280
ds_read_b128 v[148:151],v18 offset:1312
ds_read_b128 v[152:155],v18 offset:1344
ds_read_b128 v[156:159],v18 offset:1376
s_setprio 0
v_add_f32 v176,1.0,v176
v_add_f32 v177,1.0,v177
v_add_f32 v178,1.0,v178
v_add_f32 v179,1.0,v179
v_add_f32 v180,1.0,v180
v_add_f32 v181,1.0,v181
v_add_f32 v182,1.0,v182
v_add_f32 v183,1.0,v183
v_add_f32 v184,1.0,v184
v_add_f32 v185,1.0,v185
v_add_f32 v186,1.0,v186
v_add_f32 v187,1.0,v187
v_add_f32 v188,1.0,v188
v_add_f32 v189,1.0,v189
v_add_f32 v190,1.0,v190
v_add_f32 v191,1.0,v191
s_setprio 1
s_waitcnt lgkmcnt(0)
v_mfma_f32_32x32x16_bf16 v[144:159],v[200:203],v[72:75],v[144:159]
v_rcp_f32 v176,v176
v_rcp_f32 v177,v177
v_rcp_f32 v178,v178
v_mfma_f32_32x32x16_bf16 v[144:159],v[204:207],v[76:79],v[144:159]
v_rcp_f32 v179,v179
v_rcp_f32 v180,v180
v_rcp_f32 v181,v181
v_mfma_f32_32x32x16_bf16 v[144:159],v[208:211],v[80:83],v[144:159]
v_rcp_f32 v182,v182
v_rcp_f32 v183,v183
v_rcp_f32 v184,v184
v_mfma_f32_32x32x16_bf16 v[144:159],v[212:215],v[84:87],v[144:159]
v_rcp_f32 v185,v185
v_rcp_f32 v186,v186
v_rcp_f32 v187,v187
v_mfma_f32_32x32x16_bf16 v[144:159],v[216:219],v[88:91],v[144:159]
v_rcp_f32 v188,v188
v_rcp_f32 v189,v189
v_mfma_f32_32x32x16_bf16 v[144:159],v[220:223],v[92:95],v[144:159]
v_rcp_f32 v190,v190
v_rcp_f32 v191,v191
ds_read_b64_tr_b16 v[200:201],v17 offset:0
ds_read_b64_tr_b16 v[202:203],v17 offset:1536
ds_read_b64_tr_b16 v[204:205],v17 offset:3072
ds_read_b64_tr_b16 v[206:207],v17 offset:4608
ds_read_b64_tr_b16 v[208:209],v17 offset:64
ds_read_b64_tr_b16 v[210:211],v17 offset:1600
ds_read_b64_tr_b16 v[212:213],v17 offset:3136
ds_read_b64_tr_b16 v[214:215],v17 offset:4672
ds_read_b64_tr_b16 v[216:217],v17 offset:128
ds_read_b64_tr_b16 v[218:219],v17 offset:1664
ds_read_b64_tr_b16 v[220:221],v17 offset:3200
ds_read_b64_tr_b16 v[222:223],v17 offset:4736
s_setprio 0
v_mul_f32 v176,v160,v176
v_mul_f32 v177,v161,v177
v_mul_f32 v178,v162,v178
v_mul_f32 v179,v163,v179
v_mul_f32 v180,v164,v180
v_mul_f32 v181,v165,v181
v_mul_f32 v182,v166,v182
v_mul_f32 v183,v167,v183
v_mul_f32 v184,v168,v184
v_mul_f32 v185,v169,v185
v_mul_f32 v186,v170,v186
v_mul_f32 v187,v171,v187
v_mul_f32 v188,v172,v188
v_mul_f32 v189,v173,v189
v_mul_f32 v190,v174,v190
v_mul_f32 v191,v175,v191
v_cvt_pk_bf16_f32 v192,v176,v177
v_cvt_pk_bf16_f32 v193,v178,v179
v_cvt_pk_bf16_f32 v194,v180,v181
v_cvt_pk_bf16_f32 v195,v182,v183
v_cvt_pk_bf16_f32 v196,v184,v185
v_cvt_pk_bf16_f32 v197,v186,v187
v_cvt_pk_bf16_f32 v198,v188,v189
v_cvt_pk_bf16_f32 v199,v190,v191
s_waitcnt lgkmcnt(0)
s_barrier
v_fmaak_f32 v176,v144,v144,0x409d30fc
v_fmaak_f32 v177,v145,v145,0x409d30fc
v_fmaak_f32 v178,v146,v146,0x409d30fc
v_fmaak_f32 v179,v147,v147,0x409d30fc
v_fmaak_f32 v180,v148,v148,0x409d30fc
v_fmaak_f32 v181,v149,v149,0x409d30fc
v_fmaak_f32 v182,v150,v150,0x409d30fc
v_fmaak_f32 v183,v151,v151,0x409d30fc
v_fmaak_f32 v184,v152,v152,0x409d30fc
v_fmaak_f32 v185,v153,v153,0x409d30fc
v_fmaak_f32 v186,v154,v154,0x409d30fc
v_fmaak_f32 v187,v155,v155,0x409d30fc
v_fmaak_f32 v188,v156,v156,0x409d30fc
v_fmaak_f32 v189,v157,v157,0x409d30fc
v_fmaak_f32 v190,v158,v158,0x409d30fc
v_fmaak_f32 v191,v159,v159,0x409d30fc
v_mul_f32 v176,v144,v176
v_mul_f32 v177,v145,v177
v_mul_f32 v178,v146,v178
v_mul_f32 v179,v147,v179
v_mul_f32 v180,v148,v180
v_mul_f32 v181,v149,v181
v_mul_f32 v182,v150,v182
v_mul_f32 v183,v151,v183
v_mul_f32 v184,v152,v184
v_mul_f32 v185,v153,v185
v_mul_f32 v186,v154,v186
v_mul_f32 v187,v155,v187
v_mul_f32 v188,v156,v188
v_mul_f32 v189,v157,v189
v_mul_f32 v190,v158,v190
v_mul_f32 v191,v159,v191
s_setprio 1
v_mfma_f32_32x32x16_bf16 v[96:111],v[200:203],v[192:195],v[96:111]
v_exp_f32 v176,v176
v_exp_f32 v177,v177
v_exp_f32 v178,v178
v_mfma_f32_32x32x16_bf16 v[96:111],v[204:207],v[196:199],v[96:111]
v_exp_f32 v179,v179
v_exp_f32 v180,v180
v_exp_f32 v181,v181
v_mfma_f32_32x32x16_bf16 v[112:127],v[208:211],v[192:195],v[112:127]
v_exp_f32 v182,v182
v_exp_f32 v183,v183
v_exp_f32 v184,v184
v_mfma_f32_32x32x16_bf16 v[112:127],v[212:215],v[196:199],v[112:127]
v_exp_f32 v185,v185
v_exp_f32 v186,v186
v_exp_f32 v187,v187
v_mfma_f32_32x32x16_bf16 v[128:143],v[216:219],v[192:195],v[128:143]
v_exp_f32 v188,v188
v_exp_f32 v189,v189
v_mfma_f32_32x32x16_bf16 v[128:143],v[220:223],v[196:199],v[128:143]
v_exp_f32 v190,v190
v_exp_f32 v191,v191
ds_read_b64_tr_b16 v[200:201],v16 offset:12288
ds_read_b64_tr_b16 v[202:203],v16 offset:12800
ds_read_b64_tr_b16 v[204:205],v16 offset:13312
ds_read_b64_tr_b16 v[206:207],v16 offset:13824
ds_read_b64_tr_b16 v[208:209],v16 offset:14336
ds_read_b64_tr_b16 v[210:211],v16 offset:14848
ds_read_b64_tr_b16 v[212:213],v16 offset:15360
ds_read_b64_tr_b16 v[214:215],v16 offset:15872
ds_read_b64_tr_b16 v[216:217],v16 offset:16384
ds_read_b64_tr_b16 v[218:219],v16 offset:16896
ds_read_b64_tr_b16 v[220:221],v16 offset:17408
ds_read_b64_tr_b16 v[222:223],v16 offset:17920
ds_read_b128 v[160:163],v18 offset:1408
ds_read_b128 v[164:167],v18 offset:1440
ds_read_b128 v[168:171],v18 offset:1472
ds_read_b128 v[172:175],v18 offset:1504
s_setprio 0
v_add_f32 v176,1.0,v176
v_add_f32 v177,1.0,v177
v_add_f32 v178,1.0,v178
v_add_f32 v179,1.0,v179
v_add_f32 v180,1.0,v180
v_add_f32 v181,1.0,v181
v_add_f32 v182,1.0,v182
v_add_f32 v183,1.0,v183
v_add_f32 v184,1.0,v184
v_add_f32 v185,1.0,v185
v_add_f32 v186,1.0,v186
v_add_f32 v187,1.0,v187
v_add_f32 v188,1.0,v188
v_add_f32 v189,1.0,v189
v_add_f32 v190,1.0,v190
v_add_f32 v191,1.0,v191
s_setprio 1
s_waitcnt lgkmcnt(0)
v_mfma_f32_32x32x16_bf16 v[160:175],v[200:203],v[72:75],v[160:175]
v_rcp_f32 v176,v176
v_rcp_f32 v177,v177
v_rcp_f32 v178,v178
v_mfma_f32_32x32x16_bf16 v[160:175],v[204:207],v[76:79],v[160:175]
v_rcp_f32 v179,v179
v_rcp_f32 v180,v180
v_rcp_f32 v181,v181
v_mfma_f32_32x32x16_bf16 v[160:175],v[208:211],v[80:83],v[160:175]
v_rcp_f32 v182,v182
v_rcp_f32 v183,v183
v_rcp_f32 v184,v184
v_mfma_f32_32x32x16_bf16 v[160:175],v[212:215],v[84:87],v[160:175]
v_rcp_f32 v185,v185
v_rcp_f32 v186,v186
v_rcp_f32 v187,v187
v_mfma_f32_32x32x16_bf16 v[160:175],v[216:219],v[88:91],v[160:175]
v_rcp_f32 v188,v188
v_rcp_f32 v189,v189
v_mfma_f32_32x32x16_bf16 v[160:175],v[220:223],v[92:95],v[160:175]
v_rcp_f32 v190,v190
v_rcp_f32 v191,v191
ds_read_b64_tr_b16 v[200:201],v17 offset:6144
ds_read_b64_tr_b16 v[202:203],v17 offset:7680
ds_read_b64_tr_b16 v[204:205],v17 offset:9216
ds_read_b64_tr_b16 v[206:207],v17 offset:10752
ds_read_b64_tr_b16 v[208:209],v17 offset:6208
ds_read_b64_tr_b16 v[210:211],v17 offset:7744
ds_read_b64_tr_b16 v[212:213],v17 offset:9280
ds_read_b64_tr_b16 v[214:215],v17 offset:10816
ds_read_b64_tr_b16 v[216:217],v17 offset:6272
ds_read_b64_tr_b16 v[218:219],v17 offset:7808
ds_read_b64_tr_b16 v[220:221],v17 offset:9344
ds_read_b64_tr_b16 v[222:223],v17 offset:10880
s_setprio 0
v_mul_f32 v176,v144,v176
v_mul_f32 v177,v145,v177
v_mul_f32 v178,v146,v178
v_mul_f32 v179,v147,v179
v_mul_f32 v180,v148,v180
v_mul_f32 v181,v149,v181
v_mul_f32 v182,v150,v182
v_mul_f32 v183,v151,v183
v_mul_f32 v184,v152,v184
v_mul_f32 v185,v153,v185
v_mul_f32 v186,v154,v186
v_mul_f32 v187,v155,v187
v_mul_f32 v188,v156,v188
v_mul_f32 v189,v157,v189
v_mul_f32 v190,v158,v190
v_mul_f32 v191,v159,v191
v_cvt_pk_bf16_f32 v192,v176,v177
v_cvt_pk_bf16_f32 v193,v178,v179
v_cvt_pk_bf16_f32 v194,v180,v181
v_cvt_pk_bf16_f32 v195,v182,v183
v_cvt_pk_bf16_f32 v196,v184,v185
v_cvt_pk_bf16_f32 v197,v186,v187
v_cvt_pk_bf16_f32 v198,v188,v189
v_cvt_pk_bf16_f32 v199,v190,v191
s_waitcnt lgkmcnt(0)
s_barrier
v_fmaak_f32 v176,v160,v160,0x409d30fc
v_fmaak_f32 v177,v161,v161,0x409d30fc
v_fmaak_f32 v178,v162,v162,0x409d30fc
v_fmaak_f32 v179,v163,v163,0x409d30fc
v_fmaak_f32 v180,v164,v164,0x409d30fc
v_fmaak_f32 v181,v165,v165,0x409d30fc
v_fmaak_f32 v182,v166,v166,0x409d30fc
v_fmaak_f32 v183,v167,v167,0x409d30fc
v_fmaak_f32 v184,v168,v168,0x409d30fc
v_fmaak_f32 v185,v169,v169,0x409d30fc
v_fmaak_f32 v186,v170,v170,0x409d30fc
v_fmaak_f32 v187,v171,v171,0x409d30fc
v_fmaak_f32 v188,v172,v172,0x409d30fc
v_fmaak_f32 v189,v173,v173,0x409d30fc
v_fmaak_f32 v190,v174,v174,0x409d30fc
v_fmaak_f32 v191,v175,v175,0x409d30fc
v_mul_f32 v176,v160,v176
v_mul_f32 v177,v161,v177
v_mul_f32 v178,v162,v178
v_mul_f32 v179,v163,v179
v_mul_f32 v180,v164,v180
v_mul_f32 v181,v165,v181
v_mul_f32 v182,v166,v182
v_mul_f32 v183,v167,v183
v_mul_f32 v184,v168,v184
v_mul_f32 v185,v169,v185
v_mul_f32 v186,v170,v186
v_mul_f32 v187,v171,v187
v_mul_f32 v188,v172,v188
v_mul_f32 v189,v173,v189
v_mul_f32 v190,v174,v190
v_mul_f32 v191,v175,v191
s_setprio 1
v_mfma_f32_32x32x16_bf16 v[96:111],v[200:203],v[192:195],v[96:111]
v_exp_f32 v176,v176
v_exp_f32 v177,v177
v_exp_f32 v178,v178
v_mfma_f32_32x32x16_bf16 v[96:111],v[204:207],v[196:199],v[96:111]
v_exp_f32 v179,v179
v_exp_f32 v180,v180
v_exp_f32 v181,v181
v_mfma_f32_32x32x16_bf16 v[112:127],v[208:211],v[192:195],v[112:127]
v_exp_f32 v182,v182
v_exp_f32 v183,v183
v_exp_f32 v184,v184
v_mfma_f32_32x32x16_bf16 v[112:127],v[212:215],v[196:199],v[112:127]
v_exp_f32 v185,v185
v_exp_f32 v186,v186
v_exp_f32 v187,v187
v_mfma_f32_32x32x16_bf16 v[128:143],v[216:219],v[192:195],v[128:143]
v_exp_f32 v188,v188
v_exp_f32 v189,v189
v_mfma_f32_32x32x16_bf16 v[128:143],v[220:223],v[196:199],v[128:143]
v_exp_f32 v190,v190
v_exp_f32 v191,v191
ds_read_b64 v[72:73],v20 offset:0
ds_read_b64 v[74:75],v21 offset:0
ds_read_b64 v[76:77],v22 offset:0
ds_read_b64 v[78:79],v23 offset:0
ds_read_b64 v[80:81],v20 offset:64
ds_read_b64 v[82:83],v21 offset:64
ds_read_b64 v[84:85],v22 offset:64
ds_read_b64 v[86:87],v23 offset:64
ds_read_b64 v[88:89],v20 offset:128
ds_read_b64 v[90:91],v21 offset:128
ds_read_b64 v[92:93],v22 offset:128
ds_read_b64 v[94:95],v23 offset:128
s_setprio 0
v_add_f32 v176,1.0,v176
v_add_f32 v177,1.0,v177
v_add_f32 v178,1.0,v178
v_add_f32 v179,1.0,v179
v_add_f32 v180,1.0,v180
v_add_f32 v181,1.0,v181
v_add_f32 v182,1.0,v182
v_add_f32 v183,1.0,v183
v_add_f32 v184,1.0,v184
v_add_f32 v185,1.0,v185
v_add_f32 v186,1.0,v186
v_add_f32 v187,1.0,v187
v_add_f32 v188,1.0,v188
v_add_f32 v189,1.0,v189
v_add_f32 v190,1.0,v190
v_add_f32 v191,1.0,v191
s_setprio 1
s_waitcnt lgkmcnt(0)
v_rcp_f32 v176,v176
v_rcp_f32 v177,v177
v_rcp_f32 v178,v178
v_rcp_f32 v179,v179
v_rcp_f32 v180,v180
v_rcp_f32 v181,v181
v_rcp_f32 v182,v182
v_rcp_f32 v183,v183
v_rcp_f32 v184,v184
v_rcp_f32 v185,v185
v_rcp_f32 v186,v186
v_rcp_f32 v187,v187
v_rcp_f32 v188,v188
v_rcp_f32 v189,v189
v_rcp_f32 v190,v190
v_rcp_f32 v191,v191
ds_read_b64_tr_b16 v[200:201],v17 offset:12288
ds_read_b64_tr_b16 v[202:203],v17 offset:13824
ds_read_b64_tr_b16 v[204:205],v17 offset:15360
ds_read_b64_tr_b16 v[206:207],v17 offset:16896
ds_read_b64_tr_b16 v[208:209],v17 offset:12352
ds_read_b64_tr_b16 v[210:211],v17 offset:13888
ds_read_b64_tr_b16 v[212:213],v17 offset:15424
ds_read_b64_tr_b16 v[214:215],v17 offset:16960
ds_read_b64_tr_b16 v[216:217],v17 offset:12416
ds_read_b64_tr_b16 v[218:219],v17 offset:13952
ds_read_b64_tr_b16 v[220:221],v17 offset:15488
ds_read_b64_tr_b16 v[222:223],v17 offset:17024
s_setprio 0
v_mul_f32 v176,v160,v176
v_mul_f32 v177,v161,v177
v_mul_f32 v178,v162,v178
v_mul_f32 v179,v163,v179
v_mul_f32 v180,v164,v180
v_mul_f32 v181,v165,v181
v_mul_f32 v182,v166,v182
v_mul_f32 v183,v167,v183
v_mul_f32 v184,v168,v184
v_mul_f32 v185,v169,v185
v_mul_f32 v186,v170,v186
v_mul_f32 v187,v171,v187
v_mul_f32 v188,v172,v188
v_mul_f32 v189,v173,v189
v_mul_f32 v190,v174,v190
v_mul_f32 v191,v175,v191
v_cvt_pk_bf16_f32 v192,v176,v177
v_cvt_pk_bf16_f32 v193,v178,v179
v_cvt_pk_bf16_f32 v194,v180,v181
v_cvt_pk_bf16_f32 v195,v182,v183
v_cvt_pk_bf16_f32 v196,v184,v185
v_cvt_pk_bf16_f32 v197,v186,v187
v_cvt_pk_bf16_f32 v198,v188,v189
v_cvt_pk_bf16_f32 v199,v190,v191
ds_read_b128 v[144:147],v18 offset:1536
ds_read_b128 v[148:151],v18 offset:1568
ds_read_b128 v[152:155],v18 offset:1600
ds_read_b128 v[156:159],v18 offset:1632
ds_read_b128 v[160:163],v18 offset:1664
ds_read_b128 v[164:167],v18 offset:1696
ds_read_b128 v[168:171],v18 offset:1728
ds_read_b128 v[172:175],v18 offset:1760
ds_read_b128 v[176:179],v18 offset:1792
ds_read_b128 v[180:183],v18 offset:1824
ds_read_b128 v[184:187],v18 offset:1856
ds_read_b128 v[188:191],v18 offset:1888
s_waitcnt lgkmcnt(12)
s_setprio 1
v_mfma_f32_32x32x16_bf16 v[96:111],v[200:203],v[192:195],v[96:111]
v_lshlrev_b32 v232,16,v72
v_lshlrev_b32 v233,16,v73
v_lshlrev_b32 v234,16,v74
v_lshlrev_b32 v235,16,v75
v_mfma_f32_32x32x16_bf16 v[96:111],v[204:207],v[196:199],v[96:111]
v_lshlrev_b32 v236,16,v76
v_lshlrev_b32 v237,16,v77
v_lshlrev_b32 v238,16,v78
v_lshlrev_b32 v239,16,v79
v_mfma_f32_32x32x16_bf16 v[112:127],v[208:211],v[192:195],v[112:127]
v_lshlrev_b32 v240,16,v80
v_lshlrev_b32 v241,16,v81
v_lshlrev_b32 v242,16,v82
v_lshlrev_b32 v243,16,v83
v_mfma_f32_32x32x16_bf16 v[112:127],v[212:215],v[196:199],v[112:127]
v_lshlrev_b32 v244,16,v84
v_lshlrev_b32 v245,16,v85
v_lshlrev_b32 v246,16,v86
v_lshlrev_b32 v247,16,v87
v_mfma_f32_32x32x16_bf16 v[128:143],v[216:219],v[192:195],v[128:143]
v_lshlrev_b32 v248,16,v88
v_lshlrev_b32 v249,16,v89
v_lshlrev_b32 v250,16,v90
v_lshlrev_b32 v251,16,v91
v_mfma_f32_32x32x16_bf16 v[128:143],v[220:223],v[196:199],v[128:143]
v_lshlrev_b32 v252,16,v92
v_lshlrev_b32 v253,16,v93
v_lshlrev_b32 v254,16,v94
v_lshlrev_b32 v255,16,v95
s_setprio 0
s_waitcnt lgkmcnt(0)
v_and_b32 v72,0xffff0000,v72
v_and_b32 v73,0xffff0000,v73
v_and_b32 v74,0xffff0000,v74
v_and_b32 v75,0xffff0000,v75
v_and_b32 v76,0xffff0000,v76
v_and_b32 v77,0xffff0000,v77
v_and_b32 v78,0xffff0000,v78
v_and_b32 v79,0xffff0000,v79
v_and_b32 v80,0xffff0000,v80
v_and_b32 v81,0xffff0000,v81
v_and_b32 v82,0xffff0000,v82
v_and_b32 v83,0xffff0000,v83
v_and_b32 v84,0xffff0000,v84
v_and_b32 v85,0xffff0000,v85
v_and_b32 v86,0xffff0000,v86
v_and_b32 v87,0xffff0000,v87
v_and_b32 v88,0xffff0000,v88
v_and_b32 v89,0xffff0000,v89
v_and_b32 v90,0xffff0000,v90
v_and_b32 v91,0xffff0000,v91
v_and_b32 v92,0xffff0000,v92
v_and_b32 v93,0xffff0000,v93
v_and_b32 v94,0xffff0000,v94
v_and_b32 v95,0xffff0000,v95
v_fma_f32 v232,v96,v144,v232
v_fma_f32 v72,v97,v145,v72
v_fma_f32 v233,v98,v146,v233
v_fma_f32 v73,v99,v147,v73
v_fma_f32 v234,v100,v148,v234
v_fma_f32 v74,v101,v149,v74
v_fma_f32 v235,v102,v150,v235
v_fma_f32 v75,v103,v151,v75
v_fma_f32 v236,v104,v152,v236
v_fma_f32 v76,v105,v153,v76
v_fma_f32 v237,v106,v154,v237
v_fma_f32 v77,v107,v155,v77
v_fma_f32 v238,v108,v156,v238
v_fma_f32 v78,v109,v157,v78
v_fma_f32 v239,v110,v158,v239
v_fma_f32 v79,v111,v159,v79
v_fma_f32 v240,v112,v160,v240
v_fma_f32 v80,v113,v161,v80
v_fma_f32 v241,v114,v162,v241
v_fma_f32 v81,v115,v163,v81
v_fma_f32 v242,v116,v164,v242
v_fma_f32 v82,v117,v165,v82
v_fma_f32 v243,v118,v166,v243
v_fma_f32 v83,v119,v167,v83
v_fma_f32 v244,v120,v168,v244
v_fma_f32 v84,v121,v169,v84
v_fma_f32 v245,v122,v170,v245
v_fma_f32 v85,v123,v171,v85
v_fma_f32 v246,v124,v172,v246
v_fma_f32 v86,v125,v173,v86
v_fma_f32 v247,v126,v174,v247
v_fma_f32 v87,v127,v175,v87
v_fma_f32 v248,v128,v176,v248
v_fma_f32 v88,v129,v177,v88
v_fma_f32 v249,v130,v178,v249
v_fma_f32 v89,v131,v179,v89
v_fma_f32 v250,v132,v180,v250
v_fma_f32 v90,v133,v181,v90
v_fma_f32 v251,v134,v182,v251
v_fma_f32 v91,v135,v183,v91
v_fma_f32 v252,v136,v184,v252
v_fma_f32 v92,v137,v185,v92
v_fma_f32 v253,v138,v186,v253
v_fma_f32 v93,v139,v187,v93
v_fma_f32 v254,v140,v188,v254
v_fma_f32 v94,v141,v189,v94
v_fma_f32 v255,v142,v190,v255
v_fma_f32 v95,v143,v191,v95
v_add_u32 v4,s54,v4
v_add_u32 v5,s54,v5
v_add_u32 v6,s54,v6
v_add_u32 v7,s54,v7
v_add_u32 v8,s54,v8
v_add_u32 v9,s54,v9
v_add_u32 v10,s54,v10
v_add_u32 v11,s54,v11
v_add_u32 v12,s54,v12
v_add_u32 v13,s54,v13
v_add_u32 v14,s54,v14
v_add_u32 v15,s54,v15
v_cvt_pk_bf16_f32 v200,v232,v72
v_cvt_pk_bf16_f32 v201,v233,v73
v_cvt_pk_bf16_f32 v202,v234,v74
v_cvt_pk_bf16_f32 v203,v235,v75
v_cvt_pk_bf16_f32 v204,v236,v76
v_cvt_pk_bf16_f32 v205,v237,v77
v_cvt_pk_bf16_f32 v206,v238,v78
v_cvt_pk_bf16_f32 v207,v239,v79
v_cvt_pk_bf16_f32 v208,v240,v80
v_cvt_pk_bf16_f32 v209,v241,v81
v_cvt_pk_bf16_f32 v210,v242,v82
v_cvt_pk_bf16_f32 v211,v243,v83
v_cvt_pk_bf16_f32 v212,v244,v84
v_cvt_pk_bf16_f32 v213,v245,v85
v_cvt_pk_bf16_f32 v214,v246,v86
v_cvt_pk_bf16_f32 v215,v247,v87
v_cvt_pk_bf16_f32 v216,v248,v88
v_cvt_pk_bf16_f32 v217,v249,v89
v_cvt_pk_bf16_f32 v218,v250,v90
v_cvt_pk_bf16_f32 v219,v251,v91
v_cvt_pk_bf16_f32 v220,v252,v92
v_cvt_pk_bf16_f32 v221,v253,v93
v_cvt_pk_bf16_f32 v222,v254,v94
v_cvt_pk_bf16_f32 v223,v255,v95
ds_write_b64 v20,v[200:201] offset:0
ds_write_b64 v21,v[202:203] offset:0
ds_write_b64 v22,v[204:205] offset:0
ds_write_b64 v23,v[206:207] offset:0
ds_write_b64 v20,v[208:209] offset:64
ds_write_b64 v21,v[210:211] offset:64
ds_write_b64 v22,v[212:213] offset:64
ds_write_b64 v23,v[214:215] offset:64
ds_write_b64 v20,v[216:217] offset:128
ds_write_b64 v21,v[218:219] offset:128
ds_write_b64 v22,v[220:221] offset:128
ds_write_b64 v23,v[222:223] offset:128
ds_read_b128 v[72:75],v19 offset:0
ds_read_b128 v[76:79],v19 offset:1024
ds_read_b128 v[80:83],v19 offset:2048
ds_read_b128 v[84:87],v19 offset:3456
ds_read_b128 v[88:91],v19 offset:4480
ds_read_b128 v[92:95],v19 offset:5504
v_add_u32 v20,s54,v20
v_add_u32 v21,s54,v21
v_add_u32 v22,s54,v22
v_add_u32 v23,s54,v23
s_sub_u32 s68,s68,s54
s_add_u32 s59,s59,1572864
s_add_u32 s53,s53,1
s_cmp_eq_u32 s53,3
s_cselect_b32 s59,0x70000000,s59
s_add_u32 s67,s69,98304
s_waitcnt lgkmcnt(5)
buffer_store_dwordx4 v[72:75],v24,s[40:43],s69 offen nt
s_waitcnt lgkmcnt(4)
buffer_store_dwordx4 v[76:79],v25,s[40:43],s69 offen nt
s_waitcnt lgkmcnt(3)
buffer_store_dwordx4 v[80:83],v26,s[40:43],s69 offen nt
s_waitcnt lgkmcnt(2)
buffer_store_dwordx4 v[84:87],v24,s[40:43],s67 offen nt
s_waitcnt lgkmcnt(1)
buffer_store_dwordx4 v[88:91],v25,s[40:43],s67 offen nt
s_waitcnt lgkmcnt(0)
buffer_store_dwordx4 v[92:95],v26,s[40:43],s67 offen nt
v_add_u32 v19,s54,v19
s_add_u32 s69,s69,1572864
s_sub_u32 s54,0,s54
s_cmp_lt_u32 s53,4
s_cbranch_scc1 L_pass1_%=
L_end_%=:
s_waitcnt vmcnt(0) lgkmcnt(0)
s_endpgm
.p2align 12
)ASM"
    :: "s"(blockIdx.x), "v"(tid), "s"((unsigned long long)__builtin_amdgcn_kernarg_segment_ptr())
    : "v1", "v2", "v3", "v4", "v5", "v6", "v7", "v8", "v9", "v10", "v11", "v12", "v13", "v14", "v15", "v16", "v17", "v18", "v19", "v20", "v21", "v22", "v23", "v24", "v25", "v26", "v27", "v28", "v29", "v30", "v31", "v32", "v33", "v34", "v35", "v36", "v37", "v38", "v39", "v40", "v41", "v42", "v43", "v44", "v45", "v46", "v47", "v48", "v49", "v50", "v51", "v52", "v53", "v54", "v55", "v56", "v57", "v58", "v59", "v60", "v61", "v62", "v63", "v64", "v65", "v66", "v67", "v68", "v69", "v70", "v71", "v72", "v73", "v74", "v75", "v76", "v77", "v78", "v79", "v80", "v81", "v82", "v83", "v84", "v85", "v86", "v87", "v88", "v89", "v90", "v91", "v92", "v93", "v94", "v95", "v96", "v97", "v98", "v99", "v100", "v101", "v102", "v103", "v104", "v105", "v106", "v107", "v108", "v109", "v110", "v111", "v112", "v113", "v114", "v115", "v116", "v117", "v118", "v119", "v120", "v121", "v122", "v123", "v124", "v125", "v126", "v127", "v128", "v129", "v130", "v131", "v132", "v133", "v134", "v135", "v136", "v137", "v138", "v139", "v140", "v141", "v142", "v143", "v144", "v145", "v146", "v147", "v148", "v149", "v150", "v151", "v152", "v153", "v154", "v155", "v156", "v157", "v158", "v159", "v160", "v161", "v162", "v163", "v164", "v165", "v166", "v167", "v168", "v169", "v170", "v171", "v172", "v173", "v174", "v175", "v176", "v177", "v178", "v179", "v180", "v181", "v182", "v183", "v184", "v185", "v186", "v187", "v188", "v189", "v190", "v191", "v192", "v193", "v194", "v195", "v196", "v197", "v198", "v199", "v200", "v201", "v202", "v203", "v204", "v205", "v206", "v207", "v208", "v209", "v210", "v211", "v212", "v213", "v214", "v215", "v216", "v217", "v218", "v219", "v220", "v221", "v222", "v223", "v224", "v225", "v226", "v227", "v228", "v229", "v230", "v231", "v232", "v233", "v234", "v235", "v236", "v237", "v238", "v239", "v240", "v241", "v242", "v243", "v244", "v245", "v246", "v247", "v248", "v249", "v250", "v251", "v252", "v253", "v254", "v255", "s4", "s5", "s6", "s7", "s8", "s9", "s10", "s11", "s12", "s13", "s14", "s15", "s16", "s17", "s18", "s19", "s20", "s21", "s22", "s23", "s24", "s25", "s26", "s27", "s28", "s29", "s30", "s31", "s32", "s33", "s34", "s35", "s36", "s37", "s38", "s39", "s40", "s41", "s42", "s43", "s44", "s45", "s46", "s47", "s48", "s49", "s50", "s51", "s52", "s53", "s54", "s55", "s56", "s57", "s58", "s59", "s60", "s61", "s62", "s63", "s64", "s65", "s66", "s67", "s68", "s69", "s70", "s71", "s72", "s73", "s74", "s75", "s76", "s77", "s78", "s79", "s80", "s81", "s82", "s83", "s84", "s85", "s86", "s87", "s88", "s89", "s90", "s91", "s92", "s93", "s94", "s95", "s96", "s97", "s98", "s99", "s100", "s101", "vcc", "scc", "memory");
}

extern "C" void kernel_launch(void* const* d_in, const int* in_sizes, int n_in,
                              void* d_out, int out_size, void* d_ws, size_t ws_size,
                              hipStream_t stream) {
    (void)hipFuncSetAttribute((const void*)convnext_kernel, hipFuncAttributeMaxDynamicSharedMemorySize, LDS_BYTES);
    convnext_kernel<<<256, 512, LDS_BYTES, stream>>>(
        (const unsigned short*)d_in[0], (const unsigned short*)d_in[1], (const unsigned short*)d_in[2], (const unsigned short*)d_in[3],
        (const unsigned short*)d_in[4], (const unsigned short*)d_in[5], (const unsigned short*)d_in[6], (unsigned short*)d_out);
}
```
